```python
import torch

def _softmax(x, axis=-1):
    return torch.softmax(x, dim=axis)

def _gelu(x):
    return torch.nn.functional.gelu(x, approximate='tanh')

def _silu(x):
    return torch.nn.functional.silu(x)

def _sigmoid(x):
    return torch.sigmoid(x)

def _layernorm(x, axis=-1, eps=1e-05):
    m = x.mean(dim=axis, keepdim=True)
    v = x.var(dim=axis, keepdim=True, unbiased=False)
    return (x - m) / torch.sqrt(v + eps)

def _scatter_add(tgt, idx, src, axis=0):
    return tgt.clone().index_add_(axis, idx, src)

def _segment_sum(src, seg, n):
    out = torch.zeros((n,) + src.shape[1:], dtype=src.dtype)
    return out.index_add_(0, seg, src)

def _pad(x, pw):
    flat = tuple((v for pair in reversed(pw) for v in pair))
    return torch.nn.functional.pad(x, flat)
kernel_name = 'adaLN_cond__sliding_block_cached__moe_arch_mod__prompt'

def setup_inputs():

    _w = lambda a: (lambda t: t.to(torch.bfloat16) if t.is_floating_point() else t)(torch.as_tensor(a))

    def _A_setup():
        B, Sq, D = (1, 2048, 1024)
        Hq, Hkv, Sk, Dh = (32, 4, 2048, 128)
        assert Hq % Hkv == 0, (Hq, Hkv)
        return {'x': _w((lambda *s: torch.randn(*s, dtype=torch.bfloat16) * 0.05)(B, Sq, D)), 'k_cache': _w((lambda *s: torch.randn(*s, dtype=torch.bfloat16) * 3.2)(B, Hkv, Sk, Dh)), 'v_cache': _w((lambda *s: torch.randn(*s, dtype=torch.bfloat16) * 0.4)(B, Hkv, Sk, Dh)), 'wq': _w((lambda *s: torch.randn(*s, dtype=torch.bfloat16) * 3.2)(D, Hq * Dh) * D ** (-0.5)), 'wo': _w((lambda *s: torch.randn(*s, dtype=torch.bfloat16) * 0.4)(Hq * Dh, D) * (Hq * Dh) ** (-0.5))}
    _A_in = _A_setup()

    def _B_setup():
        B, S, D, F, C = (1, 2048, 1024, 3584, 1)
        return {'x': _w((lambda *s: torch.randn(*s, dtype=torch.bfloat16) * 0.05)(B, S, D)), 'w_r': _w((lambda *s: torch.randn(*s, dtype=torch.bfloat16) * 0.05)(D)), 'w_gate': _w((lambda *s: torch.randn(*s, dtype=torch.bfloat16) * 0.05)(D, F)), 'w_up': _w((lambda *s: torch.randn(*s, dtype=torch.bfloat16) * 0.05)(D, F)), 'w_down': _w((lambda *s: torch.randn(*s, dtype=torch.bfloat16) * 0.05)(F, D))}
    _B_in = _B_setup()
    return {'A_x': _A_in['x'], 'A_k_cache': _A_in['k_cache'], 'A_v_cache': _A_in['v_cache'], 'A_wq': _A_in['wq'], 'A_wo': _A_in['wo'], 'B_x': _B_in['x'], 'B_w_r': _B_in['w_r'], 'B_w_gate': _B_in['w_gate'], 'B_w_up': _B_in['w_up'], 'B_w_down': _B_in['w_down'], 'c': _w((lambda *s: torch.randn(*s, dtype=torch.bfloat16) * 0.1)(1, 1024) * 8.0), 'w_mod': _w((lambda *s: torch.randn(*s, dtype=torch.bfloat16) * 0.1)(1024, 6 * 1024) * 1024 ** (-0.5) * 8.0)}

def reference(A_x, A_k_cache, A_v_cache, A_wq, A_wo, B_x, B_w_r, B_w_gate, B_w_up, B_w_down, c, w_mod):

    def _A_ref(x, k_cache, v_cache, wq, wo):
        B, Sq, D = x.shape
        Hq, Dh = (32, 128)
        Hkv, Sk = (k_cache.shape[1], k_cache.shape[2])
        W = min(4096, max(256, Sk // 4))
        rep = Hq // Hkv
        q = (lambda x, *d: x.permute(*d))((x @ wq).reshape(B, Sq, Hq, Dh), 0, 2, 1, 3)
        k = (lambda x, n, axis: torch.repeat_interleave(x, n, dim=axis))(k_cache[:, :, Sk - W:, :], rep, axis=1)
        v = (lambda x, n, axis: torch.repeat_interleave(x, n, dim=axis))(v_cache[:, :, Sk - W:, :], rep, axis=1)
        scores = torch.einsum('bhqd,bhkd->bhqk', q, k) * Dh ** (-0.5)
        p = _softmax(scores, axis=-1)
        o = torch.einsum('bhqk,bhkd->bhqd', p, v)
        return (lambda x, *d: x.permute(*d))(o, 0, 2, 1, 3).reshape(B, Sq, Hq * Dh) @ wo

    def _B_ref(x, w_r, w_gate, w_up, w_down):
        B, S, D = x.shape
        C = 1
        r = x @ w_r
        idx = torch.argsort(-r, axis=-1)[:, :C]
        idx_e = (lambda x, n, axis: torch.repeat_interleave(x, n, dim=axis))(idx.reshape(B, C, 1), D, axis=2)
        x_sel = (lambda x, i, axis: torch.take_along_dim(x, i, dim=axis))(x, idx_e, axis=1)
        g = _silu(x_sel @ w_gate)
        u = x_sel @ w_up
        y = g * u @ w_down
        r_sel = _sigmoid((lambda x, i, axis: torch.take_along_dim(x, i, dim=axis))(r, idx, axis=1))
        y = y * r_sel.reshape(B, C, 1)
        b_off = (torch.arange(B) * S).reshape(B, 1)
        flat_idx = (idx + b_off).reshape(B * C)
        zero = (x * 0.0).reshape(B * S, D)
        upd = _scatter_add(zero, flat_idx, y.reshape(B * C, D), axis=0)
        return x + upd.reshape(B, S, D)
    x = A_x
    mod = (_silu(c) @ w_mod).reshape(c.shape[0], 6, 1, -1)
    sa, ca, ga, sb, cb, gb = (mod[:, 0].clone(), mod[:, 1].clone(), mod[:, 2].clone(), mod[:, 3].clone(), mod[:, 4].clone(), mod[:, 5].clone())
    h = _layernorm(x, axis=-1) * (1 + ca) + sa
    x = x + ga * _A_ref(h, A_k_cache, A_v_cache, A_wq, A_wo)
    h = _layernorm(x, axis=-1) * (1 + cb) + sb
    x = x + gb * _B_ref(h, B_w_r, B_w_gate, B_w_up, B_w_down)
    return x
```

```cpp
#include <hip/hip_runtime.h>
#include <hip/hip_bf16.h>
#include <stdint.h>
#include <utility>

typedef unsigned short u16;
using bf16x8 = __attribute__((ext_vector_type(8))) short;
using s16x4  = __attribute__((ext_vector_type(4))) short;
using f32x16 = __attribute__((ext_vector_type(16))) float;
using u32x16 = __attribute__((ext_vector_type(16))) unsigned;
using f32x4  = __attribute__((ext_vector_type(4))) float;
using f32x2  = __attribute__((ext_vector_type(2))) float;
using u32x4  = __attribute__((ext_vector_type(4))) unsigned;
using u32x2  = __attribute__((ext_vector_type(2))) unsigned;
using u32x8 = __attribute__((ext_vector_type(8))) unsigned;
using f32x8 = __attribute__((ext_vector_type(8))) float;
using f32x32 = __attribute__((ext_vector_type(32))) float;
using u32x32 = __attribute__((ext_vector_type(32))) unsigned;
typedef __bf16 bf16x2_t __attribute__((ext_vector_type(2)));
#define DI __device__ __forceinline__
#define NTMASK 0
#define NT_SEL(bit) ((NTMASK >> (bit)) & 1)
#if NT_SEL(0)
#define NTX " nt"
#else
#define NTX ""
#endif
#if NT_SEL(1)
#define NTM " nt"
#else
#define NTM ""
#endif
#if NT_SEL(2)
#define NTQ " nt"
#else
#define NTQ ""
#endif
#if NT_SEL(3)
#define NTO " nt"
#else
#define NTO ""
#endif
#if NT_SEL(4)
#define NTG " nt"
#else
#define NTG ""
#endif
#if NT_SEL(5)
#define NTD " nt"
#else
#define NTD ""
#endif
#if NT_SEL(6)
#define NTT " nt"
#else
#define NTT ""
#endif
#if NT_SEL(7)
#define NTF " nt"
#else
#define NTF ""
#endif

constexpr int S = 2048, DM = 1024, HQ = 32, HKV = 4, DH = 128, SKV = 2048, WIN = 512, FF = 3584;
constexpr int QD = HQ * DH;
constexpr float QSCALE = 0.12751743082f;

DI float bf2f(u16 v) { return __uint_as_float(((unsigned)v) << 16); }
DI u16 f2bf(float x) { unsigned u = __float_as_uint(x); u += 0x7fffu + ((u >> 16) & 1u); return (u16)(u >> 16); }
DI unsigned cvtpk(float lo, float hi) { f32x2 v = {lo, hi}; bf16x2_t b = __builtin_convertvector(v, bf16x2_t); return __builtin_bit_cast(unsigned, b); }
DI float lo16f(unsigned w) { return __uint_as_float(w << 16); }
DI float hi16f(unsigned w) { return __uint_as_float(w & 0xffff0000u); }
DI float rbf(float x) { return lo16f(cvtpk(x, x)); }
DI f32x2 up2(unsigned w) { return f32x2{lo16f(w), hi16f(w)}; }
DI float dot2(unsigned a, unsigned b, float c) { return __builtin_amdgcn_fdot2_f32_bf16(__builtin_bit_cast(bf16x2_t, a), __builtin_bit_cast(bf16x2_t, b), c, false); }
DI unsigned plo(unsigned a, unsigned b) { return __builtin_amdgcn_perm(b, a, 0x05040100u); }
DI unsigned phi(unsigned a, unsigned b) { return __builtin_amdgcn_perm(b, a, 0x07060302u); }
DI unsigned rfl(unsigned x) { return __builtin_amdgcn_readfirstlane(x); }
template <int CTRL> DI float dpp_f(float v) { return __int_as_float(__builtin_amdgcn_update_dpp(0, __float_as_int(v), CTRL, 0xf, 0xf, true)); }
template <int CTRL> DI unsigned dpp_u(unsigned v) { return (unsigned)__builtin_amdgcn_update_dpp(0, (int)v, CTRL, 0xf, 0xf, true); }
DI float fold16(float a, float b) { auto r = __builtin_amdgcn_permlane16_swap(__float_as_uint(a), __float_as_uint(b), false, false); return __uint_as_float(r[0]) + __uint_as_float(r[1]); }
DI float fold32(float a, float b) { auto r = __builtin_amdgcn_permlane32_swap(__float_as_uint(a), __float_as_uint(b), false, false); return __uint_as_float(r[0]) + __uint_as_float(r[1]); }
DI float sum16(float v) { return fold16(v, v); }
DI float sum32(float v) { return fold32(v, v); }
DI float sum_row16(float v) { v += dpp_f<0xB1>(v); v += dpp_f<0x4E>(v); v += dpp_f<0x141>(v); v += dpp_f<0x140>(v); return v; }
DI float wave_sum(float v) { return sum32(sum16(sum_row16(v))); }
DI unsigned wave_maxu(unsigned v) {
  v = max(v, dpp_u<0xB1>(v)); v = max(v, dpp_u<0x4E>(v)); v = max(v, dpp_u<0x141>(v)); v = max(v, dpp_u<0x140>(v));
  { auto r = __builtin_amdgcn_permlane16_swap(v, v, false, false); v = max(r[0], r[1]); }
  { auto r = __builtin_amdgcn_permlane32_swap(v, v, false, false); v = max(r[0], r[1]); }
  return v;
}
#define GAP() __builtin_amdgcn_sched_barrier(0)
template <int N> using iseq = std::make_integer_sequence<int, N>;
template <int... Is> using ilist = std::integer_sequence<int, Is...>;
typedef __amdgpu_buffer_rsrc_t srd_t;
#define SRD3 0x00020000
DI srd_t mk_srd(const void* p, unsigned bytes) { return __builtin_amdgcn_make_buffer_rsrc((void*)p, (short)0, (int)bytes, SRD3); }
DI unsigned lane_id() { unsigned l; asm volatile("v_mbcnt_lo_u32_b32 %0, -1, 0\n\tv_mbcnt_hi_u32_b32 %0, -1, %0" : "=v"(l)); return l; }
struct Args { const u16 *x, *kc, *vc, *wq, *wo, *w_r, *wg, *wu, *wd, *c, *w_mod; u16* out; char* ws; };
typedef const __attribute__((address_space(4))) Args* argp_t;
DI argp_t args() { argp_t p = (argp_t)__builtin_amdgcn_kernarg_segment_ptr(); asm volatile("" : "+s"(p)); return p; }
#define BARRIER_LGKM() do { asm volatile("s_waitcnt lgkmcnt(0)\n\ts_barrier" ::: "memory"); } while (0)

constexpr unsigned W_EPOCH = 0x260000, W_TMO = 256;
constexpr unsigned W_FLAGH = 0x261000;
constexpr unsigned W_VOTE = 0x2000, W_PROBE = 0x4000;
constexpr unsigned W_GKEY = 0x10000;
constexpr unsigned W_FLAGR = 0x8000;
constexpr unsigned W_FLAGO = 0x20000;
constexpr unsigned W_CANDH = 0x40000;
constexpr unsigned W_STAT = 0x80000;
constexpr unsigned W_YG = 0x100000;
constexpr unsigned W_CANDX = 0x1E0000;
constexpr unsigned W_ST1 = 0x200000;
constexpr unsigned W_MODA = 0x210000;
constexpr unsigned W_MODB = 0x220000;
constexpr unsigned W_SIL = 0x240000;
constexpr unsigned W_HT = 4u << 20;
constexpr unsigned W_OT = 8u << 20;
constexpr unsigned W_XCH = 24u << 20;
constexpr unsigned W_END = 40u << 20;
constexpr unsigned TAGM = 0x40004000u, TAGC = 0x9E3739B9u;
#define SPINCAP 262144
constexpr unsigned kSpinCap = SPINCAP;
#define SL0 1
#define SL1 1
#define SL2 1
#define SL3 1
template <int SL = 1> DI bool spin_fail(unsigned& spins, srd_t ws, unsigned tag) {
  __builtin_amdgcn_s_sleep(SL);
  if (++spins > kSpinCap) { __builtin_amdgcn_raw_buffer_store_b32(tag, ws, W_TMO, 0, 16); return true; }
  return (spins & 255u) == 0u && rfl(__builtin_amdgcn_raw_buffer_load_b32(ws, W_TMO, 0, 16)) == tag;
}
DI u32x4 ld16(srd_t ws, unsigned off) { return __builtin_amdgcn_raw_buffer_load_b128(ws, off, 0, 16); }
DI u32x2 ld8(srd_t ws, unsigned off) { return __builtin_amdgcn_raw_buffer_load_b64(ws, off, 0, 16); }
DI void st16(srd_t ws, unsigned off, u32x4 v) { __builtin_amdgcn_raw_buffer_store_b128(v, ws, off, 0, 16); }
DI void st8(srd_t ws, unsigned off, u32x2 v) { __builtin_amdgcn_raw_buffer_store_b64(v, ws, off, 0, 16); }
DI void st4(srd_t ws, unsigned off, unsigned v) { __builtin_amdgcn_raw_buffer_store_b32(v, ws, off, 0, 16); }
DI void wait_flags(srd_t ws, unsigned off, unsigned tag) {
  const unsigned o = off + (lane_id() & 31u) * 4u;
  for (unsigned spins = 0;;) {
    const unsigned f = __builtin_amdgcn_raw_buffer_load_b32(ws, o, 0, 16);
    if (__all(f == tag)) break;
    if (spin_fail(spins, ws, tag)) break;
  }
  asm volatile("" ::: "memory");
}
template <int OFF, bool DEV = false> DI u32x4 ald16(srd_t s, unsigned vo, unsigned so) {
  u32x4 r;
  if constexpr (DEV) asm volatile("buffer_load_dwordx4 %0, %1, %2, %3 offen offset:%4 sc1" : "=v"(r) : "v"(vo), "s"(s), "s"(so), "i"(OFF) : "memory");
  else               asm volatile("buffer_load_dwordx4 %0, %1, %2, %3 offen offset:%4" NTF : "=v"(r) : "v"(vo), "s"(s), "s"(so), "i"(OFF) : "memory");
  return r;
}
template <int OFF> DI u32x2 ald8(srd_t s, unsigned vo, unsigned so) {
  u32x2 r; asm volatile("buffer_load_dwordx2 %0, %1, %2, %3 offen offset:%4" : "=v"(r) : "v"(vo), "s"(s), "s"(so), "i"(OFF) : "memory"); return r;
}
DI u32x2 ald8d(srd_t s, unsigned vo) { u32x2 r; asm volatile("buffer_load_dwordx2 %0, %1, %2, 0 offen sc1" : "=v"(r) : "v"(vo), "s"(s) : "memory"); return r; }
template <int OFF, bool DEV = false> DI unsigned ald4(srd_t s, unsigned vo, unsigned so) {
  unsigned r;
  if constexpr (DEV) asm volatile("buffer_load_dword %0, %1, %2, %3 offen offset:%4 sc1" : "=v"(r) : "v"(vo), "s"(s), "s"(so), "i"(OFF) : "memory");
  else               asm volatile("buffer_load_dword %0, %1, %2, %3 offen offset:%4" : "=v"(r) : "v"(vo), "s"(s), "s"(so), "i"(OFF) : "memory");
  return r;
}
DI void touch(unsigned& d, const void* base, unsigned vo) { asm volatile("global_load_dword %0, %1, %2" : "+v"(d) : "v"(vo), "s"(base) : "memory"); }
DI void touchq(unsigned& d, const void* base, unsigned vo) { asm volatile("global_load_dword %0, %1, %2" NTQ : "+v"(d) : "v"(vo), "s"(base) : "memory"); }
template <class T> DI T lds_ld(unsigned a) { return *reinterpret_cast<const __attribute__((address_space(3))) T*>(a); }
template <class T> DI void lds_st(unsigned a, T v) { *reinterpret_cast<__attribute__((address_space(3))) T*>(a) = v; }
DI float silu_bf(float v) { return rbf(v * __builtin_amdgcn_rcpf(1.f + __expf(-v))); }
struct Ctx { static constexpr unsigned ldsb = 0; unsigned pm, bid, wid, xcd, j, kvh, hl, rblk, tag, cs, nb; srd_t ws; };
#define TAIL2 1
DI unsigned ffn_j(const Ctx& k) { return TAIL2 ? (k.j < 24 ? k.j : 24 + (k.j & 3)) : min(k.j, 27u); }
DI bool ffn_idle(const Ctx& k) { return TAIL2 ? (k.j >> 2) == 6 + (k.xcd & 1) : k.j >= 28; }
constexpr unsigned UNIT = 16384;
#define STAMPA(I) do { } while (0)
#define STAMPO(I) do { } while (0)
#define STAMP_DECL
#define STAMP(I) do { } while (0)
#define STAMP_FLUSH() do { } while (0)
#define STAMP_ARG
#define STAMP_PASS
#define ACLOB_LO "a0","a1","a2","a3","a4","a5","a6","a7","a8","a9","a10","a11","a12","a13","a14","a15","a16","a17","a18","a19","a20","a21","a22","a23","a24","a25","a26","a27","a28","a29","a30","a31","a32","a33","a34","a35","a36","a37","a38","a39","a40","a41","a42","a43","a44","a45","a46","a47","a48","a49","a50","a51","a52","a53","a54","a55","a56","a57","a58","a59","a60","a61","a62","a63","a64","a65","a66","a67","a68","a69","a70","a71","a72","a73","a74","a75","a76","a77","a78","a79","a80","a81","a82","a83","a84","a85","a86","a87","a88","a89","a90","a91","a92","a93","a94","a95","a96","a97","a98","a99","a100","a101","a102","a103","a104","a105","a106","a107","a108","a109","a110","a111","a112","a113","a114","a115","a116","a117","a118","a119","a120","a121","a122","a123","a124","a125","a126","a127"
#define ACLOB_HI "a128","a129","a130","a131","a132","a133","a134","a135","a136","a137","a138","a139","a140","a141","a142","a143","a144","a145","a146","a147","a148","a149","a150","a151","a152","a153","a154","a155","a156","a157","a158","a159","a160","a161","a162","a163","a164","a165","a166","a167","a168","a169","a170","a171","a172","a173","a174","a175","a176","a177","a178","a179","a180","a181","a182","a183","a184","a185","a186","a187","a188","a189","a190","a191","a192","a193","a194","a195","a196","a197","a198","a199","a200","a201","a202","a203","a204","a205","a206","a207","a208","a209","a210","a211","a212","a213","a214","a215","a216","a217","a218","a219","a220","a221","a222","a223","a224","a225","a226","a227","a228","a229","a230","a231","a232","a233","a234","a235","a236","a237","a238","a239","a240","a241","a242","a243","a244","a245","a246","a247","a248","a249","a250","a251","a252","a253","a254","a255"
#define HOP_DEFS ".set .Lhc00, .Lhs0\n\t.set .Lhs0n, .Lhs4\n\t.set .Lhs4n, .Lhs8\n\t.set .Lhs8n, .Lhr0\n\t.set .Lhc01, .Lhs1\n\t.set .Lhs1n, .Lhs5\n\t.set .Lhs5n, .Lhs9\n\t.set .Lhs9n, .Lhr0\n\t.set .Lhc02, .Lhs2\n\t.set .Lhs2n, .Lhs6\n\t.set .Lhs6n, .Lhs10\n\t.set .Lhs10n, .Lhr0\n\t.set .Lhc03, .Lhs3\n\t.set .Lhs3n, .Lhs7\n\t.set .Lhs7n, .Lhr0\n\t.set .Lhc10, .Lhs11\n\t.set .Lhs11n, .Lhs15\n\t.set .Lhs15n, .Lhr1\n\t.set .Lhc11, .Lhs12\n\t.set .Lhs12n, .Lht0\n\t.set .Lht0n, .Lhr1\n\t.set .Lhc12, .Lhs13\n\t.set .Lhs13n, .Lht1\n\t.set .Lht1n, .Lhr1\n\t.set .Lhc13, .Lhs14\n\t.set .Lhs14n, .Lhr1\n\t.set .Lhc20, .Lhf\n\t.set .Lhfn, .Lhr2\n\t.set .Lhc21, .Lhr2\n\t.set .Lhc22, .Lhr2\n\t.set .Lhc23, .Lhr2"
#define HOP_PAGE(n) asm volatile("s_branch .Lht" #n "\n\t.p2align 12\n.Lht" #n ":\n\ts_cbranch_execz .Lht" #n "n")
#define PAGE_ALIGN() asm volatile("s_branch .Lpa%=\n\t.p2align 12\n.Lpa%=:" ::: "memory")
#define HOP_FRONT() asm volatile("s_branch .Lhf\n\t.p2align 12\n.Lhf:\n\ts_cbranch_execz .Lhfn")
#define HOP_GO(g, w) asm volatile("s_mov_b64 exec, 0\n\ts_cmp_eq_u32 %0, 0\n\ts_cbranch_scc1 .Lhc" #g "0\n\ts_cmp_eq_u32 %0, 1\n\ts_cbranch_scc1 .Lhc" #g "1\n\ts_cmp_eq_u32 %0, 2\n\ts_cbranch_scc1 .Lhc" #g "2\n\ts_branch .Lhc" #g "3\n.Lhr" #g ":\n\ts_mov_b64 exec, -1" :: "s"(w) : "scc", "memory")
#define STP(x)
#define OP_ASM \
  "s_waitcnt lgkmcnt(0)\n\t" \
  "s_barrier\n\t" \
  STP("s_memrealtime s[94:95]\n\t") \
  STP("s_memtime s[92:93]\n\t") \
  "ds_read_b32 v228, v213 offset:49152\n\t" \
  "v_mfma_f32_16x16x32_bf16 a[8:11], a[140:143], v[76:79], 0\n\t" \
  "v_add_u32 v238, 14336, v240\n\t" \
  "v_add_u32 v239, 21504, v241\n\t" \
  "v_mfma_f32_16x16x32_bf16 a[8:11], a[136:139], v[72:75], a[8:11]\n\t" \
  "ds_read_b128 a[12:15], v242 offset:32768\n\t" \
  "v_add_u32 v241, 7168, v241\n\t" \
  "v_mfma_f32_16x16x32_bf16 a[8:11], a[132:135], v[68:71], a[8:11]\n\t" \
  "s_mov_b32 s79, 0x05040100\n\t" \
  "ds_read_b128 a[28:31], v242 offset:33792\n\t" \
  "s_mov_b32 s80, 0x07060302\n\t" \
  "v_mfma_f32_16x16x32_bf16 a[8:11], a[128:131], v[64:67], a[8:11]\n\t" \
  "ds_read_b128 a[44:47], v242 offset:34816\n\t" \
  "v_mfma_f32_16x16x32_bf16 a[24:27], a[144:147], v[64:67], 0\n\t" \
  "ds_read_b128 a[60:63], v242 offset:35840\n\t" \
  "v_mfma_f32_16x16x32_bf16 a[24:27], a[148:151], v[68:71], a[24:27]\n\t" \
  "v_mfma_f32_16x16x32_bf16 a[24:27], a[152:155], v[72:75], a[24:27]\n\t" \
  "s_waitcnt vmcnt(14)\n\t" \
  "buffer_store_dword v243, v254, %[ws], %[fw2] offen offset:32 sc1\n\t" \
  "v_mfma_f32_16x16x32_bf16 a[24:27], a[156:159], v[76:79], a[24:27]\n\t" \
  "ds_read_b128 a[76:79], v242 offset:36864\n\t" \
  "v_mfma_f32_16x16x32_bf16 a[104:107], a[172:175], v[76:79], 0\n\t" \
  "ds_read_b128 a[92:95], v242 offset:37888\n\t" \
  "v_mfma_f32_16x16x32_bf16 a[104:107], a[168:171], v[72:75], a[104:107]\n\t" \
  "ds_read_b128 a[108:111], v242 offset:38912\n\t" \
  "s_waitcnt lgkmcnt(7)\n\t" \
  "v_cmp_eq_u32 vcc, %[tag], v228\n\t" \
  "s_and_b32 vcc_lo, vcc_lo, %[pm]\n\t" \
  "s_cbranch_scc1 .Lop_rd\n\t" \
  "v_mfma_f32_16x16x32_bf16 a[104:107], a[164:167], v[68:71], a[104:107]\n\t" \
  "ds_read_b128 a[124:127], v242 offset:39936\n\t" \
  "v_mfma_f32_16x16x32_bf16 a[104:107], a[160:163], v[64:67], a[104:107]\n\t" \
  "ds_read_b128 a[208:211], v242 offset:40960\n\t" \
  "v_mfma_f32_16x16x32_bf16 a[120:123], a[176:179], v[64:67], 0\n\t" \
  "buffer_load_dword v246, v245, %[ws], %[fw2] offen sc1\n\t" \
  "v_mfma_f32_16x16x32_bf16 a[120:123], a[180:183], v[68:71], a[120:123]\n\t" \
  "ds_read_b128 a[212:215], v242 offset:41984\n\t" \
  "v_mfma_f32_16x16x32_bf16 a[120:123], a[184:187], v[72:75], a[120:123]\n\t" \
  "ds_read_b128 a[216:219], v242 offset:43008\n\t" \
  "v_mfma_f32_16x16x32_bf16 a[120:123], a[188:191], v[76:79], a[120:123]\n\t" \
  "s_xor_b32 s73, %[eso], 131072\n\t" \
  "ds_read_b128 a[220:223], v242 offset:44032\n\t" \
  "v_mfma_f32_16x16x32_bf16 a[88:91], v[108:111], v[76:79], 0\n\t" \
  "s_add_u32 m0, %[m], 0\n\t" \
  "ds_read_b128 a[192:195], v242 offset:45056\n\t" \
  "v_mfma_f32_16x16x32_bf16 a[88:91], v[104:107], v[72:75], a[88:91]\n\t" \
  "ds_read_b128 a[196:199], v242 offset:46080\n\t" \
  "v_mfma_f32_16x16x32_bf16 a[88:91], v[100:103], v[68:71], a[88:91]\n\t" \
  "ds_read_b128 a[200:203], v242 offset:47104\n\t" \
  "ds_read_b128 a[204:207], v242 offset:48128\n\t" \
  "v_mfma_f32_16x16x32_bf16 a[88:91], v[96:99], v[64:67], a[88:91]\n\t" \
  "s_waitcnt lgkmcnt(12)\n\t" \
  "s_mov_b64 exec, 0\n\t" \
  "s_cmp_eq_u32 %[wid], 0\n\t" \
  "s_cbranch_scc1 .Lhc10\n\t" \
  "s_cmp_eq_u32 %[wid], 1\n\t" \
  "s_cbranch_scc1 .Lhc11\n\t" \
  "s_cmp_eq_u32 %[wid], 2\n\t" \
  "s_cbranch_scc1 .Lhc12\n\t" \
  "s_branch .Lhc13\n\t" \
  ".Lhr1:\n\t" \
  "s_mov_b64 exec, -1\n\t" \
  "s_waitcnt vmcnt(6)\n\t" \
  "s_waitcnt vmcnt(0)\n\t" \
  "v_cmp_ne_u32 vcc, %[tag], v246\n\t" \
  "s_and_b32 vcc_lo, vcc_lo, 0x6\n\t" \
  "s_cbranch_scc1 .Lop_s0\n\t" \
  ".Lop_k0:\n\t" \
  "buffer_load_dwordx4 v[16:19], v242, %[ws], s73 offen offset:0 sc1\n\t" \
  "v_perm_b32 v248, v132, v128, s79\n\t" \
  "v_perm_b32 v128, v132, v128, s80\n\t" \
  "v_dot2_f32_bf16 v132, v248, v224, 0\n\t" \
  "v_dot2_f32_bf16 v128, v128, v224, 0\n\t" \
  "v_perm_b32 v249, v133, v129, s79\n\t" \
  "v_perm_b32 v129, v133, v129, s80\n\t" \
  "v_dot2_f32_bf16 v133, v249, v224, 0\n\t" \
  "v_dot2_f32_bf16 v129, v129, v224, 0\n\t" \
  "buffer_load_dwordx4 v[20:23], v242, %[ws], s73 offen offset:1024 sc1\n\t" \
  "v_perm_b32 v248, v134, v130, s79\n\t" \
  "v_perm_b32 v130, v134, v130, s80\n\t" \
  "v_dot2_f32_bf16 v134, v248, v224, 0\n\t" \
  "v_dot2_f32_bf16 v130, v130, v224, 0\n\t" \
  "v_perm_b32 v249, v135, v131, s79\n\t" \
  "v_perm_b32 v131, v135, v131, s80\n\t" \
  "v_dot2_f32_bf16 v135, v249, v224, 0\n\t" \
  "v_dot2_f32_bf16 v131, v131, v224, 0\n\t" \
  "buffer_load_dwordx4 v[24:27], v242, %[ws], s73 offen offset:2048 sc1\n\t" \
  "v_perm_b32 v248, v140, v136, s79\n\t" \
  "v_perm_b32 v136, v140, v136, s80\n\t" \
  "v_dot2_f32_bf16 v132, v248, v225, v132\n\t" \
  "v_dot2_f32_bf16 v128, v136, v225, v128\n\t" \
  "v_perm_b32 v249, v141, v137, s79\n\t" \
  "v_perm_b32 v137, v141, v137, s80\n\t" \
  "v_dot2_f32_bf16 v133, v249, v225, v133\n\t" \
  "v_dot2_f32_bf16 v129, v137, v225, v129\n\t" \
  "buffer_load_dwordx4 v[28:31], v242, %[ws], s73 offen offset:3072 sc1\n\t" \
  "v_perm_b32 v248, v142, v138, s79\n\t" \
  "v_perm_b32 v138, v142, v138, s80\n\t" \
  "v_dot2_f32_bf16 v134, v248, v225, v134\n\t" \
  "v_dot2_f32_bf16 v130, v138, v225, v130\n\t" \
  "v_perm_b32 v249, v143, v139, s79\n\t" \
  "v_perm_b32 v139, v143, v139, s80\n\t" \
  "v_dot2_f32_bf16 v135, v249, v225, v135\n\t" \
  "v_dot2_f32_bf16 v131, v139, v225, v131\n\t" \
  "s_xor_b32 s73, %[eso], 163840\n\t" \
  "buffer_load_dwordx4 v[0:3], v242, %[ws], s73 offen offset:0 sc1\n\t" \
  "v_perm_b32 v248, v148, v144, s79\n\t" \
  "v_perm_b32 v144, v148, v144, s80\n\t" \
  "v_dot2_f32_bf16 v132, v248, v226, v132\n\t" \
  "v_dot2_f32_bf16 v128, v144, v226, v128\n\t" \
  "v_perm_b32 v249, v149, v145, s79\n\t" \
  "v_perm_b32 v145, v149, v145, s80\n\t" \
  "v_dot2_f32_bf16 v133, v249, v226, v133\n\t" \
  "v_dot2_f32_bf16 v129, v145, v226, v129\n\t" \
  "buffer_load_dwordx4 v[4:7], v242, %[ws], s73 offen offset:1024 sc1\n\t" \
  "v_perm_b32 v248, v150, v146, s79\n\t" \
  "v_perm_b32 v146, v150, v146, s80\n\t" \
  "v_dot2_f32_bf16 v134, v248, v226, v134\n\t" \
  "v_dot2_f32_bf16 v130, v146, v226, v130\n\t" \
  "v_perm_b32 v249, v151, v147, s79\n\t" \
  "v_perm_b32 v147, v151, v147, s80\n\t" \
  "v_dot2_f32_bf16 v135, v249, v226, v135\n\t" \
  "v_dot2_f32_bf16 v131, v147, v226, v131\n\t" \
  "buffer_load_dwordx4 v[8:11], v242, %[ws], s73 offen offset:2048 sc1\n\t" \
  "v_perm_b32 v248, v156, v152, s79\n\t" \
  "v_perm_b32 v152, v156, v152, s80\n\t" \
  "v_dot2_f32_bf16 v132, v248, v227, v132\n\t" \
  "v_dot2_f32_bf16 v128, v152, v227, v128\n\t" \
  "v_perm_b32 v249, v157, v153, s79\n\t" \
  "v_perm_b32 v153, v157, v153, s80\n\t" \
  "v_dot2_f32_bf16 v133, v249, v227, v133\n\t" \
  "v_dot2_f32_bf16 v129, v153, v227, v129\n\t" \
  "buffer_load_dwordx4 v[12:15], v242, %[ws], s73 offen offset:3072 sc1\n\t" \
  "v_perm_b32 v248, v158, v154, s79\n\t" \
  "v_perm_b32 v154, v158, v154, s80\n\t" \
  "v_dot2_f32_bf16 v134, v248, v227, v134\n\t" \
  "v_dot2_f32_bf16 v130, v154, v227, v130\n\t" \
  "v_perm_b32 v249, v159, v155, s79\n\t" \
  "v_perm_b32 v155, v159, v155, s80\n\t" \
  "v_dot2_f32_bf16 v135, v249, v227, v135\n\t" \
  "v_dot2_f32_bf16 v131, v155, v227, v131\n\t" \
  "v_mfma_f32_16x16x32_bf16 v[96:99], a[128:131], a[12:15], 0\n\t" \
  "s_xor_b32 s73, %[eso], 262144\n\t" \
  "s_xor_b32 s71, %[wob], 1048576\n\t" \
  "v_mfma_f32_16x16x32_bf16 v[96:99], a[132:135], a[28:31], v[96:99]\n\t" \
  "v_mfma_f32_16x16x32_bf16 v[96:99], a[136:139], a[44:47], v[96:99]\n\t" \
  "v_mfma_f32_16x16x32_bf16 v[96:99], a[140:143], a[60:63], v[96:99]\n\t" \
  "v_mfma_f32_16x16x32_bf16 v[112:115], a[156:159], a[60:63], 0\n\t" \
  "buffer_load_dwordx4 v[48:51], v242, %[ws], s73 offen offset:0 sc1\n\t" \
  "v_mfma_f32_16x16x32_bf16 v[112:115], a[152:155], a[44:47], v[112:115]\n\t" \
  "v_mfma_f32_16x16x32_bf16 v[112:115], a[148:151], a[28:31], v[112:115]\n\t" \
  "v_mfma_f32_16x16x32_bf16 v[112:115], a[144:147], a[12:15], v[112:115]\n\t" \
  "s_waitcnt lgkmcnt(8)\n\t" \
  "v_mfma_f32_16x16x32_bf16 v[100:103], a[128:131], a[76:79], 0\n\t" \
  "buffer_load_dwordx4 v[52:55], v242, %[ws], s73 offen offset:1024 sc1\n\t" \
  "v_mfma_f32_16x16x32_bf16 v[100:103], a[132:135], a[92:95], v[100:103]\n\t" \
  "v_mfma_f32_16x16x32_bf16 v[100:103], a[136:139], a[108:111], v[100:103]\n\t" \
  "v_mfma_f32_16x16x32_bf16 v[100:103], a[140:143], a[124:127], v[100:103]\n\t" \
  "v_mfma_f32_16x16x32_bf16 v[116:119], a[156:159], a[124:127], 0\n\t" \
  "v_mfma_f32_16x16x32_bf16 v[116:119], a[152:155], a[108:111], v[116:119]\n\t" \
  "buffer_load_dwordx4 v[56:59], v242, %[ws], s73 offen offset:2048 sc1\n\t" \
  "v_mfma_f32_16x16x32_bf16 v[116:119], a[148:151], a[92:95], v[116:119]\n\t" \
  "v_mfma_f32_16x16x32_bf16 v[116:119], a[144:147], a[76:79], v[116:119]\n\t" \
  "s_waitcnt lgkmcnt(4)\n\t" \
  "v_mfma_f32_16x16x32_bf16 v[104:107], a[128:131], a[208:211], 0\n\t" \
  "v_xor_b32 v236, 0xc000, v234\n\t" \
  "v_mfma_f32_16x16x32_bf16 v[104:107], a[132:135], a[212:215], v[104:107]\n\t" \
  "buffer_load_dwordx4 v[60:63], v242, %[ws], s73 offen offset:3072 sc1\n\t" \
  "s_xor_b32 s73, %[eso], 294912\n\t" \
  "v_mfma_f32_16x16x32_bf16 v[104:107], a[136:139], a[216:219], v[104:107]\n\t" \
  "ds_read_b64_tr_b16 a[160:161], v236 offset:0\n\t" \
  "v_mfma_f32_16x16x32_bf16 v[104:107], a[140:143], a[220:223], v[104:107]\n\t" \
  "ds_read_b64_tr_b16 a[162:163], v236 offset:4096\n\t" \
  "v_mfma_f32_16x16x32_bf16 v[120:123], a[156:159], a[220:223], 0\n\t" \
  "ds_read_b64_tr_b16 a[164:165], v236 offset:8192\n\t" \
  "v_mfma_f32_16x16x32_bf16 v[120:123], a[152:155], a[216:219], v[120:123]\n\t" \
  "ds_read_b64_tr_b16 a[166:167], v236 offset:12288\n\t" \
  "v_mfma_f32_16x16x32_bf16 v[120:123], a[148:151], a[212:215], v[120:123]\n\t" \
  "buffer_load_dwordx4 v[80:83], v242, %[ws], s73 offen offset:0 sc1\n\t" \
  "v_mfma_f32_16x16x32_bf16 v[120:123], a[144:147], a[208:211], v[120:123]\n\t" \
  "s_waitcnt lgkmcnt(4)\n\t" \
  "ds_read_b64_tr_b16 a[168:169], v236 offset:16384\n\t" \
  "v_mfma_f32_16x16x32_bf16 v[108:111], a[128:131], a[192:195], 0\n\t" \
  "ds_read_b64_tr_b16 a[170:171], v236 offset:20480\n\t" \
  "v_mfma_f32_16x16x32_bf16 v[108:111], a[132:135], a[196:199], v[108:111]\n\t" \
  "ds_read_b64_tr_b16 a[172:173], v236 offset:24576\n\t" \
  "v_mfma_f32_16x16x32_bf16 v[108:111], a[136:139], a[200:203], v[108:111]\n\t" \
  "buffer_load_dwordx4 v[84:87], v242, %[ws], s73 offen offset:1024 sc1\n\t" \
  "v_mfma_f32_16x16x32_bf16 v[108:111], a[140:143], a[204:207], v[108:111]\n\t" \
  "ds_read_b64_tr_b16 a[174:175], v236 offset:28672\n\t" \
  "v_xor_b32 v237, 0xc020, v234\n\t" \
  "v_mfma_f32_16x16x32_bf16 v[124:127], a[156:159], a[204:207], 0\n\t" \
  "ds_read_b64_tr_b16 a[176:177], v237 offset:0\n\t" \
  "v_mfma_f32_16x16x32_bf16 v[124:127], a[152:155], a[200:203], v[124:127]\n\t" \
  "ds_read_b64_tr_b16 a[178:179], v237 offset:4096\n\t" \
  "v_mfma_f32_16x16x32_bf16 v[124:127], a[148:151], a[196:199], v[124:127]\n\t" \
  "buffer_load_dwordx4 v[88:91], v242, %[ws], s73 offen offset:2048 sc1\n\t" \
  "v_mfma_f32_16x16x32_bf16 v[124:127], a[144:147], a[192:195], v[124:127]\n\t" \
  "s_waitcnt vmcnt(11) lgkmcnt(6)\n\t" \
  "ds_read_b64_tr_b16 a[180:181], v237 offset:8192\n\t" \
  "ds_read_b64_tr_b16 a[182:183], v237 offset:12288\n\t" \
  "buffer_load_dword v246, v245, %[ws], %[fw2] offen sc1\n\t" \
  STP("v_writelane_b32 %[stv], s94, 24\n\t") \
  STP("v_writelane_b32 %[stv], s92, 25\n\t") \
  STP("s_memrealtime s[94:95]\n\t") \
  STP("s_memtime s[92:93]\n\t") \
  "v_mfma_f32_16x16x32_bf16 a[0:3], a[160:163], v[16:19], a[0:3]\n\t" \
  "ds_read_b64_tr_b16 a[184:185], v237 offset:16384\n\t" \
  "v_mfma_f32_16x16x32_bf16 a[0:3], a[164:167], v[20:23], a[0:3]\n\t" \
  "s_waitcnt lgkmcnt(5)\n\t" \
  "ds_read_b64_tr_b16 a[186:187], v237 offset:20480\n\t" \
  "v_mfma_f32_16x16x32_bf16 a[0:3], a[168:171], v[24:27], a[0:3]\n\t" \
  "ds_read_b64_tr_b16 a[188:189], v237 offset:24576\n\t" \
  "v_mfma_f32_16x16x32_bf16 a[0:3], a[172:175], v[28:31], a[0:3]\n\t" \
  "s_waitcnt vmcnt(8)\n\t" \
  "ds_read_b64_tr_b16 a[190:191], v237 offset:28672\n\t" \
  "v_xor_b32 v235, 0xc040, v234\n\t" \
  "v_mfma_f32_16x16x32_bf16 a[4:7], a[172:175], v[12:15], a[4:7]\n\t" \
  "buffer_load_dwordx4 v[92:95], v242, %[ws], s73 offen offset:3072 sc1\n\t" \
  "v_mfma_f32_16x16x32_bf16 a[4:7], a[168:171], v[8:11], a[4:7]\n\t" \
  "ds_read_b64_tr_b16 a[128:129], v235 offset:0\n\t" \
  "ds_read_b64_tr_b16 a[130:131], v235 offset:4096\n\t" \
  "v_mfma_f32_16x16x32_bf16 a[4:7], a[164:167], v[4:7], a[4:7]\n\t" \
  "ds_read_b64_tr_b16 a[132:133], v235 offset:8192\n\t" \
  "v_mfma_f32_16x16x32_bf16 a[4:7], a[160:163], v[0:3], a[4:7]\n\t" \
  "s_waitcnt lgkmcnt(7)\n\t" \
  "ds_read_b64_tr_b16 a[134:135], v235 offset:12288\n\t" \
  "v_mfma_f32_16x16x32_bf16 a[16:19], a[176:179], v[16:19], a[16:19]\n\t" \
  "buffer_load_dwordx4 v240, %[wo], s71 offen offset:0 lds\n\t" \
  "v_mfma_f32_16x16x32_bf16 a[16:19], a[180:183], v[20:23], a[16:19]\n\t" \
  "s_waitcnt lgkmcnt(4)\n\t" \
  "ds_read_b64_tr_b16 a[136:137], v235 offset:16384\n\t" \
  "ds_read_b64_tr_b16 a[138:139], v235 offset:20480\n\t" \
  "v_mfma_f32_16x16x32_bf16 a[16:19], a[184:187], v[24:27], a[16:19]\n\t" \
  "ds_read_b64_tr_b16 a[140:141], v235 offset:24576\n\t" \
  "v_mfma_f32_16x16x32_bf16 a[16:19], a[188:191], v[28:31], a[16:19]\n\t" \
  "ds_read_b64_tr_b16 a[142:143], v235 offset:28672\n\t" \
  "v_xor_b32 v236, 0xc060, v234\n\t" \
  "v_mfma_f32_16x16x32_bf16 a[20:23], a[188:191], v[12:15], a[20:23]\n\t" \
  "ds_read_b64_tr_b16 a[160:161], v236 offset:0\n\t" \
  "v_mfma_f32_16x16x32_bf16 a[20:23], a[184:187], v[8:11], a[20:23]\n\t" \
  "buffer_load_dwordx4 v241, %[wo], s71 offen offset:1024 lds\n\t" \
  "v_mfma_f32_16x16x32_bf16 a[20:23], a[180:183], v[4:7], a[20:23]\n\t" \
  "ds_read_b64_tr_b16 a[162:163], v236 offset:4096\n\t" \
  "ds_read_b64_tr_b16 a[164:165], v236 offset:8192\n\t" \
  "v_mfma_f32_16x16x32_bf16 a[20:23], a[176:179], v[0:3], a[20:23]\n\t" \
  "s_waitcnt lgkmcnt(7)\n\t" \
  "ds_read_b64_tr_b16 a[166:167], v236 offset:12288\n\t" \
  "v_mfma_f32_16x16x32_bf16 a[32:35], a[128:131], v[16:19], a[32:35]\n\t" \
  "s_xor_b32 s73, %[eso], 327680\n\t" \
  "ds_read_b64_tr_b16 a[168:169], v236 offset:16384\n\t" \
  "v_mfma_f32_16x16x32_bf16 a[32:35], a[132:135], v[20:23], a[32:35]\n\t" \
  "s_waitcnt lgkmcnt(5)\n\t" \
  "buffer_load_dwordx4 v238, %[wo], s71 offen offset:2048 lds\n\t" \
  "v_mfma_f32_16x16x32_bf16 a[32:35], a[136:139], v[24:27], a[32:35]\n\t" \
  "ds_read_b64_tr_b16 a[170:171], v236 offset:20480\n\t" \
  "ds_read_b64_tr_b16 a[172:173], v236 offset:24576\n\t" \
  "v_mfma_f32_16x16x32_bf16 a[32:35], a[140:143], v[28:31], a[32:35]\n\t" \
  "ds_read_b64_tr_b16 a[174:175], v236 offset:28672\n\t" \
  "v_xor_b32 v237, 0xc080, v234\n\t" \
  "v_mfma_f32_16x16x32_bf16 a[36:39], a[140:143], v[12:15], a[36:39]\n\t" \
  "ds_read_b64_tr_b16 a[176:177], v237 offset:0\n\t" \
  "v_mfma_f32_16x16x32_bf16 a[36:39], a[136:139], v[8:11], a[36:39]\n\t" \
  "ds_read_b64_tr_b16 a[178:179], v237 offset:4096\n\t" \
  "v_mfma_f32_16x16x32_bf16 a[36:39], a[132:135], v[4:7], a[36:39]\n\t" \
  "buffer_load_dwordx4 v239, %[wo], s71 offen offset:3072 lds\n\t" \
  "s_add_u32 m0, %[m], 16384\n\t" \
  "v_mfma_f32_16x16x32_bf16 a[36:39], a[128:131], v[0:3], a[36:39]\n\t" \
  "s_waitcnt lgkmcnt(6)\n\t" \
  "s_xor_b32 s71, %[wob], 1179648\n\t" \
  "ds_read_b64_tr_b16 a[180:181], v237 offset:8192\n\t" \
  "v_mfma_f32_16x16x32_bf16 a[48:51], a[160:163], v[16:19], a[48:51]\n\t" \
  "ds_read_b64_tr_b16 a[182:183], v237 offset:12288\n\t" \
  "ds_read_b64_tr_b16 a[184:185], v237 offset:16384\n\t" \
  "v_mfma_f32_16x16x32_bf16 a[48:51], a[164:167], v[20:23], a[48:51]\n\t" \
  "s_waitcnt lgkmcnt(5)\n\t" \
  "ds_read_b64_tr_b16 a[186:187], v237 offset:20480\n\t" \
  "s_waitcnt vmcnt(5)\n\t" \
  "v_cmp_ne_u32 vcc, %[tag], v246\n\t" \
  "s_and_b32 vcc_lo, vcc_lo, 0xc00\n\t" \
  "s_cbranch_scc1 .Lop_s1\n\t" \
  ".Lop_k1:\n\t" \
  "v_mfma_f32_16x16x32_bf16 a[48:51], a[168:171], v[24:27], a[48:51]\n\t" \
  "buffer_load_dwordx4 v[64:67], v242, %[ws], s73 offen offset:0 sc1\n\t" \
  "v_mfma_f32_16x16x32_bf16 a[48:51], a[172:175], v[28:31], a[48:51]\n\t" \
  "ds_read_b64_tr_b16 a[188:189], v237 offset:24576\n\t" \
  "ds_read_b64_tr_b16 a[190:191], v237 offset:28672\n\t" \
  "v_mfma_f32_16x16x32_bf16 a[52:55], a[172:175], v[12:15], a[52:55]\n\t" \
  "v_xor_b32 v235, 0xc0a0, v234\n\t" \
  "v_mfma_f32_16x16x32_bf16 a[52:55], a[168:171], v[8:11], a[52:55]\n\t" \
  "ds_read_b64_tr_b16 a[144:145], v235 offset:0\n\t" \
  "ds_read_b64_tr_b16 a[146:147], v235 offset:4096\n\t" \
  "v_mfma_f32_16x16x32_bf16 a[52:55], a[164:167], v[4:7], a[52:55]\n\t" \
  "buffer_load_dwordx4 v[68:71], v242, %[ws], s73 offen offset:1024 sc1\n\t" \
  "v_mfma_f32_16x16x32_bf16 a[52:55], a[160:163], v[0:3], a[52:55]\n\t" \
  "s_waitcnt lgkmcnt(6)\n\t" \
  "ds_read_b64_tr_b16 a[148:149], v235 offset:8192\n\t" \
  "ds_read_b64_tr_b16 a[150:151], v235 offset:12288\n\t" \
  "v_mfma_f32_16x16x32_bf16 a[64:67], a[176:179], v[16:19], a[64:67]\n\t" \
  "ds_read_b64_tr_b16 a[152:153], v235 offset:16384\n\t" \
  "v_mfma_f32_16x16x32_bf16 a[64:67], a[180:183], v[20:23], a[64:67]\n\t" \
  "s_waitcnt lgkmcnt(5)\n\t" \
  "ds_read_b64_tr_b16 a[154:155], v235 offset:20480\n\t" \
  "v_mfma_f32_16x16x32_bf16 a[64:67], a[184:187], v[24:27], a[64:67]\n\t" \
  "ds_read_b64_tr_b16 a[156:157], v235 offset:24576\n\t" \
  "v_mfma_f32_16x16x32_bf16 a[64:67], a[188:191], v[28:31], a[64:67]\n\t" \
  "buffer_load_dwordx4 v[72:75], v242, %[ws], s73 offen offset:2048 sc1\n\t" \
  "v_mfma_f32_16x16x32_bf16 a[68:71], a[188:191], v[12:15], a[68:71]\n\t" \
  "ds_read_b64_tr_b16 a[158:159], v235 offset:28672\n\t" \
  "v_xor_b32 v236, 0xc0c0, v234\n\t" \
  "v_mfma_f32_16x16x32_bf16 a[68:71], a[184:187], v[8:11], a[68:71]\n\t" \
  "ds_read_b64_tr_b16 a[128:129], v236 offset:0\n\t" \
  "ds_read_b64_tr_b16 a[130:131], v236 offset:4096\n\t" \
  "v_mfma_f32_16x16x32_bf16 a[68:71], a[180:183], v[4:7], a[68:71]\n\t" \
  "ds_read_b64_tr_b16 a[132:133], v236 offset:8192\n\t" \
  "v_mfma_f32_16x16x32_bf16 a[68:71], a[176:179], v[0:3], a[68:71]\n\t" \
  "s_waitcnt lgkmcnt(7)\n\t" \
  "s_barrier\n\t" \
  "v_mfma_f32_16x16x32_bf16 a[80:83], a[144:147], v[16:19], a[80:83]\n\t" \
  "buffer_load_dwordx4 v[76:79], v242, %[ws], s73 offen offset:3072 sc1\n\t" \
  "v_mfma_f32_16x16x32_bf16 a[80:83], a[148:151], v[20:23], a[80:83]\n\t" \
  "s_waitcnt lgkmcnt(3)\n\t" \
  "ds_read_b64_tr_b16 a[134:135], v236 offset:12288\n\t" \
  ".Lhs11:\n\ts_cbranch_execz .Lhs11n\n\t" \
  "ds_read_b64_tr_b16 a[136:137], v236 offset:16384\n\t" \
  "v_mfma_f32_16x16x32_bf16 a[80:83], a[152:155], v[24:27], a[80:83]\n\t" \
  "ds_read_b64_tr_b16 a[138:139], v236 offset:20480\n\t" \
  "ds_read_b64_tr_b16 a[140:141], v236 offset:24576\n\t" \
  "v_mfma_f32_16x16x32_bf16 a[80:83], a[156:159], v[28:31], a[80:83]\n\t" \
  "ds_read_b64_tr_b16 a[142:143], v236 offset:28672\n\t" \
  "v_xor_b32 v237, 0xc0e0, v234\n\t" \
  "v_mfma_f32_16x16x32_bf16 a[84:87], a[156:159], v[12:15], a[84:87]\n\t" \
  "buffer_load_dwordx4 v240, %[wo], s71 offen offset:0 lds\n\t" \
  "v_mfma_f32_16x16x32_bf16 a[84:87], a[152:155], v[8:11], a[84:87]\n\t" \
  "ds_read_b64_tr_b16 a[12:13], v237 offset:0\n\t" \
  "ds_read_b64_tr_b16 a[14:15], v237 offset:4096\n\t" \
  "v_mfma_f32_16x16x32_bf16 a[84:87], a[148:151], v[4:7], a[84:87]\n\t" \
  "ds_read_b64_tr_b16 a[28:29], v237 offset:8192\n\t" \
  "v_mfma_f32_16x16x32_bf16 a[84:87], a[144:147], v[0:3], a[84:87]\n\t" \
  "s_waitcnt lgkmcnt(7)\n\t" \
  "ds_read_b64_tr_b16 a[30:31], v237 offset:12288\n\t" \
  "v_mfma_f32_16x16x32_bf16 a[96:99], a[128:131], v[16:19], a[96:99]\n\t" \
  "buffer_load_dwordx4 v241, %[wo], s71 offen offset:1024 lds\n\t" \
  "v_mfma_f32_16x16x32_bf16 a[96:99], a[132:135], v[20:23], a[96:99]\n\t" \
  "s_waitcnt lgkmcnt(4)\n\t" \
  "ds_read_b64_tr_b16 a[44:45], v237 offset:16384\n\t" \
  "ds_read_b64_tr_b16 a[46:47], v237 offset:20480\n\t" \
  "v_mfma_f32_16x16x32_bf16 a[96:99], a[136:139], v[24:27], a[96:99]\n\t" \
  "ds_read_b64_tr_b16 a[60:61], v237 offset:24576\n\t" \
  "v_lshrrev_b32 v244, 4, v242\n\t" \
  "v_mfma_f32_16x16x32_bf16 a[96:99], a[140:143], v[28:31], a[96:99]\n\t" \
  "s_lshl_b32 s77, %[hl], 2\n\t" \
  "ds_read_b64_tr_b16 a[62:63], v237 offset:28672\n\t" \
  "v_xor_b32 v235, 0x14000, v234\n\t" \
  "v_mfma_f32_16x16x32_bf16 a[100:103], a[140:143], v[12:15], a[100:103]\n\t" \
  "buffer_load_dwordx4 v238, %[wo], s71 offen offset:2048 lds\n\t" \
  "v_mfma_f32_16x16x32_bf16 a[100:103], a[136:139], v[8:11], a[100:103]\n\t" \
  "s_add_u32 s77, s77, %[wid]\n\t" \
  "ds_read_b64_tr_b16 a[160:161], v235 offset:0\n\t" \
  "ds_read_b64_tr_b16 a[162:163], v235 offset:4096\n\t" \
  "v_mfma_f32_16x16x32_bf16 a[100:103], a[132:135], v[4:7], a[100:103]\n\t" \
  "s_lshl_b32 s77, s77, 6\n\t" \
  "ds_read_b64_tr_b16 a[164:165], v235 offset:8192\n\t" \
  "v_add_u32 v230, 0x10000, v242\n\t" \
  "v_mfma_f32_16x16x32_bf16 a[100:103], a[128:131], v[0:3], a[100:103]\n\t" \
  "s_waitcnt lgkmcnt(7)\n\t" \
  "ds_read_b64_tr_b16 a[166:167], v235 offset:12288\n\t" \
  "v_xor_b32 v236, 0x24000, v234\n\t" \
  "v_mfma_f32_16x16x32_bf16 a[112:115], a[12:15], v[16:19], a[112:115]\n\t" \
  "ds_read_b64_tr_b16 a[168:169], v236 offset:0\n\t" \
  "v_add_u32 v232, 0x20000, v242\n\t" \
  "v_mfma_f32_16x16x32_bf16 a[112:115], a[28:31], v[20:23], a[112:115]\n\t" \
  "s_waitcnt lgkmcnt(5)\n\t" \
  "buffer_load_dwordx4 v239, %[wo], s71 offen offset:3072 lds\n\t" \
  "s_add_u32 m0, %[m], 98304\n\t" \
  "v_mfma_f32_16x16x32_bf16 a[112:115], a[44:47], v[24:27], a[112:115]\n\t" \
  "s_xor_b32 s71, %[wob], 1310720\n\t" \
  "ds_read_b64_tr_b16 a[170:171], v236 offset:4096\n\t" \
  "ds_read_b64_tr_b16 a[172:173], v236 offset:8192\n\t" \
  "v_mfma_f32_16x16x32_bf16 a[112:115], a[60:63], v[28:31], a[112:115]\n\t" \
  "s_xor_b32 s73, %[eso], 393216\n\t" \
  "ds_read_b64_tr_b16 a[174:175], v236 offset:12288\n\t" \
  "s_add_u32 s70, %[m], 147456\n\t" \
  "v_mfma_f32_16x16x32_bf16 a[116:119], a[60:63], v[12:15], a[116:119]\n\t" \
  "s_lshl_b32 s69, s77, 2\n\t" \
  "v_xor_b32 v237, 0x14020, v234\n\t" \
  "s_add_u32 s69, s69, 2228224\n\t" \
  "v_mfma_f32_16x16x32_bf16 a[116:119], a[44:47], v[8:11], a[116:119]\n\t" \
  "buffer_load_dwordx4 v240, %[wo], s71 offen offset:0 lds\n\t" \
  "v_mfma_f32_16x16x32_bf16 a[116:119], a[28:31], v[4:7], a[116:119]\n\t" \
  "s_bfe_u32 s68, %[eso], 0x2000f\n\t" \
  "ds_read_b64_tr_b16 a[176:177], v237 offset:0\n\t" \
  "v_mfma_f32_16x16x32_bf16 a[116:119], a[12:15], v[0:3], a[116:119]\n\t" \
  "s_waitcnt lgkmcnt(5)\n\t" \
  "ds_read_b64_tr_b16 a[178:179], v237 offset:4096\n\t" \
  "s_lshl_b32 s60, s68, 17\n\t" \
  STP("v_writelane_b32 %[stv], s94, 26\n\t") \
  STP("v_writelane_b32 %[stv], s92, 27\n\t") \
  STP("s_memrealtime s[94:95]\n\t") \
  STP("s_memtime s[92:93]\n\t") \
  "v_mfma_f32_16x16x32_bf16 a[0:3], a[160:163], v[48:51], a[0:3]\n\t" \
  "ds_read_b64_tr_b16 a[180:181], v237 offset:8192\n\t" \
  "v_mfma_f32_16x16x32_bf16 a[0:3], a[164:167], v[52:55], a[0:3]\n\t" \
  "s_waitcnt lgkmcnt(3)\n\t" \
  "ds_read_b64_tr_b16 a[182:183], v237 offset:12288\n\t" \
  "v_xor_b32 v235, 0x24020, v234\n\t" \
  "v_mfma_f32_16x16x32_bf16 a[0:3], a[168:171], v[56:59], a[0:3]\n\t" \
  "buffer_load_dwordx4 v[16:19], v242, %[ws], s73 offen offset:0 sc1\n\t" \
  "v_mfma_f32_16x16x32_bf16 a[0:3], a[172:175], v[60:63], a[0:3]\n\t" \
  "s_waitcnt vmcnt(14)\n\t" \
  "ds_read_b64_tr_b16 a[184:185], v235 offset:0\n\t" \
  "s_add_u32 s60, s60, %[xcb]\n\t" \
  "v_mfma_f32_16x16x32_bf16 a[4:7], a[172:175], v[92:95], a[4:7]\n\t" \
  "s_lshl_b32 s63, %[wid], 10\n\t" \
  "ds_read_b64_tr_b16 a[186:187], v235 offset:4096\n\t" \
  "v_and_b32 v250, 7, v244\n\t" \
  "v_mfma_f32_16x16x32_bf16 a[4:7], a[168:171], v[88:91], a[4:7]\n\t" \
  "s_add_u32 s60, s60, s63\n\t" \
  "ds_read_b64_tr_b16 a[188:189], v235 offset:8192\n\t" \
  "v_lshlrev_b32 v250, 5, v250\n\t" \
  "v_mfma_f32_16x16x32_bf16 a[4:7], a[164:167], v[84:87], a[4:7]\n\t" \
  "buffer_load_dwordx4 v[20:23], v242, %[ws], s73 offen offset:1024 sc1\n\t" \
  "v_mfma_f32_16x16x32_bf16 a[4:7], a[160:163], v[80:83], a[4:7]\n\t" \
  "s_waitcnt vmcnt(7)\n\t" \
  "ds_read_b64_tr_b16 a[190:191], v235 offset:12288\n\t" \
  "v_lshrrev_b32 v251, 4, v244\n\t" \
  "v_mfma_f32_16x16x32_bf16 a[8:11], a[160:163], v[64:67], a[8:11]\n\t" \
  "s_xor_b32 s61, s60, 135168\n\t" \
  "v_xor_b32 v236, 0x14040, v234\n\t" \
  "v_lshl_add_u32 v250, v251, 3, v250\n\t" \
  "v_mfma_f32_16x16x32_bf16 a[8:11], a[164:167], v[68:71], a[8:11]\n\t" \
  "ds_read_b64_tr_b16 a[144:145], v236 offset:0\n\t" \
  "v_mfma_f32_16x16x32_bf16 a[8:11], a[168:171], v[72:75], a[8:11]\n\t" \
  "buffer_load_dwordx4 v[24:27], v242, %[ws], s73 offen offset:2048 sc1\n\t" \
  "v_mfma_f32_16x16x32_bf16 a[8:11], a[172:175], v[76:79], a[8:11]\n\t" \
  "s_waitcnt lgkmcnt(5)\n\t" \
  "ds_read_b64_tr_b16 a[146:147], v236 offset:4096\n\t" \
  "s_xor_b32 s62, s60, 278528\n\t" \
  "v_mfma_f32_16x16x32_bf16 a[16:19], a[176:179], v[48:51], a[16:19]\n\t" \
  "s_lshl_b32 s65, s68, 7\n\t" \
  "ds_read_b64_tr_b16 a[148:149], v236 offset:8192\n\t" \
  "v_mfma_f32_16x16x32_bf16 a[16:19], a[180:183], v[52:55], a[16:19]\n\t" \
  "s_waitcnt lgkmcnt(3)\n\t" \
  "ds_read_b64_tr_b16 a[150:151], v236 offset:12288\n\t" \
  "v_xor_b32 v237, 0x24040, v234\n\t" \
  "v_mfma_f32_16x16x32_bf16 a[16:19], a[184:187], v[56:59], a[16:19]\n\t" \
  "s_add_u32 s65, s65, %[frb]\n\t" \
  "ds_read_b64_tr_b16 a[152:153], v237 offset:0\n\t" \
  "v_mfma_f32_16x16x32_bf16 a[16:19], a[188:191], v[60:63], a[16:19]\n\t" \
  "buffer_load_dwordx4 v[28:31], v242, %[ws], s73 offen offset:3072 sc1\n\t" \
  "s_xor_b32 s73, %[eso], 425984\n\t" \
  "v_mfma_f32_16x16x32_bf16 a[20:23], a[188:191], v[92:95], a[20:23]\n\t" \
  "s_xor_b32 s66, s65, 144\n\t" \
  "ds_read_b64_tr_b16 a[154:155], v237 offset:4096\n\t" \
  "v_mfma_f32_16x16x32_bf16 a[20:23], a[184:187], v[88:91], a[20:23]\n\t" \
  "ds_read_b64_tr_b16 a[156:157], v237 offset:8192\n\t" \
  "v_mfma_f32_16x16x32_bf16 a[20:23], a[180:183], v[84:87], a[20:23]\n\t" \
  "ds_read_b64_tr_b16 a[158:159], v237 offset:12288\n\t" \
  "v_mfma_f32_16x16x32_bf16 a[20:23], a[176:179], v[80:83], a[20:23]\n\t" \
  "buffer_load_dwordx4 v[0:3], v242, %[ws], s73 offen offset:0 sc1\n\t" \
  "v_mfma_f32_16x16x32_bf16 a[24:27], a[176:179], v[64:67], a[24:27]\n\t" \
  "s_xor_b32 s67, s65, 288\n\t" \
  "v_xor_b32 v235, 0x14060, v234\n\t" \
  "v_mfma_f32_16x16x32_bf16 a[24:27], a[180:183], v[68:71], a[24:27]\n\t" \
  "s_xor_b32 s68, s65, 384\n\t" \
  "ds_read_b64_tr_b16 a[128:129], v235 offset:0\n\t" \
  "v_mfma_f32_16x16x32_bf16 a[24:27], a[184:187], v[72:75], a[24:27]\n\t" \
  "s_lshr_b32 s75, %[m], 4\n\t" \
  "s_bitset1_b32 s75, 17\n\t" \
  "v_add_u32 v250, s75, v250\n\t" \
  "v_mfma_f32_16x16x32_bf16 a[24:27], a[188:191], v[76:79], a[24:27]\n\t" \
  "s_waitcnt lgkmcnt(5)\n\t" \
  "ds_read_b64_tr_b16 a[130:131], v235 offset:4096\n\t" \
  "s_andn2_b32 s68, s68, 12\n\t" \
  "v_mfma_f32_16x16x32_bf16 a[32:35], a[144:147], v[48:51], a[32:35]\n\t" \
  "buffer_load_dwordx4 v[4:7], v242, %[ws], s73 offen offset:1024 sc1\n\t" \
  "v_mfma_f32_16x16x32_bf16 a[32:35], a[148:151], v[52:55], a[32:35]\n\t" \
  "s_waitcnt lgkmcnt(2)\n\t" \
  "ds_read_b64_tr_b16 a[132:133], v235 offset:8192\n\t" \
  "s_andn2_b32 s64, 0x2000, %[na]\n\t" \
  "v_mfma_f32_16x16x32_bf16 a[32:35], a[152:155], v[56:59], a[32:35]\n\t" \
  "v_permlane32_swap_b32 v132, v134\n\t" \
  "v_mfma_f32_16x16x32_bf16 a[32:35], a[156:159], v[60:63], a[32:35]\n\t" \
  "ds_read_b64_tr_b16 a[134:135], v235 offset:12288\n\t" \
  "v_xor_b32 v236, 0x24060, v234\n\t" \
  "v_mfma_f32_16x16x32_bf16 a[36:39], a[156:159], v[92:95], a[36:39]\n\t" \
  "buffer_load_dwordx4 v[8:11], v242, %[ws], s73 offen offset:2048 sc1\n\t" \
  "v_mfma_f32_16x16x32_bf16 a[36:39], a[152:155], v[88:91], a[36:39]\n\t" \
  "s_xor_b32 s63, s60, 393216\n\t" \
  "ds_read_b64_tr_b16 a[136:137], v236 offset:0\n\t" \
  "ds_read_b64_tr_b16 a[138:139], v236 offset:4096\n\t" \
  "v_mfma_f32_16x16x32_bf16 a[36:39], a[148:151], v[84:87], a[36:39]\n\t" \
  "s_andn2_b32 s63, s63, 98304\n\t" \
  "v_permlane32_swap_b32 v128, v130\n\t" \
  "v_mfma_f32_16x16x32_bf16 a[36:39], a[144:147], v[80:83], a[36:39]\n\t" \
  "ds_read_b64_tr_b16 a[140:141], v236 offset:8192\n\t" \
  "ds_read_b64_tr_b16 a[142:143], v236 offset:12288\n\t" \
  "v_mfma_f32_16x16x32_bf16 a[40:43], a[144:147], v[64:67], a[40:43]\n\t" \
  "buffer_load_dwordx4 v[12:15], v242, %[ws], s73 offen offset:3072 sc1\n\t" \
  "s_xor_b32 s73, %[eso], 458752\n\t" \
  "v_mfma_f32_16x16x32_bf16 a[40:43], a[148:151], v[68:71], a[40:43]\n\t" \
  "v_permlane32_swap_b32 v133, v135\n\t" \
  "v_mfma_f32_16x16x32_bf16 a[40:43], a[152:155], v[72:75], a[40:43]\n\t" \
  "v_permlane32_swap_b32 v129, v131\n\t" \
  "v_mfma_f32_16x16x32_bf16 a[40:43], a[156:159], v[76:79], a[40:43]\n\t" \
  "s_waitcnt lgkmcnt(4)\n\t" \
  "v_xor_b32 v237, 0x14080, v234\n\t" \
  "v_add_f32 v132, v132, v134\n\t" \
  "v_mfma_f32_16x16x32_bf16 a[48:51], a[128:131], v[48:51], a[48:51]\n\t" \
  "ds_read_b64_tr_b16 a[76:77], v237 offset:0\n\t" \
  "v_add_f32 v128, v128, v130\n\t" \
  "v_mfma_f32_16x16x32_bf16 a[48:51], a[132:135], v[52:55], a[48:51]\n\t" \
  "s_waitcnt lgkmcnt(1)\n\t" \
  "buffer_load_dwordx4 v[32:35], v242, %[ws], s73 offen offset:0 sc1\n\t" \
  "v_mfma_f32_16x16x32_bf16 a[48:51], a[136:139], v[56:59], a[48:51]\n\t" \
  "ds_read_b64_tr_b16 a[78:79], v237 offset:4096\n\t" \
  "ds_read_b64_tr_b16 a[92:93], v237 offset:8192\n\t" \
  "v_mfma_f32_16x16x32_bf16 a[48:51], a[140:143], v[60:63], a[48:51]\n\t" \
  "s_barrier\n\t" \
  "v_mfma_f32_16x16x32_bf16 a[52:55], a[140:143], v[92:95], a[52:55]\n\t" \
  "ds_read_b64_tr_b16 a[94:95], v237 offset:12288\n\t" \
  "v_xor_b32 v235, 0x24080, v234\n\t" \
  "v_mfma_f32_16x16x32_bf16 a[52:55], a[136:139], v[88:91], a[52:55]\n\t" \
  "buffer_load_dwordx4 v[36:39], v242, %[ws], s73 offen offset:1024 sc1\n\t" \
  "buffer_load_dword v246, v245, %[ws], %[fw2] offen sc1\n\t" \
  "v_mfma_f32_16x16x32_bf16 a[52:55], a[132:135], v[84:87], a[52:55]\n\t" \
  "ds_read_b64_tr_b16 a[108:109], v235 offset:0\n\t" \
  "ds_read_b64_tr_b16 a[110:111], v235 offset:4096\n\t" \
  "v_mfma_f32_16x16x32_bf16 a[52:55], a[128:131], v[80:83], a[52:55]\n\t" \
  "ds_read_b64_tr_b16 a[124:125], v235 offset:8192\n\t" \
  "ds_read_b64_tr_b16 a[126:127], v235 offset:12288\n\t" \
  "v_mfma_f32_16x16x32_bf16 a[56:59], a[128:131], v[64:67], a[56:59]\n\t" \
  "v_add_f32 v133, v133, v135\n\t" \
  "v_add_f32 v129, v129, v131\n\t" \
  "v_mfma_f32_16x16x32_bf16 a[56:59], a[132:135], v[68:71], a[56:59]\n\t" \
  "s_bitcmp1_b32 %[wid], 0\n\t" \
  "s_cselect_b32 s72, s64, 0\n\t" \
  "s_xor_b32 s63, s63, s72\n\t" \
  "v_mfma_f32_16x16x32_bf16 a[56:59], a[136:139], v[72:75], a[56:59]\n\t" \
  "s_xor_b32 s64, s64, s63\n\t" \
  "v_xor_b32 v236, 0x140a0, v234\n\t" \
  "v_mfma_f32_16x16x32_bf16 a[56:59], a[140:143], v[76:79], a[56:59]\n\t" \
  "s_waitcnt lgkmcnt(4)\n\t" \
  "buffer_load_dwordx4 v[40:43], v242, %[ws], s73 offen offset:2048 sc1\n\t" \
  "v_mfma_f32_16x16x32_bf16 a[64:67], a[76:79], v[48:51], a[64:67]\n\t" \
  "ds_read_b64_tr_b16 a[144:145], v236 offset:0\n\t" \
  "v_mfma_f32_16x16x32_bf16 a[64:67], a[92:95], v[52:55], a[64:67]\n\t" \
  "s_waitcnt lgkmcnt(1)\n\t" \
  "ds_read_b64_tr_b16 a[146:147], v236 offset:4096\n\t" \
  "v_mfma_f32_16x16x32_bf16 a[64:67], a[108:111], v[56:59], a[64:67]\n\t" \
  "ds_read_b64_tr_b16 a[148:149], v236 offset:8192\n\t" \
  "v_mfma_f32_16x16x32_bf16 a[64:67], a[124:127], v[60:63], a[64:67]\n\t" \
  "buffer_load_dwordx4 v[44:47], v242, %[ws], s73 offen offset:3072 sc1\n\t" \
  "v_mfma_f32_16x16x32_bf16 a[68:71], a[124:127], v[92:95], a[68:71]\n\t" \
  "v_permlane16_swap_b32 v132, v133\n\t" \
  "v_mfma_f32_16x16x32_bf16 a[68:71], a[108:111], v[88:91], a[68:71]\n\t" \
  "ds_read_b64_tr_b16 a[150:151], v236 offset:12288\n\t" \
  "v_xor_b32 v237, 0x240a0, v234\n\t" \
  "v_mfma_f32_16x16x32_bf16 a[68:71], a[92:95], v[84:87], a[68:71]\n\t" \
  "ds_read_b64_tr_b16 a[152:153], v237 offset:0\n\t" \
  "ds_read_b64_tr_b16 a[154:155], v237 offset:4096\n\t" \
  "v_mfma_f32_16x16x32_bf16 a[68:71], a[76:79], v[80:83], a[68:71]\n\t" \
  "v_permlane16_swap_b32 v128, v129\n\t" \
  "v_mfma_f32_16x16x32_bf16 a[72:75], a[76:79], v[64:67], a[72:75]\n\t" \
  "buffer_load_dwordx4 v241, %[wo], s71 offen offset:1024 lds\n\t" \
  "v_mfma_f32_16x16x32_bf16 a[72:75], a[92:95], v[68:71], a[72:75]\n\t" \
  "ds_read_b64_tr_b16 a[156:157], v237 offset:8192\n\t" \
  "ds_read_b64_tr_b16 a[158:159], v237 offset:12288\n\t" \
  "v_mfma_f32_16x16x32_bf16 a[72:75], a[108:111], v[72:75], a[72:75]\n\t" \
  "v_add_f32 v132, v132, v133\n\t" \
  "v_add_f32 v128, v128, v129\n\t" \
  "v_mfma_f32_16x16x32_bf16 a[72:75], a[124:127], v[76:79], a[72:75]\n\t" \
  "s_waitcnt lgkmcnt(4)\n\t" \
  "v_xor_b32 v235, 0x140c0, v234\n\t" \
  "v_add_f32_dpp v248, v132, v132 row_ror:8 row_mask:0xf bank_mask:0xf\n\t" \
  "v_mfma_f32_16x16x32_bf16 a[80:83], a[144:147], v[48:51], a[80:83]\n\t" \
  "buffer_load_dwordx4 v238, %[wo], s71 offen offset:2048 lds\n\t" \
  "v_mfma_f32_16x16x32_bf16 a[80:83], a[148:151], v[52:55], a[80:83]\n\t" \
  "s_waitcnt lgkmcnt(0)\n\t" \
  "ds_read_b64_tr_b16 a[12:13], v235 offset:0\n\t" \
  "ds_read_b64_tr_b16 a[14:15], v235 offset:4096\n\t" \
  "v_mfma_f32_16x16x32_bf16 a[80:83], a[152:155], v[56:59], a[80:83]\n\t" \
  "s_xor_b32 s74, %[eso], 98304\n\t" \
  "ds_read_b64_tr_b16 a[28:29], v235 offset:8192\n\t" \
  "v_add_f32_dpp v249, v128, v128 row_ror:8 row_mask:0xf bank_mask:0xf\n\t" \
  "v_mfma_f32_16x16x32_bf16 a[80:83], a[156:159], v[60:63], a[80:83]\n\t" \
  "ds_read_b64_tr_b16 a[30:31], v235 offset:12288\n\t" \
  "v_xor_b32 v236, 0x240c0, v234\n\t" \
  "v_mfma_f32_16x16x32_bf16 a[84:87], a[156:159], v[92:95], a[84:87]\n\t" \
  "ds_read_b64_tr_b16 a[44:45], v236 offset:0\n\t" \
  "v_mfma_f32_16x16x32_bf16 a[84:87], a[152:155], v[88:91], a[84:87]\n\t" \
  "buffer_load_dwordx4 v239, %[wo], s71 offen offset:3072 lds\n\t" \
  "s_add_u32 m0, %[m], 32768\n\t" \
  "v_mfma_f32_16x16x32_bf16 a[84:87], a[148:151], v[84:87], a[84:87]\n\t" \
  "s_xor_b32 s71, %[wob], 1441792\n\t" \
  "ds_read_b64_tr_b16 a[46:47], v236 offset:4096\n\t" \
  "ds_read_b64_tr_b16 a[60:61], v236 offset:8192\n\t" \
  "v_mfma_f32_16x16x32_bf16 a[84:87], a[144:147], v[80:83], a[84:87]\n\t" \
  "buffer_store_dwordx4 v[160:163], v242, %[ws], s74 offen offset:0 sc1\n\t" \
  "v_mfma_f32_16x16x32_bf16 a[88:91], a[144:147], v[64:67], a[88:91]\n\t" \
  "s_xor_b32 s74, %[eso], 98304\n\t" \
  "ds_read_b64_tr_b16 a[62:63], v236 offset:12288\n\t" \
  "v_xor_b32 v237, 0x140e0, v234\n\t" \
  "v_mfma_f32_16x16x32_bf16 a[88:91], a[148:151], v[68:71], a[88:91]\n\t" \
  "ds_read_b64_tr_b16 a[76:77], v237 offset:0\n\t" \
  "v_mfma_f32_16x16x32_bf16 a[88:91], a[152:155], v[72:75], a[88:91]\n\t" \
  "ds_read_b64_tr_b16 a[78:79], v237 offset:4096\n\t" \
  "v_mfma_f32_16x16x32_bf16 a[88:91], a[156:159], v[76:79], a[88:91]\n\t" \
  "s_waitcnt lgkmcnt(6)\n\t" \
  "buffer_load_dwordx4 v240, %[wo], s71 offen offset:0 lds\n\t" \
  "v_mfma_f32_16x16x32_bf16 a[96:99], a[12:15], v[48:51], a[96:99]\n\t" \
  "ds_read_b64_tr_b16 a[92:93], v237 offset:8192\n\t" \
  "v_mfma_f32_16x16x32_bf16 a[96:99], a[28:31], v[52:55], a[96:99]\n\t" \
  "s_waitcnt lgkmcnt(3)\n\t" \
  "buffer_store_dwordx4 v[164:167], v242, %[ws], s74 offen offset:1024 sc1\n\t" \
  "v_mfma_f32_16x16x32_bf16 a[96:99], a[44:47], v[56:59], a[96:99]\n\t" \
  "s_xor_b32 s74, %[eso], 98304\n\t" \
  "ds_read_b64_tr_b16 a[94:95], v237 offset:12288\n\t" \
  "v_xor_b32 v235, 0x240e0, v234\n\t" \
  "v_mfma_f32_16x16x32_bf16 a[96:99], a[60:63], v[60:63], a[96:99]\n\t" \
  "ds_read_b64_tr_b16 a[108:109], v235 offset:0\n\t" \
  "v_mfma_f32_16x16x32_bf16 a[100:103], a[60:63], v[92:95], a[100:103]\n\t" \
  "ds_read_b64_tr_b16 a[110:111], v235 offset:4096\n\t" \
  "v_mfma_f32_16x16x32_bf16 a[100:103], a[44:47], v[88:91], a[100:103]\n\t" \
  "buffer_load_dwordx4 v241, %[wo], s71 offen offset:1024 lds\n\t" \
  "v_mfma_f32_16x16x32_bf16 a[100:103], a[28:31], v[84:87], a[100:103]\n\t" \
  "ds_read_b64_tr_b16 a[124:125], v235 offset:8192\n\t" \
  "v_mfma_f32_16x16x32_bf16 a[100:103], a[12:15], v[80:83], a[100:103]\n\t" \
  "buffer_store_dwordx4 v[168:171], v242, %[ws], s74 offen offset:2048 sc1\n\t" \
  "s_waitcnt vmcnt(10)\n\t" \
  "v_cmp_ne_u32 vcc, %[tag], v246\n\t" \
  "s_and_b32 vcc_lo, vcc_lo, 0xfe00\n\t" \
  "s_cbranch_scc1 .Lop_s2\n\t" \
  ".Lop_k2:\n\t" \
  "v_mfma_f32_16x16x32_bf16 a[104:107], a[12:15], v[64:67], a[104:107]\n\t" \
  "s_xor_b32 s74, %[eso], 98304\n\t" \
  "ds_read_b64_tr_b16 a[126:127], v235 offset:12288\n\t" \
  "v_xor_b32 v236, 0x1c000, v234\n\t" \
  "v_mfma_f32_16x16x32_bf16 a[104:107], a[28:31], v[68:71], a[104:107]\n\t" \
  "ds_read_b64_tr_b16 a[128:129], v236 offset:0\n\t" \
  "v_mfma_f32_16x16x32_bf16 a[104:107], a[44:47], v[72:75], a[104:107]\n\t" \
  "ds_read_b64_tr_b16 a[130:131], v236 offset:4096\n\t" \
  "v_mfma_f32_16x16x32_bf16 a[104:107], a[60:63], v[76:79], a[104:107]\n\t" \
  "s_waitcnt lgkmcnt(6)\n\t" \
  "ds_read_b64_tr_b16 a[132:133], v236 offset:8192\n\t" \
  "v_mfma_f32_16x16x32_bf16 a[112:115], a[76:79], v[48:51], a[112:115]\n\t" \
  "buffer_store_dwordx4 v[172:175], v242, %[ws], s74 offen offset:3072 sc1\n\t" \
  "v_mfma_f32_16x16x32_bf16 a[112:115], a[92:95], v[52:55], a[112:115]\n\t" \
  "s_waitcnt lgkmcnt(3)\n\t" \
  "ds_read_b64_tr_b16 a[134:135], v236 offset:12288\n\t" \
  "v_mfma_f32_16x16x32_bf16 a[112:115], a[108:111], v[56:59], a[112:115]\n\t" \
  "buffer_load_dwordx4 v238, %[wo], s71 offen offset:2048 lds\n\t" \
  "v_mfma_f32_16x16x32_bf16 a[112:115], a[124:127], v[60:63], a[112:115]\n\t" \
  "ds_read_b64_tr_b16 a[136:137], v236 offset:16384\n\t" \
  "v_mfma_f32_16x16x32_bf16 a[116:119], a[124:127], v[92:95], a[116:119]\n\t" \
  "ds_read_b64_tr_b16 a[138:139], v236 offset:20480\n\t" \
  "v_mfma_f32_16x16x32_bf16 a[116:119], a[108:111], v[88:91], a[116:119]\n\t" \
  "ds_read_b64_tr_b16 a[140:141], v236 offset:24576\n\t" \
  "v_mfma_f32_16x16x32_bf16 a[116:119], a[92:95], v[84:87], a[116:119]\n\t" \
  "buffer_load_dwordx4 v239, %[wo], s71 offen offset:3072 lds\n\t" \
  "v_mfma_f32_16x16x32_bf16 a[116:119], a[76:79], v[80:83], a[116:119]\n\t" \
  "s_xor_b32 s73, %[eso], 524288\n\t" \
  "ds_read_b64_tr_b16 a[142:143], v236 offset:28672\n\t" \
  "v_mfma_f32_16x16x32_bf16 a[120:123], a[76:79], v[64:67], a[120:123]\n\t" \
  "v_xor_b32 v237, 0x1c020, v234\n\t" \
  "v_mfma_f32_16x16x32_bf16 a[120:123], a[92:95], v[68:71], a[120:123]\n\t" \
  "ds_read_b64_tr_b16 a[144:145], v237 offset:0\n\t" \
  "v_mfma_f32_16x16x32_bf16 a[120:123], a[108:111], v[72:75], a[120:123]\n\t" \
  "ds_read_b64_tr_b16 a[146:147], v237 offset:4096\n\t" \
  "v_mfma_f32_16x16x32_bf16 a[120:123], a[124:127], v[76:79], a[120:123]\n\t" \
  "s_waitcnt lgkmcnt(6)\n\t" \
  "buffer_load_dwordx4 v[48:51], v242, %[ws], s73 offen offset:0 sc1\n\t" \
  STP("v_writelane_b32 %[stv], s94, 28\n\t") \
  STP("v_writelane_b32 %[stv], s92, 29\n\t") \
  STP("s_memrealtime s[94:95]\n\t") \
  STP("s_memtime s[92:93]\n\t") \
  "v_mfma_f32_16x16x32_bf16 a[0:3], a[128:131], v[16:19], a[0:3]\n\t" \
  "ds_read_b64_tr_b16 a[148:149], v237 offset:8192\n\t" \
  "v_mfma_f32_16x16x32_bf16 a[0:3], a[132:135], v[20:23], a[0:3]\n\t" \
  "s_waitcnt lgkmcnt(3)\n\t" \
  "ds_read_b64_tr_b16 a[150:151], v237 offset:12288\n\t" \
  "v_mfma_f32_16x16x32_bf16 a[0:3], a[136:139], v[24:27], a[0:3]\n\t" \
  "ds_read_b64_tr_b16 a[152:153], v237 offset:16384\n\t" \
  "v_mfma_f32_16x16x32_bf16 a[0:3], a[140:143], v[28:31], a[0:3]\n\t" \
  "buffer_load_dwordx4 v[52:55], v242, %[ws], s73 offen offset:1024 sc1\n\t" \
  "v_mfma_f32_16x16x32_bf16 a[4:7], a[140:143], v[12:15], a[4:7]\n\t" \
  "ds_read_b64_tr_b16 a[154:155], v237 offset:20480\n\t" \
  "v_mfma_f32_16x16x32_bf16 a[4:7], a[136:139], v[8:11], a[4:7]\n\t" \
  "ds_read_b64_tr_b16 a[156:157], v237 offset:24576\n\t" \
  "v_mfma_f32_16x16x32_bf16 a[4:7], a[132:135], v[4:7], a[4:7]\n\t" \
  "ds_read_b64_tr_b16 a[158:159], v237 offset:28672\n\t" \
  "v_mfma_f32_16x16x32_bf16 a[4:7], a[128:131], v[0:3], a[4:7]\n\t" \
  "s_waitcnt vmcnt(13)\n\t" \
  "buffer_load_dwordx4 v[56:59], v242, %[ws], s73 offen offset:2048 sc1\n\t" \
  "v_mfma_f32_16x16x32_bf16 a[8:11], a[128:131], v[32:35], a[8:11]\n\t" \
  "v_xor_b32 v235, 0x1c040, v234\n\t" \
  "v_mfma_f32_16x16x32_bf16 a[8:11], a[132:135], v[36:39], a[8:11]\n\t" \
  "ds_read_b64_tr_b16 a[12:13], v235 offset:0\n\t" \
  "v_mfma_f32_16x16x32_bf16 a[8:11], a[136:139], v[40:43], a[8:11]\n\t" \
  "ds_read_b64_tr_b16 a[14:15], v235 offset:4096\n\t" \
  "v_mfma_f32_16x16x32_bf16 a[8:11], a[140:143], v[44:47], a[8:11]\n\t" \
  "s_waitcnt lgkmcnt(6)\n\t" \
  "ds_read_b64_tr_b16 a[28:29], v235 offset:8192\n\t" \
  "v_mfma_f32_16x16x32_bf16 a[16:19], a[144:147], v[16:19], a[16:19]\n\t" \
  "buffer_load_dwordx4 v[60:63], v242, %[ws], s73 offen offset:3072 sc1\n\t" \
  "s_xor_b32 s73, %[eso], 557056\n\t" \
  "v_mfma_f32_16x16x32_bf16 a[16:19], a[148:151], v[20:23], a[16:19]\n\t" \
  "s_waitcnt lgkmcnt(3)\n\t" \
  "ds_read_b64_tr_b16 a[30:31], v235 offset:12288\n\t" \
  "v_mfma_f32_16x16x32_bf16 a[16:19], a[152:155], v[24:27], a[16:19]\n\t" \
  "ds_read_b64_tr_b16 a[44:45], v235 offset:16384\n\t" \
  "v_mfma_f32_16x16x32_bf16 a[16:19], a[156:159], v[28:31], a[16:19]\n\t" \
  "ds_read_b64_tr_b16 a[46:47], v235 offset:20480\n\t" \
  "v_mfma_f32_16x16x32_bf16 a[20:23], a[156:159], v[12:15], a[20:23]\n\t" \
  "buffer_load_dwordx4 v[80:83], v242, %[ws], s73 offen offset:0 sc1\n\t" \
  "v_mfma_f32_16x16x32_bf16 a[20:23], a[152:155], v[8:11], a[20:23]\n\t" \
  "ds_read_b64_tr_b16 a[60:61], v235 offset:24576\n\t" \
  "v_mfma_f32_16x16x32_bf16 a[20:23], a[148:151], v[4:7], a[20:23]\n\t" \
  "ds_read_b64_tr_b16 a[62:63], v235 offset:28672\n\t" \
  "v_mfma_f32_16x16x32_bf16 a[20:23], a[144:147], v[0:3], a[20:23]\n\t" \
  "v_mfma_f32_16x16x32_bf16 a[24:27], a[144:147], v[32:35], a[24:27]\n\t" \
  "v_xor_b32 v236, 0x1c060, v234\n\t" \
  "v_mfma_f32_16x16x32_bf16 a[24:27], a[148:151], v[36:39], a[24:27]\n\t" \
  "buffer_load_dwordx4 v[84:87], v242, %[ws], s73 offen offset:1024 sc1\n\t" \
  "v_mfma_f32_16x16x32_bf16 a[24:27], a[152:155], v[40:43], a[24:27]\n\t" \
  "ds_read_b64_tr_b16 a[76:77], v236 offset:0\n\t" \
  "v_mfma_f32_16x16x32_bf16 a[24:27], a[156:159], v[44:47], a[24:27]\n\t" \
  "s_waitcnt lgkmcnt(5)\n\t" \
  "ds_read_b64_tr_b16 a[78:79], v236 offset:4096\n\t" \
  "v_mfma_f32_16x16x32_bf16 a[32:35], a[12:15], v[16:19], a[32:35]\n\t" \
  "ds_read_b64_tr_b16 a[92:93], v236 offset:8192\n\t" \
  "v_mfma_f32_16x16x32_bf16 a[32:35], a[28:31], v[20:23], a[32:35]\n\t" \
  "s_waitcnt lgkmcnt(3)\n\t" \
  "buffer_load_dwordx4 v[88:91], v242, %[ws], s73 offen offset:2048 sc1\n\t" \
  "v_mfma_f32_16x16x32_bf16 a[32:35], a[44:47], v[24:27], a[32:35]\n\t" \
  "ds_read_b64_tr_b16 a[94:95], v236 offset:12288\n\t" \
  "v_mfma_f32_16x16x32_bf16 a[32:35], a[60:63], v[28:31], a[32:35]\n\t" \
  "ds_read_b64_tr_b16 a[108:109], v236 offset:16384\n\t" \
  "v_mfma_f32_16x16x32_bf16 a[36:39], a[60:63], v[12:15], a[36:39]\n\t" \
  "ds_read_b64_tr_b16 a[110:111], v236 offset:20480\n\t" \
  "v_mfma_f32_16x16x32_bf16 a[36:39], a[44:47], v[8:11], a[36:39]\n\t" \
  "buffer_load_dwordx4 v[92:95], v242, %[ws], s73 offen offset:3072 sc1\n\t" \
  "s_xor_b32 s73, %[eso], 589824\n\t" \
  "v_mfma_f32_16x16x32_bf16 a[36:39], a[28:31], v[4:7], a[36:39]\n\t" \
  "ds_read_b64_tr_b16 a[124:125], v236 offset:24576\n\t" \
  "v_mfma_f32_16x16x32_bf16 a[36:39], a[12:15], v[0:3], a[36:39]\n\t" \
  "ds_read_b64_tr_b16 a[126:127], v236 offset:28672\n\t" \
  "v_mfma_f32_16x16x32_bf16 a[40:43], a[12:15], v[32:35], a[40:43]\n\t" \
  "v_xor_b32 v237, 0x1c080, v234\n\t" \
  "v_mfma_f32_16x16x32_bf16 a[40:43], a[28:31], v[36:39], a[40:43]\n\t" \
  "ds_read_b64_tr_b16 a[208:209], v237 offset:0\n\t" \
  "v_mfma_f32_16x16x32_bf16 a[40:43], a[44:47], v[40:43], a[40:43]\n\t" \
  "buffer_load_dwordx4 v[64:67], v242, %[ws], s73 offen offset:0 sc1\n\t" \
  "v_mfma_f32_16x16x32_bf16 a[40:43], a[60:63], v[44:47], a[40:43]\n\t" \
  "s_waitcnt lgkmcnt(5)\n\t" \
  "ds_read_b64_tr_b16 a[210:211], v237 offset:4096\n\t" \
  "v_mfma_f32_16x16x32_bf16 a[48:51], a[76:79], v[16:19], a[48:51]\n\t" \
  "s_add_u32 m0, %[m], 81920\n\t" \
  "ds_read_b64_tr_b16 a[212:213], v237 offset:8192\n\t" \
  "v_mfma_f32_16x16x32_bf16 a[48:51], a[92:95], v[20:23], a[48:51]\n\t" \
  "s_waitcnt lgkmcnt(3)\n\t" \
  "ds_read_b64_tr_b16 a[214:215], v237 offset:12288\n\t" \
  "v_mfma_f32_16x16x32_bf16 a[48:51], a[108:111], v[24:27], a[48:51]\n\t" \
  "buffer_load_dwordx4 v[68:71], v242, %[ws], s73 offen offset:1024 sc1\n\t" \
  "v_mfma_f32_16x16x32_bf16 a[48:51], a[124:127], v[28:31], a[48:51]\n\t" \
  "s_barrier\n\t" \
  "v_mfma_f32_16x16x32_bf16 a[52:55], a[124:127], v[12:15], a[52:55]\n\t" \
  "s_xor_b32 s71, %[wob], 1572864\n\t" \
  "ds_write_b64 v250, v[248:249] offset:19456\n\t" \
  "v_mfma_f32_16x16x32_bf16 a[52:55], a[108:111], v[8:11], a[52:55]\n\t" \
  "ds_read_b64_tr_b16 a[216:217], v237 offset:16384\n\t" \
  "ds_read_b64_tr_b16 a[218:219], v237 offset:20480\n\t" \
  "v_mfma_f32_16x16x32_bf16 a[52:55], a[92:95], v[4:7], a[52:55]\n\t" \
  "ds_read_b64_tr_b16 a[220:221], v237 offset:24576\n\t" \
  "v_mfma_f32_16x16x32_bf16 a[52:55], a[76:79], v[0:3], a[52:55]\n\t" \
  "buffer_load_dwordx4 v[72:75], v242, %[ws], s73 offen offset:2048 sc1\n\t" \
  "v_mfma_f32_16x16x32_bf16 a[56:59], a[76:79], v[32:35], a[56:59]\n\t" \
  "ds_read_b64_tr_b16 a[222:223], v237 offset:28672\n\t" \
  "v_xor_b32 v235, 0x1c0a0, v234\n\t" \
  "v_mfma_f32_16x16x32_bf16 a[56:59], a[92:95], v[36:39], a[56:59]\n\t" \
  "ds_read_b64_tr_b16 a[12:13], v235 offset:0\n\t" \
  "v_mfma_f32_16x16x32_bf16 a[56:59], a[108:111], v[40:43], a[56:59]\n\t" \
  "ds_read_b64_tr_b16 a[14:15], v235 offset:4096\n\t" \
  "v_mfma_f32_16x16x32_bf16 a[56:59], a[124:127], v[44:47], a[56:59]\n\t" \
  "s_waitcnt lgkmcnt(7)\n\t" \
  "buffer_load_dwordx4 v[76:79], v242, %[ws], s73 offen offset:3072 sc1\n\t" \
  "v_mfma_f32_16x16x32_bf16 a[64:67], a[208:211], v[16:19], a[64:67]\n\t" \
  "ds_read_b64_tr_b16 a[28:29], v235 offset:8192\n\t" \
  "v_mfma_f32_16x16x32_bf16 a[64:67], a[212:215], v[20:23], a[64:67]\n\t" \
  "s_waitcnt lgkmcnt(3)\n\t" \
  "ds_read_b64_tr_b16 a[30:31], v235 offset:12288\n\t" \
  "v_mfma_f32_16x16x32_bf16 a[64:67], a[216:219], v[24:27], a[64:67]\n\t" \
  "ds_read_b64_tr_b16 a[44:45], v235 offset:16384\n\t" \
  "v_mfma_f32_16x16x32_bf16 a[64:67], a[220:223], v[28:31], a[64:67]\n\t" \
  "buffer_load_dwordx4 v240, %[wo], s71 offen offset:0 lds\n\t" \
  "v_mfma_f32_16x16x32_bf16 a[68:71], a[220:223], v[12:15], a[68:71]\n\t" \
  "ds_read_b64_tr_b16 a[46:47], v235 offset:20480\n\t" \
  "v_mfma_f32_16x16x32_bf16 a[68:71], a[216:219], v[8:11], a[68:71]\n\t" \
  "ds_read_b64_tr_b16 a[60:61], v235 offset:24576\n\t" \
  "v_mfma_f32_16x16x32_bf16 a[68:71], a[212:215], v[4:7], a[68:71]\n\t" \
  "ds_read_b64_tr_b16 a[62:63], v235 offset:28672\n\t" \
  "v_mfma_f32_16x16x32_bf16 a[68:71], a[208:211], v[0:3], a[68:71]\n\t" \
  "v_mfma_f32_16x16x32_bf16 a[72:75], a[208:211], v[32:35], a[72:75]\n\t" \
  "buffer_load_dwordx4 v241, %[wo], s71 offen offset:1024 lds\n\t" \
  "v_mfma_f32_16x16x32_bf16 a[72:75], a[212:215], v[36:39], a[72:75]\n\t" \
  "v_xor_b32 v236, 0x1c0c0, v234\n\t" \
  "v_mfma_f32_16x16x32_bf16 a[72:75], a[216:219], v[40:43], a[72:75]\n\t" \
  "ds_read_b64_tr_b16 a[192:193], v236 offset:0\n\t" \
  "v_mfma_f32_16x16x32_bf16 a[72:75], a[220:223], v[44:47], a[72:75]\n\t" \
  "s_waitcnt lgkmcnt(5)\n\t" \
  "ds_read_b64_tr_b16 a[194:195], v236 offset:4096\n\t" \
  "v_mfma_f32_16x16x32_bf16 a[80:83], a[12:15], v[16:19], a[80:83]\n\t" \
  "buffer_load_dwordx4 v238, %[wo], s71 offen offset:2048 lds\n\t" \
  "v_mfma_f32_16x16x32_bf16 a[80:83], a[28:31], v[20:23], a[80:83]\n\t" \
  "s_waitcnt lgkmcnt(2)\n\t" \
  "ds_read_b64_tr_b16 a[196:197], v236 offset:8192\n\t" \
  "v_mfma_f32_16x16x32_bf16 a[80:83], a[44:47], v[24:27], a[80:83]\n\t" \
  "ds_read_b64_tr_b16 a[198:199], v236 offset:12288\n\t" \
  "v_mfma_f32_16x16x32_bf16 a[80:83], a[60:63], v[28:31], a[80:83]\n\t" \
  "s_barrier\n\t" \
  "v_mfma_f32_16x16x32_bf16 a[84:87], a[60:63], v[12:15], a[84:87]\n\t" \
  "s_waitcnt vmcnt(17)\n\t" \
  "buffer_store_dword v243, v254, %[ws], %[fw2] offen offset:64 sc1\n\t" \
  "v_mfma_f32_16x16x32_bf16 a[84:87], a[44:47], v[8:11], a[84:87]\n\t" \
  "ds_read_b64_tr_b16 a[200:201], v236 offset:16384\n\t" \
  "ds_read_b64_tr_b16 a[202:203], v236 offset:20480\n\t" \
  "v_mfma_f32_16x16x32_bf16 a[84:87], a[28:31], v[4:7], a[84:87]\n\t" \
  "buffer_load_dwordx4 v239, %[wo], s71 offen offset:3072 lds\n\t" \
  "v_mfma_f32_16x16x32_bf16 a[84:87], a[12:15], v[0:3], a[84:87]\n\t" \
  "ds_read_b64_tr_b16 a[204:205], v236 offset:24576\n\t" \
  "ds_read_b64_tr_b16 a[206:207], v236 offset:28672\n\t" \
  "v_mfma_f32_16x16x32_bf16 a[88:91], a[12:15], v[32:35], a[88:91]\n\t" \
  "v_and_b32 v250, 3, v244\n\t" \
  "v_xor_b32 v237, 0x1c0e0, v234\n\t" \
  "v_mfma_f32_16x16x32_bf16 a[88:91], a[28:31], v[36:39], a[88:91]\n\t" \
  "ds_read_b64_tr_b16 a[208:209], v237 offset:0\n\t" \
  "v_bfe_u32 v251, v244, 4, 1\n\t" \
  "v_mfma_f32_16x16x32_bf16 a[88:91], a[44:47], v[40:43], a[88:91]\n\t" \
  "ds_read_b64_tr_b16 a[210:211], v237 offset:4096\n\t" \
  "v_lshl_add_u32 v250, v251, 2, v250\n\t" \
  "v_mfma_f32_16x16x32_bf16 a[88:91], a[60:63], v[44:47], a[88:91]\n\t" \
  "s_waitcnt lgkmcnt(6)\n\t" \
  "ds_read_b64_tr_b16 a[212:213], v237 offset:8192\n\t" \
  "v_bfe_u32 v251, v244, 2, 2\n\t" \
  "v_mfma_f32_16x16x32_bf16 a[96:99], a[192:195], v[16:19], a[96:99]\n\t" \
  "ds_read_b64_tr_b16 a[214:215], v237 offset:12288\n\t" \
  "v_lshlrev_b32 v251, 8, v251\n\t" \
  "v_mfma_f32_16x16x32_bf16 a[96:99], a[196:199], v[20:23], a[96:99]\n\t" \
  "s_waitcnt lgkmcnt(4)\n\t" \
  "ds_read_b64_tr_b16 a[216:217], v237 offset:16384\n\t" \
  "v_lshl_add_u32 v251, v250, 3, v251\n\t" \
  "v_mfma_f32_16x16x32_bf16 a[96:99], a[200:203], v[24:27], a[96:99]\n\t" \
  "ds_read_b64_tr_b16 a[218:219], v237 offset:20480\n\t" \
  "v_mfma_f32_16x16x32_bf16 a[96:99], a[204:207], v[28:31], a[96:99]\n\t" \
  "ds_read_b64_tr_b16 a[220:221], v237 offset:24576\n\t" \
  "v_mfma_f32_16x16x32_bf16 a[100:103], a[204:207], v[12:15], a[100:103]\n\t" \
  "ds_read_b64_tr_b16 a[222:223], v237 offset:28672\n\t" \
  "v_mfma_f32_16x16x32_bf16 a[100:103], a[200:203], v[8:11], a[100:103]\n\t" \
  "s_lshr_b32 s75, %[m], 6\n\t" \
  "s_bitset1_b32 s75, 17\n\t" \
  "v_add_u32 v251, s75, v251\n\t" \
  "v_mfma_f32_16x16x32_bf16 a[100:103], a[196:199], v[4:7], a[100:103]\n\t" \
  "ds_read_b64 v[208:209], v251 offset:19456\n\t" \
  "v_lshlrev_b32 v218, 4, v250\n\t" \
  "v_mfma_f32_16x16x32_bf16 a[100:103], a[192:195], v[0:3], a[100:103]\n\t" \
  "v_mov_b32 v249, %[tag]\n\t" \
  "v_mov_b32 v251, %[tag]\n\t" \
  "v_mfma_f32_16x16x32_bf16 a[104:107], a[192:195], v[32:35], a[104:107]\n\t" \
  "s_mov_b32 s82, 0x000f000f\n\t" \
  "v_xor_b32 v235, 0x0, v234\n\t" \
  "s_mov_b32 s83, 0\n\t" \
  "v_mfma_f32_16x16x32_bf16 a[104:107], a[196:199], v[36:39], a[104:107]\n\t" \
  "ds_read_b64_tr_b16 a[12:13], v235 offset:0\n\t" \
  "v_mfma_f32_16x16x32_bf16 a[104:107], a[200:203], v[40:43], a[104:107]\n\t" \
  "ds_read_b64_tr_b16 a[14:15], v235 offset:4096\n\t" \
  "v_mfma_f32_16x16x32_bf16 a[104:107], a[204:207], v[44:47], a[104:107]\n\t" \
  "s_waitcnt lgkmcnt(7)\n\t" \
  "ds_read_b64_tr_b16 a[28:29], v235 offset:8192\n\t" \
  "v_mfma_f32_16x16x32_bf16 a[112:115], a[208:211], v[16:19], a[112:115]\n\t" \
  "ds_read_b64_tr_b16 a[30:31], v235 offset:12288\n\t" \
  "v_mfma_f32_16x16x32_bf16 a[112:115], a[212:215], v[20:23], a[112:115]\n\t" \
  "s_waitcnt lgkmcnt(5)\n\t" \
  "ds_read_b64_tr_b16 a[44:45], v235 offset:16384\n\t" \
  "v_mfma_f32_16x16x32_bf16 a[112:115], a[216:219], v[24:27], a[112:115]\n\t" \
  "s_waitcnt lgkmcnt(5)\n\t" \
  "v_add_f32_dpp v210, v208, v208 row_ror:4 row_mask:0xf bank_mask:0xf\n\t" \
  "v_add_f32_dpp v211, v209, v209 row_ror:4 row_mask:0xf bank_mask:0xf\n\t" \
  "v_mfma_f32_16x16x32_bf16 a[112:115], a[220:223], v[28:31], a[112:115]\n\t" \
  "ds_read_b64_tr_b16 a[46:47], v235 offset:20480\n\t" \
  "v_add_f32_dpp v248, v210, v210 row_ror:8 row_mask:0xf bank_mask:0xf\n\t" \
  ".Lhs12:\n\ts_cbranch_execz .Lhs12n\n\t" \
  "v_mfma_f32_16x16x32_bf16 a[116:119], a[220:223], v[12:15], a[116:119]\n\t" \
  "ds_read_b64_tr_b16 a[60:61], v235 offset:24576\n\t" \
  "v_add_f32_dpp v250, v211, v211 row_ror:8 row_mask:0xf bank_mask:0xf\n\t" \
  "v_mfma_f32_16x16x32_bf16 a[116:119], a[216:219], v[8:11], a[116:119]\n\t" \
  "s_mov_b64 exec, s[82:83]\n\t" \
  "buffer_store_dwordx4 v[248:251], v218, %[ws], %[mbp] offen sc1\n\t" \
  "s_mov_b64 exec, -1\n\t" \
  "v_mfma_f32_16x16x32_bf16 a[116:119], a[212:215], v[4:7], a[116:119]\n\t" \
  "ds_read_b64_tr_b16 a[62:63], v235 offset:28672\n\t" \
  "v_mfma_f32_16x16x32_bf16 a[116:119], a[208:211], v[0:3], a[116:119]\n\t" \
  "s_xor_b32 s73, %[eso], 655360\n\t" \
  "v_mfma_f32_16x16x32_bf16 a[120:123], a[208:211], v[32:35], a[120:123]\n\t" \
  "v_xor_b32 v236, 0x20, v234\n\t" \
  "v_mfma_f32_16x16x32_bf16 a[120:123], a[212:215], v[36:39], a[120:123]\n\t" \
  "ds_read_b64_tr_b16 a[76:77], v236 offset:0\n\t" \
  "v_mfma_f32_16x16x32_bf16 a[120:123], a[216:219], v[40:43], a[120:123]\n\t" \
  "ds_read_b64_tr_b16 a[78:79], v236 offset:4096\n\t" \
  "v_mov_b32 v160, 0\n\t" \
  "v_mfma_f32_16x16x32_bf16 a[120:123], a[220:223], v[44:47], a[120:123]\n\t" \
  "s_waitcnt vmcnt(14) lgkmcnt(6)\n\t" \
  "buffer_load_dwordx4 v[16:19], v242, %[ws], s73 offen offset:0 sc1\n\t" \
  "v_mfma_f32_16x16x32_bf16 a[0:3], a[12:15], v[48:51], a[0:3]\n\t" \
  "ds_read_b64_tr_b16 a[92:93], v236 offset:8192\n\t" \
  "v_mov_b32 v161, 0\n\t" \
  "v_mfma_f32_16x16x32_bf16 a[0:3], a[28:31], v[52:55], a[0:3]\n\t" \
  "s_waitcnt lgkmcnt(3)\n\t" \
  "buffer_load_dwordx4 v[20:23], v242, %[ws], s73 offen offset:1024 sc1\n\t" \
  "v_mfma_f32_16x16x32_bf16 a[0:3], a[44:47], v[56:59], a[0:3]\n\t" \
  "ds_read_b64_tr_b16 a[94:95], v236 offset:12288\n\t" \
  "v_mov_b32 v162, 0\n\t" \
  "v_mfma_f32_16x16x32_bf16 a[0:3], a[60:63], v[60:63], a[0:3]\n\t" \
  "s_waitcnt vmcnt(12)\n\t" \
  "ds_read_b64_tr_b16 a[108:109], v236 offset:16384\n\t" \
  "v_mov_b32 v163, 0\n\t" \
  "v_mfma_f32_16x16x32_bf16 a[4:7], a[60:63], v[92:95], a[4:7]\n\t" \
  "ds_read_b64_tr_b16 a[110:111], v236 offset:20480\n\t" \
  "v_mov_b32 v164, 0x3f803f80\n\t" \
  "v_mfma_f32_16x16x32_bf16 a[4:7], a[44:47], v[88:91], a[4:7]\n\t" \
  "buffer_load_dwordx4 v[24:27], v242, %[ws], s73 offen offset:2048 sc1\n\t" \
  "v_mfma_f32_16x16x32_bf16 a[4:7], a[28:31], v[84:87], a[4:7]\n\t" \
  "ds_read_b64_tr_b16 a[124:125], v236 offset:24576\n\t" \
  "v_add_u32 v165, s70, v242\n\t" \
  "v_mfma_f32_16x16x32_bf16 a[4:7], a[12:15], v[80:83], a[4:7]\n\t" \
  "s_waitcnt vmcnt(9)\n\t" \
  "ds_read_b64_tr_b16 a[126:127], v236 offset:28672\n\t" \
  "v_mfma_f32_16x16x32_bf16 a[8:11], a[12:15], v[64:67], a[8:11]\n\t" \
  "v_xor_b32 v237, 0x40, v234\n\t" \
  "v_mfma_f32_16x16x32_bf16 a[8:11], a[28:31], v[68:71], a[8:11]\n\t" \
  "buffer_load_dwordx4 v[28:31], v242, %[ws], s73 offen offset:3072 sc1\n\t" \
  "s_xor_b32 s73, %[eso], 688128\n\t" \
  "v_mfma_f32_16x16x32_bf16 a[8:11], a[44:47], v[72:75], a[8:11]\n\t" \
  "ds_read_b64_tr_b16 a[208:209], v237 offset:0\n\t" \
  "v_mfma_f32_16x16x32_bf16 a[8:11], a[60:63], v[76:79], a[8:11]\n\t" \
  "s_waitcnt lgkmcnt(5)\n\t" \
  "ds_read_b64_tr_b16 a[210:211], v237 offset:4096\n\t" \
  "v_mfma_f32_16x16x32_bf16 a[16:19], a[76:79], v[48:51], a[16:19]\n\t" \
  "ds_read_b64_tr_b16 a[212:213], v237 offset:8192\n\t" \
  "v_mfma_f32_16x16x32_bf16 a[16:19], a[92:95], v[52:55], a[16:19]\n\t" \
  "s_waitcnt lgkmcnt(3)\n\t" \
  "ds_read_b64_tr_b16 a[214:215], v237 offset:12288\n\t" \
  "v_mfma_f32_16x16x32_bf16 a[16:19], a[108:111], v[56:59], a[16:19]\n\t" \
  "buffer_load_dwordx4 v[0:3], v242, %[ws], s73 offen offset:0 sc1\n\t" \
  "v_mfma_f32_16x16x32_bf16 a[16:19], a[124:127], v[60:63], a[16:19]\n\t" \
  "ds_read_b64_tr_b16 a[216:217], v237 offset:16384\n\t" \
  "v_mfma_f32_16x16x32_bf16 a[20:23], a[124:127], v[92:95], a[20:23]\n\t" \
  "ds_read_b64_tr_b16 a[218:219], v237 offset:20480\n\t" \
  "v_mfma_f32_16x16x32_bf16 a[20:23], a[108:111], v[88:91], a[20:23]\n\t" \
  "ds_write_b128 v165, v[160:163]\n\t" \
  "v_mfma_f32_16x16x32_bf16 a[20:23], a[92:95], v[84:87], a[20:23]\n\t" \
  "buffer_load_dwordx4 v[4:7], v242, %[ws], s73 offen offset:1024 sc1\n\t" \
  "v_mfma_f32_16x16x32_bf16 a[20:23], a[76:79], v[80:83], a[20:23]\n\t" \
  "ds_read_b64_tr_b16 a[220:221], v237 offset:24576\n\t" \
  "ds_read_b64_tr_b16 a[222:223], v237 offset:28672\n\t" \
  "v_mfma_f32_16x16x32_bf16 a[24:27], a[76:79], v[64:67], a[24:27]\n\t" \
  "v_and_b32 v171, 15, v244\n\t" \
  "v_lshrrev_b32 v172, 4, v244\n\t" \
  "v_mfma_f32_16x16x32_bf16 a[24:27], a[92:95], v[68:71], a[24:27]\n\t" \
  "v_lshlrev_b32 v170, 13, v172\n\t" \
  "v_xor_b32 v235, 0x60, v234\n\t" \
  "v_mfma_f32_16x16x32_bf16 a[24:27], a[108:111], v[72:75], a[24:27]\n\t" \
  "buffer_load_dwordx4 v[8:11], v242, %[ws], s73 offen offset:2048 sc1\n\t" \
  "v_mfma_f32_16x16x32_bf16 a[24:27], a[124:127], v[76:79], a[24:27]\n\t" \
  "s_waitcnt lgkmcnt(5)\n\t" \
  "ds_read_b64_tr_b16 a[192:193], v235 offset:0\n\t" \
  "v_lshl_add_u32 v170, v171, 4, v170\n\t" \
  "v_mfma_f32_16x16x32_bf16 a[32:35], a[208:211], v[48:51], a[32:35]\n\t" \
  "ds_read_b64_tr_b16 a[194:195], v235 offset:4096\n\t" \
  "v_lshlrev_b32 v174, 7, v172\n\t" \
  "v_mfma_f32_16x16x32_bf16 a[32:35], a[212:215], v[52:55], a[32:35]\n\t" \
  "s_waitcnt lgkmcnt(2)\n\t" \
  "ds_read_b64_tr_b16 a[196:197], v235 offset:8192\n\t" \
  "v_lshl_add_u32 v174, v171, 3, v174\n\t" \
  "v_mfma_f32_16x16x32_bf16 a[32:35], a[216:219], v[56:59], a[32:35]\n\t" \
  "ds_read_b64_tr_b16 a[198:199], v235 offset:12288\n\t" \
  "v_add_u32 v174, s70, v174\n\t" \
  "v_mfma_f32_16x16x32_bf16 a[32:35], a[220:223], v[60:63], a[32:35]\n\t" \
  "buffer_load_dwordx4 v[12:15], v242, %[ws], s73 offen offset:3072 sc1\n\t" \
  "s_xor_b32 s73, %[eso], 720896\n\t" \
  "v_mfma_f32_16x16x32_bf16 a[36:39], a[220:223], v[92:95], a[36:39]\n\t" \
  "ds_read_b64_tr_b16 a[200:201], v235 offset:16384\n\t" \
  "v_mfma_f32_16x16x32_bf16 a[36:39], a[216:219], v[88:91], a[36:39]\n\t" \
  "ds_read_b64_tr_b16 a[202:203], v235 offset:20480\n\t" \
  "v_mfma_f32_16x16x32_bf16 a[36:39], a[212:215], v[84:87], a[36:39]\n\t" \
  "ds_read_b64_tr_b16 a[204:205], v235 offset:24576\n\t" \
  "v_mfma_f32_16x16x32_bf16 a[36:39], a[208:211], v[80:83], a[36:39]\n\t" \
  "buffer_load_dwordx4 v[32:35], v242, %[ws], s73 offen offset:0 sc1\n\t" \
  "v_mfma_f32_16x16x32_bf16 a[40:43], a[208:211], v[64:67], a[40:43]\n\t" \
  "ds_read_b64_tr_b16 a[206:207], v235 offset:28672\n\t" \
  "v_mfma_f32_16x16x32_bf16 a[40:43], a[212:215], v[68:71], a[40:43]\n\t" \
  "ds_write_b128 v165, v[160:163] offset:1024\n\t" \
  "v_mfma_f32_16x16x32_bf16 a[40:43], a[216:219], v[72:75], a[40:43]\n\t" \
  "v_lshlrev_b32 v171, 2, v171\n\t" \
  "v_mfma_f32_16x16x32_bf16 a[40:43], a[220:223], v[76:79], a[40:43]\n\t" \
  "s_waitcnt lgkmcnt(5)\n\t" \
  "v_cmp_eq_u32 vcc, 2, v172\n\t" \
  "s_nop 1\n\t" \
  "v_cndmask_b32 v172, 0, 1.0, vcc\n\t" \
  "v_mfma_f32_16x16x32_bf16 a[48:51], a[192:195], v[48:51], a[48:51]\n\t" \
  "s_add_u32 m0, %[m], 114688\n\t" \
  "buffer_load_dwordx4 v[36:39], v242, %[ws], s73 offen offset:1024 sc1\n\t" \
  "v_mfma_f32_16x16x32_bf16 a[48:51], a[196:199], v[52:55], a[48:51]\n\t" \
  "s_waitcnt lgkmcnt(1)\n\t" \
  "ds_write_b128 v165, v[160:163] offset:1536\n\t" \
  "v_mfma_f32_16x16x32_bf16 a[48:51], a[200:203], v[56:59], a[48:51]\n\t" \
  "v_xor_b32 v236, 0x80, v234\n\t" \
  "v_mfma_f32_16x16x32_bf16 a[48:51], a[204:207], v[60:63], a[48:51]\n\t" \
  "s_barrier\n\t" \
  "v_mfma_f32_16x16x32_bf16 a[52:55], a[204:207], v[92:95], a[52:55]\n\t" \
  "buffer_load_dwordx4 v[40:43], v242, %[ws], s73 offen offset:2048 sc1\n\t" \
  "v_mfma_f32_16x16x32_bf16 a[52:55], a[200:203], v[88:91], a[52:55]\n\t" \
  "s_xor_b32 s71, %[wob], 1703936\n\t" \
  "ds_read_b64_tr_b16 a[208:209], v236 offset:0\n\t" \
  "ds_read_b64_tr_b16 a[210:211], v236 offset:4096\n\t" \
  "v_mfma_f32_16x16x32_bf16 a[52:55], a[196:199], v[84:87], a[52:55]\n\t" \
  "s_waitcnt lgkmcnt(2)\n\t" \
  "v_bfe_u32 v160, v244, 3, 1\n\t" \
  "v_bfe_u32 v173, v244, 1, 2\n\t" \
  "v_mfma_f32_16x16x32_bf16 a[52:55], a[192:195], v[80:83], a[52:55]\n\t" \
  "ds_read_b64_tr_b16 a[212:213], v236 offset:8192\n\t" \
  "ds_read_b64_tr_b16 a[214:215], v236 offset:12288\n\t" \
  "v_mfma_f32_16x16x32_bf16 a[56:59], a[192:195], v[64:67], a[56:59]\n\t" \
  "buffer_load_dwordx4 v[44:47], v242, %[ws], s73 offen offset:3072 sc1\n\t" \
  "v_mfma_f32_16x16x32_bf16 a[56:59], a[196:199], v[68:71], a[56:59]\n\t" \
  "ds_read_b64_tr_b16 a[216:217], v236 offset:16384\n\t" \
  "ds_read_b64_tr_b16 a[218:219], v236 offset:20480\n\t" \
  "v_mfma_f32_16x16x32_bf16 a[56:59], a[200:203], v[72:75], a[56:59]\n\t" \
  "ds_read_b64_tr_b16 a[220:221], v236 offset:24576\n\t" \
  "v_lshlrev_b32 v173, 4, v173\n\t" \
  "ds_read_b64_tr_b16 a[222:223], v236 offset:28672\n\t" \
  "v_mfma_f32_16x16x32_bf16 a[56:59], a[204:207], v[76:79], a[56:59]\n\t" \
  "s_waitcnt lgkmcnt(4)\n\t" \
  "v_lshl_add_u32 v173, v160, 3, v173\n\t" \
  "v_and_b32 v160, 1, v244\n\t" \
  "v_mfma_f32_16x16x32_bf16 a[64:67], a[208:211], v[48:51], a[64:67]\n\t" \
  "v_lshl_add_u32 v173, v160, 2, v173\n\t" \
  "v_xor_b32 v237, 0xa0, v234\n\t" \
  "v_mfma_f32_16x16x32_bf16 a[64:67], a[212:215], v[52:55], a[64:67]\n\t" \
  "s_waitcnt lgkmcnt(0)\n\t" \
  "buffer_load_dwordx4 v240, %[wo], s71 offen offset:0 lds\n\t" \
  "v_mfma_f32_16x16x32_bf16 a[64:67], a[216:219], v[56:59], a[64:67]\n\t" \
  "ds_read_b64_tr_b16 a[192:193], v237 offset:0\n\t" \
  "v_add_u32 v173, s70, v173\n\t" \
  "v_mfma_f32_16x16x32_bf16 a[64:67], a[220:223], v[60:63], a[64:67]\n\t" \
  "ds_read_b64_tr_b16 a[194:195], v237 offset:4096\n\t" \
  "v_mfma_f32_16x16x32_bf16 a[68:71], a[220:223], v[92:95], a[68:71]\n\t" \
  "ds_read_b64_tr_b16 a[196:197], v237 offset:8192\n\t" \
  "v_mfma_f32_16x16x32_bf16 a[68:71], a[216:219], v[88:91], a[68:71]\n\t" \
  "buffer_load_dwordx4 v241, %[wo], s71 offen offset:1024 lds\n\t" \
  "v_mfma_f32_16x16x32_bf16 a[68:71], a[212:215], v[84:87], a[68:71]\n\t" \
  "s_mov_b64 exec, 0xffff\n\t" \
  "ds_write_b32 v173, v164 offset:960\n\t" \
  "s_mov_b64 exec, -1\n\t" \
  "v_mfma_f32_16x16x32_bf16 a[68:71], a[208:211], v[80:83], a[68:71]\n\t" \
  "ds_read_b64_tr_b16 a[198:199], v237 offset:12288\n\t" \
  "ds_read_b64_tr_b16 a[200:201], v237 offset:16384\n\t" \
  "v_mfma_f32_16x16x32_bf16 a[72:75], a[208:211], v[64:67], a[72:75]\n\t" \
  "ds_read_b64_tr_b16 a[202:203], v237 offset:20480\n\t" \
  "v_lshrrev_b32 v229, 4, v244\n\t" \
  "v_mfma_f32_16x16x32_bf16 a[72:75], a[212:215], v[68:71], a[72:75]\n\t" \
  "ds_read_b64_tr_b16 a[204:205], v237 offset:24576\n\t" \
  "v_and_b32 v231, 15, v244\n\t" \
  "v_mfma_f32_16x16x32_bf16 a[72:75], a[216:219], v[72:75], a[72:75]\n\t" \
  "buffer_load_dwordx4 v238, %[wo], s71 offen offset:2048 lds\n\t" \
  "ds_read_b64_tr_b16 a[206:207], v237 offset:28672\n\t" \
  "v_mfma_f32_16x16x32_bf16 a[72:75], a[220:223], v[76:79], a[72:75]\n\t" \
  "s_waitcnt lgkmcnt(4)\n\t" \
  "v_xor_b32 v248, v231, v229\n\t" \
  "v_lshlrev_b32 v248, 4, v248\n\t" \
  "v_mfma_f32_16x16x32_bf16 a[80:83], a[192:195], v[48:51], a[80:83]\n\t" \
  "v_lshl_add_u32 v248, v229, 11, v248\n\t" \
  "v_xor_b32 v249, 64, v248\n\t" \
  "v_mfma_f32_16x16x32_bf16 a[80:83], a[196:199], v[52:55], a[80:83]\n\t" \
  "s_waitcnt lgkmcnt(0)\n\t" \
  "v_xor_b32 v235, 0xc0, v234\n\t" \
  "v_xor_b32 v250, 0x80, v248\n\t" \
  "v_mfma_f32_16x16x32_bf16 a[80:83], a[200:203], v[56:59], a[80:83]\n\t" \
  "buffer_load_dwordx4 v239, %[wo], s71 offen offset:3072 lds\n\t" \
  "s_add_u32 m0, %[m], 131072\n\t" \
  "v_mfma_f32_16x16x32_bf16 a[80:83], a[204:207], v[60:63], a[80:83]\n\t" \
  "s_xor_b32 s71, %[wob], 1835008\n\t" \
  "ds_read_b64_tr_b16 a[208:209], v235 offset:0\n\t" \
  "v_xor_b32 v251, 0xc0, v248\n\t" \
  "v_mfma_f32_16x16x32_bf16 a[84:87], a[204:207], v[92:95], a[84:87]\n\t" \
  "ds_read_b64_tr_b16 a[210:211], v235 offset:4096\n\t" \
  "v_lshrrev_b32 v227, 5, v244\n\t" \
  "v_mfma_f32_16x16x32_bf16 a[84:87], a[200:203], v[88:91], a[84:87]\n\t" \
  "s_lshl_b32 s75, %[wid], 2\n\t" \
  "ds_read_b64_tr_b16 a[212:213], v235 offset:8192\n\t" \
  "v_add_u32 v227, s75, v227\n\t" \
  "v_mfma_f32_16x16x32_bf16 a[84:87], a[196:199], v[84:87], a[84:87]\n\t" \
  "buffer_load_dwordx4 v240, %[wo], s71 offen offset:0 lds\n\t" \
  "v_mfma_f32_16x16x32_bf16 a[84:87], a[192:195], v[80:83], a[84:87]\n\t" \
  "ds_read_b64_tr_b16 a[214:215], v235 offset:12288\n\t" \
  "ds_read_b64_tr_b16 a[216:217], v235 offset:16384\n\t" \
  "v_mfma_f32_16x16x32_bf16 a[88:91], a[192:195], v[64:67], a[88:91]\n\t" \
  "ds_read_b64_tr_b16 a[218:219], v235 offset:20480\n\t" \
  "v_xor_b32 v227, v227, v231\n\t" \
  "v_mfma_f32_16x16x32_bf16 a[88:91], a[196:199], v[68:71], a[88:91]\n\t" \
  "ds_read_b64_tr_b16 a[220:221], v235 offset:24576\n\t" \
  "v_lshlrev_b32 v227, 4, v227\n\t" \
  "v_mfma_f32_16x16x32_bf16 a[88:91], a[200:203], v[72:75], a[88:91]\n\t" \
  "ds_read_b64_tr_b16 a[222:223], v235 offset:28672\n\t" \
  "v_lshl_add_u32 v227, v231, 8, v227\n\t" \
  "v_mfma_f32_16x16x32_bf16 a[88:91], a[204:207], v[76:79], a[88:91]\n\t" \
  "s_waitcnt lgkmcnt(4)\n\t" \
  "buffer_load_dwordx4 v241, %[wo], s71 offen offset:1024 lds\n\t" \
  "v_mfma_f32_16x16x32_bf16 a[96:99], a[208:211], v[48:51], a[96:99]\n\t" \
  "v_xor_b32 v236, 0xe0, v234\n\t" \
  "v_and_b32 v229, 1, v229\n\t" \
  "v_mfma_f32_16x16x32_bf16 a[96:99], a[212:215], v[52:55], a[96:99]\n\t" \
  "s_waitcnt lgkmcnt(0)\n\t" \
  "ds_read_b64_tr_b16 a[192:193], v236 offset:0\n\t" \
  "v_lshl_add_u32 v227, v229, 3, v227\n\t" \
  "v_mfma_f32_16x16x32_bf16 a[96:99], a[216:219], v[56:59], a[96:99]\n\t" \
  "ds_read_b64_tr_b16 a[194:195], v236 offset:4096\n\t" \
  "v_add_u32 v231, 131072, v227\n\t" \
  "v_mfma_f32_16x16x32_bf16 a[96:99], a[220:223], v[60:63], a[96:99]\n\t" \
  "buffer_load_dwordx4 v238, %[wo], s71 offen offset:2048 lds\n\t" \
  "v_mfma_f32_16x16x32_bf16 a[100:103], a[220:223], v[92:95], a[100:103]\n\t" \
  "ds_read_b64_tr_b16 a[196:197], v236 offset:8192\n\t" \
  "v_xor_b32 v233, 32, v231\n\t" \
  "v_mfma_f32_16x16x32_bf16 a[100:103], a[216:219], v[88:91], a[100:103]\n\t" \
  "s_lshl_b32 s75, %[wid], 9\n\t" \
  "ds_read_b64_tr_b16 a[198:199], v236 offset:12288\n\t" \
  "v_lshl_add_u32 v252, v244, 3, s75\n\t" \
  "v_mfma_f32_16x16x32_bf16 a[100:103], a[212:215], v[84:87], a[100:103]\n\t" \
  "ds_read_b64_tr_b16 a[200:201], v236 offset:16384\n\t" \
  "v_mfma_f32_16x16x32_bf16 a[100:103], a[208:211], v[80:83], a[100:103]\n\t" \
  "ds_read_b64_tr_b16 a[202:203], v236 offset:20480\n\t" \
  "v_mfma_f32_16x16x32_bf16 a[104:107], a[208:211], v[64:67], a[104:107]\n\t" \
  "buffer_load_dwordx4 v239, %[wo], s71 offen offset:3072 lds\n\t" \
  "v_mfma_f32_16x16x32_bf16 a[104:107], a[212:215], v[68:71], a[104:107]\n\t" \
  "ds_read_b64_tr_b16 a[204:205], v236 offset:24576\n\t" \
  "v_mfma_f32_16x16x32_bf16 a[104:107], a[216:219], v[72:75], a[104:107]\n\t" \
  "ds_read_b64_tr_b16 a[206:207], v236 offset:28672\n\t" \
  "v_xor_b32 v237, 0x18000, v234\n\t" \
  "v_mfma_f32_16x16x32_bf16 a[104:107], a[220:223], v[76:79], a[104:107]\n\t" \
  "s_waitcnt lgkmcnt(4)\n\t" \
  "ds_read_b64_tr_b16 v[128:129], v237 offset:0\n\t" \
  "v_mfma_f32_16x16x32_bf16 a[112:115], a[192:195], v[48:51], a[112:115]\n\t" \
  "ds_read_b64_tr_b16 v[130:131], v237 offset:4096\n\t" \
  "v_mfma_f32_16x16x32_bf16 a[112:115], a[196:199], v[52:55], a[112:115]\n\t" \
  "s_waitcnt lgkmcnt(2)\n\t" \
  "ds_read_b64_tr_b16 v[132:133], v237 offset:8192\n\t" \
  "v_mfma_f32_16x16x32_bf16 a[112:115], a[200:203], v[56:59], a[112:115]\n\t" \
  "ds_read_b64_tr_b16 v[134:135], v237 offset:12288\n\t" \
  "v_xor_b32 v235, 0x8000, v234\n\t" \
  "v_mfma_f32_16x16x32_bf16 a[112:115], a[204:207], v[60:63], a[112:115]\n\t" \
  "ds_read_b64_tr_b16 v[136:137], v235 offset:0\n\t" \
  "v_mfma_f32_16x16x32_bf16 a[116:119], a[204:207], v[92:95], a[116:119]\n\t" \
  "ds_read_b64_tr_b16 v[138:139], v235 offset:4096\n\t" \
  "v_mfma_f32_16x16x32_bf16 a[116:119], a[200:203], v[88:91], a[116:119]\n\t" \
  "ds_read_b64_tr_b16 v[140:141], v235 offset:8192\n\t" \
  "v_mfma_f32_16x16x32_bf16 a[116:119], a[196:199], v[84:87], a[116:119]\n\t" \
  "ds_read_b64_tr_b16 v[142:143], v235 offset:12288\n\t" \
  "v_mfma_f32_16x16x32_bf16 a[116:119], a[192:195], v[80:83], a[116:119]\n\t" \
  "s_xor_b32 s73, %[eso], 786432\n\t" \
  "v_mfma_f32_16x16x32_bf16 a[120:123], a[192:195], v[64:67], a[120:123]\n\t" \
  "v_xor_b32 v236, 0x18020, v234\n\t" \
  "v_mfma_f32_16x16x32_bf16 a[120:123], a[196:199], v[68:71], a[120:123]\n\t" \
  "ds_read_b64_tr_b16 v[144:145], v236 offset:0\n\t" \
  "v_mfma_f32_16x16x32_bf16 a[120:123], a[200:203], v[72:75], a[120:123]\n\t" \
  "ds_read_b64_tr_b16 v[146:147], v236 offset:4096\n\t" \
  "v_mfma_f32_16x16x32_bf16 a[120:123], a[204:207], v[76:79], a[120:123]\n\t" \
  "s_waitcnt vmcnt(16) lgkmcnt(6)\n\t" \
  "buffer_load_dwordx4 v[48:51], v242, %[ws], s73 offen offset:0 sc1\n\t" \
  "v_mfma_f32_16x16x32_bf16 a[0:3], v[128:131], v[16:19], a[0:3]\n\t" \
  "ds_read_b64_tr_b16 v[148:149], v236 offset:8192\n\t" \
  "v_mfma_f32_16x16x32_bf16 a[0:3], v[132:135], v[20:23], a[0:3]\n\t" \
  "s_waitcnt lgkmcnt(3)\n\t" \
  "buffer_load_dwordx4 v[52:55], v242, %[ws], s73 offen offset:1024 sc1\n\t" \
  "v_mfma_f32_16x16x32_bf16 a[0:3], v[136:139], v[24:27], a[0:3]\n\t" \
  "ds_read_b64_tr_b16 v[150:151], v236 offset:12288\n\t" \
  "v_xor_b32 v237, 0x8020, v234\n\t" \
  "v_mfma_f32_16x16x32_bf16 a[0:3], v[140:143], v[28:31], a[0:3]\n\t" \
  "s_waitcnt vmcnt(14)\n\t" \
  "ds_read_b64_tr_b16 v[152:153], v237 offset:0\n\t" \
  "v_mfma_f32_16x16x32_bf16 a[4:7], v[140:143], v[12:15], a[4:7]\n\t" \
  "ds_read_b64_tr_b16 v[154:155], v237 offset:4096\n\t" \
  "v_mfma_f32_16x16x32_bf16 a[4:7], v[136:139], v[8:11], a[4:7]\n\t" \
  "buffer_load_dwordx4 v[56:59], v242, %[ws], s73 offen offset:2048 sc1\n\t" \
  "v_mfma_f32_16x16x32_bf16 a[4:7], v[132:135], v[4:7], a[4:7]\n\t" \
  "ds_read_b64_tr_b16 v[156:157], v237 offset:8192\n\t" \
  "v_mfma_f32_16x16x32_bf16 a[4:7], v[128:131], v[0:3], a[4:7]\n\t" \
  "s_waitcnt vmcnt(11)\n\t" \
  "ds_read_b64_tr_b16 v[158:159], v237 offset:12288\n\t" \
  "v_mfma_f32_16x16x32_bf16 a[8:11], v[128:131], v[32:35], a[8:11]\n\t" \
  "v_xor_b32 v235, 0x18040, v234\n\t" \
  "v_mfma_f32_16x16x32_bf16 a[8:11], v[132:135], v[36:39], a[8:11]\n\t" \
  "buffer_load_dwordx4 v[60:63], v242, %[ws], s73 offen offset:3072 sc1\n\t" \
  "s_xor_b32 s73, %[eso], 819200\n\t" \
  "v_mfma_f32_16x16x32_bf16 a[8:11], v[136:139], v[40:43], a[8:11]\n\t" \
  "ds_read_b64_tr_b16 a[208:209], v235 offset:0\n\t" \
  "v_mfma_f32_16x16x32_bf16 a[8:11], v[140:143], v[44:47], a[8:11]\n\t" \
  "s_waitcnt lgkmcnt(5)\n\t" \
  "ds_read_b64_tr_b16 a[210:211], v235 offset:4096\n\t" \
  "buffer_load_dword v246, v245, %[ws], %[fw2] offen sc1\n\t" \
  "v_mfma_f32_16x16x32_bf16 a[16:19], v[144:147], v[16:19], a[16:19]\n\t" \
  "ds_read_b64_tr_b16 a[212:213], v235 offset:8192\n\t" \
  "v_mfma_f32_16x16x32_bf16 a[16:19], v[148:151], v[20:23], a[16:19]\n\t" \
  "s_waitcnt lgkmcnt(3)\n\t" \
  "ds_read_b64_tr_b16 a[214:215], v235 offset:12288\n\t" \
  "v_xor_b32 v236, 0x8040, v234\n\t" \
  "v_mfma_f32_16x16x32_bf16 a[16:19], v[152:155], v[24:27], a[16:19]\n\t" \
  "ds_read_b64_tr_b16 a[216:217], v236 offset:0\n\t" \
  "v_mfma_f32_16x16x32_bf16 a[16:19], v[156:159], v[28:31], a[16:19]\n\t" \
  "buffer_load_dwordx4 v[80:83], v242, %[ws], s73 offen offset:0 sc1\n\t" \
  "v_mfma_f32_16x16x32_bf16 a[20:23], v[156:159], v[12:15], a[20:23]\n\t" \
  "ds_read_b64_tr_b16 a[218:219], v236 offset:4096\n\t" \
  "v_mfma_f32_16x16x32_bf16 a[20:23], v[152:155], v[8:11], a[20:23]\n\t" \
  "ds_read_b64_tr_b16 a[220:221], v236 offset:8192\n\t" \
  "v_mfma_f32_16x16x32_bf16 a[20:23], v[148:151], v[4:7], a[20:23]\n\t" \
  "ds_read_b64_tr_b16 a[222:223], v236 offset:12288\n\t" \
  "v_mfma_f32_16x16x32_bf16 a[20:23], v[144:147], v[0:3], a[20:23]\n\t" \
  "buffer_load_dwordx4 v[84:87], v242, %[ws], s73 offen offset:1024 sc1\n\t" \
  "v_mfma_f32_16x16x32_bf16 a[24:27], v[144:147], v[32:35], a[24:27]\n\t" \
  "v_xor_b32 v237, 0x18060, v234\n\t" \
  "v_mfma_f32_16x16x32_bf16 a[24:27], v[148:151], v[36:39], a[24:27]\n\t" \
  "ds_read_b64_tr_b16 a[192:193], v237 offset:0\n\t" \
  "v_mfma_f32_16x16x32_bf16 a[24:27], v[152:155], v[40:43], a[24:27]\n\t" \
  "ds_read_b64_tr_b16 a[194:195], v237 offset:4096\n\t" \
  "v_mfma_f32_16x16x32_bf16 a[24:27], v[156:159], v[44:47], a[24:27]\n\t" \
  "s_waitcnt lgkmcnt(6)\n\t" \
  "ds_read_b64_tr_b16 a[196:197], v237 offset:8192\n\t" \
  "v_mfma_f32_16x16x32_bf16 a[32:35], a[208:211], v[16:19], a[32:35]\n\t" \
  "buffer_load_dwordx4 v[88:91], v242, %[ws], s73 offen offset:2048 sc1\n\t" \
  "v_mfma_f32_16x16x32_bf16 a[32:35], a[212:215], v[20:23], a[32:35]\n\t" \
  "s_waitcnt lgkmcnt(3)\n\t" \
  "ds_read_b64_tr_b16 a[198:199], v237 offset:12288\n\t" \
  "v_xor_b32 v235, 0x8060, v234\n\t" \
  "v_mfma_f32_16x16x32_bf16 a[32:35], a[216:219], v[24:27], a[32:35]\n\t" \
  "ds_read_b64_tr_b16 a[200:201], v235 offset:0\n\t" \
  "v_mfma_f32_16x16x32_bf16 a[32:35], a[220:223], v[28:31], a[32:35]\n\t" \
  "ds_read_b64_tr_b16 a[202:203], v235 offset:4096\n\t" \
  "v_mfma_f32_16x16x32_bf16 a[36:39], a[220:223], v[12:15], a[36:39]\n\t" \
  "buffer_load_dwordx4 v[92:95], v242, %[ws], s73 offen offset:3072 sc1\n\t" \
  "s_xor_b32 s73, %[eso], 851968\n\t" \
  "v_mfma_f32_16x16x32_bf16 a[36:39], a[216:219], v[8:11], a[36:39]\n\t" \
  "ds_read_b64_tr_b16 a[204:205], v235 offset:8192\n\t" \
  "v_mfma_f32_16x16x32_bf16 a[36:39], a[212:215], v[4:7], a[36:39]\n\t" \
  "ds_read_b64_tr_b16 a[206:207], v235 offset:12288\n\t" \
  "v_mfma_f32_16x16x32_bf16 a[36:39], a[208:211], v[0:3], a[36:39]\n\t" \
  "v_mfma_f32_16x16x32_bf16 a[40:43], a[208:211], v[32:35], a[40:43]\n\t" \
  "buffer_load_dwordx4 v[64:67], v242, %[ws], s73 offen offset:0 sc1\n\t" \
  "v_mfma_f32_16x16x32_bf16 a[40:43], a[212:215], v[36:39], a[40:43]\n\t" \
  "v_mfma_f32_16x16x32_bf16 a[40:43], a[216:219], v[40:43], a[40:43]\n\t" \
  "v_mfma_f32_16x16x32_bf16 a[40:43], a[220:223], v[44:47], a[40:43]\n\t" \
  "s_waitcnt lgkmcnt(4)\n\t" \
  "v_xor_b32 v236, 0x18080, v234\n\t" \
  "v_mfma_f32_16x16x32_bf16 a[48:51], a[192:195], v[16:19], a[48:51]\n\t" \
  "s_add_u32 m0, %[m], 0\n\t" \
  "ds_read_b64_tr_b16 a[208:209], v236 offset:0\n\t" \
  "v_mfma_f32_16x16x32_bf16 a[48:51], a[196:199], v[20:23], a[48:51]\n\t" \
  "s_waitcnt lgkmcnt(1)\n\t" \
  "buffer_load_dwordx4 v[68:71], v242, %[ws], s73 offen offset:1024 sc1\n\t" \
  "v_mfma_f32_16x16x32_bf16 a[48:51], a[200:203], v[24:27], a[48:51]\n\t" \
  "ds_read_b64_tr_b16 a[210:211], v236 offset:4096\n\t" \
  "v_mfma_f32_16x16x32_bf16 a[48:51], a[204:207], v[28:31], a[48:51]\n\t" \
  "s_waitcnt vmcnt(15)\n\t" \
  "s_barrier\n\t" \
  "v_mfma_f32_16x16x32_bf16 a[52:55], a[204:207], v[12:15], a[52:55]\n\t" \
  "s_xor_b32 s71, %[wob], 1966080\n\t" \
  "ds_read_b64_tr_b16 a[212:213], v236 offset:8192\n\t" \
  "v_mfma_f32_16x16x32_bf16 a[52:55], a[200:203], v[8:11], a[52:55]\n\t" \
  "buffer_load_dwordx4 v[72:75], v242, %[ws], s73 offen offset:2048 sc1\n\t" \
  "v_mfma_f32_16x16x32_bf16 a[52:55], a[196:199], v[4:7], a[52:55]\n\t" \
  "ds_read_b64_tr_b16 a[214:215], v236 offset:12288\n\t" \
  "v_xor_b32 v237, 0x8080, v234\n\t" \
  "v_mfma_f32_16x16x32_bf16 a[52:55], a[192:195], v[0:3], a[52:55]\n\t" \
  "ds_read_b64_tr_b16 a[216:217], v237 offset:0\n\t" \
  "v_mfma_f32_16x16x32_bf16 a[56:59], a[192:195], v[32:35], a[56:59]\n\t" \
  "ds_read_b64_tr_b16 a[218:219], v237 offset:4096\n\t" \
  "v_mfma_f32_16x16x32_bf16 a[56:59], a[196:199], v[36:39], a[56:59]\n\t" \
  "ds_read_b64_tr_b16 a[220:221], v237 offset:8192\n\t" \
  "v_mfma_f32_16x16x32_bf16 a[56:59], a[200:203], v[40:43], a[56:59]\n\t" \
  "buffer_load_dwordx4 v[76:79], v242, %[ws], s73 offen offset:3072 sc1\n\t" \
  "ds_read_b64_tr_b16 a[222:223], v237 offset:12288\n\t" \
  "v_mfma_f32_16x16x32_bf16 a[56:59], a[204:207], v[44:47], a[56:59]\n\t" \
  "s_waitcnt lgkmcnt(4)\n\t" \
  "v_xor_b32 v235, 0x180a0, v234\n\t" \
  "v_mfma_f32_16x16x32_bf16 a[64:67], a[208:211], v[16:19], a[64:67]\n\t" \
  "s_add_u32 s75, s69, 0\n\t" \
  "ds_read_b64_tr_b16 a[192:193], v235 offset:0\n\t" \
  "v_mfma_f32_16x16x32_bf16 a[64:67], a[212:215], v[20:23], a[64:67]\n\t" \
  "s_waitcnt lgkmcnt(1)\n\t" \
  "s_waitcnt vmcnt(8)\n\t" \
  "v_cmp_ne_u32 vcc, %[tag], v246\n\t" \
  "s_and_b32 vcc_lo, vcc_lo, 0xfe0000\n\t" \
  "s_cbranch_scc1 .Lop_s3\n\t" \
  ".Lop_k3:\n\t" \
  "v_mfma_f32_16x16x32_bf16 a[64:67], a[216:219], v[24:27], a[64:67]\n\t" \
  "buffer_load_dwordx4 v240, %[wo], s71 offen offset:0 lds\n\t" \
  "v_mfma_f32_16x16x32_bf16 a[64:67], a[220:223], v[28:31], a[64:67]\n\t" \
  "ds_read_b64_tr_b16 a[194:195], v235 offset:4096\n\t" \
  "v_mfma_f32_16x16x32_bf16 a[68:71], a[220:223], v[12:15], a[68:71]\n\t" \
  "ds_read_b64_tr_b16 a[196:197], v235 offset:8192\n\t" \
  "v_mfma_f32_16x16x32_bf16 a[68:71], a[216:219], v[8:11], a[68:71]\n\t" \
  "ds_read_b64_tr_b16 a[198:199], v235 offset:12288\n\t" \
  "v_xor_b32 v236, 0x80a0, v234\n\t" \
  "v_mfma_f32_16x16x32_bf16 a[68:71], a[212:215], v[4:7], a[68:71]\n\t" \
  "buffer_load_dwordx4 v[208:211], v170, %[ws], s75 offen sc1\n\t" \
  "v_mfma_f32_16x16x32_bf16 a[68:71], a[208:211], v[0:3], a[68:71]\n\t" \
  "s_add_u32 s75, s69, 32768\n\t" \
  "ds_read_b64_tr_b16 a[200:201], v236 offset:0\n\t" \
  "v_mfma_f32_16x16x32_bf16 a[72:75], a[208:211], v[32:35], a[72:75]\n\t" \
  "ds_read_b64_tr_b16 a[202:203], v236 offset:4096\n\t" \
  "v_mfma_f32_16x16x32_bf16 a[72:75], a[212:215], v[36:39], a[72:75]\n\t" \
  "ds_read_b64_tr_b16 a[204:205], v236 offset:8192\n\t" \
  "v_mfma_f32_16x16x32_bf16 a[72:75], a[216:219], v[40:43], a[72:75]\n\t" \
  "ds_read_b64_tr_b16 a[206:207], v236 offset:12288\n\t" \
  "v_mfma_f32_16x16x32_bf16 a[72:75], a[220:223], v[44:47], a[72:75]\n\t" \
  "s_waitcnt lgkmcnt(4)\n\t" \
  "buffer_load_dwordx4 v[212:215], v170, %[ws], s75 offen sc1\n\t" \
  "v_mfma_f32_16x16x32_bf16 a[80:83], a[192:195], v[16:19], a[80:83]\n\t" \
  "s_add_u32 s75, s69, 65536\n\t" \
  "v_xor_b32 v237, 0x180c0, v234\n\t" \
  "v_mfma_f32_16x16x32_bf16 a[80:83], a[196:199], v[20:23], a[80:83]\n\t" \
  "s_waitcnt lgkmcnt(0)\n\t" \
  "ds_read_b64_tr_b16 a[208:209], v237 offset:0\n\t" \
  "v_mfma_f32_16x16x32_bf16 a[80:83], a[200:203], v[24:27], a[80:83]\n\t" \
  "ds_read_b64_tr_b16 a[210:211], v237 offset:4096\n\t" \
  "v_mfma_f32_16x16x32_bf16 a[80:83], a[204:207], v[28:31], a[80:83]\n\t" \
  "buffer_load_dwordx4 v[216:219], v170, %[ws], s75 offen sc1\n\t" \
  "v_mfma_f32_16x16x32_bf16 a[84:87], a[204:207], v[12:15], a[84:87]\n\t" \
  "s_add_u32 s75, s69, 98304\n\t" \
  "ds_read_b64_tr_b16 a[212:213], v237 offset:8192\n\t" \
  "v_mfma_f32_16x16x32_bf16 a[84:87], a[200:203], v[8:11], a[84:87]\n\t" \
  "ds_read_b64_tr_b16 a[214:215], v237 offset:12288\n\t" \
  "v_xor_b32 v235, 0x80c0, v234\n\t" \
  "v_mfma_f32_16x16x32_bf16 a[84:87], a[196:199], v[4:7], a[84:87]\n\t" \
  "ds_read_b64_tr_b16 a[216:217], v235 offset:0\n\t" \
  "v_mfma_f32_16x16x32_bf16 a[84:87], a[192:195], v[0:3], a[84:87]\n\t" \
  "ds_read_b64_tr_b16 a[218:219], v235 offset:4096\n\t" \
  "v_mfma_f32_16x16x32_bf16 a[88:91], a[192:195], v[32:35], a[88:91]\n\t" \
  "buffer_load_dwordx4 v[220:223], v170, %[ws], s75 offen sc1\n\t" \
  "v_mfma_f32_16x16x32_bf16 a[88:91], a[196:199], v[36:39], a[88:91]\n\t" \
  "ds_read_b64_tr_b16 a[220:221], v235 offset:8192\n\t" \
  "v_mfma_f32_16x16x32_bf16 a[88:91], a[200:203], v[40:43], a[88:91]\n\t" \
  "ds_read_b64_tr_b16 a[222:223], v235 offset:12288\n\t" \
  "v_mfma_f32_16x16x32_bf16 a[88:91], a[204:207], v[44:47], a[88:91]\n\t" \
  "s_waitcnt lgkmcnt(4)\n\t" \
  "v_xor_b32 v236, 0x180e0, v234\n\t" \
  "v_mfma_f32_16x16x32_bf16 a[96:99], a[208:211], v[16:19], a[96:99]\n\t" \
  "buffer_load_dwordx4 v241, %[wo], s71 offen offset:1024 lds\n\t" \
  "v_mfma_f32_16x16x32_bf16 a[96:99], a[212:215], v[20:23], a[96:99]\n\t" \
  "s_waitcnt lgkmcnt(0)\n\t" \
  "ds_read_b64_tr_b16 a[192:193], v236 offset:0\n\t" \
  "v_mfma_f32_16x16x32_bf16 a[96:99], a[216:219], v[24:27], a[96:99]\n\t" \
  "ds_read_b64_tr_b16 a[194:195], v236 offset:4096\n\t" \
  "v_mfma_f32_16x16x32_bf16 a[96:99], a[220:223], v[28:31], a[96:99]\n\t" \
  "ds_read_b64_tr_b16 a[196:197], v236 offset:8192\n\t" \
  "v_mfma_f32_16x16x32_bf16 a[100:103], a[220:223], v[12:15], a[100:103]\n\t" \
  "buffer_load_dwordx4 v238, %[wo], s71 offen offset:2048 lds\n\t" \
  "v_mfma_f32_16x16x32_bf16 a[100:103], a[216:219], v[8:11], a[100:103]\n\t" \
  "ds_read_b64_tr_b16 a[198:199], v236 offset:12288\n\t" \
  "v_xor_b32 v237, 0x80e0, v234\n\t" \
  "v_mfma_f32_16x16x32_bf16 a[100:103], a[212:215], v[4:7], a[100:103]\n\t" \
  "ds_read_b64_tr_b16 a[200:201], v237 offset:0\n\t" \
  "v_mfma_f32_16x16x32_bf16 a[100:103], a[208:211], v[0:3], a[100:103]\n\t" \
  "ds_read_b64_tr_b16 a[202:203], v237 offset:4096\n\t" \
  "v_mfma_f32_16x16x32_bf16 a[104:107], a[208:211], v[32:35], a[104:107]\n\t" \
  "ds_read_b64_tr_b16 a[204:205], v237 offset:8192\n\t" \
  "v_mfma_f32_16x16x32_bf16 a[104:107], a[212:215], v[36:39], a[104:107]\n\t" \
  "buffer_load_dwordx4 v239, %[wo], s71 offen offset:3072 lds\n\t" \
  "s_add_u32 m0, %[m], 16384\n\t" \
  "buffer_load_dword v224, v171, %[sr], s77 offen\n\t" \
  "v_mfma_f32_16x16x32_bf16 a[104:107], a[216:219], v[40:43], a[104:107]\n\t" \
  "s_xor_b32 s71, %[eso], 1015808\n\t" \
  "ds_read_b64_tr_b16 a[206:207], v237 offset:12288\n\t" \
  "v_mfma_f32_16x16x32_bf16 a[104:107], a[220:223], v[44:47], a[104:107]\n\t" \
  "s_waitcnt lgkmcnt(4)\n\t" \
  "v_xor_b32 v235, 0x14000, v234\n\t" \
  "v_mfma_f32_16x16x32_bf16 a[112:115], a[192:195], v[16:19], a[112:115]\n\t" \
  "ds_read_b64_tr_b16 a[208:209], v235 offset:0\n\t" \
  "v_mfma_f32_16x16x32_bf16 a[112:115], a[196:199], v[20:23], a[112:115]\n\t" \
  "s_waitcnt lgkmcnt(1)\n\t" \
  "ds_read_b64_tr_b16 a[210:211], v235 offset:4096\n\t" \
  "v_mfma_f32_16x16x32_bf16 a[112:115], a[200:203], v[24:27], a[112:115]\n\t" \
  "ds_read_b64_tr_b16 a[212:213], v235 offset:8192\n\t" \
  "v_mfma_f32_16x16x32_bf16 a[112:115], a[204:207], v[28:31], a[112:115]\n\t" \
  "buffer_load_dwordx4 v242, %[ws], s71 offen offset:0 sc1 lds\n\t" \
  "v_mfma_f32_16x16x32_bf16 a[116:119], a[204:207], v[12:15], a[116:119]\n\t" \
  "ds_read_b64_tr_b16 a[214:215], v235 offset:12288\n\t" \
  "v_mfma_f32_16x16x32_bf16 a[116:119], a[200:203], v[8:11], a[116:119]\n\t" \
  "ds_read_b64_tr_b16 a[216:217], v235 offset:32768\n\t" \
  "v_mfma_f32_16x16x32_bf16 a[116:119], a[196:199], v[4:7], a[116:119]\n\t" \
  "ds_read_b64_tr_b16 a[218:219], v235 offset:36864\n\t" \
  "v_mfma_f32_16x16x32_bf16 a[116:119], a[192:195], v[0:3], a[116:119]\n\t" \
  "buffer_load_dwordx4 v242, %[ws], s71 offen offset:1024 sc1 lds\n\t" \
  "v_mfma_f32_16x16x32_bf16 a[120:123], a[192:195], v[32:35], a[120:123]\n\t" \
  "s_xor_b32 s73, %[eso], 917504\n\t" \
  "ds_read_b64_tr_b16 a[220:221], v235 offset:40960\n\t" \
  "v_mfma_f32_16x16x32_bf16 a[120:123], a[196:199], v[36:39], a[120:123]\n\t" \
  "ds_read_b64_tr_b16 a[222:223], v235 offset:45056\n\t" \
  "v_mfma_f32_16x16x32_bf16 a[120:123], a[200:203], v[40:43], a[120:123]\n\t" \
  "v_mfma_f32_16x16x32_bf16 a[120:123], a[204:207], v[44:47], a[120:123]\n\t" \
  "s_waitcnt lgkmcnt(4)\n\t" \
  "buffer_load_dwordx4 v[16:19], v242, %[ws], s73 offen offset:0 sc1\n\t" \
  STP("v_writelane_b32 %[stv], s94, 30\n\t") \
  STP("v_writelane_b32 %[stv], s92, 31\n\t") \
  STP("s_memrealtime s[94:95]\n\t") \
  STP("s_memtime s[92:93]\n\t") \
  "v_mfma_f32_16x16x32_bf16 a[0:3], a[208:211], v[48:51], a[0:3]\n\t" \
  "v_xor_b32 v236, 0x14020, v234\n\t" \
  "v_mfma_f32_16x16x32_bf16 a[0:3], a[212:215], v[52:55], a[0:3]\n\t" \
  "s_waitcnt lgkmcnt(0)\n\t" \
  "ds_read_b64_tr_b16 a[192:193], v236 offset:0\n\t" \
  "v_mfma_f32_16x16x32_bf16 a[0:3], a[216:219], v[56:59], a[0:3]\n\t" \
  "ds_read_b64_tr_b16 a[194:195], v236 offset:4096\n\t" \
  "v_mfma_f32_16x16x32_bf16 a[0:3], a[220:223], v[60:63], a[0:3]\n\t" \
  "s_waitcnt vmcnt(16)\n\t" \
  "ds_read_b64_tr_b16 a[196:197], v236 offset:8192\n\t" \
  "v_mfma_f32_16x16x32_bf16 a[4:7], a[220:223], v[92:95], a[4:7]\n\t" \
  "buffer_load_dwordx4 v[20:23], v242, %[ws], s73 offen offset:1024 sc1\n\t" \
  "v_mfma_f32_16x16x32_bf16 a[4:7], a[216:219], v[88:91], a[4:7]\n\t" \
  "ds_read_b64_tr_b16 a[198:199], v236 offset:12288\n\t" \
  "v_mfma_f32_16x16x32_bf16 a[4:7], a[212:215], v[84:87], a[4:7]\n\t" \
  "ds_read_b64_tr_b16 a[200:201], v236 offset:32768\n\t" \
  "v_mfma_f32_16x16x32_bf16 a[4:7], a[208:211], v[80:83], a[4:7]\n\t" \
  "s_waitcnt vmcnt(13)\n\t" \
  "ds_read_b64_tr_b16 a[202:203], v236 offset:36864\n\t" \
  "v_mfma_f32_16x16x32_bf16 a[8:11], a[208:211], v[64:67], a[8:11]\n\t" \
  "buffer_load_dwordx4 v[24:27], v242, %[ws], s73 offen offset:2048 sc1\n\t" \
  "v_mfma_f32_16x16x32_bf16 a[8:11], a[212:215], v[68:71], a[8:11]\n\t" \
  "ds_read_b64_tr_b16 a[204:205], v236 offset:40960\n\t" \
  "v_mfma_f32_16x16x32_bf16 a[8:11], a[216:219], v[72:75], a[8:11]\n\t" \
  "ds_read_b64_tr_b16 a[206:207], v236 offset:45056\n\t" \
  "v_mfma_f32_16x16x32_bf16 a[8:11], a[220:223], v[76:79], a[8:11]\n\t" \
  "s_waitcnt lgkmcnt(4)\n\t" \
  "v_xor_b32 v237, 0x14040, v234\n\t" \
  "v_mfma_f32_16x16x32_bf16 a[16:19], a[192:195], v[48:51], a[16:19]\n\t" \
  "ds_read_b64_tr_b16 a[208:209], v237 offset:0\n\t" \
  "v_mfma_f32_16x16x32_bf16 a[16:19], a[196:199], v[52:55], a[16:19]\n\t" \
  "s_waitcnt lgkmcnt(1)\n\t" \
  "buffer_load_dwordx4 v[28:31], v242, %[ws], s73 offen offset:3072 sc1\n\t" \
  "s_xor_b32 s73, %[eso], 950272\n\t" \
  "v_mfma_f32_16x16x32_bf16 a[16:19], a[200:203], v[56:59], a[16:19]\n\t" \
  "ds_read_b64_tr_b16 a[210:211], v237 offset:4096\n\t" \
  "v_mfma_f32_16x16x32_bf16 a[16:19], a[204:207], v[60:63], a[16:19]\n\t" \
  ".Lhs13:\n\ts_cbranch_execz .Lhs13n\n\t" \
  "ds_read_b64_tr_b16 a[212:213], v237 offset:8192\n\t" \
  "v_mfma_f32_16x16x32_bf16 a[20:23], a[204:207], v[92:95], a[20:23]\n\t" \
  "ds_read_b64_tr_b16 a[214:215], v237 offset:12288\n\t" \
  "v_mfma_f32_16x16x32_bf16 a[20:23], a[200:203], v[88:91], a[20:23]\n\t" \
  "buffer_load_dwordx4 v[0:3], v242, %[ws], s73 offen offset:0 sc1\n\t" \
  "v_mfma_f32_16x16x32_bf16 a[20:23], a[196:199], v[84:87], a[20:23]\n\t" \
  "ds_read_b64_tr_b16 a[216:217], v237 offset:32768\n\t" \
  "v_mfma_f32_16x16x32_bf16 a[20:23], a[192:195], v[80:83], a[20:23]\n\t" \
  "ds_read_b64_tr_b16 a[218:219], v237 offset:36864\n\t" \
  "v_mfma_f32_16x16x32_bf16 a[24:27], a[192:195], v[64:67], a[24:27]\n\t" \
  "ds_read_b64_tr_b16 a[220:221], v237 offset:40960\n\t" \
  "v_mfma_f32_16x16x32_bf16 a[24:27], a[196:199], v[68:71], a[24:27]\n\t" \
  "buffer_load_dwordx4 v[4:7], v242, %[ws], s73 offen offset:1024 sc1\n\t" \
  "v_mfma_f32_16x16x32_bf16 a[24:27], a[200:203], v[72:75], a[24:27]\n\t" \
  "ds_read_b64_tr_b16 a[222:223], v237 offset:45056\n\t" \
  "v_mfma_f32_16x16x32_bf16 a[24:27], a[204:207], v[76:79], a[24:27]\n\t" \
  "s_waitcnt lgkmcnt(4)\n\t" \
  "s_waitcnt vmcnt(8)\n\t" \
  "v_xor_b32 v246, v243, v209\n\t" \
  "v_mfma_f32_16x16x32_bf16 a[32:35], a[208:211], v[48:51], a[32:35]\n\t" \
  "v_bitop3_b32 v246, v246, v211, v243 bitop3:0xf6\n\t" \
  "v_bitop3_b32 v246, v246, v213, v243 bitop3:0xf6\n\t" \
  "v_mfma_f32_16x16x32_bf16 a[32:35], a[212:215], v[52:55], a[32:35]\n\t" \
  "s_waitcnt lgkmcnt(0)\n\t" \
  "v_xor_b32 v235, 0x14060, v234\n\t" \
  "v_bitop3_b32 v246, v246, v215, v243 bitop3:0xf6\n\t" \
  "v_mfma_f32_16x16x32_bf16 a[32:35], a[216:219], v[56:59], a[32:35]\n\t" \
  "buffer_load_dwordx4 v[8:11], v242, %[ws], s73 offen offset:2048 sc1\n\t" \
  "v_mfma_f32_16x16x32_bf16 a[32:35], a[220:223], v[60:63], a[32:35]\n\t" \
  "ds_read_b64_tr_b16 a[192:193], v235 offset:0\n\t" \
  "v_bitop3_b32 v246, v246, v217, v243 bitop3:0xf6\n\t" \
  "v_mfma_f32_16x16x32_bf16 a[36:39], a[220:223], v[92:95], a[36:39]\n\t" \
  "ds_read_b64_tr_b16 a[194:195], v235 offset:4096\n\t" \
  "v_bitop3_b32 v246, v246, v219, v243 bitop3:0xf6\n\t" \
  "v_mfma_f32_16x16x32_bf16 a[36:39], a[216:219], v[88:91], a[36:39]\n\t" \
  "ds_read_b64_tr_b16 a[196:197], v235 offset:8192\n\t" \
  "v_bitop3_b32 v246, v246, v221, v243 bitop3:0xf6\n\t" \
  "v_mfma_f32_16x16x32_bf16 a[36:39], a[212:215], v[84:87], a[36:39]\n\t" \
  "buffer_load_dwordx4 v[12:15], v242, %[ws], s73 offen offset:3072 sc1\n\t" \
  "s_xor_b32 s73, %[eso], 983040\n\t" \
  "v_mfma_f32_16x16x32_bf16 a[36:39], a[208:211], v[80:83], a[36:39]\n\t" \
  "ds_read_b64_tr_b16 a[198:199], v235 offset:12288\n\t" \
  "ds_read_b64_tr_b16 a[200:201], v235 offset:32768\n\t" \
  "v_mfma_f32_16x16x32_bf16 a[40:43], a[208:211], v[64:67], a[40:43]\n\t" \
  "ds_read_b64_tr_b16 a[202:203], v235 offset:36864\n\t" \
  "v_bitop3_b32 v246, v246, v223, v243 bitop3:0xf6\n\t" \
  "v_mfma_f32_16x16x32_bf16 a[40:43], a[212:215], v[68:71], a[40:43]\n\t" \
  "ds_read_b64_tr_b16 a[204:205], v235 offset:40960\n\t" \
  "v_mfma_f32_16x16x32_bf16 a[40:43], a[216:219], v[72:75], a[40:43]\n\t" \
  "ds_read_b64_tr_b16 a[206:207], v235 offset:45056\n\t" \
  "v_mfma_f32_16x16x32_bf16 a[40:43], a[220:223], v[76:79], a[40:43]\n\t" \
  "s_waitcnt lgkmcnt(4)\n\t" \
  "buffer_load_dwordx4 v[32:35], v242, %[ws], s73 offen offset:0 sc1\n\t" \
  "v_mfma_f32_16x16x32_bf16 a[48:51], a[192:195], v[48:51], a[48:51]\n\t" \
  "v_cmp_ne_u32 vcc, 0, v246\n\t" \
  "s_cbranch_vccnz .Lop_s4\n\t" \
  ".Lop_k4:\n\t" \
  "v_mfma_f32_16x16x32_bf16 a[48:51], a[196:199], v[52:55], a[48:51]\n\t" \
  "s_waitcnt lgkmcnt(0)\n\t" \
  "v_add_f32 v208, v208, v212\n\t" \
  "v_add_f32 v216, v216, v220\n\t" \
  "v_mfma_f32_16x16x32_bf16 a[48:51], a[200:203], v[56:59], a[48:51]\n\t" \
  "v_add_f32 v210, v210, v214\n\t" \
  "v_xor_b32 v236, 0x14080, v234\n\t" \
  "v_mfma_f32_16x16x32_bf16 a[48:51], a[204:207], v[60:63], a[48:51]\n\t" \
  "s_barrier\n\t" \
  "v_mfma_f32_16x16x32_bf16 a[52:55], a[204:207], v[92:95], a[52:55]\n\t" \
  "buffer_load_dwordx4 v[36:39], v242, %[ws], s73 offen offset:1024 sc1\n\t" \
  "v_mfma_f32_16x16x32_bf16 a[52:55], a[200:203], v[88:91], a[52:55]\n\t" \
  "ds_read_b64_tr_b16 a[208:209], v236 offset:0\n\t" \
  "v_add_f32 v218, v218, v222\n\t" \
  "v_mfma_f32_16x16x32_bf16 a[52:55], a[196:199], v[84:87], a[52:55]\n\t" \
  "ds_read_b64_tr_b16 a[210:211], v236 offset:4096\n\t" \
  "v_add_f32 v208, v208, v216\n\t" \
  "v_mfma_f32_16x16x32_bf16 a[52:55], a[192:195], v[80:83], a[52:55]\n\t" \
  "buffer_load_dwordx4 v[40:43], v242, %[ws], s73 offen offset:2048 sc1\n\t" \
  "v_mfma_f32_16x16x32_bf16 a[56:59], a[192:195], v[64:67], a[56:59]\n\t" \
  "ds_read_b64_tr_b16 a[212:213], v236 offset:8192\n\t" \
  "v_add_f32 v210, v210, v218\n\t" \
  "v_mfma_f32_16x16x32_bf16 a[56:59], a[196:199], v[68:71], a[56:59]\n\t" \
  "ds_read_b64_tr_b16 a[214:215], v236 offset:12288\n\t" \
  "v_lshlrev_b32 v163, 16, v224\n\t" \
  "v_mfma_f32_16x16x32_bf16 a[56:59], a[200:203], v[72:75], a[56:59]\n\t" \
  "ds_read_b64_tr_b16 a[216:217], v236 offset:32768\n\t" \
  "ds_read_b64_tr_b16 a[218:219], v236 offset:36864\n\t" \
  "ds_read_b64_tr_b16 a[220:221], v236 offset:40960\n\t" \
  "ds_read_b64_tr_b16 a[222:223], v236 offset:45056\n\t" \
  "v_mfma_f32_16x16x32_bf16 a[56:59], a[204:207], v[76:79], a[56:59]\n\t" \
  "s_waitcnt lgkmcnt(4)\n\t" \
  "v_and_b32 v164, 0xffff0000, v224\n\t" \
  "v_cvt_pk_bf16_f32 v162, v208, v210\n\t" \
  "v_mfma_f32_16x16x32_bf16 a[64:67], a[208:211], v[48:51], a[64:67]\n\t" \
  "buffer_load_dwordx4 v[44:47], v242, %[ws], s73 offen offset:3072 sc1\n\t" \
  "v_mfma_f32_16x16x32_bf16 a[64:67], a[212:215], v[52:55], a[64:67]\n\t" \
  "s_waitcnt lgkmcnt(0)\n\t" \
  "v_lshlrev_b32 v160, 16, v162\n\t" \
  "v_and_b32 v161, 0xffff0000, v162\n\t" \
  "v_mfma_f32_16x16x32_bf16 a[64:67], a[216:219], v[56:59], a[64:67]\n\t" \
  "v_add_f32 v160, v160, v172\n\t" \
  "v_add_f32 v161, v161, v172\n\t" \
  "v_mfma_f32_16x16x32_bf16 a[64:67], a[220:223], v[60:63], a[64:67]\n\t" \
  "v_xor_b32 v237, 0x140a0, v234\n\t" \
  "v_cvt_pk_bf16_f32 v162, v160, v161\n\t" \
  "v_mfma_f32_16x16x32_bf16 a[68:71], a[220:223], v[92:95], a[68:71]\n\t" \
  "buffer_load_dwordx4 v242, %[ws], s71 offen offset:2048 sc1 lds\n\t" \
  "v_mfma_f32_16x16x32_bf16 a[68:71], a[216:219], v[88:91], a[68:71]\n\t" \
  "ds_read_b64_tr_b16 a[192:193], v237 offset:0\n\t" \
  "ds_read_b64_tr_b16 a[194:195], v237 offset:4096\n\t" \
  "v_mfma_f32_16x16x32_bf16 a[68:71], a[212:215], v[84:87], a[68:71]\n\t" \
  "ds_read_b64_tr_b16 a[196:197], v237 offset:8192\n\t" \
  "v_lshlrev_b32 v160, 16, v162\n\t" \
  "v_mfma_f32_16x16x32_bf16 a[68:71], a[208:211], v[80:83], a[68:71]\n\t" \
  "ds_read_b64_tr_b16 a[198:199], v237 offset:12288\n\t" \
  "v_and_b32 v161, 0xffff0000, v162\n\t" \
  "v_mfma_f32_16x16x32_bf16 a[72:75], a[208:211], v[64:67], a[72:75]\n\t" \
  "ds_read_b64_tr_b16 a[200:201], v237 offset:32768\n\t" \
  "ds_read_b64_tr_b16 a[202:203], v237 offset:36864\n\t" \
  "v_mfma_f32_16x16x32_bf16 a[72:75], a[212:215], v[68:71], a[72:75]\n\t" \
  "buffer_load_dwordx4 v242, %[ws], s71 offen offset:3072 sc1 lds\n\t" \
  "s_add_u32 m0, %[m], 32768\n\t" \
  "v_mfma_f32_16x16x32_bf16 a[72:75], a[216:219], v[72:75], a[72:75]\n\t" \
  "s_xor_b32 s71, %[eso], 753664\n\t" \
  "ds_write_b64 v174, v[160:161] offset:2560\n\t" \
  "ds_read_b64_tr_b16 a[204:205], v237 offset:40960\n\t" \
  "ds_read_b64_tr_b16 a[206:207], v237 offset:45056\n\t" \
  "v_mfma_f32_16x16x32_bf16 a[72:75], a[220:223], v[76:79], a[72:75]\n\t" \
  "s_waitcnt lgkmcnt(5)\n\t" \
  "v_mul_f32 v166, v160, v163\n\t" \
  "v_mul_f32 v167, v161, v164\n\t" \
  "v_mfma_f32_16x16x32_bf16 a[80:83], a[192:195], v[48:51], a[80:83]\n\t" \
  "v_cvt_pk_bf16_f32 v168, v166, v167\n\t" \
  "v_add_f32 v162, v166, v167\n\t" \
  "v_mfma_f32_16x16x32_bf16 a[80:83], a[196:199], v[52:55], a[80:83]\n\t" \
  "s_waitcnt lgkmcnt(0)\n\t" \
  "buffer_load_dwordx4 v242, %[ws], s71 offen offset:0 sc1 lds\n\t" \
  "v_mfma_f32_16x16x32_bf16 a[80:83], a[200:203], v[56:59], a[80:83]\n\t" \
  "v_lshlrev_b32 v163, 16, v168\n\t" \
  "v_xor_b32 v235, 0x140c0, v234\n\t" \
  "v_mfma_f32_16x16x32_bf16 a[80:83], a[204:207], v[60:63], a[80:83]\n\t" \
  "ds_read_b64_tr_b16 a[208:209], v235 offset:0\n\t" \
  "v_and_b32 v164, 0xffff0000, v168\n\t" \
  "v_mfma_f32_16x16x32_bf16 a[84:87], a[204:207], v[92:95], a[84:87]\n\t" \
  "ds_read_b64_tr_b16 a[210:211], v235 offset:4096\n\t" \
  "v_add_f32_dpp v162, v162, v162 row_ror:8 row_mask:0xf bank_mask:0xf\n\t" \
  "v_mfma_f32_16x16x32_bf16 a[84:87], a[200:203], v[88:91], a[84:87]\n\t" \
  "buffer_load_dwordx4 v242, %[ws], s71 offen offset:1024 sc1 lds\n\t" \
  "v_mfma_f32_16x16x32_bf16 a[84:87], a[196:199], v[84:87], a[84:87]\n\t" \
  "ds_read_b64_tr_b16 a[212:213], v235 offset:8192\n\t" \
  "v_sub_f32 v166, v166, v163\n\t" \
  "v_mfma_f32_16x16x32_bf16 a[84:87], a[192:195], v[80:83], a[84:87]\n\t" \
  "ds_read_b64_tr_b16 a[214:215], v235 offset:12288\n\t" \
  "v_sub_f32 v167, v167, v164\n\t" \
  "v_mfma_f32_16x16x32_bf16 a[88:91], a[192:195], v[64:67], a[88:91]\n\t" \
  "ds_read_b64_tr_b16 a[216:217], v235 offset:32768\n\t" \
  "ds_read_b64_tr_b16 a[218:219], v235 offset:36864\n\t" \
  "v_mfma_f32_16x16x32_bf16 a[88:91], a[196:199], v[68:71], a[88:91]\n\t" \
  "ds_read_b64_tr_b16 a[220:221], v235 offset:40960\n\t" \
  "v_add_f32_dpp v162, v162, v162 row_ror:4 row_mask:0xf bank_mask:0xf\n\t" \
  "v_mfma_f32_16x16x32_bf16 a[88:91], a[200:203], v[72:75], a[88:91]\n\t" \
  "buffer_load_dwordx4 v242, %[ws], s71 offen offset:2048 sc1 lds\n\t" \
  "ds_read_b64_tr_b16 a[222:223], v235 offset:45056\n\t" \
  "v_mfma_f32_16x16x32_bf16 a[88:91], a[204:207], v[76:79], a[88:91]\n\t" \
  "s_waitcnt lgkmcnt(4)\n\t" \
  "v_cvt_pk_bf16_f32 v169, v166, v167\n\t" \
  "s_mov_b32 s82, 0\n\t" \
  "v_mfma_f32_16x16x32_bf16 a[96:99], a[208:211], v[48:51], a[96:99]\n\t" \
  "v_add_f32_dpp v162, v162, v162 row_ror:2 row_mask:0xf bank_mask:0xf\n\t" \
  "s_mov_b32 s83, 0xffff\n\t" \
  "v_mfma_f32_16x16x32_bf16 a[96:99], a[212:215], v[52:55], a[96:99]\n\t" \
  "s_waitcnt lgkmcnt(0)\n\t" \
  "v_add_f32_dpp v162, v162, v162 row_ror:1 row_mask:0xf bank_mask:0xf\n\t" \
  "v_xor_b32 v236, 0x140e0, v234\n\t" \
  "v_mfma_f32_16x16x32_bf16 a[96:99], a[216:219], v[56:59], a[96:99]\n\t" \
  "buffer_load_dwordx4 v242, %[ws], s71 offen offset:3072 sc1 lds\n\t" \
  "s_add_u32 m0, %[m], 98304\n\t" \
  "v_mfma_f32_16x16x32_bf16 a[96:99], a[220:223], v[60:63], a[96:99]\n\t" \
  "s_xor_b32 s71, %[eso], 360448\n\t" \
  "ds_read_b64_tr_b16 a[192:193], v236 offset:0\n\t" \
  "v_mfma_f32_16x16x32_bf16 a[100:103], a[220:223], v[92:95], a[100:103]\n\t" \
  "s_mov_b64 exec, s[82:83]\n\t" \
  "ds_write_b32 v173, v168 offset:1040\n\t" \
  "ds_write_b32 v173, v169 offset:1120\n\t" \
  "s_mov_b64 exec, -1\n\t" \
  "v_mfma_f32_16x16x32_bf16 a[100:103], a[216:219], v[88:91], a[100:103]\n\t" \
  "ds_read_b64_tr_b16 a[194:195], v236 offset:4096\n\t" \
  "v_readlane_b32 s87, v162, 32\n\t" \
  "v_mfma_f32_16x16x32_bf16 a[100:103], a[212:215], v[84:87], a[100:103]\n\t" \
  "ds_read_b64_tr_b16 a[196:197], v236 offset:8192\n\t" \
  "ds_read_b64_tr_b16 a[198:199], v236 offset:12288\n\t" \
  "v_mfma_f32_16x16x32_bf16 a[100:103], a[208:211], v[80:83], a[100:103]\n\t" \
  "buffer_load_dwordx4 v242, %[ws], s71 offen offset:0 sc1 lds\n\t" \
  "v_mfma_f32_16x16x32_bf16 a[104:107], a[208:211], v[64:67], a[104:107]\n\t" \
  "ds_read_b64_tr_b16 a[200:201], v236 offset:32768\n\t" \
  "v_readlane_b32 s88, v162, 16\n\t" \
  "v_mfma_f32_16x16x32_bf16 a[104:107], a[212:215], v[68:71], a[104:107]\n\t" \
  "ds_read_b64_tr_b16 a[202:203], v236 offset:36864\n\t" \
  "ds_read_b64_tr_b16 a[204:205], v236 offset:40960\n\t" \
  "v_mfma_f32_16x16x32_bf16 a[104:107], a[216:219], v[72:75], a[104:107]\n\t" \
  "ds_read_b64_tr_b16 a[206:207], v236 offset:45056\n\t" \
  "v_mfma_f32_16x16x32_bf16 a[104:107], a[220:223], v[76:79], a[104:107]\n\t" \
  "s_waitcnt lgkmcnt(4)\n\t" \
  "buffer_load_dwordx4 v242, %[ws], s71 offen offset:1024 sc1 lds\n\t" \
  "v_mfma_f32_16x16x32_bf16 a[112:115], a[192:195], v[48:51], a[112:115]\n\t" \
  "v_xor_b32 v237, 0x20040, v234\n\t" \
  "v_mfma_f32_16x16x32_bf16 a[112:115], a[196:199], v[52:55], a[112:115]\n\t" \
  "s_waitcnt lgkmcnt(0)\n\t" \
  "ds_read_b64_tr_b16 a[208:209], v237 offset:0\n\t" \
  "v_mfma_f32_16x16x32_bf16 a[112:115], a[200:203], v[56:59], a[112:115]\n\t" \
  "ds_read_b64_tr_b16 a[210:211], v237 offset:4096\n\t" \
  "v_mfma_f32_16x16x32_bf16 a[112:115], a[204:207], v[60:63], a[112:115]\n\t" \
  "buffer_load_dwordx4 v242, %[ws], s71 offen offset:2048 sc1 lds\n\t" \
  "v_mfma_f32_16x16x32_bf16 a[116:119], a[204:207], v[92:95], a[116:119]\n\t" \
  "ds_read_b64_tr_b16 a[212:213], v237 offset:8192\n\t" \
  "v_mfma_f32_16x16x32_bf16 a[116:119], a[200:203], v[88:91], a[116:119]\n\t" \
  "ds_read_b64_tr_b16 a[214:215], v237 offset:12288\n\t" \
  "v_xor_b32 v235, 0x40, v234\n\t" \
  "v_mfma_f32_16x16x32_bf16 a[116:119], a[196:199], v[84:87], a[116:119]\n\t" \
  "ds_read_b64_tr_b16 a[216:217], v235 offset:0\n\t" \
  "v_mfma_f32_16x16x32_bf16 a[116:119], a[192:195], v[80:83], a[116:119]\n\t" \
  "s_xor_b32 s73, %[eso], 196608\n\t" \
  "ds_read_b64_tr_b16 a[218:219], v235 offset:4096\n\t" \
  "v_mfma_f32_16x16x32_bf16 a[120:123], a[192:195], v[64:67], a[120:123]\n\t" \
  "buffer_load_dwordx4 v242, %[ws], s71 offen offset:3072 sc1 lds\n\t" \
  "v_mfma_f32_16x16x32_bf16 a[120:123], a[196:199], v[68:71], a[120:123]\n\t" \
  "ds_read_b64_tr_b16 a[220:221], v235 offset:8192\n\t" \
  "v_mfma_f32_16x16x32_bf16 a[120:123], a[200:203], v[72:75], a[120:123]\n\t" \
  "ds_read_b64_tr_b16 a[222:223], v235 offset:12288\n\t" \
  "v_mfma_f32_16x16x32_bf16 a[120:123], a[204:207], v[76:79], a[120:123]\n\t" \
  "s_waitcnt vmcnt(18) lgkmcnt(4)\n\t" \
  "v_xor_b32 v236, 0x20060, v234\n\t" \
  STP("v_writelane_b32 %[stv], s94, 32\n\t") \
  STP("v_writelane_b32 %[stv], s92, 33\n\t") \
  STP("s_memrealtime s[94:95]\n\t") \
  STP("s_memtime s[92:93]\n\t") \
  "v_mfma_f32_16x16x32_bf16 a[32:35], a[208:211], v[16:19], a[32:35]\n\t" \
  "buffer_load_dwordx4 v[48:51], v242, %[ws], s73 offen offset:0 sc1\n\t" \
  "v_mfma_f32_16x16x32_bf16 a[32:35], a[212:215], v[20:23], a[32:35]\n\t" \
  "s_waitcnt lgkmcnt(0)\n\t" \
  "ds_read_b64_tr_b16 a[192:193], v236 offset:0\n\t" \
  "v_mfma_f32_16x16x32_bf16 a[32:35], a[216:219], v[24:27], a[32:35]\n\t" \
  "ds_read_b64_tr_b16 a[194:195], v236 offset:4096\n\t" \
  "v_mfma_f32_16x16x32_bf16 a[32:35], a[220:223], v[28:31], a[32:35]\n\t" \
  "s_waitcnt vmcnt(15)\n\t" \
  "ds_read_b64_tr_b16 a[196:197], v236 offset:8192\n\t" \
  "v_mfma_f32_16x16x32_bf16 a[36:39], a[220:223], v[12:15], a[36:39]\n\t" \
  "ds_read_b64_tr_b16 a[198:199], v236 offset:12288\n\t" \
  "v_xor_b32 v237, 0x60, v234\n\t" \
  "v_mfma_f32_16x16x32_bf16 a[36:39], a[216:219], v[8:11], a[36:39]\n\t" \
  "buffer_load_dwordx4 v[52:55], v242, %[ws], s73 offen offset:1024 sc1\n\t" \
  "v_mfma_f32_16x16x32_bf16 a[36:39], a[212:215], v[4:7], a[36:39]\n\t" \
  "ds_read_b64_tr_b16 a[200:201], v237 offset:0\n\t" \
  "v_mfma_f32_16x16x32_bf16 a[36:39], a[208:211], v[0:3], a[36:39]\n\t" \
  "s_waitcnt vmcnt(12)\n\t" \
  "ds_read_b64_tr_b16 a[202:203], v237 offset:4096\n\t" \
  "v_mfma_f32_16x16x32_bf16 a[40:43], a[208:211], v[32:35], a[40:43]\n\t" \
  "ds_read_b64_tr_b16 a[204:205], v237 offset:8192\n\t" \
  "v_mfma_f32_16x16x32_bf16 a[40:43], a[212:215], v[36:39], a[40:43]\n\t" \
  "buffer_load_dwordx4 v[56:59], v242, %[ws], s73 offen offset:2048 sc1\n\t" \
  "v_mfma_f32_16x16x32_bf16 a[40:43], a[216:219], v[40:43], a[40:43]\n\t" \
  "ds_read_b64_tr_b16 a[206:207], v237 offset:12288\n\t" \
  "v_xor_b32 v235, 0x20000, v234\n\t" \
  "v_mfma_f32_16x16x32_bf16 a[40:43], a[220:223], v[44:47], a[40:43]\n\t" \
  "s_waitcnt lgkmcnt(4)\n\t" \
  "ds_read_b64_tr_b16 v[64:65], v235 offset:0\n\t" \
  "v_mfma_f32_16x16x32_bf16 a[48:51], a[192:195], v[16:19], a[48:51]\n\t" \
  "ds_read_b64_tr_b16 v[66:67], v235 offset:4096\n\t" \
  "v_mfma_f32_16x16x32_bf16 a[48:51], a[196:199], v[20:23], a[48:51]\n\t" \
  "s_waitcnt lgkmcnt(2)\n\t" \
  "buffer_load_dwordx4 v[60:63], v242, %[ws], s73 offen offset:3072 sc1\n\t" \
  "v_mfma_f32_16x16x32_bf16 a[48:51], a[200:203], v[24:27], a[48:51]\n\t" \
  "ds_read_b64_tr_b16 v[68:69], v235 offset:8192\n\t" \
  "v_mfma_f32_16x16x32_bf16 a[48:51], a[204:207], v[28:31], a[48:51]\n\t" \
  "ds_read_b64_tr_b16 v[70:71], v235 offset:12288\n\t" \
  "v_xor_b32 v236, 0x0, v234\n\t" \
  "v_mfma_f32_16x16x32_bf16 a[52:55], a[204:207], v[12:15], a[52:55]\n\t" \
  "ds_read_b64_tr_b16 v[72:73], v236 offset:0\n\t" \
  "v_accvgpr_read_b32 v160, a32\n\t" \
  "v_mfma_f32_16x16x32_bf16 a[52:55], a[200:203], v[8:11], a[52:55]\n\t" \
  "ds_read_b64_tr_b16 v[74:75], v236 offset:4096\n\t" \
  "v_accvgpr_read_b32 v161, a33\n\t" \
  "v_mfma_f32_16x16x32_bf16 a[52:55], a[196:199], v[4:7], a[52:55]\n\t" \
  "ds_read_b64_tr_b16 v[76:77], v236 offset:8192\n\t" \
  "v_accvgpr_read_b32 v162, a34\n\t" \
  "v_mfma_f32_16x16x32_bf16 a[52:55], a[192:195], v[0:3], a[52:55]\n\t" \
  "ds_read_b64_tr_b16 v[78:79], v236 offset:12288\n\t" \
  "v_accvgpr_read_b32 v163, a35\n\t" \
  "v_mfma_f32_16x16x32_bf16 a[56:59], a[192:195], v[32:35], a[56:59]\n\t" \
  "v_xor_b32 v237, 0x20020, v234\n\t" \
  "v_accvgpr_read_b32 v164, a48\n\t" \
  "v_mfma_f32_16x16x32_bf16 a[56:59], a[196:199], v[36:39], a[56:59]\n\t" \
  "ds_read_b64_tr_b16 v[80:81], v237 offset:0\n\t" \
  "v_accvgpr_read_b32 v165, a49\n\t" \
  "v_mfma_f32_16x16x32_bf16 a[56:59], a[200:203], v[40:43], a[56:59]\n\t" \
  "ds_read_b64_tr_b16 v[82:83], v237 offset:4096\n\t" \
  "v_accvgpr_read_b32 v166, a50\n\t" \
  "v_mfma_f32_16x16x32_bf16 a[56:59], a[204:207], v[44:47], a[56:59]\n\t" \
  "s_waitcnt lgkmcnt(6)\n\t" \
  "ds_read_b64_tr_b16 v[84:85], v237 offset:8192\n\t" \
  "v_accvgpr_read_b32 v167, a51\n\t" \
  "v_mfma_f32_16x16x32_bf16 a[0:3], v[64:67], v[16:19], a[0:3]\n\t" \
  "ds_read_b64_tr_b16 v[86:87], v237 offset:12288\n\t" \
  "v_xor_b32 v235, 0x20, v234\n\t" \
  "v_mfma_f32_16x16x32_bf16 a[0:3], v[68:71], v[20:23], a[0:3]\n\t" \
  "s_waitcnt lgkmcnt(4)\n\t" \
  "ds_read_b64_tr_b16 v[88:89], v235 offset:0\n\t" \
  "v_cvt_pk_bf16_f32 v168, v160, v161\n\t" \
  "v_mfma_f32_16x16x32_bf16 a[0:3], v[72:75], v[24:27], a[0:3]\n\t" \
  "ds_read_b64_tr_b16 v[90:91], v235 offset:4096\n\t" \
  "v_cvt_pk_bf16_f32 v169, v162, v163\n\t" \
  "v_mfma_f32_16x16x32_bf16 a[0:3], v[76:79], v[28:31], a[0:3]\n\t" \
  "ds_read_b64_tr_b16 v[92:93], v235 offset:8192\n\t" \
  "v_cvt_pk_bf16_f32 v170, v164, v165\n\t" \
  "v_mfma_f32_16x16x32_bf16 a[4:7], v[76:79], v[12:15], a[4:7]\n\t" \
  "ds_read_b64_tr_b16 v[94:95], v235 offset:12288\n\t" \
  "v_cvt_pk_bf16_f32 v171, v166, v167\n\t" \
  "v_mfma_f32_16x16x32_bf16 a[4:7], v[72:75], v[8:11], a[4:7]\n\t" \
  "s_xor_b32 s76, s60, 1024\n\t" \
  "v_mfma_f32_16x16x32_bf16 a[4:7], v[68:71], v[4:7], a[4:7]\n\t" \
  "buffer_store_dwordx4 v[168:171], v242, %[ws], s76 offen sc1\n\t" \
  "v_mfma_f32_16x16x32_bf16 a[4:7], v[64:67], v[0:3], a[4:7]\n\t" \
  "v_accvgpr_read_b32 v160, a36\n\t" \
  "v_accvgpr_read_b32 v161, a37\n\t" \
  "v_mfma_f32_16x16x32_bf16 a[8:11], v[64:67], v[32:35], a[8:11]\n\t" \
  "v_accvgpr_read_b32 v162, a38\n\t" \
  "v_accvgpr_read_b32 v163, a39\n\t" \
  "v_mfma_f32_16x16x32_bf16 a[8:11], v[68:71], v[36:39], a[8:11]\n\t" \
  "v_accvgpr_read_b32 v164, a52\n\t" \
  "v_accvgpr_read_b32 v165, a53\n\t" \
  "v_mfma_f32_16x16x32_bf16 a[8:11], v[72:75], v[40:43], a[8:11]\n\t" \
  "v_accvgpr_read_b32 v166, a54\n\t" \
  "v_xor_b32 v236, 0x20080, v234\n\t" \
  "v_mfma_f32_16x16x32_bf16 a[8:11], v[76:79], v[44:47], a[8:11]\n\t" \
  "s_waitcnt lgkmcnt(4)\n\t" \
  "ds_read_b64_tr_b16 a[208:209], v236 offset:0\n\t" \
  "v_accvgpr_read_b32 v167, a55\n\t" \
  "v_mfma_f32_16x16x32_bf16 a[16:19], v[80:83], v[16:19], a[16:19]\n\t" \
  "ds_read_b64_tr_b16 a[210:211], v236 offset:4096\n\t" \
  "v_cvt_pk_bf16_f32 v172, v160, v161\n\t" \
  "v_mfma_f32_16x16x32_bf16 a[16:19], v[84:87], v[20:23], a[16:19]\n\t" \
  "s_waitcnt lgkmcnt(2)\n\t" \
  "ds_read_b64_tr_b16 a[212:213], v236 offset:8192\n\t" \
  "v_cvt_pk_bf16_f32 v173, v162, v163\n\t" \
  "v_mfma_f32_16x16x32_bf16 a[16:19], v[88:91], v[24:27], a[16:19]\n\t" \
  "ds_read_b64_tr_b16 a[214:215], v236 offset:12288\n\t" \
  "v_cvt_pk_bf16_f32 v174, v164, v165\n\t" \
  "v_mfma_f32_16x16x32_bf16 a[16:19], v[92:95], v[28:31], a[16:19]\n\t" \
  "s_barrier\n\t" \
  "v_mfma_f32_16x16x32_bf16 a[20:23], v[92:95], v[12:15], a[20:23]\n\t" \
  "v_cvt_pk_bf16_f32 v175, v166, v167\n\t" \
  "s_xor_b32 s76, s61, 1024\n\t" \
  "v_mfma_f32_16x16x32_bf16 a[20:23], v[88:91], v[8:11], a[20:23]\n\t" \
  "buffer_store_dwordx4 v[172:175], v242, %[ws], s76 offen sc1\n\t" \
  "v_mfma_f32_16x16x32_bf16 a[20:23], v[84:87], v[4:7], a[20:23]\n\t" \
  "v_xor_b32 v237, 0x80, v234\n\t" \
  "v_accvgpr_read_b32 v160, a0\n\t" \
  "v_mfma_f32_16x16x32_bf16 a[20:23], v[80:83], v[0:3], a[20:23]\n\t" \
  "ds_read_b64_tr_b16 a[216:217], v237 offset:0\n\t" \
  "ds_read_b64_tr_b16 a[218:219], v237 offset:4096\n\t" \
  "v_mfma_f32_16x16x32_bf16 a[24:27], v[80:83], v[32:35], a[24:27]\n\t" \
  "ds_read_b64_tr_b16 a[220:221], v237 offset:8192\n\t" \
  "ds_read_b64_tr_b16 a[222:223], v237 offset:12288\n\t" \
  "v_mfma_f32_16x16x32_bf16 a[24:27], v[84:87], v[36:39], a[24:27]\n\t" \
  "v_xor_b32 v235, 0x200a0, v234\n\t" \
  "v_accvgpr_read_b32 v161, a1\n\t" \
  "v_mfma_f32_16x16x32_bf16 a[24:27], v[88:91], v[40:43], a[24:27]\n\t" \
  "ds_read_b64_tr_b16 a[192:193], v235 offset:0\n\t" \
  "v_accvgpr_read_b32 v162, a2\n\t" \
  "v_mfma_f32_16x16x32_bf16 a[24:27], v[92:95], v[44:47], a[24:27]\n\t" \
  "s_waitcnt lgkmcnt(5)\n\t" \
  "ds_read_b64_tr_b16 a[194:195], v235 offset:4096\n\t" \
  "v_accvgpr_read_b32 v163, a3\n\t" \
  "v_mfma_f32_16x16x32_bf16 a[64:67], a[208:211], v[16:19], a[64:67]\n\t" \
  "ds_read_b64_tr_b16 a[196:197], v235 offset:8192\n\t" \
  "v_accvgpr_read_b32 v164, a16\n\t" \
  "v_mfma_f32_16x16x32_bf16 a[64:67], a[212:215], v[20:23], a[64:67]\n\t" \
  "s_waitcnt lgkmcnt(3)\n\t" \
  "ds_read_b64_tr_b16 a[198:199], v235 offset:12288\n\t" \
  "v_accvgpr_read_b32 v165, a17\n\t" \
  "v_mfma_f32_16x16x32_bf16 a[64:67], a[216:219], v[24:27], a[64:67]\n\t" \
  "v_accvgpr_read_b32 v166, a18\n\t" \
  "v_xor_b32 v236, 0xa0, v234\n\t" \
  "v_mfma_f32_16x16x32_bf16 a[64:67], a[220:223], v[28:31], a[64:67]\n\t" \
  "ds_read_b64_tr_b16 a[200:201], v236 offset:0\n\t" \
  "v_accvgpr_read_b32 v167, a19\n\t" \
  "v_mfma_f32_16x16x32_bf16 a[68:71], a[220:223], v[12:15], a[68:71]\n\t" \
  "ds_read_b64_tr_b16 a[202:203], v236 offset:4096\n\t" \
  "v_cvt_pk_bf16_f32 v168, v160, v161\n\t" \
  "v_mfma_f32_16x16x32_bf16 a[68:71], a[216:219], v[8:11], a[68:71]\n\t" \
  "ds_read_b64_tr_b16 a[204:205], v236 offset:8192\n\t" \
  "v_cvt_pk_bf16_f32 v169, v162, v163\n\t" \
  "v_mfma_f32_16x16x32_bf16 a[68:71], a[212:215], v[4:7], a[68:71]\n\t" \
  "ds_read_b64_tr_b16 a[206:207], v236 offset:12288\n\t" \
  "v_cvt_pk_bf16_f32 v170, v164, v165\n\t" \
  "v_mfma_f32_16x16x32_bf16 a[68:71], a[208:211], v[0:3], a[68:71]\n\t" \
  "v_cvt_pk_bf16_f32 v171, v166, v167\n\t" \
  "s_xor_b32 s76, s60, 0\n\t" \
  "v_mfma_f32_16x16x32_bf16 a[72:75], a[208:211], v[32:35], a[72:75]\n\t" \
  "buffer_store_dwordx4 v[168:171], v242, %[ws], s76 offen sc1\n\t" \
  "v_mfma_f32_16x16x32_bf16 a[72:75], a[212:215], v[36:39], a[72:75]\n\t" \
  "v_accvgpr_read_b32 v160, a4\n\t" \
  "v_accvgpr_read_b32 v161, a5\n\t" \
  "v_mfma_f32_16x16x32_bf16 a[72:75], a[216:219], v[40:43], a[72:75]\n\t" \
  "v_accvgpr_read_b32 v162, a6\n\t" \
  "v_accvgpr_read_b32 v163, a7\n\t" \
  "v_mfma_f32_16x16x32_bf16 a[72:75], a[220:223], v[44:47], a[72:75]\n\t" \
  "s_waitcnt lgkmcnt(4)\n\t" \
  "v_accvgpr_read_b32 v164, a20\n\t" \
  "v_accvgpr_read_b32 v165, a21\n\t" \
  "v_mfma_f32_16x16x32_bf16 a[80:83], a[192:195], v[16:19], a[80:83]\n\t" \
  "v_accvgpr_read_b32 v166, a22\n\t" \
  "v_accvgpr_read_b32 v167, a23\n\t" \
  "v_mfma_f32_16x16x32_bf16 a[80:83], a[196:199], v[20:23], a[80:83]\n\t" \
  "s_waitcnt lgkmcnt(0)\n\t" \
  "v_cvt_pk_bf16_f32 v172, v160, v161\n\t" \
  "v_cvt_pk_bf16_f32 v173, v162, v163\n\t" \
  "v_mfma_f32_16x16x32_bf16 a[80:83], a[200:203], v[24:27], a[80:83]\n\t" \
  "v_cvt_pk_bf16_f32 v174, v164, v165\n\t" \
  "v_cvt_pk_bf16_f32 v175, v166, v167\n\t" \
  "v_mfma_f32_16x16x32_bf16 a[80:83], a[204:207], v[28:31], a[80:83]\n\t" \
  "s_xor_b32 s76, s61, 0\n\t" \
  "v_xor_b32 v237, 0x200c0, v234\n\t" \
  "v_mfma_f32_16x16x32_bf16 a[84:87], a[204:207], v[12:15], a[84:87]\n\t" \
  "buffer_store_dwordx4 v[172:175], v242, %[ws], s76 offen sc1\n\t" \
  "v_mfma_f32_16x16x32_bf16 a[84:87], a[200:203], v[8:11], a[84:87]\n\t" \
  "ds_read_b64_tr_b16 a[208:209], v237 offset:0\n\t" \
  "ds_read_b64_tr_b16 a[210:211], v237 offset:4096\n\t" \
  "v_mfma_f32_16x16x32_bf16 a[84:87], a[196:199], v[4:7], a[84:87]\n\t" \
  "ds_read_b64_tr_b16 a[212:213], v237 offset:8192\n\t" \
  "ds_read_b64_tr_b16 a[214:215], v237 offset:12288\n\t" \
  "v_mfma_f32_16x16x32_bf16 a[84:87], a[192:195], v[0:3], a[84:87]\n\t" \
  "v_xor_b32 v235, 0xc0, v234\n\t" \
  "v_accvgpr_read_b32 v160, a64\n\t" \
  "v_mfma_f32_16x16x32_bf16 a[88:91], a[192:195], v[32:35], a[88:91]\n\t" \
  "ds_read_b64_tr_b16 a[216:217], v235 offset:0\n\t" \
  "ds_read_b64_tr_b16 a[218:219], v235 offset:4096\n\t" \
  "v_mfma_f32_16x16x32_bf16 a[88:91], a[196:199], v[36:39], a[88:91]\n\t" \
  "ds_read_b64_tr_b16 a[220:221], v235 offset:8192\n\t" \
  "v_accvgpr_read_b32 v161, a65\n\t" \
  "v_mfma_f32_16x16x32_bf16 a[88:91], a[200:203], v[40:43], a[88:91]\n\t" \
  "ds_read_b64_tr_b16 a[222:223], v235 offset:12288\n\t" \
  "v_xor_b32 v236, 0xc000, v234\n\t" \
  "v_mfma_f32_16x16x32_bf16 a[88:91], a[204:207], v[44:47], a[88:91]\n\t" \
  "s_waitcnt lgkmcnt(4)\n\t" \
  "ds_read_b64_tr_b16 a[32:33], v236 offset:0\n\t" \
  "ds_read_b64_tr_b16 a[34:35], v236 offset:4096\n\t" \
  "v_mfma_f32_16x16x32_bf16 a[96:99], a[208:211], v[16:19], a[96:99]\n\t" \
  "ds_read_b64_tr_b16 a[48:49], v236 offset:8192\n\t" \
  "v_accvgpr_read_b32 v162, a66\n\t" \
  "v_mfma_f32_16x16x32_bf16 a[96:99], a[212:215], v[20:23], a[96:99]\n\t" \
  "s_waitcnt lgkmcnt(3)\n\t" \
  "ds_read_b64_tr_b16 a[50:51], v236 offset:12288\n\t" \
  "v_accvgpr_read_b32 v163, a67\n\t" \
  "v_mfma_f32_16x16x32_bf16 a[96:99], a[216:219], v[24:27], a[96:99]\n\t" \
  "ds_read_b64_tr_b16 a[36:37], v236 offset:16384\n\t" \
  "v_accvgpr_read_b32 v164, a80\n\t" \
  "v_mfma_f32_16x16x32_bf16 a[96:99], a[220:223], v[28:31], a[96:99]\n\t" \
  "ds_read_b64_tr_b16 a[38:39], v236 offset:20480\n\t" \
  "v_accvgpr_read_b32 v165, a81\n\t" \
  "v_mfma_f32_16x16x32_bf16 a[100:103], a[220:223], v[12:15], a[100:103]\n\t" \
  "ds_read_b64_tr_b16 a[52:53], v236 offset:24576\n\t" \
  "v_accvgpr_read_b32 v166, a82\n\t" \
  "v_mfma_f32_16x16x32_bf16 a[100:103], a[216:219], v[8:11], a[100:103]\n\t" \
  "ds_read_b64_tr_b16 a[54:55], v236 offset:28672\n\t" \
  "v_xor_b32 v237, 0x200e0, v234\n\t" \
  "v_mfma_f32_16x16x32_bf16 a[100:103], a[212:215], v[4:7], a[100:103]\n\t" \
  "ds_read_b64_tr_b16 a[192:193], v237 offset:0\n\t" \
  "v_accvgpr_read_b32 v167, a83\n\t" \
  "v_mfma_f32_16x16x32_bf16 a[100:103], a[208:211], v[0:3], a[100:103]\n\t" \
  "ds_read_b64_tr_b16 a[194:195], v237 offset:4096\n\t" \
  "v_cvt_pk_bf16_f32 v168, v160, v161\n\t" \
  "v_mfma_f32_16x16x32_bf16 a[104:107], a[208:211], v[32:35], a[104:107]\n\t" \
  "ds_read_b64_tr_b16 a[196:197], v237 offset:8192\n\t" \
  "v_cvt_pk_bf16_f32 v169, v162, v163\n\t" \
  "v_mfma_f32_16x16x32_bf16 a[104:107], a[212:215], v[36:39], a[104:107]\n\t" \
  "ds_read_b64_tr_b16 a[198:199], v237 offset:12288\n\t" \
  "v_cvt_pk_bf16_f32 v170, v164, v165\n\t" \
  "v_cvt_pk_bf16_f32 v171, v166, v167\n\t" \
  "v_mfma_f32_16x16x32_bf16 a[104:107], a[216:219], v[40:43], a[104:107]\n\t" \
  "s_xor_b32 s76, s60, 2048\n\t" \
  "v_xor_b32 v235, 0xe0, v234\n\t" \
  "ds_read_b64_tr_b16 a[200:201], v235 offset:0\n\t" \
  "ds_read_b64_tr_b16 a[202:203], v235 offset:4096\n\t" \
  "ds_read_b64_tr_b16 a[204:205], v235 offset:8192\n\t" \
  "ds_read_b64_tr_b16 a[206:207], v235 offset:12288\n\t" \
  "v_mfma_f32_16x16x32_bf16 a[104:107], a[220:223], v[44:47], a[104:107]\n\t" \
  "s_waitcnt lgkmcnt(4)\n\t" \
  "v_xor_b32 v236, 0xc020, v234\n\t" \
  "v_mfma_f32_16x16x32_bf16 a[112:115], a[192:195], v[16:19], a[112:115]\n\t" \
  "ds_read_b64_tr_b16 a[0:1], v236 offset:0\n\t" \
  "ds_read_b64_tr_b16 a[2:3], v236 offset:4096\n\t" \
  "v_mfma_f32_16x16x32_bf16 a[112:115], a[196:199], v[20:23], a[112:115]\n\t" \
  "s_waitcnt lgkmcnt(2)\n\t" \
  "ds_read_b64_tr_b16 a[16:17], v236 offset:8192\n\t" \
  "ds_read_b64_tr_b16 a[18:19], v236 offset:12288\n\t" \
  "v_mfma_f32_16x16x32_bf16 a[112:115], a[200:203], v[24:27], a[112:115]\n\t" \
  "ds_read_b64_tr_b16 a[4:5], v236 offset:16384\n\t" \
  "ds_read_b64_tr_b16 a[6:7], v236 offset:20480\n\t" \
  "v_mfma_f32_16x16x32_bf16 a[112:115], a[204:207], v[28:31], a[112:115]\n\t" \
  "ds_read_b64_tr_b16 a[20:21], v236 offset:24576\n\t" \
  "ds_read_b64_tr_b16 a[22:23], v236 offset:28672\n\t" \
  "v_mfma_f32_16x16x32_bf16 a[116:119], a[204:207], v[12:15], a[116:119]\n\t" \
  "buffer_store_dwordx4 v[168:171], v242, %[ws], s76 offen sc1\n\t" \
  "v_accvgpr_read_b32 v160, a68\n\t" \
  "v_mfma_f32_16x16x32_bf16 a[116:119], a[200:203], v[8:11], a[116:119]\n\t" \
  "v_accvgpr_read_b32 v161, a69\n\t" \
  "v_accvgpr_read_b32 v162, a70\n\t" \
  "v_accvgpr_read_b32 v163, a71\n\t" \
  "v_mfma_f32_16x16x32_bf16 a[116:119], a[196:199], v[4:7], a[116:119]\n\t" \
  "v_accvgpr_read_b32 v164, a84\n\t" \
  "v_accvgpr_read_b32 v165, a85\n\t" \
  "v_accvgpr_read_b32 v166, a86\n\t" \
  "v_mfma_f32_16x16x32_bf16 a[116:119], a[192:195], v[0:3], a[116:119]\n\t" \
  "v_accvgpr_read_b32 v167, a87\n\t" \
  "v_xor_b32 v237, 0xc040, v234\n\t" \
  "v_cvt_pk_bf16_f32 v172, v160, v161\n\t" \
  "v_mfma_f32_16x16x32_bf16 a[120:123], a[192:195], v[32:35], a[120:123]\n\t" \
  "ds_read_b64_tr_b16 a[64:65], v237 offset:0\n\t" \
  "v_cvt_pk_bf16_f32 v173, v162, v163\n\t" \
  "v_cvt_pk_bf16_f32 v174, v164, v165\n\t" \
  "v_mfma_f32_16x16x32_bf16 a[120:123], a[196:199], v[36:39], a[120:123]\n\t" \
  "ds_read_b64_tr_b16 a[66:67], v237 offset:4096\n\t" \
  "ds_read_b64_tr_b16 a[80:81], v237 offset:8192\n\t" \
  "v_cvt_pk_bf16_f32 v175, v166, v167\n\t" \
  "v_mfma_f32_16x16x32_bf16 a[120:123], a[200:203], v[40:43], a[120:123]\n\t" \
  "s_xor_b32 s76, s61, 2048\n\t" \
  "ds_read_b64_tr_b16 a[82:83], v237 offset:12288\n\t" \
  "ds_read_b64_tr_b16 a[68:69], v237 offset:16384\n\t" \
  "v_mfma_f32_16x16x32_bf16 a[120:123], a[204:207], v[44:47], a[120:123]\n\t" \
  "s_waitcnt vmcnt(5)\n\t" \
  "ds_read_b64_tr_b16 a[70:71], v237 offset:20480\n\t" \
  "ds_read_b64_tr_b16 a[84:85], v237 offset:24576\n\t" \
  "v_mfma_f32_16x16x32_bf16 a[8:11], a[32:35], v[48:51], a[8:11]\n\t" \
  "ds_read_b64_tr_b16 a[86:87], v237 offset:28672\n\t" \
  "v_xor_b32 v235, 0xc060, v234\n\t" \
  "v_mfma_f32_16x16x32_bf16 a[8:11], a[48:51], v[52:55], a[8:11]\n\t" \
  "ds_read_b64_tr_b16 a[208:209], v235 offset:0\n\t" \
  "ds_read_b64_tr_b16 a[210:211], v235 offset:4096\n\t" \
  "ds_read_b64_tr_b16 a[212:213], v235 offset:8192\n\t" \
  "v_mfma_f32_16x16x32_bf16 a[8:11], a[36:39], v[56:59], a[8:11]\n\t" \
  "ds_read_b64_tr_b16 a[214:215], v235 offset:12288\n\t" \
  "ds_read_b64_tr_b16 a[216:217], v235 offset:16384\n\t" \
  "v_mfma_f32_16x16x32_bf16 a[8:11], a[52:55], v[60:63], a[8:11]\n\t" \
  "s_waitcnt lgkmcnt(15)\n\t" \
  "ds_read_b64_tr_b16 a[218:219], v235 offset:20480\n\t" \
  "ds_read_b64_tr_b16 a[220:221], v235 offset:24576\n\t" \
  "v_mfma_f32_16x16x32_bf16 a[24:27], a[0:3], v[48:51], a[24:27]\n\t" \
  "ds_read_b64_tr_b16 a[222:223], v235 offset:28672\n\t" \
  "v_xor_b32 v236, 0xc080, v234\n\t" \
  ".Lhs14:\n\ts_cbranch_execz .Lhs14n\n\t" \
  "v_mfma_f32_16x16x32_bf16 a[24:27], a[16:19], v[52:55], a[24:27]\n\t" \
  "s_waitcnt lgkmcnt(15)\n\t" \
  "buffer_store_dwordx4 v[172:175], v242, %[ws], s76 offen sc1\n\t" \
  "v_accvgpr_read_b32 v160, a96\n\t" \
  "v_mfma_f32_16x16x32_bf16 a[24:27], a[4:7], v[56:59], a[24:27]\n\t" \
  "ds_read_b64_tr_b16 v[32:33], v236 offset:0\n\t" \
  "v_accvgpr_read_b32 v161, a97\n\t" \
  "v_accvgpr_read_b32 v162, a98\n\t" \
  "v_mfma_f32_16x16x32_bf16 a[24:27], a[20:23], v[60:63], a[24:27]\n\t" \
  "s_waitcnt lgkmcnt(13)\n\t" \
  "ds_read_b64_tr_b16 v[34:35], v236 offset:4096\n\t" \
  "v_accvgpr_read_b32 v163, a99\n\t" \
  "v_accvgpr_read_b32 v164, a112\n\t" \
  "v_mfma_f32_16x16x32_bf16 a[40:43], a[64:67], v[48:51], a[40:43]\n\t" \
  "ds_read_b64_tr_b16 v[36:37], v236 offset:8192\n\t" \
  "v_accvgpr_read_b32 v165, a113\n\t" \
  "v_accvgpr_read_b32 v166, a114\n\t" \
  "v_mfma_f32_16x16x32_bf16 a[40:43], a[80:83], v[52:55], a[40:43]\n\t" \
  "s_waitcnt lgkmcnt(11)\n\t" \
  "ds_read_b64_tr_b16 v[38:39], v236 offset:12288\n\t" \
  "v_accvgpr_read_b32 v167, a115\n\t" \
  "v_cvt_pk_bf16_f32 v168, v160, v161\n\t" \
  "v_mfma_f32_16x16x32_bf16 a[40:43], a[68:71], v[56:59], a[40:43]\n\t" \
  "ds_read_b64_tr_b16 v[40:41], v236 offset:16384\n\t" \
  "v_cvt_pk_bf16_f32 v169, v162, v163\n\t" \
  "v_cvt_pk_bf16_f32 v170, v164, v165\n\t" \
  "v_mfma_f32_16x16x32_bf16 a[40:43], a[84:87], v[60:63], a[40:43]\n\t" \
  "s_waitcnt lgkmcnt(9)\n\t" \
  "ds_read_b64_tr_b16 v[42:43], v236 offset:20480\n\t" \
  "v_cvt_pk_bf16_f32 v171, v166, v167\n\t" \
  "s_xor_b32 s76, s60, 3072\n\t" \
  "v_mfma_f32_16x16x32_bf16 a[56:59], a[208:211], v[48:51], a[56:59]\n\t" \
  "s_add_u32 m0, %[m], 0\n\t" \
  "buffer_store_dwordx4 v[168:171], v242, %[ws], s76 offen sc1\n\t" \
  "v_accvgpr_read_b32 v160, a100\n\t" \
  "v_mfma_f32_16x16x32_bf16 a[56:59], a[212:215], v[52:55], a[56:59]\n\t" \
  "s_waitcnt lgkmcnt(6)\n\t" \
  "ds_read_b64_tr_b16 v[44:45], v236 offset:24576\n\t" \
  "v_accvgpr_read_b32 v161, a101\n\t" \
  "v_accvgpr_read_b32 v162, a102\n\t" \
  "v_mfma_f32_16x16x32_bf16 a[56:59], a[216:219], v[56:59], a[56:59]\n\t" \
  "ds_read_b64_tr_b16 v[46:47], v236 offset:28672\n\t" \
  "v_xor_b32 v237, 0xc0a0, v234\n\t" \
  "v_accvgpr_read_b32 v163, a103\n\t" \
  "v_mfma_f32_16x16x32_bf16 a[56:59], a[220:223], v[60:63], a[56:59]\n\t" \
  "s_waitcnt lgkmcnt(4)\n\t" \
  "s_barrier\n\t" \
  "v_mfma_f32_16x16x32_bf16 a[72:75], v[32:35], v[48:51], a[72:75]\n\t" \
  "s_xor_b32 s71, %[eso], 491520\n\t" \
  "ds_read_b64_tr_b16 a[192:193], v237 offset:0\n\t" \
  "ds_read_b64_tr_b16 a[194:195], v237 offset:4096\n\t" \
  "ds_read_b64_tr_b16 a[196:197], v237 offset:8192\n\t" \
  "v_mfma_f32_16x16x32_bf16 a[72:75], v[36:39], v[52:55], a[72:75]\n\t" \
  "s_waitcnt lgkmcnt(3)\n\t" \
  "ds_read_b64_tr_b16 a[198:199], v237 offset:12288\n\t" \
  "ds_read_b64_tr_b16 a[200:201], v237 offset:16384\n\t" \
  "ds_read_b64_tr_b16 a[202:203], v237 offset:20480\n\t" \
  "v_mfma_f32_16x16x32_bf16 a[72:75], v[40:43], v[56:59], a[72:75]\n\t" \
  "ds_read_b64_tr_b16 a[204:205], v237 offset:24576\n\t" \
  "v_accvgpr_read_b32 v164, a116\n\t" \
  "v_accvgpr_read_b32 v165, a117\n\t" \
  "ds_read_b64_tr_b16 a[206:207], v237 offset:28672\n\t" \
  "v_mfma_f32_16x16x32_bf16 a[72:75], v[44:47], v[60:63], a[72:75]\n\t" \
  "s_waitcnt lgkmcnt(4)\n\t" \
  "v_xor_b32 v235, 0xc0c0, v234\n\t" \
  "v_accvgpr_read_b32 v166, a118\n\t" \
  "v_accvgpr_read_b32 v167, a119\n\t" \
  "v_mfma_f32_16x16x32_bf16 a[88:91], a[192:195], v[48:51], a[88:91]\n\t" \
  "ds_read_b64_tr_b16 v[0:1], v235 offset:0\n\t" \
  "ds_read_b64_tr_b16 v[2:3], v235 offset:4096\n\t" \
  "ds_read_b64_tr_b16 v[4:5], v235 offset:8192\n\t" \
  "v_mfma_f32_16x16x32_bf16 a[88:91], a[196:199], v[52:55], a[88:91]\n\t" \
  "s_waitcnt lgkmcnt(3)\n\t" \
  "ds_read_b64_tr_b16 v[6:7], v235 offset:12288\n\t" \
  "v_cvt_pk_bf16_f32 v172, v160, v161\n\t" \
  "v_cvt_pk_bf16_f32 v173, v162, v163\n\t" \
  "v_mfma_f32_16x16x32_bf16 a[88:91], a[200:203], v[56:59], a[88:91]\n\t" \
  "ds_read_b64_tr_b16 v[8:9], v235 offset:16384\n\t" \
  "v_cvt_pk_bf16_f32 v174, v164, v165\n\t" \
  "v_cvt_pk_bf16_f32 v175, v166, v167\n\t" \
  "ds_read_b64_tr_b16 v[10:11], v235 offset:20480\n\t" \
  "ds_read_b64_tr_b16 v[12:13], v235 offset:24576\n\t" \
  "ds_read_b64_tr_b16 v[14:15], v235 offset:28672\n\t" \
  "v_mfma_f32_16x16x32_bf16 a[88:91], a[204:207], v[60:63], a[88:91]\n\t" \
  "s_waitcnt lgkmcnt(4)\n\t" \
  "s_xor_b32 s76, s61, 3072\n\t" \
  "v_xor_b32 v236, 0xc0e0, v234\n\t" \
  "v_mfma_f32_16x16x32_bf16 a[104:107], v[0:3], v[48:51], a[104:107]\n\t" \
  "ds_read_b64_tr_b16 v[16:17], v236 offset:0\n\t" \
  "ds_read_b64_tr_b16 v[18:19], v236 offset:4096\n\t" \
  "v_mfma_f32_16x16x32_bf16 a[104:107], v[4:7], v[52:55], a[104:107]\n\t" \
  "s_waitcnt lgkmcnt(2)\n\t" \
  "ds_read_b64_tr_b16 v[20:21], v236 offset:8192\n\t" \
  "ds_read_b64_tr_b16 v[22:23], v236 offset:12288\n\t" \
  "v_mfma_f32_16x16x32_bf16 a[104:107], v[8:11], v[56:59], a[104:107]\n\t" \
  "ds_read_b64_tr_b16 v[24:25], v236 offset:16384\n\t" \
  "ds_read_b64_tr_b16 v[26:27], v236 offset:20480\n\t" \
  "ds_read_b64_tr_b16 v[28:29], v236 offset:24576\n\t" \
  "ds_read_b64_tr_b16 v[30:31], v236 offset:28672\n\t" \
  "v_mfma_f32_16x16x32_bf16 a[104:107], v[12:15], v[60:63], a[104:107]\n\t" \
  "s_waitcnt lgkmcnt(4)\n\t" \
  "ds_read_b128 a[208:211], v242 offset:16384\n\t" \
  "ds_read_b128 a[212:215], v242 offset:17408\n\t" \
  "v_mfma_f32_16x16x32_bf16 a[120:123], v[16:19], v[48:51], a[120:123]\n\t" \
  "ds_read_b128 a[216:219], v242 offset:18432\n\t" \
  "ds_read_b128 a[220:223], v242 offset:19456\n\t" \
  "v_mfma_f32_16x16x32_bf16 a[120:123], v[20:23], v[52:55], a[120:123]\n\t" \
  "s_waitcnt lgkmcnt(4)\n\t" \
  "buffer_store_dwordx4 v[172:175], v242, %[ws], s76 offen sc1\n\t" \
  "v_accvgpr_read_b32 v160, a8\n\t" \
  "v_mfma_f32_16x16x32_bf16 a[120:123], v[24:27], v[56:59], a[120:123]\n\t" \
  "ds_read_b128 v[32:35], v242 offset:20480\n\t" \
  "v_accvgpr_read_b32 v161, a9\n\t" \
  "v_accvgpr_read_b32 v162, a10\n\t" \
  "v_mfma_f32_16x16x32_bf16 a[120:123], v[28:31], v[60:63], a[120:123]\n\t" \
  "s_waitcnt lgkmcnt(1)\n\t" \
  "ds_read_b128 v[36:39], v242 offset:21504\n\t" \
  "v_accvgpr_read_b32 v163, a11\n\t" \
  "v_accvgpr_read_b32 v164, a24\n\t" \
  STP("v_writelane_b32 %[stv], s94, 34\n\t") \
  STP("v_writelane_b32 %[stv], s92, 35\n\t") \
  STP("s_memrealtime s[94:95]\n\t") \
  STP("s_memtime s[92:93]\n\t") \
  "v_mfma_f32_16x16x32_bf16 v[96:99], v[64:67], a[208:211], v[96:99]\n\t" \
  "ds_read_b128 v[40:43], v242 offset:22528\n\t" \
  "v_accvgpr_read_b32 v165, a25\n\t" \
  "v_accvgpr_read_b32 v166, a26\n\t" \
  "v_mfma_f32_16x16x32_bf16 v[96:99], v[68:71], a[212:215], v[96:99]\n\t" \
  "ds_read_b128 v[44:47], v242 offset:23552\n\t" \
  "v_accvgpr_read_b32 v167, a27\n\t" \
  "v_cvt_pk_bf16_f32 v168, v160, v161\n\t" \
  "v_mfma_f32_16x16x32_bf16 v[96:99], v[72:75], a[216:219], v[96:99]\n\t" \
  "ds_read_b128 a[192:195], v242 offset:24576\n\t" \
  "v_cvt_pk_bf16_f32 v169, v162, v163\n\t" \
  "v_cvt_pk_bf16_f32 v170, v164, v165\n\t" \
  "v_mfma_f32_16x16x32_bf16 v[96:99], v[76:79], a[220:223], v[96:99]\n\t" \
  "ds_read_b128 a[196:199], v242 offset:25600\n\t" \
  "v_cvt_pk_bf16_f32 v171, v166, v167\n\t" \
  "s_xor_b32 s76, s62, 0\n\t" \
  "v_mfma_f32_16x16x32_bf16 v[112:115], v[92:95], a[220:223], v[112:115]\n\t" \
  "buffer_store_dwordx4 v[168:171], v242, %[ws], s76 offen sc1\n\t" \
  "v_accvgpr_read_b32 v160, a40\n\t" \
  "v_mfma_f32_16x16x32_bf16 v[112:115], v[88:91], a[216:219], v[112:115]\n\t" \
  "ds_read_b128 a[200:203], v242 offset:26624\n\t" \
  "v_accvgpr_read_b32 v161, a41\n\t" \
  "v_accvgpr_read_b32 v162, a42\n\t" \
  "v_mfma_f32_16x16x32_bf16 v[112:115], v[84:87], a[212:215], v[112:115]\n\t" \
  "ds_read_b128 a[204:207], v242 offset:27648\n\t" \
  "v_accvgpr_read_b32 v163, a43\n\t" \
  "v_accvgpr_read_b32 v164, a56\n\t" \
  "v_mfma_f32_16x16x32_bf16 v[112:115], v[80:83], a[208:211], v[112:115]\n\t" \
  "s_waitcnt lgkmcnt(4)\n\t" \
  "v_accvgpr_read_b32 v165, a57\n\t" \
  "v_accvgpr_read_b32 v166, a58\n\t" \
  "v_accvgpr_read_b32 v167, a59\n\t" \
  "v_mfma_f32_16x16x32_bf16 v[100:103], v[64:67], v[32:35], v[100:103]\n\t" \
  "v_cvt_pk_bf16_f32 v172, v160, v161\n\t" \
  "v_cvt_pk_bf16_f32 v173, v162, v163\n\t" \
  "v_cvt_pk_bf16_f32 v174, v164, v165\n\t" \
  "v_mfma_f32_16x16x32_bf16 v[100:103], v[68:71], v[36:39], v[100:103]\n\t" \
  "v_cvt_pk_bf16_f32 v175, v166, v167\n\t" \
  "s_xor_b32 s76, s62, 1024\n\t" \
  "v_mfma_f32_16x16x32_bf16 v[100:103], v[72:75], v[40:43], v[100:103]\n\t" \
  "buffer_store_dwordx4 v[172:175], v242, %[ws], s76 offen sc1\n\t" \
  "v_accvgpr_read_b32 v160, a72\n\t" \
  "v_mfma_f32_16x16x32_bf16 v[100:103], v[76:79], v[44:47], v[100:103]\n\t" \
  "v_accvgpr_read_b32 v161, a73\n\t" \
  "v_accvgpr_read_b32 v162, a74\n\t" \
  "v_accvgpr_read_b32 v163, a75\n\t" \
  "v_mfma_f32_16x16x32_bf16 v[116:119], v[92:95], v[44:47], v[116:119]\n\t" \
  "s_mov_b32 s73, 0\n\t" \
  "v_accvgpr_read_b32 v164, a88\n\t" \
  "v_accvgpr_read_b32 v165, a89\n\t" \
  "v_accvgpr_read_b32 v166, a90\n\t" \
  "v_mfma_f32_16x16x32_bf16 v[116:119], v[88:91], v[40:43], v[116:119]\n\t" \
  "v_accvgpr_read_b32 v167, a91\n\t" \
  "v_cvt_pk_bf16_f32 v168, v160, v161\n\t" \
  "v_cvt_pk_bf16_f32 v169, v162, v163\n\t" \
  "v_mfma_f32_16x16x32_bf16 v[116:119], v[84:87], v[36:39], v[116:119]\n\t" \
  "v_cvt_pk_bf16_f32 v170, v164, v165\n\t" \
  "v_cvt_pk_bf16_f32 v171, v166, v167\n\t" \
  "s_xor_b32 s76, s62, 2048\n\t" \
  "v_mfma_f32_16x16x32_bf16 v[116:119], v[80:83], v[32:35], v[116:119]\n\t" \
  "s_waitcnt lgkmcnt(0)\n\t" \
  "s_barrier\n\t" \
  "v_mfma_f32_16x16x32_bf16 v[104:107], v[64:67], a[192:195], v[104:107]\n\t" \
  "buffer_store_dwordx4 v[168:171], v242, %[ws], s76 offen sc1\n\t" \
  "v_accvgpr_read_b32 v160, a104\n\t" \
  "v_mfma_f32_16x16x32_bf16 v[104:107], v[68:71], a[196:199], v[104:107]\n\t" \
  "v_accvgpr_read_b32 v161, a105\n\t" \
  "v_accvgpr_read_b32 v162, a106\n\t" \
  "v_accvgpr_read_b32 v163, a107\n\t" \
  "v_mfma_f32_16x16x32_bf16 v[104:107], v[72:75], a[200:203], v[104:107]\n\t" \
  "v_accvgpr_read_b32 v164, a120\n\t" \
  "v_accvgpr_read_b32 v165, a121\n\t" \
  "v_accvgpr_read_b32 v166, a122\n\t" \
  "v_mfma_f32_16x16x32_bf16 v[104:107], v[76:79], a[204:207], v[104:107]\n\t" \
  "v_xor_b32 v237, 0x14000, v234\n\t" \
  "v_accvgpr_read_b32 v167, a123\n\t" \
  "v_cvt_pk_bf16_f32 v172, v160, v161\n\t" \
  "v_mfma_f32_16x16x32_bf16 v[120:123], v[92:95], a[204:207], v[120:123]\n\t" \
  "ds_read_b64_tr_b16 a[96:97], v237 offset:0\n\t" \
  "v_cvt_pk_bf16_f32 v173, v162, v163\n\t" \
  "v_cvt_pk_bf16_f32 v174, v164, v165\n\t" \
  "ds_read_b64_tr_b16 a[98:99], v237 offset:4096\n\t" \
  "ds_read_b64_tr_b16 a[112:113], v237 offset:8192\n\t" \
  "ds_read_b64_tr_b16 a[114:115], v237 offset:12288\n\t" \
  "ds_read_b64_tr_b16 a[100:101], v237 offset:32768\n\t" \
  "ds_read_b64_tr_b16 a[102:103], v237 offset:36864\n\t" \
  "ds_read_b64_tr_b16 a[116:117], v237 offset:40960\n\t" \
  "ds_read_b64_tr_b16 a[118:119], v237 offset:45056\n\t" \
  "ds_read_b128 a[208:211], v242 offset:28672\n\t" \
  "ds_read_b128 a[212:215], v242 offset:29696\n\t" \
  "ds_read_b128 a[216:219], v242 offset:30720\n\t" \
  "ds_read_b128 a[220:223], v242 offset:31744\n\t" \
  "v_mfma_f32_16x16x32_bf16 v[120:123], v[88:91], a[200:203], v[120:123]\n\t" \
  "ds_read_b128 v[0:3], v242 offset:32768\n\t" \
  "v_cvt_pk_bf16_f32 v175, v166, v167\n\t" \
  "s_xor_b32 s76, s62, 3072\n\t" \
  "v_mfma_f32_16x16x32_bf16 v[120:123], v[84:87], a[196:199], v[120:123]\n\t" \
  "buffer_store_dwordx4 v[172:175], v242, %[ws], s76 offen sc1\n\t" \
  "v_mfma_f32_16x16x32_bf16 v[120:123], v[80:83], a[192:195], v[120:123]\n\t" \
  "s_waitcnt lgkmcnt(1)\n\t" \
  "ds_read_b128 v[4:7], v242 offset:33792\n\t" \
  "ds_read_b128 v[8:11], v242 offset:34816\n\t" \
  "v_mfma_f32_16x16x32_bf16 v[108:111], v[64:67], a[208:211], v[108:111]\n\t" \
  "buffer_load_dwordx4 v242, %[ws], s71 offen offset:0 sc1 lds\n\t" \
  "v_mfma_f32_16x16x32_bf16 v[108:111], v[68:71], a[212:215], v[108:111]\n\t" \
  "ds_read_b128 v[12:15], v242 offset:35840\n\t" \
  "v_xor_b32 v235, 0x14020, v234\n\t" \
  "v_mfma_f32_16x16x32_bf16 v[108:111], v[72:75], a[216:219], v[108:111]\n\t" \
  "buffer_load_dwordx4 v242, %[ws], s71 offen offset:1024 sc1 lds\n\t" \
  "v_mfma_f32_16x16x32_bf16 v[108:111], v[76:79], a[220:223], v[108:111]\n\t" \
  "ds_read_b64_tr_b16 a[64:65], v235 offset:0\n\t" \
  "ds_read_b64_tr_b16 a[66:67], v235 offset:4096\n\t" \
  "v_mfma_f32_16x16x32_bf16 v[124:127], v[92:95], a[220:223], v[124:127]\n\t" \
  "ds_read_b64_tr_b16 a[68:69], v235 offset:8192\n\t" \
  "ds_read_b64_tr_b16 a[70:71], v235 offset:12288\n\t" \
  "v_mfma_f32_16x16x32_bf16 v[124:127], v[88:91], a[216:219], v[124:127]\n\t" \
  "ds_read_b64_tr_b16 a[80:81], v235 offset:32768\n\t" \
  "ds_read_b64_tr_b16 a[82:83], v235 offset:36864\n\t" \
  "v_mfma_f32_16x16x32_bf16 v[124:127], v[84:87], a[212:215], v[124:127]\n\t" \
  "buffer_load_dwordx4 v242, %[ws], s71 offen offset:2048 sc1 lds\n\t" \
  "v_mfma_f32_16x16x32_bf16 v[124:127], v[80:83], a[208:211], v[124:127]\n\t" \
  "s_waitcnt lgkmcnt(6)\n\t" \
  "ds_read_b64_tr_b16 a[84:85], v235 offset:40960\n\t" \
  "ds_read_b64_tr_b16 a[86:87], v235 offset:45056\n\t" \
  "v_mfma_f32_16x16x32_bf16 v[96:99], v[128:131], v[0:3], v[96:99]\n\t" \
  "ds_read_b128 a[192:195], v242 offset:36864\n\t" \
  "ds_read_b128 a[196:199], v242 offset:37888\n\t" \
  "v_mfma_f32_16x16x32_bf16 v[96:99], v[132:135], v[4:7], v[96:99]\n\t" \
  "ds_read_b128 a[200:203], v242 offset:38912\n\t" \
  "ds_read_b128 a[204:207], v242 offset:39936\n\t" \
  "v_mfma_f32_16x16x32_bf16 v[96:99], v[136:139], v[8:11], v[96:99]\n\t" \
  "buffer_load_dwordx4 v242, %[ws], s71 offen offset:3072 sc1 lds\n\t" \
  "s_add_u32 m0, %[m], 131072\n\t" \
  "v_mfma_f32_16x16x32_bf16 v[96:99], v[140:143], v[12:15], v[96:99]\n\t" \
  "s_add_u32 s71, %[xso], 0\n\t" \
  "ds_read_b128 a[208:211], v242 offset:40960\n\t" \
  "s_waitcnt vmcnt(9)\n\t" \
  "s_mov_b64 exec, 1\n\t" \
  "buffer_store_dword v243, off, %[ws], s65 sc1\n\t" \
  "s_mov_b64 exec, -1\n\t" \
  "v_mfma_f32_16x16x32_bf16 v[112:115], v[156:159], v[12:15], v[112:115]\n\t" \
  "s_waitcnt vmcnt(9)\n\t" \
  "s_mov_b64 exec, 1\n\t" \
  "buffer_store_dword v243, off, %[ws], s66 sc1\n\t" \
  "s_mov_b64 exec, -1\n\t" \
  "v_mfma_f32_16x16x32_bf16 v[112:115], v[152:155], v[8:11], v[112:115]\n\t" \
  "ds_read_b128 a[212:215], v242 offset:41984\n\t" \
  "v_mfma_f32_16x16x32_bf16 v[112:115], v[148:151], v[4:7], v[112:115]\n\t" \
  "ds_read_b128 a[216:219], v242 offset:43008\n\t" \
  "v_mfma_f32_16x16x32_bf16 v[112:115], v[144:147], v[0:3], v[112:115]\n\t" \
  "s_waitcnt lgkmcnt(3)\n\t" \
  "ds_read_b128 a[220:223], v242 offset:44032\n\t" \
  "v_mfma_f32_16x16x32_bf16 v[100:103], v[128:131], a[192:195], v[100:103]\n\t" \
  "buffer_load_dwordx4 v248, %[sx], s71 offen offset:0 lds\n\t" \
  "s_add_u32 s71, %[xso], 7168\n\t" \
  "v_mfma_f32_16x16x32_bf16 v[100:103], v[132:135], a[196:199], v[100:103]\n\t" \
  "ds_read_b128 v[16:19], v242 offset:45056\n\t" \
  "v_mfma_f32_16x16x32_bf16 v[100:103], v[136:139], a[200:203], v[100:103]\n\t" \
  "ds_read_b128 v[20:23], v242 offset:46080\n\t" \
  "v_mfma_f32_16x16x32_bf16 v[100:103], v[140:143], a[204:207], v[100:103]\n\t" \
  "ds_read_b128 v[24:27], v242 offset:47104\n\t" \
  "v_mfma_f32_16x16x32_bf16 v[116:119], v[156:159], a[204:207], v[116:119]\n\t" \
  "buffer_load_dwordx4 v249, %[sx], s71 offen offset:1024 lds\n\t" \
  "s_add_u32 s71, %[xso], 14336\n\t" \
  "v_mfma_f32_16x16x32_bf16 v[116:119], v[152:155], a[200:203], v[116:119]\n\t" \
  "ds_read_b128 v[28:31], v242 offset:48128\n\t" \
  "v_mfma_f32_16x16x32_bf16 v[116:119], v[148:151], a[196:199], v[116:119]\n\t" \
  "v_mfma_f32_16x16x32_bf16 v[116:119], v[144:147], a[192:195], v[116:119]\n\t" \
  "s_waitcnt lgkmcnt(4)\n\t" \
  "s_barrier\n\t" \
  "v_mfma_f32_16x16x32_bf16 v[104:107], v[128:131], a[208:211], v[104:107]\n\t" \
  "ds_read_b128 a[192:195], v230 offset:32768\n\t" \
  "v_and_b32 v235, 16, v242\n\t" \
  "v_mfma_f32_16x16x32_bf16 v[104:107], v[132:135], a[212:215], v[104:107]\n\t" \
  "buffer_load_dwordx4 v250, %[sx], s71 offen offset:2048 lds\n\t" \
  "s_add_u32 s71, %[xso], 21504\n\t" \
  "v_mfma_f32_16x16x32_bf16 v[104:107], v[136:139], a[216:219], v[104:107]\n\t" \
  "ds_read_b128 a[196:199], v230 offset:33792\n\t" \
  "v_mov_b32 v229, 0x3f80\n\t" \
  "v_mfma_f32_16x16x32_bf16 v[104:107], v[140:143], a[220:223], v[104:107]\n\t" \
  "ds_read_b128 a[200:203], v230 offset:34816\n\t" \
  "v_lshlrev_b32 v229, v235, v229\n\t" \
  "v_mfma_f32_16x16x32_bf16 v[120:123], v[156:159], a[220:223], v[120:123]\n\t" \
  "s_mov_b32 s82, 0x300003\n\t" \
  "ds_read_b128 a[204:207], v230 offset:35840\n\t" \
  "s_mov_b32 s83, 0x30000300\n\t" \
  "v_mfma_f32_16x16x32_bf16 v[120:123], v[152:155], a[216:219], v[120:123]\n\t" \
  "buffer_load_dwordx4 v251, %[sx], s71 offen offset:3072 lds\n\t" \
  "s_add_u32 m0, %[m], 49152\n\t" \
  "v_mfma_f32_16x16x32_bf16 v[120:123], v[148:151], a[212:215], v[120:123]\n\t" \
  "s_xor_b32 s71, %[eso], 622592\n\t" \
  "s_mov_b32 s84, 0xc0000c\n\t" \
  "s_mov_b32 s85, 0xc0000c00\n\t" \
  "v_mfma_f32_16x16x32_bf16 v[120:123], v[144:147], a[208:211], v[120:123]\n\t" \
  "s_waitcnt lgkmcnt(4)\n\t" \
  "v_cndmask_b32 v238, 0, v229, s[82:83]\n\t" \
  "v_cndmask_b32 v239, 0, v229, s[84:85]\n\t" \
  "v_mfma_f32_16x16x32_bf16 v[108:111], v[128:131], v[16:19], v[108:111]\n\t" \
  "ds_read_b128 v[0:3], v230 offset:36864\n\t" \
  "v_and_b32 v236, 15, v244\n\t" \
  "v_mfma_f32_16x16x32_bf16 v[108:111], v[132:135], v[20:23], v[108:111]\n\t" \
  "buffer_load_dwordx4 v242, %[ws], s71 offen offset:0 sc1 lds\n\t" \
  "v_mfma_f32_16x16x32_bf16 v[108:111], v[136:139], v[24:27], v[108:111]\n\t" \
  "ds_read_b128 v[4:7], v230 offset:37888\n\t" \
  "v_lshrrev_b32 v229, 4, v244\n\t" \
  "v_mfma_f32_16x16x32_bf16 v[108:111], v[140:143], v[28:31], v[108:111]\n\t" \
  "ds_read_b128 v[8:11], v230 offset:38912\n\t" \
  "v_lshlrev_b32 v235, 2, v236\n\t" \
  "v_mfma_f32_16x16x32_bf16 v[124:127], v[156:159], v[28:31], v[124:127]\n\t" \
  "ds_read_b128 v[12:15], v230 offset:39936\n\t" \
  "v_mul_u32_u24 v236, 80, v236\n\t" \
  "v_mfma_f32_16x16x32_bf16 v[124:127], v[152:155], v[24:27], v[124:127]\n\t" \
  "v_lshlrev_b32 v237, 4, v229\n\t" \
  "v_add_u32 v237, s70, v237\n\t" \
  "v_mfma_f32_16x16x32_bf16 v[124:127], v[148:151], v[20:23], v[124:127]\n\t" \
  "buffer_load_dwordx4 v242, %[ws], s71 offen offset:1024 sc1 lds\n\t" \
  "v_mfma_f32_16x16x32_bf16 v[124:127], v[144:147], v[16:19], v[124:127]\n\t" \
  "s_waitcnt lgkmcnt(4)\n\t" \
  "v_add_u32 v236, v237, v236\n\t" \
  "v_mfma_f32_16x16x32_bf16 v[96:99], a[160:163], a[192:195], v[96:99]\n\t" \
  "ds_read_b128 v[16:19], v230 offset:40960\n\t" \
  "v_mfma_f32_16x16x32_bf16 v[96:99], a[164:167], a[196:199], v[96:99]\n\t" \
  "ds_read_b128 v[20:23], v230 offset:41984\n\t" \
  "v_mfma_f32_16x16x32_bf16 v[96:99], a[168:171], a[200:203], v[96:99]\n\t" \
  "buffer_load_dwordx4 v242, %[ws], s71 offen offset:2048 sc1 lds\n\t" \
  "v_mfma_f32_16x16x32_bf16 v[96:99], a[172:175], a[204:207], v[96:99]\n\t" \
  "ds_read_b128 v[24:27], v230 offset:43008\n\t" \
  "v_mfma_f32_16x16x32_bf16 v[112:115], a[188:191], a[204:207], v[112:115]\n\t" \
  "ds_read_b128 v[28:31], v230 offset:44032\n\t" \
  "v_mfma_f32_16x16x32_bf16 v[112:115], a[184:187], a[200:203], v[112:115]\n\t" \
  "v_mfma_f32_16x16x32_bf16 v[112:115], a[180:183], a[196:199], v[112:115]\n\t" \
  "v_mfma_f32_16x16x32_bf16 v[112:115], a[176:179], a[192:195], v[112:115]\n\t" \
  "s_waitcnt lgkmcnt(4)\n\t" \
  "buffer_load_dwordx4 v242, %[ws], s71 offen offset:3072 sc1 lds\n\t" \
  "s_add_u32 m0, %[m], 65536\n\t" \
  "v_mfma_f32_16x16x32_bf16 v[100:103], a[160:163], v[0:3], v[100:103]\n\t" \
  "s_xor_b32 s71, %[eso], 884736\n\t" \
  "ds_read_b128 a[192:195], v230 offset:45056\n\t" \
  "v_mfma_f32_16x16x32_bf16 v[100:103], a[164:167], v[4:7], v[100:103]\n\t" \
  "s_waitcnt vmcnt(14)\n\t" \
  "s_mov_b64 exec, 1\n\t" \
  "buffer_store_dword v243, off, %[ws], s67 sc1\n\t" \
  "s_mov_b64 exec, -1\n\t" \
  "v_mfma_f32_16x16x32_bf16 v[100:103], a[168:171], v[8:11], v[100:103]\n\t" \
  "ds_read_b128 a[196:199], v230 offset:46080\n\t" \
  "v_mfma_f32_16x16x32_bf16 v[100:103], a[172:175], v[12:15], v[100:103]\n\t" \
  "ds_read_b128 a[200:203], v230 offset:47104\n\t" \
  "ds_read_b128 a[208:211], v236 offset:960\n\t" \
  "v_mfma_f32_16x16x32_bf16 v[116:119], a[188:191], v[12:15], v[116:119]\n\t" \
  "buffer_load_dwordx4 v242, %[ws], s71 offen offset:0 sc1 lds\n\t" \
  "v_mfma_f32_16x16x32_bf16 v[116:119], a[184:187], v[8:11], v[116:119]\n\t" \
  "ds_read_b128 a[204:207], v230 offset:48128\n\t" \
  "v_mfma_f32_16x16x32_bf16 v[116:119], a[180:183], v[4:7], v[116:119]\n\t" \
  "ds_read_b128 a[212:215], v236 offset:640\n\t" \
  "v_mfma_f32_16x16x32_bf16 v[116:119], a[176:179], v[0:3], v[116:119]\n\t" \
  "s_waitcnt vmcnt(12) lgkmcnt(6)\n\t" \
  "s_barrier\n\t" \
  "v_mfma_f32_16x16x32_bf16 v[104:107], a[160:163], v[16:19], v[104:107]\n\t" \
  "buffer_load_dwordx4 v242, %[ws], s71 offen offset:1024 sc1 lds\n\t" \
  "v_mfma_f32_16x16x32_bf16 v[104:107], a[164:167], v[20:23], v[104:107]\n\t" \
  "ds_read_b128 v[0:3], v242 offset:0\n\t" \
  "v_mfma_f32_16x16x32_bf16 v[104:107], a[168:171], v[24:27], v[104:107]\n\t" \
  "ds_read_b128 v[4:7], v242 offset:1024\n\t" \
  "v_mfma_f32_16x16x32_bf16 v[104:107], a[172:175], v[28:31], v[104:107]\n\t" \
  "ds_read_b128 v[8:11], v242 offset:2048\n\t" \
  "v_mfma_f32_16x16x32_bf16 v[120:123], a[188:191], v[28:31], v[120:123]\n\t" \
  "buffer_load_dwordx4 v242, %[ws], s71 offen offset:2048 sc1 lds\n\t" \
  "v_mfma_f32_16x16x32_bf16 v[120:123], a[184:187], v[24:27], v[120:123]\n\t" \
  "ds_read_b128 v[12:15], v242 offset:3072\n\t" \
  "v_mfma_f32_16x16x32_bf16 v[120:123], a[180:183], v[20:23], v[120:123]\n\t" \
  "ds_read_b128 a[216:219], v236 offset:320\n\t" \
  "v_mfma_f32_16x16x32_bf16 v[120:123], a[176:179], v[16:19], v[120:123]\n\t" \
  "s_waitcnt lgkmcnt(6)\n\t" \
  "ds_read_b128 a[220:223], v236 offset:0\n\t" \
  "v_mfma_f32_16x16x32_bf16 v[108:111], a[160:163], a[192:195], v[108:111]\n\t" \
  "ds_read_b128 v[16:19], v242 offset:4096\n\t" \
  "v_mfma_f32_16x16x32_bf16 v[108:111], a[164:167], a[196:199], v[108:111]\n\t" \
  "buffer_load_dwordx4 v242, %[ws], s71 offen offset:3072 sc1 lds\n\t" \
  "s_add_u32 m0, %[m], 16384\n\t" \
  "v_mfma_f32_16x16x32_bf16 v[108:111], a[168:171], a[200:203], v[108:111]\n\t" \
  "s_xor_b32 s71, %[eso], 229376\n\t" \
  "ds_read_b128 v[20:23], v242 offset:5120\n\t" \
  "v_mfma_f32_16x16x32_bf16 v[108:111], a[172:175], a[204:207], v[108:111]\n\t" \
  "ds_read_b128 v[24:27], v242 offset:6144\n\t" \
  "v_mfma_f32_16x16x32_bf16 v[124:127], a[188:191], a[204:207], v[124:127]\n\t" \
  "ds_read_b128 v[28:31], v242 offset:7168\n\t" \
  "v_mfma_f32_16x16x32_bf16 v[124:127], a[184:187], a[200:203], v[124:127]\n\t" \
  "buffer_load_dwordx4 v242, %[ws], s71 offen offset:0 sc1 lds\n\t" \
  "v_mfma_f32_16x16x32_bf16 v[124:127], a[180:183], a[196:199], v[124:127]\n\t" \
  "v_mfma_f32_16x16x32_bf16 v[124:127], a[176:179], a[192:195], v[124:127]\n\t" \
  "s_waitcnt lgkmcnt(6)\n\t" \
  "v_mfma_f32_16x16x32_bf16 v[96:99], a[128:131], v[0:3], v[96:99]\n\t" \
  "ds_read_b128 v[128:131], v242 offset:8192\n\t" \
  "v_mfma_f32_16x16x32_bf16 v[96:99], a[132:135], v[4:7], v[96:99]\n\t" \
  "ds_read_b128 v[132:135], v242 offset:9216\n\t" \
  "v_mfma_f32_16x16x32_bf16 v[96:99], a[136:139], v[8:11], v[96:99]\n\t" \
  "buffer_load_dwordx4 v242, %[ws], s71 offen offset:1024 sc1 lds\n\t" \
  "v_mfma_f32_16x16x32_bf16 v[96:99], a[140:143], v[12:15], v[96:99]\n\t" \
  "ds_read_b128 a[8:11], v242 offset:10240\n\t" \
  "v_mfma_f32_16x16x32_bf16 v[112:115], a[156:159], v[12:15], v[112:115]\n\t" \
  "ds_read_b128 a[24:27], v242 offset:11264\n\t" \
  "v_mfma_f32_16x16x32_bf16 v[112:115], a[152:155], v[8:11], v[112:115]\n\t" \
  "v_mfma_f32_16x16x32_bf16 v[112:115], a[148:151], v[4:7], v[112:115]\n\t" \
  "buffer_load_dwordx4 v242, %[ws], s71 offen offset:2048 sc1 lds\n\t" \
  "v_mfma_f32_16x16x32_bf16 v[112:115], a[144:147], v[0:3], v[112:115]\n\t" \
  "s_waitcnt lgkmcnt(4)\n\t" \
  "v_mfma_f32_16x16x32_bf16 v[100:103], a[128:131], v[16:19], v[100:103]\n\t" \
  "ds_read_b128 v[0:3], v242 offset:12288\n\t" \
  "v_accvgpr_write_b32 a192, 0\n\t" \
  "v_mfma_f32_16x16x32_bf16 v[100:103], a[132:135], v[20:23], v[100:103]\n\t" \
  "ds_read_b128 v[4:7], v242 offset:13312\n\t" \
  "v_accvgpr_write_b32 a193, 0\n\t" \
  "v_mfma_f32_16x16x32_bf16 v[100:103], a[136:139], v[24:27], v[100:103]\n\t" \
  "buffer_load_dwordx4 v242, %[ws], s71 offen offset:3072 sc1 lds\n\t" \
  "v_mfma_f32_16x16x32_bf16 v[100:103], a[140:143], v[28:31], v[100:103]\n\t" \
  "ds_read_b128 v[8:11], v242 offset:14336\n\t" \
  "v_accvgpr_write_b32 a194, v238\n\t" \
  "v_mfma_f32_16x16x32_bf16 v[116:119], a[156:159], v[28:31], v[116:119]\n\t" \
  "ds_read_b128 v[12:15], v242 offset:15360\n\t" \
  "v_accvgpr_write_b32 a195, v239\n\t" \
  "v_mfma_f32_16x16x32_bf16 v[116:119], a[152:155], v[24:27], v[116:119]\n\t" \
  "buffer_load_dwordx2 v[32:33], v252, %[sd], s73 offen offset:0\n\t" \
  "v_mfma_f32_16x16x32_bf16 v[116:119], a[148:151], v[20:23], v[116:119]\n\t" \
  "v_accvgpr_write_b32 a196, 0\n\t" \
  "v_accvgpr_write_b32 a197, 0\n\t" \
  "v_mfma_f32_16x16x32_bf16 v[116:119], a[144:147], v[16:19], v[116:119]\n\t" \
  "s_waitcnt vmcnt(10) lgkmcnt(4)\n\t" \
  "s_barrier\n\t" \
  "v_mfma_f32_16x16x32_bf16 v[104:107], a[128:131], v[128:131], v[104:107]\n\t" \
  "buffer_load_dwordx2 v[34:35], v252, %[sd], s73 offen offset:2048\n\t" \
  "s_mov_b32 s73, 4096\n\t" \
  "v_mfma_f32_16x16x32_bf16 v[104:107], a[132:135], v[132:135], v[104:107]\n\t" \
  "ds_read_b128 v[16:19], v242 offset:49152\n\t" \
  "v_mfma_f32_16x16x32_bf16 v[104:107], a[136:139], a[8:11], v[104:107]\n\t" \
  "buffer_load_dwordx2 v[36:37], v252, %[sd], s73 offen offset:0\n\t" \
  "v_mfma_f32_16x16x32_bf16 v[104:107], a[140:143], a[24:27], v[104:107]\n\t" \
  "ds_read_b128 v[20:23], v242 offset:50176\n\t" \
  "ds_read_b64 v[208:209], v231 offset:0\n\t" \
  "v_mfma_f32_16x16x32_bf16 v[120:123], a[156:159], a[24:27], v[120:123]\n\t" \
  "ds_read_b128 v[24:27], v242 offset:51200\n\t" \
  "v_mfma_f32_16x16x32_bf16 v[120:123], a[152:155], a[8:11], v[120:123]\n\t" \
  "buffer_load_dwordx2 v[38:39], v252, %[sd], s73 offen offset:2048\n\t" \
  "s_mov_b32 s73, 8192\n\t" \
  "buffer_load_dword v246, v235, %[ws], s68 offen sc1\n\t" \
  "v_mfma_f32_16x16x32_bf16 v[120:123], a[148:151], v[132:135], v[120:123]\n\t" \
  "ds_read_b128 v[28:31], v242 offset:52224\n\t" \
  "ds_read_b64 v[210:211], v231 offset:4096\n\t" \
  "v_mfma_f32_16x16x32_bf16 v[120:123], a[144:147], v[128:131], v[120:123]\n\t" \
  "s_waitcnt lgkmcnt(6)\n\t" \
  "ds_read_b64 v[212:213], v231 offset:8192\n\t" \
  "v_mfma_f32_16x16x32_bf16 v[108:111], a[128:131], v[0:3], v[108:111]\n\t" \
  "ds_read_b128 v[128:131], v242 offset:53248\n\t" \
  "ds_read_b64 v[214:215], v231 offset:12288\n\t" \
  "v_mfma_f32_16x16x32_bf16 v[108:111], a[132:135], v[4:7], v[108:111]\n\t" \
  "buffer_load_dwordx2 v[40:41], v252, %[sd], s73 offen offset:0\n\t" \
  "v_mfma_f32_16x16x32_bf16 v[108:111], a[136:139], v[8:11], v[108:111]\n\t" \
  "ds_read_b128 v[132:135], v242 offset:54272\n\t" \
  "ds_read_b64 v[216:217], v233 offset:0\n\t" \
  "v_mfma_f32_16x16x32_bf16 v[108:111], a[140:143], v[12:15], v[108:111]\n\t" \
  "buffer_load_dwordx2 v[42:43], v252, %[sd], s73 offen offset:2048\n\t" \
  "s_mov_b32 s73, 12288\n\t" \
  "v_mfma_f32_16x16x32_bf16 v[124:127], a[156:159], v[12:15], v[124:127]\n\t" \
  "ds_read_b128 a[40:43], v242 offset:55296\n\t" \
  "ds_read_b64 v[218:219], v233 offset:4096\n\t" \
  "v_mfma_f32_16x16x32_bf16 v[124:127], a[152:155], v[8:11], v[124:127]\n\t" \
  "ds_read_b128 a[56:59], v242 offset:56320\n\t" \
  "ds_read_b64 v[220:221], v233 offset:8192\n\t" \
  "ds_read_b64 v[222:223], v233 offset:12288\n\t" \
  "v_mfma_f32_16x16x32_bf16 v[124:127], a[148:151], v[4:7], v[124:127]\n\t" \
  "buffer_load_dwordx2 v[44:45], v252, %[sd], s73 offen offset:0\n\t" \
  "v_mfma_f32_16x16x32_bf16 v[124:127], a[144:147], v[0:3], v[124:127]\n\t" \
  "s_waitcnt lgkmcnt(11)\n\t" \
  "v_mfma_f32_16x16x32_bf16 v[96:99], a[12:15], v[16:19], v[96:99]\n\t" \
  "ds_read_b128 a[72:75], v242 offset:57344\n\t" \
  "v_mfma_f32_16x16x32_bf16 v[96:99], a[28:31], v[20:23], v[96:99]\n\t" \
  "buffer_load_dwordx2 v[46:47], v252, %[sd], s73 offen offset:2048\n\t" \
  "s_mov_b32 s73, 16384\n\t" \
  "v_mfma_f32_16x16x32_bf16 v[96:99], a[44:47], v[24:27], v[96:99]\n\t" \
  "ds_read_b128 a[88:91], v242 offset:58368\n\t" \
  "v_mfma_f32_16x16x32_bf16 v[96:99], a[60:63], v[28:31], v[96:99]\n\t" \
  "buffer_load_dwordx2 v[48:49], v252, %[sd], s73 offen offset:0\n\t" \
  "v_mfma_f32_16x16x32_bf16 v[112:115], a[124:127], v[28:31], v[112:115]\n\t" \
  "ds_read_b128 a[104:107], v242 offset:59392\n\t" \
  "v_mfma_f32_16x16x32_bf16 v[112:115], a[108:111], v[24:27], v[112:115]\n\t" \
  "ds_read_b128 a[120:123], v242 offset:60416\n\t" \
  "v_mfma_f32_16x16x32_bf16 v[112:115], a[92:95], v[20:23], v[112:115]\n\t" \
  "buffer_load_dwordx2 v[50:51], v252, %[sd], s73 offen offset:2048\n\t" \
  "s_mov_b32 s73, 20480\n\t" \
  "v_mfma_f32_16x16x32_bf16 v[112:115], a[76:79], v[16:19], v[112:115]\n\t" \
  "s_waitcnt lgkmcnt(6)\n\t" \
  "v_mfma_f32_16x16x32_bf16 v[100:103], a[12:15], v[128:131], v[100:103]\n\t" \
  "ds_read_b128 v[64:67], v242 offset:61440\n\t" \
  "v_mfma_f32_16x16x32_bf16 v[100:103], a[28:31], v[132:135], v[100:103]\n\t" \
  "buffer_load_dwordx2 v[52:53], v252, %[sd], s73 offen offset:0\n\t" \
  "v_mfma_f32_16x16x32_bf16 v[100:103], a[44:47], a[40:43], v[100:103]\n\t" \
  "ds_read_b128 v[68:71], v242 offset:62464\n\t" \
  "v_mfma_f32_16x16x32_bf16 v[100:103], a[60:63], a[56:59], v[100:103]\n\t" \
  "buffer_load_dwordx2 v[54:55], v252, %[sd], s73 offen offset:2048\n\t" \
  "s_mov_b32 s73, 24576\n\t" \
  "v_mfma_f32_16x16x32_bf16 v[116:119], a[124:127], a[56:59], v[116:119]\n\t" \
  "ds_read_b128 v[72:75], v242 offset:63488\n\t" \
  "v_mfma_f32_16x16x32_bf16 v[116:119], a[108:111], a[40:43], v[116:119]\n\t" \
  "ds_read_b128 v[76:79], v242 offset:64512\n\t" \
  "v_mfma_f32_16x16x32_bf16 v[116:119], a[92:95], v[132:135], v[116:119]\n\t" \
  "buffer_load_dwordx2 v[56:57], v252, %[sd], s73 offen offset:0\n\t" \
  "v_mfma_f32_16x16x32_bf16 v[116:119], a[76:79], v[128:131], v[116:119]\n\t" \
  "s_waitcnt vmcnt(18) lgkmcnt(4)\n\t" \
  "s_barrier\n\t" \
  "v_mfma_f32_16x16x32_bf16 v[104:107], a[12:15], a[72:75], v[104:107]\n\t" \
  "ds_read_b128 v[128:131], v230 offset:0\n\t" \
  "v_lshlrev_b32 v0, 16, v208\n\t" \
  "v_mfma_f32_16x16x32_bf16 v[104:107], a[28:31], a[88:91], v[104:107]\n\t" \
  "buffer_load_dwordx2 v[58:59], v252, %[sd], s73 offen offset:2048\n\t" \
  "s_mov_b32 s73, 28672\n\t" \
  "v_mfma_f32_16x16x32_bf16 v[104:107], a[44:47], a[104:107], v[104:107]\n\t" \
  "ds_read_b128 v[132:135], v230 offset:1024\n\t" \
  "v_and_b32 v1, 0xffff0000, v208\n\t" \
  "v_mfma_f32_16x16x32_bf16 v[104:107], a[60:63], a[120:123], v[104:107]\n\t" \
  "buffer_load_dwordx2 v[60:61], v252, %[sd], s73 offen offset:0\n\t" \
  "v_mfma_f32_16x16x32_bf16 v[120:123], a[124:127], a[120:123], v[120:123]\n\t" \
  "ds_read_b128 v[80:83], v230 offset:2048\n\t" \
  "v_lshlrev_b32 v2, 16, v209\n\t" \
  "v_mfma_f32_16x16x32_bf16 v[120:123], a[108:111], a[104:107], v[120:123]\n\t" \
  ".Lhs15:\n\ts_cbranch_execz .Lhs15n\n\t" \
  "ds_read_b128 v[84:87], v230 offset:3072\n\t" \
  "v_and_b32 v3, 0xffff0000, v209\n\t" \
  "v_mfma_f32_16x16x32_bf16 v[120:123], a[92:95], a[88:91], v[120:123]\n\t" \
  "buffer_load_dwordx2 v[62:63], v252, %[sd], s73 offen offset:2048\n\t" \
  "v_mfma_f32_16x16x32_bf16 v[120:123], a[76:79], a[72:75], v[120:123]\n\t" \
  "s_waitcnt lgkmcnt(4)\n\t" \
  "v_lshlrev_b32 v4, 16, v210\n\t" \
  "v_and_b32 v5, 0xffff0000, v210\n\t" \
  "v_mfma_f32_16x16x32_bf16 v[108:111], a[12:15], v[64:67], v[108:111]\n\t" \
  "ds_read_b128 a[176:179], v230 offset:4096\n\t" \
  "v_lshlrev_b32 v6, 16, v211\n\t" \
  "v_mfma_f32_16x16x32_bf16 v[108:111], a[28:31], v[68:71], v[108:111]\n\t" \
  "ds_read_b128 a[180:183], v230 offset:5120\n\t" \
  "v_and_b32 v7, 0xffff0000, v211\n\t" \
  "v_mfma_f32_16x16x32_bf16 v[108:111], a[44:47], v[72:75], v[108:111]\n\t" \
  "ds_read_b128 a[184:187], v230 offset:6144\n\t" \
  "v_lshlrev_b32 v8, 16, v212\n\t" \
  "v_mfma_f32_16x16x32_bf16 v[108:111], a[60:63], v[76:79], v[108:111]\n\t" \
  "ds_read_b128 a[188:191], v230 offset:7168\n\t" \
  "v_and_b32 v9, 0xffff0000, v212\n\t" \
  "s_waitcnt vmcnt(12)\n\t" \
  "v_cmp_ne_u32 vcc, %[tag], v246\n\t" \
  "s_and_b32 vcc_lo, vcc_lo, 0xfff\n\t" \
  "s_cselect_b32 s81, -1, 0\n\t" \
  "v_mfma_f32_16x16x32_bf16 v[124:127], a[124:127], v[76:79], v[124:127]\n\t" \
  "v_lshl_or_b32 v247, s81, 31, v242\n\t" \
  "s_add_u32 s72, s63, 0\n\t" \
  "v_mfma_f32_16x16x32_bf16 v[124:127], a[108:111], v[72:75], v[124:127]\n\t" \
  "buffer_load_dwordx4 v[88:91], v247, %[ws], s72 offen sc1\n\t" \
  "v_mfma_f32_16x16x32_bf16 v[124:127], a[92:95], v[68:71], v[124:127]\n\t" \
  "s_add_u32 s72, s64, 32768\n\t" \
  "v_lshlrev_b32 v10, 16, v213\n\t" \
  "v_and_b32 v11, 0xffff0000, v213\n\t" \
  "v_mfma_f32_16x16x32_bf16 v[124:127], a[76:79], v[64:67], v[124:127]\n\t" \
  "s_waitcnt lgkmcnt(4)\n\t" \
  "buffer_load_dwordx4 v[92:95], v247, %[ws], s72 offen sc1\n\t" \
  "v_mfma_f32_16x16x32_bf16 v[96:99], a[96:99], v[128:131], v[96:99]\n\t" \
  "s_add_u32 s72, s63, 65536\n\t" \
  "ds_read_b128 a[8:11], v230 offset:8192\n\t" \
  "v_lshlrev_b32 v12, 16, v214\n\t" \
  "v_mfma_f32_16x16x32_bf16 v[96:99], a[112:115], v[132:135], v[96:99]\n\t" \
  "ds_read_b128 a[24:27], v230 offset:9216\n\t" \
  "v_and_b32 v13, 0xffff0000, v214\n\t" \
  "v_mfma_f32_16x16x32_bf16 v[96:99], a[100:103], v[80:83], v[96:99]\n\t" \
  "ds_read_b128 a[128:131], v230 offset:10240\n\t" \
  "v_lshlrev_b32 v14, 16, v215\n\t" \
  "v_mfma_f32_16x16x32_bf16 v[96:99], a[116:119], v[84:87], v[96:99]\n\t" \
  "buffer_load_dwordx4 v[136:139], v247, %[ws], s72 offen sc1\n\t" \
  "v_mfma_f32_16x16x32_bf16 v[112:115], a[84:87], v[84:87], v[112:115]\n\t" \
  "s_add_u32 s72, s64, 98304\n\t" \
  "ds_read_b128 a[132:135], v230 offset:11264\n\t" \
  "v_and_b32 v15, 0xffff0000, v215\n\t" \
  "v_mfma_f32_16x16x32_bf16 v[112:115], a[80:83], v[80:83], v[112:115]\n\t" \
  "v_lshlrev_b32 v16, 16, v216\n\t" \
  "v_and_b32 v17, 0xffff0000, v216\n\t" \
  "v_mfma_f32_16x16x32_bf16 v[112:115], a[68:71], v[132:135], v[112:115]\n\t" \
  "v_lshlrev_b32 v18, 16, v217\n\t" \
  "v_and_b32 v19, 0xffff0000, v217\n\t" \
  "v_mfma_f32_16x16x32_bf16 v[112:115], a[64:67], v[128:131], v[112:115]\n\t" \
  "s_waitcnt lgkmcnt(4)\n\t" \
  "buffer_load_dwordx4 v[140:143], v247, %[ws], s72 offen sc1\n\t" \
  "v_mfma_f32_16x16x32_bf16 v[100:103], a[96:99], a[176:179], v[100:103]\n\t" \
  "s_add_u32 s72, s63, 4096\n\t" \
  "ds_read_b128 v[128:131], v230 offset:12288\n\t" \
  "v_lshlrev_b32 v20, 16, v218\n\t" \
  "v_mfma_f32_16x16x32_bf16 v[100:103], a[112:115], a[180:183], v[100:103]\n\t" \
  "ds_read_b128 v[132:135], v230 offset:13312\n\t" \
  "v_mfma_f32_16x16x32_bf16 v[100:103], a[100:103], a[184:187], v[100:103]\n\t" \
  "ds_read_b128 a[136:139], v230 offset:14336\n\t" \
  "v_and_b32 v21, 0xffff0000, v218\n\t" \
  "v_mfma_f32_16x16x32_bf16 v[100:103], a[116:119], a[188:191], v[100:103]\n\t" \
  "ds_read_b128 a[140:143], v230 offset:15360\n\t" \
  "v_mfma_f32_16x16x32_bf16 v[116:119], a[84:87], a[188:191], v[116:119]\n\t" \
  "buffer_load_dwordx4 v[144:147], v247, %[ws], s72 offen sc1\n\t" \
  "v_mfma_f32_16x16x32_bf16 v[116:119], a[80:83], a[184:187], v[116:119]\n\t" \
  "s_add_u32 s72, s64, 36864\n\t" \
  "v_lshlrev_b32 v22, 16, v219\n\t" \
  "v_mfma_f32_16x16x32_bf16 v[116:119], a[68:71], a[180:183], v[116:119]\n\t" \
  "v_and_b32 v23, 0xffff0000, v219\n\t" \
  "v_mfma_f32_16x16x32_bf16 v[116:119], a[64:67], a[176:179], v[116:119]\n\t" \
  "s_waitcnt lgkmcnt(4)\n\t" \
  "s_barrier\n\t" \
  "v_mfma_f32_16x16x32_bf16 v[104:107], a[96:99], a[8:11], v[104:107]\n\t" \
  "buffer_load_dwordx4 v[148:151], v247, %[ws], s72 offen sc1\n\t" \
  "v_mfma_f32_16x16x32_bf16 v[104:107], a[112:115], a[24:27], v[104:107]\n\t" \
  "s_add_u32 s72, s63, 69632\n\t" \
  "ds_read_b128 a[144:147], v242 offset:16384\n\t" \
  "v_lshlrev_b32 v24, 16, v220\n\t" \
  "v_mfma_f32_16x16x32_bf16 v[104:107], a[100:103], a[128:131], v[104:107]\n\t" \
  "ds_read_b128 a[148:151], v242 offset:17408\n\t" \
  "v_and_b32 v25, 0xffff0000, v220\n\t" \
  "v_mfma_f32_16x16x32_bf16 v[104:107], a[116:119], a[132:135], v[104:107]\n\t" \
  "ds_read_b128 a[152:155], v242 offset:18432\n\t" \
  "v_mfma_f32_16x16x32_bf16 v[120:123], a[84:87], a[132:135], v[120:123]\n\t" \
  "ds_read_b128 a[156:159], v242 offset:19456\n\t" \
  "v_lshlrev_b32 v26, 16, v221\n\t" \
  "v_mfma_f32_16x16x32_bf16 v[120:123], a[80:83], a[128:131], v[120:123]\n\t" \
  "buffer_load_dwordx4 v[152:155], v247, %[ws], s72 offen sc1\n\t" \
  "v_mfma_f32_16x16x32_bf16 v[120:123], a[68:71], a[24:27], v[120:123]\n\t" \
  "s_add_u32 s72, s64, 102400\n\t" \
  "v_and_b32 v27, 0xffff0000, v221\n\t" \
  "v_mfma_f32_16x16x32_bf16 v[120:123], a[64:67], a[8:11], v[120:123]\n\t" \
  "s_waitcnt lgkmcnt(4)\n\t" \
  "v_mfma_f32_16x16x32_bf16 v[108:111], a[96:99], v[128:131], v[108:111]\n\t" \
  "ds_read_b128 a[40:43], v242 offset:20480\n\t" \
  "v_lshlrev_b32 v28, 16, v222\n\t" \
  "v_mfma_f32_16x16x32_bf16 v[108:111], a[112:115], v[132:135], v[108:111]\n\t" \
  "buffer_load_dwordx4 v[156:159], v247, %[ws], s72 offen sc1\n\t" \
  "s_add_u32 s72, s63, 16384\n\t" \
  "v_mfma_f32_16x16x32_bf16 v[108:111], a[100:103], a[136:139], v[108:111]\n\t" \
  "ds_read_b128 a[56:59], v242 offset:21504\n\t" \
  "v_and_b32 v29, 0xffff0000, v222\n\t" \
  "v_mfma_f32_16x16x32_bf16 v[108:111], a[116:119], a[140:143], v[108:111]\n\t" \
  "ds_read_b128 a[72:75], v242 offset:22528\n\t" \
  "v_mfma_f32_16x16x32_bf16 v[124:127], a[84:87], a[140:143], v[124:127]\n\t" \
  "ds_read_b128 a[88:91], v242 offset:23552\n\t" \
  "v_lshlrev_b32 v30, 16, v223\n\t" \
  "v_mfma_f32_16x16x32_bf16 v[124:127], a[80:83], a[136:139], v[124:127]\n\t" \
  "buffer_load_dwordx4 a[160:163], v247, %[ws], s72 offen sc1\n\t" \
  "s_add_u32 s72, s64, 49152\n\t" \
  "v_mfma_f32_16x16x32_bf16 v[124:127], a[68:71], v[132:135], v[124:127]\n\t" \
  "v_and_b32 v31, 0xffff0000, v223\n\t" \
  "v_mfma_f32_16x16x32_bf16 v[124:127], a[64:67], v[128:131], v[124:127]\n\t" \
  "s_waitcnt lgkmcnt(4)\n\t" \
  "v_mfma_f32_16x16x32_bf16 v[96:99], a[32:35], a[144:147], v[96:99]\n\t" \
  "ds_read_b128 a[104:107], v242 offset:24576\n\t" \
  "v_mfma_f32_16x16x32_bf16 v[96:99], a[48:51], a[148:151], v[96:99]\n\t" \
  "ds_read_b128 a[120:123], v242 offset:25600\n\t" \
  "v_mfma_f32_16x16x32_bf16 v[96:99], a[36:39], a[152:155], v[96:99]\n\t" \
  "buffer_load_dwordx4 a[164:167], v247, %[ws], s72 offen sc1\n\t" \
  "s_add_u32 s72, s63, 81920\n\t" \
  "v_mfma_f32_16x16x32_bf16 v[96:99], a[52:55], a[156:159], v[96:99]\n\t" \
  "ds_read_b128 a[12:15], v242 offset:26624\n\t" \
  "v_mfma_f32_16x16x32_bf16 v[112:115], a[20:23], a[156:159], v[112:115]\n\t" \
  "ds_read_b128 a[28:31], v242 offset:27648\n\t" \
  "v_mfma_f32_16x16x32_bf16 v[112:115], a[4:7], a[152:155], v[112:115]\n\t" \
  "v_mfma_f32_16x16x32_bf16 v[112:115], a[16:19], a[148:151], v[112:115]\n\t" \
  "buffer_load_dwordx4 a[168:171], v247, %[ws], s72 offen sc1\n\t" \
  "s_add_u32 s72, s64, 114688\n\t" \
  "v_mfma_f32_16x16x32_bf16 v[112:115], a[0:3], a[144:147], v[112:115]\n\t" \
  "s_waitcnt lgkmcnt(4)\n\t" \
  "v_mfma_f32_16x16x32_bf16 v[100:103], a[32:35], a[40:43], v[100:103]\n\t" \
  "ds_read_b128 a[44:47], v242 offset:28672\n\t" \
  "v_mfma_f32_16x16x32_bf16 v[100:103], a[48:51], a[56:59], v[100:103]\n\t" \
  "ds_read_b128 a[60:63], v242 offset:29696\n\t" \
  "v_mfma_f32_16x16x32_bf16 v[100:103], a[36:39], a[72:75], v[100:103]\n\t" \
  "ds_read_b128 a[76:79], v242 offset:30720\n\t" \
  "v_mfma_f32_16x16x32_bf16 v[100:103], a[52:55], a[88:91], v[100:103]\n\t" \
  "buffer_load_dwordx4 a[172:175], v247, %[ws], s72 offen sc1\n\t" \
  "v_mfma_f32_16x16x32_bf16 v[116:119], a[20:23], a[88:91], v[116:119]\n\t" \
  "ds_read_b128 a[92:95], v242 offset:31744\n\t" \
  "v_mfma_f32_16x16x32_bf16 v[116:119], a[4:7], a[72:75], v[116:119]\n\t" \
  "v_mfma_f32_16x16x32_bf16 v[116:119], a[16:19], a[56:59], v[116:119]\n\t" \
  "v_mfma_f32_16x16x32_bf16 v[116:119], a[0:3], a[40:43], v[116:119]\n\t" \
  "s_waitcnt lgkmcnt(4)\n\t" \
  "v_mfma_f32_16x16x32_bf16 v[104:107], a[32:35], a[104:107], v[104:107]\n\t" \
  "ds_read_b128 v[128:131], v237 offset:2560\n\t" \
  "v_mfma_f32_16x16x32_bf16 v[104:107], a[48:51], a[120:123], v[104:107]\n\t" \
  "ds_read_b128 v[132:135], v237 offset:2624\n\t" \
  "v_mfma_f32_16x16x32_bf16 v[104:107], a[36:39], a[12:15], v[104:107]\n\t" \
  "v_mfma_f32_16x16x32_bf16 v[104:107], a[52:55], a[28:31], v[104:107]\n\t" \
  "v_mfma_f32_16x16x32_bf16 v[120:123], a[20:23], a[28:31], v[120:123]\n\t" \
  "v_mfma_f32_16x16x32_bf16 v[120:123], a[4:7], a[12:15], v[120:123]\n\t" \
  "v_mfma_f32_16x16x32_bf16 v[120:123], a[16:19], a[120:123], v[120:123]\n\t" \
  "v_mfma_f32_16x16x32_bf16 v[120:123], a[0:3], a[104:107], v[120:123]\n\t" \
  "s_waitcnt lgkmcnt(2)\n\t" \
  "v_mfma_f32_16x16x32_bf16 v[108:111], a[32:35], a[44:47], v[108:111]\n\t" \
  "v_mfma_f32_16x16x32_bf16 v[108:111], a[48:51], a[60:63], v[108:111]\n\t" \
  "v_mfma_f32_16x16x32_bf16 v[108:111], a[36:39], a[76:79], v[108:111]\n\t" \
  "v_mfma_f32_16x16x32_bf16 v[108:111], a[52:55], a[92:95], v[108:111]\n\t" \
  "v_mfma_f32_16x16x32_bf16 v[124:127], a[20:23], a[92:95], v[124:127]\n\t" \
  "v_mfma_f32_16x16x32_bf16 v[124:127], a[4:7], a[76:79], v[124:127]\n\t" \
  "v_mfma_f32_16x16x32_bf16 v[124:127], a[16:19], a[60:63], v[124:127]\n\t" \
  "v_mfma_f32_16x16x32_bf16 v[124:127], a[0:3], a[44:47], v[124:127]\n\t" \
  STP("v_writelane_b32 %[stv], s94, 36\n\t") \
  STP("v_writelane_b32 %[stv], s92, 37\n\t") \
  STP("s_memrealtime s[94:95]\n\t") \
  STP("s_memtime s[92:93]\n\t") \
  "s_cmp_lg_u32 s81, 0\n\t" \
  "s_cbranch_scc1 .Lop_s99\n\t" \
  ".Lop_d2:\n\t" \
  "s_waitcnt vmcnt(8)\n\t" \
  "v_mfma_f32_16x16x32_bf16 v[96:99], a[194:197], v[88:91], v[96:99]\n\t" \
  "v_mfma_f32_16x16x32_bf16 v[100:103], a[194:197], v[92:95], v[100:103]\n\t" \
  "v_mfma_f32_16x16x32_bf16 v[104:107], a[194:197], v[136:139], v[104:107]\n\t" \
  "v_mfma_f32_16x16x32_bf16 v[108:111], a[194:197], v[140:143], v[108:111]\n\t" \
  "v_mfma_f32_16x16x32_bf16 v[112:115], a[192:195], v[88:91], v[112:115]\n\t" \
  "v_mfma_f32_16x16x32_bf16 v[116:119], a[192:195], v[92:95], v[116:119]\n\t" \
  "v_mfma_f32_16x16x32_bf16 v[120:123], a[192:195], v[136:139], v[120:123]\n\t" \
  "v_mfma_f32_16x16x32_bf16 v[124:127], a[192:195], v[140:143], v[124:127]\n\t" \
  "s_waitcnt vmcnt(4)\n\t" \
  "v_mfma_f32_16x16x32_bf16 v[96:99], a[194:197], v[144:147], v[96:99]\n\t" \
  "v_mfma_f32_16x16x32_bf16 v[100:103], a[194:197], v[148:151], v[100:103]\n\t" \
  "v_mfma_f32_16x16x32_bf16 v[104:107], a[194:197], v[152:155], v[104:107]\n\t" \
  "v_mfma_f32_16x16x32_bf16 v[108:111], a[194:197], v[156:159], v[108:111]\n\t" \
  "v_mfma_f32_16x16x32_bf16 v[112:115], a[192:195], v[144:147], v[112:115]\n\t" \
  "v_mfma_f32_16x16x32_bf16 v[116:119], a[192:195], v[148:151], v[116:119]\n\t" \
  "v_mfma_f32_16x16x32_bf16 v[120:123], a[192:195], v[152:155], v[120:123]\n\t" \
  "v_mfma_f32_16x16x32_bf16 v[124:127], a[192:195], v[156:159], v[124:127]\n\t" \
  "s_waitcnt vmcnt(0)\n\t" \
  "v_mfma_f32_16x16x32_bf16 v[96:99], a[194:197], a[160:163], v[96:99]\n\t" \
  "v_mfma_f32_16x16x32_bf16 v[100:103], a[194:197], a[164:167], v[100:103]\n\t" \
  "v_mfma_f32_16x16x32_bf16 v[104:107], a[194:197], a[168:171], v[104:107]\n\t" \
  "v_mfma_f32_16x16x32_bf16 v[108:111], a[194:197], a[172:175], v[108:111]\n\t" \
  "v_mfma_f32_16x16x32_bf16 v[112:115], a[192:195], a[160:163], v[112:115]\n\t" \
  "v_mfma_f32_16x16x32_bf16 v[116:119], a[192:195], a[164:167], v[116:119]\n\t" \
  "v_mfma_f32_16x16x32_bf16 v[120:123], a[192:195], a[168:171], v[120:123]\n\t" \
  "v_mfma_f32_16x16x32_bf16 v[124:127], a[192:195], a[172:175], v[124:127]\n\t" \
  "s_waitcnt lgkmcnt(0)\n\t" \
  STP("v_writelane_b32 %[stv], s94, 38\n\t") \
  STP("v_writelane_b32 %[stv], s92, 39\n\t") \
  "s_nop 7\n\t" \
  ".subsection 1\n\t" \
  ".Lop_s0:\n\t" \
  "s_mov_b32 s78, 0\n\t" \
  ".Lop_r0:\n\t" \
  "s_sleep 2\n\t" \
  "buffer_load_dword v246, v245, %[ws], %[fw2] offen sc1\n\t" \
  "s_waitcnt vmcnt(0)\n\t" \
  "v_cmp_ne_u32 vcc, %[tag], v246\n\t" \
  "s_and_b32 vcc_lo, vcc_lo, 0x6\n\t" \
  "s_cbranch_scc0 .Lop_k0\n\t" \
  "s_add_u32 s78, s78, 1\n\t" \
  "s_cmp_lt_u32 s78, %[cap]\n\t" \
  "s_cbranch_scc1 .Lop_r0\n\t" \
  "buffer_store_dword v243, off, %[ws], 0 offset:256 sc1\n\t" \
  "s_branch .Lop_k0\n\t" \
  ".Lop_s1:\n\t" \
  "s_mov_b32 s78, 0\n\t" \
  ".Lop_r1:\n\t" \
  "s_sleep 2\n\t" \
  "buffer_load_dword v246, v245, %[ws], %[fw2] offen sc1\n\t" \
  "s_waitcnt vmcnt(0)\n\t" \
  "v_cmp_ne_u32 vcc, %[tag], v246\n\t" \
  "s_and_b32 vcc_lo, vcc_lo, 0xc00\n\t" \
  "s_cbranch_scc0 .Lop_k1\n\t" \
  "s_add_u32 s78, s78, 1\n\t" \
  "s_cmp_lt_u32 s78, %[cap]\n\t" \
  "s_cbranch_scc1 .Lop_r1\n\t" \
  "buffer_store_dword v243, off, %[ws], 0 offset:256 sc1\n\t" \
  "s_branch .Lop_k1\n\t" \
  ".Lop_s2:\n\t" \
  "s_mov_b32 s78, 0\n\t" \
  ".Lop_r2:\n\t" \
  "s_sleep 2\n\t" \
  "buffer_load_dword v246, v245, %[ws], %[fw2] offen sc1\n\t" \
  "s_waitcnt vmcnt(0)\n\t" \
  "v_cmp_ne_u32 vcc, %[tag], v246\n\t" \
  "s_and_b32 vcc_lo, vcc_lo, 0xfe00\n\t" \
  "s_cbranch_scc0 .Lop_k2\n\t" \
  "s_add_u32 s78, s78, 1\n\t" \
  "s_cmp_lt_u32 s78, %[cap]\n\t" \
  "s_cbranch_scc1 .Lop_r2\n\t" \
  "buffer_store_dword v243, off, %[ws], 0 offset:256 sc1\n\t" \
  "s_branch .Lop_k2\n\t" \
  ".Lop_s3:\n\t" \
  "s_mov_b32 s78, 0\n\t" \
  ".Lop_r3:\n\t" \
  "s_sleep 2\n\t" \
  "buffer_load_dword v246, v245, %[ws], %[fw2] offen sc1\n\t" \
  "s_waitcnt vmcnt(0)\n\t" \
  "v_cmp_ne_u32 vcc, %[tag], v246\n\t" \
  "s_and_b32 vcc_lo, vcc_lo, 0xfe0000\n\t" \
  "s_cbranch_scc0 .Lop_k3\n\t" \
  "s_add_u32 s78, s78, 1\n\t" \
  "s_cmp_lt_u32 s78, %[cap]\n\t" \
  "s_cbranch_scc1 .Lop_r3\n\t" \
  "buffer_store_dword v243, off, %[ws], 0 offset:256 sc1\n\t" \
  "s_branch .Lop_k3\n\t" \
  ".Lop_s4:\n\t" \
  "s_mov_b32 s78, 0\n\t" \
  ".Lop_r4:\n\t" \
  "s_sleep 4\n\t" \
  "s_add_u32 s75, s69, 0\n\t" \
  "buffer_load_dwordx4 v[208:211], v170, %[ws], s75 offen sc1\n\t" \
  "s_add_u32 s75, s69, 32768\n\t" \
  "buffer_load_dwordx4 v[212:215], v170, %[ws], s75 offen sc1\n\t" \
  "s_add_u32 s75, s69, 65536\n\t" \
  "buffer_load_dwordx4 v[216:219], v170, %[ws], s75 offen sc1\n\t" \
  "s_add_u32 s75, s69, 98304\n\t" \
  "buffer_load_dwordx4 v[220:223], v170, %[ws], s75 offen sc1\n\t" \
  "s_waitcnt vmcnt(0)\n\t" \
  "v_xor_b32 v246, v243, v209\n\t" \
  "v_bitop3_b32 v246, v246, v211, v243 bitop3:0xf6\n\t" \
  "v_bitop3_b32 v246, v246, v213, v243 bitop3:0xf6\n\t" \
  "v_bitop3_b32 v246, v246, v215, v243 bitop3:0xf6\n\t" \
  "v_bitop3_b32 v246, v246, v217, v243 bitop3:0xf6\n\t" \
  "v_bitop3_b32 v246, v246, v219, v243 bitop3:0xf6\n\t" \
  "v_bitop3_b32 v246, v246, v221, v243 bitop3:0xf6\n\t" \
  "v_bitop3_b32 v246, v246, v223, v243 bitop3:0xf6\n\t" \
  "v_cmp_ne_u32 vcc, 0, v246\n\t" \
  "s_cbranch_vccz .Lop_k4\n\t" \
  "s_add_u32 s78, s78, 1\n\t" \
  "s_cmp_lt_u32 s78, %[cap]\n\t" \
  "s_cbranch_scc1 .Lop_r4\n\t" \
  "buffer_store_dword v243, off, %[ws], 0 offset:256 sc1\n\t" \
  "s_branch .Lop_k4\n\t" \
  ".Lop_rd:\n\t" \
  "s_waitcnt vmcnt(0) lgkmcnt(0)\n\t" \
  "s_barrier\n\t" \
  "s_mov_b32 %[pm], 2\n\t" \
  "s_branch .Lop_end\n\t" \
  ".Lop_s99:\n\t" \
  "s_mov_b32 s78, 0\n\t" \
  ".Lop_r99:\n\t" \
  "buffer_load_dword v246, v235, %[ws], s68 offen sc1\n\t" \
  "s_waitcnt vmcnt(0)\n\t" \
  "v_cmp_ne_u32 vcc, %[tag], v246\n\t" \
  "s_and_b32 vcc_lo, vcc_lo, 0xfff\n\t" \
  "s_cbranch_scc0 .Lop_k99\n\t" \
  "s_add_u32 s78, s78, 1\n\t" \
  "s_cmp_lt_u32 s78, %[cap]\n\t" \
  "s_cbranch_scc1 .Lop_r99\n\t" \
  "buffer_store_dword v243, off, %[ws], 0 offset:256 sc1\n\t" \
  "s_branch .Lop_k99\n\t" \
  ".Lop_k99:\n\t" \
  "s_add_u32 s72, s63, 0\n\t" \
  "buffer_load_dwordx4 v[88:91], v242, %[ws], s72 offen sc1\n\t" \
  "s_add_u32 s72, s64, 32768\n\t" \
  "buffer_load_dwordx4 v[92:95], v242, %[ws], s72 offen sc1\n\t" \
  "s_add_u32 s72, s63, 65536\n\t" \
  "buffer_load_dwordx4 v[136:139], v242, %[ws], s72 offen sc1\n\t" \
  "s_add_u32 s72, s64, 98304\n\t" \
  "buffer_load_dwordx4 v[140:143], v242, %[ws], s72 offen sc1\n\t" \
  "s_add_u32 s72, s63, 4096\n\t" \
  "buffer_load_dwordx4 v[144:147], v242, %[ws], s72 offen sc1\n\t" \
  "s_add_u32 s72, s64, 36864\n\t" \
  "buffer_load_dwordx4 v[148:151], v242, %[ws], s72 offen sc1\n\t" \
  "s_add_u32 s72, s63, 69632\n\t" \
  "buffer_load_dwordx4 v[152:155], v242, %[ws], s72 offen sc1\n\t" \
  "s_add_u32 s72, s64, 102400\n\t" \
  "buffer_load_dwordx4 v[156:159], v242, %[ws], s72 offen sc1\n\t" \
  "s_add_u32 s72, s63, 16384\n\t" \
  "buffer_load_dwordx4 a[160:163], v242, %[ws], s72 offen sc1\n\t" \
  "s_add_u32 s72, s64, 49152\n\t" \
  "buffer_load_dwordx4 a[164:167], v242, %[ws], s72 offen sc1\n\t" \
  "s_add_u32 s72, s63, 81920\n\t" \
  "buffer_load_dwordx4 a[168:171], v242, %[ws], s72 offen sc1\n\t" \
  "s_add_u32 s72, s64, 114688\n\t" \
  "buffer_load_dwordx4 a[172:175], v242, %[ws], s72 offen sc1\n\t" \
  "s_branch .Lop_d2\n\t" \
  ".Lfz_z:\n\t" \
  ".fill 128, 4, 0xBF800000\n\t" \
  ".subsection 0\n\t" \
  ".Lop_end:\n\t" \
  ""
#define OP_CLOB "v208", "v209", "v210", "v211", "v212", "v214", "v215", "v216", "v217", "v218", "v219", "v220", "v221", "v222", "v223", "v228", "v229", "v230", "v231", "v232", "v233", "v235", "v236", "v237", "v238", "v239", "v244", "v246", "v248", "v249", "v250", "v251", "s60", "s61", "s62", "s63", "s64", "s65", "s66", "s67", "s68", "s69", "s70", "s71", "s72", "s73", "s74", "s75", "s76", "s77", "s78", "s79", "s80", "s81", "s82", "s83", "s84", "s85", "s92", "s93", "s94", "s95", "vcc", "scc", "memory"
#define ST_ASM "s_nop 1\n\t" "v_mfma_f32_16x16x32_bf16 v[80:83], a[208:211], v[160:163], 0\n\t" "v_mfma_f32_16x16x32_bf16 v[80:83], a[212:215], v[164:167], v[80:83]\n\t" "v_mfma_f32_16x16x32_bf16 v[80:83], a[216:219], v[168:171], v[80:83]\n\t" "v_mfma_f32_16x16x32_bf16 v[80:83], a[220:223], v[172:175], v[80:83]\n\t" "v_dot2_f32_bf16 v64, v160, v160, 0\n\t" "v_dot2_f32_bf16 v65, v164, v164, 0\n\t" "v_dot2_f32_bf16 v66, v168, v168, 0\n\t" "v_dot2_f32_bf16 v67, v172, v172, 0\n\t" "v_dot2_f32_bf16 v64, v161, v161, v64\n\t" "v_dot2_f32_bf16 v65, v165, v165, v65\n\t" "v_dot2_f32_bf16 v66, v169, v169, v66\n\t" "v_dot2_f32_bf16 v67, v173, v173, v67\n\t" "v_dot2_f32_bf16 v64, v162, v162, v64\n\t" "v_dot2_f32_bf16 v65, v166, v166, v65\n\t" "v_dot2_f32_bf16 v66, v170, v170, v66\n\t" "v_dot2_f32_bf16 v67, v174, v174, v67\n\t" "v_dot2_f32_bf16 v64, v163, v163, v64\n\t" "v_dot2_f32_bf16 v65, v167, v167, v65\n\t" "v_dot2_f32_bf16 v66, v171, v171, v66\n\t" "v_dot2_f32_bf16 v67, v175, v175, v67\n\t" "s_nop 2\n\t"
static_assert(W_MODB == 2228224);
constexpr int GSLOT = 49152;
constexpr unsigned KRS = 256;
struct KVP { srd_t srdK, srdV; unsigned vkb[4], vvb[2], vksrc[4], vvsrc[2], sofk; };
struct H4 {
  srd_t srdW;
  unsigned voffX, voffW[4];
  unsigned xb1, wb[2][8];
  unsigned mX, mW, soX, rot;
  unsigned pfd, fo, fl, fvo;
  u32x32 q[4];
};
#define W4(a) "+v"(a[0]), "+v"(a[1]), "+v"(a[2]), "+v"(a[3])
#define W8(a) W4(a), "+v"(a[4]), "+v"(a[5]), "+v"(a[6]), "+v"(a[7])
constexpr unsigned kXT = 65536, kWT = 64 * QD * 2;
DI void w_dma4(unsigned m0, srd_t srd, const unsigned (&vo)[4], unsigned so) {
  asm volatile("s_mov_b32 m0, %0\n\ts_nop 0\n\tbuffer_load_dwordx4 %1, %5, %6 offen lds\n\tbuffer_load_dwordx4 %2, %5, %6 offen offset:1024 lds\n\t"
               "buffer_load_dwordx4 %3, %5, %6 offen offset:2048 lds\n\tbuffer_load_dwordx4 %4, %5, %6 offen offset:3072 lds"
               :: "s"(m0), "v"(vo[0]), "v"(vo[1]), "v"(vo[2]), "v"(vo[3]), "s"(srd), "s"(so) : "memory");
}
DI void w_voffs(unsigned (&vo)[4], int ldw, unsigned wid, unsigned lane, unsigned k0) {
#pragma unroll
  for (int j = 0; j < 4; ++j) {
    const unsigned k = (4 * wid + j) * 4 + (lane >> 4), c16 = lane & 15, sw = (k & 3) | (((k >> 3) & 1) << 2);
    vo[j] = (k ^ (32 * k0)) * ldw * 2 + ((((c16 >> 1) ^ sw) << 1) + (c16 & 1)) * 16 - 1024 * j;
  }
}
DI void h4_init_addr(H4& c, unsigned ldsbase, unsigned wid, unsigned lane, unsigned k0) {
  const unsigned r16 = lane & 15, g = lane >> 4, q = (lane >> 2) & 3, p = lane & 3;
  c.xb1 = ldsbase + GSLOT + wid * 8192 + k0 * 4096 + g * 1024 + r16 * 16;
#pragma unroll
  for (int sl = 0; sl < 2; ++sl)
#pragma unroll
    for (int nb = 0; nb < 8; ++nb) { c.wb[sl][nb] = ldsbase + 2 * sl * GSLOT + 32768 + (8 * g + q) * 256 + ((nb ^ (q | ((g & 1) << 2))) << 5) + p * 8; asm volatile("" : "+v"(c.wb[sl][nb])); }
}
#define FSTP(x)
#define FSTS(x)
#define FQ_STV
#define FQ_STC
#define FQ_ASM \
  "s_mov_b32 s98, 0xffff0000\n\t" \
  ".p2align 3\n\t" \
  "ds_read_b128 v[128:131], v208 offset:0\n\t" \
  "ds_read_b128 v[132:135], v208 offset:16\n\t" \
  "ds_read_b128 v[136:139], v208 offset:256\n\t" \
  "ds_read_b128 v[140:143], v208 offset:272\n\t" \
  "ds_read_b128 v[32:35], v204 offset:49152\n\t" \
  "ds_read_b128 v[36:39], v204 offset:51200\n\t" \
  "ds_read_b128 v[40:43], v204 offset:53248\n\t" \
  "ds_read_b128 v[44:47], v204 offset:55296\n\t" \
  "ds_read_b128 v[144:147], v208 offset:128\n\t" \
  "ds_read_b128 v[148:151], v208 offset:144\n\t" \
  "ds_read_b128 v[152:155], v208 offset:384\n\t" \
  "ds_read_b128 v[156:159], v208 offset:400\n\t" \
  "ds_read_b128 v[48:51], v205 offset:49152\n\t" \
  "ds_read_b128 v[52:55], v205 offset:51200\n\t" \
  "ds_read_b128 v[56:59], v205 offset:53248\n\t" \
  "ds_read_b128 v[60:63], v205 offset:55296\n\t" \
  "v_lshlrev_b32 v176, 16, v0\n\t" \
  "v_and_b32 v177, s98, v0\n\t" \
  "v_lshlrev_b32 v178, 16, v1\n\t" \
  "v_and_b32 v179, s98, v1\n\t" \
  "v_lshlrev_b32 v180, 16, v2\n\t" \
  "v_and_b32 v181, s98, v2\n\t" \
  "v_lshlrev_b32 v182, 16, v3\n\t" \
  "v_and_b32 v183, s98, v3\n\t" \
  "v_pk_fma_f32 v[176:177], v[176:177], v[160:161], v[162:163]\n\t" \
  "v_pk_fma_f32 v[178:179], v[178:179], v[160:161], v[162:163]\n\t" \
  "v_pk_fma_f32 v[180:181], v[180:181], v[160:161], v[162:163]\n\t" \
  "v_pk_fma_f32 v[182:183], v[182:183], v[160:161], v[162:163]\n\t" \
  "s_waitcnt lgkmcnt(12)\n\t" \
  "s_nop 0\n\t" \
  "s_xor_b32 s97, %[pso], 0\n\t" \
  ".p2align 3\n\t" \
  "v_pk_fma_f32 v[176:177], v[176:177], v[128:129], v[136:137]\n\t" \
  "v_pk_fma_f32 v[178:179], v[178:179], v[130:131], v[138:139]\n\t" \
  "v_pk_fma_f32 v[180:181], v[180:181], v[132:133], v[140:141]\n\t" \
  "v_pk_fma_f32 v[182:183], v[182:183], v[134:135], v[142:143]\n\t" \
  "v_cvt_pk_bf16_f32 v0, v176, v177\n\t" \
  "v_cvt_pk_bf16_f32 v1, v178, v179\n\t" \
  "v_cvt_pk_bf16_f32 v2, v180, v181\n\t" \
  "v_cvt_pk_bf16_f32 v3, v182, v183\n\t" \
  "buffer_store_dwordx4 v[0:3], v214, %[ws], s97 offen sc1\n\t" \
  "v_lshlrev_b32 v176, 16, v4\n\t" \
  "v_and_b32 v177, s98, v4\n\t" \
  "v_lshlrev_b32 v178, 16, v5\n\t" \
  "v_and_b32 v179, s98, v5\n\t" \
  "v_lshlrev_b32 v180, 16, v6\n\t" \
  "v_and_b32 v181, s98, v6\n\t" \
  "v_lshlrev_b32 v182, 16, v7\n\t" \
  "v_and_b32 v183, s98, v7\n\t" \
  "v_pk_fma_f32 v[176:177], v[176:177], v[164:165], v[166:167]\n\t" \
  "v_pk_fma_f32 v[178:179], v[178:179], v[164:165], v[166:167]\n\t" \
  "v_pk_fma_f32 v[180:181], v[180:181], v[164:165], v[166:167]\n\t" \
  "v_pk_fma_f32 v[182:183], v[182:183], v[164:165], v[166:167]\n\t" \
  "s_xor_b32 s97, %[pso], 256\n\t" \
  ".p2align 3\n\t" \
  "v_pk_fma_f32 v[176:177], v[176:177], v[128:129], v[136:137]\n\t" \
  "v_pk_fma_f32 v[178:179], v[178:179], v[130:131], v[138:139]\n\t" \
  "v_pk_fma_f32 v[180:181], v[180:181], v[132:133], v[140:141]\n\t" \
  "v_pk_fma_f32 v[182:183], v[182:183], v[134:135], v[142:143]\n\t" \
  "v_cvt_pk_bf16_f32 v4, v176, v177\n\t" \
  "v_cvt_pk_bf16_f32 v5, v178, v179\n\t" \
  "v_cvt_pk_bf16_f32 v6, v180, v181\n\t" \
  "v_cvt_pk_bf16_f32 v7, v182, v183\n\t" \
  "buffer_store_dwordx4 v[4:7], v214, %[ws], s97 offen sc1\n\t" \
  "v_lshlrev_b32 v176, 16, v8\n\t" \
  "v_and_b32 v177, s98, v8\n\t" \
  "v_lshlrev_b32 v178, 16, v9\n\t" \
  "v_and_b32 v179, s98, v9\n\t" \
  "v_lshlrev_b32 v180, 16, v10\n\t" \
  "v_and_b32 v181, s98, v10\n\t" \
  "v_lshlrev_b32 v182, 16, v11\n\t" \
  "v_and_b32 v183, s98, v11\n\t" \
  "v_pk_fma_f32 v[176:177], v[176:177], v[168:169], v[170:171]\n\t" \
  "v_pk_fma_f32 v[178:179], v[178:179], v[168:169], v[170:171]\n\t" \
  "v_pk_fma_f32 v[180:181], v[180:181], v[168:169], v[170:171]\n\t" \
  "v_pk_fma_f32 v[182:183], v[182:183], v[168:169], v[170:171]\n\t" \
  "s_xor_b32 s97, %[pso], 512\n\t" \
  ".p2align 3\n\t" \
  "v_pk_fma_f32 v[176:177], v[176:177], v[128:129], v[136:137]\n\t" \
  "v_pk_fma_f32 v[178:179], v[178:179], v[130:131], v[138:139]\n\t" \
  "v_pk_fma_f32 v[180:181], v[180:181], v[132:133], v[140:141]\n\t" \
  "v_pk_fma_f32 v[182:183], v[182:183], v[134:135], v[142:143]\n\t" \
  "v_cvt_pk_bf16_f32 v8, v176, v177\n\t" \
  "v_cvt_pk_bf16_f32 v9, v178, v179\n\t" \
  "v_cvt_pk_bf16_f32 v10, v180, v181\n\t" \
  "v_cvt_pk_bf16_f32 v11, v182, v183\n\t" \
  "buffer_store_dwordx4 v[8:11], v214, %[ws], s97 offen sc1\n\t" \
  "v_lshlrev_b32 v176, 16, v12\n\t" \
  "v_and_b32 v177, s98, v12\n\t" \
  "v_lshlrev_b32 v178, 16, v13\n\t" \
  "v_and_b32 v179, s98, v13\n\t" \
  "v_lshlrev_b32 v180, 16, v14\n\t" \
  "v_and_b32 v181, s98, v14\n\t" \
  "v_lshlrev_b32 v182, 16, v15\n\t" \
  "v_and_b32 v183, s98, v15\n\t" \
  "v_pk_fma_f32 v[176:177], v[176:177], v[172:173], v[174:175]\n\t" \
  "v_pk_fma_f32 v[178:179], v[178:179], v[172:173], v[174:175]\n\t" \
  "v_pk_fma_f32 v[180:181], v[180:181], v[172:173], v[174:175]\n\t" \
  "v_pk_fma_f32 v[182:183], v[182:183], v[172:173], v[174:175]\n\t" \
  "s_xor_b32 s97, %[pso], 768\n\t" \
  ".p2align 3\n\t" \
  "v_pk_fma_f32 v[176:177], v[176:177], v[128:129], v[136:137]\n\t" \
  "v_pk_fma_f32 v[178:179], v[178:179], v[130:131], v[138:139]\n\t" \
  "v_pk_fma_f32 v[180:181], v[180:181], v[132:133], v[140:141]\n\t" \
  "v_pk_fma_f32 v[182:183], v[182:183], v[134:135], v[142:143]\n\t" \
  "v_cvt_pk_bf16_f32 v12, v176, v177\n\t" \
  "v_cvt_pk_bf16_f32 v13, v178, v179\n\t" \
  "v_cvt_pk_bf16_f32 v14, v180, v181\n\t" \
  "v_cvt_pk_bf16_f32 v15, v182, v183\n\t" \
  "buffer_store_dwordx4 v[12:15], v214, %[ws], s97 offen sc1\n\t" \
  "ds_read_b128 v[128:131], v208 offset:512\n\t" \
  "ds_read_b128 v[132:135], v208 offset:528\n\t" \
  "ds_read_b128 v[136:139], v208 offset:768\n\t" \
  "ds_read_b128 v[140:143], v208 offset:784\n\t" \
  FSTP("s_memrealtime s[94:95]\n\t") \
  FSTP("s_memtime s[92:93]\n\t") \
  "v_lshlrev_b32 v176, 16, v16\n\t" \
  "v_and_b32 v177, s98, v16\n\t" \
  "v_lshlrev_b32 v178, 16, v17\n\t" \
  "v_and_b32 v179, s98, v17\n\t" \
  "v_lshlrev_b32 v180, 16, v18\n\t" \
  "v_and_b32 v181, s98, v18\n\t" \
  "v_lshlrev_b32 v182, 16, v19\n\t" \
  "v_and_b32 v183, s98, v19\n\t" \
  "v_pk_fma_f32 v[176:177], v[176:177], v[160:161], v[162:163]\n\t" \
  "v_pk_fma_f32 v[178:179], v[178:179], v[160:161], v[162:163]\n\t" \
  "v_pk_fma_f32 v[180:181], v[180:181], v[160:161], v[162:163]\n\t" \
  "v_pk_fma_f32 v[182:183], v[182:183], v[160:161], v[162:163]\n\t" \
  "s_waitcnt lgkmcnt(8)\n\t" \
  "s_nop 0\n\t" \
  "v_pk_fma_f32 v[176:177], v[176:177], v[144:145], v[152:153]\n\t" \
  "v_pk_fma_f32 v[178:179], v[178:179], v[146:147], v[154:155]\n\t" \
  "v_pk_fma_f32 v[180:181], v[180:181], v[148:149], v[156:157]\n\t" \
  "v_pk_fma_f32 v[182:183], v[182:183], v[150:151], v[158:159]\n\t" \
  "v_cvt_pk_bf16_f32 v16, v176, v177\n\t" \
  "v_cvt_pk_bf16_f32 v17, v178, v179\n\t" \
  "v_cvt_pk_bf16_f32 v18, v180, v181\n\t" \
  "v_cvt_pk_bf16_f32 v19, v182, v183\n\t" \
  "s_add_u32 m0, %[mx], 98304\n\t" \
  "s_xor_b32 s96, %[c17], 0\n\t" \
  ".p2align 3\n\t" \
  "s_add_u32 s96, s96, %[sl2]\n\t" \
  "buffer_load_dwordx4 v212, %[sx], s96 offen lds\n\t" \
  ".p2align 3\n\t" \
  "v_lshlrev_b32 v176, 16, v20\n\t" \
  "v_and_b32 v177, s98, v20\n\t" \
  "v_lshlrev_b32 v178, 16, v21\n\t" \
  "v_and_b32 v179, s98, v21\n\t" \
  "v_lshlrev_b32 v180, 16, v22\n\t" \
  "v_and_b32 v181, s98, v22\n\t" \
  "v_lshlrev_b32 v182, 16, v23\n\t" \
  "v_and_b32 v183, s98, v23\n\t" \
  "v_pk_fma_f32 v[176:177], v[176:177], v[164:165], v[166:167]\n\t" \
  "v_pk_fma_f32 v[178:179], v[178:179], v[164:165], v[166:167]\n\t" \
  "v_pk_fma_f32 v[180:181], v[180:181], v[164:165], v[166:167]\n\t" \
  "v_pk_fma_f32 v[182:183], v[182:183], v[164:165], v[166:167]\n\t" \
  "v_pk_fma_f32 v[176:177], v[176:177], v[144:145], v[152:153]\n\t" \
  "v_pk_fma_f32 v[178:179], v[178:179], v[146:147], v[154:155]\n\t" \
  "v_pk_fma_f32 v[180:181], v[180:181], v[148:149], v[156:157]\n\t" \
  "v_pk_fma_f32 v[182:183], v[182:183], v[150:151], v[158:159]\n\t" \
  "v_cvt_pk_bf16_f32 v20, v176, v177\n\t" \
  "v_cvt_pk_bf16_f32 v21, v178, v179\n\t" \
  "v_cvt_pk_bf16_f32 v22, v180, v181\n\t" \
  "v_cvt_pk_bf16_f32 v23, v182, v183\n\t" \
  "buffer_load_dwordx4 v213, %[sx], s96 offen offset:1024 lds\n\t" \
  ".p2align 3\n\t" \
  "v_lshlrev_b32 v176, 16, v24\n\t" \
  "v_and_b32 v177, s98, v24\n\t" \
  "v_lshlrev_b32 v178, 16, v25\n\t" \
  "v_and_b32 v179, s98, v25\n\t" \
  "v_lshlrev_b32 v180, 16, v26\n\t" \
  "v_and_b32 v181, s98, v26\n\t" \
  "v_lshlrev_b32 v182, 16, v27\n\t" \
  "v_and_b32 v183, s98, v27\n\t" \
  "v_pk_fma_f32 v[176:177], v[176:177], v[168:169], v[170:171]\n\t" \
  "v_pk_fma_f32 v[178:179], v[178:179], v[168:169], v[170:171]\n\t" \
  "v_pk_fma_f32 v[180:181], v[180:181], v[168:169], v[170:171]\n\t" \
  "v_pk_fma_f32 v[182:183], v[182:183], v[168:169], v[170:171]\n\t" \
  "v_pk_fma_f32 v[176:177], v[176:177], v[144:145], v[152:153]\n\t" \
  "v_pk_fma_f32 v[178:179], v[178:179], v[146:147], v[154:155]\n\t" \
  "v_pk_fma_f32 v[180:181], v[180:181], v[148:149], v[156:157]\n\t" \
  "v_pk_fma_f32 v[182:183], v[182:183], v[150:151], v[158:159]\n\t" \
  "v_cvt_pk_bf16_f32 v24, v176, v177\n\t" \
  "v_cvt_pk_bf16_f32 v25, v178, v179\n\t" \
  "v_cvt_pk_bf16_f32 v26, v180, v181\n\t" \
  "v_cvt_pk_bf16_f32 v27, v182, v183\n\t" \
  "s_add_u32 m0, %[mx], 100352\n\t" \
  "s_xor_b32 s96, %[c17], 131072\n\t" \
  ".p2align 3\n\t" \
  "s_add_u32 s96, s96, %[sl2]\n\t" \
  "buffer_load_dwordx4 v212, %[sx], s96 offen lds\n\t" \
  ".p2align 3\n\t" \
  "v_lshlrev_b32 v176, 16, v28\n\t" \
  "v_and_b32 v177, s98, v28\n\t" \
  "v_lshlrev_b32 v178, 16, v29\n\t" \
  "v_and_b32 v179, s98, v29\n\t" \
  "v_lshlrev_b32 v180, 16, v30\n\t" \
  "v_and_b32 v181, s98, v30\n\t" \
  "v_lshlrev_b32 v182, 16, v31\n\t" \
  "v_and_b32 v183, s98, v31\n\t" \
  "v_pk_fma_f32 v[176:177], v[176:177], v[172:173], v[174:175]\n\t" \
  "v_pk_fma_f32 v[178:179], v[178:179], v[172:173], v[174:175]\n\t" \
  "v_pk_fma_f32 v[180:181], v[180:181], v[172:173], v[174:175]\n\t" \
  "v_pk_fma_f32 v[182:183], v[182:183], v[172:173], v[174:175]\n\t" \
  "v_pk_fma_f32 v[176:177], v[176:177], v[144:145], v[152:153]\n\t" \
  "v_pk_fma_f32 v[178:179], v[178:179], v[146:147], v[154:155]\n\t" \
  "v_pk_fma_f32 v[180:181], v[180:181], v[148:149], v[156:157]\n\t" \
  "v_pk_fma_f32 v[182:183], v[182:183], v[150:151], v[158:159]\n\t" \
  "v_cvt_pk_bf16_f32 v28, v176, v177\n\t" \
  "v_cvt_pk_bf16_f32 v29, v178, v179\n\t" \
  "v_cvt_pk_bf16_f32 v30, v180, v181\n\t" \
  "v_cvt_pk_bf16_f32 v31, v182, v183\n\t" \
  "buffer_load_dwordx4 v213, %[sx], s96 offen offset:1024 lds\n\t" \
  ".p2align 3\n\t" \
  "ds_read_b128 v[144:147], v208 offset:640\n\t" \
  "ds_read_b128 v[148:151], v208 offset:656\n\t" \
  "ds_read_b128 v[152:155], v208 offset:896\n\t" \
  "ds_read_b128 v[156:159], v208 offset:912\n\t" \
  FSTP("v_writelane_b32 %[stv], s94, 28\n\t") \
  FSTP("v_writelane_b32 %[stv], s92, 29\n\t") \
  FSTP("s_memrealtime s[94:95]\n\t") \
  FSTP("s_memtime s[92:93]\n\t") \
  "v_lshlrev_b32 v176, 16, v32\n\t" \
  "v_and_b32 v177, s98, v32\n\t" \
  "v_lshlrev_b32 v178, 16, v33\n\t" \
  "v_and_b32 v179, s98, v33\n\t" \
  "v_lshlrev_b32 v180, 16, v34\n\t" \
  "v_and_b32 v181, s98, v34\n\t" \
  "v_lshlrev_b32 v182, 16, v35\n\t" \
  "v_and_b32 v183, s98, v35\n\t" \
  "v_pk_fma_f32 v[176:177], v[176:177], v[160:161], v[162:163]\n\t" \
  "v_pk_fma_f32 v[178:179], v[178:179], v[160:161], v[162:163]\n\t" \
  "v_pk_fma_f32 v[180:181], v[180:181], v[160:161], v[162:163]\n\t" \
  "v_pk_fma_f32 v[182:183], v[182:183], v[160:161], v[162:163]\n\t" \
  "s_waitcnt lgkmcnt(4)\n\t" \
  "s_nop 0\n\t" \
  "v_pk_fma_f32 v[176:177], v[176:177], v[128:129], v[136:137]\n\t" \
  "v_pk_fma_f32 v[178:179], v[178:179], v[130:131], v[138:139]\n\t" \
  "v_pk_fma_f32 v[180:181], v[180:181], v[132:133], v[140:141]\n\t" \
  "v_pk_fma_f32 v[182:183], v[182:183], v[134:135], v[142:143]\n\t" \
  "v_cvt_pk_bf16_f32 v32, v176, v177\n\t" \
  "v_cvt_pk_bf16_f32 v33, v178, v179\n\t" \
  "v_cvt_pk_bf16_f32 v34, v180, v181\n\t" \
  "v_cvt_pk_bf16_f32 v35, v182, v183\n\t" \
  "s_add_u32 m0, %[mx], 102400\n\t" \
  "s_xor_b32 s96, %[c17], 262144\n\t" \
  ".p2align 3\n\t" \
  "s_add_u32 s96, s96, %[sl2]\n\t" \
  "buffer_load_dwordx4 v212, %[sx], s96 offen lds\n\t" \
  ".p2align 3\n\t" \
  "v_lshlrev_b32 v176, 16, v36\n\t" \
  "v_and_b32 v177, s98, v36\n\t" \
  "v_lshlrev_b32 v178, 16, v37\n\t" \
  "v_and_b32 v179, s98, v37\n\t" \
  "v_lshlrev_b32 v180, 16, v38\n\t" \
  "v_and_b32 v181, s98, v38\n\t" \
  "v_lshlrev_b32 v182, 16, v39\n\t" \
  "v_and_b32 v183, s98, v39\n\t" \
  "v_pk_fma_f32 v[176:177], v[176:177], v[164:165], v[166:167]\n\t" \
  "v_pk_fma_f32 v[178:179], v[178:179], v[164:165], v[166:167]\n\t" \
  "v_pk_fma_f32 v[180:181], v[180:181], v[164:165], v[166:167]\n\t" \
  "v_pk_fma_f32 v[182:183], v[182:183], v[164:165], v[166:167]\n\t" \
  "v_pk_fma_f32 v[176:177], v[176:177], v[128:129], v[136:137]\n\t" \
  "v_pk_fma_f32 v[178:179], v[178:179], v[130:131], v[138:139]\n\t" \
  "v_pk_fma_f32 v[180:181], v[180:181], v[132:133], v[140:141]\n\t" \
  "v_pk_fma_f32 v[182:183], v[182:183], v[134:135], v[142:143]\n\t" \
  "v_cvt_pk_bf16_f32 v36, v176, v177\n\t" \
  "v_cvt_pk_bf16_f32 v37, v178, v179\n\t" \
  "v_cvt_pk_bf16_f32 v38, v180, v181\n\t" \
  "v_cvt_pk_bf16_f32 v39, v182, v183\n\t" \
  "buffer_load_dwordx4 v213, %[sx], s96 offen offset:1024 lds\n\t" \
  ".p2align 3\n\t" \
  "v_lshlrev_b32 v176, 16, v40\n\t" \
  "v_and_b32 v177, s98, v40\n\t" \
  "v_lshlrev_b32 v178, 16, v41\n\t" \
  "v_and_b32 v179, s98, v41\n\t" \
  "v_lshlrev_b32 v180, 16, v42\n\t" \
  "v_and_b32 v181, s98, v42\n\t" \
  "v_lshlrev_b32 v182, 16, v43\n\t" \
  "v_and_b32 v183, s98, v43\n\t" \
  "v_pk_fma_f32 v[176:177], v[176:177], v[168:169], v[170:171]\n\t" \
  "v_pk_fma_f32 v[178:179], v[178:179], v[168:169], v[170:171]\n\t" \
  "v_pk_fma_f32 v[180:181], v[180:181], v[168:169], v[170:171]\n\t" \
  "v_pk_fma_f32 v[182:183], v[182:183], v[168:169], v[170:171]\n\t" \
  "v_pk_fma_f32 v[176:177], v[176:177], v[128:129], v[136:137]\n\t" \
  "v_pk_fma_f32 v[178:179], v[178:179], v[130:131], v[138:139]\n\t" \
  "v_pk_fma_f32 v[180:181], v[180:181], v[132:133], v[140:141]\n\t" \
  "v_pk_fma_f32 v[182:183], v[182:183], v[134:135], v[142:143]\n\t" \
  "v_cvt_pk_bf16_f32 v40, v176, v177\n\t" \
  "v_cvt_pk_bf16_f32 v41, v178, v179\n\t" \
  "v_cvt_pk_bf16_f32 v42, v180, v181\n\t" \
  "v_cvt_pk_bf16_f32 v43, v182, v183\n\t" \
  "s_add_u32 m0, %[mx], 104448\n\t" \
  "s_xor_b32 s96, %[c17], 393216\n\t" \
  ".p2align 3\n\t" \
  "s_add_u32 s96, s96, %[sl2]\n\t" \
  "buffer_load_dwordx4 v212, %[sx], s96 offen lds\n\t" \
  ".p2align 3\n\t" \
  "v_lshlrev_b32 v176, 16, v44\n\t" \
  "v_and_b32 v177, s98, v44\n\t" \
  "v_lshlrev_b32 v178, 16, v45\n\t" \
  "v_and_b32 v179, s98, v45\n\t" \
  "v_lshlrev_b32 v180, 16, v46\n\t" \
  "v_and_b32 v181, s98, v46\n\t" \
  "v_lshlrev_b32 v182, 16, v47\n\t" \
  "v_and_b32 v183, s98, v47\n\t" \
  "v_pk_fma_f32 v[176:177], v[176:177], v[172:173], v[174:175]\n\t" \
  "v_pk_fma_f32 v[178:179], v[178:179], v[172:173], v[174:175]\n\t" \
  "v_pk_fma_f32 v[180:181], v[180:181], v[172:173], v[174:175]\n\t" \
  "v_pk_fma_f32 v[182:183], v[182:183], v[172:173], v[174:175]\n\t" \
  "v_pk_fma_f32 v[176:177], v[176:177], v[128:129], v[136:137]\n\t" \
  "v_pk_fma_f32 v[178:179], v[178:179], v[130:131], v[138:139]\n\t" \
  "v_pk_fma_f32 v[180:181], v[180:181], v[132:133], v[140:141]\n\t" \
  "v_pk_fma_f32 v[182:183], v[182:183], v[134:135], v[142:143]\n\t" \
  "v_cvt_pk_bf16_f32 v44, v176, v177\n\t" \
  "v_cvt_pk_bf16_f32 v45, v178, v179\n\t" \
  "v_cvt_pk_bf16_f32 v46, v180, v181\n\t" \
  "v_cvt_pk_bf16_f32 v47, v182, v183\n\t" \
  "buffer_load_dwordx4 v213, %[sx], s96 offen offset:1024 lds\n\t" \
  ".p2align 3\n\t" \
  "ds_read_b128 v[128:131], v208 offset:1024\n\t" \
  "ds_read_b128 v[132:135], v208 offset:1040\n\t" \
  "ds_read_b128 v[136:139], v208 offset:1280\n\t" \
  "ds_read_b128 v[140:143], v208 offset:1296\n\t" \
  FSTP("v_writelane_b32 %[stv], s94, 30\n\t") \
  FSTP("v_writelane_b32 %[stv], s92, 31\n\t") \
  FSTP("s_memrealtime s[94:95]\n\t") \
  FSTP("s_memtime s[92:93]\n\t") \
  "v_lshlrev_b32 v176, 16, v48\n\t" \
  "v_and_b32 v177, s98, v48\n\t" \
  "v_lshlrev_b32 v178, 16, v49\n\t" \
  "v_and_b32 v179, s98, v49\n\t" \
  "v_lshlrev_b32 v180, 16, v50\n\t" \
  "v_and_b32 v181, s98, v50\n\t" \
  "v_lshlrev_b32 v182, 16, v51\n\t" \
  "v_and_b32 v183, s98, v51\n\t" \
  "v_pk_fma_f32 v[176:177], v[176:177], v[160:161], v[162:163]\n\t" \
  "v_pk_fma_f32 v[178:179], v[178:179], v[160:161], v[162:163]\n\t" \
  "v_pk_fma_f32 v[180:181], v[180:181], v[160:161], v[162:163]\n\t" \
  "v_pk_fma_f32 v[182:183], v[182:183], v[160:161], v[162:163]\n\t" \
  "s_waitcnt lgkmcnt(4)\n\t" \
  "s_nop 0\n\t" \
  "v_pk_fma_f32 v[176:177], v[176:177], v[144:145], v[152:153]\n\t" \
  "v_pk_fma_f32 v[178:179], v[178:179], v[146:147], v[154:155]\n\t" \
  "v_pk_fma_f32 v[180:181], v[180:181], v[148:149], v[156:157]\n\t" \
  "v_pk_fma_f32 v[182:183], v[182:183], v[150:151], v[158:159]\n\t" \
  "v_cvt_pk_bf16_f32 v48, v176, v177\n\t" \
  "v_cvt_pk_bf16_f32 v49, v178, v179\n\t" \
  "v_cvt_pk_bf16_f32 v50, v180, v181\n\t" \
  "v_cvt_pk_bf16_f32 v51, v182, v183\n\t" \
  "s_add_u32 m0, %[mw], 49152\n\t" \
  "s_mov_b32 s96, %[w1]\n\t" \
  ".p2align 3\n\t" \
  "buffer_load_dwordx4 v200, %[sw], s96 offen offset:0 lds\n\t" \
  ".p2align 3\n\t" \
  "ds_read_b64_tr_b16 a[128:129], v184 offset:0\n\t" \
  "ds_read_b64_tr_b16 a[130:131], v184 offset:1024\n\t" \
  "ds_read_b64_tr_b16 a[132:133], v185 offset:0\n\t" \
  "ds_read_b64_tr_b16 a[134:135], v185 offset:1024\n\t" \
  "v_lshlrev_b32 v176, 16, v52\n\t" \
  "v_and_b32 v177, s98, v52\n\t" \
  "v_lshlrev_b32 v178, 16, v53\n\t" \
  "v_and_b32 v179, s98, v53\n\t" \
  "v_lshlrev_b32 v180, 16, v54\n\t" \
  "v_and_b32 v181, s98, v54\n\t" \
  "v_lshlrev_b32 v182, 16, v55\n\t" \
  "v_and_b32 v183, s98, v55\n\t" \
  "v_pk_fma_f32 v[176:177], v[176:177], v[164:165], v[166:167]\n\t" \
  "v_pk_fma_f32 v[178:179], v[178:179], v[164:165], v[166:167]\n\t" \
  "v_pk_fma_f32 v[180:181], v[180:181], v[164:165], v[166:167]\n\t" \
  "v_pk_fma_f32 v[182:183], v[182:183], v[164:165], v[166:167]\n\t" \
  "v_pk_fma_f32 v[176:177], v[176:177], v[144:145], v[152:153]\n\t" \
  "v_pk_fma_f32 v[178:179], v[178:179], v[146:147], v[154:155]\n\t" \
  "v_pk_fma_f32 v[180:181], v[180:181], v[148:149], v[156:157]\n\t" \
  "v_pk_fma_f32 v[182:183], v[182:183], v[150:151], v[158:159]\n\t" \
  "v_cvt_pk_bf16_f32 v52, v176, v177\n\t" \
  "v_cvt_pk_bf16_f32 v53, v178, v179\n\t" \
  "v_cvt_pk_bf16_f32 v54, v180, v181\n\t" \
  "v_cvt_pk_bf16_f32 v55, v182, v183\n\t" \
  "buffer_load_dwordx4 v201, %[sw], s96 offen offset:1024 lds\n\t" \
  ".p2align 3\n\t" \
  "ds_read_b64_tr_b16 a[136:137], v186 offset:0\n\t" \
  "ds_read_b64_tr_b16 a[138:139], v186 offset:1024\n\t" \
  "ds_read_b64_tr_b16 a[140:141], v187 offset:0\n\t" \
  "ds_read_b64_tr_b16 a[142:143], v187 offset:1024\n\t" \
  "v_lshlrev_b32 v176, 16, v56\n\t" \
  "v_and_b32 v177, s98, v56\n\t" \
  "v_lshlrev_b32 v178, 16, v57\n\t" \
  "v_and_b32 v179, s98, v57\n\t" \
  "v_lshlrev_b32 v180, 16, v58\n\t" \
  "v_and_b32 v181, s98, v58\n\t" \
  "v_lshlrev_b32 v182, 16, v59\n\t" \
  "v_and_b32 v183, s98, v59\n\t" \
  "v_pk_fma_f32 v[176:177], v[176:177], v[168:169], v[170:171]\n\t" \
  "v_pk_fma_f32 v[178:179], v[178:179], v[168:169], v[170:171]\n\t" \
  "v_pk_fma_f32 v[180:181], v[180:181], v[168:169], v[170:171]\n\t" \
  "v_pk_fma_f32 v[182:183], v[182:183], v[168:169], v[170:171]\n\t" \
  "v_pk_fma_f32 v[176:177], v[176:177], v[144:145], v[152:153]\n\t" \
  "v_pk_fma_f32 v[178:179], v[178:179], v[146:147], v[154:155]\n\t" \
  "v_pk_fma_f32 v[180:181], v[180:181], v[148:149], v[156:157]\n\t" \
  "v_pk_fma_f32 v[182:183], v[182:183], v[150:151], v[158:159]\n\t" \
  "v_cvt_pk_bf16_f32 v56, v176, v177\n\t" \
  "v_cvt_pk_bf16_f32 v57, v178, v179\n\t" \
  "v_cvt_pk_bf16_f32 v58, v180, v181\n\t" \
  "v_cvt_pk_bf16_f32 v59, v182, v183\n\t" \
  "buffer_load_dwordx4 v202, %[sw], s96 offen offset:2048 lds\n\t" \
  ".p2align 3\n\t" \
  "ds_read_b64_tr_b16 a[144:145], v188 offset:0\n\t" \
  "ds_read_b64_tr_b16 a[146:147], v188 offset:1024\n\t" \
  "ds_read_b64_tr_b16 a[148:149], v189 offset:0\n\t" \
  "ds_read_b64_tr_b16 a[150:151], v189 offset:1024\n\t" \
  "v_lshlrev_b32 v176, 16, v60\n\t" \
  "v_and_b32 v177, s98, v60\n\t" \
  "v_lshlrev_b32 v178, 16, v61\n\t" \
  "v_and_b32 v179, s98, v61\n\t" \
  "v_lshlrev_b32 v180, 16, v62\n\t" \
  "v_and_b32 v181, s98, v62\n\t" \
  "v_lshlrev_b32 v182, 16, v63\n\t" \
  "v_and_b32 v183, s98, v63\n\t" \
  "v_pk_fma_f32 v[176:177], v[176:177], v[172:173], v[174:175]\n\t" \
  "v_pk_fma_f32 v[178:179], v[178:179], v[172:173], v[174:175]\n\t" \
  "v_pk_fma_f32 v[180:181], v[180:181], v[172:173], v[174:175]\n\t" \
  "v_pk_fma_f32 v[182:183], v[182:183], v[172:173], v[174:175]\n\t" \
  "v_pk_fma_f32 v[176:177], v[176:177], v[144:145], v[152:153]\n\t" \
  "v_pk_fma_f32 v[178:179], v[178:179], v[146:147], v[154:155]\n\t" \
  "v_pk_fma_f32 v[180:181], v[180:181], v[148:149], v[156:157]\n\t" \
  "v_pk_fma_f32 v[182:183], v[182:183], v[150:151], v[158:159]\n\t" \
  "v_cvt_pk_bf16_f32 v60, v176, v177\n\t" \
  "v_cvt_pk_bf16_f32 v61, v178, v179\n\t" \
  "v_cvt_pk_bf16_f32 v62, v180, v181\n\t" \
  "v_cvt_pk_bf16_f32 v63, v182, v183\n\t" \
  "buffer_load_dwordx4 v203, %[sw], s96 offen offset:3072 lds\n\t" \
  ".p2align 3\n\t" \
  "ds_read_b64_tr_b16 a[152:153], v190 offset:0\n\t" \
  "ds_read_b64_tr_b16 a[154:155], v190 offset:1024\n\t" \
  "ds_read_b64_tr_b16 a[156:157], v191 offset:0\n\t" \
  "ds_read_b64_tr_b16 a[158:159], v191 offset:1024\n\t" \
  "ds_read_b128 v[144:147], v208 offset:1152\n\t" \
  "ds_read_b128 v[148:151], v208 offset:1168\n\t" \
  "ds_read_b128 v[152:155], v208 offset:1408\n\t" \
  "ds_read_b128 v[156:159], v208 offset:1424\n\t" \
  FSTP("v_writelane_b32 %[stv], s94, 32\n\t") \
  FSTP("v_writelane_b32 %[stv], s92, 33\n\t") \
  FSTP("s_memrealtime s[94:95]\n\t") \
  FSTP("s_memtime s[92:93]\n\t") \
  "s_waitcnt vmcnt(12)\n\t" \
  "s_nop 0\n\t" \
  "s_mov_b64 exec, 1\n\t" \
  "buffer_store_dword v211, off, %[ws], %[fw] sc1\n\t" \
  "s_mov_b64 exec, -1\n\t" \
  FSTP("v_writelane_b32 %[stv], s94, 34\n\t") \
  FSTP("v_writelane_b32 %[stv], s92, 35\n\t") \
  FSTP("s_memrealtime s[94:95]\n\t") \
  FSTP("s_memtime s[92:93]\n\t") \
  "s_waitcnt lgkmcnt(4)\n\t" \
  "v_mfma_f32_16x16x32_bf16 a[0:3], a[128:131], v[0:3], 0\n\t" \
  "s_add_u32 m0, %[mw], 98304\n\t" \
  "s_mov_b32 s96, %[w2]\n\t" \
  "v_mfma_f32_16x16x32_bf16 a[4:7], a[128:131], v[4:7], 0\n\t" \
  "buffer_load_dwordx4 v200, %[sw], s96 offen offset:0 lds\n\t" \
  "ds_read_b64_tr_b16 a[160:161], v184 offset:8192\n\t" \
  "v_mfma_f32_16x16x32_bf16 a[8:11], a[128:131], v[8:11], 0\n\t" \
  "ds_read_b64_tr_b16 a[162:163], v184 offset:9216\n\t" \
  "v_mfma_f32_16x16x32_bf16 a[12:15], a[128:131], v[12:15], 0\n\t" \
  "buffer_load_dwordx4 v201, %[sw], s96 offen offset:1024 lds\n\t" \
  "ds_read_b64_tr_b16 a[164:165], v185 offset:8192\n\t" \
  "v_mfma_f32_16x16x32_bf16 a[28:31], a[132:135], v[12:15], 0\n\t" \
  "ds_read_b64_tr_b16 a[166:167], v185 offset:9216\n\t" \
  "v_mfma_f32_16x16x32_bf16 a[24:27], a[132:135], v[8:11], 0\n\t" \
  "buffer_load_dwordx4 v202, %[sw], s96 offen offset:2048 lds\n\t" \
  "ds_read_b64_tr_b16 a[168:169], v186 offset:8192\n\t" \
  "v_mfma_f32_16x16x32_bf16 a[20:23], a[132:135], v[4:7], 0\n\t" \
  "ds_read_b64_tr_b16 a[170:171], v186 offset:9216\n\t" \
  "v_mfma_f32_16x16x32_bf16 a[16:19], a[132:135], v[0:3], 0\n\t" \
  "buffer_load_dwordx4 v203, %[sw], s96 offen offset:3072 lds\n\t" \
  "ds_read_b64_tr_b16 a[172:173], v187 offset:8192\n\t" \
  "v_mfma_f32_16x16x32_bf16 a[32:35], a[136:139], v[0:3], 0\n\t" \
  "s_add_u32 m0, %[mx], 0\n\t" \
  "s_xor_b32 s96, %[c17], 0\n\t" \
  "s_waitcnt vmcnt(9)\n\t" \
  "ds_read_b128 v[64:67], v206 offset:0\n\t" \
  "ds_read_b128 v[68:71], v206 offset:2048\n\t" \
  "ds_read_b128 v[72:75], v206 offset:4096\n\t" \
  "ds_read_b128 v[76:79], v206 offset:6144\n\t" \
  "s_waitcnt lgkmcnt(0)\n\t" \
  "v_lshlrev_b32 v176, 16, v64\n\t" \
  "v_and_b32 v177, s98, v64\n\t" \
  "v_lshlrev_b32 v178, 16, v65\n\t" \
  "v_and_b32 v179, s98, v65\n\t" \
  "v_lshlrev_b32 v180, 16, v66\n\t" \
  "v_and_b32 v181, s98, v66\n\t" \
  "v_lshlrev_b32 v182, 16, v67\n\t" \
  "v_and_b32 v183, s98, v67\n\t" \
  "v_fma_f32 v176, v176, v160, v162\n\t" \
  "v_fma_f32 v177, v177, v160, v162\n\t" \
  "v_fma_f32 v178, v178, v160, v162\n\t" \
  "v_fma_f32 v179, v179, v160, v162\n\t" \
  "v_fma_f32 v180, v180, v160, v162\n\t" \
  "v_fma_f32 v181, v181, v160, v162\n\t" \
  ".Lhs0:\n\ts_cbranch_execz .Lhs0n\n\t" \
  "v_fma_f32 v182, v182, v160, v162\n\t" \
  "v_fma_f32 v183, v183, v160, v162\n\t" \
  "v_fma_f32 v176, v176, v128, v136\n\t" \
  "v_mfma_f32_16x16x32_bf16 a[36:39], a[136:139], v[4:7], 0\n\t" \
  "ds_read_b64_tr_b16 a[174:175], v187 offset:9216\n\t" \
  "v_fma_f32 v177, v177, v129, v137\n\t" \
  "v_fma_f32 v178, v178, v130, v138\n\t" \
  "v_mfma_f32_16x16x32_bf16 a[40:43], a[136:139], v[8:11], 0\n\t" \
  "s_add_u32 s96, s96, %[sl3]\n\t" \
  "buffer_load_dwordx4 v212, %[sx], s96 offen lds\n\t" \
  "v_fma_f32 v179, v179, v131, v139\n\t" \
  "v_fma_f32 v180, v180, v132, v140\n\t" \
  "v_mfma_f32_16x16x32_bf16 a[44:47], a[136:139], v[12:15], 0\n\t" \
  "ds_read_b64_tr_b16 a[176:177], v188 offset:8192\n\t" \
  "v_fma_f32 v181, v181, v133, v141\n\t" \
  "v_fma_f32 v182, v182, v134, v142\n\t" \
  "v_fma_f32 v183, v183, v135, v143\n\t" \
  "v_mfma_f32_16x16x32_bf16 a[60:63], a[140:143], v[12:15], 0\n\t" \
  "buffer_load_dwordx4 v213, %[sx], s96 offen offset:1024 lds\n\t" \
  "ds_read_b64_tr_b16 a[178:179], v188 offset:9216\n\t" \
  "v_cvt_pk_bf16_f32 v64, v176, v177\n\t" \
  "v_cvt_pk_bf16_f32 v65, v178, v179\n\t" \
  "v_mfma_f32_16x16x32_bf16 a[56:59], a[140:143], v[8:11], 0\n\t" \
  "ds_read_b64_tr_b16 a[180:181], v189 offset:8192\n\t" \
  "v_cvt_pk_bf16_f32 v66, v180, v181\n\t" \
  "v_cvt_pk_bf16_f32 v67, v182, v183\n\t" \
  "v_mfma_f32_16x16x32_bf16 a[52:55], a[140:143], v[4:7], 0\n\t" \
  "s_add_u32 m0, %[mx], 2048\n\t" \
  "s_xor_b32 s96, %[c17], 131072\n\t" \
  "v_lshlrev_b32 v176, 16, v68\n\t" \
  "v_and_b32 v177, s98, v68\n\t" \
  "v_mfma_f32_16x16x32_bf16 a[48:51], a[140:143], v[0:3], 0\n\t" \
  "s_add_u32 s96, s96, %[sl3]\n\t" \
  "buffer_load_dwordx4 v212, %[sx], s96 offen lds\n\t" \
  "v_lshlrev_b32 v178, 16, v69\n\t" \
  "v_and_b32 v179, s98, v69\n\t" \
  "v_lshlrev_b32 v180, 16, v70\n\t" \
  "v_mfma_f32_16x16x32_bf16 a[64:67], a[144:147], v[0:3], 0\n\t" \
  "ds_read_b64_tr_b16 a[182:183], v189 offset:9216\n\t" \
  "v_and_b32 v181, s98, v70\n\t" \
  "v_lshlrev_b32 v182, 16, v71\n\t" \
  "v_mfma_f32_16x16x32_bf16 a[68:71], a[144:147], v[4:7], 0\n\t" \
  "buffer_load_dwordx4 v213, %[sx], s96 offen offset:1024 lds\n\t" \
  "ds_read_b64_tr_b16 a[184:185], v190 offset:8192\n\t" \
  "v_and_b32 v183, s98, v71\n\t" \
  "v_fma_f32 v176, v176, v164, v166\n\t" \
  "v_mfma_f32_16x16x32_bf16 a[72:75], a[144:147], v[8:11], 0\n\t" \
  "ds_read_b64_tr_b16 a[186:187], v190 offset:9216\n\t" \
  "v_fma_f32 v177, v177, v164, v166\n\t" \
  "v_fma_f32 v178, v178, v164, v166\n\t" \
  "v_fma_f32 v179, v179, v164, v166\n\t" \
  "v_mfma_f32_16x16x32_bf16 a[76:79], a[144:147], v[12:15], 0\n\t" \
  "s_add_u32 m0, %[mx], 4096\n\t" \
  "s_xor_b32 s96, %[c17], 262144\n\t" \
  "v_fma_f32 v180, v180, v164, v166\n\t" \
  "v_fma_f32 v181, v181, v164, v166\n\t" \
  "v_mfma_f32_16x16x32_bf16 a[92:95], a[148:151], v[12:15], 0\n\t" \
  "ds_read_b64_tr_b16 a[188:189], v191 offset:8192\n\t" \
  "v_fma_f32 v182, v182, v164, v166\n\t" \
  "v_fma_f32 v183, v183, v164, v166\n\t" \
  "v_mfma_f32_16x16x32_bf16 a[88:91], a[148:151], v[8:11], 0\n\t" \
  "s_add_u32 s96, s96, %[sl3]\n\t" \
  "buffer_load_dwordx4 v212, %[sx], s96 offen lds\n\t" \
  "v_fma_f32 v176, v176, v128, v136\n\t" \
  "v_fma_f32 v177, v177, v129, v137\n\t" \
  "v_mfma_f32_16x16x32_bf16 a[84:87], a[148:151], v[4:7], 0\n\t" \
  "buffer_load_dwordx4 v213, %[sx], s96 offen offset:1024 lds\n\t" \
  "ds_read_b64_tr_b16 a[190:191], v191 offset:9216\n\t" \
  "v_fma_f32 v178, v178, v130, v138\n\t" \
  "v_fma_f32 v179, v179, v131, v139\n\t" \
  "v_mfma_f32_16x16x32_bf16 a[80:83], a[148:151], v[0:3], 0\n\t" \
  "v_fma_f32 v180, v180, v132, v140\n\t" \
  "v_fma_f32 v181, v181, v133, v141\n\t" \
  "v_mfma_f32_16x16x32_bf16 a[96:99], a[152:155], v[0:3], 0\n\t" \
  "s_add_u32 m0, %[mx], 6144\n\t" \
  "s_xor_b32 s96, %[c17], 393216\n\t" \
  "v_fma_f32 v182, v182, v134, v142\n\t" \
  "v_fma_f32 v183, v183, v135, v143\n\t" \
  "v_mfma_f32_16x16x32_bf16 a[100:103], a[152:155], v[4:7], 0\n\t" \
  "v_cvt_pk_bf16_f32 v68, v176, v177\n\t" \
  "v_cvt_pk_bf16_f32 v69, v178, v179\n\t" \
  "v_mfma_f32_16x16x32_bf16 a[104:107], a[152:155], v[8:11], 0\n\t" \
  "s_add_u32 s96, s96, %[sl3]\n\t" \
  "buffer_load_dwordx4 v212, %[sx], s96 offen lds\n\t" \
  "v_cvt_pk_bf16_f32 v70, v180, v181\n\t" \
  "v_cvt_pk_bf16_f32 v71, v182, v183\n\t" \
  "v_mfma_f32_16x16x32_bf16 a[108:111], a[152:155], v[12:15], 0\n\t" \
  "v_lshlrev_b32 v176, 16, v72\n\t" \
  "v_and_b32 v177, s98, v72\n\t" \
  "v_mfma_f32_16x16x32_bf16 a[124:127], a[156:159], v[12:15], 0\n\t" \
  "buffer_load_dwordx4 v213, %[sx], s96 offen offset:1024 lds\n\t" \
  "v_lshlrev_b32 v178, 16, v73\n\t" \
  "v_and_b32 v179, s98, v73\n\t" \
  "v_mfma_f32_16x16x32_bf16 a[120:123], a[156:159], v[8:11], 0\n\t" \
  "v_lshlrev_b32 v180, 16, v74\n\t" \
  "v_and_b32 v181, s98, v74\n\t" \
  "v_mfma_f32_16x16x32_bf16 a[116:119], a[156:159], v[4:7], 0\n\t" \
  "v_lshlrev_b32 v182, 16, v75\n\t" \
  "v_and_b32 v183, s98, v75\n\t" \
  "v_mfma_f32_16x16x32_bf16 a[112:115], a[156:159], v[0:3], 0\n\t" \
  FSTP("v_writelane_b32 %[stv], s94, 36\n\t") \
  FSTP("v_writelane_b32 %[stv], s92, 37\n\t") \
  FSTP("s_memrealtime s[94:95]\n\t") \
  FSTP("s_memtime s[92:93]\n\t") \
  "s_waitcnt vmcnt(13) lgkmcnt(0)\n\t" \
  "s_barrier\n\t" \
  "v_mfma_f32_16x16x32_bf16 a[0:3], a[160:163], v[16:19], a[0:3]\n\t" \
  "s_add_u32 m0, %[mw], 0\n\t" \
  "s_mov_b32 s96, %[w3]\n\t" \
  "v_fma_f32 v176, v176, v168, v170\n\t" \
  "v_fma_f32 v177, v177, v168, v170\n\t" \
  "v_fma_f32 v178, v178, v168, v170\n\t" \
  "v_mfma_f32_16x16x32_bf16 a[4:7], a[160:163], v[20:23], a[4:7]\n\t" \
  "ds_read_b64_tr_b16 a[128:129], v184 offset:49152\n\t" \
  "v_fma_f32 v179, v179, v168, v170\n\t" \
  "v_fma_f32 v180, v180, v168, v170\n\t" \
  "v_mfma_f32_16x16x32_bf16 a[8:11], a[160:163], v[24:27], a[8:11]\n\t" \
  "ds_read_b64_tr_b16 a[130:131], v184 offset:50176\n\t" \
  "v_fma_f32 v181, v181, v168, v170\n\t" \
  "v_fma_f32 v182, v182, v168, v170\n\t" \
  "v_mfma_f32_16x16x32_bf16 a[12:15], a[160:163], v[28:31], a[12:15]\n\t" \
  "ds_read_b64_tr_b16 a[132:133], v185 offset:49152\n\t" \
  "v_fma_f32 v183, v183, v168, v170\n\t" \
  "v_fma_f32 v176, v176, v128, v136\n\t" \
  "v_fma_f32 v177, v177, v129, v137\n\t" \
  "v_mfma_f32_16x16x32_bf16 a[28:31], a[164:167], v[28:31], a[28:31]\n\t" \
  "ds_read_b64_tr_b16 a[134:135], v185 offset:50176\n\t" \
  "v_fma_f32 v178, v178, v130, v138\n\t" \
  "v_fma_f32 v179, v179, v131, v139\n\t" \
  "v_mfma_f32_16x16x32_bf16 a[24:27], a[164:167], v[24:27], a[24:27]\n\t" \
  "buffer_load_dwordx4 v200, %[sw], s96 offen offset:0 lds\n\t" \
  "ds_read_b64_tr_b16 a[136:137], v186 offset:49152\n\t" \
  "v_fma_f32 v180, v180, v132, v140\n\t" \
  "v_fma_f32 v181, v181, v133, v141\n\t" \
  "v_mfma_f32_16x16x32_bf16 a[20:23], a[164:167], v[20:23], a[20:23]\n\t" \
  "ds_read_b64_tr_b16 a[138:139], v186 offset:50176\n\t" \
  "v_fma_f32 v182, v182, v134, v142\n\t" \
  "v_fma_f32 v183, v183, v135, v143\n\t" \
  "v_mfma_f32_16x16x32_bf16 a[16:19], a[164:167], v[16:19], a[16:19]\n\t" \
  "ds_read_b64_tr_b16 a[140:141], v187 offset:49152\n\t" \
  "v_cvt_pk_bf16_f32 v72, v176, v177\n\t" \
  "v_cvt_pk_bf16_f32 v73, v178, v179\n\t" \
  "v_cvt_pk_bf16_f32 v74, v180, v181\n\t" \
  "v_mfma_f32_16x16x32_bf16 a[32:35], a[168:171], v[16:19], a[32:35]\n\t" \
  "ds_read_b64_tr_b16 a[142:143], v187 offset:50176\n\t" \
  "v_cvt_pk_bf16_f32 v75, v182, v183\n\t" \
  "ds_read_b128 v[80:83], v207 offset:0\n\t" \
  "v_mfma_f32_16x16x32_bf16 a[36:39], a[168:171], v[20:23], a[36:39]\n\t" \
  "ds_read_b64_tr_b16 a[144:145], v188 offset:49152\n\t" \
  "ds_read_b128 v[84:87], v207 offset:2048\n\t" \
  "ds_read_b128 v[88:91], v207 offset:4096\n\t" \
  "v_mfma_f32_16x16x32_bf16 a[40:43], a[168:171], v[24:27], a[40:43]\n\t" \
  "buffer_load_dwordx4 v201, %[sw], s96 offen offset:1024 lds\n\t" \
  "ds_read_b64_tr_b16 a[146:147], v188 offset:50176\n\t" \
  "ds_read_b128 v[92:95], v207 offset:6144\n\t" \
  "v_lshlrev_b32 v176, 16, v76\n\t" \
  "v_mfma_f32_16x16x32_bf16 a[44:47], a[168:171], v[28:31], a[44:47]\n\t" \
  "ds_read_b64_tr_b16 a[148:149], v189 offset:49152\n\t" \
  "v_and_b32 v177, s98, v76\n\t" \
  "v_lshlrev_b32 v178, 16, v77\n\t" \
  "v_and_b32 v179, s98, v77\n\t" \
  "v_mfma_f32_16x16x32_bf16 a[60:63], a[172:175], v[28:31], a[60:63]\n\t" \
  "ds_read_b64_tr_b16 a[150:151], v189 offset:50176\n\t" \
  "v_lshlrev_b32 v180, 16, v78\n\t" \
  "v_and_b32 v181, s98, v78\n\t" \
  "v_mfma_f32_16x16x32_bf16 a[56:59], a[172:175], v[24:27], a[56:59]\n\t" \
  "ds_read_b64_tr_b16 a[152:153], v190 offset:49152\n\t" \
  "v_lshlrev_b32 v182, 16, v79\n\t" \
  "v_and_b32 v183, s98, v79\n\t" \
  "v_mfma_f32_16x16x32_bf16 a[52:55], a[172:175], v[20:23], a[52:55]\n\t" \
  "ds_read_b64_tr_b16 a[154:155], v190 offset:50176\n\t" \
  "v_fma_f32 v176, v176, v172, v174\n\t" \
  "v_fma_f32 v177, v177, v172, v174\n\t" \
  "v_mfma_f32_16x16x32_bf16 a[48:51], a[172:175], v[16:19], a[48:51]\n\t" \
  "buffer_load_dwordx4 v202, %[sw], s96 offen offset:2048 lds\n\t" \
  "ds_read_b64_tr_b16 a[156:157], v191 offset:49152\n\t" \
  "v_fma_f32 v178, v178, v172, v174\n\t" \
  "v_fma_f32 v179, v179, v172, v174\n\t" \
  "v_fma_f32 v180, v180, v172, v174\n\t" \
  "v_mfma_f32_16x16x32_bf16 a[64:67], a[176:179], v[16:19], a[64:67]\n\t" \
  "ds_read_b64_tr_b16 a[158:159], v191 offset:50176\n\t" \
  "v_fma_f32 v181, v181, v172, v174\n\t" \
  "v_fma_f32 v182, v182, v172, v174\n\t" \
  "v_mfma_f32_16x16x32_bf16 a[68:71], a[176:179], v[20:23], a[68:71]\n\t" \
  "v_fma_f32 v183, v183, v172, v174\n\t" \
  "v_fma_f32 v176, v176, v128, v136\n\t" \
  "v_mfma_f32_16x16x32_bf16 a[72:75], a[176:179], v[24:27], a[72:75]\n\t" \
  "v_fma_f32 v177, v177, v129, v137\n\t" \
  "v_fma_f32 v178, v178, v130, v138\n\t" \
  "v_fma_f32 v179, v179, v131, v139\n\t" \
  "v_mfma_f32_16x16x32_bf16 a[76:79], a[176:179], v[28:31], a[76:79]\n\t" \
  "v_fma_f32 v180, v180, v132, v140\n\t" \
  "v_fma_f32 v181, v181, v133, v141\n\t" \
  "v_mfma_f32_16x16x32_bf16 a[92:95], a[180:183], v[28:31], a[92:95]\n\t" \
  "buffer_load_dwordx4 v203, %[sw], s96 offen offset:3072 lds\n\t" \
  "v_fma_f32 v182, v182, v134, v142\n\t" \
  "v_fma_f32 v183, v183, v135, v143\n\t" \
  "v_mfma_f32_16x16x32_bf16 a[88:91], a[180:183], v[24:27], a[88:91]\n\t" \
  "v_cvt_pk_bf16_f32 v76, v176, v177\n\t" \
  "v_cvt_pk_bf16_f32 v77, v178, v179\n\t" \
  "v_mfma_f32_16x16x32_bf16 a[84:87], a[180:183], v[20:23], a[84:87]\n\t" \
  "v_cvt_pk_bf16_f32 v78, v180, v181\n\t" \
  "v_cvt_pk_bf16_f32 v79, v182, v183\n\t" \
  "v_mfma_f32_16x16x32_bf16 a[80:83], a[180:183], v[16:19], a[80:83]\n\t" \
  "ds_read_b128 v[128:131], v208 offset:1536\n\t" \
  "ds_read_b128 v[132:135], v208 offset:1552\n\t" \
  "v_mfma_f32_16x16x32_bf16 a[96:99], a[184:187], v[16:19], a[96:99]\n\t" \
  "ds_read_b128 v[136:139], v208 offset:1792\n\t" \
  "ds_read_b128 v[140:143], v208 offset:1808\n\t" \
  "v_mfma_f32_16x16x32_bf16 a[100:103], a[184:187], v[20:23], a[100:103]\n\t" \
  "global_load_dword %[pf], v215, %[wq]\n\t" \
  "s_waitcnt lgkmcnt(10)\n\t" \
  "v_lshlrev_b32 v176, 16, v80\n\t" \
  "v_and_b32 v177, s98, v80\n\t" \
  "v_mfma_f32_16x16x32_bf16 a[104:107], a[184:187], v[24:27], a[104:107]\n\t" \
  "v_lshlrev_b32 v178, 16, v81\n\t" \
  "v_and_b32 v179, s98, v81\n\t" \
  "v_mfma_f32_16x16x32_bf16 a[108:111], a[184:187], v[28:31], a[108:111]\n\t" \
  "v_lshlrev_b32 v180, 16, v82\n\t" \
  "v_and_b32 v181, s98, v82\n\t" \
  "v_mfma_f32_16x16x32_bf16 a[124:127], a[188:191], v[28:31], a[124:127]\n\t" \
  "v_lshlrev_b32 v182, 16, v83\n\t" \
  "v_and_b32 v183, s98, v83\n\t" \
  "v_mfma_f32_16x16x32_bf16 a[120:123], a[188:191], v[24:27], a[120:123]\n\t" \
  "v_fma_f32 v176, v176, v160, v162\n\t" \
  "v_fma_f32 v177, v177, v160, v162\n\t" \
  "v_mfma_f32_16x16x32_bf16 a[116:119], a[188:191], v[20:23], a[116:119]\n\t" \
  "v_fma_f32 v178, v178, v160, v162\n\t" \
  "v_fma_f32 v179, v179, v160, v162\n\t" \
  "v_mfma_f32_16x16x32_bf16 a[112:115], a[188:191], v[16:19], a[112:115]\n\t" \
  FSTP("v_writelane_b32 %[stv], s94, 38\n\t") \
  FSTP("v_writelane_b32 %[stv], s92, 39\n\t") \
  FSTP("s_memrealtime s[94:95]\n\t") \
  FSTP("s_memtime s[92:93]\n\t") \
  "s_waitcnt lgkmcnt(4)\n\t" \
  "v_mfma_f32_16x16x32_bf16 a[0:3], a[128:131], v[32:35], a[0:3]\n\t" \
  "global_load_dword %[pf], v217, %[kv]\n\t" \
  "ds_read_b64_tr_b16 a[160:161], v184 offset:57344\n\t" \
  "v_fma_f32 v180, v180, v160, v162\n\t" \
  "v_fma_f32 v181, v181, v160, v162\n\t" \
  "v_fma_f32 v182, v182, v160, v162\n\t" \
  "v_mfma_f32_16x16x32_bf16 a[4:7], a[128:131], v[36:39], a[4:7]\n\t" \
  "ds_read_b64_tr_b16 a[162:163], v184 offset:58368\n\t" \
  "v_fma_f32 v183, v183, v160, v162\n\t" \
  "v_fma_f32 v176, v176, v144, v152\n\t" \
  "v_mfma_f32_16x16x32_bf16 a[8:11], a[128:131], v[40:43], a[8:11]\n\t" \
  "ds_read_b64_tr_b16 a[164:165], v185 offset:57344\n\t" \
  "v_fma_f32 v177, v177, v145, v153\n\t" \
  "v_fma_f32 v178, v178, v146, v154\n\t" \
  "v_mfma_f32_16x16x32_bf16 a[12:15], a[128:131], v[44:47], a[12:15]\n\t" \
  "ds_read_b64_tr_b16 a[166:167], v185 offset:58368\n\t" \
  "v_fma_f32 v179, v179, v147, v155\n\t" \
  "v_fma_f32 v180, v180, v148, v156\n\t" \
  "v_fma_f32 v181, v181, v149, v157\n\t" \
  "v_mfma_f32_16x16x32_bf16 a[28:31], a[132:135], v[44:47], a[28:31]\n\t" \
  "ds_read_b64_tr_b16 a[168:169], v186 offset:57344\n\t" \
  "v_fma_f32 v182, v182, v150, v158\n\t" \
  "v_fma_f32 v183, v183, v151, v159\n\t" \
  "v_mfma_f32_16x16x32_bf16 a[24:27], a[132:135], v[40:43], a[24:27]\n\t" \
  "ds_read_b64_tr_b16 a[170:171], v186 offset:58368\n\t" \
  "v_cvt_pk_bf16_f32 v80, v176, v177\n\t" \
  "v_cvt_pk_bf16_f32 v81, v178, v179\n\t" \
  "v_mfma_f32_16x16x32_bf16 a[20:23], a[132:135], v[36:39], a[20:23]\n\t" \
  "ds_read_b64_tr_b16 a[172:173], v187 offset:57344\n\t" \
  "v_cvt_pk_bf16_f32 v82, v180, v181\n\t" \
  "v_cvt_pk_bf16_f32 v83, v182, v183\n\t" \
  "v_mfma_f32_16x16x32_bf16 a[16:19], a[132:135], v[32:35], a[16:19]\n\t" \
  "ds_read_b64_tr_b16 a[174:175], v187 offset:58368\n\t" \
  "v_lshlrev_b32 v176, 16, v84\n\t" \
  "v_and_b32 v177, s98, v84\n\t" \
  "v_lshlrev_b32 v178, 16, v85\n\t" \
  "v_mfma_f32_16x16x32_bf16 a[32:35], a[136:139], v[32:35], a[32:35]\n\t" \
  "ds_read_b64_tr_b16 a[176:177], v188 offset:57344\n\t" \
  "v_and_b32 v179, s98, v85\n\t" \
  "v_lshlrev_b32 v180, 16, v86\n\t" \
  "v_mfma_f32_16x16x32_bf16 a[36:39], a[136:139], v[36:39], a[36:39]\n\t" \
  "ds_read_b64_tr_b16 a[178:179], v188 offset:58368\n\t" \
  "v_and_b32 v181, s98, v86\n\t" \
  "v_lshlrev_b32 v182, 16, v87\n\t" \
  "v_mfma_f32_16x16x32_bf16 a[40:43], a[136:139], v[40:43], a[40:43]\n\t" \
  "ds_read_b64_tr_b16 a[180:181], v189 offset:57344\n\t" \
  "v_and_b32 v183, s98, v87\n\t" \
  "v_fma_f32 v176, v176, v164, v166\n\t" \
  "v_mfma_f32_16x16x32_bf16 a[44:47], a[136:139], v[44:47], a[44:47]\n\t" \
  "ds_read_b64_tr_b16 a[182:183], v189 offset:58368\n\t" \
  "v_fma_f32 v177, v177, v164, v166\n\t" \
  "v_fma_f32 v178, v178, v164, v166\n\t" \
  "v_fma_f32 v179, v179, v164, v166\n\t" \
  "v_mfma_f32_16x16x32_bf16 a[60:63], a[140:143], v[44:47], a[60:63]\n\t" \
  "ds_read_b64_tr_b16 a[184:185], v190 offset:57344\n\t" \
  "v_fma_f32 v180, v180, v164, v166\n\t" \
  "v_fma_f32 v181, v181, v164, v166\n\t" \
  "v_mfma_f32_16x16x32_bf16 a[56:59], a[140:143], v[40:43], a[56:59]\n\t" \
  "ds_read_b64_tr_b16 a[186:187], v190 offset:58368\n\t" \
  "v_fma_f32 v182, v182, v164, v166\n\t" \
  "v_fma_f32 v183, v183, v164, v166\n\t" \
  "v_mfma_f32_16x16x32_bf16 a[52:55], a[140:143], v[36:39], a[52:55]\n\t" \
  "ds_read_b64_tr_b16 a[188:189], v191 offset:57344\n\t" \
  "v_fma_f32 v176, v176, v144, v152\n\t" \
  "v_fma_f32 v177, v177, v145, v153\n\t" \
  "v_mfma_f32_16x16x32_bf16 a[48:51], a[140:143], v[32:35], a[48:51]\n\t" \
  "buffer_store_short v218, v219, %[ws], 0 offen sc1\n\t" \
  "ds_read_b64_tr_b16 a[190:191], v191 offset:58368\n\t" \
  "v_fma_f32 v178, v178, v146, v154\n\t" \
  "v_fma_f32 v179, v179, v147, v155\n\t" \
  "v_fma_f32 v180, v180, v148, v156\n\t" \
  "v_mfma_f32_16x16x32_bf16 a[64:67], a[144:147], v[32:35], a[64:67]\n\t" \
  "v_fma_f32 v181, v181, v149, v157\n\t" \
  "v_fma_f32 v182, v182, v150, v158\n\t" \
  "v_mfma_f32_16x16x32_bf16 a[68:71], a[144:147], v[36:39], a[68:71]\n\t" \
  "v_fma_f32 v183, v183, v151, v159\n\t" \
  "v_cvt_pk_bf16_f32 v84, v176, v177\n\t" \
  "v_mfma_f32_16x16x32_bf16 a[72:75], a[144:147], v[40:43], a[72:75]\n\t" \
  "v_cvt_pk_bf16_f32 v85, v178, v179\n\t" \
  "v_cvt_pk_bf16_f32 v86, v180, v181\n\t" \
  "v_cvt_pk_bf16_f32 v87, v182, v183\n\t" \
  "v_mfma_f32_16x16x32_bf16 a[76:79], a[144:147], v[44:47], a[76:79]\n\t" \
  "v_lshlrev_b32 v176, 16, v88\n\t" \
  "v_and_b32 v177, s98, v88\n\t" \
  "v_mfma_f32_16x16x32_bf16 a[92:95], a[148:151], v[44:47], a[92:95]\n\t" \
  "v_lshlrev_b32 v178, 16, v89\n\t" \
  "v_and_b32 v179, s98, v89\n\t" \
  "v_mfma_f32_16x16x32_bf16 a[88:91], a[148:151], v[40:43], a[88:91]\n\t" \
  "v_lshlrev_b32 v180, 16, v90\n\t" \
  "v_and_b32 v181, s98, v90\n\t" \
  "v_mfma_f32_16x16x32_bf16 a[84:87], a[148:151], v[36:39], a[84:87]\n\t" \
  "v_lshlrev_b32 v182, 16, v91\n\t" \
  "v_and_b32 v183, s98, v91\n\t" \
  "v_mfma_f32_16x16x32_bf16 a[80:83], a[148:151], v[32:35], a[80:83]\n\t" \
  "v_fma_f32 v176, v176, v168, v170\n\t" \
  "v_fma_f32 v177, v177, v168, v170\n\t" \
  "v_mfma_f32_16x16x32_bf16 a[96:99], a[152:155], v[32:35], a[96:99]\n\t" \
  "v_fma_f32 v178, v178, v168, v170\n\t" \
  "v_fma_f32 v179, v179, v168, v170\n\t" \
  "v_mfma_f32_16x16x32_bf16 a[100:103], a[152:155], v[36:39], a[100:103]\n\t" \
  "v_fma_f32 v180, v180, v168, v170\n\t" \
  "v_fma_f32 v181, v181, v168, v170\n\t" \
  "v_mfma_f32_16x16x32_bf16 a[104:107], a[152:155], v[40:43], a[104:107]\n\t" \
  "v_fma_f32 v182, v182, v168, v170\n\t" \
  "v_fma_f32 v183, v183, v168, v170\n\t" \
  "v_mfma_f32_16x16x32_bf16 a[108:111], a[152:155], v[44:47], a[108:111]\n\t" \
  "v_fma_f32 v176, v176, v144, v152\n\t" \
  "v_fma_f32 v177, v177, v145, v153\n\t" \
  "v_mfma_f32_16x16x32_bf16 a[124:127], a[156:159], v[44:47], a[124:127]\n\t" \
  "v_fma_f32 v178, v178, v146, v154\n\t" \
  "v_fma_f32 v179, v179, v147, v155\n\t" \
  "v_mfma_f32_16x16x32_bf16 a[120:123], a[156:159], v[40:43], a[120:123]\n\t" \
  "v_fma_f32 v180, v180, v148, v156\n\t" \
  "v_fma_f32 v181, v181, v149, v157\n\t" \
  "v_mfma_f32_16x16x32_bf16 a[116:119], a[156:159], v[36:39], a[116:119]\n\t" \
  "v_fma_f32 v182, v182, v150, v158\n\t" \
  "v_fma_f32 v183, v183, v151, v159\n\t" \
  "v_mfma_f32_16x16x32_bf16 a[112:115], a[156:159], v[32:35], a[112:115]\n\t" \
  FSTP("v_writelane_b32 %[stv], s94, 40\n\t") \
  FSTP("v_writelane_b32 %[stv], s92, 41\n\t") \
  FSTP("s_memrealtime s[94:95]\n\t") \
  FSTP("s_memtime s[92:93]\n\t") \
  "s_waitcnt vmcnt(15) lgkmcnt(0)\n\t" \
  "s_barrier\n\t" \
  "v_mfma_f32_16x16x32_bf16 a[0:3], a[160:163], v[48:51], a[0:3]\n\t" \
  "buffer_load_dword %[fl], v210, %[ws], %[fo] offen sc1\n\t" \
  "ds_read_b64_tr_b16 a[128:129], v192 offset:0\n\t" \
  "v_cvt_pk_bf16_f32 v88, v176, v177\n\t" \
  "v_cvt_pk_bf16_f32 v89, v178, v179\n\t" \
  "v_cvt_pk_bf16_f32 v90, v180, v181\n\t" \
  "v_mfma_f32_16x16x32_bf16 a[4:7], a[160:163], v[52:55], a[4:7]\n\t" \
  "ds_read_b64_tr_b16 a[130:131], v192 offset:1024\n\t" \
  "v_cvt_pk_bf16_f32 v91, v182, v183\n\t" \
  "s_waitcnt vmcnt(8)\n\t" \
  "ds_read_b128 v[96:99], v204 offset:0\n\t" \
  "v_mfma_f32_16x16x32_bf16 a[8:11], a[160:163], v[56:59], a[8:11]\n\t" \
  "s_add_u32 s96, %[rot19], 2097152\n\t" \
  "s_and_b32 s96, s96, 0x780000\n\t" \
  "ds_read_b128 v[100:103], v204 offset:2048\n\t" \
  "ds_read_b128 v[104:107], v204 offset:4096\n\t" \
  "v_mfma_f32_16x16x32_bf16 a[12:15], a[160:163], v[60:63], a[12:15]\n\t" \
  "ds_read_b64_tr_b16 a[132:133], v193 offset:0\n\t" \
  "ds_read_b128 v[108:111], v204 offset:6144\n\t" \
  "v_lshlrev_b32 v176, 16, v92\n\t" \
  "v_mfma_f32_16x16x32_bf16 a[28:31], a[164:167], v[60:63], a[28:31]\n\t" \
  "ds_read_b64_tr_b16 a[134:135], v193 offset:1024\n\t" \
  "v_and_b32 v177, s98, v92\n\t" \
  "v_lshlrev_b32 v178, 16, v93\n\t" \
  "v_and_b32 v179, s98, v93\n\t" \
  "v_mfma_f32_16x16x32_bf16 a[24:27], a[164:167], v[56:59], a[24:27]\n\t" \
  "s_add_u32 m0, %[mw], 49152\n\t" \
  "ds_read_b64_tr_b16 a[136:137], v194 offset:0\n\t" \
  "v_lshlrev_b32 v180, 16, v94\n\t" \
  "v_and_b32 v181, s98, v94\n\t" \
  "v_mfma_f32_16x16x32_bf16 a[20:23], a[164:167], v[52:55], a[20:23]\n\t" \
  "ds_read_b64_tr_b16 a[138:139], v194 offset:1024\n\t" \
  "v_lshlrev_b32 v182, 16, v95\n\t" \
  "v_and_b32 v183, s98, v95\n\t" \
  "v_mfma_f32_16x16x32_bf16 a[16:19], a[164:167], v[48:51], a[16:19]\n\t" \
  "buffer_load_dwordx4 v200, %[sw], s96 offen offset:0 lds\n\t" \
  "ds_read_b64_tr_b16 a[140:141], v195 offset:0\n\t" \
  "v_fma_f32 v176, v176, v172, v174\n\t" \
  "v_fma_f32 v177, v177, v172, v174\n\t" \
  "v_mfma_f32_16x16x32_bf16 a[32:35], a[168:171], v[48:51], a[32:35]\n\t" \
  "ds_read_b64_tr_b16 a[142:143], v195 offset:1024\n\t" \
  "v_fma_f32 v178, v178, v172, v174\n\t" \
  "v_fma_f32 v179, v179, v172, v174\n\t" \
  "v_fma_f32 v180, v180, v172, v174\n\t" \
  "v_mfma_f32_16x16x32_bf16 a[36:39], a[168:171], v[52:55], a[36:39]\n\t" \
  "ds_read_b64_tr_b16 a[144:145], v196 offset:0\n\t" \
  "v_fma_f32 v181, v181, v172, v174\n\t" \
  "v_fma_f32 v182, v182, v172, v174\n\t" \
  "v_mfma_f32_16x16x32_bf16 a[40:43], a[168:171], v[56:59], a[40:43]\n\t" \
  "buffer_load_dwordx4 v201, %[sw], s96 offen offset:1024 lds\n\t" \
  "ds_read_b64_tr_b16 a[146:147], v196 offset:1024\n\t" \
  "v_fma_f32 v183, v183, v172, v174\n\t" \
  "v_fma_f32 v176, v176, v144, v152\n\t" \
  "v_mfma_f32_16x16x32_bf16 a[44:47], a[168:171], v[60:63], a[44:47]\n\t" \
  "ds_read_b64_tr_b16 a[148:149], v197 offset:0\n\t" \
  "v_fma_f32 v177, v177, v145, v153\n\t" \
  "v_fma_f32 v178, v178, v146, v154\n\t" \
  "v_mfma_f32_16x16x32_bf16 a[60:63], a[172:175], v[60:63], a[60:63]\n\t" \
  "buffer_load_dwordx4 v202, %[sw], s96 offen offset:2048 lds\n\t" \
  "ds_read_b64_tr_b16 a[150:151], v197 offset:1024\n\t" \
  "v_fma_f32 v179, v179, v147, v155\n\t" \
  "v_fma_f32 v180, v180, v148, v156\n\t" \
  "v_fma_f32 v181, v181, v149, v157\n\t" \
  "v_mfma_f32_16x16x32_bf16 a[56:59], a[172:175], v[56:59], a[56:59]\n\t" \
  "ds_read_b64_tr_b16 a[152:153], v198 offset:0\n\t" \
  "v_fma_f32 v182, v182, v150, v158\n\t" \
  "v_fma_f32 v183, v183, v151, v159\n\t" \
  "v_mfma_f32_16x16x32_bf16 a[52:55], a[172:175], v[52:55], a[52:55]\n\t" \
  "ds_read_b64_tr_b16 a[154:155], v198 offset:1024\n\t" \
  "v_cvt_pk_bf16_f32 v92, v176, v177\n\t" \
  "v_cvt_pk_bf16_f32 v93, v178, v179\n\t" \
  "v_mfma_f32_16x16x32_bf16 a[48:51], a[172:175], v[48:51], a[48:51]\n\t" \
  "buffer_load_dwordx4 v203, %[sw], s96 offen offset:3072 lds\n\t" \
  "ds_read_b64_tr_b16 a[156:157], v199 offset:0\n\t" \
  "v_cvt_pk_bf16_f32 v94, v180, v181\n\t" \
  "v_cvt_pk_bf16_f32 v95, v182, v183\n\t" \
  "v_mfma_f32_16x16x32_bf16 a[64:67], a[176:179], v[48:51], a[64:67]\n\t" \
  "ds_read_b64_tr_b16 a[158:159], v199 offset:1024\n\t" \
  "ds_read_b128 v[144:147], v208 offset:1664\n\t" \
  "ds_read_b128 v[148:151], v208 offset:1680\n\t" \
  "v_mfma_f32_16x16x32_bf16 a[68:71], a[176:179], v[52:55], a[68:71]\n\t" \
  "global_load_dword %[pf], v216, %[wq]\n\t" \
  "ds_read_b128 v[152:155], v208 offset:1920\n\t" \
  "ds_read_b128 v[156:159], v208 offset:1936\n\t" \
  "s_waitcnt lgkmcnt(15)\n\t" \
  "v_lshlrev_b32 v176, 16, v96\n\t" \
  "v_mfma_f32_16x16x32_bf16 a[72:75], a[176:179], v[56:59], a[72:75]\n\t" \
  "v_and_b32 v177, s98, v96\n\t" \
  "v_lshlrev_b32 v178, 16, v97\n\t" \
  "v_mfma_f32_16x16x32_bf16 a[76:79], a[176:179], v[60:63], a[76:79]\n\t" \
  "v_and_b32 v179, s98, v97\n\t" \
  "v_lshlrev_b32 v180, 16, v98\n\t" \
  "v_mfma_f32_16x16x32_bf16 a[92:95], a[180:183], v[60:63], a[92:95]\n\t" \
  "s_lshr_b32 s99, %[c17], 9\n\t" \
  "v_xor_b32 v248, s99, v214\n\t" \
  "v_and_b32 v181, s98, v98\n\t" \
  "v_lshlrev_b32 v182, 16, v99\n\t" \
  "v_mfma_f32_16x16x32_bf16 a[88:91], a[180:183], v[56:59], a[88:91]\n\t" \
  "v_and_b32 v183, s98, v99\n\t" \
  "v_fma_f32 v176, v176, v160, v162\n\t" \
  "v_mfma_f32_16x16x32_bf16 a[84:87], a[180:183], v[52:55], a[84:87]\n\t" \
  "v_xor_b32 v249, 256, v248\n\t" \
  "v_fma_f32 v177, v177, v160, v162\n\t" \
  "v_fma_f32 v178, v178, v160, v162\n\t" \
  "v_mfma_f32_16x16x32_bf16 a[80:83], a[180:183], v[48:51], a[80:83]\n\t" \
  "v_fma_f32 v179, v179, v160, v162\n\t" \
  "v_fma_f32 v180, v180, v160, v162\n\t" \
  "v_mfma_f32_16x16x32_bf16 a[96:99], a[184:187], v[48:51], a[96:99]\n\t" \
  "v_fma_f32 v181, v181, v160, v162\n\t" \
  "v_fma_f32 v182, v182, v160, v162\n\t" \
  "v_mfma_f32_16x16x32_bf16 a[100:103], a[184:187], v[52:55], a[100:103]\n\t" \
  "v_xor_b32 v250, 512, v248\n\t" \
  "v_fma_f32 v183, v183, v160, v162\n\t" \
  "v_fma_f32 v176, v176, v128, v136\n\t" \
  "v_mfma_f32_16x16x32_bf16 a[104:107], a[184:187], v[56:59], a[104:107]\n\t" \
  "v_fma_f32 v177, v177, v129, v137\n\t" \
  "v_fma_f32 v178, v178, v130, v138\n\t" \
  "v_mfma_f32_16x16x32_bf16 a[108:111], a[184:187], v[60:63], a[108:111]\n\t" \
  "v_xor_b32 v251, 768, v248\n\t" \
  "v_fma_f32 v179, v179, v131, v139\n\t" \
  "v_fma_f32 v180, v180, v132, v140\n\t" \
  "v_mfma_f32_16x16x32_bf16 a[124:127], a[188:191], v[60:63], a[124:127]\n\t" \
  "v_fma_f32 v181, v181, v133, v141\n\t" \
  "v_fma_f32 v182, v182, v134, v142\n\t" \
  "v_mfma_f32_16x16x32_bf16 a[120:123], a[188:191], v[56:59], a[120:123]\n\t" \
  "v_fma_f32 v183, v183, v135, v143\n\t" \
  "v_cvt_pk_bf16_f32 v96, v176, v177\n\t" \
  "v_mfma_f32_16x16x32_bf16 a[116:119], a[188:191], v[52:55], a[116:119]\n\t" \
  "v_cvt_pk_bf16_f32 v97, v178, v179\n\t" \
  "v_cvt_pk_bf16_f32 v98, v180, v181\n\t" \
  "v_mfma_f32_16x16x32_bf16 a[112:115], a[188:191], v[48:51], a[112:115]\n\t" \
  FSTP("v_writelane_b32 %[stv], s94, 42\n\t") \
  FSTP("v_writelane_b32 %[stv], s92, 43\n\t") \
  FSTP("s_memrealtime s[94:95]\n\t") \
  FSTP("s_memtime s[92:93]\n\t") \
  "s_waitcnt lgkmcnt(4)\n\t" \
  "v_mfma_f32_16x16x32_bf16 a[0:3], a[128:131], v[64:67], a[0:3]\n\t" \
  "s_waitcnt vmcnt(5)\n\t" \
  "v_cmp_ne_u32 vcc, v211, %[fl]\n\t" \
  "s_cbranch_vccnz .Lfq_s\n\t" \
  ".Lfq_k:\n\t" \
  "v_cvt_pk_bf16_f32 v99, v182, v183\n\t" \
  "v_lshlrev_b32 v176, 16, v100\n\t" \
  "v_and_b32 v177, s98, v100\n\t" \
  "v_mfma_f32_16x16x32_bf16 a[4:7], a[128:131], v[68:71], a[4:7]\n\t" \
  "ds_read_b64_tr_b16 a[160:161], v192 offset:8192\n\t" \
  "v_lshlrev_b32 v178, 16, v101\n\t" \
  "v_and_b32 v179, s98, v101\n\t" \
  "v_mfma_f32_16x16x32_bf16 a[8:11], a[128:131], v[72:75], a[8:11]\n\t" \
  "s_add_u32 s97, %[rot19], 2097152\n\t" \
  "s_bfe_u32 s97, s97, 0x140003\n\t" \
  "v_lshlrev_b32 v180, 16, v102\n\t" \
  "v_and_b32 v181, s98, v102\n\t" \
  "v_mfma_f32_16x16x32_bf16 a[12:15], a[128:131], v[76:79], a[12:15]\n\t" \
  "ds_read_b64_tr_b16 a[162:163], v192 offset:9216\n\t" \
  "v_lshlrev_b32 v182, 16, v103\n\t" \
  "v_and_b32 v183, s98, v103\n\t" \
  "v_mfma_f32_16x16x32_bf16 a[28:31], a[132:135], v[76:79], a[28:31]\n\t" \
  "ds_read_b64_tr_b16 a[164:165], v193 offset:8192\n\t" \
  "v_fma_f32 v176, v176, v164, v166\n\t" \
  "v_fma_f32 v177, v177, v164, v166\n\t" \
  "v_fma_f32 v178, v178, v164, v166\n\t" \
  "v_mfma_f32_16x16x32_bf16 a[24:27], a[132:135], v[72:75], a[24:27]\n\t" \
  "s_add_u32 s97, s97, %[sox]\n\t" \
  "ds_read_b64_tr_b16 a[166:167], v193 offset:9216\n\t" \
  "v_fma_f32 v179, v179, v164, v166\n\t" \
  "v_fma_f32 v180, v180, v164, v166\n\t" \
  "v_mfma_f32_16x16x32_bf16 a[20:23], a[132:135], v[68:71], a[20:23]\n\t" \
  "ds_read_b64_tr_b16 a[168:169], v194 offset:8192\n\t" \
  "v_fma_f32 v181, v181, v164, v166\n\t" \
  "v_fma_f32 v182, v182, v164, v166\n\t" \
  "v_mfma_f32_16x16x32_bf16 a[16:19], a[132:135], v[64:67], a[16:19]\n\t" \
  "buffer_load_dwordx4 a[192:195], v248, %[ws], s97 offen sc1\n\t" \
  "ds_read_b64_tr_b16 a[170:171], v194 offset:9216\n\t" \
  "v_fma_f32 v183, v183, v164, v166\n\t" \
  "v_fma_f32 v176, v176, v128, v136\n\t" \
  "v_mfma_f32_16x16x32_bf16 a[32:35], a[136:139], v[64:67], a[32:35]\n\t" \
  "ds_read_b64_tr_b16 a[172:173], v195 offset:8192\n\t" \
  "v_fma_f32 v177, v177, v129, v137\n\t" \
  "v_fma_f32 v178, v178, v130, v138\n\t" \
  "v_fma_f32 v179, v179, v131, v139\n\t" \
  "v_mfma_f32_16x16x32_bf16 a[36:39], a[136:139], v[68:71], a[36:39]\n\t" \
  "ds_read_b64_tr_b16 a[174:175], v195 offset:9216\n\t" \
  "v_fma_f32 v180, v180, v132, v140\n\t" \
  "v_fma_f32 v181, v181, v133, v141\n\t" \
  "v_mfma_f32_16x16x32_bf16 a[40:43], a[136:139], v[72:75], a[40:43]\n\t" \
  "buffer_load_dwordx4 a[196:199], v249, %[ws], s97 offen sc1\n\t" \
  "ds_read_b64_tr_b16 a[176:177], v196 offset:8192\n\t" \
  "v_fma_f32 v182, v182, v134, v142\n\t" \
  "v_fma_f32 v183, v183, v135, v143\n\t" \
  "v_mfma_f32_16x16x32_bf16 a[44:47], a[136:139], v[76:79], a[44:47]\n\t" \
  "ds_read_b64_tr_b16 a[178:179], v196 offset:9216\n\t" \
  "v_cvt_pk_bf16_f32 v100, v176, v177\n\t" \
  "v_cvt_pk_bf16_f32 v101, v178, v179\n\t" \
  "v_mfma_f32_16x16x32_bf16 a[60:63], a[140:143], v[76:79], a[60:63]\n\t" \
  "buffer_load_dwordx4 a[200:203], v250, %[ws], s97 offen sc1\n\t" \
  "ds_read_b64_tr_b16 a[180:181], v197 offset:8192\n\t" \
  "v_cvt_pk_bf16_f32 v102, v180, v181\n\t" \
  "v_cvt_pk_bf16_f32 v103, v182, v183\n\t" \
  "v_lshlrev_b32 v176, 16, v104\n\t" \
  "v_mfma_f32_16x16x32_bf16 a[56:59], a[140:143], v[72:75], a[56:59]\n\t" \
  "ds_read_b64_tr_b16 a[182:183], v197 offset:9216\n\t" \
  "v_and_b32 v177, s98, v104\n\t" \
  "v_lshlrev_b32 v178, 16, v105\n\t" \
  "v_mfma_f32_16x16x32_bf16 a[52:55], a[140:143], v[68:71], a[52:55]\n\t" \
  "ds_read_b64_tr_b16 a[184:185], v198 offset:8192\n\t" \
  "v_and_b32 v179, s98, v105\n\t" \
  "v_lshlrev_b32 v180, 16, v106\n\t" \
  "v_mfma_f32_16x16x32_bf16 a[48:51], a[140:143], v[64:67], a[48:51]\n\t" \
  "buffer_load_dwordx4 a[204:207], v251, %[ws], s97 offen sc1\n\t" \
  "ds_read_b64_tr_b16 a[186:187], v198 offset:9216\n\t" \
  "v_and_b32 v181, s98, v106\n\t" \
  "v_lshlrev_b32 v182, 16, v107\n\t" \
  "v_mfma_f32_16x16x32_bf16 a[64:67], a[144:147], v[64:67], a[64:67]\n\t" \
  "ds_read_b64_tr_b16 a[188:189], v199 offset:8192\n\t" \
  "v_and_b32 v183, s98, v107\n\t" \
  "v_fma_f32 v176, v176, v168, v170\n\t" \
  "v_mfma_f32_16x16x32_bf16 a[68:71], a[144:147], v[68:71], a[68:71]\n\t" \
  "s_add_u32 s97, s97, %[dk]\n\t" \
  "ds_read_b64_tr_b16 a[190:191], v199 offset:9216\n\t" \
  "v_fma_f32 v177, v177, v168, v170\n\t" \
  ".Lhs1:\n\ts_cbranch_execz .Lhs1n\n\t" \
  "v_fma_f32 v178, v178, v168, v170\n\t" \
  "v_fma_f32 v179, v179, v168, v170\n\t" \
  "v_mfma_f32_16x16x32_bf16 a[72:75], a[144:147], v[72:75], a[72:75]\n\t" \
  "v_fma_f32 v180, v180, v168, v170\n\t" \
  "v_fma_f32 v181, v181, v168, v170\n\t" \
  "v_mfma_f32_16x16x32_bf16 a[76:79], a[144:147], v[76:79], a[76:79]\n\t" \
  "v_fma_f32 v182, v182, v168, v170\n\t" \
  "v_fma_f32 v183, v183, v168, v170\n\t" \
  "v_mfma_f32_16x16x32_bf16 a[92:95], a[148:151], v[76:79], a[92:95]\n\t" \
  "buffer_load_dwordx4 a[208:211], v248, %[ws], s97 offen sc1\n\t" \
  "v_fma_f32 v176, v176, v128, v136\n\t" \
  "v_fma_f32 v177, v177, v129, v137\n\t" \
  "v_mfma_f32_16x16x32_bf16 a[88:91], a[148:151], v[72:75], a[88:91]\n\t" \
  "v_fma_f32 v178, v178, v130, v138\n\t" \
  "v_fma_f32 v179, v179, v131, v139\n\t" \
  "v_mfma_f32_16x16x32_bf16 a[84:87], a[148:151], v[68:71], a[84:87]\n\t" \
  "buffer_load_dwordx4 a[212:215], v249, %[ws], s97 offen sc1\n\t" \
  "v_fma_f32 v180, v180, v132, v140\n\t" \
  "v_fma_f32 v181, v181, v133, v141\n\t" \
  "v_mfma_f32_16x16x32_bf16 a[80:83], a[148:151], v[64:67], a[80:83]\n\t" \
  "v_fma_f32 v182, v182, v134, v142\n\t" \
  "v_fma_f32 v183, v183, v135, v143\n\t" \
  "v_mfma_f32_16x16x32_bf16 a[96:99], a[152:155], v[64:67], a[96:99]\n\t" \
  "v_cvt_pk_bf16_f32 v104, v176, v177\n\t" \
  "v_cvt_pk_bf16_f32 v105, v178, v179\n\t" \
  "v_mfma_f32_16x16x32_bf16 a[100:103], a[152:155], v[68:71], a[100:103]\n\t" \
  "buffer_load_dwordx4 a[216:219], v250, %[ws], s97 offen sc1\n\t" \
  "v_cvt_pk_bf16_f32 v106, v180, v181\n\t" \
  "v_cvt_pk_bf16_f32 v107, v182, v183\n\t" \
  "v_mfma_f32_16x16x32_bf16 a[104:107], a[152:155], v[72:75], a[104:107]\n\t" \
  "ds_read_b128 v[112:115], v205 offset:0\n\t" \
  "ds_read_b128 v[116:119], v205 offset:2048\n\t" \
  "v_mfma_f32_16x16x32_bf16 a[108:111], a[152:155], v[76:79], a[108:111]\n\t" \
  "buffer_load_dwordx4 a[220:223], v251, %[ws], s97 offen sc1\n\t" \
  "ds_read_b128 v[120:123], v205 offset:4096\n\t" \
  "ds_read_b128 v[124:127], v205 offset:6144\n\t" \
  "v_mfma_f32_16x16x32_bf16 a[124:127], a[156:159], v[76:79], a[124:127]\n\t" \
  "v_lshlrev_b32 v176, 16, v108\n\t" \
  "v_and_b32 v177, s98, v108\n\t" \
  "v_mfma_f32_16x16x32_bf16 a[120:123], a[156:159], v[72:75], a[120:123]\n\t" \
  "v_lshlrev_b32 v178, 16, v109\n\t" \
  "v_and_b32 v179, s98, v109\n\t" \
  "v_mfma_f32_16x16x32_bf16 a[116:119], a[156:159], v[68:71], a[116:119]\n\t" \
  "v_lshlrev_b32 v180, 16, v110\n\t" \
  "v_and_b32 v181, s98, v110\n\t" \
  "v_mfma_f32_16x16x32_bf16 a[112:115], a[156:159], v[64:67], a[112:115]\n\t" \
  FSTP("v_writelane_b32 %[stv], s94, 44\n\t") \
  FSTP("v_writelane_b32 %[stv], s92, 45\n\t") \
  FSTP("s_memrealtime s[94:95]\n\t") \
  FSTP("s_memtime s[92:93]\n\t") \
  "s_waitcnt lgkmcnt(4)\n\t" \
  "s_barrier\n\t" \
  "v_mfma_f32_16x16x32_bf16 a[0:3], a[160:163], v[80:83], a[0:3]\n\t" \
  "s_add_u32 s96, %[rot19], 2621440\n\t" \
  "s_and_b32 s96, s96, 0x780000\n\t" \
  "v_lshlrev_b32 v182, 16, v111\n\t" \
  "v_and_b32 v183, s98, v111\n\t" \
  "v_fma_f32 v176, v176, v172, v174\n\t" \
  "v_mfma_f32_16x16x32_bf16 a[4:7], a[160:163], v[84:87], a[4:7]\n\t" \
  "ds_read_b64_tr_b16 a[128:129], v184 offset:0\n\t" \
  "v_fma_f32 v177, v177, v172, v174\n\t" \
  "v_fma_f32 v178, v178, v172, v174\n\t" \
  "v_mfma_f32_16x16x32_bf16 a[8:11], a[160:163], v[88:91], a[8:11]\n\t" \
  "s_add_u32 m0, %[mw], 98304\n\t" \
  "ds_read_b64_tr_b16 a[130:131], v184 offset:1024\n\t" \
  "v_fma_f32 v179, v179, v172, v174\n\t" \
  "v_fma_f32 v180, v180, v172, v174\n\t" \
  "v_mfma_f32_16x16x32_bf16 a[12:15], a[160:163], v[92:95], a[12:15]\n\t" \
  "ds_read_b64_tr_b16 a[132:133], v185 offset:0\n\t" \
  "v_fma_f32 v181, v181, v172, v174\n\t" \
  "v_fma_f32 v182, v182, v172, v174\n\t" \
  "v_mfma_f32_16x16x32_bf16 a[28:31], a[164:167], v[92:95], a[28:31]\n\t" \
  "ds_read_b64_tr_b16 a[134:135], v185 offset:1024\n\t" \
  "v_fma_f32 v183, v183, v172, v174\n\t" \
  "v_fma_f32 v176, v176, v128, v136\n\t" \
  "v_fma_f32 v177, v177, v129, v137\n\t" \
  "v_mfma_f32_16x16x32_bf16 a[24:27], a[164:167], v[88:91], a[24:27]\n\t" \
  "buffer_load_dwordx4 v200, %[sw], s96 offen offset:0 lds\n\t" \
  "ds_read_b64_tr_b16 a[136:137], v186 offset:0\n\t" \
  "v_fma_f32 v178, v178, v130, v138\n\t" \
  "v_fma_f32 v179, v179, v131, v139\n\t" \
  "v_mfma_f32_16x16x32_bf16 a[20:23], a[164:167], v[84:87], a[20:23]\n\t" \
  "ds_read_b64_tr_b16 a[138:139], v186 offset:1024\n\t" \
  "v_fma_f32 v180, v180, v132, v140\n\t" \
  "v_fma_f32 v181, v181, v133, v141\n\t" \
  "v_mfma_f32_16x16x32_bf16 a[16:19], a[164:167], v[80:83], a[16:19]\n\t" \
  "buffer_load_dwordx4 v201, %[sw], s96 offen offset:1024 lds\n\t" \
  "ds_read_b64_tr_b16 a[140:141], v187 offset:0\n\t" \
  "v_fma_f32 v182, v182, v134, v142\n\t" \
  "v_fma_f32 v183, v183, v135, v143\n\t" \
  "v_mfma_f32_16x16x32_bf16 a[32:35], a[168:171], v[80:83], a[32:35]\n\t" \
  "ds_read_b64_tr_b16 a[142:143], v187 offset:1024\n\t" \
  "v_cvt_pk_bf16_f32 v108, v176, v177\n\t" \
  "v_cvt_pk_bf16_f32 v109, v178, v179\n\t" \
  "v_cvt_pk_bf16_f32 v110, v180, v181\n\t" \
  "v_mfma_f32_16x16x32_bf16 a[36:39], a[168:171], v[84:87], a[36:39]\n\t" \
  "ds_read_b64_tr_b16 a[144:145], v188 offset:0\n\t" \
  "v_cvt_pk_bf16_f32 v111, v182, v183\n\t" \
  "s_waitcnt lgkmcnt(9)\n\t" \
  "v_lshlrev_b32 v176, 16, v112\n\t" \
  "v_mfma_f32_16x16x32_bf16 a[40:43], a[168:171], v[88:91], a[40:43]\n\t" \
  "buffer_load_dwordx4 v202, %[sw], s96 offen offset:2048 lds\n\t" \
  "ds_read_b64_tr_b16 a[146:147], v188 offset:1024\n\t" \
  "v_and_b32 v177, s98, v112\n\t" \
  "v_lshlrev_b32 v178, 16, v113\n\t" \
  "v_mfma_f32_16x16x32_bf16 a[44:47], a[168:171], v[92:95], a[44:47]\n\t" \
  "ds_read_b64_tr_b16 a[148:149], v189 offset:0\n\t" \
  "v_and_b32 v179, s98, v113\n\t" \
  "v_lshlrev_b32 v180, 16, v114\n\t" \
  "v_mfma_f32_16x16x32_bf16 a[60:63], a[172:175], v[92:95], a[60:63]\n\t" \
  "buffer_load_dwordx4 v203, %[sw], s96 offen offset:3072 lds\n\t" \
  "ds_read_b64_tr_b16 a[150:151], v189 offset:1024\n\t" \
  "v_and_b32 v181, s98, v114\n\t" \
  "v_lshlrev_b32 v182, 16, v115\n\t" \
  "v_and_b32 v183, s98, v115\n\t" \
  "v_mfma_f32_16x16x32_bf16 a[56:59], a[172:175], v[88:91], a[56:59]\n\t" \
  "ds_read_b64_tr_b16 a[152:153], v190 offset:0\n\t" \
  "v_fma_f32 v176, v176, v160, v162\n\t" \
  "v_fma_f32 v177, v177, v160, v162\n\t" \
  "v_mfma_f32_16x16x32_bf16 a[52:55], a[172:175], v[84:87], a[52:55]\n\t" \
  "ds_read_b64_tr_b16 a[154:155], v190 offset:1024\n\t" \
  "v_fma_f32 v178, v178, v160, v162\n\t" \
  "v_fma_f32 v179, v179, v160, v162\n\t" \
  "v_mfma_f32_16x16x32_bf16 a[48:51], a[172:175], v[80:83], a[48:51]\n\t" \
  "s_add_u32 s97, %[rot19], 2621440\n\t" \
  "s_bfe_u32 s97, s97, 0x140003\n\t" \
  "v_fma_f32 v180, v180, v160, v162\n\t" \
  "v_fma_f32 v181, v181, v160, v162\n\t" \
  "v_mfma_f32_16x16x32_bf16 a[64:67], a[176:179], v[80:83], a[64:67]\n\t" \
  "ds_read_b64_tr_b16 a[156:157], v191 offset:0\n\t" \
  "v_fma_f32 v182, v182, v160, v162\n\t" \
  "v_fma_f32 v183, v183, v160, v162\n\t" \
  "v_mfma_f32_16x16x32_bf16 a[68:71], a[176:179], v[84:87], a[68:71]\n\t" \
  "s_add_u32 s97, s97, %[sox]\n\t" \
  "ds_read_b64_tr_b16 a[158:159], v191 offset:1024\n\t" \
  "v_fma_f32 v176, v176, v144, v152\n\t" \
  "v_fma_f32 v177, v177, v145, v153\n\t" \
  "v_fma_f32 v178, v178, v146, v154\n\t" \
  "v_mfma_f32_16x16x32_bf16 a[72:75], a[176:179], v[88:91], a[72:75]\n\t" \
  "v_fma_f32 v179, v179, v147, v155\n\t" \
  "v_fma_f32 v180, v180, v148, v156\n\t" \
  "v_mfma_f32_16x16x32_bf16 a[76:79], a[176:179], v[92:95], a[76:79]\n\t" \
  "v_fma_f32 v181, v181, v149, v157\n\t" \
  "v_fma_f32 v182, v182, v150, v158\n\t" \
  "v_mfma_f32_16x16x32_bf16 a[92:95], a[180:183], v[92:95], a[92:95]\n\t" \
  "buffer_load_dwordx4 a[224:227], v248, %[ws], s97 offen sc1\n\t" \
  "v_fma_f32 v183, v183, v151, v159\n\t" \
  "v_cvt_pk_bf16_f32 v112, v176, v177\n\t" \
  "v_mfma_f32_16x16x32_bf16 a[88:91], a[180:183], v[88:91], a[88:91]\n\t" \
  "v_cvt_pk_bf16_f32 v113, v178, v179\n\t" \
  "v_cvt_pk_bf16_f32 v114, v180, v181\n\t" \
  "v_mfma_f32_16x16x32_bf16 a[84:87], a[180:183], v[84:87], a[84:87]\n\t" \
  "buffer_load_dwordx4 a[228:231], v249, %[ws], s97 offen sc1\n\t" \
  "v_cvt_pk_bf16_f32 v115, v182, v183\n\t" \
  "v_lshlrev_b32 v176, 16, v116\n\t" \
  "v_mfma_f32_16x16x32_bf16 a[80:83], a[180:183], v[80:83], a[80:83]\n\t" \
  "v_and_b32 v177, s98, v116\n\t" \
  "v_lshlrev_b32 v178, 16, v117\n\t" \
  "v_mfma_f32_16x16x32_bf16 a[96:99], a[184:187], v[80:83], a[96:99]\n\t" \
  "v_and_b32 v179, s98, v117\n\t" \
  "v_lshlrev_b32 v180, 16, v118\n\t" \
  "v_mfma_f32_16x16x32_bf16 a[100:103], a[184:187], v[84:87], a[100:103]\n\t" \
  "buffer_load_dwordx4 a[232:235], v250, %[ws], s97 offen sc1\n\t" \
  "v_and_b32 v181, s98, v118\n\t" \
  "v_lshlrev_b32 v182, 16, v119\n\t" \
  "v_mfma_f32_16x16x32_bf16 a[104:107], a[184:187], v[88:91], a[104:107]\n\t" \
  "v_and_b32 v183, s98, v119\n\t" \
  "v_fma_f32 v176, v176, v164, v166\n\t" \
  "v_mfma_f32_16x16x32_bf16 a[108:111], a[184:187], v[92:95], a[108:111]\n\t" \
  "buffer_load_dwordx4 a[236:239], v251, %[ws], s97 offen sc1\n\t" \
  "v_fma_f32 v177, v177, v164, v166\n\t" \
  "v_fma_f32 v178, v178, v164, v166\n\t" \
  "v_mfma_f32_16x16x32_bf16 a[124:127], a[188:191], v[92:95], a[124:127]\n\t" \
  "v_fma_f32 v179, v179, v164, v166\n\t" \
  "v_fma_f32 v180, v180, v164, v166\n\t" \
  "v_mfma_f32_16x16x32_bf16 a[120:123], a[188:191], v[88:91], a[120:123]\n\t" \
  "v_fma_f32 v181, v181, v164, v166\n\t" \
  "v_fma_f32 v182, v182, v164, v166\n\t" \
  "v_mfma_f32_16x16x32_bf16 a[116:119], a[188:191], v[84:87], a[116:119]\n\t" \
  "v_fma_f32 v183, v183, v164, v166\n\t" \
  "v_fma_f32 v176, v176, v144, v152\n\t" \
  "v_mfma_f32_16x16x32_bf16 a[112:115], a[188:191], v[80:83], a[112:115]\n\t" \
  FSTP("v_writelane_b32 %[stv], s94, 46\n\t") \
  FSTP("v_writelane_b32 %[stv], s92, 47\n\t") \
  FSTP("s_memrealtime s[94:95]\n\t") \
  FSTP("s_memtime s[92:93]\n\t") \
  "s_waitcnt lgkmcnt(0)\n\t" \
  "v_mfma_f32_16x16x32_bf16 a[0:3], a[128:131], v[96:99], a[0:3]\n\t" \
  "s_add_u32 s96, %[rot19], 3145728\n\t" \
  "s_and_b32 s96, s96, 0x780000\n\t" \
  "v_fma_f32 v177, v177, v145, v153\n\t" \
  "v_fma_f32 v178, v178, v146, v154\n\t" \
  "v_fma_f32 v179, v179, v147, v155\n\t" \
  "v_mfma_f32_16x16x32_bf16 a[4:7], a[128:131], v[100:103], a[4:7]\n\t" \
  "ds_read_b64_tr_b16 a[160:161], v184 offset:8192\n\t" \
  "v_fma_f32 v180, v180, v148, v156\n\t" \
  "v_fma_f32 v181, v181, v149, v157\n\t" \
  "v_mfma_f32_16x16x32_bf16 a[8:11], a[128:131], v[104:107], a[8:11]\n\t" \
  "ds_read_b64_tr_b16 a[162:163], v184 offset:9216\n\t" \
  "v_fma_f32 v182, v182, v150, v158\n\t" \
  "v_fma_f32 v183, v183, v151, v159\n\t" \
  "v_mfma_f32_16x16x32_bf16 a[12:15], a[128:131], v[108:111], a[12:15]\n\t" \
  "s_add_u32 m0, %[mw], 16384\n\t" \
  "ds_read_b64_tr_b16 a[164:165], v185 offset:8192\n\t" \
  "v_cvt_pk_bf16_f32 v116, v176, v177\n\t" \
  "v_cvt_pk_bf16_f32 v117, v178, v179\n\t" \
  "v_mfma_f32_16x16x32_bf16 a[28:31], a[132:135], v[108:111], a[28:31]\n\t" \
  "ds_read_b64_tr_b16 a[166:167], v185 offset:9216\n\t" \
  "v_cvt_pk_bf16_f32 v118, v180, v181\n\t" \
  "v_cvt_pk_bf16_f32 v119, v182, v183\n\t" \
  "v_lshlrev_b32 v176, 16, v120\n\t" \
  "v_mfma_f32_16x16x32_bf16 a[24:27], a[132:135], v[104:107], a[24:27]\n\t" \
  "ds_read_b64_tr_b16 a[168:169], v186 offset:8192\n\t" \
  "v_and_b32 v177, s98, v120\n\t" \
  "v_lshlrev_b32 v178, 16, v121\n\t" \
  "v_mfma_f32_16x16x32_bf16 a[20:23], a[132:135], v[100:103], a[20:23]\n\t" \
  "buffer_load_dwordx4 v200, %[sw], s96 offen offset:0 lds\n\t" \
  "ds_read_b64_tr_b16 a[170:171], v186 offset:9216\n\t" \
  "v_and_b32 v179, s98, v121\n\t" \
  "v_lshlrev_b32 v180, 16, v122\n\t" \
  "v_mfma_f32_16x16x32_bf16 a[16:19], a[132:135], v[96:99], a[16:19]\n\t" \
  "ds_read_b64_tr_b16 a[172:173], v187 offset:8192\n\t" \
  "v_and_b32 v181, s98, v122\n\t" \
  "v_lshlrev_b32 v182, 16, v123\n\t" \
  "v_mfma_f32_16x16x32_bf16 a[32:35], a[136:139], v[96:99], a[32:35]\n\t" \
  "ds_read_b64_tr_b16 a[174:175], v187 offset:9216\n\t" \
  "v_and_b32 v183, s98, v123\n\t" \
  "v_fma_f32 v176, v176, v168, v170\n\t" \
  "v_fma_f32 v177, v177, v168, v170\n\t" \
  "v_mfma_f32_16x16x32_bf16 a[36:39], a[136:139], v[100:103], a[36:39]\n\t" \
  "ds_read_b64_tr_b16 a[176:177], v188 offset:8192\n\t" \
  "v_fma_f32 v178, v178, v168, v170\n\t" \
  "v_fma_f32 v179, v179, v168, v170\n\t" \
  "v_mfma_f32_16x16x32_bf16 a[40:43], a[136:139], v[104:107], a[40:43]\n\t" \
  "buffer_load_dwordx4 v201, %[sw], s96 offen offset:1024 lds\n\t" \
  "ds_read_b64_tr_b16 a[178:179], v188 offset:9216\n\t" \
  "v_fma_f32 v180, v180, v168, v170\n\t" \
  "v_fma_f32 v181, v181, v168, v170\n\t" \
  "v_mfma_f32_16x16x32_bf16 a[44:47], a[136:139], v[108:111], a[44:47]\n\t" \
  "ds_read_b64_tr_b16 a[180:181], v189 offset:8192\n\t" \
  "v_fma_f32 v182, v182, v168, v170\n\t" \
  "v_fma_f32 v183, v183, v168, v170\n\t" \
  "v_mfma_f32_16x16x32_bf16 a[60:63], a[140:143], v[108:111], a[60:63]\n\t" \
  "ds_read_b64_tr_b16 a[182:183], v189 offset:9216\n\t" \
  "v_fma_f32 v176, v176, v144, v152\n\t" \
  "v_fma_f32 v177, v177, v145, v153\n\t" \
  "v_fma_f32 v178, v178, v146, v154\n\t" \
  "v_mfma_f32_16x16x32_bf16 a[56:59], a[140:143], v[104:107], a[56:59]\n\t" \
  "buffer_load_dwordx4 v202, %[sw], s96 offen offset:2048 lds\n\t" \
  "ds_read_b64_tr_b16 a[184:185], v190 offset:8192\n\t" \
  "v_fma_f32 v179, v179, v147, v155\n\t" \
  "v_fma_f32 v180, v180, v148, v156\n\t" \
  "v_mfma_f32_16x16x32_bf16 a[52:55], a[140:143], v[100:103], a[52:55]\n\t" \
  "ds_read_b64_tr_b16 a[186:187], v190 offset:9216\n\t" \
  "v_fma_f32 v181, v181, v149, v157\n\t" \
  "v_fma_f32 v182, v182, v150, v158\n\t" \
  "v_mfma_f32_16x16x32_bf16 a[48:51], a[140:143], v[96:99], a[48:51]\n\t" \
  "ds_read_b64_tr_b16 a[188:189], v191 offset:8192\n\t" \
  "v_fma_f32 v183, v183, v151, v159\n\t" \
  "v_cvt_pk_bf16_f32 v120, v176, v177\n\t" \
  "v_mfma_f32_16x16x32_bf16 a[64:67], a[144:147], v[96:99], a[64:67]\n\t" \
  "buffer_load_dwordx4 v203, %[sw], s96 offen offset:3072 lds\n\t" \
  "ds_read_b64_tr_b16 a[190:191], v191 offset:9216\n\t" \
  "v_cvt_pk_bf16_f32 v121, v178, v179\n\t" \
  "v_cvt_pk_bf16_f32 v122, v180, v181\n\t" \
  "v_mfma_f32_16x16x32_bf16 a[68:71], a[144:147], v[100:103], a[68:71]\n\t" \
  "v_cvt_pk_bf16_f32 v123, v182, v183\n\t" \
  "v_lshlrev_b32 v176, 16, v124\n\t" \
  "v_and_b32 v177, s98, v124\n\t" \
  "v_mfma_f32_16x16x32_bf16 a[72:75], a[144:147], v[104:107], a[72:75]\n\t" \
  "v_lshlrev_b32 v178, 16, v125\n\t" \
  "v_and_b32 v179, s98, v125\n\t" \
  "v_mfma_f32_16x16x32_bf16 a[76:79], a[144:147], v[108:111], a[76:79]\n\t" \
  "v_lshlrev_b32 v180, 16, v126\n\t" \
  "v_and_b32 v181, s98, v126\n\t" \
  "v_mfma_f32_16x16x32_bf16 a[92:95], a[148:151], v[108:111], a[92:95]\n\t" \
  "v_mbcnt_lo_u32_b32 v217, -1, 0\n\t" \
  "v_mbcnt_hi_u32_b32 v217, -1, v217\n\t" \
  "v_lshlrev_b32 v182, 16, v127\n\t" \
  "v_and_b32 v183, s98, v127\n\t" \
  "v_mfma_f32_16x16x32_bf16 a[88:91], a[148:151], v[104:107], a[88:91]\n\t" \
  "v_fma_f32 v176, v176, v172, v174\n\t" \
  "v_fma_f32 v177, v177, v172, v174\n\t" \
  "v_mfma_f32_16x16x32_bf16 a[84:87], a[148:151], v[100:103], a[84:87]\n\t" \
  "v_fma_f32 v178, v178, v172, v174\n\t" \
  "v_fma_f32 v179, v179, v172, v174\n\t" \
  "v_mfma_f32_16x16x32_bf16 a[80:83], a[148:151], v[96:99], a[80:83]\n\t" \
  "v_cmp_gt_u32 vcc, 16, v217\n\t" \
  "v_lshlrev_b32 v217, 7, v217\n\t" \
  "v_fma_f32 v180, v180, v172, v174\n\t" \
  "v_fma_f32 v181, v181, v172, v174\n\t" \
  "v_mfma_f32_16x16x32_bf16 a[96:99], a[152:155], v[96:99], a[96:99]\n\t" \
  "v_fma_f32 v182, v182, v172, v174\n\t" \
  "v_fma_f32 v183, v183, v172, v174\n\t" \
  "v_mfma_f32_16x16x32_bf16 a[100:103], a[152:155], v[100:103], a[100:103]\n\t" \
  "v_fma_f32 v176, v176, v144, v152\n\t" \
  "v_fma_f32 v177, v177, v145, v153\n\t" \
  "v_mfma_f32_16x16x32_bf16 a[104:107], a[152:155], v[104:107], a[104:107]\n\t" \
  "v_bfrev_b32 v215, 1\n\t" \
  "v_cndmask_b32 v217, v215, v217, vcc\n\t" \
  "v_fma_f32 v178, v178, v146, v154\n\t" \
  "v_fma_f32 v179, v179, v147, v155\n\t" \
  "v_mfma_f32_16x16x32_bf16 a[108:111], a[152:155], v[108:111], a[108:111]\n\t" \
  "v_fma_f32 v180, v180, v148, v156\n\t" \
  "v_fma_f32 v181, v181, v149, v157\n\t" \
  "v_mfma_f32_16x16x32_bf16 a[124:127], a[156:159], v[108:111], a[124:127]\n\t" \
  "v_fma_f32 v182, v182, v150, v158\n\t" \
  "v_fma_f32 v183, v183, v151, v159\n\t" \
  "v_mfma_f32_16x16x32_bf16 a[120:123], a[156:159], v[104:107], a[120:123]\n\t" \
  "v_cvt_pk_bf16_f32 v124, v176, v177\n\t" \
  "v_cvt_pk_bf16_f32 v125, v178, v179\n\t" \
  "v_mfma_f32_16x16x32_bf16 a[116:119], a[156:159], v[100:103], a[116:119]\n\t" \
  "v_cvt_pk_bf16_f32 v126, v180, v181\n\t" \
  "v_cvt_pk_bf16_f32 v127, v182, v183\n\t" \
  "v_mfma_f32_16x16x32_bf16 a[112:115], a[156:159], v[96:99], a[112:115]\n\t" \
  FSTP("v_writelane_b32 %[stv], s94, 48\n\t") \
  FSTP("v_writelane_b32 %[stv], s92, 49\n\t") \
  FSTP("s_memrealtime s[94:95]\n\t") \
  FSTP("s_memtime s[92:93]\n\t") \
  "s_waitcnt vmcnt(21) lgkmcnt(0)\n\t" \
  "s_barrier\n\t" \
  "v_mfma_f32_16x16x32_bf16 a[0:3], a[160:163], v[112:115], a[0:3]\n\t" \
  "s_add_u32 s97, s97, %[dk]\n\t" \
  "ds_read_b64_tr_b16 a[128:129], v184 offset:49152\n\t" \
  "v_mfma_f32_16x16x32_bf16 a[4:7], a[160:163], v[116:119], a[4:7]\n\t" \
  "ds_read_b64_tr_b16 a[130:131], v184 offset:50176\n\t" \
  "v_mfma_f32_16x16x32_bf16 a[8:11], a[160:163], v[120:123], a[8:11]\n\t" \
  "buffer_load_dwordx4 a[240:243], v248, %[ws], s97 offen sc1\n\t" \
  "ds_read_b64_tr_b16 a[132:133], v185 offset:49152\n\t" \
  "v_mfma_f32_16x16x32_bf16 a[12:15], a[160:163], v[124:127], a[12:15]\n\t" \
  "ds_read_b64_tr_b16 a[134:135], v185 offset:50176\n\t" \
  "v_mfma_f32_16x16x32_bf16 a[28:31], a[164:167], v[124:127], a[28:31]\n\t" \
  "ds_read_b64_tr_b16 a[136:137], v186 offset:49152\n\t" \
  "v_mfma_f32_16x16x32_bf16 a[24:27], a[164:167], v[120:123], a[24:27]\n\t" \
  "buffer_load_dwordx4 a[244:247], v249, %[ws], s97 offen sc1\n\t" \
  "ds_read_b64_tr_b16 a[138:139], v186 offset:50176\n\t" \
  "v_mfma_f32_16x16x32_bf16 a[20:23], a[164:167], v[116:119], a[20:23]\n\t" \
  "ds_read_b64_tr_b16 a[140:141], v187 offset:49152\n\t" \
  "v_mfma_f32_16x16x32_bf16 a[16:19], a[164:167], v[112:115], a[16:19]\n\t" \
  "buffer_load_dwordx4 a[248:251], v250, %[ws], s97 offen sc1\n\t" \
  "ds_read_b64_tr_b16 a[142:143], v187 offset:50176\n\t" \
  "v_mfma_f32_16x16x32_bf16 a[32:35], a[168:171], v[112:115], a[32:35]\n\t" \
  "ds_read_b64_tr_b16 a[144:145], v188 offset:49152\n\t" \
  "v_mfma_f32_16x16x32_bf16 a[36:39], a[168:171], v[116:119], a[36:39]\n\t" \
  "ds_read_b64_tr_b16 a[146:147], v188 offset:50176\n\t" \
  "v_mfma_f32_16x16x32_bf16 a[40:43], a[168:171], v[120:123], a[40:43]\n\t" \
  "buffer_load_dwordx4 a[252:255], v251, %[ws], s97 offen sc1\n\t" \
  "ds_read_b64_tr_b16 a[148:149], v189 offset:49152\n\t" \
  "v_mfma_f32_16x16x32_bf16 a[44:47], a[168:171], v[124:127], a[44:47]\n\t" \
  "ds_read_b64_tr_b16 a[150:151], v189 offset:50176\n\t" \
  "v_mfma_f32_16x16x32_bf16 a[60:63], a[172:175], v[124:127], a[60:63]\n\t" \
  "s_add_u32 s96, %[rot19], 3670016\n\t" \
  "s_and_b32 s96, s96, 0x780000\n\t" \
  "v_mfma_f32_16x16x32_bf16 a[56:59], a[172:175], v[120:123], a[56:59]\n\t" \
  "ds_read_b64_tr_b16 a[152:153], v190 offset:49152\n\t" \
  "v_mfma_f32_16x16x32_bf16 a[52:55], a[172:175], v[116:119], a[52:55]\n\t" \
  "ds_read_b64_tr_b16 a[154:155], v190 offset:50176\n\t" \
  "v_mfma_f32_16x16x32_bf16 a[48:51], a[172:175], v[112:115], a[48:51]\n\t" \
  "s_add_u32 m0, %[mw], 32768\n\t" \
  "ds_read_b64_tr_b16 a[156:157], v191 offset:49152\n\t" \
  "v_mfma_f32_16x16x32_bf16 a[64:67], a[176:179], v[112:115], a[64:67]\n\t" \
  "ds_read_b64_tr_b16 a[158:159], v191 offset:50176\n\t" \
  "v_mfma_f32_16x16x32_bf16 a[68:71], a[176:179], v[116:119], a[68:71]\n\t" \
  "buffer_load_dwordx4 v200, %[sw], s96 offen offset:0 lds\n\t" \
  "v_mfma_f32_16x16x32_bf16 a[72:75], a[176:179], v[120:123], a[72:75]\n\t" \
  "v_mfma_f32_16x16x32_bf16 a[76:79], a[176:179], v[124:127], a[76:79]\n\t" \
  "v_mfma_f32_16x16x32_bf16 a[92:95], a[180:183], v[124:127], a[92:95]\n\t" \
  "buffer_load_dwordx4 v201, %[sw], s96 offen offset:1024 lds\n\t" \
  "v_mfma_f32_16x16x32_bf16 a[88:91], a[180:183], v[120:123], a[88:91]\n\t" \
  "v_mfma_f32_16x16x32_bf16 a[84:87], a[180:183], v[116:119], a[84:87]\n\t" \
  "buffer_load_dwordx4 v202, %[sw], s96 offen offset:2048 lds\n\t" \
  "v_mfma_f32_16x16x32_bf16 a[80:83], a[180:183], v[112:115], a[80:83]\n\t" \
  "v_mfma_f32_16x16x32_bf16 a[96:99], a[184:187], v[112:115], a[96:99]\n\t" \
  "v_mfma_f32_16x16x32_bf16 a[100:103], a[184:187], v[116:119], a[100:103]\n\t" \
  "buffer_load_dwordx4 v203, %[sw], s96 offen offset:3072 lds\n\t" \
  "v_mfma_f32_16x16x32_bf16 a[104:107], a[184:187], v[120:123], a[104:107]\n\t" \
  "v_mfma_f32_16x16x32_bf16 a[108:111], a[184:187], v[124:127], a[108:111]\n\t" \
  "s_add_u32 s99, %[pf1], 0\n\t" \
  "buffer_load_dword %[pf], v217, %[wo], s99 offen\n\t" \
  "v_mfma_f32_16x16x32_bf16 a[124:127], a[188:191], v[124:127], a[124:127]\n\t" \
  "v_mfma_f32_16x16x32_bf16 a[120:123], a[188:191], v[120:123], a[120:123]\n\t" \
  "v_mfma_f32_16x16x32_bf16 a[116:119], a[188:191], v[116:119], a[116:119]\n\t" \
  "v_mfma_f32_16x16x32_bf16 a[112:115], a[188:191], v[112:115], a[112:115]\n\t" \
  FSTP("v_writelane_b32 %[stv], s94, 50\n\t") \
  FSTP("v_writelane_b32 %[stv], s92, 51\n\t") \
  FSTP("s_memrealtime s[94:95]\n\t") \
  FSTP("s_memtime s[92:93]\n\t") \
  FSTS("s_memrealtime s[94:95]\n\t") \
  FSTS("s_waitcnt lgkmcnt(0)\n\t") \
  FSTS("v_writelane_b32 %[stv], s94, 14\n\t") \
  "s_waitcnt vmcnt(25) lgkmcnt(0)\n\t" \
  "v_mfma_f32_16x16x32_bf16 a[0:3], a[128:131], a[192:195], a[0:3]\n\t" \
  "s_add_u32 s96, %[rot19], 4194304\n\t" \
  "s_and_b32 s96, s96, 0x780000\n\t" \
  "v_mfma_f32_16x16x32_bf16 a[4:7], a[128:131], a[196:199], a[4:7]\n\t" \
  "ds_read_b64_tr_b16 a[160:161], v184 offset:57344\n\t" \
  "v_mfma_f32_16x16x32_bf16 a[8:11], a[128:131], a[200:203], a[8:11]\n\t" \
  "ds_read_b64_tr_b16 a[162:163], v184 offset:58368\n\t" \
  "v_mfma_f32_16x16x32_bf16 a[12:15], a[128:131], a[204:207], a[12:15]\n\t" \
  "ds_read_b64_tr_b16 a[164:165], v185 offset:57344\n\t" \
  "v_mfma_f32_16x16x32_bf16 a[28:31], a[132:135], a[204:207], a[28:31]\n\t" \
  "v_mfma_f32_16x16x32_bf16 a[24:27], a[132:135], a[200:203], a[24:27]\n\t" \
  "s_add_u32 m0, %[mw], 0\n\t" \
  "ds_read_b64_tr_b16 a[166:167], v185 offset:58368\n\t" \
  "v_mfma_f32_16x16x32_bf16 a[20:23], a[132:135], a[196:199], a[20:23]\n\t" \
  "ds_read_b64_tr_b16 a[168:169], v186 offset:57344\n\t" \
  "v_mfma_f32_16x16x32_bf16 a[16:19], a[132:135], a[192:195], a[16:19]\n\t" \
  "v_mfma_f32_16x16x32_bf16 a[32:35], a[136:139], a[192:195], a[32:35]\n\t" \
  "ds_read_b64_tr_b16 a[170:171], v186 offset:58368\n\t" \
  "v_accvgpr_read_b32 v0, a0\n\t" \
  "v_mfma_f32_16x16x32_bf16 a[36:39], a[136:139], a[196:199], a[36:39]\n\t" \
  "ds_read_b64_tr_b16 a[172:173], v187 offset:57344\n\t" \
  "v_accvgpr_read_b32 v1, a1\n\t" \
  "v_mfma_f32_16x16x32_bf16 a[40:43], a[136:139], a[200:203], a[40:43]\n\t" \
  "buffer_load_dwordx4 v200, %[sw], s96 offen offset:0 lds\n\t" \
  "v_accvgpr_read_b32 v2, a2\n\t" \
  "v_mfma_f32_16x16x32_bf16 a[44:47], a[136:139], a[204:207], a[44:47]\n\t" \
  "ds_read_b64_tr_b16 a[174:175], v187 offset:58368\n\t" \
  "v_accvgpr_read_b32 v3, a3\n\t" \
  "v_mfma_f32_16x16x32_bf16 a[60:63], a[140:143], a[204:207], a[60:63]\n\t" \
  "ds_read_b64_tr_b16 a[176:177], v188 offset:57344\n\t" \
  "v_accvgpr_read_b32 v32, a4\n\t" \
  "v_mfma_f32_16x16x32_bf16 a[56:59], a[140:143], a[200:203], a[56:59]\n\t" \
  "v_accvgpr_read_b32 v33, a5\n\t" \
  "v_accvgpr_read_b32 v34, a6\n\t" \
  "v_mfma_f32_16x16x32_bf16 a[52:55], a[140:143], a[196:199], a[52:55]\n\t" \
  "ds_read_b64_tr_b16 a[178:179], v188 offset:58368\n\t" \
  "v_accvgpr_read_b32 v35, a7\n\t" \
  "v_mfma_f32_16x16x32_bf16 a[48:51], a[140:143], a[192:195], a[48:51]\n\t" \
  "buffer_load_dwordx4 v201, %[sw], s96 offen offset:1024 lds\n\t" \
  "ds_read_b64_tr_b16 a[180:181], v189 offset:57344\n\t" \
  "v_mfma_f32_16x16x32_bf16 a[64:67], a[144:147], a[192:195], a[64:67]\n\t" \
  "v_accvgpr_read_b32 v64, a8\n\t" \
  "v_accvgpr_read_b32 v65, a9\n\t" \
  "v_mfma_f32_16x16x32_bf16 a[68:71], a[144:147], a[196:199], a[68:71]\n\t" \
  "ds_read_b64_tr_b16 a[182:183], v189 offset:58368\n\t" \
  "v_accvgpr_read_b32 v66, a10\n\t" \
  "v_mfma_f32_16x16x32_bf16 a[72:75], a[144:147], a[200:203], a[72:75]\n\t" \
  "ds_read_b64_tr_b16 a[184:185], v190 offset:57344\n\t" \
  "v_accvgpr_read_b32 v67, a11\n\t" \
  "v_mfma_f32_16x16x32_bf16 a[76:79], a[144:147], a[204:207], a[76:79]\n\t" \
  "v_accvgpr_read_b32 v96, a12\n\t" \
  "v_accvgpr_read_b32 v97, a13\n\t" \
  "v_mfma_f32_16x16x32_bf16 a[92:95], a[148:151], a[204:207], a[92:95]\n\t" \
  "buffer_load_dwordx4 v202, %[sw], s96 offen offset:2048 lds\n\t" \
  "ds_read_b64_tr_b16 a[186:187], v190 offset:58368\n\t" \
  "v_mfma_f32_16x16x32_bf16 a[88:91], a[148:151], a[200:203], a[88:91]\n\t" \
  "ds_read_b64_tr_b16 a[188:189], v191 offset:57344\n\t" \
  "v_accvgpr_read_b32 v98, a14\n\t" \
  "v_mfma_f32_16x16x32_bf16 a[84:87], a[148:151], a[196:199], a[84:87]\n\t" \
  "v_accvgpr_read_b32 v99, a15\n\t" \
  "v_accvgpr_read_b32 v100, a28\n\t" \
  "v_mfma_f32_16x16x32_bf16 a[80:83], a[148:151], a[192:195], a[80:83]\n\t" \
  "ds_read_b64_tr_b16 a[190:191], v191 offset:58368\n\t" \
  "v_accvgpr_read_b32 v101, a29\n\t" \
  "v_mfma_f32_16x16x32_bf16 a[96:99], a[152:155], a[192:195], a[96:99]\n\t" \
  "v_accvgpr_read_b32 v102, a30\n\t" \
  "v_accvgpr_read_b32 v103, a31\n\t" \
  "v_mfma_f32_16x16x32_bf16 a[100:103], a[152:155], a[196:199], a[100:103]\n\t" \
  "buffer_load_dwordx4 v203, %[sw], s96 offen offset:3072 lds\n\t" \
  "v_accvgpr_read_b32 v68, a24\n\t" \
  "v_accvgpr_read_b32 v69, a25\n\t" \
  "v_mfma_f32_16x16x32_bf16 a[104:107], a[152:155], a[200:203], a[104:107]\n\t" \
  "v_accvgpr_read_b32 v70, a26\n\t" \
  "v_accvgpr_read_b32 v71, a27\n\t" \
  "v_mfma_f32_16x16x32_bf16 a[108:111], a[152:155], a[204:207], a[108:111]\n\t" \
  "v_accvgpr_read_b32 v36, a20\n\t" \
  "v_accvgpr_read_b32 v37, a21\n\t" \
  "v_mfma_f32_16x16x32_bf16 a[124:127], a[156:159], a[204:207], a[124:127]\n\t" \
  "v_accvgpr_read_b32 v38, a22\n\t" \
  "v_accvgpr_read_b32 v39, a23\n\t" \
  "v_mfma_f32_16x16x32_bf16 a[120:123], a[156:159], a[200:203], a[120:123]\n\t" \
  "v_accvgpr_read_b32 v4, a16\n\t" \
  "v_accvgpr_read_b32 v5, a17\n\t" \
  "v_mfma_f32_16x16x32_bf16 a[116:119], a[156:159], a[196:199], a[116:119]\n\t" \
  "v_accvgpr_read_b32 v6, a18\n\t" \
  "v_accvgpr_read_b32 v7, a19\n\t" \
  "v_mfma_f32_16x16x32_bf16 a[112:115], a[156:159], a[192:195], a[112:115]\n\t" \
  "s_waitcnt vmcnt(21) lgkmcnt(0)\n\t" \
  "s_barrier\n\t" \
  "v_mfma_f32_16x16x32_bf16 a[32:35], a[168:171], a[208:211], a[32:35]\n\t" \
  "s_add_u32 s97, %[rot19], 3145728\n\t" \
  "s_bfe_u32 s97, s97, 0x140003\n\t" \
  "v_mfma_f32_16x16x32_bf16 a[36:39], a[168:171], a[212:215], a[36:39]\n\t" \
  "ds_read_b64_tr_b16 a[128:129], v192 offset:0\n\t" \
  "v_mfma_f32_16x16x32_bf16 a[40:43], a[168:171], a[216:219], a[40:43]\n\t" \
  "s_add_u32 s97, s97, %[sox]\n\t" \
  "ds_read_b64_tr_b16 a[130:131], v192 offset:1024\n\t" \
  "v_mfma_f32_16x16x32_bf16 a[44:47], a[168:171], a[220:223], a[44:47]\n\t" \
  "ds_read_b64_tr_b16 a[132:133], v193 offset:0\n\t" \
  "v_mfma_f32_16x16x32_bf16 a[60:63], a[172:175], a[220:223], a[60:63]\n\t" \
  "v_mfma_f32_16x16x32_bf16 a[56:59], a[172:175], a[216:219], a[56:59]\n\t" \
  "buffer_load_dwordx4 a[192:195], v248, %[ws], s97 offen sc1\n\t" \
  "ds_read_b64_tr_b16 a[134:135], v193 offset:1024\n\t" \
  "v_mfma_f32_16x16x32_bf16 a[52:55], a[172:175], a[212:215], a[52:55]\n\t" \
  "ds_read_b64_tr_b16 a[136:137], v194 offset:0\n\t" \
  "v_mfma_f32_16x16x32_bf16 a[48:51], a[172:175], a[208:211], a[48:51]\n\t" \
  "buffer_load_dwordx4 a[196:199], v249, %[ws], s97 offen sc1\n\t" \
  "v_mfma_f32_16x16x32_bf16 a[64:67], a[176:179], a[208:211], a[64:67]\n\t" \
  "ds_read_b64_tr_b16 a[138:139], v194 offset:1024\n\t" \
  "v_accvgpr_read_b32 v8, a32\n\t" \
  "v_mfma_f32_16x16x32_bf16 a[68:71], a[176:179], a[212:215], a[68:71]\n\t" \
  "ds_read_b64_tr_b16 a[140:141], v195 offset:0\n\t" \
  "v_accvgpr_read_b32 v9, a33\n\t" \
  "v_mfma_f32_16x16x32_bf16 a[72:75], a[176:179], a[216:219], a[72:75]\n\t" \
  "buffer_load_dwordx4 a[200:203], v250, %[ws], s97 offen sc1\n\t" \
  "v_accvgpr_read_b32 v10, a34\n\t" \
  "v_mfma_f32_16x16x32_bf16 a[76:79], a[176:179], a[220:223], a[76:79]\n\t" \
  "ds_read_b64_tr_b16 a[142:143], v195 offset:1024\n\t" \
  "v_accvgpr_read_b32 v11, a35\n\t" \
  "v_mfma_f32_16x16x32_bf16 a[92:95], a[180:183], a[220:223], a[92:95]\n\t" \
  "buffer_load_dwordx4 a[204:207], v251, %[ws], s97 offen sc1\n\t" \
  "ds_read_b64_tr_b16 a[144:145], v196 offset:0\n\t" \
  "v_mfma_f32_16x16x32_bf16 a[88:91], a[180:183], a[216:219], a[88:91]\n\t" \
  "v_accvgpr_read_b32 v40, a36\n\t" \
  "v_accvgpr_read_b32 v41, a37\n\t" \
  "v_mfma_f32_16x16x32_bf16 a[84:87], a[180:183], a[212:215], a[84:87]\n\t" \
  "ds_read_b64_tr_b16 a[146:147], v196 offset:1024\n\t" \
  "v_accvgpr_read_b32 v42, a38\n\t" \
  "v_mfma_f32_16x16x32_bf16 a[80:83], a[180:183], a[208:211], a[80:83]\n\t" \
  "s_add_u32 s96, %[rot19], 4718592\n\t" \
  "s_and_b32 s96, s96, 0x780000\n\t" \
  "v_mfma_f32_16x16x32_bf16 a[96:99], a[184:187], a[208:211], a[96:99]\n\t" \
  "ds_read_b64_tr_b16 a[148:149], v197 offset:0\n\t" \
  "v_accvgpr_read_b32 v43, a39\n\t" \
  "v_mfma_f32_16x16x32_bf16 a[100:103], a[184:187], a[212:215], a[100:103]\n\t" \
  "s_add_u32 m0, %[mw], 49152\n\t" \
  "ds_read_b64_tr_b16 a[150:151], v197 offset:1024\n\t" \
  "v_mfma_f32_16x16x32_bf16 a[104:107], a[184:187], a[216:219], a[104:107]\n\t" \
  "ds_read_b64_tr_b16 a[152:153], v198 offset:0\n\t" \
  "v_accvgpr_read_b32 v72, a40\n\t" \
  "v_mfma_f32_16x16x32_bf16 a[108:111], a[184:187], a[220:223], a[108:111]\n\t" \
  "v_accvgpr_read_b32 v73, a41\n\t" \
  "v_accvgpr_read_b32 v74, a42\n\t" \
  "v_mfma_f32_16x16x32_bf16 a[124:127], a[188:191], a[220:223], a[124:127]\n\t" \
  "buffer_load_dwordx4 v200, %[sw], s96 offen offset:0 lds\n\t" \
  "ds_read_b64_tr_b16 a[154:155], v198 offset:1024\n\t" \
  "v_accvgpr_read_b32 v75, a43\n\t" \
  "v_mfma_f32_16x16x32_bf16 a[120:123], a[188:191], a[216:219], a[120:123]\n\t" \
  "ds_read_b64_tr_b16 a[156:157], v199 offset:0\n\t" \
  "v_accvgpr_read_b32 v104, a44\n\t" \
  "v_accvgpr_read_b32 v105, a45\n\t" \
  "v_mfma_f32_16x16x32_bf16 a[116:119], a[188:191], a[212:215], a[116:119]\n\t" \
  "buffer_load_dwordx4 v201, %[sw], s96 offen offset:1024 lds\n\t" \
  "v_accvgpr_read_b32 v106, a46\n\t" \
  "v_accvgpr_read_b32 v107, a47\n\t" \
  "v_mfma_f32_16x16x32_bf16 a[112:115], a[188:191], a[208:211], a[112:115]\n\t" \
  "ds_read_b64_tr_b16 a[158:159], v199 offset:1024\n\t" \
  "v_accvgpr_read_b32 v108, a60\n\t" \
  "v_accvgpr_read_b32 v109, a61\n\t" \
  "v_mfma_f32_16x16x32_bf16 v[0:3], a[160:163], a[208:211], v[0:3]\n\t" \
  "v_accvgpr_read_b32 v110, a62\n\t" \
  "v_accvgpr_read_b32 v111, a63\n\t" \
  "v_mfma_f32_16x16x32_bf16 v[32:35], a[160:163], a[212:215], v[32:35]\n\t" \
  "buffer_load_dwordx4 v202, %[sw], s96 offen offset:2048 lds\n\t" \
  "v_accvgpr_read_b32 v76, a56\n\t" \
  "v_accvgpr_read_b32 v77, a57\n\t" \
  "v_mfma_f32_16x16x32_bf16 v[64:67], a[160:163], a[216:219], v[64:67]\n\t" \
  "v_accvgpr_read_b32 v78, a58\n\t" \
  ".Lhs2:\n\ts_cbranch_execz .Lhs2n\n\t" \
  "v_accvgpr_read_b32 v79, a59\n\t" \
  "v_mfma_f32_16x16x32_bf16 v[96:99], a[160:163], a[220:223], v[96:99]\n\t" \
  "buffer_load_dwordx4 v203, %[sw], s96 offen offset:3072 lds\n\t" \
  "v_accvgpr_read_b32 v44, a52\n\t" \
  "v_accvgpr_read_b32 v45, a53\n\t" \
  "v_mfma_f32_16x16x32_bf16 v[100:103], a[164:167], a[220:223], v[100:103]\n\t" \
  "v_accvgpr_read_b32 v46, a54\n\t" \
  "v_accvgpr_read_b32 v47, a55\n\t" \
  "v_mfma_f32_16x16x32_bf16 v[68:71], a[164:167], a[216:219], v[68:71]\n\t" \
  "v_accvgpr_read_b32 v12, a48\n\t" \
  "v_accvgpr_read_b32 v13, a49\n\t" \
  "v_mfma_f32_16x16x32_bf16 v[36:39], a[164:167], a[212:215], v[36:39]\n\t" \
  "v_accvgpr_read_b32 v14, a50\n\t" \
  "v_accvgpr_read_b32 v15, a51\n\t" \
  "v_mfma_f32_16x16x32_bf16 v[4:7], a[164:167], a[208:211], v[4:7]\n\t" \
  "s_waitcnt vmcnt(25) lgkmcnt(0)\n\t" \
  "v_mfma_f32_16x16x32_bf16 a[64:67], a[144:147], a[224:227], a[64:67]\n\t" \
  "v_add_u32 v216, 3145728, v216\n\t" \
  "v_and_b32 v216, 0x7fffff, v216\n\t" \
  "v_mfma_f32_16x16x32_bf16 a[68:71], a[144:147], a[228:231], a[68:71]\n\t" \
  "ds_read_b64_tr_b16 a[160:161], v192 offset:8192\n\t" \
  "v_mfma_f32_16x16x32_bf16 a[72:75], a[144:147], a[232:235], a[72:75]\n\t" \
  "ds_read_b64_tr_b16 a[162:163], v192 offset:9216\n\t" \
  "v_mfma_f32_16x16x32_bf16 a[76:79], a[144:147], a[236:239], a[76:79]\n\t" \
  "ds_read_b64_tr_b16 a[164:165], v193 offset:8192\n\t" \
  "v_mfma_f32_16x16x32_bf16 a[92:95], a[148:151], a[236:239], a[92:95]\n\t" \
  "global_load_dword %[pf], v216, %[wq]\n\t" \
  "v_mfma_f32_16x16x32_bf16 a[88:91], a[148:151], a[232:235], a[88:91]\n\t" \
  "ds_read_b64_tr_b16 a[166:167], v193 offset:9216\n\t" \
  "v_mfma_f32_16x16x32_bf16 a[84:87], a[148:151], a[228:231], a[84:87]\n\t" \
  "ds_read_b64_tr_b16 a[168:169], v194 offset:8192\n\t" \
  "v_mfma_f32_16x16x32_bf16 a[80:83], a[148:151], a[224:227], a[80:83]\n\t" \
  "v_mfma_f32_16x16x32_bf16 a[96:99], a[152:155], a[224:227], a[96:99]\n\t" \
  "s_add_u32 s97, s97, %[dk]\n\t" \
  "ds_read_b64_tr_b16 a[170:171], v194 offset:9216\n\t" \
  "v_mfma_f32_16x16x32_bf16 a[100:103], a[152:155], a[228:231], a[100:103]\n\t" \
  "ds_read_b64_tr_b16 a[172:173], v195 offset:8192\n\t" \
  "v_accvgpr_read_b32 v16, a64\n\t" \
  "v_mfma_f32_16x16x32_bf16 a[104:107], a[152:155], a[232:235], a[104:107]\n\t" \
  "v_accvgpr_read_b32 v17, a65\n\t" \
  "v_accvgpr_read_b32 v18, a66\n\t" \
  "v_mfma_f32_16x16x32_bf16 a[108:111], a[152:155], a[236:239], a[108:111]\n\t" \
  "ds_read_b64_tr_b16 a[174:175], v195 offset:9216\n\t" \
  "v_accvgpr_read_b32 v19, a67\n\t" \
  "v_mfma_f32_16x16x32_bf16 a[124:127], a[156:159], a[236:239], a[124:127]\n\t" \
  "buffer_load_dwordx4 a[208:211], v248, %[ws], s97 offen sc1\n\t" \
  "ds_read_b64_tr_b16 a[176:177], v196 offset:8192\n\t" \
  "v_mfma_f32_16x16x32_bf16 a[120:123], a[156:159], a[232:235], a[120:123]\n\t" \
  "v_accvgpr_read_b32 v48, a68\n\t" \
  "v_accvgpr_read_b32 v49, a69\n\t" \
  "v_mfma_f32_16x16x32_bf16 a[116:119], a[156:159], a[228:231], a[116:119]\n\t" \
  "ds_read_b64_tr_b16 a[178:179], v196 offset:9216\n\t" \
  "v_accvgpr_read_b32 v50, a70\n\t" \
  "v_mfma_f32_16x16x32_bf16 a[112:115], a[156:159], a[224:227], a[112:115]\n\t" \
  "ds_read_b64_tr_b16 a[180:181], v197 offset:8192\n\t" \
  "v_accvgpr_read_b32 v51, a71\n\t" \
  "v_mfma_f32_16x16x32_bf16 v[0:3], a[128:131], a[224:227], v[0:3]\n\t" \
  "v_accvgpr_read_b32 v80, a72\n\t" \
  "v_accvgpr_read_b32 v81, a73\n\t" \
  "v_mfma_f32_16x16x32_bf16 v[32:35], a[128:131], a[228:231], v[32:35]\n\t" \
  "buffer_load_dwordx4 a[212:215], v249, %[ws], s97 offen sc1\n\t" \
  "ds_read_b64_tr_b16 a[182:183], v197 offset:9216\n\t" \
  "v_mfma_f32_16x16x32_bf16 v[64:67], a[128:131], a[232:235], v[64:67]\n\t" \
  "ds_read_b64_tr_b16 a[184:185], v198 offset:8192\n\t" \
  "v_accvgpr_read_b32 v82, a74\n\t" \
  "v_mfma_f32_16x16x32_bf16 v[96:99], a[128:131], a[236:239], v[96:99]\n\t" \
  "v_accvgpr_read_b32 v83, a75\n\t" \
  "v_accvgpr_read_b32 v112, a76\n\t" \
  "v_mfma_f32_16x16x32_bf16 v[100:103], a[132:135], a[236:239], v[100:103]\n\t" \
  "ds_read_b64_tr_b16 a[186:187], v198 offset:9216\n\t" \
  "v_accvgpr_read_b32 v113, a77\n\t" \
  "v_mfma_f32_16x16x32_bf16 v[68:71], a[132:135], a[232:235], v[68:71]\n\t" \
  "buffer_load_dwordx4 a[216:219], v250, %[ws], s97 offen sc1\n\t" \
  "ds_read_b64_tr_b16 a[188:189], v199 offset:8192\n\t" \
  "v_mfma_f32_16x16x32_bf16 v[36:39], a[132:135], a[228:231], v[36:39]\n\t" \
  "v_accvgpr_read_b32 v114, a78\n\t" \
  "v_accvgpr_read_b32 v115, a79\n\t" \
  "v_mfma_f32_16x16x32_bf16 v[4:7], a[132:135], a[224:227], v[4:7]\n\t" \
  "ds_read_b64_tr_b16 a[190:191], v199 offset:9216\n\t" \
  "v_accvgpr_read_b32 v116, a92\n\t" \
  "v_accvgpr_read_b32 v117, a93\n\t" \
  "v_mfma_f32_16x16x32_bf16 v[8:11], a[136:139], a[224:227], v[8:11]\n\t" \
  "v_accvgpr_read_b32 v118, a94\n\t" \
  "v_accvgpr_read_b32 v119, a95\n\t" \
  "v_mfma_f32_16x16x32_bf16 v[40:43], a[136:139], a[228:231], v[40:43]\n\t" \
  "buffer_load_dwordx4 a[220:223], v251, %[ws], s97 offen sc1\n\t" \
  "v_accvgpr_read_b32 v84, a88\n\t" \
  "v_accvgpr_read_b32 v85, a89\n\t" \
  "v_mfma_f32_16x16x32_bf16 v[72:75], a[136:139], a[232:235], v[72:75]\n\t" \
  "v_accvgpr_read_b32 v86, a90\n\t" \
  "v_accvgpr_read_b32 v87, a91\n\t" \
  "v_mfma_f32_16x16x32_bf16 v[104:107], a[136:139], a[236:239], v[104:107]\n\t" \
  "v_accvgpr_read_b32 v52, a84\n\t" \
  "v_accvgpr_read_b32 v53, a85\n\t" \
  "v_mfma_f32_16x16x32_bf16 v[108:111], a[140:143], a[236:239], v[108:111]\n\t" \
  "v_accvgpr_read_b32 v54, a86\n\t" \
  "v_accvgpr_read_b32 v55, a87\n\t" \
  "v_mfma_f32_16x16x32_bf16 v[76:79], a[140:143], a[232:235], v[76:79]\n\t" \
  "v_accvgpr_read_b32 v20, a80\n\t" \
  "v_accvgpr_read_b32 v21, a81\n\t" \
  "v_mfma_f32_16x16x32_bf16 v[44:47], a[140:143], a[228:231], v[44:47]\n\t" \
  "v_accvgpr_read_b32 v22, a82\n\t" \
  "v_accvgpr_read_b32 v23, a83\n\t" \
  "v_mfma_f32_16x16x32_bf16 v[12:15], a[140:143], a[224:227], v[12:15]\n\t" \
  "s_waitcnt vmcnt(22) lgkmcnt(0)\n\t" \
  "s_barrier\n\t" \
  "v_mfma_f32_16x16x32_bf16 a[96:99], a[184:187], a[240:243], a[96:99]\n\t" \
  "s_add_u32 s97, %[rot19], 3670016\n\t" \
  "s_bfe_u32 s97, s97, 0x140003\n\t" \
  "v_mfma_f32_16x16x32_bf16 a[100:103], a[184:187], a[244:247], a[100:103]\n\t" \
  "ds_read_b64_tr_b16 a[128:129], v184 offset:16384\n\t" \
  "v_mfma_f32_16x16x32_bf16 a[104:107], a[184:187], a[248:251], a[104:107]\n\t" \
  "s_add_u32 s97, s97, %[sox]\n\t" \
  "ds_read_b64_tr_b16 a[130:131], v184 offset:17408\n\t" \
  "v_mfma_f32_16x16x32_bf16 a[108:111], a[184:187], a[252:255], a[108:111]\n\t" \
  "ds_read_b64_tr_b16 a[132:133], v185 offset:16384\n\t" \
  "v_mfma_f32_16x16x32_bf16 a[124:127], a[188:191], a[252:255], a[124:127]\n\t" \
  "v_mfma_f32_16x16x32_bf16 a[120:123], a[188:191], a[248:251], a[120:123]\n\t" \
  "buffer_load_dwordx4 a[224:227], v248, %[ws], s97 offen sc1\n\t" \
  "ds_read_b64_tr_b16 a[134:135], v185 offset:17408\n\t" \
  "v_mfma_f32_16x16x32_bf16 a[116:119], a[188:191], a[244:247], a[116:119]\n\t" \
  "ds_read_b64_tr_b16 a[136:137], v186 offset:16384\n\t" \
  "v_mfma_f32_16x16x32_bf16 a[112:115], a[188:191], a[240:243], a[112:115]\n\t" \
  "buffer_load_dwordx4 a[228:231], v249, %[ws], s97 offen sc1\n\t" \
  "v_mfma_f32_16x16x32_bf16 v[0:3], a[160:163], a[240:243], v[0:3]\n\t" \
  "ds_read_b64_tr_b16 a[138:139], v186 offset:17408\n\t" \
  "v_accvgpr_read_b32 v24, a96\n\t" \
  "v_mfma_f32_16x16x32_bf16 v[32:35], a[160:163], a[244:247], v[32:35]\n\t" \
  "ds_read_b64_tr_b16 a[140:141], v187 offset:16384\n\t" \
  "v_accvgpr_read_b32 v25, a97\n\t" \
  "v_mfma_f32_16x16x32_bf16 v[64:67], a[160:163], a[248:251], v[64:67]\n\t" \
  "buffer_load_dwordx4 a[232:235], v250, %[ws], s97 offen sc1\n\t" \
  "v_accvgpr_read_b32 v26, a98\n\t" \
  "v_mfma_f32_16x16x32_bf16 v[96:99], a[160:163], a[252:255], v[96:99]\n\t" \
  "ds_read_b64_tr_b16 a[142:143], v187 offset:17408\n\t" \
  "v_accvgpr_read_b32 v27, a99\n\t" \
  "v_mfma_f32_16x16x32_bf16 v[100:103], a[164:167], a[252:255], v[100:103]\n\t" \
  "buffer_load_dwordx4 a[236:239], v251, %[ws], s97 offen sc1\n\t" \
  "ds_read_b64_tr_b16 a[144:145], v188 offset:16384\n\t" \
  "v_mfma_f32_16x16x32_bf16 v[68:71], a[164:167], a[248:251], v[68:71]\n\t" \
  "v_accvgpr_read_b32 v56, a100\n\t" \
  "v_accvgpr_read_b32 v57, a101\n\t" \
  "v_mfma_f32_16x16x32_bf16 v[36:39], a[164:167], a[244:247], v[36:39]\n\t" \
  "ds_read_b64_tr_b16 a[146:147], v188 offset:17408\n\t" \
  "v_accvgpr_read_b32 v58, a102\n\t" \
  "v_mfma_f32_16x16x32_bf16 v[4:7], a[164:167], a[240:243], v[4:7]\n\t" \
  "s_add_u32 s96, %[rot19], 5242880\n\t" \
  "s_and_b32 s96, s96, 0x780000\n\t" \
  "v_mfma_f32_16x16x32_bf16 v[8:11], a[168:171], a[240:243], v[8:11]\n\t" \
  "ds_read_b64_tr_b16 a[148:149], v189 offset:16384\n\t" \
  "v_accvgpr_read_b32 v59, a103\n\t" \
  "v_mfma_f32_16x16x32_bf16 v[40:43], a[168:171], a[244:247], v[40:43]\n\t" \
  "s_add_u32 m0, %[mw], 98304\n\t" \
  "ds_read_b64_tr_b16 a[150:151], v189 offset:17408\n\t" \
  "v_mfma_f32_16x16x32_bf16 v[72:75], a[168:171], a[248:251], v[72:75]\n\t" \
  "ds_read_b64_tr_b16 a[152:153], v190 offset:16384\n\t" \
  "v_accvgpr_read_b32 v88, a104\n\t" \
  "v_mfma_f32_16x16x32_bf16 v[104:107], a[168:171], a[252:255], v[104:107]\n\t" \
  "v_accvgpr_read_b32 v89, a105\n\t" \
  "v_accvgpr_read_b32 v90, a106\n\t" \
  "v_mfma_f32_16x16x32_bf16 v[108:111], a[172:175], a[252:255], v[108:111]\n\t" \
  "buffer_load_dwordx4 v200, %[sw], s96 offen offset:0 lds\n\t" \
  "ds_read_b64_tr_b16 a[154:155], v190 offset:17408\n\t" \
  "v_accvgpr_read_b32 v91, a107\n\t" \
  "v_mfma_f32_16x16x32_bf16 v[76:79], a[172:175], a[248:251], v[76:79]\n\t" \
  "ds_read_b64_tr_b16 a[156:157], v191 offset:16384\n\t" \
  "v_accvgpr_read_b32 v120, a108\n\t" \
  "v_accvgpr_read_b32 v121, a109\n\t" \
  "v_mfma_f32_16x16x32_bf16 v[44:47], a[172:175], a[244:247], v[44:47]\n\t" \
  "buffer_load_dwordx4 v201, %[sw], s96 offen offset:1024 lds\n\t" \
  "v_accvgpr_read_b32 v122, a110\n\t" \
  "v_accvgpr_read_b32 v123, a111\n\t" \
  "v_mfma_f32_16x16x32_bf16 v[12:15], a[172:175], a[240:243], v[12:15]\n\t" \
  "ds_read_b64_tr_b16 a[158:159], v191 offset:17408\n\t" \
  "v_accvgpr_read_b32 v124, a124\n\t" \
  "v_accvgpr_read_b32 v125, a125\n\t" \
  "v_mfma_f32_16x16x32_bf16 v[16:19], a[176:179], a[240:243], v[16:19]\n\t" \
  "v_accvgpr_read_b32 v126, a126\n\t" \
  "v_accvgpr_read_b32 v127, a127\n\t" \
  "v_mfma_f32_16x16x32_bf16 v[48:51], a[176:179], a[244:247], v[48:51]\n\t" \
  "buffer_load_dwordx4 v202, %[sw], s96 offen offset:2048 lds\n\t" \
  "v_accvgpr_read_b32 v92, a120\n\t" \
  "v_accvgpr_read_b32 v93, a121\n\t" \
  "v_mfma_f32_16x16x32_bf16 v[80:83], a[176:179], a[248:251], v[80:83]\n\t" \
  "v_accvgpr_read_b32 v94, a122\n\t" \
  "v_accvgpr_read_b32 v95, a123\n\t" \
  "v_mfma_f32_16x16x32_bf16 v[112:115], a[176:179], a[252:255], v[112:115]\n\t" \
  "buffer_load_dwordx4 v203, %[sw], s96 offen offset:3072 lds\n\t" \
  "v_accvgpr_read_b32 v60, a116\n\t" \
  "v_accvgpr_read_b32 v61, a117\n\t" \
  "v_mfma_f32_16x16x32_bf16 v[116:119], a[180:183], a[252:255], v[116:119]\n\t" \
  "v_accvgpr_read_b32 v62, a118\n\t" \
  "v_accvgpr_read_b32 v63, a119\n\t" \
  "v_mfma_f32_16x16x32_bf16 v[84:87], a[180:183], a[248:251], v[84:87]\n\t" \
  "v_accvgpr_read_b32 v28, a112\n\t" \
  "v_accvgpr_read_b32 v29, a113\n\t" \
  "v_mfma_f32_16x16x32_bf16 v[52:55], a[180:183], a[244:247], v[52:55]\n\t" \
  "v_accvgpr_read_b32 v30, a114\n\t" \
  "v_accvgpr_read_b32 v31, a115\n\t" \
  "v_mfma_f32_16x16x32_bf16 v[20:23], a[180:183], a[240:243], v[20:23]\n\t" \
  "s_waitcnt vmcnt(17) lgkmcnt(0)\n\t" \
  "v_mfma_f32_16x16x32_bf16 v[0:3], a[128:131], a[192:195], v[0:3]\n\t" \
  "s_add_u32 s97, s97, %[dk]\n\t" \
  "ds_read_b64_tr_b16 a[160:161], v184 offset:24576\n\t" \
  "v_mfma_f32_16x16x32_bf16 v[32:35], a[128:131], a[196:199], v[32:35]\n\t" \
  "v_mfma_f32_16x16x32_bf16 v[64:67], a[128:131], a[200:203], v[64:67]\n\t" \
  "buffer_load_dwordx4 a[240:243], v248, %[ws], s97 offen sc1\n\t" \
  "ds_read_b64_tr_b16 a[162:163], v184 offset:25600\n\t" \
  "v_mfma_f32_16x16x32_bf16 v[96:99], a[128:131], a[204:207], v[96:99]\n\t" \
  "ds_read_b64_tr_b16 a[164:165], v185 offset:24576\n\t" \
  "v_mfma_f32_16x16x32_bf16 v[100:103], a[132:135], a[204:207], v[100:103]\n\t" \
  "v_mfma_f32_16x16x32_bf16 v[68:71], a[132:135], a[200:203], v[68:71]\n\t" \
  "buffer_load_dwordx4 a[244:247], v249, %[ws], s97 offen sc1\n\t" \
  "ds_read_b64_tr_b16 a[166:167], v185 offset:25600\n\t" \
  "v_mfma_f32_16x16x32_bf16 v[36:39], a[132:135], a[196:199], v[36:39]\n\t" \
  "ds_read_b64_tr_b16 a[168:169], v186 offset:24576\n\t" \
  "v_mfma_f32_16x16x32_bf16 v[4:7], a[132:135], a[192:195], v[4:7]\n\t" \
  "v_mfma_f32_16x16x32_bf16 v[8:11], a[136:139], a[192:195], v[8:11]\n\t" \
  "buffer_load_dwordx4 a[248:251], v250, %[ws], s97 offen sc1\n\t" \
  "ds_read_b64_tr_b16 a[170:171], v186 offset:25600\n\t" \
  "v_mfma_f32_16x16x32_bf16 v[40:43], a[136:139], a[196:199], v[40:43]\n\t" \
  "ds_read_b64_tr_b16 a[172:173], v187 offset:24576\n\t" \
  "v_mfma_f32_16x16x32_bf16 v[72:75], a[136:139], a[200:203], v[72:75]\n\t" \
  "buffer_load_dwordx4 a[252:255], v251, %[ws], s97 offen sc1\n\t" \
  "v_mfma_f32_16x16x32_bf16 v[104:107], a[136:139], a[204:207], v[104:107]\n\t" \
  "ds_read_b64_tr_b16 a[174:175], v187 offset:25600\n\t" \
  "v_mfma_f32_16x16x32_bf16 v[108:111], a[140:143], a[204:207], v[108:111]\n\t" \
  "ds_read_b64_tr_b16 a[176:177], v188 offset:24576\n\t" \
  "v_mfma_f32_16x16x32_bf16 v[76:79], a[140:143], a[200:203], v[76:79]\n\t" \
  "s_add_u32 s97, %[rot19], 4194304\n\t" \
  "s_bfe_u32 s97, s97, 0x140003\n\t" \
  "v_mfma_f32_16x16x32_bf16 v[44:47], a[140:143], a[196:199], v[44:47]\n\t" \
  "ds_read_b64_tr_b16 a[178:179], v188 offset:25600\n\t" \
  "v_mfma_f32_16x16x32_bf16 v[12:15], a[140:143], a[192:195], v[12:15]\n\t" \
  "ds_read_b64_tr_b16 a[180:181], v189 offset:24576\n\t" \
  "v_mfma_f32_16x16x32_bf16 v[16:19], a[144:147], a[192:195], v[16:19]\n\t" \
  "s_add_u32 s97, s97, %[sox]\n\t" \
  "v_mfma_f32_16x16x32_bf16 v[48:51], a[144:147], a[196:199], v[48:51]\n\t" \
  "ds_read_b64_tr_b16 a[182:183], v189 offset:25600\n\t" \
  "v_mfma_f32_16x16x32_bf16 v[80:83], a[144:147], a[200:203], v[80:83]\n\t" \
  "ds_read_b64_tr_b16 a[184:185], v190 offset:24576\n\t" \
  "v_mfma_f32_16x16x32_bf16 v[112:115], a[144:147], a[204:207], v[112:115]\n\t" \
  "buffer_load_dwordx4 a[32:35], v248, %[ws], s97 offen sc1\n\t" \
  "v_mfma_f32_16x16x32_bf16 v[116:119], a[148:151], a[204:207], v[116:119]\n\t" \
  "ds_read_b64_tr_b16 a[186:187], v190 offset:25600\n\t" \
  "v_mfma_f32_16x16x32_bf16 v[84:87], a[148:151], a[200:203], v[84:87]\n\t" \
  "buffer_load_dwordx4 a[36:39], v249, %[ws], s97 offen sc1\n\t" \
  "ds_read_b64_tr_b16 a[188:189], v191 offset:24576\n\t" \
  "v_mfma_f32_16x16x32_bf16 v[52:55], a[148:151], a[196:199], v[52:55]\n\t" \
  "v_mfma_f32_16x16x32_bf16 v[20:23], a[148:151], a[192:195], v[20:23]\n\t" \
  "ds_read_b64_tr_b16 a[190:191], v191 offset:25600\n\t" \
  "v_mfma_f32_16x16x32_bf16 v[24:27], a[152:155], a[192:195], v[24:27]\n\t" \
  "buffer_load_dwordx4 a[40:43], v250, %[ws], s97 offen sc1\n\t" \
  "v_mfma_f32_16x16x32_bf16 v[56:59], a[152:155], a[196:199], v[56:59]\n\t" \
  "v_mfma_f32_16x16x32_bf16 v[88:91], a[152:155], a[200:203], v[88:91]\n\t" \
  "v_mfma_f32_16x16x32_bf16 v[120:123], a[152:155], a[204:207], v[120:123]\n\t" \
  "buffer_load_dwordx4 a[44:47], v251, %[ws], s97 offen sc1\n\t" \
  "v_mfma_f32_16x16x32_bf16 v[124:127], a[156:159], a[204:207], v[124:127]\n\t" \
  "v_mfma_f32_16x16x32_bf16 v[92:95], a[156:159], a[200:203], v[92:95]\n\t" \
  "v_mfma_f32_16x16x32_bf16 v[60:63], a[156:159], a[196:199], v[60:63]\n\t" \
  "v_mfma_f32_16x16x32_bf16 v[28:31], a[156:159], a[192:195], v[28:31]\n\t" \
  "s_waitcnt vmcnt(16) lgkmcnt(0)\n\t" \
  "s_barrier\n\t" \
  "v_mfma_f32_16x16x32_bf16 v[0:3], a[160:163], a[208:211], v[0:3]\n\t" \
  "s_add_u32 s97, s97, %[dk]\n\t" \
  "ds_read_b64_tr_b16 a[128:129], v184 offset:32768\n\t" \
  "v_mfma_f32_16x16x32_bf16 v[32:35], a[160:163], a[212:215], v[32:35]\n\t" \
  "ds_read_b64_tr_b16 a[130:131], v184 offset:33792\n\t" \
  "v_mbcnt_lo_u32_b32 v233, -1, 0\n\t" \
  "v_mfma_f32_16x16x32_bf16 v[64:67], a[160:163], a[216:219], v[64:67]\n\t" \
  "buffer_load_dwordx4 a[48:51], v248, %[ws], s97 offen sc1\n\t" \
  "ds_read_b64_tr_b16 a[132:133], v185 offset:32768\n\t" \
  "v_mfma_f32_16x16x32_bf16 v[96:99], a[160:163], a[220:223], v[96:99]\n\t" \
  "ds_read_b64_tr_b16 a[134:135], v185 offset:33792\n\t" \
  "ds_read_b64_tr_b16 a[136:137], v186 offset:32768\n\t" \
  "v_mfma_f32_16x16x32_bf16 v[100:103], a[164:167], a[220:223], v[100:103]\n\t" \
  "ds_read_b64_tr_b16 a[138:139], v186 offset:33792\n\t" \
  "v_mbcnt_hi_u32_b32 v233, -1, v233\n\t" \
  "v_mfma_f32_16x16x32_bf16 v[68:71], a[164:167], a[216:219], v[68:71]\n\t" \
  "buffer_load_dwordx4 a[52:55], v249, %[ws], s97 offen sc1\n\t" \
  "ds_read_b64_tr_b16 a[140:141], v187 offset:32768\n\t" \
  "v_mfma_f32_16x16x32_bf16 v[36:39], a[164:167], a[212:215], v[36:39]\n\t" \
  "ds_read_b64_tr_b16 a[142:143], v187 offset:33792\n\t" \
  "ds_read_b64_tr_b16 a[144:145], v188 offset:32768\n\t" \
  "v_mfma_f32_16x16x32_bf16 v[4:7], a[164:167], a[208:211], v[4:7]\n\t" \
  "buffer_load_dwordx4 a[56:59], v250, %[ws], s97 offen sc1\n\t" \
  "ds_read_b64_tr_b16 a[146:147], v188 offset:33792\n\t" \
  "v_mfma_f32_16x16x32_bf16 v[8:11], a[168:171], a[208:211], v[8:11]\n\t" \
  "ds_read_b64_tr_b16 a[148:149], v189 offset:32768\n\t" \
  "s_bfe_u32 %[hl], %[bid], 0x30003\n\t" \
  "v_mfma_f32_16x16x32_bf16 v[40:43], a[168:171], a[212:215], v[40:43]\n\t" \
  "ds_read_b64_tr_b16 a[150:151], v189 offset:33792\n\t" \
  "ds_read_b64_tr_b16 a[152:153], v190 offset:32768\n\t" \
  "v_mfma_f32_16x16x32_bf16 v[72:75], a[168:171], a[216:219], v[72:75]\n\t" \
  "buffer_load_dwordx4 a[60:63], v251, %[ws], s97 offen sc1\n\t" \
  "ds_read_b64_tr_b16 a[154:155], v190 offset:33792\n\t" \
  "v_mfma_f32_16x16x32_bf16 v[104:107], a[168:171], a[220:223], v[104:107]\n\t" \
  "ds_read_b64_tr_b16 a[156:157], v191 offset:32768\n\t" \
  "s_lshl_b32 %[hl18], %[hl], 18\n\t" \
  "v_mfma_f32_16x16x32_bf16 v[108:111], a[172:175], a[220:223], v[108:111]\n\t" \
  "s_add_u32 s97, %[rot19], 4718592\n\t" \
  "s_bfe_u32 s97, s97, 0x140003\n\t" \
  "v_mfma_f32_16x16x32_bf16 v[76:79], a[172:175], a[216:219], v[76:79]\n\t" \
  "ds_read_b64_tr_b16 a[158:159], v191 offset:33792\n\t" \
  "ds_read_b64_tr_b16 a[64:65], v184 offset:40960\n\t" \
  "v_mfma_f32_16x16x32_bf16 v[44:47], a[172:175], a[212:215], v[44:47]\n\t" \
  "ds_read_b64_tr_b16 a[66:67], v184 offset:41984\n\t" \
  "ds_read_b64_tr_b16 a[68:69], v185 offset:40960\n\t" \
  "v_mfma_f32_16x16x32_bf16 v[12:15], a[172:175], a[208:211], v[12:15]\n\t" \
  "s_add_u32 s97, s97, %[sox]\n\t" \
  "ds_read_b64_tr_b16 a[70:71], v185 offset:41984\n\t" \
  "v_mfma_f32_16x16x32_bf16 v[16:19], a[176:179], a[208:211], v[16:19]\n\t" \
  "ds_read_b64_tr_b16 a[72:73], v186 offset:40960\n\t" \
  "ds_read_b64_tr_b16 a[74:75], v186 offset:41984\n\t" \
  "v_mfma_f32_16x16x32_bf16 v[48:51], a[176:179], a[212:215], v[48:51]\n\t" \
  "buffer_load_dwordx4 a[96:99], v248, %[ws], s97 offen sc1\n\t" \
  "ds_read_b64_tr_b16 a[76:77], v187 offset:40960\n\t" \
  "v_mfma_f32_16x16x32_bf16 v[80:83], a[176:179], a[216:219], v[80:83]\n\t" \
  "ds_read_b64_tr_b16 a[78:79], v187 offset:41984\n\t" \
  "ds_read_b64_tr_b16 a[80:81], v188 offset:40960\n\t" \
  "v_mfma_f32_16x16x32_bf16 v[112:115], a[176:179], a[220:223], v[112:115]\n\t" \
  "ds_read_b64_tr_b16 a[82:83], v188 offset:41984\n\t" \
  "v_lshrrev_b32 v129, 4, v233\n\t" \
  "v_mfma_f32_16x16x32_bf16 v[116:119], a[180:183], a[220:223], v[116:119]\n\t" \
  "buffer_load_dwordx4 a[100:103], v249, %[ws], s97 offen sc1\n\t" \
  "ds_read_b64_tr_b16 a[84:85], v189 offset:40960\n\t" \
  "v_mfma_f32_16x16x32_bf16 v[84:87], a[180:183], a[216:219], v[84:87]\n\t" \
  "ds_read_b64_tr_b16 a[86:87], v189 offset:41984\n\t" \
  "ds_read_b64_tr_b16 a[88:89], v190 offset:40960\n\t" \
  "v_mfma_f32_16x16x32_bf16 v[52:55], a[180:183], a[212:215], v[52:55]\n\t" \
  "buffer_load_dwordx4 a[104:107], v250, %[ws], s97 offen sc1\n\t" \
  "ds_read_b64_tr_b16 a[90:91], v190 offset:41984\n\t" \
  "v_mfma_f32_16x16x32_bf16 v[20:23], a[180:183], a[208:211], v[20:23]\n\t" \
  "ds_read_b64_tr_b16 a[92:93], v191 offset:40960\n\t" \
  "s_lshr_b32 s98, %[bid], 3\n\t" \
  "v_mfma_f32_16x16x32_bf16 v[24:27], a[184:187], a[208:211], v[24:27]\n\t" \
  "ds_read_b64_tr_b16 a[94:95], v191 offset:41984\n\t" \
  "s_lshl_b32 %[cto], %[wid], 5\n\t" \
  "v_mfma_f32_16x16x32_bf16 v[56:59], a[184:187], a[212:215], v[56:59]\n\t" \
  "buffer_load_dwordx4 a[108:111], v251, %[ws], s97 offen sc1\n\t" \
  "v_and_b32 v130, 15, v233\n\t" \
  "v_mfma_f32_16x16x32_bf16 v[88:91], a[184:187], a[216:219], v[88:91]\n\t" \
  "s_add_u32 %[cto], %[cto], s98\n\t" \
  "v_mfma_f32_16x16x32_bf16 v[120:123], a[184:187], a[220:223], v[120:123]\n\t" \
  "buffer_load_dword %[pf], v217, %[ws], %[pto] offen sc1\n\t" \
  "s_lshl_b32 %[cto], %[cto], 7\n\t" \
  "v_mfma_f32_16x16x32_bf16 v[124:127], a[188:191], a[220:223], v[124:127]\n\t" \
  "v_bfe_u32 v131, v233, 2, 2\n\t" \
  "v_mfma_f32_16x16x32_bf16 v[92:95], a[188:191], a[216:219], v[92:95]\n\t" \
  "s_lshl2_add_u32 %[pf2], s98, %[wid]\n\t" \
  "v_mfma_f32_16x16x32_bf16 v[60:63], a[188:191], a[212:215], v[60:63]\n\t" \
  "v_mfma_f32_16x16x32_bf16 v[28:31], a[188:191], a[208:211], v[28:31]\n\t" \
  "s_waitcnt vmcnt(13) lgkmcnt(0)\n\t" \
  "s_barrier\n\t" \
  "v_mfma_f32_16x16x32_bf16 v[0:3], a[128:131], a[224:227], v[0:3]\n\t" \
  "s_add_u32 s97, s97, %[dk]\n\t" \
  "ds_read_b64_tr_b16 a[160:161], v184 offset:0\n\t" \
  "v_mfma_f32_16x16x32_bf16 v[0:3], a[64:67], a[240:243], v[0:3]\n\t" \
  "v_and_b32 v132, 3, v233\n\t" \
  "v_mfma_f32_16x16x32_bf16 v[32:35], a[64:67], a[244:247], v[32:35]\n\t" \
  "buffer_load_dwordx4 a[112:115], v248, %[ws], s97 offen sc1\n\t" \
  "ds_read_b64_tr_b16 a[162:163], v184 offset:1024\n\t" \
  "v_mfma_f32_16x16x32_bf16 v[32:35], a[128:131], a[228:231], v[32:35]\n\t" \
  "ds_read_b64_tr_b16 a[164:165], v185 offset:0\n\t" \
  "s_mul_i32 %[pf2], %[pf2], 98304\n\t" \
  "v_mfma_f32_16x16x32_bf16 v[64:67], a[128:131], a[232:235], v[64:67]\n\t" \
  "v_cmp_lt_u32 vcc, 6, v233\n\t" \
  "v_mfma_f32_16x16x32_bf16 v[64:67], a[64:67], a[248:251], v[64:67]\n\t" \
  "buffer_load_dwordx4 a[116:119], v249, %[ws], s97 offen sc1\n\t" \
  "ds_read_b64_tr_b16 a[166:167], v185 offset:1024\n\t" \
  "v_mfma_f32_16x16x32_bf16 v[96:99], a[64:67], a[252:255], v[96:99]\n\t" \
  "ds_read_b64_tr_b16 a[168:169], v186 offset:0\n\t" \
  "s_lshr_b32 s98, %[bid], 2\n\t" \
  "v_mfma_f32_16x16x32_bf16 v[96:99], a[128:131], a[236:239], v[96:99]\n\t" \
  "v_mov_b32 v133, 6272\n\t" \
  "v_mfma_f32_16x16x32_bf16 v[4:7], a[132:135], a[224:227], v[4:7]\n\t" \
  "buffer_load_dwordx4 a[120:123], v250, %[ws], s97 offen sc1\n\t" \
  "ds_read_b64_tr_b16 a[170:171], v186 offset:1024\n\t" \
  "v_mfma_f32_16x16x32_bf16 v[4:7], a[68:71], a[240:243], v[4:7]\n\t" \
  "ds_read_b64_tr_b16 a[172:173], v187 offset:0\n\t" \
  "s_xor_b32 s98, s98, %[wid]\n\t" \
  "v_mfma_f32_16x16x32_bf16 v[36:39], a[68:71], a[244:247], v[36:39]\n\t" \
  "v_cndmask_b32 v133, 0, v133, vcc\n\t" \
  "v_mfma_f32_16x16x32_bf16 v[36:39], a[132:135], a[228:231], v[36:39]\n\t" \
  "buffer_load_dwordx4 a[124:127], v251, %[ws], s97 offen sc1\n\t" \
  "ds_read_b64_tr_b16 a[174:175], v187 offset:1024\n\t" \
  "v_mfma_f32_16x16x32_bf16 v[68:71], a[132:135], a[232:235], v[68:71]\n\t" \
  "ds_read_b64_tr_b16 a[176:177], v188 offset:0\n\t" \
  "s_lshl_b32 s98, s98, 13\n\t" \
  "v_mfma_f32_16x16x32_bf16 v[68:71], a[68:71], a[248:251], v[68:71]\n\t" \
  "v_lshl_add_u32 v133, v233, 7, v133\n\t" \
  "v_mfma_f32_16x16x32_bf16 v[100:103], a[68:71], a[252:255], v[100:103]\n\t" \
  "s_add_u32 s96, %[rot19], 5767168\n\t" \
  "s_and_b32 s96, s96, 0x780000\n\t" \
  "v_mfma_f32_16x16x32_bf16 v[100:103], a[132:135], a[236:239], v[100:103]\n\t" \
  "ds_read_b64_tr_b16 a[178:179], v188 offset:1024\n\t" \
  "s_and_b32 %[po], s98, 0x2000\n\t" \
  "v_mfma_f32_16x16x32_bf16 v[8:11], a[136:139], a[224:227], v[8:11]\n\t" \
  "s_add_u32 m0, %[mw], 16384\n\t" \
  "ds_read_b64_tr_b16 a[180:181], v189 offset:0\n\t" \
  "v_mfma_f32_16x16x32_bf16 v[8:11], a[72:75], a[240:243], v[8:11]\n\t" \
  "ds_read_b64_tr_b16 a[182:183], v189 offset:1024\n\t" \
  "s_lshr_b32 s98, %[bid], 6\n\t" \
  "v_mfma_f32_16x16x32_bf16 v[40:43], a[72:75], a[244:247], v[40:43]\n\t" \
  "ds_read_b64_tr_b16 a[184:185], v190 offset:0\n\t" \
  "s_lshl_b32 s98, s98, 22\n\t" \
  "v_mfma_f32_16x16x32_bf16 v[40:43], a[136:139], a[228:231], v[40:43]\n\t" \
  "buffer_load_dwordx4 v200, %[sw], s96 offen offset:0 lds\n\t" \
  "v_cmp_gt_u32 vcc, 14, v233\n\t" \
  "v_mfma_f32_16x16x32_bf16 v[72:75], a[136:139], a[232:235], v[72:75]\n\t" \
  "ds_read_b64_tr_b16 a[186:187], v190 offset:1024\n\t" \
  "s_add_u32 %[po], %[po], s98\n\t" \
  "v_mfma_f32_16x16x32_bf16 v[72:75], a[72:75], a[248:251], v[72:75]\n\t" \
  "ds_read_b64_tr_b16 a[188:189], v191 offset:0\n\t" \
  "s_lshl_b32 %[xcb], %[wid], 15\n\t" \
  "v_mfma_f32_16x16x32_bf16 v[104:107], a[72:75], a[252:255], v[104:107]\n\t" \
  "buffer_load_dwordx4 v201, %[sw], s96 offen offset:1024 lds\n\t" \
  "v_bfrev_b32 v247, 1\n\t" \
  "v_mfma_f32_16x16x32_bf16 v[104:107], a[136:139], a[236:239], v[104:107]\n\t" \
  "ds_read_b64_tr_b16 a[190:191], v191 offset:1024\n\t" \
  "s_add_u32 %[xcb], %[xcb], 0x1800000\n\t" \
  "v_mfma_f32_16x16x32_bf16 v[12:15], a[140:143], a[224:227], v[12:15]\n\t" \
  "ds_read_b64_tr_b16 a[0:1], v184 offset:8192\n\t" \
  "s_add_u32 %[xcb], %[xcb], %[po]\n\t" \
  "v_mfma_f32_16x16x32_bf16 v[12:15], a[76:79], a[240:243], v[12:15]\n\t" \
  "buffer_load_dwordx4 v202, %[sw], s96 offen offset:2048 lds\n\t" \
  "v_cndmask_b32 v245, v247, v133, vcc\n\t" \
  "v_mfma_f32_16x16x32_bf16 v[44:47], a[76:79], a[244:247], v[44:47]\n\t" \
  "ds_read_b64_tr_b16 a[2:3], v184 offset:9216\n\t" \
  "s_and_b32 s98, %[bid], 0x38\n\t" \
  "v_mfma_f32_16x16x32_bf16 v[44:47], a[140:143], a[228:231], v[44:47]\n\t" \
  "ds_read_b64_tr_b16 a[4:5], v185 offset:8192\n\t" \
  "s_lshl_b32 s98, s98, 16\n\t" \
  "v_mfma_f32_16x16x32_bf16 v[76:79], a[140:143], a[232:235], v[76:79]\n\t" \
  "buffer_load_dwordx4 v203, %[sw], s96 offen offset:3072 lds\n\t" \
  "v_writelane_b32 v247, %[prb], 0\n\t" \
  "v_mfma_f32_16x16x32_bf16 v[76:79], a[76:79], a[248:251], v[76:79]\n\t" \
  "ds_read_b64_tr_b16 a[6:7], v185 offset:9216\n\t" \
  "s_add_u32 %[xcb], %[xcb], s98\n\t" \
  "v_mfma_f32_16x16x32_bf16 v[108:111], a[76:79], a[252:255], v[108:111]\n\t" \
  "ds_read_b64_tr_b16 a[8:9], v186 offset:8192\n\t" \
  "s_add_u32 %[otb], %[po], 0x800000\n\t" \
  "v_mfma_f32_16x16x32_bf16 v[108:111], a[140:143], a[236:239], v[108:111]\n\t" \
  "s_add_u32 s97, %[rot19], 5242880\n\t" \
  "s_bfe_u32 s97, s97, 0x140003\n\t" \
  "v_mfma_f32_16x16x32_bf16 v[16:19], a[144:147], a[224:227], v[16:19]\n\t" \
  "ds_read_b64_tr_b16 a[10:11], v186 offset:9216\n\t" \
  "s_and_b32 s98, %[bid], 0x3\n\t" \
  "v_mfma_f32_16x16x32_bf16 v[16:19], a[80:83], a[240:243], v[16:19]\n\t" \
  "s_add_u32 s97, s97, %[sox]\n\t" \
  "ds_read_b64_tr_b16 a[12:13], v187 offset:8192\n\t" \
  "v_mfma_f32_16x16x32_bf16 v[48:51], a[80:83], a[244:247], v[48:51]\n\t" \
  "v_lshrrev_b32 v133, 1, v129\n\t" \
  "v_mfma_f32_16x16x32_bf16 v[48:51], a[144:147], a[228:231], v[48:51]\n\t" \
  "ds_read_b64_tr_b16 a[14:15], v187 offset:9216\n\t" \
  "s_lshl_b32 s98, s98, 20\n\t" \
  "v_mfma_f32_16x16x32_bf16 v[80:83], a[144:147], a[232:235], v[80:83]\n\t" \
  "buffer_load_dwordx4 a[192:195], v248, %[ws], s97 offen sc1\n\t" \
  "ds_read_b64_tr_b16 a[16:17], v188 offset:8192\n\t" \
  "v_mfma_f32_16x16x32_bf16 v[80:83], a[80:83], a[248:251], v[80:83]\n\t" \
  "v_and_b32 v134, 1, v129\n\t" \
  "v_mfma_f32_16x16x32_bf16 v[112:115], a[80:83], a[252:255], v[112:115]\n\t" \
  "ds_read_b64_tr_b16 a[18:19], v188 offset:9216\n\t" \
  "s_add_u32 %[otb], %[otb], s98\n\t" \
  "v_mfma_f32_16x16x32_bf16 v[112:115], a[144:147], a[236:239], v[112:115]\n\t" \
  "buffer_load_dwordx4 a[196:199], v249, %[ws], s97 offen sc1\n\t" \
  "ds_read_b64_tr_b16 a[20:21], v189 offset:8192\n\t" \
  "v_mfma_f32_16x16x32_bf16 v[20:23], a[148:151], a[224:227], v[20:23]\n\t" \
  "v_lshl_or_b32 v133, v134, 1, v133\n\t" \
  "v_mfma_f32_16x16x32_bf16 v[20:23], a[84:87], a[240:243], v[20:23]\n\t" \
  "ds_read_b64_tr_b16 a[22:23], v189 offset:9216\n\t" \
  "s_and_b32 s98, %[wid], 2\n\t" \
  "v_mfma_f32_16x16x32_bf16 v[52:55], a[84:87], a[244:247], v[52:55]\n\t" \
  "buffer_load_dwordx4 a[200:203], v250, %[ws], s97 offen sc1\n\t" \
  "ds_read_b64_tr_b16 a[24:25], v190 offset:8192\n\t" \
  "v_mfma_f32_16x16x32_bf16 v[52:55], a[148:151], a[228:231], v[52:55]\n\t" \
  "v_xor_b32 v133, v133, v130\n\t" \
  "v_mfma_f32_16x16x32_bf16 v[84:87], a[148:151], a[232:235], v[84:87]\n\t" \
  "ds_read_b64_tr_b16 a[26:27], v190 offset:9216\n\t" \
  "s_lshl_b32 s98, s98, 13\n\t" \
  "v_mfma_f32_16x16x32_bf16 v[84:87], a[84:87], a[248:251], v[84:87]\n\t" \
  "buffer_load_dwordx4 a[204:207], v251, %[ws], s97 offen sc1\n\t" \
  "ds_read_b64_tr_b16 a[28:29], v191 offset:8192\n\t" \
  "v_mfma_f32_16x16x32_bf16 v[116:119], a[84:87], a[252:255], v[116:119]\n\t" \
  "v_lshlrev_b32 v133, 4, v133\n\t" \
  "v_mfma_f32_16x16x32_bf16 v[116:119], a[148:151], a[236:239], v[116:119]\n\t" \
  "s_add_u32 s97, s97, %[dk]\n\t" \
  "ds_read_b64_tr_b16 a[30:31], v191 offset:9216\n\t" \
  "v_mfma_f32_16x16x32_bf16 v[24:27], a[152:155], a[224:227], v[24:27]\n\t" \
  "s_add_u32 %[otb], %[otb], s98\n\t" \
  "v_mfma_f32_16x16x32_bf16 v[24:27], a[88:91], a[240:243], v[24:27]\n\t" \
  "v_lshl_add_u32 v133, v130, 8, v133\n\t" \
  "v_mfma_f32_16x16x32_bf16 v[56:59], a[88:91], a[244:247], v[56:59]\n\t" \
  "buffer_load_dwordx4 a[208:211], v248, %[ws], s97 offen sc1\n\t" \
  "s_and_b32 s98, %[wid], 1\n\t" \
  "v_mfma_f32_16x16x32_bf16 v[56:59], a[152:155], a[228:231], v[56:59]\n\t" \
  "s_lshl_b32 s98, s98, 12\n\t" \
  "v_mfma_f32_16x16x32_bf16 v[88:91], a[152:155], a[232:235], v[88:91]\n\t" \
  "v_add_u32 v220, 49152, v133\n\t" \
  "v_mfma_f32_16x16x32_bf16 v[88:91], a[88:91], a[248:251], v[88:91]\n\t" \
  "buffer_load_dwordx4 a[212:215], v249, %[ws], s97 offen sc1\n\t" \
  "s_add_u32 %[otb], %[otb], s98\n\t" \
  "v_mfma_f32_16x16x32_bf16 v[120:123], a[88:91], a[252:255], v[120:123]\n\t" \
  "s_lshr_b32 %[eso], %[c17], 2\n\t" \
  "v_mfma_f32_16x16x32_bf16 v[120:123], a[152:155], a[236:239], v[120:123]\n\t" \
  "v_xor_b32 v221, 64, v220\n\t" \
  "v_mfma_f32_16x16x32_bf16 v[28:31], a[156:159], a[224:227], v[28:31]\n\t" \
  "buffer_load_dwordx4 a[216:219], v250, %[ws], s97 offen sc1\n\t" \
  "s_add_u32 %[eso], %[eso], %[otb]\n\t" \
  "v_mfma_f32_16x16x32_bf16 v[28:31], a[92:95], a[240:243], v[28:31]\n\t" \
  "s_and_b32 s98, %[bid], 0x38\n\t" \
  "v_mfma_f32_16x16x32_bf16 v[60:63], a[92:95], a[244:247], v[60:63]\n\t" \
  "v_xor_b32 v222, 128, v220\n\t" \
  "v_mfma_f32_16x16x32_bf16 v[60:63], a[156:159], a[228:231], v[60:63]\n\t" \
  "buffer_load_dwordx4 a[220:223], v251, %[ws], s97 offen sc1\n\t" \
  "s_lshl_b32 s98, s98, 14\n\t" \
  "v_mfma_f32_16x16x32_bf16 v[92:95], a[156:159], a[232:235], v[92:95]\n\t" \
  "s_add_u32 %[eso], %[eso], s98\n\t" \
  "v_mfma_f32_16x16x32_bf16 v[92:95], a[92:95], a[248:251], v[92:95]\n\t" \
  "v_xor_b32 v223, 192, v220\n\t" \
  "v_mfma_f32_16x16x32_bf16 v[124:127], a[92:95], a[252:255], v[124:127]\n\t" \
  "v_mfma_f32_16x16x32_bf16 v[124:127], a[156:159], a[236:239], v[124:127]\n\t" \
  "s_waitcnt vmcnt(21) lgkmcnt(0)\n\t" \
  "s_barrier\n\t" \
  "v_mfma_f32_16x16x32_bf16 v[0:3], a[160:163], a[32:35], v[0:3]\n\t" \
  "s_add_u32 s96, %[rot19], 6291456\n\t" \
  "s_and_b32 s96, s96, 0x780000\n\t" \
  "v_mfma_f32_16x16x32_bf16 v[0:3], a[0:3], a[48:51], v[0:3]\n\t" \
  "ds_read_b64_tr_b16 a[128:129], v184 offset:49152\n\t" \
  "v_xor_b32 v133, v130, v129\n\t" \
  "v_mfma_f32_16x16x32_bf16 v[32:35], a[0:3], a[52:55], v[32:35]\n\t" \
  "ds_read_b64_tr_b16 a[130:131], v184 offset:50176\n\t" \
  "s_lshr_b32 s98, %[bid], 1\n\t" \
  "v_mfma_f32_16x16x32_bf16 v[32:35], a[160:163], a[36:39], v[32:35]\n\t" \
  "s_add_u32 m0, %[mw], 32768\n\t" \
  "ds_read_b64_tr_b16 a[132:133], v185 offset:49152\n\t" \
  "v_mfma_f32_16x16x32_bf16 v[64:67], a[160:163], a[40:43], v[64:67]\n\t" \
  "v_lshlrev_b32 v133, 4, v133\n\t" \
  "v_mfma_f32_16x16x32_bf16 v[64:67], a[0:3], a[56:59], v[64:67]\n\t" \
  "ds_read_b64_tr_b16 a[134:135], v185 offset:50176\n\t" \
  "s_xor_b32 s98, s98, %[wid]\n\t" \
  "v_mfma_f32_16x16x32_bf16 v[96:99], a[0:3], a[60:63], v[96:99]\n\t" \
  "buffer_load_dwordx4 v200, %[sw], s96 offen offset:0 lds\n\t" \
  "ds_read_b64_tr_b16 a[136:137], v186 offset:49152\n\t" \
  "v_mfma_f32_16x16x32_bf16 v[96:99], a[160:163], a[44:47], v[96:99]\n\t" \
  ".Lhs3:\n\ts_cbranch_execz .Lhs3n\n\t" \
  "v_lshl_add_u32 v224, v129, 8, v133\n\t" \
  "v_mfma_f32_16x16x32_bf16 v[4:7], a[164:167], a[32:35], v[4:7]\n\t" \
  "ds_read_b64_tr_b16 a[138:139], v186 offset:50176\n\t" \
  "s_lshl_b32 s98, s98, 13\n\t" \
  "v_mfma_f32_16x16x32_bf16 v[4:7], a[4:7], a[48:51], v[4:7]\n\t" \
  "ds_read_b64_tr_b16 a[140:141], v187 offset:49152\n\t" \
  "s_and_b32 %[po], s98, 0x2000\n\t" \
  "v_mfma_f32_16x16x32_bf16 v[36:39], a[4:7], a[52:55], v[36:39]\n\t" \
  "buffer_load_dwordx4 v201, %[sw], s96 offen offset:1024 lds\n\t" \
  "v_xor_b32 v225, 64, v224\n\t" \
  "v_mfma_f32_16x16x32_bf16 v[36:39], a[164:167], a[36:39], v[36:39]\n\t" \
  "ds_read_b64_tr_b16 a[142:143], v187 offset:50176\n\t" \
  "s_lshr_b32 s98, %[bid], 5\n\t" \
  "v_mfma_f32_16x16x32_bf16 v[68:71], a[164:167], a[40:43], v[68:71]\n\t" \
  "ds_read_b64_tr_b16 a[144:145], v188 offset:49152\n\t" \
  "s_xor_b32 s98, s98, %[wid]\n\t" \
  "v_mfma_f32_16x16x32_bf16 v[68:71], a[4:7], a[56:59], v[68:71]\n\t" \
  "buffer_load_dwordx4 v202, %[sw], s96 offen offset:2048 lds\n\t" \
  "v_xor_b32 v226, 128, v224\n\t" \
  "v_mfma_f32_16x16x32_bf16 v[100:103], a[4:7], a[60:63], v[100:103]\n\t" \
  "ds_read_b64_tr_b16 a[146:147], v188 offset:50176\n\t" \
  "s_lshl_b32 s98, s98, 13\n\t" \
  "v_mfma_f32_16x16x32_bf16 v[100:103], a[164:167], a[44:47], v[100:103]\n\t" \
  "ds_read_b64_tr_b16 a[148:149], v189 offset:49152\n\t" \
  "s_and_b32 %[px], s98, 0x2000\n\t" \
  "v_mfma_f32_16x16x32_bf16 v[8:11], a[168:171], a[32:35], v[8:11]\n\t" \
  "v_xor_b32 v227, 192, v224\n\t" \
  "v_mfma_f32_16x16x32_bf16 v[8:11], a[8:11], a[48:51], v[8:11]\n\t" \
  "buffer_load_dwordx4 v203, %[sw], s96 offen offset:3072 lds\n\t" \
  "ds_read_b64_tr_b16 a[150:151], v189 offset:50176\n\t" \
  "v_mfma_f32_16x16x32_bf16 v[40:43], a[8:11], a[52:55], v[40:43]\n\t" \
  "ds_read_b64_tr_b16 a[152:153], v190 offset:49152\n\t" \
  "s_lshl_b32 %[frb], %[wid], 2\n\t" \
  "v_mfma_f32_16x16x32_bf16 v[40:43], a[168:171], a[36:39], v[40:43]\n\t" \
  "v_lshlrev_b32 v133, 3, v132\n\t" \
  "v_mfma_f32_16x16x32_bf16 v[72:75], a[168:171], a[40:43], v[72:75]\n\t" \
  "s_add_u32 s97, %[rot19], 5767168\n\t" \
  "s_bfe_u32 s97, s97, 0x140003\n\t" \
  "v_mfma_f32_16x16x32_bf16 v[72:75], a[8:11], a[56:59], v[72:75]\n\t" \
  "ds_read_b64_tr_b16 a[154:155], v190 offset:50176\n\t" \
  "s_add_u32 %[frb], %[frb], 0x8000\n\t" \
  "v_mfma_f32_16x16x32_bf16 v[104:107], a[8:11], a[60:63], v[104:107]\n\t" \
  "ds_read_b64_tr_b16 a[156:157], v191 offset:49152\n\t" \
  "v_lshl_add_u32 v133, v129, 10, v133\n\t" \
  "v_mfma_f32_16x16x32_bf16 v[104:107], a[168:171], a[44:47], v[104:107]\n\t" \
  "ds_read_b64_tr_b16 a[158:159], v191 offset:50176\n\t" \
  "s_and_b32 s98, %[bid], 0x80\n\t" \
  "v_mfma_f32_16x16x32_bf16 v[12:15], a[172:175], a[32:35], v[12:15]\n\t" \
  "s_add_u32 s97, s97, %[sox]\n\t" \
  "ds_read_b64_tr_b16 a[64:65], v184 offset:57344\n\t" \
  "v_mfma_f32_16x16x32_bf16 v[12:15], a[12:15], a[48:51], v[12:15]\n\t" \
  "v_lshl_add_u32 v228, v131, 6, v133\n\t" \
  "v_mfma_f32_16x16x32_bf16 v[44:47], a[12:15], a[52:55], v[44:47]\n\t" \
  "ds_read_b64_tr_b16 a[66:67], v184 offset:58368\n\t" \
  "s_lshl_b32 s98, s98, 7\n\t" \
  "v_mfma_f32_16x16x32_bf16 v[44:47], a[172:175], a[36:39], v[44:47]\n\t" \
  "buffer_load_dwordx4 a[224:227], v248, %[ws], s97 offen sc1\n\t" \
  "ds_read_b64_tr_b16 a[68:69], v185 offset:57344\n\t" \
  "v_mfma_f32_16x16x32_bf16 v[76:79], a[172:175], a[40:43], v[76:79]\n\t" \
  "v_lshl_add_u32 v228, v134, 5, v228\n\t" \
  "v_mfma_f32_16x16x32_bf16 v[76:79], a[12:15], a[56:59], v[76:79]\n\t" \
  "ds_read_b64_tr_b16 a[70:71], v185 offset:58368\n\t" \
  "s_add_u32 %[frb], %[frb], s98\n\t" \
  "v_mfma_f32_16x16x32_bf16 v[108:111], a[12:15], a[60:63], v[108:111]\n\t" \
  "ds_read_b64_tr_b16 a[72:73], v186 offset:57344\n\t" \
  "s_and_b32 s98, %[bid], 0x4\n\t" \
  "v_mfma_f32_16x16x32_bf16 v[108:111], a[172:175], a[44:47], v[108:111]\n\t" \
  "buffer_load_dwordx4 a[228:231], v249, %[ws], s97 offen sc1\n\t" \
  "v_add_u32 v228, 81920, v228\n\t" \
  "v_mfma_f32_16x16x32_bf16 v[16:19], a[176:179], a[32:35], v[16:19]\n\t" \
  "ds_read_b64_tr_b16 a[74:75], v186 offset:58368\n\t" \
  "s_lshl_b32 s98, s98, 11\n\t" \
  "v_mfma_f32_16x16x32_bf16 v[16:19], a[16:19], a[48:51], v[16:19]\n\t" \
  "ds_read_b64_tr_b16 a[76:77], v187 offset:57344\n\t" \
  "s_add_u32 %[frb], %[frb], s98\n\t" \
  "v_mfma_f32_16x16x32_bf16 v[48:51], a[16:19], a[52:55], v[48:51]\n\t" \
  "buffer_load_dwordx4 a[232:235], v250, %[ws], s97 offen sc1\n\t" \
  "v_xor_b32 v229, 32, v228\n\t" \
  "v_mfma_f32_16x16x32_bf16 v[48:51], a[176:179], a[36:39], v[48:51]\n\t" \
  "ds_read_b64_tr_b16 a[78:79], v187 offset:58368\n\t" \
  "s_and_b32 s98, %[bid], 0x78\n\t" \
  "v_mfma_f32_16x16x32_bf16 v[80:83], a[176:179], a[40:43], v[80:83]\n\t" \
  "ds_read_b64_tr_b16 a[80:81], v188 offset:57344\n\t" \
  "s_lshl_b32 s98, s98, 6\n\t" \
  "v_mfma_f32_16x16x32_bf16 v[80:83], a[16:19], a[56:59], v[80:83]\n\t" \
  "buffer_load_dwordx4 a[236:239], v251, %[ws], s97 offen sc1\n\t" \
  "v_lshl_add_u32 v244, v131, 8, v133\n\t" \
  "v_mfma_f32_16x16x32_bf16 v[112:115], a[16:19], a[60:63], v[112:115]\n\t" \
  "ds_read_b64_tr_b16 a[82:83], v188 offset:58368\n\t" \
  "s_add_u32 %[frb], %[frb], s98\n\t" \
  "v_mfma_f32_16x16x32_bf16 v[112:115], a[176:179], a[44:47], v[112:115]\n\t" \
  "ds_read_b64_tr_b16 a[84:85], v189 offset:57344\n\t" \
  "s_lshl_b32 %[fw2], %[wid], 7\n\t" \
  "v_mfma_f32_16x16x32_bf16 v[20:23], a[180:183], a[32:35], v[20:23]\n\t" \
  "v_lshl_add_u32 v244, v131, 5, v244\n\t" \
  "v_mfma_f32_16x16x32_bf16 v[20:23], a[20:23], a[48:51], v[20:23]\n\t" \
  "s_add_u32 s99, %[pf1], 2048\n\t" \
  "buffer_load_dword %[pf], v217, %[wo], s99 offen\n\t" \
  "v_mfma_f32_16x16x32_bf16 v[52:55], a[20:23], a[52:55], v[52:55]\n\t" \
  "ds_read_b64_tr_b16 a[86:87], v189 offset:58368\n\t" \
  "s_add_u32 %[fw2], %[fw2], 0x20000\n\t" \
  "v_mfma_f32_16x16x32_bf16 v[52:55], a[180:183], a[36:39], v[52:55]\n\t" \
  "ds_read_b64_tr_b16 a[88:89], v190 offset:57344\n\t" \
  "v_lshl_add_u32 v244, v134, 7, v244\n\t" \
  "v_mfma_f32_16x16x32_bf16 v[84:87], a[180:183], a[40:43], v[84:87]\n\t" \
  "s_add_u32 s97, s97, %[dk]\n\t" \
  "ds_read_b64_tr_b16 a[90:91], v190 offset:58368\n\t" \
  "v_mfma_f32_16x16x32_bf16 v[84:87], a[20:23], a[56:59], v[84:87]\n\t" \
  "ds_read_b64_tr_b16 a[92:93], v191 offset:57344\n\t" \
  "s_and_b32 s98, %[bid], 0xc0\n\t" \
  "v_mfma_f32_16x16x32_bf16 v[116:119], a[20:23], a[60:63], v[116:119]\n\t" \
  "v_lshlrev_b32 v133, 4, v132\n\t" \
  "v_mfma_f32_16x16x32_bf16 v[116:119], a[180:183], a[44:47], v[116:119]\n\t" \
  "ds_read_b64_tr_b16 a[94:95], v191 offset:58368\n\t" \
  "s_lshl_b32 s98, s98, 9\n\t" \
  "v_mfma_f32_16x16x32_bf16 v[24:27], a[184:187], a[32:35], v[24:27]\n\t" \
  "buffer_load_dwordx4 a[240:243], v248, %[ws], s97 offen sc1\n\t" \
  "s_add_u32 %[fw2], %[fw2], s98\n\t" \
  "v_mfma_f32_16x16x32_bf16 v[24:27], a[24:27], a[48:51], v[24:27]\n\t" \
  "v_lshl_add_u32 v133, v129, 6, v133\n\t" \
  "v_mfma_f32_16x16x32_bf16 v[56:59], a[24:27], a[52:55], v[56:59]\n\t" \
  "s_and_b32 s98, %[bid], 0x2\n\t" \
  "v_mfma_f32_16x16x32_bf16 v[56:59], a[184:187], a[36:39], v[56:59]\n\t" \
  "buffer_load_dwordx4 a[244:247], v249, %[ws], s97 offen sc1\n\t" \
  "s_lshl_b32 s98, s98, 13\n\t" \
  "v_mfma_f32_16x16x32_bf16 v[88:91], a[184:187], a[40:43], v[88:91]\n\t" \
  "v_lshl_add_u32 v230, v131, 8, v133\n\t" \
  "v_mfma_f32_16x16x32_bf16 v[88:91], a[24:27], a[56:59], v[88:91]\n\t" \
  "s_add_u32 %[fw2], %[fw2], s98\n\t" \
  "v_mfma_f32_16x16x32_bf16 v[120:123], a[24:27], a[60:63], v[120:123]\n\t" \
  "s_and_b32 s98, %[bid], 0x4\n\t" \
  "v_mfma_f32_16x16x32_bf16 v[120:123], a[184:187], a[44:47], v[120:123]\n\t" \
  "buffer_load_dwordx4 a[248:251], v250, %[ws], s97 offen sc1\n\t" \
  "v_xor_b32 v231, 32, v230\n\t" \
  "v_mfma_f32_16x16x32_bf16 v[28:31], a[188:191], a[32:35], v[28:31]\n\t" \
  "s_lshl_b32 s98, s98, 11\n\t" \
  "v_mfma_f32_16x16x32_bf16 v[28:31], a[28:31], a[48:51], v[28:31]\n\t" \
  "s_add_u32 %[fw2], %[fw2], s98\n\t" \
  "v_mfma_f32_16x16x32_bf16 v[60:63], a[28:31], a[52:55], v[60:63]\n\t" \
  "buffer_load_dwordx4 a[252:255], v251, %[ws], s97 offen sc1\n\t" \
  "v_lshlrev_b32 v133, 1, v129\n\t" \
  "v_mfma_f32_16x16x32_bf16 v[60:63], a[188:191], a[36:39], v[60:63]\n\t" \
  "s_and_b32 s98, %[bid], 0x1\n\t" \
  "v_mfma_f32_16x16x32_bf16 v[92:95], a[188:191], a[40:43], v[92:95]\n\t" \
  "s_lshl_b32 s98, s98, 12\n\t" \
  "v_mfma_f32_16x16x32_bf16 v[92:95], a[28:31], a[56:59], v[92:95]\n\t" \
  "v_xor_b32 v133, v133, v130\n\t" \
  "v_mfma_f32_16x16x32_bf16 v[124:127], a[28:31], a[60:63], v[124:127]\n\t" \
  "v_mfma_f32_16x16x32_bf16 v[124:127], a[188:191], a[44:47], v[124:127]\n\t" \
  "s_waitcnt vmcnt(25) lgkmcnt(0)\n\t" \
  "s_barrier\n\t" \
  "v_mfma_f32_16x16x32_bf16 v[0:3], a[128:131], a[96:99], v[0:3]\n\t" \
  "s_add_u32 s96, %[rot19], 6815744\n\t" \
  "s_and_b32 s96, s96, 0x780000\n\t" \
  "v_mfma_f32_16x16x32_bf16 v[0:3], a[64:67], a[112:115], v[0:3]\n\t" \
  "ds_read_b64_tr_b16 a[160:161], v192 offset:0\n\t" \
  "v_lshlrev_b32 v133, 4, v133\n\t" \
  "v_mfma_f32_16x16x32_bf16 v[32:35], a[64:67], a[116:119], v[32:35]\n\t" \
  "ds_read_b64_tr_b16 a[162:163], v192 offset:1024\n\t" \
  "s_add_u32 %[fw2], %[fw2], s98\n\t" \
  "v_mfma_f32_16x16x32_bf16 v[32:35], a[128:131], a[100:103], v[32:35]\n\t" \
  "s_add_u32 m0, %[mw], 49152\n\t" \
  "ds_read_b64_tr_b16 a[164:165], v193 offset:0\n\t" \
  "v_mfma_f32_16x16x32_bf16 v[64:67], a[128:131], a[104:107], v[64:67]\n\t" \
  "v_lshl_add_u32 v240, v129, 11, v133\n\t" \
  "v_mfma_f32_16x16x32_bf16 v[64:67], a[64:67], a[120:123], v[64:67]\n\t" \
  "ds_read_b64_tr_b16 a[166:167], v193 offset:1024\n\t" \
  "s_lshl2_add_u32 %[fw2], %[hl], %[fw2]\n\t" \
  "v_mfma_f32_16x16x32_bf16 v[96:99], a[64:67], a[124:127], v[96:99]\n\t" \
  "buffer_load_dwordx4 v200, %[sw], s96 offen offset:0 lds\n\t" \
  "ds_read_b64_tr_b16 a[168:169], v194 offset:0\n\t" \
  "v_mfma_f32_16x16x32_bf16 v[96:99], a[128:131], a[108:111], v[96:99]\n\t" \
  "v_xor_b32 v241, 0x80, v240\n\t" \
  "v_mfma_f32_16x16x32_bf16 v[4:7], a[132:135], a[96:99], v[4:7]\n\t" \
  "ds_read_b64_tr_b16 a[170:171], v194 offset:1024\n\t" \
  "s_lshl_b32 %[xso], %[wid], 15\n\t" \
  "v_mfma_f32_16x16x32_bf16 v[4:7], a[68:71], a[112:115], v[4:7]\n\t" \
  "ds_read_b64_tr_b16 a[172:173], v195 offset:0\n\t" \
  "s_and_b32 s98, %[bid], 0x4\n\t" \
  "v_mfma_f32_16x16x32_bf16 v[36:39], a[68:71], a[116:119], v[36:39]\n\t" \
  "buffer_load_dwordx4 v201, %[sw], s96 offen offset:1024 lds\n\t" \
  "v_lshrrev_b32 v133, 1, v233\n\t" \
  "v_mfma_f32_16x16x32_bf16 v[36:39], a[132:135], a[100:103], v[36:39]\n\t" \
  "ds_read_b64_tr_b16 a[174:175], v195 offset:1024\n\t" \
  "s_lshl_b32 s98, s98, 19\n\t" \
  "v_mfma_f32_16x16x32_bf16 v[68:71], a[132:135], a[104:107], v[68:71]\n\t" \
  "ds_read_b64_tr_b16 a[176:177], v196 offset:0\n\t" \
  "s_add_u32 %[xso], %[xso], s98\n\t" \
  "v_mfma_f32_16x16x32_bf16 v[68:71], a[68:71], a[120:123], v[68:71]\n\t" \
  "buffer_load_dwordx4 v202, %[sw], s96 offen offset:2048 lds\n\t" \
  "v_mul_u32_u24 v133, 57344, v133\n\t" \
  "v_mfma_f32_16x16x32_bf16 v[100:103], a[68:71], a[124:127], v[100:103]\n\t" \
  "ds_read_b64_tr_b16 a[178:179], v196 offset:1024\n\t" \
  "s_and_b32 s98, %[bid], 0xc0\n\t" \
  "v_mfma_f32_16x16x32_bf16 v[100:103], a[132:135], a[108:111], v[100:103]\n\t" \
  "ds_read_b64_tr_b16 a[180:181], v197 offset:0\n\t" \
  "s_lshl_b32 s98, s98, 13\n\t" \
  "v_mfma_f32_16x16x32_bf16 v[8:11], a[136:139], a[96:99], v[8:11]\n\t" \
  "v_and_b32 v134, 1, v233\n\t" \
  "v_mfma_f32_16x16x32_bf16 v[8:11], a[72:75], a[112:115], v[8:11]\n\t" \
  "buffer_load_dwordx4 v203, %[sw], s96 offen offset:3072 lds\n\t" \
  "ds_read_b64_tr_b16 a[182:183], v197 offset:1024\n\t" \
  "v_mfma_f32_16x16x32_bf16 v[40:43], a[72:75], a[116:119], v[40:43]\n\t" \
  "ds_read_b64_tr_b16 a[184:185], v198 offset:0\n\t" \
  "s_add_u32 %[xso], %[xso], s98\n\t" \
  "v_mfma_f32_16x16x32_bf16 v[40:43], a[136:139], a[100:103], v[40:43]\n\t" \
  "v_lshl_add_u32 v133, v134, 4, v133\n\t" \
  "v_mfma_f32_16x16x32_bf16 v[72:75], a[136:139], a[104:107], v[72:75]\n\t" \
  "s_add_u32 s97, %[rot19], 6291456\n\t" \
  "s_bfe_u32 s97, s97, 0x140003\n\t" \
  "v_mfma_f32_16x16x32_bf16 v[72:75], a[72:75], a[120:123], v[72:75]\n\t" \
  "ds_read_b64_tr_b16 a[186:187], v198 offset:1024\n\t" \
  "s_and_b32 s98, %[bid], 0x3\n\t" \
  "v_mfma_f32_16x16x32_bf16 v[104:107], a[72:75], a[124:127], v[104:107]\n\t" \
  "ds_read_b64_tr_b16 a[188:189], v199 offset:0\n\t" \
  "v_or_b32 v232, %[idm], v133\n\t" \
  "v_mfma_f32_16x16x32_bf16 v[104:107], a[136:139], a[108:111], v[104:107]\n\t" \
  "ds_read_b64_tr_b16 a[190:191], v199 offset:1024\n\t" \
  "s_lshl_b32 s98, s98, 17\n\t" \
  "v_mfma_f32_16x16x32_bf16 v[12:15], a[140:143], a[96:99], v[12:15]\n\t" \
  "s_add_u32 s97, s97, %[sox]\n\t" \
  "ds_read_b64_tr_b16 a[0:1], v192 offset:8192\n\t" \
  "v_mfma_f32_16x16x32_bf16 v[12:15], a[76:79], a[112:115], v[12:15]\n\t" \
  "v_mfma_f32_16x16x32_bf16 v[44:47], a[76:79], a[116:119], v[44:47]\n\t" \
  "ds_read_b64_tr_b16 a[2:3], v192 offset:9216\n\t" \
  "s_add_u32 %[xso], %[xso], s98\n\t" \
  "v_mfma_f32_16x16x32_bf16 v[44:47], a[140:143], a[100:103], v[44:47]\n\t" \
  "buffer_load_dwordx4 a[32:35], v248, %[ws], s97 offen sc1\n\t" \
  "ds_read_b64_tr_b16 a[4:5], v193 offset:8192\n\t" \
  "v_mfma_f32_16x16x32_bf16 v[76:79], a[140:143], a[104:107], v[76:79]\n\t" \
  "v_mfma_f32_16x16x32_bf16 v[76:79], a[76:79], a[120:123], v[76:79]\n\t" \
  "ds_read_b64_tr_b16 a[6:7], v193 offset:9216\n\t" \
  "s_and_b32 s98, %[bid], 0x38\n\t" \
  "v_mfma_f32_16x16x32_bf16 v[108:111], a[76:79], a[124:127], v[108:111]\n\t" \
  "ds_read_b64_tr_b16 a[8:9], v194 offset:8192\n\t" \
  "s_lshl_b32 s98, s98, 5\n\t" \
  "v_mfma_f32_16x16x32_bf16 v[108:111], a[140:143], a[108:111], v[108:111]\n\t" \
  "buffer_load_dwordx4 a[36:39], v249, %[ws], s97 offen sc1\n\t" \
  "v_mfma_f32_16x16x32_bf16 v[16:19], a[144:147], a[96:99], v[16:19]\n\t" \
  "ds_read_b64_tr_b16 a[10:11], v194 offset:9216\n\t" \
  "s_add_u32 %[xso], %[xso], s98\n\t" \
  "v_mfma_f32_16x16x32_bf16 v[16:19], a[80:83], a[112:115], v[16:19]\n\t" \
  "ds_read_b64_tr_b16 a[12:13], v195 offset:8192\n\t" \
  "s_lshl_b32 %[ss], %[wid], 7\n\t" \
  "v_mfma_f32_16x16x32_bf16 v[48:51], a[80:83], a[116:119], v[48:51]\n\t" \
  "buffer_load_dwordx4 a[40:43], v250, %[ws], s97 offen sc1\n\t" \
  "v_mfma_f32_16x16x32_bf16 v[48:51], a[144:147], a[100:103], v[48:51]\n\t" \
  "ds_read_b64_tr_b16 a[14:15], v195 offset:9216\n\t" \
  "s_add_u32 %[ss], %[ss], 0x240000\n\t" \
  "v_mfma_f32_16x16x32_bf16 v[80:83], a[144:147], a[104:107], v[80:83]\n\t" \
  "ds_read_b64_tr_b16 a[16:17], v196 offset:8192\n\t" \
  "s_and_b32 s98, %[bid], 0xff\n\t" \
  "v_mfma_f32_16x16x32_bf16 v[80:83], a[80:83], a[120:123], v[80:83]\n\t" \
  "buffer_load_dwordx4 a[44:47], v251, %[ws], s97 offen sc1\n\t" \
  "v_mfma_f32_16x16x32_bf16 v[112:115], a[80:83], a[124:127], v[112:115]\n\t" \
  "ds_read_b64_tr_b16 a[18:19], v196 offset:9216\n\t" \
  "s_lshl_b32 s98, s98, 9\n\t" \
  "v_mfma_f32_16x16x32_bf16 v[112:115], a[144:147], a[108:111], v[112:115]\n\t" \
  "ds_read_b64_tr_b16 a[20:21], v197 offset:8192\n\t" \
  "s_add_u32 %[ss], %[ss], s98\n\t" \
  "v_mfma_f32_16x16x32_bf16 v[20:23], a[148:151], a[96:99], v[20:23]\n\t" \
  "v_mfma_f32_16x16x32_bf16 v[20:23], a[84:87], a[112:115], v[20:23]\n\t" \
  "s_add_u32 s97, s97, %[dk]\n\t" \
  "ds_read_b64_tr_b16 a[22:23], v197 offset:9216\n\t" \
  "v_mfma_f32_16x16x32_bf16 v[52:55], a[84:87], a[116:119], v[52:55]\n\t" \
  "ds_read_b64_tr_b16 a[24:25], v198 offset:8192\n\t" \
  "s_lshl_b32 %[mbp], %[wid], 7\n\t" \
  "v_mfma_f32_16x16x32_bf16 v[52:55], a[148:151], a[100:103], v[52:55]\n\t" \
  "v_mfma_f32_16x16x32_bf16 v[84:87], a[148:151], a[104:107], v[84:87]\n\t" \
  "buffer_load_dwordx4 a[48:51], v248, %[ws], s97 offen sc1\n\t" \
  "ds_read_b64_tr_b16 a[26:27], v198 offset:9216\n\t" \
  "v_mfma_f32_16x16x32_bf16 v[84:87], a[84:87], a[120:123], v[84:87]\n\t" \
  "ds_read_b64_tr_b16 a[28:29], v199 offset:8192\n\t" \
  "s_add_u32 %[mbp], %[mbp], 0x220000\n\t" \
  "v_mfma_f32_16x16x32_bf16 v[116:119], a[84:87], a[124:127], v[116:119]\n\t" \
  "v_mfma_f32_16x16x32_bf16 v[116:119], a[148:151], a[108:111], v[116:119]\n\t" \
  "ds_read_b64_tr_b16 a[30:31], v199 offset:9216\n\t" \
  "s_and_b32 s98, %[bid], 0xc0\n\t" \
  "v_mfma_f32_16x16x32_bf16 v[24:27], a[152:155], a[96:99], v[24:27]\n\t" \
  "buffer_load_dwordx4 a[52:55], v249, %[ws], s97 offen sc1\n\t" \
  "s_lshl_b32 s98, s98, 9\n\t" \
  "v_mfma_f32_16x16x32_bf16 v[24:27], a[88:91], a[112:115], v[24:27]\n\t" \
  "v_mfma_f32_16x16x32_bf16 v[56:59], a[88:91], a[116:119], v[56:59]\n\t" \
  "s_add_u32 %[mbp], %[mbp], s98\n\t" \
  "v_mfma_f32_16x16x32_bf16 v[56:59], a[152:155], a[100:103], v[56:59]\n\t" \
  "buffer_load_dwordx4 a[56:59], v250, %[ws], s97 offen sc1\n\t" \
  "s_and_b32 s98, %[bid], 0x3\n\t" \
  "v_mfma_f32_16x16x32_bf16 v[88:91], a[152:155], a[104:107], v[88:91]\n\t" \
  "v_mfma_f32_16x16x32_bf16 v[88:91], a[88:91], a[120:123], v[88:91]\n\t" \
  "s_lshl_b32 s98, s98, 13\n\t" \
  "v_mfma_f32_16x16x32_bf16 v[120:123], a[88:91], a[124:127], v[120:123]\n\t" \
  "s_add_u32 %[mbp], %[mbp], s98\n\t" \
  "v_mfma_f32_16x16x32_bf16 v[120:123], a[152:155], a[108:111], v[120:123]\n\t" \
  "buffer_load_dwordx4 a[60:63], v251, %[ws], s97 offen sc1\n\t" \
  "v_mfma_f32_16x16x32_bf16 v[28:31], a[156:159], a[96:99], v[28:31]\n\t" \
  "s_and_b32 s98, %[bid], 0x4\n\t" \
  "v_mfma_f32_16x16x32_bf16 v[28:31], a[92:95], a[112:115], v[28:31]\n\t" \
  "s_lshl_b32 s98, s98, 10\n\t" \
  "v_mfma_f32_16x16x32_bf16 v[60:63], a[92:95], a[116:119], v[60:63]\n\t" \
  "s_add_u32 s99, %[pf1], 4096\n\t" \
  "buffer_load_dword %[pf], v217, %[wo], s99 offen\n\t" \
  "v_mfma_f32_16x16x32_bf16 v[60:63], a[156:159], a[100:103], v[60:63]\n\t" \
  "s_add_u32 %[mbp], %[mbp], s98\n\t" \
  "v_mfma_f32_16x16x32_bf16 v[92:95], a[156:159], a[104:107], v[92:95]\n\t" \
  "s_and_b32 s98, %[bid], 0x38\n\t" \
  "v_mfma_f32_16x16x32_bf16 v[92:95], a[92:95], a[120:123], v[92:95]\n\t" \
  "v_mfma_f32_16x16x32_bf16 v[124:127], a[92:95], a[124:127], v[124:127]\n\t" \
  "v_mfma_f32_16x16x32_bf16 v[124:127], a[156:159], a[108:111], v[124:127]\n\t" \
  "s_waitcnt vmcnt(26) lgkmcnt(0)\n\t" \
  "s_barrier\n\t" \
  "v_mfma_f32_16x16x32_bf16 v[0:3], a[160:163], a[192:195], v[0:3]\n\t" \
  "s_add_u32 s96, %[rot19], 7340032\n\t" \
  "s_and_b32 s96, s96, 0x780000\n\t" \
  "v_mfma_f32_16x16x32_bf16 v[0:3], a[0:3], a[208:211], v[0:3]\n\t" \
  "ds_read_b64_tr_b16 a[128:129], v184 offset:16384\n\t" \
  "v_mfma_f32_16x16x32_bf16 v[32:35], a[0:3], a[212:215], v[32:35]\n\t" \
  "ds_read_b64_tr_b16 a[130:131], v184 offset:17408\n\t" \
  "s_lshl_b32 s98, s98, 6\n\t" \
  "v_mfma_f32_16x16x32_bf16 v[32:35], a[160:163], a[196:199], v[32:35]\n\t" \
  "s_add_u32 m0, %[mw], 0\n\t" \
  "ds_read_b64_tr_b16 a[132:133], v185 offset:16384\n\t" \
  "v_mfma_f32_16x16x32_bf16 v[64:67], a[160:163], a[200:203], v[64:67]\n\t" \
  "v_mfma_f32_16x16x32_bf16 v[64:67], a[0:3], a[216:219], v[64:67]\n\t" \
  "ds_read_b64_tr_b16 a[134:135], v185 offset:17408\n\t" \
  "s_add_u32 %[mbp], %[mbp], s98\n\t" \
  "v_mfma_f32_16x16x32_bf16 v[96:99], a[0:3], a[220:223], v[96:99]\n\t" \
  "ds_read_b64_tr_b16 a[136:137], v186 offset:16384\n\t" \
  "s_lshr_b32 s98, %[bid], 6\n\t" \
  "v_mfma_f32_16x16x32_bf16 v[96:99], a[160:163], a[204:207], v[96:99]\n\t" \
  "buffer_load_dwordx4 v200, %[sw], s96 offen offset:0 lds\n\t" \
  "v_mfma_f32_16x16x32_bf16 v[4:7], a[164:167], a[192:195], v[4:7]\n\t" \
  "ds_read_b64_tr_b16 a[138:139], v186 offset:17408\n\t" \
  "s_lshl2_add_u32 %[gvo], s98, %[wid]\n\t" \
  "v_mfma_f32_16x16x32_bf16 v[4:7], a[4:7], a[208:211], v[4:7]\n\t" \
  "ds_read_b64_tr_b16 a[140:141], v187 offset:16384\n\t" \
  "s_mul_i32 %[gvo], %[gvo], 786432\n\t" \
  "v_mfma_f32_16x16x32_bf16 v[36:39], a[4:7], a[212:215], v[36:39]\n\t" \
  "buffer_load_dwordx4 v201, %[sw], s96 offen offset:1024 lds\n\t" \
  "v_mfma_f32_16x16x32_bf16 v[36:39], a[164:167], a[196:199], v[36:39]\n\t" \
  "ds_read_b64_tr_b16 a[142:143], v187 offset:17408\n\t" \
  "s_and_b32 s98, %[bid], 0x38\n\t" \
  "v_mfma_f32_16x16x32_bf16 v[68:71], a[164:167], a[200:203], v[68:71]\n\t" \
  "ds_read_b64_tr_b16 a[144:145], v188 offset:16384\n\t" \
  "s_lshl_b32 s98, s98, 4\n\t" \
  "v_mfma_f32_16x16x32_bf16 v[68:71], a[4:7], a[216:219], v[68:71]\n\t" \
  "v_mfma_f32_16x16x32_bf16 v[100:103], a[4:7], a[220:223], v[100:103]\n\t" \
  "buffer_load_dwordx4 v202, %[sw], s96 offen offset:2048 lds\n\t" \
  "ds_read_b64_tr_b16 a[146:147], v188 offset:17408\n\t" \
  "v_mfma_f32_16x16x32_bf16 v[100:103], a[164:167], a[204:207], v[100:103]\n\t" \
  "ds_read_b64_tr_b16 a[148:149], v189 offset:16384\n\t" \
  "s_add_u32 %[gvo], %[gvo], s98\n\t" \
  "v_mfma_f32_16x16x32_bf16 v[8:11], a[168:171], a[192:195], v[8:11]\n\t" \
  "v_mfma_f32_16x16x32_bf16 v[8:11], a[8:11], a[208:211], v[8:11]\n\t" \
  "ds_read_b64_tr_b16 a[150:151], v189 offset:17408\n\t" \
  "s_lshr_b32 s98, %[bid], 2\n\t" \
  "v_mfma_f32_16x16x32_bf16 v[40:43], a[8:11], a[212:215], v[40:43]\n\t" \
  "buffer_load_dwordx4 v203, %[sw], s96 offen offset:3072 lds\n\t" \
  "ds_read_b64_tr_b16 a[152:153], v190 offset:16384\n\t" \
  "v_mfma_f32_16x16x32_bf16 v[40:43], a[168:171], a[196:199], v[40:43]\n\t" \
  "v_mfma_f32_16x16x32_bf16 v[72:75], a[168:171], a[200:203], v[72:75]\n\t" \
  "ds_read_b64_tr_b16 a[154:155], v190 offset:17408\n\t" \
  "s_xor_b32 s98, s98, %[bid]\n\t" \
  "v_mfma_f32_16x16x32_bf16 v[72:75], a[8:11], a[216:219], v[72:75]\n\t" \
  "s_add_u32 s97, %[rot19], 6815744\n\t" \
  "s_bfe_u32 s97, s97, 0x140003\n\t" \
  "v_mfma_f32_16x16x32_bf16 v[104:107], a[8:11], a[220:223], v[104:107]\n\t" \
  "ds_read_b64_tr_b16 a[156:157], v191 offset:16384\n\t" \
  "v_mfma_f32_16x16x32_bf16 v[104:107], a[168:171], a[204:207], v[104:107]\n\t" \
  "ds_read_b64_tr_b16 a[158:159], v191 offset:17408\n\t" \
  "s_and_b32 s98, s98, 4\n\t" \
  "v_mfma_f32_16x16x32_bf16 v[12:15], a[172:175], a[192:195], v[12:15]\n\t" \
  "ds_read_b64_tr_b16 a[64:65], v184 offset:24576\n\t" \
  "s_lshl_b32 %[vo], s98, 4\n\t" \
  "v_mfma_f32_16x16x32_bf16 v[12:15], a[12:15], a[208:211], v[12:15]\n\t" \
  "s_add_u32 s97, s97, %[sox]\n\t" \
  "v_mfma_f32_16x16x32_bf16 v[44:47], a[12:15], a[212:215], v[44:47]\n\t" \
  "ds_read_b64_tr_b16 a[66:67], v184 offset:25600\n\t" \
  "s_and_b32 s98, %[bid], 0x8\n\t" \
  "v_mfma_f32_16x16x32_bf16 v[44:47], a[172:175], a[196:199], v[44:47]\n\t" \
  "ds_read_b64_tr_b16 a[68:69], v185 offset:24576\n\t" \
  "s_lshl_b32 s98, s98, 2\n\t" \
  "v_mfma_f32_16x16x32_bf16 v[76:79], a[172:175], a[200:203], v[76:79]\n\t" \
  "v_mfma_f32_16x16x32_bf16 v[76:79], a[12:15], a[216:219], v[76:79]\n\t" \
  "buffer_load_dwordx4 a[96:99], v248, %[ws], s97 offen sc1\n\t" \
  "ds_read_b64_tr_b16 a[70:71], v185 offset:25600\n\t" \
  "v_mfma_f32_16x16x32_bf16 v[108:111], a[12:15], a[220:223], v[108:111]\n\t" \
  "ds_read_b64_tr_b16 a[72:73], v186 offset:24576\n\t" \
  "s_add_u32 %[vo], %[vo], s98\n\t" \
  "v_mfma_f32_16x16x32_bf16 v[108:111], a[172:175], a[204:207], v[108:111]\n\t" \
  "v_mfma_f32_16x16x32_bf16 v[16:19], a[176:179], a[192:195], v[16:19]\n\t" \
  "buffer_load_dwordx4 a[100:103], v249, %[ws], s97 offen sc1\n\t" \
  "ds_read_b64_tr_b16 a[74:75], v186 offset:25600\n\t" \
  "v_mfma_f32_16x16x32_bf16 v[16:19], a[16:19], a[208:211], v[16:19]\n\t" \
  "ds_read_b64_tr_b16 a[76:77], v187 offset:24576\n\t" \
  "s_and_b32 s98, %[bid], 0x3\n\t" \
  "v_mfma_f32_16x16x32_bf16 v[48:51], a[16:19], a[212:215], v[48:51]\n\t" \
  "v_mfma_f32_16x16x32_bf16 v[48:51], a[176:179], a[196:199], v[48:51]\n\t" \
  "ds_read_b64_tr_b16 a[78:79], v187 offset:25600\n\t" \
  "s_lshl_b32 s98, s98, 3\n\t" \
  "v_mfma_f32_16x16x32_bf16 v[80:83], a[176:179], a[200:203], v[80:83]\n\t" \
  "buffer_load_dwordx4 a[104:107], v250, %[ws], s97 offen sc1\n\t" \
  "ds_read_b64_tr_b16 a[80:81], v188 offset:24576\n\t" \
  "v_mfma_f32_16x16x32_bf16 v[80:83], a[16:19], a[216:219], v[80:83]\n\t" \
  "v_mfma_f32_16x16x32_bf16 v[112:115], a[16:19], a[220:223], v[112:115]\n\t" \
  "ds_read_b64_tr_b16 a[82:83], v188 offset:25600\n\t" \
  "s_add_u32 %[vo], %[vo], s98\n\t" \
  "v_mfma_f32_16x16x32_bf16 v[112:115], a[176:179], a[204:207], v[112:115]\n\t" \
  "ds_read_b64_tr_b16 a[84:85], v189 offset:24576\n\t" \
  "s_and_b32 s98, %[bid], 0x4\n\t" \
  "v_mfma_f32_16x16x32_bf16 v[20:23], a[180:183], a[192:195], v[20:23]\n\t" \
  "buffer_load_dwordx4 a[108:111], v251, %[ws], s97 offen sc1\n\t" \
  "v_mfma_f32_16x16x32_bf16 v[20:23], a[20:23], a[208:211], v[20:23]\n\t" \
  "ds_read_b64_tr_b16 a[86:87], v189 offset:25600\n\t" \
  "s_lshl_b32 s98, s98, 0\n\t" \
  "v_mfma_f32_16x16x32_bf16 v[52:55], a[20:23], a[212:215], v[52:55]\n\t" \
  "ds_read_b64_tr_b16 a[88:89], v190 offset:24576\n\t" \
  "s_add_u32 %[vo], %[vo], s98\n\t" \
  "v_mfma_f32_16x16x32_bf16 v[52:55], a[180:183], a[196:199], v[52:55]\n\t" \
  "s_add_u32 s97, s97, %[dk]\n\t" \
  "v_mfma_f32_16x16x32_bf16 v[84:87], a[180:183], a[200:203], v[84:87]\n\t" \
  "ds_read_b64_tr_b16 a[90:91], v190 offset:25600\n\t" \
  "s_add_u32 %[vo], %[vo], 0x2000\n\t" \
  "v_mfma_f32_16x16x32_bf16 v[84:87], a[20:23], a[216:219], v[84:87]\n\t" \
  "ds_read_b64_tr_b16 a[92:93], v191 offset:24576\n\t" \
  "s_cmp_lt_i32 %[prb], 0\n\t" \
  "s_cselect_b32 %[vo], %[prb], %[vo]\n\t" \
  "v_mfma_f32_16x16x32_bf16 v[116:119], a[20:23], a[220:223], v[116:119]\n\t" \
  "v_mfma_f32_16x16x32_bf16 v[116:119], a[180:183], a[204:207], v[116:119]\n\t" \
  "buffer_load_dwordx4 a[112:115], v248, %[ws], s97 offen sc1\n\t" \
  "ds_read_b64_tr_b16 a[94:95], v191 offset:25600\n\t" \
  "v_mfma_f32_16x16x32_bf16 v[24:27], a[184:187], a[192:195], v[24:27]\n\t" \
  "v_mfma_f32_16x16x32_bf16 v[24:27], a[24:27], a[208:211], v[24:27]\n\t" \
  "v_mfma_f32_16x16x32_bf16 v[56:59], a[24:27], a[212:215], v[56:59]\n\t" \
  "v_mfma_f32_16x16x32_bf16 v[56:59], a[184:187], a[196:199], v[56:59]\n\t" \
  "buffer_load_dwordx4 a[116:119], v249, %[ws], s97 offen sc1\n\t" \
  "v_mfma_f32_16x16x32_bf16 v[88:91], a[184:187], a[200:203], v[88:91]\n\t" \
  "v_mfma_f32_16x16x32_bf16 v[88:91], a[24:27], a[216:219], v[88:91]\n\t" \
  "v_mfma_f32_16x16x32_bf16 v[120:123], a[24:27], a[220:223], v[120:123]\n\t" \
  "buffer_load_dwordx4 a[120:123], v250, %[ws], s97 offen sc1\n\t" \
  "v_mfma_f32_16x16x32_bf16 v[120:123], a[184:187], a[204:207], v[120:123]\n\t" \
  "v_mfma_f32_16x16x32_bf16 v[28:31], a[188:191], a[192:195], v[28:31]\n\t" \
  "v_mfma_f32_16x16x32_bf16 v[28:31], a[28:31], a[208:211], v[28:31]\n\t" \
  "v_mfma_f32_16x16x32_bf16 v[60:63], a[28:31], a[212:215], v[60:63]\n\t" \
  "buffer_load_dwordx4 a[124:127], v251, %[ws], s97 offen sc1\n\t" \
  "v_mfma_f32_16x16x32_bf16 v[60:63], a[188:191], a[196:199], v[60:63]\n\t" \
  "v_mfma_f32_16x16x32_bf16 v[92:95], a[188:191], a[200:203], v[92:95]\n\t" \
  "v_mfma_f32_16x16x32_bf16 v[92:95], a[28:31], a[216:219], v[92:95]\n\t" \
  "v_mfma_f32_16x16x32_bf16 v[124:127], a[28:31], a[220:223], v[124:127]\n\t" \
  "v_mfma_f32_16x16x32_bf16 v[124:127], a[188:191], a[204:207], v[124:127]\n\t" \
  "s_waitcnt vmcnt(25) lgkmcnt(0)\n\t" \
  "s_barrier\n\t" \
  "v_mfma_f32_16x16x32_bf16 v[0:3], a[128:131], a[224:227], v[0:3]\n\t" \
  "s_add_u32 s96, %[rot19], 7864320\n\t" \
  "s_and_b32 s96, s96, 0x780000\n\t" \
  "v_mfma_f32_16x16x32_bf16 v[0:3], a[64:67], a[240:243], v[0:3]\n\t" \
  "ds_read_b64_tr_b16 a[160:161], v184 offset:32768\n\t" \
  "v_mfma_f32_16x16x32_bf16 v[32:35], a[64:67], a[244:247], v[32:35]\n\t" \
  "ds_read_b64_tr_b16 a[162:163], v184 offset:33792\n\t" \
  "v_mfma_f32_16x16x32_bf16 v[32:35], a[128:131], a[228:231], v[32:35]\n\t" \
  "s_add_u32 m0, %[mw], 98304\n\t" \
  "ds_read_b64_tr_b16 a[164:165], v185 offset:32768\n\t" \
  "v_mfma_f32_16x16x32_bf16 v[64:67], a[128:131], a[232:235], v[64:67]\n\t" \
  "v_mfma_f32_16x16x32_bf16 v[64:67], a[64:67], a[248:251], v[64:67]\n\t" \
  "ds_read_b64_tr_b16 a[166:167], v185 offset:33792\n\t" \
  "v_mfma_f32_16x16x32_bf16 v[96:99], a[64:67], a[252:255], v[96:99]\n\t" \
  "buffer_load_dwordx4 v200, %[sw], s96 offen offset:0 lds\n\t" \
  "ds_read_b64_tr_b16 a[168:169], v186 offset:32768\n\t" \
  "v_mfma_f32_16x16x32_bf16 v[96:99], a[128:131], a[236:239], v[96:99]\n\t" \
  "v_mfma_f32_16x16x32_bf16 v[4:7], a[132:135], a[224:227], v[4:7]\n\t" \
  "ds_read_b64_tr_b16 a[170:171], v186 offset:33792\n\t" \
  "v_mfma_f32_16x16x32_bf16 v[4:7], a[68:71], a[240:243], v[4:7]\n\t" \
  "ds_read_b64_tr_b16 a[172:173], v187 offset:32768\n\t" \
  "v_mfma_f32_16x16x32_bf16 v[36:39], a[68:71], a[244:247], v[36:39]\n\t" \
  "buffer_load_dwordx4 v201, %[sw], s96 offen offset:1024 lds\n\t" \
  "v_mfma_f32_16x16x32_bf16 v[36:39], a[132:135], a[228:231], v[36:39]\n\t" \
  "ds_read_b64_tr_b16 a[174:175], v187 offset:33792\n\t" \
  "v_mfma_f32_16x16x32_bf16 v[68:71], a[132:135], a[232:235], v[68:71]\n\t" \
  "ds_read_b64_tr_b16 a[176:177], v188 offset:32768\n\t" \
  "v_mfma_f32_16x16x32_bf16 v[68:71], a[68:71], a[248:251], v[68:71]\n\t" \
  "buffer_load_dwordx4 v202, %[sw], s96 offen offset:2048 lds\n\t" \
  "v_mfma_f32_16x16x32_bf16 v[100:103], a[68:71], a[252:255], v[100:103]\n\t" \
  "ds_read_b64_tr_b16 a[178:179], v188 offset:33792\n\t" \
  "v_mfma_f32_16x16x32_bf16 v[100:103], a[132:135], a[236:239], v[100:103]\n\t" \
  "ds_read_b64_tr_b16 a[180:181], v189 offset:32768\n\t" \
  "v_mfma_f32_16x16x32_bf16 v[8:11], a[136:139], a[224:227], v[8:11]\n\t" \
  "v_mfma_f32_16x16x32_bf16 v[8:11], a[72:75], a[240:243], v[8:11]\n\t" \
  "buffer_load_dwordx4 v203, %[sw], s96 offen offset:3072 lds\n\t" \
  "ds_read_b64_tr_b16 a[182:183], v189 offset:33792\n\t" \
  "v_mfma_f32_16x16x32_bf16 v[40:43], a[72:75], a[244:247], v[40:43]\n\t" \
  "ds_read_b64_tr_b16 a[184:185], v190 offset:32768\n\t" \
  "v_mfma_f32_16x16x32_bf16 v[40:43], a[136:139], a[228:231], v[40:43]\n\t" \
  "v_mfma_f32_16x16x32_bf16 v[72:75], a[136:139], a[232:235], v[72:75]\n\t" \
  "s_add_u32 s97, %[rot19], 7340032\n\t" \
  "s_bfe_u32 s97, s97, 0x140003\n\t" \
  "v_mfma_f32_16x16x32_bf16 v[72:75], a[72:75], a[248:251], v[72:75]\n\t" \
  "ds_read_b64_tr_b16 a[186:187], v190 offset:33792\n\t" \
  "v_mfma_f32_16x16x32_bf16 v[104:107], a[72:75], a[252:255], v[104:107]\n\t" \
  "ds_read_b64_tr_b16 a[188:189], v191 offset:32768\n\t" \
  "v_mfma_f32_16x16x32_bf16 v[104:107], a[136:139], a[236:239], v[104:107]\n\t" \
  "ds_read_b64_tr_b16 a[190:191], v191 offset:33792\n\t" \
  "v_mfma_f32_16x16x32_bf16 v[12:15], a[140:143], a[224:227], v[12:15]\n\t" \
  "s_add_u32 s97, s97, %[sox]\n\t" \
  "ds_read_b64_tr_b16 a[0:1], v184 offset:40960\n\t" \
  "v_mfma_f32_16x16x32_bf16 v[12:15], a[76:79], a[240:243], v[12:15]\n\t" \
  "v_mfma_f32_16x16x32_bf16 v[44:47], a[76:79], a[244:247], v[44:47]\n\t" \
  "ds_read_b64_tr_b16 a[2:3], v184 offset:41984\n\t" \
  "v_mfma_f32_16x16x32_bf16 v[44:47], a[140:143], a[228:231], v[44:47]\n\t" \
  "buffer_load_dwordx4 a[192:195], v248, %[ws], s97 offen sc1\n\t" \
  "ds_read_b64_tr_b16 a[4:5], v185 offset:40960\n\t" \
  "v_mfma_f32_16x16x32_bf16 v[76:79], a[140:143], a[232:235], v[76:79]\n\t" \
  "v_mfma_f32_16x16x32_bf16 v[76:79], a[76:79], a[248:251], v[76:79]\n\t" \
  "ds_read_b64_tr_b16 a[6:7], v185 offset:41984\n\t" \
  "v_mfma_f32_16x16x32_bf16 v[108:111], a[76:79], a[252:255], v[108:111]\n\t" \
  "ds_read_b64_tr_b16 a[8:9], v186 offset:40960\n\t" \
  "v_mfma_f32_16x16x32_bf16 v[108:111], a[140:143], a[236:239], v[108:111]\n\t" \
  "buffer_load_dwordx4 a[196:199], v249, %[ws], s97 offen sc1\n\t" \
  "v_mfma_f32_16x16x32_bf16 v[16:19], a[144:147], a[224:227], v[16:19]\n\t" \
  "ds_read_b64_tr_b16 a[10:11], v186 offset:41984\n\t" \
  "v_mfma_f32_16x16x32_bf16 v[16:19], a[80:83], a[240:243], v[16:19]\n\t" \
  "ds_read_b64_tr_b16 a[12:13], v187 offset:40960\n\t" \
  "v_mfma_f32_16x16x32_bf16 v[48:51], a[80:83], a[244:247], v[48:51]\n\t" \
  "buffer_load_dwordx4 a[200:203], v250, %[ws], s97 offen sc1\n\t" \
  "v_mfma_f32_16x16x32_bf16 v[48:51], a[144:147], a[228:231], v[48:51]\n\t" \
  "ds_read_b64_tr_b16 a[14:15], v187 offset:41984\n\t" \
  "v_mfma_f32_16x16x32_bf16 v[80:83], a[144:147], a[232:235], v[80:83]\n\t" \
  "ds_read_b64_tr_b16 a[16:17], v188 offset:40960\n\t" \
  "v_mfma_f32_16x16x32_bf16 v[80:83], a[80:83], a[248:251], v[80:83]\n\t" \
  "buffer_load_dwordx4 a[204:207], v251, %[ws], s97 offen sc1\n\t" \
  "v_mfma_f32_16x16x32_bf16 v[112:115], a[80:83], a[252:255], v[112:115]\n\t" \
  "ds_read_b64_tr_b16 a[18:19], v188 offset:41984\n\t" \
  "v_mfma_f32_16x16x32_bf16 v[112:115], a[144:147], a[236:239], v[112:115]\n\t" \
  "ds_read_b64_tr_b16 a[20:21], v189 offset:40960\n\t" \
  "v_mfma_f32_16x16x32_bf16 v[20:23], a[148:151], a[224:227], v[20:23]\n\t" \
  "v_mfma_f32_16x16x32_bf16 v[20:23], a[84:87], a[240:243], v[20:23]\n\t" \
  "s_add_u32 s99, %[pf1], 6144\n\t" \
  "buffer_load_dword %[pf], v217, %[wo], s99 offen\n\t" \
  "v_mfma_f32_16x16x32_bf16 v[52:55], a[84:87], a[244:247], v[52:55]\n\t" \
  "ds_read_b64_tr_b16 a[22:23], v189 offset:41984\n\t" \
  "v_mfma_f32_16x16x32_bf16 v[52:55], a[148:151], a[228:231], v[52:55]\n\t" \
  "ds_read_b64_tr_b16 a[24:25], v190 offset:40960\n\t" \
  "v_mfma_f32_16x16x32_bf16 v[84:87], a[148:151], a[232:235], v[84:87]\n\t" \
  "s_add_u32 s97, s97, %[dk]\n\t" \
  "ds_read_b64_tr_b16 a[26:27], v190 offset:41984\n\t" \
  "v_mfma_f32_16x16x32_bf16 v[84:87], a[84:87], a[248:251], v[84:87]\n\t" \
  "ds_read_b64_tr_b16 a[28:29], v191 offset:40960\n\t" \
  "v_mfma_f32_16x16x32_bf16 v[116:119], a[84:87], a[252:255], v[116:119]\n\t" \
  "v_mfma_f32_16x16x32_bf16 v[116:119], a[148:151], a[236:239], v[116:119]\n\t" \
  "ds_read_b64_tr_b16 a[30:31], v191 offset:41984\n\t" \
  "v_mfma_f32_16x16x32_bf16 v[24:27], a[152:155], a[224:227], v[24:27]\n\t" \
  "buffer_load_dwordx4 a[208:211], v248, %[ws], s97 offen sc1\n\t" \
  "v_mfma_f32_16x16x32_bf16 v[24:27], a[88:91], a[240:243], v[24:27]\n\t" \
  "v_mfma_f32_16x16x32_bf16 v[56:59], a[88:91], a[244:247], v[56:59]\n\t" \
  "v_mfma_f32_16x16x32_bf16 v[56:59], a[152:155], a[228:231], v[56:59]\n\t" \
  "buffer_load_dwordx4 a[212:215], v249, %[ws], s97 offen sc1\n\t" \
  "v_mfma_f32_16x16x32_bf16 v[88:91], a[152:155], a[232:235], v[88:91]\n\t" \
  "v_mfma_f32_16x16x32_bf16 v[88:91], a[88:91], a[248:251], v[88:91]\n\t" \
  "v_mfma_f32_16x16x32_bf16 v[120:123], a[88:91], a[252:255], v[120:123]\n\t" \
  "v_mfma_f32_16x16x32_bf16 v[120:123], a[152:155], a[236:239], v[120:123]\n\t" \
  "buffer_load_dwordx4 a[216:219], v250, %[ws], s97 offen sc1\n\t" \
  "v_mfma_f32_16x16x32_bf16 v[28:31], a[156:159], a[224:227], v[28:31]\n\t" \
  "v_mfma_f32_16x16x32_bf16 v[28:31], a[92:95], a[240:243], v[28:31]\n\t" \
  ".Lhs4:\n\ts_cbranch_execz .Lhs4n\n\t" \
  "v_mfma_f32_16x16x32_bf16 v[60:63], a[92:95], a[244:247], v[60:63]\n\t" \
  "buffer_load_dwordx4 a[220:223], v251, %[ws], s97 offen sc1\n\t" \
  "v_mfma_f32_16x16x32_bf16 v[60:63], a[156:159], a[228:231], v[60:63]\n\t" \
  "v_mfma_f32_16x16x32_bf16 v[92:95], a[156:159], a[232:235], v[92:95]\n\t" \
  "v_mfma_f32_16x16x32_bf16 v[92:95], a[92:95], a[248:251], v[92:95]\n\t" \
  "v_mfma_f32_16x16x32_bf16 v[124:127], a[92:95], a[252:255], v[124:127]\n\t" \
  "v_mfma_f32_16x16x32_bf16 v[124:127], a[156:159], a[236:239], v[124:127]\n\t" \
  "s_waitcnt vmcnt(26) lgkmcnt(0)\n\t" \
  "s_barrier\n\t" \
  "v_mfma_f32_16x16x32_bf16 v[0:3], a[160:163], a[32:35], v[0:3]\n\t" \
  "s_add_u32 s97, %[rot19], 7864320\n\t" \
  "s_bfe_u32 s97, s97, 0x140003\n\t" \
  "v_mfma_f32_16x16x32_bf16 v[0:3], a[0:3], a[48:51], v[0:3]\n\t" \
  "ds_read_b64_tr_b16 a[128:129], v184 offset:49152\n\t" \
  "v_mfma_f32_16x16x32_bf16 v[32:35], a[0:3], a[52:55], v[32:35]\n\t" \
  "ds_read_b64_tr_b16 a[130:131], v184 offset:50176\n\t" \
  "v_mfma_f32_16x16x32_bf16 v[32:35], a[160:163], a[36:39], v[32:35]\n\t" \
  "ds_read_b64_tr_b16 a[132:133], v185 offset:49152\n\t" \
  "v_mfma_f32_16x16x32_bf16 v[64:67], a[160:163], a[40:43], v[64:67]\n\t" \
  "v_mfma_f32_16x16x32_bf16 v[64:67], a[0:3], a[56:59], v[64:67]\n\t" \
  "s_add_u32 s97, s97, %[sox]\n\t" \
  "ds_read_b64_tr_b16 a[134:135], v185 offset:50176\n\t" \
  "v_mfma_f32_16x16x32_bf16 v[96:99], a[0:3], a[60:63], v[96:99]\n\t" \
  "ds_read_b64_tr_b16 a[136:137], v186 offset:49152\n\t" \
  "v_mfma_f32_16x16x32_bf16 v[96:99], a[160:163], a[44:47], v[96:99]\n\t" \
  "v_mfma_f32_16x16x32_bf16 v[4:7], a[164:167], a[32:35], v[4:7]\n\t" \
  "ds_read_b64_tr_b16 a[138:139], v186 offset:50176\n\t" \
  "v_mfma_f32_16x16x32_bf16 v[4:7], a[4:7], a[48:51], v[4:7]\n\t" \
  "ds_read_b64_tr_b16 a[140:141], v187 offset:49152\n\t" \
  "v_mfma_f32_16x16x32_bf16 v[36:39], a[4:7], a[52:55], v[36:39]\n\t" \
  "buffer_load_dwordx4 a[224:227], v248, %[ws], s97 offen sc1\n\t" \
  "v_mfma_f32_16x16x32_bf16 v[36:39], a[164:167], a[36:39], v[36:39]\n\t" \
  "ds_read_b64_tr_b16 a[142:143], v187 offset:50176\n\t" \
  "v_mfma_f32_16x16x32_bf16 v[68:71], a[164:167], a[40:43], v[68:71]\n\t" \
  "ds_read_b64_tr_b16 a[144:145], v188 offset:49152\n\t" \
  "v_mfma_f32_16x16x32_bf16 v[68:71], a[4:7], a[56:59], v[68:71]\n\t" \
  "v_mfma_f32_16x16x32_bf16 v[100:103], a[4:7], a[60:63], v[100:103]\n\t" \
  "ds_read_b64_tr_b16 a[146:147], v188 offset:50176\n\t" \
  "v_mfma_f32_16x16x32_bf16 v[100:103], a[164:167], a[44:47], v[100:103]\n\t" \
  "buffer_load_dwordx4 a[228:231], v249, %[ws], s97 offen sc1\n\t" \
  "ds_read_b64_tr_b16 a[148:149], v189 offset:49152\n\t" \
  "v_mfma_f32_16x16x32_bf16 v[8:11], a[168:171], a[32:35], v[8:11]\n\t" \
  "v_mfma_f32_16x16x32_bf16 v[8:11], a[8:11], a[48:51], v[8:11]\n\t" \
  "ds_read_b64_tr_b16 a[150:151], v189 offset:50176\n\t" \
  "v_mfma_f32_16x16x32_bf16 v[40:43], a[8:11], a[52:55], v[40:43]\n\t" \
  "ds_read_b64_tr_b16 a[152:153], v190 offset:49152\n\t" \
  "v_mfma_f32_16x16x32_bf16 v[40:43], a[168:171], a[36:39], v[40:43]\n\t" \
  "v_mfma_f32_16x16x32_bf16 v[72:75], a[168:171], a[40:43], v[72:75]\n\t" \
  "buffer_load_dwordx4 a[232:235], v250, %[ws], s97 offen sc1\n\t" \
  "ds_read_b64_tr_b16 a[154:155], v190 offset:50176\n\t" \
  "v_mfma_f32_16x16x32_bf16 v[72:75], a[8:11], a[56:59], v[72:75]\n\t" \
  "ds_read_b64_tr_b16 a[156:157], v191 offset:49152\n\t" \
  "v_mfma_f32_16x16x32_bf16 v[104:107], a[8:11], a[60:63], v[104:107]\n\t" \
  "v_mfma_f32_16x16x32_bf16 v[104:107], a[168:171], a[44:47], v[104:107]\n\t" \
  "ds_read_b64_tr_b16 a[158:159], v191 offset:50176\n\t" \
  "v_mfma_f32_16x16x32_bf16 v[12:15], a[172:175], a[32:35], v[12:15]\n\t" \
  "ds_read_b64_tr_b16 a[64:65], v184 offset:57344\n\t" \
  "v_mfma_f32_16x16x32_bf16 v[12:15], a[12:15], a[48:51], v[12:15]\n\t" \
  "buffer_load_dwordx4 a[236:239], v251, %[ws], s97 offen sc1\n\t" \
  "v_mfma_f32_16x16x32_bf16 v[44:47], a[12:15], a[52:55], v[44:47]\n\t" \
  "ds_read_b64_tr_b16 a[66:67], v184 offset:58368\n\t" \
  "v_mfma_f32_16x16x32_bf16 v[44:47], a[172:175], a[36:39], v[44:47]\n\t" \
  "ds_read_b64_tr_b16 a[68:69], v185 offset:57344\n\t" \
  "v_mfma_f32_16x16x32_bf16 v[76:79], a[172:175], a[40:43], v[76:79]\n\t" \
  "v_mfma_f32_16x16x32_bf16 v[76:79], a[12:15], a[56:59], v[76:79]\n\t" \
  "ds_read_b64_tr_b16 a[70:71], v185 offset:58368\n\t" \
  "v_mfma_f32_16x16x32_bf16 v[108:111], a[12:15], a[60:63], v[108:111]\n\t" \
  "ds_read_b64_tr_b16 a[72:73], v186 offset:57344\n\t" \
  "v_mfma_f32_16x16x32_bf16 v[108:111], a[172:175], a[44:47], v[108:111]\n\t" \
  "s_add_u32 s97, s97, %[dk]\n\t" \
  "v_mfma_f32_16x16x32_bf16 v[16:19], a[176:179], a[32:35], v[16:19]\n\t" \
  "ds_read_b64_tr_b16 a[74:75], v186 offset:58368\n\t" \
  "v_mfma_f32_16x16x32_bf16 v[16:19], a[16:19], a[48:51], v[16:19]\n\t" \
  "ds_read_b64_tr_b16 a[76:77], v187 offset:57344\n\t" \
  "v_mfma_f32_16x16x32_bf16 v[48:51], a[16:19], a[52:55], v[48:51]\n\t" \
  "v_mfma_f32_16x16x32_bf16 v[48:51], a[176:179], a[36:39], v[48:51]\n\t" \
  "ds_read_b64_tr_b16 a[78:79], v187 offset:58368\n\t" \
  "v_mfma_f32_16x16x32_bf16 v[80:83], a[176:179], a[40:43], v[80:83]\n\t" \
  "buffer_load_dwordx4 a[240:243], v248, %[ws], s97 offen sc1\n\t" \
  "ds_read_b64_tr_b16 a[80:81], v188 offset:57344\n\t" \
  "v_mfma_f32_16x16x32_bf16 v[80:83], a[16:19], a[56:59], v[80:83]\n\t" \
  "v_mfma_f32_16x16x32_bf16 v[112:115], a[16:19], a[60:63], v[112:115]\n\t" \
  "ds_read_b64_tr_b16 a[82:83], v188 offset:58368\n\t" \
  "v_mfma_f32_16x16x32_bf16 v[112:115], a[176:179], a[44:47], v[112:115]\n\t" \
  "ds_read_b64_tr_b16 a[84:85], v189 offset:57344\n\t" \
  "v_mfma_f32_16x16x32_bf16 v[20:23], a[180:183], a[32:35], v[20:23]\n\t" \
  "v_mfma_f32_16x16x32_bf16 v[20:23], a[20:23], a[48:51], v[20:23]\n\t" \
  "buffer_load_dwordx4 a[244:247], v249, %[ws], s97 offen sc1\n\t" \
  "ds_read_b64_tr_b16 a[86:87], v189 offset:58368\n\t" \
  "v_mfma_f32_16x16x32_bf16 v[52:55], a[20:23], a[52:55], v[52:55]\n\t" \
  "ds_read_b64_tr_b16 a[88:89], v190 offset:57344\n\t" \
  "v_mfma_f32_16x16x32_bf16 v[52:55], a[180:183], a[36:39], v[52:55]\n\t" \
  "v_mfma_f32_16x16x32_bf16 v[84:87], a[180:183], a[40:43], v[84:87]\n\t" \
  "ds_read_b64_tr_b16 a[90:91], v190 offset:58368\n\t" \
  "v_mfma_f32_16x16x32_bf16 v[84:87], a[20:23], a[56:59], v[84:87]\n\t" \
  "ds_read_b64_tr_b16 a[92:93], v191 offset:57344\n\t" \
  "v_mfma_f32_16x16x32_bf16 v[116:119], a[20:23], a[60:63], v[116:119]\n\t" \
  "buffer_load_dwordx4 a[248:251], v250, %[ws], s97 offen sc1\n\t" \
  "v_mfma_f32_16x16x32_bf16 v[116:119], a[180:183], a[44:47], v[116:119]\n\t" \
  "ds_read_b64_tr_b16 a[94:95], v191 offset:58368\n\t" \
  "v_mfma_f32_16x16x32_bf16 v[24:27], a[184:187], a[32:35], v[24:27]\n\t" \
  "v_mfma_f32_16x16x32_bf16 v[24:27], a[24:27], a[48:51], v[24:27]\n\t" \
  "v_mfma_f32_16x16x32_bf16 v[56:59], a[24:27], a[52:55], v[56:59]\n\t" \
  "v_mfma_f32_16x16x32_bf16 v[56:59], a[184:187], a[36:39], v[56:59]\n\t" \
  "buffer_load_dwordx4 a[252:255], v251, %[ws], s97 offen sc1\n\t" \
  "v_mfma_f32_16x16x32_bf16 v[88:91], a[184:187], a[40:43], v[88:91]\n\t" \
  "v_mfma_f32_16x16x32_bf16 v[88:91], a[24:27], a[56:59], v[88:91]\n\t" \
  "v_mfma_f32_16x16x32_bf16 v[120:123], a[24:27], a[60:63], v[120:123]\n\t" \
  "v_mfma_f32_16x16x32_bf16 v[120:123], a[184:187], a[44:47], v[120:123]\n\t" \
  "v_mfma_f32_16x16x32_bf16 v[28:31], a[188:191], a[32:35], v[28:31]\n\t" \
  "s_add_u32 s99, %[pf4], 0\n\t" \
  "buffer_load_dword %[pf], v245, %[sG], s99 offen\n\t" \
  "v_mfma_f32_16x16x32_bf16 v[28:31], a[28:31], a[48:51], v[28:31]\n\t" \
  "v_mfma_f32_16x16x32_bf16 v[60:63], a[28:31], a[52:55], v[60:63]\n\t" \
  "v_mfma_f32_16x16x32_bf16 v[60:63], a[188:191], a[36:39], v[60:63]\n\t" \
  "v_mfma_f32_16x16x32_bf16 v[92:95], a[188:191], a[40:43], v[92:95]\n\t" \
  "v_mfma_f32_16x16x32_bf16 v[92:95], a[28:31], a[56:59], v[92:95]\n\t" \
  "v_mfma_f32_16x16x32_bf16 v[124:127], a[28:31], a[60:63], v[124:127]\n\t" \
  "v_mfma_f32_16x16x32_bf16 v[124:127], a[188:191], a[44:47], v[124:127]\n\t" \
  "s_waitcnt vmcnt(22) lgkmcnt(0)\n\t" \
  "s_barrier\n\t" \
  "v_mfma_f32_16x16x32_bf16 v[0:3], a[128:131], a[96:99], v[0:3]\n\t" \
  "s_add_u32 s99, %[pf4], 14336\n\t" \
  "buffer_load_dword %[pf], v245, %[sG], s99 offen\n\t" \
  "v_mfma_f32_16x16x32_bf16 v[0:3], a[64:67], a[112:115], v[0:3]\n\t" \
  "ds_read_b64_tr_b16 a[160:161], v184 offset:0\n\t" \
  "v_mfma_f32_16x16x32_bf16 v[32:35], a[64:67], a[116:119], v[32:35]\n\t" \
  "ds_read_b64_tr_b16 a[162:163], v184 offset:1024\n\t" \
  "v_mfma_f32_16x16x32_bf16 v[32:35], a[128:131], a[100:103], v[32:35]\n\t" \
  "ds_read_b64_tr_b16 a[164:165], v185 offset:0\n\t" \
  "v_mfma_f32_16x16x32_bf16 v[64:67], a[128:131], a[104:107], v[64:67]\n\t" \
  "v_mfma_f32_16x16x32_bf16 v[64:67], a[64:67], a[120:123], v[64:67]\n\t" \
  "ds_read_b64_tr_b16 a[166:167], v185 offset:1024\n\t" \
  "v_mfma_f32_16x16x32_bf16 v[96:99], a[64:67], a[124:127], v[96:99]\n\t" \
  "s_add_u32 m0, %[mw], 16384\n\t" \
  "s_add_u32 s96, %[sofk], 0\n\t" \
  "v_mfma_f32_16x16x32_bf16 v[96:99], a[128:131], a[108:111], v[96:99]\n\t" \
  "ds_read_b64_tr_b16 a[168:169], v186 offset:0\n\t" \
  "v_mfma_f32_16x16x32_bf16 v[4:7], a[132:135], a[96:99], v[4:7]\n\t" \
  "ds_read_b64_tr_b16 a[170:171], v186 offset:1024\n\t" \
  "v_mfma_f32_16x16x32_bf16 v[4:7], a[68:71], a[112:115], v[4:7]\n\t" \
  "ds_read_b64_tr_b16 a[172:173], v187 offset:0\n\t" \
  "v_mfma_f32_16x16x32_bf16 v[36:39], a[68:71], a[116:119], v[36:39]\n\t" \
  "v_mfma_f32_16x16x32_bf16 v[36:39], a[132:135], a[100:103], v[36:39]\n\t" \
  "ds_read_b64_tr_b16 a[174:175], v187 offset:1024\n\t" \
  "v_mfma_f32_16x16x32_bf16 v[68:71], a[132:135], a[104:107], v[68:71]\n\t" \
  "ds_read_b64_tr_b16 a[176:177], v188 offset:0\n\t" \
  "v_mfma_f32_16x16x32_bf16 v[68:71], a[68:71], a[120:123], v[68:71]\n\t" \
  "buffer_load_dwordx4 v224, %[sK], s96 offen offset:0 lds\n\t" \
  "v_mfma_f32_16x16x32_bf16 v[100:103], a[68:71], a[124:127], v[100:103]\n\t" \
  "ds_read_b64_tr_b16 a[178:179], v188 offset:1024\n\t" \
  "v_mfma_f32_16x16x32_bf16 v[100:103], a[132:135], a[108:111], v[100:103]\n\t" \
  "ds_read_b64_tr_b16 a[180:181], v189 offset:0\n\t" \
  "v_mfma_f32_16x16x32_bf16 v[8:11], a[136:139], a[96:99], v[8:11]\n\t" \
  "v_mfma_f32_16x16x32_bf16 v[8:11], a[72:75], a[112:115], v[8:11]\n\t" \
  "ds_read_b64_tr_b16 a[182:183], v189 offset:1024\n\t" \
  "v_mfma_f32_16x16x32_bf16 v[40:43], a[72:75], a[116:119], v[40:43]\n\t" \
  "ds_read_b64_tr_b16 a[184:185], v190 offset:0\n\t" \
  "v_mfma_f32_16x16x32_bf16 v[40:43], a[136:139], a[100:103], v[40:43]\n\t" \
  "v_mfma_f32_16x16x32_bf16 v[72:75], a[136:139], a[104:107], v[72:75]\n\t" \
  "buffer_load_dwordx4 v225, %[sK], s96 offen offset:1024 lds\n\t" \
  "ds_read_b64_tr_b16 a[186:187], v190 offset:1024\n\t" \
  "v_mfma_f32_16x16x32_bf16 v[72:75], a[72:75], a[120:123], v[72:75]\n\t" \
  "ds_read_b64_tr_b16 a[188:189], v191 offset:0\n\t" \
  "v_mfma_f32_16x16x32_bf16 v[104:107], a[72:75], a[124:127], v[104:107]\n\t" \
  "v_mfma_f32_16x16x32_bf16 v[104:107], a[136:139], a[108:111], v[104:107]\n\t" \
  "ds_read_b64_tr_b16 a[190:191], v191 offset:1024\n\t" \
  "v_mfma_f32_16x16x32_bf16 v[12:15], a[140:143], a[96:99], v[12:15]\n\t" \
  "ds_read_b64_tr_b16 a[0:1], v184 offset:8192\n\t" \
  "v_mfma_f32_16x16x32_bf16 v[12:15], a[76:79], a[112:115], v[12:15]\n\t" \
  "v_mfma_f32_16x16x32_bf16 v[44:47], a[76:79], a[116:119], v[44:47]\n\t" \
  "ds_read_b64_tr_b16 a[2:3], v184 offset:9216\n\t" \
  "v_mfma_f32_16x16x32_bf16 v[44:47], a[140:143], a[100:103], v[44:47]\n\t" \
  "buffer_load_dwordx4 v226, %[sK], s96 offen offset:2048 lds\n\t" \
  "ds_read_b64_tr_b16 a[4:5], v185 offset:8192\n\t" \
  "v_mfma_f32_16x16x32_bf16 v[76:79], a[140:143], a[104:107], v[76:79]\n\t" \
  "v_mfma_f32_16x16x32_bf16 v[76:79], a[76:79], a[120:123], v[76:79]\n\t" \
  "ds_read_b64_tr_b16 a[6:7], v185 offset:9216\n\t" \
  "v_mfma_f32_16x16x32_bf16 v[108:111], a[76:79], a[124:127], v[108:111]\n\t" \
  "ds_read_b64_tr_b16 a[8:9], v186 offset:8192\n\t" \
  "v_mfma_f32_16x16x32_bf16 v[108:111], a[140:143], a[108:111], v[108:111]\n\t" \
  "v_mfma_f32_16x16x32_bf16 v[16:19], a[144:147], a[96:99], v[16:19]\n\t" \
  "ds_read_b64_tr_b16 a[10:11], v186 offset:9216\n\t" \
  "v_mfma_f32_16x16x32_bf16 v[16:19], a[80:83], a[112:115], v[16:19]\n\t" \
  "ds_read_b64_tr_b16 a[12:13], v187 offset:8192\n\t" \
  "v_mfma_f32_16x16x32_bf16 v[48:51], a[80:83], a[116:119], v[48:51]\n\t" \
  "buffer_load_dwordx4 v227, %[sK], s96 offen offset:3072 lds\n\t" \
  "v_mfma_f32_16x16x32_bf16 v[48:51], a[144:147], a[100:103], v[48:51]\n\t" \
  "ds_read_b64_tr_b16 a[14:15], v187 offset:9216\n\t" \
  "v_mfma_f32_16x16x32_bf16 v[80:83], a[144:147], a[104:107], v[80:83]\n\t" \
  "ds_read_b64_tr_b16 a[16:17], v188 offset:8192\n\t" \
  "v_mfma_f32_16x16x32_bf16 v[80:83], a[80:83], a[120:123], v[80:83]\n\t" \
  "v_mfma_f32_16x16x32_bf16 v[112:115], a[80:83], a[124:127], v[112:115]\n\t" \
  "ds_read_b64_tr_b16 a[18:19], v188 offset:9216\n\t" \
  "v_mfma_f32_16x16x32_bf16 v[112:115], a[144:147], a[108:111], v[112:115]\n\t" \
  "ds_read_b64_tr_b16 a[20:21], v189 offset:8192\n\t" \
  "v_mfma_f32_16x16x32_bf16 v[20:23], a[148:151], a[96:99], v[20:23]\n\t" \
  "v_mfma_f32_16x16x32_bf16 v[20:23], a[84:87], a[112:115], v[20:23]\n\t" \
  "s_add_u32 m0, %[mw], 49152\n\t" \
  "s_add_u32 s96, %[sofk], 0\n\t" \
  "v_mfma_f32_16x16x32_bf16 v[52:55], a[84:87], a[116:119], v[52:55]\n\t" \
  "ds_read_b64_tr_b16 a[22:23], v189 offset:9216\n\t" \
  "v_mfma_f32_16x16x32_bf16 v[52:55], a[148:151], a[100:103], v[52:55]\n\t" \
  "ds_read_b64_tr_b16 a[24:25], v190 offset:8192\n\t" \
  "v_mfma_f32_16x16x32_bf16 v[84:87], a[148:151], a[104:107], v[84:87]\n\t" \
  "ds_read_b64_tr_b16 a[26:27], v190 offset:9216\n\t" \
  "v_mfma_f32_16x16x32_bf16 v[84:87], a[84:87], a[120:123], v[84:87]\n\t" \
  "ds_read_b64_tr_b16 a[28:29], v191 offset:8192\n\t" \
  "v_mfma_f32_16x16x32_bf16 v[116:119], a[84:87], a[124:127], v[116:119]\n\t" \
  "v_mfma_f32_16x16x32_bf16 v[116:119], a[148:151], a[108:111], v[116:119]\n\t" \
  "ds_read_b64_tr_b16 a[30:31], v191 offset:9216\n\t" \
  "v_mfma_f32_16x16x32_bf16 v[24:27], a[152:155], a[96:99], v[24:27]\n\t" \
  "buffer_load_dwordx4 v230, %[sV], s96 offen offset:0 lds\n\t" \
  "v_mfma_f32_16x16x32_bf16 v[24:27], a[88:91], a[112:115], v[24:27]\n\t" \
  "v_mfma_f32_16x16x32_bf16 v[56:59], a[88:91], a[116:119], v[56:59]\n\t" \
  "v_mfma_f32_16x16x32_bf16 v[56:59], a[152:155], a[100:103], v[56:59]\n\t" \
  "v_mfma_f32_16x16x32_bf16 v[88:91], a[152:155], a[104:107], v[88:91]\n\t" \
  "v_mfma_f32_16x16x32_bf16 v[88:91], a[88:91], a[120:123], v[88:91]\n\t" \
  "v_mfma_f32_16x16x32_bf16 v[120:123], a[88:91], a[124:127], v[120:123]\n\t" \
  "v_mfma_f32_16x16x32_bf16 v[120:123], a[152:155], a[108:111], v[120:123]\n\t" \
  "buffer_load_dwordx4 v231, %[sV], s96 offen offset:1024 lds\n\t" \
  "v_mfma_f32_16x16x32_bf16 v[28:31], a[156:159], a[96:99], v[28:31]\n\t" \
  "v_mfma_f32_16x16x32_bf16 v[28:31], a[92:95], a[112:115], v[28:31]\n\t" \
  "v_mfma_f32_16x16x32_bf16 v[60:63], a[92:95], a[116:119], v[60:63]\n\t" \
  "v_mfma_f32_16x16x32_bf16 v[60:63], a[156:159], a[100:103], v[60:63]\n\t" \
  "v_mfma_f32_16x16x32_bf16 v[92:95], a[156:159], a[104:107], v[92:95]\n\t" \
  "v_mfma_f32_16x16x32_bf16 v[92:95], a[92:95], a[120:123], v[92:95]\n\t" \
  "v_mfma_f32_16x16x32_bf16 v[124:127], a[92:95], a[124:127], v[124:127]\n\t" \
  "v_mfma_f32_16x16x32_bf16 v[124:127], a[156:159], a[108:111], v[124:127]\n\t" \
  "s_waitcnt vmcnt(16) lgkmcnt(0)\n\t" \
  "s_barrier\n\t" \
  "v_mfma_f32_16x16x32_bf16 v[0:3], a[160:163], a[192:195], v[0:3]\n\t" \
  "buffer_load_dwordx4 v230, %[sV], s96 offen offset:2048 lds\n\t" \
  "ds_read_b64_tr_b16 a[128:129], v192 offset:0\n\t" \
  "v_mfma_f32_16x16x32_bf16 v[0:3], a[0:3], a[208:211], v[0:3]\n\t" \
  "v_mfma_f32_16x16x32_bf16 v[32:35], a[0:3], a[212:215], v[32:35]\n\t" \
  "ds_read_b64_tr_b16 a[130:131], v192 offset:1024\n\t" \
  "v_mfma_f32_16x16x32_bf16 v[32:35], a[160:163], a[196:199], v[32:35]\n\t" \
  "ds_read_b64_tr_b16 a[132:133], v193 offset:0\n\t" \
  "v_mfma_f32_16x16x32_bf16 v[64:67], a[160:163], a[200:203], v[64:67]\n\t" \
  "v_mfma_f32_16x16x32_bf16 v[64:67], a[0:3], a[216:219], v[64:67]\n\t" \
  "ds_read_b64_tr_b16 a[134:135], v193 offset:1024\n\t" \
  "v_mfma_f32_16x16x32_bf16 v[96:99], a[0:3], a[220:223], v[96:99]\n\t" \
  "buffer_load_dwordx4 v231, %[sV], s96 offen offset:3072 lds\n\t" \
  "ds_read_b64_tr_b16 a[136:137], v194 offset:0\n\t" \
  "v_mfma_f32_16x16x32_bf16 v[96:99], a[160:163], a[204:207], v[96:99]\n\t" \
  "v_mfma_f32_16x16x32_bf16 v[4:7], a[164:167], a[192:195], v[4:7]\n\t" \
  "ds_read_b64_tr_b16 a[138:139], v194 offset:1024\n\t" \
  "v_mfma_f32_16x16x32_bf16 v[4:7], a[4:7], a[208:211], v[4:7]\n\t" \
  "ds_read_b64_tr_b16 a[140:141], v195 offset:0\n\t" \
  "v_mfma_f32_16x16x32_bf16 v[36:39], a[4:7], a[212:215], v[36:39]\n\t" \
  "v_mfma_f32_16x16x32_bf16 v[36:39], a[164:167], a[196:199], v[36:39]\n\t" \
  "ds_read_b64_tr_b16 a[142:143], v195 offset:1024\n\t" \
  "v_mfma_f32_16x16x32_bf16 v[68:71], a[164:167], a[200:203], v[68:71]\n\t" \
  "ds_read_b64_tr_b16 a[144:145], v196 offset:0\n\t" \
  "v_mfma_f32_16x16x32_bf16 v[68:71], a[4:7], a[216:219], v[68:71]\n\t" \
  "s_add_u32 m0, %[mw], 32768\n\t" \
  "s_add_u32 s96, %[sofk], 16384\n\t" \
  "v_mfma_f32_16x16x32_bf16 v[100:103], a[4:7], a[220:223], v[100:103]\n\t" \
  "ds_read_b64_tr_b16 a[146:147], v196 offset:1024\n\t" \
  "v_mfma_f32_16x16x32_bf16 v[100:103], a[164:167], a[204:207], v[100:103]\n\t" \
  "ds_read_b64_tr_b16 a[148:149], v197 offset:0\n\t" \
  "v_mfma_f32_16x16x32_bf16 v[8:11], a[168:171], a[192:195], v[8:11]\n\t" \
  "v_mfma_f32_16x16x32_bf16 v[8:11], a[8:11], a[208:211], v[8:11]\n\t" \
  "ds_read_b64_tr_b16 a[150:151], v197 offset:1024\n\t" \
  "v_mfma_f32_16x16x32_bf16 v[40:43], a[8:11], a[212:215], v[40:43]\n\t" \
  "ds_read_b64_tr_b16 a[152:153], v198 offset:0\n\t" \
  "v_mfma_f32_16x16x32_bf16 v[40:43], a[168:171], a[196:199], v[40:43]\n\t" \
  "v_mfma_f32_16x16x32_bf16 v[72:75], a[168:171], a[200:203], v[72:75]\n\t" \
  "buffer_load_dwordx4 v224, %[sK], s96 offen offset:0 lds\n\t" \
  "ds_read_b64_tr_b16 a[154:155], v198 offset:1024\n\t" \
  "v_mfma_f32_16x16x32_bf16 v[72:75], a[8:11], a[216:219], v[72:75]\n\t" \
  "ds_read_b64_tr_b16 a[156:157], v199 offset:0\n\t" \
  "v_mfma_f32_16x16x32_bf16 v[104:107], a[8:11], a[220:223], v[104:107]\n\t" \
  "v_mfma_f32_16x16x32_bf16 v[104:107], a[168:171], a[204:207], v[104:107]\n\t" \
  "ds_read_b64_tr_b16 a[158:159], v199 offset:1024\n\t" \
  "v_mfma_f32_16x16x32_bf16 v[12:15], a[172:175], a[192:195], v[12:15]\n\t" \
  "ds_read_b64_tr_b16 a[64:65], v192 offset:8192\n\t" \
  "v_mfma_f32_16x16x32_bf16 v[12:15], a[12:15], a[208:211], v[12:15]\n\t" \
  "v_mfma_f32_16x16x32_bf16 v[44:47], a[12:15], a[212:215], v[44:47]\n\t" \
  "ds_read_b64_tr_b16 a[66:67], v192 offset:9216\n\t" \
  "v_mfma_f32_16x16x32_bf16 v[44:47], a[172:175], a[196:199], v[44:47]\n\t" \
  "buffer_load_dwordx4 v225, %[sK], s96 offen offset:1024 lds\n\t" \
  "ds_read_b64_tr_b16 a[68:69], v193 offset:8192\n\t" \
  "v_mfma_f32_16x16x32_bf16 v[76:79], a[172:175], a[200:203], v[76:79]\n\t" \
  "v_mfma_f32_16x16x32_bf16 v[76:79], a[12:15], a[216:219], v[76:79]\n\t" \
  "ds_read_b64_tr_b16 a[70:71], v193 offset:9216\n\t" \
  "v_mfma_f32_16x16x32_bf16 v[108:111], a[12:15], a[220:223], v[108:111]\n\t" \
  "ds_read_b64_tr_b16 a[72:73], v194 offset:8192\n\t" \
  "v_mfma_f32_16x16x32_bf16 v[108:111], a[172:175], a[204:207], v[108:111]\n\t" \
  "v_mfma_f32_16x16x32_bf16 v[16:19], a[176:179], a[192:195], v[16:19]\n\t" \
  "ds_read_b64_tr_b16 a[74:75], v194 offset:9216\n\t" \
  "v_mfma_f32_16x16x32_bf16 v[16:19], a[16:19], a[208:211], v[16:19]\n\t" \
  "ds_read_b64_tr_b16 a[76:77], v195 offset:8192\n\t" \
  "v_mfma_f32_16x16x32_bf16 v[48:51], a[16:19], a[212:215], v[48:51]\n\t" \
  "buffer_load_dwordx4 v226, %[sK], s96 offen offset:2048 lds\n\t" \
  "v_mfma_f32_16x16x32_bf16 v[48:51], a[176:179], a[196:199], v[48:51]\n\t" \
  "ds_read_b64_tr_b16 a[78:79], v195 offset:9216\n\t" \
  "v_mfma_f32_16x16x32_bf16 v[80:83], a[176:179], a[200:203], v[80:83]\n\t" \
  "ds_read_b64_tr_b16 a[80:81], v196 offset:8192\n\t" \
  "v_mfma_f32_16x16x32_bf16 v[80:83], a[16:19], a[216:219], v[80:83]\n\t" \
  "v_mfma_f32_16x16x32_bf16 v[112:115], a[16:19], a[220:223], v[112:115]\n\t" \
  "ds_read_b64_tr_b16 a[82:83], v196 offset:9216\n\t" \
  "v_mfma_f32_16x16x32_bf16 v[112:115], a[176:179], a[204:207], v[112:115]\n\t" \
  "ds_read_b64_tr_b16 a[84:85], v197 offset:8192\n\t" \
  "v_mfma_f32_16x16x32_bf16 v[20:23], a[180:183], a[192:195], v[20:23]\n\t" \
  "v_mfma_f32_16x16x32_bf16 v[20:23], a[20:23], a[208:211], v[20:23]\n\t" \
  "buffer_load_dwordx4 v227, %[sK], s96 offen offset:3072 lds\n\t" \
  "ds_read_b64_tr_b16 a[86:87], v197 offset:9216\n\t" \
  "v_mfma_f32_16x16x32_bf16 v[52:55], a[20:23], a[212:215], v[52:55]\n\t" \
  "ds_read_b64_tr_b16 a[88:89], v198 offset:8192\n\t" \
  "v_mfma_f32_16x16x32_bf16 v[52:55], a[180:183], a[196:199], v[52:55]\n\t" \
  "v_mfma_f32_16x16x32_bf16 v[84:87], a[180:183], a[200:203], v[84:87]\n\t" \
  "ds_read_b64_tr_b16 a[90:91], v198 offset:9216\n\t" \
  "v_mfma_f32_16x16x32_bf16 v[84:87], a[20:23], a[216:219], v[84:87]\n\t" \
  "ds_read_b64_tr_b16 a[92:93], v199 offset:8192\n\t" \
  "v_mfma_f32_16x16x32_bf16 v[116:119], a[20:23], a[220:223], v[116:119]\n\t" \
  "v_mfma_f32_16x16x32_bf16 v[116:119], a[180:183], a[204:207], v[116:119]\n\t" \
  "ds_read_b64_tr_b16 a[94:95], v199 offset:9216\n\t" \
  "v_mfma_f32_16x16x32_bf16 v[24:27], a[184:187], a[192:195], v[24:27]\n\t" \
  "s_add_u32 s99, %[pf4], 28672\n\t" \
  "buffer_load_dword %[pf], v245, %[sG], s99 offen\n\t" \
  "v_mfma_f32_16x16x32_bf16 v[24:27], a[24:27], a[208:211], v[24:27]\n\t" \
  "v_mfma_f32_16x16x32_bf16 v[56:59], a[24:27], a[212:215], v[56:59]\n\t" \
  "v_mfma_f32_16x16x32_bf16 v[56:59], a[184:187], a[196:199], v[56:59]\n\t" \
  "v_mfma_f32_16x16x32_bf16 v[88:91], a[184:187], a[200:203], v[88:91]\n\t" \
  "v_mfma_f32_16x16x32_bf16 v[88:91], a[24:27], a[216:219], v[88:91]\n\t" \
  "v_mfma_f32_16x16x32_bf16 v[120:123], a[24:27], a[220:223], v[120:123]\n\t" \
  "v_mfma_f32_16x16x32_bf16 v[120:123], a[184:187], a[204:207], v[120:123]\n\t" \
  "s_add_u32 s99, %[pf4], 43008\n\t" \
  "buffer_load_dword %[pf], v245, %[sG], s99 offen\n\t" \
  "v_mfma_f32_16x16x32_bf16 v[28:31], a[188:191], a[192:195], v[28:31]\n\t" \
  "v_mfma_f32_16x16x32_bf16 v[28:31], a[28:31], a[208:211], v[28:31]\n\t" \
  "v_mfma_f32_16x16x32_bf16 v[60:63], a[28:31], a[212:215], v[60:63]\n\t" \
  "v_mfma_f32_16x16x32_bf16 v[60:63], a[188:191], a[196:199], v[60:63]\n\t" \
  "v_mfma_f32_16x16x32_bf16 v[92:95], a[188:191], a[200:203], v[92:95]\n\t" \
  "v_mfma_f32_16x16x32_bf16 v[92:95], a[28:31], a[216:219], v[92:95]\n\t" \
  "v_mfma_f32_16x16x32_bf16 v[124:127], a[28:31], a[220:223], v[124:127]\n\t" \
  "v_mfma_f32_16x16x32_bf16 v[124:127], a[188:191], a[204:207], v[124:127]\n\t" \
  "s_waitcnt vmcnt(16)\n\t" \
  "s_waitcnt lgkmcnt(0)\n\t" \
  FSTP("s_waitcnt lgkmcnt(0)\n\t") \
  FSTP("v_writelane_b32 %[stv], s94, 52\n\t") \
  FSTP("v_writelane_b32 %[stv], s92, 53\n\t") \
  ".subsection 1\n\t" \
  ".Lfq_s:\n\t" \
  "s_mov_b32 s97, 0\n\t" \
  ".Lfq_r:\n\t" \
  "buffer_load_dword %[fl], v210, %[ws], %[fo] offen sc1\n\t" \
  "s_waitcnt vmcnt(0)\n\t" \
  "v_cmp_ne_u32 vcc, v211, %[fl]\n\t" \
  "s_cbranch_vccz .Lfq_k\n\t" \
  "s_add_u32 s97, s97, 1\n\t" \
  "s_cmp_lt_u32 s97, " FQ_CAP "\n\t" \
  "s_cbranch_scc1 .Lfq_r\n\t" \
  "buffer_store_dword v211, off, %[ws], 0 offset:256 sc1\n\t" \
  "s_branch .Lfq_k\n\t" \
  ".subsection 0\n\t"
#define FQ_CL "memory", "scc", "vcc", "s96", "s97", "s98", "s99", "v128", "v129", "v130", "v131", "v132", "v133", "v134", "v135", "v136", "v137", "v138", "v139", "v140", "v141", "v142", "v143", "v144", "v145", "v146", "v147", "v148", "v149", "v150", "v151", "v152", "v153", "v154", "v155", "v156", "v157", "v158", "v159", "v176", "v177", "v178", "v179", "v180", "v181", "v182", "v183", "v249", "v250", "v251" FQ_STC
#define FQ_X2E 0
#define FQ_W1L 1
#define FQ_W2E 0
static_assert(W_VOTE == 8192 && W_FLAGR == 32768 && W_FLAGO == 131072 && W_MODB == 2228224 && W_SIL == 2359296 && W_OT == 8388608 && W_XCH == 25165824);
#define FQ_EV 0
#define FQ_CTO
#define FQ_XD 1
#define STS(x)
#define AT_STV
#define STA(x)
#define AT_ASM \
  "s_cmp_eq_u32 %[pm], 0\n\t" \
  "s_cbranch_scc1 .Lsf_go\n\t" \
  "v_mfma_f32_16x16x32_bf16 v[28:31], a[156:159], a[224:227], v[28:31]\n\t" \
  "v_mov_b32 v216, 0x3f803f80\n\t" \
  "v_lshlrev_b32 v204, 7, v233\n\t" \
  "v_mfma_f32_16x16x32_bf16 v[28:31], a[92:95], a[240:243], v[28:31]\n\t" \
  "v_cmp_gt_u32 vcc, 16, v233\n\t" \
  "v_mov_b32 v217, 0x3f803f80\n\t" \
  "v_mfma_f32_16x16x32_bf16 v[24:27], a[152:155], a[224:227], v[24:27]\n\t" \
  "v_lshrrev_b32 v202, 3, v233\n\t" \
  "v_mov_b32 v218, 0x3f803f80\n\t" \
  "v_mfma_f32_16x16x32_bf16 v[24:27], a[88:91], a[240:243], v[24:27]\n\t" \
  "v_mul_u32_u24 v202, 11264, v202\n\t" \
  "v_mov_b32 v219, 0x3f803f80\n\t" \
  "v_mfma_f32_16x16x32_bf16 v[20:23], a[148:151], a[224:227], v[20:23]\n\t" \
  "v_cvt_pk_bf16_f32 v31, v30, v31\n\t" \
  "v_add_u32 v202, v202, v204\n\t" \
  "v_mfma_f32_16x16x32_bf16 v[20:23], a[84:87], a[240:243], v[20:23]\n\t" \
  "v_cvt_pk_bf16_f32 v30, v28, v29\n\t" \
  "v_lshlrev_b32 v242, 4, v233\n\t" \
  "v_mfma_f32_16x16x32_bf16 v[16:19], a[144:147], a[224:227], v[16:19]\n\t" \
  "s_mov_b32 s99, 0\n\t" \
  "v_bfrev_b32 v195, 1\n\t" \
  "v_cvt_pk_bf16_f32 v28, v24, v25\n\t" \
  "v_mfma_f32_16x16x32_bf16 v[16:19], a[80:83], a[240:243], v[16:19]\n\t" \
  "v_cvt_pk_bf16_f32 v29, v26, v27\n\t" \
  "v_add_u32 v237, 65536, v228\n\t" \
  "v_mfma_f32_16x16x32_bf16 v[12:15], a[140:143], a[224:227], v[12:15]\n\t" \
  "v_cndmask_b32 v204, v195, v204, vcc\n\t" \
  "v_cndmask_b32 v202, v195, v202, vcc\n\t" \
  "v_mfma_f32_16x16x32_bf16 v[12:15], a[76:79], a[240:243], v[12:15]\n\t" \
  "v_cvt_pk_bf16_f32 v27, v22, v23\n\t" \
  "v_add_u32 v238, 65536, v229\n\t" \
  "v_mfma_f32_16x16x32_bf16 v[8:11], a[136:139], a[224:227], v[8:11]\n\t" \
  "s_mov_b32 %[na], 0\n\t" \
  "v_permlane16_swap_b32 v28, v30\n\t" \
  "v_mfma_f32_16x16x32_bf16 v[8:11], a[72:75], a[240:243], v[8:11]\n\t" \
  "v_cvt_pk_bf16_f32 v26, v20, v21\n\t" \
  "v_cvt_pk_bf16_f32 v24, v16, v17\n\t" \
  "v_mfma_f32_16x16x32_bf16 v[4:7], a[132:135], a[224:227], v[4:7]\n\t" \
  "v_permlane16_swap_b32 v29, v31\n\t" \
  "v_mfma_f32_16x16x32_bf16 v[4:7], a[68:71], a[240:243], v[4:7]\n\t" \
  "v_cvt_pk_bf16_f32 v25, v18, v19\n\t" \
  "v_cvt_pk_bf16_f32 v23, v14, v15\n\t" \
  "v_mfma_f32_16x16x32_bf16 v[0:3], a[128:131], a[224:227], v[0:3]\n\t" \
  "v_cvt_pk_bf16_f32 v22, v12, v13\n\t" \
  "v_cvt_pk_bf16_f32 v20, v8, v9\n\t" \
  "v_mfma_f32_16x16x32_bf16 v[0:3], a[64:67], a[240:243], v[0:3]\n\t" \
  "s_waitcnt vmcnt(2)\n\t" \
  "s_barrier\n\t" \
  "v_mfma_f32_16x16x32_bf16 v[60:63], a[156:159], a[228:231], v[60:63]\n\t" \
  "ds_read_b128 a[0:3], v220 offset:0\n\t" \
  "v_cvt_pk_bf16_f32 v21, v10, v11\n\t" \
  "v_mfma_f32_16x16x32_bf16 v[60:63], a[92:95], a[244:247], v[60:63]\n\t" \
  "ds_read_b128 a[4:7], v221 offset:0\n\t" \
  "v_cvt_pk_bf16_f32 v19, v6, v7\n\t" \
  "v_mfma_f32_16x16x32_bf16 v[56:59], a[152:155], a[228:231], v[56:59]\n\t" \
  "ds_read_b128 a[8:11], v222 offset:0\n\t" \
  "v_cvt_pk_bf16_f32 v18, v4, v5\n\t" \
  "v_mfma_f32_16x16x32_bf16 v[56:59], a[88:91], a[244:247], v[56:59]\n\t" \
  "ds_read_b128 a[12:15], v223 offset:0\n\t" \
  "v_cvt_pk_bf16_f32 v16, v0, v1\n\t" \
  "v_mfma_f32_16x16x32_bf16 v[52:55], a[148:151], a[228:231], v[52:55]\n\t" \
  "ds_read_b128 a[16:19], v220 offset:4096\n\t" \
  "v_cvt_pk_bf16_f32 v17, v2, v3\n\t" \
  "v_mfma_f32_16x16x32_bf16 v[52:55], a[84:87], a[244:247], v[52:55]\n\t" \
  "ds_read_b128 a[20:23], v221 offset:4096\n\t" \
  "v_cvt_pk_bf16_f32 v63, v62, v63\n\t" \
  "v_mfma_f32_16x16x32_bf16 v[48:51], a[144:147], a[228:231], v[48:51]\n\t" \
  "ds_read_b128 a[24:27], v222 offset:4096\n\t" \
  "v_cvt_pk_bf16_f32 v62, v60, v61\n\t" \
  "v_mfma_f32_16x16x32_bf16 v[48:51], a[80:83], a[244:247], v[48:51]\n\t" \
  "ds_read_b128 a[28:31], v223 offset:4096\n\t" \
  "v_cvt_pk_bf16_f32 v60, v56, v57\n\t" \
  "v_mfma_f32_16x16x32_bf16 v[44:47], a[140:143], a[228:231], v[44:47]\n\t" \
  "ds_read_b128 a[32:35], v220 offset:8192\n\t" \
  "v_cvt_pk_bf16_f32 v61, v58, v59\n\t" \
  "v_mfma_f32_16x16x32_bf16 v[44:47], a[76:79], a[244:247], v[44:47]\n\t" \
  "ds_read_b128 a[36:39], v221 offset:8192\n\t" \
  "v_cvt_pk_bf16_f32 v59, v54, v55\n\t" \
  "v_mfma_f32_16x16x32_bf16 v[40:43], a[136:139], a[228:231], v[40:43]\n\t" \
  "ds_read_b128 a[40:43], v222 offset:8192\n\t" \
  "v_cvt_pk_bf16_f32 v58, v52, v53\n\t" \
  "v_mfma_f32_16x16x32_bf16 v[40:43], a[72:75], a[244:247], v[40:43]\n\t" \
  "ds_read_b128 a[44:47], v223 offset:8192\n\t" \
  "v_cvt_pk_bf16_f32 v56, v48, v49\n\t" \
  "v_mfma_f32_16x16x32_bf16 v[36:39], a[132:135], a[228:231], v[36:39]\n\t" \
  "ds_read_b128 a[48:51], v220 offset:12288\n\t" \
  "v_cvt_pk_bf16_f32 v57, v50, v51\n\t" \
  "v_mfma_f32_16x16x32_bf16 v[36:39], a[68:71], a[244:247], v[36:39]\n\t" \
  "s_add_u32 s98, %[pf4], 0\n\t" \
  "ds_read_b128 a[52:55], v221 offset:12288\n\t" \
  "v_cvt_pk_bf16_f32 v55, v46, v47\n\t" \
  "v_mfma_f32_16x16x32_bf16 v[32:35], a[128:131], a[228:231], v[32:35]\n\t" \
  "buffer_load_dword %[pfd], v245, %[sU], s98 offen\n\t" \
  "v_mfma_f32_16x16x32_bf16 v[32:35], a[64:67], a[244:247], v[32:35]\n\t" \
  "ds_read_b128 a[56:59], v222 offset:12288\n\t" \
  "v_cvt_pk_bf16_f32 v54, v44, v45\n\t" \
  "v_mfma_f32_16x16x32_bf16 v[92:95], a[156:159], a[232:235], v[92:95]\n\t" \
  "ds_read_b128 a[60:63], v223 offset:12288\n\t" \
  "v_cvt_pk_bf16_f32 v52, v40, v41\n\t" \
  "v_mfma_f32_16x16x32_bf16 v[92:95], a[92:95], a[248:251], v[92:95]\n\t" \
  "v_permlane16_swap_b32 v24, v26\n\t" \
  "v_mfma_f32_16x16x32_bf16 v[88:91], a[152:155], a[232:235], v[88:91]\n\t" \
  "v_cvt_pk_bf16_f32 v53, v42, v43\n\t" \
  "v_cvt_pk_bf16_f32 v51, v38, v39\n\t" \
  "v_mfma_f32_16x16x32_bf16 v[88:91], a[88:91], a[248:251], v[88:91]\n\t" \
  "v_permlane16_swap_b32 v25, v27\n\t" \
  "v_mfma_f32_16x16x32_bf16 v[84:87], a[148:151], a[232:235], v[84:87]\n\t" \
  "v_cvt_pk_bf16_f32 v50, v36, v37\n\t" \
  "v_cvt_pk_bf16_f32 v48, v32, v33\n\t" \
  "v_mfma_f32_16x16x32_bf16 v[84:87], a[84:87], a[248:251], v[84:87]\n\t" \
  "v_permlane16_swap_b32 v20, v22\n\t" \
  "v_mfma_f32_16x16x32_bf16 v[80:83], a[144:147], a[232:235], v[80:83]\n\t" \
  "v_cvt_pk_bf16_f32 v49, v34, v35\n\t" \
  "v_cvt_pk_bf16_f32 v95, v94, v95\n\t" \
  "v_mfma_f32_16x16x32_bf16 v[80:83], a[80:83], a[248:251], v[80:83]\n\t" \
  "v_permlane16_swap_b32 v21, v23\n\t" \
  "v_mfma_f32_16x16x32_bf16 v[76:79], a[140:143], a[232:235], v[76:79]\n\t" \
  "buffer_load_dword %[pfd], v247, %[ws], 0 offen sc1\n\t" \
  "v_mfma_f32_16x16x32_bf16 v[76:79], a[76:79], a[248:251], v[76:79]\n\t" \
  "v_cvt_pk_bf16_f32 v94, v92, v93\n\t" \
  "v_cvt_pk_bf16_f32 v92, v88, v89\n\t" \
  "v_mfma_f32_16x16x32_bf16 v[72:75], a[136:139], a[232:235], v[72:75]\n\t" \
  "v_permlane16_swap_b32 v16, v18\n\t" \
  "v_mfma_f32_16x16x32_bf16 v[72:75], a[72:75], a[248:251], v[72:75]\n\t" \
  "v_cvt_pk_bf16_f32 v93, v90, v91\n\t" \
  "v_cvt_pk_bf16_f32 v91, v86, v87\n\t" \
  "v_mfma_f32_16x16x32_bf16 v[68:71], a[132:135], a[232:235], v[68:71]\n\t" \
  "v_permlane16_swap_b32 v17, v19\n\t" \
  "v_mfma_f32_16x16x32_bf16 v[68:71], a[68:71], a[248:251], v[68:71]\n\t" \
  "v_cvt_pk_bf16_f32 v90, v84, v85\n\t" \
  "v_cvt_pk_bf16_f32 v88, v80, v81\n\t" \
  "v_mfma_f32_16x16x32_bf16 v[64:67], a[128:131], a[232:235], v[64:67]\n\t" \
  "v_permlane16_swap_b32 v60, v62\n\t" \
  "v_mfma_f32_16x16x32_bf16 v[64:67], a[64:67], a[248:251], v[64:67]\n\t" \
  "v_cvt_pk_bf16_f32 v89, v82, v83\n\t" \
  "v_cvt_pk_bf16_f32 v87, v78, v79\n\t" \
  "v_mfma_f32_16x16x32_bf16 v[124:127], a[156:159], a[236:239], v[124:127]\n\t" \
  "v_permlane16_swap_b32 v61, v63\n\t" \
  "v_mfma_f32_16x16x32_bf16 v[124:127], a[92:95], a[252:255], v[124:127]\n\t" \
  "v_cvt_pk_bf16_f32 v86, v76, v77\n\t" \
  "v_cvt_pk_bf16_f32 v84, v72, v73\n\t" \
  "v_mfma_f32_16x16x32_bf16 v[120:123], a[152:155], a[236:239], v[120:123]\n\t" \
  "v_permlane16_swap_b32 v56, v58\n\t" \
  "v_mfma_f32_16x16x32_bf16 v[120:123], a[88:91], a[252:255], v[120:123]\n\t" \
  "v_cvt_pk_bf16_f32 v85, v74, v75\n\t" \
  "v_cvt_pk_bf16_f32 v83, v70, v71\n\t" \
  "v_mfma_f32_16x16x32_bf16 v[116:119], a[148:151], a[236:239], v[116:119]\n\t" \
  "v_permlane16_swap_b32 v57, v59\n\t" \
  "v_mfma_f32_16x16x32_bf16 v[116:119], a[84:87], a[252:255], v[116:119]\n\t" \
  "v_cvt_pk_bf16_f32 v82, v68, v69\n\t" \
  "v_cvt_pk_bf16_f32 v80, v64, v65\n\t" \
  "v_mfma_f32_16x16x32_bf16 v[112:115], a[144:147], a[236:239], v[112:115]\n\t" \
  "v_permlane16_swap_b32 v52, v54\n\t" \
  "v_mfma_f32_16x16x32_bf16 v[112:115], a[80:83], a[252:255], v[112:115]\n\t" \
  "v_cvt_pk_bf16_f32 v81, v66, v67\n\t" \
  "v_cvt_pk_bf16_f32 v127, v126, v127\n\t" \
  "v_mfma_f32_16x16x32_bf16 v[108:111], a[140:143], a[236:239], v[108:111]\n\t" \
  "v_permlane16_swap_b32 v53, v55\n\t" \
  "v_mfma_f32_16x16x32_bf16 v[108:111], a[76:79], a[252:255], v[108:111]\n\t" \
  "v_cvt_pk_bf16_f32 v126, v124, v125\n\t" \
  "v_cvt_pk_bf16_f32 v124, v120, v121\n\t" \
  "v_mfma_f32_16x16x32_bf16 v[104:107], a[136:139], a[236:239], v[104:107]\n\t" \
  "v_permlane16_swap_b32 v48, v50\n\t" \
  "v_mfma_f32_16x16x32_bf16 v[104:107], a[72:75], a[252:255], v[104:107]\n\t" \
  "v_cvt_pk_bf16_f32 v125, v122, v123\n\t" \
  "v_cvt_pk_bf16_f32 v123, v118, v119\n\t" \
  "v_mfma_f32_16x16x32_bf16 v[100:103], a[132:135], a[236:239], v[100:103]\n\t" \
  "v_permlane16_swap_b32 v49, v51\n\t" \
  "v_mfma_f32_16x16x32_bf16 v[100:103], a[68:71], a[252:255], v[100:103]\n\t" \
  "v_cvt_pk_bf16_f32 v122, v116, v117\n\t" \
  "v_cvt_pk_bf16_f32 v120, v112, v113\n\t" \
  "v_mfma_f32_16x16x32_bf16 v[96:99], a[128:131], a[236:239], v[96:99]\n\t" \
  "v_permlane16_swap_b32 v92, v94\n\t" \
  "v_mfma_f32_16x16x32_bf16 v[96:99], a[64:67], a[252:255], v[96:99]\n\t" \
  "s_waitcnt lgkmcnt(0)\n\t" \
  "s_barrier\n\t" \
  STS("s_memrealtime s[94:95]\n\t") \
  STS("s_memtime s[92:93]\n\t") \
  "v_mfma_f32_16x16x32_bf16 v[0:3], a[0:3], v[16:19], 0\n\t" \
  "s_add_u32 m0, %[m], 49152\n\t" \
  "ds_read_b128 a[192:195], v220 offset:16384\n\t" \
  "v_cvt_pk_bf16_f32 v121, v114, v115\n\t" \
  "v_mfma_f32_16x16x32_bf16 v[0:3], a[4:7], v[20:23], v[0:3]\n\t" \
  "s_add_u32 s96, %[sofk], 32768\n\t" \
  "ds_read_b128 a[196:199], v221 offset:16384\n\t" \
  "v_cvt_pk_bf16_f32 v119, v110, v111\n\t" \
  "v_mfma_f32_16x16x32_bf16 v[0:3], a[8:11], v[24:27], v[0:3]\n\t" \
  "ds_read_b128 a[200:203], v222 offset:16384\n\t" \
  "v_cvt_pk_bf16_f32 v118, v108, v109\n\t" \
  "v_mfma_f32_16x16x32_bf16 v[0:3], a[12:15], v[28:31], v[0:3]\n\t" \
  "ds_read_b128 a[204:207], v223 offset:16384\n\t" \
  "v_cvt_pk_bf16_f32 v116, v104, v105\n\t" \
  "v_mfma_f32_16x16x32_bf16 v[32:35], a[0:3], v[48:51], 0\n\t" \
  ".Lhs5:\n\ts_cbranch_execz .Lhs5n\n\t" \
  "ds_read_b128 a[208:211], v220 offset:20480\n\t" \
  "v_cvt_pk_bf16_f32 v117, v106, v107\n\t" \
  "v_mfma_f32_16x16x32_bf16 v[32:35], a[4:7], v[52:55], v[32:35]\n\t" \
  "ds_read_b128 a[212:215], v221 offset:20480\n\t" \
  "v_cvt_pk_bf16_f32 v115, v102, v103\n\t" \
  "v_mfma_f32_16x16x32_bf16 v[32:35], a[8:11], v[56:59], v[32:35]\n\t" \
  "ds_read_b128 a[216:219], v222 offset:20480\n\t" \
  "v_max3_f32 v194, v0, v1, v2\n\t" \
  "v_mfma_f32_16x16x32_bf16 v[32:35], a[12:15], v[60:63], v[32:35]\n\t" \
  "ds_read_b128 a[220:223], v223 offset:20480\n\t" \
  "v_max_f32 v194, v194, v3\n\t" \
  "v_mov_b32 v195, v194\n\t" \
  "s_nop 1\n\t" \
  "v_permlane32_swap_b32 v194, v195\n\t" \
  "v_max_f32 v194, v194, v195\n\t" \
  "v_mov_b32 v195, v194\n\t" \
  "s_nop 1\n\t" \
  "v_permlane16_swap_b32 v194, v195\n\t" \
  "v_max_f32 v194, v194, v195\n\t" \
  "v_sub_f32 v176, 0xc2000000, v194\n\t" \
  "v_sub_f32 v177, 0xc2000000, v194\n\t" \
  "v_sub_f32 v178, 0xc2000000, v194\n\t" \
  "v_sub_f32 v179, 0xc2000000, v194\n\t" \
  "s_nop 1\n\t" \
  "v_mfma_f32_16x16x32_bf16 v[4:7], a[16:19], v[16:19], v[176:179]\n\t" \
  "ds_read_b128 a[224:227], v220 offset:24576\n\t" \
  "v_add_f32 v0, v0, v176\n\t" \
  "v_mfma_f32_16x16x32_bf16 v[4:7], a[20:23], v[20:23], v[4:7]\n\t" \
  "v_add_f32 v1, v1, v176\n\t" \
  "v_add_f32 v2, v2, v176\n\t" \
  "v_mfma_f32_16x16x32_bf16 v[4:7], a[24:27], v[24:27], v[4:7]\n\t" \
  "ds_read_b128 a[228:231], v221 offset:24576\n\t" \
  "v_add_f32 v3, v3, v176\n\t" \
  "v_mfma_f32_16x16x32_bf16 v[4:7], a[28:31], v[28:31], v[4:7]\n\t" \
  "v_max3_f32 v194, v32, v33, v34\n\t" \
  "v_max_f32 v194, v194, v35\n\t" \
  "v_mfma_f32_16x16x32_bf16 v[8:11], a[32:35], v[16:19], v[176:179]\n\t" \
  STS("v_writelane_b32 %[stv], s94, 18\n\t") \
  STS("v_writelane_b32 %[stv], s92, 19\n\t") \
  "ds_read_b128 a[232:235], v222 offset:24576\n\t" \
  "v_mov_b32 v195, v194\n\t" \
  "v_mfma_f32_16x16x32_bf16 v[8:11], a[36:39], v[20:23], v[8:11]\n\t" \
  "buffer_load_dwordx4 v224, %[sK], s96 offen offset:0 lds\n\t" \
  "v_mfma_f32_16x16x32_bf16 v[8:11], a[40:43], v[24:27], v[8:11]\n\t" \
  "v_permlane32_swap_b32 v194, v195\n\t" \
  "v_mfma_f32_16x16x32_bf16 v[8:11], a[44:47], v[28:31], v[8:11]\n\t" \
  "s_add_u32 s98, %[pf4], 14336\n\t" \
  "ds_read_b128 a[236:239], v223 offset:24576\n\t" \
  "v_max_f32 v194, v194, v195\n\t" \
  "v_mfma_f32_16x16x32_bf16 v[12:15], a[48:51], v[16:19], v[176:179]\n\t" \
  "ds_read_b128 a[240:243], v220 offset:28672\n\t" \
  "v_mov_b32 v195, v194\n\t" \
  "v_mfma_f32_16x16x32_bf16 v[12:15], a[52:55], v[20:23], v[12:15]\n\t" \
  "buffer_load_dword %[pfd], v245, %[sU], s98 offen\n\t" \
  "v_mfma_f32_16x16x32_bf16 v[12:15], a[56:59], v[24:27], v[12:15]\n\t" \
  "v_permlane16_swap_b32 v194, v195\n\t" \
  "v_mfma_f32_16x16x32_bf16 v[12:15], a[60:63], v[28:31], v[12:15]\n\t" \
  "ds_read_b128 a[244:247], v221 offset:28672\n\t" \
  "v_max_f32 v194, v194, v195\n\t" \
  "v_permlane16_swap_b32 v93, v95\n\t" \
  "v_permlane16_swap_b32 v88, v90\n\t" \
  "v_permlane16_swap_b32 v89, v91\n\t" \
  "v_permlane16_swap_b32 v84, v86\n\t" \
  "v_permlane16_swap_b32 v85, v87\n\t" \
  "v_permlane16_swap_b32 v80, v82\n\t" \
  "v_permlane16_swap_b32 v81, v83\n\t" \
  "s_nop 1\n\t" \
  "v_mfma_f32_16x16x32_bf16 v[64:67], a[0:3], v[80:83], 0\n\t" \
  "ds_read_b128 a[248:251], v222 offset:28672\n\t" \
  "v_sub_f32 v180, 0xc2000000, v194\n\t" \
  "v_mfma_f32_16x16x32_bf16 v[64:67], a[4:7], v[84:87], v[64:67]\n\t" \
  "v_sub_f32 v181, 0xc2000000, v194\n\t" \
  "v_sub_f32 v182, 0xc2000000, v194\n\t" \
  "v_mfma_f32_16x16x32_bf16 v[64:67], a[8:11], v[88:91], v[64:67]\n\t" \
  "ds_read_b128 a[252:255], v223 offset:28672\n\t" \
  "v_sub_f32 v183, 0xc2000000, v194\n\t" \
  "v_mfma_f32_16x16x32_bf16 v[64:67], a[12:15], v[92:95], v[64:67]\n\t" \
  "v_add_f32 v32, v32, v180\n\t" \
  "v_add_f32 v33, v33, v180\n\t" \
  "v_mfma_f32_16x16x32_bf16 v[36:39], a[16:19], v[48:51], v[180:183]\n\t" \
  "v_add_f32 v34, v34, v180\n\t" \
  "v_add_f32 v35, v35, v180\n\t" \
  "v_mfma_f32_16x16x32_bf16 v[36:39], a[20:23], v[52:55], v[36:39]\n\t" \
  "buffer_load_dwordx4 v225, %[sK], s96 offen offset:1024 lds\n\t" \
  "v_mfma_f32_16x16x32_bf16 v[36:39], a[24:27], v[56:59], v[36:39]\n\t" \
  "v_max3_f32 v194, v64, v65, v66\n\t" \
  "v_max_f32 v194, v194, v67\n\t" \
  "v_mfma_f32_16x16x32_bf16 v[36:39], a[28:31], v[60:63], v[36:39]\n\t" \
  "v_mov_b32 v195, v194\n\t" \
  "v_cvt_pk_bf16_f32 v114, v100, v101\n\t" \
  "v_mfma_f32_16x16x32_bf16 v[40:43], a[32:35], v[48:51], v[180:183]\n\t" \
  "v_permlane32_swap_b32 v194, v195\n\t" \
  "v_mfma_f32_16x16x32_bf16 v[40:43], a[36:39], v[52:55], v[40:43]\n\t" \
  "v_max_f32 v194, v194, v195\n\t" \
  "v_mov_b32 v195, v194\n\t" \
  "v_mfma_f32_16x16x32_bf16 v[40:43], a[40:43], v[56:59], v[40:43]\n\t" \
  "buffer_load_dwordx4 v226, %[sK], s96 offen offset:2048 lds\n\t" \
  "v_mfma_f32_16x16x32_bf16 v[40:43], a[44:47], v[60:63], v[40:43]\n\t" \
  "v_permlane16_swap_b32 v194, v195\n\t" \
  "v_mfma_f32_16x16x32_bf16 v[44:47], a[48:51], v[48:51], v[180:183]\n\t" \
  "v_max_f32 v194, v194, v195\n\t" \
  "v_sub_f32 v184, 0xc2000000, v194\n\t" \
  "v_mfma_f32_16x16x32_bf16 v[44:47], a[52:55], v[52:55], v[44:47]\n\t" \
  "v_sub_f32 v185, 0xc2000000, v194\n\t" \
  "v_sub_f32 v186, 0xc2000000, v194\n\t" \
  "v_mfma_f32_16x16x32_bf16 v[44:47], a[56:59], v[56:59], v[44:47]\n\t" \
  "v_sub_f32 v187, 0xc2000000, v194\n\t" \
  "v_add_f32 v64, v64, v184\n\t" \
  "v_mfma_f32_16x16x32_bf16 v[44:47], a[60:63], v[60:63], v[44:47]\n\t" \
  "ds_read_b64_tr_b16 a[128:129], v228 offset:0\n\t" \
  "v_add_f32 v65, v65, v184\n\t" \
  "v_cvt_pk_bf16_f32 v112, v96, v97\n\t" \
  "v_cvt_pk_bf16_f32 v113, v98, v99\n\t" \
  "v_permlane16_swap_b32 v124, v126\n\t" \
  "v_permlane16_swap_b32 v125, v127\n\t" \
  "v_permlane16_swap_b32 v120, v122\n\t" \
  "v_permlane16_swap_b32 v121, v123\n\t" \
  "v_permlane16_swap_b32 v116, v118\n\t" \
  "v_permlane16_swap_b32 v117, v119\n\t" \
  "v_permlane16_swap_b32 v112, v114\n\t" \
  "v_permlane16_swap_b32 v113, v115\n\t" \
  "s_nop 1\n\t" \
  "v_mfma_f32_16x16x32_bf16 v[96:99], a[0:3], v[112:115], 0\n\t" \
  "ds_read_b64_tr_b16 a[130:131], v228 offset:4096\n\t" \
  "v_add_f32 v66, v66, v184\n\t" \
  "v_mfma_f32_16x16x32_bf16 v[96:99], a[4:7], v[116:119], v[96:99]\n\t" \
  "ds_read_b64_tr_b16 a[132:133], v228 offset:8192\n\t" \
  "v_add_f32 v67, v67, v184\n\t" \
  "v_mfma_f32_16x16x32_bf16 v[96:99], a[8:11], v[120:123], v[96:99]\n\t" \
  "buffer_load_dwordx4 v227, %[sK], s96 offen offset:3072 lds\n\t" \
  "v_mfma_f32_16x16x32_bf16 v[96:99], a[12:15], v[124:127], v[96:99]\n\t" \
  "s_add_u32 m0, %[m], 147456\n\t" \
  "v_exp_f32 v0, v0\n\t" \
  "v_mfma_f32_16x16x32_bf16 v[68:71], a[16:19], v[80:83], v[184:187]\n\t" \
  "s_add_u32 s96, %[sofk], 16384\n\t" \
  "v_exp_f32 v1, v1\n\t" \
  "v_mfma_f32_16x16x32_bf16 v[68:71], a[20:23], v[84:87], v[68:71]\n\t" \
  "v_exp_f32 v2, v2\n\t" \
  "v_mfma_f32_16x16x32_bf16 v[68:71], a[24:27], v[88:91], v[68:71]\n\t" \
  "ds_read_b64_tr_b16 a[134:135], v228 offset:12288\n\t" \
  "v_max3_f32 v194, v96, v97, v98\n\t" \
  "v_mfma_f32_16x16x32_bf16 v[68:71], a[28:31], v[92:95], v[68:71]\n\t" \
  "ds_read_b64_tr_b16 a[136:137], v229 offset:0\n\t" \
  "v_max_f32 v194, v194, v99\n\t" \
  "v_mfma_f32_16x16x32_bf16 v[72:75], a[32:35], v[80:83], v[184:187]\n\t" \
  "ds_read_b64_tr_b16 a[138:139], v229 offset:4096\n\t" \
  "v_mov_b32 v195, v194\n\t" \
  "v_mfma_f32_16x16x32_bf16 v[72:75], a[36:39], v[84:87], v[72:75]\n\t" \
  "v_exp_f32 v3, v3\n\t" \
  "v_mfma_f32_16x16x32_bf16 v[72:75], a[40:43], v[88:91], v[72:75]\n\t" \
  "v_permlane32_swap_b32 v194, v195\n\t" \
  "v_mfma_f32_16x16x32_bf16 v[72:75], a[44:47], v[92:95], v[72:75]\n\t" \
  "ds_read_b64_tr_b16 a[140:141], v229 offset:8192\n\t" \
  "v_max_f32 v194, v194, v195\n\t" \
  "v_mfma_f32_16x16x32_bf16 v[76:79], a[48:51], v[80:83], v[184:187]\n\t" \
  "ds_read_b64_tr_b16 a[142:143], v229 offset:12288\n\t" \
  "v_mov_b32 v195, v194\n\t" \
  "v_mfma_f32_16x16x32_bf16 v[76:79], a[52:55], v[84:87], v[76:79]\n\t" \
  "buffer_load_dwordx4 v230, %[sV], s96 offen offset:0 lds\n\t" \
  "v_mfma_f32_16x16x32_bf16 v[76:79], a[56:59], v[88:91], v[76:79]\n\t" \
  "v_permlane16_swap_b32 v194, v195\n\t" \
  "v_mfma_f32_16x16x32_bf16 v[76:79], a[60:63], v[92:95], v[76:79]\n\t" \
  "ds_read_b64_tr_b16 a[144:145], v228 offset:256\n\t" \
  "v_max_f32 v194, v194, v195\n\t" \
  "v_sub_f32 v188, 0xc2000000, v194\n\t" \
  "v_sub_f32 v189, 0xc2000000, v194\n\t" \
  "v_sub_f32 v190, 0xc2000000, v194\n\t" \
  "v_sub_f32 v191, 0xc2000000, v194\n\t" \
  "s_nop 1\n\t" \
  "v_mfma_f32_16x16x32_bf16 v[100:103], a[16:19], v[112:115], v[188:191]\n\t" \
  "ds_read_b64_tr_b16 a[146:147], v228 offset:4352\n\t" \
  "v_add_f32 v96, v96, v188\n\t" \
  "v_mfma_f32_16x16x32_bf16 v[100:103], a[20:23], v[116:119], v[100:103]\n\t" \
  "ds_read_b64_tr_b16 a[148:149], v228 offset:8448\n\t" \
  "v_add_f32 v97, v97, v188\n\t" \
  "v_mfma_f32_16x16x32_bf16 v[100:103], a[24:27], v[120:123], v[100:103]\n\t" \
  "v_add_f32 v98, v98, v188\n\t" \
  "v_add_f32 v99, v99, v188\n\t" \
  "v_mfma_f32_16x16x32_bf16 v[100:103], a[28:31], v[124:127], v[100:103]\n\t" \
  "buffer_load_dwordx4 v231, %[sV], s96 offen offset:1024 lds\n\t" \
  "v_mfma_f32_16x16x32_bf16 v[104:107], a[32:35], v[112:115], v[188:191]\n\t" \
  "ds_read_b64_tr_b16 a[150:151], v228 offset:12544\n\t" \
  "v_cvt_pk_bf16_f32 v128, v0, v1\n\t" \
  "v_mfma_f32_16x16x32_bf16 v[104:107], a[36:39], v[116:119], v[104:107]\n\t" \
  "v_exp_f32 v4, v4\n\t" \
  "v_mfma_f32_16x16x32_bf16 v[104:107], a[40:43], v[120:123], v[104:107]\n\t" \
  "v_exp_f32 v5, v5\n\t" \
  "v_mfma_f32_16x16x32_bf16 v[104:107], a[44:47], v[124:127], v[104:107]\n\t" \
  "v_exp_f32 v6, v6\n\t" \
  "v_mfma_f32_16x16x32_bf16 v[108:111], a[48:51], v[112:115], v[188:191]\n\t" \
  "buffer_load_dwordx4 v230, %[sV], s96 offen offset:2048 lds\n\t" \
  "v_mfma_f32_16x16x32_bf16 v[108:111], a[52:55], v[116:119], v[108:111]\n\t" \
  "v_exp_f32 v7, v7\n\t" \
  "v_mfma_f32_16x16x32_bf16 v[108:111], a[56:59], v[120:123], v[108:111]\n\t" \
  "ds_read_b64_tr_b16 a[152:153], v229 offset:256\n\t" \
  "v_cvt_pk_bf16_f32 v129, v2, v3\n\t" \
  "v_mfma_f32_16x16x32_bf16 v[108:111], a[60:63], v[124:127], v[108:111]\n\t" \
  "s_waitcnt lgkmcnt(13)\n\t" \
  "v_exp_f32 v8, v8\n\t" \
  STA("s_memrealtime s[94:95]\n\t") \
  STA("s_memtime s[92:93]\n\t") \
  "v_mfma_f32_16x16x32_bf16 v[0:3], a[192:195], v[16:19], v[176:179]\n\t" \
  "v_exp_f32 v9, v9\n\t" \
  "v_mfma_f32_16x16x32_bf16 v[0:3], a[196:199], v[20:23], v[0:3]\n\t" \
  "ds_read_b64_tr_b16 a[154:155], v229 offset:4352\n\t" \
  "v_cvt_pk_bf16_f32 v130, v4, v5\n\t" \
  "v_mfma_f32_16x16x32_bf16 v[0:3], a[200:203], v[24:27], v[0:3]\n\t" \
  "buffer_load_dwordx4 v231, %[sV], s96 offen offset:3072 lds\n\t" \
  "v_mfma_f32_16x16x32_bf16 v[0:3], a[204:207], v[28:31], v[0:3]\n\t" \
  "s_add_u32 s98, %[pf4], 28672\n\t" \
  "ds_read_b64_tr_b16 a[156:157], v229 offset:8448\n\t" \
  "v_cvt_pk_bf16_f32 v131, v6, v7\n\t" \
  "v_mfma_f32_16x16x32_bf16 v[4:7], a[220:223], v[28:31], v[176:179]\n\t" \
  "v_exp_f32 v10, v10\n\t" \
  "v_mfma_f32_16x16x32_bf16 v[4:7], a[216:219], v[24:27], v[4:7]\n\t" \
  "v_exp_f32 v11, v11\n\t" \
  "v_mfma_f32_16x16x32_bf16 v[4:7], a[212:215], v[20:23], v[4:7]\n\t" \
  "buffer_load_dword %[pfd], v245, %[sU], s98 offen\n\t" \
  "v_mfma_f32_16x16x32_bf16 v[4:7], a[208:211], v[16:19], v[4:7]\n\t" \
  "v_exp_f32 v12, v12\n\t" \
  "v_cvt_pk_bf16_f32 v132, v8, v9\n\t" \
  "v_cvt_pk_bf16_f32 v133, v10, v11\n\t" \
  "v_mfma_f32_16x16x32_bf16 v[8:11], a[224:227], v[16:19], v[176:179]\n\t" \
  "v_exp_f32 v13, v13\n\t" \
  "v_mfma_f32_16x16x32_bf16 v[8:11], a[228:231], v[20:23], v[8:11]\n\t" \
  "v_exp_f32 v14, v14\n\t" \
  "v_mfma_f32_16x16x32_bf16 v[8:11], a[232:235], v[24:27], v[8:11]\n\t" \
  "v_exp_f32 v15, v15\n\t" \
  "v_mfma_f32_16x16x32_bf16 v[8:11], a[236:239], v[28:31], v[8:11]\n\t" \
  "v_exp_f32 v32, v32\n\t" \
  "v_cvt_pk_bf16_f32 v134, v12, v13\n\t" \
  "v_cvt_pk_bf16_f32 v135, v14, v15\n\t" \
  "v_mfma_f32_16x16x32_bf16 v[12:15], a[252:255], v[28:31], v[176:179]\n\t" \
  STA("v_writelane_b32 %[stv], s94, 26\n\t") \
  STA("v_writelane_b32 %[stv], s92, 27\n\t") \
  "v_exp_f32 v33, v33\n\t" \
  "v_mfma_f32_16x16x32_bf16 v[12:15], a[248:251], v[24:27], v[12:15]\n\t" \
  "v_exp_f32 v34, v34\n\t" \
  "v_mfma_f32_16x16x32_bf16 v[12:15], a[244:247], v[20:23], v[12:15]\n\t" \
  "v_exp_f32 v35, v35\n\t" \
  "v_mfma_f32_16x16x32_bf16 v[12:15], a[240:243], v[16:19], v[12:15]\n\t" \
  "ds_read_b64_tr_b16 a[158:159], v229 offset:12544\n\t" \
  "v_cvt_pk_bf16_f32 v136, v32, v33\n\t" \
  "v_cvt_pk_bf16_f32 v137, v34, v35\n\t" \
  "v_mfma_f32_16x16x32_bf16 v[32:35], a[192:195], v[48:51], v[180:183]\n\t" \
  "v_exp_f32 v36, v36\n\t" \
  "v_mfma_f32_16x16x32_bf16 v[32:35], a[196:199], v[52:55], v[32:35]\n\t" \
  "v_exp_f32 v37, v37\n\t" \
  "v_mfma_f32_16x16x32_bf16 v[32:35], a[200:203], v[56:59], v[32:35]\n\t" \
  "ds_read_b64_tr_b16 a[160:161], v228 offset:512\n\t" \
  "v_cvt_pk_bf16_f32 v138, v36, v37\n\t" \
  "v_exp_f32 v38, v38\n\t" \
  "v_mfma_f32_16x16x32_bf16 v[32:35], a[204:207], v[60:63], v[32:35]\n\t" \
  "v_exp_f32 v39, v39\n\t" \
  "v_lshl_add_u32 v207, v233, 14, %[cto]\n\t" \
  "v_min_u32 v207, .Lfz_z-.Lat_t0-4, v207\n\t" \
  "v_cvt_pk_bf16_f32 v139, v38, v39\n\t" \
  "v_mfma_f32_16x16x32_bf16 v[36:39], a[220:223], v[60:63], v[180:183]\n\t" \
  "v_exp_f32 v40, v40\n\t" \
  "v_mfma_f32_16x16x32_bf16 v[36:39], a[216:219], v[56:59], v[36:39]\n\t" \
  "ds_read_b64_tr_b16 a[162:163], v228 offset:4608\n\t" \
  "ds_read_b64_tr_b16 a[164:165], v228 offset:8704\n\t" \
  "v_exp_f32 v41, v41\n\t" \
  "s_getpc_b64 s[96:97]\n\t" \
  ".Lat_t0:\n\t" \
  "global_load_dword %[pfd], v207, s[96:97]\n\t" \
  "v_mfma_f32_16x16x32_bf16 v[36:39], a[212:215], v[52:55], v[36:39]\n\t" \
  "s_add_u32 m0, %[m], 0\n\t" \
  "v_exp_f32 v42, v42\n\t" \
  "v_mfma_f32_16x16x32_bf16 v[36:39], a[208:211], v[48:51], v[36:39]\n\t" \
  "s_xor_b32 s97, %[wob], 0\n\t" \
  "v_exp_f32 v43, v43\n\t" \
  "v_cvt_pk_bf16_f32 v140, v40, v41\n\t" \
  "v_cvt_pk_bf16_f32 v141, v42, v43\n\t" \
  "v_mfma_f32_16x16x32_bf16 v[40:43], a[224:227], v[48:51], v[180:183]\n\t" \
  "v_exp_f32 v44, v44\n\t" \
  "v_mfma_f32_16x16x32_bf16 v[40:43], a[228:231], v[52:55], v[40:43]\n\t" \
  "v_exp_f32 v45, v45\n\t" \
  "v_mfma_f32_16x16x32_bf16 v[40:43], a[232:235], v[56:59], v[40:43]\n\t" \
  "ds_read_b64_tr_b16 a[166:167], v228 offset:12800\n\t" \
  "v_cvt_pk_bf16_f32 v142, v44, v45\n\t" \
  "v_exp_f32 v46, v46\n\t" \
  "v_mfma_f32_16x16x32_bf16 v[40:43], a[236:239], v[60:63], v[40:43]\n\t" \
  "v_exp_f32 v47, v47\n\t" \
  "s_nop 0\n\t" \
  "v_cvt_pk_bf16_f32 v143, v46, v47\n\t" \
  "v_mfma_f32_16x16x32_bf16 v[44:47], a[252:255], v[60:63], v[180:183]\n\t" \
  "v_exp_f32 v64, v64\n\t" \
  "v_mfma_f32_16x16x32_bf16 v[44:47], a[248:251], v[56:59], v[44:47]\n\t" \
  "ds_read_b64_tr_b16 a[168:169], v229 offset:512\n\t" \
  "ds_read_b64_tr_b16 a[170:171], v229 offset:4608\n\t" \
  "v_exp_f32 v65, v65\n\t" \
  "v_mfma_f32_16x16x32_bf16 v[44:47], a[244:247], v[52:55], v[44:47]\n\t" \
  "v_exp_f32 v66, v66\n\t" \
  "v_mfma_f32_16x16x32_bf16 v[44:47], a[240:243], v[48:51], v[44:47]\n\t" \
  "v_exp_f32 v67, v67\n\t" \
  "v_cvt_pk_bf16_f32 v144, v64, v65\n\t" \
  "v_cvt_pk_bf16_f32 v145, v66, v67\n\t" \
  "v_mfma_f32_16x16x32_bf16 v[64:67], a[192:195], v[80:83], v[184:187]\n\t" \
  "v_exp_f32 v68, v68\n\t" \
  "v_mfma_f32_16x16x32_bf16 v[64:67], a[196:199], v[84:87], v[64:67]\n\t" \
  "v_exp_f32 v69, v69\n\t" \
  "v_mfma_f32_16x16x32_bf16 v[64:67], a[200:203], v[88:91], v[64:67]\n\t" \
  "buffer_load_dwordx4 v240, %[wo], s97 offen offset:0 lds\n\t" \
  "v_exp_f32 v70, v70\n\t" \
  "v_mfma_f32_16x16x32_bf16 v[64:67], a[204:207], v[92:95], v[64:67]\n\t" \
  "s_add_u32 s97, s97, 7168\n\t" \
  "ds_read_b64_tr_b16 a[172:173], v229 offset:8704\n\t" \
  "v_cvt_pk_bf16_f32 v146, v68, v69\n\t" \
  "v_exp_f32 v71, v71\n\t" \
  "s_nop 0\n\t" \
  "v_cvt_pk_bf16_f32 v147, v70, v71\n\t" \
  "v_mfma_f32_16x16x32_bf16 v[68:71], a[220:223], v[92:95], v[184:187]\n\t" \
  "v_exp_f32 v72, v72\n\t" \
  "v_mfma_f32_16x16x32_bf16 v[68:71], a[216:219], v[88:91], v[68:71]\n\t" \
  "ds_read_b64_tr_b16 a[174:175], v229 offset:12800\n\t" \
  "ds_read_b64_tr_b16 a[176:177], v228 offset:768\n\t" \
  "v_exp_f32 v73, v73\n\t" \
  "v_mfma_f32_16x16x32_bf16 v[68:71], a[212:215], v[84:87], v[68:71]\n\t" \
  "v_exp_f32 v74, v74\n\t" \
  "v_mfma_f32_16x16x32_bf16 v[68:71], a[208:211], v[80:83], v[68:71]\n\t" \
  "v_exp_f32 v75, v75\n\t" \
  "v_cvt_pk_bf16_f32 v148, v72, v73\n\t" \
  "v_cvt_pk_bf16_f32 v149, v74, v75\n\t" \
  "v_mfma_f32_16x16x32_bf16 v[72:75], a[224:227], v[80:83], v[184:187]\n\t" \
  "v_exp_f32 v76, v76\n\t" \
  "v_mfma_f32_16x16x32_bf16 v[72:75], a[228:231], v[84:87], v[72:75]\n\t" \
  "v_exp_f32 v77, v77\n\t" \
  "v_mfma_f32_16x16x32_bf16 v[72:75], a[232:235], v[88:91], v[72:75]\n\t" \
  "buffer_load_dwordx4 v241, %[wo], s97 offen offset:1024 lds\n\t" \
  "v_exp_f32 v78, v78\n\t" \
  "v_mfma_f32_16x16x32_bf16 v[72:75], a[236:239], v[92:95], v[72:75]\n\t" \
  "s_add_u32 s97, s97, 7168\n\t" \
  "ds_read_b64_tr_b16 a[178:179], v228 offset:4864\n\t" \
  "v_cvt_pk_bf16_f32 v150, v76, v77\n\t" \
  "v_exp_f32 v79, v79\n\t" \
  "s_nop 0\n\t" \
  "v_cvt_pk_bf16_f32 v151, v78, v79\n\t" \
  "v_mfma_f32_16x16x32_bf16 v[76:79], a[252:255], v[92:95], v[184:187]\n\t" \
  "v_exp_f32 v96, v96\n\t" \
  "v_mfma_f32_16x16x32_bf16 v[76:79], a[248:251], v[88:91], v[76:79]\n\t" \
  "ds_read_b64_tr_b16 a[180:181], v228 offset:8960\n\t" \
  "ds_read_b64_tr_b16 a[182:183], v228 offset:13056\n\t" \
  "v_exp_f32 v97, v97\n\t" \
  "v_mfma_f32_16x16x32_bf16 v[76:79], a[244:247], v[84:87], v[76:79]\n\t" \
  "v_exp_f32 v98, v98\n\t" \
  "v_mfma_f32_16x16x32_bf16 v[76:79], a[240:243], v[80:83], v[76:79]\n\t" \
  "v_exp_f32 v99, v99\n\t" \
  "v_cvt_pk_bf16_f32 v152, v96, v97\n\t" \
  "v_cvt_pk_bf16_f32 v153, v98, v99\n\t" \
  "v_mfma_f32_16x16x32_bf16 v[96:99], a[192:195], v[112:115], v[188:191]\n\t" \
  "v_exp_f32 v100, v100\n\t" \
  "v_mfma_f32_16x16x32_bf16 v[96:99], a[196:199], v[116:119], v[96:99]\n\t" \
  "v_exp_f32 v101, v101\n\t" \
  "v_mfma_f32_16x16x32_bf16 v[96:99], a[200:203], v[120:123], v[96:99]\n\t" \
  "buffer_load_dwordx4 v240, %[wo], s97 offen offset:2048 lds\n\t" \
  "v_exp_f32 v102, v102\n\t" \
  "v_mfma_f32_16x16x32_bf16 v[96:99], a[204:207], v[124:127], v[96:99]\n\t" \
  "s_add_u32 s97, s97, 7168\n\t" \
  "ds_read_b64_tr_b16 a[184:185], v229 offset:768\n\t" \
  "v_cvt_pk_bf16_f32 v154, v100, v101\n\t" \
  "v_exp_f32 v103, v103\n\t" \
  "s_nop 0\n\t" \
  "v_cvt_pk_bf16_f32 v155, v102, v103\n\t" \
  "v_mfma_f32_16x16x32_bf16 v[100:103], a[220:223], v[124:127], v[188:191]\n\t" \
  "v_exp_f32 v104, v104\n\t" \
  "v_mfma_f32_16x16x32_bf16 v[100:103], a[216:219], v[120:123], v[100:103]\n\t" \
  "ds_read_b64_tr_b16 a[186:187], v229 offset:4864\n\t" \
  "ds_read_b64_tr_b16 a[188:189], v229 offset:8960\n\t" \
  "v_exp_f32 v105, v105\n\t" \
  "v_mfma_f32_16x16x32_bf16 v[100:103], a[212:215], v[116:119], v[100:103]\n\t" \
  "v_exp_f32 v106, v106\n\t" \
  "v_mfma_f32_16x16x32_bf16 v[100:103], a[208:211], v[112:115], v[100:103]\n\t" \
  "v_exp_f32 v107, v107\n\t" \
  "v_cvt_pk_bf16_f32 v156, v104, v105\n\t" \
  "v_cvt_pk_bf16_f32 v157, v106, v107\n\t" \
  "v_mfma_f32_16x16x32_bf16 v[104:107], a[224:227], v[112:115], v[188:191]\n\t" \
  "v_exp_f32 v108, v108\n\t" \
  "v_mfma_f32_16x16x32_bf16 v[104:107], a[228:231], v[116:119], v[104:107]\n\t" \
  "v_exp_f32 v109, v109\n\t" \
  "v_mfma_f32_16x16x32_bf16 v[104:107], a[232:235], v[120:123], v[104:107]\n\t" \
  "buffer_load_dwordx4 v241, %[wo], s97 offen offset:3072 lds\n\t" \
  "v_exp_f32 v110, v110\n\t" \
  "v_mfma_f32_16x16x32_bf16 v[104:107], a[236:239], v[124:127], v[104:107]\n\t" \
  "s_add_u32 s98, %[pf4], 43008\n\t" \
  "ds_read_b64_tr_b16 a[190:191], v229 offset:13056\n\t" \
  "v_cvt_pk_bf16_f32 v158, v108, v109\n\t" \
  "v_exp_f32 v111, v111\n\t" \
  "s_nop 0\n\t" \
  "v_cvt_pk_bf16_f32 v159, v110, v111\n\t" \
  "v_mfma_f32_16x16x32_bf16 v[108:111], a[252:255], v[124:127], v[188:191]\n\t" \
  "buffer_load_dword %[pfd], v245, %[sU], s98 offen\n\t" \
  "v_mfma_f32_16x16x32_bf16 v[108:111], a[248:251], v[120:123], v[108:111]\n\t" \
  "v_exp_f32 v0, v0\n\t" \
  "v_mfma_f32_16x16x32_bf16 v[108:111], a[244:247], v[116:119], v[108:111]\n\t" \
  "v_exp_f32 v1, v1\n\t" \
  "v_mfma_f32_16x16x32_bf16 v[108:111], a[240:243], v[112:115], v[108:111]\n\t" \
  "s_waitcnt vmcnt(7) lgkmcnt(0)\n\t" \
  "s_barrier\n\t" \
  STA("s_memrealtime s[94:95]\n\t") \
  STA("s_memtime s[92:93]\n\t") \
  "v_mfma_f32_16x16x32_bf16 a[0:3], a[128:131], v[128:131], 0\n\t" \
  "v_exp_f32 v2, v2\n\t" \
  "v_mfma_f32_16x16x32_bf16 a[0:3], a[132:135], v[132:135], a[0:3]\n\t" \
  "v_exp_f32 v3, v3\n\t" \
  "v_mfma_f32_16x16x32_bf16 a[16:19], a[136:139], v[128:131], 0\n\t" \
  "ds_read_b128 a[192:195], v220 offset:0\n\t" \
  "ds_read_b128 a[196:199], v221 offset:0\n\t" \
  "v_mfma_f32_16x16x32_bf16 a[16:19], a[140:143], v[132:135], a[16:19]\n\t" \
  "v_exp_f32 v4, v4\n\t" \
  "v_mfma_f32_16x16x32_bf16 a[32:35], a[144:147], v[128:131], 0\n\t" \
  "v_exp_f32 v5, v5\n\t" \
  "v_mfma_f32_16x16x32_bf16 a[32:35], a[148:151], v[132:135], a[32:35]\n\t" \
  "v_exp_f32 v6, v6\n\t" \
  "v_mfma_f32_16x16x32_bf16 a[48:51], a[152:155], v[128:131], 0\n\t" \
  "v_exp_f32 v7, v7\n\t" \
  "v_mfma_f32_16x16x32_bf16 a[48:51], a[156:159], v[132:135], a[48:51]\n\t" \
  "v_exp_f32 v8, v8\n\t" \
  "v_mfma_f32_16x16x32_bf16 a[64:67], a[160:163], v[128:131], 0\n\t" \
  "v_exp_f32 v9, v9\n\t" \
  "v_mfma_f32_16x16x32_bf16 a[64:67], a[164:167], v[132:135], a[64:67]\n\t" \
  "v_exp_f32 v10, v10\n\t" \
  "v_mfma_f32_16x16x32_bf16 a[80:83], a[168:171], v[128:131], 0\n\t" \
  "ds_read_b128 a[200:203], v222 offset:0\n\t" \
  "ds_read_b128 a[204:207], v223 offset:0\n\t" \
  "v_mfma_f32_16x16x32_bf16 a[80:83], a[172:175], v[132:135], a[80:83]\n\t" \
  "v_exp_f32 v11, v11\n\t" \
  "v_mfma_f32_16x16x32_bf16 a[96:99], a[176:179], v[128:131], 0\n\t" \
  STA("v_writelane_b32 %[stv], s94, 28\n\t") \
  STA("v_writelane_b32 %[stv], s92, 29\n\t") \
  "v_exp_f32 v12, v12\n\t" \
  "v_mfma_f32_16x16x32_bf16 a[96:99], a[180:183], v[132:135], a[96:99]\n\t" \
  "v_exp_f32 v13, v13\n\t" \
  "v_mfma_f32_16x16x32_bf16 a[112:115], a[184:187], v[128:131], 0\n\t" \
  "v_exp_f32 v14, v14\n\t" \
  "v_mfma_f32_16x16x32_bf16 a[112:115], a[188:191], v[132:135], a[112:115]\n\t" \
  "v_exp_f32 v15, v15\n\t" \
  "v_mfma_f32_16x16x32_bf16 v[160:163], v[216:219], v[128:131], 0\n\t" \
  "v_exp_f32 v32, v32\n\t" \
  "v_mfma_f32_16x16x32_bf16 v[160:163], v[216:219], v[132:135], v[160:163]\n\t" \
  "ds_read_b128 a[208:211], v220 offset:4096\n\t" \
  "v_cvt_pk_bf16_f32 v128, v0, v1\n\t" \
  "v_mfma_f32_16x16x32_bf16 a[4:7], a[128:131], v[136:139], 0\n\t" \
  "v_exp_f32 v33, v33\n\t" \
  "v_mfma_f32_16x16x32_bf16 a[4:7], a[132:135], v[140:143], a[4:7]\n\t" \
  "ds_read_b128 a[212:215], v221 offset:4096\n\t" \
  "v_cvt_pk_bf16_f32 v129, v2, v3\n\t" \
  "v_mfma_f32_16x16x32_bf16 a[20:23], a[136:139], v[136:139], 0\n\t" \
  "s_add_u32 m0, %[m], 65536\n\t" \
  "v_exp_f32 v34, v34\n\t" \
  "v_mfma_f32_16x16x32_bf16 a[20:23], a[140:143], v[140:143], a[20:23]\n\t" \
  "s_add_u32 s96, %[sofk], 49152\n\t" \
  "v_exp_f32 v35, v35\n\t" \
  "v_mfma_f32_16x16x32_bf16 a[36:39], a[144:147], v[136:139], 0\n\t" \
  "v_exp_f32 v36, v36\n\t" \
  "v_mfma_f32_16x16x32_bf16 a[36:39], a[148:151], v[140:143], a[36:39]\n\t" \
  "ds_read_b128 a[216:219], v222 offset:4096\n\t" \
  "v_cvt_pk_bf16_f32 v130, v4, v5\n\t" \
  "v_mfma_f32_16x16x32_bf16 a[52:55], a[152:155], v[136:139], 0\n\t" \
  "v_exp_f32 v37, v37\n\t" \
  "v_mfma_f32_16x16x32_bf16 a[52:55], a[156:159], v[140:143], a[52:55]\n\t" \
  "v_exp_f32 v38, v38\n\t" \
  "v_mfma_f32_16x16x32_bf16 a[68:71], a[160:163], v[136:139], 0\n\t" \
  "v_exp_f32 v39, v39\n\t" \
  "v_mfma_f32_16x16x32_bf16 a[68:71], a[164:167], v[140:143], a[68:71]\n\t" \
  "ds_read_b128 a[220:223], v223 offset:4096\n\t" \
  "v_cvt_pk_bf16_f32 v131, v6, v7\n\t" \
  "v_mfma_f32_16x16x32_bf16 a[84:87], a[168:171], v[136:139], 0\n\t" \
  "buffer_load_dwordx4 v224, %[sK], s96 offen offset:0 lds\n\t" \
  "v_mfma_f32_16x16x32_bf16 a[84:87], a[172:175], v[140:143], a[84:87]\n\t" \
  "v_exp_f32 v40, v40\n\t" \
  "v_mfma_f32_16x16x32_bf16 a[100:103], a[176:179], v[136:139], 0\n\t" \
  "ds_read_b128 a[224:227], v220 offset:8192\n\t" \
  "v_cvt_pk_bf16_f32 v132, v8, v9\n\t" \
  "v_mfma_f32_16x16x32_bf16 a[100:103], a[180:183], v[140:143], a[100:103]\n\t" \
  "v_exp_f32 v41, v41\n\t" \
  "v_mfma_f32_16x16x32_bf16 a[116:119], a[184:187], v[136:139], 0\n\t" \
  "buffer_load_dwordx4 v225, %[sK], s96 offen offset:1024 lds\n\t" \
  "v_mfma_f32_16x16x32_bf16 a[116:119], a[188:191], v[140:143], a[116:119]\n\t" \
  "v_exp_f32 v42, v42\n\t" \
  "v_mfma_f32_16x16x32_bf16 v[164:167], v[216:219], v[136:139], 0\n\t" \
  "ds_read_b128 a[228:231], v221 offset:8192\n\t" \
  "v_cvt_pk_bf16_f32 v133, v10, v11\n\t" \
  "v_mfma_f32_16x16x32_bf16 v[164:167], v[216:219], v[140:143], v[164:167]\n\t" \
  "v_exp_f32 v43, v43\n\t" \
  "v_mfma_f32_16x16x32_bf16 a[8:11], a[128:131], v[144:147], 0\n\t" \
  "buffer_load_dwordx4 v226, %[sK], s96 offen offset:2048 lds\n\t" \
  "v_mfma_f32_16x16x32_bf16 a[8:11], a[132:135], v[148:151], a[8:11]\n\t" \
  "v_exp_f32 v44, v44\n\t" \
  "v_mfma_f32_16x16x32_bf16 a[24:27], a[136:139], v[144:147], 0\n\t" \
  "ds_read_b128 a[232:235], v222 offset:8192\n\t" \
  "v_cvt_pk_bf16_f32 v134, v12, v13\n\t" \
  "v_mfma_f32_16x16x32_bf16 a[24:27], a[140:143], v[148:151], a[24:27]\n\t" \
  "v_exp_f32 v45, v45\n\t" \
  "v_mfma_f32_16x16x32_bf16 a[40:43], a[144:147], v[144:147], 0\n\t" \
  "buffer_load_dwordx4 v227, %[sK], s96 offen offset:3072 lds\n\t" \
  "v_mfma_f32_16x16x32_bf16 a[40:43], a[148:151], v[148:151], a[40:43]\n\t" \
  "s_add_u32 m0, %[m], 81920\n\t" \
  "v_exp_f32 v46, v46\n\t" \
  "v_mfma_f32_16x16x32_bf16 a[56:59], a[152:155], v[144:147], 0\n\t" \
  "s_add_u32 s96, %[sofk], 32768\n\t" \
  "ds_read_b128 a[236:239], v223 offset:8192\n\t" \
  "v_cvt_pk_bf16_f32 v135, v14, v15\n\t" \
  "v_mfma_f32_16x16x32_bf16 a[56:59], a[156:159], v[148:151], a[56:59]\n\t" \
  "v_exp_f32 v47, v47\n\t" \
  "v_mfma_f32_16x16x32_bf16 a[72:75], a[160:163], v[144:147], 0\n\t" \
  "v_exp_f32 v64, v64\n\t" \
  "v_mfma_f32_16x16x32_bf16 a[72:75], a[164:167], v[148:151], a[72:75]\n\t" \
  "ds_read_b128 a[240:243], v220 offset:12288\n\t" \
  "v_cvt_pk_bf16_f32 v136, v32, v33\n\t" \
  "v_mfma_f32_16x16x32_bf16 a[88:91], a[168:171], v[144:147], 0\n\t" \
  "v_exp_f32 v65, v65\n\t" \
  "v_mfma_f32_16x16x32_bf16 a[88:91], a[172:175], v[148:151], a[88:91]\n\t" \
  "v_exp_f32 v66, v66\n\t" \
  "v_mfma_f32_16x16x32_bf16 a[104:107], a[176:179], v[144:147], 0\n\t" \
  "v_exp_f32 v67, v67\n\t" \
  "v_mfma_f32_16x16x32_bf16 a[104:107], a[180:183], v[148:151], a[104:107]\n\t" \
  "ds_read_b128 a[244:247], v221 offset:12288\n\t" \
  "v_cvt_pk_bf16_f32 v137, v34, v35\n\t" \
  "v_mfma_f32_16x16x32_bf16 a[120:123], a[184:187], v[144:147], 0\n\t" \
  "v_exp_f32 v68, v68\n\t" \
  "v_mfma_f32_16x16x32_bf16 a[120:123], a[188:191], v[148:151], a[120:123]\n\t" \
  "v_exp_f32 v69, v69\n\t" \
  "v_mfma_f32_16x16x32_bf16 v[168:171], v[216:219], v[144:147], 0\n\t" \
  "buffer_load_dwordx4 v230, %[sV], s96 offen offset:0 lds\n\t" \
  "v_mfma_f32_16x16x32_bf16 v[168:171], v[216:219], v[148:151], v[168:171]\n\t" \
  "ds_read_b128 a[248:251], v222 offset:12288\n\t" \
  "v_cvt_pk_bf16_f32 v138, v36, v37\n\t" \
  "v_mfma_f32_16x16x32_bf16 a[12:15], a[128:131], v[152:155], 0\n\t" \
  "v_exp_f32 v70, v70\n\t" \
  "v_mfma_f32_16x16x32_bf16 a[12:15], a[132:135], v[156:159], a[12:15]\n\t" \
  "v_exp_f32 v71, v71\n\t" \
  "v_mfma_f32_16x16x32_bf16 a[28:31], a[136:139], v[152:155], 0\n\t" \
  "buffer_load_dwordx4 v231, %[sV], s96 offen offset:1024 lds\n\t" \
  "v_mfma_f32_16x16x32_bf16 a[28:31], a[140:143], v[156:159], a[28:31]\n\t" \
  "ds_read_b128 a[252:255], v223 offset:12288\n\t" \
  "v_cvt_pk_bf16_f32 v139, v38, v39\n\t" \
  "v_mfma_f32_16x16x32_bf16 a[44:47], a[144:147], v[152:155], 0\n\t" \
  "v_exp_f32 v72, v72\n\t" \
  "v_mfma_f32_16x16x32_bf16 a[44:47], a[148:151], v[156:159], a[44:47]\n\t" \
  "v_exp_f32 v73, v73\n\t" \
  "v_mfma_f32_16x16x32_bf16 a[60:63], a[152:155], v[152:155], 0\n\t" \
  "buffer_load_dwordx4 v230, %[sV], s96 offen offset:2048 lds\n\t" \
  "v_mfma_f32_16x16x32_bf16 a[60:63], a[156:159], v[156:159], a[60:63]\n\t" \
  "v_exp_f32 v74, v74\n\t" \
  "v_mfma_f32_16x16x32_bf16 a[76:79], a[160:163], v[152:155], 0\n\t" \
  "v_exp_f32 v75, v75\n\t" \
  "v_mfma_f32_16x16x32_bf16 a[76:79], a[164:167], v[156:159], a[76:79]\n\t" \
  "v_exp_f32 v76, v76\n\t" \
  "v_mfma_f32_16x16x32_bf16 a[92:95], a[168:171], v[152:155], 0\n\t" \
  "buffer_load_dwordx4 v231, %[sV], s96 offen offset:3072 lds\n\t" \
  "v_mfma_f32_16x16x32_bf16 a[92:95], a[172:175], v[156:159], a[92:95]\n\t" \
  "v_exp_f32 v77, v77\n\t" \
  "v_mfma_f32_16x16x32_bf16 a[108:111], a[176:179], v[152:155], 0\n\t" \
  "v_exp_f32 v78, v78\n\t" \
  "v_mfma_f32_16x16x32_bf16 a[108:111], a[180:183], v[156:159], a[108:111]\n\t" \
  "v_cvt_pk_bf16_f32 v140, v40, v41\n\t" \
  "v_cvt_pk_bf16_f32 v141, v42, v43\n\t" \
  "v_mfma_f32_16x16x32_bf16 a[124:127], a[184:187], v[152:155], 0\n\t" \
  "v_exp_f32 v79, v79\n\t" \
  "v_mfma_f32_16x16x32_bf16 a[124:127], a[188:191], v[156:159], a[124:127]\n\t" \
  "v_exp_f32 v96, v96\n\t" \
  "v_mfma_f32_16x16x32_bf16 v[172:175], v[216:219], v[152:155], 0\n\t" \
  "v_exp_f32 v97, v97\n\t" \
  "v_mfma_f32_16x16x32_bf16 v[172:175], v[216:219], v[156:159], v[172:175]\n\t" \
  "s_waitcnt lgkmcnt(0)\n\t" \
  "v_exp_f32 v98, v98\n\t" \
  STA("s_memrealtime s[94:95]\n\t") \
  STA("s_memtime s[92:93]\n\t") \
  "v_mfma_f32_16x16x32_bf16 v[0:3], a[192:195], v[16:19], v[176:179]\n\t" \
  "ds_read_b64_tr_b16 a[128:129], v237 offset:0\n\t" \
  "v_cvt_pk_bf16_f32 v142, v44, v45\n\t" \
  "v_mfma_f32_16x16x32_bf16 v[0:3], a[196:199], v[20:23], v[0:3]\n\t" \
  "v_exp_f32 v99, v99\n\t" \
  "v_mfma_f32_16x16x32_bf16 v[0:3], a[200:203], v[24:27], v[0:3]\n\t" \
  "ds_read_b64_tr_b16 a[130:131], v237 offset:4096\n\t" \
  "v_cvt_pk_bf16_f32 v143, v46, v47\n\t" \
  "v_mfma_f32_16x16x32_bf16 v[0:3], a[204:207], v[28:31], v[0:3]\n\t" \
  "v_exp_f32 v100, v100\n\t" \
  "v_mfma_f32_16x16x32_bf16 v[4:7], a[220:223], v[28:31], v[176:179]\n\t" \
  "ds_read_b64_tr_b16 a[132:133], v237 offset:8192\n\t" \
  "v_cvt_pk_bf16_f32 v144, v64, v65\n\t" \
  "v_mfma_f32_16x16x32_bf16 v[4:7], a[216:219], v[24:27], v[4:7]\n\t" \
  "v_exp_f32 v101, v101\n\t" \
  "v_mfma_f32_16x16x32_bf16 v[4:7], a[212:215], v[20:23], v[4:7]\n\t" \
  "ds_read_b64_tr_b16 a[134:135], v237 offset:12288\n\t" \
  "v_cvt_pk_bf16_f32 v145, v66, v67\n\t" \
  "v_mfma_f32_16x16x32_bf16 v[4:7], a[208:211], v[16:19], v[4:7]\n\t" \
  "ds_read_b64_tr_b16 a[136:137], v238 offset:0\n\t" \
  "v_cvt_pk_bf16_f32 v146, v68, v69\n\t" \
  "v_mfma_f32_16x16x32_bf16 v[8:11], a[224:227], v[16:19], v[176:179]\n\t" \
  "v_exp_f32 v102, v102\n\t" \
  "v_mfma_f32_16x16x32_bf16 v[8:11], a[228:231], v[20:23], v[8:11]\n\t" \
  "ds_read_b64_tr_b16 a[138:139], v238 offset:4096\n\t" \
  "v_cvt_pk_bf16_f32 v147, v70, v71\n\t" \
  "v_mfma_f32_16x16x32_bf16 v[8:11], a[232:235], v[24:27], v[8:11]\n\t" \
  "v_exp_f32 v103, v103\n\t" \
  "v_mfma_f32_16x16x32_bf16 v[8:11], a[236:239], v[28:31], v[8:11]\n\t" \
  "ds_read_b64_tr_b16 a[140:141], v238 offset:8192\n\t" \
  "v_cvt_pk_bf16_f32 v148, v72, v73\n\t" \
  "v_mfma_f32_16x16x32_bf16 v[12:15], a[252:255], v[28:31], v[176:179]\n\t" \
  STA("v_writelane_b32 %[stv], s94, 30\n\t") \
  STA("v_writelane_b32 %[stv], s92, 31\n\t") \
  "v_exp_f32 v104, v104\n\t" \
  "v_mfma_f32_16x16x32_bf16 v[12:15], a[248:251], v[24:27], v[12:15]\n\t" \
  "ds_read_b64_tr_b16 a[142:143], v238 offset:12288\n\t" \
  "v_cvt_pk_bf16_f32 v149, v74, v75\n\t" \
  "v_mfma_f32_16x16x32_bf16 v[12:15], a[244:247], v[20:23], v[12:15]\n\t" \
  "ds_read_b64_tr_b16 a[144:145], v237 offset:256\n\t" \
  "v_cvt_pk_bf16_f32 v150, v76, v77\n\t" \
  "v_mfma_f32_16x16x32_bf16 v[12:15], a[240:243], v[16:19], v[12:15]\n\t" \
  "v_exp_f32 v105, v105\n\t" \
  "v_mfma_f32_16x16x32_bf16 v[32:35], a[192:195], v[48:51], v[180:183]\n\t" \
  "ds_read_b64_tr_b16 a[146:147], v237 offset:4352\n\t" \
  "v_cvt_pk_bf16_f32 v151, v78, v79\n\t" \
  "v_mfma_f32_16x16x32_bf16 v[32:35], a[196:199], v[52:55], v[32:35]\n\t" \
  "v_exp_f32 v106, v106\n\t" \
  "v_mfma_f32_16x16x32_bf16 v[32:35], a[200:203], v[56:59], v[32:35]\n\t" \
  "ds_read_b64_tr_b16 a[148:149], v237 offset:8448\n\t" \
  "v_cvt_pk_bf16_f32 v152, v96, v97\n\t" \
  "v_mfma_f32_16x16x32_bf16 v[32:35], a[204:207], v[60:63], v[32:35]\n\t" \
  "v_exp_f32 v107, v107\n\t" \
  "v_mfma_f32_16x16x32_bf16 v[36:39], a[220:223], v[60:63], v[180:183]\n\t" \
  "ds_read_b64_tr_b16 a[150:151], v237 offset:12544\n\t" \
  "v_cvt_pk_bf16_f32 v153, v98, v99\n\t" \
  "v_mfma_f32_16x16x32_bf16 v[36:39], a[216:219], v[56:59], v[36:39]\n\t" \
  "ds_read_b64_tr_b16 a[152:153], v238 offset:256\n\t" \
  ".Lhs6:\n\ts_cbranch_execz .Lhs6n\n\t" \
  "v_cvt_pk_bf16_f32 v154, v100, v101\n\t" \
  "v_mfma_f32_16x16x32_bf16 v[36:39], a[212:215], v[52:55], v[36:39]\n\t" \
  "s_add_u32 m0, %[m], 32768\n\t" \
  "s_mov_b32 s96, 0\n\t" \
  "ds_read_b64_tr_b16 a[154:155], v238 offset:4352\n\t" \
  "v_mfma_f32_16x16x32_bf16 v[36:39], a[208:211], v[48:51], v[36:39]\n\t" \
  "buffer_load_dwordx4 v232, %[sG], s96 offen offset:0 lds\n\t" \
  "v_mfma_f32_16x16x32_bf16 v[40:43], a[224:227], v[48:51], v[180:183]\n\t" \
  "s_mov_b32 s96, 6144\n\t" \
  "v_exp_f32 v108, v108\n\t" \
  "v_mfma_f32_16x16x32_bf16 v[40:43], a[228:231], v[52:55], v[40:43]\n\t" \
  "ds_read_b64_tr_b16 a[156:157], v238 offset:8448\n\t" \
  "v_cvt_pk_bf16_f32 v155, v102, v103\n\t" \
  "v_mfma_f32_16x16x32_bf16 v[40:43], a[232:235], v[56:59], v[40:43]\n\t" \
  "v_exp_f32 v109, v109\n\t" \
  "v_mfma_f32_16x16x32_bf16 v[40:43], a[236:239], v[60:63], v[40:43]\n\t" \
  "ds_read_b64_tr_b16 a[158:159], v238 offset:12544\n\t" \
  "v_cvt_pk_bf16_f32 v156, v104, v105\n\t" \
  "v_mfma_f32_16x16x32_bf16 v[44:47], a[252:255], v[60:63], v[180:183]\n\t" \
  "ds_read_b64_tr_b16 a[160:161], v237 offset:512\n\t" \
  "v_cvt_pk_bf16_f32 v157, v106, v107\n\t" \
  "v_mfma_f32_16x16x32_bf16 v[44:47], a[248:251], v[56:59], v[44:47]\n\t" \
  "v_exp_f32 v110, v110\n\t" \
  "v_mfma_f32_16x16x32_bf16 v[44:47], a[244:247], v[52:55], v[44:47]\n\t" \
  "buffer_load_dwordx4 v232, %[sG], s96 offen offset:1024 lds\n\t" \
  "v_mfma_f32_16x16x32_bf16 v[44:47], a[240:243], v[48:51], v[44:47]\n\t" \
  "s_mov_b32 s96, 12288\n\t" \
  "ds_read_b64_tr_b16 a[162:163], v237 offset:4608\n\t" \
  "v_cvt_pk_bf16_f32 v158, v108, v109\n\t" \
  "v_mfma_f32_16x16x32_bf16 v[64:67], a[192:195], v[80:83], v[184:187]\n\t" \
  "v_exp_f32 v111, v111\n\t" \
  "v_mfma_f32_16x16x32_bf16 v[64:67], a[196:199], v[84:87], v[64:67]\n\t" \
  "ds_read_b64_tr_b16 a[164:165], v237 offset:8704\n\t" \
  "v_cvt_pk_bf16_f32 v159, v110, v111\n\t" \
  "v_mfma_f32_16x16x32_bf16 v[64:67], a[200:203], v[88:91], v[64:67]\n\t" \
  "v_exp_f32 v0, v0\n\t" \
  "v_mfma_f32_16x16x32_bf16 v[64:67], a[204:207], v[92:95], v[64:67]\n\t" \
  "v_exp_f32 v1, v1\n\t" \
  "v_mfma_f32_16x16x32_bf16 v[68:71], a[220:223], v[92:95], v[184:187]\n\t" \
  "ds_read_b64_tr_b16 a[166:167], v237 offset:12800\n\t" \
  "ds_read_b64_tr_b16 a[168:169], v238 offset:512\n\t" \
  "v_mfma_f32_16x16x32_bf16 v[68:71], a[216:219], v[88:91], v[68:71]\n\t" \
  "v_exp_f32 v2, v2\n\t" \
  "v_mfma_f32_16x16x32_bf16 v[68:71], a[212:215], v[84:87], v[68:71]\n\t" \
  "v_exp_f32 v3, v3\n\t" \
  "v_mfma_f32_16x16x32_bf16 v[68:71], a[208:211], v[80:83], v[68:71]\n\t" \
  "ds_read_b64_tr_b16 a[170:171], v238 offset:4608\n\t" \
  "ds_read_b64_tr_b16 a[172:173], v238 offset:8704\n\t" \
  "v_mfma_f32_16x16x32_bf16 v[72:75], a[224:227], v[80:83], v[184:187]\n\t" \
  "buffer_load_dwordx4 v232, %[sG], s96 offen offset:2048 lds\n\t" \
  "v_mfma_f32_16x16x32_bf16 v[72:75], a[228:231], v[84:87], v[72:75]\n\t" \
  "s_mov_b32 s96, 18432\n\t" \
  "v_exp_f32 v4, v4\n\t" \
  "v_mfma_f32_16x16x32_bf16 v[72:75], a[232:235], v[88:91], v[72:75]\n\t" \
  "v_exp_f32 v5, v5\n\t" \
  "v_mfma_f32_16x16x32_bf16 v[72:75], a[236:239], v[92:95], v[72:75]\n\t" \
  "ds_read_b64_tr_b16 a[174:175], v238 offset:12800\n\t" \
  "ds_read_b64_tr_b16 a[176:177], v237 offset:768\n\t" \
  "v_mfma_f32_16x16x32_bf16 v[76:79], a[252:255], v[92:95], v[184:187]\n\t" \
  "v_exp_f32 v6, v6\n\t" \
  "v_mfma_f32_16x16x32_bf16 v[76:79], a[248:251], v[88:91], v[76:79]\n\t" \
  "v_exp_f32 v7, v7\n\t" \
  "v_mfma_f32_16x16x32_bf16 v[76:79], a[244:247], v[84:87], v[76:79]\n\t" \
  "ds_read_b64_tr_b16 a[178:179], v237 offset:4864\n\t" \
  "ds_read_b64_tr_b16 a[180:181], v237 offset:8960\n\t" \
  "v_mfma_f32_16x16x32_bf16 v[76:79], a[240:243], v[80:83], v[76:79]\n\t" \
  "v_exp_f32 v8, v8\n\t" \
  "v_mfma_f32_16x16x32_bf16 v[96:99], a[192:195], v[112:115], v[188:191]\n\t" \
  "v_exp_f32 v9, v9\n\t" \
  "v_mfma_f32_16x16x32_bf16 v[96:99], a[196:199], v[116:119], v[96:99]\n\t" \
  "v_exp_f32 v10, v10\n\t" \
  "v_mfma_f32_16x16x32_bf16 v[96:99], a[200:203], v[120:123], v[96:99]\n\t" \
  "buffer_load_dwordx4 v232, %[sG], s96 offen offset:3072 lds\n\t" \
  "v_mfma_f32_16x16x32_bf16 v[96:99], a[204:207], v[124:127], v[96:99]\n\t" \
  "ds_read_b64_tr_b16 a[182:183], v237 offset:13056\n\t" \
  "ds_read_b64_tr_b16 a[184:185], v238 offset:768\n\t" \
  "v_mfma_f32_16x16x32_bf16 v[100:103], a[220:223], v[124:127], v[188:191]\n\t" \
  "v_exp_f32 v11, v11\n\t" \
  "v_mfma_f32_16x16x32_bf16 v[100:103], a[216:219], v[120:123], v[100:103]\n\t" \
  "ds_read_b64_tr_b16 a[186:187], v238 offset:4864\n\t" \
  "ds_read_b64_tr_b16 a[188:189], v238 offset:8960\n\t" \
  "v_mfma_f32_16x16x32_bf16 v[100:103], a[212:215], v[116:119], v[100:103]\n\t" \
  "v_exp_f32 v12, v12\n\t" \
  "v_mfma_f32_16x16x32_bf16 v[100:103], a[208:211], v[112:115], v[100:103]\n\t" \
  "v_exp_f32 v13, v13\n\t" \
  "v_mfma_f32_16x16x32_bf16 v[104:107], a[224:227], v[112:115], v[188:191]\n\t" \
  "v_exp_f32 v14, v14\n\t" \
  "v_mfma_f32_16x16x32_bf16 v[104:107], a[228:231], v[116:119], v[104:107]\n\t" \
  "ds_read_b64_tr_b16 a[190:191], v238 offset:13056\n\t" \
  "v_mfma_f32_16x16x32_bf16 v[104:107], a[232:235], v[120:123], v[104:107]\n\t" \
  "v_exp_f32 v15, v15\n\t" \
  "v_mfma_f32_16x16x32_bf16 v[104:107], a[236:239], v[124:127], v[104:107]\n\t" \
  "v_exp_f32 v32, v32\n\t" \
  "v_mfma_f32_16x16x32_bf16 v[108:111], a[252:255], v[124:127], v[188:191]\n\t" \
  "v_exp_f32 v33, v33\n\t" \
  "v_mfma_f32_16x16x32_bf16 v[108:111], a[248:251], v[120:123], v[108:111]\n\t" \
  "v_exp_f32 v34, v34\n\t" \
  "v_mfma_f32_16x16x32_bf16 v[108:111], a[244:247], v[116:119], v[108:111]\n\t" \
  "v_exp_f32 v35, v35\n\t" \
  "v_mfma_f32_16x16x32_bf16 v[108:111], a[240:243], v[112:115], v[108:111]\n\t" \
  "s_waitcnt vmcnt(4) lgkmcnt(0)\n\t" \
  "s_barrier\n\t" \
  STA("s_memrealtime s[94:95]\n\t") \
  STA("s_memtime s[92:93]\n\t") \
  "v_mfma_f32_16x16x32_bf16 a[0:3], a[128:131], v[128:131], a[0:3]\n\t" \
  "v_exp_f32 v36, v36\n\t" \
  "v_mfma_f32_16x16x32_bf16 a[0:3], a[132:135], v[132:135], a[0:3]\n\t" \
  "v_exp_f32 v37, v37\n\t" \
  "v_mfma_f32_16x16x32_bf16 a[16:19], a[136:139], v[128:131], a[16:19]\n\t" \
  "ds_read_b128 a[192:195], v220 offset:16384\n\t" \
  "ds_read_b128 a[196:199], v221 offset:16384\n\t" \
  "v_mfma_f32_16x16x32_bf16 a[16:19], a[140:143], v[132:135], a[16:19]\n\t" \
  "v_exp_f32 v38, v38\n\t" \
  "v_mfma_f32_16x16x32_bf16 a[32:35], a[144:147], v[128:131], a[32:35]\n\t" \
  "v_exp_f32 v39, v39\n\t" \
  "v_mfma_f32_16x16x32_bf16 a[32:35], a[148:151], v[132:135], a[32:35]\n\t" \
  "v_exp_f32 v40, v40\n\t" \
  "v_mfma_f32_16x16x32_bf16 a[48:51], a[152:155], v[128:131], a[48:51]\n\t" \
  "v_exp_f32 v41, v41\n\t" \
  "v_mfma_f32_16x16x32_bf16 a[48:51], a[156:159], v[132:135], a[48:51]\n\t" \
  "v_exp_f32 v42, v42\n\t" \
  "v_mfma_f32_16x16x32_bf16 a[64:67], a[160:163], v[128:131], a[64:67]\n\t" \
  "v_exp_f32 v43, v43\n\t" \
  "v_mfma_f32_16x16x32_bf16 a[64:67], a[164:167], v[132:135], a[64:67]\n\t" \
  "v_exp_f32 v44, v44\n\t" \
  "v_mfma_f32_16x16x32_bf16 a[80:83], a[168:171], v[128:131], a[80:83]\n\t" \
  "ds_read_b128 a[200:203], v222 offset:16384\n\t" \
  "ds_read_b128 a[204:207], v223 offset:16384\n\t" \
  "v_mfma_f32_16x16x32_bf16 a[80:83], a[172:175], v[132:135], a[80:83]\n\t" \
  "v_exp_f32 v45, v45\n\t" \
  "v_mfma_f32_16x16x32_bf16 a[96:99], a[176:179], v[128:131], a[96:99]\n\t" \
  STA("v_writelane_b32 %[stv], s94, 32\n\t") \
  STA("v_writelane_b32 %[stv], s92, 33\n\t") \
  "buffer_store_dword v233, v247, %[ws], 0 offen sc1\n\t" \
  "v_mfma_f32_16x16x32_bf16 a[96:99], a[180:183], v[132:135], a[96:99]\n\t" \
  "v_exp_f32 v46, v46\n\t" \
  "v_mfma_f32_16x16x32_bf16 a[112:115], a[184:187], v[128:131], a[112:115]\n\t" \
  "v_exp_f32 v47, v47\n\t" \
  "v_mfma_f32_16x16x32_bf16 a[112:115], a[188:191], v[132:135], a[112:115]\n\t" \
  "v_exp_f32 v64, v64\n\t" \
  "v_mfma_f32_16x16x32_bf16 v[160:163], v[216:219], v[128:131], v[160:163]\n\t" \
  "v_exp_f32 v65, v65\n\t" \
  "v_mfma_f32_16x16x32_bf16 v[160:163], v[216:219], v[132:135], v[160:163]\n\t" \
  "ds_read_b128 a[208:211], v220 offset:20480\n\t" \
  "v_cvt_pk_bf16_f32 v128, v0, v1\n\t" \
  "v_mfma_f32_16x16x32_bf16 a[4:7], a[128:131], v[136:139], a[4:7]\n\t" \
  "v_exp_f32 v66, v66\n\t" \
  "v_mfma_f32_16x16x32_bf16 a[4:7], a[132:135], v[140:143], a[4:7]\n\t" \
  "ds_read_b128 a[212:215], v221 offset:20480\n\t" \
  "v_cvt_pk_bf16_f32 v129, v2, v3\n\t" \
  "v_mfma_f32_16x16x32_bf16 a[20:23], a[136:139], v[136:139], a[20:23]\n\t" \
  "s_add_u32 m0, %[m], 49152\n\t" \
  "v_exp_f32 v67, v67\n\t" \
  "v_mfma_f32_16x16x32_bf16 a[20:23], a[140:143], v[140:143], a[20:23]\n\t" \
  "s_add_u32 s96, %[sofk], 65536\n\t" \
  "v_exp_f32 v68, v68\n\t" \
  "v_mfma_f32_16x16x32_bf16 a[36:39], a[144:147], v[136:139], a[36:39]\n\t" \
  "v_exp_f32 v69, v69\n\t" \
  "v_mfma_f32_16x16x32_bf16 a[36:39], a[148:151], v[140:143], a[36:39]\n\t" \
  "ds_read_b128 a[216:219], v222 offset:20480\n\t" \
  "v_cvt_pk_bf16_f32 v130, v4, v5\n\t" \
  "v_mfma_f32_16x16x32_bf16 a[52:55], a[152:155], v[136:139], a[52:55]\n\t" \
  "v_exp_f32 v70, v70\n\t" \
  "v_mfma_f32_16x16x32_bf16 a[52:55], a[156:159], v[140:143], a[52:55]\n\t" \
  "v_exp_f32 v71, v71\n\t" \
  "v_mfma_f32_16x16x32_bf16 a[68:71], a[160:163], v[136:139], a[68:71]\n\t" \
  "buffer_load_dwordx4 v224, %[sK], s96 offen offset:0 lds\n\t" \
  "v_mfma_f32_16x16x32_bf16 a[68:71], a[164:167], v[140:143], a[68:71]\n\t" \
  "ds_read_b128 a[220:223], v223 offset:20480\n\t" \
  "v_cvt_pk_bf16_f32 v131, v6, v7\n\t" \
  "v_mfma_f32_16x16x32_bf16 a[84:87], a[168:171], v[136:139], a[84:87]\n\t" \
  "v_exp_f32 v72, v72\n\t" \
  "v_mfma_f32_16x16x32_bf16 a[84:87], a[172:175], v[140:143], a[84:87]\n\t" \
  "buffer_load_dwordx4 v225, %[sK], s96 offen offset:1024 lds\n\t" \
  "v_mfma_f32_16x16x32_bf16 a[100:103], a[176:179], v[136:139], a[100:103]\n\t" \
  "ds_read_b128 a[224:227], v220 offset:24576\n\t" \
  "v_cvt_pk_bf16_f32 v132, v8, v9\n\t" \
  "v_mfma_f32_16x16x32_bf16 a[100:103], a[180:183], v[140:143], a[100:103]\n\t" \
  "v_exp_f32 v73, v73\n\t" \
  "v_mfma_f32_16x16x32_bf16 a[116:119], a[184:187], v[136:139], a[116:119]\n\t" \
  "buffer_load_dwordx4 v226, %[sK], s96 offen offset:2048 lds\n\t" \
  "v_mfma_f32_16x16x32_bf16 a[116:119], a[188:191], v[140:143], a[116:119]\n\t" \
  "v_exp_f32 v74, v74\n\t" \
  "v_mfma_f32_16x16x32_bf16 v[164:167], v[216:219], v[136:139], v[164:167]\n\t" \
  "ds_read_b128 a[228:231], v221 offset:24576\n\t" \
  "v_cvt_pk_bf16_f32 v133, v10, v11\n\t" \
  "v_mfma_f32_16x16x32_bf16 v[164:167], v[216:219], v[140:143], v[164:167]\n\t" \
  "v_exp_f32 v75, v75\n\t" \
  "v_mfma_f32_16x16x32_bf16 a[8:11], a[128:131], v[144:147], a[8:11]\n\t" \
  "buffer_load_dwordx4 v227, %[sK], s96 offen offset:3072 lds\n\t" \
  "v_mfma_f32_16x16x32_bf16 a[8:11], a[132:135], v[148:151], a[8:11]\n\t" \
  "s_add_u32 m0, %[m], 147456\n\t" \
  "v_exp_f32 v76, v76\n\t" \
  "v_mfma_f32_16x16x32_bf16 a[24:27], a[136:139], v[144:147], a[24:27]\n\t" \
  "s_add_u32 s96, %[sofk], 49152\n\t" \
  "ds_read_b128 a[232:235], v222 offset:24576\n\t" \
  "v_cvt_pk_bf16_f32 v134, v12, v13\n\t" \
  "v_mfma_f32_16x16x32_bf16 a[24:27], a[140:143], v[148:151], a[24:27]\n\t" \
  "v_exp_f32 v77, v77\n\t" \
  "v_mfma_f32_16x16x32_bf16 a[40:43], a[144:147], v[144:147], a[40:43]\n\t" \
  "v_exp_f32 v78, v78\n\t" \
  "v_mfma_f32_16x16x32_bf16 a[40:43], a[148:151], v[148:151], a[40:43]\n\t" \
  "v_exp_f32 v79, v79\n\t" \
  "v_mfma_f32_16x16x32_bf16 a[56:59], a[152:155], v[144:147], a[56:59]\n\t" \
  "ds_read_b128 a[236:239], v223 offset:24576\n\t" \
  "v_cvt_pk_bf16_f32 v135, v14, v15\n\t" \
  "v_mfma_f32_16x16x32_bf16 a[56:59], a[156:159], v[148:151], a[56:59]\n\t" \
  "v_exp_f32 v96, v96\n\t" \
  "v_mfma_f32_16x16x32_bf16 a[72:75], a[160:163], v[144:147], a[72:75]\n\t" \
  "buffer_load_dwordx4 v230, %[sV], s96 offen offset:0 lds\n\t" \
  "v_mfma_f32_16x16x32_bf16 a[72:75], a[164:167], v[148:151], a[72:75]\n\t" \
  "ds_read_b128 a[240:243], v220 offset:28672\n\t" \
  "v_cvt_pk_bf16_f32 v136, v32, v33\n\t" \
  "v_mfma_f32_16x16x32_bf16 a[88:91], a[168:171], v[144:147], a[88:91]\n\t" \
  "v_exp_f32 v97, v97\n\t" \
  "v_mfma_f32_16x16x32_bf16 a[88:91], a[172:175], v[148:151], a[88:91]\n\t" \
  "buffer_load_dwordx4 v231, %[sV], s96 offen offset:1024 lds\n\t" \
  "v_mfma_f32_16x16x32_bf16 a[104:107], a[176:179], v[144:147], a[104:107]\n\t" \
  "v_exp_f32 v98, v98\n\t" \
  "v_mfma_f32_16x16x32_bf16 a[104:107], a[180:183], v[148:151], a[104:107]\n\t" \
  "ds_read_b128 a[244:247], v221 offset:28672\n\t" \
  "v_cvt_pk_bf16_f32 v137, v34, v35\n\t" \
  "v_mfma_f32_16x16x32_bf16 a[120:123], a[184:187], v[144:147], a[120:123]\n\t" \
  "buffer_load_dwordx4 v230, %[sV], s96 offen offset:2048 lds\n\t" \
  "v_mfma_f32_16x16x32_bf16 a[120:123], a[188:191], v[148:151], a[120:123]\n\t" \
  "v_exp_f32 v99, v99\n\t" \
  "v_mfma_f32_16x16x32_bf16 v[168:171], v[216:219], v[144:147], v[168:171]\n\t" \
  "v_exp_f32 v100, v100\n\t" \
  "v_mfma_f32_16x16x32_bf16 v[168:171], v[216:219], v[148:151], v[168:171]\n\t" \
  "ds_read_b128 a[248:251], v222 offset:28672\n\t" \
  "v_cvt_pk_bf16_f32 v138, v36, v37\n\t" \
  "v_mfma_f32_16x16x32_bf16 a[12:15], a[128:131], v[152:155], a[12:15]\n\t" \
  "buffer_load_dwordx4 v231, %[sV], s96 offen offset:3072 lds\n\t" \
  "v_mfma_f32_16x16x32_bf16 a[12:15], a[132:135], v[156:159], a[12:15]\n\t" \
  "v_exp_f32 v101, v101\n\t" \
  "v_mfma_f32_16x16x32_bf16 a[28:31], a[136:139], v[152:155], a[28:31]\n\t" \
  "v_cvt_pk_bf16_f32 v139, v38, v39\n\t" \
  "v_cvt_pk_bf16_f32 v140, v40, v41\n\t" \
  "v_mfma_f32_16x16x32_bf16 a[28:31], a[140:143], v[156:159], a[28:31]\n\t" \
  "ds_read_b128 a[252:255], v223 offset:28672\n\t" \
  "v_cvt_pk_bf16_f32 v141, v42, v43\n\t" \
  "v_mfma_f32_16x16x32_bf16 a[44:47], a[144:147], v[152:155], a[44:47]\n\t" \
  "s_add_u32 m0, %[m], 98304\n\t" \
  "s_mov_b32 s96, 28672\n\t" \
  "v_cvt_pk_bf16_f32 v142, v44, v45\n\t" \
  "v_mfma_f32_16x16x32_bf16 a[44:47], a[148:151], v[156:159], a[44:47]\n\t" \
  "buffer_load_dwordx4 v232, %[sG], s96 offen offset:0 lds\n\t" \
  "v_mfma_f32_16x16x32_bf16 a[60:63], a[152:155], v[152:155], a[60:63]\n\t" \
  "s_mov_b32 s96, 34816\n\t" \
  "v_exp_f32 v102, v102\n\t" \
  "v_mfma_f32_16x16x32_bf16 a[60:63], a[156:159], v[156:159], a[60:63]\n\t" \
  "v_exp_f32 v103, v103\n\t" \
  "v_mfma_f32_16x16x32_bf16 a[76:79], a[160:163], v[152:155], a[76:79]\n\t" \
  "v_exp_f32 v104, v104\n\t" \
  "v_mfma_f32_16x16x32_bf16 a[76:79], a[164:167], v[156:159], a[76:79]\n\t" \
  "buffer_load_dwordx4 v232, %[sG], s96 offen offset:1024 lds\n\t" \
  "v_mfma_f32_16x16x32_bf16 a[92:95], a[168:171], v[152:155], a[92:95]\n\t" \
  "s_mov_b32 s96, 40960\n\t" \
  "v_exp_f32 v105, v105\n\t" \
  "v_mfma_f32_16x16x32_bf16 a[92:95], a[172:175], v[156:159], a[92:95]\n\t" \
  "v_exp_f32 v106, v106\n\t" \
  "v_mfma_f32_16x16x32_bf16 a[108:111], a[176:179], v[152:155], a[108:111]\n\t" \
  "v_exp_f32 v107, v107\n\t" \
  "v_mfma_f32_16x16x32_bf16 a[108:111], a[180:183], v[156:159], a[108:111]\n\t" \
  "buffer_load_dwordx4 v232, %[sG], s96 offen offset:2048 lds\n\t" \
  "v_mfma_f32_16x16x32_bf16 a[124:127], a[184:187], v[152:155], a[124:127]\n\t" \
  "s_mov_b32 s96, 47104\n\t" \
  "v_cvt_pk_bf16_f32 v143, v46, v47\n\t" \
  "v_cvt_pk_bf16_f32 v144, v64, v65\n\t" \
  "v_mfma_f32_16x16x32_bf16 a[124:127], a[188:191], v[156:159], a[124:127]\n\t" \
  "v_exp_f32 v108, v108\n\t" \
  "v_mfma_f32_16x16x32_bf16 v[172:175], v[216:219], v[152:155], v[172:175]\n\t" \
  "v_exp_f32 v109, v109\n\t" \
  "v_mfma_f32_16x16x32_bf16 v[172:175], v[216:219], v[156:159], v[172:175]\n\t" \
  "s_waitcnt lgkmcnt(0)\n\t" \
  "v_exp_f32 v110, v110\n\t" \
  "buffer_load_dwordx4 v232, %[sG], s96 offen offset:3072 lds\n\t" \
  STA("s_memrealtime s[94:95]\n\t") \
  STA("s_memtime s[92:93]\n\t") \
  "v_mfma_f32_16x16x32_bf16 v[0:3], a[192:195], v[16:19], v[176:179]\n\t" \
  "ds_read_b64_tr_b16 a[128:129], v228 offset:0\n\t" \
  "v_cvt_pk_bf16_f32 v145, v66, v67\n\t" \
  "v_mfma_f32_16x16x32_bf16 v[0:3], a[196:199], v[20:23], v[0:3]\n\t" \
  "s_mul_i32 s98, %[sofk], 5\n\t" \
  "s_xor_b32 s98, s98, 0\n\t" \
  "s_lshr_b32 s98, s98, 1\n\t" \
  "buffer_load_dword %[pfd], v204, %[sD], s98 offen\n\t" \
  "v_mfma_f32_16x16x32_bf16 v[0:3], a[200:203], v[24:27], v[0:3]\n\t" \
  "ds_read_b64_tr_b16 a[130:131], v228 offset:4096\n\t" \
  "v_cvt_pk_bf16_f32 v146, v68, v69\n\t" \
  "v_mfma_f32_16x16x32_bf16 v[0:3], a[204:207], v[28:31], v[0:3]\n\t" \
  "v_exp_f32 v111, v111\n\t" \
  "v_mfma_f32_16x16x32_bf16 v[4:7], a[220:223], v[28:31], v[176:179]\n\t" \
  "ds_read_b64_tr_b16 a[132:133], v228 offset:8192\n\t" \
  "v_cvt_pk_bf16_f32 v147, v70, v71\n\t" \
  "v_mfma_f32_16x16x32_bf16 v[4:7], a[216:219], v[24:27], v[4:7]\n\t" \
  "ds_read_b64_tr_b16 a[134:135], v228 offset:12288\n\t" \
  "v_cvt_pk_bf16_f32 v148, v72, v73\n\t" \
  "v_mfma_f32_16x16x32_bf16 v[4:7], a[212:215], v[20:23], v[4:7]\n\t" \
  "v_exp_f32 v0, v0\n\t" \
  "v_mfma_f32_16x16x32_bf16 v[4:7], a[208:211], v[16:19], v[4:7]\n\t" \
  "ds_read_b64_tr_b16 a[136:137], v229 offset:0\n\t" \
  "v_cvt_pk_bf16_f32 v149, v74, v75\n\t" \
  "v_mfma_f32_16x16x32_bf16 v[8:11], a[224:227], v[16:19], v[176:179]\n\t" \
  "v_exp_f32 v1, v1\n\t" \
  "v_mfma_f32_16x16x32_bf16 v[8:11], a[228:231], v[20:23], v[8:11]\n\t" \
  "ds_read_b64_tr_b16 a[138:139], v229 offset:4096\n\t" \
  "v_cvt_pk_bf16_f32 v150, v76, v77\n\t" \
  "v_mfma_f32_16x16x32_bf16 v[8:11], a[232:235], v[24:27], v[8:11]\n\t" \
  "v_exp_f32 v2, v2\n\t" \
  "v_mfma_f32_16x16x32_bf16 v[8:11], a[236:239], v[28:31], v[8:11]\n\t" \
  "ds_read_b64_tr_b16 a[140:141], v229 offset:8192\n\t" \
  "v_cvt_pk_bf16_f32 v151, v78, v79\n\t" \
  "v_mfma_f32_16x16x32_bf16 v[12:15], a[252:255], v[28:31], v[176:179]\n\t" \
  STA("v_writelane_b32 %[stv], s94, 34\n\t") \
  STA("v_writelane_b32 %[stv], s92, 35\n\t") \
  "s_mul_i32 s98, %[sofk], 5\n\t" \
  "s_xor_b32 s98, s98, 4096\n\t" \
  "s_lshr_b32 s98, s98, 1\n\t" \
  "v_cvt_pk_bf16_f32 v152, v96, v97\n\t" \
  "v_mfma_f32_16x16x32_bf16 v[12:15], a[248:251], v[24:27], v[12:15]\n\t" \
  "buffer_load_dword %[pfd], v204, %[sD], s98 offen\n\t" \
  "v_mfma_f32_16x16x32_bf16 v[12:15], a[244:247], v[20:23], v[12:15]\n\t" \
  "ds_read_b64_tr_b16 a[142:143], v229 offset:12288\n\t" \
  "v_cvt_pk_bf16_f32 v153, v98, v99\n\t" \
  "v_mfma_f32_16x16x32_bf16 v[12:15], a[240:243], v[16:19], v[12:15]\n\t" \
  "ds_read_b64_tr_b16 a[144:145], v228 offset:256\n\t" \
  "v_cvt_pk_bf16_f32 v154, v100, v101\n\t" \
  "v_mfma_f32_16x16x32_bf16 v[32:35], a[192:195], v[48:51], v[180:183]\n\t" \
  "ds_read_b64_tr_b16 a[146:147], v228 offset:4352\n\t" \
  "v_cvt_pk_bf16_f32 v155, v102, v103\n\t" \
  "v_mfma_f32_16x16x32_bf16 v[32:35], a[196:199], v[52:55], v[32:35]\n\t" \
  "v_exp_f32 v3, v3\n\t" \
  "v_mfma_f32_16x16x32_bf16 v[32:35], a[200:203], v[56:59], v[32:35]\n\t" \
  "ds_read_b64_tr_b16 a[148:149], v228 offset:8448\n\t" \
  "v_cvt_pk_bf16_f32 v156, v104, v105\n\t" \
  "v_mfma_f32_16x16x32_bf16 v[32:35], a[204:207], v[60:63], v[32:35]\n\t" \
  "v_exp_f32 v4, v4\n\t" \
  "v_mfma_f32_16x16x32_bf16 v[36:39], a[220:223], v[60:63], v[180:183]\n\t" \
  "ds_read_b64_tr_b16 a[150:151], v228 offset:12544\n\t" \
  "v_cvt_pk_bf16_f32 v157, v106, v107\n\t" \
  "v_mfma_f32_16x16x32_bf16 v[36:39], a[216:219], v[56:59], v[36:39]\n\t" \
  "ds_read_b64_tr_b16 a[152:153], v229 offset:256\n\t" \
  "v_cvt_pk_bf16_f32 v158, v108, v109\n\t" \
  "v_mfma_f32_16x16x32_bf16 v[36:39], a[212:215], v[52:55], v[36:39]\n\t" \
  "v_exp_f32 v5, v5\n\t" \
  "v_mfma_f32_16x16x32_bf16 v[36:39], a[208:211], v[48:51], v[36:39]\n\t" \
  "ds_read_b64_tr_b16 a[154:155], v229 offset:4352\n\t" \
  "v_cvt_pk_bf16_f32 v159, v110, v111\n\t" \
  "v_mfma_f32_16x16x32_bf16 v[40:43], a[224:227], v[48:51], v[180:183]\n\t" \
  "v_exp_f32 v6, v6\n\t" \
  "v_mfma_f32_16x16x32_bf16 v[40:43], a[228:231], v[52:55], v[40:43]\n\t" \
  "v_exp_f32 v7, v7\n\t" \
  "v_mfma_f32_16x16x32_bf16 v[40:43], a[232:235], v[56:59], v[40:43]\n\t" \
  "v_exp_f32 v8, v8\n\t" \
  "v_mfma_f32_16x16x32_bf16 v[40:43], a[236:239], v[60:63], v[40:43]\n\t" \
  "ds_read_b64_tr_b16 a[156:157], v229 offset:8448\n\t" \
  "ds_read_b64_tr_b16 a[158:159], v229 offset:12544\n\t" \
  "v_mfma_f32_16x16x32_bf16 v[44:47], a[252:255], v[60:63], v[180:183]\n\t" \
  "v_exp_f32 v9, v9\n\t" \
  "v_mfma_f32_16x16x32_bf16 v[44:47], a[248:251], v[56:59], v[44:47]\n\t" \
  "v_exp_f32 v10, v10\n\t" \
  "v_mfma_f32_16x16x32_bf16 v[44:47], a[244:247], v[52:55], v[44:47]\n\t" \
  "ds_read_b64_tr_b16 a[160:161], v228 offset:512\n\t" \
  "ds_read_b64_tr_b16 a[162:163], v228 offset:4608\n\t" \
  "v_mfma_f32_16x16x32_bf16 v[44:47], a[240:243], v[48:51], v[44:47]\n\t" \
  "v_exp_f32 v11, v11\n\t" \
  "v_mfma_f32_16x16x32_bf16 v[64:67], a[192:195], v[80:83], v[184:187]\n\t" \
  "buffer_load_dword %[pfd], v247, %[ws], 0 offen sc1\n\t" \
  "v_mfma_f32_16x16x32_bf16 v[64:67], a[196:199], v[84:87], v[64:67]\n\t" \
  "v_exp_f32 v12, v12\n\t" \
  "v_mfma_f32_16x16x32_bf16 v[64:67], a[200:203], v[88:91], v[64:67]\n\t" \
  "ds_read_b64_tr_b16 a[164:165], v228 offset:8704\n\t" \
  "ds_read_b64_tr_b16 a[166:167], v228 offset:12800\n\t" \
  "v_mfma_f32_16x16x32_bf16 v[64:67], a[204:207], v[92:95], v[64:67]\n\t" \
  "v_exp_f32 v13, v13\n\t" \
  "v_mfma_f32_16x16x32_bf16 v[68:71], a[220:223], v[92:95], v[184:187]\n\t" \
  "v_exp_f32 v14, v14\n\t" \
  "v_mfma_f32_16x16x32_bf16 v[68:71], a[216:219], v[88:91], v[68:71]\n\t" \
  "ds_read_b64_tr_b16 a[168:169], v229 offset:512\n\t" \
  "ds_read_b64_tr_b16 a[170:171], v229 offset:4608\n\t" \
  "v_mfma_f32_16x16x32_bf16 v[68:71], a[212:215], v[84:87], v[68:71]\n\t" \
  "v_exp_f32 v15, v15\n\t" \
  "v_mfma_f32_16x16x32_bf16 v[68:71], a[208:211], v[80:83], v[68:71]\n\t" \
  "v_exp_f32 v32, v32\n\t" \
  "v_mfma_f32_16x16x32_bf16 v[72:75], a[224:227], v[80:83], v[184:187]\n\t" \
  "v_exp_f32 v33, v33\n\t" \
  "v_mfma_f32_16x16x32_bf16 v[72:75], a[228:231], v[84:87], v[72:75]\n\t" \
  "ds_read_b64_tr_b16 a[172:173], v229 offset:8704\n\t" \
  "ds_read_b64_tr_b16 a[174:175], v229 offset:12800\n\t" \
  "v_mfma_f32_16x16x32_bf16 v[72:75], a[232:235], v[88:91], v[72:75]\n\t" \
  "v_exp_f32 v34, v34\n\t" \
  "v_mfma_f32_16x16x32_bf16 v[72:75], a[236:239], v[92:95], v[72:75]\n\t" \
  "v_exp_f32 v35, v35\n\t" \
  "v_mfma_f32_16x16x32_bf16 v[76:79], a[252:255], v[92:95], v[184:187]\n\t" \
  "ds_read_b64_tr_b16 a[176:177], v228 offset:768\n\t" \
  "ds_read_b64_tr_b16 a[178:179], v228 offset:4864\n\t" \
  "v_mfma_f32_16x16x32_bf16 v[76:79], a[248:251], v[88:91], v[76:79]\n\t" \
  "v_exp_f32 v36, v36\n\t" \
  "v_mfma_f32_16x16x32_bf16 v[76:79], a[244:247], v[84:87], v[76:79]\n\t" \
  "v_exp_f32 v37, v37\n\t" \
  "v_mfma_f32_16x16x32_bf16 v[76:79], a[240:243], v[80:83], v[76:79]\n\t" \
  "v_exp_f32 v38, v38\n\t" \
  "v_mfma_f32_16x16x32_bf16 v[96:99], a[192:195], v[112:115], v[188:191]\n\t" \
  "ds_read_b64_tr_b16 a[180:181], v228 offset:8960\n\t" \
  "ds_read_b64_tr_b16 a[182:183], v228 offset:13056\n\t" \
  "v_mfma_f32_16x16x32_bf16 v[96:99], a[196:199], v[116:119], v[96:99]\n\t" \
  "v_exp_f32 v39, v39\n\t" \
  "v_mfma_f32_16x16x32_bf16 v[96:99], a[200:203], v[120:123], v[96:99]\n\t" \
  "v_exp_f32 v40, v40\n\t" \
  "v_mfma_f32_16x16x32_bf16 v[96:99], a[204:207], v[124:127], v[96:99]\n\t" \
  "ds_read_b64_tr_b16 a[184:185], v229 offset:768\n\t" \
  "ds_read_b64_tr_b16 a[186:187], v229 offset:4864\n\t" \
  "v_mfma_f32_16x16x32_bf16 v[100:103], a[220:223], v[124:127], v[188:191]\n\t" \
  "v_exp_f32 v41, v41\n\t" \
  "v_mfma_f32_16x16x32_bf16 v[100:103], a[216:219], v[120:123], v[100:103]\n\t" \
  "v_exp_f32 v42, v42\n\t" \
  "v_mfma_f32_16x16x32_bf16 v[100:103], a[212:215], v[116:119], v[100:103]\n\t" \
  "v_exp_f32 v43, v43\n\t" \
  "v_mfma_f32_16x16x32_bf16 v[100:103], a[208:211], v[112:115], v[100:103]\n\t" \
  "ds_read_b64_tr_b16 a[188:189], v229 offset:8960\n\t" \
  "ds_read_b64_tr_b16 a[190:191], v229 offset:13056\n\t" \
  "v_mfma_f32_16x16x32_bf16 v[104:107], a[224:227], v[112:115], v[188:191]\n\t" \
  "v_exp_f32 v44, v44\n\t" \
  "v_mfma_f32_16x16x32_bf16 v[104:107], a[228:231], v[116:119], v[104:107]\n\t" \
  "v_exp_f32 v45, v45\n\t" \
  "v_mfma_f32_16x16x32_bf16 v[104:107], a[232:235], v[120:123], v[104:107]\n\t" \
  "s_getreg_b32 s98, hwreg(HW_REG_IB_STS, 0, 4)\n\t" \
  "s_cmp_lg_u32 s98, 0\n\t" \
  "s_addc_u32 %[na], %[na], 0\n\t" \
  "v_mfma_f32_16x16x32_bf16 v[104:107], a[236:239], v[124:127], v[104:107]\n\t" \
  "v_exp_f32 v46, v46\n\t" \
  "v_mfma_f32_16x16x32_bf16 v[108:111], a[252:255], v[124:127], v[188:191]\n\t" \
  "v_exp_f32 v47, v47\n\t" \
  "v_mfma_f32_16x16x32_bf16 v[108:111], a[248:251], v[120:123], v[108:111]\n\t" \
  "s_getreg_b32 s98, hwreg(HW_REG_IB_STS, 0, 4)\n\t" \
  "s_cmp_lg_u32 s98, 0\n\t" \
  "s_addc_u32 %[na], %[na], 0\n\t" \
  "v_mfma_f32_16x16x32_bf16 v[108:111], a[244:247], v[116:119], v[108:111]\n\t" \
  "v_exp_f32 v64, v64\n\t" \
  "v_mfma_f32_16x16x32_bf16 v[108:111], a[240:243], v[112:115], v[108:111]\n\t" \
  "s_waitcnt vmcnt(7) lgkmcnt(0)\n\t" \
  "s_barrier\n\t" \
  STA("s_memrealtime s[94:95]\n\t") \
  STA("s_memtime s[92:93]\n\t") \
  "v_mfma_f32_16x16x32_bf16 a[0:3], a[128:131], v[128:131], a[0:3]\n\t" \
  "v_exp_f32 v65, v65\n\t" \
  "v_mfma_f32_16x16x32_bf16 a[0:3], a[132:135], v[132:135], a[0:3]\n\t" \
  "v_exp_f32 v66, v66\n\t" \
  "v_mfma_f32_16x16x32_bf16 a[16:19], a[136:139], v[128:131], a[16:19]\n\t" \
  "ds_read_b128 a[192:195], v220 offset:0\n\t" \
  "ds_read_b128 a[196:199], v221 offset:0\n\t" \
  "v_mfma_f32_16x16x32_bf16 a[16:19], a[140:143], v[132:135], a[16:19]\n\t" \
  "v_exp_f32 v67, v67\n\t" \
  "v_mfma_f32_16x16x32_bf16 a[32:35], a[144:147], v[128:131], a[32:35]\n\t" \
  "v_exp_f32 v68, v68\n\t" \
  "v_mfma_f32_16x16x32_bf16 a[32:35], a[148:151], v[132:135], a[32:35]\n\t" \
  "v_exp_f32 v69, v69\n\t" \
  "v_mfma_f32_16x16x32_bf16 a[48:51], a[152:155], v[128:131], a[48:51]\n\t" \
  STS("v_writelane_b32 %[stv], %[na], 58\n\t") \
  "s_cmp_ge_u32 %[na], 1\n\t" \
  "s_cselect_b32 s98, 0x4000, 0\n\t" \
  "s_or_b32 s98, s98, %[tag]\n\t" \
  "s_cmp_ge_u32 %[na], 2\n\t" \
  "s_cselect_b32 %[na], 2.0, 0\n\t" \
  "s_or_b32 s98, s98, %[na]\n\t" \
  "v_mov_b32 v209, s98\n\t" \
  "v_mfma_f32_16x16x32_bf16 a[48:51], a[156:159], v[132:135], a[48:51]\n\t" \
  "v_exp_f32 v70, v70\n\t" \
  "v_mfma_f32_16x16x32_bf16 a[64:67], a[160:163], v[128:131], a[64:67]\n\t" \
  "ds_read_b128 a[200:203], v222 offset:0\n\t" \
  "ds_read_b128 a[204:207], v223 offset:0\n\t" \
  "v_mfma_f32_16x16x32_bf16 a[64:67], a[164:167], v[132:135], a[64:67]\n\t" \
  "v_exp_f32 v71, v71\n\t" \
  "v_mfma_f32_16x16x32_bf16 a[80:83], a[168:171], v[128:131], a[80:83]\n\t" \
  "ds_read_b128 a[208:211], v220 offset:4096\n\t" \
  "ds_read_b128 a[212:215], v221 offset:4096\n\t" \
  "v_mfma_f32_16x16x32_bf16 a[80:83], a[172:175], v[132:135], a[80:83]\n\t" \
  "v_bfrev_b32 v205, 1\n\t" \
  "v_writelane_b32 v205, %[vo], 0\n\t" \
  "v_mfma_f32_16x16x32_bf16 a[96:99], a[176:179], v[128:131], a[96:99]\n\t" \
  STA("v_writelane_b32 %[stv], s94, 36\n\t") \
  STA("v_writelane_b32 %[stv], s92, 37\n\t") \
  "buffer_store_dword v209, v205, %[ws], 0 offen sc1\n\t" \
  "v_mfma_f32_16x16x32_bf16 a[96:99], a[180:183], v[132:135], a[96:99]\n\t" \
  "v_exp_f32 v72, v72\n\t" \
  "v_mfma_f32_16x16x32_bf16 a[112:115], a[184:187], v[128:131], a[112:115]\n\t" \
  "v_exp_f32 v73, v73\n\t" \
  "v_mfma_f32_16x16x32_bf16 a[112:115], a[188:191], v[132:135], a[112:115]\n\t" \
  "v_exp_f32 v74, v74\n\t" \
  "v_mfma_f32_16x16x32_bf16 v[160:163], v[216:219], v[128:131], v[160:163]\n\t" \
  "v_exp_f32 v75, v75\n\t" \
  "v_mfma_f32_16x16x32_bf16 v[160:163], v[216:219], v[132:135], v[160:163]\n\t" \
  "v_exp_f32 v76, v76\n\t" \
  "v_mfma_f32_16x16x32_bf16 a[4:7], a[128:131], v[136:139], a[4:7]\n\t" \
  "v_exp_f32 v77, v77\n\t" \
  "v_mfma_f32_16x16x32_bf16 a[4:7], a[132:135], v[140:143], a[4:7]\n\t" \
  "v_exp_f32 v78, v78\n\t" \
  "v_mfma_f32_16x16x32_bf16 a[20:23], a[136:139], v[136:139], a[20:23]\n\t" \
  "s_add_u32 m0, %[m], 65536\n\t" \
  "v_exp_f32 v79, v79\n\t" \
  "v_mfma_f32_16x16x32_bf16 a[20:23], a[140:143], v[140:143], a[20:23]\n\t" \
  "s_add_u32 s96, %[sofk], 81920\n\t" \
  "v_exp_f32 v96, v96\n\t" \
  "v_mfma_f32_16x16x32_bf16 a[36:39], a[144:147], v[136:139], a[36:39]\n\t" \
  "v_exp_f32 v97, v97\n\t" \
  "v_mfma_f32_16x16x32_bf16 a[36:39], a[148:151], v[140:143], a[36:39]\n\t" \
  "ds_read_b128 a[216:219], v222 offset:4096\n\t" \
  "v_cvt_pk_bf16_f32 v128, v0, v1\n\t" \
  "v_mfma_f32_16x16x32_bf16 a[52:55], a[152:155], v[136:139], a[52:55]\n\t" \
  "v_exp_f32 v98, v98\n\t" \
  "v_mfma_f32_16x16x32_bf16 a[52:55], a[156:159], v[140:143], a[52:55]\n\t" \
  "v_exp_f32 v99, v99\n\t" \
  "v_mfma_f32_16x16x32_bf16 a[68:71], a[160:163], v[136:139], a[68:71]\n\t" \
  "v_exp_f32 v100, v100\n\t" \
  "v_mfma_f32_16x16x32_bf16 a[68:71], a[164:167], v[140:143], a[68:71]\n\t" \
  "buffer_load_dwordx4 v224, %[sK], s96 offen offset:0 lds\n\t" \
  "v_mfma_f32_16x16x32_bf16 a[84:87], a[168:171], v[136:139], a[84:87]\n\t" \
  "ds_read_b128 a[220:223], v223 offset:4096\n\t" \
  "v_cvt_pk_bf16_f32 v129, v2, v3\n\t" \
  "v_mfma_f32_16x16x32_bf16 a[84:87], a[172:175], v[140:143], a[84:87]\n\t" \
  "v_exp_f32 v101, v101\n\t" \
  "v_mfma_f32_16x16x32_bf16 a[100:103], a[176:179], v[136:139], a[100:103]\n\t" \
  "buffer_load_dwordx4 v225, %[sK], s96 offen offset:1024 lds\n\t" \
  "v_mfma_f32_16x16x32_bf16 a[100:103], a[180:183], v[140:143], a[100:103]\n\t" \
  "ds_read_b128 a[224:227], v220 offset:8192\n\t" \
  "v_cvt_pk_bf16_f32 v130, v4, v5\n\t" \
  "v_mfma_f32_16x16x32_bf16 a[116:119], a[184:187], v[136:139], a[116:119]\n\t" \
  "v_exp_f32 v102, v102\n\t" \
  "v_mfma_f32_16x16x32_bf16 a[116:119], a[188:191], v[140:143], a[116:119]\n\t" \
  "buffer_load_dwordx4 v226, %[sK], s96 offen offset:2048 lds\n\t" \
  "v_mfma_f32_16x16x32_bf16 v[164:167], v[216:219], v[136:139], v[164:167]\n\t" \
  "ds_read_b128 a[228:231], v221 offset:8192\n\t" \
  "v_cvt_pk_bf16_f32 v131, v6, v7\n\t" \
  "v_mfma_f32_16x16x32_bf16 v[164:167], v[216:219], v[140:143], v[164:167]\n\t" \
  "v_exp_f32 v103, v103\n\t" \
  "v_mfma_f32_16x16x32_bf16 a[8:11], a[128:131], v[144:147], a[8:11]\n\t" \
  "v_exp_f32 v104, v104\n\t" \
  "v_mfma_f32_16x16x32_bf16 a[8:11], a[132:135], v[148:151], a[8:11]\n\t" \
  "buffer_load_dwordx4 v227, %[sK], s96 offen offset:3072 lds\n\t" \
  "v_mfma_f32_16x16x32_bf16 a[24:27], a[136:139], v[144:147], a[24:27]\n\t" \
  "s_add_u32 m0, %[m], 81920\n\t" \
  "ds_read_b128 a[232:235], v222 offset:8192\n\t" \
  "v_cvt_pk_bf16_f32 v132, v8, v9\n\t" \
  "v_mfma_f32_16x16x32_bf16 a[24:27], a[140:143], v[148:151], a[24:27]\n\t" \
  "s_add_u32 s96, %[sofk], 65536\n\t" \
  "v_exp_f32 v105, v105\n\t" \
  "v_mfma_f32_16x16x32_bf16 a[40:43], a[144:147], v[144:147], a[40:43]\n\t" \
  "v_exp_f32 v106, v106\n\t" \
  "v_mfma_f32_16x16x32_bf16 a[40:43], a[148:151], v[148:151], a[40:43]\n\t" \
  "v_exp_f32 v107, v107\n\t" \
  "v_mfma_f32_16x16x32_bf16 a[56:59], a[152:155], v[144:147], a[56:59]\n\t" \
  "ds_read_b128 a[236:239], v223 offset:8192\n\t" \
  "v_cvt_pk_bf16_f32 v133, v10, v11\n\t" \
  "v_mfma_f32_16x16x32_bf16 a[56:59], a[156:159], v[148:151], a[56:59]\n\t" \
  "v_exp_f32 v108, v108\n\t" \
  "v_mfma_f32_16x16x32_bf16 a[72:75], a[160:163], v[144:147], a[72:75]\n\t" \
  "v_exp_f32 v109, v109\n\t" \
  "v_mfma_f32_16x16x32_bf16 a[72:75], a[164:167], v[148:151], a[72:75]\n\t" \
  "ds_read_b128 a[240:243], v220 offset:12288\n\t" \
  "v_cvt_pk_bf16_f32 v134, v12, v13\n\t" \
  "v_mfma_f32_16x16x32_bf16 a[88:91], a[168:171], v[144:147], a[88:91]\n\t" \
  "buffer_load_dwordx4 v230, %[sV], s96 offen offset:0 lds\n\t" \
  "v_mfma_f32_16x16x32_bf16 a[88:91], a[172:175], v[148:151], a[88:91]\n\t" \
  "v_exp_f32 v110, v110\n\t" \
  "v_mfma_f32_16x16x32_bf16 a[104:107], a[176:179], v[144:147], a[104:107]\n\t" \
  "v_exp_f32 v111, v111\n\t" \
  "v_mfma_f32_16x16x32_bf16 a[104:107], a[180:183], v[148:151], a[104:107]\n\t" \
  "buffer_load_dwordx4 v231, %[sV], s96 offen offset:1024 lds\n\t" \
  "v_mfma_f32_16x16x32_bf16 a[120:123], a[184:187], v[144:147], a[120:123]\n\t" \
  "ds_read_b128 a[244:247], v221 offset:12288\n\t" \
  "v_cvt_pk_bf16_f32 v135, v14, v15\n\t" \
  "v_mfma_f32_16x16x32_bf16 a[120:123], a[188:191], v[148:151], a[120:123]\n\t" \
  "ds_read_b128 a[248:251], v222 offset:12288\n\t" \
  "v_cvt_pk_bf16_f32 v136, v32, v33\n\t" \
  "v_mfma_f32_16x16x32_bf16 v[168:171], v[216:219], v[144:147], v[168:171]\n\t" \
  "buffer_load_dwordx4 v230, %[sV], s96 offen offset:2048 lds\n\t" \
  "v_mfma_f32_16x16x32_bf16 v[168:171], v[216:219], v[148:151], v[168:171]\n\t" \
  "ds_read_b128 a[252:255], v223 offset:12288\n\t" \
  "v_cvt_pk_bf16_f32 v137, v34, v35\n\t" \
  "v_mfma_f32_16x16x32_bf16 a[12:15], a[128:131], v[152:155], a[12:15]\n\t" \
  "v_cvt_pk_bf16_f32 v138, v36, v37\n\t" \
  "v_cvt_pk_bf16_f32 v139, v38, v39\n\t" \
  "v_mfma_f32_16x16x32_bf16 a[12:15], a[132:135], v[156:159], a[12:15]\n\t" \
  "v_cvt_pk_bf16_f32 v140, v40, v41\n\t" \
  "v_cvt_pk_bf16_f32 v141, v42, v43\n\t" \
  "v_mfma_f32_16x16x32_bf16 a[28:31], a[136:139], v[152:155], a[28:31]\n\t" \
  "buffer_load_dwordx4 v231, %[sV], s96 offen offset:3072 lds\n\t" \
  "v_mfma_f32_16x16x32_bf16 a[28:31], a[140:143], v[156:159], a[28:31]\n\t" \
  "v_cvt_pk_bf16_f32 v142, v44, v45\n\t" \
  "v_cvt_pk_bf16_f32 v143, v46, v47\n\t" \
  "v_mfma_f32_16x16x32_bf16 a[44:47], a[144:147], v[152:155], a[44:47]\n\t" \
  "s_add_u32 m0, %[m], 114688\n\t" \
  "s_mov_b32 s96, 0\n\t" \
  "v_cvt_pk_bf16_f32 v144, v64, v65\n\t" \
  "v_mfma_f32_16x16x32_bf16 a[44:47], a[148:151], v[156:159], a[44:47]\n\t" \
  "v_cvt_pk_bf16_f32 v145, v66, v67\n\t" \
  "v_cvt_pk_bf16_f32 v146, v68, v69\n\t" \
  "v_mfma_f32_16x16x32_bf16 a[60:63], a[152:155], v[152:155], a[60:63]\n\t" \
  "v_cvt_pk_bf16_f32 v147, v70, v71\n\t" \
  "v_cvt_pk_bf16_f32 v148, v72, v73\n\t" \
  "v_mfma_f32_16x16x32_bf16 a[60:63], a[156:159], v[156:159], a[60:63]\n\t" \
  "buffer_load_dwordx4 v232, %[sU], s96 offen offset:0 lds\n\t" \
  ".Lhs7:\n\ts_cbranch_execz .Lhs7n\n\t" \
  "v_mfma_f32_16x16x32_bf16 a[76:79], a[160:163], v[152:155], a[76:79]\n\t" \
  "s_mov_b32 s96, 6144\n\t" \
  "v_cvt_pk_bf16_f32 v149, v74, v75\n\t" \
  "v_cvt_pk_bf16_f32 v150, v76, v77\n\t" \
  "v_mfma_f32_16x16x32_bf16 a[76:79], a[164:167], v[156:159], a[76:79]\n\t" \
  "v_cvt_pk_bf16_f32 v151, v78, v79\n\t" \
  "v_mfma_f32_16x16x32_bf16 a[92:95], a[168:171], v[152:155], a[92:95]\n\t" \
  "v_mfma_f32_16x16x32_bf16 a[92:95], a[172:175], v[156:159], a[92:95]\n\t" \
  "buffer_load_dwordx4 v232, %[sU], s96 offen offset:1024 lds\n\t" \
  "v_mfma_f32_16x16x32_bf16 a[108:111], a[176:179], v[152:155], a[108:111]\n\t" \
  "s_mov_b32 s96, 12288\n\t" \
  "v_mfma_f32_16x16x32_bf16 a[108:111], a[180:183], v[156:159], a[108:111]\n\t" \
  "v_mfma_f32_16x16x32_bf16 a[124:127], a[184:187], v[152:155], a[124:127]\n\t" \
  "v_mfma_f32_16x16x32_bf16 a[124:127], a[188:191], v[156:159], a[124:127]\n\t" \
  "buffer_load_dwordx4 v232, %[sU], s96 offen offset:2048 lds\n\t" \
  "v_mfma_f32_16x16x32_bf16 v[172:175], v[216:219], v[152:155], v[172:175]\n\t" \
  "s_add_u32 s98, %[pf2], 0\n\t" \
  "v_mfma_f32_16x16x32_bf16 v[172:175], v[216:219], v[156:159], v[172:175]\n\t" \
  "s_waitcnt lgkmcnt(0)\n\t" \
  "buffer_load_dword %[pfd], v202, %[sM], s98 offen\n\t" \
  STA("s_memrealtime s[94:95]\n\t") \
  STA("s_memtime s[92:93]\n\t") \
  "v_mfma_f32_16x16x32_bf16 v[0:3], a[192:195], v[16:19], v[176:179]\n\t" \
  "ds_read_b64_tr_b16 a[128:129], v237 offset:0\n\t" \
  "v_cvt_pk_bf16_f32 v152, v96, v97\n\t" \
  "v_mfma_f32_16x16x32_bf16 v[0:3], a[196:199], v[20:23], v[0:3]\n\t" \
  "ds_read_b64_tr_b16 a[130:131], v237 offset:4096\n\t" \
  "v_cvt_pk_bf16_f32 v153, v98, v99\n\t" \
  "v_mfma_f32_16x16x32_bf16 v[0:3], a[200:203], v[24:27], v[0:3]\n\t" \
  "ds_read_b64_tr_b16 a[132:133], v237 offset:8192\n\t" \
  "v_cvt_pk_bf16_f32 v154, v100, v101\n\t" \
  "v_mfma_f32_16x16x32_bf16 v[0:3], a[204:207], v[28:31], v[0:3]\n\t" \
  "ds_read_b64_tr_b16 a[134:135], v237 offset:12288\n\t" \
  "v_cvt_pk_bf16_f32 v155, v102, v103\n\t" \
  "v_mfma_f32_16x16x32_bf16 v[4:7], a[220:223], v[28:31], v[176:179]\n\t" \
  "ds_read_b64_tr_b16 a[136:137], v238 offset:0\n\t" \
  "v_cvt_pk_bf16_f32 v156, v104, v105\n\t" \
  "v_mfma_f32_16x16x32_bf16 v[4:7], a[216:219], v[24:27], v[4:7]\n\t" \
  "ds_read_b64_tr_b16 a[138:139], v238 offset:4096\n\t" \
  "v_cvt_pk_bf16_f32 v157, v106, v107\n\t" \
  "v_mfma_f32_16x16x32_bf16 v[4:7], a[212:215], v[20:23], v[4:7]\n\t" \
  "v_exp_f32 v0, v0\n\t" \
  "v_mfma_f32_16x16x32_bf16 v[4:7], a[208:211], v[16:19], v[4:7]\n\t" \
  "v_exp_f32 v1, v1\n\t" \
  "v_mfma_f32_16x16x32_bf16 v[8:11], a[224:227], v[16:19], v[176:179]\n\t" \
  "v_exp_f32 v2, v2\n\t" \
  "v_mfma_f32_16x16x32_bf16 v[8:11], a[228:231], v[20:23], v[8:11]\n\t" \
  "v_exp_f32 v3, v3\n\t" \
  "v_mfma_f32_16x16x32_bf16 v[8:11], a[232:235], v[24:27], v[8:11]\n\t" \
  "ds_read_b64_tr_b16 a[140:141], v238 offset:8192\n\t" \
  "v_cvt_pk_bf16_f32 v158, v108, v109\n\t" \
  "v_mfma_f32_16x16x32_bf16 v[8:11], a[236:239], v[28:31], v[8:11]\n\t" \
  "s_add_u32 s98, %[pf2], 24576\n\t" \
  "v_exp_f32 v4, v4\n\t" \
  "v_mfma_f32_16x16x32_bf16 v[12:15], a[252:255], v[28:31], v[176:179]\n\t" \
  STA("v_writelane_b32 %[stv], s94, 38\n\t") \
  STA("v_writelane_b32 %[stv], s92, 39\n\t") \
  "buffer_load_dword %[pfd], v202, %[sM], s98 offen\n\t" \
  "v_mfma_f32_16x16x32_bf16 v[12:15], a[248:251], v[24:27], v[12:15]\n\t" \
  "ds_read_b64_tr_b16 a[142:143], v238 offset:12288\n\t" \
  "v_cvt_pk_bf16_f32 v159, v110, v111\n\t" \
  "v_mfma_f32_16x16x32_bf16 v[12:15], a[244:247], v[20:23], v[12:15]\n\t" \
  "v_exp_f32 v5, v5\n\t" \
  "v_mfma_f32_16x16x32_bf16 v[12:15], a[240:243], v[16:19], v[12:15]\n\t" \
  "v_exp_f32 v6, v6\n\t" \
  "v_mfma_f32_16x16x32_bf16 v[32:35], a[192:195], v[48:51], v[180:183]\n\t" \
  "ds_read_b64_tr_b16 a[144:145], v237 offset:256\n\t" \
  "ds_read_b64_tr_b16 a[146:147], v237 offset:4352\n\t" \
  "v_mfma_f32_16x16x32_bf16 v[32:35], a[196:199], v[52:55], v[32:35]\n\t" \
  "v_exp_f32 v7, v7\n\t" \
  "v_mfma_f32_16x16x32_bf16 v[32:35], a[200:203], v[56:59], v[32:35]\n\t" \
  "v_exp_f32 v8, v8\n\t" \
  "v_mfma_f32_16x16x32_bf16 v[32:35], a[204:207], v[60:63], v[32:35]\n\t" \
  "v_exp_f32 v9, v9\n\t" \
  "v_mfma_f32_16x16x32_bf16 v[36:39], a[220:223], v[60:63], v[180:183]\n\t" \
  "ds_read_b64_tr_b16 a[148:149], v237 offset:8448\n\t" \
  "ds_read_b64_tr_b16 a[150:151], v237 offset:12544\n\t" \
  "v_mfma_f32_16x16x32_bf16 v[36:39], a[216:219], v[56:59], v[36:39]\n\t" \
  "v_exp_f32 v10, v10\n\t" \
  "v_mfma_f32_16x16x32_bf16 v[36:39], a[212:215], v[52:55], v[36:39]\n\t" \
  "v_exp_f32 v11, v11\n\t" \
  "v_mfma_f32_16x16x32_bf16 v[36:39], a[208:211], v[48:51], v[36:39]\n\t" \
  "ds_read_b64_tr_b16 a[152:153], v238 offset:256\n\t" \
  "ds_read_b64_tr_b16 a[154:155], v238 offset:4352\n\t" \
  "v_mfma_f32_16x16x32_bf16 v[40:43], a[224:227], v[48:51], v[180:183]\n\t" \
  "v_exp_f32 v12, v12\n\t" \
  "v_mfma_f32_16x16x32_bf16 v[40:43], a[228:231], v[52:55], v[40:43]\n\t" \
  "v_exp_f32 v13, v13\n\t" \
  "v_mfma_f32_16x16x32_bf16 v[40:43], a[232:235], v[56:59], v[40:43]\n\t" \
  "v_exp_f32 v14, v14\n\t" \
  "v_mfma_f32_16x16x32_bf16 v[40:43], a[236:239], v[60:63], v[40:43]\n\t" \
  "ds_read_b64_tr_b16 a[156:157], v238 offset:8448\n\t" \
  "ds_read_b64_tr_b16 a[158:159], v238 offset:12544\n\t" \
  "v_mfma_f32_16x16x32_bf16 v[44:47], a[252:255], v[60:63], v[180:183]\n\t" \
  "v_exp_f32 v15, v15\n\t" \
  "v_mfma_f32_16x16x32_bf16 v[44:47], a[248:251], v[56:59], v[44:47]\n\t" \
  "v_exp_f32 v32, v32\n\t" \
  "v_mfma_f32_16x16x32_bf16 v[44:47], a[244:247], v[52:55], v[44:47]\n\t" \
  "ds_read_b64_tr_b16 a[160:161], v237 offset:512\n\t" \
  "ds_read_b64_tr_b16 a[162:163], v237 offset:4608\n\t" \
  "v_mfma_f32_16x16x32_bf16 v[44:47], a[240:243], v[48:51], v[44:47]\n\t" \
  "v_exp_f32 v33, v33\n\t" \
  "v_mfma_f32_16x16x32_bf16 v[64:67], a[192:195], v[80:83], v[184:187]\n\t" \
  "v_exp_f32 v34, v34\n\t" \
  "v_mfma_f32_16x16x32_bf16 v[64:67], a[196:199], v[84:87], v[64:67]\n\t" \
  "v_exp_f32 v35, v35\n\t" \
  "v_mfma_f32_16x16x32_bf16 v[64:67], a[200:203], v[88:91], v[64:67]\n\t" \
  "ds_read_b64_tr_b16 a[164:165], v237 offset:8704\n\t" \
  "ds_read_b64_tr_b16 a[166:167], v237 offset:12800\n\t" \
  "v_mfma_f32_16x16x32_bf16 v[64:67], a[204:207], v[92:95], v[64:67]\n\t" \
  "v_exp_f32 v36, v36\n\t" \
  "v_mfma_f32_16x16x32_bf16 v[68:71], a[220:223], v[92:95], v[184:187]\n\t" \
  "v_exp_f32 v37, v37\n\t" \
  "v_mfma_f32_16x16x32_bf16 v[68:71], a[216:219], v[88:91], v[68:71]\n\t" \
  "ds_read_b64_tr_b16 a[168:169], v238 offset:512\n\t" \
  "ds_read_b64_tr_b16 a[170:171], v238 offset:4608\n\t" \
  "v_mfma_f32_16x16x32_bf16 v[68:71], a[212:215], v[84:87], v[68:71]\n\t" \
  "v_exp_f32 v38, v38\n\t" \
  "v_mfma_f32_16x16x32_bf16 v[68:71], a[208:211], v[80:83], v[68:71]\n\t" \
  "v_exp_f32 v39, v39\n\t" \
  "v_mfma_f32_16x16x32_bf16 v[72:75], a[224:227], v[80:83], v[184:187]\n\t" \
  "v_exp_f32 v40, v40\n\t" \
  "v_mfma_f32_16x16x32_bf16 v[72:75], a[228:231], v[84:87], v[72:75]\n\t" \
  "ds_read_b64_tr_b16 a[172:173], v238 offset:8704\n\t" \
  "ds_read_b64_tr_b16 a[174:175], v238 offset:12800\n\t" \
  "v_mfma_f32_16x16x32_bf16 v[72:75], a[232:235], v[88:91], v[72:75]\n\t" \
  "v_exp_f32 v41, v41\n\t" \
  "v_mfma_f32_16x16x32_bf16 v[72:75], a[236:239], v[92:95], v[72:75]\n\t" \
  "v_exp_f32 v42, v42\n\t" \
  "v_mfma_f32_16x16x32_bf16 v[76:79], a[252:255], v[92:95], v[184:187]\n\t" \
  "ds_read_b64_tr_b16 a[176:177], v237 offset:768\n\t" \
  "ds_read_b64_tr_b16 a[178:179], v237 offset:4864\n\t" \
  "v_mfma_f32_16x16x32_bf16 v[76:79], a[248:251], v[88:91], v[76:79]\n\t" \
  "v_exp_f32 v43, v43\n\t" \
  "v_mfma_f32_16x16x32_bf16 v[76:79], a[244:247], v[84:87], v[76:79]\n\t" \
  "v_exp_f32 v44, v44\n\t" \
  "v_mfma_f32_16x16x32_bf16 v[76:79], a[240:243], v[80:83], v[76:79]\n\t" \
  "v_exp_f32 v45, v45\n\t" \
  "v_mfma_f32_16x16x32_bf16 v[96:99], a[192:195], v[112:115], v[188:191]\n\t" \
  "ds_read_b64_tr_b16 a[180:181], v237 offset:8960\n\t" \
  "ds_read_b64_tr_b16 a[182:183], v237 offset:13056\n\t" \
  "v_mfma_f32_16x16x32_bf16 v[96:99], a[196:199], v[116:119], v[96:99]\n\t" \
  "v_exp_f32 v46, v46\n\t" \
  "v_mfma_f32_16x16x32_bf16 v[96:99], a[200:203], v[120:123], v[96:99]\n\t" \
  "v_exp_f32 v47, v47\n\t" \
  "v_mfma_f32_16x16x32_bf16 v[96:99], a[204:207], v[124:127], v[96:99]\n\t" \
  "ds_read_b64_tr_b16 a[184:185], v238 offset:768\n\t" \
  "ds_read_b64_tr_b16 a[186:187], v238 offset:4864\n\t" \
  "v_mfma_f32_16x16x32_bf16 v[100:103], a[220:223], v[124:127], v[188:191]\n\t" \
  "v_exp_f32 v64, v64\n\t" \
  "v_mfma_f32_16x16x32_bf16 v[100:103], a[216:219], v[120:123], v[100:103]\n\t" \
  "v_exp_f32 v65, v65\n\t" \
  "v_mfma_f32_16x16x32_bf16 v[100:103], a[212:215], v[116:119], v[100:103]\n\t" \
  "v_exp_f32 v66, v66\n\t" \
  "v_mfma_f32_16x16x32_bf16 v[100:103], a[208:211], v[112:115], v[100:103]\n\t" \
  "ds_read_b64_tr_b16 a[188:189], v238 offset:8960\n\t" \
  "ds_read_b64_tr_b16 a[190:191], v238 offset:13056\n\t" \
  "v_mfma_f32_16x16x32_bf16 v[104:107], a[224:227], v[112:115], v[188:191]\n\t" \
  "v_exp_f32 v67, v67\n\t" \
  "v_mfma_f32_16x16x32_bf16 v[104:107], a[228:231], v[116:119], v[104:107]\n\t" \
  "v_exp_f32 v68, v68\n\t" \
  "v_mfma_f32_16x16x32_bf16 v[104:107], a[232:235], v[120:123], v[104:107]\n\t" \
  "v_exp_f32 v69, v69\n\t" \
  "v_mfma_f32_16x16x32_bf16 v[104:107], a[236:239], v[124:127], v[104:107]\n\t" \
  "v_exp_f32 v70, v70\n\t" \
  "v_mfma_f32_16x16x32_bf16 v[108:111], a[252:255], v[124:127], v[188:191]\n\t" \
  "v_exp_f32 v71, v71\n\t" \
  "v_mfma_f32_16x16x32_bf16 v[108:111], a[248:251], v[120:123], v[108:111]\n\t" \
  "v_exp_f32 v72, v72\n\t" \
  "v_mfma_f32_16x16x32_bf16 v[108:111], a[244:247], v[116:119], v[108:111]\n\t" \
  "v_exp_f32 v73, v73\n\t" \
  "v_mfma_f32_16x16x32_bf16 v[108:111], a[240:243], v[112:115], v[108:111]\n\t" \
  "s_waitcnt vmcnt(5) lgkmcnt(0)\n\t" \
  "s_barrier\n\t" \
  STA("s_memrealtime s[94:95]\n\t") \
  STA("s_memtime s[92:93]\n\t") \
  "v_mfma_f32_16x16x32_bf16 a[0:3], a[128:131], v[128:131], a[0:3]\n\t" \
  "v_exp_f32 v74, v74\n\t" \
  "v_mfma_f32_16x16x32_bf16 a[0:3], a[132:135], v[132:135], a[0:3]\n\t" \
  "v_exp_f32 v75, v75\n\t" \
  "v_mfma_f32_16x16x32_bf16 a[16:19], a[136:139], v[128:131], a[16:19]\n\t" \
  "ds_read_b128 a[192:195], v220 offset:16384\n\t" \
  "ds_read_b128 a[196:199], v221 offset:16384\n\t" \
  "v_mfma_f32_16x16x32_bf16 a[16:19], a[140:143], v[132:135], a[16:19]\n\t" \
  "v_exp_f32 v76, v76\n\t" \
  "v_mfma_f32_16x16x32_bf16 a[32:35], a[144:147], v[128:131], a[32:35]\n\t" \
  "s_lshr_b32 s98, %[sofk], 6\n\t" \
  "v_xor_b32 v234, s98, v244\n\t" \
  "v_add_u32 v213, %[m], v242\n\t" \
  "v_mfma_f32_16x16x32_bf16 a[32:35], a[148:151], v[132:135], a[32:35]\n\t" \
  "v_exp_f32 v77, v77\n\t" \
  "v_mfma_f32_16x16x32_bf16 a[48:51], a[152:155], v[128:131], a[48:51]\n\t" \
  "v_exp_f32 v78, v78\n\t" \
  "v_mfma_f32_16x16x32_bf16 a[48:51], a[156:159], v[132:135], a[48:51]\n\t" \
  "v_exp_f32 v79, v79\n\t" \
  "v_mfma_f32_16x16x32_bf16 a[64:67], a[160:163], v[128:131], a[64:67]\n\t" \
  "ds_read_b128 a[200:203], v222 offset:16384\n\t" \
  "v_add_u32 v215, 98304, v213\n\t" \
  "v_mfma_f32_16x16x32_bf16 a[64:67], a[164:167], v[132:135], a[64:67]\n\t" \
  "v_exp_f32 v96, v96\n\t" \
  "v_mfma_f32_16x16x32_bf16 a[80:83], a[168:171], v[128:131], a[80:83]\n\t" \
  "v_exp_f32 v97, v97\n\t" \
  "v_mfma_f32_16x16x32_bf16 a[80:83], a[172:175], v[132:135], a[80:83]\n\t" \
  "v_exp_f32 v98, v98\n\t" \
  "v_mfma_f32_16x16x32_bf16 a[96:99], a[176:179], v[128:131], a[96:99]\n\t" \
  STA("v_writelane_b32 %[stv], s94, 40\n\t") \
  STA("v_writelane_b32 %[stv], s92, 41\n\t") \
  "ds_read_b128 a[204:207], v223 offset:16384\n\t" \
  "v_mov_b32 v243, %[tag]\n\t" \
  "v_mfma_f32_16x16x32_bf16 a[96:99], a[180:183], v[132:135], a[96:99]\n\t" \
  "v_exp_f32 v99, v99\n\t" \
  "v_mfma_f32_16x16x32_bf16 a[112:115], a[184:187], v[128:131], a[112:115]\n\t" \
  "ds_read_b128 a[208:211], v220 offset:20480\n\t" \
  "v_lshrrev_b32 v254, 3, v233\n\t" \
  "v_mfma_f32_16x16x32_bf16 a[112:115], a[188:191], v[132:135], a[112:115]\n\t" \
  "ds_read_b128 a[212:215], v221 offset:20480\n\t" \
  "v_min_u32 v254, 1, v254\n\t" \
  "v_mfma_f32_16x16x32_bf16 v[160:163], v[216:219], v[128:131], v[160:163]\n\t" \
  "ds_read_b128 a[216:219], v222 offset:20480\n\t" \
  "v_lshlrev_b32 v254, 31, v254\n\t" \
  "v_mfma_f32_16x16x32_bf16 v[160:163], v[216:219], v[132:135], v[160:163]\n\t" \
  "ds_read_b128 a[220:223], v223 offset:20480\n\t" \
  "v_lshl_or_b32 v254, v233, 9, v254\n\t" \
  "v_mfma_f32_16x16x32_bf16 a[4:7], a[128:131], v[136:139], a[4:7]\n\t" \
  "s_lshr_b32 m0, %[hl18], 18\n\t" \
  "s_brev_b32 s98, 1\n\t" \
  "v_writelane_b32 v254, s98, m0\n\t" \
  "v_mfma_f32_16x16x32_bf16 a[4:7], a[132:135], v[140:143], a[4:7]\n\t" \
  "s_lshr_b32 s98, %[hl18], 18\n\t" \
  "v_xor_b32 v245, s98, v233\n\t" \
  "v_and_b32 v245, 31, v245\n\t" \
  "s_lshr_b32 s98, %[hl18], 18\n\t" \
  "s_mul_i32 s98, s98, 127\n\t" \
  "v_add_lshl_u32 v245, v245, s98, 2\n\t" \
  "v_mfma_f32_16x16x32_bf16 a[20:23], a[136:139], v[136:139], a[20:23]\n\t" \
  "s_add_u32 m0, %[m], 49152\n\t" \
  "v_exp_f32 v100, v100\n\t" \
  "v_mfma_f32_16x16x32_bf16 a[20:23], a[140:143], v[140:143], a[20:23]\n\t" \
  "s_add_u32 s96, %[sofk], 98304\n\t" \
  "v_exp_f32 v101, v101\n\t" \
  "v_mfma_f32_16x16x32_bf16 a[36:39], a[144:147], v[136:139], a[36:39]\n\t" \
  "v_exp_f32 v102, v102\n\t" \
  "v_mfma_f32_16x16x32_bf16 a[36:39], a[148:151], v[140:143], a[36:39]\n\t" \
  "v_exp_f32 v103, v103\n\t" \
  "v_mfma_f32_16x16x32_bf16 a[52:55], a[152:155], v[136:139], a[52:55]\n\t" \
  "v_exp_f32 v104, v104\n\t" \
  "v_mfma_f32_16x16x32_bf16 a[52:55], a[156:159], v[140:143], a[52:55]\n\t" \
  "v_exp_f32 v105, v105\n\t" \
  "v_mfma_f32_16x16x32_bf16 a[68:71], a[160:163], v[136:139], a[68:71]\n\t" \
  "v_exp_f32 v106, v106\n\t" \
  "v_mfma_f32_16x16x32_bf16 a[68:71], a[164:167], v[140:143], a[68:71]\n\t" \
  "buffer_load_dwordx4 v224, %[sK], s96 offen offset:0 lds\n\t" \
  "v_mfma_f32_16x16x32_bf16 a[84:87], a[168:171], v[136:139], a[84:87]\n\t" \
  "ds_read_b128 a[224:227], v220 offset:24576\n\t" \
  "v_cvt_pk_bf16_f32 v128, v0, v1\n\t" \
  "v_mfma_f32_16x16x32_bf16 a[84:87], a[172:175], v[140:143], a[84:87]\n\t" \
  "v_exp_f32 v107, v107\n\t" \
  "v_mfma_f32_16x16x32_bf16 a[100:103], a[176:179], v[136:139], a[100:103]\n\t" \
  "buffer_load_dwordx4 v225, %[sK], s96 offen offset:1024 lds\n\t" \
  "v_mfma_f32_16x16x32_bf16 a[100:103], a[180:183], v[140:143], a[100:103]\n\t" \
  "v_exp_f32 v108, v108\n\t" \
  "v_mfma_f32_16x16x32_bf16 a[116:119], a[184:187], v[136:139], a[116:119]\n\t" \
  "v_exp_f32 v109, v109\n\t" \
  "v_mfma_f32_16x16x32_bf16 a[116:119], a[188:191], v[140:143], a[116:119]\n\t" \
  "buffer_load_dwordx4 v226, %[sK], s96 offen offset:2048 lds\n\t" \
  "v_mfma_f32_16x16x32_bf16 v[164:167], v[216:219], v[136:139], v[164:167]\n\t" \
  "ds_read_b128 a[228:231], v221 offset:24576\n\t" \
  "v_cvt_pk_bf16_f32 v129, v2, v3\n\t" \
  "v_mfma_f32_16x16x32_bf16 v[164:167], v[216:219], v[140:143], v[164:167]\n\t" \
  "v_exp_f32 v110, v110\n\t" \
  "v_mfma_f32_16x16x32_bf16 a[8:11], a[128:131], v[144:147], a[8:11]\n\t" \
  "v_exp_f32 v111, v111\n\t" \
  "v_mfma_f32_16x16x32_bf16 a[8:11], a[132:135], v[148:151], a[8:11]\n\t" \
  "buffer_load_dwordx4 v227, %[sK], s96 offen offset:3072 lds\n\t" \
  "v_mfma_f32_16x16x32_bf16 a[24:27], a[136:139], v[144:147], a[24:27]\n\t" \
  "s_add_u32 m0, %[m], 147456\n\t" \
  "ds_read_b128 a[232:235], v222 offset:24576\n\t" \
  "v_cvt_pk_bf16_f32 v130, v4, v5\n\t" \
  "v_mfma_f32_16x16x32_bf16 a[24:27], a[140:143], v[148:151], a[24:27]\n\t" \
  "s_add_u32 s96, %[sofk], 81920\n\t" \
  "ds_read_b128 a[236:239], v223 offset:24576\n\t" \
  "v_cvt_pk_bf16_f32 v131, v6, v7\n\t" \
  "v_mfma_f32_16x16x32_bf16 a[40:43], a[144:147], v[144:147], a[40:43]\n\t" \
  "ds_read_b128 a[240:243], v220 offset:28672\n\t" \
  "v_cvt_pk_bf16_f32 v132, v8, v9\n\t" \
  "v_mfma_f32_16x16x32_bf16 a[40:43], a[148:151], v[148:151], a[40:43]\n\t" \
  "ds_read_b128 a[244:247], v221 offset:28672\n\t" \
  "v_cvt_pk_bf16_f32 v133, v10, v11\n\t" \
  "v_mfma_f32_16x16x32_bf16 a[56:59], a[152:155], v[144:147], a[56:59]\n\t" \
  "v_cvt_pk_bf16_f32 v134, v12, v13\n\t" \
  "v_cvt_pk_bf16_f32 v135, v14, v15\n\t" \
  "v_mfma_f32_16x16x32_bf16 a[56:59], a[156:159], v[148:151], a[56:59]\n\t" \
  "ds_read_b128 a[248:251], v222 offset:28672\n\t" \
  "v_cvt_pk_bf16_f32 v136, v32, v33\n\t" \
  "v_mfma_f32_16x16x32_bf16 a[72:75], a[160:163], v[144:147], a[72:75]\n\t" \
  "v_cvt_pk_bf16_f32 v137, v34, v35\n\t" \
  "v_cvt_pk_bf16_f32 v138, v36, v37\n\t" \
  "v_mfma_f32_16x16x32_bf16 a[72:75], a[164:167], v[148:151], a[72:75]\n\t" \
  "v_cvt_pk_bf16_f32 v139, v38, v39\n\t" \
  "v_cvt_pk_bf16_f32 v140, v40, v41\n\t" \
  "v_mfma_f32_16x16x32_bf16 a[88:91], a[168:171], v[144:147], a[88:91]\n\t" \
  "buffer_load_dwordx4 v230, %[sV], s96 offen offset:0 lds\n\t" \
  "v_mfma_f32_16x16x32_bf16 a[88:91], a[172:175], v[148:151], a[88:91]\n\t" \
  "ds_read_b128 a[252:255], v223 offset:28672\n\t" \
  "v_cvt_pk_bf16_f32 v141, v42, v43\n\t" \
  "v_mfma_f32_16x16x32_bf16 a[104:107], a[176:179], v[144:147], a[104:107]\n\t" \
  "v_cvt_pk_bf16_f32 v142, v44, v45\n\t" \
  "v_cvt_pk_bf16_f32 v143, v46, v47\n\t" \
  "v_mfma_f32_16x16x32_bf16 a[104:107], a[180:183], v[148:151], a[104:107]\n\t" \
  "buffer_load_dwordx4 v231, %[sV], s96 offen offset:1024 lds\n\t" \
  "v_mfma_f32_16x16x32_bf16 a[120:123], a[184:187], v[144:147], a[120:123]\n\t" \
  "v_mfma_f32_16x16x32_bf16 a[120:123], a[188:191], v[148:151], a[120:123]\n\t" \
  "v_mfma_f32_16x16x32_bf16 v[168:171], v[216:219], v[144:147], v[168:171]\n\t" \
  "buffer_load_dwordx4 v230, %[sV], s96 offen offset:2048 lds\n\t" \
  "v_mfma_f32_16x16x32_bf16 v[168:171], v[216:219], v[148:151], v[168:171]\n\t" \
  "v_cvt_pk_bf16_f32 v144, v64, v65\n\t" \
  "v_cvt_pk_bf16_f32 v145, v66, v67\n\t" \
  "v_mfma_f32_16x16x32_bf16 a[12:15], a[128:131], v[152:155], a[12:15]\n\t" \
  "v_cvt_pk_bf16_f32 v146, v68, v69\n\t" \
  "v_cvt_pk_bf16_f32 v147, v70, v71\n\t" \
  "v_mfma_f32_16x16x32_bf16 a[12:15], a[132:135], v[156:159], a[12:15]\n\t" \
  "v_cvt_pk_bf16_f32 v148, v72, v73\n\t" \
  "v_cvt_pk_bf16_f32 v149, v74, v75\n\t" \
  "v_mfma_f32_16x16x32_bf16 a[28:31], a[136:139], v[152:155], a[28:31]\n\t" \
  "buffer_load_dwordx4 v231, %[sV], s96 offen offset:3072 lds\n\t" \
  "v_mfma_f32_16x16x32_bf16 a[28:31], a[140:143], v[156:159], a[28:31]\n\t" \
  "v_cvt_pk_bf16_f32 v150, v76, v77\n\t" \
  "v_cvt_pk_bf16_f32 v151, v78, v79\n\t" \
  "v_mfma_f32_16x16x32_bf16 a[44:47], a[144:147], v[152:155], a[44:47]\n\t" \
  "s_add_u32 m0, %[m], 114688\n\t" \
  "s_mov_b32 s96, 18432\n\t" \
  "v_mfma_f32_16x16x32_bf16 a[44:47], a[148:151], v[156:159], a[44:47]\n\t" \
  "v_mfma_f32_16x16x32_bf16 a[60:63], a[152:155], v[152:155], a[60:63]\n\t" \
  "v_mfma_f32_16x16x32_bf16 a[60:63], a[156:159], v[156:159], a[60:63]\n\t" \
  "buffer_load_dwordx4 v232, %[sU], s96 offen offset:3072 lds\n\t" \
  "v_mfma_f32_16x16x32_bf16 a[76:79], a[160:163], v[152:155], a[76:79]\n\t" \
  "s_add_u32 m0, %[m], 131072\n\t" \
  "s_mov_b32 s96, 28672\n\t" \
  "v_mfma_f32_16x16x32_bf16 a[76:79], a[164:167], v[156:159], a[76:79]\n\t" \
  "v_mfma_f32_16x16x32_bf16 a[92:95], a[168:171], v[152:155], a[92:95]\n\t" \
  "v_mfma_f32_16x16x32_bf16 a[92:95], a[172:175], v[156:159], a[92:95]\n\t" \
  "buffer_load_dwordx4 v232, %[sU], s96 offen offset:0 lds\n\t" \
  "v_mfma_f32_16x16x32_bf16 a[108:111], a[176:179], v[152:155], a[108:111]\n\t" \
  "s_mov_b32 s96, 34816\n\t" \
  "v_mfma_f32_16x16x32_bf16 a[108:111], a[180:183], v[156:159], a[108:111]\n\t" \
  "v_mfma_f32_16x16x32_bf16 a[124:127], a[184:187], v[152:155], a[124:127]\n\t" \
  "v_mfma_f32_16x16x32_bf16 a[124:127], a[188:191], v[156:159], a[124:127]\n\t" \
  "buffer_load_dwordx4 v232, %[sU], s96 offen offset:1024 lds\n\t" \
  "v_mfma_f32_16x16x32_bf16 v[172:175], v[216:219], v[152:155], v[172:175]\n\t" \
  "s_add_u32 s98, %[pf2], 49152\n\t" \
  "v_mfma_f32_16x16x32_bf16 v[172:175], v[216:219], v[156:159], v[172:175]\n\t" \
  "s_waitcnt lgkmcnt(0)\n\t" \
  "buffer_load_dword %[pfd], v202, %[sM], s98 offen\n\t" \
  STA("s_memrealtime s[94:95]\n\t") \
  STA("s_memtime s[92:93]\n\t") \
  "v_mfma_f32_16x16x32_bf16 v[0:3], a[192:195], v[16:19], v[176:179]\n\t" \
  "ds_read_b64_tr_b16 a[128:129], v228 offset:0\n\t" \
  "v_cvt_pk_bf16_f32 v152, v96, v97\n\t" \
  "v_mfma_f32_16x16x32_bf16 v[0:3], a[196:199], v[20:23], v[0:3]\n\t" \
  "ds_read_b64_tr_b16 a[130:131], v228 offset:4096\n\t" \
  "v_cvt_pk_bf16_f32 v153, v98, v99\n\t" \
  "v_mfma_f32_16x16x32_bf16 v[0:3], a[200:203], v[24:27], v[0:3]\n\t" \
  "ds_read_b64_tr_b16 a[132:133], v228 offset:8192\n\t" \
  "v_lshrrev_b32 v252, 3, v233\n\t" \
  "v_mfma_f32_16x16x32_bf16 v[0:3], a[204:207], v[28:31], v[0:3]\n\t" \
  "ds_read_b64_tr_b16 a[134:135], v228 offset:12288\n\t" \
  "v_cvt_pk_bf16_f32 v154, v100, v101\n\t" \
  "v_mfma_f32_16x16x32_bf16 v[4:7], a[220:223], v[28:31], v[176:179]\n\t" \
  "ds_read_b64_tr_b16 a[136:137], v229 offset:0\n\t" \
  "v_cvt_pk_bf16_f32 v155, v102, v103\n\t" \
  "v_mfma_f32_16x16x32_bf16 v[4:7], a[216:219], v[24:27], v[4:7]\n\t" \
  "ds_read_b64_tr_b16 a[138:139], v229 offset:4096\n\t" \
  "v_cvt_pk_bf16_f32 v156, v104, v105\n\t" \
  "v_mfma_f32_16x16x32_bf16 v[4:7], a[212:215], v[20:23], v[4:7]\n\t" \
  "v_exp_f32 v0, v0\n\t" \
  "v_mfma_f32_16x16x32_bf16 v[4:7], a[208:211], v[16:19], v[4:7]\n\t" \
  "v_exp_f32 v1, v1\n\t" \
  "v_mfma_f32_16x16x32_bf16 v[8:11], a[224:227], v[16:19], v[176:179]\n\t" \
  "v_exp_f32 v2, v2\n\t" \
  "v_mfma_f32_16x16x32_bf16 v[8:11], a[228:231], v[20:23], v[8:11]\n\t" \
  "v_exp_f32 v3, v3\n\t" \
  "v_mfma_f32_16x16x32_bf16 v[8:11], a[232:235], v[24:27], v[8:11]\n\t" \
  "ds_read_b64_tr_b16 a[140:141], v229 offset:8192\n\t" \
  "v_mul_u32_u24 v247, 98176, v252\n\t" \
  "v_mfma_f32_16x16x32_bf16 v[8:11], a[236:239], v[28:31], v[8:11]\n\t" \
  "s_add_u32 s98, %[pf2], 73728\n\t" \
  "v_exp_f32 v4, v4\n\t" \
  "v_mfma_f32_16x16x32_bf16 v[12:15], a[252:255], v[28:31], v[176:179]\n\t" \
  STA("v_writelane_b32 %[stv], s94, 42\n\t") \
  STA("v_writelane_b32 %[stv], s92, 43\n\t") \
  "buffer_load_dword %[pfd], v202, %[sM], s98 offen\n\t" \
  "v_mfma_f32_16x16x32_bf16 v[12:15], a[248:251], v[24:27], v[12:15]\n\t" \
  "ds_read_b64_tr_b16 a[142:143], v229 offset:12288\n\t" \
  "v_cvt_pk_bf16_f32 v157, v106, v107\n\t" \
  "v_mfma_f32_16x16x32_bf16 v[12:15], a[244:247], v[20:23], v[12:15]\n\t" \
  "ds_read_b64_tr_b16 a[144:145], v228 offset:256\n\t" \
  "v_lshl_add_u32 v247, v233, 4, v247\n\t" \
  "v_mfma_f32_16x16x32_bf16 v[12:15], a[240:243], v[16:19], v[12:15]\n\t" \
  "v_exp_f32 v5, v5\n\t" \
  "v_mfma_f32_16x16x32_bf16 v[32:35], a[192:195], v[48:51], v[180:183]\n\t" \
  "ds_read_b64_tr_b16 a[146:147], v228 offset:4352\n\t" \
  "v_lshlrev_b32 v252, 4, v252\n\t" \
  "v_mfma_f32_16x16x32_bf16 v[32:35], a[196:199], v[52:55], v[32:35]\n\t" \
  "v_exp_f32 v6, v6\n\t" \
  "v_mfma_f32_16x16x32_bf16 v[32:35], a[200:203], v[56:59], v[32:35]\n\t" \
  "ds_read_b64_tr_b16 a[148:149], v228 offset:8448\n\t" \
  "v_cvt_pk_bf16_f32 v158, v108, v109\n\t" \
  "v_mfma_f32_16x16x32_bf16 v[32:35], a[204:207], v[60:63], v[32:35]\n\t" \
  "v_exp_f32 v7, v7\n\t" \
  "v_mfma_f32_16x16x32_bf16 v[36:39], a[220:223], v[60:63], v[180:183]\n\t" \
  "ds_read_b64_tr_b16 a[150:151], v228 offset:12544\n\t" \
  "v_cvt_pk_bf16_f32 v159, v110, v111\n\t" \
  "v_mfma_f32_16x16x32_bf16 v[36:39], a[216:219], v[56:59], v[36:39]\n\t" \
  "v_exp_f32 v8, v8\n\t" \
  "v_mfma_f32_16x16x32_bf16 v[36:39], a[212:215], v[52:55], v[36:39]\n\t" \
  "v_exp_f32 v9, v9\n\t" \
  "v_mfma_f32_16x16x32_bf16 v[36:39], a[208:211], v[48:51], v[36:39]\n\t" \
  "ds_read_b64_tr_b16 a[152:153], v229 offset:256\n\t" \
  "ds_read_b64_tr_b16 a[154:155], v229 offset:4352\n\t" \
  "v_mfma_f32_16x16x32_bf16 v[40:43], a[224:227], v[48:51], v[180:183]\n\t" \
  "v_exp_f32 v10, v10\n\t" \
  "v_mfma_f32_16x16x32_bf16 v[40:43], a[228:231], v[52:55], v[40:43]\n\t" \
  "v_exp_f32 v11, v11\n\t" \
  "v_mfma_f32_16x16x32_bf16 v[40:43], a[232:235], v[56:59], v[40:43]\n\t" \
  "v_exp_f32 v12, v12\n\t" \
  "v_mfma_f32_16x16x32_bf16 v[40:43], a[236:239], v[60:63], v[40:43]\n\t" \
  "ds_read_b64_tr_b16 a[156:157], v229 offset:8448\n\t" \
  "ds_read_b64_tr_b16 a[158:159], v229 offset:12544\n\t" \
  "v_mfma_f32_16x16x32_bf16 v[44:47], a[252:255], v[60:63], v[180:183]\n\t" \
  "v_exp_f32 v13, v13\n\t" \
  "v_mfma_f32_16x16x32_bf16 v[44:47], a[248:251], v[56:59], v[44:47]\n\t" \
  "v_exp_f32 v14, v14\n\t" \
  "v_mfma_f32_16x16x32_bf16 v[44:47], a[244:247], v[52:55], v[44:47]\n\t" \
  "ds_read_b64_tr_b16 a[160:161], v228 offset:512\n\t" \
  "ds_read_b64_tr_b16 a[162:163], v228 offset:4608\n\t" \
  "v_mfma_f32_16x16x32_bf16 v[44:47], a[240:243], v[48:51], v[44:47]\n\t" \
  "v_exp_f32 v15, v15\n\t" \
  "v_mfma_f32_16x16x32_bf16 v[64:67], a[192:195], v[80:83], v[184:187]\n\t" \
  "v_exp_f32 v32, v32\n\t" \
  "v_mfma_f32_16x16x32_bf16 v[64:67], a[196:199], v[84:87], v[64:67]\n\t" \
  "v_exp_f32 v33, v33\n\t" \
  "v_mfma_f32_16x16x32_bf16 v[64:67], a[200:203], v[88:91], v[64:67]\n\t" \
  "ds_read_b64_tr_b16 a[164:165], v228 offset:8704\n\t" \
  "ds_read_b64_tr_b16 a[166:167], v228 offset:12800\n\t" \
  "v_mfma_f32_16x16x32_bf16 v[64:67], a[204:207], v[92:95], v[64:67]\n\t" \
  "v_exp_f32 v34, v34\n\t" \
  "v_mfma_f32_16x16x32_bf16 v[68:71], a[220:223], v[92:95], v[184:187]\n\t" \
  "v_exp_f32 v35, v35\n\t" \
  "v_mfma_f32_16x16x32_bf16 v[68:71], a[216:219], v[88:91], v[68:71]\n\t" \
  "ds_read_b64_tr_b16 a[168:169], v229 offset:512\n\t" \
  "ds_read_b64_tr_b16 a[170:171], v229 offset:4608\n\t" \
  "v_mfma_f32_16x16x32_bf16 v[68:71], a[212:215], v[84:87], v[68:71]\n\t" \
  "v_exp_f32 v36, v36\n\t" \
  "v_mfma_f32_16x16x32_bf16 v[68:71], a[208:211], v[80:83], v[68:71]\n\t" \
  "v_exp_f32 v37, v37\n\t" \
  "v_mfma_f32_16x16x32_bf16 v[72:75], a[224:227], v[80:83], v[184:187]\n\t" \
  "v_exp_f32 v38, v38\n\t" \
  "v_mfma_f32_16x16x32_bf16 v[72:75], a[228:231], v[84:87], v[72:75]\n\t" \
  "ds_read_b64_tr_b16 a[172:173], v229 offset:8704\n\t" \
  "ds_read_b64_tr_b16 a[174:175], v229 offset:12800\n\t" \
  "v_mfma_f32_16x16x32_bf16 v[72:75], a[232:235], v[88:91], v[72:75]\n\t" \
  "v_exp_f32 v39, v39\n\t" \
  "v_mfma_f32_16x16x32_bf16 v[72:75], a[236:239], v[92:95], v[72:75]\n\t" \
  "v_exp_f32 v40, v40\n\t" \
  "v_mfma_f32_16x16x32_bf16 v[76:79], a[252:255], v[92:95], v[184:187]\n\t" \
  "ds_read_b64_tr_b16 a[176:177], v228 offset:768\n\t" \
  "ds_read_b64_tr_b16 a[178:179], v228 offset:4864\n\t" \
  "v_mfma_f32_16x16x32_bf16 v[76:79], a[248:251], v[88:91], v[76:79]\n\t" \
  "v_exp_f32 v41, v41\n\t" \
  "v_mfma_f32_16x16x32_bf16 v[76:79], a[244:247], v[84:87], v[76:79]\n\t" \
  "v_exp_f32 v42, v42\n\t" \
  "v_mfma_f32_16x16x32_bf16 v[76:79], a[240:243], v[80:83], v[76:79]\n\t" \
  "v_exp_f32 v43, v43\n\t" \
  "v_mfma_f32_16x16x32_bf16 v[96:99], a[192:195], v[112:115], v[188:191]\n\t" \
  "ds_read_b64_tr_b16 a[180:181], v228 offset:8960\n\t" \
  "ds_read_b64_tr_b16 a[182:183], v228 offset:13056\n\t" \
  "v_mfma_f32_16x16x32_bf16 v[96:99], a[196:199], v[116:119], v[96:99]\n\t" \
  "v_exp_f32 v44, v44\n\t" \
  "v_mfma_f32_16x16x32_bf16 v[96:99], a[200:203], v[120:123], v[96:99]\n\t" \
  "v_exp_f32 v45, v45\n\t" \
  "v_mfma_f32_16x16x32_bf16 v[96:99], a[204:207], v[124:127], v[96:99]\n\t" \
  "ds_read_b64_tr_b16 a[184:185], v229 offset:768\n\t" \
  "ds_read_b64_tr_b16 a[186:187], v229 offset:4864\n\t" \
  "v_mfma_f32_16x16x32_bf16 v[100:103], a[220:223], v[124:127], v[188:191]\n\t" \
  "v_exp_f32 v46, v46\n\t" \
  "v_mfma_f32_16x16x32_bf16 v[100:103], a[216:219], v[120:123], v[100:103]\n\t" \
  "v_exp_f32 v47, v47\n\t" \
  "v_mfma_f32_16x16x32_bf16 v[100:103], a[212:215], v[116:119], v[100:103]\n\t" \
  "v_exp_f32 v64, v64\n\t" \
  "v_mfma_f32_16x16x32_bf16 v[100:103], a[208:211], v[112:115], v[100:103]\n\t" \
  "ds_read_b64_tr_b16 a[188:189], v229 offset:8960\n\t" \
  "ds_read_b64_tr_b16 a[190:191], v229 offset:13056\n\t" \
  "v_mfma_f32_16x16x32_bf16 v[104:107], a[224:227], v[112:115], v[188:191]\n\t" \
  "v_exp_f32 v65, v65\n\t" \
  "v_mfma_f32_16x16x32_bf16 v[104:107], a[228:231], v[116:119], v[104:107]\n\t" \
  "v_exp_f32 v66, v66\n\t" \
  "v_mfma_f32_16x16x32_bf16 v[104:107], a[232:235], v[120:123], v[104:107]\n\t" \
  "v_exp_f32 v67, v67\n\t" \
  "v_mfma_f32_16x16x32_bf16 v[104:107], a[236:239], v[124:127], v[104:107]\n\t" \
  "v_exp_f32 v68, v68\n\t" \
  "v_mfma_f32_16x16x32_bf16 v[108:111], a[252:255], v[124:127], v[188:191]\n\t" \
  "v_exp_f32 v69, v69\n\t" \
  "v_mfma_f32_16x16x32_bf16 v[108:111], a[248:251], v[120:123], v[108:111]\n\t" \
  "v_exp_f32 v70, v70\n\t" \
  "v_mfma_f32_16x16x32_bf16 v[108:111], a[244:247], v[116:119], v[108:111]\n\t" \
  "v_exp_f32 v71, v71\n\t" \
  "v_mfma_f32_16x16x32_bf16 v[108:111], a[240:243], v[112:115], v[108:111]\n\t" \
  "s_waitcnt vmcnt(5) lgkmcnt(0)\n\t" \
  "s_barrier\n\t" \
  STA("s_memrealtime s[94:95]\n\t") \
  STA("s_memtime s[92:93]\n\t") \
  "v_mfma_f32_16x16x32_bf16 a[0:3], a[128:131], v[128:131], a[0:3]\n\t" \
  "v_exp_f32 v72, v72\n\t" \
  "v_mfma_f32_16x16x32_bf16 a[0:3], a[132:135], v[132:135], a[0:3]\n\t" \
  "v_exp_f32 v73, v73\n\t" \
  "v_mfma_f32_16x16x32_bf16 a[16:19], a[136:139], v[128:131], a[16:19]\n\t" \
  "ds_read_b128 a[192:195], v220 offset:0\n\t" \
  "ds_read_b128 a[196:199], v221 offset:0\n\t" \
  "v_mfma_f32_16x16x32_bf16 a[16:19], a[140:143], v[132:135], a[16:19]\n\t" \
  "v_exp_f32 v74, v74\n\t" \
  "v_mfma_f32_16x16x32_bf16 a[32:35], a[144:147], v[128:131], a[32:35]\n\t" \
  "v_exp_f32 v75, v75\n\t" \
  "v_mfma_f32_16x16x32_bf16 a[32:35], a[148:151], v[132:135], a[32:35]\n\t" \
  "v_exp_f32 v76, v76\n\t" \
  "v_mfma_f32_16x16x32_bf16 a[48:51], a[152:155], v[128:131], a[48:51]\n\t" \
  "v_exp_f32 v77, v77\n\t" \
  "v_mfma_f32_16x16x32_bf16 a[48:51], a[156:159], v[132:135], a[48:51]\n\t" \
  "v_exp_f32 v78, v78\n\t" \
  "v_mfma_f32_16x16x32_bf16 a[64:67], a[160:163], v[128:131], a[64:67]\n\t" \
  "v_exp_f32 v79, v79\n\t" \
  "v_mfma_f32_16x16x32_bf16 a[64:67], a[164:167], v[132:135], a[64:67]\n\t" \
  "v_exp_f32 v96, v96\n\t" \
  "v_mfma_f32_16x16x32_bf16 a[80:83], a[168:171], v[128:131], a[80:83]\n\t" \
  "ds_read_b128 a[200:203], v222 offset:0\n\t" \
  "ds_read_b128 a[204:207], v223 offset:0\n\t" \
  "v_mfma_f32_16x16x32_bf16 a[80:83], a[172:175], v[132:135], a[80:83]\n\t" \
  "v_exp_f32 v97, v97\n\t" \
  "v_mfma_f32_16x16x32_bf16 a[96:99], a[176:179], v[128:131], a[96:99]\n\t" \
  STA("v_writelane_b32 %[stv], s94, 44\n\t") \
  STA("v_writelane_b32 %[stv], s92, 45\n\t") \
  "v_exp_f32 v98, v98\n\t" \
  "v_mfma_f32_16x16x32_bf16 a[96:99], a[180:183], v[132:135], a[96:99]\n\t" \
  "v_exp_f32 v99, v99\n\t" \
  "v_mfma_f32_16x16x32_bf16 a[112:115], a[184:187], v[128:131], a[112:115]\n\t" \
  "v_exp_f32 v100, v100\n\t" \
  "v_mfma_f32_16x16x32_bf16 a[112:115], a[188:191], v[132:135], a[112:115]\n\t" \
  "v_exp_f32 v101, v101\n\t" \
  "v_mfma_f32_16x16x32_bf16 v[160:163], v[216:219], v[128:131], v[160:163]\n\t" \
  "v_exp_f32 v102, v102\n\t" \
  "v_mfma_f32_16x16x32_bf16 v[160:163], v[216:219], v[132:135], v[160:163]\n\t" \
  "ds_read_b128 a[208:211], v220 offset:4096\n\t" \
  "v_cvt_pk_bf16_f32 v128, v0, v1\n\t" \
  "v_mfma_f32_16x16x32_bf16 a[4:7], a[128:131], v[136:139], a[4:7]\n\t" \
  "v_exp_f32 v103, v103\n\t" \
  "v_mfma_f32_16x16x32_bf16 a[4:7], a[132:135], v[140:143], a[4:7]\n\t" \
  "ds_read_b128 a[212:215], v221 offset:4096\n\t" \
  "v_cvt_pk_bf16_f32 v129, v2, v3\n\t" \
  "v_mfma_f32_16x16x32_bf16 a[20:23], a[136:139], v[136:139], a[20:23]\n\t" \
  "s_add_u32 m0, %[m], 65536\n\t" \
  "v_exp_f32 v104, v104\n\t" \
  "v_mfma_f32_16x16x32_bf16 a[20:23], a[140:143], v[140:143], a[20:23]\n\t" \
  "s_add_u32 s96, %[sofk], 114688\n\t" \
  "v_exp_f32 v105, v105\n\t" \
  "v_mfma_f32_16x16x32_bf16 a[36:39], a[144:147], v[136:139], a[36:39]\n\t" \
  "v_exp_f32 v106, v106\n\t" \
  "v_mfma_f32_16x16x32_bf16 a[36:39], a[148:151], v[140:143], a[36:39]\n\t" \
  "ds_read_b128 a[216:219], v222 offset:4096\n\t" \
  "v_cvt_pk_bf16_f32 v130, v4, v5\n\t" \
  "v_mfma_f32_16x16x32_bf16 a[52:55], a[152:155], v[136:139], a[52:55]\n\t" \
  "v_exp_f32 v107, v107\n\t" \
  "v_mfma_f32_16x16x32_bf16 a[52:55], a[156:159], v[140:143], a[52:55]\n\t" \
  "v_exp_f32 v108, v108\n\t" \
  "v_mfma_f32_16x16x32_bf16 a[68:71], a[160:163], v[136:139], a[68:71]\n\t" \
  "v_exp_f32 v109, v109\n\t" \
  "v_mfma_f32_16x16x32_bf16 a[68:71], a[164:167], v[140:143], a[68:71]\n\t" \
  "buffer_load_dwordx4 v224, %[sK], s96 offen offset:0 lds\n\t" \
  "v_mfma_f32_16x16x32_bf16 a[84:87], a[168:171], v[136:139], a[84:87]\n\t" \
  "ds_read_b128 a[220:223], v223 offset:4096\n\t" \
  "v_cvt_pk_bf16_f32 v131, v6, v7\n\t" \
  "v_mfma_f32_16x16x32_bf16 a[84:87], a[172:175], v[140:143], a[84:87]\n\t" \
  "v_exp_f32 v110, v110\n\t" \
  "v_mfma_f32_16x16x32_bf16 a[100:103], a[176:179], v[136:139], a[100:103]\n\t" \
  "ds_read_b128 a[224:227], v220 offset:8192\n\t" \
  "v_cvt_pk_bf16_f32 v132, v8, v9\n\t" \
  "v_mfma_f32_16x16x32_bf16 a[100:103], a[180:183], v[140:143], a[100:103]\n\t" \
  "buffer_load_dwordx4 v225, %[sK], s96 offen offset:1024 lds\n\t" \
  "v_mfma_f32_16x16x32_bf16 a[116:119], a[184:187], v[136:139], a[116:119]\n\t" \
  "v_exp_f32 v111, v111\n\t" \
  "v_mfma_f32_16x16x32_bf16 a[116:119], a[188:191], v[140:143], a[116:119]\n\t" \
  "ds_read_b128 a[228:231], v221 offset:8192\n\t" \
  "v_cvt_pk_bf16_f32 v133, v10, v11\n\t" \
  "v_mfma_f32_16x16x32_bf16 v[164:167], v[216:219], v[136:139], v[164:167]\n\t" \
  "buffer_load_dwordx4 v226, %[sK], s96 offen offset:2048 lds\n\t" \
  "v_mfma_f32_16x16x32_bf16 v[164:167], v[216:219], v[140:143], v[164:167]\n\t" \
  "ds_read_b128 a[232:235], v222 offset:8192\n\t" \
  "v_cvt_pk_bf16_f32 v134, v12, v13\n\t" \
  "v_mfma_f32_16x16x32_bf16 a[8:11], a[128:131], v[144:147], a[8:11]\n\t" \
  "ds_read_b128 a[236:239], v223 offset:8192\n\t" \
  "v_cvt_pk_bf16_f32 v135, v14, v15\n\t" \
  "v_mfma_f32_16x16x32_bf16 a[8:11], a[132:135], v[148:151], a[8:11]\n\t" \
  "ds_read_b128 a[240:243], v220 offset:12288\n\t" \
  "v_cvt_pk_bf16_f32 v136, v32, v33\n\t" \
  "v_mfma_f32_16x16x32_bf16 a[24:27], a[136:139], v[144:147], a[24:27]\n\t" \
  ".Lhs8:\n\ts_cbranch_execz .Lhs8n\n\t" \
  "buffer_load_dwordx4 v227, %[sK], s96 offen offset:3072 lds\n\t" \
  "v_mfma_f32_16x16x32_bf16 a[24:27], a[140:143], v[148:151], a[24:27]\n\t" \
  "s_add_u32 m0, %[m], 81920\n\t" \
  "ds_read_b128 a[244:247], v221 offset:12288\n\t" \
  "v_cvt_pk_bf16_f32 v137, v34, v35\n\t" \
  "v_mfma_f32_16x16x32_bf16 a[40:43], a[144:147], v[144:147], a[40:43]\n\t" \
  "s_add_u32 s96, %[sofk], 98304\n\t" \
  "v_cvt_pk_bf16_f32 v138, v36, v37\n\t" \
  "v_cvt_pk_bf16_f32 v139, v38, v39\n\t" \
  "v_mfma_f32_16x16x32_bf16 a[40:43], a[148:151], v[148:151], a[40:43]\n\t" \
  "v_cvt_pk_bf16_f32 v140, v40, v41\n\t" \
  "v_cvt_pk_bf16_f32 v141, v42, v43\n\t" \
  "v_mfma_f32_16x16x32_bf16 a[56:59], a[152:155], v[144:147], a[56:59]\n\t" \
  "v_cvt_pk_bf16_f32 v142, v44, v45\n\t" \
  "v_cvt_pk_bf16_f32 v143, v46, v47\n\t" \
  "v_mfma_f32_16x16x32_bf16 a[56:59], a[156:159], v[148:151], a[56:59]\n\t" \
  "v_mfma_f32_16x16x32_bf16 a[72:75], a[160:163], v[144:147], a[72:75]\n\t" \
  "v_mfma_f32_16x16x32_bf16 a[72:75], a[164:167], v[148:151], a[72:75]\n\t" \
  "v_mfma_f32_16x16x32_bf16 a[88:91], a[168:171], v[144:147], a[88:91]\n\t" \
  "v_mfma_f32_16x16x32_bf16 a[88:91], a[172:175], v[148:151], a[88:91]\n\t" \
  "v_mfma_f32_16x16x32_bf16 a[104:107], a[176:179], v[144:147], a[104:107]\n\t" \
  "buffer_load_dwordx4 v230, %[sV], s96 offen offset:0 lds\n\t" \
  "v_mfma_f32_16x16x32_bf16 a[104:107], a[180:183], v[148:151], a[104:107]\n\t" \
  "v_mfma_f32_16x16x32_bf16 a[120:123], a[184:187], v[144:147], a[120:123]\n\t" \
  "v_mfma_f32_16x16x32_bf16 a[120:123], a[188:191], v[148:151], a[120:123]\n\t" \
  "buffer_load_dwordx4 v231, %[sV], s96 offen offset:1024 lds\n\t" \
  "v_mfma_f32_16x16x32_bf16 v[168:171], v[216:219], v[144:147], v[168:171]\n\t" \
  "v_mfma_f32_16x16x32_bf16 v[168:171], v[216:219], v[148:151], v[168:171]\n\t" \
  "ds_read_b128 a[248:251], v222 offset:12288\n\t" \
  "v_cvt_pk_bf16_f32 v144, v64, v65\n\t" \
  "v_mfma_f32_16x16x32_bf16 a[12:15], a[128:131], v[152:155], a[12:15]\n\t" \
  "ds_read_b128 a[252:255], v223 offset:12288\n\t" \
  "v_cvt_pk_bf16_f32 v145, v66, v67\n\t" \
  "v_mfma_f32_16x16x32_bf16 a[12:15], a[132:135], v[156:159], a[12:15]\n\t" \
  "buffer_load_dwordx4 v230, %[sV], s96 offen offset:2048 lds\n\t" \
  "v_mfma_f32_16x16x32_bf16 a[28:31], a[136:139], v[152:155], a[28:31]\n\t" \
  "v_cvt_pk_bf16_f32 v146, v68, v69\n\t" \
  "v_cvt_pk_bf16_f32 v147, v70, v71\n\t" \
  "v_mfma_f32_16x16x32_bf16 a[28:31], a[140:143], v[156:159], a[28:31]\n\t" \
  "v_cvt_pk_bf16_f32 v148, v72, v73\n\t" \
  "v_cvt_pk_bf16_f32 v149, v74, v75\n\t" \
  "v_mfma_f32_16x16x32_bf16 a[44:47], a[144:147], v[152:155], a[44:47]\n\t" \
  "buffer_load_dwordx4 v231, %[sV], s96 offen offset:3072 lds\n\t" \
  "v_mfma_f32_16x16x32_bf16 a[44:47], a[148:151], v[156:159], a[44:47]\n\t" \
  "v_cvt_pk_bf16_f32 v150, v76, v77\n\t" \
  "v_cvt_pk_bf16_f32 v151, v78, v79\n\t" \
  "v_mfma_f32_16x16x32_bf16 a[60:63], a[152:155], v[152:155], a[60:63]\n\t" \
  "s_add_u32 m0, %[m], 131072\n\t" \
  "s_mov_b32 s96, 40960\n\t" \
  "v_mfma_f32_16x16x32_bf16 a[60:63], a[156:159], v[156:159], a[60:63]\n\t" \
  "v_mfma_f32_16x16x32_bf16 a[76:79], a[160:163], v[152:155], a[76:79]\n\t" \
  "v_mfma_f32_16x16x32_bf16 a[76:79], a[164:167], v[156:159], a[76:79]\n\t" \
  "buffer_load_dwordx4 v232, %[sU], s96 offen offset:2048 lds\n\t" \
  "v_mfma_f32_16x16x32_bf16 a[92:95], a[168:171], v[152:155], a[92:95]\n\t" \
  "v_lshrrev_b32 v205, 1, v233\n\t" \
  "v_lshlrev_b32 v205, 2, v205\n\t" \
  "v_mfma_f32_16x16x32_bf16 a[92:95], a[172:175], v[156:159], a[92:95]\n\t" \
  "v_add_u32 v205, 8192, v205\n\t" \
  "v_mfma_f32_16x16x32_bf16 a[108:111], a[176:179], v[152:155], a[108:111]\n\t" \
  "buffer_load_dword v209, v205, %[ws], 0 offen sc1\n\t" \
  "v_mfma_f32_16x16x32_bf16 a[108:111], a[180:183], v[156:159], a[108:111]\n\t" \
  "s_mov_b32 s96, 47104\n\t" \
  "v_mfma_f32_16x16x32_bf16 a[124:127], a[184:187], v[152:155], a[124:127]\n\t" \
  "buffer_load_dwordx4 v232, %[sU], s96 offen offset:3072 lds\n\t" \
  "v_mfma_f32_16x16x32_bf16 a[124:127], a[188:191], v[156:159], a[124:127]\n\t" \
  "s_mul_i32 s98, %[sofk], 5\n\t" \
  "s_xor_b32 s98, s98, 8192\n\t" \
  "s_lshr_b32 s98, s98, 1\n\t" \
  "v_mfma_f32_16x16x32_bf16 v[172:175], v[216:219], v[152:155], v[172:175]\n\t" \
  "buffer_load_dword %[pfd], v204, %[sD], s98 offen\n\t" \
  "v_mfma_f32_16x16x32_bf16 v[172:175], v[216:219], v[156:159], v[172:175]\n\t" \
  "s_waitcnt lgkmcnt(0)\n\t" \
  "v_cvt_pk_bf16_f32 v152, v96, v97\n\t" \
  "v_cvt_pk_bf16_f32 v153, v98, v99\n\t" \
  STA("s_memrealtime s[94:95]\n\t") \
  STA("s_memtime s[92:93]\n\t") \
  "v_mfma_f32_16x16x32_bf16 v[0:3], a[192:195], v[16:19], v[176:179]\n\t" \
  "ds_read_b64_tr_b16 a[128:129], v237 offset:0\n\t" \
  "v_cvt_pk_bf16_f32 v154, v100, v101\n\t" \
  "v_mfma_f32_16x16x32_bf16 v[0:3], a[196:199], v[20:23], v[0:3]\n\t" \
  "ds_read_b64_tr_b16 a[130:131], v237 offset:4096\n\t" \
  "v_cvt_pk_bf16_f32 v155, v102, v103\n\t" \
  "v_mfma_f32_16x16x32_bf16 v[0:3], a[200:203], v[24:27], v[0:3]\n\t" \
  "ds_read_b64_tr_b16 a[132:133], v237 offset:8192\n\t" \
  "v_cvt_pk_bf16_f32 v156, v104, v105\n\t" \
  "v_mfma_f32_16x16x32_bf16 v[0:3], a[204:207], v[28:31], v[0:3]\n\t" \
  "ds_read_b64_tr_b16 a[134:135], v237 offset:12288\n\t" \
  "v_cvt_pk_bf16_f32 v157, v106, v107\n\t" \
  "v_mfma_f32_16x16x32_bf16 v[4:7], a[220:223], v[28:31], v[176:179]\n\t" \
  "ds_read_b64_tr_b16 a[136:137], v238 offset:0\n\t" \
  "v_cvt_pk_bf16_f32 v158, v108, v109\n\t" \
  "v_mfma_f32_16x16x32_bf16 v[4:7], a[216:219], v[24:27], v[4:7]\n\t" \
  "ds_read_b64_tr_b16 a[138:139], v238 offset:4096\n\t" \
  "v_cvt_pk_bf16_f32 v159, v110, v111\n\t" \
  "v_mfma_f32_16x16x32_bf16 v[4:7], a[212:215], v[20:23], v[4:7]\n\t" \
  "v_exp_f32 v0, v0\n\t" \
  "v_mfma_f32_16x16x32_bf16 v[4:7], a[208:211], v[16:19], v[4:7]\n\t" \
  "v_exp_f32 v1, v1\n\t" \
  "v_mfma_f32_16x16x32_bf16 v[8:11], a[224:227], v[16:19], v[176:179]\n\t" \
  "v_exp_f32 v2, v2\n\t" \
  "v_mfma_f32_16x16x32_bf16 v[8:11], a[228:231], v[20:23], v[8:11]\n\t" \
  "v_exp_f32 v3, v3\n\t" \
  "v_mfma_f32_16x16x32_bf16 v[8:11], a[232:235], v[24:27], v[8:11]\n\t" \
  "ds_read_b64_tr_b16 a[140:141], v238 offset:8192\n\t" \
  "ds_read_b64_tr_b16 a[142:143], v238 offset:12288\n\t" \
  "v_mfma_f32_16x16x32_bf16 v[8:11], a[236:239], v[28:31], v[8:11]\n\t" \
  "v_exp_f32 v4, v4\n\t" \
  "v_mfma_f32_16x16x32_bf16 v[12:15], a[252:255], v[28:31], v[176:179]\n\t" \
  STA("v_writelane_b32 %[stv], s94, 46\n\t") \
  STA("v_writelane_b32 %[stv], s92, 47\n\t") \
  "ds_read_b64_tr_b16 a[144:145], v237 offset:256\n\t" \
  "ds_read_b64_tr_b16 a[146:147], v237 offset:4352\n\t" \
  "v_mfma_f32_16x16x32_bf16 v[12:15], a[248:251], v[24:27], v[12:15]\n\t" \
  "ds_read_b64_tr_b16 a[148:149], v237 offset:8448\n\t" \
  "ds_read_b64_tr_b16 a[150:151], v237 offset:12544\n\t" \
  "v_lshl_add_u32 v207, v233, 14, %[cto]\n\t" \
  "v_min_u32 v207, .Lfz_z-.Lat_t1-4, v207\n\t" \
  "v_mfma_f32_16x16x32_bf16 v[12:15], a[244:247], v[20:23], v[12:15]\n\t" \
  "s_getpc_b64 s[96:97]\n\t" \
  ".Lat_t1:\n\t" \
  "global_load_dword %[pfd], v207, s[96:97]\n\t" \
  "v_mfma_f32_16x16x32_bf16 v[12:15], a[240:243], v[16:19], v[12:15]\n\t" \
  "s_mul_i32 s98, %[sofk], 5\n\t" \
  "s_xor_b32 s98, s98, 12288\n\t" \
  "s_lshr_b32 s98, s98, 1\n\t" \
  "v_mfma_f32_16x16x32_bf16 v[32:35], a[192:195], v[48:51], v[180:183]\n\t" \
  "buffer_load_dword %[pfd], v204, %[sD], s98 offen\n\t" \
  "v_mfma_f32_16x16x32_bf16 v[32:35], a[196:199], v[52:55], v[32:35]\n\t" \
  "v_exp_f32 v5, v5\n\t" \
  "v_mfma_f32_16x16x32_bf16 v[32:35], a[200:203], v[56:59], v[32:35]\n\t" \
  "v_exp_f32 v6, v6\n\t" \
  "v_mfma_f32_16x16x32_bf16 v[32:35], a[204:207], v[60:63], v[32:35]\n\t" \
  "v_exp_f32 v7, v7\n\t" \
  "v_mfma_f32_16x16x32_bf16 v[36:39], a[220:223], v[60:63], v[180:183]\n\t" \
  "v_exp_f32 v8, v8\n\t" \
  "v_mfma_f32_16x16x32_bf16 v[36:39], a[216:219], v[56:59], v[36:39]\n\t" \
  "v_exp_f32 v9, v9\n\t" \
  "v_mfma_f32_16x16x32_bf16 v[36:39], a[212:215], v[52:55], v[36:39]\n\t" \
  "v_exp_f32 v10, v10\n\t" \
  "v_mfma_f32_16x16x32_bf16 v[36:39], a[208:211], v[48:51], v[36:39]\n\t" \
  "ds_read_b64_tr_b16 a[152:153], v238 offset:256\n\t" \
  "ds_read_b64_tr_b16 a[154:155], v238 offset:4352\n\t" \
  "v_mfma_f32_16x16x32_bf16 v[40:43], a[224:227], v[48:51], v[180:183]\n\t" \
  "v_exp_f32 v11, v11\n\t" \
  "v_mfma_f32_16x16x32_bf16 v[40:43], a[228:231], v[52:55], v[40:43]\n\t" \
  "v_exp_f32 v12, v12\n\t" \
  "v_mfma_f32_16x16x32_bf16 v[40:43], a[232:235], v[56:59], v[40:43]\n\t" \
  "v_exp_f32 v13, v13\n\t" \
  "v_mfma_f32_16x16x32_bf16 v[40:43], a[236:239], v[60:63], v[40:43]\n\t" \
  "ds_read_b64_tr_b16 a[156:157], v238 offset:8448\n\t" \
  "ds_read_b64_tr_b16 a[158:159], v238 offset:12544\n\t" \
  "v_mfma_f32_16x16x32_bf16 v[44:47], a[252:255], v[60:63], v[180:183]\n\t" \
  "v_exp_f32 v14, v14\n\t" \
  "v_mfma_f32_16x16x32_bf16 v[44:47], a[248:251], v[56:59], v[44:47]\n\t" \
  "v_exp_f32 v15, v15\n\t" \
  "v_mfma_f32_16x16x32_bf16 v[44:47], a[244:247], v[52:55], v[44:47]\n\t" \
  "ds_read_b64_tr_b16 a[160:161], v237 offset:512\n\t" \
  "ds_read_b64_tr_b16 a[162:163], v237 offset:4608\n\t" \
  "v_mfma_f32_16x16x32_bf16 v[44:47], a[240:243], v[48:51], v[44:47]\n\t" \
  "v_exp_f32 v32, v32\n\t" \
  "v_mfma_f32_16x16x32_bf16 v[64:67], a[192:195], v[80:83], v[184:187]\n\t" \
  "v_exp_f32 v33, v33\n\t" \
  "v_mfma_f32_16x16x32_bf16 v[64:67], a[196:199], v[84:87], v[64:67]\n\t" \
  "v_exp_f32 v34, v34\n\t" \
  "v_mfma_f32_16x16x32_bf16 v[64:67], a[200:203], v[88:91], v[64:67]\n\t" \
  "ds_read_b64_tr_b16 a[164:165], v237 offset:8704\n\t" \
  "ds_read_b64_tr_b16 a[166:167], v237 offset:12800\n\t" \
  "v_mfma_f32_16x16x32_bf16 v[64:67], a[204:207], v[92:95], v[64:67]\n\t" \
  "v_exp_f32 v35, v35\n\t" \
  "v_mfma_f32_16x16x32_bf16 v[68:71], a[220:223], v[92:95], v[184:187]\n\t" \
  "v_exp_f32 v36, v36\n\t" \
  "v_mfma_f32_16x16x32_bf16 v[68:71], a[216:219], v[88:91], v[68:71]\n\t" \
  "ds_read_b64_tr_b16 a[168:169], v238 offset:512\n\t" \
  "ds_read_b64_tr_b16 a[170:171], v238 offset:4608\n\t" \
  "v_mfma_f32_16x16x32_bf16 v[68:71], a[212:215], v[84:87], v[68:71]\n\t" \
  "v_exp_f32 v37, v37\n\t" \
  "v_mfma_f32_16x16x32_bf16 v[68:71], a[208:211], v[80:83], v[68:71]\n\t" \
  "v_exp_f32 v38, v38\n\t" \
  "v_mfma_f32_16x16x32_bf16 v[72:75], a[224:227], v[80:83], v[184:187]\n\t" \
  "v_exp_f32 v39, v39\n\t" \
  "v_mfma_f32_16x16x32_bf16 v[72:75], a[228:231], v[84:87], v[72:75]\n\t" \
  "ds_read_b64_tr_b16 a[172:173], v238 offset:8704\n\t" \
  "ds_read_b64_tr_b16 a[174:175], v238 offset:12800\n\t" \
  "v_mfma_f32_16x16x32_bf16 v[72:75], a[232:235], v[88:91], v[72:75]\n\t" \
  "v_exp_f32 v40, v40\n\t" \
  "v_mfma_f32_16x16x32_bf16 v[72:75], a[236:239], v[92:95], v[72:75]\n\t" \
  "v_exp_f32 v41, v41\n\t" \
  "v_mfma_f32_16x16x32_bf16 v[76:79], a[252:255], v[92:95], v[184:187]\n\t" \
  "ds_read_b64_tr_b16 a[176:177], v237 offset:768\n\t" \
  "ds_read_b64_tr_b16 a[178:179], v237 offset:4864\n\t" \
  "v_mfma_f32_16x16x32_bf16 v[76:79], a[248:251], v[88:91], v[76:79]\n\t" \
  "v_exp_f32 v42, v42\n\t" \
  "v_mfma_f32_16x16x32_bf16 v[76:79], a[244:247], v[84:87], v[76:79]\n\t" \
  "v_exp_f32 v43, v43\n\t" \
  "v_mfma_f32_16x16x32_bf16 v[76:79], a[240:243], v[80:83], v[76:79]\n\t" \
  "v_exp_f32 v44, v44\n\t" \
  "v_mfma_f32_16x16x32_bf16 v[96:99], a[192:195], v[112:115], v[188:191]\n\t" \
  "ds_read_b64_tr_b16 a[180:181], v237 offset:8960\n\t" \
  "ds_read_b64_tr_b16 a[182:183], v237 offset:13056\n\t" \
  "v_mfma_f32_16x16x32_bf16 v[96:99], a[196:199], v[116:119], v[96:99]\n\t" \
  "v_exp_f32 v45, v45\n\t" \
  "v_mfma_f32_16x16x32_bf16 v[96:99], a[200:203], v[120:123], v[96:99]\n\t" \
  "v_exp_f32 v46, v46\n\t" \
  "v_mfma_f32_16x16x32_bf16 v[96:99], a[204:207], v[124:127], v[96:99]\n\t" \
  "ds_read_b64_tr_b16 a[184:185], v238 offset:768\n\t" \
  "ds_read_b64_tr_b16 a[186:187], v238 offset:4864\n\t" \
  "v_mfma_f32_16x16x32_bf16 v[100:103], a[220:223], v[124:127], v[188:191]\n\t" \
  "v_exp_f32 v47, v47\n\t" \
  "v_mfma_f32_16x16x32_bf16 v[100:103], a[216:219], v[120:123], v[100:103]\n\t" \
  "v_exp_f32 v64, v64\n\t" \
  "v_mfma_f32_16x16x32_bf16 v[100:103], a[212:215], v[116:119], v[100:103]\n\t" \
  "v_exp_f32 v65, v65\n\t" \
  "v_mfma_f32_16x16x32_bf16 v[100:103], a[208:211], v[112:115], v[100:103]\n\t" \
  "ds_read_b64_tr_b16 a[188:189], v238 offset:8960\n\t" \
  "ds_read_b64_tr_b16 a[190:191], v238 offset:13056\n\t" \
  "v_mfma_f32_16x16x32_bf16 v[104:107], a[224:227], v[112:115], v[188:191]\n\t" \
  "v_exp_f32 v66, v66\n\t" \
  "v_mfma_f32_16x16x32_bf16 v[104:107], a[228:231], v[116:119], v[104:107]\n\t" \
  "v_exp_f32 v67, v67\n\t" \
  "v_mfma_f32_16x16x32_bf16 v[104:107], a[232:235], v[120:123], v[104:107]\n\t" \
  "v_exp_f32 v68, v68\n\t" \
  "v_mfma_f32_16x16x32_bf16 v[104:107], a[236:239], v[124:127], v[104:107]\n\t" \
  "v_exp_f32 v69, v69\n\t" \
  "v_mfma_f32_16x16x32_bf16 v[108:111], a[252:255], v[124:127], v[188:191]\n\t" \
  "v_exp_f32 v70, v70\n\t" \
  "v_mfma_f32_16x16x32_bf16 v[108:111], a[248:251], v[120:123], v[108:111]\n\t" \
  "v_exp_f32 v71, v71\n\t" \
  "v_mfma_f32_16x16x32_bf16 v[108:111], a[244:247], v[116:119], v[108:111]\n\t" \
  "v_exp_f32 v72, v72\n\t" \
  "v_mfma_f32_16x16x32_bf16 v[108:111], a[240:243], v[112:115], v[108:111]\n\t" \
  "s_waitcnt vmcnt(6) lgkmcnt(0)\n\t" \
  "s_barrier\n\t" \
  STA("s_memrealtime s[94:95]\n\t") \
  STA("s_memtime s[92:93]\n\t") \
  "v_mfma_f32_16x16x32_bf16 a[0:3], a[128:131], v[128:131], a[0:3]\n\t" \
  "v_exp_f32 v73, v73\n\t" \
  "v_mfma_f32_16x16x32_bf16 a[0:3], a[132:135], v[132:135], a[0:3]\n\t" \
  "v_exp_f32 v74, v74\n\t" \
  "v_mfma_f32_16x16x32_bf16 a[16:19], a[136:139], v[128:131], a[16:19]\n\t" \
  "ds_read_b128 a[192:195], v220 offset:16384\n\t" \
  "ds_read_b128 a[196:199], v221 offset:16384\n\t" \
  "v_mfma_f32_16x16x32_bf16 a[16:19], a[140:143], v[132:135], a[16:19]\n\t" \
  "s_waitcnt vmcnt(4)\n\t" \
  "v_and_b32 v206, 0xbfffbfff, v209\n\t" \
  "v_cmp_ne_u32_e64 s[96:97], %[tag], v206\n\t" \
  "s_cmp_lg_u64 s[96:97], 0\n\t" \
  "s_cbranch_scc1 .Lat_v\n\t" \
  ".Lat_w:\n\t" \
  "v_mfma_f32_16x16x32_bf16 a[32:35], a[144:147], v[128:131], a[32:35]\n\t" \
  "v_exp_f32 v75, v75\n\t" \
  "v_mfma_f32_16x16x32_bf16 a[32:35], a[148:151], v[132:135], a[32:35]\n\t" \
  "v_exp_f32 v76, v76\n\t" \
  "v_mfma_f32_16x16x32_bf16 a[48:51], a[152:155], v[128:131], a[48:51]\n\t" \
  "ds_read_b128 a[200:203], v222 offset:16384\n\t" \
  "ds_read_b128 a[204:207], v223 offset:16384\n\t" \
  "v_mfma_f32_16x16x32_bf16 a[48:51], a[156:159], v[132:135], a[48:51]\n\t" \
  "ds_read_b128 a[208:211], v220 offset:20480\n\t" \
  "v_and_b32 v206, 1, v233\n\t" \
  "v_lshlrev_b32 v206, 4, v206\n\t" \
  "v_mfma_f32_16x16x32_bf16 a[64:67], a[160:163], v[128:131], a[64:67]\n\t" \
  "v_lshrrev_b32 v206, v206, v209\n\t" \
  "v_and_b32 v206, 0x4000, v206\n\t" \
  "v_mfma_f32_16x16x32_bf16 a[64:67], a[164:167], v[132:135], a[64:67]\n\t" \
  "v_cmp_ne_u32_e64 s[96:97], 0, v206\n\t" \
  STS("s_nop 4\n\t") \
  STS("v_writelane_b32 %[stv], s96, 60\n\t") \
  STS("v_writelane_b32 %[stv], s97, 61\n\t") \
  "s_bcnt1_i32_b32 s98, s96\n\t" \
  "s_bcnt1_i32_b32 %[na], s97\n\t" \
  "s_cmp_gt_u32 s98, %[na]\n\t" \
  "s_cselect_b32 %[nb], 0x2000, 0\n\t" \
  "s_cselect_b32 s98, s96, s97\n\t" \
  "s_cselect_b32 s96, s97, s96\n\t" \
  "v_mfma_f32_16x16x32_bf16 a[80:83], a[168:171], v[128:131], a[80:83]\n\t" \
  "s_lshr_b32 s97, s98, 16\n\t" \
  "s_bcnt1_i32_b32 s97, s97\n\t" \
  "s_lshr_b32 s96, s96, 16\n\t" \
  "s_bcnt1_i32_b32 s96, s96\n\t" \
  "s_add_u32 s96, s96, 7\n\t" \
  "s_cmp_ge_u32 s97, s96\n\t" \
  "s_cselect_b32 %[na], -1, 0\n\t" \
  "s_xor_b32 s98, %[po], %[nb]\n\t" \
  "s_and_b32 s98, s98, %[na]\n\t" \
  "s_xor_b32 %[eso], %[eso], s98\n\t" \
  STS("v_writelane_b32 %[stv], %[na], 57\n\t") \
  STS("v_writelane_b32 %[stv], %[nb], 59\n\t") \
  "v_mfma_f32_16x16x32_bf16 a[80:83], a[172:175], v[132:135], a[80:83]\n\t" \
  "v_exp_f32 v77, v77\n\t" \
  "v_mfma_f32_16x16x32_bf16 a[96:99], a[176:179], v[128:131], a[96:99]\n\t" \
  STA("v_writelane_b32 %[stv], s94, 48\n\t") \
  STA("v_writelane_b32 %[stv], s92, 49\n\t") \
  "v_exp_f32 v78, v78\n\t" \
  "v_mfma_f32_16x16x32_bf16 a[96:99], a[180:183], v[132:135], a[96:99]\n\t" \
  "s_xor_b32 s98, %[px], %[nb]\n\t" \
  "s_and_b32 s98, s98, %[na]\n\t" \
  "s_xor_b32 %[xcb], %[xcb], s98\n\t" \
  "s_xor_b32 %[frb], %[frb], %[nb]\n\t" \
  "s_xor_b32 %[fw2], %[fw2], %[nb]\n\t" \
  "v_mfma_f32_16x16x32_bf16 a[112:115], a[184:187], v[128:131], a[112:115]\n\t" \
  "v_exp_f32 v79, v79\n\t" \
  "v_mfma_f32_16x16x32_bf16 a[112:115], a[188:191], v[132:135], a[112:115]\n\t" \
  "v_exp_f32 v96, v96\n\t" \
  "v_mfma_f32_16x16x32_bf16 v[160:163], v[216:219], v[128:131], v[160:163]\n\t" \
  "v_exp_f32 v97, v97\n\t" \
  "v_mfma_f32_16x16x32_bf16 v[160:163], v[216:219], v[132:135], v[160:163]\n\t" \
  "v_exp_f32 v98, v98\n\t" \
  "v_mfma_f32_16x16x32_bf16 a[4:7], a[128:131], v[136:139], a[4:7]\n\t" \
  "v_exp_f32 v99, v99\n\t" \
  "v_mfma_f32_16x16x32_bf16 a[4:7], a[132:135], v[140:143], a[4:7]\n\t" \
  "ds_read_b128 a[212:215], v221 offset:20480\n\t" \
  "v_cvt_pk_bf16_f32 v128, v0, v1\n\t" \
  "v_mfma_f32_16x16x32_bf16 a[20:23], a[136:139], v[136:139], a[20:23]\n\t" \
  "s_add_u32 m0, %[m], 147456\n\t" \
  "v_exp_f32 v100, v100\n\t" \
  "v_mfma_f32_16x16x32_bf16 a[20:23], a[140:143], v[140:143], a[20:23]\n\t" \
  "s_add_u32 s96, %[sofk], 114688\n\t" \
  "v_exp_f32 v101, v101\n\t" \
  "v_mfma_f32_16x16x32_bf16 a[36:39], a[144:147], v[136:139], a[36:39]\n\t" \
  "v_exp_f32 v102, v102\n\t" \
  "v_mfma_f32_16x16x32_bf16 a[36:39], a[148:151], v[140:143], a[36:39]\n\t" \
  "ds_read_b128 a[216:219], v222 offset:20480\n\t" \
  "v_cvt_pk_bf16_f32 v129, v2, v3\n\t" \
  "v_mfma_f32_16x16x32_bf16 a[52:55], a[152:155], v[136:139], a[52:55]\n\t" \
  "v_exp_f32 v103, v103\n\t" \
  "v_mfma_f32_16x16x32_bf16 a[52:55], a[156:159], v[140:143], a[52:55]\n\t" \
  "v_exp_f32 v104, v104\n\t" \
  "v_mfma_f32_16x16x32_bf16 a[68:71], a[160:163], v[136:139], a[68:71]\n\t" \
  "buffer_load_dwordx4 v230, %[sV], s96 offen offset:0 lds\n\t" \
  "v_mfma_f32_16x16x32_bf16 a[68:71], a[164:167], v[140:143], a[68:71]\n\t" \
  "ds_read_b128 a[220:223], v223 offset:20480\n\t" \
  "v_cvt_pk_bf16_f32 v130, v4, v5\n\t" \
  "v_mfma_f32_16x16x32_bf16 a[84:87], a[168:171], v[136:139], a[84:87]\n\t" \
  "buffer_load_dwordx4 v231, %[sV], s96 offen offset:1024 lds\n\t" \
  "v_mfma_f32_16x16x32_bf16 a[84:87], a[172:175], v[140:143], a[84:87]\n\t" \
  "v_exp_f32 v105, v105\n\t" \
  "v_mfma_f32_16x16x32_bf16 a[100:103], a[176:179], v[136:139], a[100:103]\n\t" \
  "ds_read_b128 a[224:227], v220 offset:24576\n\t" \
  "v_cvt_pk_bf16_f32 v131, v6, v7\n\t" \
  "v_mfma_f32_16x16x32_bf16 a[100:103], a[180:183], v[140:143], a[100:103]\n\t" \
  "v_exp_f32 v106, v106\n\t" \
  "v_mfma_f32_16x16x32_bf16 a[116:119], a[184:187], v[136:139], a[116:119]\n\t" \
  "buffer_load_dwordx4 v230, %[sV], s96 offen offset:2048 lds\n\t" \
  "v_mfma_f32_16x16x32_bf16 a[116:119], a[188:191], v[140:143], a[116:119]\n\t" \
  "v_exp_f32 v107, v107\n\t" \
  "v_mfma_f32_16x16x32_bf16 v[164:167], v[216:219], v[136:139], v[164:167]\n\t" \
  "ds_read_b128 a[228:231], v221 offset:24576\n\t" \
  "v_cvt_pk_bf16_f32 v132, v8, v9\n\t" \
  "v_mfma_f32_16x16x32_bf16 v[164:167], v[216:219], v[140:143], v[164:167]\n\t" \
  "v_exp_f32 v108, v108\n\t" \
  "v_mfma_f32_16x16x32_bf16 a[8:11], a[128:131], v[144:147], a[8:11]\n\t" \
  "buffer_load_dwordx4 v231, %[sV], s96 offen offset:3072 lds\n\t" \
  "v_mfma_f32_16x16x32_bf16 a[8:11], a[132:135], v[148:151], a[8:11]\n\t" \
  "s_add_u32 m0, %[m], 16384\n\t" \
  "v_exp_f32 v109, v109\n\t" \
  "v_mfma_f32_16x16x32_bf16 a[24:27], a[136:139], v[144:147], a[24:27]\n\t" \
  "s_xor_b32 s97, %[wob], 131072\n\t" \
  "ds_read_b128 a[232:235], v222 offset:24576\n\t" \
  "v_cvt_pk_bf16_f32 v133, v10, v11\n\t" \
  "v_mfma_f32_16x16x32_bf16 a[24:27], a[140:143], v[148:151], a[24:27]\n\t" \
  "v_exp_f32 v110, v110\n\t" \
  "v_mfma_f32_16x16x32_bf16 a[40:43], a[144:147], v[144:147], a[40:43]\n\t" \
  "v_exp_f32 v111, v111\n\t" \
  "v_mfma_f32_16x16x32_bf16 a[40:43], a[148:151], v[148:151], a[40:43]\n\t" \
  "ds_read_b128 a[236:239], v223 offset:24576\n\t" \
  "v_cvt_pk_bf16_f32 v134, v12, v13\n\t" \
  "v_mfma_f32_16x16x32_bf16 a[56:59], a[152:155], v[144:147], a[56:59]\n\t" \
  "buffer_load_dwordx4 v240, %[wo], s97 offen offset:0 lds\n\t" \
  "v_mfma_f32_16x16x32_bf16 a[56:59], a[156:159], v[148:151], a[56:59]\n\t" \
  "s_add_u32 s97, s97, 7168\n\t" \
  "ds_read_b128 a[240:243], v220 offset:28672\n\t" \
  "v_cvt_pk_bf16_f32 v135, v14, v15\n\t" \
  "v_mfma_f32_16x16x32_bf16 a[72:75], a[160:163], v[144:147], a[72:75]\n\t" \
  "ds_read_b128 a[244:247], v221 offset:28672\n\t" \
  "v_cvt_pk_bf16_f32 v136, v32, v33\n\t" \
  "v_mfma_f32_16x16x32_bf16 a[72:75], a[164:167], v[148:151], a[72:75]\n\t" \
  "buffer_load_dwordx4 v241, %[wo], s97 offen offset:1024 lds\n\t" \
  "v_mfma_f32_16x16x32_bf16 a[88:91], a[168:171], v[144:147], a[88:91]\n\t" \
  "s_add_u32 s97, s97, 7168\n\t" \
  "ds_read_b128 a[248:251], v222 offset:28672\n\t" \
  "v_cvt_pk_bf16_f32 v137, v34, v35\n\t" \
  "v_mfma_f32_16x16x32_bf16 a[88:91], a[172:175], v[148:151], a[88:91]\n\t" \
  "ds_read_b128 a[252:255], v223 offset:28672\n\t" \
  "v_cvt_pk_bf16_f32 v138, v36, v37\n\t" \
  "v_mfma_f32_16x16x32_bf16 a[104:107], a[176:179], v[144:147], a[104:107]\n\t" \
  "v_cvt_pk_bf16_f32 v139, v38, v39\n\t" \
  "v_cvt_pk_bf16_f32 v140, v40, v41\n\t" \
  "v_mfma_f32_16x16x32_bf16 a[104:107], a[180:183], v[148:151], a[104:107]\n\t" \
  "buffer_load_dwordx4 v240, %[wo], s97 offen offset:2048 lds\n\t" \
  "v_mfma_f32_16x16x32_bf16 a[120:123], a[184:187], v[144:147], a[120:123]\n\t" \
  "s_add_u32 s97, s97, 7168\n\t" \
  "v_cvt_pk_bf16_f32 v141, v42, v43\n\t" \
  "v_cvt_pk_bf16_f32 v142, v44, v45\n\t" \
  "v_mfma_f32_16x16x32_bf16 a[120:123], a[188:191], v[148:151], a[120:123]\n\t" \
  "v_cvt_pk_bf16_f32 v143, v46, v47\n\t" \
  "v_mfma_f32_16x16x32_bf16 v[168:171], v[216:219], v[144:147], v[168:171]\n\t" \
  "v_mfma_f32_16x16x32_bf16 v[168:171], v[216:219], v[148:151], v[168:171]\n\t" \
  "v_cvt_pk_bf16_f32 v144, v64, v65\n\t" \
  "v_cvt_pk_bf16_f32 v145, v66, v67\n\t" \
  "v_mfma_f32_16x16x32_bf16 a[12:15], a[128:131], v[152:155], a[12:15]\n\t" \
  "buffer_load_dwordx4 v241, %[wo], s97 offen offset:3072 lds\n\t" \
  "v_mfma_f32_16x16x32_bf16 a[12:15], a[132:135], v[156:159], a[12:15]\n\t" \
  "s_add_u32 m0, %[m], 49152\n\t" \
  "v_cvt_pk_bf16_f32 v146, v68, v69\n\t" \
  "v_cvt_pk_bf16_f32 v147, v70, v71\n\t" \
  "v_mfma_f32_16x16x32_bf16 a[28:31], a[136:139], v[152:155], a[28:31]\n\t" \
  "s_xor_b32 s97, %[wob], 262144\n\t" \
  "v_cvt_pk_bf16_f32 v148, v72, v73\n\t" \
  "v_cvt_pk_bf16_f32 v149, v74, v75\n\t" \
  "v_mfma_f32_16x16x32_bf16 a[28:31], a[140:143], v[156:159], a[28:31]\n\t" \
  "v_cvt_pk_bf16_f32 v150, v76, v77\n\t" \
  "v_cvt_pk_bf16_f32 v151, v78, v79\n\t" \
  "v_mfma_f32_16x16x32_bf16 a[44:47], a[144:147], v[152:155], a[44:47]\n\t" \
  "buffer_load_dwordx4 v240, %[wo], s97 offen offset:0 lds\n\t" \
  "v_mfma_f32_16x16x32_bf16 a[44:47], a[148:151], v[156:159], a[44:47]\n\t" \
  "s_add_u32 s97, s97, 7168\n\t" \
  "v_mfma_f32_16x16x32_bf16 a[60:63], a[152:155], v[152:155], a[60:63]\n\t" \
  "v_mfma_f32_16x16x32_bf16 a[60:63], a[156:159], v[156:159], a[60:63]\n\t" \
  "v_mfma_f32_16x16x32_bf16 a[76:79], a[160:163], v[152:155], a[76:79]\n\t" \
  "v_mfma_f32_16x16x32_bf16 a[76:79], a[164:167], v[156:159], a[76:79]\n\t" \
  "buffer_load_dwordx4 v241, %[wo], s97 offen offset:1024 lds\n\t" \
  "v_mfma_f32_16x16x32_bf16 a[92:95], a[168:171], v[152:155], a[92:95]\n\t" \
  "s_add_u32 s97, s97, 7168\n\t" \
  "v_mfma_f32_16x16x32_bf16 a[92:95], a[172:175], v[156:159], a[92:95]\n\t" \
  "v_mfma_f32_16x16x32_bf16 a[108:111], a[176:179], v[152:155], a[108:111]\n\t" \
  "v_mfma_f32_16x16x32_bf16 a[108:111], a[180:183], v[156:159], a[108:111]\n\t" \
  "buffer_load_dwordx4 v240, %[wo], s97 offen offset:2048 lds\n\t" \
  "v_mfma_f32_16x16x32_bf16 a[124:127], a[184:187], v[152:155], a[124:127]\n\t" \
  "s_add_u32 s97, s97, 7168\n\t" \
  "v_mfma_f32_16x16x32_bf16 a[124:127], a[188:191], v[156:159], a[124:127]\n\t" \
  "v_mfma_f32_16x16x32_bf16 v[172:175], v[216:219], v[152:155], v[172:175]\n\t" \
  "v_mfma_f32_16x16x32_bf16 v[172:175], v[216:219], v[156:159], v[172:175]\n\t" \
  "buffer_load_dwordx4 v241, %[wo], s97 offen offset:3072 lds\n\t" \
  "s_waitcnt lgkmcnt(0)\n\t" \
  "s_barrier\n\t" \
  STA("s_memrealtime s[94:95]\n\t") \
  STA("s_memtime s[92:93]\n\t") \
  "v_mfma_f32_16x16x32_bf16 v[0:3], a[192:195], v[16:19], v[176:179]\n\t" \
  "ds_read_b64_tr_b16 a[128:129], v228 offset:0\n\t" \
  "v_cvt_pk_bf16_f32 v152, v96, v97\n\t" \
  "v_mfma_f32_16x16x32_bf16 v[0:3], a[196:199], v[20:23], v[0:3]\n\t" \
  "ds_read_b64_tr_b16 a[130:131], v228 offset:4096\n\t" \
  "v_cvt_pk_bf16_f32 v153, v98, v99\n\t" \
  "v_mfma_f32_16x16x32_bf16 v[0:3], a[200:203], v[24:27], v[0:3]\n\t" \
  "ds_read_b64_tr_b16 a[132:133], v228 offset:8192\n\t" \
  "v_cvt_pk_bf16_f32 v154, v100, v101\n\t" \
  "v_mfma_f32_16x16x32_bf16 v[0:3], a[204:207], v[28:31], v[0:3]\n\t" \
  "ds_read_b64_tr_b16 a[134:135], v228 offset:12288\n\t" \
  "v_cvt_pk_bf16_f32 v155, v102, v103\n\t" \
  "v_mfma_f32_16x16x32_bf16 v[4:7], a[220:223], v[28:31], v[176:179]\n\t" \
  "ds_read_b64_tr_b16 a[136:137], v229 offset:0\n\t" \
  "v_cvt_pk_bf16_f32 v156, v104, v105\n\t" \
  "v_mfma_f32_16x16x32_bf16 v[4:7], a[216:219], v[24:27], v[4:7]\n\t" \
  "ds_read_b64_tr_b16 a[138:139], v229 offset:4096\n\t" \
  "v_cvt_pk_bf16_f32 v157, v106, v107\n\t" \
  "v_mfma_f32_16x16x32_bf16 v[4:7], a[212:215], v[20:23], v[4:7]\n\t" \
  "v_exp_f32 v0, v0\n\t" \
  "v_mfma_f32_16x16x32_bf16 v[4:7], a[208:211], v[16:19], v[4:7]\n\t" \
  "v_exp_f32 v1, v1\n\t" \
  "v_mfma_f32_16x16x32_bf16 v[8:11], a[224:227], v[16:19], v[176:179]\n\t" \
  "v_exp_f32 v2, v2\n\t" \
  "v_mfma_f32_16x16x32_bf16 v[8:11], a[228:231], v[20:23], v[8:11]\n\t" \
  "v_exp_f32 v3, v3\n\t" \
  "v_mfma_f32_16x16x32_bf16 v[8:11], a[232:235], v[24:27], v[8:11]\n\t" \
  "ds_read_b64_tr_b16 a[140:141], v229 offset:8192\n\t" \
  "v_cvt_pk_bf16_f32 v158, v108, v109\n\t" \
  "v_mfma_f32_16x16x32_bf16 v[8:11], a[236:239], v[28:31], v[8:11]\n\t" \
  "v_exp_f32 v4, v4\n\t" \
  "v_mfma_f32_16x16x32_bf16 v[12:15], a[252:255], v[28:31], v[176:179]\n\t" \
  STA("v_writelane_b32 %[stv], s94, 50\n\t") \
  STA("v_writelane_b32 %[stv], s92, 51\n\t") \
  "v_exp_f32 v5, v5\n\t" \
  "v_mfma_f32_16x16x32_bf16 v[12:15], a[248:251], v[24:27], v[12:15]\n\t" \
  "ds_read_b64_tr_b16 a[142:143], v229 offset:12288\n\t" \
  "v_cvt_pk_bf16_f32 v159, v110, v111\n\t" \
  "v_mfma_f32_16x16x32_bf16 v[12:15], a[244:247], v[20:23], v[12:15]\n\t" \
  "v_exp_f32 v6, v6\n\t" \
  "v_mfma_f32_16x16x32_bf16 v[12:15], a[240:243], v[16:19], v[12:15]\n\t" \
  "v_exp_f32 v7, v7\n\t" \
  "v_mfma_f32_16x16x32_bf16 v[32:35], a[192:195], v[48:51], v[180:183]\n\t" \
  "ds_read_b64_tr_b16 a[144:145], v228 offset:256\n\t" \
  "ds_read_b64_tr_b16 a[146:147], v228 offset:4352\n\t" \
  "v_mfma_f32_16x16x32_bf16 v[32:35], a[196:199], v[52:55], v[32:35]\n\t" \
  "v_exp_f32 v8, v8\n\t" \
  "v_mfma_f32_16x16x32_bf16 v[32:35], a[200:203], v[56:59], v[32:35]\n\t" \
  "v_exp_f32 v9, v9\n\t" \
  "v_mfma_f32_16x16x32_bf16 v[32:35], a[204:207], v[60:63], v[32:35]\n\t" \
  "v_exp_f32 v10, v10\n\t" \
  "v_mfma_f32_16x16x32_bf16 v[36:39], a[220:223], v[60:63], v[180:183]\n\t" \
  "s_add_u32 m0, %[m], 65536\n\t" \
  "ds_read_b64_tr_b16 a[148:149], v228 offset:8448\n\t" \
  "ds_read_b64_tr_b16 a[150:151], v228 offset:12544\n\t" \
  "v_mfma_f32_16x16x32_bf16 v[36:39], a[216:219], v[56:59], v[36:39]\n\t" \
  "s_xor_b32 s97, %[wob], 393216\n\t" \
  "v_exp_f32 v11, v11\n\t" \
  "v_mfma_f32_16x16x32_bf16 v[36:39], a[212:215], v[52:55], v[36:39]\n\t" \
  "v_exp_f32 v12, v12\n\t" \
  "v_mfma_f32_16x16x32_bf16 v[36:39], a[208:211], v[48:51], v[36:39]\n\t" \
  "ds_read_b64_tr_b16 a[152:153], v229 offset:256\n\t" \
  "ds_read_b64_tr_b16 a[154:155], v229 offset:4352\n\t" \
  "v_mfma_f32_16x16x32_bf16 v[40:43], a[224:227], v[48:51], v[180:183]\n\t" \
  "v_exp_f32 v13, v13\n\t" \
  "v_mfma_f32_16x16x32_bf16 v[40:43], a[228:231], v[52:55], v[40:43]\n\t" \
  "v_exp_f32 v14, v14\n\t" \
  "v_mfma_f32_16x16x32_bf16 v[40:43], a[232:235], v[56:59], v[40:43]\n\t" \
  "v_exp_f32 v15, v15\n\t" \
  "v_mfma_f32_16x16x32_bf16 v[40:43], a[236:239], v[60:63], v[40:43]\n\t" \
  "ds_read_b64_tr_b16 a[156:157], v229 offset:8448\n\t" \
  "ds_read_b64_tr_b16 a[158:159], v229 offset:12544\n\t" \
  "v_mfma_f32_16x16x32_bf16 v[44:47], a[252:255], v[60:63], v[180:183]\n\t" \
  "v_exp_f32 v32, v32\n\t" \
  "v_mfma_f32_16x16x32_bf16 v[44:47], a[248:251], v[56:59], v[44:47]\n\t" \
  "v_exp_f32 v33, v33\n\t" \
  "v_mfma_f32_16x16x32_bf16 v[44:47], a[244:247], v[52:55], v[44:47]\n\t" \
  "buffer_load_dwordx4 v240, %[wo], s97 offen offset:0 lds\n\t" \
  "v_mfma_f32_16x16x32_bf16 v[44:47], a[240:243], v[48:51], v[44:47]\n\t" \
  "s_add_u32 s97, s97, 7168\n\t" \
  "ds_read_b64_tr_b16 a[160:161], v228 offset:512\n\t" \
  "ds_read_b64_tr_b16 a[162:163], v228 offset:4608\n\t" \
  "v_mfma_f32_16x16x32_bf16 v[64:67], a[192:195], v[80:83], v[184:187]\n\t" \
  "v_exp_f32 v34, v34\n\t" \
  "v_mfma_f32_16x16x32_bf16 v[64:67], a[196:199], v[84:87], v[64:67]\n\t" \
  "v_exp_f32 v35, v35\n\t" \
  "v_mfma_f32_16x16x32_bf16 v[64:67], a[200:203], v[88:91], v[64:67]\n\t" \
  "ds_read_b64_tr_b16 a[164:165], v228 offset:8704\n\t" \
  "ds_read_b64_tr_b16 a[166:167], v228 offset:12800\n\t" \
  "v_mfma_f32_16x16x32_bf16 v[64:67], a[204:207], v[92:95], v[64:67]\n\t" \
  "buffer_load_dwordx4 v241, %[wo], s97 offen offset:1024 lds\n\t" \
  "v_mfma_f32_16x16x32_bf16 v[68:71], a[220:223], v[92:95], v[184:187]\n\t" \
  "s_add_u32 s97, s97, 7168\n\t" \
  "v_exp_f32 v36, v36\n\t" \
  "v_mfma_f32_16x16x32_bf16 v[68:71], a[216:219], v[88:91], v[68:71]\n\t" \
  "ds_read_b64_tr_b16 a[168:169], v229 offset:512\n\t" \
  "ds_read_b64_tr_b16 a[170:171], v229 offset:4608\n\t" \
  "v_mfma_f32_16x16x32_bf16 v[68:71], a[212:215], v[84:87], v[68:71]\n\t" \
  "v_exp_f32 v37, v37\n\t" \
  "v_mfma_f32_16x16x32_bf16 v[68:71], a[208:211], v[80:83], v[68:71]\n\t" \
  "v_exp_f32 v38, v38\n\t" \
  "v_mfma_f32_16x16x32_bf16 v[72:75], a[224:227], v[80:83], v[184:187]\n\t" \
  "buffer_load_dwordx4 v240, %[wo], s97 offen offset:2048 lds\n\t" \
  "v_mfma_f32_16x16x32_bf16 v[72:75], a[228:231], v[84:87], v[72:75]\n\t" \
  "s_add_u32 s97, s97, 7168\n\t" \
  "ds_read_b64_tr_b16 a[172:173], v229 offset:8704\n\t" \
  "ds_read_b64_tr_b16 a[174:175], v229 offset:12800\n\t" \
  "v_mfma_f32_16x16x32_bf16 v[72:75], a[232:235], v[88:91], v[72:75]\n\t" \
  "v_exp_f32 v39, v39\n\t" \
  "v_mfma_f32_16x16x32_bf16 v[72:75], a[236:239], v[92:95], v[72:75]\n\t" \
  "v_exp_f32 v40, v40\n\t" \
  "v_mfma_f32_16x16x32_bf16 v[76:79], a[252:255], v[92:95], v[184:187]\n\t" \
  "ds_read_b64_tr_b16 a[176:177], v228 offset:768\n\t" \
  "ds_read_b64_tr_b16 a[178:179], v228 offset:4864\n\t" \
  "v_mfma_f32_16x16x32_bf16 v[76:79], a[248:251], v[88:91], v[76:79]\n\t" \
  "buffer_load_dwordx4 v241, %[wo], s97 offen offset:3072 lds\n\t" \
  "v_mfma_f32_16x16x32_bf16 v[76:79], a[244:247], v[84:87], v[76:79]\n\t" \
  "v_exp_f32 v41, v41\n\t" \
  "v_mfma_f32_16x16x32_bf16 v[76:79], a[240:243], v[80:83], v[76:79]\n\t" \
  "v_exp_f32 v42, v42\n\t" \
  "v_mfma_f32_16x16x32_bf16 v[96:99], a[192:195], v[112:115], v[188:191]\n\t" \
  "ds_read_b64_tr_b16 a[180:181], v228 offset:8960\n\t" \
  "ds_read_b64_tr_b16 a[182:183], v228 offset:13056\n\t" \
  "v_mfma_f32_16x16x32_bf16 v[96:99], a[196:199], v[116:119], v[96:99]\n\t" \
  "v_exp_f32 v43, v43\n\t" \
  "v_mfma_f32_16x16x32_bf16 v[96:99], a[200:203], v[120:123], v[96:99]\n\t" \
  "v_exp_f32 v44, v44\n\t" \
  "v_mfma_f32_16x16x32_bf16 v[96:99], a[204:207], v[124:127], v[96:99]\n\t" \
  "ds_read_b64_tr_b16 a[184:185], v229 offset:768\n\t" \
  "ds_read_b64_tr_b16 a[186:187], v229 offset:4864\n\t" \
  "v_mfma_f32_16x16x32_bf16 v[100:103], a[220:223], v[124:127], v[188:191]\n\t" \
  "v_exp_f32 v45, v45\n\t" \
  "v_mfma_f32_16x16x32_bf16 v[100:103], a[216:219], v[120:123], v[100:103]\n\t" \
  "v_exp_f32 v46, v46\n\t" \
  "v_mfma_f32_16x16x32_bf16 v[100:103], a[212:215], v[116:119], v[100:103]\n\t" \
  "v_exp_f32 v47, v47\n\t" \
  "v_mfma_f32_16x16x32_bf16 v[100:103], a[208:211], v[112:115], v[100:103]\n\t" \
  "ds_read_b64_tr_b16 a[188:189], v229 offset:8960\n\t" \
  "ds_read_b64_tr_b16 a[190:191], v229 offset:13056\n\t" \
  "v_mfma_f32_16x16x32_bf16 v[104:107], a[224:227], v[112:115], v[188:191]\n\t" \
  "v_exp_f32 v64, v64\n\t" \
  "v_mfma_f32_16x16x32_bf16 v[104:107], a[228:231], v[116:119], v[104:107]\n\t" \
  "v_exp_f32 v65, v65\n\t" \
  "v_mfma_f32_16x16x32_bf16 v[104:107], a[232:235], v[120:123], v[104:107]\n\t" \
  "v_exp_f32 v66, v66\n\t" \
  "v_mfma_f32_16x16x32_bf16 v[104:107], a[236:239], v[124:127], v[104:107]\n\t" \
  "v_exp_f32 v67, v67\n\t" \
  "v_mfma_f32_16x16x32_bf16 v[108:111], a[252:255], v[124:127], v[188:191]\n\t" \
  "v_exp_f32 v68, v68\n\t" \
  "v_mfma_f32_16x16x32_bf16 v[108:111], a[248:251], v[120:123], v[108:111]\n\t" \
  "v_exp_f32 v69, v69\n\t" \
  "v_mfma_f32_16x16x32_bf16 v[108:111], a[244:247], v[116:119], v[108:111]\n\t" \
  "v_exp_f32 v70, v70\n\t" \
  "v_mfma_f32_16x16x32_bf16 v[108:111], a[240:243], v[112:115], v[108:111]\n\t" \
  "s_waitcnt vmcnt(4) lgkmcnt(0)\n\t" \
  "s_barrier\n\t" \
  STA("s_memrealtime s[94:95]\n\t") \
  STA("s_memtime s[92:93]\n\t") \
  "v_mfma_f32_16x16x32_bf16 a[0:3], a[128:131], v[128:131], a[0:3]\n\t" \
  "s_add_u32 m0, %[m], 81920\n\t" \
  "v_exp_f32 v71, v71\n\t" \
  "v_mfma_f32_16x16x32_bf16 a[0:3], a[132:135], v[132:135], a[0:3]\n\t" \
  "s_xor_b32 s97, %[wob], 524288\n\t" \
  "v_exp_f32 v72, v72\n\t" \
  "v_mfma_f32_16x16x32_bf16 a[16:19], a[136:139], v[128:131], a[16:19]\n\t" \
  "ds_read_b64_tr_b16 a[192:193], v237 offset:0\n\t" \
  "ds_read_b64_tr_b16 a[194:195], v237 offset:4096\n\t" \
  "v_mfma_f32_16x16x32_bf16 a[16:19], a[140:143], v[132:135], a[16:19]\n\t" \
  "v_exp_f32 v73, v73\n\t" \
  ".Lhs9:\n\ts_cbranch_execz .Lhs9n\n\t" \
  "v_mfma_f32_16x16x32_bf16 a[32:35], a[144:147], v[128:131], a[32:35]\n\t" \
  "ds_read_b128 v[176:179], v215 offset:16384\n\t" \
  "ds_read_b64_tr_b16 a[196:197], v237 offset:8192\n\t" \
  "v_mfma_f32_16x16x32_bf16 a[32:35], a[148:151], v[132:135], a[32:35]\n\t" \
  "v_exp_f32 v74, v74\n\t" \
  "v_mfma_f32_16x16x32_bf16 a[48:51], a[152:155], v[128:131], a[48:51]\n\t" \
  "buffer_load_dwordx4 v240, %[wo], s97 offen offset:0 lds\n\t" \
  "v_mfma_f32_16x16x32_bf16 a[48:51], a[156:159], v[132:135], a[48:51]\n\t" \
  "s_add_u32 s97, s97, 7168\n\t" \
  "ds_read_b128 v[180:183], v215 offset:17408\n\t" \
  "ds_read_b64_tr_b16 a[198:199], v237 offset:12288\n\t" \
  "v_mfma_f32_16x16x32_bf16 a[64:67], a[160:163], v[128:131], a[64:67]\n\t" \
  "buffer_load_dwordx4 v241, %[wo], s97 offen offset:1024 lds\n\t" \
  "v_mfma_f32_16x16x32_bf16 a[64:67], a[164:167], v[132:135], a[64:67]\n\t" \
  "s_add_u32 s97, s97, 7168\n\t" \
  "ds_read_b64_tr_b16 a[200:201], v238 offset:0\n\t" \
  "ds_read_b128 v[184:187], v215 offset:18432\n\t" \
  "v_mfma_f32_16x16x32_bf16 a[80:83], a[168:171], v[128:131], a[80:83]\n\t" \
  "ds_read_b64_tr_b16 a[202:203], v238 offset:4096\n\t" \
  "ds_read_b128 v[188:191], v215 offset:19456\n\t" \
  "v_mfma_f32_16x16x32_bf16 a[80:83], a[172:175], v[132:135], a[80:83]\n\t" \
  "ds_read_b64_tr_b16 a[204:205], v238 offset:8192\n\t" \
  "ds_read_b128 v[192:195], v215 offset:32768\n\t" \
  "v_mfma_f32_16x16x32_bf16 a[96:99], a[176:179], v[128:131], a[96:99]\n\t" \
  STA("v_writelane_b32 %[stv], s94, 52\n\t") \
  STA("v_writelane_b32 %[stv], s92, 53\n\t") \
  "buffer_load_dwordx4 v240, %[wo], s97 offen offset:2048 lds\n\t" \
  "v_mfma_f32_16x16x32_bf16 a[96:99], a[180:183], v[132:135], a[96:99]\n\t" \
  "s_add_u32 s97, s97, 7168\n\t" \
  "ds_read_b64_tr_b16 a[206:207], v238 offset:12288\n\t" \
  "ds_read_b128 v[196:199], v215 offset:33792\n\t" \
  "v_mfma_f32_16x16x32_bf16 a[112:115], a[184:187], v[128:131], a[112:115]\n\t" \
  "ds_read_b64_tr_b16 a[208:209], v237 offset:256\n\t" \
  "ds_read_b128 v[200:203], v215 offset:34816\n\t" \
  "v_mfma_f32_16x16x32_bf16 a[112:115], a[188:191], v[132:135], a[112:115]\n\t" \
  "ds_read_b64_tr_b16 a[210:211], v237 offset:4352\n\t" \
  "ds_read_b64_tr_b16 a[212:213], v237 offset:8448\n\t" \
  "v_mfma_f32_16x16x32_bf16 v[160:163], v[216:219], v[128:131], v[160:163]\n\t" \
  "buffer_load_dwordx4 v241, %[wo], s97 offen offset:3072 lds\n\t" \
  "v_mfma_f32_16x16x32_bf16 v[160:163], v[216:219], v[132:135], v[160:163]\n\t" \
  "ds_read_b128 v[204:207], v215 offset:35840\n\t" \
  "v_cvt_pk_bf16_f32 v128, v0, v1\n\t" \
  "v_mfma_f32_16x16x32_bf16 a[4:7], a[128:131], v[136:139], a[4:7]\n\t" \
  "ds_read_b64_tr_b16 a[214:215], v237 offset:12544\n\t" \
  "v_cvt_pk_bf16_f32 v129, v2, v3\n\t" \
  "v_mfma_f32_16x16x32_bf16 a[4:7], a[132:135], v[140:143], a[4:7]\n\t" \
  "ds_read_b64_tr_b16 a[216:217], v238 offset:256\n\t" \
  "v_cvt_pk_bf16_f32 v130, v4, v5\n\t" \
  "v_mfma_f32_16x16x32_bf16 a[20:23], a[136:139], v[136:139], a[20:23]\n\t" \
  "s_add_u32 m0, %[m], 114688\n\t" \
  "ds_read_b64_tr_b16 a[218:219], v238 offset:4352\n\t" \
  "v_cvt_pk_bf16_f32 v131, v6, v7\n\t" \
  "v_mfma_f32_16x16x32_bf16 a[20:23], a[140:143], v[140:143], a[20:23]\n\t" \
  "s_xor_b32 s97, %[wob], 786432\n\t" \
  "ds_read_b64_tr_b16 a[220:221], v238 offset:8448\n\t" \
  "v_cvt_pk_bf16_f32 v132, v8, v9\n\t" \
  "v_mfma_f32_16x16x32_bf16 a[36:39], a[144:147], v[136:139], a[36:39]\n\t" \
  "s_waitcnt lgkmcnt(13)\n\t" \
  "ds_read_b64_tr_b16 a[222:223], v238 offset:12544\n\t" \
  "v_cvt_pk_bf16_f32 v133, v10, v11\n\t" \
  "v_mfma_f32_16x16x32_bf16 a[36:39], a[148:151], v[140:143], a[36:39]\n\t" \
  "ds_read_b64_tr_b16 a[224:225], v237 offset:512\n\t" \
  "v_cvt_pk_bf16_f32 v134, v12, v13\n\t" \
  "v_mfma_f32_16x16x32_bf16 a[52:55], a[152:155], v[136:139], a[52:55]\n\t" \
  "ds_read_b64_tr_b16 a[226:227], v237 offset:4608\n\t" \
  "v_cvt_pk_bf16_f32 v135, v14, v15\n\t" \
  "v_mfma_f32_16x16x32_bf16 a[52:55], a[156:159], v[140:143], a[52:55]\n\t" \
  "ds_read_b64_tr_b16 a[228:229], v237 offset:8704\n\t" \
  "ds_read_b64_tr_b16 a[230:231], v237 offset:12800\n\t" \
  "v_mfma_f32_16x16x32_bf16 a[68:71], a[160:163], v[136:139], a[68:71]\n\t" \
  "v_exp_f32 v75, v75\n\t" \
  "v_mfma_f32_16x16x32_bf16 a[68:71], a[164:167], v[140:143], a[68:71]\n\t" \
  "ds_read_b64_tr_b16 a[232:233], v238 offset:512\n\t" \
  "ds_read_b64_tr_b16 a[234:235], v238 offset:4608\n\t" \
  "v_mfma_f32_16x16x32_bf16 a[84:87], a[168:171], v[136:139], a[84:87]\n\t" \
  "buffer_load_dwordx4 v240, %[wo], s97 offen offset:0 lds\n\t" \
  "v_mfma_f32_16x16x32_bf16 a[84:87], a[172:175], v[140:143], a[84:87]\n\t" \
  "s_add_u32 s97, s97, 7168\n\t" \
  "v_exp_f32 v76, v76\n\t" \
  "v_mfma_f32_16x16x32_bf16 a[100:103], a[176:179], v[136:139], a[100:103]\n\t" \
  "ds_read_b64_tr_b16 a[236:237], v238 offset:8704\n\t" \
  "ds_read_b64_tr_b16 a[238:239], v238 offset:12800\n\t" \
  "v_mfma_f32_16x16x32_bf16 a[100:103], a[180:183], v[140:143], a[100:103]\n\t" \
  "buffer_load_dwordx4 v241, %[wo], s97 offen offset:1024 lds\n\t" \
  "v_mfma_f32_16x16x32_bf16 a[116:119], a[184:187], v[136:139], a[116:119]\n\t" \
  "s_add_u32 s97, s97, 7168\n\t" \
  "ds_read_b64_tr_b16 a[240:241], v237 offset:768\n\t" \
  "ds_read_b64_tr_b16 a[242:243], v237 offset:4864\n\t" \
  "v_mfma_f32_16x16x32_bf16 a[116:119], a[188:191], v[140:143], a[116:119]\n\t" \
  "v_exp_f32 v77, v77\n\t" \
  "v_mfma_f32_16x16x32_bf16 v[164:167], v[216:219], v[136:139], v[164:167]\n\t" \
  "buffer_load_dwordx4 v240, %[wo], s97 offen offset:2048 lds\n\t" \
  "v_mfma_f32_16x16x32_bf16 v[164:167], v[216:219], v[140:143], v[164:167]\n\t" \
  "s_add_u32 s97, s97, 7168\n\t" \
  "ds_read_b64_tr_b16 a[244:245], v237 offset:8960\n\t" \
  "v_cvt_pk_bf16_f32 v136, v32, v33\n\t" \
  "v_mfma_f32_16x16x32_bf16 a[0:3], a[192:195], v[128:131], a[0:3]\n\t" \
  "ds_read_b64_tr_b16 a[246:247], v237 offset:13056\n\t" \
  "v_cvt_pk_bf16_f32 v137, v34, v35\n\t" \
  "v_mfma_f32_16x16x32_bf16 a[0:3], a[196:199], v[132:135], a[0:3]\n\t" \
  "ds_read_b64_tr_b16 a[248:249], v238 offset:768\n\t" \
  "v_cvt_pk_bf16_f32 v138, v36, v37\n\t" \
  "v_mfma_f32_16x16x32_bf16 v[160:163], v[216:219], v[128:131], v[160:163]\n\t" \
  "buffer_load_dwordx4 v241, %[wo], s97 offen offset:3072 lds\n\t" \
  "v_mfma_f32_16x16x32_bf16 v[160:163], v[216:219], v[132:135], v[160:163]\n\t" \
  "s_add_u32 m0, %[m], 131072\n\t" \
  "ds_read_b64_tr_b16 a[250:251], v238 offset:4864\n\t" \
  "v_cvt_pk_bf16_f32 v139, v38, v39\n\t" \
  "v_mfma_f32_16x16x32_bf16 a[16:19], a[200:203], v[128:131], a[16:19]\n\t" \
  "s_waitcnt lgkmcnt(15)\n\t" \
  "ds_read_b64_tr_b16 a[252:253], v238 offset:8960\n\t" \
  "v_cvt_pk_bf16_f32 v140, v40, v41\n\t" \
  "v_mfma_f32_16x16x32_bf16 a[16:19], a[204:207], v[132:135], a[16:19]\n\t" \
  "s_xor_b32 s97, %[wob], 917504\n\t" \
  "ds_read_b64_tr_b16 a[254:255], v238 offset:13056\n\t" \
  "v_cvt_pk_bf16_f32 v141, v42, v43\n\t" \
  "v_xor_b32 v235, 64, v234\n\t" \
  "v_mfma_f32_16x16x32_bf16 a[32:35], a[208:211], v[128:131], a[32:35]\n\t" \
  "v_exp_f32 v78, v78\n\t" \
  "v_cvt_pk_bf16_f32 v142, v44, v45\n\t" \
  "v_mfma_f32_16x16x32_bf16 a[32:35], a[212:215], v[132:135], a[32:35]\n\t" \
  "ds_read_b64_tr_b16 v[48:49], v235 offset:0\n\t" \
  "v_cvt_pk_bf16_f32 v143, v46, v47\n\t" \
  "v_mfma_f32_16x16x32_bf16 a[48:51], a[216:219], v[128:131], a[48:51]\n\t" \
  "s_waitcnt lgkmcnt(15)\n\t" \
  "v_exp_f32 v79, v79\n\t" \
  "v_mfma_f32_16x16x32_bf16 a[48:51], a[220:223], v[132:135], a[48:51]\n\t" \
  "ds_read_b64_tr_b16 v[50:51], v235 offset:4096\n\t" \
  "ds_read_b64_tr_b16 v[52:53], v235 offset:8192\n\t" \
  "v_mfma_f32_16x16x32_bf16 a[64:67], a[224:227], v[128:131], a[64:67]\n\t" \
  "s_waitcnt lgkmcnt(15)\n\t" \
  "buffer_load_dwordx4 v240, %[wo], s97 offen offset:0 lds\n\t" \
  "v_mfma_f32_16x16x32_bf16 a[64:67], a[228:231], v[132:135], a[64:67]\n\t" \
  "s_waitcnt lgkmcnt(13)\n\t" \
  "v_rcp_f32 v208, v160\n\t" \
  "v_mfma_f32_16x16x32_bf16 a[80:83], a[232:235], v[128:131], a[80:83]\n\t" \
  "s_waitcnt lgkmcnt(11)\n\t" \
  "v_cmp_lt_f32 vcc, 0x5f800000, v160\n\t" \
  "s_cbranch_vccnz .Lat_x0\n\t" \
  ".Lat_y0:\n\t" \
  "v_accvgpr_read_b32 v16, a0\n\t" \
  "v_mfma_f32_16x16x32_bf16 a[80:83], a[236:239], v[132:135], a[80:83]\n\t" \
  "s_waitcnt lgkmcnt(9)\n\t" \
  "ds_read_b64_tr_b16 v[54:55], v235 offset:12288\n\t" \
  "v_accvgpr_read_b32 v17, a1\n\t" \
  "v_accvgpr_read_b32 v18, a2\n\t" \
  "v_accvgpr_read_b32 v19, a3\n\t" \
  "v_accvgpr_read_b32 v20, a16\n\t" \
  "v_mfma_f32_16x16x32_bf16 a[96:99], a[240:243], v[128:131], a[96:99]\n\t" \
  "s_waitcnt lgkmcnt(8)\n\t" \
  "ds_read_b64_tr_b16 v[56:57], v235 offset:16384\n\t" \
  "v_accvgpr_read_b32 v21, a17\n\t" \
  "v_accvgpr_read_b32 v22, a18\n\t" \
  "v_accvgpr_read_b32 v23, a19\n\t" \
  "v_mul_f32 v16, v16, v208\n\t" \
  "v_mfma_f32_16x16x32_bf16 a[96:99], a[244:247], v[132:135], a[96:99]\n\t" \
  "s_waitcnt lgkmcnt(7)\n\t" \
  "ds_read_b64_tr_b16 v[58:59], v235 offset:20480\n\t" \
  "v_mul_f32 v17, v17, v208\n\t" \
  "v_mul_f32 v18, v18, v208\n\t" \
  "v_mul_f32 v19, v19, v208\n\t" \
  "v_mul_f32 v20, v20, v208\n\t" \
  "v_mfma_f32_16x16x32_bf16 a[112:115], a[248:251], v[128:131], a[112:115]\n\t" \
  "s_waitcnt lgkmcnt(6)\n\t" \
  "ds_read_b64_tr_b16 v[60:61], v235 offset:24576\n\t" \
  "v_mul_f32 v21, v21, v208\n\t" \
  "v_mul_f32 v22, v22, v208\n\t" \
  "v_mul_f32 v23, v23, v208\n\t" \
  "v_cvt_pk_bf16_f32 v0, v16, v17\n\t" \
  "v_mfma_f32_16x16x32_bf16 a[112:115], a[252:255], v[132:135], a[112:115]\n\t" \
  "s_waitcnt vmcnt(9)\n\t" \
  "s_barrier\n\t" \
  "v_mfma_f32_16x16x32_bf16 a[8:11], a[128:131], v[144:147], a[8:11]\n\t" \
  "s_add_u32 s97, s97, 7168\n\t" \
  "ds_read_b64_tr_b16 v[62:63], v235 offset:28672\n\t" \
  "v_cvt_pk_bf16_f32 v1, v18, v19\n\t" \
  "v_cvt_pk_bf16_f32 v2, v20, v21\n\t" \
  "v_cvt_pk_bf16_f32 v3, v22, v23\n\t" \
  "v_accvgpr_read_b32 v24, a32\n\t" \
  "v_mfma_f32_16x16x32_bf16 a[8:11], a[132:135], v[148:151], a[8:11]\n\t" \
  "buffer_load_dwordx4 v241, %[wo], s97 offen offset:1024 lds\n\t" \
  "v_accvgpr_read_b32 v25, a33\n\t" \
  "v_accvgpr_read_b32 v26, a34\n\t" \
  "v_accvgpr_read_b32 v27, a35\n\t" \
  "v_mfma_f32_16x16x32_bf16 a[24:27], a[136:139], v[144:147], a[24:27]\n\t" \
  "s_add_u32 s97, s97, 7168\n\t" \
  "v_accvgpr_read_b32 v28, a48\n\t" \
  "v_accvgpr_read_b32 v29, a49\n\t" \
  "v_accvgpr_read_b32 v30, a50\n\t" \
  "v_accvgpr_read_b32 v31, a51\n\t" \
  "v_mul_f32 v24, v24, v208\n\t" \
  "v_mfma_f32_16x16x32_bf16 a[24:27], a[140:143], v[148:151], a[24:27]\n\t" \
  "buffer_load_dwordx4 v240, %[wo], s97 offen offset:2048 lds\n\t" \
  "v_mul_f32 v25, v25, v208\n\t" \
  "v_mul_f32 v26, v26, v208\n\t" \
  "v_mul_f32 v27, v27, v208\n\t" \
  "v_mfma_f32_16x16x32_bf16 a[40:43], a[144:147], v[144:147], a[40:43]\n\t" \
  "s_add_u32 s97, s97, 7168\n\t" \
  "v_mul_f32 v28, v28, v208\n\t" \
  "v_mul_f32 v29, v29, v208\n\t" \
  "v_mul_f32 v30, v30, v208\n\t" \
  "v_mul_f32 v31, v31, v208\n\t" \
  "v_cvt_pk_bf16_f32 v4, v24, v25\n\t" \
  "v_mfma_f32_16x16x32_bf16 a[40:43], a[148:151], v[148:151], a[40:43]\n\t" \
  "s_xor_b32 s98, %[eso], 0\n\t" \
  "buffer_store_dwordx4 v[0:3], v242, %[ws], s98 offen offset:0 sc1\n\t" \
  "v_cvt_pk_bf16_f32 v5, v26, v27\n\t" \
  "v_cvt_pk_bf16_f32 v6, v28, v29\n\t" \
  "v_mfma_f32_16x16x32_bf16 a[56:59], a[152:155], v[144:147], a[56:59]\n\t" \
  "v_cvt_pk_bf16_f32 v7, v30, v31\n\t" \
  "v_accvgpr_read_b32 v16, a64\n\t" \
  "v_accvgpr_read_b32 v17, a65\n\t" \
  "v_accvgpr_read_b32 v18, a66\n\t" \
  "v_accvgpr_read_b32 v19, a67\n\t" \
  "v_mfma_f32_16x16x32_bf16 a[56:59], a[156:159], v[148:151], a[56:59]\n\t" \
  "buffer_load_dwordx4 v241, %[wo], s97 offen offset:3072 lds\n\t" \
  "v_accvgpr_read_b32 v20, a80\n\t" \
  "v_accvgpr_read_b32 v21, a81\n\t" \
  "v_accvgpr_read_b32 v22, a82\n\t" \
  "v_mfma_f32_16x16x32_bf16 a[72:75], a[160:163], v[144:147], a[72:75]\n\t" \
  "v_accvgpr_read_b32 v23, a83\n\t" \
  "v_mul_f32 v16, v16, v208\n\t" \
  "v_mul_f32 v17, v17, v208\n\t" \
  "v_mul_f32 v18, v18, v208\n\t" \
  "v_mul_f32 v19, v19, v208\n\t" \
  "v_mfma_f32_16x16x32_bf16 a[72:75], a[164:167], v[148:151], a[72:75]\n\t" \
  "s_xor_b32 s98, %[eso], 0\n\t" \
  "v_mul_f32 v20, v20, v208\n\t" \
  "v_mul_f32 v21, v21, v208\n\t" \
  "v_mul_f32 v22, v22, v208\n\t" \
  "v_mul_f32 v23, v23, v208\n\t" \
  "v_mfma_f32_16x16x32_bf16 a[88:91], a[168:171], v[144:147], a[88:91]\n\t" \
  "buffer_store_dwordx4 v[4:7], v242, %[ws], s98 offen offset:1024 sc1\n\t" \
  "v_xor_b32 v236, 96, v234\n\t" \
  "v_mfma_f32_16x16x32_bf16 a[88:91], a[172:175], v[148:151], a[88:91]\n\t" \
  "ds_read_b64_tr_b16 v[80:81], v236 offset:0\n\t" \
  "v_cvt_pk_bf16_f32 v8, v16, v17\n\t" \
  "v_cvt_pk_bf16_f32 v9, v18, v19\n\t" \
  "v_cvt_pk_bf16_f32 v10, v20, v21\n\t" \
  "v_cvt_pk_bf16_f32 v11, v22, v23\n\t" \
  "v_mfma_f32_16x16x32_bf16 a[104:107], a[176:179], v[144:147], a[104:107]\n\t" \
  "ds_read_b64_tr_b16 v[82:83], v236 offset:4096\n\t" \
  "v_accvgpr_read_b32 v24, a96\n\t" \
  "v_mfma_f32_16x16x32_bf16 a[104:107], a[180:183], v[148:151], a[104:107]\n\t" \
  "ds_read_b64_tr_b16 v[84:85], v236 offset:8192\n\t" \
  "v_accvgpr_read_b32 v25, a97\n\t" \
  "v_accvgpr_read_b32 v26, a98\n\t" \
  "v_accvgpr_read_b32 v27, a99\n\t" \
  "v_accvgpr_read_b32 v28, a112\n\t" \
  "v_mfma_f32_16x16x32_bf16 a[120:123], a[184:187], v[144:147], a[120:123]\n\t" \
  "ds_read_b64_tr_b16 v[86:87], v236 offset:12288\n\t" \
  "v_accvgpr_read_b32 v29, a113\n\t" \
  "v_accvgpr_read_b32 v30, a114\n\t" \
  "v_accvgpr_read_b32 v31, a115\n\t" \
  "v_mfma_f32_16x16x32_bf16 a[120:123], a[188:191], v[148:151], a[120:123]\n\t" \
  "ds_read_b64_tr_b16 v[88:89], v236 offset:16384\n\t" \
  "v_mul_f32 v24, v24, v208\n\t" \
  "v_mfma_f32_16x16x32_bf16 v[168:171], v[216:219], v[144:147], v[168:171]\n\t" \
  "ds_read_b64_tr_b16 v[90:91], v236 offset:20480\n\t" \
  "v_mul_f32 v25, v25, v208\n\t" \
  "v_mul_f32 v26, v26, v208\n\t" \
  "v_mul_f32 v27, v27, v208\n\t" \
  "v_mul_f32 v28, v28, v208\n\t" \
  "v_mfma_f32_16x16x32_bf16 v[168:171], v[216:219], v[148:151], v[168:171]\n\t" \
  "s_xor_b32 s98, %[eso], 0\n\t" \
  "ds_read_b64_tr_b16 v[92:93], v236 offset:24576\n\t" \
  "v_mul_f32 v29, v29, v208\n\t" \
  "v_mul_f32 v30, v30, v208\n\t" \
  "v_mul_f32 v31, v31, v208\n\t" \
  STS("s_memrealtime s[94:95]\n\t") \
  STS("s_memtime s[92:93]\n\t") \
  "v_mfma_f32_16x16x32_bf16 a[4:7], a[192:195], v[136:139], a[4:7]\n\t" \
  "buffer_store_dwordx4 v[8:11], v242, %[ws], s98 offen offset:2048 sc1\n\t" \
  "v_cvt_pk_bf16_f32 v144, v64, v65\n\t" \
  "v_mfma_f32_16x16x32_bf16 a[4:7], a[196:199], v[140:143], a[4:7]\n\t" \
  "ds_read_b64_tr_b16 v[94:95], v236 offset:28672\n\t" \
  "v_cvt_pk_bf16_f32 v12, v24, v25\n\t" \
  "v_cvt_pk_bf16_f32 v13, v26, v27\n\t" \
  "v_cvt_pk_bf16_f32 v14, v28, v29\n\t" \
  "v_cvt_pk_bf16_f32 v15, v30, v31\n\t" \
  "v_mfma_f32_16x16x32_bf16 v[164:167], v[216:219], v[136:139], v[164:167]\n\t" \
  "v_exp_f32 v96, v96\n\t" \
  "v_cvt_pk_bf16_f32 v145, v66, v67\n\t" \
  "v_mfma_f32_16x16x32_bf16 v[164:167], v[216:219], v[140:143], v[164:167]\n\t" \
  "v_exp_f32 v97, v97\n\t" \
  "v_cvt_pk_bf16_f32 v146, v68, v69\n\t" \
  "v_mfma_f32_16x16x32_bf16 a[20:23], a[200:203], v[136:139], a[20:23]\n\t" \
  "v_exp_f32 v98, v98\n\t" \
  "v_cvt_pk_bf16_f32 v147, v70, v71\n\t" \
  "v_mfma_f32_16x16x32_bf16 a[20:23], a[204:207], v[140:143], a[20:23]\n\t" \
  "v_exp_f32 v99, v99\n\t" \
  "v_cvt_pk_bf16_f32 v148, v72, v73\n\t" \
  "v_mfma_f32_16x16x32_bf16 a[36:39], a[208:211], v[136:139], a[36:39]\n\t" \
  "v_exp_f32 v100, v100\n\t" \
  "v_cvt_pk_bf16_f32 v149, v74, v75\n\t" \
  "v_mfma_f32_16x16x32_bf16 a[36:39], a[212:215], v[140:143], a[36:39]\n\t" \
  "s_xor_b32 s98, %[eso], 0\n\t" \
  "v_exp_f32 v101, v101\n\t" \
  "v_cvt_pk_bf16_f32 v150, v76, v77\n\t" \
  "v_mfma_f32_16x16x32_bf16 a[52:55], a[216:219], v[136:139], a[52:55]\n\t" \
  "buffer_store_dwordx4 v[12:15], v242, %[ws], s98 offen offset:3072 sc1\n\t" \
  "v_cvt_pk_bf16_f32 v151, v78, v79\n\t" \
  "v_mfma_f32_16x16x32_bf16 a[52:55], a[220:223], v[140:143], a[52:55]\n\t" \
  "v_rcp_f32 v210, v164\n\t" \
  "v_mfma_f32_16x16x32_bf16 a[68:71], a[224:227], v[136:139], a[68:71]\n\t" \
  "v_cmp_lt_f32 vcc, 0x5f800000, v164\n\t" \
  "s_cbranch_vccnz .Lat_x1\n\t" \
  ".Lat_y1:\n\t" \
  "v_accvgpr_read_b32 v16, a4\n\t" \
  "v_mfma_f32_16x16x32_bf16 a[68:71], a[228:231], v[140:143], a[68:71]\n\t" \
  "v_accvgpr_read_b32 v17, a5\n\t" \
  "v_accvgpr_read_b32 v18, a6\n\t" \
  "v_accvgpr_read_b32 v19, a7\n\t" \
  "v_accvgpr_read_b32 v20, a20\n\t" \
  "v_accvgpr_read_b32 v21, a21\n\t" \
  "v_mfma_f32_16x16x32_bf16 a[84:87], a[232:235], v[136:139], a[84:87]\n\t" \
  STS("v_writelane_b32 %[stv], s94, 20\n\t") \
  STS("v_writelane_b32 %[stv], s92, 21\n\t") \
  "v_accvgpr_read_b32 v22, a22\n\t" \
  "v_accvgpr_read_b32 v23, a23\n\t" \
  "v_mul_f32 v16, v16, v210\n\t" \
  "v_mul_f32 v17, v17, v210\n\t" \
  "v_mul_f32 v18, v18, v210\n\t" \
  "v_mfma_f32_16x16x32_bf16 a[84:87], a[236:239], v[140:143], a[84:87]\n\t" \
  "v_mul_f32 v19, v19, v210\n\t" \
  "v_mul_f32 v20, v20, v210\n\t" \
  "v_mul_f32 v21, v21, v210\n\t" \
  "v_mul_f32 v22, v22, v210\n\t" \
  "v_mul_f32 v23, v23, v210\n\t" \
  "v_mfma_f32_16x16x32_bf16 a[100:103], a[240:243], v[136:139], a[100:103]\n\t" \
  "v_xor_b32 v235, 128, v234\n\t" \
  "v_cvt_pk_bf16_f32 v32, v16, v17\n\t" \
  "v_cvt_pk_bf16_f32 v33, v18, v19\n\t" \
  "v_cvt_pk_bf16_f32 v34, v20, v21\n\t" \
  "v_cvt_pk_bf16_f32 v35, v22, v23\n\t" \
  "v_mfma_f32_16x16x32_bf16 a[100:103], a[244:247], v[140:143], a[100:103]\n\t" \
  "s_xor_b32 s98, %[eso], 32768\n\t" \
  "ds_read_b64_tr_b16 v[112:113], v235 offset:0\n\t" \
  "v_accvgpr_read_b32 v24, a36\n\t" \
  "v_mfma_f32_16x16x32_bf16 a[116:119], a[248:251], v[136:139], a[116:119]\n\t" \
  "buffer_store_dwordx4 v[32:35], v242, %[ws], s98 offen offset:0 sc1\n\t" \
  "v_accvgpr_read_b32 v25, a37\n\t" \
  "v_accvgpr_read_b32 v26, a38\n\t" \
  "v_accvgpr_read_b32 v27, a39\n\t" \
  "v_mfma_f32_16x16x32_bf16 a[116:119], a[252:255], v[140:143], a[116:119]\n\t" \
  "ds_read_b64_tr_b16 v[114:115], v235 offset:4096\n\t" \
  "v_accvgpr_read_b32 v28, a52\n\t" \
  "v_accvgpr_read_b32 v29, a53\n\t" \
  "v_accvgpr_read_b32 v30, a54\n\t" \
  "v_accvgpr_read_b32 v31, a55\n\t" \
  "v_mfma_f32_16x16x32_bf16 a[12:15], a[128:131], v[152:155], a[12:15]\n\t" \
  "ds_read_b64_tr_b16 v[116:117], v235 offset:8192\n\t" \
  "v_mul_f32 v24, v24, v210\n\t" \
  "v_mfma_f32_16x16x32_bf16 a[12:15], a[132:135], v[156:159], a[12:15]\n\t" \
  "ds_read_b64_tr_b16 v[118:119], v235 offset:12288\n\t" \
  "v_mul_f32 v25, v25, v210\n\t" \
  "v_mul_f32 v26, v26, v210\n\t" \
  "v_mul_f32 v27, v27, v210\n\t" \
  "v_mul_f32 v28, v28, v210\n\t" \
  "v_mfma_f32_16x16x32_bf16 a[28:31], a[136:139], v[152:155], a[28:31]\n\t" \
  "ds_read_b64_tr_b16 v[120:121], v235 offset:16384\n\t" \
  "v_mul_f32 v29, v29, v210\n\t" \
  "v_mul_f32 v30, v30, v210\n\t" \
  "v_mul_f32 v31, v31, v210\n\t" \
  "v_mfma_f32_16x16x32_bf16 a[28:31], a[140:143], v[156:159], a[28:31]\n\t" \
  "ds_read_b64_tr_b16 v[122:123], v235 offset:20480\n\t" \
  "v_cvt_pk_bf16_f32 v36, v24, v25\n\t" \
  "v_mfma_f32_16x16x32_bf16 a[44:47], a[144:147], v[152:155], a[44:47]\n\t" \
  "ds_read_b64_tr_b16 v[124:125], v235 offset:24576\n\t" \
  "v_cvt_pk_bf16_f32 v37, v26, v27\n\t" \
  "v_cvt_pk_bf16_f32 v38, v28, v29\n\t" \
  "v_cvt_pk_bf16_f32 v39, v30, v31\n\t" \
  "v_mfma_f32_16x16x32_bf16 a[44:47], a[148:151], v[156:159], a[44:47]\n\t" \
  "s_xor_b32 s98, %[eso], 32768\n\t" \
  "ds_read_b64_tr_b16 v[126:127], v235 offset:28672\n\t" \
  "v_accvgpr_read_b32 v16, a68\n\t" \
  "v_accvgpr_read_b32 v17, a69\n\t" \
  "v_accvgpr_read_b32 v18, a70\n\t" \
  "v_accvgpr_read_b32 v19, a71\n\t" \
  "v_mfma_f32_16x16x32_bf16 a[60:63], a[152:155], v[152:155], a[60:63]\n\t" \
  "buffer_store_dwordx4 v[36:39], v242, %[ws], s98 offen offset:1024 sc1\n\t" \
  "v_accvgpr_read_b32 v20, a84\n\t" \
  "v_accvgpr_read_b32 v21, a85\n\t" \
  "v_accvgpr_read_b32 v22, a86\n\t" \
  "v_mfma_f32_16x16x32_bf16 a[60:63], a[156:159], v[156:159], a[60:63]\n\t" \
  "v_accvgpr_read_b32 v23, a87\n\t" \
  "v_mul_f32 v16, v16, v210\n\t" \
  "v_mfma_f32_16x16x32_bf16 a[76:79], a[160:163], v[152:155], a[76:79]\n\t" \
  "v_mul_f32 v17, v17, v210\n\t" \
  "v_mul_f32 v18, v18, v210\n\t" \
  "v_mul_f32 v19, v19, v210\n\t" \
  "v_mul_f32 v20, v20, v210\n\t" \
  "v_mul_f32 v21, v21, v210\n\t" \
  "v_mfma_f32_16x16x32_bf16 a[76:79], a[164:167], v[156:159], a[76:79]\n\t" \
  "v_mul_f32 v22, v22, v210\n\t" \
  "v_mul_f32 v23, v23, v210\n\t" \
  "v_mfma_f32_16x16x32_bf16 a[92:95], a[168:171], v[152:155], a[92:95]\n\t" \
  "v_cvt_pk_bf16_f32 v40, v16, v17\n\t" \
  "v_cvt_pk_bf16_f32 v41, v18, v19\n\t" \
  "v_mfma_f32_16x16x32_bf16 a[92:95], a[172:175], v[156:159], a[92:95]\n\t" \
  "v_cvt_pk_bf16_f32 v42, v20, v21\n\t" \
  "v_cvt_pk_bf16_f32 v43, v22, v23\n\t" \
  "v_mfma_f32_16x16x32_bf16 a[108:111], a[176:179], v[152:155], a[108:111]\n\t" \
  "v_accvgpr_read_b32 v24, a100\n\t" \
  "v_accvgpr_read_b32 v25, a101\n\t" \
  "v_mfma_f32_16x16x32_bf16 a[108:111], a[180:183], v[156:159], a[108:111]\n\t" \
  "s_xor_b32 s98, %[eso], 32768\n\t" \
  "v_accvgpr_read_b32 v26, a102\n\t" \
  "v_accvgpr_read_b32 v27, a103\n\t" \
  "v_accvgpr_read_b32 v28, a116\n\t" \
  "v_accvgpr_read_b32 v29, a117\n\t" \
  "v_accvgpr_read_b32 v30, a118\n\t" \
  "v_mfma_f32_16x16x32_bf16 a[124:127], a[184:187], v[152:155], a[124:127]\n\t" \
  "buffer_store_dwordx4 v[40:43], v242, %[ws], s98 offen offset:2048 sc1\n\t" \
  "v_accvgpr_read_b32 v31, a119\n\t" \
  "v_mfma_f32_16x16x32_bf16 a[124:127], a[188:191], v[156:159], a[124:127]\n\t" \
  "v_mul_f32 v24, v24, v210\n\t" \
  "v_mul_f32 v25, v25, v210\n\t" \
  "v_mfma_f32_16x16x32_bf16 v[172:175], v[216:219], v[152:155], v[172:175]\n\t" \
  "v_mul_f32 v26, v26, v210\n\t" \
  "v_mul_f32 v27, v27, v210\n\t" \
  "v_mul_f32 v28, v28, v210\n\t" \
  "v_mul_f32 v29, v29, v210\n\t" \
  "v_mul_f32 v30, v30, v210\n\t" \
  "v_mfma_f32_16x16x32_bf16 v[172:175], v[216:219], v[156:159], v[172:175]\n\t" \
  "ds_read_b64_tr_b16 a[128:129], v234 offset:0\n\t" \
  "v_mul_f32 v31, v31, v210\n\t" \
  "v_mfma_f32_16x16x32_bf16 a[8:11], a[192:195], v[144:147], a[8:11]\n\t" \
  "ds_read_b64_tr_b16 a[130:131], v234 offset:4096\n\t" \
  "v_cvt_pk_bf16_f32 v152, v96, v97\n\t" \
  "v_mfma_f32_16x16x32_bf16 a[8:11], a[196:199], v[148:151], a[8:11]\n\t" \
  "ds_read_b64_tr_b16 a[132:133], v234 offset:8192\n\t" \
  "v_cvt_pk_bf16_f32 v44, v24, v25\n\t" \
  "v_cvt_pk_bf16_f32 v45, v26, v27\n\t" \
  "v_cvt_pk_bf16_f32 v46, v28, v29\n\t" \
  "v_cvt_pk_bf16_f32 v47, v30, v31\n\t" \
  "v_mfma_f32_16x16x32_bf16 v[168:171], v[216:219], v[144:147], v[168:171]\n\t" \
  "v_exp_f32 v102, v102\n\t" \
  "v_cvt_pk_bf16_f32 v153, v98, v99\n\t" \
  "v_mfma_f32_16x16x32_bf16 v[168:171], v[216:219], v[148:151], v[168:171]\n\t" \
  "s_xor_b32 s98, %[eso], 32768\n\t" \
  "ds_read_b64_tr_b16 a[134:135], v234 offset:12288\n\t" \
  "v_cvt_pk_bf16_f32 v154, v100, v101\n\t" \
  "v_mfma_f32_16x16x32_bf16 a[24:27], a[200:203], v[144:147], a[24:27]\n\t" \
  "buffer_store_dwordx4 v[44:47], v242, %[ws], s98 offen offset:3072 sc1\n\t" \
  "v_mfma_f32_16x16x32_bf16 a[24:27], a[204:207], v[148:151], a[24:27]\n\t" \
  "v_exp_f32 v103, v103\n\t" \
  "v_mfma_f32_16x16x32_bf16 a[40:43], a[208:211], v[144:147], a[40:43]\n\t" \
  "ds_read_b64_tr_b16 a[136:137], v234 offset:16384\n\t" \
  "v_cvt_pk_bf16_f32 v155, v102, v103\n\t" \
  "v_mfma_f32_16x16x32_bf16 a[40:43], a[212:215], v[148:151], a[40:43]\n\t" \
  "ds_read_b64_tr_b16 a[138:139], v234 offset:20480\n\t" \
  "ds_read_b64_tr_b16 a[140:141], v234 offset:24576\n\t" \
  "v_mfma_f32_16x16x32_bf16 a[56:59], a[216:219], v[144:147], a[56:59]\n\t" \
  "v_exp_f32 v104, v104\n\t" \
  "v_mfma_f32_16x16x32_bf16 a[56:59], a[220:223], v[148:151], a[56:59]\n\t" \
  "ds_read_b64_tr_b16 a[142:143], v234 offset:28672\n\t" \
  "v_xor_b32 v236, 32, v234\n\t" \
  "v_mfma_f32_16x16x32_bf16 a[72:75], a[224:227], v[144:147], a[72:75]\n\t" \
  "v_exp_f32 v105, v105\n\t" \
  "v_mfma_f32_16x16x32_bf16 a[72:75], a[228:231], v[148:151], a[72:75]\n\t" \
  "v_exp_f32 v106, v106\n\t" \
  "v_mfma_f32_16x16x32_bf16 a[88:91], a[232:235], v[144:147], a[88:91]\n\t" \
  "v_exp_f32 v107, v107\n\t" \
  "v_cvt_pk_bf16_f32 v156, v104, v105\n\t" \
  "v_mfma_f32_16x16x32_bf16 a[88:91], a[236:239], v[148:151], a[88:91]\n\t" \
  "v_exp_f32 v108, v108\n\t" \
  "v_cvt_pk_bf16_f32 v157, v106, v107\n\t" \
  "v_mfma_f32_16x16x32_bf16 a[104:107], a[240:243], v[144:147], a[104:107]\n\t" \
  "ds_read_b64_tr_b16 a[144:145], v236 offset:0\n\t" \
  "ds_read_b64_tr_b16 a[146:147], v236 offset:4096\n\t" \
  "v_mfma_f32_16x16x32_bf16 a[104:107], a[244:247], v[148:151], a[104:107]\n\t" \
  "v_exp_f32 v109, v109\n\t" \
  "v_mfma_f32_16x16x32_bf16 a[120:123], a[248:251], v[144:147], a[120:123]\n\t" \
  "ds_read_b64_tr_b16 a[148:149], v236 offset:8192\n\t" \
  "v_cvt_pk_bf16_f32 v158, v108, v109\n\t" \
  "v_mfma_f32_16x16x32_bf16 a[120:123], a[252:255], v[148:151], a[120:123]\n\t" \
  "ds_read_b64_tr_b16 a[150:151], v236 offset:12288\n\t" \
  "ds_read_b64_tr_b16 a[152:153], v236 offset:16384\n\t" \
  "v_exp_f32 v110, v110\n\t" \
  "v_mfma_f32_16x16x32_bf16 a[12:15], a[192:195], v[152:155], a[12:15]\n\t" \
  "v_exp_f32 v111, v111\n\t" \
  "s_nop 0\n\t" \
  "v_cvt_pk_bf16_f32 v159, v110, v111\n\t" \
  "s_nop 1\n\t" \
  "v_mfma_f32_16x16x32_bf16 a[12:15], a[196:199], v[156:159], a[12:15]\n\t" \
  "ds_read_b64_tr_b16 a[154:155], v236 offset:20480\n\t" \
  "ds_read_b64_tr_b16 a[156:157], v236 offset:24576\n\t" \
  "v_mfma_f32_16x16x32_bf16 v[172:175], v[216:219], v[152:155], v[172:175]\n\t" \
  "ds_read_b64_tr_b16 a[158:159], v236 offset:28672\n\t" \
  "v_xor_b32 v235, 192, v234\n\t" \
  "v_mfma_f32_16x16x32_bf16 v[172:175], v[216:219], v[156:159], v[172:175]\n\t" \
  "ds_read_b64_tr_b16 a[160:161], v235 offset:0\n\t" \
  "ds_read_b64_tr_b16 a[162:163], v235 offset:4096\n\t" \
  "v_mfma_f32_16x16x32_bf16 a[28:31], a[200:203], v[152:155], a[28:31]\n\t" \
  "v_rcp_f32 v212, v168\n\t" \
  "v_mfma_f32_16x16x32_bf16 a[28:31], a[204:207], v[156:159], a[28:31]\n\t" \
  "ds_read_b64_tr_b16 a[164:165], v235 offset:8192\n\t" \
  "ds_read_b64_tr_b16 a[166:167], v235 offset:12288\n\t" \
  "v_mfma_f32_16x16x32_bf16 a[44:47], a[208:211], v[152:155], a[44:47]\n\t" \
  "v_cmp_lt_f32 vcc, 0x5f800000, v168\n\t" \
  "s_cbranch_vccnz .Lat_x2\n\t" \
  ".Lat_y2:\n\t" \
  "v_accvgpr_read_b32 v16, a8\n\t" \
  "v_mfma_f32_16x16x32_bf16 a[44:47], a[212:215], v[156:159], a[44:47]\n\t" \
  "ds_read_b64_tr_b16 a[168:169], v235 offset:16384\n\t" \
  "v_accvgpr_read_b32 v17, a9\n\t" \
  "v_accvgpr_read_b32 v18, a10\n\t" \
  "v_accvgpr_read_b32 v19, a11\n\t" \
  "v_accvgpr_read_b32 v20, a24\n\t" \
  "v_mfma_f32_16x16x32_bf16 a[60:63], a[216:219], v[152:155], a[60:63]\n\t" \
  "ds_read_b64_tr_b16 a[170:171], v235 offset:20480\n\t" \
  "v_accvgpr_read_b32 v21, a25\n\t" \
  "v_accvgpr_read_b32 v22, a26\n\t" \
  "v_accvgpr_read_b32 v23, a27\n\t" \
  "v_mul_f32 v16, v16, v212\n\t" \
  "v_mfma_f32_16x16x32_bf16 a[60:63], a[220:223], v[156:159], a[60:63]\n\t" \
  "ds_read_b64_tr_b16 a[172:173], v235 offset:24576\n\t" \
  "v_mul_f32 v17, v17, v212\n\t" \
  "v_mul_f32 v18, v18, v212\n\t" \
  "v_mul_f32 v19, v19, v212\n\t" \
  "v_mul_f32 v20, v20, v212\n\t" \
  "v_mfma_f32_16x16x32_bf16 a[76:79], a[224:227], v[152:155], a[76:79]\n\t" \
  "ds_read_b64_tr_b16 a[174:175], v235 offset:28672\n\t" \
  "v_mul_f32 v21, v21, v212\n\t" \
  "v_mul_f32 v22, v22, v212\n\t" \
  "v_mul_f32 v23, v23, v212\n\t" \
  "v_cvt_pk_bf16_f32 v64, v16, v17\n\t" \
  "v_mfma_f32_16x16x32_bf16 a[76:79], a[228:231], v[156:159], a[76:79]\n\t" \
  "v_cvt_pk_bf16_f32 v65, v18, v19\n\t" \
  "v_cvt_pk_bf16_f32 v66, v20, v21\n\t" \
  "v_cvt_pk_bf16_f32 v67, v22, v23\n\t" \
  "v_accvgpr_read_b32 v24, a40\n\t" \
  "v_accvgpr_read_b32 v25, a41\n\t" \
  "v_mfma_f32_16x16x32_bf16 a[92:95], a[232:235], v[152:155], a[92:95]\n\t" \
  "s_xor_b32 s98, %[eso], 65536\n\t" \
  "buffer_store_dwordx4 v[64:67], v242, %[ws], s98 offen offset:0 sc1\n\t" \
  "v_accvgpr_read_b32 v26, a42\n\t" \
  "v_accvgpr_read_b32 v27, a43\n\t" \
  "v_mfma_f32_16x16x32_bf16 a[92:95], a[236:239], v[156:159], a[92:95]\n\t" \
  "v_accvgpr_read_b32 v28, a56\n\t" \
  "v_accvgpr_read_b32 v29, a57\n\t" \
  "v_accvgpr_read_b32 v30, a58\n\t" \
  "v_accvgpr_read_b32 v31, a59\n\t" \
  "v_mul_f32 v24, v24, v212\n\t" \
  "v_mfma_f32_16x16x32_bf16 a[108:111], a[240:243], v[152:155], a[108:111]\n\t" \
  "v_mul_f32 v25, v25, v212\n\t" \
  "v_mul_f32 v26, v26, v212\n\t" \
  "v_mul_f32 v27, v27, v212\n\t" \
  "v_mul_f32 v28, v28, v212\n\t" \
  "v_mul_f32 v29, v29, v212\n\t" \
  "v_mfma_f32_16x16x32_bf16 a[108:111], a[244:247], v[156:159], a[108:111]\n\t" \
  "v_mul_f32 v30, v30, v212\n\t" \
  "v_mul_f32 v31, v31, v212\n\t" \
  "v_cvt_pk_bf16_f32 v68, v24, v25\n\t" \
  "v_cvt_pk_bf16_f32 v69, v26, v27\n\t" \
  "v_cvt_pk_bf16_f32 v70, v28, v29\n\t" \
  "v_mfma_f32_16x16x32_bf16 a[124:127], a[248:251], v[152:155], a[124:127]\n\t" \
  "v_cvt_pk_bf16_f32 v71, v30, v31\n\t" \
  "v_accvgpr_read_b32 v16, a72\n\t" \
  "v_accvgpr_read_b32 v17, a73\n\t" \
  "v_accvgpr_read_b32 v18, a74\n\t" \
  "v_accvgpr_read_b32 v19, a75\n\t" \
  "v_mfma_f32_16x16x32_bf16 a[124:127], a[252:255], v[156:159], a[124:127]\n\t" \
  "s_waitcnt lgkmcnt(15)\n\t" \
  "s_xor_b32 s98, %[eso], 65536\n\t" \
  "buffer_store_dwordx4 v[68:71], v242, %[ws], s98 offen offset:1024 sc1\n\t" \
  "v_accvgpr_read_b32 v20, a88\n\t" \
  "v_mfma_f32_16x16x32_bf16 a[32:35], v[48:51], v[0:3], 0\n\t" \
  "v_accvgpr_read_b32 v21, a89\n\t" \
  "v_accvgpr_read_b32 v22, a90\n\t" \
  "v_accvgpr_read_b32 v23, a91\n\t" \
  "v_mul_f32 v16, v16, v212\n\t" \
  "v_mul_f32 v17, v17, v212\n\t" \
  "v_mfma_f32_16x16x32_bf16 a[32:35], v[52:55], v[4:7], a[32:35]\n\t" \
  "v_mul_f32 v18, v18, v212\n\t" \
  "v_mul_f32 v19, v19, v212\n\t" \
  "v_mul_f32 v20, v20, v212\n\t" \
  "v_mul_f32 v21, v21, v212\n\t" \
  "v_mul_f32 v22, v22, v212\n\t" \
  "v_mfma_f32_16x16x32_bf16 a[32:35], v[56:59], v[8:11], a[32:35]\n\t" \
  "v_mul_f32 v23, v23, v212\n\t" \
  "v_cvt_pk_bf16_f32 v72, v16, v17\n\t" \
  "v_cvt_pk_bf16_f32 v73, v18, v19\n\t" \
  "v_cvt_pk_bf16_f32 v74, v20, v21\n\t" \
  "v_cvt_pk_bf16_f32 v75, v22, v23\n\t" \
  "v_mfma_f32_16x16x32_bf16 a[32:35], v[60:63], v[12:15], a[32:35]\n\t" \
  "s_xor_b32 s98, %[eso], 65536\n\t" \
  "buffer_store_dwordx4 v[72:75], v242, %[ws], s98 offen offset:2048 sc1\n\t" \
  ".Lhs10:\n\ts_cbranch_execz .Lhs10n\n\t" \
  "v_xor_b32 v236, 224, v234\n\t" \
  "v_accvgpr_read_b32 v24, a104\n\t" \
  "v_mfma_f32_16x16x32_bf16 a[48:51], v[92:95], v[12:15], 0\n\t" \
  "ds_read_b64_tr_b16 a[176:177], v236 offset:0\n\t" \
  "v_accvgpr_read_b32 v25, a105\n\t" \
  "v_accvgpr_read_b32 v26, a106\n\t" \
  "v_accvgpr_read_b32 v27, a107\n\t" \
  "v_accvgpr_read_b32 v28, a120\n\t" \
  "v_mfma_f32_16x16x32_bf16 a[48:51], v[88:91], v[8:11], a[48:51]\n\t" \
  "ds_read_b64_tr_b16 a[178:179], v236 offset:4096\n\t" \
  "v_accvgpr_read_b32 v29, a121\n\t" \
  "v_accvgpr_read_b32 v30, a122\n\t" \
  "v_accvgpr_read_b32 v31, a123\n\t" \
  "v_mul_f32 v24, v24, v212\n\t" \
  "v_mfma_f32_16x16x32_bf16 a[48:51], v[84:87], v[4:7], a[48:51]\n\t" \
  "ds_read_b64_tr_b16 a[180:181], v236 offset:8192\n\t" \
  "v_mul_f32 v25, v25, v212\n\t" \
  "v_mul_f32 v26, v26, v212\n\t" \
  "v_mul_f32 v27, v27, v212\n\t" \
  "v_mul_f32 v28, v28, v212\n\t" \
  "v_mfma_f32_16x16x32_bf16 a[48:51], v[80:83], v[0:3], a[48:51]\n\t" \
  "ds_read_b64_tr_b16 a[182:183], v236 offset:12288\n\t" \
  "v_mul_f32 v29, v29, v212\n\t" \
  "v_mul_f32 v30, v30, v212\n\t" \
  "v_mul_f32 v31, v31, v212\n\t" \
  "v_cvt_pk_bf16_f32 v76, v24, v25\n\t" \
  "v_mfma_f32_16x16x32_bf16 a[64:67], v[112:115], v[0:3], 0\n\t" \
  "ds_read_b64_tr_b16 a[184:185], v236 offset:16384\n\t" \
  "v_cvt_pk_bf16_f32 v77, v26, v27\n\t" \
  "v_cvt_pk_bf16_f32 v78, v28, v29\n\t" \
  "v_cvt_pk_bf16_f32 v79, v30, v31\n\t" \
  "v_mfma_f32_16x16x32_bf16 a[64:67], v[116:119], v[4:7], a[64:67]\n\t" \
  "s_xor_b32 s98, %[eso], 65536\n\t" \
  "ds_read_b64_tr_b16 a[186:187], v236 offset:20480\n\t" \
  "ds_read_b64_tr_b16 a[188:189], v236 offset:24576\n\t" \
  "v_mfma_f32_16x16x32_bf16 a[64:67], v[120:123], v[8:11], a[64:67]\n\t" \
  "buffer_store_dwordx4 v[76:79], v242, %[ws], s98 offen offset:3072 sc1\n\t" \
  "v_mfma_f32_16x16x32_bf16 a[64:67], v[124:127], v[12:15], a[64:67]\n\t" \
  "ds_read_b64_tr_b16 a[190:191], v236 offset:28672\n\t" \
  "v_xor_b32 v236, 160, v234\n\t" \
  "v_mfma_f32_16x16x32_bf16 a[0:3], a[140:143], v[12:15], 0\n\t" \
  "ds_read_b64_tr_b16 v[96:97], v236 offset:0\n\t" \
  "ds_read_b64_tr_b16 v[98:99], v236 offset:4096\n\t" \
  "v_mfma_f32_16x16x32_bf16 a[0:3], a[136:139], v[8:11], a[0:3]\n\t" \
  "ds_read_b64_tr_b16 v[100:101], v236 offset:8192\n\t" \
  "ds_read_b64_tr_b16 v[102:103], v236 offset:12288\n\t" \
  "v_mfma_f32_16x16x32_bf16 a[0:3], a[132:135], v[4:7], a[0:3]\n\t" \
  "ds_read_b64_tr_b16 v[104:105], v236 offset:16384\n\t" \
  "ds_read_b64_tr_b16 v[106:107], v236 offset:20480\n\t" \
  "v_mfma_f32_16x16x32_bf16 a[0:3], a[128:131], v[0:3], a[0:3]\n\t" \
  "s_waitcnt lgkmcnt(15)\n\t" \
  "ds_read_b64_tr_b16 v[108:109], v236 offset:24576\n\t" \
  "ds_read_b64_tr_b16 v[110:111], v236 offset:28672\n\t" \
  "v_mfma_f32_16x16x32_bf16 a[16:19], a[144:147], v[0:3], 0\n\t" \
  "ds_read_b128 a[224:227], v213 offset:32768\n\t" \
  "ds_read_b128 a[228:231], v213 offset:33792\n\t" \
  "v_mfma_f32_16x16x32_bf16 a[16:19], a[148:151], v[4:7], a[16:19]\n\t" \
  "ds_read_b128 a[232:235], v213 offset:34816\n\t" \
  "ds_read_b128 a[236:239], v213 offset:35840\n\t" \
  "v_mfma_f32_16x16x32_bf16 a[16:19], a[152:155], v[8:11], a[16:19]\n\t" \
  "v_rcp_f32 v214, v172\n\t" \
  "v_mfma_f32_16x16x32_bf16 a[16:19], a[156:159], v[12:15], a[16:19]\n\t" \
  "s_waitcnt lgkmcnt(15)\n\t" \
  "ds_read_b128 a[240:243], v215 offset:0\n\t" \
  "ds_read_b128 a[244:247], v215 offset:1024\n\t" \
  "v_mfma_f32_16x16x32_bf16 a[96:99], a[172:175], v[12:15], 0\n\t" \
  "buffer_load_dwordx4 v[224:227], v252, %[ws], %[ss] offen sc1\n\t" \
  "v_mfma_f32_16x16x32_bf16 a[96:99], a[168:171], v[8:11], a[96:99]\n\t" \
  "v_cmp_lt_f32 vcc, 0x5f800000, v172\n\t" \
  "s_cbranch_vccnz .Lat_x3\n\t" \
  ".Lat_y3:\n\t" \
  "v_mfma_f32_16x16x32_bf16 a[96:99], a[164:167], v[4:7], a[96:99]\n\t" \
  "ds_read_b128 a[248:251], v215 offset:2048\n\t" \
  "v_accvgpr_read_b32 v16, a12\n\t" \
  "v_mfma_f32_16x16x32_bf16 a[96:99], a[160:163], v[0:3], a[96:99]\n\t" \
  "ds_read_b128 a[252:255], v215 offset:3072\n\t" \
  "v_accvgpr_read_b32 v17, a13\n\t" \
  "v_mfma_f32_16x16x32_bf16 a[112:115], a[176:179], v[0:3], 0\n\t" \
  "buffer_load_dwordx4 v[128:131], v247, %[sM], %[gvo] offen\n\t" \
  "v_mfma_f32_16x16x32_bf16 a[112:115], a[180:183], v[4:7], a[112:115]\n\t" \
  "s_waitcnt lgkmcnt(15)\n\t" \
  "v_accvgpr_read_b32 v18, a14\n\t" \
  "v_accvgpr_read_b32 v19, a15\n\t" \
  "v_mfma_f32_16x16x32_bf16 a[112:115], a[184:187], v[8:11], a[112:115]\n\t" \
  "s_add_u32 s98, %[gvo], 12288\n\t" \
  "v_accvgpr_read_b32 v20, a28\n\t" \
  "v_accvgpr_read_b32 v21, a29\n\t" \
  "v_mfma_f32_16x16x32_bf16 a[112:115], a[188:191], v[12:15], a[112:115]\n\t" \
  "s_waitcnt lgkmcnt(8)\n\t" \
  "v_accvgpr_read_b32 v22, a30\n\t" \
  "v_accvgpr_read_b32 v23, a31\n\t" \
  "v_mfma_f32_16x16x32_bf16 a[80:83], v[108:111], v[12:15], 0\n\t" \
  "buffer_load_dwordx4 v[132:135], v247, %[sM], s98 offen\n\t" \
  "v_mfma_f32_16x16x32_bf16 a[80:83], v[104:107], v[8:11], a[80:83]\n\t" \
  "s_add_u32 s98, %[gvo], 24576\n\t" \
  "v_mul_f32 v16, v16, v214\n\t" \
  "v_mul_f32 v17, v17, v214\n\t" \
  "v_mfma_f32_16x16x32_bf16 a[80:83], v[100:103], v[4:7], a[80:83]\n\t" \
  "v_mul_f32 v18, v18, v214\n\t" \
  "v_mul_f32 v19, v19, v214\n\t" \
  "v_mfma_f32_16x16x32_bf16 a[80:83], v[96:99], v[0:3], a[80:83]\n\t" \
  "v_mul_f32 v20, v20, v214\n\t" \
  "v_mul_f32 v21, v21, v214\n\t" \
  "v_mfma_f32_16x16x32_bf16 a[36:39], v[48:51], v[32:35], 0\n\t" \
  "buffer_load_dwordx4 v[136:139], v247, %[sM], s98 offen\n\t" \
  "v_mfma_f32_16x16x32_bf16 a[36:39], v[52:55], v[36:39], a[36:39]\n\t" \
  "s_add_u32 s98, %[gvo], 36864\n\t" \
  "v_mul_f32 v22, v22, v214\n\t" \
  "v_mul_f32 v23, v23, v214\n\t" \
  "v_mfma_f32_16x16x32_bf16 a[36:39], v[56:59], v[40:43], a[36:39]\n\t" \
  "v_cvt_pk_bf16_f32 v160, v16, v17\n\t" \
  "v_cvt_pk_bf16_f32 v161, v18, v19\n\t" \
  "v_mfma_f32_16x16x32_bf16 a[36:39], v[60:63], v[44:47], a[36:39]\n\t" \
  "v_cvt_pk_bf16_f32 v162, v20, v21\n\t" \
  "v_cvt_pk_bf16_f32 v163, v22, v23\n\t" \
  "s_waitcnt vmcnt(8)\n\t" \
  "buffer_store_dword v243, v254, %[ws], %[fw2] offen offset:0 sc1\n\t" \
  "v_mfma_f32_16x16x32_bf16 a[52:55], v[92:95], v[44:47], 0\n\t" \
  "buffer_load_dwordx4 v[140:143], v247, %[sM], s98 offen\n\t" \
  "v_mfma_f32_16x16x32_bf16 a[52:55], v[88:91], v[40:43], a[52:55]\n\t" \
  "s_add_u32 s98, %[gvo], 49152\n\t" \
  "s_waitcnt lgkmcnt(4)\n\t" \
  "ds_write_b128 v213, v[160:163] offset:32768\n\t" \
  "v_mfma_f32_16x16x32_bf16 a[52:55], v[84:87], v[36:39], a[52:55]\n\t" \
  "v_accvgpr_read_b32 v24, a44\n\t" \
  "v_accvgpr_read_b32 v25, a45\n\t" \
  "v_mfma_f32_16x16x32_bf16 a[52:55], v[80:83], v[32:35], a[52:55]\n\t" \
  "v_accvgpr_read_b32 v26, a46\n\t" \
  "v_accvgpr_read_b32 v27, a47\n\t" \
  "v_mfma_f32_16x16x32_bf16 a[68:71], v[112:115], v[32:35], 0\n\t" \
  "buffer_load_dwordx4 v[144:147], v247, %[sM], s98 offen\n\t" \
  "v_mfma_f32_16x16x32_bf16 a[68:71], v[116:119], v[36:39], a[68:71]\n\t" \
  "s_add_u32 s98, %[gvo], 61440\n\t" \
  "v_accvgpr_read_b32 v28, a60\n\t" \
  "v_accvgpr_read_b32 v29, a61\n\t" \
  "v_mfma_f32_16x16x32_bf16 a[68:71], v[120:123], v[40:43], a[68:71]\n\t" \
  "v_accvgpr_read_b32 v30, a62\n\t" \
  "v_accvgpr_read_b32 v31, a63\n\t" \
  "v_mfma_f32_16x16x32_bf16 a[68:71], v[124:127], v[44:47], a[68:71]\n\t" \
  "v_mul_f32 v24, v24, v214\n\t" \
  "v_mul_f32 v25, v25, v214\n\t" \
  "v_mfma_f32_16x16x32_bf16 a[4:7], a[140:143], v[44:47], 0\n\t" \
  "buffer_load_dwordx4 v[148:151], v247, %[sM], s98 offen\n\t" \
  "v_mfma_f32_16x16x32_bf16 a[4:7], a[136:139], v[40:43], a[4:7]\n\t" \
  "s_add_u32 s98, %[gvo], 73728\n\t" \
  "v_mul_f32 v26, v26, v214\n\t" \
  "v_mul_f32 v27, v27, v214\n\t" \
  "v_mfma_f32_16x16x32_bf16 a[4:7], a[132:135], v[36:39], a[4:7]\n\t" \
  "v_mul_f32 v28, v28, v214\n\t" \
  "v_mul_f32 v29, v29, v214\n\t" \
  "v_mfma_f32_16x16x32_bf16 a[4:7], a[128:131], v[32:35], a[4:7]\n\t" \
  "v_mul_f32 v30, v30, v214\n\t" \
  "v_mul_f32 v31, v31, v214\n\t" \
  "v_mfma_f32_16x16x32_bf16 a[20:23], a[144:147], v[32:35], 0\n\t" \
  "buffer_load_dwordx4 v[152:155], v247, %[sM], s98 offen\n\t" \
  "v_mfma_f32_16x16x32_bf16 a[20:23], a[148:151], v[36:39], a[20:23]\n\t" \
  "s_add_u32 s98, %[gvo], 86016\n\t" \
  "v_cvt_pk_bf16_f32 v164, v24, v25\n\t" \
  "v_cvt_pk_bf16_f32 v165, v26, v27\n\t" \
  "v_mfma_f32_16x16x32_bf16 a[20:23], a[152:155], v[40:43], a[20:23]\n\t" \
  "v_cvt_pk_bf16_f32 v166, v28, v29\n\t" \
  "v_cvt_pk_bf16_f32 v167, v30, v31\n\t" \
  "v_mfma_f32_16x16x32_bf16 a[20:23], a[156:159], v[44:47], a[20:23]\n\t" \
  "ds_write_b128 v213, v[164:167] offset:33792\n\t" \
  "v_mfma_f32_16x16x32_bf16 a[100:103], a[172:175], v[44:47], 0\n\t" \
  "buffer_load_dwordx4 v[156:159], v247, %[sM], s98 offen\n\t" \
  "v_mfma_f32_16x16x32_bf16 a[100:103], a[168:171], v[40:43], a[100:103]\n\t" \
  "v_accvgpr_read_b32 v16, a76\n\t" \
  "v_accvgpr_read_b32 v17, a77\n\t" \
  "v_mfma_f32_16x16x32_bf16 a[100:103], a[164:167], v[36:39], a[100:103]\n\t" \
  "v_accvgpr_read_b32 v18, a78\n\t" \
  "v_accvgpr_read_b32 v19, a79\n\t" \
  "v_mfma_f32_16x16x32_bf16 a[100:103], a[160:163], v[32:35], a[100:103]\n\t" \
  "v_accvgpr_read_b32 v20, a92\n\t" \
  "v_accvgpr_read_b32 v21, a93\n\t" \
  "v_mfma_f32_16x16x32_bf16 a[116:119], a[176:179], v[32:35], 0\n\t" \
  "v_accvgpr_read_b32 v22, a94\n\t" \
  "v_accvgpr_read_b32 v23, a95\n\t" \
  "v_mfma_f32_16x16x32_bf16 a[116:119], a[180:183], v[36:39], a[116:119]\n\t" \
  "v_mul_f32 v16, v16, v214\n\t" \
  "v_mul_f32 v17, v17, v214\n\t" \
  "v_mfma_f32_16x16x32_bf16 a[116:119], a[184:187], v[40:43], a[116:119]\n\t" \
  "v_mul_f32 v18, v18, v214\n\t" \
  "v_mul_f32 v19, v19, v214\n\t" \
  "v_mfma_f32_16x16x32_bf16 a[116:119], a[188:191], v[44:47], a[116:119]\n\t" \
  "v_mul_f32 v20, v20, v214\n\t" \
  "v_mul_f32 v21, v21, v214\n\t" \
  "v_mfma_f32_16x16x32_bf16 a[84:87], v[108:111], v[44:47], 0\n\t" \
  "s_add_u32 m0, %[m], 147456\n\t" \
  "v_mul_f32 v22, v22, v214\n\t" \
  "v_mul_f32 v23, v23, v214\n\t" \
  "v_mfma_f32_16x16x32_bf16 a[84:87], v[104:107], v[40:43], a[84:87]\n\t" \
  "s_xor_b32 s97, %[wob], 655360\n\t" \
  "v_cvt_pk_bf16_f32 v168, v16, v17\n\t" \
  "v_cvt_pk_bf16_f32 v169, v18, v19\n\t" \
  "v_mfma_f32_16x16x32_bf16 a[84:87], v[100:103], v[36:39], a[84:87]\n\t" \
  "v_cvt_pk_bf16_f32 v170, v20, v21\n\t" \
  "v_cvt_pk_bf16_f32 v171, v22, v23\n\t" \
  "v_mfma_f32_16x16x32_bf16 a[84:87], v[96:99], v[32:35], a[84:87]\n\t" \
  "ds_write_b128 v213, v[168:171] offset:34816\n\t" \
  "v_mfma_f32_16x16x32_bf16 a[40:43], v[48:51], v[64:67], 0\n\t" \
  "v_accvgpr_read_b32 v24, a108\n\t" \
  "v_accvgpr_read_b32 v25, a109\n\t" \
  "v_mfma_f32_16x16x32_bf16 a[40:43], v[52:55], v[68:71], a[40:43]\n\t" \
  "v_accvgpr_read_b32 v26, a110\n\t" \
  "v_accvgpr_read_b32 v27, a111\n\t" \
  "v_mfma_f32_16x16x32_bf16 a[40:43], v[56:59], v[72:75], a[40:43]\n\t" \
  "buffer_load_dwordx4 v240, %[wo], s97 offen offset:0 lds\n\t" \
  "v_mfma_f32_16x16x32_bf16 a[40:43], v[60:63], v[76:79], a[40:43]\n\t" \
  "s_add_u32 s97, s97, 7168\n\t" \
  "v_accvgpr_read_b32 v28, a124\n\t" \
  "v_accvgpr_read_b32 v29, a125\n\t" \
  "v_mfma_f32_16x16x32_bf16 a[56:59], v[92:95], v[76:79], 0\n\t" \
  "v_accvgpr_read_b32 v30, a126\n\t" \
  "v_accvgpr_read_b32 v31, a127\n\t" \
  "v_mfma_f32_16x16x32_bf16 a[56:59], v[88:91], v[72:75], a[56:59]\n\t" \
  "buffer_load_dwordx4 v241, %[wo], s97 offen offset:1024 lds\n\t" \
  "v_mfma_f32_16x16x32_bf16 a[56:59], v[84:87], v[68:71], a[56:59]\n\t" \
  "s_add_u32 s97, s97, 7168\n\t" \
  "v_mul_f32 v24, v24, v214\n\t" \
  "v_mul_f32 v25, v25, v214\n\t" \
  "v_mfma_f32_16x16x32_bf16 a[56:59], v[80:83], v[64:67], a[56:59]\n\t" \
  "v_mul_f32 v26, v26, v214\n\t" \
  "v_mul_f32 v27, v27, v214\n\t" \
  "v_mfma_f32_16x16x32_bf16 a[72:75], v[112:115], v[64:67], 0\n\t" \
  "buffer_load_dwordx4 v240, %[wo], s97 offen offset:2048 lds\n\t" \
  "v_mul_f32 v28, v28, v214\n\t" \
  "v_mul_f32 v29, v29, v214\n\t" \
  "v_mul_f32 v30, v30, v214\n\t" \
  "v_mul_f32 v31, v31, v214\n\t" \
  "v_cvt_pk_bf16_f32 v172, v24, v25\n\t" \
  "v_cvt_pk_bf16_f32 v173, v26, v27\n\t" \
  "v_cvt_pk_bf16_f32 v174, v28, v29\n\t" \
  "v_cvt_pk_bf16_f32 v175, v30, v31\n\t" \
  "ds_write_b128 v213, v[172:175] offset:35840\n\t" \
  "v_mfma_f32_16x16x32_bf16 a[72:75], v[116:119], v[68:71], a[72:75]\n\t" \
  "s_add_u32 s97, s97, 7168\n\t" \
  "v_mfma_f32_16x16x32_bf16 a[72:75], v[120:123], v[72:75], a[72:75]\n\t" \
  "v_mfma_f32_16x16x32_bf16 a[72:75], v[124:127], v[76:79], a[72:75]\n\t" \
  "buffer_load_dwordx4 v241, %[wo], s97 offen offset:3072 lds\n\t" \
  STS("s_memrealtime s[94:95]\n\t") \
  STS("s_memtime s[92:93]\n\t") \
  STS("s_waitcnt lgkmcnt(0)\n\t") \
  STS("v_writelane_b32 %[stv], s94, 22\n\t") \
  STS("v_writelane_b32 %[stv], s92, 23\n\t") \
  ".subsection 1\n\t" \
  "s_endpgm\n\t" \
  ".Lat_v:\n\t" \
  "s_mov_b32 s98, 0\n\t" \
  ".Lat_v1:\n\t" \
  "s_sleep 4\n\t" \
  "buffer_load_dword v209, v205, %[ws], 0 offen sc1\n\t" \
  "s_waitcnt vmcnt(0)\n\t" \
  "v_and_b32 v206, 0xbfffbfff, v209\n\t" \
  "v_cmp_ne_u32_e64 s[96:97], %[tag], v206\n\t" \
  "s_cmp_lg_u64 s[96:97], 0\n\t" \
  "s_cbranch_scc0 .Lat_w\n\t" \
  "s_add_u32 s98, s98, 1\n\t" \
  "s_cmp_lt_u32 s98, %[cap]\n\t" \
  "s_cbranch_scc1 .Lat_v1\n\t" \
  "v_mov_b32 v206, %[tag]\n\t" \
  "buffer_store_dword v206, off, %[ws], 0 offset:256 sc1\n\t" \
  "s_branch .Lat_w\n\t" \
  ".Lat_x0:\n\t" \
  "v_lshlrev_b32 v209, 12, v233\n\t" \
  "v_bfrev_b32 v254, 1\n\t" \
  "s_mov_b64 exec, 15\n\t" \
  "ds_write_b32 v209, v243 offset:49152\n\t" \
  "s_mov_b64 exec, -1\n\t" \
  "s_waitcnt lgkmcnt(0)\n\t" \
  "s_branch .Lat_y0\n\t" \
  ".Lat_x1:\n\t" \
  "v_lshlrev_b32 v209, 12, v233\n\t" \
  "v_bfrev_b32 v254, 1\n\t" \
  "s_mov_b64 exec, 15\n\t" \
  "ds_write_b32 v209, v243 offset:49152\n\t" \
  "s_mov_b64 exec, -1\n\t" \
  "s_waitcnt lgkmcnt(0)\n\t" \
  "s_branch .Lat_y1\n\t" \
  ".Lat_x2:\n\t" \
  "v_lshlrev_b32 v209, 12, v233\n\t" \
  "v_bfrev_b32 v254, 1\n\t" \
  "s_mov_b64 exec, 15\n\t" \
  "ds_write_b32 v209, v243 offset:49152\n\t" \
  "s_mov_b64 exec, -1\n\t" \
  "s_waitcnt lgkmcnt(0)\n\t" \
  "s_branch .Lat_y2\n\t" \
  ".Lat_x3:\n\t" \
  "v_lshlrev_b32 v209, 12, v233\n\t" \
  "v_bfrev_b32 v254, 1\n\t" \
  "s_mov_b64 exec, 15\n\t" \
  "ds_write_b32 v209, v243 offset:49152\n\t" \
  "s_mov_b64 exec, -1\n\t" \
  "s_waitcnt lgkmcnt(0)\n\t" \
  "s_branch .Lat_y3\n\t" \
  ".subsection 2\n\t" \
  ".Lsf_go:\n\t" \
  "v_mfma_f32_16x16x32_bf16 v[28:31], a[156:159], a[224:227], v[28:31]\n\t" \
  "v_mov_b32 v216, 0x3f803f80\n\t" \
  "v_lshlrev_b32 v204, 7, v233\n\t" \
  "v_mfma_f32_16x16x32_bf16 v[28:31], a[92:95], a[240:243], v[28:31]\n\t" \
  "v_cmp_gt_u32 vcc, 16, v233\n\t" \
  "v_lshrrev_b32 v202, 3, v233\n\t" \
  "v_mfma_f32_16x16x32_bf16 v[24:27], a[152:155], a[224:227], v[24:27]\n\t" \
  "v_mul_u32_u24 v202, 11264, v202\n\t" \
  "v_add_u32 v202, v202, v204\n\t" \
  "v_mfma_f32_16x16x32_bf16 v[24:27], a[88:91], a[240:243], v[24:27]\n\t" \
  "v_bfrev_b32 v195, 1\n\t" \
  "v_mov_b32 v217, 0x3f803f80\n\t" \
  "v_mfma_f32_16x16x32_bf16 v[20:23], a[148:151], a[224:227], v[20:23]\n\t" \
  "v_cvt_pk_bf16_f32 v31, v30, v31\n\t" \
  "v_cvt_pk_bf16_f32 v30, v28, v29\n\t" \
  "v_mfma_f32_16x16x32_bf16 v[20:23], a[84:87], a[240:243], v[20:23]\n\t" \
  "v_cndmask_b32 v204, v195, v204, vcc\n\t" \
  "v_cndmask_b32 v202, v195, v202, vcc\n\t" \
  "v_mfma_f32_16x16x32_bf16 v[16:19], a[144:147], a[224:227], v[16:19]\n\t" \
  "v_cvt_pk_bf16_f32 v28, v24, v25\n\t" \
  "v_cvt_pk_bf16_f32 v29, v26, v27\n\t" \
  "v_mfma_f32_16x16x32_bf16 v[16:19], a[80:83], a[240:243], v[16:19]\n\t" \
  "v_permlane16_swap_b32 v28, v30\n\t" \
  "v_permlane16_swap_b32 v29, v31\n\t" \
  "v_mfma_f32_16x16x32_bf16 v[12:15], a[140:143], a[224:227], v[12:15]\n\t" \
  "v_cvt_pk_bf16_f32 v27, v22, v23\n\t" \
  "v_cvt_pk_bf16_f32 v26, v20, v21\n\t" \
  "v_mfma_f32_16x16x32_bf16 v[12:15], a[76:79], a[240:243], v[12:15]\n\t" \
  "v_mov_b32 v218, 0x3f803f80\n\t" \
  "v_mov_b32 v219, 0x3f803f80\n\t" \
  "v_mfma_f32_16x16x32_bf16 v[8:11], a[136:139], a[224:227], v[8:11]\n\t" \
  "v_cvt_pk_bf16_f32 v24, v16, v17\n\t" \
  "v_cvt_pk_bf16_f32 v25, v18, v19\n\t" \
  "v_mfma_f32_16x16x32_bf16 v[8:11], a[72:75], a[240:243], v[8:11]\n\t" \
  "v_permlane16_swap_b32 v24, v26\n\t" \
  "v_permlane16_swap_b32 v25, v27\n\t" \
  "v_mfma_f32_16x16x32_bf16 v[4:7], a[132:135], a[224:227], v[4:7]\n\t" \
  "v_cvt_pk_bf16_f32 v23, v14, v15\n\t" \
  "v_cvt_pk_bf16_f32 v22, v12, v13\n\t" \
  "v_mfma_f32_16x16x32_bf16 v[4:7], a[68:71], a[240:243], v[4:7]\n\t" \
  "v_mov_b32 v196, 1.0\n\t" \
  "v_mov_b32 v197, 1.0\n\t" \
  "v_mfma_f32_16x16x32_bf16 v[0:3], a[128:131], a[224:227], v[0:3]\n\t" \
  "v_cvt_pk_bf16_f32 v20, v8, v9\n\t" \
  "v_cvt_pk_bf16_f32 v21, v10, v11\n\t" \
  "v_mfma_f32_16x16x32_bf16 v[0:3], a[64:67], a[240:243], v[0:3]\n\t" \
  "v_permlane16_swap_b32 v20, v22\n\t" \
  "v_permlane16_swap_b32 v21, v23\n\t" \
  "s_waitcnt vmcnt(2)\n\t" \
  "s_barrier\n\t" \
  "v_mfma_f32_16x16x32_bf16 v[60:63], a[156:159], a[228:231], v[60:63]\n\t" \
  "v_cvt_pk_bf16_f32 v19, v6, v7\n\t" \
  "v_cvt_pk_bf16_f32 v18, v4, v5\n\t" \
  "v_mfma_f32_16x16x32_bf16 v[60:63], a[92:95], a[244:247], v[60:63]\n\t" \
  "v_cvt_pk_bf16_f32 v16, v0, v1\n\t" \
  "v_cvt_pk_bf16_f32 v17, v2, v3\n\t" \
  "v_mfma_f32_16x16x32_bf16 v[56:59], a[152:155], a[228:231], v[56:59]\n\t" \
  "v_permlane16_swap_b32 v16, v18\n\t" \
  "v_permlane16_swap_b32 v17, v19\n\t" \
  "v_mfma_f32_16x16x32_bf16 v[56:59], a[88:91], a[244:247], v[56:59]\n\t" \
  "v_mov_b32 v198, 1.0\n\t" \
  "v_mov_b32 v199, 1.0\n\t" \
  "v_mfma_f32_16x16x32_bf16 v[52:55], a[148:151], a[228:231], v[52:55]\n\t" \
  "v_cvt_pk_bf16_f32 v63, v62, v63\n\t" \
  "v_cvt_pk_bf16_f32 v62, v60, v61\n\t" \
  "v_mfma_f32_16x16x32_bf16 v[52:55], a[84:87], a[244:247], v[52:55]\n\t" \
  "v_lshlrev_b32 v242, 4, v233\n\t" \
  "s_mov_b32 s99, 0\n\t" \
  "v_mfma_f32_16x16x32_bf16 v[48:51], a[144:147], a[228:231], v[48:51]\n\t" \
  "v_cvt_pk_bf16_f32 v60, v56, v57\n\t" \
  "v_cvt_pk_bf16_f32 v61, v58, v59\n\t" \
  "v_mfma_f32_16x16x32_bf16 v[48:51], a[80:83], a[244:247], v[48:51]\n\t" \
  "v_permlane16_swap_b32 v60, v62\n\t" \
  "v_permlane16_swap_b32 v61, v63\n\t" \
  "v_mfma_f32_16x16x32_bf16 v[44:47], a[140:143], a[228:231], v[44:47]\n\t" \
  "v_cvt_pk_bf16_f32 v59, v54, v55\n\t" \
  "v_cvt_pk_bf16_f32 v58, v52, v53\n\t" \
  "v_mfma_f32_16x16x32_bf16 v[44:47], a[76:79], a[244:247], v[44:47]\n\t" \
  "v_add_u32 v237, 65536, v228\n\t" \
  "v_add_u32 v238, 65536, v229\n\t" \
  "v_mfma_f32_16x16x32_bf16 v[40:43], a[136:139], a[228:231], v[40:43]\n\t" \
  "v_cvt_pk_bf16_f32 v56, v48, v49\n\t" \
  "v_cvt_pk_bf16_f32 v57, v50, v51\n\t" \
  "v_mfma_f32_16x16x32_bf16 v[40:43], a[72:75], a[244:247], v[40:43]\n\t" \
  "v_permlane16_swap_b32 v56, v58\n\t" \
  "v_permlane16_swap_b32 v57, v59\n\t" \
  "v_mfma_f32_16x16x32_bf16 v[36:39], a[132:135], a[228:231], v[36:39]\n\t" \
  "v_cvt_pk_bf16_f32 v55, v46, v47\n\t" \
  "v_cvt_pk_bf16_f32 v54, v44, v45\n\t" \
  "v_mfma_f32_16x16x32_bf16 v[36:39], a[68:71], a[244:247], v[36:39]\n\t" \
  "ds_read_b128 a[0:3], v220 offset:0\n\t" \
  "ds_read_b128 a[4:7], v221 offset:0\n\t" \
  "v_mfma_f32_16x16x32_bf16 v[32:35], a[128:131], a[228:231], v[32:35]\n\t" \
  "v_cvt_pk_bf16_f32 v52, v40, v41\n\t" \
  "v_cvt_pk_bf16_f32 v53, v42, v43\n\t" \
  "v_mfma_f32_16x16x32_bf16 v[32:35], a[64:67], a[244:247], v[32:35]\n\t" \
  "v_permlane16_swap_b32 v52, v54\n\t" \
  "v_permlane16_swap_b32 v53, v55\n\t" \
  "v_mfma_f32_16x16x32_bf16 v[92:95], a[156:159], a[232:235], v[92:95]\n\t" \
  "v_cvt_pk_bf16_f32 v51, v38, v39\n\t" \
  "v_cvt_pk_bf16_f32 v50, v36, v37\n\t" \
  "v_mfma_f32_16x16x32_bf16 v[92:95], a[92:95], a[248:251], v[92:95]\n\t" \
  "ds_read_b128 a[8:11], v222 offset:0\n\t" \
  "ds_read_b128 a[12:15], v223 offset:0\n\t" \
  "v_mfma_f32_16x16x32_bf16 v[88:91], a[152:155], a[232:235], v[88:91]\n\t" \
  "v_cvt_pk_bf16_f32 v48, v32, v33\n\t" \
  "v_cvt_pk_bf16_f32 v49, v34, v35\n\t" \
  "v_mfma_f32_16x16x32_bf16 v[88:91], a[88:91], a[248:251], v[88:91]\n\t" \
  "v_permlane16_swap_b32 v48, v50\n\t" \
  "v_permlane16_swap_b32 v49, v51\n\t" \
  "v_mfma_f32_16x16x32_bf16 v[84:87], a[148:151], a[232:235], v[84:87]\n\t" \
  "v_cvt_pk_bf16_f32 v95, v94, v95\n\t" \
  "v_cvt_pk_bf16_f32 v94, v92, v93\n\t" \
  "v_mfma_f32_16x16x32_bf16 v[84:87], a[84:87], a[248:251], v[84:87]\n\t" \
  "ds_read_b128 a[16:19], v220 offset:4096\n\t" \
  "ds_read_b128 a[20:23], v221 offset:4096\n\t" \
  "v_mfma_f32_16x16x32_bf16 v[80:83], a[144:147], a[232:235], v[80:83]\n\t" \
  "v_cvt_pk_bf16_f32 v92, v88, v89\n\t" \
  "v_cvt_pk_bf16_f32 v93, v90, v91\n\t" \
  "v_mfma_f32_16x16x32_bf16 v[80:83], a[80:83], a[248:251], v[80:83]\n\t" \
  "v_permlane16_swap_b32 v92, v94\n\t" \
  "v_permlane16_swap_b32 v93, v95\n\t" \
  "v_mfma_f32_16x16x32_bf16 v[76:79], a[140:143], a[232:235], v[76:79]\n\t" \
  "v_cvt_pk_bf16_f32 v91, v86, v87\n\t" \
  "v_cvt_pk_bf16_f32 v90, v84, v85\n\t" \
  "v_mfma_f32_16x16x32_bf16 v[76:79], a[76:79], a[248:251], v[76:79]\n\t" \
  "ds_read_b128 a[24:27], v222 offset:4096\n\t" \
  "ds_read_b128 a[28:31], v223 offset:4096\n\t" \
  "v_mfma_f32_16x16x32_bf16 v[72:75], a[136:139], a[232:235], v[72:75]\n\t" \
  "v_cvt_pk_bf16_f32 v88, v80, v81\n\t" \
  "v_cvt_pk_bf16_f32 v89, v82, v83\n\t" \
  "v_mfma_f32_16x16x32_bf16 v[72:75], a[72:75], a[248:251], v[72:75]\n\t" \
  "v_permlane16_swap_b32 v88, v90\n\t" \
  "v_permlane16_swap_b32 v89, v91\n\t" \
  "v_mfma_f32_16x16x32_bf16 v[68:71], a[132:135], a[232:235], v[68:71]\n\t" \
  "v_cvt_pk_bf16_f32 v87, v78, v79\n\t" \
  "v_cvt_pk_bf16_f32 v86, v76, v77\n\t" \
  "v_mfma_f32_16x16x32_bf16 v[68:71], a[68:71], a[248:251], v[68:71]\n\t" \
  "ds_read_b128 a[32:35], v220 offset:8192\n\t" \
  "ds_read_b128 a[36:39], v221 offset:8192\n\t" \
  "s_add_u32 s98, %[pf4], 0\n\t" \
  "buffer_load_dword %[pfd], v245, %[sU], s98 offen\n\t" \
  "v_mfma_f32_16x16x32_bf16 v[64:67], a[128:131], a[232:235], v[64:67]\n\t" \
  "v_cvt_pk_bf16_f32 v84, v72, v73\n\t" \
  "v_cvt_pk_bf16_f32 v85, v74, v75\n\t" \
  "v_mfma_f32_16x16x32_bf16 v[64:67], a[64:67], a[248:251], v[64:67]\n\t" \
  "v_cvt_pk_bf16_f32 v83, v70, v71\n\t" \
  "v_cvt_pk_bf16_f32 v82, v68, v69\n\t" \
  "v_mfma_f32_16x16x32_bf16 v[124:127], a[156:159], a[236:239], v[124:127]\n\t" \
  "v_permlane16_swap_b32 v84, v86\n\t" \
  "v_permlane16_swap_b32 v85, v87\n\t" \
  "v_mfma_f32_16x16x32_bf16 v[124:127], a[92:95], a[252:255], v[124:127]\n\t" \
  "ds_read_b128 a[40:43], v222 offset:8192\n\t" \
  "ds_read_b128 a[44:47], v223 offset:8192\n\t" \
  "v_mfma_f32_16x16x32_bf16 v[120:123], a[152:155], a[236:239], v[120:123]\n\t" \
  "v_cvt_pk_bf16_f32 v80, v64, v65\n\t" \
  "v_cvt_pk_bf16_f32 v81, v66, v67\n\t" \
  "v_mfma_f32_16x16x32_bf16 v[120:123], a[88:91], a[252:255], v[120:123]\n\t" \
  "v_permlane16_swap_b32 v80, v82\n\t" \
  "v_permlane16_swap_b32 v81, v83\n\t" \
  "v_mfma_f32_16x16x32_bf16 v[116:119], a[148:151], a[236:239], v[116:119]\n\t" \
  "v_cvt_pk_bf16_f32 v127, v126, v127\n\t" \
  "v_cvt_pk_bf16_f32 v126, v124, v125\n\t" \
  "v_mfma_f32_16x16x32_bf16 v[116:119], a[84:87], a[252:255], v[116:119]\n\t" \
  "ds_read_b128 a[48:51], v220 offset:12288\n\t" \
  "ds_read_b128 a[52:55], v221 offset:12288\n\t" \
  "v_mfma_f32_16x16x32_bf16 v[112:115], a[144:147], a[236:239], v[112:115]\n\t" \
  "v_cvt_pk_bf16_f32 v124, v120, v121\n\t" \
  "v_cvt_pk_bf16_f32 v125, v122, v123\n\t" \
  "v_mfma_f32_16x16x32_bf16 v[112:115], a[80:83], a[252:255], v[112:115]\n\t" \
  "v_permlane16_swap_b32 v124, v126\n\t" \
  "v_permlane16_swap_b32 v125, v127\n\t" \
  "ds_read_b128 a[56:59], v222 offset:12288\n\t" \
  "ds_read_b128 a[60:63], v223 offset:12288\n\t" \
  "v_mfma_f32_16x16x32_bf16 v[108:111], a[140:143], a[236:239], v[108:111]\n\t" \
  "v_cvt_pk_bf16_f32 v123, v118, v119\n\t" \
  "v_cvt_pk_bf16_f32 v122, v116, v117\n\t" \
  "v_mfma_f32_16x16x32_bf16 v[108:111], a[76:79], a[252:255], v[108:111]\n\t" \
  "v_cvt_pk_bf16_f32 v120, v112, v113\n\t" \
  "v_cvt_pk_bf16_f32 v121, v114, v115\n\t" \
  "v_mfma_f32_16x16x32_bf16 v[104:107], a[136:139], a[236:239], v[104:107]\n\t" \
  "v_permlane16_swap_b32 v120, v122\n\t" \
  "v_permlane16_swap_b32 v121, v123\n\t" \
  "v_mfma_f32_16x16x32_bf16 v[104:107], a[72:75], a[252:255], v[104:107]\n\t" \
  "v_mfma_f32_16x16x32_bf16 v[100:103], a[132:135], a[236:239], v[100:103]\n\t" \
  "v_mfma_f32_16x16x32_bf16 v[100:103], a[68:71], a[252:255], v[100:103]\n\t" \
  "v_cvt_pk_bf16_f32 v119, v110, v111\n\t" \
  "v_cvt_pk_bf16_f32 v118, v108, v109\n\t" \
  "v_mfma_f32_16x16x32_bf16 v[96:99], a[128:131], a[236:239], v[96:99]\n\t" \
  "v_mfma_f32_16x16x32_bf16 v[96:99], a[64:67], a[252:255], v[96:99]\n\t" \
  "s_waitcnt lgkmcnt(0)\n\t" \
  "s_barrier\n\t" \
  "v_mfma_f32_16x16x32_bf16 v[0:3], a[0:3], v[16:19], 0\n\t" \
  "v_cvt_pk_bf16_f32 v116, v104, v105\n\t" \
  "v_cvt_pk_bf16_f32 v117, v106, v107\n\t" \
  "v_mfma_f32_16x16x32_bf16 v[0:3], a[4:7], v[20:23], v[0:3]\n\t" \
  "v_cvt_pk_bf16_f32 v115, v102, v103\n\t" \
  "v_cvt_pk_bf16_f32 v114, v100, v101\n\t" \
  "v_mfma_f32_16x16x32_bf16 v[0:3], a[8:11], v[24:27], v[0:3]\n\t" \
  "v_cvt_pk_bf16_f32 v112, v96, v97\n\t" \
  "v_cvt_pk_bf16_f32 v113, v98, v99\n\t" \
  "v_mfma_f32_16x16x32_bf16 v[0:3], a[12:15], v[28:31], v[0:3]\n\t" \
  "v_permlane16_swap_b32 v116, v118\n\t" \
  "v_permlane16_swap_b32 v117, v119\n\t" \
  "v_mfma_f32_16x16x32_bf16 v[32:35], a[0:3], v[48:51], 0\n\t" \
  "v_permlane16_swap_b32 v112, v114\n\t" \
  "v_permlane16_swap_b32 v113, v115\n\t" \
  "v_mfma_f32_16x16x32_bf16 v[32:35], a[4:7], v[52:55], v[32:35]\n\t" \
  "ds_read_b128 a[192:195], v220 offset:16384\n\t" \
  "s_add_u32 m0, %[m], 49152\n\t" \
  "v_mfma_f32_16x16x32_bf16 v[32:35], a[8:11], v[56:59], v[32:35]\n\t" \
  "ds_read_b128 a[196:199], v221 offset:16384\n\t" \
  "v_max3_f32 v194, v0, v1, v2\n\t" \
  "v_mfma_f32_16x16x32_bf16 v[32:35], a[12:15], v[60:63], v[32:35]\n\t" \
  "v_max_f32 v194, v194, v3\n\t" \
  "v_mov_b32 v195, v194\n\t" \
  "s_nop 1\n\t" \
  "v_permlane32_swap_b32 v194, v195\n\t" \
  "v_max_f32 v194, v194, v195\n\t" \
  "v_mov_b32 v195, v194\n\t" \
  "s_nop 1\n\t" \
  "v_permlane16_swap_b32 v194, v195\n\t" \
  "v_max_f32 v194, v194, v195\n\t" \
  "v_sub_f32 v176, 0xc2000000, v194\n\t" \
  "v_sub_f32 v177, 0xc2000000, v194\n\t" \
  "v_sub_f32 v178, 0xc2000000, v194\n\t" \
  "v_sub_f32 v179, 0xc2000000, v194\n\t" \
  "s_nop 1\n\t" \
  "v_mfma_f32_16x16x32_bf16 v[4:7], a[16:19], v[16:19], v[176:179]\n\t" \
  "v_add_f32 v0, v0, v176\n\t" \
  "v_add_f32 v1, v1, v176\n\t" \
  "v_mfma_f32_16x16x32_bf16 v[4:7], a[20:23], v[20:23], v[4:7]\n\t" \
  "v_add_f32 v2, v2, v176\n\t" \
  "v_add_f32 v3, v3, v176\n\t" \
  "v_mfma_f32_16x16x32_bf16 v[4:7], a[24:27], v[24:27], v[4:7]\n\t" \
  "ds_read_b128 a[200:203], v222 offset:16384\n\t" \
  "s_add_u32 s96, %[sofk], 32768\n\t" \
  "v_mfma_f32_16x16x32_bf16 v[4:7], a[28:31], v[28:31], v[4:7]\n\t" \
  "ds_read_b128 a[204:207], v223 offset:16384\n\t" \
  "v_max3_f32 v194, v32, v33, v34\n\t" \
  "v_mfma_f32_16x16x32_bf16 v[8:11], a[32:35], v[16:19], v[176:179]\n\t" \
  "v_max_f32 v194, v194, v35\n\t" \
  "v_mov_b32 v195, v194\n\t" \
  "v_mfma_f32_16x16x32_bf16 v[8:11], a[36:39], v[20:23], v[8:11]\n\t" \
  "ds_read_b128 a[208:211], v220 offset:20480\n\t" \
  "v_permlane32_swap_b32 v194, v195\n\t" \
  "v_mfma_f32_16x16x32_bf16 v[8:11], a[40:43], v[24:27], v[8:11]\n\t" \
  "v_max_f32 v194, v194, v195\n\t" \
  "v_mov_b32 v195, v194\n\t" \
  "v_mfma_f32_16x16x32_bf16 v[8:11], a[44:47], v[28:31], v[8:11]\n\t" \
  "ds_read_b128 a[212:215], v221 offset:20480\n\t" \
  "v_permlane16_swap_b32 v194, v195\n\t" \
  "v_mfma_f32_16x16x32_bf16 v[12:15], a[48:51], v[16:19], v[176:179]\n\t" \
  "v_max_f32 v194, v194, v195\n\t" \
  "v_sub_f32 v180, 0xc2000000, v194\n\t" \
  "v_mfma_f32_16x16x32_bf16 v[12:15], a[52:55], v[20:23], v[12:15]\n\t" \
  "v_sub_f32 v181, 0xc2000000, v194\n\t" \
  "v_sub_f32 v182, 0xc2000000, v194\n\t" \
  "v_mfma_f32_16x16x32_bf16 v[12:15], a[56:59], v[24:27], v[12:15]\n\t" \
  "v_sub_f32 v183, 0xc2000000, v194\n\t" \
  "v_add_f32 v32, v32, v180\n\t" \
  "v_mfma_f32_16x16x32_bf16 v[12:15], a[60:63], v[28:31], v[12:15]\n\t" \
  "v_add_f32 v33, v33, v180\n\t" \
  "v_add_f32 v34, v34, v180\n\t" \
  "v_mfma_f32_16x16x32_bf16 v[64:67], a[0:3], v[80:83], 0\n\t" \
  "v_add_f32 v35, v35, v180\n\t" \
  "v_max3_f32 v192, v4, v5, v6\n\t" \
  "v_mfma_f32_16x16x32_bf16 v[64:67], a[4:7], v[84:87], v[64:67]\n\t" \
  "buffer_load_dwordx4 v224, %[sK], s96 offen offset:0 lds\n\t" \
  "ds_read_b128 a[216:219], v222 offset:20480\n\t" \
  "v_mfma_f32_16x16x32_bf16 v[64:67], a[8:11], v[88:91], v[64:67]\n\t" \
  "ds_read_b128 a[220:223], v223 offset:20480\n\t" \
  "v_max3_f32 v203, v7, v8, v9\n\t" \
  "v_mfma_f32_16x16x32_bf16 v[64:67], a[12:15], v[92:95], v[64:67]\n\t" \
  "v_max3_f32 v192, v192, v10, v11\n\t" \
  "ds_read_b128 a[224:227], v220 offset:24576\n\t" \
  "v_mfma_f32_16x16x32_bf16 v[36:39], a[16:19], v[48:51], v[180:183]\n\t" \
  "s_add_u32 s98, %[pf4], 14336\n\t" \
  "buffer_load_dword %[pfd], v245, %[sU], s98 offen\n\t" \
  "v_mfma_f32_16x16x32_bf16 v[36:39], a[20:23], v[52:55], v[36:39]\n\t" \
  "buffer_load_dwordx4 v225, %[sK], s96 offen offset:1024 lds\n\t" \
  "ds_read_b128 a[228:231], v221 offset:24576\n\t" \
  "v_mfma_f32_16x16x32_bf16 v[36:39], a[24:27], v[56:59], v[36:39]\n\t" \
  "v_max3_f32 v203, v203, v12, v13\n\t" \
  "v_max3_f32 v192, v192, v14, v15\n\t" \
  "v_mfma_f32_16x16x32_bf16 v[36:39], a[28:31], v[60:63], v[36:39]\n\t" \
  "v_max_f32 v192, v192, v203\n\t" \
  "ds_read_b128 a[232:235], v222 offset:24576\n\t" \
  "v_mfma_f32_16x16x32_bf16 v[40:43], a[32:35], v[48:51], v[180:183]\n\t" \
  "v_cmp_lt_f32 vcc, 0x42800000, v192\n\t" \
  "v_max3_f32 v194, v64, v65, v66\n\t" \
  "v_mfma_f32_16x16x32_bf16 v[40:43], a[36:39], v[52:55], v[40:43]\n\t" \
  "s_cbranch_vccnz .Lsf_c00\n\t" \
  ".Lsf_r00:\n\t" \
  "v_max_f32 v194, v194, v67\n\t" \
  "v_mfma_f32_16x16x32_bf16 v[40:43], a[40:43], v[56:59], v[40:43]\n\t" \
  "v_mov_b32 v195, v194\n\t" \
  "ds_read_b128 a[236:239], v223 offset:24576\n\t" \
  "v_mfma_f32_16x16x32_bf16 v[40:43], a[44:47], v[60:63], v[40:43]\n\t" \
  "v_permlane32_swap_b32 v194, v195\n\t" \
  "v_max_f32 v194, v194, v195\n\t" \
  "v_mfma_f32_16x16x32_bf16 v[44:47], a[48:51], v[48:51], v[180:183]\n\t" \
  "v_mov_b32 v195, v194\n\t" \
  "buffer_load_dwordx4 v226, %[sK], s96 offen offset:2048 lds\n\t" \
  "v_mfma_f32_16x16x32_bf16 v[44:47], a[52:55], v[52:55], v[44:47]\n\t" \
  "v_permlane16_swap_b32 v194, v195\n\t" \
  "v_max_f32 v194, v194, v195\n\t" \
  "v_mfma_f32_16x16x32_bf16 v[44:47], a[56:59], v[56:59], v[44:47]\n\t" \
  "v_sub_f32 v184, 0xc2000000, v194\n\t" \
  "v_sub_f32 v185, 0xc2000000, v194\n\t" \
  "v_mfma_f32_16x16x32_bf16 v[44:47], a[60:63], v[60:63], v[44:47]\n\t" \
  "v_sub_f32 v186, 0xc2000000, v194\n\t" \
  "v_sub_f32 v187, 0xc2000000, v194\n\t" \
  "v_mfma_f32_16x16x32_bf16 v[96:99], a[0:3], v[112:115], 0\n\t" \
  "v_add_f32 v64, v64, v184\n\t" \
  "v_add_f32 v65, v65, v184\n\t" \
  "v_mfma_f32_16x16x32_bf16 v[96:99], a[4:7], v[116:119], v[96:99]\n\t" \
  "v_add_f32 v66, v66, v184\n\t" \
  "v_add_f32 v67, v67, v184\n\t" \
  "v_mfma_f32_16x16x32_bf16 v[96:99], a[8:11], v[120:123], v[96:99]\n\t" \
  "ds_read_b128 a[240:243], v220 offset:28672\n\t" \
  "ds_read_b128 a[244:247], v221 offset:28672\n\t" \
  "v_mfma_f32_16x16x32_bf16 v[96:99], a[12:15], v[124:127], v[96:99]\n\t" \
  "v_max3_f32 v192, v36, v37, v38\n\t" \
  "ds_read_b128 a[248:251], v222 offset:28672\n\t" \
  "v_mfma_f32_16x16x32_bf16 v[68:71], a[16:19], v[80:83], v[184:187]\n\t" \
  "buffer_load_dwordx4 v227, %[sK], s96 offen offset:3072 lds\n\t" \
  "ds_read_b128 a[252:255], v223 offset:28672\n\t" \
  "v_mfma_f32_16x16x32_bf16 v[68:71], a[20:23], v[84:87], v[68:71]\n\t" \
  "v_max3_f32 v203, v39, v40, v41\n\t" \
  "v_max3_f32 v192, v192, v42, v43\n\t" \
  "v_mfma_f32_16x16x32_bf16 v[68:71], a[24:27], v[88:91], v[68:71]\n\t" \
  "ds_read_b64_tr_b16 a[128:129], v228 offset:0\n\t" \
  "v_max3_f32 v203, v203, v44, v45\n\t" \
  "v_mfma_f32_16x16x32_bf16 v[68:71], a[28:31], v[92:95], v[68:71]\n\t" \
  "v_max3_f32 v192, v192, v46, v47\n\t" \
  "v_max_f32 v192, v192, v203\n\t" \
  "v_mfma_f32_16x16x32_bf16 v[72:75], a[32:35], v[80:83], v[184:187]\n\t" \
  "v_cmp_lt_f32 vcc, 0x42800000, v192\n\t" \
  "s_add_u32 m0, %[m], 147456\n\t" \
  "v_mfma_f32_16x16x32_bf16 v[72:75], a[36:39], v[84:87], v[72:75]\n\t" \
  "s_cbranch_vccnz .Lsf_c01\n\t" \
  ".Lsf_r01:\n\t" \
  "ds_read_b64_tr_b16 a[130:131], v228 offset:4096\n\t" \
  "v_mfma_f32_16x16x32_bf16 v[72:75], a[40:43], v[88:91], v[72:75]\n\t" \
  "v_max3_f32 v194, v96, v97, v98\n\t" \
  "v_max_f32 v194, v194, v99\n\t" \
  "v_mfma_f32_16x16x32_bf16 v[72:75], a[44:47], v[92:95], v[72:75]\n\t" \
  "v_mov_b32 v195, v194\n\t" \
  "ds_read_b64_tr_b16 a[132:133], v228 offset:8192\n\t" \
  "v_mfma_f32_16x16x32_bf16 v[76:79], a[48:51], v[80:83], v[184:187]\n\t" \
  "v_permlane32_swap_b32 v194, v195\n\t" \
  "v_max_f32 v194, v194, v195\n\t" \
  "v_mfma_f32_16x16x32_bf16 v[76:79], a[52:55], v[84:87], v[76:79]\n\t" \
  "v_mov_b32 v195, v194\n\t" \
  "s_add_u32 s96, %[sofk], 16384\n\t" \
  "v_mfma_f32_16x16x32_bf16 v[76:79], a[56:59], v[88:91], v[76:79]\n\t" \
  "v_permlane16_swap_b32 v194, v195\n\t" \
  "v_max_f32 v194, v194, v195\n\t" \
  "v_mfma_f32_16x16x32_bf16 v[76:79], a[60:63], v[92:95], v[76:79]\n\t" \
  "v_sub_f32 v188, 0xc2000000, v194\n\t" \
  "v_sub_f32 v189, 0xc2000000, v194\n\t" \
  "v_sub_f32 v190, 0xc2000000, v194\n\t" \
  "v_sub_f32 v191, 0xc2000000, v194\n\t" \
  "s_nop 1\n\t" \
  "v_mfma_f32_16x16x32_bf16 v[100:103], a[16:19], v[112:115], v[188:191]\n\t" \
  "v_add_f32 v96, v96, v188\n\t" \
  "v_add_f32 v97, v97, v188\n\t" \
  "v_mfma_f32_16x16x32_bf16 v[100:103], a[20:23], v[116:119], v[100:103]\n\t" \
  "v_add_f32 v98, v98, v188\n\t" \
  "v_add_f32 v99, v99, v188\n\t" \
  "v_mfma_f32_16x16x32_bf16 v[100:103], a[24:27], v[120:123], v[100:103]\n\t" \
  "ds_read_b64_tr_b16 a[134:135], v228 offset:12288\n\t" \
  "v_max3_f32 v192, v68, v69, v70\n\t" \
  "v_mfma_f32_16x16x32_bf16 v[100:103], a[28:31], v[124:127], v[100:103]\n\t" \
  "ds_read_b64_tr_b16 a[136:137], v229 offset:0\n\t" \
  "v_max3_f32 v203, v71, v72, v73\n\t" \
  "v_mfma_f32_16x16x32_bf16 v[104:107], a[32:35], v[112:115], v[188:191]\n\t" \
  "v_max3_f32 v192, v192, v74, v75\n\t" \
  "buffer_load_dwordx4 v230, %[sV], s96 offen offset:0 lds\n\t" \
  "v_mfma_f32_16x16x32_bf16 v[104:107], a[36:39], v[116:119], v[104:107]\n\t" \
  "ds_read_b64_tr_b16 a[138:139], v229 offset:4096\n\t" \
  "ds_read_b64_tr_b16 a[140:141], v229 offset:8192\n\t" \
  "v_mfma_f32_16x16x32_bf16 v[104:107], a[40:43], v[120:123], v[104:107]\n\t" \
  "v_max3_f32 v203, v203, v76, v77\n\t" \
  "v_max3_f32 v192, v192, v78, v79\n\t" \
  "v_mfma_f32_16x16x32_bf16 v[104:107], a[44:47], v[124:127], v[104:107]\n\t" \
  "v_max_f32 v192, v192, v203\n\t" \
  "ds_read_b64_tr_b16 a[142:143], v229 offset:12288\n\t" \
  "v_mfma_f32_16x16x32_bf16 v[108:111], a[48:51], v[112:115], v[188:191]\n\t" \
  "v_cmp_lt_f32 vcc, 0x42800000, v192\n\t" \
  "buffer_load_dwordx4 v231, %[sV], s96 offen offset:1024 lds\n\t" \
  "v_mfma_f32_16x16x32_bf16 v[108:111], a[52:55], v[116:119], v[108:111]\n\t" \
  "s_cbranch_vccnz .Lsf_c02\n\t" \
  ".Lsf_r02:\n\t" \
  "v_max3_f32 v192, v100, v101, v102\n\t" \
  "v_mfma_f32_16x16x32_bf16 v[108:111], a[56:59], v[120:123], v[108:111]\n\t" \
  "ds_read_b64_tr_b16 a[144:145], v228 offset:256\n\t" \
  "ds_read_b64_tr_b16 a[146:147], v228 offset:4352\n\t" \
  "v_mfma_f32_16x16x32_bf16 v[108:111], a[60:63], v[124:127], v[108:111]\n\t" \
  "v_max3_f32 v203, v103, v104, v105\n\t" \
  "v_max3_f32 v192, v192, v106, v107\n\t" \
  "v_exp_f32 v0, v0\n\t" \
  "v_exp_f32 v1, v1\n\t" \
  "v_exp_f32 v2, v2\n\t" \
  "v_exp_f32 v3, v3\n\t" \
  "v_cvt_pk_bf16_f32 v128, v0, v1\n\t" \
  "v_cvt_pk_bf16_f32 v129, v2, v3\n\t" \
  "s_waitcnt lgkmcnt(10)\n\t" \
  "v_mfma_f32_16x16x32_bf16 v[0:3], a[192:195], v[16:19], v[176:179]\n\t" \
  "buffer_load_dwordx4 v230, %[sV], s96 offen offset:2048 lds\n\t" \
  "ds_read_b64_tr_b16 a[148:149], v228 offset:8448\n\t" \
  "v_mfma_f32_16x16x32_bf16 v[0:3], a[196:199], v[20:23], v[0:3]\n\t" \
  "v_max3_f32 v203, v203, v108, v109\n\t" \
  "v_max3_f32 v192, v192, v110, v111\n\t" \
  "v_mfma_f32_16x16x32_bf16 v[0:3], a[200:203], v[24:27], v[0:3]\n\t" \
  "v_max_f32 v192, v192, v203\n\t" \
  "ds_read_b64_tr_b16 a[150:151], v228 offset:12544\n\t" \
  "v_mfma_f32_16x16x32_bf16 v[0:3], a[204:207], v[28:31], v[0:3]\n\t" \
  "v_cmp_lt_f32 vcc, 0x42800000, v192\n\t" \
  "buffer_load_dwordx4 v231, %[sV], s96 offen offset:3072 lds\n\t" \
  "v_exp_f32 v4, v4\n\t" \
  "v_exp_f32 v5, v5\n\t" \
  "v_exp_f32 v6, v6\n\t" \
  "v_exp_f32 v7, v7\n\t" \
  "v_cvt_pk_bf16_f32 v130, v4, v5\n\t" \
  "v_cvt_pk_bf16_f32 v131, v6, v7\n\t" \
  "v_mfma_f32_16x16x32_bf16 v[4:7], a[220:223], v[28:31], v[176:179]\n\t" \
  "s_cbranch_vccnz .Lsf_c03\n\t" \
  ".Lsf_r03:\n\t" \
  "ds_read_b64_tr_b16 a[152:153], v229 offset:256\n\t" \
  "v_mfma_f32_16x16x32_bf16 v[4:7], a[216:219], v[24:27], v[4:7]\n\t" \
  "s_cmp_lg_u32 s99, 0\n\t" \
  "s_cbranch_scc1 .Lsf_p0\n\t" \
  ".Lsf_q0:\n\t" \
  "v_mfma_f32_16x16x32_bf16 v[4:7], a[212:215], v[20:23], v[4:7]\n\t" \
  "s_add_u32 s98, %[pf4], 28672\n\t" \
  "buffer_load_dword %[pfd], v245, %[sU], s98 offen\n\t" \
  "v_mfma_f32_16x16x32_bf16 v[4:7], a[208:211], v[16:19], v[4:7]\n\t" \
  "v_max3_f32 v192, v0, v1, v2\n\t" \
  "ds_read_b64_tr_b16 a[154:155], v229 offset:4352\n\t" \
  "v_exp_f32 v8, v8\n\t" \
  "v_exp_f32 v9, v9\n\t" \
  "v_exp_f32 v10, v10\n\t" \
  "v_exp_f32 v11, v11\n\t" \
  "v_cvt_pk_bf16_f32 v132, v8, v9\n\t" \
  "v_cvt_pk_bf16_f32 v133, v10, v11\n\t" \
  "v_mfma_f32_16x16x32_bf16 v[8:11], a[224:227], v[16:19], v[176:179]\n\t" \
  "v_exp_f32 v12, v12\n\t" \
  "v_mfma_f32_16x16x32_bf16 v[8:11], a[228:231], v[20:23], v[8:11]\n\t" \
  "v_max3_f32 v203, v3, v4, v5\n\t" \
  "v_max3_f32 v192, v192, v6, v7\n\t" \
  "v_mfma_f32_16x16x32_bf16 v[8:11], a[232:235], v[24:27], v[8:11]\n\t" \
  "v_exp_f32 v13, v13\n\t" \
  "v_mfma_f32_16x16x32_bf16 v[8:11], a[236:239], v[28:31], v[8:11]\n\t" \
  "v_cvt_pk_bf16_f32 v134, v12, v13\n\t" \
  "ds_read_b64_tr_b16 a[156:157], v229 offset:8448\n\t" \
  "v_exp_f32 v14, v14\n\t" \
  "v_exp_f32 v15, v15\n\t" \
  "s_nop 0\n\t" \
  "v_cvt_pk_bf16_f32 v135, v14, v15\n\t" \
  "v_mfma_f32_16x16x32_bf16 v[12:15], a[252:255], v[28:31], v[176:179]\n\t" \
  "v_exp_f32 v32, v32\n\t" \
  "v_mfma_f32_16x16x32_bf16 v[12:15], a[248:251], v[24:27], v[12:15]\n\t" \
  "ds_read_b64_tr_b16 a[158:159], v229 offset:12544\n\t" \
  "v_max3_f32 v203, v203, v8, v9\n\t" \
  "v_mfma_f32_16x16x32_bf16 v[12:15], a[244:247], v[20:23], v[12:15]\n\t" \
  "v_max3_f32 v192, v192, v10, v11\n\t" \
  "ds_read_b64_tr_b16 a[160:161], v228 offset:512\n\t" \
  "v_mfma_f32_16x16x32_bf16 v[12:15], a[240:243], v[16:19], v[12:15]\n\t" \
  "v_exp_f32 v33, v33\n\t" \
  "v_exp_f32 v34, v34\n\t" \
  "v_exp_f32 v35, v35\n\t" \
  "v_cvt_pk_bf16_f32 v136, v32, v33\n\t" \
  "v_cvt_pk_bf16_f32 v137, v34, v35\n\t" \
  "v_mfma_f32_16x16x32_bf16 v[32:35], a[192:195], v[48:51], v[180:183]\n\t" \
  "v_exp_f32 v36, v36\n\t" \
  "v_mfma_f32_16x16x32_bf16 v[32:35], a[196:199], v[52:55], v[32:35]\n\t" \
  "v_max3_f32 v203, v203, v12, v13\n\t" \
  "v_max3_f32 v192, v192, v14, v15\n\t" \
  "v_mfma_f32_16x16x32_bf16 v[32:35], a[200:203], v[56:59], v[32:35]\n\t" \
  "v_max_f32 v192, v192, v203\n\t" \
  "ds_read_b64_tr_b16 a[162:163], v228 offset:4608\n\t" \
  "v_mfma_f32_16x16x32_bf16 v[32:35], a[204:207], v[60:63], v[32:35]\n\t" \
  "v_cmp_lt_f32 vcc, 0x42800000, v192\n\t" \
  "ds_read_b64_tr_b16 a[164:165], v228 offset:8704\n\t" \
  "v_exp_f32 v37, v37\n\t" \
  "v_exp_f32 v38, v38\n\t" \
  "v_exp_f32 v39, v39\n\t" \
  "v_cvt_pk_bf16_f32 v138, v36, v37\n\t" \
  "v_cvt_pk_bf16_f32 v139, v38, v39\n\t" \
  "v_mfma_f32_16x16x32_bf16 v[36:39], a[220:223], v[60:63], v[180:183]\n\t" \
  "s_cbranch_vccnz .Lsf_c10\n\t" \
  ".Lsf_r10:\n\t" \
  "v_max3_f32 v192, v32, v33, v34\n\t" \
  "v_mfma_f32_16x16x32_bf16 v[36:39], a[216:219], v[56:59], v[36:39]\n\t" \
  "v_exp_f32 v40, v40\n\t" \
  "v_mfma_f32_16x16x32_bf16 v[36:39], a[212:215], v[52:55], v[36:39]\n\t" \
  "s_add_u32 m0, %[m], 0\n\t" \
  "ds_read_b64_tr_b16 a[166:167], v228 offset:12800\n\t" \
  "v_mfma_f32_16x16x32_bf16 v[36:39], a[208:211], v[48:51], v[36:39]\n\t" \
  "v_exp_f32 v41, v41\n\t" \
  "v_exp_f32 v42, v42\n\t" \
  "v_exp_f32 v43, v43\n\t" \
  "v_cvt_pk_bf16_f32 v140, v40, v41\n\t" \
  "v_cvt_pk_bf16_f32 v141, v42, v43\n\t" \
  "v_mfma_f32_16x16x32_bf16 v[40:43], a[224:227], v[48:51], v[180:183]\n\t" \
  "v_exp_f32 v44, v44\n\t" \
  "v_mfma_f32_16x16x32_bf16 v[40:43], a[228:231], v[52:55], v[40:43]\n\t" \
  "v_max3_f32 v203, v35, v36, v37\n\t" \
  "v_max3_f32 v192, v192, v38, v39\n\t" \
  "v_mfma_f32_16x16x32_bf16 v[40:43], a[232:235], v[56:59], v[40:43]\n\t" \
  "v_exp_f32 v45, v45\n\t" \
  "v_mfma_f32_16x16x32_bf16 v[40:43], a[236:239], v[60:63], v[40:43]\n\t" \
  "v_cvt_pk_bf16_f32 v142, v44, v45\n\t" \
  "ds_read_b64_tr_b16 a[168:169], v229 offset:512\n\t" \
  "v_exp_f32 v46, v46\n\t" \
  "v_exp_f32 v47, v47\n\t" \
  "s_nop 0\n\t" \
  "v_cvt_pk_bf16_f32 v143, v46, v47\n\t" \
  "v_mfma_f32_16x16x32_bf16 v[44:47], a[252:255], v[60:63], v[180:183]\n\t" \
  "s_xor_b32 s97, %[wob], 0\n\t" \
  "ds_read_b64_tr_b16 a[170:171], v229 offset:4608\n\t" \
  "v_mfma_f32_16x16x32_bf16 v[44:47], a[248:251], v[56:59], v[44:47]\n\t" \
  "v_exp_f32 v64, v64\n\t" \
  "v_mfma_f32_16x16x32_bf16 v[44:47], a[244:247], v[52:55], v[44:47]\n\t" \
  "v_max3_f32 v203, v203, v40, v41\n\t" \
  "v_max3_f32 v192, v192, v42, v43\n\t" \
  "v_mfma_f32_16x16x32_bf16 v[44:47], a[240:243], v[48:51], v[44:47]\n\t" \
  "v_exp_f32 v65, v65\n\t" \
  "v_exp_f32 v66, v66\n\t" \
  "v_exp_f32 v67, v67\n\t" \
  "v_cvt_pk_bf16_f32 v144, v64, v65\n\t" \
  "v_cvt_pk_bf16_f32 v145, v66, v67\n\t" \
  "v_mfma_f32_16x16x32_bf16 v[64:67], a[192:195], v[80:83], v[184:187]\n\t" \
  "ds_read_b64_tr_b16 a[172:173], v229 offset:8704\n\t" \
  "ds_read_b64_tr_b16 a[174:175], v229 offset:12800\n\t" \
  "v_mfma_f32_16x16x32_bf16 v[64:67], a[196:199], v[84:87], v[64:67]\n\t" \
  "v_exp_f32 v68, v68\n\t" \
  "v_mfma_f32_16x16x32_bf16 v[64:67], a[200:203], v[88:91], v[64:67]\n\t" \
  "v_max3_f32 v203, v203, v44, v45\n\t" \
  "v_max3_f32 v192, v192, v46, v47\n\t" \
  "v_mfma_f32_16x16x32_bf16 v[64:67], a[204:207], v[92:95], v[64:67]\n\t" \
  "v_max_f32 v192, v192, v203\n\t" \
  "buffer_load_dwordx4 v240, %[wo], s97 offen offset:0 lds\n\t" \
  "v_exp_f32 v69, v69\n\t" \
  "v_exp_f32 v70, v70\n\t" \
  "v_exp_f32 v71, v71\n\t" \
  "v_cvt_pk_bf16_f32 v146, v68, v69\n\t" \
  "v_cvt_pk_bf16_f32 v147, v70, v71\n\t" \
  "v_mfma_f32_16x16x32_bf16 v[68:71], a[220:223], v[92:95], v[184:187]\n\t" \
  "v_cmp_lt_f32 vcc, 0x42800000, v192\n\t" \
  "ds_read_b64_tr_b16 a[176:177], v228 offset:768\n\t" \
  "v_mfma_f32_16x16x32_bf16 v[68:71], a[216:219], v[88:91], v[68:71]\n\t" \
  "s_cbranch_vccnz .Lsf_c11\n\t" \
  ".Lsf_r11:\n\t" \
  "v_max3_f32 v192, v64, v65, v66\n\t" \
  "v_mfma_f32_16x16x32_bf16 v[68:71], a[212:215], v[84:87], v[68:71]\n\t" \
  "v_exp_f32 v72, v72\n\t" \
  "v_mfma_f32_16x16x32_bf16 v[68:71], a[208:211], v[80:83], v[68:71]\n\t" \
  "v_exp_f32 v73, v73\n\t" \
  "v_exp_f32 v74, v74\n\t" \
  "v_exp_f32 v75, v75\n\t" \
  "v_cvt_pk_bf16_f32 v148, v72, v73\n\t" \
  "v_cvt_pk_bf16_f32 v149, v74, v75\n\t" \
  "v_mfma_f32_16x16x32_bf16 v[72:75], a[224:227], v[80:83], v[184:187]\n\t" \
  "ds_read_b64_tr_b16 a[178:179], v228 offset:4864\n\t" \
  "ds_read_b64_tr_b16 a[180:181], v228 offset:8960\n\t" \
  "v_mfma_f32_16x16x32_bf16 v[72:75], a[228:231], v[84:87], v[72:75]\n\t" \
  "v_max3_f32 v203, v67, v68, v69\n\t" \
  "v_max3_f32 v192, v192, v70, v71\n\t" \
  "v_mfma_f32_16x16x32_bf16 v[72:75], a[232:235], v[88:91], v[72:75]\n\t" \
  "s_add_u32 s97, s97, 7168\n\t" \
  "buffer_load_dwordx4 v241, %[wo], s97 offen offset:1024 lds\n\t" \
  "v_mfma_f32_16x16x32_bf16 v[72:75], a[236:239], v[92:95], v[72:75]\n\t" \
  "v_exp_f32 v76, v76\n\t" \
  "v_exp_f32 v77, v77\n\t" \
  "v_exp_f32 v78, v78\n\t" \
  "v_exp_f32 v79, v79\n\t" \
  "v_cvt_pk_bf16_f32 v150, v76, v77\n\t" \
  "v_cvt_pk_bf16_f32 v151, v78, v79\n\t" \
  "v_mfma_f32_16x16x32_bf16 v[76:79], a[252:255], v[92:95], v[184:187]\n\t" \
  "v_exp_f32 v96, v96\n\t" \
  "v_mfma_f32_16x16x32_bf16 v[76:79], a[248:251], v[88:91], v[76:79]\n\t" \
  "ds_read_b64_tr_b16 a[182:183], v228 offset:13056\n\t" \
  "v_max3_f32 v203, v203, v72, v73\n\t" \
  "v_mfma_f32_16x16x32_bf16 v[76:79], a[244:247], v[84:87], v[76:79]\n\t" \
  "v_max3_f32 v192, v192, v74, v75\n\t" \
  "ds_read_b64_tr_b16 a[184:185], v229 offset:768\n\t" \
  "v_mfma_f32_16x16x32_bf16 v[76:79], a[240:243], v[80:83], v[76:79]\n\t" \
  "v_exp_f32 v97, v97\n\t" \
  "v_exp_f32 v98, v98\n\t" \
  "v_exp_f32 v99, v99\n\t" \
  "v_cvt_pk_bf16_f32 v152, v96, v97\n\t" \
  "v_cvt_pk_bf16_f32 v153, v98, v99\n\t" \
  "v_mfma_f32_16x16x32_bf16 v[96:99], a[192:195], v[112:115], v[188:191]\n\t" \
  "v_exp_f32 v100, v100\n\t" \
  "v_mfma_f32_16x16x32_bf16 v[96:99], a[196:199], v[116:119], v[96:99]\n\t" \
  "s_add_u32 s97, s97, 7168\n\t" \
  "buffer_load_dwordx4 v240, %[wo], s97 offen offset:2048 lds\n\t" \
  "v_mfma_f32_16x16x32_bf16 v[96:99], a[200:203], v[120:123], v[96:99]\n\t" \
  "v_max3_f32 v203, v203, v76, v77\n\t" \
  "v_max3_f32 v192, v192, v78, v79\n\t" \
  "v_mfma_f32_16x16x32_bf16 v[96:99], a[204:207], v[124:127], v[96:99]\n\t" \
  "v_max_f32 v192, v192, v203\n\t" \
  "ds_read_b64_tr_b16 a[186:187], v229 offset:4864\n\t" \
  "v_exp_f32 v101, v101\n\t" \
  "v_exp_f32 v102, v102\n\t" \
  "v_exp_f32 v103, v103\n\t" \
  "v_cvt_pk_bf16_f32 v154, v100, v101\n\t" \
  "v_cvt_pk_bf16_f32 v155, v102, v103\n\t" \
  "v_mfma_f32_16x16x32_bf16 v[100:103], a[220:223], v[124:127], v[188:191]\n\t" \
  "v_cmp_lt_f32 vcc, 0x42800000, v192\n\t" \
  "ds_read_b64_tr_b16 a[188:189], v229 offset:8960\n\t" \
  "v_mfma_f32_16x16x32_bf16 v[100:103], a[216:219], v[120:123], v[100:103]\n\t" \
  "s_cbranch_vccnz .Lsf_c12\n\t" \
  ".Lsf_r12:\n\t" \
  "v_max3_f32 v192, v96, v97, v98\n\t" \
  "v_mfma_f32_16x16x32_bf16 v[100:103], a[212:215], v[116:119], v[100:103]\n\t" \
  "s_add_u32 s97, s97, 7168\n\t" \
  "buffer_load_dwordx4 v241, %[wo], s97 offen offset:3072 lds\n\t" \
  "v_mfma_f32_16x16x32_bf16 v[100:103], a[208:211], v[112:115], v[100:103]\n\t" \
  "s_add_u32 s98, %[pf4], 43008\n\t" \
  "buffer_load_dword %[pfd], v245, %[sU], s98 offen\n\t" \
  "v_exp_f32 v104, v104\n\t" \
  "v_exp_f32 v105, v105\n\t" \
  "v_exp_f32 v106, v106\n\t" \
  "v_exp_f32 v107, v107\n\t" \
  "v_cvt_pk_bf16_f32 v156, v104, v105\n\t" \
  "v_cvt_pk_bf16_f32 v157, v106, v107\n\t" \
  "v_mfma_f32_16x16x32_bf16 v[104:107], a[224:227], v[112:115], v[188:191]\n\t" \
  "ds_read_b64_tr_b16 a[190:191], v229 offset:13056\n\t" \
  "v_max3_f32 v203, v99, v100, v101\n\t" \
  "v_mfma_f32_16x16x32_bf16 v[104:107], a[228:231], v[116:119], v[104:107]\n\t" \
  "v_exp_f32 v108, v108\n\t" \
  "v_mfma_f32_16x16x32_bf16 v[104:107], a[232:235], v[120:123], v[104:107]\n\t" \
  "v_max3_f32 v192, v192, v102, v103\n\t" \
  "v_mfma_f32_16x16x32_bf16 v[104:107], a[236:239], v[124:127], v[104:107]\n\t" \
  "v_exp_f32 v109, v109\n\t" \
  "v_exp_f32 v110, v110\n\t" \
  "v_exp_f32 v111, v111\n\t" \
  "v_cvt_pk_bf16_f32 v158, v108, v109\n\t" \
  "v_cvt_pk_bf16_f32 v159, v110, v111\n\t" \
  "v_mfma_f32_16x16x32_bf16 v[108:111], a[252:255], v[124:127], v[188:191]\n\t" \
  "v_exp_f32 v0, v0\n\t" \
  "v_mfma_f32_16x16x32_bf16 v[108:111], a[248:251], v[120:123], v[108:111]\n\t" \
  "v_exp_f32 v1, v1\n\t" \
  "v_mfma_f32_16x16x32_bf16 v[108:111], a[244:247], v[116:119], v[108:111]\n\t" \
  "v_max3_f32 v203, v203, v104, v105\n\t" \
  "v_max3_f32 v192, v192, v106, v107\n\t" \
  "v_mfma_f32_16x16x32_bf16 v[108:111], a[240:243], v[112:115], v[108:111]\n\t" \
  "v_exp_f32 v2, v2\n\t" \
  "s_waitcnt vmcnt(6) lgkmcnt(0)\n\t" \
  "s_barrier\n\t" \
  "v_mfma_f32_16x16x32_bf16 a[0:3], a[128:131], v[128:131], 0\n\t" \
  "v_exp_f32 v3, v3\n\t" \
  "v_mfma_f32_16x16x32_bf16 a[0:3], a[132:135], v[132:135], a[0:3]\n\t" \
  "v_exp_f32 v4, v4\n\t" \
  "v_mfma_f32_16x16x32_bf16 a[16:19], a[136:139], v[128:131], 0\n\t" \
  "ds_read_b128 a[192:195], v220 offset:0\n\t" \
  "v_max3_f32 v203, v203, v108, v109\n\t" \
  "v_mfma_f32_16x16x32_bf16 a[16:19], a[140:143], v[132:135], a[16:19]\n\t" \
  "v_max3_f32 v192, v192, v110, v111\n\t" \
  "v_max_f32 v192, v192, v203\n\t" \
  "v_mfma_f32_16x16x32_bf16 a[32:35], a[144:147], v[128:131], 0\n\t" \
  "v_cmp_lt_f32 vcc, 0x42800000, v192\n\t" \
  "ds_read_b128 a[196:199], v221 offset:0\n\t" \
  "v_mfma_f32_16x16x32_bf16 a[32:35], a[148:151], v[132:135], a[32:35]\n\t" \
  "s_cbranch_vccnz .Lsf_c13\n\t" \
  ".Lsf_r13:\n\t" \
  "ds_read_b128 a[200:203], v222 offset:0\n\t" \
  "v_mfma_f32_16x16x32_bf16 a[48:51], a[152:155], v[128:131], 0\n\t" \
  "v_exp_f32 v5, v5\n\t" \
  "v_mfma_f32_16x16x32_bf16 a[48:51], a[156:159], v[132:135], a[48:51]\n\t" \
  "v_exp_f32 v6, v6\n\t" \
  "v_mfma_f32_16x16x32_bf16 a[64:67], a[160:163], v[128:131], 0\n\t" \
  "v_exp_f32 v7, v7\n\t" \
  "v_mfma_f32_16x16x32_bf16 a[64:67], a[164:167], v[132:135], a[64:67]\n\t" \
  "v_exp_f32 v8, v8\n\t" \
  "v_mfma_f32_16x16x32_bf16 a[80:83], a[168:171], v[128:131], 0\n\t" \
  "v_exp_f32 v9, v9\n\t" \
  "v_mfma_f32_16x16x32_bf16 a[80:83], a[172:175], v[132:135], a[80:83]\n\t" \
  "v_exp_f32 v10, v10\n\t" \
  "v_mfma_f32_16x16x32_bf16 a[96:99], a[176:179], v[128:131], 0\n\t" \
  "v_exp_f32 v11, v11\n\t" \
  "v_mfma_f32_16x16x32_bf16 a[96:99], a[180:183], v[132:135], a[96:99]\n\t" \
  "v_exp_f32 v12, v12\n\t" \
  "v_mfma_f32_16x16x32_bf16 a[112:115], a[184:187], v[128:131], 0\n\t" \
  "ds_read_b128 a[204:207], v223 offset:0\n\t" \
  "ds_read_b128 a[208:211], v220 offset:4096\n\t" \
  "v_mfma_f32_16x16x32_bf16 a[112:115], a[188:191], v[132:135], a[112:115]\n\t" \
  "v_exp_f32 v13, v13\n\t" \
  "v_mfma_f32_16x16x32_bf16 v[160:163], v[216:219], v[128:131], 0\n\t" \
  "v_exp_f32 v14, v14\n\t" \
  "v_mfma_f32_16x16x32_bf16 v[160:163], v[216:219], v[132:135], v[160:163]\n\t" \
  "v_cvt_pk_bf16_f32 v128, v0, v1\n\t" \
  "v_cvt_pk_bf16_f32 v129, v2, v3\n\t" \
  "v_mfma_f32_16x16x32_bf16 a[4:7], a[128:131], v[136:139], 0\n\t" \
  "v_cvt_pk_bf16_f32 v130, v4, v5\n\t" \
  "v_cvt_pk_bf16_f32 v131, v6, v7\n\t" \
  "v_mfma_f32_16x16x32_bf16 a[4:7], a[132:135], v[140:143], a[4:7]\n\t" \
  "v_cvt_pk_bf16_f32 v132, v8, v9\n\t" \
  "v_cvt_pk_bf16_f32 v133, v10, v11\n\t" \
  "v_mfma_f32_16x16x32_bf16 a[20:23], a[136:139], v[136:139], 0\n\t" \
  "v_cvt_pk_bf16_f32 v134, v12, v13\n\t" \
  "ds_read_b128 a[212:215], v221 offset:4096\n\t" \
  "v_mfma_f32_16x16x32_bf16 a[20:23], a[140:143], v[140:143], a[20:23]\n\t" \
  "v_exp_f32 v15, v15\n\t" \
  "v_mfma_f32_16x16x32_bf16 a[36:39], a[144:147], v[136:139], 0\n\t" \
  "v_cvt_pk_bf16_f32 v135, v14, v15\n\t" \
  "s_add_u32 m0, %[m], 65536\n\t" \
  "v_mfma_f32_16x16x32_bf16 a[36:39], a[148:151], v[140:143], a[36:39]\n\t" \
  "v_exp_f32 v32, v32\n\t" \
  "v_mfma_f32_16x16x32_bf16 a[52:55], a[152:155], v[136:139], 0\n\t" \
  "ds_read_b128 a[216:219], v222 offset:4096\n\t" \
  "s_add_u32 s96, %[sofk], 49152\n\t" \
  "v_mfma_f32_16x16x32_bf16 a[52:55], a[156:159], v[140:143], a[52:55]\n\t" \
  "v_exp_f32 v33, v33\n\t" \
  "v_mfma_f32_16x16x32_bf16 a[68:71], a[160:163], v[136:139], 0\n\t" \
  "v_exp_f32 v34, v34\n\t" \
  "v_mfma_f32_16x16x32_bf16 a[68:71], a[164:167], v[140:143], a[68:71]\n\t" \
  "v_exp_f32 v35, v35\n\t" \
  "v_mfma_f32_16x16x32_bf16 a[84:87], a[168:171], v[136:139], 0\n\t" \
  "ds_read_b128 a[220:223], v223 offset:4096\n\t" \
  "buffer_load_dwordx4 v224, %[sK], s96 offen offset:0 lds\n\t" \
  "v_mfma_f32_16x16x32_bf16 a[84:87], a[172:175], v[140:143], a[84:87]\n\t" \
  "v_exp_f32 v36, v36\n\t" \
  "v_mfma_f32_16x16x32_bf16 a[100:103], a[176:179], v[136:139], 0\n\t" \
  "v_exp_f32 v37, v37\n\t" \
  "v_mfma_f32_16x16x32_bf16 a[100:103], a[180:183], v[140:143], a[100:103]\n\t" \
  "v_exp_f32 v38, v38\n\t" \
  "v_mfma_f32_16x16x32_bf16 a[116:119], a[184:187], v[136:139], 0\n\t" \
  "ds_read_b128 a[224:227], v220 offset:8192\n\t" \
  "buffer_load_dwordx4 v225, %[sK], s96 offen offset:1024 lds\n\t" \
  "v_mfma_f32_16x16x32_bf16 a[116:119], a[188:191], v[140:143], a[116:119]\n\t" \
  "v_exp_f32 v39, v39\n\t" \
  "v_mfma_f32_16x16x32_bf16 v[164:167], v[216:219], v[136:139], 0\n\t" \
  "v_exp_f32 v40, v40\n\t" \
  "v_mfma_f32_16x16x32_bf16 v[164:167], v[216:219], v[140:143], v[164:167]\n\t" \
  "v_cvt_pk_bf16_f32 v136, v32, v33\n\t" \
  "v_cvt_pk_bf16_f32 v137, v34, v35\n\t" \
  "v_mfma_f32_16x16x32_bf16 a[8:11], a[128:131], v[144:147], 0\n\t" \
  "v_cvt_pk_bf16_f32 v138, v36, v37\n\t" \
  "v_cvt_pk_bf16_f32 v139, v38, v39\n\t" \
  "v_mfma_f32_16x16x32_bf16 a[8:11], a[132:135], v[148:151], a[8:11]\n\t" \
  "ds_read_b128 a[228:231], v221 offset:8192\n\t" \
  "buffer_load_dwordx4 v226, %[sK], s96 offen offset:2048 lds\n\t" \
  "v_mfma_f32_16x16x32_bf16 a[24:27], a[136:139], v[144:147], 0\n\t" \
  "v_exp_f32 v41, v41\n\t" \
  "v_mfma_f32_16x16x32_bf16 a[24:27], a[140:143], v[148:151], a[24:27]\n\t" \
  "v_cvt_pk_bf16_f32 v140, v40, v41\n\t" \
  "ds_read_b128 a[232:235], v222 offset:8192\n\t" \
  "v_mfma_f32_16x16x32_bf16 a[40:43], a[144:147], v[144:147], 0\n\t" \
  "v_exp_f32 v42, v42\n\t" \
  "v_mfma_f32_16x16x32_bf16 a[40:43], a[148:151], v[148:151], a[40:43]\n\t" \
  "v_exp_f32 v43, v43\n\t" \
  "v_mfma_f32_16x16x32_bf16 a[56:59], a[152:155], v[144:147], 0\n\t" \
  "v_cvt_pk_bf16_f32 v141, v42, v43\n\t" \
  "buffer_load_dwordx4 v227, %[sK], s96 offen offset:3072 lds\n\t" \
  "v_mfma_f32_16x16x32_bf16 a[56:59], a[156:159], v[148:151], a[56:59]\n\t" \
  "v_exp_f32 v44, v44\n\t" \
  "v_mfma_f32_16x16x32_bf16 a[72:75], a[160:163], v[144:147], 0\n\t" \
  "ds_read_b128 a[236:239], v223 offset:8192\n\t" \
  "s_add_u32 m0, %[m], 81920\n\t" \
  "v_mfma_f32_16x16x32_bf16 a[72:75], a[164:167], v[148:151], a[72:75]\n\t" \
  "v_exp_f32 v45, v45\n\t" \
  "v_mfma_f32_16x16x32_bf16 a[88:91], a[168:171], v[144:147], 0\n\t" \
  "v_cvt_pk_bf16_f32 v142, v44, v45\n\t" \
  "ds_read_b128 a[240:243], v220 offset:12288\n\t" \
  "v_mfma_f32_16x16x32_bf16 a[88:91], a[172:175], v[148:151], a[88:91]\n\t" \
  "v_exp_f32 v46, v46\n\t" \
  "v_mfma_f32_16x16x32_bf16 a[104:107], a[176:179], v[144:147], 0\n\t" \
  "v_exp_f32 v47, v47\n\t" \
  "v_mfma_f32_16x16x32_bf16 a[104:107], a[180:183], v[148:151], a[104:107]\n\t" \
  "v_cvt_pk_bf16_f32 v143, v46, v47\n\t" \
  "s_add_u32 s96, %[sofk], 32768\n\t" \
  "v_mfma_f32_16x16x32_bf16 a[120:123], a[184:187], v[144:147], 0\n\t" \
  "v_exp_f32 v64, v64\n\t" \
  "v_mfma_f32_16x16x32_bf16 a[120:123], a[188:191], v[148:151], a[120:123]\n\t" \
  "ds_read_b128 a[244:247], v221 offset:12288\n\t" \
  "buffer_load_dwordx4 v230, %[sV], s96 offen offset:0 lds\n\t" \
  "v_mfma_f32_16x16x32_bf16 v[168:171], v[216:219], v[144:147], 0\n\t" \
  "v_exp_f32 v65, v65\n\t" \
  "v_mfma_f32_16x16x32_bf16 v[168:171], v[216:219], v[148:151], v[168:171]\n\t" \
  "v_cvt_pk_bf16_f32 v144, v64, v65\n\t" \
  "ds_read_b128 a[248:251], v222 offset:12288\n\t" \
  "v_mfma_f32_16x16x32_bf16 a[12:15], a[128:131], v[152:155], 0\n\t" \
  "v_exp_f32 v66, v66\n\t" \
  "v_mfma_f32_16x16x32_bf16 a[12:15], a[132:135], v[156:159], a[12:15]\n\t" \
  "v_exp_f32 v67, v67\n\t" \
  "v_mfma_f32_16x16x32_bf16 a[28:31], a[136:139], v[152:155], 0\n\t" \
  "v_cvt_pk_bf16_f32 v145, v66, v67\n\t" \
  "buffer_load_dwordx4 v231, %[sV], s96 offen offset:1024 lds\n\t" \
  "v_mfma_f32_16x16x32_bf16 a[28:31], a[140:143], v[156:159], a[28:31]\n\t" \
  "v_exp_f32 v68, v68\n\t" \
  "v_mfma_f32_16x16x32_bf16 a[44:47], a[144:147], v[152:155], 0\n\t" \
  "v_exp_f32 v69, v69\n\t" \
  "v_mfma_f32_16x16x32_bf16 a[44:47], a[148:151], v[156:159], a[44:47]\n\t" \
  "v_cvt_pk_bf16_f32 v146, v68, v69\n\t" \
  "ds_read_b128 a[252:255], v223 offset:12288\n\t" \
  "v_mfma_f32_16x16x32_bf16 a[60:63], a[152:155], v[152:155], 0\n\t" \
  "v_exp_f32 v70, v70\n\t" \
  "v_mfma_f32_16x16x32_bf16 a[60:63], a[156:159], v[156:159], a[60:63]\n\t" \
  "v_exp_f32 v71, v71\n\t" \
  "v_mfma_f32_16x16x32_bf16 a[76:79], a[160:163], v[152:155], 0\n\t" \
  "v_cvt_pk_bf16_f32 v147, v70, v71\n\t" \
  "buffer_load_dwordx4 v230, %[sV], s96 offen offset:2048 lds\n\t" \
  "v_mfma_f32_16x16x32_bf16 a[76:79], a[164:167], v[156:159], a[76:79]\n\t" \
  "v_exp_f32 v72, v72\n\t" \
  "v_mfma_f32_16x16x32_bf16 a[92:95], a[168:171], v[152:155], 0\n\t" \
  "v_exp_f32 v73, v73\n\t" \
  "v_mfma_f32_16x16x32_bf16 a[92:95], a[172:175], v[156:159], a[92:95]\n\t" \
  "v_cvt_pk_bf16_f32 v148, v72, v73\n\t" \
  "buffer_load_dwordx4 v231, %[sV], s96 offen offset:3072 lds\n\t" \
  "v_mfma_f32_16x16x32_bf16 a[108:111], a[176:179], v[152:155], 0\n\t" \
  "v_exp_f32 v74, v74\n\t" \
  "v_mfma_f32_16x16x32_bf16 a[108:111], a[180:183], v[156:159], a[108:111]\n\t" \
  "v_exp_f32 v75, v75\n\t" \
  "v_mfma_f32_16x16x32_bf16 a[124:127], a[184:187], v[152:155], 0\n\t" \
  "v_cvt_pk_bf16_f32 v149, v74, v75\n\t" \
  "v_mfma_f32_16x16x32_bf16 a[124:127], a[188:191], v[156:159], a[124:127]\n\t" \
  "v_exp_f32 v76, v76\n\t" \
  "v_mfma_f32_16x16x32_bf16 v[172:175], v[216:219], v[152:155], 0\n\t" \
  "v_exp_f32 v77, v77\n\t" \
  "v_mfma_f32_16x16x32_bf16 v[172:175], v[216:219], v[156:159], v[172:175]\n\t" \
  "v_cvt_pk_bf16_f32 v150, v76, v77\n\t" \
  "s_cmp_lg_u32 s99, 0\n\t" \
  "s_cbranch_scc1 .Lsf_p1\n\t" \
  ".Lsf_q1:\n\t" \
  "s_waitcnt lgkmcnt(0)\n\t" \
  "v_mfma_f32_16x16x32_bf16 v[0:3], a[192:195], v[16:19], v[176:179]\n\t" \
  "v_exp_f32 v78, v78\n\t" \
  "v_mfma_f32_16x16x32_bf16 v[0:3], a[196:199], v[20:23], v[0:3]\n\t" \
  "v_exp_f32 v79, v79\n\t" \
  "v_mfma_f32_16x16x32_bf16 v[0:3], a[200:203], v[24:27], v[0:3]\n\t" \
  "v_cvt_pk_bf16_f32 v151, v78, v79\n\t" \
  "ds_read_b64_tr_b16 a[128:129], v237 offset:0\n\t" \
  "v_mfma_f32_16x16x32_bf16 v[0:3], a[204:207], v[28:31], v[0:3]\n\t" \
  "v_exp_f32 v96, v96\n\t" \
  "v_mfma_f32_16x16x32_bf16 v[4:7], a[220:223], v[28:31], v[176:179]\n\t" \
  "ds_read_b64_tr_b16 a[130:131], v237 offset:4096\n\t" \
  "ds_read_b64_tr_b16 a[132:133], v237 offset:8192\n\t" \
  "v_mfma_f32_16x16x32_bf16 v[4:7], a[216:219], v[24:27], v[4:7]\n\t" \
  "v_exp_f32 v97, v97\n\t" \
  "v_mfma_f32_16x16x32_bf16 v[4:7], a[212:215], v[20:23], v[4:7]\n\t" \
  "v_cvt_pk_bf16_f32 v152, v96, v97\n\t" \
  "v_max3_f32 v192, v0, v1, v2\n\t" \
  "v_mfma_f32_16x16x32_bf16 v[4:7], a[208:211], v[16:19], v[4:7]\n\t" \
  "v_exp_f32 v98, v98\n\t" \
  "v_mfma_f32_16x16x32_bf16 v[8:11], a[224:227], v[16:19], v[176:179]\n\t" \
  "ds_read_b64_tr_b16 a[134:135], v237 offset:12288\n\t" \
  "ds_read_b64_tr_b16 a[136:137], v238 offset:0\n\t" \
  "v_mfma_f32_16x16x32_bf16 v[8:11], a[228:231], v[20:23], v[8:11]\n\t" \
  "v_exp_f32 v99, v99\n\t" \
  "v_mfma_f32_16x16x32_bf16 v[8:11], a[232:235], v[24:27], v[8:11]\n\t" \
  "v_cvt_pk_bf16_f32 v153, v98, v99\n\t" \
  "v_max3_f32 v203, v3, v4, v5\n\t" \
  "v_mfma_f32_16x16x32_bf16 v[8:11], a[236:239], v[28:31], v[8:11]\n\t" \
  "v_max3_f32 v192, v192, v6, v7\n\t" \
  "ds_read_b64_tr_b16 a[138:139], v238 offset:4096\n\t" \
  "v_mfma_f32_16x16x32_bf16 v[12:15], a[252:255], v[28:31], v[176:179]\n\t" \
  "v_exp_f32 v100, v100\n\t" \
  "v_mfma_f32_16x16x32_bf16 v[12:15], a[248:251], v[24:27], v[12:15]\n\t" \
  "ds_read_b64_tr_b16 a[140:141], v238 offset:8192\n\t" \
  "ds_read_b64_tr_b16 a[142:143], v238 offset:12288\n\t" \
  "v_mfma_f32_16x16x32_bf16 v[12:15], a[244:247], v[20:23], v[12:15]\n\t" \
  "v_max3_f32 v203, v203, v8, v9\n\t" \
  "v_max3_f32 v192, v192, v10, v11\n\t" \
  "v_mfma_f32_16x16x32_bf16 v[12:15], a[240:243], v[16:19], v[12:15]\n\t" \
  "v_exp_f32 v101, v101\n\t" \
  "v_mfma_f32_16x16x32_bf16 v[32:35], a[192:195], v[48:51], v[180:183]\n\t" \
  "v_cvt_pk_bf16_f32 v154, v100, v101\n\t" \
  "ds_read_b64_tr_b16 a[144:145], v237 offset:256\n\t" \
  "v_mfma_f32_16x16x32_bf16 v[32:35], a[196:199], v[52:55], v[32:35]\n\t" \
  "v_exp_f32 v102, v102\n\t" \
  "v_mfma_f32_16x16x32_bf16 v[32:35], a[200:203], v[56:59], v[32:35]\n\t" \
  "ds_read_b64_tr_b16 a[146:147], v237 offset:4352\n\t" \
  "v_max3_f32 v203, v203, v12, v13\n\t" \
  "v_mfma_f32_16x16x32_bf16 v[32:35], a[204:207], v[60:63], v[32:35]\n\t" \
  "v_max3_f32 v192, v192, v14, v15\n\t" \
  "v_max_f32 v192, v192, v203\n\t" \
  "v_mfma_f32_16x16x32_bf16 v[36:39], a[220:223], v[60:63], v[180:183]\n\t" \
  "v_cmp_lt_f32 vcc, 0x42800000, v192\n\t" \
  "ds_read_b64_tr_b16 a[148:149], v237 offset:8448\n\t" \
  "v_mfma_f32_16x16x32_bf16 v[36:39], a[216:219], v[56:59], v[36:39]\n\t" \
  "s_cbranch_vccnz .Lsf_c20\n\t" \
  ".Lsf_r20:\n\t" \
  "ds_read_b64_tr_b16 a[150:151], v237 offset:12544\n\t" \
  "v_mfma_f32_16x16x32_bf16 v[36:39], a[212:215], v[52:55], v[36:39]\n\t" \
  "v_max3_f32 v192, v32, v33, v34\n\t" \
  "ds_read_b64_tr_b16 a[152:153], v238 offset:256\n\t" \
  "v_mfma_f32_16x16x32_bf16 v[36:39], a[208:211], v[48:51], v[36:39]\n\t" \
  "s_add_u32 m0, %[m], 32768\n\t" \
  "s_mov_b32 s96, 0\n\t" \
  "buffer_load_dwordx4 v232, %[sG], s96 offen offset:0 lds\n\t" \
  "v_mfma_f32_16x16x32_bf16 v[40:43], a[224:227], v[48:51], v[180:183]\n\t" \
  "v_exp_f32 v103, v103\n\t" \
  "v_mfma_f32_16x16x32_bf16 v[40:43], a[228:231], v[52:55], v[40:43]\n\t" \
  "v_cvt_pk_bf16_f32 v155, v102, v103\n\t" \
  "ds_read_b64_tr_b16 a[154:155], v238 offset:4352\n\t" \
  "v_mfma_f32_16x16x32_bf16 v[40:43], a[232:235], v[56:59], v[40:43]\n\t" \
  "v_max3_f32 v203, v35, v36, v37\n\t" \
  "v_max3_f32 v192, v192, v38, v39\n\t" \
  "v_mfma_f32_16x16x32_bf16 v[40:43], a[236:239], v[60:63], v[40:43]\n\t" \
  "ds_read_b64_tr_b16 a[156:157], v238 offset:8448\n\t" \
  "ds_read_b64_tr_b16 a[158:159], v238 offset:12544\n\t" \
  "v_mfma_f32_16x16x32_bf16 v[44:47], a[252:255], v[60:63], v[180:183]\n\t" \
  "v_exp_f32 v104, v104\n\t" \
  "v_mfma_f32_16x16x32_bf16 v[44:47], a[248:251], v[56:59], v[44:47]\n\t" \
  "v_exp_f32 v105, v105\n\t" \
  "v_mfma_f32_16x16x32_bf16 v[44:47], a[244:247], v[52:55], v[44:47]\n\t" \
  "v_cvt_pk_bf16_f32 v156, v104, v105\n\t" \
  "ds_read_b64_tr_b16 a[160:161], v237 offset:512\n\t" \
  "v_mfma_f32_16x16x32_bf16 v[44:47], a[240:243], v[48:51], v[44:47]\n\t" \
  "v_max3_f32 v203, v203, v40, v41\n\t" \
  "v_max3_f32 v192, v192, v42, v43\n\t" \
  "v_mfma_f32_16x16x32_bf16 v[64:67], a[192:195], v[80:83], v[184:187]\n\t" \
  "ds_read_b64_tr_b16 a[162:163], v237 offset:4608\n\t" \
  "ds_read_b64_tr_b16 a[164:165], v237 offset:8704\n\t" \
  "v_mfma_f32_16x16x32_bf16 v[64:67], a[196:199], v[84:87], v[64:67]\n\t" \
  "s_mov_b32 s96, 6144\n\t" \
  "buffer_load_dwordx4 v232, %[sG], s96 offen offset:1024 lds\n\t" \
  "v_mfma_f32_16x16x32_bf16 v[64:67], a[200:203], v[88:91], v[64:67]\n\t" \
  "v_max3_f32 v203, v203, v44, v45\n\t" \
  "v_max3_f32 v192, v192, v46, v47\n\t" \
  "v_mfma_f32_16x16x32_bf16 v[64:67], a[204:207], v[92:95], v[64:67]\n\t" \
  "v_max_f32 v192, v192, v203\n\t" \
  "ds_read_b64_tr_b16 a[166:167], v237 offset:12800\n\t" \
  "v_mfma_f32_16x16x32_bf16 v[68:71], a[220:223], v[92:95], v[184:187]\n\t" \
  "v_cmp_lt_f32 vcc, 0x42800000, v192\n\t" \
  "ds_read_b64_tr_b16 a[168:169], v238 offset:512\n\t" \
  "v_mfma_f32_16x16x32_bf16 v[68:71], a[216:219], v[88:91], v[68:71]\n\t" \
  "s_cbranch_vccnz .Lsf_c21\n\t" \
  ".Lsf_r21:\n\t" \
  "ds_read_b64_tr_b16 a[170:171], v238 offset:4608\n\t" \
  "v_mfma_f32_16x16x32_bf16 v[68:71], a[212:215], v[84:87], v[68:71]\n\t" \
  "v_max3_f32 v192, v64, v65, v66\n\t" \
  "ds_read_b64_tr_b16 a[172:173], v238 offset:8704\n\t" \
  "v_mfma_f32_16x16x32_bf16 v[68:71], a[208:211], v[80:83], v[68:71]\n\t" \
  "v_exp_f32 v106, v106\n\t" \
  "v_mfma_f32_16x16x32_bf16 v[72:75], a[224:227], v[80:83], v[184:187]\n\t" \
  "v_exp_f32 v107, v107\n\t" \
  "v_mfma_f32_16x16x32_bf16 v[72:75], a[228:231], v[84:87], v[72:75]\n\t" \
  "v_cvt_pk_bf16_f32 v157, v106, v107\n\t" \
  "ds_read_b64_tr_b16 a[174:175], v238 offset:12800\n\t" \
  "v_mfma_f32_16x16x32_bf16 v[72:75], a[232:235], v[88:91], v[72:75]\n\t" \
  "s_mov_b32 s96, 12288\n\t" \
  "buffer_load_dwordx4 v232, %[sG], s96 offen offset:2048 lds\n\t" \
  "v_mfma_f32_16x16x32_bf16 v[72:75], a[236:239], v[92:95], v[72:75]\n\t" \
  "v_max3_f32 v203, v67, v68, v69\n\t" \
  "v_max3_f32 v192, v192, v70, v71\n\t" \
  "v_mfma_f32_16x16x32_bf16 v[76:79], a[252:255], v[92:95], v[184:187]\n\t" \
  "ds_read_b64_tr_b16 a[176:177], v237 offset:768\n\t" \
  "ds_read_b64_tr_b16 a[178:179], v237 offset:4864\n\t" \
  "v_mfma_f32_16x16x32_bf16 v[76:79], a[248:251], v[88:91], v[76:79]\n\t" \
  "v_exp_f32 v108, v108\n\t" \
  "v_mfma_f32_16x16x32_bf16 v[76:79], a[244:247], v[84:87], v[76:79]\n\t" \
  "v_max3_f32 v203, v203, v72, v73\n\t" \
  "v_max3_f32 v192, v192, v74, v75\n\t" \
  "v_mfma_f32_16x16x32_bf16 v[76:79], a[240:243], v[80:83], v[76:79]\n\t" \
  "v_exp_f32 v109, v109\n\t" \
  "v_mfma_f32_16x16x32_bf16 v[96:99], a[192:195], v[112:115], v[188:191]\n\t" \
  "v_cvt_pk_bf16_f32 v158, v108, v109\n\t" \
  "ds_read_b64_tr_b16 a[180:181], v237 offset:8960\n\t" \
  "v_mfma_f32_16x16x32_bf16 v[96:99], a[196:199], v[116:119], v[96:99]\n\t" \
  "v_exp_f32 v110, v110\n\t" \
  "v_mfma_f32_16x16x32_bf16 v[96:99], a[200:203], v[120:123], v[96:99]\n\t" \
  "ds_read_b64_tr_b16 a[182:183], v237 offset:13056\n\t" \
  "v_max3_f32 v203, v203, v76, v77\n\t" \
  "v_mfma_f32_16x16x32_bf16 v[96:99], a[204:207], v[124:127], v[96:99]\n\t" \
  "v_max3_f32 v192, v192, v78, v79\n\t" \
  "v_max_f32 v192, v192, v203\n\t" \
  "v_mfma_f32_16x16x32_bf16 v[100:103], a[220:223], v[124:127], v[188:191]\n\t" \
  "v_cmp_lt_f32 vcc, 0x42800000, v192\n\t" \
  "ds_read_b64_tr_b16 a[184:185], v238 offset:768\n\t" \
  "v_mfma_f32_16x16x32_bf16 v[100:103], a[216:219], v[120:123], v[100:103]\n\t" \
  "s_cbranch_vccnz .Lsf_c22\n\t" \
  ".Lsf_r22:\n\t" \
  "ds_read_b64_tr_b16 a[186:187], v238 offset:4864\n\t" \
  "v_mfma_f32_16x16x32_bf16 v[100:103], a[212:215], v[116:119], v[100:103]\n\t" \
  "s_mov_b32 s96, 18432\n\t" \
  "buffer_load_dwordx4 v232, %[sG], s96 offen offset:3072 lds\n\t" \
  "v_mfma_f32_16x16x32_bf16 v[100:103], a[208:211], v[112:115], v[100:103]\n\t" \
  "v_max3_f32 v192, v96, v97, v98\n\t" \
  "ds_read_b64_tr_b16 a[188:189], v238 offset:8960\n\t" \
  "v_mfma_f32_16x16x32_bf16 v[104:107], a[224:227], v[112:115], v[188:191]\n\t" \
  "v_exp_f32 v111, v111\n\t" \
  "v_mfma_f32_16x16x32_bf16 v[104:107], a[228:231], v[116:119], v[104:107]\n\t" \
  "v_cvt_pk_bf16_f32 v159, v110, v111\n\t" \
  "ds_read_b64_tr_b16 a[190:191], v238 offset:13056\n\t" \
  "v_mfma_f32_16x16x32_bf16 v[104:107], a[232:235], v[120:123], v[104:107]\n\t" \
  "v_max3_f32 v203, v99, v100, v101\n\t" \
  "v_max3_f32 v192, v192, v102, v103\n\t" \
  "v_mfma_f32_16x16x32_bf16 v[104:107], a[236:239], v[124:127], v[104:107]\n\t" \
  "v_exp_f32 v0, v0\n\t" \
  "v_mfma_f32_16x16x32_bf16 v[108:111], a[252:255], v[124:127], v[188:191]\n\t" \
  "v_exp_f32 v1, v1\n\t" \
  "v_mfma_f32_16x16x32_bf16 v[108:111], a[248:251], v[120:123], v[108:111]\n\t" \
  "v_exp_f32 v2, v2\n\t" \
  "v_mfma_f32_16x16x32_bf16 v[108:111], a[244:247], v[116:119], v[108:111]\n\t" \
  "v_exp_f32 v3, v3\n\t" \
  "v_mfma_f32_16x16x32_bf16 v[108:111], a[240:243], v[112:115], v[108:111]\n\t" \
  "v_max3_f32 v203, v203, v104, v105\n\t" \
  "v_max3_f32 v192, v192, v106, v107\n\t" \
  "s_waitcnt vmcnt(4) lgkmcnt(0)\n\t" \
  "s_barrier\n\t" \
  "v_mfma_f32_16x16x32_bf16 a[0:3], a[128:131], v[128:131], a[0:3]\n\t" \
  "v_exp_f32 v4, v4\n\t" \
  "v_mfma_f32_16x16x32_bf16 a[0:3], a[132:135], v[132:135], a[0:3]\n\t" \
  "v_exp_f32 v5, v5\n\t" \
  "v_mfma_f32_16x16x32_bf16 a[16:19], a[136:139], v[128:131], a[16:19]\n\t" \
  "ds_read_b128 a[192:195], v220 offset:16384\n\t" \
  "v_max3_f32 v203, v203, v108, v109\n\t" \
  "v_mfma_f32_16x16x32_bf16 a[16:19], a[140:143], v[132:135], a[16:19]\n\t" \
  "v_max3_f32 v192, v192, v110, v111\n\t" \
  "v_max_f32 v192, v192, v203\n\t" \
  "v_mfma_f32_16x16x32_bf16 a[32:35], a[144:147], v[128:131], a[32:35]\n\t" \
  "v_cmp_lt_f32 vcc, 0x42800000, v192\n\t" \
  "ds_read_b128 a[196:199], v221 offset:16384\n\t" \
  "v_mfma_f32_16x16x32_bf16 a[32:35], a[148:151], v[132:135], a[32:35]\n\t" \
  "s_cbranch_vccnz .Lsf_c23\n\t" \
  ".Lsf_r23:\n\t" \
  "ds_read_b128 a[200:203], v222 offset:16384\n\t" \
  "v_mfma_f32_16x16x32_bf16 a[48:51], a[152:155], v[128:131], a[48:51]\n\t" \
  "v_exp_f32 v6, v6\n\t" \
  "v_mfma_f32_16x16x32_bf16 a[48:51], a[156:159], v[132:135], a[48:51]\n\t" \
  "v_exp_f32 v7, v7\n\t" \
  "v_mfma_f32_16x16x32_bf16 a[64:67], a[160:163], v[128:131], a[64:67]\n\t" \
  "v_exp_f32 v8, v8\n\t" \
  "v_mfma_f32_16x16x32_bf16 a[64:67], a[164:167], v[132:135], a[64:67]\n\t" \
  "v_exp_f32 v9, v9\n\t" \
  "v_mfma_f32_16x16x32_bf16 a[80:83], a[168:171], v[128:131], a[80:83]\n\t" \
  "v_exp_f32 v10, v10\n\t" \
  "v_mfma_f32_16x16x32_bf16 a[80:83], a[172:175], v[132:135], a[80:83]\n\t" \
  "v_exp_f32 v11, v11\n\t" \
  "v_mfma_f32_16x16x32_bf16 a[96:99], a[176:179], v[128:131], a[96:99]\n\t" \
  "v_exp_f32 v12, v12\n\t" \
  "v_mfma_f32_16x16x32_bf16 a[96:99], a[180:183], v[132:135], a[96:99]\n\t" \
  "v_exp_f32 v13, v13\n\t" \
  "v_mfma_f32_16x16x32_bf16 a[112:115], a[184:187], v[128:131], a[112:115]\n\t" \
  "ds_read_b128 a[204:207], v223 offset:16384\n\t" \
  "ds_read_b128 a[208:211], v220 offset:20480\n\t" \
  "v_mfma_f32_16x16x32_bf16 a[112:115], a[188:191], v[132:135], a[112:115]\n\t" \
  "v_exp_f32 v14, v14\n\t" \
  "v_mfma_f32_16x16x32_bf16 v[160:163], v[216:219], v[128:131], v[160:163]\n\t" \
  "v_exp_f32 v15, v15\n\t" \
  "v_mfma_f32_16x16x32_bf16 v[160:163], v[216:219], v[132:135], v[160:163]\n\t" \
  "v_cvt_pk_bf16_f32 v128, v0, v1\n\t" \
  "v_cvt_pk_bf16_f32 v129, v2, v3\n\t" \
  "v_mfma_f32_16x16x32_bf16 a[4:7], a[128:131], v[136:139], a[4:7]\n\t" \
  "v_cvt_pk_bf16_f32 v130, v4, v5\n\t" \
  "v_cvt_pk_bf16_f32 v131, v6, v7\n\t" \
  "v_mfma_f32_16x16x32_bf16 a[4:7], a[132:135], v[140:143], a[4:7]\n\t" \
  "v_cvt_pk_bf16_f32 v132, v8, v9\n\t" \
  "v_cvt_pk_bf16_f32 v133, v10, v11\n\t" \
  "v_mfma_f32_16x16x32_bf16 a[20:23], a[136:139], v[136:139], a[20:23]\n\t" \
  "v_cvt_pk_bf16_f32 v134, v12, v13\n\t" \
  "v_cvt_pk_bf16_f32 v135, v14, v15\n\t" \
  "v_mfma_f32_16x16x32_bf16 a[20:23], a[140:143], v[140:143], a[20:23]\n\t" \
  "ds_read_b128 a[212:215], v221 offset:20480\n\t" \
  "s_add_u32 m0, %[m], 49152\n\t" \
  "v_mfma_f32_16x16x32_bf16 a[36:39], a[144:147], v[136:139], a[36:39]\n\t" \
  "v_exp_f32 v32, v32\n\t" \
  "v_mfma_f32_16x16x32_bf16 a[36:39], a[148:151], v[140:143], a[36:39]\n\t" \
  "ds_read_b128 a[216:219], v222 offset:20480\n\t" \
  "s_add_u32 s96, %[sofk], 65536\n\t" \
  "v_mfma_f32_16x16x32_bf16 a[52:55], a[152:155], v[136:139], a[52:55]\n\t" \
  "v_exp_f32 v33, v33\n\t" \
  "v_mfma_f32_16x16x32_bf16 a[52:55], a[156:159], v[140:143], a[52:55]\n\t" \
  "v_exp_f32 v34, v34\n\t" \
  "v_mfma_f32_16x16x32_bf16 a[68:71], a[160:163], v[136:139], a[68:71]\n\t" \
  "v_exp_f32 v35, v35\n\t" \
  "v_mfma_f32_16x16x32_bf16 a[68:71], a[164:167], v[140:143], a[68:71]\n\t" \
  "buffer_load_dwordx4 v224, %[sK], s96 offen offset:0 lds\n\t" \
  "ds_read_b128 a[220:223], v223 offset:20480\n\t" \
  "v_mfma_f32_16x16x32_bf16 a[84:87], a[168:171], v[136:139], a[84:87]\n\t" \
  "v_exp_f32 v36, v36\n\t" \
  "v_mfma_f32_16x16x32_bf16 a[84:87], a[172:175], v[140:143], a[84:87]\n\t" \
  "v_exp_f32 v37, v37\n\t" \
  "v_mfma_f32_16x16x32_bf16 a[100:103], a[176:179], v[136:139], a[100:103]\n\t" \
  "buffer_load_dwordx4 v225, %[sK], s96 offen offset:1024 lds\n\t" \
  "ds_read_b128 a[224:227], v220 offset:24576\n\t" \
  "v_mfma_f32_16x16x32_bf16 a[100:103], a[180:183], v[140:143], a[100:103]\n\t" \
  "v_exp_f32 v38, v38\n\t" \
  "v_mfma_f32_16x16x32_bf16 a[116:119], a[184:187], v[136:139], a[116:119]\n\t" \
  "v_exp_f32 v39, v39\n\t" \
  "v_mfma_f32_16x16x32_bf16 a[116:119], a[188:191], v[140:143], a[116:119]\n\t" \
  "v_exp_f32 v40, v40\n\t" \
  "v_mfma_f32_16x16x32_bf16 v[164:167], v[216:219], v[136:139], v[164:167]\n\t" \
  "buffer_load_dwordx4 v226, %[sK], s96 offen offset:2048 lds\n\t" \
  "ds_read_b128 a[228:231], v221 offset:24576\n\t" \
  "v_mfma_f32_16x16x32_bf16 v[164:167], v[216:219], v[140:143], v[164:167]\n\t" \
  "v_cvt_pk_bf16_f32 v136, v32, v33\n\t" \
  "v_cvt_pk_bf16_f32 v137, v34, v35\n\t" \
  "v_mfma_f32_16x16x32_bf16 a[8:11], a[128:131], v[144:147], a[8:11]\n\t" \
  "v_cvt_pk_bf16_f32 v138, v36, v37\n\t" \
  "v_cvt_pk_bf16_f32 v139, v38, v39\n\t" \
  "v_mfma_f32_16x16x32_bf16 a[8:11], a[132:135], v[148:151], a[8:11]\n\t" \
  "buffer_load_dwordx4 v227, %[sK], s96 offen offset:3072 lds\n\t" \
  "ds_read_b128 a[232:235], v222 offset:24576\n\t" \
  "v_mfma_f32_16x16x32_bf16 a[24:27], a[136:139], v[144:147], a[24:27]\n\t" \
  "v_exp_f32 v41, v41\n\t" \
  "v_mfma_f32_16x16x32_bf16 a[24:27], a[140:143], v[148:151], a[24:27]\n\t" \
  "v_cvt_pk_bf16_f32 v140, v40, v41\n\t" \
  "s_add_u32 m0, %[m], 147456\n\t" \
  "v_mfma_f32_16x16x32_bf16 a[40:43], a[144:147], v[144:147], a[40:43]\n\t" \
  "v_exp_f32 v42, v42\n\t" \
  "v_mfma_f32_16x16x32_bf16 a[40:43], a[148:151], v[148:151], a[40:43]\n\t" \
  "v_exp_f32 v43, v43\n\t" \
  "v_mfma_f32_16x16x32_bf16 a[56:59], a[152:155], v[144:147], a[56:59]\n\t" \
  "v_cvt_pk_bf16_f32 v141, v42, v43\n\t" \
  "s_add_u32 s96, %[sofk], 49152\n\t" \
  "v_mfma_f32_16x16x32_bf16 a[56:59], a[156:159], v[148:151], a[56:59]\n\t" \
  "v_exp_f32 v44, v44\n\t" \
  "v_mfma_f32_16x16x32_bf16 a[72:75], a[160:163], v[144:147], a[72:75]\n\t" \
  "ds_read_b128 a[236:239], v223 offset:24576\n\t" \
  "buffer_load_dwordx4 v230, %[sV], s96 offen offset:0 lds\n\t" \
  "v_mfma_f32_16x16x32_bf16 a[72:75], a[164:167], v[148:151], a[72:75]\n\t" \
  "v_exp_f32 v45, v45\n\t" \
  "v_mfma_f32_16x16x32_bf16 a[88:91], a[168:171], v[144:147], a[88:91]\n\t" \
  "v_cvt_pk_bf16_f32 v142, v44, v45\n\t" \
  "ds_read_b128 a[240:243], v220 offset:28672\n\t" \
  "v_mfma_f32_16x16x32_bf16 a[88:91], a[172:175], v[148:151], a[88:91]\n\t" \
  "v_exp_f32 v46, v46\n\t" \
  "v_mfma_f32_16x16x32_bf16 a[104:107], a[176:179], v[144:147], a[104:107]\n\t" \
  "v_exp_f32 v47, v47\n\t" \
  "v_mfma_f32_16x16x32_bf16 a[104:107], a[180:183], v[148:151], a[104:107]\n\t" \
  "v_cvt_pk_bf16_f32 v143, v46, v47\n\t" \
  "buffer_load_dwordx4 v231, %[sV], s96 offen offset:1024 lds\n\t" \
  "v_mfma_f32_16x16x32_bf16 a[120:123], a[184:187], v[144:147], a[120:123]\n\t" \
  "ds_read_b128 a[244:247], v221 offset:28672\n\t" \
  "buffer_load_dwordx4 v230, %[sV], s96 offen offset:2048 lds\n\t" \
  "v_mfma_f32_16x16x32_bf16 a[120:123], a[188:191], v[148:151], a[120:123]\n\t" \
  "v_exp_f32 v64, v64\n\t" \
  "v_mfma_f32_16x16x32_bf16 v[168:171], v[216:219], v[144:147], v[168:171]\n\t" \
  "v_exp_f32 v65, v65\n\t" \
  "v_mfma_f32_16x16x32_bf16 v[168:171], v[216:219], v[148:151], v[168:171]\n\t" \
  "v_cvt_pk_bf16_f32 v144, v64, v65\n\t" \
  "ds_read_b128 a[248:251], v222 offset:28672\n\t" \
  "v_mfma_f32_16x16x32_bf16 a[12:15], a[128:131], v[152:155], a[12:15]\n\t" \
  "v_exp_f32 v66, v66\n\t" \
  "v_mfma_f32_16x16x32_bf16 a[12:15], a[132:135], v[156:159], a[12:15]\n\t" \
  "buffer_load_dwordx4 v231, %[sV], s96 offen offset:3072 lds\n\t" \
  "ds_read_b128 a[252:255], v223 offset:28672\n\t" \
  "v_mfma_f32_16x16x32_bf16 a[28:31], a[136:139], v[152:155], a[28:31]\n\t" \
  "v_exp_f32 v67, v67\n\t" \
  "v_mfma_f32_16x16x32_bf16 a[28:31], a[140:143], v[156:159], a[28:31]\n\t" \
  "v_cvt_pk_bf16_f32 v145, v66, v67\n\t" \
  "v_mfma_f32_16x16x32_bf16 a[44:47], a[144:147], v[152:155], a[44:47]\n\t" \
  "s_add_u32 m0, %[m], 98304\n\t" \
  "s_mov_b32 s96, 28672\n\t" \
  "buffer_load_dwordx4 v232, %[sG], s96 offen offset:0 lds\n\t" \
  "v_mfma_f32_16x16x32_bf16 a[44:47], a[148:151], v[156:159], a[44:47]\n\t" \
  "v_exp_f32 v68, v68\n\t" \
  "v_mfma_f32_16x16x32_bf16 a[60:63], a[152:155], v[152:155], a[60:63]\n\t" \
  "v_exp_f32 v69, v69\n\t" \
  "v_mfma_f32_16x16x32_bf16 a[60:63], a[156:159], v[156:159], a[60:63]\n\t" \
  "v_cvt_pk_bf16_f32 v146, v68, v69\n\t" \
  "v_mfma_f32_16x16x32_bf16 a[76:79], a[160:163], v[152:155], a[76:79]\n\t" \
  "s_mov_b32 s96, 34816\n\t" \
  "buffer_load_dwordx4 v232, %[sG], s96 offen offset:1024 lds\n\t" \
  "v_mfma_f32_16x16x32_bf16 a[76:79], a[164:167], v[156:159], a[76:79]\n\t" \
  "v_exp_f32 v70, v70\n\t" \
  "v_mfma_f32_16x16x32_bf16 a[92:95], a[168:171], v[152:155], a[92:95]\n\t" \
  "s_mov_b32 s96, 40960\n\t" \
  "buffer_load_dwordx4 v232, %[sG], s96 offen offset:2048 lds\n\t" \
  "v_mfma_f32_16x16x32_bf16 a[92:95], a[172:175], v[156:159], a[92:95]\n\t" \
  "v_exp_f32 v71, v71\n\t" \
  "v_mfma_f32_16x16x32_bf16 a[108:111], a[176:179], v[152:155], a[108:111]\n\t" \
  "v_cvt_pk_bf16_f32 v147, v70, v71\n\t" \
  "v_mfma_f32_16x16x32_bf16 a[108:111], a[180:183], v[156:159], a[108:111]\n\t" \
  "s_mov_b32 s96, 47104\n\t" \
  "buffer_load_dwordx4 v232, %[sG], s96 offen offset:3072 lds\n\t" \
  "v_mfma_f32_16x16x32_bf16 a[124:127], a[184:187], v[152:155], a[124:127]\n\t" \
  "v_exp_f32 v72, v72\n\t" \
  "v_mfma_f32_16x16x32_bf16 a[124:127], a[188:191], v[156:159], a[124:127]\n\t" \
  "v_exp_f32 v73, v73\n\t" \
  "v_mfma_f32_16x16x32_bf16 v[172:175], v[216:219], v[152:155], v[172:175]\n\t" \
  "v_cvt_pk_bf16_f32 v148, v72, v73\n\t" \
  "v_mfma_f32_16x16x32_bf16 v[172:175], v[216:219], v[156:159], v[172:175]\n\t" \
  "v_exp_f32 v74, v74\n\t" \
  "s_cmp_lg_u32 s99, 0\n\t" \
  "s_cbranch_scc1 .Lsf_p2\n\t" \
  ".Lsf_q2:\n\t" \
  "s_waitcnt lgkmcnt(0)\n\t" \
  "v_mfma_f32_16x16x32_bf16 v[0:3], a[192:195], v[16:19], v[176:179]\n\t" \
  "s_mul_i32 s98, %[sofk], 5\n\t" \
  "s_xor_b32 s98, s98, 0\n\t" \
  "s_lshr_b32 s98, s98, 1\n\t" \
  "buffer_load_dword %[pfd], v204, %[sD], s98 offen\n\t" \
  "v_mfma_f32_16x16x32_bf16 v[0:3], a[196:199], v[20:23], v[0:3]\n\t" \
  "v_exp_f32 v75, v75\n\t" \
  "v_mfma_f32_16x16x32_bf16 v[0:3], a[200:203], v[24:27], v[0:3]\n\t" \
  "v_cvt_pk_bf16_f32 v149, v74, v75\n\t" \
  "ds_read_b64_tr_b16 a[128:129], v228 offset:0\n\t" \
  "v_mfma_f32_16x16x32_bf16 v[0:3], a[204:207], v[28:31], v[0:3]\n\t" \
  "v_exp_f32 v76, v76\n\t" \
  "v_mfma_f32_16x16x32_bf16 v[4:7], a[220:223], v[28:31], v[176:179]\n\t" \
  "ds_read_b64_tr_b16 a[130:131], v228 offset:4096\n\t" \
  "ds_read_b64_tr_b16 a[132:133], v228 offset:8192\n\t" \
  "v_mfma_f32_16x16x32_bf16 v[4:7], a[216:219], v[24:27], v[4:7]\n\t" \
  "v_exp_f32 v77, v77\n\t" \
  "v_mfma_f32_16x16x32_bf16 v[4:7], a[212:215], v[20:23], v[4:7]\n\t" \
  "v_cvt_pk_bf16_f32 v150, v76, v77\n\t" \
  "v_max3_f32 v192, v0, v1, v2\n\t" \
  "v_mfma_f32_16x16x32_bf16 v[4:7], a[208:211], v[16:19], v[4:7]\n\t" \
  "v_exp_f32 v78, v78\n\t" \
  "v_mfma_f32_16x16x32_bf16 v[8:11], a[224:227], v[16:19], v[176:179]\n\t" \
  "ds_read_b64_tr_b16 a[134:135], v228 offset:12288\n\t" \
  "ds_read_b64_tr_b16 a[136:137], v229 offset:0\n\t" \
  "v_mfma_f32_16x16x32_bf16 v[8:11], a[228:231], v[20:23], v[8:11]\n\t" \
  "v_exp_f32 v79, v79\n\t" \
  "v_mfma_f32_16x16x32_bf16 v[8:11], a[232:235], v[24:27], v[8:11]\n\t" \
  "v_cvt_pk_bf16_f32 v151, v78, v79\n\t" \
  "v_max3_f32 v203, v3, v4, v5\n\t" \
  "v_mfma_f32_16x16x32_bf16 v[8:11], a[236:239], v[28:31], v[8:11]\n\t" \
  "v_max3_f32 v192, v192, v6, v7\n\t" \
  "ds_read_b64_tr_b16 a[138:139], v229 offset:4096\n\t" \
  "v_mfma_f32_16x16x32_bf16 v[12:15], a[252:255], v[28:31], v[176:179]\n\t" \
  "ds_read_b64_tr_b16 a[140:141], v229 offset:8192\n\t" \
  "ds_read_b64_tr_b16 a[142:143], v229 offset:12288\n\t" \
  "v_mfma_f32_16x16x32_bf16 v[12:15], a[248:251], v[24:27], v[12:15]\n\t" \
  "v_exp_f32 v96, v96\n\t" \
  "v_mfma_f32_16x16x32_bf16 v[12:15], a[244:247], v[20:23], v[12:15]\n\t" \
  "v_max3_f32 v203, v203, v8, v9\n\t" \
  "v_max3_f32 v192, v192, v10, v11\n\t" \
  "v_mfma_f32_16x16x32_bf16 v[12:15], a[240:243], v[16:19], v[12:15]\n\t" \
  "s_mul_i32 s98, %[sofk], 5\n\t" \
  "s_xor_b32 s98, s98, 4096\n\t" \
  "s_lshr_b32 s98, s98, 1\n\t" \
  "buffer_load_dword %[pfd], v204, %[sD], s98 offen\n\t" \
  "v_mfma_f32_16x16x32_bf16 v[32:35], a[192:195], v[48:51], v[180:183]\n\t" \
  "ds_read_b64_tr_b16 a[144:145], v228 offset:256\n\t" \
  "ds_read_b64_tr_b16 a[146:147], v228 offset:4352\n\t" \
  "v_mfma_f32_16x16x32_bf16 v[32:35], a[196:199], v[52:55], v[32:35]\n\t" \
  "v_exp_f32 v97, v97\n\t" \
  "v_mfma_f32_16x16x32_bf16 v[32:35], a[200:203], v[56:59], v[32:35]\n\t" \
  "v_cvt_pk_bf16_f32 v152, v96, v97\n\t" \
  "v_max3_f32 v203, v203, v12, v13\n\t" \
  "v_mfma_f32_16x16x32_bf16 v[32:35], a[204:207], v[60:63], v[32:35]\n\t" \
  "v_max3_f32 v192, v192, v14, v15\n\t" \
  "v_max_f32 v192, v192, v203\n\t" \
  "v_mfma_f32_16x16x32_bf16 v[36:39], a[220:223], v[60:63], v[180:183]\n\t" \
  "v_cmp_lt_f32 vcc, 0x42800000, v192\n\t" \
  "ds_read_b64_tr_b16 a[148:149], v228 offset:8448\n\t" \
  "v_mfma_f32_16x16x32_bf16 v[36:39], a[216:219], v[56:59], v[36:39]\n\t" \
  "s_cbranch_vccnz .Lsf_c30\n\t" \
  ".Lsf_r30:\n\t" \
  "ds_read_b64_tr_b16 a[150:151], v228 offset:12544\n\t" \
  "v_mfma_f32_16x16x32_bf16 v[36:39], a[212:215], v[52:55], v[36:39]\n\t" \
  "v_max3_f32 v192, v32, v33, v34\n\t" \
  "ds_read_b64_tr_b16 a[152:153], v229 offset:256\n\t" \
  "v_mfma_f32_16x16x32_bf16 v[36:39], a[208:211], v[48:51], v[36:39]\n\t" \
  "v_exp_f32 v98, v98\n\t" \
  "v_mfma_f32_16x16x32_bf16 v[40:43], a[224:227], v[48:51], v[180:183]\n\t" \
  "v_exp_f32 v99, v99\n\t" \
  "v_mfma_f32_16x16x32_bf16 v[40:43], a[228:231], v[52:55], v[40:43]\n\t" \
  "v_cvt_pk_bf16_f32 v153, v98, v99\n\t" \
  "ds_read_b64_tr_b16 a[154:155], v229 offset:4352\n\t" \
  "v_mfma_f32_16x16x32_bf16 v[40:43], a[232:235], v[56:59], v[40:43]\n\t" \
  "v_exp_f32 v100, v100\n\t" \
  "v_mfma_f32_16x16x32_bf16 v[40:43], a[236:239], v[60:63], v[40:43]\n\t" \
  "v_max3_f32 v203, v35, v36, v37\n\t" \
  "v_max3_f32 v192, v192, v38, v39\n\t" \
  "v_mfma_f32_16x16x32_bf16 v[44:47], a[252:255], v[60:63], v[180:183]\n\t" \
  "ds_read_b64_tr_b16 a[156:157], v229 offset:8448\n\t" \
  "ds_read_b64_tr_b16 a[158:159], v229 offset:12544\n\t" \
  "v_mfma_f32_16x16x32_bf16 v[44:47], a[248:251], v[56:59], v[44:47]\n\t" \
  "v_exp_f32 v101, v101\n\t" \
  "v_mfma_f32_16x16x32_bf16 v[44:47], a[244:247], v[52:55], v[44:47]\n\t" \
  "v_cvt_pk_bf16_f32 v154, v100, v101\n\t" \
  "ds_read_b64_tr_b16 a[160:161], v228 offset:512\n\t" \
  "v_mfma_f32_16x16x32_bf16 v[44:47], a[240:243], v[48:51], v[44:47]\n\t" \
  "v_max3_f32 v203, v203, v40, v41\n\t" \
  "v_max3_f32 v192, v192, v42, v43\n\t" \
  "v_mfma_f32_16x16x32_bf16 v[64:67], a[192:195], v[80:83], v[184:187]\n\t" \
  "ds_read_b64_tr_b16 a[162:163], v228 offset:4608\n\t" \
  "ds_read_b64_tr_b16 a[164:165], v228 offset:8704\n\t" \
  "v_mfma_f32_16x16x32_bf16 v[64:67], a[196:199], v[84:87], v[64:67]\n\t" \
  "v_exp_f32 v102, v102\n\t" \
  "v_mfma_f32_16x16x32_bf16 v[64:67], a[200:203], v[88:91], v[64:67]\n\t" \
  "v_max3_f32 v203, v203, v44, v45\n\t" \
  "v_max3_f32 v192, v192, v46, v47\n\t" \
  "v_mfma_f32_16x16x32_bf16 v[64:67], a[204:207], v[92:95], v[64:67]\n\t" \
  "v_max_f32 v192, v192, v203\n\t" \
  "ds_read_b64_tr_b16 a[166:167], v228 offset:12800\n\t" \
  "v_mfma_f32_16x16x32_bf16 v[68:71], a[220:223], v[92:95], v[184:187]\n\t" \
  "v_cmp_lt_f32 vcc, 0x42800000, v192\n\t" \
  "ds_read_b64_tr_b16 a[168:169], v229 offset:512\n\t" \
  "v_mfma_f32_16x16x32_bf16 v[68:71], a[216:219], v[88:91], v[68:71]\n\t" \
  "s_cbranch_vccnz .Lsf_c31\n\t" \
  ".Lsf_r31:\n\t" \
  "ds_read_b64_tr_b16 a[170:171], v229 offset:4608\n\t" \
  "v_mfma_f32_16x16x32_bf16 v[68:71], a[212:215], v[84:87], v[68:71]\n\t" \
  "v_max3_f32 v192, v64, v65, v66\n\t" \
  "ds_read_b64_tr_b16 a[172:173], v229 offset:8704\n\t" \
  "v_mfma_f32_16x16x32_bf16 v[68:71], a[208:211], v[80:83], v[68:71]\n\t" \
  "v_exp_f32 v103, v103\n\t" \
  "v_mfma_f32_16x16x32_bf16 v[72:75], a[224:227], v[80:83], v[184:187]\n\t" \
  "v_cvt_pk_bf16_f32 v155, v102, v103\n\t" \
  "ds_read_b64_tr_b16 a[174:175], v229 offset:12800\n\t" \
  "v_mfma_f32_16x16x32_bf16 v[72:75], a[228:231], v[84:87], v[72:75]\n\t" \
  "v_exp_f32 v104, v104\n\t" \
  "v_mfma_f32_16x16x32_bf16 v[72:75], a[232:235], v[88:91], v[72:75]\n\t" \
  "v_exp_f32 v105, v105\n\t" \
  "v_mfma_f32_16x16x32_bf16 v[72:75], a[236:239], v[92:95], v[72:75]\n\t" \
  "v_cvt_pk_bf16_f32 v156, v104, v105\n\t" \
  "v_max3_f32 v203, v67, v68, v69\n\t" \
  "v_mfma_f32_16x16x32_bf16 v[76:79], a[252:255], v[92:95], v[184:187]\n\t" \
  "v_max3_f32 v192, v192, v70, v71\n\t" \
  "ds_read_b64_tr_b16 a[176:177], v228 offset:768\n\t" \
  "v_mfma_f32_16x16x32_bf16 v[76:79], a[248:251], v[88:91], v[76:79]\n\t" \
  "v_exp_f32 v106, v106\n\t" \
  "v_mfma_f32_16x16x32_bf16 v[76:79], a[244:247], v[84:87], v[76:79]\n\t" \
  "ds_read_b64_tr_b16 a[178:179], v228 offset:4864\n\t" \
  "v_max3_f32 v203, v203, v72, v73\n\t" \
  "v_mfma_f32_16x16x32_bf16 v[76:79], a[240:243], v[80:83], v[76:79]\n\t" \
  "v_max3_f32 v192, v192, v74, v75\n\t" \
  "ds_read_b64_tr_b16 a[180:181], v228 offset:8960\n\t" \
  "v_mfma_f32_16x16x32_bf16 v[96:99], a[192:195], v[112:115], v[188:191]\n\t" \
  "v_exp_f32 v107, v107\n\t" \
  "v_mfma_f32_16x16x32_bf16 v[96:99], a[196:199], v[116:119], v[96:99]\n\t" \
  "v_cvt_pk_bf16_f32 v157, v106, v107\n\t" \
  "ds_read_b64_tr_b16 a[182:183], v228 offset:13056\n\t" \
  "v_mfma_f32_16x16x32_bf16 v[96:99], a[200:203], v[120:123], v[96:99]\n\t" \
  "v_max3_f32 v203, v203, v76, v77\n\t" \
  "v_max3_f32 v192, v192, v78, v79\n\t" \
  "v_mfma_f32_16x16x32_bf16 v[96:99], a[204:207], v[124:127], v[96:99]\n\t" \
  "v_max_f32 v192, v192, v203\n\t" \
  "ds_read_b64_tr_b16 a[184:185], v229 offset:768\n\t" \
  "v_mfma_f32_16x16x32_bf16 v[100:103], a[220:223], v[124:127], v[188:191]\n\t" \
  "v_cmp_lt_f32 vcc, 0x42800000, v192\n\t" \
  "ds_read_b64_tr_b16 a[186:187], v229 offset:4864\n\t" \
  "v_mfma_f32_16x16x32_bf16 v[100:103], a[216:219], v[120:123], v[100:103]\n\t" \
  "s_cbranch_vccnz .Lsf_c32\n\t" \
  ".Lsf_r32:\n\t" \
  "ds_read_b64_tr_b16 a[188:189], v229 offset:8960\n\t" \
  "v_mfma_f32_16x16x32_bf16 v[100:103], a[212:215], v[116:119], v[100:103]\n\t" \
  "v_max3_f32 v192, v96, v97, v98\n\t" \
  "ds_read_b64_tr_b16 a[190:191], v229 offset:13056\n\t" \
  "v_mfma_f32_16x16x32_bf16 v[100:103], a[208:211], v[112:115], v[100:103]\n\t" \
  "v_exp_f32 v108, v108\n\t" \
  "v_mfma_f32_16x16x32_bf16 v[104:107], a[224:227], v[112:115], v[188:191]\n\t" \
  "v_exp_f32 v109, v109\n\t" \
  "v_mfma_f32_16x16x32_bf16 v[104:107], a[228:231], v[116:119], v[104:107]\n\t" \
  "v_cvt_pk_bf16_f32 v158, v108, v109\n\t" \
  "v_mfma_f32_16x16x32_bf16 v[104:107], a[232:235], v[120:123], v[104:107]\n\t" \
  "v_exp_f32 v110, v110\n\t" \
  "v_mfma_f32_16x16x32_bf16 v[104:107], a[236:239], v[124:127], v[104:107]\n\t" \
  "v_max3_f32 v203, v99, v100, v101\n\t" \
  "v_max3_f32 v192, v192, v102, v103\n\t" \
  "v_exp_f32 v111, v111\n\t" \
  "s_nop 0\n\t" \
  "v_cvt_pk_bf16_f32 v159, v110, v111\n\t" \
  "v_mfma_f32_16x16x32_bf16 v[108:111], a[252:255], v[124:127], v[188:191]\n\t" \
  "v_exp_f32 v0, v0\n\t" \
  "v_mfma_f32_16x16x32_bf16 v[108:111], a[248:251], v[120:123], v[108:111]\n\t" \
  "v_exp_f32 v1, v1\n\t" \
  "v_mfma_f32_16x16x32_bf16 v[108:111], a[244:247], v[116:119], v[108:111]\n\t" \
  "v_max3_f32 v203, v203, v104, v105\n\t" \
  "v_max3_f32 v192, v192, v106, v107\n\t" \
  "v_mfma_f32_16x16x32_bf16 v[108:111], a[240:243], v[112:115], v[108:111]\n\t" \
  "v_exp_f32 v2, v2\n\t" \
  "s_waitcnt vmcnt(6) lgkmcnt(0)\n\t" \
  "s_barrier\n\t" \
  "v_mfma_f32_16x16x32_bf16 a[0:3], a[128:131], v[128:131], a[0:3]\n\t" \
  "v_exp_f32 v3, v3\n\t" \
  "v_mfma_f32_16x16x32_bf16 a[0:3], a[132:135], v[132:135], a[0:3]\n\t" \
  "v_exp_f32 v4, v4\n\t" \
  "v_mfma_f32_16x16x32_bf16 a[16:19], a[136:139], v[128:131], a[16:19]\n\t" \
  "ds_read_b128 a[192:195], v220 offset:0\n\t" \
  "v_max3_f32 v203, v203, v108, v109\n\t" \
  "v_mfma_f32_16x16x32_bf16 a[16:19], a[140:143], v[132:135], a[16:19]\n\t" \
  "v_max3_f32 v192, v192, v110, v111\n\t" \
  "v_max_f32 v192, v192, v203\n\t" \
  "v_mfma_f32_16x16x32_bf16 a[32:35], a[144:147], v[128:131], a[32:35]\n\t" \
  "v_cmp_lt_f32 vcc, 0x42800000, v192\n\t" \
  "ds_read_b128 a[196:199], v221 offset:0\n\t" \
  "v_mfma_f32_16x16x32_bf16 a[32:35], a[148:151], v[132:135], a[32:35]\n\t" \
  "s_cbranch_vccnz .Lsf_c33\n\t" \
  ".Lsf_r33:\n\t" \
  "ds_read_b128 a[200:203], v222 offset:0\n\t" \
  "v_mfma_f32_16x16x32_bf16 a[48:51], a[152:155], v[128:131], a[48:51]\n\t" \
  "v_exp_f32 v5, v5\n\t" \
  "v_mfma_f32_16x16x32_bf16 a[48:51], a[156:159], v[132:135], a[48:51]\n\t" \
  "v_exp_f32 v6, v6\n\t" \
  "v_mfma_f32_16x16x32_bf16 a[64:67], a[160:163], v[128:131], a[64:67]\n\t" \
  "v_exp_f32 v7, v7\n\t" \
  "v_mfma_f32_16x16x32_bf16 a[64:67], a[164:167], v[132:135], a[64:67]\n\t" \
  "v_exp_f32 v8, v8\n\t" \
  "v_mfma_f32_16x16x32_bf16 a[80:83], a[168:171], v[128:131], a[80:83]\n\t" \
  "v_exp_f32 v9, v9\n\t" \
  "v_mfma_f32_16x16x32_bf16 a[80:83], a[172:175], v[132:135], a[80:83]\n\t" \
  "v_exp_f32 v10, v10\n\t" \
  "v_mfma_f32_16x16x32_bf16 a[96:99], a[176:179], v[128:131], a[96:99]\n\t" \
  "v_exp_f32 v11, v11\n\t" \
  "v_mfma_f32_16x16x32_bf16 a[96:99], a[180:183], v[132:135], a[96:99]\n\t" \
  "v_exp_f32 v12, v12\n\t" \
  "v_mfma_f32_16x16x32_bf16 a[112:115], a[184:187], v[128:131], a[112:115]\n\t" \
  "ds_read_b128 a[204:207], v223 offset:0\n\t" \
  "ds_read_b128 a[208:211], v220 offset:4096\n\t" \
  "v_mfma_f32_16x16x32_bf16 a[112:115], a[188:191], v[132:135], a[112:115]\n\t" \
  "v_exp_f32 v13, v13\n\t" \
  "v_mfma_f32_16x16x32_bf16 v[160:163], v[216:219], v[128:131], v[160:163]\n\t" \
  "v_exp_f32 v14, v14\n\t" \
  "v_mfma_f32_16x16x32_bf16 v[160:163], v[216:219], v[132:135], v[160:163]\n\t" \
  "v_cvt_pk_bf16_f32 v128, v0, v1\n\t" \
  "v_cvt_pk_bf16_f32 v129, v2, v3\n\t" \
  "v_mfma_f32_16x16x32_bf16 a[4:7], a[128:131], v[136:139], a[4:7]\n\t" \
  "v_cvt_pk_bf16_f32 v130, v4, v5\n\t" \
  "v_cvt_pk_bf16_f32 v131, v6, v7\n\t" \
  "v_mfma_f32_16x16x32_bf16 a[4:7], a[132:135], v[140:143], a[4:7]\n\t" \
  "v_cvt_pk_bf16_f32 v132, v8, v9\n\t" \
  "v_cvt_pk_bf16_f32 v133, v10, v11\n\t" \
  "v_mfma_f32_16x16x32_bf16 a[20:23], a[136:139], v[136:139], a[20:23]\n\t" \
  "v_cvt_pk_bf16_f32 v134, v12, v13\n\t" \
  "ds_read_b128 a[212:215], v221 offset:4096\n\t" \
  "v_mfma_f32_16x16x32_bf16 a[20:23], a[140:143], v[140:143], a[20:23]\n\t" \
  "v_exp_f32 v15, v15\n\t" \
  "v_mfma_f32_16x16x32_bf16 a[36:39], a[144:147], v[136:139], a[36:39]\n\t" \
  "v_cvt_pk_bf16_f32 v135, v14, v15\n\t" \
  "s_add_u32 m0, %[m], 65536\n\t" \
  "v_mfma_f32_16x16x32_bf16 a[36:39], a[148:151], v[140:143], a[36:39]\n\t" \
  "v_exp_f32 v32, v32\n\t" \
  "v_mfma_f32_16x16x32_bf16 a[52:55], a[152:155], v[136:139], a[52:55]\n\t" \
  "ds_read_b128 a[216:219], v222 offset:4096\n\t" \
  "s_add_u32 s96, %[sofk], 81920\n\t" \
  "v_mfma_f32_16x16x32_bf16 a[52:55], a[156:159], v[140:143], a[52:55]\n\t" \
  "v_exp_f32 v33, v33\n\t" \
  "v_mfma_f32_16x16x32_bf16 a[68:71], a[160:163], v[136:139], a[68:71]\n\t" \
  "v_exp_f32 v34, v34\n\t" \
  "v_mfma_f32_16x16x32_bf16 a[68:71], a[164:167], v[140:143], a[68:71]\n\t" \
  "ds_read_b128 a[220:223], v223 offset:4096\n\t" \
  "buffer_load_dwordx4 v224, %[sK], s96 offen offset:0 lds\n\t" \
  "v_mfma_f32_16x16x32_bf16 a[84:87], a[168:171], v[136:139], a[84:87]\n\t" \
  "v_exp_f32 v35, v35\n\t" \
  "v_mfma_f32_16x16x32_bf16 a[84:87], a[172:175], v[140:143], a[84:87]\n\t" \
  "v_exp_f32 v36, v36\n\t" \
  "v_mfma_f32_16x16x32_bf16 a[100:103], a[176:179], v[136:139], a[100:103]\n\t" \
  "buffer_load_dwordx4 v225, %[sK], s96 offen offset:1024 lds\n\t" \
  "ds_read_b128 a[224:227], v220 offset:8192\n\t" \
  "v_mfma_f32_16x16x32_bf16 a[100:103], a[180:183], v[140:143], a[100:103]\n\t" \
  "v_exp_f32 v37, v37\n\t" \
  "v_mfma_f32_16x16x32_bf16 a[116:119], a[184:187], v[136:139], a[116:119]\n\t" \
  "v_exp_f32 v38, v38\n\t" \
  "v_mfma_f32_16x16x32_bf16 a[116:119], a[188:191], v[140:143], a[116:119]\n\t" \
  "v_exp_f32 v39, v39\n\t" \
  "v_mfma_f32_16x16x32_bf16 v[164:167], v[216:219], v[136:139], v[164:167]\n\t" \
  "buffer_load_dwordx4 v226, %[sK], s96 offen offset:2048 lds\n\t" \
  "ds_read_b128 a[228:231], v221 offset:8192\n\t" \
  "v_mfma_f32_16x16x32_bf16 v[164:167], v[216:219], v[140:143], v[164:167]\n\t" \
  "v_cvt_pk_bf16_f32 v136, v32, v33\n\t" \
  "v_cvt_pk_bf16_f32 v137, v34, v35\n\t" \
  "v_mfma_f32_16x16x32_bf16 a[8:11], a[128:131], v[144:147], a[8:11]\n\t" \
  "v_cvt_pk_bf16_f32 v138, v36, v37\n\t" \
  "v_cvt_pk_bf16_f32 v139, v38, v39\n\t" \
  "v_mfma_f32_16x16x32_bf16 a[8:11], a[132:135], v[148:151], a[8:11]\n\t" \
  "v_exp_f32 v40, v40\n\t" \
  "v_mfma_f32_16x16x32_bf16 a[24:27], a[136:139], v[144:147], a[24:27]\n\t" \
  "buffer_load_dwordx4 v227, %[sK], s96 offen offset:3072 lds\n\t" \
  "ds_read_b128 a[232:235], v222 offset:8192\n\t" \
  "v_mfma_f32_16x16x32_bf16 a[24:27], a[140:143], v[148:151], a[24:27]\n\t" \
  "v_exp_f32 v41, v41\n\t" \
  "v_mfma_f32_16x16x32_bf16 a[40:43], a[144:147], v[144:147], a[40:43]\n\t" \
  "v_cvt_pk_bf16_f32 v140, v40, v41\n\t" \
  "s_add_u32 m0, %[m], 81920\n\t" \
  "v_mfma_f32_16x16x32_bf16 a[40:43], a[148:151], v[148:151], a[40:43]\n\t" \
  "v_exp_f32 v42, v42\n\t" \
  "v_mfma_f32_16x16x32_bf16 a[56:59], a[152:155], v[144:147], a[56:59]\n\t" \
  "v_exp_f32 v43, v43\n\t" \
  "v_mfma_f32_16x16x32_bf16 a[56:59], a[156:159], v[148:151], a[56:59]\n\t" \
  "v_cvt_pk_bf16_f32 v141, v42, v43\n\t" \
  "ds_read_b128 a[236:239], v223 offset:8192\n\t" \
  "v_mfma_f32_16x16x32_bf16 a[72:75], a[160:163], v[144:147], a[72:75]\n\t" \
  "v_exp_f32 v44, v44\n\t" \
  "v_mfma_f32_16x16x32_bf16 a[72:75], a[164:167], v[148:151], a[72:75]\n\t" \
  "s_add_u32 s96, %[sofk], 65536\n\t" \
  "ds_read_b128 a[240:243], v220 offset:12288\n\t" \
  "v_mfma_f32_16x16x32_bf16 a[88:91], a[168:171], v[144:147], a[88:91]\n\t" \
  "v_exp_f32 v45, v45\n\t" \
  "v_mfma_f32_16x16x32_bf16 a[88:91], a[172:175], v[148:151], a[88:91]\n\t" \
  "v_cvt_pk_bf16_f32 v142, v44, v45\n\t" \
  "buffer_load_dwordx4 v230, %[sV], s96 offen offset:0 lds\n\t" \
  "v_mfma_f32_16x16x32_bf16 a[104:107], a[176:179], v[144:147], a[104:107]\n\t" \
  "v_exp_f32 v46, v46\n\t" \
  "v_mfma_f32_16x16x32_bf16 a[104:107], a[180:183], v[148:151], a[104:107]\n\t" \
  "ds_read_b128 a[244:247], v221 offset:12288\n\t" \
  "buffer_load_dwordx4 v231, %[sV], s96 offen offset:1024 lds\n\t" \
  "v_mfma_f32_16x16x32_bf16 a[120:123], a[184:187], v[144:147], a[120:123]\n\t" \
  "v_exp_f32 v47, v47\n\t" \
  "v_mfma_f32_16x16x32_bf16 a[120:123], a[188:191], v[148:151], a[120:123]\n\t" \
  "v_cvt_pk_bf16_f32 v143, v46, v47\n\t" \
  "buffer_load_dwordx4 v230, %[sV], s96 offen offset:2048 lds\n\t" \
  "v_mfma_f32_16x16x32_bf16 v[168:171], v[216:219], v[144:147], v[168:171]\n\t" \
  "v_exp_f32 v64, v64\n\t" \
  "v_mfma_f32_16x16x32_bf16 v[168:171], v[216:219], v[148:151], v[168:171]\n\t" \
  "v_exp_f32 v65, v65\n\t" \
  "v_mfma_f32_16x16x32_bf16 a[12:15], a[128:131], v[152:155], a[12:15]\n\t" \
  "v_cvt_pk_bf16_f32 v144, v64, v65\n\t" \
  "ds_read_b128 a[248:251], v222 offset:12288\n\t" \
  "v_mfma_f32_16x16x32_bf16 a[12:15], a[132:135], v[156:159], a[12:15]\n\t" \
  "v_exp_f32 v66, v66\n\t" \
  "v_mfma_f32_16x16x32_bf16 a[28:31], a[136:139], v[152:155], a[28:31]\n\t" \
  "v_exp_f32 v67, v67\n\t" \
  "v_mfma_f32_16x16x32_bf16 a[28:31], a[140:143], v[156:159], a[28:31]\n\t" \
  "v_cvt_pk_bf16_f32 v145, v66, v67\n\t" \
  "buffer_load_dwordx4 v231, %[sV], s96 offen offset:3072 lds\n\t" \
  "v_mfma_f32_16x16x32_bf16 a[44:47], a[144:147], v[152:155], a[44:47]\n\t" \
  "v_exp_f32 v68, v68\n\t" \
  "v_mfma_f32_16x16x32_bf16 a[44:47], a[148:151], v[156:159], a[44:47]\n\t" \
  "ds_read_b128 a[252:255], v223 offset:12288\n\t" \
  "v_mfma_f32_16x16x32_bf16 a[60:63], a[152:155], v[152:155], a[60:63]\n\t" \
  "v_exp_f32 v69, v69\n\t" \
  "v_mfma_f32_16x16x32_bf16 a[60:63], a[156:159], v[156:159], a[60:63]\n\t" \
  "v_cvt_pk_bf16_f32 v146, v68, v69\n\t" \
  "v_mfma_f32_16x16x32_bf16 a[76:79], a[160:163], v[152:155], a[76:79]\n\t" \
  "s_add_u32 m0, %[m], 114688\n\t" \
  "s_mov_b32 s96, 0\n\t" \
  "buffer_load_dwordx4 v232, %[sU], s96 offen offset:0 lds\n\t" \
  "v_mfma_f32_16x16x32_bf16 a[76:79], a[164:167], v[156:159], a[76:79]\n\t" \
  "v_exp_f32 v70, v70\n\t" \
  "v_mfma_f32_16x16x32_bf16 a[92:95], a[168:171], v[152:155], a[92:95]\n\t" \
  "s_mov_b32 s96, 6144\n\t" \
  "buffer_load_dwordx4 v232, %[sU], s96 offen offset:1024 lds\n\t" \
  "v_mfma_f32_16x16x32_bf16 a[92:95], a[172:175], v[156:159], a[92:95]\n\t" \
  "v_exp_f32 v71, v71\n\t" \
  "v_mfma_f32_16x16x32_bf16 a[108:111], a[176:179], v[152:155], a[108:111]\n\t" \
  "v_cvt_pk_bf16_f32 v147, v70, v71\n\t" \
  "v_mfma_f32_16x16x32_bf16 a[108:111], a[180:183], v[156:159], a[108:111]\n\t" \
  "s_mov_b32 s96, 12288\n\t" \
  "buffer_load_dwordx4 v232, %[sU], s96 offen offset:2048 lds\n\t" \
  "v_mfma_f32_16x16x32_bf16 a[124:127], a[184:187], v[152:155], a[124:127]\n\t" \
  "v_exp_f32 v72, v72\n\t" \
  "v_mfma_f32_16x16x32_bf16 a[124:127], a[188:191], v[156:159], a[124:127]\n\t" \
  "v_exp_f32 v73, v73\n\t" \
  "v_mfma_f32_16x16x32_bf16 v[172:175], v[216:219], v[152:155], v[172:175]\n\t" \
  "v_cvt_pk_bf16_f32 v148, v72, v73\n\t" \
  "v_mfma_f32_16x16x32_bf16 v[172:175], v[216:219], v[156:159], v[172:175]\n\t" \
  "v_exp_f32 v74, v74\n\t" \
  "s_cmp_lg_u32 s99, 0\n\t" \
  "s_cbranch_scc1 .Lsf_p3\n\t" \
  ".Lsf_q3:\n\t" \
  "s_waitcnt lgkmcnt(0)\n\t" \
  "v_mfma_f32_16x16x32_bf16 v[0:3], a[192:195], v[16:19], v[176:179]\n\t" \
  "s_add_u32 s98, %[pf2], 0\n\t" \
  "buffer_load_dword %[pfd], v202, %[sM], s98 offen\n\t" \
  "v_mfma_f32_16x16x32_bf16 v[0:3], a[196:199], v[20:23], v[0:3]\n\t" \
  "v_exp_f32 v75, v75\n\t" \
  "v_mfma_f32_16x16x32_bf16 v[0:3], a[200:203], v[24:27], v[0:3]\n\t" \
  "v_cvt_pk_bf16_f32 v149, v74, v75\n\t" \
  "ds_read_b64_tr_b16 a[128:129], v237 offset:0\n\t" \
  "v_mfma_f32_16x16x32_bf16 v[0:3], a[204:207], v[28:31], v[0:3]\n\t" \
  "v_exp_f32 v76, v76\n\t" \
  "v_mfma_f32_16x16x32_bf16 v[4:7], a[220:223], v[28:31], v[176:179]\n\t" \
  "ds_read_b64_tr_b16 a[130:131], v237 offset:4096\n\t" \
  "ds_read_b64_tr_b16 a[132:133], v237 offset:8192\n\t" \
  "v_mfma_f32_16x16x32_bf16 v[4:7], a[216:219], v[24:27], v[4:7]\n\t" \
  "v_exp_f32 v77, v77\n\t" \
  "v_mfma_f32_16x16x32_bf16 v[4:7], a[212:215], v[20:23], v[4:7]\n\t" \
  "v_cvt_pk_bf16_f32 v150, v76, v77\n\t" \
  "v_max3_f32 v192, v0, v1, v2\n\t" \
  "v_mfma_f32_16x16x32_bf16 v[4:7], a[208:211], v[16:19], v[4:7]\n\t" \
  "v_exp_f32 v78, v78\n\t" \
  "v_mfma_f32_16x16x32_bf16 v[8:11], a[224:227], v[16:19], v[176:179]\n\t" \
  "ds_read_b64_tr_b16 a[134:135], v237 offset:12288\n\t" \
  "ds_read_b64_tr_b16 a[136:137], v238 offset:0\n\t" \
  "v_mfma_f32_16x16x32_bf16 v[8:11], a[228:231], v[20:23], v[8:11]\n\t" \
  "v_exp_f32 v79, v79\n\t" \
  "v_mfma_f32_16x16x32_bf16 v[8:11], a[232:235], v[24:27], v[8:11]\n\t" \
  "v_cvt_pk_bf16_f32 v151, v78, v79\n\t" \
  "v_max3_f32 v203, v3, v4, v5\n\t" \
  "v_mfma_f32_16x16x32_bf16 v[8:11], a[236:239], v[28:31], v[8:11]\n\t" \
  "v_max3_f32 v192, v192, v6, v7\n\t" \
  "ds_read_b64_tr_b16 a[138:139], v238 offset:4096\n\t" \
  "v_mfma_f32_16x16x32_bf16 v[12:15], a[252:255], v[28:31], v[176:179]\n\t" \
  "ds_read_b64_tr_b16 a[140:141], v238 offset:8192\n\t" \
  "ds_read_b64_tr_b16 a[142:143], v238 offset:12288\n\t" \
  "v_mfma_f32_16x16x32_bf16 v[12:15], a[248:251], v[24:27], v[12:15]\n\t" \
  "v_exp_f32 v96, v96\n\t" \
  "v_mfma_f32_16x16x32_bf16 v[12:15], a[244:247], v[20:23], v[12:15]\n\t" \
  "v_max3_f32 v203, v203, v8, v9\n\t" \
  "v_max3_f32 v192, v192, v10, v11\n\t" \
  "v_mfma_f32_16x16x32_bf16 v[12:15], a[240:243], v[16:19], v[12:15]\n\t" \
  "s_add_u32 s98, %[pf2], 24576\n\t" \
  "buffer_load_dword %[pfd], v202, %[sM], s98 offen\n\t" \
  "v_mfma_f32_16x16x32_bf16 v[32:35], a[192:195], v[48:51], v[180:183]\n\t" \
  "ds_read_b64_tr_b16 a[144:145], v237 offset:256\n\t" \
  "ds_read_b64_tr_b16 a[146:147], v237 offset:4352\n\t" \
  "v_mfma_f32_16x16x32_bf16 v[32:35], a[196:199], v[52:55], v[32:35]\n\t" \
  "v_exp_f32 v97, v97\n\t" \
  "v_mfma_f32_16x16x32_bf16 v[32:35], a[200:203], v[56:59], v[32:35]\n\t" \
  "v_cvt_pk_bf16_f32 v152, v96, v97\n\t" \
  "v_max3_f32 v203, v203, v12, v13\n\t" \
  "v_mfma_f32_16x16x32_bf16 v[32:35], a[204:207], v[60:63], v[32:35]\n\t" \
  "v_max3_f32 v192, v192, v14, v15\n\t" \
  "v_max_f32 v192, v192, v203\n\t" \
  "v_mfma_f32_16x16x32_bf16 v[36:39], a[220:223], v[60:63], v[180:183]\n\t" \
  "v_cmp_lt_f32 vcc, 0x42800000, v192\n\t" \
  "ds_read_b64_tr_b16 a[148:149], v237 offset:8448\n\t" \
  "v_mfma_f32_16x16x32_bf16 v[36:39], a[216:219], v[56:59], v[36:39]\n\t" \
  "s_cbranch_vccnz .Lsf_c40\n\t" \
  ".Lsf_r40:\n\t" \
  "ds_read_b64_tr_b16 a[150:151], v237 offset:12544\n\t" \
  "v_mfma_f32_16x16x32_bf16 v[36:39], a[212:215], v[52:55], v[36:39]\n\t" \
  "v_max3_f32 v192, v32, v33, v34\n\t" \
  "ds_read_b64_tr_b16 a[152:153], v238 offset:256\n\t" \
  "v_mfma_f32_16x16x32_bf16 v[36:39], a[208:211], v[48:51], v[36:39]\n\t" \
  "v_exp_f32 v98, v98\n\t" \
  "v_mfma_f32_16x16x32_bf16 v[40:43], a[224:227], v[48:51], v[180:183]\n\t" \
  "v_exp_f32 v99, v99\n\t" \
  "v_mfma_f32_16x16x32_bf16 v[40:43], a[228:231], v[52:55], v[40:43]\n\t" \
  "v_cvt_pk_bf16_f32 v153, v98, v99\n\t" \
  "ds_read_b64_tr_b16 a[154:155], v238 offset:4352\n\t" \
  "v_mfma_f32_16x16x32_bf16 v[40:43], a[232:235], v[56:59], v[40:43]\n\t" \
  "v_exp_f32 v100, v100\n\t" \
  "v_mfma_f32_16x16x32_bf16 v[40:43], a[236:239], v[60:63], v[40:43]\n\t" \
  "v_max3_f32 v203, v35, v36, v37\n\t" \
  "v_max3_f32 v192, v192, v38, v39\n\t" \
  "v_mfma_f32_16x16x32_bf16 v[44:47], a[252:255], v[60:63], v[180:183]\n\t" \
  "ds_read_b64_tr_b16 a[156:157], v238 offset:8448\n\t" \
  "ds_read_b64_tr_b16 a[158:159], v238 offset:12544\n\t" \
  "v_mfma_f32_16x16x32_bf16 v[44:47], a[248:251], v[56:59], v[44:47]\n\t" \
  "v_exp_f32 v101, v101\n\t" \
  "v_mfma_f32_16x16x32_bf16 v[44:47], a[244:247], v[52:55], v[44:47]\n\t" \
  "v_cvt_pk_bf16_f32 v154, v100, v101\n\t" \
  "ds_read_b64_tr_b16 a[160:161], v237 offset:512\n\t" \
  "v_mfma_f32_16x16x32_bf16 v[44:47], a[240:243], v[48:51], v[44:47]\n\t" \
  "v_max3_f32 v203, v203, v40, v41\n\t" \
  "v_max3_f32 v192, v192, v42, v43\n\t" \
  "v_mfma_f32_16x16x32_bf16 v[64:67], a[192:195], v[80:83], v[184:187]\n\t" \
  "ds_read_b64_tr_b16 a[162:163], v237 offset:4608\n\t" \
  "ds_read_b64_tr_b16 a[164:165], v237 offset:8704\n\t" \
  "v_mfma_f32_16x16x32_bf16 v[64:67], a[196:199], v[84:87], v[64:67]\n\t" \
  "v_exp_f32 v102, v102\n\t" \
  "v_mfma_f32_16x16x32_bf16 v[64:67], a[200:203], v[88:91], v[64:67]\n\t" \
  "v_max3_f32 v203, v203, v44, v45\n\t" \
  "v_max3_f32 v192, v192, v46, v47\n\t" \
  "v_mfma_f32_16x16x32_bf16 v[64:67], a[204:207], v[92:95], v[64:67]\n\t" \
  "v_max_f32 v192, v192, v203\n\t" \
  "ds_read_b64_tr_b16 a[166:167], v237 offset:12800\n\t" \
  "v_mfma_f32_16x16x32_bf16 v[68:71], a[220:223], v[92:95], v[184:187]\n\t" \
  "v_cmp_lt_f32 vcc, 0x42800000, v192\n\t" \
  "ds_read_b64_tr_b16 a[168:169], v238 offset:512\n\t" \
  "v_mfma_f32_16x16x32_bf16 v[68:71], a[216:219], v[88:91], v[68:71]\n\t" \
  "s_cbranch_vccnz .Lsf_c41\n\t" \
  ".Lsf_r41:\n\t" \
  "ds_read_b64_tr_b16 a[170:171], v238 offset:4608\n\t" \
  "v_mfma_f32_16x16x32_bf16 v[68:71], a[212:215], v[84:87], v[68:71]\n\t" \
  "v_max3_f32 v192, v64, v65, v66\n\t" \
  "ds_read_b64_tr_b16 a[172:173], v238 offset:8704\n\t" \
  "v_mfma_f32_16x16x32_bf16 v[68:71], a[208:211], v[80:83], v[68:71]\n\t" \
  "v_exp_f32 v103, v103\n\t" \
  "v_mfma_f32_16x16x32_bf16 v[72:75], a[224:227], v[80:83], v[184:187]\n\t" \
  "v_cvt_pk_bf16_f32 v155, v102, v103\n\t" \
  "ds_read_b64_tr_b16 a[174:175], v238 offset:12800\n\t" \
  "v_mfma_f32_16x16x32_bf16 v[72:75], a[228:231], v[84:87], v[72:75]\n\t" \
  "v_exp_f32 v104, v104\n\t" \
  "v_mfma_f32_16x16x32_bf16 v[72:75], a[232:235], v[88:91], v[72:75]\n\t" \
  "v_exp_f32 v105, v105\n\t" \
  "v_mfma_f32_16x16x32_bf16 v[72:75], a[236:239], v[92:95], v[72:75]\n\t" \
  "v_cvt_pk_bf16_f32 v156, v104, v105\n\t" \
  "v_max3_f32 v203, v67, v68, v69\n\t" \
  "v_mfma_f32_16x16x32_bf16 v[76:79], a[252:255], v[92:95], v[184:187]\n\t" \
  "v_max3_f32 v192, v192, v70, v71\n\t" \
  "ds_read_b64_tr_b16 a[176:177], v237 offset:768\n\t" \
  "v_mfma_f32_16x16x32_bf16 v[76:79], a[248:251], v[88:91], v[76:79]\n\t" \
  "v_exp_f32 v106, v106\n\t" \
  "v_mfma_f32_16x16x32_bf16 v[76:79], a[244:247], v[84:87], v[76:79]\n\t" \
  "ds_read_b64_tr_b16 a[178:179], v237 offset:4864\n\t" \
  "v_max3_f32 v203, v203, v72, v73\n\t" \
  "v_mfma_f32_16x16x32_bf16 v[76:79], a[240:243], v[80:83], v[76:79]\n\t" \
  "v_max3_f32 v192, v192, v74, v75\n\t" \
  "ds_read_b64_tr_b16 a[180:181], v237 offset:8960\n\t" \
  "v_mfma_f32_16x16x32_bf16 v[96:99], a[192:195], v[112:115], v[188:191]\n\t" \
  "v_exp_f32 v107, v107\n\t" \
  "v_mfma_f32_16x16x32_bf16 v[96:99], a[196:199], v[116:119], v[96:99]\n\t" \
  "v_cvt_pk_bf16_f32 v157, v106, v107\n\t" \
  "ds_read_b64_tr_b16 a[182:183], v237 offset:13056\n\t" \
  "v_mfma_f32_16x16x32_bf16 v[96:99], a[200:203], v[120:123], v[96:99]\n\t" \
  "v_max3_f32 v203, v203, v76, v77\n\t" \
  "v_max3_f32 v192, v192, v78, v79\n\t" \
  "v_mfma_f32_16x16x32_bf16 v[96:99], a[204:207], v[124:127], v[96:99]\n\t" \
  "v_max_f32 v192, v192, v203\n\t" \
  "ds_read_b64_tr_b16 a[184:185], v238 offset:768\n\t" \
  "v_mfma_f32_16x16x32_bf16 v[100:103], a[220:223], v[124:127], v[188:191]\n\t" \
  "v_cmp_lt_f32 vcc, 0x42800000, v192\n\t" \
  "ds_read_b64_tr_b16 a[186:187], v238 offset:4864\n\t" \
  "v_mfma_f32_16x16x32_bf16 v[100:103], a[216:219], v[120:123], v[100:103]\n\t" \
  "s_cbranch_vccnz .Lsf_c42\n\t" \
  ".Lsf_r42:\n\t" \
  "ds_read_b64_tr_b16 a[188:189], v238 offset:8960\n\t" \
  "v_mfma_f32_16x16x32_bf16 v[100:103], a[212:215], v[116:119], v[100:103]\n\t" \
  "v_max3_f32 v192, v96, v97, v98\n\t" \
  "ds_read_b64_tr_b16 a[190:191], v238 offset:13056\n\t" \
  "v_mfma_f32_16x16x32_bf16 v[100:103], a[208:211], v[112:115], v[100:103]\n\t" \
  "v_exp_f32 v108, v108\n\t" \
  "v_mfma_f32_16x16x32_bf16 v[104:107], a[224:227], v[112:115], v[188:191]\n\t" \
  "v_exp_f32 v109, v109\n\t" \
  "v_mfma_f32_16x16x32_bf16 v[104:107], a[228:231], v[116:119], v[104:107]\n\t" \
  "v_cvt_pk_bf16_f32 v158, v108, v109\n\t" \
  "v_mfma_f32_16x16x32_bf16 v[104:107], a[232:235], v[120:123], v[104:107]\n\t" \
  "v_exp_f32 v110, v110\n\t" \
  "v_mfma_f32_16x16x32_bf16 v[104:107], a[236:239], v[124:127], v[104:107]\n\t" \
  "v_max3_f32 v203, v99, v100, v101\n\t" \
  "v_max3_f32 v192, v192, v102, v103\n\t" \
  "v_exp_f32 v111, v111\n\t" \
  "s_nop 0\n\t" \
  "v_cvt_pk_bf16_f32 v159, v110, v111\n\t" \
  "v_mfma_f32_16x16x32_bf16 v[108:111], a[252:255], v[124:127], v[188:191]\n\t" \
  "v_exp_f32 v0, v0\n\t" \
  "v_mfma_f32_16x16x32_bf16 v[108:111], a[248:251], v[120:123], v[108:111]\n\t" \
  "v_exp_f32 v1, v1\n\t" \
  "v_mfma_f32_16x16x32_bf16 v[108:111], a[244:247], v[116:119], v[108:111]\n\t" \
  "v_max3_f32 v203, v203, v104, v105\n\t" \
  "v_max3_f32 v192, v192, v106, v107\n\t" \
  "v_mfma_f32_16x16x32_bf16 v[108:111], a[240:243], v[112:115], v[108:111]\n\t" \
  "v_exp_f32 v2, v2\n\t" \
  "s_waitcnt vmcnt(5) lgkmcnt(0)\n\t" \
  "s_barrier\n\t" \
  "v_mfma_f32_16x16x32_bf16 a[0:3], a[128:131], v[128:131], a[0:3]\n\t" \
  "v_exp_f32 v3, v3\n\t" \
  "v_mfma_f32_16x16x32_bf16 a[0:3], a[132:135], v[132:135], a[0:3]\n\t" \
  "v_exp_f32 v4, v4\n\t" \
  "v_mfma_f32_16x16x32_bf16 a[16:19], a[136:139], v[128:131], a[16:19]\n\t" \
  "ds_read_b128 a[192:195], v220 offset:16384\n\t" \
  "v_max3_f32 v203, v203, v108, v109\n\t" \
  "v_mfma_f32_16x16x32_bf16 a[16:19], a[140:143], v[132:135], a[16:19]\n\t" \
  "v_max3_f32 v192, v192, v110, v111\n\t" \
  "v_max_f32 v192, v192, v203\n\t" \
  "v_mfma_f32_16x16x32_bf16 a[32:35], a[144:147], v[128:131], a[32:35]\n\t" \
  "v_cmp_lt_f32 vcc, 0x42800000, v192\n\t" \
  "ds_read_b128 a[196:199], v221 offset:16384\n\t" \
  "v_mfma_f32_16x16x32_bf16 a[32:35], a[148:151], v[132:135], a[32:35]\n\t" \
  "s_cbranch_vccnz .Lsf_c43\n\t" \
  ".Lsf_r43:\n\t" \
  "ds_read_b128 a[200:203], v222 offset:16384\n\t" \
  "v_mfma_f32_16x16x32_bf16 a[48:51], a[152:155], v[128:131], a[48:51]\n\t" \
  "s_lshr_b32 s98, %[sofk], 6\n\t" \
  "v_xor_b32 v234, s98, v244\n\t" \
  "v_mfma_f32_16x16x32_bf16 a[48:51], a[156:159], v[132:135], a[48:51]\n\t" \
  "v_add_u32 v213, %[m], v242\n\t" \
  "v_add_u32 v215, 98304, v213\n\t" \
  "v_mfma_f32_16x16x32_bf16 a[64:67], a[160:163], v[128:131], a[64:67]\n\t" \
  "v_mov_b32 v243, %[tag]\n\t" \
  "v_lshrrev_b32 v254, 3, v233\n\t" \
  "v_mfma_f32_16x16x32_bf16 a[64:67], a[164:167], v[132:135], a[64:67]\n\t" \
  "v_exp_f32 v5, v5\n\t" \
  "v_mfma_f32_16x16x32_bf16 a[80:83], a[168:171], v[128:131], a[80:83]\n\t" \
  "v_exp_f32 v6, v6\n\t" \
  "v_mfma_f32_16x16x32_bf16 a[80:83], a[172:175], v[132:135], a[80:83]\n\t" \
  "v_min_u32 v254, 1, v254\n\t" \
  "v_lshlrev_b32 v254, 31, v254\n\t" \
  "v_mfma_f32_16x16x32_bf16 a[96:99], a[176:179], v[128:131], a[96:99]\n\t" \
  "ds_read_b128 a[204:207], v223 offset:16384\n\t" \
  "v_lshl_or_b32 v254, v233, 9, v254\n\t" \
  "v_mfma_f32_16x16x32_bf16 a[96:99], a[180:183], v[132:135], a[96:99]\n\t" \
  "v_exp_f32 v7, v7\n\t" \
  "v_mfma_f32_16x16x32_bf16 a[112:115], a[184:187], v[128:131], a[112:115]\n\t" \
  "v_exp_f32 v8, v8\n\t" \
  "v_mfma_f32_16x16x32_bf16 a[112:115], a[188:191], v[132:135], a[112:115]\n\t" \
  "s_lshr_b32 m0, %[hl18], 18\n\t" \
  "s_brev_b32 s98, 1\n\t" \
  "v_writelane_b32 v254, s98, m0\n\t" \
  "v_mfma_f32_16x16x32_bf16 v[160:163], v[216:219], v[128:131], v[160:163]\n\t" \
  "s_lshr_b32 s98, %[hl18], 18\n\t" \
  "v_xor_b32 v245, s98, v233\n\t" \
  "v_mfma_f32_16x16x32_bf16 v[160:163], v[216:219], v[132:135], v[160:163]\n\t" \
  "v_cvt_pk_bf16_f32 v128, v0, v1\n\t" \
  "v_cvt_pk_bf16_f32 v129, v2, v3\n\t" \
  "v_mfma_f32_16x16x32_bf16 a[4:7], a[128:131], v[136:139], a[4:7]\n\t" \
  "v_cvt_pk_bf16_f32 v130, v4, v5\n\t" \
  "v_cvt_pk_bf16_f32 v131, v6, v7\n\t" \
  "v_mfma_f32_16x16x32_bf16 a[4:7], a[132:135], v[140:143], a[4:7]\n\t" \
  "ds_read_b128 a[208:211], v220 offset:20480\n\t" \
  "v_and_b32 v245, 31, v245\n\t" \
  "s_lshr_b32 s98, %[hl18], 18\n\t" \
  "s_mul_i32 s98, s98, 127\n\t" \
  "v_add_lshl_u32 v245, v245, s98, 2\n\t" \
  "v_mfma_f32_16x16x32_bf16 a[20:23], a[136:139], v[136:139], a[20:23]\n\t" \
  "ds_read_b128 a[212:215], v221 offset:20480\n\t" \
  "s_add_u32 m0, %[m], 49152\n\t" \
  "v_mfma_f32_16x16x32_bf16 a[20:23], a[140:143], v[140:143], a[20:23]\n\t" \
  "v_exp_f32 v9, v9\n\t" \
  "v_mfma_f32_16x16x32_bf16 a[36:39], a[144:147], v[136:139], a[36:39]\n\t" \
  "v_cvt_pk_bf16_f32 v132, v8, v9\n\t" \
  "ds_read_b128 a[216:219], v222 offset:20480\n\t" \
  "v_mfma_f32_16x16x32_bf16 a[36:39], a[148:151], v[140:143], a[36:39]\n\t" \
  "v_exp_f32 v10, v10\n\t" \
  "v_mfma_f32_16x16x32_bf16 a[52:55], a[152:155], v[136:139], a[52:55]\n\t" \
  "v_exp_f32 v11, v11\n\t" \
  "v_mfma_f32_16x16x32_bf16 a[52:55], a[156:159], v[140:143], a[52:55]\n\t" \
  "v_cvt_pk_bf16_f32 v133, v10, v11\n\t" \
  "s_add_u32 s96, %[sofk], 98304\n\t" \
  "v_mfma_f32_16x16x32_bf16 a[68:71], a[160:163], v[136:139], a[68:71]\n\t" \
  "v_exp_f32 v12, v12\n\t" \
  "v_mfma_f32_16x16x32_bf16 a[68:71], a[164:167], v[140:143], a[68:71]\n\t" \
  "ds_read_b128 a[220:223], v223 offset:20480\n\t" \
  "buffer_load_dwordx4 v224, %[sK], s96 offen offset:0 lds\n\t" \
  "v_mfma_f32_16x16x32_bf16 a[84:87], a[168:171], v[136:139], a[84:87]\n\t" \
  "v_exp_f32 v13, v13\n\t" \
  "v_mfma_f32_16x16x32_bf16 a[84:87], a[172:175], v[140:143], a[84:87]\n\t" \
  "v_cvt_pk_bf16_f32 v134, v12, v13\n\t" \
  "buffer_load_dwordx4 v225, %[sK], s96 offen offset:1024 lds\n\t" \
  "v_mfma_f32_16x16x32_bf16 a[100:103], a[176:179], v[136:139], a[100:103]\n\t" \
  "v_exp_f32 v14, v14\n\t" \
  "v_mfma_f32_16x16x32_bf16 a[100:103], a[180:183], v[140:143], a[100:103]\n\t" \
  "v_exp_f32 v15, v15\n\t" \
  "v_mfma_f32_16x16x32_bf16 a[116:119], a[184:187], v[136:139], a[116:119]\n\t" \
  "v_cvt_pk_bf16_f32 v135, v14, v15\n\t" \
  "ds_read_b128 a[224:227], v220 offset:24576\n\t" \
  "v_mfma_f32_16x16x32_bf16 a[116:119], a[188:191], v[140:143], a[116:119]\n\t" \
  "v_exp_f32 v32, v32\n\t" \
  "v_mfma_f32_16x16x32_bf16 v[164:167], v[216:219], v[136:139], v[164:167]\n\t" \
  "buffer_load_dwordx4 v226, %[sK], s96 offen offset:2048 lds\n\t" \
  "ds_read_b128 a[228:231], v221 offset:24576\n\t" \
  "v_mfma_f32_16x16x32_bf16 v[164:167], v[216:219], v[140:143], v[164:167]\n\t" \
  "v_exp_f32 v33, v33\n\t" \
  "v_mfma_f32_16x16x32_bf16 a[8:11], a[128:131], v[144:147], a[8:11]\n\t" \
  "v_cvt_pk_bf16_f32 v136, v32, v33\n\t" \
  "buffer_load_dwordx4 v227, %[sK], s96 offen offset:3072 lds\n\t" \
  "v_mfma_f32_16x16x32_bf16 a[8:11], a[132:135], v[148:151], a[8:11]\n\t" \
  "v_exp_f32 v34, v34\n\t" \
  "v_mfma_f32_16x16x32_bf16 a[24:27], a[136:139], v[144:147], a[24:27]\n\t" \
  "v_exp_f32 v35, v35\n\t" \
  "v_mfma_f32_16x16x32_bf16 a[24:27], a[140:143], v[148:151], a[24:27]\n\t" \
  "v_cvt_pk_bf16_f32 v137, v34, v35\n\t" \
  "ds_read_b128 a[232:235], v222 offset:24576\n\t" \
  "v_mfma_f32_16x16x32_bf16 a[40:43], a[144:147], v[144:147], a[40:43]\n\t" \
  "v_exp_f32 v36, v36\n\t" \
  "v_mfma_f32_16x16x32_bf16 a[40:43], a[148:151], v[148:151], a[40:43]\n\t" \
  "v_exp_f32 v37, v37\n\t" \
  "v_mfma_f32_16x16x32_bf16 a[56:59], a[152:155], v[144:147], a[56:59]\n\t" \
  "v_cvt_pk_bf16_f32 v138, v36, v37\n\t" \
  "s_add_u32 m0, %[m], 147456\n\t" \
  "v_mfma_f32_16x16x32_bf16 a[56:59], a[156:159], v[148:151], a[56:59]\n\t" \
  "ds_read_b128 a[236:239], v223 offset:24576\n\t" \
  "s_add_u32 s96, %[sofk], 81920\n\t" \
  "v_mfma_f32_16x16x32_bf16 a[72:75], a[160:163], v[144:147], a[72:75]\n\t" \
  "v_exp_f32 v38, v38\n\t" \
  "v_mfma_f32_16x16x32_bf16 a[72:75], a[164:167], v[148:151], a[72:75]\n\t" \
  "v_exp_f32 v39, v39\n\t" \
  "v_mfma_f32_16x16x32_bf16 a[88:91], a[168:171], v[144:147], a[88:91]\n\t" \
  "v_cvt_pk_bf16_f32 v139, v38, v39\n\t" \
  "ds_read_b128 a[240:243], v220 offset:28672\n\t" \
  "v_mfma_f32_16x16x32_bf16 a[88:91], a[172:175], v[148:151], a[88:91]\n\t" \
  "v_exp_f32 v40, v40\n\t" \
  "v_mfma_f32_16x16x32_bf16 a[104:107], a[176:179], v[144:147], a[104:107]\n\t" \
  "buffer_load_dwordx4 v230, %[sV], s96 offen offset:0 lds\n\t" \
  "ds_read_b128 a[244:247], v221 offset:28672\n\t" \
  "v_mfma_f32_16x16x32_bf16 a[104:107], a[180:183], v[148:151], a[104:107]\n\t" \
  "v_exp_f32 v41, v41\n\t" \
  "v_mfma_f32_16x16x32_bf16 a[120:123], a[184:187], v[144:147], a[120:123]\n\t" \
  "v_cvt_pk_bf16_f32 v140, v40, v41\n\t" \
  "buffer_load_dwordx4 v231, %[sV], s96 offen offset:1024 lds\n\t" \
  "v_mfma_f32_16x16x32_bf16 a[120:123], a[188:191], v[148:151], a[120:123]\n\t" \
  "v_exp_f32 v42, v42\n\t" \
  "v_mfma_f32_16x16x32_bf16 v[168:171], v[216:219], v[144:147], v[168:171]\n\t" \
  "v_exp_f32 v43, v43\n\t" \
  "v_mfma_f32_16x16x32_bf16 v[168:171], v[216:219], v[148:151], v[168:171]\n\t" \
  "v_cvt_pk_bf16_f32 v141, v42, v43\n\t" \
  "buffer_load_dwordx4 v230, %[sV], s96 offen offset:2048 lds\n\t" \
  "v_mfma_f32_16x16x32_bf16 a[12:15], a[128:131], v[152:155], a[12:15]\n\t" \
  "v_exp_f32 v44, v44\n\t" \
  "v_mfma_f32_16x16x32_bf16 a[12:15], a[132:135], v[156:159], a[12:15]\n\t" \
  "ds_read_b128 a[248:251], v222 offset:28672\n\t" \
  "buffer_load_dwordx4 v231, %[sV], s96 offen offset:3072 lds\n\t" \
  "v_mfma_f32_16x16x32_bf16 a[28:31], a[136:139], v[152:155], a[28:31]\n\t" \
  "v_exp_f32 v45, v45\n\t" \
  "v_mfma_f32_16x16x32_bf16 a[28:31], a[140:143], v[156:159], a[28:31]\n\t" \
  "v_cvt_pk_bf16_f32 v142, v44, v45\n\t" \
  "ds_read_b128 a[252:255], v223 offset:28672\n\t" \
  "v_mfma_f32_16x16x32_bf16 a[44:47], a[144:147], v[152:155], a[44:47]\n\t" \
  "v_exp_f32 v46, v46\n\t" \
  "v_mfma_f32_16x16x32_bf16 a[44:47], a[148:151], v[156:159], a[44:47]\n\t" \
  "v_exp_f32 v47, v47\n\t" \
  "v_mfma_f32_16x16x32_bf16 a[60:63], a[152:155], v[152:155], a[60:63]\n\t" \
  "v_cvt_pk_bf16_f32 v143, v46, v47\n\t" \
  "v_mfma_f32_16x16x32_bf16 a[60:63], a[156:159], v[156:159], a[60:63]\n\t" \
  "s_add_u32 m0, %[m], 114688\n\t" \
  "s_mov_b32 s96, 18432\n\t" \
  "buffer_load_dwordx4 v232, %[sU], s96 offen offset:3072 lds\n\t" \
  "v_mfma_f32_16x16x32_bf16 a[76:79], a[160:163], v[152:155], a[76:79]\n\t" \
  "v_exp_f32 v64, v64\n\t" \
  "v_mfma_f32_16x16x32_bf16 a[76:79], a[164:167], v[156:159], a[76:79]\n\t" \
  "v_exp_f32 v65, v65\n\t" \
  "v_mfma_f32_16x16x32_bf16 a[92:95], a[168:171], v[152:155], a[92:95]\n\t" \
  "v_cvt_pk_bf16_f32 v144, v64, v65\n\t" \
  "v_mfma_f32_16x16x32_bf16 a[92:95], a[172:175], v[156:159], a[92:95]\n\t" \
  "s_add_u32 m0, %[m], 131072\n\t" \
  "s_mov_b32 s96, 28672\n\t" \
  "buffer_load_dwordx4 v232, %[sU], s96 offen offset:0 lds\n\t" \
  "v_mfma_f32_16x16x32_bf16 a[108:111], a[176:179], v[152:155], a[108:111]\n\t" \
  "v_exp_f32 v66, v66\n\t" \
  "v_mfma_f32_16x16x32_bf16 a[108:111], a[180:183], v[156:159], a[108:111]\n\t" \
  "s_mov_b32 s96, 34816\n\t" \
  "buffer_load_dwordx4 v232, %[sU], s96 offen offset:1024 lds\n\t" \
  "v_mfma_f32_16x16x32_bf16 a[124:127], a[184:187], v[152:155], a[124:127]\n\t" \
  "v_exp_f32 v67, v67\n\t" \
  "v_mfma_f32_16x16x32_bf16 a[124:127], a[188:191], v[156:159], a[124:127]\n\t" \
  "v_cvt_pk_bf16_f32 v145, v66, v67\n\t" \
  "v_mfma_f32_16x16x32_bf16 v[172:175], v[216:219], v[152:155], v[172:175]\n\t" \
  "v_exp_f32 v68, v68\n\t" \
  "v_mfma_f32_16x16x32_bf16 v[172:175], v[216:219], v[156:159], v[172:175]\n\t" \
  "v_exp_f32 v69, v69\n\t" \
  "s_cmp_lg_u32 s99, 0\n\t" \
  "s_cbranch_scc1 .Lsf_p4\n\t" \
  ".Lsf_q4:\n\t" \
  "s_waitcnt lgkmcnt(0)\n\t" \
  "v_mfma_f32_16x16x32_bf16 v[0:3], a[192:195], v[16:19], v[176:179]\n\t" \
  "v_cvt_pk_bf16_f32 v146, v68, v69\n\t" \
  "ds_read_b64_tr_b16 a[128:129], v228 offset:0\n\t" \
  "v_mfma_f32_16x16x32_bf16 v[0:3], a[196:199], v[20:23], v[0:3]\n\t" \
  "s_add_u32 s98, %[pf2], 49152\n\t" \
  "buffer_load_dword %[pfd], v202, %[sM], s98 offen\n\t" \
  "v_mfma_f32_16x16x32_bf16 v[0:3], a[200:203], v[24:27], v[0:3]\n\t" \
  "v_lshrrev_b32 v252, 3, v233\n\t" \
  "ds_read_b64_tr_b16 a[130:131], v228 offset:4096\n\t" \
  "v_mfma_f32_16x16x32_bf16 v[0:3], a[204:207], v[28:31], v[0:3]\n\t" \
  "v_exp_f32 v70, v70\n\t" \
  "v_mfma_f32_16x16x32_bf16 v[4:7], a[220:223], v[28:31], v[176:179]\n\t" \
  "ds_read_b64_tr_b16 a[132:133], v228 offset:8192\n\t" \
  "v_mul_u32_u24 v247, 98176, v252\n\t" \
  "v_mfma_f32_16x16x32_bf16 v[4:7], a[216:219], v[24:27], v[4:7]\n\t" \
  "v_exp_f32 v71, v71\n\t" \
  "v_mfma_f32_16x16x32_bf16 v[4:7], a[212:215], v[20:23], v[4:7]\n\t" \
  "v_cvt_pk_bf16_f32 v147, v70, v71\n\t" \
  "v_max3_f32 v192, v0, v1, v2\n\t" \
  "v_mfma_f32_16x16x32_bf16 v[4:7], a[208:211], v[16:19], v[4:7]\n\t" \
  "v_exp_f32 v72, v72\n\t" \
  "v_mfma_f32_16x16x32_bf16 v[8:11], a[224:227], v[16:19], v[176:179]\n\t" \
  "ds_read_b64_tr_b16 a[134:135], v228 offset:12288\n\t" \
  "ds_read_b64_tr_b16 a[136:137], v229 offset:0\n\t" \
  "v_mfma_f32_16x16x32_bf16 v[8:11], a[228:231], v[20:23], v[8:11]\n\t" \
  "v_lshl_add_u32 v247, v233, 4, v247\n\t" \
  "ds_read_b64_tr_b16 a[138:139], v229 offset:4096\n\t" \
  "v_mfma_f32_16x16x32_bf16 v[8:11], a[232:235], v[24:27], v[8:11]\n\t" \
  "v_max3_f32 v203, v3, v4, v5\n\t" \
  "v_max3_f32 v192, v192, v6, v7\n\t" \
  "v_mfma_f32_16x16x32_bf16 v[8:11], a[236:239], v[28:31], v[8:11]\n\t" \
  "v_exp_f32 v73, v73\n\t" \
  "v_mfma_f32_16x16x32_bf16 v[12:15], a[252:255], v[28:31], v[176:179]\n\t" \
  "v_cvt_pk_bf16_f32 v148, v72, v73\n\t" \
  "ds_read_b64_tr_b16 a[140:141], v229 offset:8192\n\t" \
  "v_mfma_f32_16x16x32_bf16 v[12:15], a[248:251], v[24:27], v[12:15]\n\t" \
  "v_lshlrev_b32 v252, 4, v252\n\t" \
  "ds_read_b64_tr_b16 a[142:143], v229 offset:12288\n\t" \
  "v_mfma_f32_16x16x32_bf16 v[12:15], a[244:247], v[20:23], v[12:15]\n\t" \
  "v_max3_f32 v203, v203, v8, v9\n\t" \
  "v_max3_f32 v192, v192, v10, v11\n\t" \
  "v_mfma_f32_16x16x32_bf16 v[12:15], a[240:243], v[16:19], v[12:15]\n\t" \
  "s_add_u32 s98, %[pf2], 73728\n\t" \
  "buffer_load_dword %[pfd], v202, %[sM], s98 offen\n\t" \
  "v_mfma_f32_16x16x32_bf16 v[32:35], a[192:195], v[48:51], v[180:183]\n\t" \
  "ds_read_b64_tr_b16 a[144:145], v228 offset:256\n\t" \
  "ds_read_b64_tr_b16 a[146:147], v228 offset:4352\n\t" \
  "v_mfma_f32_16x16x32_bf16 v[32:35], a[196:199], v[52:55], v[32:35]\n\t" \
  "v_exp_f32 v74, v74\n\t" \
  "v_mfma_f32_16x16x32_bf16 v[32:35], a[200:203], v[56:59], v[32:35]\n\t" \
  "v_max3_f32 v203, v203, v12, v13\n\t" \
  "v_max3_f32 v192, v192, v14, v15\n\t" \
  "v_mfma_f32_16x16x32_bf16 v[32:35], a[204:207], v[60:63], v[32:35]\n\t" \
  "v_max_f32 v192, v192, v203\n\t" \
  "ds_read_b64_tr_b16 a[148:149], v228 offset:8448\n\t" \
  "v_mfma_f32_16x16x32_bf16 v[36:39], a[220:223], v[60:63], v[180:183]\n\t" \
  "v_cmp_lt_f32 vcc, 0x42800000, v192\n\t" \
  "ds_read_b64_tr_b16 a[150:151], v228 offset:12544\n\t" \
  "v_mfma_f32_16x16x32_bf16 v[36:39], a[216:219], v[56:59], v[36:39]\n\t" \
  "s_cbranch_vccnz .Lsf_c50\n\t" \
  ".Lsf_r50:\n\t" \
  "ds_read_b64_tr_b16 a[152:153], v229 offset:256\n\t" \
  "v_mfma_f32_16x16x32_bf16 v[36:39], a[212:215], v[52:55], v[36:39]\n\t" \
  "v_max3_f32 v192, v32, v33, v34\n\t" \
  "ds_read_b64_tr_b16 a[154:155], v229 offset:4352\n\t" \
  "v_mfma_f32_16x16x32_bf16 v[36:39], a[208:211], v[48:51], v[36:39]\n\t" \
  "v_exp_f32 v75, v75\n\t" \
  "v_mfma_f32_16x16x32_bf16 v[40:43], a[224:227], v[48:51], v[180:183]\n\t" \
  "v_cvt_pk_bf16_f32 v149, v74, v75\n\t" \
  "ds_read_b64_tr_b16 a[156:157], v229 offset:8448\n\t" \
  "v_mfma_f32_16x16x32_bf16 v[40:43], a[228:231], v[52:55], v[40:43]\n\t" \
  "v_exp_f32 v76, v76\n\t" \
  "v_mfma_f32_16x16x32_bf16 v[40:43], a[232:235], v[56:59], v[40:43]\n\t" \
  "v_exp_f32 v77, v77\n\t" \
  "v_mfma_f32_16x16x32_bf16 v[40:43], a[236:239], v[60:63], v[40:43]\n\t" \
  "v_cvt_pk_bf16_f32 v150, v76, v77\n\t" \
  "v_max3_f32 v203, v35, v36, v37\n\t" \
  "v_mfma_f32_16x16x32_bf16 v[44:47], a[252:255], v[60:63], v[180:183]\n\t" \
  "v_max3_f32 v192, v192, v38, v39\n\t" \
  "ds_read_b64_tr_b16 a[158:159], v229 offset:12544\n\t" \
  "v_mfma_f32_16x16x32_bf16 v[44:47], a[248:251], v[56:59], v[44:47]\n\t" \
  "v_exp_f32 v78, v78\n\t" \
  "v_mfma_f32_16x16x32_bf16 v[44:47], a[244:247], v[52:55], v[44:47]\n\t" \
  "ds_read_b64_tr_b16 a[160:161], v228 offset:512\n\t" \
  "v_max3_f32 v203, v203, v40, v41\n\t" \
  "v_mfma_f32_16x16x32_bf16 v[44:47], a[240:243], v[48:51], v[44:47]\n\t" \
  "v_max3_f32 v192, v192, v42, v43\n\t" \
  "ds_read_b64_tr_b16 a[162:163], v228 offset:4608\n\t" \
  "v_mfma_f32_16x16x32_bf16 v[64:67], a[192:195], v[80:83], v[184:187]\n\t" \
  "v_exp_f32 v79, v79\n\t" \
  "v_mfma_f32_16x16x32_bf16 v[64:67], a[196:199], v[84:87], v[64:67]\n\t" \
  "v_cvt_pk_bf16_f32 v151, v78, v79\n\t" \
  "ds_read_b64_tr_b16 a[164:165], v228 offset:8704\n\t" \
  "v_mfma_f32_16x16x32_bf16 v[64:67], a[200:203], v[88:91], v[64:67]\n\t" \
  "v_max3_f32 v203, v203, v44, v45\n\t" \
  "v_max3_f32 v192, v192, v46, v47\n\t" \
  "v_mfma_f32_16x16x32_bf16 v[64:67], a[204:207], v[92:95], v[64:67]\n\t" \
  "v_max_f32 v192, v192, v203\n\t" \
  "ds_read_b64_tr_b16 a[166:167], v228 offset:12800\n\t" \
  "v_mfma_f32_16x16x32_bf16 v[68:71], a[220:223], v[92:95], v[184:187]\n\t" \
  "v_cmp_lt_f32 vcc, 0x42800000, v192\n\t" \
  "ds_read_b64_tr_b16 a[168:169], v229 offset:512\n\t" \
  "v_mfma_f32_16x16x32_bf16 v[68:71], a[216:219], v[88:91], v[68:71]\n\t" \
  "s_cbranch_vccnz .Lsf_c51\n\t" \
  ".Lsf_r51:\n\t" \
  "ds_read_b64_tr_b16 a[170:171], v229 offset:4608\n\t" \
  "v_mfma_f32_16x16x32_bf16 v[68:71], a[212:215], v[84:87], v[68:71]\n\t" \
  "v_max3_f32 v192, v64, v65, v66\n\t" \
  "ds_read_b64_tr_b16 a[172:173], v229 offset:8704\n\t" \
  "v_mfma_f32_16x16x32_bf16 v[68:71], a[208:211], v[80:83], v[68:71]\n\t" \
  "v_exp_f32 v96, v96\n\t" \
  "v_mfma_f32_16x16x32_bf16 v[72:75], a[224:227], v[80:83], v[184:187]\n\t" \
  "v_exp_f32 v97, v97\n\t" \
  "v_mfma_f32_16x16x32_bf16 v[72:75], a[228:231], v[84:87], v[72:75]\n\t" \
  "v_cvt_pk_bf16_f32 v152, v96, v97\n\t" \
  "ds_read_b64_tr_b16 a[174:175], v229 offset:12800\n\t" \
  "v_mfma_f32_16x16x32_bf16 v[72:75], a[232:235], v[88:91], v[72:75]\n\t" \
  "v_exp_f32 v98, v98\n\t" \
  "v_mfma_f32_16x16x32_bf16 v[72:75], a[236:239], v[92:95], v[72:75]\n\t" \
  "v_max3_f32 v203, v67, v68, v69\n\t" \
  "v_max3_f32 v192, v192, v70, v71\n\t" \
  "v_mfma_f32_16x16x32_bf16 v[76:79], a[252:255], v[92:95], v[184:187]\n\t" \
  "ds_read_b64_tr_b16 a[176:177], v228 offset:768\n\t" \
  "ds_read_b64_tr_b16 a[178:179], v228 offset:4864\n\t" \
  "v_mfma_f32_16x16x32_bf16 v[76:79], a[248:251], v[88:91], v[76:79]\n\t" \
  "v_exp_f32 v99, v99\n\t" \
  "v_mfma_f32_16x16x32_bf16 v[76:79], a[244:247], v[84:87], v[76:79]\n\t" \
  "v_cvt_pk_bf16_f32 v153, v98, v99\n\t" \
  "v_max3_f32 v203, v203, v72, v73\n\t" \
  "v_mfma_f32_16x16x32_bf16 v[76:79], a[240:243], v[80:83], v[76:79]\n\t" \
  "v_max3_f32 v192, v192, v74, v75\n\t" \
  "ds_read_b64_tr_b16 a[180:181], v228 offset:8960\n\t" \
  "v_mfma_f32_16x16x32_bf16 v[96:99], a[192:195], v[112:115], v[188:191]\n\t" \
  "v_exp_f32 v100, v100\n\t" \
  "v_mfma_f32_16x16x32_bf16 v[96:99], a[196:199], v[116:119], v[96:99]\n\t" \
  "v_exp_f32 v101, v101\n\t" \
  "v_mfma_f32_16x16x32_bf16 v[96:99], a[200:203], v[120:123], v[96:99]\n\t" \
  "v_cvt_pk_bf16_f32 v154, v100, v101\n\t" \
  "ds_read_b64_tr_b16 a[182:183], v228 offset:13056\n\t" \
  "v_mfma_f32_16x16x32_bf16 v[96:99], a[204:207], v[124:127], v[96:99]\n\t" \
  "v_max3_f32 v203, v203, v76, v77\n\t" \
  "v_max3_f32 v192, v192, v78, v79\n\t" \
  "v_exp_f32 v102, v102\n\t" \
  "v_exp_f32 v103, v103\n\t" \
  "s_nop 0\n\t" \
  "v_cvt_pk_bf16_f32 v155, v102, v103\n\t" \
  "v_mfma_f32_16x16x32_bf16 v[100:103], a[220:223], v[124:127], v[188:191]\n\t" \
  "v_max_f32 v192, v192, v203\n\t" \
  "ds_read_b64_tr_b16 a[184:185], v229 offset:768\n\t" \
  "v_mfma_f32_16x16x32_bf16 v[100:103], a[216:219], v[120:123], v[100:103]\n\t" \
  "v_cmp_lt_f32 vcc, 0x42800000, v192\n\t" \
  "ds_read_b64_tr_b16 a[186:187], v229 offset:4864\n\t" \
  "v_mfma_f32_16x16x32_bf16 v[100:103], a[212:215], v[116:119], v[100:103]\n\t" \
  "s_cbranch_vccnz .Lsf_c52\n\t" \
  ".Lsf_r52:\n\t" \
  "v_max3_f32 v192, v96, v97, v98\n\t" \
  "v_mfma_f32_16x16x32_bf16 v[100:103], a[208:211], v[112:115], v[100:103]\n\t" \
  "ds_read_b64_tr_b16 a[188:189], v229 offset:8960\n\t" \
  "ds_read_b64_tr_b16 a[190:191], v229 offset:13056\n\t" \
  "v_exp_f32 v104, v104\n\t" \
  "v_exp_f32 v105, v105\n\t" \
  "v_exp_f32 v106, v106\n\t" \
  "v_exp_f32 v107, v107\n\t" \
  "v_cvt_pk_bf16_f32 v156, v104, v105\n\t" \
  "v_cvt_pk_bf16_f32 v157, v106, v107\n\t" \
  "v_mfma_f32_16x16x32_bf16 v[104:107], a[224:227], v[112:115], v[188:191]\n\t" \
  "v_exp_f32 v108, v108\n\t" \
  "v_mfma_f32_16x16x32_bf16 v[104:107], a[228:231], v[116:119], v[104:107]\n\t" \
  "v_exp_f32 v109, v109\n\t" \
  "v_mfma_f32_16x16x32_bf16 v[104:107], a[232:235], v[120:123], v[104:107]\n\t" \
  "v_cvt_pk_bf16_f32 v158, v108, v109\n\t" \
  "v_max3_f32 v203, v99, v100, v101\n\t" \
  "v_mfma_f32_16x16x32_bf16 v[104:107], a[236:239], v[124:127], v[104:107]\n\t" \
  "v_max3_f32 v192, v192, v102, v103\n\t" \
  "v_exp_f32 v110, v110\n\t" \
  "v_exp_f32 v111, v111\n\t" \
  "s_nop 0\n\t" \
  "v_cvt_pk_bf16_f32 v159, v110, v111\n\t" \
  "v_mfma_f32_16x16x32_bf16 v[108:111], a[252:255], v[124:127], v[188:191]\n\t" \
  "v_exp_f32 v0, v0\n\t" \
  "v_mfma_f32_16x16x32_bf16 v[108:111], a[248:251], v[120:123], v[108:111]\n\t" \
  "v_exp_f32 v1, v1\n\t" \
  "v_mfma_f32_16x16x32_bf16 v[108:111], a[244:247], v[116:119], v[108:111]\n\t" \
  "v_max3_f32 v203, v203, v104, v105\n\t" \
  "v_max3_f32 v192, v192, v106, v107\n\t" \
  "v_mfma_f32_16x16x32_bf16 v[108:111], a[240:243], v[112:115], v[108:111]\n\t" \
  "v_exp_f32 v2, v2\n\t" \
  "s_waitcnt vmcnt(5) lgkmcnt(0)\n\t" \
  "s_barrier\n\t" \
  "v_mfma_f32_16x16x32_bf16 a[0:3], a[128:131], v[128:131], a[0:3]\n\t" \
  "v_exp_f32 v3, v3\n\t" \
  "v_mfma_f32_16x16x32_bf16 a[0:3], a[132:135], v[132:135], a[0:3]\n\t" \
  "v_exp_f32 v4, v4\n\t" \
  "v_mfma_f32_16x16x32_bf16 a[16:19], a[136:139], v[128:131], a[16:19]\n\t" \
  "ds_read_b128 a[192:195], v220 offset:0\n\t" \
  "v_max3_f32 v203, v203, v108, v109\n\t" \
  "v_mfma_f32_16x16x32_bf16 a[16:19], a[140:143], v[132:135], a[16:19]\n\t" \
  "v_max3_f32 v192, v192, v110, v111\n\t" \
  "v_max_f32 v192, v192, v203\n\t" \
  "v_mfma_f32_16x16x32_bf16 a[32:35], a[144:147], v[128:131], a[32:35]\n\t" \
  "v_cmp_lt_f32 vcc, 0x42800000, v192\n\t" \
  "ds_read_b128 a[196:199], v221 offset:0\n\t" \
  "v_mfma_f32_16x16x32_bf16 a[32:35], a[148:151], v[132:135], a[32:35]\n\t" \
  "s_cbranch_vccnz .Lsf_c53\n\t" \
  ".Lsf_r53:\n\t" \
  "ds_read_b128 a[200:203], v222 offset:0\n\t" \
  "v_mfma_f32_16x16x32_bf16 a[48:51], a[152:155], v[128:131], a[48:51]\n\t" \
  "v_exp_f32 v5, v5\n\t" \
  "v_mfma_f32_16x16x32_bf16 a[48:51], a[156:159], v[132:135], a[48:51]\n\t" \
  "v_exp_f32 v6, v6\n\t" \
  "v_mfma_f32_16x16x32_bf16 a[64:67], a[160:163], v[128:131], a[64:67]\n\t" \
  "v_exp_f32 v7, v7\n\t" \
  "v_mfma_f32_16x16x32_bf16 a[64:67], a[164:167], v[132:135], a[64:67]\n\t" \
  "v_exp_f32 v8, v8\n\t" \
  "v_mfma_f32_16x16x32_bf16 a[80:83], a[168:171], v[128:131], a[80:83]\n\t" \
  "v_exp_f32 v9, v9\n\t" \
  "v_mfma_f32_16x16x32_bf16 a[80:83], a[172:175], v[132:135], a[80:83]\n\t" \
  "v_exp_f32 v10, v10\n\t" \
  "v_mfma_f32_16x16x32_bf16 a[96:99], a[176:179], v[128:131], a[96:99]\n\t" \
  "v_exp_f32 v11, v11\n\t" \
  "v_mfma_f32_16x16x32_bf16 a[96:99], a[180:183], v[132:135], a[96:99]\n\t" \
  "v_exp_f32 v12, v12\n\t" \
  "v_mfma_f32_16x16x32_bf16 a[112:115], a[184:187], v[128:131], a[112:115]\n\t" \
  "ds_read_b128 a[204:207], v223 offset:0\n\t" \
  "ds_read_b128 a[208:211], v220 offset:4096\n\t" \
  "v_mfma_f32_16x16x32_bf16 a[112:115], a[188:191], v[132:135], a[112:115]\n\t" \
  "v_exp_f32 v13, v13\n\t" \
  "v_mfma_f32_16x16x32_bf16 v[160:163], v[216:219], v[128:131], v[160:163]\n\t" \
  "v_exp_f32 v14, v14\n\t" \
  "v_mfma_f32_16x16x32_bf16 v[160:163], v[216:219], v[132:135], v[160:163]\n\t" \
  "v_cvt_pk_bf16_f32 v128, v0, v1\n\t" \
  "v_cvt_pk_bf16_f32 v129, v2, v3\n\t" \
  "v_mfma_f32_16x16x32_bf16 a[4:7], a[128:131], v[136:139], a[4:7]\n\t" \
  "v_cvt_pk_bf16_f32 v130, v4, v5\n\t" \
  "v_cvt_pk_bf16_f32 v131, v6, v7\n\t" \
  "v_mfma_f32_16x16x32_bf16 a[4:7], a[132:135], v[140:143], a[4:7]\n\t" \
  "v_cvt_pk_bf16_f32 v132, v8, v9\n\t" \
  "v_cvt_pk_bf16_f32 v133, v10, v11\n\t" \
  "v_mfma_f32_16x16x32_bf16 a[20:23], a[136:139], v[136:139], a[20:23]\n\t" \
  "v_cvt_pk_bf16_f32 v134, v12, v13\n\t" \
  "ds_read_b128 a[212:215], v221 offset:4096\n\t" \
  "v_mfma_f32_16x16x32_bf16 a[20:23], a[140:143], v[140:143], a[20:23]\n\t" \
  "v_exp_f32 v15, v15\n\t" \
  "v_mfma_f32_16x16x32_bf16 a[36:39], a[144:147], v[136:139], a[36:39]\n\t" \
  "v_cvt_pk_bf16_f32 v135, v14, v15\n\t" \
  "s_add_u32 m0, %[m], 65536\n\t" \
  "v_mfma_f32_16x16x32_bf16 a[36:39], a[148:151], v[140:143], a[36:39]\n\t" \
  "v_exp_f32 v32, v32\n\t" \
  "v_mfma_f32_16x16x32_bf16 a[52:55], a[152:155], v[136:139], a[52:55]\n\t" \
  "ds_read_b128 a[216:219], v222 offset:4096\n\t" \
  "s_add_u32 s96, %[sofk], 114688\n\t" \
  "v_mfma_f32_16x16x32_bf16 a[52:55], a[156:159], v[140:143], a[52:55]\n\t" \
  "v_exp_f32 v33, v33\n\t" \
  "v_mfma_f32_16x16x32_bf16 a[68:71], a[160:163], v[136:139], a[68:71]\n\t" \
  "v_exp_f32 v34, v34\n\t" \
  "v_mfma_f32_16x16x32_bf16 a[68:71], a[164:167], v[140:143], a[68:71]\n\t" \
  "ds_read_b128 a[220:223], v223 offset:4096\n\t" \
  "buffer_load_dwordx4 v224, %[sK], s96 offen offset:0 lds\n\t" \
  "v_mfma_f32_16x16x32_bf16 a[84:87], a[168:171], v[136:139], a[84:87]\n\t" \
  "v_exp_f32 v35, v35\n\t" \
  "v_mfma_f32_16x16x32_bf16 a[84:87], a[172:175], v[140:143], a[84:87]\n\t" \
  "v_exp_f32 v36, v36\n\t" \
  "v_mfma_f32_16x16x32_bf16 a[100:103], a[176:179], v[136:139], a[100:103]\n\t" \
  "v_exp_f32 v37, v37\n\t" \
  "v_mfma_f32_16x16x32_bf16 a[100:103], a[180:183], v[140:143], a[100:103]\n\t" \
  "ds_read_b128 a[224:227], v220 offset:8192\n\t" \
  "buffer_load_dwordx4 v225, %[sK], s96 offen offset:1024 lds\n\t" \
  "v_mfma_f32_16x16x32_bf16 a[116:119], a[184:187], v[136:139], a[116:119]\n\t" \
  "v_exp_f32 v38, v38\n\t" \
  "v_mfma_f32_16x16x32_bf16 a[116:119], a[188:191], v[140:143], a[116:119]\n\t" \
  "v_exp_f32 v39, v39\n\t" \
  "v_mfma_f32_16x16x32_bf16 v[164:167], v[216:219], v[136:139], v[164:167]\n\t" \
  "buffer_load_dwordx4 v226, %[sK], s96 offen offset:2048 lds\n\t" \
  "ds_read_b128 a[228:231], v221 offset:8192\n\t" \
  "v_mfma_f32_16x16x32_bf16 v[164:167], v[216:219], v[140:143], v[164:167]\n\t" \
  "v_cvt_pk_bf16_f32 v136, v32, v33\n\t" \
  "v_cvt_pk_bf16_f32 v137, v34, v35\n\t" \
  "v_mfma_f32_16x16x32_bf16 a[8:11], a[128:131], v[144:147], a[8:11]\n\t" \
  "v_cvt_pk_bf16_f32 v138, v36, v37\n\t" \
  "v_cvt_pk_bf16_f32 v139, v38, v39\n\t" \
  "v_mfma_f32_16x16x32_bf16 a[8:11], a[132:135], v[148:151], a[8:11]\n\t" \
  "v_exp_f32 v40, v40\n\t" \
  "v_mfma_f32_16x16x32_bf16 a[24:27], a[136:139], v[144:147], a[24:27]\n\t" \
  "buffer_load_dwordx4 v227, %[sK], s96 offen offset:3072 lds\n\t" \
  "ds_read_b128 a[232:235], v222 offset:8192\n\t" \
  "v_mfma_f32_16x16x32_bf16 a[24:27], a[140:143], v[148:151], a[24:27]\n\t" \
  "v_exp_f32 v41, v41\n\t" \
  "v_mfma_f32_16x16x32_bf16 a[40:43], a[144:147], v[144:147], a[40:43]\n\t" \
  "v_cvt_pk_bf16_f32 v140, v40, v41\n\t" \
  "s_add_u32 m0, %[m], 81920\n\t" \
  "v_mfma_f32_16x16x32_bf16 a[40:43], a[148:151], v[148:151], a[40:43]\n\t" \
  "v_exp_f32 v42, v42\n\t" \
  "v_mfma_f32_16x16x32_bf16 a[56:59], a[152:155], v[144:147], a[56:59]\n\t" \
  "v_exp_f32 v43, v43\n\t" \
  "v_mfma_f32_16x16x32_bf16 a[56:59], a[156:159], v[148:151], a[56:59]\n\t" \
  "v_cvt_pk_bf16_f32 v141, v42, v43\n\t" \
  "ds_read_b128 a[236:239], v223 offset:8192\n\t" \
  "v_mfma_f32_16x16x32_bf16 a[72:75], a[160:163], v[144:147], a[72:75]\n\t" \
  "v_exp_f32 v44, v44\n\t" \
  "v_mfma_f32_16x16x32_bf16 a[72:75], a[164:167], v[148:151], a[72:75]\n\t" \
  "s_add_u32 s96, %[sofk], 98304\n\t" \
  "ds_read_b128 a[240:243], v220 offset:12288\n\t" \
  "v_mfma_f32_16x16x32_bf16 a[88:91], a[168:171], v[144:147], a[88:91]\n\t" \
  "v_exp_f32 v45, v45\n\t" \
  "v_mfma_f32_16x16x32_bf16 a[88:91], a[172:175], v[148:151], a[88:91]\n\t" \
  "v_cvt_pk_bf16_f32 v142, v44, v45\n\t" \
  "buffer_load_dwordx4 v230, %[sV], s96 offen offset:0 lds\n\t" \
  "v_mfma_f32_16x16x32_bf16 a[104:107], a[176:179], v[144:147], a[104:107]\n\t" \
  "v_exp_f32 v46, v46\n\t" \
  "v_mfma_f32_16x16x32_bf16 a[104:107], a[180:183], v[148:151], a[104:107]\n\t" \
  "v_exp_f32 v47, v47\n\t" \
  "v_mfma_f32_16x16x32_bf16 a[120:123], a[184:187], v[144:147], a[120:123]\n\t" \
  "v_cvt_pk_bf16_f32 v143, v46, v47\n\t" \
  "ds_read_b128 a[244:247], v221 offset:12288\n\t" \
  "v_mfma_f32_16x16x32_bf16 a[120:123], a[188:191], v[148:151], a[120:123]\n\t" \
  "v_exp_f32 v64, v64\n\t" \
  "v_mfma_f32_16x16x32_bf16 v[168:171], v[216:219], v[144:147], v[168:171]\n\t" \
  "v_exp_f32 v65, v65\n\t" \
  "v_mfma_f32_16x16x32_bf16 v[168:171], v[216:219], v[148:151], v[168:171]\n\t" \
  "v_cvt_pk_bf16_f32 v144, v64, v65\n\t" \
  "buffer_load_dwordx4 v231, %[sV], s96 offen offset:1024 lds\n\t" \
  "v_mfma_f32_16x16x32_bf16 a[12:15], a[128:131], v[152:155], a[12:15]\n\t" \
  "v_exp_f32 v66, v66\n\t" \
  "v_mfma_f32_16x16x32_bf16 a[12:15], a[132:135], v[156:159], a[12:15]\n\t" \
  "ds_read_b128 a[248:251], v222 offset:12288\n\t" \
  "buffer_load_dwordx4 v230, %[sV], s96 offen offset:2048 lds\n\t" \
  "v_mfma_f32_16x16x32_bf16 a[28:31], a[136:139], v[152:155], a[28:31]\n\t" \
  "v_exp_f32 v67, v67\n\t" \
  "v_mfma_f32_16x16x32_bf16 a[28:31], a[140:143], v[156:159], a[28:31]\n\t" \
  "v_cvt_pk_bf16_f32 v145, v66, v67\n\t" \
  "ds_read_b128 a[252:255], v223 offset:12288\n\t" \
  "v_mfma_f32_16x16x32_bf16 a[44:47], a[144:147], v[152:155], a[44:47]\n\t" \
  "v_exp_f32 v68, v68\n\t" \
  "v_mfma_f32_16x16x32_bf16 a[44:47], a[148:151], v[156:159], a[44:47]\n\t" \
  "v_exp_f32 v69, v69\n\t" \
  "v_mfma_f32_16x16x32_bf16 a[60:63], a[152:155], v[152:155], a[60:63]\n\t" \
  "v_cvt_pk_bf16_f32 v146, v68, v69\n\t" \
  "buffer_load_dwordx4 v231, %[sV], s96 offen offset:3072 lds\n\t" \
  "v_mfma_f32_16x16x32_bf16 a[60:63], a[156:159], v[156:159], a[60:63]\n\t" \
  "v_exp_f32 v70, v70\n\t" \
  "v_mfma_f32_16x16x32_bf16 a[76:79], a[160:163], v[152:155], a[76:79]\n\t" \
  "v_exp_f32 v71, v71\n\t" \
  "v_mfma_f32_16x16x32_bf16 a[76:79], a[164:167], v[156:159], a[76:79]\n\t" \
  "v_cvt_pk_bf16_f32 v147, v70, v71\n\t" \
  "v_mfma_f32_16x16x32_bf16 a[92:95], a[168:171], v[152:155], a[92:95]\n\t" \
  "s_add_u32 m0, %[m], 131072\n\t" \
  "s_mov_b32 s96, 40960\n\t" \
  "buffer_load_dwordx4 v232, %[sU], s96 offen offset:2048 lds\n\t" \
  "v_mfma_f32_16x16x32_bf16 a[92:95], a[172:175], v[156:159], a[92:95]\n\t" \
  "v_lshrrev_b32 v205, 1, v233\n\t" \
  "v_lshlrev_b32 v205, 2, v205\n\t" \
  "v_mfma_f32_16x16x32_bf16 a[108:111], a[176:179], v[152:155], a[108:111]\n\t" \
  "v_add_u32 v205, 8192, v205\n\t" \
  "buffer_load_dword v209, v205, %[ws], 0 offen sc1\n\t" \
  "v_mfma_f32_16x16x32_bf16 a[108:111], a[180:183], v[156:159], a[108:111]\n\t" \
  "s_mov_b32 s96, 47104\n\t" \
  "buffer_load_dwordx4 v232, %[sU], s96 offen offset:3072 lds\n\t" \
  "v_mfma_f32_16x16x32_bf16 a[124:127], a[184:187], v[152:155], a[124:127]\n\t" \
  "v_exp_f32 v72, v72\n\t" \
  "v_mfma_f32_16x16x32_bf16 a[124:127], a[188:191], v[156:159], a[124:127]\n\t" \
  "v_exp_f32 v73, v73\n\t" \
  "v_mfma_f32_16x16x32_bf16 v[172:175], v[216:219], v[152:155], v[172:175]\n\t" \
  "v_cvt_pk_bf16_f32 v148, v72, v73\n\t" \
  "v_mfma_f32_16x16x32_bf16 v[172:175], v[216:219], v[156:159], v[172:175]\n\t" \
  "v_exp_f32 v74, v74\n\t" \
  "s_cmp_lg_u32 s99, 0\n\t" \
  "s_cbranch_scc1 .Lsf_p5\n\t" \
  ".Lsf_q5:\n\t" \
  "s_waitcnt lgkmcnt(0)\n\t" \
  "v_mfma_f32_16x16x32_bf16 v[0:3], a[192:195], v[16:19], v[176:179]\n\t" \
  "s_mul_i32 s98, %[sofk], 5\n\t" \
  "s_xor_b32 s98, s98, 8192\n\t" \
  "s_lshr_b32 s98, s98, 1\n\t" \
  "buffer_load_dword %[pfd], v204, %[sD], s98 offen\n\t" \
  "v_mfma_f32_16x16x32_bf16 v[0:3], a[196:199], v[20:23], v[0:3]\n\t" \
  "v_exp_f32 v75, v75\n\t" \
  "v_mfma_f32_16x16x32_bf16 v[0:3], a[200:203], v[24:27], v[0:3]\n\t" \
  "v_cvt_pk_bf16_f32 v149, v74, v75\n\t" \
  "ds_read_b64_tr_b16 a[128:129], v237 offset:0\n\t" \
  "v_mfma_f32_16x16x32_bf16 v[0:3], a[204:207], v[28:31], v[0:3]\n\t" \
  "v_exp_f32 v76, v76\n\t" \
  "v_mfma_f32_16x16x32_bf16 v[4:7], a[220:223], v[28:31], v[176:179]\n\t" \
  "ds_read_b64_tr_b16 a[130:131], v237 offset:4096\n\t" \
  "ds_read_b64_tr_b16 a[132:133], v237 offset:8192\n\t" \
  "v_mfma_f32_16x16x32_bf16 v[4:7], a[216:219], v[24:27], v[4:7]\n\t" \
  "v_exp_f32 v77, v77\n\t" \
  "v_mfma_f32_16x16x32_bf16 v[4:7], a[212:215], v[20:23], v[4:7]\n\t" \
  "v_cvt_pk_bf16_f32 v150, v76, v77\n\t" \
  "v_max3_f32 v192, v0, v1, v2\n\t" \
  "v_mfma_f32_16x16x32_bf16 v[4:7], a[208:211], v[16:19], v[4:7]\n\t" \
  "v_exp_f32 v78, v78\n\t" \
  "v_mfma_f32_16x16x32_bf16 v[8:11], a[224:227], v[16:19], v[176:179]\n\t" \
  "ds_read_b64_tr_b16 a[134:135], v237 offset:12288\n\t" \
  "ds_read_b64_tr_b16 a[136:137], v238 offset:0\n\t" \
  "v_mfma_f32_16x16x32_bf16 v[8:11], a[228:231], v[20:23], v[8:11]\n\t" \
  "v_exp_f32 v79, v79\n\t" \
  "v_mfma_f32_16x16x32_bf16 v[8:11], a[232:235], v[24:27], v[8:11]\n\t" \
  "v_cvt_pk_bf16_f32 v151, v78, v79\n\t" \
  "v_max3_f32 v203, v3, v4, v5\n\t" \
  "v_mfma_f32_16x16x32_bf16 v[8:11], a[236:239], v[28:31], v[8:11]\n\t" \
  "v_max3_f32 v192, v192, v6, v7\n\t" \
  "ds_read_b64_tr_b16 a[138:139], v238 offset:4096\n\t" \
  "v_mfma_f32_16x16x32_bf16 v[12:15], a[252:255], v[28:31], v[176:179]\n\t" \
  "ds_read_b64_tr_b16 a[140:141], v238 offset:8192\n\t" \
  "ds_read_b64_tr_b16 a[142:143], v238 offset:12288\n\t" \
  "v_mfma_f32_16x16x32_bf16 v[12:15], a[248:251], v[24:27], v[12:15]\n\t" \
  "v_exp_f32 v96, v96\n\t" \
  "v_mfma_f32_16x16x32_bf16 v[12:15], a[244:247], v[20:23], v[12:15]\n\t" \
  "v_max3_f32 v203, v203, v8, v9\n\t" \
  "v_max3_f32 v192, v192, v10, v11\n\t" \
  "v_mfma_f32_16x16x32_bf16 v[12:15], a[240:243], v[16:19], v[12:15]\n\t" \
  "s_mul_i32 s98, %[sofk], 5\n\t" \
  "s_xor_b32 s98, s98, 12288\n\t" \
  "s_lshr_b32 s98, s98, 1\n\t" \
  "buffer_load_dword %[pfd], v204, %[sD], s98 offen\n\t" \
  "v_mfma_f32_16x16x32_bf16 v[32:35], a[192:195], v[48:51], v[180:183]\n\t" \
  "ds_read_b64_tr_b16 a[144:145], v237 offset:256\n\t" \
  "ds_read_b64_tr_b16 a[146:147], v237 offset:4352\n\t" \
  "v_mfma_f32_16x16x32_bf16 v[32:35], a[196:199], v[52:55], v[32:35]\n\t" \
  "v_exp_f32 v97, v97\n\t" \
  "v_mfma_f32_16x16x32_bf16 v[32:35], a[200:203], v[56:59], v[32:35]\n\t" \
  "v_cvt_pk_bf16_f32 v152, v96, v97\n\t" \
  "v_max3_f32 v203, v203, v12, v13\n\t" \
  "v_mfma_f32_16x16x32_bf16 v[32:35], a[204:207], v[60:63], v[32:35]\n\t" \
  "v_max3_f32 v192, v192, v14, v15\n\t" \
  "v_max_f32 v192, v192, v203\n\t" \
  "v_mfma_f32_16x16x32_bf16 v[36:39], a[220:223], v[60:63], v[180:183]\n\t" \
  "v_cmp_lt_f32 vcc, 0x42800000, v192\n\t" \
  "ds_read_b64_tr_b16 a[148:149], v237 offset:8448\n\t" \
  "v_mfma_f32_16x16x32_bf16 v[36:39], a[216:219], v[56:59], v[36:39]\n\t" \
  "s_cbranch_vccnz .Lsf_c60\n\t" \
  ".Lsf_r60:\n\t" \
  "ds_read_b64_tr_b16 a[150:151], v237 offset:12544\n\t" \
  "v_mfma_f32_16x16x32_bf16 v[36:39], a[212:215], v[52:55], v[36:39]\n\t" \
  "v_max3_f32 v192, v32, v33, v34\n\t" \
  "ds_read_b64_tr_b16 a[152:153], v238 offset:256\n\t" \
  "v_mfma_f32_16x16x32_bf16 v[36:39], a[208:211], v[48:51], v[36:39]\n\t" \
  "v_exp_f32 v98, v98\n\t" \
  "v_mfma_f32_16x16x32_bf16 v[40:43], a[224:227], v[48:51], v[180:183]\n\t" \
  "v_exp_f32 v99, v99\n\t" \
  "v_mfma_f32_16x16x32_bf16 v[40:43], a[228:231], v[52:55], v[40:43]\n\t" \
  "v_cvt_pk_bf16_f32 v153, v98, v99\n\t" \
  "ds_read_b64_tr_b16 a[154:155], v238 offset:4352\n\t" \
  "v_mfma_f32_16x16x32_bf16 v[40:43], a[232:235], v[56:59], v[40:43]\n\t" \
  "v_exp_f32 v100, v100\n\t" \
  "v_mfma_f32_16x16x32_bf16 v[40:43], a[236:239], v[60:63], v[40:43]\n\t" \
  "v_max3_f32 v203, v35, v36, v37\n\t" \
  "v_max3_f32 v192, v192, v38, v39\n\t" \
  "v_mfma_f32_16x16x32_bf16 v[44:47], a[252:255], v[60:63], v[180:183]\n\t" \
  "ds_read_b64_tr_b16 a[156:157], v238 offset:8448\n\t" \
  "ds_read_b64_tr_b16 a[158:159], v238 offset:12544\n\t" \
  "v_mfma_f32_16x16x32_bf16 v[44:47], a[248:251], v[56:59], v[44:47]\n\t" \
  "v_exp_f32 v101, v101\n\t" \
  "v_mfma_f32_16x16x32_bf16 v[44:47], a[244:247], v[52:55], v[44:47]\n\t" \
  "v_cvt_pk_bf16_f32 v154, v100, v101\n\t" \
  "ds_read_b64_tr_b16 a[160:161], v237 offset:512\n\t" \
  "v_mfma_f32_16x16x32_bf16 v[44:47], a[240:243], v[48:51], v[44:47]\n\t" \
  "v_max3_f32 v203, v203, v40, v41\n\t" \
  "v_max3_f32 v192, v192, v42, v43\n\t" \
  "v_mfma_f32_16x16x32_bf16 v[64:67], a[192:195], v[80:83], v[184:187]\n\t" \
  "ds_read_b64_tr_b16 a[162:163], v237 offset:4608\n\t" \
  "ds_read_b64_tr_b16 a[164:165], v237 offset:8704\n\t" \
  "v_mfma_f32_16x16x32_bf16 v[64:67], a[196:199], v[84:87], v[64:67]\n\t" \
  "v_exp_f32 v102, v102\n\t" \
  "v_mfma_f32_16x16x32_bf16 v[64:67], a[200:203], v[88:91], v[64:67]\n\t" \
  "v_max3_f32 v203, v203, v44, v45\n\t" \
  "v_max3_f32 v192, v192, v46, v47\n\t" \
  "v_mfma_f32_16x16x32_bf16 v[64:67], a[204:207], v[92:95], v[64:67]\n\t" \
  "v_max_f32 v192, v192, v203\n\t" \
  "ds_read_b64_tr_b16 a[166:167], v237 offset:12800\n\t" \
  "v_mfma_f32_16x16x32_bf16 v[68:71], a[220:223], v[92:95], v[184:187]\n\t" \
  "v_cmp_lt_f32 vcc, 0x42800000, v192\n\t" \
  "ds_read_b64_tr_b16 a[168:169], v238 offset:512\n\t" \
  "v_mfma_f32_16x16x32_bf16 v[68:71], a[216:219], v[88:91], v[68:71]\n\t" \
  "s_cbranch_vccnz .Lsf_c61\n\t" \
  ".Lsf_r61:\n\t" \
  "ds_read_b64_tr_b16 a[170:171], v238 offset:4608\n\t" \
  "v_mfma_f32_16x16x32_bf16 v[68:71], a[212:215], v[84:87], v[68:71]\n\t" \
  "v_max3_f32 v192, v64, v65, v66\n\t" \
  "ds_read_b64_tr_b16 a[172:173], v238 offset:8704\n\t" \
  "v_mfma_f32_16x16x32_bf16 v[68:71], a[208:211], v[80:83], v[68:71]\n\t" \
  "v_exp_f32 v103, v103\n\t" \
  "v_mfma_f32_16x16x32_bf16 v[72:75], a[224:227], v[80:83], v[184:187]\n\t" \
  "v_cvt_pk_bf16_f32 v155, v102, v103\n\t" \
  "ds_read_b64_tr_b16 a[174:175], v238 offset:12800\n\t" \
  "v_mfma_f32_16x16x32_bf16 v[72:75], a[228:231], v[84:87], v[72:75]\n\t" \
  "v_exp_f32 v104, v104\n\t" \
  "v_mfma_f32_16x16x32_bf16 v[72:75], a[232:235], v[88:91], v[72:75]\n\t" \
  "v_exp_f32 v105, v105\n\t" \
  "v_mfma_f32_16x16x32_bf16 v[72:75], a[236:239], v[92:95], v[72:75]\n\t" \
  "v_cvt_pk_bf16_f32 v156, v104, v105\n\t" \
  "v_max3_f32 v203, v67, v68, v69\n\t" \
  "v_mfma_f32_16x16x32_bf16 v[76:79], a[252:255], v[92:95], v[184:187]\n\t" \
  "v_max3_f32 v192, v192, v70, v71\n\t" \
  "ds_read_b64_tr_b16 a[176:177], v237 offset:768\n\t" \
  "v_mfma_f32_16x16x32_bf16 v[76:79], a[248:251], v[88:91], v[76:79]\n\t" \
  "v_exp_f32 v106, v106\n\t" \
  "v_mfma_f32_16x16x32_bf16 v[76:79], a[244:247], v[84:87], v[76:79]\n\t" \
  "ds_read_b64_tr_b16 a[178:179], v237 offset:4864\n\t" \
  "v_max3_f32 v203, v203, v72, v73\n\t" \
  "v_mfma_f32_16x16x32_bf16 v[76:79], a[240:243], v[80:83], v[76:79]\n\t" \
  "v_max3_f32 v192, v192, v74, v75\n\t" \
  "ds_read_b64_tr_b16 a[180:181], v237 offset:8960\n\t" \
  "v_mfma_f32_16x16x32_bf16 v[96:99], a[192:195], v[112:115], v[188:191]\n\t" \
  "v_exp_f32 v107, v107\n\t" \
  "v_mfma_f32_16x16x32_bf16 v[96:99], a[196:199], v[116:119], v[96:99]\n\t" \
  "v_cvt_pk_bf16_f32 v157, v106, v107\n\t" \
  "ds_read_b64_tr_b16 a[182:183], v237 offset:13056\n\t" \
  "v_mfma_f32_16x16x32_bf16 v[96:99], a[200:203], v[120:123], v[96:99]\n\t" \
  "v_max3_f32 v203, v203, v76, v77\n\t" \
  "v_max3_f32 v192, v192, v78, v79\n\t" \
  "v_mfma_f32_16x16x32_bf16 v[96:99], a[204:207], v[124:127], v[96:99]\n\t" \
  "v_max_f32 v192, v192, v203\n\t" \
  "ds_read_b64_tr_b16 a[184:185], v238 offset:768\n\t" \
  "v_mfma_f32_16x16x32_bf16 v[100:103], a[220:223], v[124:127], v[188:191]\n\t" \
  "v_cmp_lt_f32 vcc, 0x42800000, v192\n\t" \
  "ds_read_b64_tr_b16 a[186:187], v238 offset:4864\n\t" \
  "v_mfma_f32_16x16x32_bf16 v[100:103], a[216:219], v[120:123], v[100:103]\n\t" \
  "s_cbranch_vccnz .Lsf_c62\n\t" \
  ".Lsf_r62:\n\t" \
  "ds_read_b64_tr_b16 a[188:189], v238 offset:8960\n\t" \
  "v_mfma_f32_16x16x32_bf16 v[100:103], a[212:215], v[116:119], v[100:103]\n\t" \
  "v_max3_f32 v192, v96, v97, v98\n\t" \
  "ds_read_b64_tr_b16 a[190:191], v238 offset:13056\n\t" \
  "v_mfma_f32_16x16x32_bf16 v[100:103], a[208:211], v[112:115], v[100:103]\n\t" \
  "v_exp_f32 v108, v108\n\t" \
  "v_mfma_f32_16x16x32_bf16 v[104:107], a[224:227], v[112:115], v[188:191]\n\t" \
  "v_exp_f32 v109, v109\n\t" \
  "v_mfma_f32_16x16x32_bf16 v[104:107], a[228:231], v[116:119], v[104:107]\n\t" \
  "v_cvt_pk_bf16_f32 v158, v108, v109\n\t" \
  "v_mfma_f32_16x16x32_bf16 v[104:107], a[232:235], v[120:123], v[104:107]\n\t" \
  "v_exp_f32 v110, v110\n\t" \
  "v_mfma_f32_16x16x32_bf16 v[104:107], a[236:239], v[124:127], v[104:107]\n\t" \
  "v_max3_f32 v203, v99, v100, v101\n\t" \
  "v_max3_f32 v192, v192, v102, v103\n\t" \
  "v_exp_f32 v111, v111\n\t" \
  "s_nop 0\n\t" \
  "v_cvt_pk_bf16_f32 v159, v110, v111\n\t" \
  "v_mfma_f32_16x16x32_bf16 v[108:111], a[252:255], v[124:127], v[188:191]\n\t" \
  "v_exp_f32 v0, v0\n\t" \
  "v_mfma_f32_16x16x32_bf16 v[108:111], a[248:251], v[120:123], v[108:111]\n\t" \
  "v_exp_f32 v1, v1\n\t" \
  "v_mfma_f32_16x16x32_bf16 v[108:111], a[244:247], v[116:119], v[108:111]\n\t" \
  "v_max3_f32 v203, v203, v104, v105\n\t" \
  "v_max3_f32 v192, v192, v106, v107\n\t" \
  "v_mfma_f32_16x16x32_bf16 v[108:111], a[240:243], v[112:115], v[108:111]\n\t" \
  "v_exp_f32 v2, v2\n\t" \
  "s_waitcnt vmcnt(5) lgkmcnt(0)\n\t" \
  "s_barrier\n\t" \
  "v_mfma_f32_16x16x32_bf16 a[0:3], a[128:131], v[128:131], a[0:3]\n\t" \
  "v_exp_f32 v3, v3\n\t" \
  "v_mfma_f32_16x16x32_bf16 a[0:3], a[132:135], v[132:135], a[0:3]\n\t" \
  "ds_read_b128 a[192:195], v220 offset:16384\n\t" \
  "ds_read_b128 a[196:199], v221 offset:16384\n\t" \
  "v_mfma_f32_16x16x32_bf16 a[16:19], a[136:139], v[128:131], a[16:19]\n\t" \
  "v_max3_f32 v203, v203, v108, v109\n\t" \
  "v_max3_f32 v192, v192, v110, v111\n\t" \
  "v_mfma_f32_16x16x32_bf16 a[16:19], a[140:143], v[132:135], a[16:19]\n\t" \
  "v_max_f32 v192, v192, v203\n\t" \
  "ds_read_b128 a[200:203], v222 offset:16384\n\t" \
  "v_mfma_f32_16x16x32_bf16 a[32:35], a[144:147], v[128:131], a[32:35]\n\t" \
  "v_cmp_lt_f32 vcc, 0x42800000, v192\n\t" \
  "ds_read_b128 a[204:207], v223 offset:16384\n\t" \
  "v_mfma_f32_16x16x32_bf16 a[32:35], a[148:151], v[132:135], a[32:35]\n\t" \
  "s_cbranch_vccnz .Lsf_c63\n\t" \
  ".Lsf_r63:\n\t" \
  "ds_read_b128 a[208:211], v220 offset:20480\n\t" \
  "v_mfma_f32_16x16x32_bf16 a[48:51], a[152:155], v[128:131], a[48:51]\n\t" \
  "s_waitcnt vmcnt(3)\n\t" \
  "v_and_b32 v206, 0xbfffbfff, v209\n\t" \
  "v_cmp_ne_u32_e64 s[96:97], %[tag], v206\n\t" \
  "s_cmp_lg_u64 s[96:97], 0\n\t" \
  "s_cbranch_scc1 .Lsf_v\n\t" \
  ".Lsf_w:\n\t" \
  "v_mfma_f32_16x16x32_bf16 a[48:51], a[156:159], v[132:135], a[48:51]\n\t" \
  "v_and_b32 v206, 1, v233\n\t" \
  "v_lshlrev_b32 v206, 4, v206\n\t" \
  "v_mfma_f32_16x16x32_bf16 a[64:67], a[160:163], v[128:131], a[64:67]\n\t" \
  "v_lshrrev_b32 v206, v206, v209\n\t" \
  "v_and_b32 v206, 0x4000, v206\n\t" \
  "v_mfma_f32_16x16x32_bf16 a[64:67], a[164:167], v[132:135], a[64:67]\n\t" \
  "v_cmp_ne_u32_e64 s[96:97], 0, v206\n\t" \
  "s_bcnt1_i32_b32 s98, s96\n\t" \
  "s_bcnt1_i32_b32 %[na], s97\n\t" \
  "s_cmp_gt_u32 s98, %[na]\n\t" \
  "s_cselect_b32 %[nb], 0x2000, 0\n\t" \
  "s_cselect_b32 s98, s96, s97\n\t" \
  "s_cselect_b32 s96, s97, s96\n\t" \
  "v_mfma_f32_16x16x32_bf16 a[80:83], a[168:171], v[128:131], a[80:83]\n\t" \
  "s_lshr_b32 s97, s98, 16\n\t" \
  "s_bcnt1_i32_b32 s97, s97\n\t" \
  "v_mfma_f32_16x16x32_bf16 a[80:83], a[172:175], v[132:135], a[80:83]\n\t" \
  "s_lshr_b32 s96, s96, 16\n\t" \
  "s_bcnt1_i32_b32 s96, s96\n\t" \
  "v_mfma_f32_16x16x32_bf16 a[96:99], a[176:179], v[128:131], a[96:99]\n\t" \
  "s_add_u32 s96, s96, 7\n\t" \
  "s_cmp_ge_u32 s97, s96\n\t" \
  "s_cselect_b32 %[na], -1, 0\n\t" \
  "v_mfma_f32_16x16x32_bf16 a[96:99], a[180:183], v[132:135], a[96:99]\n\t" \
  "s_xor_b32 s98, %[po], %[nb]\n\t" \
  "s_and_b32 s98, s98, %[na]\n\t" \
  "s_xor_b32 %[eso], %[eso], s98\n\t" \
  "v_mfma_f32_16x16x32_bf16 a[112:115], a[184:187], v[128:131], a[112:115]\n\t" \
  "s_xor_b32 s98, %[px], %[nb]\n\t" \
  "s_and_b32 s98, s98, %[na]\n\t" \
  "s_xor_b32 %[xcb], %[xcb], s98\n\t" \
  "v_mfma_f32_16x16x32_bf16 a[112:115], a[188:191], v[132:135], a[112:115]\n\t" \
  "s_xor_b32 %[frb], %[frb], %[nb]\n\t" \
  "s_xor_b32 %[fw2], %[fw2], %[nb]\n\t" \
  "v_mfma_f32_16x16x32_bf16 v[160:163], v[216:219], v[128:131], v[160:163]\n\t" \
  "v_exp_f32 v4, v4\n\t" \
  "v_mfma_f32_16x16x32_bf16 v[160:163], v[216:219], v[132:135], v[160:163]\n\t" \
  "v_cvt_pk_bf16_f32 v128, v0, v1\n\t" \
  "v_cvt_pk_bf16_f32 v129, v2, v3\n\t" \
  "v_mfma_f32_16x16x32_bf16 a[4:7], a[128:131], v[136:139], a[4:7]\n\t" \
  "v_exp_f32 v5, v5\n\t" \
  "v_mfma_f32_16x16x32_bf16 a[4:7], a[132:135], v[140:143], a[4:7]\n\t" \
  "v_cvt_pk_bf16_f32 v130, v4, v5\n\t" \
  "ds_read_b128 a[212:215], v221 offset:20480\n\t" \
  "v_mfma_f32_16x16x32_bf16 a[20:23], a[136:139], v[136:139], a[20:23]\n\t" \
  "v_exp_f32 v6, v6\n\t" \
  "v_mfma_f32_16x16x32_bf16 a[20:23], a[140:143], v[140:143], a[20:23]\n\t" \
  "v_exp_f32 v7, v7\n\t" \
  "v_mfma_f32_16x16x32_bf16 a[36:39], a[144:147], v[136:139], a[36:39]\n\t" \
  "v_cvt_pk_bf16_f32 v131, v6, v7\n\t" \
  "s_add_u32 m0, %[m], 147456\n\t" \
  "v_mfma_f32_16x16x32_bf16 a[36:39], a[148:151], v[140:143], a[36:39]\n\t" \
  "ds_read_b128 a[216:219], v222 offset:20480\n\t" \
  "s_add_u32 s96, %[sofk], 114688\n\t" \
  "v_mfma_f32_16x16x32_bf16 a[52:55], a[152:155], v[136:139], a[52:55]\n\t" \
  "v_exp_f32 v8, v8\n\t" \
  "v_mfma_f32_16x16x32_bf16 a[52:55], a[156:159], v[140:143], a[52:55]\n\t" \
  "v_exp_f32 v9, v9\n\t" \
  "v_mfma_f32_16x16x32_bf16 a[68:71], a[160:163], v[136:139], a[68:71]\n\t" \
  "v_cvt_pk_bf16_f32 v132, v8, v9\n\t" \
  "buffer_load_dwordx4 v230, %[sV], s96 offen offset:0 lds\n\t" \
  "v_mfma_f32_16x16x32_bf16 a[68:71], a[164:167], v[140:143], a[68:71]\n\t" \
  "v_exp_f32 v10, v10\n\t" \
  "v_mfma_f32_16x16x32_bf16 a[84:87], a[168:171], v[136:139], a[84:87]\n\t" \
  "ds_read_b128 a[220:223], v223 offset:20480\n\t" \
  "buffer_load_dwordx4 v231, %[sV], s96 offen offset:1024 lds\n\t" \
  "v_mfma_f32_16x16x32_bf16 a[84:87], a[172:175], v[140:143], a[84:87]\n\t" \
  "v_exp_f32 v11, v11\n\t" \
  "v_mfma_f32_16x16x32_bf16 a[100:103], a[176:179], v[136:139], a[100:103]\n\t" \
  "v_cvt_pk_bf16_f32 v133, v10, v11\n\t" \
  "ds_read_b128 a[224:227], v220 offset:24576\n\t" \
  "v_mfma_f32_16x16x32_bf16 a[100:103], a[180:183], v[140:143], a[100:103]\n\t" \
  "v_exp_f32 v12, v12\n\t" \
  "v_mfma_f32_16x16x32_bf16 a[116:119], a[184:187], v[136:139], a[116:119]\n\t" \
  "v_exp_f32 v13, v13\n\t" \
  "v_mfma_f32_16x16x32_bf16 a[116:119], a[188:191], v[140:143], a[116:119]\n\t" \
  "v_cvt_pk_bf16_f32 v134, v12, v13\n\t" \
  "buffer_load_dwordx4 v230, %[sV], s96 offen offset:2048 lds\n\t" \
  "v_mfma_f32_16x16x32_bf16 v[164:167], v[216:219], v[136:139], v[164:167]\n\t" \
  "buffer_load_dwordx4 v231, %[sV], s96 offen offset:3072 lds\n\t" \
  "ds_read_b128 a[228:231], v221 offset:24576\n\t" \
  "v_mfma_f32_16x16x32_bf16 v[164:167], v[216:219], v[140:143], v[164:167]\n\t" \
  "v_exp_f32 v14, v14\n\t" \
  "v_mfma_f32_16x16x32_bf16 a[8:11], a[128:131], v[144:147], a[8:11]\n\t" \
  "v_exp_f32 v15, v15\n\t" \
  "v_mfma_f32_16x16x32_bf16 a[8:11], a[132:135], v[148:151], a[8:11]\n\t" \
  "v_cvt_pk_bf16_f32 v135, v14, v15\n\t" \
  "s_add_u32 m0, %[m], 16384\n\t" \
  "v_mfma_f32_16x16x32_bf16 a[24:27], a[136:139], v[144:147], a[24:27]\n\t" \
  "v_exp_f32 v32, v32\n\t" \
  "v_mfma_f32_16x16x32_bf16 a[24:27], a[140:143], v[148:151], a[24:27]\n\t" \
  "ds_read_b128 a[232:235], v222 offset:24576\n\t" \
  "s_xor_b32 s97, %[wob], 131072\n\t" \
  "v_mfma_f32_16x16x32_bf16 a[40:43], a[144:147], v[144:147], a[40:43]\n\t" \
  "v_exp_f32 v33, v33\n\t" \
  "v_mfma_f32_16x16x32_bf16 a[40:43], a[148:151], v[148:151], a[40:43]\n\t" \
  "v_cvt_pk_bf16_f32 v136, v32, v33\n\t" \
  "ds_read_b128 a[236:239], v223 offset:24576\n\t" \
  "v_mfma_f32_16x16x32_bf16 a[56:59], a[152:155], v[144:147], a[56:59]\n\t" \
  "v_exp_f32 v34, v34\n\t" \
  "v_mfma_f32_16x16x32_bf16 a[56:59], a[156:159], v[148:151], a[56:59]\n\t" \
  "v_exp_f32 v35, v35\n\t" \
  "v_mfma_f32_16x16x32_bf16 a[72:75], a[160:163], v[144:147], a[72:75]\n\t" \
  "v_cvt_pk_bf16_f32 v137, v34, v35\n\t" \
  "buffer_load_dwordx4 v240, %[wo], s97 offen offset:0 lds\n\t" \
  "v_mfma_f32_16x16x32_bf16 a[72:75], a[164:167], v[148:151], a[72:75]\n\t" \
  "s_add_u32 s97, s97, 7168\n\t" \
  "buffer_load_dwordx4 v241, %[wo], s97 offen offset:1024 lds\n\t" \
  "v_mfma_f32_16x16x32_bf16 a[88:91], a[168:171], v[144:147], a[88:91]\n\t" \
  "v_exp_f32 v36, v36\n\t" \
  "v_mfma_f32_16x16x32_bf16 a[88:91], a[172:175], v[148:151], a[88:91]\n\t" \
  "ds_read_b128 a[240:243], v220 offset:28672\n\t" \
  "ds_read_b128 a[244:247], v221 offset:28672\n\t" \
  "v_mfma_f32_16x16x32_bf16 a[104:107], a[176:179], v[144:147], a[104:107]\n\t" \
  "v_exp_f32 v37, v37\n\t" \
  "v_mfma_f32_16x16x32_bf16 a[104:107], a[180:183], v[148:151], a[104:107]\n\t" \
  "v_cvt_pk_bf16_f32 v138, v36, v37\n\t" \
  "ds_read_b128 a[248:251], v222 offset:28672\n\t" \
  "v_mfma_f32_16x16x32_bf16 a[120:123], a[184:187], v[144:147], a[120:123]\n\t" \
  "s_add_u32 s97, s97, 7168\n\t" \
  "buffer_load_dwordx4 v240, %[wo], s97 offen offset:2048 lds\n\t" \
  "v_mfma_f32_16x16x32_bf16 a[120:123], a[188:191], v[148:151], a[120:123]\n\t" \
  "v_exp_f32 v38, v38\n\t" \
  "v_mfma_f32_16x16x32_bf16 v[168:171], v[216:219], v[144:147], v[168:171]\n\t" \
  "s_add_u32 s97, s97, 7168\n\t" \
  "buffer_load_dwordx4 v241, %[wo], s97 offen offset:3072 lds\n\t" \
  "v_mfma_f32_16x16x32_bf16 v[168:171], v[216:219], v[148:151], v[168:171]\n\t" \
  "v_exp_f32 v39, v39\n\t" \
  "v_mfma_f32_16x16x32_bf16 a[12:15], a[128:131], v[152:155], a[12:15]\n\t" \
  "v_cvt_pk_bf16_f32 v139, v38, v39\n\t" \
  "s_add_u32 m0, %[m], 49152\n\t" \
  "v_mfma_f32_16x16x32_bf16 a[12:15], a[132:135], v[156:159], a[12:15]\n\t" \
  "v_exp_f32 v40, v40\n\t" \
  "v_mfma_f32_16x16x32_bf16 a[28:31], a[136:139], v[152:155], a[28:31]\n\t" \
  "v_exp_f32 v41, v41\n\t" \
  "v_mfma_f32_16x16x32_bf16 a[28:31], a[140:143], v[156:159], a[28:31]\n\t" \
  "v_cvt_pk_bf16_f32 v140, v40, v41\n\t" \
  "s_xor_b32 s97, %[wob], 262144\n\t" \
  "v_mfma_f32_16x16x32_bf16 a[44:47], a[144:147], v[152:155], a[44:47]\n\t" \
  "ds_read_b128 a[252:255], v223 offset:28672\n\t" \
  "buffer_load_dwordx4 v240, %[wo], s97 offen offset:0 lds\n\t" \
  "v_mfma_f32_16x16x32_bf16 a[44:47], a[148:151], v[156:159], a[44:47]\n\t" \
  "v_exp_f32 v42, v42\n\t" \
  "v_mfma_f32_16x16x32_bf16 a[60:63], a[152:155], v[152:155], a[60:63]\n\t" \
  "v_exp_f32 v43, v43\n\t" \
  "v_mfma_f32_16x16x32_bf16 a[60:63], a[156:159], v[156:159], a[60:63]\n\t" \
  "v_cvt_pk_bf16_f32 v141, v42, v43\n\t" \
  "v_mfma_f32_16x16x32_bf16 a[76:79], a[160:163], v[152:155], a[76:79]\n\t" \
  "s_add_u32 s97, s97, 7168\n\t" \
  "buffer_load_dwordx4 v241, %[wo], s97 offen offset:1024 lds\n\t" \
  "v_mfma_f32_16x16x32_bf16 a[76:79], a[164:167], v[156:159], a[76:79]\n\t" \
  "v_exp_f32 v44, v44\n\t" \
  "v_mfma_f32_16x16x32_bf16 a[92:95], a[168:171], v[152:155], a[92:95]\n\t" \
  "v_exp_f32 v45, v45\n\t" \
  "v_mfma_f32_16x16x32_bf16 a[92:95], a[172:175], v[156:159], a[92:95]\n\t" \
  "v_cvt_pk_bf16_f32 v142, v44, v45\n\t" \
  "v_mfma_f32_16x16x32_bf16 a[108:111], a[176:179], v[152:155], a[108:111]\n\t" \
  "s_add_u32 s97, s97, 7168\n\t" \
  "buffer_load_dwordx4 v240, %[wo], s97 offen offset:2048 lds\n\t" \
  "v_mfma_f32_16x16x32_bf16 a[108:111], a[180:183], v[156:159], a[108:111]\n\t" \
  "v_exp_f32 v46, v46\n\t" \
  "v_mfma_f32_16x16x32_bf16 a[124:127], a[184:187], v[152:155], a[124:127]\n\t" \
  "s_add_u32 s97, s97, 7168\n\t" \
  "buffer_load_dwordx4 v241, %[wo], s97 offen offset:3072 lds\n\t" \
  "v_mfma_f32_16x16x32_bf16 a[124:127], a[188:191], v[156:159], a[124:127]\n\t" \
  "v_exp_f32 v47, v47\n\t" \
  "v_mfma_f32_16x16x32_bf16 v[172:175], v[216:219], v[152:155], v[172:175]\n\t" \
  "v_cvt_pk_bf16_f32 v143, v46, v47\n\t" \
  "v_mfma_f32_16x16x32_bf16 v[172:175], v[216:219], v[156:159], v[172:175]\n\t" \
  "v_exp_f32 v64, v64\n\t" \
  "s_cmp_lg_u32 s99, 0\n\t" \
  "s_cbranch_scc1 .Lsf_p6\n\t" \
  ".Lsf_q6:\n\t" \
  "s_waitcnt lgkmcnt(0)\n\t" \
  "s_barrier\n\t" \
  "v_mfma_f32_16x16x32_bf16 v[0:3], a[192:195], v[16:19], v[176:179]\n\t" \
  "v_exp_f32 v65, v65\n\t" \
  "v_mfma_f32_16x16x32_bf16 v[0:3], a[196:199], v[20:23], v[0:3]\n\t" \
  "v_cvt_pk_bf16_f32 v144, v64, v65\n\t" \
  "ds_read_b64_tr_b16 a[128:129], v228 offset:0\n\t" \
  "v_mfma_f32_16x16x32_bf16 v[0:3], a[200:203], v[24:27], v[0:3]\n\t" \
  "v_exp_f32 v66, v66\n\t" \
  "v_mfma_f32_16x16x32_bf16 v[0:3], a[204:207], v[28:31], v[0:3]\n\t" \
  "v_exp_f32 v67, v67\n\t" \
  "v_mfma_f32_16x16x32_bf16 v[4:7], a[220:223], v[28:31], v[176:179]\n\t" \
  "v_cvt_pk_bf16_f32 v145, v66, v67\n\t" \
  "ds_read_b64_tr_b16 a[130:131], v228 offset:4096\n\t" \
  "v_mfma_f32_16x16x32_bf16 v[4:7], a[216:219], v[24:27], v[4:7]\n\t" \
  "v_exp_f32 v68, v68\n\t" \
  "v_mfma_f32_16x16x32_bf16 v[4:7], a[212:215], v[20:23], v[4:7]\n\t" \
  "ds_read_b64_tr_b16 a[132:133], v228 offset:8192\n\t" \
  "v_max3_f32 v192, v0, v1, v2\n\t" \
  "v_mfma_f32_16x16x32_bf16 v[4:7], a[208:211], v[16:19], v[4:7]\n\t" \
  "v_exp_f32 v69, v69\n\t" \
  "v_mfma_f32_16x16x32_bf16 v[8:11], a[224:227], v[16:19], v[176:179]\n\t" \
  "v_cvt_pk_bf16_f32 v146, v68, v69\n\t" \
  "ds_read_b64_tr_b16 a[134:135], v228 offset:12288\n\t" \
  "v_mfma_f32_16x16x32_bf16 v[8:11], a[228:231], v[20:23], v[8:11]\n\t" \
  "ds_read_b64_tr_b16 a[136:137], v229 offset:0\n\t" \
  "ds_read_b64_tr_b16 a[138:139], v229 offset:4096\n\t" \
  "v_mfma_f32_16x16x32_bf16 v[8:11], a[232:235], v[24:27], v[8:11]\n\t" \
  "v_max3_f32 v203, v3, v4, v5\n\t" \
  "v_max3_f32 v192, v192, v6, v7\n\t" \
  "v_mfma_f32_16x16x32_bf16 v[8:11], a[236:239], v[28:31], v[8:11]\n\t" \
  "v_exp_f32 v70, v70\n\t" \
  "v_mfma_f32_16x16x32_bf16 v[12:15], a[252:255], v[28:31], v[176:179]\n\t" \
  "v_exp_f32 v71, v71\n\t" \
  "v_mfma_f32_16x16x32_bf16 v[12:15], a[248:251], v[24:27], v[12:15]\n\t" \
  "v_cvt_pk_bf16_f32 v147, v70, v71\n\t" \
  "ds_read_b64_tr_b16 a[140:141], v229 offset:8192\n\t" \
  "v_mfma_f32_16x16x32_bf16 v[12:15], a[244:247], v[20:23], v[12:15]\n\t" \
  "v_exp_f32 v72, v72\n\t" \
  "v_mfma_f32_16x16x32_bf16 v[12:15], a[240:243], v[16:19], v[12:15]\n\t" \
  "v_max3_f32 v203, v203, v8, v9\n\t" \
  "v_max3_f32 v192, v192, v10, v11\n\t" \
  "v_mfma_f32_16x16x32_bf16 v[32:35], a[192:195], v[48:51], v[180:183]\n\t" \
  "ds_read_b64_tr_b16 a[142:143], v229 offset:12288\n\t" \
  "ds_read_b64_tr_b16 a[144:145], v228 offset:256\n\t" \
  "v_mfma_f32_16x16x32_bf16 v[32:35], a[196:199], v[52:55], v[32:35]\n\t" \
  "v_exp_f32 v73, v73\n\t" \
  "v_mfma_f32_16x16x32_bf16 v[32:35], a[200:203], v[56:59], v[32:35]\n\t" \
  "v_cvt_pk_bf16_f32 v148, v72, v73\n\t" \
  "ds_read_b64_tr_b16 a[146:147], v228 offset:4352\n\t" \
  "v_mfma_f32_16x16x32_bf16 v[32:35], a[204:207], v[60:63], v[32:35]\n\t" \
  "v_max3_f32 v203, v203, v12, v13\n\t" \
  "v_max3_f32 v192, v192, v14, v15\n\t" \
  "v_mfma_f32_16x16x32_bf16 v[36:39], a[220:223], v[60:63], v[180:183]\n\t" \
  "v_max_f32 v192, v192, v203\n\t" \
  "ds_read_b64_tr_b16 a[148:149], v228 offset:8448\n\t" \
  "v_mfma_f32_16x16x32_bf16 v[36:39], a[216:219], v[56:59], v[36:39]\n\t" \
  "v_cmp_lt_f32 vcc, 0x42800000, v192\n\t" \
  "ds_read_b64_tr_b16 a[150:151], v228 offset:12544\n\t" \
  "v_mfma_f32_16x16x32_bf16 v[36:39], a[212:215], v[52:55], v[36:39]\n\t" \
  "s_cbranch_vccnz .Lsf_c70\n\t" \
  ".Lsf_r70:\n\t" \
  "v_max3_f32 v192, v32, v33, v34\n\t" \
  "v_mfma_f32_16x16x32_bf16 v[36:39], a[208:211], v[48:51], v[36:39]\n\t" \
  "s_add_u32 m0, %[m], 65536\n\t" \
  "ds_read_b64_tr_b16 a[152:153], v229 offset:256\n\t" \
  "v_mfma_f32_16x16x32_bf16 v[40:43], a[224:227], v[48:51], v[180:183]\n\t" \
  "v_exp_f32 v74, v74\n\t" \
  "v_mfma_f32_16x16x32_bf16 v[40:43], a[228:231], v[52:55], v[40:43]\n\t" \
  "ds_read_b64_tr_b16 a[154:155], v229 offset:4352\n\t" \
  "ds_read_b64_tr_b16 a[156:157], v229 offset:8448\n\t" \
  "v_mfma_f32_16x16x32_bf16 v[40:43], a[232:235], v[56:59], v[40:43]\n\t" \
  "v_max3_f32 v203, v35, v36, v37\n\t" \
  "v_max3_f32 v192, v192, v38, v39\n\t" \
  "v_mfma_f32_16x16x32_bf16 v[40:43], a[236:239], v[60:63], v[40:43]\n\t" \
  "s_xor_b32 s97, %[wob], 393216\n\t" \
  "ds_read_b64_tr_b16 a[158:159], v229 offset:12544\n\t" \
  "v_mfma_f32_16x16x32_bf16 v[44:47], a[252:255], v[60:63], v[180:183]\n\t" \
  "v_exp_f32 v75, v75\n\t" \
  "v_mfma_f32_16x16x32_bf16 v[44:47], a[248:251], v[56:59], v[44:47]\n\t" \
  "v_cvt_pk_bf16_f32 v149, v74, v75\n\t" \
  "ds_read_b64_tr_b16 a[160:161], v228 offset:512\n\t" \
  "v_mfma_f32_16x16x32_bf16 v[44:47], a[244:247], v[52:55], v[44:47]\n\t" \
  "v_max3_f32 v203, v203, v40, v41\n\t" \
  "v_max3_f32 v192, v192, v42, v43\n\t" \
  "v_mfma_f32_16x16x32_bf16 v[44:47], a[240:243], v[48:51], v[44:47]\n\t" \
  "ds_read_b64_tr_b16 a[162:163], v228 offset:4608\n\t" \
  "buffer_load_dwordx4 v240, %[wo], s97 offen offset:0 lds\n\t" \
  "v_mfma_f32_16x16x32_bf16 v[64:67], a[192:195], v[80:83], v[184:187]\n\t" \
  "v_exp_f32 v76, v76\n\t" \
  "v_mfma_f32_16x16x32_bf16 v[64:67], a[196:199], v[84:87], v[64:67]\n\t" \
  "v_exp_f32 v77, v77\n\t" \
  "v_mfma_f32_16x16x32_bf16 v[64:67], a[200:203], v[88:91], v[64:67]\n\t" \
  "v_cvt_pk_bf16_f32 v150, v76, v77\n\t" \
  "ds_read_b64_tr_b16 a[164:165], v228 offset:8704\n\t" \
  "v_mfma_f32_16x16x32_bf16 v[64:67], a[204:207], v[92:95], v[64:67]\n\t" \
  "v_max3_f32 v203, v203, v44, v45\n\t" \
  "v_max3_f32 v192, v192, v46, v47\n\t" \
  "v_mfma_f32_16x16x32_bf16 v[68:71], a[220:223], v[92:95], v[184:187]\n\t" \
  "v_max_f32 v192, v192, v203\n\t" \
  "ds_read_b64_tr_b16 a[166:167], v228 offset:12800\n\t" \
  "v_mfma_f32_16x16x32_bf16 v[68:71], a[216:219], v[88:91], v[68:71]\n\t" \
  "v_cmp_lt_f32 vcc, 0x42800000, v192\n\t" \
  "ds_read_b64_tr_b16 a[168:169], v229 offset:512\n\t" \
  "v_mfma_f32_16x16x32_bf16 v[68:71], a[212:215], v[84:87], v[68:71]\n\t" \
  "s_cbranch_vccnz .Lsf_c71\n\t" \
  ".Lsf_r71:\n\t" \
  "v_max3_f32 v192, v64, v65, v66\n\t" \
  "v_mfma_f32_16x16x32_bf16 v[68:71], a[208:211], v[80:83], v[68:71]\n\t" \
  "s_add_u32 s97, s97, 7168\n\t" \
  "buffer_load_dwordx4 v241, %[wo], s97 offen offset:1024 lds\n\t" \
  "v_mfma_f32_16x16x32_bf16 v[72:75], a[224:227], v[80:83], v[184:187]\n\t" \
  "ds_read_b64_tr_b16 a[170:171], v229 offset:4608\n\t" \
  "ds_read_b64_tr_b16 a[172:173], v229 offset:8704\n\t" \
  "v_mfma_f32_16x16x32_bf16 v[72:75], a[228:231], v[84:87], v[72:75]\n\t" \
  "s_add_u32 s97, s97, 7168\n\t" \
  "buffer_load_dwordx4 v240, %[wo], s97 offen offset:2048 lds\n\t" \
  "v_mfma_f32_16x16x32_bf16 v[72:75], a[232:235], v[88:91], v[72:75]\n\t" \
  "v_max3_f32 v203, v67, v68, v69\n\t" \
  "v_max3_f32 v192, v192, v70, v71\n\t" \
  "v_mfma_f32_16x16x32_bf16 v[72:75], a[236:239], v[92:95], v[72:75]\n\t" \
  "ds_read_b64_tr_b16 a[174:175], v229 offset:12800\n\t" \
  "ds_read_b64_tr_b16 a[176:177], v228 offset:768\n\t" \
  "v_exp_f32 v78, v78\n\t" \
  "v_exp_f32 v79, v79\n\t" \
  "s_nop 0\n\t" \
  "v_cvt_pk_bf16_f32 v151, v78, v79\n\t" \
  "v_mfma_f32_16x16x32_bf16 v[76:79], a[252:255], v[92:95], v[184:187]\n\t" \
  "v_exp_f32 v96, v96\n\t" \
  "v_mfma_f32_16x16x32_bf16 v[76:79], a[248:251], v[88:91], v[76:79]\n\t" \
  "ds_read_b64_tr_b16 a[178:179], v228 offset:4864\n\t" \
  "v_max3_f32 v203, v203, v72, v73\n\t" \
  "v_mfma_f32_16x16x32_bf16 v[76:79], a[244:247], v[84:87], v[76:79]\n\t" \
  "v_max3_f32 v192, v192, v74, v75\n\t" \
  "ds_read_b64_tr_b16 a[180:181], v228 offset:8960\n\t" \
  "v_mfma_f32_16x16x32_bf16 v[76:79], a[240:243], v[80:83], v[76:79]\n\t" \
  "s_add_u32 s97, s97, 7168\n\t" \
  "buffer_load_dwordx4 v241, %[wo], s97 offen offset:3072 lds\n\t" \
  "v_exp_f32 v97, v97\n\t" \
  "v_exp_f32 v98, v98\n\t" \
  "v_exp_f32 v99, v99\n\t" \
  "v_cvt_pk_bf16_f32 v152, v96, v97\n\t" \
  "v_cvt_pk_bf16_f32 v153, v98, v99\n\t" \
  "v_mfma_f32_16x16x32_bf16 v[96:99], a[192:195], v[112:115], v[188:191]\n\t" \
  "ds_read_b64_tr_b16 a[182:183], v228 offset:13056\n\t" \
  "v_max3_f32 v203, v203, v76, v77\n\t" \
  "v_mfma_f32_16x16x32_bf16 v[96:99], a[196:199], v[116:119], v[96:99]\n\t" \
  "v_max3_f32 v192, v192, v78, v79\n\t" \
  "v_max_f32 v192, v192, v203\n\t" \
  "v_mfma_f32_16x16x32_bf16 v[96:99], a[200:203], v[120:123], v[96:99]\n\t" \
  "v_cmp_lt_f32 vcc, 0x42800000, v192\n\t" \
  "ds_read_b64_tr_b16 a[184:185], v229 offset:768\n\t" \
  "v_mfma_f32_16x16x32_bf16 v[96:99], a[204:207], v[124:127], v[96:99]\n\t" \
  "s_cbranch_vccnz .Lsf_c72\n\t" \
  ".Lsf_r72:\n\t" \
  "ds_read_b64_tr_b16 a[186:187], v229 offset:4864\n\t" \
  "v_exp_f32 v100, v100\n\t" \
  "v_exp_f32 v101, v101\n\t" \
  "v_exp_f32 v102, v102\n\t" \
  "v_exp_f32 v103, v103\n\t" \
  "v_cvt_pk_bf16_f32 v154, v100, v101\n\t" \
  "v_cvt_pk_bf16_f32 v155, v102, v103\n\t" \
  "v_mfma_f32_16x16x32_bf16 v[100:103], a[220:223], v[124:127], v[188:191]\n\t" \
  "v_exp_f32 v104, v104\n\t" \
  "v_mfma_f32_16x16x32_bf16 v[100:103], a[216:219], v[120:123], v[100:103]\n\t" \
  "v_max3_f32 v192, v96, v97, v98\n\t" \
  "ds_read_b64_tr_b16 a[188:189], v229 offset:8960\n\t" \
  "v_mfma_f32_16x16x32_bf16 v[100:103], a[212:215], v[116:119], v[100:103]\n\t" \
  "v_exp_f32 v105, v105\n\t" \
  "v_mfma_f32_16x16x32_bf16 v[100:103], a[208:211], v[112:115], v[100:103]\n\t" \
  "v_cvt_pk_bf16_f32 v156, v104, v105\n\t" \
  "ds_read_b64_tr_b16 a[190:191], v229 offset:13056\n\t" \
  "v_exp_f32 v106, v106\n\t" \
  "v_exp_f32 v107, v107\n\t" \
  "s_nop 0\n\t" \
  "v_cvt_pk_bf16_f32 v157, v106, v107\n\t" \
  "v_mfma_f32_16x16x32_bf16 v[104:107], a[224:227], v[112:115], v[188:191]\n\t" \
  "v_exp_f32 v108, v108\n\t" \
  "v_mfma_f32_16x16x32_bf16 v[104:107], a[228:231], v[116:119], v[104:107]\n\t" \
  "v_exp_f32 v109, v109\n\t" \
  "v_mfma_f32_16x16x32_bf16 v[104:107], a[232:235], v[120:123], v[104:107]\n\t" \
  "v_cvt_pk_bf16_f32 v158, v108, v109\n\t" \
  "v_max3_f32 v203, v99, v100, v101\n\t" \
  "v_mfma_f32_16x16x32_bf16 v[104:107], a[236:239], v[124:127], v[104:107]\n\t" \
  "v_max3_f32 v192, v192, v102, v103\n\t" \
  "v_exp_f32 v110, v110\n\t" \
  "v_exp_f32 v111, v111\n\t" \
  "s_nop 0\n\t" \
  "v_cvt_pk_bf16_f32 v159, v110, v111\n\t" \
  "v_mfma_f32_16x16x32_bf16 v[108:111], a[252:255], v[124:127], v[188:191]\n\t" \
  "v_exp_f32 v0, v0\n\t" \
  "v_mfma_f32_16x16x32_bf16 v[108:111], a[248:251], v[120:123], v[108:111]\n\t" \
  "v_exp_f32 v1, v1\n\t" \
  "v_mfma_f32_16x16x32_bf16 v[108:111], a[244:247], v[116:119], v[108:111]\n\t" \
  "v_max3_f32 v203, v203, v104, v105\n\t" \
  "v_max3_f32 v192, v192, v106, v107\n\t" \
  "v_mfma_f32_16x16x32_bf16 v[108:111], a[240:243], v[112:115], v[108:111]\n\t" \
  "v_exp_f32 v2, v2\n\t" \
  "s_waitcnt vmcnt(4) lgkmcnt(0)\n\t" \
  "s_barrier\n\t" \
  "v_mfma_f32_16x16x32_bf16 a[0:3], a[128:131], v[128:131], a[0:3]\n\t" \
  "ds_read_b64_tr_b16 a[192:193], v237 offset:0\n\t" \
  "s_add_u32 m0, %[m], 81920\n\t" \
  "v_mfma_f32_16x16x32_bf16 a[0:3], a[132:135], v[132:135], a[0:3]\n\t" \
  "v_exp_f32 v3, v3\n\t" \
  "v_mfma_f32_16x16x32_bf16 a[16:19], a[136:139], v[128:131], a[16:19]\n\t" \
  "v_max3_f32 v203, v203, v108, v109\n\t" \
  "v_max3_f32 v192, v192, v110, v111\n\t" \
  "v_mfma_f32_16x16x32_bf16 a[16:19], a[140:143], v[132:135], a[16:19]\n\t" \
  "v_max_f32 v192, v192, v203\n\t" \
  "ds_read_b64_tr_b16 a[194:195], v237 offset:4096\n\t" \
  "v_mfma_f32_16x16x32_bf16 a[32:35], a[144:147], v[128:131], a[32:35]\n\t" \
  "v_cmp_lt_f32 vcc, 0x42800000, v192\n\t" \
  "ds_read_b64_tr_b16 a[196:197], v237 offset:8192\n\t" \
  "v_mfma_f32_16x16x32_bf16 a[32:35], a[148:151], v[132:135], a[32:35]\n\t" \
  "s_cbranch_vccnz .Lsf_c73\n\t" \
  ".Lsf_r73:\n\t" \
  "s_xor_b32 s97, %[wob], 524288\n\t" \
  "v_mfma_f32_16x16x32_bf16 a[48:51], a[152:155], v[128:131], a[48:51]\n\t" \
  "ds_read_b64_tr_b16 a[198:199], v237 offset:12288\n\t" \
  "ds_read_b64_tr_b16 a[200:201], v238 offset:0\n\t" \
  "v_mfma_f32_16x16x32_bf16 a[48:51], a[156:159], v[132:135], a[48:51]\n\t" \
  "buffer_load_dwordx4 v240, %[wo], s97 offen offset:0 lds\n\t" \
  "ds_read_b64_tr_b16 a[202:203], v238 offset:4096\n\t" \
  "v_mfma_f32_16x16x32_bf16 a[64:67], a[160:163], v[128:131], a[64:67]\n\t" \
  "ds_read_b64_tr_b16 a[204:205], v238 offset:8192\n\t" \
  "ds_read_b64_tr_b16 a[206:207], v238 offset:12288\n\t" \
  "v_mfma_f32_16x16x32_bf16 a[64:67], a[164:167], v[132:135], a[64:67]\n\t" \
  "v_exp_f32 v4, v4\n\t" \
  "v_mfma_f32_16x16x32_bf16 a[80:83], a[168:171], v[128:131], a[80:83]\n\t" \
  "s_add_u32 s97, s97, 7168\n\t" \
  "buffer_load_dwordx4 v241, %[wo], s97 offen offset:1024 lds\n\t" \
  "v_mfma_f32_16x16x32_bf16 a[80:83], a[172:175], v[132:135], a[80:83]\n\t" \
  "ds_read_b64_tr_b16 a[208:209], v237 offset:256\n\t" \
  "ds_read_b64_tr_b16 a[210:211], v237 offset:4352\n\t" \
  "v_mfma_f32_16x16x32_bf16 a[96:99], a[176:179], v[128:131], a[96:99]\n\t" \
  "v_exp_f32 v5, v5\n\t" \
  "v_mfma_f32_16x16x32_bf16 a[96:99], a[180:183], v[132:135], a[96:99]\n\t" \
  "s_add_u32 s97, s97, 7168\n\t" \
  "buffer_load_dwordx4 v240, %[wo], s97 offen offset:2048 lds\n\t" \
  "v_mfma_f32_16x16x32_bf16 a[112:115], a[184:187], v[128:131], a[112:115]\n\t" \
  "ds_read_b64_tr_b16 a[212:213], v237 offset:8448\n\t" \
  "ds_read_b64_tr_b16 a[214:215], v237 offset:12544\n\t" \
  "v_mfma_f32_16x16x32_bf16 a[112:115], a[188:191], v[132:135], a[112:115]\n\t" \
  "s_add_u32 s97, s97, 7168\n\t" \
  "buffer_load_dwordx4 v241, %[wo], s97 offen offset:3072 lds\n\t" \
  "v_mfma_f32_16x16x32_bf16 v[160:163], v[216:219], v[128:131], v[160:163]\n\t" \
  "v_exp_f32 v6, v6\n\t" \
  "v_mfma_f32_16x16x32_bf16 v[160:163], v[216:219], v[132:135], v[160:163]\n\t" \
  "v_cvt_pk_bf16_f32 v128, v0, v1\n\t" \
  "v_cvt_pk_bf16_f32 v129, v2, v3\n\t" \
  "v_mfma_f32_16x16x32_bf16 a[4:7], a[128:131], v[136:139], a[4:7]\n\t" \
  "v_cvt_pk_bf16_f32 v130, v4, v5\n\t" \
  "ds_read_b64_tr_b16 a[216:217], v238 offset:256\n\t" \
  "v_mfma_f32_16x16x32_bf16 a[4:7], a[132:135], v[140:143], a[4:7]\n\t" \
  "ds_read_b64_tr_b16 a[218:219], v238 offset:4352\n\t" \
  "ds_read_b64_tr_b16 a[220:221], v238 offset:8448\n\t" \
  "v_mfma_f32_16x16x32_bf16 a[20:23], a[136:139], v[136:139], a[20:23]\n\t" \
  "ds_read_b64_tr_b16 a[222:223], v238 offset:12544\n\t" \
  "ds_read_b64_tr_b16 a[224:225], v237 offset:512\n\t" \
  "v_mfma_f32_16x16x32_bf16 a[20:23], a[140:143], v[140:143], a[20:23]\n\t" \
  "v_exp_f32 v7, v7\n\t" \
  "v_mfma_f32_16x16x32_bf16 a[36:39], a[144:147], v[136:139], a[36:39]\n\t" \
  "v_cvt_pk_bf16_f32 v131, v6, v7\n\t" \
  "s_add_u32 m0, %[m], 114688\n\t" \
  "v_mfma_f32_16x16x32_bf16 a[36:39], a[148:151], v[140:143], a[36:39]\n\t" \
  "ds_read_b64_tr_b16 a[226:227], v237 offset:4608\n\t" \
  "ds_read_b64_tr_b16 a[228:229], v237 offset:8704\n\t" \
  "v_mfma_f32_16x16x32_bf16 a[52:55], a[152:155], v[136:139], a[52:55]\n\t" \
  "s_xor_b32 s97, %[wob], 786432\n\t" \
  "ds_read_b64_tr_b16 a[230:231], v237 offset:12800\n\t" \
  "v_mfma_f32_16x16x32_bf16 a[52:55], a[156:159], v[140:143], a[52:55]\n\t" \
  "v_exp_f32 v8, v8\n\t" \
  "v_mfma_f32_16x16x32_bf16 a[68:71], a[160:163], v[136:139], a[68:71]\n\t" \
  "v_exp_f32 v9, v9\n\t" \
  "v_mfma_f32_16x16x32_bf16 a[68:71], a[164:167], v[140:143], a[68:71]\n\t" \
  "v_cvt_pk_bf16_f32 v132, v8, v9\n\t" \
  "ds_read_b64_tr_b16 a[232:233], v238 offset:512\n\t" \
  "v_mfma_f32_16x16x32_bf16 a[84:87], a[168:171], v[136:139], a[84:87]\n\t" \
  "v_exp_f32 v10, v10\n\t" \
  "v_mfma_f32_16x16x32_bf16 a[84:87], a[172:175], v[140:143], a[84:87]\n\t" \
  "ds_read_b64_tr_b16 a[234:235], v238 offset:4608\n\t" \
  "ds_read_b64_tr_b16 a[236:237], v238 offset:8704\n\t" \
  "v_mfma_f32_16x16x32_bf16 a[100:103], a[176:179], v[136:139], a[100:103]\n\t" \
  "v_exp_f32 v11, v11\n\t" \
  "v_mfma_f32_16x16x32_bf16 a[100:103], a[180:183], v[140:143], a[100:103]\n\t" \
  "v_cvt_pk_bf16_f32 v133, v10, v11\n\t" \
  "ds_read_b64_tr_b16 a[238:239], v238 offset:12800\n\t" \
  "v_mfma_f32_16x16x32_bf16 a[116:119], a[184:187], v[136:139], a[116:119]\n\t" \
  "ds_read_b64_tr_b16 a[240:241], v237 offset:768\n\t" \
  "ds_read_b64_tr_b16 a[242:243], v237 offset:4864\n\t" \
  "v_mfma_f32_16x16x32_bf16 a[116:119], a[188:191], v[140:143], a[116:119]\n\t" \
  "v_exp_f32 v12, v12\n\t" \
  "v_mfma_f32_16x16x32_bf16 v[164:167], v[216:219], v[136:139], v[164:167]\n\t" \
  "v_exp_f32 v13, v13\n\t" \
  "v_mfma_f32_16x16x32_bf16 v[164:167], v[216:219], v[140:143], v[164:167]\n\t" \
  "v_cvt_pk_bf16_f32 v134, v12, v13\n\t" \
  "ds_read_b64_tr_b16 a[244:245], v237 offset:8960\n\t" \
  "v_mfma_f32_16x16x32_bf16 a[8:11], a[128:131], v[144:147], a[8:11]\n\t" \
  "ds_read_b64_tr_b16 a[246:247], v237 offset:13056\n\t" \
  "ds_read_b64_tr_b16 a[248:249], v238 offset:768\n\t" \
  "v_mfma_f32_16x16x32_bf16 a[8:11], a[132:135], v[148:151], a[8:11]\n\t" \
  "v_exp_f32 v14, v14\n\t" \
  "v_mfma_f32_16x16x32_bf16 a[24:27], a[136:139], v[144:147], a[24:27]\n\t" \
  "v_exp_f32 v15, v15\n\t" \
  "v_mfma_f32_16x16x32_bf16 a[24:27], a[140:143], v[148:151], a[24:27]\n\t" \
  "v_cvt_pk_bf16_f32 v135, v14, v15\n\t" \
  "ds_read_b64_tr_b16 a[250:251], v238 offset:4864\n\t" \
  "v_mfma_f32_16x16x32_bf16 a[40:43], a[144:147], v[144:147], a[40:43]\n\t" \
  "ds_read_b64_tr_b16 a[252:253], v238 offset:8960\n\t" \
  "ds_read_b64_tr_b16 a[254:255], v238 offset:13056\n\t" \
  "v_mfma_f32_16x16x32_bf16 a[40:43], a[148:151], v[148:151], a[40:43]\n\t" \
  "v_exp_f32 v32, v32\n\t" \
  "v_mfma_f32_16x16x32_bf16 a[56:59], a[152:155], v[144:147], a[56:59]\n\t" \
  "v_xor_b32 v235, 64, v234\n\t" \
  "ds_read_b64_tr_b16 v[48:49], v235 offset:0\n\t" \
  "v_mfma_f32_16x16x32_bf16 a[56:59], a[156:159], v[148:151], a[56:59]\n\t" \
  "v_exp_f32 v33, v33\n\t" \
  "v_mfma_f32_16x16x32_bf16 a[72:75], a[160:163], v[144:147], a[72:75]\n\t" \
  "v_cvt_pk_bf16_f32 v136, v32, v33\n\t" \
  "ds_read_b64_tr_b16 v[50:51], v235 offset:4096\n\t" \
  "v_mfma_f32_16x16x32_bf16 a[72:75], a[164:167], v[148:151], a[72:75]\n\t" \
  "v_exp_f32 v34, v34\n\t" \
  "v_mfma_f32_16x16x32_bf16 a[88:91], a[168:171], v[144:147], a[88:91]\n\t" \
  "v_exp_f32 v35, v35\n\t" \
  "v_mfma_f32_16x16x32_bf16 a[88:91], a[172:175], v[148:151], a[88:91]\n\t" \
  "v_cvt_pk_bf16_f32 v137, v34, v35\n\t" \
  "ds_read_b64_tr_b16 v[52:53], v235 offset:8192\n\t" \
  "v_mfma_f32_16x16x32_bf16 a[104:107], a[176:179], v[144:147], a[104:107]\n\t" \
  "v_exp_f32 v36, v36\n\t" \
  "v_mfma_f32_16x16x32_bf16 a[104:107], a[180:183], v[148:151], a[104:107]\n\t" \
  "v_exp_f32 v37, v37\n\t" \
  "v_mfma_f32_16x16x32_bf16 a[120:123], a[184:187], v[144:147], a[120:123]\n\t" \
  "v_cvt_pk_bf16_f32 v138, v36, v37\n\t" \
  "ds_read_b64_tr_b16 v[54:55], v235 offset:12288\n\t" \
  "v_mfma_f32_16x16x32_bf16 a[120:123], a[188:191], v[148:151], a[120:123]\n\t" \
  "v_exp_f32 v38, v38\n\t" \
  "v_mfma_f32_16x16x32_bf16 v[168:171], v[216:219], v[144:147], v[168:171]\n\t" \
  "v_exp_f32 v39, v39\n\t" \
  "v_mfma_f32_16x16x32_bf16 v[168:171], v[216:219], v[148:151], v[168:171]\n\t" \
  "v_cvt_pk_bf16_f32 v139, v38, v39\n\t" \
  "ds_read_b64_tr_b16 v[56:57], v235 offset:16384\n\t" \
  "v_mfma_f32_16x16x32_bf16 a[12:15], a[128:131], v[152:155], a[12:15]\n\t" \
  "v_exp_f32 v40, v40\n\t" \
  "v_mfma_f32_16x16x32_bf16 a[12:15], a[132:135], v[156:159], a[12:15]\n\t" \
  "v_exp_f32 v41, v41\n\t" \
  "v_mfma_f32_16x16x32_bf16 a[28:31], a[136:139], v[152:155], a[28:31]\n\t" \
  "v_cvt_pk_bf16_f32 v140, v40, v41\n\t" \
  "ds_read_b64_tr_b16 v[58:59], v235 offset:20480\n\t" \
  "v_mfma_f32_16x16x32_bf16 a[28:31], a[140:143], v[156:159], a[28:31]\n\t" \
  "v_exp_f32 v42, v42\n\t" \
  "v_mfma_f32_16x16x32_bf16 a[44:47], a[144:147], v[152:155], a[44:47]\n\t" \
  "ds_read_b64_tr_b16 v[60:61], v235 offset:24576\n\t" \
  "ds_read_b64_tr_b16 v[62:63], v235 offset:28672\n\t" \
  "v_mfma_f32_16x16x32_bf16 a[44:47], a[148:151], v[156:159], a[44:47]\n\t" \
  "v_exp_f32 v43, v43\n\t" \
  "v_mfma_f32_16x16x32_bf16 a[60:63], a[152:155], v[152:155], a[60:63]\n\t" \
  "v_cvt_pk_bf16_f32 v141, v42, v43\n\t" \
  "v_xor_b32 v236, 96, v234\n\t" \
  "v_mfma_f32_16x16x32_bf16 a[60:63], a[156:159], v[156:159], a[60:63]\n\t" \
  "v_exp_f32 v44, v44\n\t" \
  "v_mfma_f32_16x16x32_bf16 a[76:79], a[160:163], v[152:155], a[76:79]\n\t" \
  "v_exp_f32 v45, v45\n\t" \
  "v_mfma_f32_16x16x32_bf16 a[76:79], a[164:167], v[156:159], a[76:79]\n\t" \
  "v_cvt_pk_bf16_f32 v142, v44, v45\n\t" \
  "ds_read_b64_tr_b16 v[80:81], v236 offset:0\n\t" \
  "v_mfma_f32_16x16x32_bf16 a[92:95], a[168:171], v[152:155], a[92:95]\n\t" \
  "v_exp_f32 v46, v46\n\t" \
  "v_mfma_f32_16x16x32_bf16 a[92:95], a[172:175], v[156:159], a[92:95]\n\t" \
  "v_exp_f32 v47, v47\n\t" \
  "v_mfma_f32_16x16x32_bf16 a[108:111], a[176:179], v[152:155], a[108:111]\n\t" \
  "v_cvt_pk_bf16_f32 v143, v46, v47\n\t" \
  "ds_read_b64_tr_b16 v[82:83], v236 offset:4096\n\t" \
  "v_mfma_f32_16x16x32_bf16 a[108:111], a[180:183], v[156:159], a[108:111]\n\t" \
  "v_exp_f32 v64, v64\n\t" \
  "v_mfma_f32_16x16x32_bf16 a[124:127], a[184:187], v[152:155], a[124:127]\n\t" \
  "v_exp_f32 v65, v65\n\t" \
  "v_mfma_f32_16x16x32_bf16 a[124:127], a[188:191], v[156:159], a[124:127]\n\t" \
  "v_cvt_pk_bf16_f32 v144, v64, v65\n\t" \
  "ds_read_b64_tr_b16 v[84:85], v236 offset:8192\n\t" \
  "v_mfma_f32_16x16x32_bf16 v[172:175], v[216:219], v[152:155], v[172:175]\n\t" \
  "v_exp_f32 v66, v66\n\t" \
  "v_mfma_f32_16x16x32_bf16 v[172:175], v[216:219], v[156:159], v[172:175]\n\t" \
  "ds_read_b64_tr_b16 v[86:87], v236 offset:12288\n\t" \
  "ds_read_b64_tr_b16 v[88:89], v236 offset:16384\n\t" \
  "s_cmp_lg_u32 s99, 0\n\t" \
  "s_cbranch_scc1 .Lsf_p7\n\t" \
  ".Lsf_q7:\n\t" \
  "s_waitcnt lgkmcnt(15)\n\t" \
  "v_mfma_f32_16x16x32_bf16 a[0:3], a[192:195], v[128:131], a[0:3]\n\t" \
  "v_exp_f32 v67, v67\n\t" \
  "v_mfma_f32_16x16x32_bf16 a[0:3], a[196:199], v[132:135], a[0:3]\n\t" \
  "v_cvt_pk_bf16_f32 v145, v66, v67\n\t" \
  "ds_read_b128 v[176:179], v215 offset:16384\n\t" \
  "v_mfma_f32_16x16x32_bf16 v[160:163], v[216:219], v[128:131], v[160:163]\n\t" \
  "v_exp_f32 v68, v68\n\t" \
  "v_mfma_f32_16x16x32_bf16 v[160:163], v[216:219], v[132:135], v[160:163]\n\t" \
  "ds_read_b64_tr_b16 v[90:91], v236 offset:20480\n\t" \
  "ds_read_b128 v[180:183], v215 offset:17408\n\t" \
  "v_mfma_f32_16x16x32_bf16 a[16:19], a[200:203], v[128:131], a[16:19]\n\t" \
  "ds_read_b64_tr_b16 v[92:93], v236 offset:24576\n\t" \
  "ds_read_b128 v[184:187], v215 offset:18432\n\t" \
  "v_mfma_f32_16x16x32_bf16 a[16:19], a[204:207], v[132:135], a[16:19]\n\t" \
  "v_exp_f32 v69, v69\n\t" \
  "v_mfma_f32_16x16x32_bf16 a[32:35], a[208:211], v[128:131], a[32:35]\n\t" \
  "v_cvt_pk_bf16_f32 v146, v68, v69\n\t" \
  "ds_read_b128 v[188:191], v215 offset:19456\n\t" \
  "v_mfma_f32_16x16x32_bf16 a[32:35], a[212:215], v[132:135], a[32:35]\n\t" \
  "s_waitcnt lgkmcnt(0)\n\t" \
  "buffer_load_dwordx4 v240, %[wo], s97 offen offset:0 lds\n\t" \
  "v_mfma_f32_16x16x32_bf16 a[48:51], a[216:219], v[128:131], a[48:51]\n\t" \
  "s_add_u32 s97, s97, 7168\n\t" \
  "buffer_load_dwordx4 v241, %[wo], s97 offen offset:1024 lds\n\t" \
  "v_mfma_f32_16x16x32_bf16 a[48:51], a[220:223], v[132:135], a[48:51]\n\t" \
  "s_add_u32 s97, s97, 7168\n\t" \
  "buffer_load_dwordx4 v240, %[wo], s97 offen offset:2048 lds\n\t" \
  "v_mfma_f32_16x16x32_bf16 a[64:67], a[224:227], v[128:131], a[64:67]\n\t" \
  "s_add_u32 s97, s97, 7168\n\t" \
  "buffer_load_dwordx4 v241, %[wo], s97 offen offset:3072 lds\n\t" \
  "v_mfma_f32_16x16x32_bf16 a[64:67], a[228:231], v[132:135], a[64:67]\n\t" \
  "s_add_u32 m0, %[m], 131072\n\t" \
  "s_xor_b32 s97, %[wob], 917504\n\t" \
  "v_mfma_f32_16x16x32_bf16 a[80:83], a[232:235], v[128:131], a[80:83]\n\t" \
  "ds_read_b64_tr_b16 v[94:95], v236 offset:28672\n\t" \
  "ds_read_b128 v[192:195], v215 offset:32768\n\t" \
  "v_mfma_f32_16x16x32_bf16 a[80:83], a[236:239], v[132:135], a[80:83]\n\t" \
  "v_rcp_f32 v208, v160\n\t" \
  "v_mfma_f32_16x16x32_bf16 a[96:99], a[240:243], v[128:131], a[96:99]\n\t" \
  "v_accvgpr_read_b32 v16, a0\n\t" \
  "v_accvgpr_read_b32 v17, a1\n\t" \
  "v_mfma_f32_16x16x32_bf16 a[96:99], a[244:247], v[132:135], a[96:99]\n\t" \
  "v_accvgpr_read_b32 v18, a2\n\t" \
  "v_accvgpr_read_b32 v19, a3\n\t" \
  "v_mfma_f32_16x16x32_bf16 a[112:115], a[248:251], v[128:131], a[112:115]\n\t" \
  "v_accvgpr_read_b32 v20, a16\n\t" \
  "v_accvgpr_read_b32 v21, a17\n\t" \
  "v_mfma_f32_16x16x32_bf16 a[112:115], a[252:255], v[132:135], a[112:115]\n\t" \
  "v_accvgpr_read_b32 v22, a18\n\t" \
  "v_accvgpr_read_b32 v23, a19\n\t" \
  "s_waitcnt vmcnt(8)\n\t" \
  "s_barrier\n\t" \
  "v_mfma_f32_16x16x32_bf16 a[4:7], a[192:195], v[136:139], a[4:7]\n\t" \
  "v_xor_b32 v235, 128, v234\n\t" \
  "ds_read_b128 v[196:199], v215 offset:33792\n\t" \
  "v_mfma_f32_16x16x32_bf16 a[4:7], a[196:199], v[140:143], a[4:7]\n\t" \
  "v_mul_f32 v16, v16, v208\n\t" \
  "v_mul_f32 v17, v17, v208\n\t" \
  "v_mfma_f32_16x16x32_bf16 v[164:167], v[216:219], v[136:139], v[164:167]\n\t" \
  "v_mul_f32 v18, v18, v208\n\t" \
  "v_mul_f32 v19, v19, v208\n\t" \
  "v_mfma_f32_16x16x32_bf16 v[164:167], v[216:219], v[140:143], v[164:167]\n\t" \
  "v_mul_f32 v20, v20, v208\n\t" \
  "v_mul_f32 v21, v21, v208\n\t" \
  "v_mfma_f32_16x16x32_bf16 a[20:23], a[200:203], v[136:139], a[20:23]\n\t" \
  "v_mul_f32 v22, v22, v208\n\t" \
  "v_mul_f32 v23, v23, v208\n\t" \
  "v_mfma_f32_16x16x32_bf16 a[20:23], a[204:207], v[140:143], a[20:23]\n\t" \
  "ds_read_b128 v[200:203], v215 offset:34816\n\t" \
  "ds_read_b64_tr_b16 v[112:113], v235 offset:0\n\t" \
  "v_mfma_f32_16x16x32_bf16 a[36:39], a[208:211], v[136:139], a[36:39]\n\t" \
  "ds_read_b128 v[204:207], v215 offset:35840\n\t" \
  "s_waitcnt lgkmcnt(0)\n\t" \
  "v_mfma_f32_16x16x32_bf16 a[36:39], a[212:215], v[140:143], a[36:39]\n\t" \
  "buffer_load_dwordx4 v240, %[wo], s97 offen offset:0 lds\n\t" \
  "ds_read_b64_tr_b16 v[114:115], v235 offset:4096\n\t" \
  "v_mfma_f32_16x16x32_bf16 a[52:55], a[216:219], v[136:139], a[52:55]\n\t" \
  "s_add_u32 s97, s97, 7168\n\t" \
  "buffer_load_dwordx4 v241, %[wo], s97 offen offset:1024 lds\n\t" \
  "v_mfma_f32_16x16x32_bf16 a[52:55], a[220:223], v[140:143], a[52:55]\n\t" \
  "s_add_u32 s97, s97, 7168\n\t" \
  "buffer_load_dwordx4 v240, %[wo], s97 offen offset:2048 lds\n\t" \
  "v_mfma_f32_16x16x32_bf16 a[68:71], a[224:227], v[136:139], a[68:71]\n\t" \
  "s_add_u32 s97, s97, 7168\n\t" \
  "buffer_load_dwordx4 v241, %[wo], s97 offen offset:3072 lds\n\t" \
  "v_mfma_f32_16x16x32_bf16 a[68:71], a[228:231], v[140:143], a[68:71]\n\t" \
  "v_cvt_pk_bf16_f32 v0, v16, v17\n\t" \
  "v_cvt_pk_bf16_f32 v1, v18, v19\n\t" \
  "v_mfma_f32_16x16x32_bf16 a[84:87], a[232:235], v[136:139], a[84:87]\n\t" \
  "v_cvt_pk_bf16_f32 v2, v20, v21\n\t" \
  "v_cvt_pk_bf16_f32 v3, v22, v23\n\t" \
  "v_mfma_f32_16x16x32_bf16 a[84:87], a[236:239], v[140:143], a[84:87]\n\t" \
  "s_xor_b32 s98, %[eso], 0\n\t" \
  "buffer_store_dwordx4 v[0:3], v242, %[ws], s98 offen offset:0 sc1\n\t" \
  "v_mfma_f32_16x16x32_bf16 a[100:103], a[240:243], v[136:139], a[100:103]\n\t" \
  "v_accvgpr_read_b32 v24, a32\n\t" \
  "v_accvgpr_read_b32 v25, a33\n\t" \
  "v_mfma_f32_16x16x32_bf16 a[100:103], a[244:247], v[140:143], a[100:103]\n\t" \
  "v_accvgpr_read_b32 v26, a34\n\t" \
  "v_accvgpr_read_b32 v27, a35\n\t" \
  "v_mfma_f32_16x16x32_bf16 a[116:119], a[248:251], v[136:139], a[116:119]\n\t" \
  "v_accvgpr_read_b32 v28, a48\n\t" \
  "v_accvgpr_read_b32 v29, a49\n\t" \
  "v_mfma_f32_16x16x32_bf16 a[116:119], a[252:255], v[140:143], a[116:119]\n\t" \
  "v_accvgpr_read_b32 v30, a50\n\t" \
  "v_accvgpr_read_b32 v31, a51\n\t" \
  "v_exp_f32 v70, v70\n\t" \
  "v_exp_f32 v71, v71\n\t" \
  "s_nop 0\n\t" \
  "v_cvt_pk_bf16_f32 v147, v70, v71\n\t" \
  "s_nop 1\n\t" \
  "v_mfma_f32_16x16x32_bf16 a[8:11], a[192:195], v[144:147], a[8:11]\n\t" \
  "ds_read_b64_tr_b16 v[116:117], v235 offset:8192\n\t" \
  "ds_read_b64_tr_b16 v[118:119], v235 offset:12288\n\t" \
  "v_exp_f32 v72, v72\n\t" \
  "v_exp_f32 v73, v73\n\t" \
  "v_exp_f32 v74, v74\n\t" \
  "v_exp_f32 v75, v75\n\t" \
  "v_exp_f32 v76, v76\n\t" \
  "v_exp_f32 v77, v77\n\t" \
  "v_exp_f32 v78, v78\n\t" \
  "v_exp_f32 v79, v79\n\t" \
  "v_cvt_pk_bf16_f32 v148, v72, v73\n\t" \
  "v_cvt_pk_bf16_f32 v149, v74, v75\n\t" \
  "v_cvt_pk_bf16_f32 v150, v76, v77\n\t" \
  "v_cvt_pk_bf16_f32 v151, v78, v79\n\t" \
  "s_nop 1\n\t" \
  "v_mfma_f32_16x16x32_bf16 a[8:11], a[196:199], v[148:151], a[8:11]\n\t" \
  "v_mul_f32 v24, v24, v208\n\t" \
  "v_mul_f32 v25, v25, v208\n\t" \
  "v_mfma_f32_16x16x32_bf16 v[168:171], v[216:219], v[144:147], v[168:171]\n\t" \
  "v_mul_f32 v26, v26, v208\n\t" \
  "v_mul_f32 v27, v27, v208\n\t" \
  "v_mfma_f32_16x16x32_bf16 v[168:171], v[216:219], v[148:151], v[168:171]\n\t" \
  "v_mul_f32 v28, v28, v208\n\t" \
  "v_mul_f32 v29, v29, v208\n\t" \
  "v_mfma_f32_16x16x32_bf16 a[24:27], a[200:203], v[144:147], a[24:27]\n\t" \
  "v_mul_f32 v30, v30, v208\n\t" \
  "v_mul_f32 v31, v31, v208\n\t" \
  "v_mfma_f32_16x16x32_bf16 a[24:27], a[204:207], v[148:151], a[24:27]\n\t" \
  "ds_read_b64_tr_b16 v[120:121], v235 offset:16384\n\t" \
  "v_cvt_pk_bf16_f32 v4, v24, v25\n\t" \
  "v_mfma_f32_16x16x32_bf16 a[40:43], a[208:211], v[144:147], a[40:43]\n\t" \
  "v_cvt_pk_bf16_f32 v5, v26, v27\n\t" \
  "v_cvt_pk_bf16_f32 v6, v28, v29\n\t" \
  "v_mfma_f32_16x16x32_bf16 a[40:43], a[212:215], v[148:151], a[40:43]\n\t" \
  "v_cvt_pk_bf16_f32 v7, v30, v31\n\t" \
  "ds_read_b64_tr_b16 v[122:123], v235 offset:20480\n\t" \
  "v_mfma_f32_16x16x32_bf16 a[56:59], a[216:219], v[144:147], a[56:59]\n\t" \
  "s_xor_b32 s98, %[eso], 0\n\t" \
  "buffer_store_dwordx4 v[4:7], v242, %[ws], s98 offen offset:1024 sc1\n\t" \
  "v_mfma_f32_16x16x32_bf16 a[56:59], a[220:223], v[148:151], a[56:59]\n\t" \
  "v_accvgpr_read_b32 v16, a64\n\t" \
  "v_accvgpr_read_b32 v17, a65\n\t" \
  "v_mfma_f32_16x16x32_bf16 a[72:75], a[224:227], v[144:147], a[72:75]\n\t" \
  "v_accvgpr_read_b32 v18, a66\n\t" \
  "v_accvgpr_read_b32 v19, a67\n\t" \
  "v_mfma_f32_16x16x32_bf16 a[72:75], a[228:231], v[148:151], a[72:75]\n\t" \
  "v_accvgpr_read_b32 v20, a80\n\t" \
  "v_accvgpr_read_b32 v21, a81\n\t" \
  "v_mfma_f32_16x16x32_bf16 a[88:91], a[232:235], v[144:147], a[88:91]\n\t" \
  "v_accvgpr_read_b32 v22, a82\n\t" \
  "v_accvgpr_read_b32 v23, a83\n\t" \
  "v_mfma_f32_16x16x32_bf16 a[88:91], a[236:239], v[148:151], a[88:91]\n\t" \
  "ds_read_b64_tr_b16 v[124:125], v235 offset:24576\n\t" \
  "v_mul_f32 v16, v16, v208\n\t" \
  "v_mfma_f32_16x16x32_bf16 a[104:107], a[240:243], v[144:147], a[104:107]\n\t" \
  "v_mul_f32 v17, v17, v208\n\t" \
  "v_mul_f32 v18, v18, v208\n\t" \
  "v_mfma_f32_16x16x32_bf16 a[104:107], a[244:247], v[148:151], a[104:107]\n\t" \
  "v_mul_f32 v19, v19, v208\n\t" \
  "v_mul_f32 v20, v20, v208\n\t" \
  "v_mfma_f32_16x16x32_bf16 a[120:123], a[248:251], v[144:147], a[120:123]\n\t" \
  "v_mul_f32 v21, v21, v208\n\t" \
  "v_mul_f32 v22, v22, v208\n\t" \
  "v_mfma_f32_16x16x32_bf16 a[120:123], a[252:255], v[148:151], a[120:123]\n\t" \
  "v_mul_f32 v23, v23, v208\n\t" \
  "ds_read_b64_tr_b16 v[126:127], v235 offset:28672\n\t" \
  "v_exp_f32 v96, v96\n\t" \
  "v_exp_f32 v97, v97\n\t" \
  "v_exp_f32 v98, v98\n\t" \
  "v_exp_f32 v99, v99\n\t" \
  "v_exp_f32 v100, v100\n\t" \
  "v_exp_f32 v101, v101\n\t" \
  "v_exp_f32 v102, v102\n\t" \
  "v_exp_f32 v103, v103\n\t" \
  "v_cvt_pk_bf16_f32 v152, v96, v97\n\t" \
  "v_cvt_pk_bf16_f32 v153, v98, v99\n\t" \
  "v_cvt_pk_bf16_f32 v154, v100, v101\n\t" \
  "v_cvt_pk_bf16_f32 v155, v102, v103\n\t" \
  "s_nop 1\n\t" \
  "v_mfma_f32_16x16x32_bf16 a[12:15], a[192:195], v[152:155], a[12:15]\n\t" \
  "v_cvt_pk_bf16_f32 v8, v16, v17\n\t" \
  "v_cvt_pk_bf16_f32 v9, v18, v19\n\t" \
  "v_exp_f32 v104, v104\n\t" \
  "v_exp_f32 v105, v105\n\t" \
  "v_exp_f32 v106, v106\n\t" \
  "v_exp_f32 v107, v107\n\t" \
  "v_exp_f32 v108, v108\n\t" \
  "v_exp_f32 v109, v109\n\t" \
  "v_exp_f32 v110, v110\n\t" \
  "v_exp_f32 v111, v111\n\t" \
  "v_cvt_pk_bf16_f32 v156, v104, v105\n\t" \
  "v_cvt_pk_bf16_f32 v157, v106, v107\n\t" \
  "v_cvt_pk_bf16_f32 v158, v108, v109\n\t" \
  "v_cvt_pk_bf16_f32 v159, v110, v111\n\t" \
  "s_nop 1\n\t" \
  "v_mfma_f32_16x16x32_bf16 a[12:15], a[196:199], v[156:159], a[12:15]\n\t" \
  "v_cvt_pk_bf16_f32 v10, v20, v21\n\t" \
  "v_cvt_pk_bf16_f32 v11, v22, v23\n\t" \
  "v_mfma_f32_16x16x32_bf16 v[172:175], v[216:219], v[152:155], v[172:175]\n\t" \
  "s_xor_b32 s98, %[eso], 0\n\t" \
  "buffer_store_dwordx4 v[8:11], v242, %[ws], s98 offen offset:2048 sc1\n\t" \
  "v_mfma_f32_16x16x32_bf16 v[172:175], v[216:219], v[156:159], v[172:175]\n\t" \
  "ds_read_b64_tr_b16 a[128:129], v234 offset:0\n\t" \
  "ds_read_b64_tr_b16 a[130:131], v234 offset:4096\n\t" \
  "v_mfma_f32_16x16x32_bf16 a[28:31], a[200:203], v[152:155], a[28:31]\n\t" \
  "v_accvgpr_read_b32 v24, a96\n\t" \
  "v_accvgpr_read_b32 v25, a97\n\t" \
  "v_mfma_f32_16x16x32_bf16 a[28:31], a[204:207], v[156:159], a[28:31]\n\t" \
  "v_accvgpr_read_b32 v26, a98\n\t" \
  "v_accvgpr_read_b32 v27, a99\n\t" \
  "v_mfma_f32_16x16x32_bf16 a[44:47], a[208:211], v[152:155], a[44:47]\n\t" \
  "v_accvgpr_read_b32 v28, a112\n\t" \
  "v_accvgpr_read_b32 v29, a113\n\t" \
  "v_mfma_f32_16x16x32_bf16 a[44:47], a[212:215], v[156:159], a[44:47]\n\t" \
  "v_accvgpr_read_b32 v30, a114\n\t" \
  "v_accvgpr_read_b32 v31, a115\n\t" \
  "v_mfma_f32_16x16x32_bf16 a[60:63], a[216:219], v[152:155], a[60:63]\n\t" \
  "ds_read_b64_tr_b16 a[132:133], v234 offset:8192\n\t" \
  "ds_read_b64_tr_b16 a[134:135], v234 offset:12288\n\t" \
  "v_mfma_f32_16x16x32_bf16 a[60:63], a[220:223], v[156:159], a[60:63]\n\t" \
  "v_mul_f32 v24, v24, v208\n\t" \
  "v_mul_f32 v25, v25, v208\n\t" \
  "v_mfma_f32_16x16x32_bf16 a[76:79], a[224:227], v[152:155], a[76:79]\n\t" \
  "v_mul_f32 v26, v26, v208\n\t" \
  "v_mul_f32 v27, v27, v208\n\t" \
  "v_mfma_f32_16x16x32_bf16 a[76:79], a[228:231], v[156:159], a[76:79]\n\t" \
  "v_mul_f32 v28, v28, v208\n\t" \
  "v_mul_f32 v29, v29, v208\n\t" \
  "v_mfma_f32_16x16x32_bf16 a[92:95], a[232:235], v[152:155], a[92:95]\n\t" \
  "v_mul_f32 v30, v30, v208\n\t" \
  "v_mul_f32 v31, v31, v208\n\t" \
  "v_mfma_f32_16x16x32_bf16 a[92:95], a[236:239], v[156:159], a[92:95]\n\t" \
  "ds_read_b64_tr_b16 a[136:137], v234 offset:16384\n\t" \
  "ds_read_b64_tr_b16 a[138:139], v234 offset:20480\n\t" \
  "v_mfma_f32_16x16x32_bf16 a[108:111], a[240:243], v[152:155], a[108:111]\n\t" \
  "v_cvt_pk_bf16_f32 v12, v24, v25\n\t" \
  "v_cvt_pk_bf16_f32 v13, v26, v27\n\t" \
  "v_mfma_f32_16x16x32_bf16 a[108:111], a[244:247], v[156:159], a[108:111]\n\t" \
  "v_cvt_pk_bf16_f32 v14, v28, v29\n\t" \
  "v_cvt_pk_bf16_f32 v15, v30, v31\n\t" \
  "v_mfma_f32_16x16x32_bf16 a[124:127], a[248:251], v[152:155], a[124:127]\n\t" \
  "s_xor_b32 s98, %[eso], 0\n\t" \
  "buffer_store_dwordx4 v[12:15], v242, %[ws], s98 offen offset:3072 sc1\n\t" \
  "v_mfma_f32_16x16x32_bf16 a[124:127], a[252:255], v[156:159], a[124:127]\n\t" \
  "ds_read_b64_tr_b16 a[140:141], v234 offset:24576\n\t" \
  "ds_read_b64_tr_b16 a[142:143], v234 offset:28672\n\t" \
  "v_mfma_f32_16x16x32_bf16 a[32:35], v[48:51], v[0:3], 0\n\t" \
  "v_rcp_f32 v210, v164\n\t" \
  "v_mfma_f32_16x16x32_bf16 a[32:35], v[52:55], v[4:7], a[32:35]\n\t" \
  "v_accvgpr_read_b32 v16, a4\n\t" \
  "v_accvgpr_read_b32 v17, a5\n\t" \
  "v_mfma_f32_16x16x32_bf16 a[32:35], v[56:59], v[8:11], a[32:35]\n\t" \
  "v_accvgpr_read_b32 v18, a6\n\t" \
  "v_accvgpr_read_b32 v19, a7\n\t" \
  "v_mfma_f32_16x16x32_bf16 a[32:35], v[60:63], v[12:15], a[32:35]\n\t" \
  "v_accvgpr_read_b32 v20, a20\n\t" \
  "v_accvgpr_read_b32 v21, a21\n\t" \
  "v_mfma_f32_16x16x32_bf16 a[48:51], v[92:95], v[12:15], 0\n\t" \
  "v_accvgpr_read_b32 v22, a22\n\t" \
  "v_accvgpr_read_b32 v23, a23\n\t" \
  "v_mfma_f32_16x16x32_bf16 a[48:51], v[88:91], v[8:11], a[48:51]\n\t" \
  "v_xor_b32 v236, 32, v234\n\t" \
  "ds_read_b64_tr_b16 a[144:145], v236 offset:0\n\t" \
  "v_mfma_f32_16x16x32_bf16 a[48:51], v[84:87], v[4:7], a[48:51]\n\t" \
  "v_mul_f32 v16, v16, v210\n\t" \
  "v_mul_f32 v17, v17, v210\n\t" \
  "v_mfma_f32_16x16x32_bf16 a[48:51], v[80:83], v[0:3], a[48:51]\n\t" \
  "v_mul_f32 v18, v18, v210\n\t" \
  "v_mul_f32 v19, v19, v210\n\t" \
  "s_waitcnt lgkmcnt(15)\n\t" \
  "v_mfma_f32_16x16x32_bf16 a[64:67], v[112:115], v[0:3], 0\n\t" \
  "v_mul_f32 v20, v20, v210\n\t" \
  "v_mul_f32 v21, v21, v210\n\t" \
  "s_waitcnt lgkmcnt(13)\n\t" \
  "v_mfma_f32_16x16x32_bf16 a[64:67], v[116:119], v[4:7], a[64:67]\n\t" \
  "v_mul_f32 v22, v22, v210\n\t" \
  "v_mul_f32 v23, v23, v210\n\t" \
  "s_waitcnt lgkmcnt(11)\n\t" \
  "v_mfma_f32_16x16x32_bf16 a[64:67], v[120:123], v[8:11], a[64:67]\n\t" \
  "ds_read_b64_tr_b16 a[146:147], v236 offset:4096\n\t" \
  "ds_read_b64_tr_b16 a[148:149], v236 offset:8192\n\t" \
  "s_waitcnt lgkmcnt(11)\n\t" \
  "v_mfma_f32_16x16x32_bf16 a[64:67], v[124:127], v[12:15], a[64:67]\n\t" \
  "v_cvt_pk_bf16_f32 v32, v16, v17\n\t" \
  "v_cvt_pk_bf16_f32 v33, v18, v19\n\t" \
  "ds_read_b64_tr_b16 a[150:151], v236 offset:12288\n\t" \
  "ds_read_b64_tr_b16 a[152:153], v236 offset:16384\n\t" \
  "ds_read_b64_tr_b16 a[154:155], v236 offset:20480\n\t" \
  "ds_read_b64_tr_b16 a[156:157], v236 offset:24576\n\t" \
  "ds_read_b64_tr_b16 a[158:159], v236 offset:28672\n\t" \
  "s_waitcnt lgkmcnt(8)\n\t" \
  "v_mfma_f32_16x16x32_bf16 a[0:3], a[140:143], v[12:15], 0\n\t" \
  "v_cvt_pk_bf16_f32 v34, v20, v21\n\t" \
  "v_cvt_pk_bf16_f32 v35, v22, v23\n\t" \
  "v_mfma_f32_16x16x32_bf16 a[0:3], a[136:139], v[8:11], a[0:3]\n\t" \
  "s_xor_b32 s98, %[eso], 32768\n\t" \
  "buffer_store_dwordx4 v[32:35], v242, %[ws], s98 offen offset:0 sc1\n\t" \
  "v_mfma_f32_16x16x32_bf16 a[0:3], a[132:135], v[4:7], a[0:3]\n\t" \
  "v_accvgpr_read_b32 v24, a36\n\t" \
  "v_accvgpr_read_b32 v25, a37\n\t" \
  "v_mfma_f32_16x16x32_bf16 a[0:3], a[128:131], v[0:3], a[0:3]\n\t" \
  "v_accvgpr_read_b32 v26, a38\n\t" \
  "v_accvgpr_read_b32 v27, a39\n\t" \
  "v_xor_b32 v235, 192, v234\n\t" \
  "ds_read_b64_tr_b16 a[160:161], v235 offset:0\n\t" \
  "ds_read_b64_tr_b16 a[162:163], v235 offset:4096\n\t" \
  "ds_read_b64_tr_b16 a[164:165], v235 offset:8192\n\t" \
  "ds_read_b64_tr_b16 a[166:167], v235 offset:12288\n\t" \
  "ds_read_b64_tr_b16 a[168:169], v235 offset:16384\n\t" \
  "ds_read_b64_tr_b16 a[170:171], v235 offset:20480\n\t" \
  "ds_read_b64_tr_b16 a[172:173], v235 offset:24576\n\t" \
  "ds_read_b64_tr_b16 a[174:175], v235 offset:28672\n\t" \
  "s_waitcnt lgkmcnt(14)\n\t" \
  "v_mfma_f32_16x16x32_bf16 a[16:19], a[144:147], v[0:3], 0\n\t" \
  "v_accvgpr_read_b32 v28, a52\n\t" \
  "v_accvgpr_read_b32 v29, a53\n\t" \
  "s_waitcnt lgkmcnt(12)\n\t" \
  "v_mfma_f32_16x16x32_bf16 a[16:19], a[148:151], v[4:7], a[16:19]\n\t" \
  "v_accvgpr_read_b32 v30, a54\n\t" \
  "v_accvgpr_read_b32 v31, a55\n\t" \
  "s_waitcnt lgkmcnt(10)\n\t" \
  "v_mfma_f32_16x16x32_bf16 a[16:19], a[152:155], v[8:11], a[16:19]\n\t" \
  "v_mul_f32 v24, v24, v210\n\t" \
  "v_mul_f32 v25, v25, v210\n\t" \
  "s_waitcnt lgkmcnt(8)\n\t" \
  "v_mfma_f32_16x16x32_bf16 a[16:19], a[156:159], v[12:15], a[16:19]\n\t" \
  "v_mul_f32 v26, v26, v210\n\t" \
  "v_mul_f32 v27, v27, v210\n\t" \
  "v_xor_b32 v236, 224, v234\n\t" \
  "ds_read_b64_tr_b16 a[176:177], v236 offset:0\n\t" \
  "ds_read_b64_tr_b16 a[178:179], v236 offset:4096\n\t" \
  "ds_read_b64_tr_b16 a[180:181], v236 offset:8192\n\t" \
  "ds_read_b64_tr_b16 a[182:183], v236 offset:12288\n\t" \
  "ds_read_b64_tr_b16 a[184:185], v236 offset:16384\n\t" \
  "ds_read_b64_tr_b16 a[186:187], v236 offset:20480\n\t" \
  "ds_read_b64_tr_b16 a[188:189], v236 offset:24576\n\t" \
  "ds_read_b64_tr_b16 a[190:191], v236 offset:28672\n\t" \
  "s_waitcnt lgkmcnt(8)\n\t" \
  "v_mfma_f32_16x16x32_bf16 a[96:99], a[172:175], v[12:15], 0\n\t" \
  "v_mul_f32 v28, v28, v210\n\t" \
  "v_mul_f32 v29, v29, v210\n\t" \
  "v_mfma_f32_16x16x32_bf16 a[96:99], a[168:171], v[8:11], a[96:99]\n\t" \
  "v_mul_f32 v30, v30, v210\n\t" \
  "v_mul_f32 v31, v31, v210\n\t" \
  "v_mfma_f32_16x16x32_bf16 a[96:99], a[164:167], v[4:7], a[96:99]\n\t" \
  "v_cvt_pk_bf16_f32 v36, v24, v25\n\t" \
  "v_cvt_pk_bf16_f32 v37, v26, v27\n\t" \
  "v_mfma_f32_16x16x32_bf16 a[96:99], a[160:163], v[0:3], a[96:99]\n\t" \
  "v_cvt_pk_bf16_f32 v38, v28, v29\n\t" \
  "v_cvt_pk_bf16_f32 v39, v30, v31\n\t" \
  "v_xor_b32 v236, 160, v234\n\t" \
  "ds_read_b64_tr_b16 v[96:97], v236 offset:0\n\t" \
  "ds_read_b64_tr_b16 v[98:99], v236 offset:4096\n\t" \
  "ds_read_b64_tr_b16 v[100:101], v236 offset:8192\n\t" \
  "ds_read_b64_tr_b16 v[102:103], v236 offset:12288\n\t" \
  "ds_read_b64_tr_b16 v[104:105], v236 offset:16384\n\t" \
  "ds_read_b64_tr_b16 v[106:107], v236 offset:20480\n\t" \
  "ds_read_b64_tr_b16 v[108:109], v236 offset:24576\n\t" \
  "ds_read_b64_tr_b16 v[110:111], v236 offset:28672\n\t" \
  "s_waitcnt lgkmcnt(14)\n\t" \
  "v_mfma_f32_16x16x32_bf16 a[112:115], a[176:179], v[0:3], 0\n\t" \
  "s_xor_b32 s98, %[eso], 32768\n\t" \
  "buffer_store_dwordx4 v[36:39], v242, %[ws], s98 offen offset:1024 sc1\n\t" \
  "s_waitcnt lgkmcnt(12)\n\t" \
  "v_mfma_f32_16x16x32_bf16 a[112:115], a[180:183], v[4:7], a[112:115]\n\t" \
  "v_accvgpr_read_b32 v16, a68\n\t" \
  "v_accvgpr_read_b32 v17, a69\n\t" \
  "s_waitcnt lgkmcnt(10)\n\t" \
  "v_mfma_f32_16x16x32_bf16 a[112:115], a[184:187], v[8:11], a[112:115]\n\t" \
  "v_accvgpr_read_b32 v18, a70\n\t" \
  "v_accvgpr_read_b32 v19, a71\n\t" \
  "s_waitcnt lgkmcnt(8)\n\t" \
  "v_mfma_f32_16x16x32_bf16 a[112:115], a[188:191], v[12:15], a[112:115]\n\t" \
  "v_accvgpr_read_b32 v20, a84\n\t" \
  "v_accvgpr_read_b32 v21, a85\n\t" \
  "s_waitcnt lgkmcnt(0)\n\t" \
  "v_mfma_f32_16x16x32_bf16 a[80:83], v[108:111], v[12:15], 0\n\t" \
  "v_accvgpr_read_b32 v22, a86\n\t" \
  "v_accvgpr_read_b32 v23, a87\n\t" \
  "v_mfma_f32_16x16x32_bf16 a[80:83], v[104:107], v[8:11], a[80:83]\n\t" \
  "v_mul_f32 v16, v16, v210\n\t" \
  "v_mul_f32 v17, v17, v210\n\t" \
  "v_mfma_f32_16x16x32_bf16 a[80:83], v[100:103], v[4:7], a[80:83]\n\t" \
  "v_mul_f32 v18, v18, v210\n\t" \
  "v_mul_f32 v19, v19, v210\n\t" \
  "v_mfma_f32_16x16x32_bf16 a[80:83], v[96:99], v[0:3], a[80:83]\n\t" \
  "v_mul_f32 v20, v20, v210\n\t" \
  "v_mul_f32 v21, v21, v210\n\t" \
  "v_mul_f32 v22, v22, v210\n\t" \
  "v_mul_f32 v23, v23, v210\n\t" \
  "v_cvt_pk_bf16_f32 v40, v16, v17\n\t" \
  "v_cvt_pk_bf16_f32 v41, v18, v19\n\t" \
  "v_cvt_pk_bf16_f32 v42, v20, v21\n\t" \
  "v_cvt_pk_bf16_f32 v43, v22, v23\n\t" \
  "v_accvgpr_read_b32 v24, a100\n\t" \
  "v_accvgpr_read_b32 v25, a101\n\t" \
  "v_accvgpr_read_b32 v26, a102\n\t" \
  "v_accvgpr_read_b32 v27, a103\n\t" \
  "v_accvgpr_read_b32 v28, a116\n\t" \
  "v_accvgpr_read_b32 v29, a117\n\t" \
  "v_accvgpr_read_b32 v30, a118\n\t" \
  "v_accvgpr_read_b32 v31, a119\n\t" \
  "v_mul_f32 v24, v24, v210\n\t" \
  "v_mul_f32 v25, v25, v210\n\t" \
  "v_mul_f32 v26, v26, v210\n\t" \
  "v_mul_f32 v27, v27, v210\n\t" \
  "v_mul_f32 v28, v28, v210\n\t" \
  "v_mul_f32 v29, v29, v210\n\t" \
  "v_mul_f32 v30, v30, v210\n\t" \
  "v_mul_f32 v31, v31, v210\n\t" \
  "v_cvt_pk_bf16_f32 v44, v24, v25\n\t" \
  "v_cvt_pk_bf16_f32 v45, v26, v27\n\t" \
  "v_cvt_pk_bf16_f32 v46, v28, v29\n\t" \
  "v_cvt_pk_bf16_f32 v47, v30, v31\n\t" \
  "v_mfma_f32_16x16x32_bf16 a[36:39], v[48:51], v[32:35], 0\n\t" \
  "s_xor_b32 s98, %[eso], 32768\n\t" \
  "buffer_store_dwordx4 v[40:43], v242, %[ws], s98 offen offset:2048 sc1\n\t" \
  "v_mfma_f32_16x16x32_bf16 a[36:39], v[52:55], v[36:39], a[36:39]\n\t" \
  "s_xor_b32 s98, %[eso], 32768\n\t" \
  "buffer_store_dwordx4 v[44:47], v242, %[ws], s98 offen offset:3072 sc1\n\t" \
  "v_mfma_f32_16x16x32_bf16 a[36:39], v[56:59], v[40:43], a[36:39]\n\t" \
  "v_rcp_f32 v212, v168\n\t" \
  "v_mfma_f32_16x16x32_bf16 a[36:39], v[60:63], v[44:47], a[36:39]\n\t" \
  "v_accvgpr_read_b32 v16, a8\n\t" \
  "v_accvgpr_read_b32 v17, a9\n\t" \
  "s_waitcnt vmcnt(0)\n\t" \
  "buffer_store_dword v243, v254, %[ws], %[fw2] offen offset:0 sc1\n\t" \
  "v_mfma_f32_16x16x32_bf16 a[52:55], v[92:95], v[44:47], 0\n\t" \
  "v_accvgpr_read_b32 v18, a10\n\t" \
  "v_accvgpr_read_b32 v19, a11\n\t" \
  "v_mfma_f32_16x16x32_bf16 a[52:55], v[88:91], v[40:43], a[52:55]\n\t" \
  "v_accvgpr_read_b32 v20, a24\n\t" \
  "v_accvgpr_read_b32 v21, a25\n\t" \
  "v_mfma_f32_16x16x32_bf16 a[52:55], v[84:87], v[36:39], a[52:55]\n\t" \
  "v_accvgpr_read_b32 v22, a26\n\t" \
  "v_accvgpr_read_b32 v23, a27\n\t" \
  "v_mfma_f32_16x16x32_bf16 a[52:55], v[80:83], v[32:35], a[52:55]\n\t" \
  "v_mul_f32 v16, v16, v212\n\t" \
  "v_mul_f32 v17, v17, v212\n\t" \
  "ds_read_b128 a[224:227], v213 offset:32768\n\t" \
  "ds_read_b128 a[228:231], v213 offset:33792\n\t" \
  "ds_read_b128 a[232:235], v213 offset:34816\n\t" \
  "ds_read_b128 a[236:239], v213 offset:35840\n\t" \
  "ds_read_b128 a[240:243], v215 offset:0\n\t" \
  "ds_read_b128 a[244:247], v215 offset:1024\n\t" \
  "ds_read_b128 a[248:251], v215 offset:2048\n\t" \
  "ds_read_b128 a[252:255], v215 offset:3072\n\t" \
  "v_mfma_f32_16x16x32_bf16 a[68:71], v[112:115], v[32:35], 0\n\t" \
  "v_mul_f32 v18, v18, v212\n\t" \
  "v_mul_f32 v19, v19, v212\n\t" \
  "v_mfma_f32_16x16x32_bf16 a[68:71], v[116:119], v[36:39], a[68:71]\n\t" \
  "v_mul_f32 v20, v20, v212\n\t" \
  "v_mul_f32 v21, v21, v212\n\t" \
  "v_mfma_f32_16x16x32_bf16 a[68:71], v[120:123], v[40:43], a[68:71]\n\t" \
  "v_mul_f32 v22, v22, v212\n\t" \
  "v_mul_f32 v23, v23, v212\n\t" \
  "v_mfma_f32_16x16x32_bf16 a[68:71], v[124:127], v[44:47], a[68:71]\n\t" \
  "v_cvt_pk_bf16_f32 v64, v16, v17\n\t" \
  "v_cvt_pk_bf16_f32 v65, v18, v19\n\t" \
  "v_mfma_f32_16x16x32_bf16 a[4:7], a[140:143], v[44:47], 0\n\t" \
  "v_cvt_pk_bf16_f32 v66, v20, v21\n\t" \
  "v_cvt_pk_bf16_f32 v67, v22, v23\n\t" \
  "v_mfma_f32_16x16x32_bf16 a[4:7], a[136:139], v[40:43], a[4:7]\n\t" \
  "s_xor_b32 s98, %[eso], 65536\n\t" \
  "buffer_store_dwordx4 v[64:67], v242, %[ws], s98 offen offset:0 sc1\n\t" \
  "v_mfma_f32_16x16x32_bf16 a[4:7], a[132:135], v[36:39], a[4:7]\n\t" \
  "v_accvgpr_read_b32 v24, a40\n\t" \
  "v_accvgpr_read_b32 v25, a41\n\t" \
  "v_mfma_f32_16x16x32_bf16 a[4:7], a[128:131], v[32:35], a[4:7]\n\t" \
  "v_accvgpr_read_b32 v26, a42\n\t" \
  "v_accvgpr_read_b32 v27, a43\n\t" \
  "v_mfma_f32_16x16x32_bf16 a[20:23], a[144:147], v[32:35], 0\n\t" \
  "v_accvgpr_read_b32 v28, a56\n\t" \
  "v_accvgpr_read_b32 v29, a57\n\t" \
  "v_mfma_f32_16x16x32_bf16 a[20:23], a[148:151], v[36:39], a[20:23]\n\t" \
  "v_accvgpr_read_b32 v30, a58\n\t" \
  "v_accvgpr_read_b32 v31, a59\n\t" \
  "v_mfma_f32_16x16x32_bf16 a[20:23], a[152:155], v[40:43], a[20:23]\n\t" \
  "v_mul_f32 v24, v24, v212\n\t" \
  "v_mul_f32 v25, v25, v212\n\t" \
  "v_mfma_f32_16x16x32_bf16 a[20:23], a[156:159], v[44:47], a[20:23]\n\t" \
  "v_mul_f32 v26, v26, v212\n\t" \
  "v_mul_f32 v27, v27, v212\n\t" \
  "v_mfma_f32_16x16x32_bf16 a[100:103], a[172:175], v[44:47], 0\n\t" \
  "v_mul_f32 v28, v28, v212\n\t" \
  "v_mul_f32 v29, v29, v212\n\t" \
  "v_mfma_f32_16x16x32_bf16 a[100:103], a[168:171], v[40:43], a[100:103]\n\t" \
  "v_mul_f32 v30, v30, v212\n\t" \
  "v_mul_f32 v31, v31, v212\n\t" \
  "v_mfma_f32_16x16x32_bf16 a[100:103], a[164:167], v[36:39], a[100:103]\n\t" \
  "v_cvt_pk_bf16_f32 v68, v24, v25\n\t" \
  "v_cvt_pk_bf16_f32 v69, v26, v27\n\t" \
  "v_mfma_f32_16x16x32_bf16 a[100:103], a[160:163], v[32:35], a[100:103]\n\t" \
  "v_cvt_pk_bf16_f32 v70, v28, v29\n\t" \
  "v_cvt_pk_bf16_f32 v71, v30, v31\n\t" \
  "v_mfma_f32_16x16x32_bf16 a[116:119], a[176:179], v[32:35], 0\n\t" \
  "s_xor_b32 s98, %[eso], 65536\n\t" \
  "buffer_store_dwordx4 v[68:71], v242, %[ws], s98 offen offset:1024 sc1\n\t" \
  "v_mfma_f32_16x16x32_bf16 a[116:119], a[180:183], v[36:39], a[116:119]\n\t" \
  "v_accvgpr_read_b32 v16, a72\n\t" \
  "v_accvgpr_read_b32 v17, a73\n\t" \
  "v_mfma_f32_16x16x32_bf16 a[116:119], a[184:187], v[40:43], a[116:119]\n\t" \
  "v_accvgpr_read_b32 v18, a74\n\t" \
  "v_accvgpr_read_b32 v19, a75\n\t" \
  "v_mfma_f32_16x16x32_bf16 a[116:119], a[188:191], v[44:47], a[116:119]\n\t" \
  "v_accvgpr_read_b32 v20, a88\n\t" \
  "v_accvgpr_read_b32 v21, a89\n\t" \
  "v_mfma_f32_16x16x32_bf16 a[84:87], v[108:111], v[44:47], 0\n\t" \
  "v_accvgpr_read_b32 v22, a90\n\t" \
  "v_accvgpr_read_b32 v23, a91\n\t" \
  "v_mfma_f32_16x16x32_bf16 a[84:87], v[104:107], v[40:43], a[84:87]\n\t" \
  "v_mul_f32 v16, v16, v212\n\t" \
  "v_mul_f32 v17, v17, v212\n\t" \
  "v_mfma_f32_16x16x32_bf16 a[84:87], v[100:103], v[36:39], a[84:87]\n\t" \
  "v_mul_f32 v18, v18, v212\n\t" \
  "v_mul_f32 v19, v19, v212\n\t" \
  "v_mfma_f32_16x16x32_bf16 a[84:87], v[96:99], v[32:35], a[84:87]\n\t" \
  "v_mul_f32 v20, v20, v212\n\t" \
  "v_mul_f32 v21, v21, v212\n\t" \
  "v_mul_f32 v22, v22, v212\n\t" \
  "v_mul_f32 v23, v23, v212\n\t" \
  "v_cvt_pk_bf16_f32 v72, v16, v17\n\t" \
  "v_cvt_pk_bf16_f32 v73, v18, v19\n\t" \
  "v_cvt_pk_bf16_f32 v74, v20, v21\n\t" \
  "v_cvt_pk_bf16_f32 v75, v22, v23\n\t" \
  "v_accvgpr_read_b32 v24, a104\n\t" \
  "v_accvgpr_read_b32 v25, a105\n\t" \
  "v_accvgpr_read_b32 v26, a106\n\t" \
  "v_accvgpr_read_b32 v27, a107\n\t" \
  "v_accvgpr_read_b32 v28, a120\n\t" \
  "v_accvgpr_read_b32 v29, a121\n\t" \
  "v_accvgpr_read_b32 v30, a122\n\t" \
  "v_accvgpr_read_b32 v31, a123\n\t" \
  "v_mul_f32 v24, v24, v212\n\t" \
  "v_mul_f32 v25, v25, v212\n\t" \
  "v_mul_f32 v26, v26, v212\n\t" \
  "v_mul_f32 v27, v27, v212\n\t" \
  "v_mul_f32 v28, v28, v212\n\t" \
  "v_mul_f32 v29, v29, v212\n\t" \
  "v_mul_f32 v30, v30, v212\n\t" \
  "v_mul_f32 v31, v31, v212\n\t" \
  "v_cvt_pk_bf16_f32 v76, v24, v25\n\t" \
  "v_cvt_pk_bf16_f32 v77, v26, v27\n\t" \
  "v_cvt_pk_bf16_f32 v78, v28, v29\n\t" \
  "v_cvt_pk_bf16_f32 v79, v30, v31\n\t" \
  "v_mfma_f32_16x16x32_bf16 a[40:43], v[48:51], v[64:67], 0\n\t" \
  "s_xor_b32 s98, %[eso], 65536\n\t" \
  "buffer_store_dwordx4 v[72:75], v242, %[ws], s98 offen offset:2048 sc1\n\t" \
  "v_mfma_f32_16x16x32_bf16 a[40:43], v[52:55], v[68:71], a[40:43]\n\t" \
  "s_xor_b32 s98, %[eso], 65536\n\t" \
  "buffer_store_dwordx4 v[76:79], v242, %[ws], s98 offen offset:3072 sc1\n\t" \
  "v_mfma_f32_16x16x32_bf16 a[40:43], v[56:59], v[72:75], a[40:43]\n\t" \
  "buffer_load_dwordx4 v[224:227], v252, %[ws], %[ss] offen sc1\n\t" \
  "buffer_load_dwordx4 v[128:131], v247, %[sM], %[gvo] offen\n\t" \
  "v_mfma_f32_16x16x32_bf16 a[40:43], v[60:63], v[76:79], a[40:43]\n\t" \
  "v_rcp_f32 v214, v172\n\t" \
  "v_mfma_f32_16x16x32_bf16 a[56:59], v[92:95], v[76:79], 0\n\t" \
  "v_accvgpr_read_b32 v16, a12\n\t" \
  "v_accvgpr_read_b32 v17, a13\n\t" \
  "v_mfma_f32_16x16x32_bf16 a[56:59], v[88:91], v[72:75], a[56:59]\n\t" \
  "v_accvgpr_read_b32 v18, a14\n\t" \
  "v_accvgpr_read_b32 v19, a15\n\t" \
  "v_mfma_f32_16x16x32_bf16 a[56:59], v[84:87], v[68:71], a[56:59]\n\t" \
  "v_accvgpr_read_b32 v20, a28\n\t" \
  "v_accvgpr_read_b32 v21, a29\n\t" \
  "v_mfma_f32_16x16x32_bf16 a[56:59], v[80:83], v[64:67], a[56:59]\n\t" \
  "v_accvgpr_read_b32 v22, a30\n\t" \
  "v_accvgpr_read_b32 v23, a31\n\t" \
  "v_mfma_f32_16x16x32_bf16 a[72:75], v[112:115], v[64:67], 0\n\t" \
  "v_mul_f32 v16, v16, v214\n\t" \
  "v_mul_f32 v17, v17, v214\n\t" \
  "v_mul_f32 v18, v18, v214\n\t" \
  "v_mul_f32 v19, v19, v214\n\t" \
  "v_mul_f32 v20, v20, v214\n\t" \
  "v_mul_f32 v21, v21, v214\n\t" \
  "v_mul_f32 v22, v22, v214\n\t" \
  "v_mul_f32 v23, v23, v214\n\t" \
  "v_cvt_pk_bf16_f32 v160, v16, v17\n\t" \
  "v_cvt_pk_bf16_f32 v161, v18, v19\n\t" \
  "v_cvt_pk_bf16_f32 v162, v20, v21\n\t" \
  "v_cvt_pk_bf16_f32 v163, v22, v23\n\t" \
  "s_waitcnt lgkmcnt(4)\n\t" \
  "ds_write_b128 v213, v[160:163] offset:32768\n\t" \
  "v_accvgpr_read_b32 v24, a44\n\t" \
  "v_accvgpr_read_b32 v25, a45\n\t" \
  "v_accvgpr_read_b32 v26, a46\n\t" \
  "v_accvgpr_read_b32 v27, a47\n\t" \
  "v_accvgpr_read_b32 v28, a60\n\t" \
  "v_accvgpr_read_b32 v29, a61\n\t" \
  "v_accvgpr_read_b32 v30, a62\n\t" \
  "v_accvgpr_read_b32 v31, a63\n\t" \
  "v_mul_f32 v24, v24, v214\n\t" \
  "v_mul_f32 v25, v25, v214\n\t" \
  "v_mul_f32 v26, v26, v214\n\t" \
  "v_mul_f32 v27, v27, v214\n\t" \
  "v_mul_f32 v28, v28, v214\n\t" \
  "v_mul_f32 v29, v29, v214\n\t" \
  "v_mul_f32 v30, v30, v214\n\t" \
  "v_mul_f32 v31, v31, v214\n\t" \
  "v_cvt_pk_bf16_f32 v164, v24, v25\n\t" \
  "v_cvt_pk_bf16_f32 v165, v26, v27\n\t" \
  "v_cvt_pk_bf16_f32 v166, v28, v29\n\t" \
  "v_cvt_pk_bf16_f32 v167, v30, v31\n\t" \
  "ds_write_b128 v213, v[164:167] offset:33792\n\t" \
  "v_accvgpr_read_b32 v16, a76\n\t" \
  "v_accvgpr_read_b32 v17, a77\n\t" \
  "v_accvgpr_read_b32 v18, a78\n\t" \
  "v_accvgpr_read_b32 v19, a79\n\t" \
  "v_accvgpr_read_b32 v20, a92\n\t" \
  "v_accvgpr_read_b32 v21, a93\n\t" \
  "v_accvgpr_read_b32 v22, a94\n\t" \
  "v_accvgpr_read_b32 v23, a95\n\t" \
  "v_mul_f32 v16, v16, v214\n\t" \
  "v_mul_f32 v17, v17, v214\n\t" \
  "v_mul_f32 v18, v18, v214\n\t" \
  "v_mul_f32 v19, v19, v214\n\t" \
  "v_mul_f32 v20, v20, v214\n\t" \
  "v_mul_f32 v21, v21, v214\n\t" \
  "v_mul_f32 v22, v22, v214\n\t" \
  "v_mul_f32 v23, v23, v214\n\t" \
  "v_cvt_pk_bf16_f32 v168, v16, v17\n\t" \
  "v_cvt_pk_bf16_f32 v169, v18, v19\n\t" \
  "v_cvt_pk_bf16_f32 v170, v20, v21\n\t" \
  "v_cvt_pk_bf16_f32 v171, v22, v23\n\t" \
  "ds_write_b128 v213, v[168:171] offset:34816\n\t" \
  "v_accvgpr_read_b32 v24, a108\n\t" \
  "v_accvgpr_read_b32 v25, a109\n\t" \
  "v_accvgpr_read_b32 v26, a110\n\t" \
  "v_accvgpr_read_b32 v27, a111\n\t" \
  "v_accvgpr_read_b32 v28, a124\n\t" \
  "v_accvgpr_read_b32 v29, a125\n\t" \
  "v_accvgpr_read_b32 v30, a126\n\t" \
  "v_accvgpr_read_b32 v31, a127\n\t" \
  "v_mul_f32 v24, v24, v214\n\t" \
  "v_mul_f32 v25, v25, v214\n\t" \
  "v_mul_f32 v26, v26, v214\n\t" \
  "v_mul_f32 v27, v27, v214\n\t" \
  "v_mul_f32 v28, v28, v214\n\t" \
  "v_mul_f32 v29, v29, v214\n\t" \
  "v_mul_f32 v30, v30, v214\n\t" \
  "v_mul_f32 v31, v31, v214\n\t" \
  "v_cvt_pk_bf16_f32 v172, v24, v25\n\t" \
  "v_cvt_pk_bf16_f32 v173, v26, v27\n\t" \
  "v_cvt_pk_bf16_f32 v174, v28, v29\n\t" \
  "v_cvt_pk_bf16_f32 v175, v30, v31\n\t" \
  "ds_write_b128 v213, v[172:175] offset:35840\n\t" \
  "v_mfma_f32_16x16x32_bf16 a[72:75], v[116:119], v[68:71], a[72:75]\n\t" \
  "s_add_u32 s98, %[gvo], 12288\n\t" \
  "buffer_load_dwordx4 v[132:135], v247, %[sM], s98 offen\n\t" \
  "v_mfma_f32_16x16x32_bf16 a[72:75], v[120:123], v[72:75], a[72:75]\n\t" \
  "s_add_u32 s98, %[gvo], 24576\n\t" \
  "buffer_load_dwordx4 v[136:139], v247, %[sM], s98 offen\n\t" \
  "v_mfma_f32_16x16x32_bf16 a[72:75], v[124:127], v[76:79], a[72:75]\n\t" \
  "s_add_u32 s98, %[gvo], 36864\n\t" \
  "buffer_load_dwordx4 v[140:143], v247, %[sM], s98 offen\n\t" \
  "s_add_u32 m0, %[m], 147456\n\t" \
  "s_xor_b32 s97, %[wob], 655360\n\t" \
  "buffer_load_dwordx4 v240, %[wo], s97 offen offset:0 lds\n\t" \
  "s_add_u32 s97, s97, 7168\n\t" \
  "buffer_load_dwordx4 v241, %[wo], s97 offen offset:1024 lds\n\t" \
  "s_add_u32 s97, s97, 7168\n\t" \
  "buffer_load_dwordx4 v240, %[wo], s97 offen offset:2048 lds\n\t" \
  "s_add_u32 s97, s97, 7168\n\t" \
  "buffer_load_dwordx4 v241, %[wo], s97 offen offset:3072 lds\n\t" \
  "s_add_u32 s98, %[gvo], 49152\n\t" \
  "buffer_load_dwordx4 v[144:147], v247, %[sM], s98 offen\n\t" \
  "s_add_u32 s98, %[gvo], 61440\n\t" \
  "buffer_load_dwordx4 v[148:151], v247, %[sM], s98 offen\n\t" \
  "s_add_u32 s98, %[gvo], 73728\n\t" \
  "buffer_load_dwordx4 v[152:155], v247, %[sM], s98 offen\n\t" \
  "s_add_u32 s98, %[gvo], 86016\n\t" \
  "buffer_load_dwordx4 v[156:159], v247, %[sM], s98 offen\n\t" \
  "s_waitcnt vmcnt(0)\n\t" \
  "s_branch .Lat_end\n\t" \
  ".Lsf_c00:\n\t" \
  "s_mov_b32 vcc_lo, 0\n\t" \
  "s_branch .Lsf_s0\n\t" \
  ".Lsf_c10:\n\t" \
  "s_mov_b32 vcc_lo, 1\n\t" \
  "s_branch .Lsf_s0\n\t" \
  ".Lsf_c20:\n\t" \
  "s_mov_b32 vcc_lo, 2\n\t" \
  "s_branch .Lsf_s0\n\t" \
  ".Lsf_c30:\n\t" \
  "s_mov_b32 vcc_lo, 3\n\t" \
  "s_branch .Lsf_s0\n\t" \
  ".Lsf_c40:\n\t" \
  "s_mov_b32 vcc_lo, 4\n\t" \
  "s_branch .Lsf_s0\n\t" \
  ".Lsf_c50:\n\t" \
  "s_mov_b32 vcc_lo, 5\n\t" \
  "s_branch .Lsf_s0\n\t" \
  ".Lsf_c60:\n\t" \
  "s_mov_b32 vcc_lo, 6\n\t" \
  "s_branch .Lsf_s0\n\t" \
  ".Lsf_c70:\n\t" \
  "s_mov_b32 vcc_lo, 7\n\t" \
  "s_branch .Lsf_s0\n\t" \
  ".Lsf_s0:\n\t" \
  "s_nop 7\n\t" \
  "s_nop 1\n\t" \
  "v_mov_b32 v193, v192\n\t" \
  "s_nop 1\n\t" \
  "v_permlane32_swap_b32 v192, v193\n\t" \
  "s_nop 1\n\t" \
  "v_max_f32 v192, v192, v193\n\t" \
  "s_nop 1\n\t" \
  "v_mov_b32 v193, v192\n\t" \
  "s_nop 1\n\t" \
  "v_permlane16_swap_b32 v192, v193\n\t" \
  "s_nop 1\n\t" \
  "v_max_f32 v192, v192, v193\n\t" \
  "v_max_f32 v192, 0, v192\n\t" \
  "v_sub_f32 v0, v0, v192\n\t" \
  "v_sub_f32 v1, v1, v192\n\t" \
  "v_sub_f32 v2, v2, v192\n\t" \
  "v_sub_f32 v3, v3, v192\n\t" \
  "v_sub_f32 v4, v4, v192\n\t" \
  "v_sub_f32 v5, v5, v192\n\t" \
  "v_sub_f32 v6, v6, v192\n\t" \
  "v_sub_f32 v7, v7, v192\n\t" \
  "v_sub_f32 v8, v8, v192\n\t" \
  "v_sub_f32 v9, v9, v192\n\t" \
  "v_sub_f32 v10, v10, v192\n\t" \
  "v_sub_f32 v11, v11, v192\n\t" \
  "v_sub_f32 v12, v12, v192\n\t" \
  "v_sub_f32 v13, v13, v192\n\t" \
  "v_sub_f32 v14, v14, v192\n\t" \
  "v_sub_f32 v15, v15, v192\n\t" \
  "v_sub_f32 v176, v176, v192\n\t" \
  "v_sub_f32 v177, v177, v192\n\t" \
  "v_sub_f32 v178, v178, v192\n\t" \
  "v_sub_f32 v179, v179, v192\n\t" \
  "v_exp_f32 v196, -v192\n\t" \
  "s_mov_b32 s99, 1\n\t" \
  "s_nop 1\n\t" \
  "s_cselect_b32 vcc_hi, 1, 0\n\t" \
  "s_cmp_eq_u32 vcc_lo, 0\n\t" \
  "s_cbranch_scc1 .Lsf_b00\n\t" \
  "s_cmp_eq_u32 vcc_lo, 1\n\t" \
  "s_cbranch_scc1 .Lsf_b10\n\t" \
  "s_cmp_eq_u32 vcc_lo, 2\n\t" \
  "s_cbranch_scc1 .Lsf_b20\n\t" \
  "s_cmp_eq_u32 vcc_lo, 3\n\t" \
  "s_cbranch_scc1 .Lsf_b30\n\t" \
  "s_cmp_eq_u32 vcc_lo, 4\n\t" \
  "s_cbranch_scc1 .Lsf_b40\n\t" \
  "s_cmp_eq_u32 vcc_lo, 5\n\t" \
  "s_cbranch_scc1 .Lsf_b50\n\t" \
  "s_cmp_eq_u32 vcc_lo, 6\n\t" \
  "s_cbranch_scc1 .Lsf_b60\n\t" \
  "s_cmp_eq_u32 vcc_lo, 7\n\t" \
  "s_cbranch_scc1 .Lsf_b70\n\t" \
  ".Lsf_b00:\n\t" \
  "s_cmp_lg_u32 vcc_hi, 0\n\t" \
  "s_branch .Lsf_r00\n\t" \
  ".Lsf_b10:\n\t" \
  "s_cmp_lg_u32 vcc_hi, 0\n\t" \
  "s_branch .Lsf_r10\n\t" \
  ".Lsf_b20:\n\t" \
  "s_cmp_lg_u32 vcc_hi, 0\n\t" \
  "s_branch .Lsf_r20\n\t" \
  ".Lsf_b30:\n\t" \
  "s_cmp_lg_u32 vcc_hi, 0\n\t" \
  "s_branch .Lsf_r30\n\t" \
  ".Lsf_b40:\n\t" \
  "s_cmp_lg_u32 vcc_hi, 0\n\t" \
  "s_branch .Lsf_r40\n\t" \
  ".Lsf_b50:\n\t" \
  "s_cmp_lg_u32 vcc_hi, 0\n\t" \
  "s_branch .Lsf_r50\n\t" \
  ".Lsf_b60:\n\t" \
  "s_cmp_lg_u32 vcc_hi, 0\n\t" \
  "s_branch .Lsf_r60\n\t" \
  ".Lsf_b70:\n\t" \
  "s_cmp_lg_u32 vcc_hi, 0\n\t" \
  "s_branch .Lsf_r70\n\t" \
  ".Lsf_c01:\n\t" \
  "s_mov_b32 vcc_lo, 0\n\t" \
  "s_branch .Lsf_s1\n\t" \
  ".Lsf_c11:\n\t" \
  "s_mov_b32 vcc_lo, 1\n\t" \
  "s_branch .Lsf_s1\n\t" \
  ".Lsf_c21:\n\t" \
  "s_mov_b32 vcc_lo, 2\n\t" \
  "s_branch .Lsf_s1\n\t" \
  ".Lsf_c31:\n\t" \
  "s_mov_b32 vcc_lo, 3\n\t" \
  "s_branch .Lsf_s1\n\t" \
  ".Lsf_c41:\n\t" \
  "s_mov_b32 vcc_lo, 4\n\t" \
  "s_branch .Lsf_s1\n\t" \
  ".Lsf_c51:\n\t" \
  "s_mov_b32 vcc_lo, 5\n\t" \
  "s_branch .Lsf_s1\n\t" \
  ".Lsf_c61:\n\t" \
  "s_mov_b32 vcc_lo, 6\n\t" \
  "s_branch .Lsf_s1\n\t" \
  ".Lsf_c71:\n\t" \
  "s_mov_b32 vcc_lo, 7\n\t" \
  "s_branch .Lsf_s1\n\t" \
  ".Lsf_s1:\n\t" \
  "s_nop 7\n\t" \
  "s_nop 1\n\t" \
  "v_mov_b32 v193, v192\n\t" \
  "s_nop 1\n\t" \
  "v_permlane32_swap_b32 v192, v193\n\t" \
  "s_nop 1\n\t" \
  "v_max_f32 v192, v192, v193\n\t" \
  "s_nop 1\n\t" \
  "v_mov_b32 v193, v192\n\t" \
  "s_nop 1\n\t" \
  "v_permlane16_swap_b32 v192, v193\n\t" \
  "s_nop 1\n\t" \
  "v_max_f32 v192, v192, v193\n\t" \
  "v_max_f32 v192, 0, v192\n\t" \
  "v_sub_f32 v32, v32, v192\n\t" \
  "v_sub_f32 v33, v33, v192\n\t" \
  "v_sub_f32 v34, v34, v192\n\t" \
  "v_sub_f32 v35, v35, v192\n\t" \
  "v_sub_f32 v36, v36, v192\n\t" \
  "v_sub_f32 v37, v37, v192\n\t" \
  "v_sub_f32 v38, v38, v192\n\t" \
  "v_sub_f32 v39, v39, v192\n\t" \
  "v_sub_f32 v40, v40, v192\n\t" \
  "v_sub_f32 v41, v41, v192\n\t" \
  "v_sub_f32 v42, v42, v192\n\t" \
  "v_sub_f32 v43, v43, v192\n\t" \
  "v_sub_f32 v44, v44, v192\n\t" \
  "v_sub_f32 v45, v45, v192\n\t" \
  "v_sub_f32 v46, v46, v192\n\t" \
  "v_sub_f32 v47, v47, v192\n\t" \
  "v_sub_f32 v180, v180, v192\n\t" \
  "v_sub_f32 v181, v181, v192\n\t" \
  "v_sub_f32 v182, v182, v192\n\t" \
  "v_sub_f32 v183, v183, v192\n\t" \
  "v_exp_f32 v197, -v192\n\t" \
  "s_mov_b32 s99, 1\n\t" \
  "s_nop 1\n\t" \
  "s_cselect_b32 vcc_hi, 1, 0\n\t" \
  "s_cmp_eq_u32 vcc_lo, 0\n\t" \
  "s_cbranch_scc1 .Lsf_b01\n\t" \
  "s_cmp_eq_u32 vcc_lo, 1\n\t" \
  "s_cbranch_scc1 .Lsf_b11\n\t" \
  "s_cmp_eq_u32 vcc_lo, 2\n\t" \
  "s_cbranch_scc1 .Lsf_b21\n\t" \
  "s_cmp_eq_u32 vcc_lo, 3\n\t" \
  "s_cbranch_scc1 .Lsf_b31\n\t" \
  "s_cmp_eq_u32 vcc_lo, 4\n\t" \
  "s_cbranch_scc1 .Lsf_b41\n\t" \
  "s_cmp_eq_u32 vcc_lo, 5\n\t" \
  "s_cbranch_scc1 .Lsf_b51\n\t" \
  "s_cmp_eq_u32 vcc_lo, 6\n\t" \
  "s_cbranch_scc1 .Lsf_b61\n\t" \
  "s_cmp_eq_u32 vcc_lo, 7\n\t" \
  "s_cbranch_scc1 .Lsf_b71\n\t" \
  ".Lsf_b01:\n\t" \
  "s_cmp_lg_u32 vcc_hi, 0\n\t" \
  "s_branch .Lsf_r01\n\t" \
  ".Lsf_b11:\n\t" \
  "s_cmp_lg_u32 vcc_hi, 0\n\t" \
  "s_branch .Lsf_r11\n\t" \
  ".Lsf_b21:\n\t" \
  "s_cmp_lg_u32 vcc_hi, 0\n\t" \
  "s_branch .Lsf_r21\n\t" \
  ".Lsf_b31:\n\t" \
  "s_cmp_lg_u32 vcc_hi, 0\n\t" \
  "s_branch .Lsf_r31\n\t" \
  ".Lsf_b41:\n\t" \
  "s_cmp_lg_u32 vcc_hi, 0\n\t" \
  "s_branch .Lsf_r41\n\t" \
  ".Lsf_b51:\n\t" \
  "s_cmp_lg_u32 vcc_hi, 0\n\t" \
  "s_branch .Lsf_r51\n\t" \
  ".Lsf_b61:\n\t" \
  "s_cmp_lg_u32 vcc_hi, 0\n\t" \
  "s_branch .Lsf_r61\n\t" \
  ".Lsf_b71:\n\t" \
  "s_cmp_lg_u32 vcc_hi, 0\n\t" \
  "s_branch .Lsf_r71\n\t" \
  ".Lsf_c02:\n\t" \
  "s_mov_b32 vcc_lo, 0\n\t" \
  "s_branch .Lsf_s2\n\t" \
  ".Lsf_c12:\n\t" \
  "s_mov_b32 vcc_lo, 1\n\t" \
  "s_branch .Lsf_s2\n\t" \
  ".Lsf_c22:\n\t" \
  "s_mov_b32 vcc_lo, 2\n\t" \
  "s_branch .Lsf_s2\n\t" \
  ".Lsf_c32:\n\t" \
  "s_mov_b32 vcc_lo, 3\n\t" \
  "s_branch .Lsf_s2\n\t" \
  ".Lsf_c42:\n\t" \
  "s_mov_b32 vcc_lo, 4\n\t" \
  "s_branch .Lsf_s2\n\t" \
  ".Lsf_c52:\n\t" \
  "s_mov_b32 vcc_lo, 5\n\t" \
  "s_branch .Lsf_s2\n\t" \
  ".Lsf_c62:\n\t" \
  "s_mov_b32 vcc_lo, 6\n\t" \
  "s_branch .Lsf_s2\n\t" \
  ".Lsf_c72:\n\t" \
  "s_mov_b32 vcc_lo, 7\n\t" \
  "s_branch .Lsf_s2\n\t" \
  ".Lsf_s2:\n\t" \
  "s_nop 7\n\t" \
  "s_nop 1\n\t" \
  "v_mov_b32 v193, v192\n\t" \
  "s_nop 1\n\t" \
  "v_permlane32_swap_b32 v192, v193\n\t" \
  "s_nop 1\n\t" \
  "v_max_f32 v192, v192, v193\n\t" \
  "s_nop 1\n\t" \
  "v_mov_b32 v193, v192\n\t" \
  "s_nop 1\n\t" \
  "v_permlane16_swap_b32 v192, v193\n\t" \
  "s_nop 1\n\t" \
  "v_max_f32 v192, v192, v193\n\t" \
  "v_max_f32 v192, 0, v192\n\t" \
  "v_sub_f32 v64, v64, v192\n\t" \
  "v_sub_f32 v65, v65, v192\n\t" \
  "v_sub_f32 v66, v66, v192\n\t" \
  "v_sub_f32 v67, v67, v192\n\t" \
  "v_sub_f32 v68, v68, v192\n\t" \
  "v_sub_f32 v69, v69, v192\n\t" \
  "v_sub_f32 v70, v70, v192\n\t" \
  "v_sub_f32 v71, v71, v192\n\t" \
  "v_sub_f32 v72, v72, v192\n\t" \
  "v_sub_f32 v73, v73, v192\n\t" \
  "v_sub_f32 v74, v74, v192\n\t" \
  "v_sub_f32 v75, v75, v192\n\t" \
  "v_sub_f32 v76, v76, v192\n\t" \
  "v_sub_f32 v77, v77, v192\n\t" \
  "v_sub_f32 v78, v78, v192\n\t" \
  "v_sub_f32 v79, v79, v192\n\t" \
  "v_sub_f32 v184, v184, v192\n\t" \
  "v_sub_f32 v185, v185, v192\n\t" \
  "v_sub_f32 v186, v186, v192\n\t" \
  "v_sub_f32 v187, v187, v192\n\t" \
  "v_exp_f32 v198, -v192\n\t" \
  "s_mov_b32 s99, 1\n\t" \
  "s_nop 1\n\t" \
  "s_cselect_b32 vcc_hi, 1, 0\n\t" \
  "s_cmp_eq_u32 vcc_lo, 0\n\t" \
  "s_cbranch_scc1 .Lsf_b02\n\t" \
  "s_cmp_eq_u32 vcc_lo, 1\n\t" \
  "s_cbranch_scc1 .Lsf_b12\n\t" \
  "s_cmp_eq_u32 vcc_lo, 2\n\t" \
  "s_cbranch_scc1 .Lsf_b22\n\t" \
  "s_cmp_eq_u32 vcc_lo, 3\n\t" \
  "s_cbranch_scc1 .Lsf_b32\n\t" \
  "s_cmp_eq_u32 vcc_lo, 4\n\t" \
  "s_cbranch_scc1 .Lsf_b42\n\t" \
  "s_cmp_eq_u32 vcc_lo, 5\n\t" \
  "s_cbranch_scc1 .Lsf_b52\n\t" \
  "s_cmp_eq_u32 vcc_lo, 6\n\t" \
  "s_cbranch_scc1 .Lsf_b62\n\t" \
  "s_cmp_eq_u32 vcc_lo, 7\n\t" \
  "s_cbranch_scc1 .Lsf_b72\n\t" \
  ".Lsf_b02:\n\t" \
  "s_cmp_lg_u32 vcc_hi, 0\n\t" \
  "s_branch .Lsf_r02\n\t" \
  ".Lsf_b12:\n\t" \
  "s_cmp_lg_u32 vcc_hi, 0\n\t" \
  "s_branch .Lsf_r12\n\t" \
  ".Lsf_b22:\n\t" \
  "s_cmp_lg_u32 vcc_hi, 0\n\t" \
  "s_branch .Lsf_r22\n\t" \
  ".Lsf_b32:\n\t" \
  "s_cmp_lg_u32 vcc_hi, 0\n\t" \
  "s_branch .Lsf_r32\n\t" \
  ".Lsf_b42:\n\t" \
  "s_cmp_lg_u32 vcc_hi, 0\n\t" \
  "s_branch .Lsf_r42\n\t" \
  ".Lsf_b52:\n\t" \
  "s_cmp_lg_u32 vcc_hi, 0\n\t" \
  "s_branch .Lsf_r52\n\t" \
  ".Lsf_b62:\n\t" \
  "s_cmp_lg_u32 vcc_hi, 0\n\t" \
  "s_branch .Lsf_r62\n\t" \
  ".Lsf_b72:\n\t" \
  "s_cmp_lg_u32 vcc_hi, 0\n\t" \
  "s_branch .Lsf_r72\n\t" \
  ".Lsf_c03:\n\t" \
  "s_mov_b32 vcc_lo, 0\n\t" \
  "s_branch .Lsf_s3\n\t" \
  ".Lsf_c13:\n\t" \
  "s_mov_b32 vcc_lo, 1\n\t" \
  "s_branch .Lsf_s3\n\t" \
  ".Lsf_c23:\n\t" \
  "s_mov_b32 vcc_lo, 2\n\t" \
  "s_branch .Lsf_s3\n\t" \
  ".Lsf_c33:\n\t" \
  "s_mov_b32 vcc_lo, 3\n\t" \
  "s_branch .Lsf_s3\n\t" \
  ".Lsf_c43:\n\t" \
  "s_mov_b32 vcc_lo, 4\n\t" \
  "s_branch .Lsf_s3\n\t" \
  ".Lsf_c53:\n\t" \
  "s_mov_b32 vcc_lo, 5\n\t" \
  "s_branch .Lsf_s3\n\t" \
  ".Lsf_c63:\n\t" \
  "s_mov_b32 vcc_lo, 6\n\t" \
  "s_branch .Lsf_s3\n\t" \
  ".Lsf_c73:\n\t" \
  "s_mov_b32 vcc_lo, 7\n\t" \
  "s_branch .Lsf_s3\n\t" \
  ".Lsf_s3:\n\t" \
  "s_nop 7\n\t" \
  "s_nop 1\n\t" \
  "v_mov_b32 v193, v192\n\t" \
  "s_nop 1\n\t" \
  "v_permlane32_swap_b32 v192, v193\n\t" \
  "s_nop 1\n\t" \
  "v_max_f32 v192, v192, v193\n\t" \
  "s_nop 1\n\t" \
  "v_mov_b32 v193, v192\n\t" \
  "s_nop 1\n\t" \
  "v_permlane16_swap_b32 v192, v193\n\t" \
  "s_nop 1\n\t" \
  "v_max_f32 v192, v192, v193\n\t" \
  "v_max_f32 v192, 0, v192\n\t" \
  "v_sub_f32 v96, v96, v192\n\t" \
  "v_sub_f32 v97, v97, v192\n\t" \
  "v_sub_f32 v98, v98, v192\n\t" \
  "v_sub_f32 v99, v99, v192\n\t" \
  "v_sub_f32 v100, v100, v192\n\t" \
  "v_sub_f32 v101, v101, v192\n\t" \
  "v_sub_f32 v102, v102, v192\n\t" \
  "v_sub_f32 v103, v103, v192\n\t" \
  "v_sub_f32 v104, v104, v192\n\t" \
  "v_sub_f32 v105, v105, v192\n\t" \
  "v_sub_f32 v106, v106, v192\n\t" \
  "v_sub_f32 v107, v107, v192\n\t" \
  "v_sub_f32 v108, v108, v192\n\t" \
  "v_sub_f32 v109, v109, v192\n\t" \
  "v_sub_f32 v110, v110, v192\n\t" \
  "v_sub_f32 v111, v111, v192\n\t" \
  "v_sub_f32 v188, v188, v192\n\t" \
  "v_sub_f32 v189, v189, v192\n\t" \
  "v_sub_f32 v190, v190, v192\n\t" \
  "v_sub_f32 v191, v191, v192\n\t" \
  "v_exp_f32 v199, -v192\n\t" \
  "s_mov_b32 s99, 1\n\t" \
  "s_nop 1\n\t" \
  "s_cselect_b32 vcc_hi, 1, 0\n\t" \
  "s_cmp_eq_u32 vcc_lo, 0\n\t" \
  "s_cbranch_scc1 .Lsf_b03\n\t" \
  "s_cmp_eq_u32 vcc_lo, 1\n\t" \
  "s_cbranch_scc1 .Lsf_b13\n\t" \
  "s_cmp_eq_u32 vcc_lo, 2\n\t" \
  "s_cbranch_scc1 .Lsf_b23\n\t" \
  "s_cmp_eq_u32 vcc_lo, 3\n\t" \
  "s_cbranch_scc1 .Lsf_b33\n\t" \
  "s_cmp_eq_u32 vcc_lo, 4\n\t" \
  "s_cbranch_scc1 .Lsf_b43\n\t" \
  "s_cmp_eq_u32 vcc_lo, 5\n\t" \
  "s_cbranch_scc1 .Lsf_b53\n\t" \
  "s_cmp_eq_u32 vcc_lo, 6\n\t" \
  "s_cbranch_scc1 .Lsf_b63\n\t" \
  "s_cmp_eq_u32 vcc_lo, 7\n\t" \
  "s_cbranch_scc1 .Lsf_b73\n\t" \
  ".Lsf_b03:\n\t" \
  "s_cmp_lg_u32 vcc_hi, 0\n\t" \
  "s_branch .Lsf_r03\n\t" \
  ".Lsf_b13:\n\t" \
  "s_cmp_lg_u32 vcc_hi, 0\n\t" \
  "s_branch .Lsf_r13\n\t" \
  ".Lsf_b23:\n\t" \
  "s_cmp_lg_u32 vcc_hi, 0\n\t" \
  "s_branch .Lsf_r23\n\t" \
  ".Lsf_b33:\n\t" \
  "s_cmp_lg_u32 vcc_hi, 0\n\t" \
  "s_branch .Lsf_r33\n\t" \
  ".Lsf_b43:\n\t" \
  "s_cmp_lg_u32 vcc_hi, 0\n\t" \
  "s_branch .Lsf_r43\n\t" \
  ".Lsf_b53:\n\t" \
  "s_cmp_lg_u32 vcc_hi, 0\n\t" \
  "s_branch .Lsf_r53\n\t" \
  ".Lsf_b63:\n\t" \
  "s_cmp_lg_u32 vcc_hi, 0\n\t" \
  "s_branch .Lsf_r63\n\t" \
  ".Lsf_b73:\n\t" \
  "s_cmp_lg_u32 vcc_hi, 0\n\t" \
  "s_branch .Lsf_r73\n\t" \
  ".Lsf_p0:\n\t" \
  "s_mov_b32 vcc_lo, 0\n\t" \
  "s_branch .Lsf_rs\n\t" \
  ".Lsf_p1:\n\t" \
  "s_mov_b32 vcc_lo, 1\n\t" \
  "s_branch .Lsf_rs\n\t" \
  ".Lsf_p2:\n\t" \
  "s_mov_b32 vcc_lo, 2\n\t" \
  "s_branch .Lsf_rs\n\t" \
  ".Lsf_p3:\n\t" \
  "s_mov_b32 vcc_lo, 3\n\t" \
  "s_branch .Lsf_rs\n\t" \
  ".Lsf_p4:\n\t" \
  "s_mov_b32 vcc_lo, 4\n\t" \
  "s_branch .Lsf_rs\n\t" \
  ".Lsf_p5:\n\t" \
  "s_mov_b32 vcc_lo, 5\n\t" \
  "s_branch .Lsf_rs\n\t" \
  ".Lsf_p6:\n\t" \
  "s_mov_b32 vcc_lo, 6\n\t" \
  "s_branch .Lsf_rs\n\t" \
  ".Lsf_p7:\n\t" \
  "s_mov_b32 vcc_lo, 7\n\t" \
  "s_branch .Lsf_rs\n\t" \
  ".Lsf_rs:\n\t" \
  "s_nop 7\n\t" \
  "s_nop 7\n\t" \
  "v_accvgpr_read_b32 v193, a0\n\t" \
  "v_mul_f32 v193, v196, v193\n\t" \
  "v_accvgpr_write_b32 a0, v193\n\t" \
  "v_accvgpr_read_b32 v193, a1\n\t" \
  "v_mul_f32 v193, v196, v193\n\t" \
  "v_accvgpr_write_b32 a1, v193\n\t" \
  "v_accvgpr_read_b32 v193, a2\n\t" \
  "v_mul_f32 v193, v196, v193\n\t" \
  "v_accvgpr_write_b32 a2, v193\n\t" \
  "v_accvgpr_read_b32 v193, a3\n\t" \
  "v_mul_f32 v193, v196, v193\n\t" \
  "v_accvgpr_write_b32 a3, v193\n\t" \
  "v_accvgpr_read_b32 v193, a4\n\t" \
  "v_mul_f32 v193, v197, v193\n\t" \
  "v_accvgpr_write_b32 a4, v193\n\t" \
  "v_accvgpr_read_b32 v193, a5\n\t" \
  "v_mul_f32 v193, v197, v193\n\t" \
  "v_accvgpr_write_b32 a5, v193\n\t" \
  "v_accvgpr_read_b32 v193, a6\n\t" \
  "v_mul_f32 v193, v197, v193\n\t" \
  "v_accvgpr_write_b32 a6, v193\n\t" \
  "v_accvgpr_read_b32 v193, a7\n\t" \
  "v_mul_f32 v193, v197, v193\n\t" \
  "v_accvgpr_write_b32 a7, v193\n\t" \
  "v_accvgpr_read_b32 v193, a8\n\t" \
  "v_mul_f32 v193, v198, v193\n\t" \
  "v_accvgpr_write_b32 a8, v193\n\t" \
  "v_accvgpr_read_b32 v193, a9\n\t" \
  "v_mul_f32 v193, v198, v193\n\t" \
  "v_accvgpr_write_b32 a9, v193\n\t" \
  "v_accvgpr_read_b32 v193, a10\n\t" \
  "v_mul_f32 v193, v198, v193\n\t" \
  "v_accvgpr_write_b32 a10, v193\n\t" \
  "v_accvgpr_read_b32 v193, a11\n\t" \
  "v_mul_f32 v193, v198, v193\n\t" \
  "v_accvgpr_write_b32 a11, v193\n\t" \
  "v_accvgpr_read_b32 v193, a12\n\t" \
  "v_mul_f32 v193, v199, v193\n\t" \
  "v_accvgpr_write_b32 a12, v193\n\t" \
  "v_accvgpr_read_b32 v193, a13\n\t" \
  "v_mul_f32 v193, v199, v193\n\t" \
  "v_accvgpr_write_b32 a13, v193\n\t" \
  "v_accvgpr_read_b32 v193, a14\n\t" \
  "v_mul_f32 v193, v199, v193\n\t" \
  "v_accvgpr_write_b32 a14, v193\n\t" \
  "v_accvgpr_read_b32 v193, a15\n\t" \
  "v_mul_f32 v193, v199, v193\n\t" \
  "v_accvgpr_write_b32 a15, v193\n\t" \
  "v_accvgpr_read_b32 v193, a16\n\t" \
  "v_mul_f32 v193, v196, v193\n\t" \
  "v_accvgpr_write_b32 a16, v193\n\t" \
  "v_accvgpr_read_b32 v193, a17\n\t" \
  "v_mul_f32 v193, v196, v193\n\t" \
  "v_accvgpr_write_b32 a17, v193\n\t" \
  "v_accvgpr_read_b32 v193, a18\n\t" \
  "v_mul_f32 v193, v196, v193\n\t" \
  "v_accvgpr_write_b32 a18, v193\n\t" \
  "v_accvgpr_read_b32 v193, a19\n\t" \
  "v_mul_f32 v193, v196, v193\n\t" \
  "v_accvgpr_write_b32 a19, v193\n\t" \
  "v_accvgpr_read_b32 v193, a20\n\t" \
  "v_mul_f32 v193, v197, v193\n\t" \
  "v_accvgpr_write_b32 a20, v193\n\t" \
  "v_accvgpr_read_b32 v193, a21\n\t" \
  "v_mul_f32 v193, v197, v193\n\t" \
  "v_accvgpr_write_b32 a21, v193\n\t" \
  "v_accvgpr_read_b32 v193, a22\n\t" \
  "v_mul_f32 v193, v197, v193\n\t" \
  "v_accvgpr_write_b32 a22, v193\n\t" \
  "v_accvgpr_read_b32 v193, a23\n\t" \
  "v_mul_f32 v193, v197, v193\n\t" \
  "v_accvgpr_write_b32 a23, v193\n\t" \
  "v_accvgpr_read_b32 v193, a24\n\t" \
  "v_mul_f32 v193, v198, v193\n\t" \
  "v_accvgpr_write_b32 a24, v193\n\t" \
  "v_accvgpr_read_b32 v193, a25\n\t" \
  "v_mul_f32 v193, v198, v193\n\t" \
  "v_accvgpr_write_b32 a25, v193\n\t" \
  "v_accvgpr_read_b32 v193, a26\n\t" \
  "v_mul_f32 v193, v198, v193\n\t" \
  "v_accvgpr_write_b32 a26, v193\n\t" \
  "v_accvgpr_read_b32 v193, a27\n\t" \
  "v_mul_f32 v193, v198, v193\n\t" \
  "v_accvgpr_write_b32 a27, v193\n\t" \
  "v_accvgpr_read_b32 v193, a28\n\t" \
  "v_mul_f32 v193, v199, v193\n\t" \
  "v_accvgpr_write_b32 a28, v193\n\t" \
  "v_accvgpr_read_b32 v193, a29\n\t" \
  "v_mul_f32 v193, v199, v193\n\t" \
  "v_accvgpr_write_b32 a29, v193\n\t" \
  "v_accvgpr_read_b32 v193, a30\n\t" \
  "v_mul_f32 v193, v199, v193\n\t" \
  "v_accvgpr_write_b32 a30, v193\n\t" \
  "v_accvgpr_read_b32 v193, a31\n\t" \
  "v_mul_f32 v193, v199, v193\n\t" \
  "v_accvgpr_write_b32 a31, v193\n\t" \
  "v_accvgpr_read_b32 v193, a32\n\t" \
  "v_mul_f32 v193, v196, v193\n\t" \
  "v_accvgpr_write_b32 a32, v193\n\t" \
  "v_accvgpr_read_b32 v193, a33\n\t" \
  "v_mul_f32 v193, v196, v193\n\t" \
  "v_accvgpr_write_b32 a33, v193\n\t" \
  "v_accvgpr_read_b32 v193, a34\n\t" \
  "v_mul_f32 v193, v196, v193\n\t" \
  "v_accvgpr_write_b32 a34, v193\n\t" \
  "v_accvgpr_read_b32 v193, a35\n\t" \
  "v_mul_f32 v193, v196, v193\n\t" \
  "v_accvgpr_write_b32 a35, v193\n\t" \
  "v_accvgpr_read_b32 v193, a36\n\t" \
  "v_mul_f32 v193, v197, v193\n\t" \
  "v_accvgpr_write_b32 a36, v193\n\t" \
  "v_accvgpr_read_b32 v193, a37\n\t" \
  "v_mul_f32 v193, v197, v193\n\t" \
  "v_accvgpr_write_b32 a37, v193\n\t" \
  "v_accvgpr_read_b32 v193, a38\n\t" \
  "v_mul_f32 v193, v197, v193\n\t" \
  "v_accvgpr_write_b32 a38, v193\n\t" \
  "v_accvgpr_read_b32 v193, a39\n\t" \
  "v_mul_f32 v193, v197, v193\n\t" \
  "v_accvgpr_write_b32 a39, v193\n\t" \
  "v_accvgpr_read_b32 v193, a40\n\t" \
  "v_mul_f32 v193, v198, v193\n\t" \
  "v_accvgpr_write_b32 a40, v193\n\t" \
  "v_accvgpr_read_b32 v193, a41\n\t" \
  "v_mul_f32 v193, v198, v193\n\t" \
  "v_accvgpr_write_b32 a41, v193\n\t" \
  "v_accvgpr_read_b32 v193, a42\n\t" \
  "v_mul_f32 v193, v198, v193\n\t" \
  "v_accvgpr_write_b32 a42, v193\n\t" \
  "v_accvgpr_read_b32 v193, a43\n\t" \
  "v_mul_f32 v193, v198, v193\n\t" \
  "v_accvgpr_write_b32 a43, v193\n\t" \
  "v_accvgpr_read_b32 v193, a44\n\t" \
  "v_mul_f32 v193, v199, v193\n\t" \
  "v_accvgpr_write_b32 a44, v193\n\t" \
  "v_accvgpr_read_b32 v193, a45\n\t" \
  "v_mul_f32 v193, v199, v193\n\t" \
  "v_accvgpr_write_b32 a45, v193\n\t" \
  "v_accvgpr_read_b32 v193, a46\n\t" \
  "v_mul_f32 v193, v199, v193\n\t" \
  "v_accvgpr_write_b32 a46, v193\n\t" \
  "v_accvgpr_read_b32 v193, a47\n\t" \
  "v_mul_f32 v193, v199, v193\n\t" \
  "v_accvgpr_write_b32 a47, v193\n\t" \
  "v_accvgpr_read_b32 v193, a48\n\t" \
  "v_mul_f32 v193, v196, v193\n\t" \
  "v_accvgpr_write_b32 a48, v193\n\t" \
  "v_accvgpr_read_b32 v193, a49\n\t" \
  "v_mul_f32 v193, v196, v193\n\t" \
  "v_accvgpr_write_b32 a49, v193\n\t" \
  "v_accvgpr_read_b32 v193, a50\n\t" \
  "v_mul_f32 v193, v196, v193\n\t" \
  "v_accvgpr_write_b32 a50, v193\n\t" \
  "v_accvgpr_read_b32 v193, a51\n\t" \
  "v_mul_f32 v193, v196, v193\n\t" \
  "v_accvgpr_write_b32 a51, v193\n\t" \
  "v_accvgpr_read_b32 v193, a52\n\t" \
  "v_mul_f32 v193, v197, v193\n\t" \
  "v_accvgpr_write_b32 a52, v193\n\t" \
  "v_accvgpr_read_b32 v193, a53\n\t" \
  "v_mul_f32 v193, v197, v193\n\t" \
  "v_accvgpr_write_b32 a53, v193\n\t" \
  "v_accvgpr_read_b32 v193, a54\n\t" \
  "v_mul_f32 v193, v197, v193\n\t" \
  "v_accvgpr_write_b32 a54, v193\n\t" \
  "v_accvgpr_read_b32 v193, a55\n\t" \
  "v_mul_f32 v193, v197, v193\n\t" \
  "v_accvgpr_write_b32 a55, v193\n\t" \
  "v_accvgpr_read_b32 v193, a56\n\t" \
  "v_mul_f32 v193, v198, v193\n\t" \
  "v_accvgpr_write_b32 a56, v193\n\t" \
  "v_accvgpr_read_b32 v193, a57\n\t" \
  "v_mul_f32 v193, v198, v193\n\t" \
  "v_accvgpr_write_b32 a57, v193\n\t" \
  "v_accvgpr_read_b32 v193, a58\n\t" \
  "v_mul_f32 v193, v198, v193\n\t" \
  "v_accvgpr_write_b32 a58, v193\n\t" \
  "v_accvgpr_read_b32 v193, a59\n\t" \
  "v_mul_f32 v193, v198, v193\n\t" \
  "v_accvgpr_write_b32 a59, v193\n\t" \
  "v_accvgpr_read_b32 v193, a60\n\t" \
  "v_mul_f32 v193, v199, v193\n\t" \
  "v_accvgpr_write_b32 a60, v193\n\t" \
  "v_accvgpr_read_b32 v193, a61\n\t" \
  "v_mul_f32 v193, v199, v193\n\t" \
  "v_accvgpr_write_b32 a61, v193\n\t" \
  "v_accvgpr_read_b32 v193, a62\n\t" \
  "v_mul_f32 v193, v199, v193\n\t" \
  "v_accvgpr_write_b32 a62, v193\n\t" \
  "v_accvgpr_read_b32 v193, a63\n\t" \
  "v_mul_f32 v193, v199, v193\n\t" \
  "v_accvgpr_write_b32 a63, v193\n\t" \
  "v_accvgpr_read_b32 v193, a64\n\t" \
  "v_mul_f32 v193, v196, v193\n\t" \
  "v_accvgpr_write_b32 a64, v193\n\t" \
  "v_accvgpr_read_b32 v193, a65\n\t" \
  "v_mul_f32 v193, v196, v193\n\t" \
  "v_accvgpr_write_b32 a65, v193\n\t" \
  "v_accvgpr_read_b32 v193, a66\n\t" \
  "v_mul_f32 v193, v196, v193\n\t" \
  "v_accvgpr_write_b32 a66, v193\n\t" \
  "v_accvgpr_read_b32 v193, a67\n\t" \
  "v_mul_f32 v193, v196, v193\n\t" \
  "v_accvgpr_write_b32 a67, v193\n\t" \
  "v_accvgpr_read_b32 v193, a68\n\t" \
  "v_mul_f32 v193, v197, v193\n\t" \
  "v_accvgpr_write_b32 a68, v193\n\t" \
  "v_accvgpr_read_b32 v193, a69\n\t" \
  "v_mul_f32 v193, v197, v193\n\t" \
  "v_accvgpr_write_b32 a69, v193\n\t" \
  "v_accvgpr_read_b32 v193, a70\n\t" \
  "v_mul_f32 v193, v197, v193\n\t" \
  "v_accvgpr_write_b32 a70, v193\n\t" \
  "v_accvgpr_read_b32 v193, a71\n\t" \
  "v_mul_f32 v193, v197, v193\n\t" \
  "v_accvgpr_write_b32 a71, v193\n\t" \
  "v_accvgpr_read_b32 v193, a72\n\t" \
  "v_mul_f32 v193, v198, v193\n\t" \
  "v_accvgpr_write_b32 a72, v193\n\t" \
  "v_accvgpr_read_b32 v193, a73\n\t" \
  "v_mul_f32 v193, v198, v193\n\t" \
  "v_accvgpr_write_b32 a73, v193\n\t" \
  "v_accvgpr_read_b32 v193, a74\n\t" \
  "v_mul_f32 v193, v198, v193\n\t" \
  "v_accvgpr_write_b32 a74, v193\n\t" \
  "v_accvgpr_read_b32 v193, a75\n\t" \
  "v_mul_f32 v193, v198, v193\n\t" \
  "v_accvgpr_write_b32 a75, v193\n\t" \
  "v_accvgpr_read_b32 v193, a76\n\t" \
  "v_mul_f32 v193, v199, v193\n\t" \
  "v_accvgpr_write_b32 a76, v193\n\t" \
  "v_accvgpr_read_b32 v193, a77\n\t" \
  "v_mul_f32 v193, v199, v193\n\t" \
  "v_accvgpr_write_b32 a77, v193\n\t" \
  "v_accvgpr_read_b32 v193, a78\n\t" \
  "v_mul_f32 v193, v199, v193\n\t" \
  "v_accvgpr_write_b32 a78, v193\n\t" \
  "v_accvgpr_read_b32 v193, a79\n\t" \
  "v_mul_f32 v193, v199, v193\n\t" \
  "v_accvgpr_write_b32 a79, v193\n\t" \
  "v_accvgpr_read_b32 v193, a80\n\t" \
  "v_mul_f32 v193, v196, v193\n\t" \
  "v_accvgpr_write_b32 a80, v193\n\t" \
  "v_accvgpr_read_b32 v193, a81\n\t" \
  "v_mul_f32 v193, v196, v193\n\t" \
  "v_accvgpr_write_b32 a81, v193\n\t" \
  "v_accvgpr_read_b32 v193, a82\n\t" \
  "v_mul_f32 v193, v196, v193\n\t" \
  "v_accvgpr_write_b32 a82, v193\n\t" \
  "v_accvgpr_read_b32 v193, a83\n\t" \
  "v_mul_f32 v193, v196, v193\n\t" \
  "v_accvgpr_write_b32 a83, v193\n\t" \
  "v_accvgpr_read_b32 v193, a84\n\t" \
  "v_mul_f32 v193, v197, v193\n\t" \
  "v_accvgpr_write_b32 a84, v193\n\t" \
  "v_accvgpr_read_b32 v193, a85\n\t" \
  "v_mul_f32 v193, v197, v193\n\t" \
  "v_accvgpr_write_b32 a85, v193\n\t" \
  "v_accvgpr_read_b32 v193, a86\n\t" \
  "v_mul_f32 v193, v197, v193\n\t" \
  "v_accvgpr_write_b32 a86, v193\n\t" \
  "v_accvgpr_read_b32 v193, a87\n\t" \
  "v_mul_f32 v193, v197, v193\n\t" \
  "v_accvgpr_write_b32 a87, v193\n\t" \
  "v_accvgpr_read_b32 v193, a88\n\t" \
  "v_mul_f32 v193, v198, v193\n\t" \
  "v_accvgpr_write_b32 a88, v193\n\t" \
  "v_accvgpr_read_b32 v193, a89\n\t" \
  "v_mul_f32 v193, v198, v193\n\t" \
  "v_accvgpr_write_b32 a89, v193\n\t" \
  "v_accvgpr_read_b32 v193, a90\n\t" \
  "v_mul_f32 v193, v198, v193\n\t" \
  "v_accvgpr_write_b32 a90, v193\n\t" \
  "v_accvgpr_read_b32 v193, a91\n\t" \
  "v_mul_f32 v193, v198, v193\n\t" \
  "v_accvgpr_write_b32 a91, v193\n\t" \
  "v_accvgpr_read_b32 v193, a92\n\t" \
  "v_mul_f32 v193, v199, v193\n\t" \
  "v_accvgpr_write_b32 a92, v193\n\t" \
  "v_accvgpr_read_b32 v193, a93\n\t" \
  "v_mul_f32 v193, v199, v193\n\t" \
  "v_accvgpr_write_b32 a93, v193\n\t" \
  "v_accvgpr_read_b32 v193, a94\n\t" \
  "v_mul_f32 v193, v199, v193\n\t" \
  "v_accvgpr_write_b32 a94, v193\n\t" \
  "v_accvgpr_read_b32 v193, a95\n\t" \
  "v_mul_f32 v193, v199, v193\n\t" \
  "v_accvgpr_write_b32 a95, v193\n\t" \
  "v_accvgpr_read_b32 v193, a96\n\t" \
  "v_mul_f32 v193, v196, v193\n\t" \
  "v_accvgpr_write_b32 a96, v193\n\t" \
  "v_accvgpr_read_b32 v193, a97\n\t" \
  "v_mul_f32 v193, v196, v193\n\t" \
  "v_accvgpr_write_b32 a97, v193\n\t" \
  "v_accvgpr_read_b32 v193, a98\n\t" \
  "v_mul_f32 v193, v196, v193\n\t" \
  "v_accvgpr_write_b32 a98, v193\n\t" \
  "v_accvgpr_read_b32 v193, a99\n\t" \
  "v_mul_f32 v193, v196, v193\n\t" \
  "v_accvgpr_write_b32 a99, v193\n\t" \
  "v_accvgpr_read_b32 v193, a100\n\t" \
  "v_mul_f32 v193, v197, v193\n\t" \
  "v_accvgpr_write_b32 a100, v193\n\t" \
  "v_accvgpr_read_b32 v193, a101\n\t" \
  "v_mul_f32 v193, v197, v193\n\t" \
  "v_accvgpr_write_b32 a101, v193\n\t" \
  "v_accvgpr_read_b32 v193, a102\n\t" \
  "v_mul_f32 v193, v197, v193\n\t" \
  "v_accvgpr_write_b32 a102, v193\n\t" \
  "v_accvgpr_read_b32 v193, a103\n\t" \
  "v_mul_f32 v193, v197, v193\n\t" \
  "v_accvgpr_write_b32 a103, v193\n\t" \
  "v_accvgpr_read_b32 v193, a104\n\t" \
  "v_mul_f32 v193, v198, v193\n\t" \
  "v_accvgpr_write_b32 a104, v193\n\t" \
  "v_accvgpr_read_b32 v193, a105\n\t" \
  "v_mul_f32 v193, v198, v193\n\t" \
  "v_accvgpr_write_b32 a105, v193\n\t" \
  "v_accvgpr_read_b32 v193, a106\n\t" \
  "v_mul_f32 v193, v198, v193\n\t" \
  "v_accvgpr_write_b32 a106, v193\n\t" \
  "v_accvgpr_read_b32 v193, a107\n\t" \
  "v_mul_f32 v193, v198, v193\n\t" \
  "v_accvgpr_write_b32 a107, v193\n\t" \
  "v_accvgpr_read_b32 v193, a108\n\t" \
  "v_mul_f32 v193, v199, v193\n\t" \
  "v_accvgpr_write_b32 a108, v193\n\t" \
  "v_accvgpr_read_b32 v193, a109\n\t" \
  "v_mul_f32 v193, v199, v193\n\t" \
  "v_accvgpr_write_b32 a109, v193\n\t" \
  "v_accvgpr_read_b32 v193, a110\n\t" \
  "v_mul_f32 v193, v199, v193\n\t" \
  "v_accvgpr_write_b32 a110, v193\n\t" \
  "v_accvgpr_read_b32 v193, a111\n\t" \
  "v_mul_f32 v193, v199, v193\n\t" \
  "v_accvgpr_write_b32 a111, v193\n\t" \
  "v_accvgpr_read_b32 v193, a112\n\t" \
  "v_mul_f32 v193, v196, v193\n\t" \
  "v_accvgpr_write_b32 a112, v193\n\t" \
  "v_accvgpr_read_b32 v193, a113\n\t" \
  "v_mul_f32 v193, v196, v193\n\t" \
  "v_accvgpr_write_b32 a113, v193\n\t" \
  "v_accvgpr_read_b32 v193, a114\n\t" \
  "v_mul_f32 v193, v196, v193\n\t" \
  "v_accvgpr_write_b32 a114, v193\n\t" \
  "v_accvgpr_read_b32 v193, a115\n\t" \
  "v_mul_f32 v193, v196, v193\n\t" \
  "v_accvgpr_write_b32 a115, v193\n\t" \
  "v_accvgpr_read_b32 v193, a116\n\t" \
  "v_mul_f32 v193, v197, v193\n\t" \
  "v_accvgpr_write_b32 a116, v193\n\t" \
  "v_accvgpr_read_b32 v193, a117\n\t" \
  "v_mul_f32 v193, v197, v193\n\t" \
  "v_accvgpr_write_b32 a117, v193\n\t" \
  "v_accvgpr_read_b32 v193, a118\n\t" \
  "v_mul_f32 v193, v197, v193\n\t" \
  "v_accvgpr_write_b32 a118, v193\n\t" \
  "v_accvgpr_read_b32 v193, a119\n\t" \
  "v_mul_f32 v193, v197, v193\n\t" \
  "v_accvgpr_write_b32 a119, v193\n\t" \
  "v_accvgpr_read_b32 v193, a120\n\t" \
  "v_mul_f32 v193, v198, v193\n\t" \
  "v_accvgpr_write_b32 a120, v193\n\t" \
  "v_accvgpr_read_b32 v193, a121\n\t" \
  "v_mul_f32 v193, v198, v193\n\t" \
  "v_accvgpr_write_b32 a121, v193\n\t" \
  "v_accvgpr_read_b32 v193, a122\n\t" \
  "v_mul_f32 v193, v198, v193\n\t" \
  "v_accvgpr_write_b32 a122, v193\n\t" \
  "v_accvgpr_read_b32 v193, a123\n\t" \
  "v_mul_f32 v193, v198, v193\n\t" \
  "v_accvgpr_write_b32 a123, v193\n\t" \
  "v_accvgpr_read_b32 v193, a124\n\t" \
  "v_mul_f32 v193, v199, v193\n\t" \
  "v_accvgpr_write_b32 a124, v193\n\t" \
  "v_accvgpr_read_b32 v193, a125\n\t" \
  "v_mul_f32 v193, v199, v193\n\t" \
  "v_accvgpr_write_b32 a125, v193\n\t" \
  "v_accvgpr_read_b32 v193, a126\n\t" \
  "v_mul_f32 v193, v199, v193\n\t" \
  "v_accvgpr_write_b32 a126, v193\n\t" \
  "v_accvgpr_read_b32 v193, a127\n\t" \
  "v_mul_f32 v193, v199, v193\n\t" \
  "v_accvgpr_write_b32 a127, v193\n\t" \
  "v_mul_f32 v160, v196, v160\n\t" \
  "v_mul_f32 v161, v196, v161\n\t" \
  "v_mul_f32 v162, v196, v162\n\t" \
  "v_mul_f32 v163, v196, v163\n\t" \
  "v_mov_b32 v196, 1.0\n\t" \
  "v_mul_f32 v164, v197, v164\n\t" \
  "v_mul_f32 v165, v197, v165\n\t" \
  "v_mul_f32 v166, v197, v166\n\t" \
  "v_mul_f32 v167, v197, v167\n\t" \
  "v_mov_b32 v197, 1.0\n\t" \
  "v_mul_f32 v168, v198, v168\n\t" \
  "v_mul_f32 v169, v198, v169\n\t" \
  "v_mul_f32 v170, v198, v170\n\t" \
  "v_mul_f32 v171, v198, v171\n\t" \
  "v_mov_b32 v198, 1.0\n\t" \
  "v_mul_f32 v172, v199, v172\n\t" \
  "v_mul_f32 v173, v199, v173\n\t" \
  "v_mul_f32 v174, v199, v174\n\t" \
  "v_mul_f32 v175, v199, v175\n\t" \
  "v_mov_b32 v199, 1.0\n\t" \
  "s_mov_b32 s99, 0\n\t" \
  "s_nop 1\n\t" \
  "s_cmp_eq_u32 vcc_lo, 0\n\t" \
  "s_cbranch_scc1 .Lsf_q0\n\t" \
  "s_cmp_eq_u32 vcc_lo, 1\n\t" \
  "s_cbranch_scc1 .Lsf_q1\n\t" \
  "s_cmp_eq_u32 vcc_lo, 2\n\t" \
  "s_cbranch_scc1 .Lsf_q2\n\t" \
  "s_cmp_eq_u32 vcc_lo, 3\n\t" \
  "s_cbranch_scc1 .Lsf_q3\n\t" \
  "s_cmp_eq_u32 vcc_lo, 4\n\t" \
  "s_cbranch_scc1 .Lsf_q4\n\t" \
  "s_cmp_eq_u32 vcc_lo, 5\n\t" \
  "s_cbranch_scc1 .Lsf_q5\n\t" \
  "s_cmp_eq_u32 vcc_lo, 6\n\t" \
  "s_cbranch_scc1 .Lsf_q6\n\t" \
  "s_cmp_eq_u32 vcc_lo, 7\n\t" \
  "s_cbranch_scc1 .Lsf_q7\n\t" \
  "s_endpgm\n\t" \
  ".Lsf_v:\n\t" \
  "s_mov_b32 s98, 0\n\t" \
  ".Lsf_v1:\n\t" \
  "s_sleep 4\n\t" \
  "buffer_load_dword v209, v205, %[ws], 0 offen sc1\n\t" \
  "s_waitcnt vmcnt(0)\n\t" \
  "v_and_b32 v206, 0xbfffbfff, v209\n\t" \
  "v_cmp_ne_u32_e64 s[96:97], %[tag], v206\n\t" \
  "s_cmp_lg_u64 s[96:97], 0\n\t" \
  "s_cbranch_scc0 .Lsf_w\n\t" \
  "s_add_u32 s98, s98, 1\n\t" \
  "s_cmp_lt_u32 s98, %[cap]\n\t" \
  "s_cbranch_scc1 .Lsf_v1\n\t" \
  "v_mov_b32 v206, %[tag]\n\t" \
  "buffer_store_dword v206, off, %[ws], 0 offset:256 sc1\n\t" \
  "s_branch .Lsf_w\n\t" \
  ".subsection 0\n\t" \
  ".Lat_end:\n\t" \
  ""
#define AT_CLOB "v208", "v209", "v210", "v211", "v212", "v214", "v215", "v216", "v217", "v218", "v219", "v235", "v236", "v237", "v238", "s92", "s93", "s94", "s95", "s96", "s97", "s98", "s99", "vcc", "scc", "memory"
#define AT_NU 1
#define AT_BN 0
#define AT_XF "+{v[0:15]}"
#define AT_WF "+{v[16:47]}"
struct WoP { srd_t srd; unsigned wv[2], m, hl, base, wa; };
struct W4C {
  KVP kv;
  unsigned pf1, pf2, pf4, cto, po, px, vo, eso, hl, hl18, frb, xcb, xso, ss, mbp, gvo, l16, gvm, vs, c3;
  WoP wo;
  srd_t sG, sU, sD, sX, sR, sM;
  unsigned gv, wa0, fw2, tagv, fbv, tlb, pre, otb, na, gra, fl; u32x32 q[4], up; f32x32 wm; u32x16 o3;
};
struct Ptrs { const u16 *wo, *w_r, *wg, *wu, *wd; };
DI void attn_setup(const Ctx& k, W4C& c, const u16* kc, const u16* vc, const u16* pm, srd_t srdx, const Ptrs& a) {
  const unsigned wid = k.wid, fb = k.xcd * 28 + ffn_j(k);
  c.kv.sofk = KRS * 16u * wid;
  const size_t kvoff = ((size_t)k.kvh * SKV + (SKV - WIN)) * DH;
  c.kv.srdK = mk_srd(kc + kvoff, WIN * DH * 2); c.kv.srdV = mk_srd(vc + kvoff, WIN * DH * 2);
  const unsigned go = wid * 256 * FF + fb * 16, nG = (DM * FF - go) * 2;
  c.sG = mk_srd(a.wg + go, nG); c.sU = mk_srd(a.wu + go, nG);
  c.sM = mk_srd(pm + 2048 + k.xcd * 512, DM * 6 * DM * 2 - 4096 - k.xcd * 1024);
  c.wo.srd = mk_srd(a.wo, QD * DM * 2); c.sX = srdx;
  c.sD = mk_srd(a.wd + (size_t)fb * 16 * DM, ffn_idle(k) ? 0u : 32768u); c.sR = mk_srd(a.w_r, DM * 2);
  asm volatile("" : "+s"(c.kv.srdK), "+s"(c.kv.srdV), "+s"(c.sG), "+s"(c.sU), "+s"(c.sM), "+s"(c.wo.srd), "+s"(c.sD), "+s"(c.sR));
  c.wo.m = wid * 4096; c.wo.base = (k.kvh * 1024 + k.hl * 128 + wid * 16) * (DM * 2) + k.hl * 256;
  c.pf1 = (k.kvh * 1024 + k.j * 32 + wid * 8) * (DM * 2) + (k.xcd & 1) * 8192;
  c.pf4 = k.j * (8 * FF * 2) - ffn_j(k) * 32;
}
DI void attention(Ctx& k, H4& g, W4C& c STAMP_ARG) {
  unsigned nb;
  asm volatile(AT_ASM
               : "+{v[0:31]}"(g.q[0]), "+{v[32:63]}"(g.q[1]), "+{v[64:95]}"(g.q[2]), "+{v[96:127]}"(g.q[3]), [pfd] "+{v246}"(g.pfd), "=&{v[128:159]}"(c.wm), "=&{v[160:175]}"(c.o3), "+{v224}"(c.kv.vksrc[0]), "+{v225}"(c.kv.vksrc[1]), "+{v226}"(c.kv.vksrc[2]), "+{v227}"(c.kv.vksrc[3]), "=&{v[176:207]}"(c.up),
                 "=&{v234}"(c.wo.wa), "=&{v213}"(c.gra), "+{v245}"(g.fvo), "=&{v243}"(c.tagv), "=&{v254}"(c.fbv),
                 "=&{v242}"(c.l16), "+{v247}"(c.tlb), "=&{v252}"(c.vs), [na] "=&s"(c.na), [nb] "=&s"(nb), [eso] "+s"(c.eso), [xcb] "+s"(c.xcb), [frb] "+s"(c.frb), [fw2] "+s"(c.fw2) AT_STV
               : "{v220}"(c.kv.vkb[0]), "{v221}"(c.kv.vkb[1]), "{v222}"(c.kv.vkb[2]), "{v223}"(c.kv.vkb[3]),
                 "{v228}"(c.kv.vvb[0]), "{v229}"(c.kv.vvb[1]), "{v230}"(c.kv.vvsrc[0]), "{v231}"(c.kv.vvsrc[1]), "{v232}"(c.gv), "{v233}"(c.gvm), "{v240}"(c.wo.wv[0]), "{v241}"(c.wo.wv[1]), "{v244}"(c.wa0), "{v248}"(c.c3),
                 [ws] "s"(k.ws), [sK] "s"(c.kv.srdK), [sV] "s"(c.kv.srdV), [sG] "s"(c.sG), [sU] "s"(c.sU), [wo] "s"(c.wo.srd), [sM] "s"(c.sM), [sD] "s"(c.sD), [sR] "s"(c.sR),
                 [m] "s"(c.wo.m), [sofk] "s"(c.wo.m), [hl18] "s"(c.hl18), [wob] "s"(c.wo.base), [pf2] "s"(c.pf2), [pf4] "s"(c.pf4),
                 [tag] "s"(k.tag), [cap] "i"(kSpinCap), [cto] "s"(c.cto), [po] "s"(c.po), [px] "s"(c.px), [vo] "s"(c.vo), [pm] "s"(k.pm), [ss] "s"(c.ss), [gvo] "s"(c.gvo)
               : AT_CLOB, ACLOB_LO, ACLOB_HI);
  k.cs ^= nb; k.nb = nb; c.fl = g.fvo;
#pragma unroll
  for (int i = 0; i < 4; ++i) c.q[i] = g.q[i];
  GAP();
}
constexpr unsigned SCR = 9 * UNIT;
#define STAMPF(I) do { } while (0)
#define FQ_STR2(x) #x
#define FQ_STR(x) FQ_STR2(x)
#define FQ_CAP FQ_STR(SPINCAP)
DI bool tags2(const u32x4& a, unsigned tag) { return (a[1] == tag) & (a[3] == tag); }
DI void bcast4(float v, float (&o)[4]) {
  auto a = __builtin_amdgcn_permlane32_swap(__float_as_uint(v), __float_as_uint(v), false, false);
  auto b = __builtin_amdgcn_permlane16_swap(a[0], a[0], false, false);
  auto c = __builtin_amdgcn_permlane16_swap(a[1], a[1], false, false);
  o[0] = __uint_as_float(b[0]); o[1] = __uint_as_float(b[1]); o[2] = __uint_as_float(c[0]); o[3] = __uint_as_float(c[1]);
}
DI void x_raw2(unsigned m0, srd_t srd, unsigned v0, unsigned v1, unsigned so) {
  asm volatile("s_mov_b32 m0, %0\n\ts_nop 0\n\tbuffer_load_dwordx4 %1, %3, %4 offen lds\n\tbuffer_load_dwordx4 %2, %3, %4 offen offset:1024 lds"
               :: "s"(m0), "v"(v0), "v"(v1), "s"(srd), "s"(so) : "memory");
}
DI void w_touch2(H4& g, const u16* wq, unsigned base, unsigned t) {
  touch(g.pfd, wq, base + ((g.rot + t) & 15u) * kWT);
}
constexpr unsigned TB0 = 6, TBN = 6;
#define SE 0
#define F1 0
#define FT 0
#define MK 0
#define PGA 0
#define WT 0
#define LT 0
#define EL 0
#define PT0 0
#define PPOS 2
DI void front(Ctx& k, H4& g, W4C& c STAMP_ARG) {
  argp_t ap = args();
  const u16 *px = ap->x, *kc = ap->kc, *vc = ap->vc, *wqp = ap->wq, *pc_ = ap->c, *pm = ap->w_mod; char* pw = ap->ws;
  GAP();
  const unsigned lane = lane_id(), wid = k.wid, tid = wid * 64 + lane;
  g.pfd = 0;
  {
    unsigned long long pc; unsigned len;
    asm volatile("s_getpc_b64 %0\n.Lfz_a:\n\ts_mov_b32 %1, .Lfz_z-.Lfz_a-4" : "=s"(pc), "=s"(len));
    touch(g.pfd, (const void*)pc, min(((lane * 4 + wid) * 32 + k.j) * 128, len));
  }
  GAP();
  k.ws = mk_srd((char*)(((uintptr_t)pw + 0x1fffff) & ~(uintptr_t)0x1fffff), W_END);
  const srd_t srdx = mk_srd(px, S * DM * 2), srdm = mk_srd(pm, DM * 6 * DM * 2), srdc = mk_srd(pc_, DM * 2);
  asm volatile("" : "+s"(kc), "+s"(vc));
  const unsigned head = k.kvh * 8 + k.hl, row64 = k.rblk * 256 + k.kvh * 64, rot = k.kvh * 4, u0 = k.hl >> 1, kt = rot + ((u0 + wid) & 3), k0 = k.hl & 1, cq = k.kvh ^ 3;
  g.srdW = mk_srd(wqp + head * DH, (DM * QD - head * DH) * 2); g.rot = rot;
  u32x32 xa;
  const unsigned st1 = W_ST1 + (k.rblk << 12);
  u32x4 xs[4], wm[4];
  const unsigned cbk = (k.xcd & 1) * 32 + k.j, kp = k.kvh;
  u32x2 cv = ald8<0>(srdc, (tid >> 2) * 8, kp * 512);
  { const unsigned vo = (tid >> 2) * (4 * 6 * DM * 2) + (tid & 3) * 16, so = kp * (256 * 6 * DM * 2) + cbk * 64;
#pragma unroll
    for (int i = 0; i < 4; ++i) wm[i] = ald16<0>(srdm, vo, so + i * (6 * DM * 2)); }
  unsigned E = ald4<0, true>(k.ws, EL == 0 || lane == 0 ? W_EPOCH : lane == 1 ? W_HT + ((k.rblk & 3) << 20) : lane == 2 ? st1 + (k.kvh * 8 + k.hl) * 128 : W_FLAGH + (k.rblk * 4 + wid) * 128, 0u);
  { const unsigned so = (row64 + k.hl * 8 + 2 * wid) * (DM * 2);
    xs[0] = ald16<0>(srdx, lane * 16, so); xs[1] = ald16<1024>(srdx, lane * 16, so); xs[2] = ald16<2048>(srdx, lane * 16, so); xs[3] = ald16<3072>(srdx, lane * 16, so); }
  unsigned vx = (lane >> 3) * (DM * 2) + (((lane & 7) ^ (lane >> 4)) << 4), vx1 = (vx ^ 64) + 15360, sl[4];
  g.mX = k.ldsb + wid * 8192;
#pragma unroll
  for (int u = 0; u < 4; ++u) sl[u] = (k.rblk * 256 + wid * 16) * (DM * 2) + (rot + ((u0 + u) & 3)) * 128;
#define XRAW(u, rb) x_raw2(g.mX + (u) * GSLOT + (rb) * 2048, srdx, vx, vx1, sl[u] + (((rb) ^ cq) << 17))
  if (!(LT & 2)) { XRAW(0, 0); XRAW(0, 1); }
  const unsigned tw2 = (k.j * 2 + (wid >> 1)) * (QD * 2) + k.kvh * 2048 + (lane & 7) * 256 + (wid & 1) * 128;
  if (!(LT & 1)) w_touch2(g, wqp, tw2, (lane >> 3) & 3);
  constexpr int NL = (LT & 1) + 2 * (LT & 2);
  GAP();
  g.mW = k.ldsb + 32768 + wid * 4096;
#define X01() do { XRAW(0, 2); XRAW(0, 3); XRAW(1, 0); XRAW(1, 1); XRAW(1, 2); XRAW(1, 3); } while (0)
#define X2() do { XRAW(2, 0); XRAW(2, 1); XRAW(2, 2); XRAW(2, 3); } while (0)
  const unsigned tb = k.ldsb + SCR + 1024;
  u32x16 wbs; u32x8 vw, ma; u32x4 mb; const u16* kvt; unsigned pso, fw, w2, w3, gso, gpo;
  auto setup_w = [&]() {
    w_voffs(g.voffW, QD, wid, lane, k0);
    gso = st1 + (((lane >> 4) ^ cq) * 64 + wid * 16 + (lane & 15)) * 16; gpo = W_MODA + ((lane >> 5) * 1024 + kt * 64 + (lane & 31) * 2) * 8;
    asm volatile("" : W4(g.voffW), "+v"(gso), "+v"(gpo));
    GAP();
  };
  auto setup_b = [&]() {
    g.voffX = wid * 8192 + ((lane >> 4) ^ cq) * 256 + (lane & 15) * 16; g.soX = W_HT + ((k.rblk & 3) << 20) + (((k.xcd ^ wid) & 1) << 13) + wid * 8192; g.fo = W_FLAGH + (k.rblk * 4 + wid) * 128;
    h4_init_addr(g, k.ldsb, wid, lane, k0);
    const unsigned xr = g.mX + (lane & 15) * 128 + (((4 * k0 + (lane >> 4)) ^ ((lane >> 1) & 7)) << 4);
    vw = u32x8{g.voffW[0], g.voffW[1], g.voffW[2], g.voffW[3], xr, xr ^ 64, xr + 2 * GSLOT, (xr ^ 64) + 2 * GSLOT};
#pragma unroll
    for (int i = 0; i < 8; ++i) { wbs[i] = g.wb[0][i]; wbs[8 + i] = g.wb[1][i]; }
    ma = u32x8{tb + (lane >> 4) * 32, g.voffX, (lane & 31) * 4, 0u, vx, vx1, g.xb1,
               WT == 1 ? tw2 + ((rot + 4 + (k.xcd & 1) + 2 * min(lane >> 3, 5u)) & 15u) * kWT : WT == 2 ? tw2 + ((rot + 4 + (k.xcd & 1) + 2 * min(lane >> 3, 2u)) & 15u) * kWT
                       : tw2 - (lane & 7) * 256 + (1 + 4 * (lane & 1)) * 256 + ((rot + 4 + min(lane >> 1, 11u)) & 15u) * kWT};
    mb = u32x4{WT == 2 ? tw2 + ((rot + 10 + (k.xcd & 1) + 2 * min(lane >> 3, 2u)) & 15u) * kWT : (tw2 & ~(((lane & 3) == 1) ? 256u : 0u)) + ((rot + TB0 + min(lane >> 3, TBN - 1u)) & 15u) * kWT, ((k.j & 15) * 64 + wid * 16 + (lane & 15)) * 128, 0u, W_SIL + k.bid * 512 + tid * 2};
    kvt = ((k.j & 16) ? vc : kc) + ((size_t)k.kvh * SKV + (SKV - WIN)) * DH;
    pso = g.soX + (rot + u0) * kXT + cq * 256 - GSLOT - k.ldsb; fw = g.fo + head * 4; w2 = (rot + ((u0 + 2) & 3)) * kWT; w3 = (rot + ((u0 + 3) & 3)) * kWT;
    asm volatile("" : "+{v[184:199]}"(wbs), "+{v[200:207]}"(vw), "+{v[208:215]}"(ma), "+{v[216:219]}"(mb), "+s"(kvt), "+s"(pso), "+s"(fw), "+s"(w2), "+s"(w3));
    GAP();
  };
  if constexpr (SE & 2) { setup_w(); setup_b(); }
  unsigned tag;
  HOP_GO(2, wid);
  asm volatile("s_waitcnt vmcnt(%1)" : "+v"(cv) : "i"(14 + PT0 - NL) : "memory");
  float sc[4];
#pragma unroll
  for (int i = 0; i < 4; ++i) sc[i] = silu_bf((i & 1) ? hi16f(cv[i >> 1]) : lo16f(cv[i >> 1]));
  asm volatile("" : W4(sc));
  GAP();
  STAMPF(27);
  asm volatile("s_waitcnt vmcnt(%4)" : W4(wm) : "i"(10 + PT0 - NL) : "memory");
  STAMP(1);
  if (LT & 2) { XRAW(0, 0); XRAW(0, 1); }
  if (LT & 1) w_touch2(g, wqp, tw2, (lane >> 3) & 3);
  GAP();
  {
    f32x2 a[4] = {};
#pragma unroll
    for (int i = 0; i < 4; ++i)
#pragma unroll
      for (int e = 0; e < 4; ++e) a[e] = __builtin_elementwise_fma(f32x2{sc[i], sc[i]}, up2(wm[i][e]), a[e]);
    const float b0 = fold32(a[0][0], a[2][0]), b1 = fold32(a[0][1], a[2][1]), b2 = fold32(a[1][0], a[3][0]), b3 = fold32(a[1][1], a[3][1]);
    float c0 = fold16(b0, b2), c1 = fold16(b1, b3);
    c0 += dpp_f<0x128>(c0); c1 += dpp_f<0x128>(c1);
    c0 += dpp_f<0x124>(c0); c1 += dpp_f<0x124>(c1);
    const unsigned p = 4 * (lane & 3) + 2 * (lane >> 5) + ((lane >> 4) & 1);
    if (!(lane & 12)) lds_st(k.ldsb + SCR + wid * 256 + p * 8, f32x2{c0, c1});
    asm volatile("s_waitcnt vmcnt(%1) lgkmcnt(0)\n\ts_barrier" : "+v"(E) : "i"(9 + PT0) : "memory");
    tag = ((rfl(E) | TAGM) + TAGC) & ~TAGM;
    k.tag = tag;
    if (wid == 0 && lane < 16) {
      const unsigned a0 = k.ldsb + SCR + lane * 8;
      const f32x2 s = (lds_ld<f32x2>(a0) + lds_ld<f32x2>(a0 + 256)) + (lds_ld<f32x2>(a0 + 512) + lds_ld<f32x2>(a0 + 768));
      st16(k.ws, W_MODA + (kp * 2048 + cbk * 32 + lane * 2) * 8, u32x4{__float_as_uint(s[0]), tag, __float_as_uint(s[1]), tag});
    }
  }
  GAP();
  asm volatile("s_waitcnt vmcnt(%5)\n\ts_cmp_eq_u32 %4, 0\n\ts_cbranch_scc1 .Lxs%=\n\ts_waitcnt vmcnt(%6)\n.Lxs%=:" : W4(xs) : "s"(wid), "i"(6 + PT0), "i"(5 + PT0) : "memory", "scc");
  STAMP(2);
  {
    float sm[2][2] = {}, sq[2][2] = {};
#pragma unroll
    for (int e = 0; e < 4; ++e)
#pragma unroll
      for (int i = 0; i < 4; ++i) { sm[i >> 1][i & 1] = dot2(xs[i][e], 0x3f803f80u, sm[i >> 1][i & 1]); sq[i >> 1][i & 1] = dot2(xs[i][e], xs[i][e], sq[i >> 1][i & 1]); }
    const float z = sum_row16(fold16(fold32(sm[0][0] + sm[0][1], sm[1][0] + sm[1][1]), fold32(sq[0][0] + sq[0][1], sq[1][0] + sq[1][1]))) * (1.f / DM);
    auto zz = __builtin_amdgcn_permlane16_swap(__float_as_uint(z), __float_as_uint(z), false, false);
    const float mean = __uint_as_float(zz[0]);
    const float rstd = rsqrtf(fmaxf(fmaf(-mean, mean, __uint_as_float(zz[1])), 0.f) + 1e-5f);
    if ((lane & 31) == 0) st16(k.ws, st1 + (k.kvh * 64 + k.hl * 8 + 2 * wid + (lane >> 5)) * 16, u32x4{__float_as_uint(rstd), tag, __float_as_uint(-mean * rstd), tag});
  }
  GAP();
  STAMP(3);
  Ptrs pa;
  { argp_t aq = args(); pa = Ptrs{aq->wo, aq->w_r, aq->wg, aq->wu, aq->wd}; }
  X01();
  unsigned cb;
  asm volatile("buffer_load_ushort %0, %1, %2, %3 offen" : "=v"(cb) : "v"(tid * 2), "s"(srdc), "s"((k.j >> 3) * 512) : "memory");
  if constexpr (!(SE & 2)) setup_w();
  w_dma4(g.mW, g.srdW, g.voffW, (rot + u0) * kWT);
#define W1() w_dma4(g.mW + GSLOT, g.srdW, g.voffW, (rot + ((u0 + 1) & 3)) * kWT)
#define W2() w_dma4(g.mW + 2 * GSLOT, g.srdW, g.voffW, (rot + ((u0 + 2) & 3)) * kWT)
  if constexpr (FQ_W1L == 0) W1();
  if constexpr (FQ_X2E == 2) X2();
  if constexpr (FQ_W2E == 2) W2();
  u32x4 st, p[4];
#define POLL() do { st = ald16<0, true>(k.ws, gso, 0u); _Pragma("unroll") for (int i = 0; i < 4; ++i) p[i] = ald16<0, true>(k.ws, gpo, i * 16384u); GAP(); } while (0)
  GAP();
  if constexpr (PPOS == 0) POLL();
  if constexpr (!(SE & 2)) setup_b();
  if constexpr (PPOS == 2) POLL();
  attn_setup(k, c, kc, vc, pm, srdx, pa);
  GAP();
  ma[3] = tag;
  asm volatile("" : "+{v[208:215]}"(ma));
  if constexpr (FQ_W1L == 2) W1();
  if constexpr (FQ_X2E == 1) X2();
  if constexpr (FQ_W2E == 1) W2();
  GAP();
  STAMPF(28);
  if (PGA & 1) PAGE_ALIGN();
  HOP_GO(0, wid);
  if (PGA & 2) PAGE_ALIGN();
  HOP_FRONT();
  f32x16 rs;
  {
    asm volatile("s_waitcnt vmcnt(%7)" : W4(p), "+v"(st), "+v"(g.pfd), "+v"(cb) : "i"((FQ_X2E == 1 ? 8 : 0) + (FQ_W1L == 2 ? 4 : 0) + (FQ_W2E == 1 ? 4 : 0) + (FT & 1) + (FT >> 1)) : "memory");
    asm volatile("ds_read_b128 v[0:3], %1\n\tds_read_b128 v[4:7], %1 offset:2048\n\tds_read_b128 v[8:11], %1 offset:4096\n\tds_read_b128 v[12:15], %1 offset:6144\n\t"
                 "ds_read_b128 v[16:19], %2\n\tds_read_b128 v[20:23], %2 offset:2048\n\tds_read_b128 v[24:27], %2 offset:4096\n\tds_read_b128 v[28:31], %2 offset:6144"
                 : "=&{v[0:31]}"(xa) : "v"(vw[4]), "v"(vw[5]) : "memory");
    mb[2] = __float_as_uint(silu_bf(lo16f(cb))) >> 16;
    asm volatile("" : "+{v[216:219]}"(mb));
    STAMPF(29);
    for (unsigned spins = 0;;) {
      if (__all(tags2(st, tag) & tags2(p[0], tag) & tags2(p[1], tag) & tags2(p[2], tag) & tags2(p[3], tag))) break;
      if (spin_fail<SL0>(spins, k.ws, tag)) break;
      st = ld16(k.ws, gso);
#pragma unroll
      for (int i = 0; i < 4; ++i) p[i] = ld16(k.ws, gpo + i * 16384);
    }
    STAMP(4);
    f32x2 m[4];
#pragma unroll
    for (int i = 0; i < 4; ++i) m[i] = f32x2{__uint_as_float(p[i][0]), __uint_as_float(p[i][2])};
    f32x2 s = (m[0] + m[1]) + (m[2] + m[3]);
    s = up2(cvtpk(s[0], s[1]));
    if (lane >= 32) { s += 1.f; s = up2(cvtpk(s[0], s[1])); }
    lds_st(tb + wid * 512 + (((lane ^ 32) * 8) ^ (k0 * 128)), s * QSCALE);
    GAP();
    float ra[4], rn[4];
    bcast4(__uint_as_float(st[0]), ra); bcast4(__uint_as_float(st[2]), rn);
    rs = f32x16{ra[0], ra[0], rn[0], rn[0], ra[1], ra[1], rn[1], rn[1], ra[2], ra[2], rn[2], rn[2], ra[3], ra[3], rn[3], rn[3]};
    asm volatile("" : "+{v[160:175]}"(rs));
    STAMPF(12);
    BARRIER_LGKM();
    STAMPF(13);
  }
  g.q[0] = xa;
  static_assert(W_XCH == W_OT + (16u << 20));
  const unsigned vp = k.xcd * 2 + ((k.j >> 1) & 1);
  const unsigned evw = (blockIdx.x >> 1) * 4 + wid;
  asm volatile(FQ_ASM : "+{v[0:31]}"(g.q[0]), "=&{v[32:63]}"(g.q[1]), "=&{v[64:95]}"(g.q[2]), "=&{v[96:127]}"(g.q[3]), [fl] "=&v"(g.fl), [pf] "+{v246}"(g.pfd), "+{v[200:207]}"(vw), "+{v[208:215]}"(ma), "+{v[216:219]}"(mb),
                 "=&{v220}"(c.kv.vkb[0]), "=&{v221}"(c.kv.vkb[1]), "=&{v222}"(c.kv.vkb[2]), "=&{v223}"(c.kv.vkb[3]), "=&{v224}"(c.kv.vksrc[0]), "=&{v225}"(c.kv.vksrc[1]), "=&{v226}"(c.kv.vksrc[2]), "=&{v227}"(c.kv.vksrc[3]),
                 "=&{v228}"(c.kv.vvb[0]), "=&{v229}"(c.kv.vvb[1]), "=&{v230}"(c.kv.vvsrc[0]), "=&{v231}"(c.kv.vvsrc[1]), "=&{v232}"(c.gv), "=&{v240}"(c.wo.wv[0]), "=&{v241}"(c.wo.wv[1]), "=&{v244}"(c.wa0),
                 "=&{v245}"(g.fvo), "=&{v247}"(c.tlb), "=&{v233}"(c.gvm), "=&{v248}"(c.c3),
                 [hl] "=&s"(c.hl), [hl18] "=&s"(c.hl18), [cto] "=&s"(c.cto), [pf2] "=&s"(c.pf2), [po] "=&s"(c.po), [px] "=&s"(c.px), [vo] "=&s"(c.vo), [eso] "=&s"(c.eso), [otb] "=&s"(c.otb),
                 [xcb] "=&s"(c.xcb), [frb] "=&s"(c.frb), [fw2] "=&s"(c.fw2), [xso] "=&s"(c.xso), [ss] "=&s"(c.ss), [mbp] "=&s"(c.mbp), [gvo] "=&s"(c.gvo) FQ_STV
               : "{v[160:175]}"(rs), "{v[184:199]}"(wbs),
                 [ws] "s"(k.ws), [sw] "s"(g.srdW), [sx] "s"(srdx), [sK] "s"(c.kv.srdK), [sV] "s"(c.kv.srdV), [sG] "s"(c.sG), [sU] "s"(c.sU), [mw] "s"(g.mW), [mx] "s"(g.mX),
                 [rot19] "s"(rot << 19), [w2] "s"(w2), [w3] "s"(w3), [sox] "s"(FQ_XD ? g.soX - GSLOT - k.ldsb : g.soX), [dk] "s"(4096u - k0 * 8192u), [fo] "s"(g.fo), [fw] "s"(fw), [pso] "s"(pso), [c17] "s"(cq << 17), [sl2] "s"(sl[2]), [w1] "s"((rot + ((u0 + 1) & 3)) * kWT),
                 [sl3] "s"(sl[3]), [wq] "s"(wqp), [kv] "s"(kvt), [sofk] "s"(c.wo.m), [pf4] "s"(c.pf4), [pf1] "s"(FQ_EV ? evw * 16384 : c.pf1), [tln] "s"((blockIdx.x & 1) ? 0u : 32u), [wo] "s"(c.wo.srd),
                 [bid] "s"(k.bid), [wid] "s"(wid), [idm] "s"(ffn_idle(k) ? 0x80000000u : 0u), [prb] "s"(ffn_idle(k) && wid == 0 ? ((k.j & 1) ? W_OT + (vp << 21) : W_PROBE + ((vp & 1) << 13) + vp * 128) : 0x80000000u), [pto] "s"(FQ_EV ? evw * 4096 : (blockIdx.x * 4 + wid) * 2048)
               : FQ_CL, ACLOB_LO, ACLOB_HI);
  GAP();
}
constexpr unsigned TABU = 9 * UNIT;
#define OAUX 17
DI int key_row(unsigned k) { return S - 1 - (int)(k & 0xffffu); }
DI float key_val(unsigned k) { unsigned b = k >> 16; b = (b & 0x8000u) ? (b & 0x7fffu) : (~b & 0xffffu); return bf2f((u16)b); }
template <int R> DI unsigned acc_readu() { unsigned r; asm volatile("v_accvgpr_read_b32 %0, a%c1" : "=v"(r) : "i"(R)); return r; }
template <int... Is> DI void gate_read(u32x32& g, ilist<Is...>) { ((g[Is] = acc_readu<224 + Is>()), ...); }
#define ABT 0
#define RC 16
#define TP 1
#define TA 0
#define TB 33
#define TE 0
#define OP_FX
DI void oproj(Ctx& k, W4C& c STAMP_ARG) {
  const unsigned wid = k.wid, tag = k.tag, bid = k.bid, grp = k.rblk * 4 + k.kvh, cblk = k.hl;
  u32x32 UP = c.up, DN = c.q[1]; f32x32 A3 = __builtin_bit_cast(f32x32, c.q[3]), XT = __builtin_bit_cast(f32x32, c.q[0]); float cww, bbw;
  asm volatile(OP_ASM
               : "+{v[176:207]}"(UP), "+{v[160:175]}"(c.o3), "+{v[32:63]}"(DN), "+{v[96:127]}"(A3), "+{v[0:31]}"(XT), "+{v[64:95]}"(c.q[2]), "+{v[128:159]}"(c.wm), "+{v224}"(c.kv.vksrc[0]), "+{v225}"(c.kv.vksrc[1]), "+{v226}"(c.kv.vksrc[2]), "+{v227}"(c.kv.vksrc[3]), "=&{s87}"(cww), "=&{s88}"(bbw),
                 "+{v213}"(c.gra), "+{v240}"(c.wo.wv[0]), "+{v241}"(c.wo.wv[1]), "+{v247}"(c.tlb), "+{v252}"(c.vs), [pm] "+s"(k.pm)
               : "{v234}"(c.wo.wa), "{v242}"(c.l16), "{v243}"(c.tagv), "{v245}"(c.fl), "{v254}"(c.fbv),
                 [ws] "s"(k.ws), [wo] "s"(c.wo.srd), [sd] "s"(c.sD), [sx] "s"(c.sX), [sr] "s"(c.sR),
                 [tag] "s"(tag), [wid] "s"(wid), [hl] "s"(c.hl), [eso] "s"(c.eso), [na] "s"(c.na), [frb] "s"(c.frb), [xcb] "s"(c.xcb), [xso] "s"(c.xso),
                 [m] "s"(c.wo.m), [wob] "s"(c.wo.base), [ss] "s"(c.ss), [mbp] "s"(c.mbp), [cap] "i"(kSpinCap), [fw2] "s"(c.fw2)
               : OP_CLOB, ACLOB_LO, ACLOB_HI);
  const f32x4 ga0 = {c.wm[0], c.wm[1], c.wm[2], c.wm[3]}, ga1 = {c.wm[4], c.wm[5], c.wm[6], c.wm[7]};
  if (__builtin_expect(k.pm == 2, 0)) {
    asm volatile("s_getpc_b64 vcc\n.Lrs_a:\n\ts_sub_u32 vcc_lo, vcc_lo, .Lrs_a-.Lfz_a\n\ts_subb_u32 vcc_hi, vcc_hi, 0\n\ts_and_b32 vcc_lo, vcc_lo, 0xfffff000\n\ts_setpc_b64 vcc"
                 :: "{s[0:1]}"(__builtin_amdgcn_kernarg_segment_ptr()), "{s2}"(blockIdx.x), "{v0}"((wid * 64 + lane_id()) | 0x10000u) : "vcc", "scc", "memory");
    __builtin_unreachable();
  }
  const unsigned lane = lane_id(), r16 = lane & 15, g = lane >> 4, tid = wid * 64 + lane;
  const unsigned sc = k.ldsb, sto = W_STAT + ((grp & 15) << 15) + k.cs, stw = ((wid >> 1) << 14) + ((wid & 1) << 12);
  const bool own0 = TA && cblk == 2 * wid, own1 = TA && cblk == 2 * wid + 1;
  const unsigned o0 = (sto + stw + lane * 16) | (own0 ? 0x80000000u : 0u), o1 = (sto + stw + 2048 + lane * 16) | (own1 ? 0x80000000u : 0u);
  u32x4 a0, a1, b0, b1;
#define STATS() do { a0 = ld16(k.ws, o0); b0 = ld16(k.ws, o0 + 1024); a1 = ld16(k.ws, o1); b1 = ld16(k.ws, o1 + 1024); } while (0)
  u32x16 xp; f32x4 q, sr;
#pragma unroll
  for (int rb = 0; rb < 4; ++rb)
#pragma unroll
    for (int e = 0; e < 4; ++e) {
      const int i = ((e >> 1) * 4 + rb) * 4 + (e & 1) * 2;
      const f32x4 gq = (e >> 1) ? ga1 : ga0;
      const f32x2 t = __builtin_elementwise_fma(f32x2{gq[(e & 1) * 2], gq[(e & 1) * 2 + 1]}, f32x2{A3[i], A3[i + 1]}, f32x2{XT[i], XT[i + 1]});
      xp[4 * rb + e] = cvtpk(t[0], t[1]);
    }
  asm volatile(ST_ASM : "=&{v[64:67]}"(q), "=&{v[80:83]}"(sr) : "{v[160:175]}"(xp));
  lds_st(sc + wid * 1024 + lane * 16, f32x4{sr[0], fold16(fold32(q[0], q[2]), fold32(q[1], q[3])), sr[1] + sr[2], (lane & 1) ? bbw : cww});
  BARRIER_LGKM();
  f32x4 so;
  {
    f32x4 p[4];
#pragma unroll
    for (int w = 0; w < 4; ++w) p[w] = lds_ld<f32x4>(sc + w * 1024 + lane * 16);
    so = (p[0] + p[1]) + (p[2] + p[3]);
    if (wid < 2) st16(k.ws, sto + ((cblk >> 2) << 14) + ((cblk & 3) << 11) + wid * 1024 + lane * 16, u32x4{__float_as_uint(wid ? so[2] : so[0]), tag, __float_as_uint(wid ? so[3] : so[1]), tag});
  }
  STAMPO(20);
  f32x2 xq[4][4];
#define XQ(rb, e) xq[rb][e]
  f32x4 tsb[2], tc2[2], tgb[2];
#pragma unroll
  for (int nb = 0; nb < 2; ++nb) {
    const unsigned ta = k.ldsb + TABU + wid * 4096 + 2560 + nb * 64 + g * 16;
    tsb[nb] = lds_ld<f32x4>(ta + 128); tc2[nb] = lds_ld<f32x4>(ta + 256); tgb[nb] = lds_ld<f32x4>(ta + 384);
  }
  float mu_d, rstd; unsigned gbest;
  srd_t srdo = mk_srd(args()->out, S * DM * 2);
  asm volatile("" : "+s"(srdo));
  const unsigned ri = ((bid & 3) | ((bid >> 3) << 2)) & (RC - 1);
  const unsigned ko = W_GKEY + ((ri >> 5) << 14) + k.cs + (ri & 31) * 256 + (lane & 31) * 8, ho = W_CANDH + k.cs + (tid >> 1) * 32;
  u32x2 kk; u32x4 h0, h1; unsigned spec;
#define KEY1() asm volatile("buffer_load_dwordx2 %0, %1, %2, 0 offen sc1" : "={v[224:225]}"(kk) : "v"(ko), "s"(k.ws) : "memory")
#define KEYS() asm volatile("buffer_load_dwordx2 %0, %3, %5, 0 offen sc1\n\tbuffer_load_dwordx4 %1, %4, %5, %6 offen sc1\n\tbuffer_load_dwordx4 %2, %4, %5, %6 offen offset:16 sc1" \
                            : "=&{v[224:225]}"(kk), "=&{v[208:211]}"(h0), "=&{v[212:215]}"(h1) : "v"(ko), "v"(ho), "s"(k.ws), "s"(((spec >> 1) << 14) + ((spec & 1) << 12)) : "memory")
#define KEYS_WAIT(N) asm volatile("s_waitcnt vmcnt(%3)" : "+{v[224:225]}"(kk), "+{v[208:211]}"(h0), "+{v[212:215]}"(h1) : "i"(N) : "memory")
#define PERMS() do { \
      u32x32 gt; gate_read(gt, iseq<32>{}); \
      _Pragma("unroll") for (int rp = 0; rp < 4; ++rp) _Pragma("unroll") for (int e = 0; e < 4; ++e) { \
          const unsigned u0 = UP[8 * rp + e], u1 = UP[8 * rp + 4 + e]; \
          UP[8 * rp + e] = plo(u0, u1); UP[8 * rp + 4 + e] = phi(u0, u1); \
          A3[8 * rp + e] = __uint_as_float(plo(gt[8 * rp + e], gt[8 * rp + 4 + e])); A3[8 * rp + 4 + e] = __uint_as_float(phi(gt[8 * rp + e], gt[8 * rp + 4 + e])); } \
      _Pragma("unroll") for (int rp = 0; rp < 8; ++rp) _Pragma("unroll") for (int e = 0; e < 2; ++e) { \
          const unsigned u0 = DN[4 * rp + e], u1 = DN[4 * rp + 2 + e]; DN[4 * rp + e] = plo(u0, u1); DN[4 * rp + 2 + e] = phi(u0, u1); } \
      asm volatile("" : "+v"(UP), "+v"(DN), "+v"(A3)); } while (0)
  {
    unsigned spins = 0;
    STATS();
    GAP();
    HOP_PAGE(1);
    GAP();
    PERMS();
#pragma unroll
    for (int i = 0; i < 16; ++i) { xq[i >> 2][i & 3] = up2(xp[i]); asm volatile("" : "+v"(xq[i >> 2][i & 3])); }
    while (!__all((own0 | (tags2(a0, tag) & tags2(b0, tag))) & (own1 | (tags2(a1, tag) & tags2(b1, tag))))) {
      if (spin_fail<SL1>(spins, k.ws, tag)) break;
      STATS();
    }
    const f32x4 s0 = own0 ? so : f32x4{__uint_as_float(a0[0]), __uint_as_float(a0[2]), __uint_as_float(b0[0]), __uint_as_float(b0[2])};
    const f32x4 s1 = own1 ? so : f32x4{__uint_as_float(a1[0]), __uint_as_float(a1[2]), __uint_as_float(b1[0]), __uint_as_float(b1[2])};
    lds_st(sc + 4096 + wid * 1024 + lane * 16, s0 + s1);
    BARRIER_LGKM();
    KEY1();
    GAP();
    STAMPO(21);
    f32x4 p[4];
#pragma unroll
    for (int w = 0; w < 4; ++w) p[w] = lds_ld<f32x4>(sc + 4096 + w * 1024 + lane * 16);
    const f32x4 s = (p[0] + p[1]) + (p[2] + p[3]);
    const float mu = s[0] * (1.f / DM);
    rstd = rsqrtf(fmaxf(fmaf(-mu, mu, s[1] * (1.f / DM)), 0.f) + 1e-5f);
    const float cw = __uint_as_float(__builtin_amdgcn_readlane(__float_as_uint(s[3]), 0)), bb = __uint_as_float(__builtin_amdgcn_readlane(__float_as_uint(s[3]), 1));
    const float r = fmaf(rstd, fmaf(-mu, cw, s[2]), bb);
    unsigned kb = cvtpk(r, r) & 0xffffu; kb = (kb & 0x8000u) ? (~kb & 0xffffu) : (kb | 0x8000u);
    gbest = rfl(wave_maxu((kb << 16) | (unsigned)(S - 1 - (grp * 64 + lane))));
    const unsigned rp = cblk * 4 + wid + 32 * lane, wi = rp >> 1;
    st8(k.ws, (W_GKEY + ((wi >> 5) << 14) + (((rp & 1) << 13) ^ k.nb) + (wi & 31) * 256 + grp * 8) | (lane < RC / 16 ? 0u : 0x80000000u), u32x2{gbest, tag});
    mu_d = -mu * rstd;
  }
  STAMPO(22);
  const unsigned lrow = (unsigned)key_row(gbest) & 63u;
  const unsigned oo = (grp * 64 + r16) * (DM * 2) + cblk * 256 + wid * 64 + (g & 1) * 32 + (g >> 1) * 16;
  u32x4 hp[4], ow[4]; unsigned best; bool hold, early;
  {
    float ra[4], rb_[4];
    bcast4(rstd, ra); bcast4(mu_d, rb_);
#pragma unroll
    for (int rb = 0; rb < 4; ++rb)
#pragma unroll
      for (int e = 0; e < 4; ++e) {
        const int nb = e >> 1, h = (e & 1) * 2;
        const f32x2 n = __builtin_elementwise_fma(XQ(rb, e), f32x2{ra[rb], ra[rb]}, f32x2{rb_[rb], rb_[rb]});
        const f32x2 hh = __builtin_elementwise_fma(n, f32x2{tc2[nb][h], tc2[nb][h + 1]}, f32x2{tsb[nb][h], tsb[nb][h + 1]});
        hp[rb][e] = cvtpk(hh[0], hh[1]);
      }
    u32x4 hc = hp[0], xc = {xp[0], xp[1], xp[2], xp[3]};
#pragma unroll
    for (int rb = 1; rb < 4; ++rb) if ((lrow >> 4) == (unsigned)rb) { hc = hp[rb]; xc = u32x4{xp[4 * rb], xp[4 * rb + 1], xp[4 * rb + 2], xp[4 * rb + 3]}; }
    GAP();
#define CAND(H) do { const unsigned off = ((H) && r16 == (lrow & 15) ? 0u : 0x80000000u) | ((wid * 16 + g * 2) * 8); \
    const unsigned co = (W_CANDH + ((grp >> 1) << 14) + ((grp & 1) << 12) + cblk * 512 + k.cs) + off, cx = (W_CANDX + ((grp >> 2) << 14) + (((cblk >> 2) << 13) ^ k.nb) + ((grp & 3) << 11) + (cblk & 3) * 512) + off; \
    st16(k.ws, co, u32x4{hc[0], tag, hc[1], tag}); st16(k.ws, co + 64, u32x4{hc[2], tag, hc[3], tag}); \
    st16(k.ws, co ^ 8192, u32x4{hc[0], tag, hc[1], tag}); st16(k.ws, (co ^ 8192) + 64, u32x4{hc[2], tag, hc[3], tag}); \
    st16(k.ws, cx, u32x4{xc[0], tag, xc[1], tag}); st16(k.ws, cx + 64, u32x4{xc[2], tag, xc[3], tag}); } while (0)
    asm volatile("s_waitcnt vmcnt(%1)" : "+{v[224:225]}"(kk) : "i"(TE ? 7 : 1) : "memory");
    best = max(rfl(wave_maxu(kk[1] == tag ? kk[0] : 0u)), gbest);
    hold = best == gbest;
    early = __builtin_popcountll(__ballot(kk[1] == tag)) < 2 * TB;
    spec = ((unsigned)key_row(best) >> 6) & 31u;
    KEYS();
    GAP();
    CAND(hold);
    GAP();
#pragma unroll
    for (int rb = 0; rb < 4; ++rb) {
      u32x4 op;
#pragma unroll
      for (int e = 0; e < 4; ++e) {
        const int nb = e >> 1, h = (e & 1) * 2;
        const f32x2 o = __builtin_elementwise_fma(f32x2{tgb[nb][h], tgb[nb][h + 1]}, up2(hp[rb][e]), XQ(rb, e));
        op[e] = cvtpk(o[0], o[1]);
      }
      auto s0 = __builtin_amdgcn_permlane16_swap(op[0], op[2], false, false);
      auto s1 = __builtin_amdgcn_permlane16_swap(op[1], op[3], false, false);
      ow[rb] = u32x4{s0[0], s1[0], s0[1], s1[1]};
      __builtin_amdgcn_raw_buffer_store_b128(ow[rb], srdo, (oo + rb * (16 * DM * 2)) | (!early || (hold && lrow == 16 * rb + r16) ? 0x80000000u : 0u), 0, OAUX);
    }
    GAP();
  }
  STAMPO(23);
  const unsigned fb = k.xcd * 28 + ffn_j(k);
  unsigned ga_ = sc + 8192 + wid * 128 + (lane >> 5) * 64 + (lane & 1) * 32 + ((lane >> 4) & 1) * 16, gr_ = sc + 8192 + (lane & 31) * 4, gy_ = W_YG + ((fb >> 2) << 14) + (((wid >> 1) << 13) ^ k.nb) + ((fb & 3) << 11) + (tid & 127) * 16;
  asm volatile("" : "+v"(ga_), "+v"(gr_), "+v"(gy_));
  HOP_PAGE(0);
  KEYS_WAIT(TE ? 4 : 10);
  for (unsigned spins = 0;;) {
    if ((lane & 31) == grp) kk = u32x2{gbest, tag};
    const bool kv = kk[1] == tag;
    best = rfl(wave_maxu(kv ? kk[0] : 0u));
    const unsigned w = ((unsigned)key_row(best) >> 6) & 31u;
    if (__all(kv & tags2(h0, tag) & tags2(h1, tag)) && w == spec) break;
    spec = w;
    if (spin_fail<SL2>(spins, k.ws, tag)) break;
    KEYS(); KEYS_WAIT(0);
  }
  STAMPO(24);
  const unsigned idx = (unsigned)key_row(best) & (S - 1), wgrp = idx >> 6;
  if (!ffn_idle(k)) {
    float a[16];
    const unsigned hk[4] = {h0[0], h0[2], h1[0], h1[2]};
#pragma unroll
    for (int e = 0; e < 8; ++e) {
      float sg = 0.f, su = 0.f;
#pragma unroll
      for (int rp = 0; rp < 4; ++rp) { sg = dot2(__float_as_uint(A3[8 * rp + (e & 1) * 4 + (e >> 1)]), hk[rp], sg); su = dot2(UP[8 * rp + (e & 1) * 4 + (e >> 1)], hk[rp], su); }
      a[e] = sg; a[8 + e] = su;
    }
    float b[8], cc[4];
#pragma unroll
    for (int e = 0; e < 8; ++e) b[e] = fold32(a[e], a[8 + e]);
#pragma unroll
    for (int e = 0; e < 4; ++e) cc[e] = fold16(b[e], b[4 + e]);
#pragma unroll
    for (int e = 0; e < 4; ++e) { cc[e] += dpp_f<0x128>(cc[e]); cc[e] += dpp_f<0x124>(cc[e]); cc[e] += dpp_f<0x122>(cc[e]); }
    if ((lane & 14) == 0) lds_st(ga_, f32x4{cc[0], cc[1], cc[2], cc[3]});
    BARRIER_LGKM();
    const unsigned ra = gr_;
    const float s = rbf((lds_ld<float>(ra) + lds_ld<float>(ra + 128)) + (lds_ld<float>(ra + 256) + lds_ld<float>(ra + 384)));
    auto su = __builtin_amdgcn_permlane16_swap(__float_as_uint(s), __float_as_uint(s), false, false);
    const float gg = __uint_as_float(su[0]);
    const float act = silu_bf(gg) * __uint_as_float(su[1]);
    const unsigned ap = cvtpk(act, dpp_f<0xB1>(act));
    f32x4 y = {0.f, 0.f, 0.f, 0.f};
#pragma unroll
    for (int rp = 0; rp < 8; ++rp) {
      const unsigned w = __builtin_amdgcn_readlane(ap, 2 * rp);
#pragma unroll
      for (int e = 0; e < 4; ++e) y[e] = dot2(DN[4 * rp + (e & 1) * 2 + (e >> 1)], w, y[e]);
    }
    st16(k.ws, gy_, u32x4{cvtpk(y[0], y[1]), tag, cvtpk(y[2], y[3]), tag});
  }
  STAMPO(25);
  GAP();
  if (!early || hold) {
#pragma unroll
    for (int rb = 0; rb < 4; ++rb) {
      const bool cr = lrow == 16 * rb + r16;
      __builtin_amdgcn_raw_buffer_store_b128(ow[rb], srdo, (oo + rb * (16 * DM * 2)) | ((wgrp == grp && cr) || (early && !cr) ? 0x80000000u : 0u), 0, OAUX);
    }
  }
  GAP();
  if (bid == 0 && tid == 0) st4(k.ws, W_EPOCH, tag);
  constexpr unsigned NFIN = 16, FST = 128;
  if (ffn_idle(k)) {
    const unsigned cl = 32 * k.kvh + 2 * (tid & 15), col = cblk * 128 + cl;
    const unsigned yo = W_YG + ((tid >> 5) << 14) + k.cs + (((tid >> 3) & 3) << 11) + (cblk & 3) * 512 + k.kvh * 128 + (tid & 7) * 16;
    const unsigned co = W_CANDH + ((wgrp >> 1) << 14) + k.cs + ((wgrp & 1) << 12) + col * 4, cx = W_CANDX + ((wgrp >> 2) << 14) + k.cs + ((wgrp & 3) << 11) + (col & 511) * 4;
    f32x2 gb2 = lds_ld<f32x2>(k.ldsb + TABU + k.kvh * 4096 + 2560 + 384 + (cl & 31) * 4);
    float rs = rbf(__builtin_amdgcn_rcpf(1.f + __expf(-key_val(best))));
    unsigned oaddr = idx * (DM * 2) + col * 2, fa = sc + 12288 + tid * 8, fw = sc + 12288 + wid * 128 + (lane & 7) * 16 + (lane >> 4) * 4;
    u32x4 p[7]; u32x2 hh, xx;
    asm volatile("" : "+v"(rs), "+v"(oaddr), "+v"(fa), "+v"(fw), "+v"(gb2));
    for (unsigned spins = 0;;) {
      hh = ld8(k.ws, co); xx = ld8(k.ws, cx);
#pragma unroll
      for (int r = 0; r < 7; ++r) p[r] = __builtin_amdgcn_raw_buffer_load_b128(k.ws, yo, r * 131072, 16);
      bool ok = (hh[1] == tag) & (xx[1] == tag);
#pragma unroll
      for (int r = 0; r < 7; ++r) ok &= tags2(p[r], tag);
      if (__all(ok)) break;
      if (spin_fail<SL3>(spins, k.ws, tag)) break;
    }
    f32x2 ya = up2(p[0][0]), yb = up2(p[0][2]);
#pragma unroll
    for (int r = 1; r < 7; ++r) { ya += up2(p[r][0]); yb += up2(p[r][2]); }
    float z = fold16(fold32(ya[0], yb[0]), fold32(ya[1], yb[1]));
    z += dpp_f<0x128>(z);
    if (!(lane & 8)) lds_st(fw, z);
    BARRIER_LGKM();
    if (tid < NFIN) {
      f32x2 yy = (lds_ld<f32x2>(fa) + lds_ld<f32x2>(fa + FST)) + (lds_ld<f32x2>(fa + 2 * FST) + lds_ld<f32x2>(fa + 3 * FST));
      yy = up2(cvtpk(yy[0], yy[1])) * rs;
      yy = up2(cvtpk(yy[0], yy[1])) + up2(hh[0]);
      yy = up2(cvtpk(yy[0], yy[1])) * gb2;
      yy = up2(cvtpk(yy[0], yy[1])) + up2(xx[0]);
      __builtin_amdgcn_raw_buffer_store_b32(cvtpk(yy[0], yy[1]), srdo, oaddr, 0, OAUX);
    }
  }
  STAMPO(26);
}
#define PERM 0
__global__ __launch_bounds__(256, 1) __attribute__((aligned(4096))) void k_fused(Args a) {
  Ctx k;
  const unsigned bid = PERM ? ((blockIdx.x << 3) | (blockIdx.x >> 5)) & 255u : blockIdx.x;
  unsigned t0;
  asm volatile("v_readfirstlane_b32 %0, %1" : "=s"(t0) : "v"(threadIdx.x));
  const unsigned wid = (t0 >> 6) & 3;
  k.pm = 1 - (t0 >> 16);
  k.wid = wid; k.bid = bid; k.xcd = ((bid & 3) << 1) | ((bid >> 2) & 1); k.j = bid >> 3; k.kvh = k.xcd >> 1; k.hl = k.j & 7; k.rblk = (k.xcd & 1) * 4 + (k.j >> 3);
  k.cs = (k.xcd & 1) << 13; k.nb = 0;
  STAMP_DECL;
  STAMP(0);
#define SPINTO(i) do { } while (0)
  W4C c;
  SPINTO(0);
  {
    H4 g;
    front(k, g, c STAMP_PASS);
    STAMP(8);
    SPINTO(1);
    attention(k, g, c STAMP_PASS);
  }
  SPINTO(2);
  oproj(k, c STAMP_PASS);
  SPINTO(3);
  STAMP_FLUSH();
  asm volatile(HOP_DEFS);
}

extern "C" void kernel_launch(void* const* d_in, const int* in_sizes, int n_in, void* d_out, int out_size, void* d_ws, size_t ws_size,
                              hipStream_t stream) {
  Args a;
  a.x = (const u16*)d_in[0]; a.kc = (const u16*)d_in[1]; a.vc = (const u16*)d_in[2]; a.wq = (const u16*)d_in[3]; a.wo = (const u16*)d_in[4];
  a.w_r = (const u16*)d_in[6]; a.wg = (const u16*)d_in[7]; a.wu = (const u16*)d_in[8]; a.wd = (const u16*)d_in[9]; a.c = (const u16*)d_in[10];
  a.w_mod = (const u16*)d_in[11]; a.out = (u16*)d_out; a.ws = (char*)d_ws;
  (void)hipFuncSetAttribute((const void*)k_fused, hipFuncAttributeMaxDynamicSharedMemorySize, 163840);
  k_fused<<<256, 256, 163840, stream>>>(a);
}
```

```cpp
#include <hip/hip_runtime.h>
#include <hip/hip_bf16.h>
#include <stdint.h>
#include <utility>

typedef unsigned short u16;
using bf16x8 = __attribute__((ext_vector_type(8))) short;
using s16x4  = __attribute__((ext_vector_type(4))) short;
using f32x16 = __attribute__((ext_vector_type(16))) float;
using u32x16 = __attribute__((ext_vector_type(16))) unsigned;
using f32x4  = __attribute__((ext_vector_type(4))) float;
using f32x2  = __attribute__((ext_vector_type(2))) float;
using u32x4  = __attribute__((ext_vector_type(4))) unsigned;
using u32x2  = __attribute__((ext_vector_type(2))) unsigned;
using u32x8 = __attribute__((ext_vector_type(8))) unsigned;
using f32x8 = __attribute__((ext_vector_type(8))) float;
using f32x32 = __attribute__((ext_vector_type(32))) float;
using u32x32 = __attribute__((ext_vector_type(32))) unsigned;
typedef __bf16 bf16x2_t __attribute__((ext_vector_type(2)));
#define DI __device__ __forceinline__
#define NTMASK 0
#define NT_SEL(bit) ((NTMASK >> (bit)) & 1)
#if NT_SEL(0)
#define NTX " nt"
#else
#define NTX ""
#endif
#if NT_SEL(1)
#define NTM " nt"
#else
#define NTM ""
#endif
#if NT_SEL(2)
#define NTQ " nt"
#else
#define NTQ ""
#endif
#if NT_SEL(3)
#define NTO " nt"
#else
#define NTO ""
#endif
#if NT_SEL(4)
#define NTG " nt"
#else
#define NTG ""
#endif
#if NT_SEL(5)
#define NTD " nt"
#else
#define NTD ""
#endif
#if NT_SEL(6)
#define NTT " nt"
#else
#define NTT ""
#endif
#if NT_SEL(7)
#define NTF " nt"
#else
#define NTF ""
#endif

constexpr int S = 2048, DM = 1024, HQ = 32, HKV = 4, DH = 128, SKV = 2048, WIN = 512, FF = 3584;
constexpr int QD = HQ * DH;
constexpr float QSCALE = 0.12751743082f;

DI float bf2f(u16 v) { return __uint_as_float(((unsigned)v) << 16); }
DI u16 f2bf(float x) { unsigned u = __float_as_uint(x); u += 0x7fffu + ((u >> 16) & 1u); return (u16)(u >> 16); }
DI unsigned cvtpk(float lo, float hi) { f32x2 v = {lo, hi}; bf16x2_t b = __builtin_convertvector(v, bf16x2_t); return __builtin_bit_cast(unsigned, b); }
DI float lo16f(unsigned w) { return __uint_as_float(w << 16); }
DI float hi16f(unsigned w) { return __uint_as_float(w & 0xffff0000u); }
DI float rbf(float x) { return lo16f(cvtpk(x, x)); }
DI f32x2 up2(unsigned w) { return f32x2{lo16f(w), hi16f(w)}; }
DI float dot2(unsigned a, unsigned b, float c) { return __builtin_amdgcn_fdot2_f32_bf16(__builtin_bit_cast(bf16x2_t, a), __builtin_bit_cast(bf16x2_t, b), c, false); }
DI float dot2z(unsigned a, unsigned b) { return __builtin_amdgcn_fdot2_f32_bf16(__builtin_bit_cast(bf16x2_t, a), __builtin_bit_cast(bf16x2_t, b), 0.f, false); }
DI unsigned plo(unsigned a, unsigned b) { return __builtin_amdgcn_perm(b, a, 0x05040100u); }
DI unsigned phi(unsigned a, unsigned b) { return __builtin_amdgcn_perm(b, a, 0x07060302u); }
DI unsigned rfl(unsigned x) { return __builtin_amdgcn_readfirstlane(x); }
template <int CTRL> DI float dpp_f(float v) { return __int_as_float(__builtin_amdgcn_update_dpp(0, __float_as_int(v), CTRL, 0xf, 0xf, true)); }
template <int CTRL> DI unsigned dpp_u(unsigned v) { return (unsigned)__builtin_amdgcn_update_dpp(0, (int)v, CTRL, 0xf, 0xf, true); }
DI float fold16(float a, float b) { auto r = __builtin_amdgcn_permlane16_swap(__float_as_uint(a), __float_as_uint(b), false, false); return __uint_as_float(r[0]) + __uint_as_float(r[1]); }
DI float fold32(float a, float b) { auto r = __builtin_amdgcn_permlane32_swap(__float_as_uint(a), __float_as_uint(b), false, false); return __uint_as_float(r[0]) + __uint_as_float(r[1]); }
DI float sum16(float v) { return fold16(v, v); }
DI float sum32(float v) { return fold32(v, v); }
DI float sum_row16(float v) { v += dpp_f<0xB1>(v); v += dpp_f<0x4E>(v); v += dpp_f<0x141>(v); v += dpp_f<0x140>(v); return v; }
DI float wave_sum(float v) { return sum32(sum16(sum_row16(v))); }
DI unsigned wave_maxu(unsigned v) {
  v = max(v, dpp_u<0xB1>(v)); v = max(v, dpp_u<0x4E>(v)); v = max(v, dpp_u<0x141>(v)); v = max(v, dpp_u<0x140>(v));
  { auto r = __builtin_amdgcn_permlane16_swap(v, v, false, false); v = max(r[0], r[1]); }
  { auto r = __builtin_amdgcn_permlane32_swap(v, v, false, false); v = max(r[0], r[1]); }
  return v;
}
#define GAP() __builtin_amdgcn_sched_barrier(0)
template <int N> using iseq = std::make_integer_sequence<int, N>;
template <int... Is> using ilist = std::integer_sequence<int, Is...>;
typedef __amdgpu_buffer_rsrc_t srd_t;
#define SRD3 0x00020000
DI srd_t mk_srd(const void* p, unsigned bytes) { return __builtin_amdgcn_make_buffer_rsrc((void*)p, (short)0, (int)bytes, SRD3); }
DI unsigned lane_id() { unsigned l; asm volatile("v_mbcnt_lo_u32_b32 %0, -1, 0\n\tv_mbcnt_hi_u32_b32 %0, -1, %0" : "=v"(l)); return l; }
struct Args { const u16 *x, *kc, *vc, *wq, *wo, *w_r, *wg, *wu, *wd, *c, *w_mod; u16* out; char* ws; };
typedef const __attribute__((address_space(4))) Args* argp_t;
DI argp_t args() { argp_t p = (argp_t)__builtin_amdgcn_kernarg_segment_ptr(); asm volatile("" : "+s"(p)); return p; }
#define BARRIER_LGKM() do { asm volatile("s_waitcnt lgkmcnt(0)\n\ts_barrier" ::: "memory"); } while (0)

constexpr unsigned W_EPOCH = 0x260000, W_TMO = 256;
constexpr unsigned W_FLAGH = 0x261000;
constexpr unsigned W_VOTE = 0x2000, W_PROBE = 0x4000;
constexpr unsigned W_GKEY = 0x10000;
constexpr unsigned W_FLAGR = 0x8000;
constexpr unsigned W_FLAGO = 0x20000;
constexpr unsigned W_CANDH = 0x40000;
constexpr unsigned W_STAT = 0x80000;
constexpr unsigned W_YG = 0x100000;
constexpr unsigned W_CANDX = 0x1E0000;
constexpr unsigned W_ST1 = 0x200000;
constexpr unsigned W_MODA = 0x210000;
constexpr unsigned W_MODC = 0x270000;
constexpr unsigned W_MODB = 0x220000;
constexpr unsigned W_SIL = 0x240000;
constexpr unsigned W_HT = 4u << 20;
constexpr unsigned W_OT = 8u << 20;
constexpr unsigned W_XCH = 24u << 20;
constexpr unsigned W_END = 40u << 20;
constexpr unsigned TAGM = 0x40004000u, TAGC = 0x9E3739B9u;
#define SPINCAP 262144
constexpr unsigned kSpinCap = SPINCAP;
#define SL0 1
#define SL1 1
#define SL2 1
#define SL3 1
template <int SL = 1> DI bool spin_fail(unsigned& spins, srd_t ws, unsigned tag) {
  __builtin_amdgcn_s_sleep(SL);
  if (++spins > kSpinCap) { __builtin_amdgcn_raw_buffer_store_b32(tag, ws, W_TMO, 0, 16); return true; }
  return (spins & 255u) == 0u && rfl(__builtin_amdgcn_raw_buffer_load_b32(ws, W_TMO, 0, 16)) == tag;
}
DI u32x4 ld16(srd_t ws, unsigned off) { return __builtin_amdgcn_raw_buffer_load_b128(ws, off, 0, 16); }
DI u32x2 ld8(srd_t ws, unsigned off) { return __builtin_amdgcn_raw_buffer_load_b64(ws, off, 0, 16); }
DI void st16(srd_t ws, unsigned off, u32x4 v) { __builtin_amdgcn_raw_buffer_store_b128(v, ws, off, 0, 16); }
DI void st8(srd_t ws, unsigned off, u32x2 v) { __builtin_amdgcn_raw_buffer_store_b64(v, ws, off, 0, 16); }
DI void st4(srd_t ws, unsigned off, unsigned v) { __builtin_amdgcn_raw_buffer_store_b32(v, ws, off, 0, 16); }
DI void wait_flags(srd_t ws, unsigned off, unsigned tag) {
  const unsigned o = off + (lane_id() & 31u) * 4u;
  for (unsigned spins = 0;;) {
    const unsigned f = __builtin_amdgcn_raw_buffer_load_b32(ws, o, 0, 16);
    if (__all(f == tag)) break;
    if (spin_fail(spins, ws, tag)) break;
  }
  asm volatile("" ::: "memory");
}
template <int OFF, bool DEV = false> DI u32x4 ald16(srd_t s, unsigned vo, unsigned so) {
  u32x4 r;
  if constexpr (DEV) asm volatile("buffer_load_dwordx4 %0, %1, %2, %3 offen offset:%4 sc1" : "=v"(r) : "v"(vo), "s"(s), "s"(so), "i"(OFF) : "memory");
  else               asm volatile("buffer_load_dwordx4 %0, %1, %2, %3 offen offset:%4" NTF : "=v"(r) : "v"(vo), "s"(s), "s"(so), "i"(OFF) : "memory");
  return r;
}
template <int OFF> DI u32x2 ald8(srd_t s, unsigned vo, unsigned so) {
  u32x2 r; asm volatile("buffer_load_dwordx2 %0, %1, %2, %3 offen offset:%4" : "=v"(r) : "v"(vo), "s"(s), "s"(so), "i"(OFF) : "memory"); return r;
}
DI u32x2 ald8d(srd_t s, unsigned vo) { u32x2 r; asm volatile("buffer_load_dwordx2 %0, %1, %2, 0 offen sc1" : "=v"(r) : "v"(vo), "s"(s) : "memory"); return r; }
template <int OFF, bool DEV = false> DI unsigned ald4(srd_t s, unsigned vo, unsigned so) {
  unsigned r;
  if constexpr (DEV) asm volatile("buffer_load_dword %0, %1, %2, %3 offen offset:%4 sc1" : "=v"(r) : "v"(vo), "s"(s), "s"(so), "i"(OFF) : "memory");
  else               asm volatile("buffer_load_dword %0, %1, %2, %3 offen offset:%4" : "=v"(r) : "v"(vo), "s"(s), "s"(so), "i"(OFF) : "memory");
  return r;
}
DI void touch(unsigned& d, const void* base, unsigned vo) { asm volatile("global_load_dword %0, %1, %2" : "+v"(d) : "v"(vo), "s"(base) : "memory"); }
DI void touchq(unsigned& d, const void* base, unsigned vo) { asm volatile("global_load_dword %0, %1, %2" NTQ : "+v"(d) : "v"(vo), "s"(base) : "memory"); }
template <class T> DI T lds_ld(unsigned a) { return *reinterpret_cast<const __attribute__((address_space(3))) T*>(a); }
template <class T> DI void lds_st(unsigned a, T v) { *reinterpret_cast<__attribute__((address_space(3))) T*>(a) = v; }
DI float silu_bf(float v) { return rbf(v * __builtin_amdgcn_rcpf(1.f + __expf(-v))); }
struct Ctx { static constexpr unsigned ldsb = 0; unsigned pm, bid, wid, xcd, j, kvh, hl, rblk, tag, cs, nb; srd_t ws; };
#define TAIL2 1
DI unsigned ffn_j(const Ctx& k) { return TAIL2 ? (k.j < 24 ? k.j : 24 + (k.j & 3)) : min(k.j, 27u); }
DI bool ffn_idle(const Ctx& k) { return TAIL2 ? (k.j >> 2) == 6 + (k.xcd & 1) : k.j >= 28; }
constexpr unsigned UNIT = 16384;
#define STAMPA(I) do { } while (0)
#define STAMPO(I) do { } while (0)
#define STAMP_DECL
#define STAMP(I) do { } while (0)
#define STAMP_FLUSH() do { } while (0)
#define STAMP_ARG
#define STAMP_PASS
#define ACLOB_LO "a0","a1","a2","a3","a4","a5","a6","a7","a8","a9","a10","a11","a12","a13","a14","a15","a16","a17","a18","a19","a20","a21","a22","a23","a24","a25","a26","a27","a28","a29","a30","a31","a32","a33","a34","a35","a36","a37","a38","a39","a40","a41","a42","a43","a44","a45","a46","a47","a48","a49","a50","a51","a52","a53","a54","a55","a56","a57","a58","a59","a60","a61","a62","a63","a64","a65","a66","a67","a68","a69","a70","a71","a72","a73","a74","a75","a76","a77","a78","a79","a80","a81","a82","a83","a84","a85","a86","a87","a88","a89","a90","a91","a92","a93","a94","a95","a96","a97","a98","a99","a100","a101","a102","a103","a104","a105","a106","a107","a108","a109","a110","a111","a112","a113","a114","a115","a116","a117","a118","a119","a120","a121","a122","a123","a124","a125","a126","a127"
#define ACLOB_HI "a128","a129","a130","a131","a132","a133","a134","a135","a136","a137","a138","a139","a140","a141","a142","a143","a144","a145","a146","a147","a148","a149","a150","a151","a152","a153","a154","a155","a156","a157","a158","a159","a160","a161","a162","a163","a164","a165","a166","a167","a168","a169","a170","a171","a172","a173","a174","a175","a176","a177","a178","a179","a180","a181","a182","a183","a184","a185","a186","a187","a188","a189","a190","a191","a192","a193","a194","a195","a196","a197","a198","a199","a200","a201","a202","a203","a204","a205","a206","a207","a208","a209","a210","a211","a212","a213","a214","a215","a216","a217","a218","a219","a220","a221","a222","a223","a224","a225","a226","a227","a228","a229","a230","a231","a232","a233","a234","a235","a236","a237","a238","a239","a240","a241","a242","a243","a244","a245","a246","a247","a248","a249","a250","a251","a252","a253","a254","a255"
#define HOP_DEFS ".set .Lhc00, .Lhs0\n\t.set .Lhs0n, .Lhs4\n\t.set .Lhs4n, .Lhs8\n\t.set .Lhs8n, .Lhr0\n\t.set .Lhc01, .Lhs1\n\t.set .Lhs1n, .Lhs5\n\t.set .Lhs5n, .Lhs9\n\t.set .Lhs9n, .Lhr0\n\t.set .Lhc02, .Lhs2\n\t.set .Lhs2n, .Lhs6\n\t.set .Lhs6n, .Lhs10\n\t.set .Lhs10n, .Lhr0\n\t.set .Lhc03, .Lhs3\n\t.set .Lhs3n, .Lhs7\n\t.set .Lhs7n, .Lhr0\n\t.set .Lhc10, .Lhs11\n\t.set .Lhs11n, .Lhs15\n\t.set .Lhs15n, .Lhr1\n\t.set .Lhc11, .Lhs12\n\t.set .Lhs12n, .Lht0\n\t.set .Lht0n, .Lhr1\n\t.set .Lhc12, .Lhs13\n\t.set .Lhs13n, .Lht1\n\t.set .Lht1n, .Lhr1\n\t.set .Lhc13, .Lhs14\n\t.set .Lhs14n, .Lhr1\n\t.set .Lhc20, .Lhf\n\t.set .Lhfn, .Lhr2\n\t.set .Lhc21, .Lhr2\n\t.set .Lhc22, .Lhr2\n\t.set .Lhc23, .Lhr2"
#define HOP_PAGE(n) asm volatile("s_branch .Lht" #n "\n\t.p2align 12\n.Lht" #n ":\n\ts_cbranch_execz .Lht" #n "n")
#define PAGE_ALIGN() asm volatile("s_branch .Lpa%=\n\t.p2align 12\n.Lpa%=:" ::: "memory")
#define HOP_FRONT() asm volatile("s_branch .Lhf\n\t.p2align 12\n.Lhf:\n\ts_cbranch_execz .Lhfn")
#define HOP_GO(g, w) asm volatile("s_mov_b64 exec, 0\n\ts_cmp_eq_u32 %0, 0\n\ts_cbranch_scc1 .Lhc" #g "0\n\ts_cmp_eq_u32 %0, 1\n\ts_cbranch_scc1 .Lhc" #g "1\n\ts_cmp_eq_u32 %0, 2\n\ts_cbranch_scc1 .Lhc" #g "2\n\ts_branch .Lhc" #g "3\n.Lhr" #g ":\n\ts_mov_b64 exec, -1" :: "s"(w) : "scc", "memory")
#define STP(x)
#define OP_ASM \
  "s_waitcnt lgkmcnt(0)\n\t" \
  "s_barrier\n\t" \
  STP("s_memrealtime s[94:95]\n\t") \
  STP("s_memtime s[92:93]\n\t") \
  "ds_read_b32 v228, v213 offset:49152\n\t" \
  "v_mfma_f32_16x16x32_bf16 a[8:11], a[140:143], v[76:79], 0\n\t" \
  "v_add_u32 v238, 14336, v240\n\t" \
  "v_add_u32 v239, 21504, v241\n\t" \
  "v_mfma_f32_16x16x32_bf16 a[8:11], a[136:139], v[72:75], a[8:11]\n\t" \
  "ds_read_b128 a[12:15], v242 offset:32768\n\t" \
  "v_add_u32 v241, 7168, v241\n\t" \
  "v_mfma_f32_16x16x32_bf16 a[8:11], a[132:135], v[68:71], a[8:11]\n\t" \
  "s_mov_b32 s79, 0x05040100\n\t" \
  "ds_read_b128 a[28:31], v242 offset:33792\n\t" \
  "s_mov_b32 s80, 0x07060302\n\t" \
  "v_mfma_f32_16x16x32_bf16 a[8:11], a[128:131], v[64:67], a[8:11]\n\t" \
  "ds_read_b128 a[44:47], v242 offset:34816\n\t" \
  "v_mfma_f32_16x16x32_bf16 a[24:27], a[144:147], v[64:67], 0\n\t" \
  "ds_read_b128 a[60:63], v242 offset:35840\n\t" \
  "v_mfma_f32_16x16x32_bf16 a[24:27], a[148:151], v[68:71], a[24:27]\n\t" \
  "v_mfma_f32_16x16x32_bf16 a[24:27], a[152:155], v[72:75], a[24:27]\n\t" \
  "s_waitcnt vmcnt(14)\n\t" \
  "buffer_store_dword v243, v254, %[ws], %[fw2] offen offset:32 sc1\n\t" \
  "v_mfma_f32_16x16x32_bf16 a[24:27], a[156:159], v[76:79], a[24:27]\n\t" \
  "ds_read_b128 a[76:79], v242 offset:36864\n\t" \
  "v_mfma_f32_16x16x32_bf16 a[104:107], a[172:175], v[76:79], 0\n\t" \
  "ds_read_b128 a[92:95], v242 offset:37888\n\t" \
  "v_mfma_f32_16x16x32_bf16 a[104:107], a[168:171], v[72:75], a[104:107]\n\t" \
  "ds_read_b128 a[108:111], v242 offset:38912\n\t" \
  "s_waitcnt lgkmcnt(7)\n\t" \
  "v_cmp_eq_u32 vcc, %[tag], v228\n\t" \
  "s_and_b32 vcc_lo, vcc_lo, %[pm]\n\t" \
  "s_cbranch_scc1 .Lop_rd\n\t" \
  "v_mfma_f32_16x16x32_bf16 a[104:107], a[164:167], v[68:71], a[104:107]\n\t" \
  "ds_read_b128 a[124:127], v242 offset:39936\n\t" \
  "v_mfma_f32_16x16x32_bf16 a[104:107], a[160:163], v[64:67], a[104:107]\n\t" \
  "ds_read_b128 a[208:211], v242 offset:40960\n\t" \
  "v_mfma_f32_16x16x32_bf16 a[120:123], a[176:179], v[64:67], 0\n\t" \
  "buffer_load_dword v246, v245, %[ws], %[fw2] offen sc1\n\t" \
  "v_mfma_f32_16x16x32_bf16 a[120:123], a[180:183], v[68:71], a[120:123]\n\t" \
  "ds_read_b128 a[212:215], v242 offset:41984\n\t" \
  "v_mfma_f32_16x16x32_bf16 a[120:123], a[184:187], v[72:75], a[120:123]\n\t" \
  "ds_read_b128 a[216:219], v242 offset:43008\n\t" \
  "v_mfma_f32_16x16x32_bf16 a[120:123], a[188:191], v[76:79], a[120:123]\n\t" \
  "s_xor_b32 s73, %[eso], 131072\n\t" \
  "ds_read_b128 a[220:223], v242 offset:44032\n\t" \
  "v_mfma_f32_16x16x32_bf16 a[88:91], v[108:111], v[76:79], 0\n\t" \
  "s_add_u32 m0, %[m], 0\n\t" \
  "ds_read_b128 a[192:195], v242 offset:45056\n\t" \
  "v_mfma_f32_16x16x32_bf16 a[88:91], v[104:107], v[72:75], a[88:91]\n\t" \
  "ds_read_b128 a[196:199], v242 offset:46080\n\t" \
  "v_mfma_f32_16x16x32_bf16 a[88:91], v[100:103], v[68:71], a[88:91]\n\t" \
  "ds_read_b128 a[200:203], v242 offset:47104\n\t" \
  "ds_read_b128 a[204:207], v242 offset:48128\n\t" \
  "v_mfma_f32_16x16x32_bf16 a[88:91], v[96:99], v[64:67], a[88:91]\n\t" \
  "s_waitcnt lgkmcnt(12)\n\t" \
  "s_mov_b64 exec, 0\n\t" \
  "s_cmp_eq_u32 %[wid], 0\n\t" \
  "s_cbranch_scc1 .Lhc10\n\t" \
  "s_cmp_eq_u32 %[wid], 1\n\t" \
  "s_cbranch_scc1 .Lhc11\n\t" \
  "s_cmp_eq_u32 %[wid], 2\n\t" \
  "s_cbranch_scc1 .Lhc12\n\t" \
  "s_branch .Lhc13\n\t" \
  ".Lhr1:\n\t" \
  "s_mov_b64 exec, -1\n\t" \
  "s_waitcnt vmcnt(6)\n\t" \
  "s_waitcnt vmcnt(0)\n\t" \
  "v_cmp_ne_u32 vcc, %[tag], v246\n\t" \
  "s_and_b32 vcc_lo, vcc_lo, 0x6\n\t" \
  "s_cbranch_scc1 .Lop_s0\n\t" \
  ".Lop_k0:\n\t" \
  "buffer_load_dwordx4 v[16:19], v242, %[ws], s73 offen offset:0 sc1\n\t" \
  "v_perm_b32 v248, v132, v128, s79\n\t" \
  "v_perm_b32 v128, v132, v128, s80\n\t" \
  "v_dot2_f32_bf16 v132, v248, v224, 0\n\t" \
  "v_dot2_f32_bf16 v128, v128, v224, 0\n\t" \
  "v_perm_b32 v249, v133, v129, s79\n\t" \
  "v_perm_b32 v129, v133, v129, s80\n\t" \
  "v_dot2_f32_bf16 v133, v249, v224, 0\n\t" \
  "v_dot2_f32_bf16 v129, v129, v224, 0\n\t" \
  "buffer_load_dwordx4 v[20:23], v242, %[ws], s73 offen offset:1024 sc1\n\t" \
  "v_perm_b32 v248, v134, v130, s79\n\t" \
  "v_perm_b32 v130, v134, v130, s80\n\t" \
  "v_dot2_f32_bf16 v134, v248, v224, 0\n\t" \
  "v_dot2_f32_bf16 v130, v130, v224, 0\n\t" \
  "v_perm_b32 v249, v135, v131, s79\n\t" \
  "v_perm_b32 v131, v135, v131, s80\n\t" \
  "v_dot2_f32_bf16 v135, v249, v224, 0\n\t" \
  "v_dot2_f32_bf16 v131, v131, v224, 0\n\t" \
  "buffer_load_dwordx4 v[24:27], v242, %[ws], s73 offen offset:2048 sc1\n\t" \
  "v_perm_b32 v248, v140, v136, s79\n\t" \
  "v_perm_b32 v136, v140, v136, s80\n\t" \
  "v_dot2_f32_bf16 v132, v248, v225, v132\n\t" \
  "v_dot2_f32_bf16 v128, v136, v225, v128\n\t" \
  "v_perm_b32 v249, v141, v137, s79\n\t" \
  "v_perm_b32 v137, v141, v137, s80\n\t" \
  "v_dot2_f32_bf16 v133, v249, v225, v133\n\t" \
  "v_dot2_f32_bf16 v129, v137, v225, v129\n\t" \
  "buffer_load_dwordx4 v[28:31], v242, %[ws], s73 offen offset:3072 sc1\n\t" \
  "v_perm_b32 v248, v142, v138, s79\n\t" \
  "v_perm_b32 v138, v142, v138, s80\n\t" \
  "v_dot2_f32_bf16 v134, v248, v225, v134\n\t" \
  "v_dot2_f32_bf16 v130, v138, v225, v130\n\t" \
  "v_perm_b32 v249, v143, v139, s79\n\t" \
  "v_perm_b32 v139, v143, v139, s80\n\t" \
  "v_dot2_f32_bf16 v135, v249, v225, v135\n\t" \
  "v_dot2_f32_bf16 v131, v139, v225, v131\n\t" \
  "s_xor_b32 s73, %[eso], 163840\n\t" \
  "buffer_load_dwordx4 v[0:3], v242, %[ws], s73 offen offset:0 sc1\n\t" \
  "v_perm_b32 v248, v148, v144, s79\n\t" \
  "v_perm_b32 v144, v148, v144, s80\n\t" \
  "v_dot2_f32_bf16 v132, v248, v226, v132\n\t" \
  "v_dot2_f32_bf16 v128, v144, v226, v128\n\t" \
  "v_perm_b32 v249, v149, v145, s79\n\t" \
  "v_perm_b32 v145, v149, v145, s80\n\t" \
  "v_dot2_f32_bf16 v133, v249, v226, v133\n\t" \
  "v_dot2_f32_bf16 v129, v145, v226, v129\n\t" \
  "buffer_load_dwordx4 v[4:7], v242, %[ws], s73 offen offset:1024 sc1\n\t" \
  "v_perm_b32 v248, v150, v146, s79\n\t" \
  "v_perm_b32 v146, v150, v146, s80\n\t" \
  "v_dot2_f32_bf16 v134, v248, v226, v134\n\t" \
  "v_dot2_f32_bf16 v130, v146, v226, v130\n\t" \
  "v_perm_b32 v249, v151, v147, s79\n\t" \
  "v_perm_b32 v147, v151, v147, s80\n\t" \
  "v_dot2_f32_bf16 v135, v249, v226, v135\n\t" \
  "v_dot2_f32_bf16 v131, v147, v226, v131\n\t" \
  "buffer_load_dwordx4 v[8:11], v242, %[ws], s73 offen offset:2048 sc1\n\t" \
  "v_perm_b32 v248, v156, v152, s79\n\t" \
  "v_perm_b32 v152, v156, v152, s80\n\t" \
  "v_dot2_f32_bf16 v132, v248, v227, v132\n\t" \
  "v_dot2_f32_bf16 v128, v152, v227, v128\n\t" \
  "v_perm_b32 v249, v157, v153, s79\n\t" \
  "v_perm_b32 v153, v157, v153, s80\n\t" \
  "v_dot2_f32_bf16 v133, v249, v227, v133\n\t" \
  "v_dot2_f32_bf16 v129, v153, v227, v129\n\t" \
  "buffer_load_dwordx4 v[12:15], v242, %[ws], s73 offen offset:3072 sc1\n\t" \
  "v_perm_b32 v248, v158, v154, s79\n\t" \
  "v_perm_b32 v154, v158, v154, s80\n\t" \
  "v_dot2_f32_bf16 v134, v248, v227, v134\n\t" \
  "v_dot2_f32_bf16 v130, v154, v227, v130\n\t" \
  "v_perm_b32 v249, v159, v155, s79\n\t" \
  "v_perm_b32 v155, v159, v155, s80\n\t" \
  "v_dot2_f32_bf16 v135, v249, v227, v135\n\t" \
  "v_dot2_f32_bf16 v131, v155, v227, v131\n\t" \
  "v_mfma_f32_16x16x32_bf16 v[96:99], a[128:131], a[12:15], 0\n\t" \
  "s_xor_b32 s73, %[eso], 262144\n\t" \
  "s_xor_b32 s71, %[wob], 1048576\n\t" \
  "v_mfma_f32_16x16x32_bf16 v[96:99], a[132:135], a[28:31], v[96:99]\n\t" \
  "v_mfma_f32_16x16x32_bf16 v[96:99], a[136:139], a[44:47], v[96:99]\n\t" \
  "v_mfma_f32_16x16x32_bf16 v[96:99], a[140:143], a[60:63], v[96:99]\n\t" \
  "v_mfma_f32_16x16x32_bf16 v[112:115], a[156:159], a[60:63], 0\n\t" \
  "buffer_load_dwordx4 v[48:51], v242, %[ws], s73 offen offset:0 sc1\n\t" \
  "v_mfma_f32_16x16x32_bf16 v[112:115], a[152:155], a[44:47], v[112:115]\n\t" \
  "v_mfma_f32_16x16x32_bf16 v[112:115], a[148:151], a[28:31], v[112:115]\n\t" \
  "v_mfma_f32_16x16x32_bf16 v[112:115], a[144:147], a[12:15], v[112:115]\n\t" \
  "s_waitcnt lgkmcnt(8)\n\t" \
  "v_mfma_f32_16x16x32_bf16 v[100:103], a[128:131], a[76:79], 0\n\t" \
  "buffer_load_dwordx4 v[52:55], v242, %[ws], s73 offen offset:1024 sc1\n\t" \
  "v_mfma_f32_16x16x32_bf16 v[100:103], a[132:135], a[92:95], v[100:103]\n\t" \
  "v_mfma_f32_16x16x32_bf16 v[100:103], a[136:139], a[108:111], v[100:103]\n\t" \
  "v_mfma_f32_16x16x32_bf16 v[100:103], a[140:143], a[124:127], v[100:103]\n\t" \
  "v_mfma_f32_16x16x32_bf16 v[116:119], a[156:159], a[124:127], 0\n\t" \
  "v_mfma_f32_16x16x32_bf16 v[116:119], a[152:155], a[108:111], v[116:119]\n\t" \
  "buffer_load_dwordx4 v[56:59], v242, %[ws], s73 offen offset:2048 sc1\n\t" \
  "v_mfma_f32_16x16x32_bf16 v[116:119], a[148:151], a[92:95], v[116:119]\n\t" \
  "v_mfma_f32_16x16x32_bf16 v[116:119], a[144:147], a[76:79], v[116:119]\n\t" \
  "s_waitcnt lgkmcnt(4)\n\t" \
  "v_mfma_f32_16x16x32_bf16 v[104:107], a[128:131], a[208:211], 0\n\t" \
  "v_xor_b32 v236, 0xc000, v234\n\t" \
  "v_mfma_f32_16x16x32_bf16 v[104:107], a[132:135], a[212:215], v[104:107]\n\t" \
  "buffer_load_dwordx4 v[60:63], v242, %[ws], s73 offen offset:3072 sc1\n\t" \
  "s_xor_b32 s73, %[eso], 294912\n\t" \
  "v_mfma_f32_16x16x32_bf16 v[104:107], a[136:139], a[216:219], v[104:107]\n\t" \
  "ds_read_b64_tr_b16 a[160:161], v236 offset:0\n\t" \
  "v_mfma_f32_16x16x32_bf16 v[104:107], a[140:143], a[220:223], v[104:107]\n\t" \
  "ds_read_b64_tr_b16 a[162:163], v236 offset:4096\n\t" \
  "v_mfma_f32_16x16x32_bf16 v[120:123], a[156:159], a[220:223], 0\n\t" \
  "ds_read_b64_tr_b16 a[164:165], v236 offset:8192\n\t" \
  "v_mfma_f32_16x16x32_bf16 v[120:123], a[152:155], a[216:219], v[120:123]\n\t" \
  "ds_read_b64_tr_b16 a[166:167], v236 offset:12288\n\t" \
  "v_mfma_f32_16x16x32_bf16 v[120:123], a[148:151], a[212:215], v[120:123]\n\t" \
  "buffer_load_dwordx4 v[80:83], v242, %[ws], s73 offen offset:0 sc1\n\t" \
  "v_mfma_f32_16x16x32_bf16 v[120:123], a[144:147], a[208:211], v[120:123]\n\t" \
  "s_waitcnt lgkmcnt(4)\n\t" \
  "ds_read_b64_tr_b16 a[168:169], v236 offset:16384\n\t" \
  "v_mfma_f32_16x16x32_bf16 v[108:111], a[128:131], a[192:195], 0\n\t" \
  "ds_read_b64_tr_b16 a[170:171], v236 offset:20480\n\t" \
  "v_mfma_f32_16x16x32_bf16 v[108:111], a[132:135], a[196:199], v[108:111]\n\t" \
  "ds_read_b64_tr_b16 a[172:173], v236 offset:24576\n\t" \
  "v_mfma_f32_16x16x32_bf16 v[108:111], a[136:139], a[200:203], v[108:111]\n\t" \
  "buffer_load_dwordx4 v[84:87], v242, %[ws], s73 offen offset:1024 sc1\n\t" \
  "v_mfma_f32_16x16x32_bf16 v[108:111], a[140:143], a[204:207], v[108:111]\n\t" \
  "ds_read_b64_tr_b16 a[174:175], v236 offset:28672\n\t" \
  "v_xor_b32 v237, 0xc020, v234\n\t" \
  "v_mfma_f32_16x16x32_bf16 v[124:127], a[156:159], a[204:207], 0\n\t" \
  "ds_read_b64_tr_b16 a[176:177], v237 offset:0\n\t" \
  "v_mfma_f32_16x16x32_bf16 v[124:127], a[152:155], a[200:203], v[124:127]\n\t" \
  "ds_read_b64_tr_b16 a[178:179], v237 offset:4096\n\t" \
  "v_mfma_f32_16x16x32_bf16 v[124:127], a[148:151], a[196:199], v[124:127]\n\t" \
  "buffer_load_dwordx4 v[88:91], v242, %[ws], s73 offen offset:2048 sc1\n\t" \
  "v_mfma_f32_16x16x32_bf16 v[124:127], a[144:147], a[192:195], v[124:127]\n\t" \
  "s_waitcnt vmcnt(11) lgkmcnt(6)\n\t" \
  "ds_read_b64_tr_b16 a[180:181], v237 offset:8192\n\t" \
  "ds_read_b64_tr_b16 a[182:183], v237 offset:12288\n\t" \
  "buffer_load_dword v246, v245, %[ws], %[fw2] offen sc1\n\t" \
  STP("v_writelane_b32 %[stv], s94, 24\n\t") \
  STP("v_writelane_b32 %[stv], s92, 25\n\t") \
  STP("s_memrealtime s[94:95]\n\t") \
  STP("s_memtime s[92:93]\n\t") \
  "v_mfma_f32_16x16x32_bf16 a[0:3], a[160:163], v[16:19], a[0:3]\n\t" \
  "ds_read_b64_tr_b16 a[184:185], v237 offset:16384\n\t" \
  "v_mfma_f32_16x16x32_bf16 a[0:3], a[164:167], v[20:23], a[0:3]\n\t" \
  "s_waitcnt lgkmcnt(5)\n\t" \
  "ds_read_b64_tr_b16 a[186:187], v237 offset:20480\n\t" \
  "v_mfma_f32_16x16x32_bf16 a[0:3], a[168:171], v[24:27], a[0:3]\n\t" \
  "ds_read_b64_tr_b16 a[188:189], v237 offset:24576\n\t" \
  "v_mfma_f32_16x16x32_bf16 a[0:3], a[172:175], v[28:31], a[0:3]\n\t" \
  "s_waitcnt vmcnt(8)\n\t" \
  "ds_read_b64_tr_b16 a[190:191], v237 offset:28672\n\t" \
  "v_xor_b32 v235, 0xc040, v234\n\t" \
  "v_mfma_f32_16x16x32_bf16 a[4:7], a[172:175], v[12:15], a[4:7]\n\t" \
  "buffer_load_dwordx4 v[92:95], v242, %[ws], s73 offen offset:3072 sc1\n\t" \
  "v_mfma_f32_16x16x32_bf16 a[4:7], a[168:171], v[8:11], a[4:7]\n\t" \
  "ds_read_b64_tr_b16 a[128:129], v235 offset:0\n\t" \
  "ds_read_b64_tr_b16 a[130:131], v235 offset:4096\n\t" \
  "v_mfma_f32_16x16x32_bf16 a[4:7], a[164:167], v[4:7], a[4:7]\n\t" \
  "ds_read_b64_tr_b16 a[132:133], v235 offset:8192\n\t" \
  "v_mfma_f32_16x16x32_bf16 a[4:7], a[160:163], v[0:3], a[4:7]\n\t" \
  "s_waitcnt lgkmcnt(7)\n\t" \
  "ds_read_b64_tr_b16 a[134:135], v235 offset:12288\n\t" \
  "v_mfma_f32_16x16x32_bf16 a[16:19], a[176:179], v[16:19], a[16:19]\n\t" \
  "buffer_load_dwordx4 v240, %[wo], s71 offen offset:0 lds\n\t" \
  "v_mfma_f32_16x16x32_bf16 a[16:19], a[180:183], v[20:23], a[16:19]\n\t" \
  "s_waitcnt lgkmcnt(4)\n\t" \
  "ds_read_b64_tr_b16 a[136:137], v235 offset:16384\n\t" \
  "ds_read_b64_tr_b16 a[138:139], v235 offset:20480\n\t" \
  "v_mfma_f32_16x16x32_bf16 a[16:19], a[184:187], v[24:27], a[16:19]\n\t" \
  "ds_read_b64_tr_b16 a[140:141], v235 offset:24576\n\t" \
  "v_mfma_f32_16x16x32_bf16 a[16:19], a[188:191], v[28:31], a[16:19]\n\t" \
  "ds_read_b64_tr_b16 a[142:143], v235 offset:28672\n\t" \
  "v_xor_b32 v236, 0xc060, v234\n\t" \
  "v_mfma_f32_16x16x32_bf16 a[20:23], a[188:191], v[12:15], a[20:23]\n\t" \
  "ds_read_b64_tr_b16 a[160:161], v236 offset:0\n\t" \
  "v_mfma_f32_16x16x32_bf16 a[20:23], a[184:187], v[8:11], a[20:23]\n\t" \
  "buffer_load_dwordx4 v241, %[wo], s71 offen offset:1024 lds\n\t" \
  "v_mfma_f32_16x16x32_bf16 a[20:23], a[180:183], v[4:7], a[20:23]\n\t" \
  "ds_read_b64_tr_b16 a[162:163], v236 offset:4096\n\t" \
  "ds_read_b64_tr_b16 a[164:165], v236 offset:8192\n\t" \
  "v_mfma_f32_16x16x32_bf16 a[20:23], a[176:179], v[0:3], a[20:23]\n\t" \
  "s_waitcnt lgkmcnt(7)\n\t" \
  "ds_read_b64_tr_b16 a[166:167], v236 offset:12288\n\t" \
  "v_mfma_f32_16x16x32_bf16 a[32:35], a[128:131], v[16:19], a[32:35]\n\t" \
  "s_xor_b32 s73, %[eso], 327680\n\t" \
  "ds_read_b64_tr_b16 a[168:169], v236 offset:16384\n\t" \
  "v_mfma_f32_16x16x32_bf16 a[32:35], a[132:135], v[20:23], a[32:35]\n\t" \
  "s_waitcnt lgkmcnt(5)\n\t" \
  "buffer_load_dwordx4 v238, %[wo], s71 offen offset:2048 lds\n\t" \
  "v_mfma_f32_16x16x32_bf16 a[32:35], a[136:139], v[24:27], a[32:35]\n\t" \
  "ds_read_b64_tr_b16 a[170:171], v236 offset:20480\n\t" \
  "ds_read_b64_tr_b16 a[172:173], v236 offset:24576\n\t" \
  "v_mfma_f32_16x16x32_bf16 a[32:35], a[140:143], v[28:31], a[32:35]\n\t" \
  "ds_read_b64_tr_b16 a[174:175], v236 offset:28672\n\t" \
  "v_xor_b32 v237, 0xc080, v234\n\t" \
  "v_mfma_f32_16x16x32_bf16 a[36:39], a[140:143], v[12:15], a[36:39]\n\t" \
  "ds_read_b64_tr_b16 a[176:177], v237 offset:0\n\t" \
  "v_mfma_f32_16x16x32_bf16 a[36:39], a[136:139], v[8:11], a[36:39]\n\t" \
  "ds_read_b64_tr_b16 a[178:179], v237 offset:4096\n\t" \
  "v_mfma_f32_16x16x32_bf16 a[36:39], a[132:135], v[4:7], a[36:39]\n\t" \
  "buffer_load_dwordx4 v239, %[wo], s71 offen offset:3072 lds\n\t" \
  "s_add_u32 m0, %[m], 16384\n\t" \
  "v_mfma_f32_16x16x32_bf16 a[36:39], a[128:131], v[0:3], a[36:39]\n\t" \
  "s_waitcnt lgkmcnt(6)\n\t" \
  "s_xor_b32 s71, %[wob], 1179648\n\t" \
  "ds_read_b64_tr_b16 a[180:181], v237 offset:8192\n\t" \
  "v_mfma_f32_16x16x32_bf16 a[48:51], a[160:163], v[16:19], a[48:51]\n\t" \
  "ds_read_b64_tr_b16 a[182:183], v237 offset:12288\n\t" \
  "ds_read_b64_tr_b16 a[184:185], v237 offset:16384\n\t" \
  "v_mfma_f32_16x16x32_bf16 a[48:51], a[164:167], v[20:23], a[48:51]\n\t" \
  "s_waitcnt lgkmcnt(5)\n\t" \
  ".Lhs11:\n\ts_cbranch_execz .Lhs11n\n\t" \
  "ds_read_b64_tr_b16 a[186:187], v237 offset:20480\n\t" \
  "s_waitcnt vmcnt(5)\n\t" \
  "v_cmp_ne_u32 vcc, %[tag], v246\n\t" \
  "s_and_b32 vcc_lo, vcc_lo, 0xe00\n\t" \
  "s_cbranch_scc1 .Lop_s1\n\t" \
  ".Lop_k1:\n\t" \
  "v_mfma_f32_16x16x32_bf16 a[48:51], a[168:171], v[24:27], a[48:51]\n\t" \
  "buffer_load_dwordx4 v[64:67], v242, %[ws], s73 offen offset:0 sc1\n\t" \
  "v_mfma_f32_16x16x32_bf16 a[48:51], a[172:175], v[28:31], a[48:51]\n\t" \
  "ds_read_b64_tr_b16 a[188:189], v237 offset:24576\n\t" \
  "ds_read_b64_tr_b16 a[190:191], v237 offset:28672\n\t" \
  "v_mfma_f32_16x16x32_bf16 a[52:55], a[172:175], v[12:15], a[52:55]\n\t" \
  "v_xor_b32 v235, 0xc0a0, v234\n\t" \
  "v_mfma_f32_16x16x32_bf16 a[52:55], a[168:171], v[8:11], a[52:55]\n\t" \
  "ds_read_b64_tr_b16 a[144:145], v235 offset:0\n\t" \
  "ds_read_b64_tr_b16 a[146:147], v235 offset:4096\n\t" \
  "v_mfma_f32_16x16x32_bf16 a[52:55], a[164:167], v[4:7], a[52:55]\n\t" \
  "buffer_load_dwordx4 v[68:71], v242, %[ws], s73 offen offset:1024 sc1\n\t" \
  "v_mfma_f32_16x16x32_bf16 a[52:55], a[160:163], v[0:3], a[52:55]\n\t" \
  "s_waitcnt lgkmcnt(6)\n\t" \
  "ds_read_b64_tr_b16 a[148:149], v235 offset:8192\n\t" \
  "ds_read_b64_tr_b16 a[150:151], v235 offset:12288\n\t" \
  "v_mfma_f32_16x16x32_bf16 a[64:67], a[176:179], v[16:19], a[64:67]\n\t" \
  "ds_read_b64_tr_b16 a[152:153], v235 offset:16384\n\t" \
  "v_mfma_f32_16x16x32_bf16 a[64:67], a[180:183], v[20:23], a[64:67]\n\t" \
  "s_waitcnt lgkmcnt(5)\n\t" \
  "ds_read_b64_tr_b16 a[154:155], v235 offset:20480\n\t" \
  "v_mfma_f32_16x16x32_bf16 a[64:67], a[184:187], v[24:27], a[64:67]\n\t" \
  "ds_read_b64_tr_b16 a[156:157], v235 offset:24576\n\t" \
  "v_mfma_f32_16x16x32_bf16 a[64:67], a[188:191], v[28:31], a[64:67]\n\t" \
  "buffer_load_dwordx4 v[72:75], v242, %[ws], s73 offen offset:2048 sc1\n\t" \
  "v_mfma_f32_16x16x32_bf16 a[68:71], a[188:191], v[12:15], a[68:71]\n\t" \
  "ds_read_b64_tr_b16 a[158:159], v235 offset:28672\n\t" \
  "v_xor_b32 v236, 0xc0c0, v234\n\t" \
  "v_mfma_f32_16x16x32_bf16 a[68:71], a[184:187], v[8:11], a[68:71]\n\t" \
  "ds_read_b64_tr_b16 a[128:129], v236 offset:0\n\t" \
  "ds_read_b64_tr_b16 a[130:131], v236 offset:4096\n\t" \
  "v_mfma_f32_16x16x32_bf16 a[68:71], a[180:183], v[4:7], a[68:71]\n\t" \
  "ds_read_b64_tr_b16 a[132:133], v236 offset:8192\n\t" \
  "v_mfma_f32_16x16x32_bf16 a[68:71], a[176:179], v[0:3], a[68:71]\n\t" \
  "s_waitcnt lgkmcnt(7)\n\t" \
  "s_barrier\n\t" \
  "v_mfma_f32_16x16x32_bf16 a[80:83], a[144:147], v[16:19], a[80:83]\n\t" \
  "buffer_load_dwordx4 v[76:79], v242, %[ws], s73 offen offset:3072 sc1\n\t" \
  "v_mfma_f32_16x16x32_bf16 a[80:83], a[148:151], v[20:23], a[80:83]\n\t" \
  "s_waitcnt lgkmcnt(3)\n\t" \
  "ds_read_b64_tr_b16 a[134:135], v236 offset:12288\n\t" \
  "ds_read_b64_tr_b16 a[136:137], v236 offset:16384\n\t" \
  "v_mfma_f32_16x16x32_bf16 a[80:83], a[152:155], v[24:27], a[80:83]\n\t" \
  "ds_read_b64_tr_b16 a[138:139], v236 offset:20480\n\t" \
  "ds_read_b64_tr_b16 a[140:141], v236 offset:24576\n\t" \
  "v_mfma_f32_16x16x32_bf16 a[80:83], a[156:159], v[28:31], a[80:83]\n\t" \
  "ds_read_b64_tr_b16 a[142:143], v236 offset:28672\n\t" \
  "v_xor_b32 v237, 0xc0e0, v234\n\t" \
  "v_mfma_f32_16x16x32_bf16 a[84:87], a[156:159], v[12:15], a[84:87]\n\t" \
  "buffer_load_dwordx4 v240, %[wo], s71 offen offset:0 lds\n\t" \
  "v_mfma_f32_16x16x32_bf16 a[84:87], a[152:155], v[8:11], a[84:87]\n\t" \
  "ds_read_b64_tr_b16 a[12:13], v237 offset:0\n\t" \
  "ds_read_b64_tr_b16 a[14:15], v237 offset:4096\n\t" \
  "v_mfma_f32_16x16x32_bf16 a[84:87], a[148:151], v[4:7], a[84:87]\n\t" \
  "ds_read_b64_tr_b16 a[28:29], v237 offset:8192\n\t" \
  "v_mfma_f32_16x16x32_bf16 a[84:87], a[144:147], v[0:3], a[84:87]\n\t" \
  "s_waitcnt lgkmcnt(7)\n\t" \
  "ds_read_b64_tr_b16 a[30:31], v237 offset:12288\n\t" \
  "v_mfma_f32_16x16x32_bf16 a[96:99], a[128:131], v[16:19], a[96:99]\n\t" \
  "buffer_load_dwordx4 v241, %[wo], s71 offen offset:1024 lds\n\t" \
  "v_mfma_f32_16x16x32_bf16 a[96:99], a[132:135], v[20:23], a[96:99]\n\t" \
  "s_waitcnt lgkmcnt(4)\n\t" \
  "ds_read_b64_tr_b16 a[44:45], v237 offset:16384\n\t" \
  "ds_read_b64_tr_b16 a[46:47], v237 offset:20480\n\t" \
  "v_mfma_f32_16x16x32_bf16 a[96:99], a[136:139], v[24:27], a[96:99]\n\t" \
  "ds_read_b64_tr_b16 a[60:61], v237 offset:24576\n\t" \
  "v_lshrrev_b32 v244, 4, v242\n\t" \
  "v_mfma_f32_16x16x32_bf16 a[96:99], a[140:143], v[28:31], a[96:99]\n\t" \
  "s_lshl_b32 s77, %[hl], 2\n\t" \
  "ds_read_b64_tr_b16 a[62:63], v237 offset:28672\n\t" \
  "v_xor_b32 v235, 0x14000, v234\n\t" \
  "v_mfma_f32_16x16x32_bf16 a[100:103], a[140:143], v[12:15], a[100:103]\n\t" \
  "buffer_load_dwordx4 v238, %[wo], s71 offen offset:2048 lds\n\t" \
  "v_mfma_f32_16x16x32_bf16 a[100:103], a[136:139], v[8:11], a[100:103]\n\t" \
  "s_add_u32 s77, s77, %[wid]\n\t" \
  "ds_read_b64_tr_b16 a[160:161], v235 offset:0\n\t" \
  "ds_read_b64_tr_b16 a[162:163], v235 offset:4096\n\t" \
  "v_mfma_f32_16x16x32_bf16 a[100:103], a[132:135], v[4:7], a[100:103]\n\t" \
  "s_lshl_b32 s77, s77, 6\n\t" \
  "ds_read_b64_tr_b16 a[164:165], v235 offset:8192\n\t" \
  "v_add_u32 v230, 0x10000, v242\n\t" \
  "v_mfma_f32_16x16x32_bf16 a[100:103], a[128:131], v[0:3], a[100:103]\n\t" \
  "s_waitcnt lgkmcnt(7)\n\t" \
  "ds_read_b64_tr_b16 a[166:167], v235 offset:12288\n\t" \
  "v_xor_b32 v236, 0x24000, v234\n\t" \
  "v_mfma_f32_16x16x32_bf16 a[112:115], a[12:15], v[16:19], a[112:115]\n\t" \
  "ds_read_b64_tr_b16 a[168:169], v236 offset:0\n\t" \
  "v_add_u32 v232, 0x20000, v242\n\t" \
  "v_mfma_f32_16x16x32_bf16 a[112:115], a[28:31], v[20:23], a[112:115]\n\t" \
  "s_waitcnt lgkmcnt(5)\n\t" \
  "buffer_load_dwordx4 v239, %[wo], s71 offen offset:3072 lds\n\t" \
  "s_add_u32 m0, %[m], 98304\n\t" \
  "v_mfma_f32_16x16x32_bf16 a[112:115], a[44:47], v[24:27], a[112:115]\n\t" \
  "s_xor_b32 s71, %[wob], 1310720\n\t" \
  "ds_read_b64_tr_b16 a[170:171], v236 offset:4096\n\t" \
  "ds_read_b64_tr_b16 a[172:173], v236 offset:8192\n\t" \
  "v_mfma_f32_16x16x32_bf16 a[112:115], a[60:63], v[28:31], a[112:115]\n\t" \
  "s_xor_b32 s73, %[eso], 393216\n\t" \
  "ds_read_b64_tr_b16 a[174:175], v236 offset:12288\n\t" \
  "s_add_u32 s70, %[m], 147456\n\t" \
  "v_mfma_f32_16x16x32_bf16 a[116:119], a[60:63], v[12:15], a[116:119]\n\t" \
  "s_lshl_b32 s69, s77, 2\n\t" \
  "v_xor_b32 v237, 0x14020, v234\n\t" \
  "s_add_u32 s69, s69, 2228224\n\t" \
  "v_mfma_f32_16x16x32_bf16 a[116:119], a[44:47], v[8:11], a[116:119]\n\t" \
  "buffer_load_dwordx4 v240, %[wo], s71 offen offset:0 lds\n\t" \
  "v_mfma_f32_16x16x32_bf16 a[116:119], a[28:31], v[4:7], a[116:119]\n\t" \
  "s_bfe_u32 s68, %[eso], 0x2000f\n\t" \
  "ds_read_b64_tr_b16 a[176:177], v237 offset:0\n\t" \
  "v_mfma_f32_16x16x32_bf16 a[116:119], a[12:15], v[0:3], a[116:119]\n\t" \
  "s_waitcnt lgkmcnt(5)\n\t" \
  "ds_read_b64_tr_b16 a[178:179], v237 offset:4096\n\t" \
  "s_lshl_b32 s60, s68, 17\n\t" \
  STP("v_writelane_b32 %[stv], s94, 26\n\t") \
  STP("v_writelane_b32 %[stv], s92, 27\n\t") \
  STP("s_memrealtime s[94:95]\n\t") \
  STP("s_memtime s[92:93]\n\t") \
  "v_mfma_f32_16x16x32_bf16 a[0:3], a[160:163], v[48:51], a[0:3]\n\t" \
  "ds_read_b64_tr_b16 a[180:181], v237 offset:8192\n\t" \
  "v_mfma_f32_16x16x32_bf16 a[0:3], a[164:167], v[52:55], a[0:3]\n\t" \
  "s_waitcnt lgkmcnt(3)\n\t" \
  "ds_read_b64_tr_b16 a[182:183], v237 offset:12288\n\t" \
  "v_xor_b32 v235, 0x24020, v234\n\t" \
  "v_mfma_f32_16x16x32_bf16 a[0:3], a[168:171], v[56:59], a[0:3]\n\t" \
  "buffer_load_dwordx4 v[16:19], v242, %[ws], s73 offen offset:0 sc1\n\t" \
  "v_mfma_f32_16x16x32_bf16 a[0:3], a[172:175], v[60:63], a[0:3]\n\t" \
  "s_waitcnt vmcnt(14)\n\t" \
  "ds_read_b64_tr_b16 a[184:185], v235 offset:0\n\t" \
  "s_add_u32 s60, s60, %[xcb]\n\t" \
  "v_mfma_f32_16x16x32_bf16 a[4:7], a[172:175], v[92:95], a[4:7]\n\t" \
  "s_lshl_b32 s63, %[wid], 10\n\t" \
  "ds_read_b64_tr_b16 a[186:187], v235 offset:4096\n\t" \
  "v_and_b32 v250, 7, v244\n\t" \
  "v_mfma_f32_16x16x32_bf16 a[4:7], a[168:171], v[88:91], a[4:7]\n\t" \
  "s_add_u32 s60, s60, s63\n\t" \
  "ds_read_b64_tr_b16 a[188:189], v235 offset:8192\n\t" \
  "v_lshlrev_b32 v250, 5, v250\n\t" \
  "v_mfma_f32_16x16x32_bf16 a[4:7], a[164:167], v[84:87], a[4:7]\n\t" \
  "buffer_load_dwordx4 v[20:23], v242, %[ws], s73 offen offset:1024 sc1\n\t" \
  "v_mfma_f32_16x16x32_bf16 a[4:7], a[160:163], v[80:83], a[4:7]\n\t" \
  "s_waitcnt vmcnt(7)\n\t" \
  "ds_read_b64_tr_b16 a[190:191], v235 offset:12288\n\t" \
  "v_lshrrev_b32 v251, 4, v244\n\t" \
  "v_mfma_f32_16x16x32_bf16 a[8:11], a[160:163], v[64:67], a[8:11]\n\t" \
  "s_xor_b32 s61, s60, 135168\n\t" \
  "v_xor_b32 v236, 0x14040, v234\n\t" \
  "v_lshl_add_u32 v250, v251, 3, v250\n\t" \
  "v_mfma_f32_16x16x32_bf16 a[8:11], a[164:167], v[68:71], a[8:11]\n\t" \
  "ds_read_b64_tr_b16 a[144:145], v236 offset:0\n\t" \
  "v_mfma_f32_16x16x32_bf16 a[8:11], a[168:171], v[72:75], a[8:11]\n\t" \
  "buffer_load_dwordx4 v[24:27], v242, %[ws], s73 offen offset:2048 sc1\n\t" \
  "v_mfma_f32_16x16x32_bf16 a[8:11], a[172:175], v[76:79], a[8:11]\n\t" \
  "s_waitcnt lgkmcnt(5)\n\t" \
  "ds_read_b64_tr_b16 a[146:147], v236 offset:4096\n\t" \
  "s_xor_b32 s62, s60, 278528\n\t" \
  "v_mfma_f32_16x16x32_bf16 a[16:19], a[176:179], v[48:51], a[16:19]\n\t" \
  "s_lshl_b32 s65, s68, 7\n\t" \
  "ds_read_b64_tr_b16 a[148:149], v236 offset:8192\n\t" \
  "v_mfma_f32_16x16x32_bf16 a[16:19], a[180:183], v[52:55], a[16:19]\n\t" \
  "s_waitcnt lgkmcnt(3)\n\t" \
  "ds_read_b64_tr_b16 a[150:151], v236 offset:12288\n\t" \
  "v_xor_b32 v237, 0x24040, v234\n\t" \
  "v_mfma_f32_16x16x32_bf16 a[16:19], a[184:187], v[56:59], a[16:19]\n\t" \
  "s_add_u32 s65, s65, %[frb]\n\t" \
  "ds_read_b64_tr_b16 a[152:153], v237 offset:0\n\t" \
  "v_mfma_f32_16x16x32_bf16 a[16:19], a[188:191], v[60:63], a[16:19]\n\t" \
  "buffer_load_dwordx4 v[28:31], v242, %[ws], s73 offen offset:3072 sc1\n\t" \
  "s_xor_b32 s73, %[eso], 425984\n\t" \
  "v_mfma_f32_16x16x32_bf16 a[20:23], a[188:191], v[92:95], a[20:23]\n\t" \
  "s_xor_b32 s66, s65, 144\n\t" \
  "ds_read_b64_tr_b16 a[154:155], v237 offset:4096\n\t" \
  "v_mfma_f32_16x16x32_bf16 a[20:23], a[184:187], v[88:91], a[20:23]\n\t" \
  "ds_read_b64_tr_b16 a[156:157], v237 offset:8192\n\t" \
  "v_mfma_f32_16x16x32_bf16 a[20:23], a[180:183], v[84:87], a[20:23]\n\t" \
  "ds_read_b64_tr_b16 a[158:159], v237 offset:12288\n\t" \
  "v_mfma_f32_16x16x32_bf16 a[20:23], a[176:179], v[80:83], a[20:23]\n\t" \
  "buffer_load_dwordx4 v[0:3], v242, %[ws], s73 offen offset:0 sc1\n\t" \
  "v_mfma_f32_16x16x32_bf16 a[24:27], a[176:179], v[64:67], a[24:27]\n\t" \
  "s_xor_b32 s67, s65, 288\n\t" \
  "v_xor_b32 v235, 0x14060, v234\n\t" \
  "v_mfma_f32_16x16x32_bf16 a[24:27], a[180:183], v[68:71], a[24:27]\n\t" \
  "s_xor_b32 s68, s65, 384\n\t" \
  "ds_read_b64_tr_b16 a[128:129], v235 offset:0\n\t" \
  "v_mfma_f32_16x16x32_bf16 a[24:27], a[184:187], v[72:75], a[24:27]\n\t" \
  "s_lshr_b32 s75, %[m], 4\n\t" \
  "s_bitset1_b32 s75, 17\n\t" \
  "v_add_u32 v250, s75, v250\n\t" \
  "v_mfma_f32_16x16x32_bf16 a[24:27], a[188:191], v[76:79], a[24:27]\n\t" \
  "s_waitcnt lgkmcnt(5)\n\t" \
  "ds_read_b64_tr_b16 a[130:131], v235 offset:4096\n\t" \
  "s_andn2_b32 s68, s68, 12\n\t" \
  "v_mfma_f32_16x16x32_bf16 a[32:35], a[144:147], v[48:51], a[32:35]\n\t" \
  "buffer_load_dwordx4 v[4:7], v242, %[ws], s73 offen offset:1024 sc1\n\t" \
  "v_mfma_f32_16x16x32_bf16 a[32:35], a[148:151], v[52:55], a[32:35]\n\t" \
  "s_waitcnt lgkmcnt(2)\n\t" \
  "ds_read_b64_tr_b16 a[132:133], v235 offset:8192\n\t" \
  "s_andn2_b32 s64, 0x2000, %[na]\n\t" \
  "v_mfma_f32_16x16x32_bf16 a[32:35], a[152:155], v[56:59], a[32:35]\n\t" \
  "v_permlane32_swap_b32 v132, v134\n\t" \
  "v_mfma_f32_16x16x32_bf16 a[32:35], a[156:159], v[60:63], a[32:35]\n\t" \
  "ds_read_b64_tr_b16 a[134:135], v235 offset:12288\n\t" \
  "v_xor_b32 v236, 0x24060, v234\n\t" \
  "v_mfma_f32_16x16x32_bf16 a[36:39], a[156:159], v[92:95], a[36:39]\n\t" \
  "buffer_load_dwordx4 v[8:11], v242, %[ws], s73 offen offset:2048 sc1\n\t" \
  "v_mfma_f32_16x16x32_bf16 a[36:39], a[152:155], v[88:91], a[36:39]\n\t" \
  "s_xor_b32 s63, s60, 393216\n\t" \
  "ds_read_b64_tr_b16 a[136:137], v236 offset:0\n\t" \
  "ds_read_b64_tr_b16 a[138:139], v236 offset:4096\n\t" \
  "v_mfma_f32_16x16x32_bf16 a[36:39], a[148:151], v[84:87], a[36:39]\n\t" \
  "s_andn2_b32 s63, s63, 98304\n\t" \
  "v_permlane32_swap_b32 v128, v130\n\t" \
  "v_mfma_f32_16x16x32_bf16 a[36:39], a[144:147], v[80:83], a[36:39]\n\t" \
  "ds_read_b64_tr_b16 a[140:141], v236 offset:8192\n\t" \
  "ds_read_b64_tr_b16 a[142:143], v236 offset:12288\n\t" \
  "v_mfma_f32_16x16x32_bf16 a[40:43], a[144:147], v[64:67], a[40:43]\n\t" \
  "buffer_load_dwordx4 v[12:15], v242, %[ws], s73 offen offset:3072 sc1\n\t" \
  "s_xor_b32 s73, %[eso], 458752\n\t" \
  "v_mfma_f32_16x16x32_bf16 a[40:43], a[148:151], v[68:71], a[40:43]\n\t" \
  "v_permlane32_swap_b32 v133, v135\n\t" \
  "v_mfma_f32_16x16x32_bf16 a[40:43], a[152:155], v[72:75], a[40:43]\n\t" \
  "v_permlane32_swap_b32 v129, v131\n\t" \
  "v_mfma_f32_16x16x32_bf16 a[40:43], a[156:159], v[76:79], a[40:43]\n\t" \
  "s_waitcnt lgkmcnt(4)\n\t" \
  "v_xor_b32 v237, 0x14080, v234\n\t" \
  "v_add_f32 v132, v132, v134\n\t" \
  "v_mfma_f32_16x16x32_bf16 a[48:51], a[128:131], v[48:51], a[48:51]\n\t" \
  "ds_read_b64_tr_b16 a[76:77], v237 offset:0\n\t" \
  "v_add_f32 v128, v128, v130\n\t" \
  "v_mfma_f32_16x16x32_bf16 a[48:51], a[132:135], v[52:55], a[48:51]\n\t" \
  "s_waitcnt lgkmcnt(1)\n\t" \
  "buffer_load_dwordx4 v[32:35], v242, %[ws], s73 offen offset:0 sc1\n\t" \
  "v_mfma_f32_16x16x32_bf16 a[48:51], a[136:139], v[56:59], a[48:51]\n\t" \
  "ds_read_b64_tr_b16 a[78:79], v237 offset:4096\n\t" \
  "ds_read_b64_tr_b16 a[92:93], v237 offset:8192\n\t" \
  "v_mfma_f32_16x16x32_bf16 a[48:51], a[140:143], v[60:63], a[48:51]\n\t" \
  "s_barrier\n\t" \
  "v_mfma_f32_16x16x32_bf16 a[52:55], a[140:143], v[92:95], a[52:55]\n\t" \
  "ds_read_b64_tr_b16 a[94:95], v237 offset:12288\n\t" \
  "v_xor_b32 v235, 0x24080, v234\n\t" \
  "v_mfma_f32_16x16x32_bf16 a[52:55], a[136:139], v[88:91], a[52:55]\n\t" \
  "buffer_load_dwordx4 v[36:39], v242, %[ws], s73 offen offset:1024 sc1\n\t" \
  "buffer_load_dword v246, v245, %[ws], %[fw2] offen sc1\n\t" \
  "v_mfma_f32_16x16x32_bf16 a[52:55], a[132:135], v[84:87], a[52:55]\n\t" \
  "ds_read_b64_tr_b16 a[108:109], v235 offset:0\n\t" \
  "ds_read_b64_tr_b16 a[110:111], v235 offset:4096\n\t" \
  "v_add_f32 v133, v133, v135\n\t" \
  "v_mfma_f32_16x16x32_bf16 a[52:55], a[128:131], v[80:83], a[52:55]\n\t" \
  "ds_read_b64_tr_b16 a[124:125], v235 offset:8192\n\t" \
  "ds_read_b64_tr_b16 a[126:127], v235 offset:12288\n\t" \
  "v_mfma_f32_16x16x32_bf16 a[56:59], a[128:131], v[64:67], a[56:59]\n\t" \
  "s_bitcmp1_b32 %[wid], 0\n\t" \
  "s_cselect_b32 s72, s64, 0\n\t" \
  "s_xor_b32 s63, s63, s72\n\t" \
  "v_mfma_f32_16x16x32_bf16 a[56:59], a[132:135], v[68:71], a[56:59]\n\t" \
  "s_xor_b32 s64, s64, s63\n\t" \
  "v_add_f32 v129, v129, v131\n\t" \
  "v_xor_b32 v236, 0x140a0, v234\n\t" \
  "v_mfma_f32_16x16x32_bf16 a[56:59], a[136:139], v[72:75], a[56:59]\n\t" \
  "ds_read_b64_tr_b16 a[144:145], v236 offset:0\n\t" \
  "v_mfma_f32_16x16x32_bf16 a[56:59], a[140:143], v[76:79], a[56:59]\n\t" \
  "s_waitcnt lgkmcnt(5)\n\t" \
  "buffer_load_dwordx4 v[40:43], v242, %[ws], s73 offen offset:2048 sc1\n\t" \
  "v_mfma_f32_16x16x32_bf16 a[64:67], a[76:79], v[48:51], a[64:67]\n\t" \
  "ds_read_b64_tr_b16 a[146:147], v236 offset:4096\n\t" \
  "v_permlane16_swap_b32 v132, v133\n\t" \
  "v_mfma_f32_16x16x32_bf16 a[64:67], a[92:95], v[52:55], a[64:67]\n\t" \
  "s_waitcnt lgkmcnt(2)\n\t" \
  "ds_read_b64_tr_b16 a[148:149], v236 offset:8192\n\t" \
  "v_mfma_f32_16x16x32_bf16 a[64:67], a[108:111], v[56:59], a[64:67]\n\t" \
  "ds_read_b64_tr_b16 a[150:151], v236 offset:12288\n\t" \
  "v_xor_b32 v237, 0x240a0, v234\n\t" \
  "v_mfma_f32_16x16x32_bf16 a[64:67], a[124:127], v[60:63], a[64:67]\n\t" \
  "buffer_load_dwordx4 v[44:47], v242, %[ws], s73 offen offset:3072 sc1\n\t" \
  "v_mfma_f32_16x16x32_bf16 a[68:71], a[124:127], v[92:95], a[68:71]\n\t" \
  "ds_read_b64_tr_b16 a[152:153], v237 offset:0\n\t" \
  "v_permlane16_swap_b32 v128, v129\n\t" \
  "v_mfma_f32_16x16x32_bf16 a[68:71], a[108:111], v[88:91], a[68:71]\n\t" \
  "ds_read_b64_tr_b16 a[154:155], v237 offset:4096\n\t" \
  "v_add_f32 v132, v132, v133\n\t" \
  "v_mfma_f32_16x16x32_bf16 a[68:71], a[92:95], v[84:87], a[68:71]\n\t" \
  "ds_read_b64_tr_b16 a[156:157], v237 offset:8192\n\t" \
  "v_add_f32 v128, v128, v129\n\t" \
  "v_mfma_f32_16x16x32_bf16 a[68:71], a[76:79], v[80:83], a[68:71]\n\t" \
  "ds_read_b64_tr_b16 a[158:159], v237 offset:12288\n\t" \
  "v_add_f32_dpp v248, v132, v132 row_ror:8 row_mask:0xf bank_mask:0xf\n\t" \
  "v_mfma_f32_16x16x32_bf16 a[72:75], a[76:79], v[64:67], a[72:75]\n\t" \
  "buffer_load_dwordx4 v241, %[wo], s71 offen offset:1024 lds\n\t" \
  "v_mfma_f32_16x16x32_bf16 a[72:75], a[92:95], v[68:71], a[72:75]\n\t" \
  "v_xor_b32 v235, 0x140c0, v234\n\t" \
  "v_add_f32_dpp v249, v128, v128 row_ror:8 row_mask:0xf bank_mask:0xf\n\t" \
  "v_mfma_f32_16x16x32_bf16 a[72:75], a[108:111], v[72:75], a[72:75]\n\t" \
  "ds_read_b64_tr_b16 a[12:13], v235 offset:0\n\t" \
  "v_mfma_f32_16x16x32_bf16 a[72:75], a[124:127], v[76:79], a[72:75]\n\t" \
  "s_waitcnt lgkmcnt(5)\n\t" \
  "ds_read_b64_tr_b16 a[14:15], v235 offset:4096\n\t" \
  "v_mfma_f32_16x16x32_bf16 a[80:83], a[144:147], v[48:51], a[80:83]\n\t" \
  "buffer_load_dwordx4 v238, %[wo], s71 offen offset:2048 lds\n\t" \
  "v_mfma_f32_16x16x32_bf16 a[80:83], a[148:151], v[52:55], a[80:83]\n\t" \
  "s_waitcnt lgkmcnt(2)\n\t" \
  "ds_read_b64_tr_b16 a[28:29], v235 offset:8192\n\t" \
  "v_mfma_f32_16x16x32_bf16 a[80:83], a[152:155], v[56:59], a[80:83]\n\t" \
  "s_xor_b32 s74, %[eso], 98304\n\t" \
  "ds_read_b64_tr_b16 a[30:31], v235 offset:12288\n\t" \
  "v_xor_b32 v236, 0x240c0, v234\n\t" \
  "v_mfma_f32_16x16x32_bf16 a[80:83], a[156:159], v[60:63], a[80:83]\n\t" \
  "ds_read_b64_tr_b16 a[44:45], v236 offset:0\n\t" \
  "v_mfma_f32_16x16x32_bf16 a[84:87], a[156:159], v[92:95], a[84:87]\n\t" \
  "ds_read_b64_tr_b16 a[46:47], v236 offset:4096\n\t" \
  "v_mfma_f32_16x16x32_bf16 a[84:87], a[152:155], v[88:91], a[84:87]\n\t" \
  "buffer_load_dwordx4 v239, %[wo], s71 offen offset:3072 lds\n\t" \
  "s_add_u32 m0, %[m], 32768\n\t" \
  "v_mfma_f32_16x16x32_bf16 a[84:87], a[148:151], v[84:87], a[84:87]\n\t" \
  "s_xor_b32 s71, %[wob], 1441792\n\t" \
  "ds_read_b64_tr_b16 a[60:61], v236 offset:8192\n\t" \
  "v_mfma_f32_16x16x32_bf16 a[84:87], a[144:147], v[80:83], a[84:87]\n\t" \
  "buffer_store_dwordx4 v[160:163], v242, %[ws], s74 offen offset:0 sc1\n\t" \
  "v_mfma_f32_16x16x32_bf16 a[88:91], a[144:147], v[64:67], a[88:91]\n\t" \
  "s_xor_b32 s74, %[eso], 98304\n\t" \
  "ds_read_b64_tr_b16 a[62:63], v236 offset:12288\n\t" \
  "v_xor_b32 v237, 0x140e0, v234\n\t" \
  "v_mfma_f32_16x16x32_bf16 a[88:91], a[148:151], v[68:71], a[88:91]\n\t" \
  "ds_read_b64_tr_b16 a[76:77], v237 offset:0\n\t" \
  "v_mfma_f32_16x16x32_bf16 a[88:91], a[152:155], v[72:75], a[88:91]\n\t" \
  "ds_read_b64_tr_b16 a[78:79], v237 offset:4096\n\t" \
  "v_mfma_f32_16x16x32_bf16 a[88:91], a[156:159], v[76:79], a[88:91]\n\t" \
  "s_waitcnt lgkmcnt(6)\n\t" \
  "buffer_load_dwordx4 v240, %[wo], s71 offen offset:0 lds\n\t" \
  "v_mfma_f32_16x16x32_bf16 a[96:99], a[12:15], v[48:51], a[96:99]\n\t" \
  "ds_read_b64_tr_b16 a[92:93], v237 offset:8192\n\t" \
  "v_mfma_f32_16x16x32_bf16 a[96:99], a[28:31], v[52:55], a[96:99]\n\t" \
  "s_waitcnt lgkmcnt(3)\n\t" \
  "buffer_store_dwordx4 v[164:167], v242, %[ws], s74 offen offset:1024 sc1\n\t" \
  "v_mfma_f32_16x16x32_bf16 a[96:99], a[44:47], v[56:59], a[96:99]\n\t" \
  "s_xor_b32 s74, %[eso], 98304\n\t" \
  "ds_read_b64_tr_b16 a[94:95], v237 offset:12288\n\t" \
  "v_xor_b32 v235, 0x240e0, v234\n\t" \
  "v_mfma_f32_16x16x32_bf16 a[96:99], a[60:63], v[60:63], a[96:99]\n\t" \
  "ds_read_b64_tr_b16 a[108:109], v235 offset:0\n\t" \
  "v_mfma_f32_16x16x32_bf16 a[100:103], a[60:63], v[92:95], a[100:103]\n\t" \
  "ds_read_b64_tr_b16 a[110:111], v235 offset:4096\n\t" \
  "v_mfma_f32_16x16x32_bf16 a[100:103], a[44:47], v[88:91], a[100:103]\n\t" \
  "buffer_load_dwordx4 v241, %[wo], s71 offen offset:1024 lds\n\t" \
  "v_mfma_f32_16x16x32_bf16 a[100:103], a[28:31], v[84:87], a[100:103]\n\t" \
  "ds_read_b64_tr_b16 a[124:125], v235 offset:8192\n\t" \
  "v_mfma_f32_16x16x32_bf16 a[100:103], a[12:15], v[80:83], a[100:103]\n\t" \
  "buffer_store_dwordx4 v[168:171], v242, %[ws], s74 offen offset:2048 sc1\n\t" \
  "s_waitcnt vmcnt(10)\n\t" \
  "v_cmp_ne_u32 vcc, %[tag], v246\n\t" \
  "s_and_b32 vcc_lo, vcc_lo, 0xfe00\n\t" \
  "s_cbranch_scc1 .Lop_s2\n\t" \
  ".Lop_k2:\n\t" \
  "v_mfma_f32_16x16x32_bf16 a[104:107], a[12:15], v[64:67], a[104:107]\n\t" \
  "s_xor_b32 s74, %[eso], 98304\n\t" \
  "ds_read_b64_tr_b16 a[126:127], v235 offset:12288\n\t" \
  "v_xor_b32 v236, 0x1c000, v234\n\t" \
  "v_mfma_f32_16x16x32_bf16 a[104:107], a[28:31], v[68:71], a[104:107]\n\t" \
  "ds_read_b64_tr_b16 a[128:129], v236 offset:0\n\t" \
  "v_mfma_f32_16x16x32_bf16 a[104:107], a[44:47], v[72:75], a[104:107]\n\t" \
  "ds_read_b64_tr_b16 a[130:131], v236 offset:4096\n\t" \
  "v_mfma_f32_16x16x32_bf16 a[104:107], a[60:63], v[76:79], a[104:107]\n\t" \
  "s_waitcnt lgkmcnt(6)\n\t" \
  "ds_read_b64_tr_b16 a[132:133], v236 offset:8192\n\t" \
  "v_mfma_f32_16x16x32_bf16 a[112:115], a[76:79], v[48:51], a[112:115]\n\t" \
  "buffer_store_dwordx4 v[172:175], v242, %[ws], s74 offen offset:3072 sc1\n\t" \
  "v_mfma_f32_16x16x32_bf16 a[112:115], a[92:95], v[52:55], a[112:115]\n\t" \
  "s_waitcnt lgkmcnt(3)\n\t" \
  "ds_read_b64_tr_b16 a[134:135], v236 offset:12288\n\t" \
  "v_mfma_f32_16x16x32_bf16 a[112:115], a[108:111], v[56:59], a[112:115]\n\t" \
  "buffer_load_dwordx4 v238, %[wo], s71 offen offset:2048 lds\n\t" \
  "v_mfma_f32_16x16x32_bf16 a[112:115], a[124:127], v[60:63], a[112:115]\n\t" \
  "ds_read_b64_tr_b16 a[136:137], v236 offset:16384\n\t" \
  "v_mfma_f32_16x16x32_bf16 a[116:119], a[124:127], v[92:95], a[116:119]\n\t" \
  "ds_read_b64_tr_b16 a[138:139], v236 offset:20480\n\t" \
  "v_mfma_f32_16x16x32_bf16 a[116:119], a[108:111], v[88:91], a[116:119]\n\t" \
  "ds_read_b64_tr_b16 a[140:141], v236 offset:24576\n\t" \
  "v_mfma_f32_16x16x32_bf16 a[116:119], a[92:95], v[84:87], a[116:119]\n\t" \
  "buffer_load_dwordx4 v239, %[wo], s71 offen offset:3072 lds\n\t" \
  "v_mfma_f32_16x16x32_bf16 a[116:119], a[76:79], v[80:83], a[116:119]\n\t" \
  "s_xor_b32 s73, %[eso], 524288\n\t" \
  "ds_read_b64_tr_b16 a[142:143], v236 offset:28672\n\t" \
  "v_mfma_f32_16x16x32_bf16 a[120:123], a[76:79], v[64:67], a[120:123]\n\t" \
  "v_xor_b32 v237, 0x1c020, v234\n\t" \
  "v_mfma_f32_16x16x32_bf16 a[120:123], a[92:95], v[68:71], a[120:123]\n\t" \
  "ds_read_b64_tr_b16 a[144:145], v237 offset:0\n\t" \
  "v_mfma_f32_16x16x32_bf16 a[120:123], a[108:111], v[72:75], a[120:123]\n\t" \
  "ds_read_b64_tr_b16 a[146:147], v237 offset:4096\n\t" \
  "v_mfma_f32_16x16x32_bf16 a[120:123], a[124:127], v[76:79], a[120:123]\n\t" \
  "s_waitcnt lgkmcnt(6)\n\t" \
  "buffer_load_dwordx4 v[48:51], v242, %[ws], s73 offen offset:0 sc1\n\t" \
  STP("v_writelane_b32 %[stv], s94, 28\n\t") \
  STP("v_writelane_b32 %[stv], s92, 29\n\t") \
  STP("s_memrealtime s[94:95]\n\t") \
  STP("s_memtime s[92:93]\n\t") \
  "v_mfma_f32_16x16x32_bf16 a[0:3], a[128:131], v[16:19], a[0:3]\n\t" \
  "ds_read_b64_tr_b16 a[148:149], v237 offset:8192\n\t" \
  "v_mfma_f32_16x16x32_bf16 a[0:3], a[132:135], v[20:23], a[0:3]\n\t" \
  "s_waitcnt lgkmcnt(3)\n\t" \
  "ds_read_b64_tr_b16 a[150:151], v237 offset:12288\n\t" \
  "v_mfma_f32_16x16x32_bf16 a[0:3], a[136:139], v[24:27], a[0:3]\n\t" \
  "ds_read_b64_tr_b16 a[152:153], v237 offset:16384\n\t" \
  "v_mfma_f32_16x16x32_bf16 a[0:3], a[140:143], v[28:31], a[0:3]\n\t" \
  "buffer_load_dwordx4 v[52:55], v242, %[ws], s73 offen offset:1024 sc1\n\t" \
  "v_mfma_f32_16x16x32_bf16 a[4:7], a[140:143], v[12:15], a[4:7]\n\t" \
  "ds_read_b64_tr_b16 a[154:155], v237 offset:20480\n\t" \
  "v_mfma_f32_16x16x32_bf16 a[4:7], a[136:139], v[8:11], a[4:7]\n\t" \
  "ds_read_b64_tr_b16 a[156:157], v237 offset:24576\n\t" \
  "v_mfma_f32_16x16x32_bf16 a[4:7], a[132:135], v[4:7], a[4:7]\n\t" \
  "ds_read_b64_tr_b16 a[158:159], v237 offset:28672\n\t" \
  "v_mfma_f32_16x16x32_bf16 a[4:7], a[128:131], v[0:3], a[4:7]\n\t" \
  "s_waitcnt vmcnt(13)\n\t" \
  "buffer_load_dwordx4 v[56:59], v242, %[ws], s73 offen offset:2048 sc1\n\t" \
  "v_mfma_f32_16x16x32_bf16 a[8:11], a[128:131], v[32:35], a[8:11]\n\t" \
  "v_xor_b32 v235, 0x1c040, v234\n\t" \
  "v_mfma_f32_16x16x32_bf16 a[8:11], a[132:135], v[36:39], a[8:11]\n\t" \
  "ds_read_b64_tr_b16 a[12:13], v235 offset:0\n\t" \
  "v_mfma_f32_16x16x32_bf16 a[8:11], a[136:139], v[40:43], a[8:11]\n\t" \
  "ds_read_b64_tr_b16 a[14:15], v235 offset:4096\n\t" \
  "v_mfma_f32_16x16x32_bf16 a[8:11], a[140:143], v[44:47], a[8:11]\n\t" \
  "s_waitcnt lgkmcnt(6)\n\t" \
  "ds_read_b64_tr_b16 a[28:29], v235 offset:8192\n\t" \
  "v_mfma_f32_16x16x32_bf16 a[16:19], a[144:147], v[16:19], a[16:19]\n\t" \
  "buffer_load_dwordx4 v[60:63], v242, %[ws], s73 offen offset:3072 sc1\n\t" \
  "s_xor_b32 s73, %[eso], 557056\n\t" \
  "v_mfma_f32_16x16x32_bf16 a[16:19], a[148:151], v[20:23], a[16:19]\n\t" \
  "s_waitcnt lgkmcnt(3)\n\t" \
  "ds_read_b64_tr_b16 a[30:31], v235 offset:12288\n\t" \
  "v_mfma_f32_16x16x32_bf16 a[16:19], a[152:155], v[24:27], a[16:19]\n\t" \
  "ds_read_b64_tr_b16 a[44:45], v235 offset:16384\n\t" \
  "v_mfma_f32_16x16x32_bf16 a[16:19], a[156:159], v[28:31], a[16:19]\n\t" \
  "ds_read_b64_tr_b16 a[46:47], v235 offset:20480\n\t" \
  "v_mfma_f32_16x16x32_bf16 a[20:23], a[156:159], v[12:15], a[20:23]\n\t" \
  "buffer_load_dwordx4 v[80:83], v242, %[ws], s73 offen offset:0 sc1\n\t" \
  "v_mfma_f32_16x16x32_bf16 a[20:23], a[152:155], v[8:11], a[20:23]\n\t" \
  "ds_read_b64_tr_b16 a[60:61], v235 offset:24576\n\t" \
  "v_mfma_f32_16x16x32_bf16 a[20:23], a[148:151], v[4:7], a[20:23]\n\t" \
  "ds_read_b64_tr_b16 a[62:63], v235 offset:28672\n\t" \
  "v_mfma_f32_16x16x32_bf16 a[20:23], a[144:147], v[0:3], a[20:23]\n\t" \
  "v_mfma_f32_16x16x32_bf16 a[24:27], a[144:147], v[32:35], a[24:27]\n\t" \
  "v_xor_b32 v236, 0x1c060, v234\n\t" \
  "v_mfma_f32_16x16x32_bf16 a[24:27], a[148:151], v[36:39], a[24:27]\n\t" \
  "buffer_load_dwordx4 v[84:87], v242, %[ws], s73 offen offset:1024 sc1\n\t" \
  "v_mfma_f32_16x16x32_bf16 a[24:27], a[152:155], v[40:43], a[24:27]\n\t" \
  "ds_read_b64_tr_b16 a[76:77], v236 offset:0\n\t" \
  "v_mfma_f32_16x16x32_bf16 a[24:27], a[156:159], v[44:47], a[24:27]\n\t" \
  "s_waitcnt lgkmcnt(5)\n\t" \
  "ds_read_b64_tr_b16 a[78:79], v236 offset:4096\n\t" \
  "v_mfma_f32_16x16x32_bf16 a[32:35], a[12:15], v[16:19], a[32:35]\n\t" \
  "ds_read_b64_tr_b16 a[92:93], v236 offset:8192\n\t" \
  "v_mfma_f32_16x16x32_bf16 a[32:35], a[28:31], v[20:23], a[32:35]\n\t" \
  "s_waitcnt lgkmcnt(3)\n\t" \
  "buffer_load_dwordx4 v[88:91], v242, %[ws], s73 offen offset:2048 sc1\n\t" \
  "v_mfma_f32_16x16x32_bf16 a[32:35], a[44:47], v[24:27], a[32:35]\n\t" \
  "ds_read_b64_tr_b16 a[94:95], v236 offset:12288\n\t" \
  "v_mfma_f32_16x16x32_bf16 a[32:35], a[60:63], v[28:31], a[32:35]\n\t" \
  "ds_read_b64_tr_b16 a[108:109], v236 offset:16384\n\t" \
  "v_mfma_f32_16x16x32_bf16 a[36:39], a[60:63], v[12:15], a[36:39]\n\t" \
  "ds_read_b64_tr_b16 a[110:111], v236 offset:20480\n\t" \
  "v_mfma_f32_16x16x32_bf16 a[36:39], a[44:47], v[8:11], a[36:39]\n\t" \
  "buffer_load_dwordx4 v[92:95], v242, %[ws], s73 offen offset:3072 sc1\n\t" \
  "s_xor_b32 s73, %[eso], 589824\n\t" \
  "v_mfma_f32_16x16x32_bf16 a[36:39], a[28:31], v[4:7], a[36:39]\n\t" \
  "ds_read_b64_tr_b16 a[124:125], v236 offset:24576\n\t" \
  "v_mfma_f32_16x16x32_bf16 a[36:39], a[12:15], v[0:3], a[36:39]\n\t" \
  "ds_read_b64_tr_b16 a[126:127], v236 offset:28672\n\t" \
  "v_mfma_f32_16x16x32_bf16 a[40:43], a[12:15], v[32:35], a[40:43]\n\t" \
  "v_xor_b32 v237, 0x1c080, v234\n\t" \
  "v_mfma_f32_16x16x32_bf16 a[40:43], a[28:31], v[36:39], a[40:43]\n\t" \
  "ds_read_b64_tr_b16 v[128:129], v237 offset:0\n\t" \
  "v_mfma_f32_16x16x32_bf16 a[40:43], a[44:47], v[40:43], a[40:43]\n\t" \
  "buffer_load_dwordx4 v[64:67], v242, %[ws], s73 offen offset:0 sc1\n\t" \
  "v_mfma_f32_16x16x32_bf16 a[40:43], a[60:63], v[44:47], a[40:43]\n\t" \
  "s_waitcnt lgkmcnt(5)\n\t" \
  "ds_read_b64_tr_b16 v[130:131], v237 offset:4096\n\t" \
  "v_mfma_f32_16x16x32_bf16 a[48:51], a[76:79], v[16:19], a[48:51]\n\t" \
  "s_add_u32 m0, %[m], 81920\n\t" \
  "ds_read_b64_tr_b16 v[132:133], v237 offset:8192\n\t" \
  "v_mfma_f32_16x16x32_bf16 a[48:51], a[92:95], v[20:23], a[48:51]\n\t" \
  "s_waitcnt lgkmcnt(3)\n\t" \
  "ds_read_b64_tr_b16 v[134:135], v237 offset:12288\n\t" \
  "v_mfma_f32_16x16x32_bf16 a[48:51], a[108:111], v[24:27], a[48:51]\n\t" \
  "buffer_load_dwordx4 v[68:71], v242, %[ws], s73 offen offset:1024 sc1\n\t" \
  "v_mfma_f32_16x16x32_bf16 a[48:51], a[124:127], v[28:31], a[48:51]\n\t" \
  "s_barrier\n\t" \
  "v_mfma_f32_16x16x32_bf16 a[52:55], a[124:127], v[12:15], a[52:55]\n\t" \
  "s_xor_b32 s71, %[wob], 1572864\n\t" \
  "ds_write_b64 v250, v[248:249] offset:19456\n\t" \
  "v_mfma_f32_16x16x32_bf16 a[52:55], a[108:111], v[8:11], a[52:55]\n\t" \
  "ds_read_b64_tr_b16 v[136:137], v237 offset:16384\n\t" \
  "ds_read_b64_tr_b16 v[138:139], v237 offset:20480\n\t" \
  "v_mfma_f32_16x16x32_bf16 a[52:55], a[92:95], v[4:7], a[52:55]\n\t" \
  "ds_read_b64_tr_b16 v[140:141], v237 offset:24576\n\t" \
  "v_mfma_f32_16x16x32_bf16 a[52:55], a[76:79], v[0:3], a[52:55]\n\t" \
  "buffer_load_dwordx4 v[72:75], v242, %[ws], s73 offen offset:2048 sc1\n\t" \
  "v_mfma_f32_16x16x32_bf16 a[56:59], a[76:79], v[32:35], a[56:59]\n\t" \
  "ds_read_b64_tr_b16 v[142:143], v237 offset:28672\n\t" \
  "v_xor_b32 v235, 0x1c0a0, v234\n\t" \
  "v_mfma_f32_16x16x32_bf16 a[56:59], a[92:95], v[36:39], a[56:59]\n\t" \
  "ds_read_b64_tr_b16 a[12:13], v235 offset:0\n\t" \
  "v_mfma_f32_16x16x32_bf16 a[56:59], a[108:111], v[40:43], a[56:59]\n\t" \
  "ds_read_b64_tr_b16 a[14:15], v235 offset:4096\n\t" \
  "v_mfma_f32_16x16x32_bf16 a[56:59], a[124:127], v[44:47], a[56:59]\n\t" \
  "s_waitcnt lgkmcnt(7)\n\t" \
  "buffer_load_dwordx4 v[76:79], v242, %[ws], s73 offen offset:3072 sc1\n\t" \
  "v_mfma_f32_16x16x32_bf16 a[64:67], v[128:131], v[16:19], a[64:67]\n\t" \
  "ds_read_b64_tr_b16 a[28:29], v235 offset:8192\n\t" \
  "v_mfma_f32_16x16x32_bf16 a[64:67], v[132:135], v[20:23], a[64:67]\n\t" \
  "s_waitcnt lgkmcnt(3)\n\t" \
  "ds_read_b64_tr_b16 a[30:31], v235 offset:12288\n\t" \
  "v_mfma_f32_16x16x32_bf16 a[64:67], v[136:139], v[24:27], a[64:67]\n\t" \
  "ds_read_b64_tr_b16 a[44:45], v235 offset:16384\n\t" \
  "v_mfma_f32_16x16x32_bf16 a[64:67], v[140:143], v[28:31], a[64:67]\n\t" \
  "buffer_load_dwordx4 v240, %[wo], s71 offen offset:0 lds\n\t" \
  "v_mfma_f32_16x16x32_bf16 a[68:71], v[140:143], v[12:15], a[68:71]\n\t" \
  "ds_read_b64_tr_b16 a[46:47], v235 offset:20480\n\t" \
  "v_mfma_f32_16x16x32_bf16 a[68:71], v[136:139], v[8:11], a[68:71]\n\t" \
  "ds_read_b64_tr_b16 a[60:61], v235 offset:24576\n\t" \
  "v_mfma_f32_16x16x32_bf16 a[68:71], v[132:135], v[4:7], a[68:71]\n\t" \
  "ds_read_b64_tr_b16 a[62:63], v235 offset:28672\n\t" \
  "v_mfma_f32_16x16x32_bf16 a[68:71], v[128:131], v[0:3], a[68:71]\n\t" \
  "v_mfma_f32_16x16x32_bf16 a[72:75], v[128:131], v[32:35], a[72:75]\n\t" \
  "buffer_load_dwordx4 v241, %[wo], s71 offen offset:1024 lds\n\t" \
  "v_mfma_f32_16x16x32_bf16 a[72:75], v[132:135], v[36:39], a[72:75]\n\t" \
  "v_xor_b32 v236, 0x1c0c0, v234\n\t" \
  "v_mfma_f32_16x16x32_bf16 a[72:75], v[136:139], v[40:43], a[72:75]\n\t" \
  "ds_read_b64_tr_b16 v[144:145], v236 offset:0\n\t" \
  "v_mfma_f32_16x16x32_bf16 a[72:75], v[140:143], v[44:47], a[72:75]\n\t" \
  "s_waitcnt lgkmcnt(5)\n\t" \
  "ds_read_b64_tr_b16 v[146:147], v236 offset:4096\n\t" \
  "v_mfma_f32_16x16x32_bf16 a[80:83], a[12:15], v[16:19], a[80:83]\n\t" \
  "buffer_load_dwordx4 v238, %[wo], s71 offen offset:2048 lds\n\t" \
  "v_mfma_f32_16x16x32_bf16 a[80:83], a[28:31], v[20:23], a[80:83]\n\t" \
  "s_waitcnt lgkmcnt(2)\n\t" \
  "ds_read_b64_tr_b16 v[148:149], v236 offset:8192\n\t" \
  "v_mfma_f32_16x16x32_bf16 a[80:83], a[44:47], v[24:27], a[80:83]\n\t" \
  "ds_read_b64_tr_b16 v[150:151], v236 offset:12288\n\t" \
  "v_mfma_f32_16x16x32_bf16 a[80:83], a[60:63], v[28:31], a[80:83]\n\t" \
  "s_barrier\n\t" \
  "v_mfma_f32_16x16x32_bf16 a[84:87], a[60:63], v[12:15], a[84:87]\n\t" \
  "s_waitcnt vmcnt(17)\n\t" \
  "buffer_store_dword v243, v254, %[ws], %[fw2] offen offset:64 sc1\n\t" \
  "v_mfma_f32_16x16x32_bf16 a[84:87], a[44:47], v[8:11], a[84:87]\n\t" \
  "ds_read_b64_tr_b16 v[152:153], v236 offset:16384\n\t" \
  "ds_read_b64_tr_b16 v[154:155], v236 offset:20480\n\t" \
  "v_mfma_f32_16x16x32_bf16 a[84:87], a[28:31], v[4:7], a[84:87]\n\t" \
  "buffer_load_dwordx4 v239, %[wo], s71 offen offset:3072 lds\n\t" \
  "v_mfma_f32_16x16x32_bf16 a[84:87], a[12:15], v[0:3], a[84:87]\n\t" \
  "ds_read_b64_tr_b16 v[156:157], v236 offset:24576\n\t" \
  "ds_read_b64_tr_b16 v[158:159], v236 offset:28672\n\t" \
  "v_mfma_f32_16x16x32_bf16 a[88:91], a[12:15], v[32:35], a[88:91]\n\t" \
  "v_and_b32 v250, 3, v244\n\t" \
  "v_xor_b32 v237, 0x1c0e0, v234\n\t" \
  "v_mfma_f32_16x16x32_bf16 a[88:91], a[28:31], v[36:39], a[88:91]\n\t" \
  "ds_read_b64_tr_b16 v[128:129], v237 offset:0\n\t" \
  "v_bfe_u32 v251, v244, 4, 1\n\t" \
  "v_mfma_f32_16x16x32_bf16 a[88:91], a[44:47], v[40:43], a[88:91]\n\t" \
  "ds_read_b64_tr_b16 v[130:131], v237 offset:4096\n\t" \
  "v_lshl_add_u32 v250, v251, 2, v250\n\t" \
  "v_mfma_f32_16x16x32_bf16 a[88:91], a[60:63], v[44:47], a[88:91]\n\t" \
  ".Lhs12:\n\ts_cbranch_execz .Lhs12n\n\t" \
  "s_waitcnt lgkmcnt(6)\n\t" \
  "ds_read_b64_tr_b16 v[132:133], v237 offset:8192\n\t" \
  "v_bfe_u32 v251, v244, 2, 2\n\t" \
  "v_mfma_f32_16x16x32_bf16 a[96:99], v[144:147], v[16:19], a[96:99]\n\t" \
  "ds_read_b64_tr_b16 v[134:135], v237 offset:12288\n\t" \
  "v_lshlrev_b32 v251, 8, v251\n\t" \
  "v_mfma_f32_16x16x32_bf16 a[96:99], v[148:151], v[20:23], a[96:99]\n\t" \
  "s_waitcnt lgkmcnt(4)\n\t" \
  "ds_read_b64_tr_b16 v[136:137], v237 offset:16384\n\t" \
  "v_lshl_add_u32 v251, v250, 3, v251\n\t" \
  "v_mfma_f32_16x16x32_bf16 a[96:99], v[152:155], v[24:27], a[96:99]\n\t" \
  "ds_read_b64_tr_b16 v[138:139], v237 offset:20480\n\t" \
  "v_mfma_f32_16x16x32_bf16 a[96:99], v[156:159], v[28:31], a[96:99]\n\t" \
  "ds_read_b64_tr_b16 v[140:141], v237 offset:24576\n\t" \
  "v_mfma_f32_16x16x32_bf16 a[100:103], v[156:159], v[12:15], a[100:103]\n\t" \
  "ds_read_b64_tr_b16 v[142:143], v237 offset:28672\n\t" \
  "v_mfma_f32_16x16x32_bf16 a[100:103], v[152:155], v[8:11], a[100:103]\n\t" \
  "s_lshr_b32 s75, %[m], 6\n\t" \
  "s_bitset1_b32 s75, 17\n\t" \
  "v_add_u32 v251, s75, v251\n\t" \
  "v_mfma_f32_16x16x32_bf16 a[100:103], v[148:151], v[4:7], a[100:103]\n\t" \
  "ds_read_b64 v[224:225], v251 offset:19456\n\t" \
  "v_lshlrev_b32 v228, 4, v250\n\t" \
  "v_mfma_f32_16x16x32_bf16 a[100:103], v[144:147], v[0:3], a[100:103]\n\t" \
  "v_mov_b32 v249, %[tag]\n\t" \
  "v_mov_b32 v251, %[tag]\n\t" \
  "v_mfma_f32_16x16x32_bf16 a[104:107], v[144:147], v[32:35], a[104:107]\n\t" \
  "s_mov_b32 s82, 0x000f000f\n\t" \
  "v_xor_b32 v235, 0x0, v234\n\t" \
  "s_mov_b32 s83, 0\n\t" \
  "v_mfma_f32_16x16x32_bf16 a[104:107], v[148:151], v[36:39], a[104:107]\n\t" \
  "ds_read_b64_tr_b16 a[12:13], v235 offset:0\n\t" \
  "v_mfma_f32_16x16x32_bf16 a[104:107], v[152:155], v[40:43], a[104:107]\n\t" \
  "ds_read_b64_tr_b16 a[14:15], v235 offset:4096\n\t" \
  "v_mfma_f32_16x16x32_bf16 a[104:107], v[156:159], v[44:47], a[104:107]\n\t" \
  "s_waitcnt lgkmcnt(7)\n\t" \
  "ds_read_b64_tr_b16 a[28:29], v235 offset:8192\n\t" \
  "v_mfma_f32_16x16x32_bf16 a[112:115], v[128:131], v[16:19], a[112:115]\n\t" \
  "ds_read_b64_tr_b16 a[30:31], v235 offset:12288\n\t" \
  "v_mfma_f32_16x16x32_bf16 a[112:115], v[132:135], v[20:23], a[112:115]\n\t" \
  "s_waitcnt lgkmcnt(5)\n\t" \
  "ds_read_b64_tr_b16 a[44:45], v235 offset:16384\n\t" \
  "v_mfma_f32_16x16x32_bf16 a[112:115], v[136:139], v[24:27], a[112:115]\n\t" \
  "s_waitcnt lgkmcnt(5)\n\t" \
  "v_add_f32_dpp v226, v224, v224 row_ror:4 row_mask:0xf bank_mask:0xf\n\t" \
  "v_add_f32_dpp v227, v225, v225 row_ror:4 row_mask:0xf bank_mask:0xf\n\t" \
  "v_mfma_f32_16x16x32_bf16 a[112:115], v[140:143], v[28:31], a[112:115]\n\t" \
  "ds_read_b64_tr_b16 a[46:47], v235 offset:20480\n\t" \
  "v_add_f32_dpp v248, v226, v226 row_ror:8 row_mask:0xf bank_mask:0xf\n\t" \
  "v_mfma_f32_16x16x32_bf16 a[116:119], v[140:143], v[12:15], a[116:119]\n\t" \
  "ds_read_b64_tr_b16 a[60:61], v235 offset:24576\n\t" \
  "v_add_f32_dpp v250, v227, v227 row_ror:8 row_mask:0xf bank_mask:0xf\n\t" \
  "v_mfma_f32_16x16x32_bf16 a[116:119], v[136:139], v[8:11], a[116:119]\n\t" \
  "s_mov_b64 exec, s[82:83]\n\t" \
  "buffer_store_dwordx4 v[248:251], v228, %[ws], %[mbp] offen sc1\n\t" \
  "s_mov_b64 exec, -1\n\t" \
  "v_mfma_f32_16x16x32_bf16 a[116:119], v[132:135], v[4:7], a[116:119]\n\t" \
  "ds_read_b64_tr_b16 a[62:63], v235 offset:28672\n\t" \
  "v_mfma_f32_16x16x32_bf16 a[116:119], v[128:131], v[0:3], a[116:119]\n\t" \
  "s_xor_b32 s73, %[eso], 655360\n\t" \
  "v_mfma_f32_16x16x32_bf16 a[120:123], v[128:131], v[32:35], a[120:123]\n\t" \
  "v_xor_b32 v236, 0x20, v234\n\t" \
  "v_mfma_f32_16x16x32_bf16 a[120:123], v[132:135], v[36:39], a[120:123]\n\t" \
  "ds_read_b64_tr_b16 a[76:77], v236 offset:0\n\t" \
  "v_mfma_f32_16x16x32_bf16 a[120:123], v[136:139], v[40:43], a[120:123]\n\t" \
  "ds_read_b64_tr_b16 a[78:79], v236 offset:4096\n\t" \
  "v_mov_b32 v160, 0\n\t" \
  "v_mfma_f32_16x16x32_bf16 a[120:123], v[140:143], v[44:47], a[120:123]\n\t" \
  "s_waitcnt vmcnt(14) lgkmcnt(6)\n\t" \
  "buffer_load_dwordx4 v[16:19], v242, %[ws], s73 offen offset:0 sc1\n\t" \
  "v_mfma_f32_16x16x32_bf16 a[0:3], a[12:15], v[48:51], a[0:3]\n\t" \
  "ds_read_b64_tr_b16 a[92:93], v236 offset:8192\n\t" \
  "v_mov_b32 v161, 0\n\t" \
  "v_mfma_f32_16x16x32_bf16 a[0:3], a[28:31], v[52:55], a[0:3]\n\t" \
  "s_waitcnt lgkmcnt(3)\n\t" \
  "buffer_load_dwordx4 v[20:23], v242, %[ws], s73 offen offset:1024 sc1\n\t" \
  "v_mfma_f32_16x16x32_bf16 a[0:3], a[44:47], v[56:59], a[0:3]\n\t" \
  "ds_read_b64_tr_b16 a[94:95], v236 offset:12288\n\t" \
  "v_mov_b32 v162, 0\n\t" \
  "v_mfma_f32_16x16x32_bf16 a[0:3], a[60:63], v[60:63], a[0:3]\n\t" \
  "s_waitcnt vmcnt(12)\n\t" \
  "ds_read_b64_tr_b16 a[108:109], v236 offset:16384\n\t" \
  "v_mov_b32 v163, 0\n\t" \
  "v_mfma_f32_16x16x32_bf16 a[4:7], a[60:63], v[92:95], a[4:7]\n\t" \
  "ds_read_b64_tr_b16 a[110:111], v236 offset:20480\n\t" \
  "v_mov_b32 v164, 0x3f803f80\n\t" \
  "v_mfma_f32_16x16x32_bf16 a[4:7], a[44:47], v[88:91], a[4:7]\n\t" \
  "buffer_load_dwordx4 v[24:27], v242, %[ws], s73 offen offset:2048 sc1\n\t" \
  "v_mfma_f32_16x16x32_bf16 a[4:7], a[28:31], v[84:87], a[4:7]\n\t" \
  "ds_read_b64_tr_b16 a[124:125], v236 offset:24576\n\t" \
  "v_add_u32 v165, s70, v242\n\t" \
  "v_mfma_f32_16x16x32_bf16 a[4:7], a[12:15], v[80:83], a[4:7]\n\t" \
  "s_waitcnt vmcnt(9)\n\t" \
  "ds_read_b64_tr_b16 a[126:127], v236 offset:28672\n\t" \
  "v_mfma_f32_16x16x32_bf16 a[8:11], a[12:15], v[64:67], a[8:11]\n\t" \
  "v_xor_b32 v237, 0x40, v234\n\t" \
  "v_mfma_f32_16x16x32_bf16 a[8:11], a[28:31], v[68:71], a[8:11]\n\t" \
  "buffer_load_dwordx4 v[28:31], v242, %[ws], s73 offen offset:3072 sc1\n\t" \
  "s_xor_b32 s73, %[eso], 688128\n\t" \
  "v_mfma_f32_16x16x32_bf16 a[8:11], a[44:47], v[72:75], a[8:11]\n\t" \
  "ds_read_b64_tr_b16 v[144:145], v237 offset:0\n\t" \
  "v_mfma_f32_16x16x32_bf16 a[8:11], a[60:63], v[76:79], a[8:11]\n\t" \
  "s_waitcnt lgkmcnt(5)\n\t" \
  "ds_read_b64_tr_b16 v[146:147], v237 offset:4096\n\t" \
  "v_mfma_f32_16x16x32_bf16 a[16:19], a[76:79], v[48:51], a[16:19]\n\t" \
  "ds_read_b64_tr_b16 v[148:149], v237 offset:8192\n\t" \
  "v_mfma_f32_16x16x32_bf16 a[16:19], a[92:95], v[52:55], a[16:19]\n\t" \
  "s_waitcnt lgkmcnt(3)\n\t" \
  "ds_read_b64_tr_b16 v[150:151], v237 offset:12288\n\t" \
  "v_mfma_f32_16x16x32_bf16 a[16:19], a[108:111], v[56:59], a[16:19]\n\t" \
  "buffer_load_dwordx4 v[0:3], v242, %[ws], s73 offen offset:0 sc1\n\t" \
  "v_mfma_f32_16x16x32_bf16 a[16:19], a[124:127], v[60:63], a[16:19]\n\t" \
  "ds_read_b64_tr_b16 v[152:153], v237 offset:16384\n\t" \
  "ds_write_b128 v165, v[160:163]\n\t" \
  "v_mfma_f32_16x16x32_bf16 a[20:23], a[124:127], v[92:95], a[20:23]\n\t" \
  "ds_read_b64_tr_b16 v[154:155], v237 offset:20480\n\t" \
  "v_and_b32 v171, 15, v244\n\t" \
  "v_mfma_f32_16x16x32_bf16 a[20:23], a[108:111], v[88:91], a[20:23]\n\t" \
  "ds_read_b64_tr_b16 v[156:157], v237 offset:24576\n\t" \
  "v_lshrrev_b32 v172, 4, v244\n\t" \
  "v_mfma_f32_16x16x32_bf16 a[20:23], a[92:95], v[84:87], a[20:23]\n\t" \
  "buffer_load_dwordx4 v[4:7], v242, %[ws], s73 offen offset:1024 sc1\n\t" \
  "v_mfma_f32_16x16x32_bf16 a[20:23], a[76:79], v[80:83], a[20:23]\n\t" \
  "ds_read_b64_tr_b16 v[158:159], v237 offset:28672\n\t" \
  "v_lshlrev_b32 v170, 13, v172\n\t" \
  "v_mfma_f32_16x16x32_bf16 a[24:27], a[76:79], v[64:67], a[24:27]\n\t" \
  "v_xor_b32 v235, 0x60, v234\n\t" \
  "v_lshl_add_u32 v170, v171, 4, v170\n\t" \
  "v_mfma_f32_16x16x32_bf16 a[24:27], a[92:95], v[68:71], a[24:27]\n\t" \
  "ds_read_b64_tr_b16 v[128:129], v235 offset:0\n\t" \
  "v_lshlrev_b32 v174, 7, v172\n\t" \
  "v_mfma_f32_16x16x32_bf16 a[24:27], a[108:111], v[72:75], a[24:27]\n\t" \
  "buffer_load_dwordx4 v[8:11], v242, %[ws], s73 offen offset:2048 sc1\n\t" \
  "v_mfma_f32_16x16x32_bf16 a[24:27], a[124:127], v[76:79], a[24:27]\n\t" \
  "s_waitcnt lgkmcnt(6)\n\t" \
  "ds_read_b64_tr_b16 v[130:131], v235 offset:4096\n\t" \
  "v_lshl_add_u32 v174, v171, 3, v174\n\t" \
  "v_mfma_f32_16x16x32_bf16 a[32:35], v[144:147], v[48:51], a[32:35]\n\t" \
  "ds_read_b64_tr_b16 v[132:133], v235 offset:8192\n\t" \
  "v_add_u32 v174, s70, v174\n\t" \
  "v_mfma_f32_16x16x32_bf16 a[32:35], v[148:151], v[52:55], a[32:35]\n\t" \
  "s_waitcnt lgkmcnt(3)\n\t" \
  "ds_read_b64_tr_b16 v[134:135], v235 offset:12288\n\t" \
  "v_mfma_f32_16x16x32_bf16 a[32:35], v[152:155], v[56:59], a[32:35]\n\t" \
  "ds_read_b64_tr_b16 v[136:137], v235 offset:16384\n\t" \
  "v_mfma_f32_16x16x32_bf16 a[32:35], v[156:159], v[60:63], a[32:35]\n\t" \
  "buffer_load_dwordx4 v[12:15], v242, %[ws], s73 offen offset:3072 sc1\n\t" \
  "s_xor_b32 s73, %[eso], 720896\n\t" \
  "v_mfma_f32_16x16x32_bf16 a[36:39], v[156:159], v[92:95], a[36:39]\n\t" \
  "ds_read_b64_tr_b16 v[138:139], v235 offset:20480\n\t" \
  "v_mfma_f32_16x16x32_bf16 a[36:39], v[152:155], v[88:91], a[36:39]\n\t" \
  "ds_read_b64_tr_b16 v[140:141], v235 offset:24576\n\t" \
  "v_mfma_f32_16x16x32_bf16 a[36:39], v[148:151], v[84:87], a[36:39]\n\t" \
  "ds_read_b64_tr_b16 v[142:143], v235 offset:28672\n\t" \
  "ds_write_b128 v165, v[160:163] offset:1024\n\t" \
  "v_mfma_f32_16x16x32_bf16 a[36:39], v[144:147], v[80:83], a[36:39]\n\t" \
  "buffer_load_dwordx4 v[32:35], v242, %[ws], s73 offen offset:0 sc1\n\t" \
  "v_mfma_f32_16x16x32_bf16 a[40:43], v[144:147], v[64:67], a[40:43]\n\t" \
  "v_lshlrev_b32 v171, 2, v171\n\t" \
  "v_xor_b32 v236, 0x80, v234\n\t" \
  "v_mfma_f32_16x16x32_bf16 a[40:43], v[148:151], v[68:71], a[40:43]\n\t" \
  "v_cmp_eq_u32 vcc, 2, v172\n\t" \
  "s_nop 1\n\t" \
  "v_cndmask_b32 v172, 0, 1.0, vcc\n\t" \
  "v_mfma_f32_16x16x32_bf16 a[40:43], v[152:155], v[72:75], a[40:43]\n\t" \
  "ds_read_b64_tr_b16 a[208:209], v236 offset:0\n\t" \
  "v_mfma_f32_16x16x32_bf16 a[40:43], v[156:159], v[76:79], a[40:43]\n\t" \
  "s_waitcnt lgkmcnt(6)\n\t" \
  "ds_read_b64_tr_b16 a[210:211], v236 offset:4096\n\t" \
  "v_mfma_f32_16x16x32_bf16 a[48:51], v[128:131], v[48:51], a[48:51]\n\t" \
  "s_add_u32 m0, %[m], 114688\n\t" \
  "buffer_load_dwordx4 v[36:39], v242, %[ws], s73 offen offset:1024 sc1\n\t" \
  "v_mfma_f32_16x16x32_bf16 a[48:51], v[132:135], v[52:55], a[48:51]\n\t" \
  "s_waitcnt lgkmcnt(3)\n\t" \
  "ds_read_b64_tr_b16 a[212:213], v236 offset:8192\n\t" \
  "ds_write_b128 v165, v[160:163] offset:1536\n\t" \
  "v_mfma_f32_16x16x32_bf16 a[48:51], v[136:139], v[56:59], a[48:51]\n\t" \
  "ds_read_b64_tr_b16 a[214:215], v236 offset:12288\n\t" \
  "s_waitcnt lgkmcnt(1)\n\t" \
  "v_bfe_u32 v160, v244, 3, 1\n\t" \
  "v_mfma_f32_16x16x32_bf16 a[48:51], v[140:143], v[60:63], a[48:51]\n\t" \
  "s_barrier\n\t" \
  "v_mfma_f32_16x16x32_bf16 a[52:55], v[140:143], v[92:95], a[52:55]\n\t" \
  "buffer_load_dwordx4 v[40:43], v242, %[ws], s73 offen offset:2048 sc1\n\t" \
  "v_bfe_u32 v173, v244, 1, 2\n\t" \
  "v_mfma_f32_16x16x32_bf16 a[52:55], v[136:139], v[88:91], a[52:55]\n\t" \
  "s_xor_b32 s71, %[wob], 1703936\n\t" \
  "ds_read_b64_tr_b16 a[216:217], v236 offset:16384\n\t" \
  "ds_read_b64_tr_b16 a[218:219], v236 offset:20480\n\t" \
  "v_mfma_f32_16x16x32_bf16 a[52:55], v[132:135], v[84:87], a[52:55]\n\t" \
  "ds_read_b64_tr_b16 a[220:221], v236 offset:24576\n\t" \
  "v_lshlrev_b32 v173, 4, v173\n\t" \
  "v_mfma_f32_16x16x32_bf16 a[52:55], v[128:131], v[80:83], a[52:55]\n\t" \
  "ds_read_b64_tr_b16 a[222:223], v236 offset:28672\n\t" \
  "v_lshl_add_u32 v173, v160, 3, v173\n\t" \
  "v_mfma_f32_16x16x32_bf16 a[56:59], v[128:131], v[64:67], a[56:59]\n\t" \
  "buffer_load_dwordx4 v[44:47], v242, %[ws], s73 offen offset:3072 sc1\n\t" \
  "v_mfma_f32_16x16x32_bf16 a[56:59], v[132:135], v[68:71], a[56:59]\n\t" \
  "v_xor_b32 v237, 0xa0, v234\n\t" \
  "v_and_b32 v160, 1, v244\n\t" \
  "v_mfma_f32_16x16x32_bf16 a[56:59], v[136:139], v[72:75], a[56:59]\n\t" \
  "ds_read_b64_tr_b16 v[144:145], v237 offset:0\n\t" \
  "v_lshl_add_u32 v173, v160, 2, v173\n\t" \
  "v_mfma_f32_16x16x32_bf16 a[56:59], v[140:143], v[76:79], a[56:59]\n\t" \
  "s_waitcnt lgkmcnt(5)\n\t" \
  "ds_read_b64_tr_b16 v[146:147], v237 offset:4096\n\t" \
  "v_add_u32 v173, s70, v173\n\t" \
  "v_mfma_f32_16x16x32_bf16 a[64:67], a[208:211], v[48:51], a[64:67]\n\t" \
  "ds_read_b64_tr_b16 v[148:149], v237 offset:8192\n\t" \
  "v_mfma_f32_16x16x32_bf16 a[64:67], a[212:215], v[52:55], a[64:67]\n\t" \
  "s_waitcnt lgkmcnt(3)\n\t" \
  "buffer_load_dwordx4 v240, %[wo], s71 offen offset:0 lds\n\t" \
  "v_mfma_f32_16x16x32_bf16 a[64:67], a[216:219], v[56:59], a[64:67]\n\t" \
  "ds_read_b64_tr_b16 v[150:151], v237 offset:12288\n\t" \
  "v_mfma_f32_16x16x32_bf16 a[64:67], a[220:223], v[60:63], a[64:67]\n\t" \
  "ds_read_b64_tr_b16 v[152:153], v237 offset:16384\n\t" \
  "v_mfma_f32_16x16x32_bf16 a[68:71], a[220:223], v[92:95], a[68:71]\n\t" \
  "ds_read_b64_tr_b16 v[154:155], v237 offset:20480\n\t" \
  "v_mfma_f32_16x16x32_bf16 a[68:71], a[216:219], v[88:91], a[68:71]\n\t" \
  "buffer_load_dwordx4 v241, %[wo], s71 offen offset:1024 lds\n\t" \
  "v_mfma_f32_16x16x32_bf16 a[68:71], a[212:215], v[84:87], a[68:71]\n\t" \
  "s_mov_b64 exec, 0xffff\n\t" \
  "ds_write_b32 v173, v164 offset:960\n\t" \
  "s_mov_b64 exec, -1\n\t" \
  "v_mfma_f32_16x16x32_bf16 a[68:71], a[208:211], v[80:83], a[68:71]\n\t" \
  "ds_read_b64_tr_b16 v[156:157], v237 offset:24576\n\t" \
  "ds_read_b64_tr_b16 v[158:159], v237 offset:28672\n\t" \
  "v_lshrrev_b32 v229, 4, v244\n\t" \
  "v_mfma_f32_16x16x32_bf16 a[72:75], a[208:211], v[64:67], a[72:75]\n\t" \
  "v_and_b32 v231, 15, v244\n\t" \
  "v_xor_b32 v248, v231, v229\n\t" \
  "v_mfma_f32_16x16x32_bf16 a[72:75], a[212:215], v[68:71], a[72:75]\n\t" \
  "v_lshlrev_b32 v248, 4, v248\n\t" \
  "v_xor_b32 v235, 0xc0, v234\n\t" \
  "v_mfma_f32_16x16x32_bf16 a[72:75], a[216:219], v[72:75], a[72:75]\n\t" \
  "buffer_load_dwordx4 v238, %[wo], s71 offen offset:2048 lds\n\t" \
  "v_lshl_add_u32 v248, v229, 11, v248\n\t" \
  "v_mfma_f32_16x16x32_bf16 a[72:75], a[220:223], v[76:79], a[72:75]\n\t" \
  "s_waitcnt lgkmcnt(5)\n\t" \
  "ds_read_b64_tr_b16 a[192:193], v235 offset:0\n\t" \
  "v_xor_b32 v249, 64, v248\n\t" \
  "v_mfma_f32_16x16x32_bf16 a[80:83], v[144:147], v[48:51], a[80:83]\n\t" \
  "ds_read_b64_tr_b16 a[194:195], v235 offset:4096\n\t" \
  "v_xor_b32 v250, 0x80, v248\n\t" \
  "v_mfma_f32_16x16x32_bf16 a[80:83], v[148:151], v[52:55], a[80:83]\n\t" \
  "s_waitcnt lgkmcnt(2)\n\t" \
  "ds_read_b64_tr_b16 a[196:197], v235 offset:8192\n\t" \
  "v_xor_b32 v251, 0xc0, v248\n\t" \
  "v_mfma_f32_16x16x32_bf16 a[80:83], v[152:155], v[56:59], a[80:83]\n\t" \
  "buffer_load_dwordx4 v239, %[wo], s71 offen offset:3072 lds\n\t" \
  "s_add_u32 m0, %[m], 131072\n\t" \
  "v_mfma_f32_16x16x32_bf16 a[80:83], v[156:159], v[60:63], a[80:83]\n\t" \
  "s_xor_b32 s71, %[wob], 1835008\n\t" \
  "ds_read_b64_tr_b16 a[198:199], v235 offset:12288\n\t" \
  "v_lshrrev_b32 v227, 5, v244\n\t" \
  "s_lshl_b32 s75, %[wid], 2\n\t" \
  "v_mfma_f32_16x16x32_bf16 a[84:87], v[156:159], v[92:95], a[84:87]\n\t" \
  "ds_read_b64_tr_b16 a[200:201], v235 offset:16384\n\t" \
  "v_add_u32 v227, s75, v227\n\t" \
  "v_mfma_f32_16x16x32_bf16 a[84:87], v[152:155], v[88:91], a[84:87]\n\t" \
  "ds_read_b64_tr_b16 a[202:203], v235 offset:20480\n\t" \
  "v_xor_b32 v227, v227, v231\n\t" \
  "v_mfma_f32_16x16x32_bf16 a[84:87], v[148:151], v[84:87], a[84:87]\n\t" \
  "buffer_load_dwordx4 v240, %[wo], s71 offen offset:0 lds\n\t" \
  "v_mfma_f32_16x16x32_bf16 a[84:87], v[144:147], v[80:83], a[84:87]\n\t" \
  "ds_read_b64_tr_b16 a[204:205], v235 offset:24576\n\t" \
  "ds_read_b64_tr_b16 a[206:207], v235 offset:28672\n\t" \
  "v_mfma_f32_16x16x32_bf16 a[88:91], v[144:147], v[64:67], a[88:91]\n\t" \
  "v_xor_b32 v236, 0xe0, v234\n\t" \
  "v_lshlrev_b32 v227, 4, v227\n\t" \
  "v_mfma_f32_16x16x32_bf16 a[88:91], v[148:151], v[68:71], a[88:91]\n\t" \
  "ds_read_b64_tr_b16 a[208:209], v236 offset:0\n\t" \
  "v_lshl_add_u32 v227, v231, 8, v227\n\t" \
  "v_mfma_f32_16x16x32_bf16 a[88:91], v[152:155], v[72:75], a[88:91]\n\t" \
  "ds_read_b64_tr_b16 a[210:211], v236 offset:4096\n\t" \
  "v_and_b32 v229, 1, v229\n\t" \
  "v_mfma_f32_16x16x32_bf16 a[88:91], v[156:159], v[76:79], a[88:91]\n\t" \
  "s_waitcnt lgkmcnt(6)\n\t" \
  "buffer_load_dwordx4 v241, %[wo], s71 offen offset:1024 lds\n\t" \
  "v_mfma_f32_16x16x32_bf16 a[96:99], a[192:195], v[48:51], a[96:99]\n\t" \
  "ds_read_b64_tr_b16 a[212:213], v236 offset:8192\n\t" \
  "v_lshl_add_u32 v227, v229, 3, v227\n\t" \
  "v_mfma_f32_16x16x32_bf16 a[96:99], a[196:199], v[52:55], a[96:99]\n\t" \
  "s_waitcnt lgkmcnt(3)\n\t" \
  "ds_read_b64_tr_b16 a[214:215], v236 offset:12288\n\t" \
  "v_add_u32 v231, 131072, v227\n\t" \
  "v_mfma_f32_16x16x32_bf16 a[96:99], a[200:203], v[56:59], a[96:99]\n\t" \
  "ds_read_b64_tr_b16 a[216:217], v236 offset:16384\n\t" \
  "v_xor_b32 v233, 32, v231\n\t" \
  "v_mfma_f32_16x16x32_bf16 a[96:99], a[204:207], v[60:63], a[96:99]\n\t" \
  "buffer_load_dwordx4 v238, %[wo], s71 offen offset:2048 lds\n\t" \
  "v_mfma_f32_16x16x32_bf16 a[100:103], a[204:207], v[92:95], a[100:103]\n\t" \
  "s_lshl_b32 s75, %[wid], 9\n\t" \
  "ds_read_b64_tr_b16 a[218:219], v236 offset:20480\n\t" \
  "v_mfma_f32_16x16x32_bf16 a[100:103], a[200:203], v[88:91], a[100:103]\n\t" \
  "ds_read_b64_tr_b16 a[220:221], v236 offset:24576\n\t" \
  "v_lshl_add_u32 v252, v244, 3, s75\n\t" \
  "v_mfma_f32_16x16x32_bf16 a[100:103], a[196:199], v[84:87], a[100:103]\n\t" \
  "ds_read_b64_tr_b16 a[222:223], v236 offset:28672\n\t" \
  "v_mfma_f32_16x16x32_bf16 a[100:103], a[192:195], v[80:83], a[100:103]\n\t" \
  "v_mfma_f32_16x16x32_bf16 a[104:107], a[192:195], v[64:67], a[104:107]\n\t" \
  "buffer_load_dwordx4 v239, %[wo], s71 offen offset:3072 lds\n\t" \
  "v_mfma_f32_16x16x32_bf16 a[104:107], a[196:199], v[68:71], a[104:107]\n\t" \
  "v_xor_b32 v237, 0x18000, v234\n\t" \
  "v_mfma_f32_16x16x32_bf16 a[104:107], a[200:203], v[72:75], a[104:107]\n\t" \
  "ds_read_b64_tr_b16 v[128:129], v237 offset:0\n\t" \
  "v_mfma_f32_16x16x32_bf16 a[104:107], a[204:207], v[76:79], a[104:107]\n\t" \
  "s_waitcnt lgkmcnt(5)\n\t" \
  "ds_read_b64_tr_b16 v[130:131], v237 offset:4096\n\t" \
  "v_mfma_f32_16x16x32_bf16 a[112:115], a[208:211], v[48:51], a[112:115]\n\t" \
  "ds_read_b64_tr_b16 v[132:133], v237 offset:8192\n\t" \
  "v_mfma_f32_16x16x32_bf16 a[112:115], a[212:215], v[52:55], a[112:115]\n\t" \
  "s_waitcnt lgkmcnt(3)\n\t" \
  "ds_read_b64_tr_b16 v[134:135], v237 offset:12288\n\t" \
  "v_xor_b32 v235, 0x8000, v234\n\t" \
  "v_mfma_f32_16x16x32_bf16 a[112:115], a[216:219], v[56:59], a[112:115]\n\t" \
  "ds_read_b64_tr_b16 v[136:137], v235 offset:0\n\t" \
  "v_mfma_f32_16x16x32_bf16 a[112:115], a[220:223], v[60:63], a[112:115]\n\t" \
  "ds_read_b64_tr_b16 v[138:139], v235 offset:4096\n\t" \
  "v_mfma_f32_16x16x32_bf16 a[116:119], a[220:223], v[92:95], a[116:119]\n\t" \
  "ds_read_b64_tr_b16 v[140:141], v235 offset:8192\n\t" \
  "v_mfma_f32_16x16x32_bf16 a[116:119], a[216:219], v[88:91], a[116:119]\n\t" \
  "ds_read_b64_tr_b16 v[142:143], v235 offset:12288\n\t" \
  "v_mfma_f32_16x16x32_bf16 a[116:119], a[212:215], v[84:87], a[116:119]\n\t" \
  "v_mfma_f32_16x16x32_bf16 a[116:119], a[208:211], v[80:83], a[116:119]\n\t" \
  "s_xor_b32 s73, %[eso], 786432\n\t" \
  "v_mfma_f32_16x16x32_bf16 a[120:123], a[208:211], v[64:67], a[120:123]\n\t" \
  "v_xor_b32 v236, 0x18020, v234\n\t" \
  "v_mfma_f32_16x16x32_bf16 a[120:123], a[212:215], v[68:71], a[120:123]\n\t" \
  "ds_read_b64_tr_b16 v[144:145], v236 offset:0\n\t" \
  "v_mfma_f32_16x16x32_bf16 a[120:123], a[216:219], v[72:75], a[120:123]\n\t" \
  "ds_read_b64_tr_b16 v[146:147], v236 offset:4096\n\t" \
  "v_mfma_f32_16x16x32_bf16 a[120:123], a[220:223], v[76:79], a[120:123]\n\t" \
  "s_waitcnt vmcnt(16) lgkmcnt(6)\n\t" \
  "buffer_load_dwordx4 v[48:51], v242, %[ws], s73 offen offset:0 sc1\n\t" \
  "v_mfma_f32_16x16x32_bf16 a[0:3], v[128:131], v[16:19], a[0:3]\n\t" \
  "ds_read_b64_tr_b16 v[148:149], v236 offset:8192\n\t" \
  "v_mfma_f32_16x16x32_bf16 a[0:3], v[132:135], v[20:23], a[0:3]\n\t" \
  "s_waitcnt lgkmcnt(3)\n\t" \
  "buffer_load_dwordx4 v[52:55], v242, %[ws], s73 offen offset:1024 sc1\n\t" \
  "v_mfma_f32_16x16x32_bf16 a[0:3], v[136:139], v[24:27], a[0:3]\n\t" \
  "ds_read_b64_tr_b16 v[150:151], v236 offset:12288\n\t" \
  "v_xor_b32 v237, 0x8020, v234\n\t" \
  "v_mfma_f32_16x16x32_bf16 a[0:3], v[140:143], v[28:31], a[0:3]\n\t" \
  "s_waitcnt vmcnt(14)\n\t" \
  "ds_read_b64_tr_b16 v[152:153], v237 offset:0\n\t" \
  "v_mfma_f32_16x16x32_bf16 a[4:7], v[140:143], v[12:15], a[4:7]\n\t" \
  "ds_read_b64_tr_b16 v[154:155], v237 offset:4096\n\t" \
  "v_mfma_f32_16x16x32_bf16 a[4:7], v[136:139], v[8:11], a[4:7]\n\t" \
  "buffer_load_dwordx4 v[56:59], v242, %[ws], s73 offen offset:2048 sc1\n\t" \
  "v_mfma_f32_16x16x32_bf16 a[4:7], v[132:135], v[4:7], a[4:7]\n\t" \
  "ds_read_b64_tr_b16 v[156:157], v237 offset:8192\n\t" \
  "v_mfma_f32_16x16x32_bf16 a[4:7], v[128:131], v[0:3], a[4:7]\n\t" \
  "s_waitcnt vmcnt(11)\n\t" \
  "ds_read_b64_tr_b16 v[158:159], v237 offset:12288\n\t" \
  "v_mfma_f32_16x16x32_bf16 a[8:11], v[128:131], v[32:35], a[8:11]\n\t" \
  "v_xor_b32 v235, 0x18040, v234\n\t" \
  "v_mfma_f32_16x16x32_bf16 a[8:11], v[132:135], v[36:39], a[8:11]\n\t" \
  "buffer_load_dwordx4 v[60:63], v242, %[ws], s73 offen offset:3072 sc1\n\t" \
  "s_xor_b32 s73, %[eso], 819200\n\t" \
  "v_mfma_f32_16x16x32_bf16 a[8:11], v[136:139], v[40:43], a[8:11]\n\t" \
  "ds_read_b64_tr_b16 a[208:209], v235 offset:0\n\t" \
  "v_mfma_f32_16x16x32_bf16 a[8:11], v[140:143], v[44:47], a[8:11]\n\t" \
  "s_waitcnt lgkmcnt(5)\n\t" \
  "ds_read_b64_tr_b16 a[210:211], v235 offset:4096\n\t" \
  "buffer_load_dword v246, v245, %[ws], %[fw2] offen sc1\n\t" \
  "v_mfma_f32_16x16x32_bf16 a[16:19], v[144:147], v[16:19], a[16:19]\n\t" \
  "ds_read_b64_tr_b16 a[212:213], v235 offset:8192\n\t" \
  "v_mfma_f32_16x16x32_bf16 a[16:19], v[148:151], v[20:23], a[16:19]\n\t" \
  "s_waitcnt lgkmcnt(3)\n\t" \
  "ds_read_b64_tr_b16 a[214:215], v235 offset:12288\n\t" \
  "v_xor_b32 v236, 0x8040, v234\n\t" \
  "v_mfma_f32_16x16x32_bf16 a[16:19], v[152:155], v[24:27], a[16:19]\n\t" \
  "ds_read_b64_tr_b16 a[216:217], v236 offset:0\n\t" \
  "v_mfma_f32_16x16x32_bf16 a[16:19], v[156:159], v[28:31], a[16:19]\n\t" \
  "buffer_load_dwordx4 v[80:83], v242, %[ws], s73 offen offset:0 sc1\n\t" \
  "v_mfma_f32_16x16x32_bf16 a[20:23], v[156:159], v[12:15], a[20:23]\n\t" \
  "ds_read_b64_tr_b16 a[218:219], v236 offset:4096\n\t" \
  "v_mfma_f32_16x16x32_bf16 a[20:23], v[152:155], v[8:11], a[20:23]\n\t" \
  "ds_read_b64_tr_b16 a[220:221], v236 offset:8192\n\t" \
  "v_mfma_f32_16x16x32_bf16 a[20:23], v[148:151], v[4:7], a[20:23]\n\t" \
  "ds_read_b64_tr_b16 a[222:223], v236 offset:12288\n\t" \
  "v_mfma_f32_16x16x32_bf16 a[20:23], v[144:147], v[0:3], a[20:23]\n\t" \
  "buffer_load_dwordx4 v[84:87], v242, %[ws], s73 offen offset:1024 sc1\n\t" \
  "v_mfma_f32_16x16x32_bf16 a[24:27], v[144:147], v[32:35], a[24:27]\n\t" \
  "v_xor_b32 v237, 0x18060, v234\n\t" \
  "v_mfma_f32_16x16x32_bf16 a[24:27], v[148:151], v[36:39], a[24:27]\n\t" \
  "ds_read_b64_tr_b16 a[192:193], v237 offset:0\n\t" \
  "v_mfma_f32_16x16x32_bf16 a[24:27], v[152:155], v[40:43], a[24:27]\n\t" \
  "ds_read_b64_tr_b16 a[194:195], v237 offset:4096\n\t" \
  "v_mfma_f32_16x16x32_bf16 a[24:27], v[156:159], v[44:47], a[24:27]\n\t" \
  "s_waitcnt lgkmcnt(6)\n\t" \
  "ds_read_b64_tr_b16 a[196:197], v237 offset:8192\n\t" \
  "v_mfma_f32_16x16x32_bf16 a[32:35], a[208:211], v[16:19], a[32:35]\n\t" \
  "buffer_load_dwordx4 v[88:91], v242, %[ws], s73 offen offset:2048 sc1\n\t" \
  "v_mfma_f32_16x16x32_bf16 a[32:35], a[212:215], v[20:23], a[32:35]\n\t" \
  "s_waitcnt lgkmcnt(3)\n\t" \
  "ds_read_b64_tr_b16 a[198:199], v237 offset:12288\n\t" \
  "v_xor_b32 v235, 0x8060, v234\n\t" \
  "v_mfma_f32_16x16x32_bf16 a[32:35], a[216:219], v[24:27], a[32:35]\n\t" \
  "ds_read_b64_tr_b16 a[200:201], v235 offset:0\n\t" \
  "v_mfma_f32_16x16x32_bf16 a[32:35], a[220:223], v[28:31], a[32:35]\n\t" \
  "ds_read_b64_tr_b16 a[202:203], v235 offset:4096\n\t" \
  "v_mfma_f32_16x16x32_bf16 a[36:39], a[220:223], v[12:15], a[36:39]\n\t" \
  "buffer_load_dwordx4 v[92:95], v242, %[ws], s73 offen offset:3072 sc1\n\t" \
  "s_xor_b32 s73, %[eso], 851968\n\t" \
  "v_mfma_f32_16x16x32_bf16 a[36:39], a[216:219], v[8:11], a[36:39]\n\t" \
  "ds_read_b64_tr_b16 a[204:205], v235 offset:8192\n\t" \
  "v_mfma_f32_16x16x32_bf16 a[36:39], a[212:215], v[4:7], a[36:39]\n\t" \
  "ds_read_b64_tr_b16 a[206:207], v235 offset:12288\n\t" \
  "v_mfma_f32_16x16x32_bf16 a[36:39], a[208:211], v[0:3], a[36:39]\n\t" \
  "v_mfma_f32_16x16x32_bf16 a[40:43], a[208:211], v[32:35], a[40:43]\n\t" \
  "buffer_load_dwordx4 v[64:67], v242, %[ws], s73 offen offset:0 sc1\n\t" \
  "v_mfma_f32_16x16x32_bf16 a[40:43], a[212:215], v[36:39], a[40:43]\n\t" \
  "v_mfma_f32_16x16x32_bf16 a[40:43], a[216:219], v[40:43], a[40:43]\n\t" \
  "v_mfma_f32_16x16x32_bf16 a[40:43], a[220:223], v[44:47], a[40:43]\n\t" \
  "s_waitcnt lgkmcnt(4)\n\t" \
  "v_xor_b32 v236, 0x18080, v234\n\t" \
  "v_mfma_f32_16x16x32_bf16 a[48:51], a[192:195], v[16:19], a[48:51]\n\t" \
  "s_add_u32 m0, %[m], 0\n\t" \
  "ds_read_b64_tr_b16 a[208:209], v236 offset:0\n\t" \
  "v_mfma_f32_16x16x32_bf16 a[48:51], a[196:199], v[20:23], a[48:51]\n\t" \
  "s_waitcnt lgkmcnt(1)\n\t" \
  "buffer_load_dwordx4 v[68:71], v242, %[ws], s73 offen offset:1024 sc1\n\t" \
  "v_mfma_f32_16x16x32_bf16 a[48:51], a[200:203], v[24:27], a[48:51]\n\t" \
  "ds_read_b64_tr_b16 a[210:211], v236 offset:4096\n\t" \
  "v_mfma_f32_16x16x32_bf16 a[48:51], a[204:207], v[28:31], a[48:51]\n\t" \
  "s_waitcnt vmcnt(15)\n\t" \
  "s_barrier\n\t" \
  "v_mfma_f32_16x16x32_bf16 a[52:55], a[204:207], v[12:15], a[52:55]\n\t" \
  "s_xor_b32 s71, %[wob], 1966080\n\t" \
  "ds_read_b64_tr_b16 a[212:213], v236 offset:8192\n\t" \
  "v_mfma_f32_16x16x32_bf16 a[52:55], a[200:203], v[8:11], a[52:55]\n\t" \
  "buffer_load_dwordx4 v[72:75], v242, %[ws], s73 offen offset:2048 sc1\n\t" \
  "v_mfma_f32_16x16x32_bf16 a[52:55], a[196:199], v[4:7], a[52:55]\n\t" \
  "ds_read_b64_tr_b16 a[214:215], v236 offset:12288\n\t" \
  "v_xor_b32 v237, 0x8080, v234\n\t" \
  "v_mfma_f32_16x16x32_bf16 a[52:55], a[192:195], v[0:3], a[52:55]\n\t" \
  "ds_read_b64_tr_b16 a[216:217], v237 offset:0\n\t" \
  "v_mfma_f32_16x16x32_bf16 a[56:59], a[192:195], v[32:35], a[56:59]\n\t" \
  "ds_read_b64_tr_b16 a[218:219], v237 offset:4096\n\t" \
  "v_mfma_f32_16x16x32_bf16 a[56:59], a[196:199], v[36:39], a[56:59]\n\t" \
  "ds_read_b64_tr_b16 a[220:221], v237 offset:8192\n\t" \
  "v_mfma_f32_16x16x32_bf16 a[56:59], a[200:203], v[40:43], a[56:59]\n\t" \
  "buffer_load_dwordx4 v[76:79], v242, %[ws], s73 offen offset:3072 sc1\n\t" \
  "ds_read_b64_tr_b16 a[222:223], v237 offset:12288\n\t" \
  "v_mfma_f32_16x16x32_bf16 a[56:59], a[204:207], v[44:47], a[56:59]\n\t" \
  "s_waitcnt lgkmcnt(4)\n\t" \
  "v_xor_b32 v235, 0x180a0, v234\n\t" \
  "v_mfma_f32_16x16x32_bf16 a[64:67], a[208:211], v[16:19], a[64:67]\n\t" \
  "s_add_u32 s75, s69, 0\n\t" \
  "ds_read_b64_tr_b16 a[192:193], v235 offset:0\n\t" \
  "v_mfma_f32_16x16x32_bf16 a[64:67], a[212:215], v[20:23], a[64:67]\n\t" \
  "s_waitcnt lgkmcnt(1)\n\t" \
  "s_waitcnt vmcnt(8)\n\t" \
  "v_cmp_ne_u32 vcc, %[tag], v246\n\t" \
  "s_and_b32 vcc_lo, vcc_lo, 0xfe0000\n\t" \
  "s_cbranch_scc1 .Lop_s3\n\t" \
  ".Lop_k3:\n\t" \
  "v_mfma_f32_16x16x32_bf16 a[64:67], a[216:219], v[24:27], a[64:67]\n\t" \
  "buffer_load_dwordx4 v240, %[wo], s71 offen offset:0 lds\n\t" \
  "v_mfma_f32_16x16x32_bf16 a[64:67], a[220:223], v[28:31], a[64:67]\n\t" \
  "ds_read_b64_tr_b16 a[194:195], v235 offset:4096\n\t" \
  "v_mfma_f32_16x16x32_bf16 a[68:71], a[220:223], v[12:15], a[68:71]\n\t" \
  "ds_read_b64_tr_b16 a[196:197], v235 offset:8192\n\t" \
  "v_mfma_f32_16x16x32_bf16 a[68:71], a[216:219], v[8:11], a[68:71]\n\t" \
  "ds_read_b64_tr_b16 a[198:199], v235 offset:12288\n\t" \
  "v_xor_b32 v236, 0x80a0, v234\n\t" \
  "v_mfma_f32_16x16x32_bf16 a[68:71], a[212:215], v[4:7], a[68:71]\n\t" \
  "buffer_load_dwordx4 v[208:211], v170, %[ws], s75 offen sc1\n\t" \
  "v_mfma_f32_16x16x32_bf16 a[68:71], a[208:211], v[0:3], a[68:71]\n\t" \
  "s_add_u32 s75, s69, 32768\n\t" \
  "ds_read_b64_tr_b16 a[200:201], v236 offset:0\n\t" \
  "v_mfma_f32_16x16x32_bf16 a[72:75], a[208:211], v[32:35], a[72:75]\n\t" \
  "ds_read_b64_tr_b16 a[202:203], v236 offset:4096\n\t" \
  "v_mfma_f32_16x16x32_bf16 a[72:75], a[212:215], v[36:39], a[72:75]\n\t" \
  "ds_read_b64_tr_b16 a[204:205], v236 offset:8192\n\t" \
  "v_mfma_f32_16x16x32_bf16 a[72:75], a[216:219], v[40:43], a[72:75]\n\t" \
  "ds_read_b64_tr_b16 a[206:207], v236 offset:12288\n\t" \
  "v_mfma_f32_16x16x32_bf16 a[72:75], a[220:223], v[44:47], a[72:75]\n\t" \
  "s_waitcnt lgkmcnt(4)\n\t" \
  "buffer_load_dwordx4 v[212:215], v170, %[ws], s75 offen sc1\n\t" \
  "v_mfma_f32_16x16x32_bf16 a[80:83], a[192:195], v[16:19], a[80:83]\n\t" \
  "s_add_u32 s75, s69, 65536\n\t" \
  "v_xor_b32 v237, 0x180c0, v234\n\t" \
  "v_mfma_f32_16x16x32_bf16 a[80:83], a[196:199], v[20:23], a[80:83]\n\t" \
  "s_waitcnt lgkmcnt(0)\n\t" \
  "ds_read_b64_tr_b16 a[208:209], v237 offset:0\n\t" \
  "v_mfma_f32_16x16x32_bf16 a[80:83], a[200:203], v[24:27], a[80:83]\n\t" \
  "ds_read_b64_tr_b16 a[210:211], v237 offset:4096\n\t" \
  "v_mfma_f32_16x16x32_bf16 a[80:83], a[204:207], v[28:31], a[80:83]\n\t" \
  "buffer_load_dwordx4 v[216:219], v170, %[ws], s75 offen sc1\n\t" \
  "v_mfma_f32_16x16x32_bf16 a[84:87], a[204:207], v[12:15], a[84:87]\n\t" \
  "s_add_u32 s75, s69, 98304\n\t" \
  "ds_read_b64_tr_b16 a[212:213], v237 offset:8192\n\t" \
  "v_mfma_f32_16x16x32_bf16 a[84:87], a[200:203], v[8:11], a[84:87]\n\t" \
  "ds_read_b64_tr_b16 a[214:215], v237 offset:12288\n\t" \
  "v_xor_b32 v235, 0x80c0, v234\n\t" \
  "v_mfma_f32_16x16x32_bf16 a[84:87], a[196:199], v[4:7], a[84:87]\n\t" \
  "ds_read_b64_tr_b16 a[216:217], v235 offset:0\n\t" \
  "v_mfma_f32_16x16x32_bf16 a[84:87], a[192:195], v[0:3], a[84:87]\n\t" \
  "ds_read_b64_tr_b16 a[218:219], v235 offset:4096\n\t" \
  "v_mfma_f32_16x16x32_bf16 a[88:91], a[192:195], v[32:35], a[88:91]\n\t" \
  "buffer_load_dwordx4 v[220:223], v170, %[ws], s75 offen sc1\n\t" \
  "v_mfma_f32_16x16x32_bf16 a[88:91], a[196:199], v[36:39], a[88:91]\n\t" \
  "ds_read_b64_tr_b16 a[220:221], v235 offset:8192\n\t" \
  "v_mfma_f32_16x16x32_bf16 a[88:91], a[200:203], v[40:43], a[88:91]\n\t" \
  "ds_read_b64_tr_b16 a[222:223], v235 offset:12288\n\t" \
  "v_mfma_f32_16x16x32_bf16 a[88:91], a[204:207], v[44:47], a[88:91]\n\t" \
  "s_waitcnt lgkmcnt(4)\n\t" \
  "v_xor_b32 v236, 0x180e0, v234\n\t" \
  "v_mfma_f32_16x16x32_bf16 a[96:99], a[208:211], v[16:19], a[96:99]\n\t" \
  "buffer_load_dwordx4 v241, %[wo], s71 offen offset:1024 lds\n\t" \
  "v_mfma_f32_16x16x32_bf16 a[96:99], a[212:215], v[20:23], a[96:99]\n\t" \
  "s_waitcnt lgkmcnt(0)\n\t" \
  "ds_read_b64_tr_b16 a[192:193], v236 offset:0\n\t" \
  "v_mfma_f32_16x16x32_bf16 a[96:99], a[216:219], v[24:27], a[96:99]\n\t" \
  "ds_read_b64_tr_b16 a[194:195], v236 offset:4096\n\t" \
  "v_mfma_f32_16x16x32_bf16 a[96:99], a[220:223], v[28:31], a[96:99]\n\t" \
  "ds_read_b64_tr_b16 a[196:197], v236 offset:8192\n\t" \
  "v_mfma_f32_16x16x32_bf16 a[100:103], a[220:223], v[12:15], a[100:103]\n\t" \
  "buffer_load_dwordx4 v238, %[wo], s71 offen offset:2048 lds\n\t" \
  "v_mfma_f32_16x16x32_bf16 a[100:103], a[216:219], v[8:11], a[100:103]\n\t" \
  "ds_read_b64_tr_b16 a[198:199], v236 offset:12288\n\t" \
  "v_xor_b32 v237, 0x80e0, v234\n\t" \
  "v_mfma_f32_16x16x32_bf16 a[100:103], a[212:215], v[4:7], a[100:103]\n\t" \
  "ds_read_b64_tr_b16 a[200:201], v237 offset:0\n\t" \
  "v_mfma_f32_16x16x32_bf16 a[100:103], a[208:211], v[0:3], a[100:103]\n\t" \
  "ds_read_b64_tr_b16 a[202:203], v237 offset:4096\n\t" \
  "v_mfma_f32_16x16x32_bf16 a[104:107], a[208:211], v[32:35], a[104:107]\n\t" \
  "ds_read_b64_tr_b16 a[204:205], v237 offset:8192\n\t" \
  "v_mfma_f32_16x16x32_bf16 a[104:107], a[212:215], v[36:39], a[104:107]\n\t" \
  "buffer_load_dwordx4 v239, %[wo], s71 offen offset:3072 lds\n\t" \
  "s_add_u32 m0, %[m], 16384\n\t" \
  "buffer_load_dword v224, v171, %[sr], s77 offen\n\t" \
  "v_mfma_f32_16x16x32_bf16 a[104:107], a[216:219], v[40:43], a[104:107]\n\t" \
  "s_xor_b32 s71, %[eso], 1015808\n\t" \
  "ds_read_b64_tr_b16 a[206:207], v237 offset:12288\n\t" \
  "v_mfma_f32_16x16x32_bf16 a[104:107], a[220:223], v[44:47], a[104:107]\n\t" \
  "s_waitcnt lgkmcnt(4)\n\t" \
  "v_xor_b32 v235, 0x14000, v234\n\t" \
  "v_mfma_f32_16x16x32_bf16 a[112:115], a[192:195], v[16:19], a[112:115]\n\t" \
  "ds_read_b64_tr_b16 a[208:209], v235 offset:0\n\t" \
  "v_mfma_f32_16x16x32_bf16 a[112:115], a[196:199], v[20:23], a[112:115]\n\t" \
  "s_waitcnt lgkmcnt(1)\n\t" \
  "ds_read_b64_tr_b16 a[210:211], v235 offset:4096\n\t" \
  "v_mfma_f32_16x16x32_bf16 a[112:115], a[200:203], v[24:27], a[112:115]\n\t" \
  "ds_read_b64_tr_b16 a[212:213], v235 offset:8192\n\t" \
  "v_mfma_f32_16x16x32_bf16 a[112:115], a[204:207], v[28:31], a[112:115]\n\t" \
  "buffer_load_dwordx4 v242, %[ws], s71 offen offset:0 sc1 lds\n\t" \
  "v_mfma_f32_16x16x32_bf16 a[116:119], a[204:207], v[12:15], a[116:119]\n\t" \
  "ds_read_b64_tr_b16 a[214:215], v235 offset:12288\n\t" \
  "v_mfma_f32_16x16x32_bf16 a[116:119], a[200:203], v[8:11], a[116:119]\n\t" \
  "ds_read_b64_tr_b16 a[216:217], v235 offset:32768\n\t" \
  "v_mfma_f32_16x16x32_bf16 a[116:119], a[196:199], v[4:7], a[116:119]\n\t" \
  "ds_read_b64_tr_b16 a[218:219], v235 offset:36864\n\t" \
  "v_mfma_f32_16x16x32_bf16 a[116:119], a[192:195], v[0:3], a[116:119]\n\t" \
  "buffer_load_dwordx4 v242, %[ws], s71 offen offset:1024 sc1 lds\n\t" \
  "v_mfma_f32_16x16x32_bf16 a[120:123], a[192:195], v[32:35], a[120:123]\n\t" \
  ".Lhs13:\n\ts_cbranch_execz .Lhs13n\n\t" \
  "s_xor_b32 s73, %[eso], 917504\n\t" \
  "ds_read_b64_tr_b16 a[220:221], v235 offset:40960\n\t" \
  "v_mfma_f32_16x16x32_bf16 a[120:123], a[196:199], v[36:39], a[120:123]\n\t" \
  "ds_read_b64_tr_b16 a[222:223], v235 offset:45056\n\t" \
  "v_mfma_f32_16x16x32_bf16 a[120:123], a[200:203], v[40:43], a[120:123]\n\t" \
  "v_mfma_f32_16x16x32_bf16 a[120:123], a[204:207], v[44:47], a[120:123]\n\t" \
  "s_waitcnt lgkmcnt(4)\n\t" \
  "buffer_load_dwordx4 v[16:19], v242, %[ws], s73 offen offset:0 sc1\n\t" \
  STP("v_writelane_b32 %[stv], s94, 30\n\t") \
  STP("v_writelane_b32 %[stv], s92, 31\n\t") \
  STP("s_memrealtime s[94:95]\n\t") \
  STP("s_memtime s[92:93]\n\t") \
  "v_mfma_f32_16x16x32_bf16 a[0:3], a[208:211], v[48:51], a[0:3]\n\t" \
  "v_xor_b32 v236, 0x14020, v234\n\t" \
  "v_perm_b32 v241, v180, v176, s79\n\t" \
  "v_mfma_f32_16x16x32_bf16 a[0:3], a[212:215], v[52:55], a[0:3]\n\t" \
  "s_waitcnt lgkmcnt(0)\n\t" \
  "ds_read_b64_tr_b16 a[192:193], v236 offset:0\n\t" \
  "v_perm_b32 v180, v180, v176, s80\n\t" \
  "v_mfma_f32_16x16x32_bf16 a[0:3], a[216:219], v[56:59], a[0:3]\n\t" \
  "ds_read_b64_tr_b16 a[194:195], v236 offset:4096\n\t" \
  "v_perm_b32 v176, v181, v177, s79\n\t" \
  "v_mfma_f32_16x16x32_bf16 a[0:3], a[220:223], v[60:63], a[0:3]\n\t" \
  "s_waitcnt vmcnt(16)\n\t" \
  "ds_read_b64_tr_b16 a[196:197], v236 offset:8192\n\t" \
  "v_perm_b32 v181, v181, v177, s80\n\t" \
  "v_mfma_f32_16x16x32_bf16 a[4:7], a[220:223], v[92:95], a[4:7]\n\t" \
  "buffer_load_dwordx4 v[20:23], v242, %[ws], s73 offen offset:1024 sc1\n\t" \
  "v_mfma_f32_16x16x32_bf16 a[4:7], a[216:219], v[88:91], a[4:7]\n\t" \
  "ds_read_b64_tr_b16 a[198:199], v236 offset:12288\n\t" \
  "v_perm_b32 v177, v182, v178, s79\n\t" \
  "v_mfma_f32_16x16x32_bf16 a[4:7], a[212:215], v[84:87], a[4:7]\n\t" \
  "ds_read_b64_tr_b16 a[200:201], v236 offset:32768\n\t" \
  "v_mfma_f32_16x16x32_bf16 a[4:7], a[208:211], v[80:83], a[4:7]\n\t" \
  "s_waitcnt vmcnt(13)\n\t" \
  "ds_read_b64_tr_b16 a[202:203], v236 offset:36864\n\t" \
  "v_mfma_f32_16x16x32_bf16 a[8:11], a[208:211], v[64:67], a[8:11]\n\t" \
  "buffer_load_dwordx4 v[24:27], v242, %[ws], s73 offen offset:2048 sc1\n\t" \
  "v_mfma_f32_16x16x32_bf16 a[8:11], a[212:215], v[68:71], a[8:11]\n\t" \
  "ds_read_b64_tr_b16 a[204:205], v236 offset:40960\n\t" \
  "v_perm_b32 v182, v182, v178, s80\n\t" \
  "v_mfma_f32_16x16x32_bf16 a[8:11], a[216:219], v[72:75], a[8:11]\n\t" \
  "ds_read_b64_tr_b16 a[206:207], v236 offset:45056\n\t" \
  "v_mfma_f32_16x16x32_bf16 a[8:11], a[220:223], v[76:79], a[8:11]\n\t" \
  "s_waitcnt lgkmcnt(4)\n\t" \
  "v_xor_b32 v237, 0x14040, v234\n\t" \
  "v_perm_b32 v178, v183, v179, s79\n\t" \
  "v_mfma_f32_16x16x32_bf16 a[16:19], a[192:195], v[48:51], a[16:19]\n\t" \
  "ds_read_b64_tr_b16 a[208:209], v237 offset:0\n\t" \
  "v_mfma_f32_16x16x32_bf16 a[16:19], a[196:199], v[52:55], a[16:19]\n\t" \
  "s_waitcnt lgkmcnt(1)\n\t" \
  "buffer_load_dwordx4 v[28:31], v242, %[ws], s73 offen offset:3072 sc1\n\t" \
  "s_xor_b32 s73, %[eso], 950272\n\t" \
  "v_mfma_f32_16x16x32_bf16 a[16:19], a[200:203], v[56:59], a[16:19]\n\t" \
  "ds_read_b64_tr_b16 a[210:211], v237 offset:4096\n\t" \
  "v_perm_b32 v183, v183, v179, s80\n\t" \
  "v_mfma_f32_16x16x32_bf16 a[16:19], a[204:207], v[60:63], a[16:19]\n\t" \
  "ds_read_b64_tr_b16 a[212:213], v237 offset:8192\n\t" \
  "v_mfma_f32_16x16x32_bf16 a[20:23], a[204:207], v[92:95], a[20:23]\n\t" \
  "ds_read_b64_tr_b16 a[214:215], v237 offset:12288\n\t" \
  "v_bfrev_b32 v225, 1\n\t" \
  "v_mfma_f32_16x16x32_bf16 a[20:23], a[200:203], v[88:91], a[20:23]\n\t" \
  "buffer_load_dwordx4 v[0:3], v242, %[ws], s73 offen offset:0 sc1\n\t" \
  "v_mfma_f32_16x16x32_bf16 a[20:23], a[196:199], v[84:87], a[20:23]\n\t" \
  "ds_read_b64_tr_b16 a[216:217], v237 offset:32768\n\t" \
  "v_bfrev_b32 v226, 1\n\t" \
  "v_mfma_f32_16x16x32_bf16 a[20:23], a[192:195], v[80:83], a[20:23]\n\t" \
  "ds_read_b64_tr_b16 a[218:219], v237 offset:36864\n\t" \
  "v_writelane_b32 v225, s65, 0\n\t" \
  "v_mfma_f32_16x16x32_bf16 a[24:27], a[192:195], v[64:67], a[24:27]\n\t" \
  "ds_read_b64_tr_b16 a[220:221], v237 offset:40960\n\t" \
  "v_perm_b32 v179, v188, v184, s79\n\t" \
  "v_mfma_f32_16x16x32_bf16 a[24:27], a[196:199], v[68:71], a[24:27]\n\t" \
  "buffer_load_dwordx4 v[4:7], v242, %[ws], s73 offen offset:1024 sc1\n\t" \
  "v_mfma_f32_16x16x32_bf16 a[24:27], a[200:203], v[72:75], a[24:27]\n\t" \
  "ds_read_b64_tr_b16 a[222:223], v237 offset:45056\n\t" \
  "v_writelane_b32 v225, s66, 1\n\t" \
  "v_mfma_f32_16x16x32_bf16 a[24:27], a[204:207], v[76:79], a[24:27]\n\t" \
  "s_waitcnt lgkmcnt(4)\n\t" \
  "s_waitcnt vmcnt(8)\n\t" \
  "v_xor_b32 v246, v243, v209\n\t" \
  "v_bitop3_b32 v246, v246, v211, v243 bitop3:0xf6\n\t" \
  "v_mfma_f32_16x16x32_bf16 a[32:35], a[208:211], v[48:51], a[32:35]\n\t" \
  "v_bitop3_b32 v246, v246, v213, v243 bitop3:0xf6\n\t" \
  "v_xor_b32 v235, 0x14060, v234\n\t" \
  "v_mfma_f32_16x16x32_bf16 a[32:35], a[212:215], v[52:55], a[32:35]\n\t" \
  "s_waitcnt lgkmcnt(0)\n\t" \
  "ds_read_b64_tr_b16 a[192:193], v235 offset:0\n\t" \
  "v_bitop3_b32 v246, v246, v215, v243 bitop3:0xf6\n\t" \
  "v_mfma_f32_16x16x32_bf16 a[32:35], a[216:219], v[56:59], a[32:35]\n\t" \
  "buffer_load_dwordx4 v[8:11], v242, %[ws], s73 offen offset:2048 sc1\n\t" \
  "v_mfma_f32_16x16x32_bf16 a[32:35], a[220:223], v[60:63], a[32:35]\n\t" \
  "ds_read_b64_tr_b16 a[194:195], v235 offset:4096\n\t" \
  "v_bitop3_b32 v246, v246, v217, v243 bitop3:0xf6\n\t" \
  "v_mfma_f32_16x16x32_bf16 a[36:39], a[220:223], v[92:95], a[36:39]\n\t" \
  "ds_read_b64_tr_b16 a[196:197], v235 offset:8192\n\t" \
  "v_bitop3_b32 v246, v246, v219, v243 bitop3:0xf6\n\t" \
  "v_mfma_f32_16x16x32_bf16 a[36:39], a[216:219], v[88:91], a[36:39]\n\t" \
  "ds_read_b64_tr_b16 a[198:199], v235 offset:12288\n\t" \
  "v_bitop3_b32 v246, v246, v221, v243 bitop3:0xf6\n\t" \
  "v_mfma_f32_16x16x32_bf16 a[36:39], a[212:215], v[84:87], a[36:39]\n\t" \
  "buffer_load_dwordx4 v[12:15], v242, %[ws], s73 offen offset:3072 sc1\n\t" \
  "s_xor_b32 s73, %[eso], 983040\n\t" \
  "v_mfma_f32_16x16x32_bf16 a[36:39], a[208:211], v[80:83], a[36:39]\n\t" \
  "ds_read_b64_tr_b16 a[200:201], v235 offset:32768\n\t" \
  "v_bitop3_b32 v246, v246, v223, v243 bitop3:0xf6\n\t" \
  "v_mfma_f32_16x16x32_bf16 a[40:43], a[208:211], v[64:67], a[40:43]\n\t" \
  "ds_read_b64_tr_b16 a[202:203], v235 offset:36864\n\t" \
  "v_perm_b32 v188, v188, v184, s80\n\t" \
  "v_mfma_f32_16x16x32_bf16 a[40:43], a[212:215], v[68:71], a[40:43]\n\t" \
  "ds_read_b64_tr_b16 a[204:205], v235 offset:40960\n\t" \
  "v_writelane_b32 v226, s67, 2\n\t" \
  "v_mfma_f32_16x16x32_bf16 a[40:43], a[216:219], v[72:75], a[40:43]\n\t" \
  "s_mov_b32 s81, 0\n\t" \
  "ds_read_b64_tr_b16 a[206:207], v235 offset:45056\n\t" \
  "v_perm_b32 v184, v189, v185, s79\n\t" \
  "v_mfma_f32_16x16x32_bf16 a[40:43], a[220:223], v[76:79], a[40:43]\n\t" \
  "s_waitcnt lgkmcnt(4)\n\t" \
  "buffer_load_dwordx4 v[32:35], v242, %[ws], s73 offen offset:0 sc1\n\t" \
  "v_mfma_f32_16x16x32_bf16 a[48:51], a[192:195], v[48:51], a[48:51]\n\t" \
  "v_cmp_ne_u32 vcc, 0, v246\n\t" \
  "s_cbranch_vccnz .Lop_s4\n\t" \
  ".Lop_k4:\n\t" \
  "v_mfma_f32_16x16x32_bf16 a[48:51], a[196:199], v[52:55], a[48:51]\n\t" \
  "s_waitcnt lgkmcnt(0)\n\t" \
  "v_add_f32 v208, v208, v212\n\t" \
  "v_add_f32 v216, v216, v220\n\t" \
  "v_add_f32 v210, v210, v214\n\t" \
  "v_mfma_f32_16x16x32_bf16 a[48:51], a[200:203], v[56:59], a[48:51]\n\t" \
  "v_xor_b32 v236, 0x14080, v234\n\t" \
  "v_add_f32 v218, v218, v222\n\t" \
  "v_add_f32 v208, v208, v216\n\t" \
  "v_mfma_f32_16x16x32_bf16 a[48:51], a[204:207], v[60:63], a[48:51]\n\t" \
  "s_barrier\n\t" \
  "v_mfma_f32_16x16x32_bf16 a[52:55], a[204:207], v[92:95], a[52:55]\n\t" \
  "buffer_load_dwordx4 v[36:39], v242, %[ws], s73 offen offset:1024 sc1\n\t" \
  "v_add_f32 v210, v210, v218\n\t" \
  "v_mfma_f32_16x16x32_bf16 a[52:55], a[200:203], v[88:91], a[52:55]\n\t" \
  "ds_read_b64_tr_b16 a[208:209], v236 offset:0\n\t" \
  "ds_read_b64_tr_b16 a[210:211], v236 offset:4096\n\t" \
  "v_perm_b32 v189, v189, v185, s80\n\t" \
  "v_mfma_f32_16x16x32_bf16 a[52:55], a[196:199], v[84:87], a[52:55]\n\t" \
  "ds_read_b64_tr_b16 a[212:213], v236 offset:8192\n\t" \
  "ds_read_b64_tr_b16 a[214:215], v236 offset:12288\n\t" \
  "v_lshlrev_b32 v163, 16, v224\n\t" \
  "v_mfma_f32_16x16x32_bf16 a[52:55], a[192:195], v[80:83], a[52:55]\n\t" \
  "buffer_load_dwordx4 v[40:43], v242, %[ws], s73 offen offset:2048 sc1\n\t" \
  "v_and_b32 v164, 0xffff0000, v224\n\t" \
  "v_mfma_f32_16x16x32_bf16 a[56:59], a[192:195], v[64:67], a[56:59]\n\t" \
  "ds_read_b64_tr_b16 a[216:217], v236 offset:32768\n\t" \
  "ds_read_b64_tr_b16 a[218:219], v236 offset:36864\n\t" \
  "v_perm_b32 v185, v190, v186, s79\n\t" \
  "v_mfma_f32_16x16x32_bf16 a[56:59], a[196:199], v[68:71], a[56:59]\n\t" \
  "ds_read_b64_tr_b16 a[220:221], v236 offset:40960\n\t" \
  "v_cvt_pk_bf16_f32 v162, v208, v210\n\t" \
  "v_lshlrev_b32 v160, 16, v162\n\t" \
  "v_mfma_f32_16x16x32_bf16 a[56:59], a[200:203], v[72:75], a[56:59]\n\t" \
  "ds_read_b64_tr_b16 a[222:223], v236 offset:45056\n\t" \
  "v_and_b32 v161, 0xffff0000, v162\n\t" \
  "v_perm_b32 v190, v190, v186, s80\n\t" \
  "v_mfma_f32_16x16x32_bf16 a[56:59], a[204:207], v[76:79], a[56:59]\n\t" \
  "s_waitcnt lgkmcnt(4)\n\t" \
  "v_add_f32 v160, v160, v172\n\t" \
  "v_add_f32 v161, v161, v172\n\t" \
  "v_mfma_f32_16x16x32_bf16 a[64:67], a[208:211], v[48:51], a[64:67]\n\t" \
  "buffer_load_dwordx4 v[44:47], v242, %[ws], s73 offen offset:3072 sc1\n\t" \
  "v_mfma_f32_16x16x32_bf16 a[64:67], a[212:215], v[52:55], a[64:67]\n\t" \
  "s_waitcnt lgkmcnt(0)\n\t" \
  "v_xor_b32 v237, 0x140a0, v234\n\t" \
  "v_perm_b32 v186, v191, v187, s79\n\t" \
  "v_cvt_pk_bf16_f32 v162, v160, v161\n\t" \
  "v_mfma_f32_16x16x32_bf16 a[64:67], a[216:219], v[56:59], a[64:67]\n\t" \
  "ds_read_b64_tr_b16 a[192:193], v237 offset:0\n\t" \
  "v_lshlrev_b32 v160, 16, v162\n\t" \
  "v_mfma_f32_16x16x32_bf16 a[64:67], a[220:223], v[60:63], a[64:67]\n\t" \
  "ds_read_b64_tr_b16 a[194:195], v237 offset:4096\n\t" \
  "v_and_b32 v161, 0xffff0000, v162\n\t" \
  "v_mfma_f32_16x16x32_bf16 a[68:71], a[220:223], v[92:95], a[68:71]\n\t" \
  "buffer_load_dwordx4 v242, %[ws], s71 offen offset:2048 sc1 lds\n\t" \
  "v_perm_b32 v191, v191, v187, s80\n\t" \
  "v_mfma_f32_16x16x32_bf16 a[68:71], a[216:219], v[88:91], a[68:71]\n\t" \
  "ds_read_b64_tr_b16 a[196:197], v237 offset:8192\n\t" \
  "v_perm_b32 v187, v196, v192, s79\n\t" \
  "v_mfma_f32_16x16x32_bf16 a[68:71], a[212:215], v[84:87], a[68:71]\n\t" \
  "ds_read_b64_tr_b16 a[198:199], v237 offset:12288\n\t" \
  "v_perm_b32 v196, v196, v192, s80\n\t" \
  "v_mfma_f32_16x16x32_bf16 a[68:71], a[208:211], v[80:83], a[68:71]\n\t" \
  "ds_read_b64_tr_b16 a[200:201], v237 offset:32768\n\t" \
  "v_perm_b32 v192, v197, v193, s79\n\t" \
  "v_mfma_f32_16x16x32_bf16 a[72:75], a[208:211], v[64:67], a[72:75]\n\t" \
  "ds_write_b64 v174, v[160:161] offset:2560\n\t" \
  "v_mul_f32 v166, v160, v163\n\t" \
  "v_mfma_f32_16x16x32_bf16 a[72:75], a[212:215], v[68:71], a[72:75]\n\t" \
  "buffer_load_dwordx4 v242, %[ws], s71 offen offset:3072 sc1 lds\n\t" \
  "s_add_u32 m0, %[m], 32768\n\t" \
  "v_mfma_f32_16x16x32_bf16 a[72:75], a[216:219], v[72:75], a[72:75]\n\t" \
  "s_xor_b32 s71, %[eso], 360448\n\t" \
  "ds_read_b64_tr_b16 a[202:203], v237 offset:36864\n\t" \
  "ds_read_b64_tr_b16 a[204:205], v237 offset:40960\n\t" \
  "v_mul_f32 v167, v161, v164\n\t" \
  "ds_read_b64_tr_b16 a[206:207], v237 offset:45056\n\t" \
  "v_mfma_f32_16x16x32_bf16 a[72:75], a[220:223], v[76:79], a[72:75]\n\t" \
  "s_waitcnt lgkmcnt(5)\n\t" \
  "v_cvt_pk_bf16_f32 v168, v166, v167\n\t" \
  "v_add_f32 v162, v166, v167\n\t" \
  "v_mfma_f32_16x16x32_bf16 a[80:83], a[192:195], v[48:51], a[80:83]\n\t" \
  "v_lshlrev_b32 v163, 16, v168\n\t" \
  "v_xor_b32 v235, 0x140c0, v234\n\t" \
  "v_mfma_f32_16x16x32_bf16 a[80:83], a[196:199], v[52:55], a[80:83]\n\t" \
  "s_waitcnt lgkmcnt(0)\n\t" \
  "buffer_load_dwordx4 v242, %[ws], s71 offen offset:0 sc1 lds\n\t" \
  "v_and_b32 v164, 0xffff0000, v168\n\t" \
  "v_mfma_f32_16x16x32_bf16 a[80:83], a[200:203], v[56:59], a[80:83]\n\t" \
  "ds_read_b64_tr_b16 a[208:209], v235 offset:0\n\t" \
  "v_add_f32_dpp v162, v162, v162 row_ror:8 row_mask:0xf bank_mask:0xf\n\t" \
  "v_mfma_f32_16x16x32_bf16 a[80:83], a[204:207], v[60:63], a[80:83]\n\t" \
  "ds_read_b64_tr_b16 a[210:211], v235 offset:4096\n\t" \
  "v_sub_f32 v166, v166, v163\n\t" \
  "v_mfma_f32_16x16x32_bf16 a[84:87], a[204:207], v[92:95], a[84:87]\n\t" \
  "ds_read_b64_tr_b16 a[212:213], v235 offset:8192\n\t" \
  "v_sub_f32 v167, v167, v164\n\t" \
  "v_mfma_f32_16x16x32_bf16 a[84:87], a[200:203], v[88:91], a[84:87]\n\t" \
  "buffer_load_dwordx4 v242, %[ws], s71 offen offset:1024 sc1 lds\n\t" \
  "v_perm_b32 v197, v197, v193, s80\n\t" \
  "v_mfma_f32_16x16x32_bf16 a[84:87], a[196:199], v[84:87], a[84:87]\n\t" \
  "ds_read_b64_tr_b16 a[214:215], v235 offset:12288\n\t" \
  "v_add_f32_dpp v162, v162, v162 row_ror:4 row_mask:0xf bank_mask:0xf\n\t" \
  "v_mfma_f32_16x16x32_bf16 a[84:87], a[192:195], v[80:83], a[84:87]\n\t" \
  "ds_read_b64_tr_b16 a[216:217], v235 offset:32768\n\t" \
  "v_cvt_pk_bf16_f32 v169, v166, v167\n\t" \
  "v_mfma_f32_16x16x32_bf16 a[88:91], a[192:195], v[64:67], a[88:91]\n\t" \
  "s_mov_b32 s82, 0\n\t" \
  "ds_read_b64_tr_b16 a[218:219], v235 offset:36864\n\t" \
  "v_perm_b32 v193, v198, v194, s79\n\t" \
  "v_mfma_f32_16x16x32_bf16 a[88:91], a[196:199], v[68:71], a[88:91]\n\t" \
  "ds_read_b64_tr_b16 a[220:221], v235 offset:40960\n\t" \
  "v_add_f32_dpp v162, v162, v162 row_ror:2 row_mask:0xf bank_mask:0xf\n\t" \
  "v_mfma_f32_16x16x32_bf16 a[88:91], a[200:203], v[72:75], a[88:91]\n\t" \
  "buffer_load_dwordx4 v242, %[ws], s71 offen offset:2048 sc1 lds\n\t" \
  "ds_read_b64_tr_b16 a[222:223], v235 offset:45056\n\t" \
  "v_mfma_f32_16x16x32_bf16 a[88:91], a[204:207], v[76:79], a[88:91]\n\t" \
  "s_waitcnt lgkmcnt(4)\n\t" \
  "v_perm_b32 v198, v198, v194, s80\n\t" \
  "s_mov_b32 s83, 0xffff\n\t" \
  "v_mfma_f32_16x16x32_bf16 a[96:99], a[208:211], v[48:51], a[96:99]\n\t" \
  "v_add_f32_dpp v162, v162, v162 row_ror:1 row_mask:0xf bank_mask:0xf\n\t" \
  "v_xor_b32 v236, 0x140e0, v234\n\t" \
  "v_mfma_f32_16x16x32_bf16 a[96:99], a[212:215], v[52:55], a[96:99]\n\t" \
  "s_waitcnt lgkmcnt(0)\n\t" \
  "ds_read_b64_tr_b16 a[192:193], v236 offset:0\n\t" \
  "v_perm_b32 v194, v199, v195, s79\n\t" \
  "v_mfma_f32_16x16x32_bf16 a[96:99], a[216:219], v[56:59], a[96:99]\n\t" \
  "buffer_load_dwordx4 v242, %[ws], s71 offen offset:3072 sc1 lds\n\t" \
  "s_add_u32 m0, %[m], 98304\n\t" \
  "v_mfma_f32_16x16x32_bf16 a[96:99], a[220:223], v[60:63], a[96:99]\n\t" \
  "s_xor_b32 s71, %[eso], 491520\n\t" \
  "s_mov_b64 exec, s[82:83]\n\t" \
  "ds_write_b32 v173, v168 offset:1040\n\t" \
  "ds_write_b32 v173, v169 offset:1120\n\t" \
  "s_mov_b64 exec, -1\n\t" \
  "v_mfma_f32_16x16x32_bf16 a[100:103], a[220:223], v[92:95], a[100:103]\n\t" \
  "ds_read_b64_tr_b16 a[194:195], v236 offset:4096\n\t" \
  "v_readlane_b32 s87, v162, 32\n\t" \
  "v_readlane_b32 s88, v162, 16\n\t" \
  "v_mfma_f32_16x16x32_bf16 a[100:103], a[216:219], v[88:91], a[100:103]\n\t" \
  "ds_read_b64_tr_b16 a[196:197], v236 offset:8192\n\t" \
  "v_perm_b32 v199, v199, v195, s80\n\t" \
  "v_mfma_f32_16x16x32_bf16 a[100:103], a[212:215], v[84:87], a[100:103]\n\t" \
  "s_xor_b32 s73, %[eso], 196608\n\t" \
  "ds_read_b64_tr_b16 a[198:199], v236 offset:12288\n\t" \
  "v_perm_b32 v195, v204, v200, s79\n\t" \
  "v_mfma_f32_16x16x32_bf16 a[100:103], a[208:211], v[80:83], a[100:103]\n\t" \
  "buffer_load_dwordx4 v242, %[ws], s71 offen offset:0 sc1 lds\n\t" \
  "v_mfma_f32_16x16x32_bf16 a[104:107], a[208:211], v[64:67], a[104:107]\n\t" \
  "ds_read_b64_tr_b16 a[200:201], v236 offset:32768\n\t" \
  "ds_read_b64_tr_b16 a[202:203], v236 offset:36864\n\t" \
  "v_mfma_f32_16x16x32_bf16 a[104:107], a[212:215], v[68:71], a[104:107]\n\t" \
  "ds_read_b64_tr_b16 a[204:205], v236 offset:40960\n\t" \
  "v_perm_b32 v204, v204, v200, s80\n\t" \
  "v_mfma_f32_16x16x32_bf16 a[104:107], a[216:219], v[72:75], a[104:107]\n\t" \
  "ds_read_b64_tr_b16 a[206:207], v236 offset:45056\n\t" \
  "v_perm_b32 v200, v205, v201, s79\n\t" \
  "v_mfma_f32_16x16x32_bf16 a[104:107], a[220:223], v[76:79], a[104:107]\n\t" \
  "s_waitcnt lgkmcnt(4)\n\t" \
  "buffer_load_dwordx4 v[208:211], v242, %[ws], s73 offen offset:0 sc1\n\t" \
  "v_mfma_f32_16x16x32_bf16 a[112:115], a[192:195], v[48:51], a[112:115]\n\t" \
  "v_xor_b32 v237, 0x20040, v234\n\t" \
  "v_perm_b32 v205, v205, v201, s80\n\t" \
  "v_mfma_f32_16x16x32_bf16 a[112:115], a[196:199], v[52:55], a[112:115]\n\t" \
  "s_waitcnt lgkmcnt(0)\n\t" \
  "ds_read_b64_tr_b16 a[208:209], v237 offset:0\n\t" \
  "v_perm_b32 v201, v206, v202, s79\n\t" \
  "v_mfma_f32_16x16x32_bf16 a[112:115], a[200:203], v[56:59], a[112:115]\n\t" \
  "ds_read_b64_tr_b16 a[210:211], v237 offset:4096\n\t" \
  "v_perm_b32 v206, v206, v202, s80\n\t" \
  "v_mfma_f32_16x16x32_bf16 a[112:115], a[204:207], v[60:63], a[112:115]\n\t" \
  "buffer_load_dwordx4 v[212:215], v242, %[ws], s73 offen offset:1024 sc1\n\t" \
  "v_mfma_f32_16x16x32_bf16 a[116:119], a[204:207], v[92:95], a[116:119]\n\t" \
  "ds_read_b64_tr_b16 a[212:213], v237 offset:8192\n\t" \
  "v_perm_b32 v202, v207, v203, s79\n\t" \
  "v_mfma_f32_16x16x32_bf16 a[116:119], a[200:203], v[88:91], a[116:119]\n\t" \
  "ds_read_b64_tr_b16 a[214:215], v237 offset:12288\n\t" \
  "v_xor_b32 v235, 0x40, v234\n\t" \
  "v_mfma_f32_16x16x32_bf16 a[116:119], a[196:199], v[84:87], a[116:119]\n\t" \
  "ds_read_b64_tr_b16 a[216:217], v235 offset:0\n\t" \
  "v_perm_b32 v207, v207, v203, s80\n\t" \
  "v_mfma_f32_16x16x32_bf16 a[116:119], a[192:195], v[80:83], a[116:119]\n\t" \
  "ds_read_b64_tr_b16 a[218:219], v235 offset:4096\n\t" \
  "v_mfma_f32_16x16x32_bf16 a[120:123], a[192:195], v[64:67], a[120:123]\n\t" \
  "buffer_load_dwordx4 v[216:219], v242, %[ws], s73 offen offset:2048 sc1\n\t" \
  "v_mfma_f32_16x16x32_bf16 a[120:123], a[196:199], v[68:71], a[120:123]\n\t" \
  "s_mov_b32 s74, 16384\n\t" \
  "ds_read_b64_tr_b16 a[220:221], v235 offset:8192\n\t" \
  "v_mfma_f32_16x16x32_bf16 a[120:123], a[200:203], v[72:75], a[120:123]\n\t" \
  "ds_read_b64_tr_b16 a[222:223], v235 offset:12288\n\t" \
  "v_mfma_f32_16x16x32_bf16 a[120:123], a[204:207], v[76:79], a[120:123]\n\t" \
  "s_waitcnt vmcnt(18) lgkmcnt(4)\n\t" \
  "v_xor_b32 v236, 0x20060, v234\n\t" \
  STP("v_writelane_b32 %[stv], s94, 32\n\t") \
  STP("v_writelane_b32 %[stv], s92, 33\n\t") \
  STP("s_memrealtime s[94:95]\n\t") \
  STP("s_memtime s[92:93]\n\t") \
  "v_mfma_f32_16x16x32_bf16 a[32:35], a[208:211], v[16:19], a[32:35]\n\t" \
  "buffer_load_dwordx4 v[220:223], v242, %[ws], s73 offen offset:3072 sc1\n\t" \
  "v_mfma_f32_16x16x32_bf16 a[32:35], a[212:215], v[20:23], a[32:35]\n\t" \
  "s_waitcnt lgkmcnt(0)\n\t" \
  "ds_read_b64_tr_b16 a[192:193], v236 offset:0\n\t" \
  "v_mfma_f32_16x16x32_bf16 a[32:35], a[216:219], v[24:27], a[32:35]\n\t" \
  "ds_read_b64_tr_b16 a[194:195], v236 offset:4096\n\t" \
  "v_mfma_f32_16x16x32_bf16 a[32:35], a[220:223], v[28:31], a[32:35]\n\t" \
  "s_waitcnt vmcnt(15)\n\t" \
  "ds_read_b64_tr_b16 a[196:197], v236 offset:8192\n\t" \
  "v_mfma_f32_16x16x32_bf16 a[36:39], a[220:223], v[12:15], a[36:39]\n\t" \
  "ds_read_b64_tr_b16 a[198:199], v236 offset:12288\n\t" \
  "v_xor_b32 v237, 0x60, v234\n\t" \
  "v_mfma_f32_16x16x32_bf16 a[36:39], a[216:219], v[8:11], a[36:39]\n\t" \
  "buffer_load_dwordx4 v242, %[ws], s71 offen offset:1024 sc1 lds\n\t" \
  "v_mfma_f32_16x16x32_bf16 a[36:39], a[212:215], v[4:7], a[36:39]\n\t" \
  "ds_read_b64_tr_b16 a[200:201], v237 offset:0\n\t" \
  "v_mfma_f32_16x16x32_bf16 a[36:39], a[208:211], v[0:3], a[36:39]\n\t" \
  "s_waitcnt vmcnt(12)\n\t" \
  "ds_read_b64_tr_b16 a[202:203], v237 offset:4096\n\t" \
  "v_mfma_f32_16x16x32_bf16 a[40:43], a[208:211], v[32:35], a[40:43]\n\t" \
  "ds_read_b64_tr_b16 a[204:205], v237 offset:8192\n\t" \
  "v_mfma_f32_16x16x32_bf16 a[40:43], a[212:215], v[36:39], a[40:43]\n\t" \
  "buffer_load_dwordx4 v242, %[ws], s71 offen offset:2048 sc1 lds\n\t" \
  "v_mfma_f32_16x16x32_bf16 a[40:43], a[216:219], v[40:43], a[40:43]\n\t" \
  "ds_read_b64_tr_b16 a[206:207], v237 offset:12288\n\t" \
  "v_xor_b32 v235, 0x20000, v234\n\t" \
  "v_mfma_f32_16x16x32_bf16 a[40:43], a[220:223], v[44:47], a[40:43]\n\t" \
  "s_waitcnt lgkmcnt(4)\n\t" \
  "ds_read_b64_tr_b16 v[64:65], v235 offset:0\n\t" \
  "v_mfma_f32_16x16x32_bf16 a[48:51], a[192:195], v[16:19], a[48:51]\n\t" \
  "ds_read_b64_tr_b16 v[66:67], v235 offset:4096\n\t" \
  "v_mfma_f32_16x16x32_bf16 a[48:51], a[196:199], v[20:23], a[48:51]\n\t" \
  "s_waitcnt lgkmcnt(2)\n\t" \
  "buffer_load_dwordx4 v242, %[ws], s71 offen offset:3072 sc1 lds\n\t" \
  "v_mfma_f32_16x16x32_bf16 a[48:51], a[200:203], v[24:27], a[48:51]\n\t" \
  "ds_read_b64_tr_b16 v[68:69], v235 offset:8192\n\t" \
  "v_mfma_f32_16x16x32_bf16 a[48:51], a[204:207], v[28:31], a[48:51]\n\t" \
  "ds_read_b64_tr_b16 v[70:71], v235 offset:12288\n\t" \
  "v_xor_b32 v236, 0x0, v234\n\t" \
  "v_mfma_f32_16x16x32_bf16 a[52:55], a[204:207], v[12:15], a[52:55]\n\t" \
  "ds_read_b64_tr_b16 v[72:73], v236 offset:0\n\t" \
  "v_accvgpr_read_b32 v160, a32\n\t" \
  "v_mfma_f32_16x16x32_bf16 a[52:55], a[200:203], v[8:11], a[52:55]\n\t" \
  "buffer_load_dwordx2 v[48:49], v252, %[sd], s74 offen offset:0\n\t" \
  "v_mfma_f32_16x16x32_bf16 a[52:55], a[196:199], v[4:7], a[52:55]\n\t" \
  "ds_read_b64_tr_b16 v[74:75], v236 offset:4096\n\t" \
  "ds_read_b64_tr_b16 v[76:77], v236 offset:8192\n\t" \
  "v_mfma_f32_16x16x32_bf16 a[52:55], a[192:195], v[0:3], a[52:55]\n\t" \
  "ds_read_b64_tr_b16 v[78:79], v236 offset:12288\n\t" \
  "v_accvgpr_read_b32 v161, a33\n\t" \
  "v_mfma_f32_16x16x32_bf16 a[56:59], a[192:195], v[32:35], a[56:59]\n\t" \
  "v_xor_b32 v237, 0x20020, v234\n\t" \
  "v_accvgpr_read_b32 v162, a34\n\t" \
  "v_mfma_f32_16x16x32_bf16 a[56:59], a[196:199], v[36:39], a[56:59]\n\t" \
  "buffer_load_dwordx2 v[50:51], v252, %[sd], s74 offen offset:2048\n\t" \
  "s_mov_b32 s74, 20480\n\t" \
  "v_mfma_f32_16x16x32_bf16 a[56:59], a[200:203], v[40:43], a[56:59]\n\t" \
  "ds_read_b64_tr_b16 v[80:81], v237 offset:0\n\t" \
  "v_accvgpr_read_b32 v163, a35\n\t" \
  "v_mfma_f32_16x16x32_bf16 a[56:59], a[204:207], v[44:47], a[56:59]\n\t" \
  "s_waitcnt lgkmcnt(5)\n\t" \
  "ds_read_b64_tr_b16 v[82:83], v237 offset:4096\n\t" \
  "v_accvgpr_read_b32 v164, a48\n\t" \
  "v_mfma_f32_16x16x32_bf16 a[0:3], v[64:67], v[16:19], a[0:3]\n\t" \
  "ds_read_b64_tr_b16 v[84:85], v237 offset:8192\n\t" \
  "v_accvgpr_read_b32 v165, a49\n\t" \
  "v_mfma_f32_16x16x32_bf16 a[0:3], v[68:71], v[20:23], a[0:3]\n\t" \
  "s_waitcnt lgkmcnt(3)\n\t" \
  "buffer_load_dwordx2 v[52:53], v252, %[sd], s74 offen offset:0\n\t" \
  "v_accvgpr_read_b32 v166, a50\n\t" \
  "v_mfma_f32_16x16x32_bf16 a[0:3], v[72:75], v[24:27], a[0:3]\n\t" \
  "ds_read_b64_tr_b16 v[86:87], v237 offset:12288\n\t" \
  "v_accvgpr_read_b32 v167, a51\n\t" \
  "v_cvt_pk_bf16_f32 v168, v160, v161\n\t" \
  "v_mfma_f32_16x16x32_bf16 a[0:3], v[76:79], v[28:31], a[0:3]\n\t" \
  "v_xor_b32 v235, 0x20, v234\n\t" \
  "v_cvt_pk_bf16_f32 v169, v162, v163\n\t" \
  "v_cvt_pk_bf16_f32 v170, v164, v165\n\t" \
  "v_mfma_f32_16x16x32_bf16 a[4:7], v[76:79], v[12:15], a[4:7]\n\t" \
  "ds_read_b64_tr_b16 v[88:89], v235 offset:0\n\t" \
  "v_cvt_pk_bf16_f32 v171, v166, v167\n\t" \
  "s_xor_b32 s76, s60, 1024\n\t" \
  "v_mfma_f32_16x16x32_bf16 a[4:7], v[72:75], v[8:11], a[4:7]\n\t" \
  "buffer_store_dwordx4 v[168:171], v242, %[ws], s76 offen sc1\n\t" \
  "v_accvgpr_read_b32 v160, a36\n\t" \
  "v_mfma_f32_16x16x32_bf16 a[4:7], v[68:71], v[4:7], a[4:7]\n\t" \
  "ds_read_b64_tr_b16 v[90:91], v235 offset:4096\n\t" \
  "ds_read_b64_tr_b16 v[92:93], v235 offset:8192\n\t" \
  "v_accvgpr_read_b32 v161, a37\n\t" \
  "v_mfma_f32_16x16x32_bf16 a[4:7], v[64:67], v[0:3], a[4:7]\n\t" \
  "buffer_load_dwordx2 v[54:55], v252, %[sd], s74 offen offset:2048\n\t" \
  "s_mov_b32 s74, 24576\n\t" \
  "v_mfma_f32_16x16x32_bf16 a[8:11], v[64:67], v[32:35], a[8:11]\n\t" \
  "ds_read_b64_tr_b16 v[94:95], v235 offset:12288\n\t" \
  "v_accvgpr_read_b32 v162, a38\n\t" \
  "v_accvgpr_read_b32 v163, a39\n\t" \
  "v_mfma_f32_16x16x32_bf16 a[8:11], v[68:71], v[36:39], a[8:11]\n\t" \
  "v_accvgpr_read_b32 v164, a52\n\t" \
  "v_accvgpr_read_b32 v165, a53\n\t" \
  "v_accvgpr_read_b32 v166, a54\n\t" \
  "v_mfma_f32_16x16x32_bf16 a[8:11], v[72:75], v[40:43], a[8:11]\n\t" \
  "v_accvgpr_read_b32 v167, a55\n\t" \
  "v_cvt_pk_bf16_f32 v174, v160, v161\n\t" \
  "v_cvt_pk_bf16_f32 v175, v162, v163\n\t" \
  "v_mfma_f32_16x16x32_bf16 a[8:11], v[76:79], v[44:47], a[8:11]\n\t" \
  "s_waitcnt lgkmcnt(4)\n\t" \
  "buffer_load_dwordx2 v[56:57], v252, %[sd], s74 offen offset:0\n\t" \
  "v_cvt_pk_bf16_f32 v172, v164, v165\n\t" \
  "v_mfma_f32_16x16x32_bf16 a[16:19], v[80:83], v[16:19], a[16:19]\n\t" \
  "v_xor_b32 v236, 0x20080, v234\n\t" \
  "v_cvt_pk_bf16_f32 v173, v166, v167\n\t" \
  "s_xor_b32 s76, s61, 1024\n\t" \
  "v_mfma_f32_16x16x32_bf16 a[16:19], v[84:87], v[20:23], a[16:19]\n\t" \
  "s_waitcnt lgkmcnt(0)\n\t" \
  "buffer_store_dwordx4 v[172:175], v242, %[ws], s76 offen sc1\n\t" \
  "v_mfma_f32_16x16x32_bf16 a[16:19], v[88:91], v[24:27], a[16:19]\n\t" \
  "ds_read_b64_tr_b16 a[208:209], v236 offset:0\n\t" \
  "ds_read_b64_tr_b16 a[210:211], v236 offset:4096\n\t" \
  "v_mfma_f32_16x16x32_bf16 a[16:19], v[92:95], v[28:31], a[16:19]\n\t" \
  "s_barrier\n\t" \
  "v_mfma_f32_16x16x32_bf16 a[20:23], v[92:95], v[12:15], a[20:23]\n\t" \
  "ds_read_b64_tr_b16 a[212:213], v236 offset:8192\n\t" \
  "ds_read_b64_tr_b16 a[214:215], v236 offset:12288\n\t" \
  "v_mfma_f32_16x16x32_bf16 a[20:23], v[88:91], v[8:11], a[20:23]\n\t" \
  "buffer_load_dwordx2 v[58:59], v252, %[sd], s74 offen offset:2048\n\t" \
  "s_mov_b32 s74, 28672\n\t" \
  "v_mfma_f32_16x16x32_bf16 a[20:23], v[84:87], v[4:7], a[20:23]\n\t" \
  "v_xor_b32 v237, 0x80, v234\n\t" \
  "v_accvgpr_read_b32 v160, a0\n\t" \
  "v_mfma_f32_16x16x32_bf16 a[20:23], v[80:83], v[0:3], a[20:23]\n\t" \
  "ds_read_b64_tr_b16 a[216:217], v237 offset:0\n\t" \
  "ds_read_b64_tr_b16 a[218:219], v237 offset:4096\n\t" \
  "v_mfma_f32_16x16x32_bf16 a[24:27], v[80:83], v[32:35], a[24:27]\n\t" \
  "ds_read_b64_tr_b16 a[220:221], v237 offset:8192\n\t" \
  "ds_read_b64_tr_b16 a[222:223], v237 offset:12288\n\t" \
  "v_mfma_f32_16x16x32_bf16 a[24:27], v[84:87], v[36:39], a[24:27]\n\t" \
  "buffer_load_dwordx2 v[60:61], v252, %[sd], s74 offen offset:0\n\t" \
  "v_mfma_f32_16x16x32_bf16 a[24:27], v[88:91], v[40:43], a[24:27]\n\t" \
  "v_xor_b32 v235, 0x200a0, v234\n\t" \
  "v_accvgpr_read_b32 v161, a1\n\t" \
  "v_mfma_f32_16x16x32_bf16 a[24:27], v[92:95], v[44:47], a[24:27]\n\t" \
  "s_waitcnt lgkmcnt(4)\n\t" \
  "ds_read_b64_tr_b16 a[192:193], v235 offset:0\n\t" \
  "v_accvgpr_read_b32 v162, a2\n\t" \
  "v_accvgpr_read_b32 v163, a3\n\t" \
  "v_mfma_f32_16x16x32_bf16 a[64:67], a[208:211], v[16:19], a[64:67]\n\t" \
  "ds_read_b64_tr_b16 a[194:195], v235 offset:4096\n\t" \
  "v_accvgpr_read_b32 v164, a16\n\t" \
  "v_accvgpr_read_b32 v165, a17\n\t" \
  "v_mfma_f32_16x16x32_bf16 a[64:67], a[212:215], v[20:23], a[64:67]\n\t" \
  "s_waitcnt lgkmcnt(2)\n\t" \
  "buffer_load_dwordx2 v[62:63], v252, %[sd], s74 offen offset:2048\n\t" \
  "v_accvgpr_read_b32 v166, a18\n\t" \
  "v_mfma_f32_16x16x32_bf16 a[64:67], a[216:219], v[24:27], a[64:67]\n\t" \
  "ds_read_b64_tr_b16 a[196:197], v235 offset:8192\n\t" \
  "v_accvgpr_read_b32 v167, a19\n\t" \
  "v_cvt_pk_bf16_f32 v168, v160, v161\n\t" \
  "v_mfma_f32_16x16x32_bf16 a[64:67], a[220:223], v[28:31], a[64:67]\n\t" \
  "ds_read_b64_tr_b16 a[198:199], v235 offset:12288\n\t" \
  "v_xor_b32 v236, 0xa0, v234\n\t" \
  "v_cvt_pk_bf16_f32 v169, v162, v163\n\t" \
  "v_mfma_f32_16x16x32_bf16 a[68:71], a[220:223], v[12:15], a[68:71]\n\t" \
  "ds_read_b64_tr_b16 a[200:201], v236 offset:0\n\t" \
  "v_cvt_pk_bf16_f32 v170, v164, v165\n\t" \
  "v_cvt_pk_bf16_f32 v171, v166, v167\n\t" \
  "v_mfma_f32_16x16x32_bf16 a[68:71], a[216:219], v[8:11], a[68:71]\n\t" \
  "s_xor_b32 s76, s60, 0\n\t" \
  "ds_read_b64_tr_b16 a[202:203], v236 offset:4096\n\t" \
  "buffer_store_dwordx4 v[168:171], v242, %[ws], s76 offen sc1\n\t" \
  "v_mfma_f32_16x16x32_bf16 a[68:71], a[212:215], v[4:7], a[68:71]\n\t" \
  "ds_read_b64_tr_b16 a[204:205], v236 offset:8192\n\t" \
  "v_accvgpr_read_b32 v160, a4\n\t" \
  "v_accvgpr_read_b32 v161, a5\n\t" \
  "v_mfma_f32_16x16x32_bf16 a[68:71], a[208:211], v[0:3], a[68:71]\n\t" \
  "ds_read_b64_tr_b16 a[206:207], v236 offset:12288\n\t" \
  "v_accvgpr_read_b32 v162, a6\n\t" \
  "v_accvgpr_read_b32 v163, a7\n\t" \
  "v_mfma_f32_16x16x32_bf16 a[72:75], a[208:211], v[32:35], a[72:75]\n\t" \
  "v_accvgpr_read_b32 v164, a20\n\t" \
  "v_accvgpr_read_b32 v165, a21\n\t" \
  "v_accvgpr_read_b32 v166, a22\n\t" \
  "v_mfma_f32_16x16x32_bf16 a[72:75], a[212:215], v[36:39], a[72:75]\n\t" \
  "v_accvgpr_read_b32 v167, a23\n\t" \
  "v_cvt_pk_bf16_f32 v174, v160, v161\n\t" \
  "v_cvt_pk_bf16_f32 v175, v162, v163\n\t" \
  "v_mfma_f32_16x16x32_bf16 a[72:75], a[216:219], v[40:43], a[72:75]\n\t" \
  "v_cvt_pk_bf16_f32 v172, v164, v165\n\t" \
  "v_cvt_pk_bf16_f32 v173, v166, v167\n\t" \
  "s_xor_b32 s76, s61, 0\n\t" \
  "v_mfma_f32_16x16x32_bf16 a[72:75], a[220:223], v[44:47], a[72:75]\n\t" \
  "s_waitcnt lgkmcnt(4)\n\t" \
  "buffer_store_dwordx4 v[172:175], v242, %[ws], s76 offen sc1\n\t" \
  "v_mfma_f32_16x16x32_bf16 a[80:83], a[192:195], v[16:19], a[80:83]\n\t" \
  "v_xor_b32 v237, 0x200c0, v234\n\t" \
  "v_mfma_f32_16x16x32_bf16 a[80:83], a[196:199], v[20:23], a[80:83]\n\t" \
  "s_waitcnt lgkmcnt(0)\n\t" \
  "ds_read_b64_tr_b16 a[208:209], v237 offset:0\n\t" \
  "v_mfma_f32_16x16x32_bf16 a[80:83], a[200:203], v[24:27], a[80:83]\n\t" \
  "ds_read_b64_tr_b16 a[210:211], v237 offset:4096\n\t" \
  "v_mfma_f32_16x16x32_bf16 a[80:83], a[204:207], v[28:31], a[80:83]\n\t" \
  "ds_read_b64_tr_b16 a[212:213], v237 offset:8192\n\t" \
  "v_mfma_f32_16x16x32_bf16 a[84:87], a[204:207], v[12:15], a[84:87]\n\t" \
  "ds_read_b64_tr_b16 a[214:215], v237 offset:12288\n\t" \
  "v_xor_b32 v235, 0xc0, v234\n\t" \
  "v_mfma_f32_16x16x32_bf16 a[84:87], a[200:203], v[8:11], a[84:87]\n\t" \
  "ds_read_b64_tr_b16 a[216:217], v235 offset:0\n\t" \
  "v_accvgpr_read_b32 v160, a64\n\t" \
  "v_mfma_f32_16x16x32_bf16 a[84:87], a[196:199], v[4:7], a[84:87]\n\t" \
  "ds_read_b64_tr_b16 a[218:219], v235 offset:4096\n\t" \
  "v_accvgpr_read_b32 v161, a65\n\t" \
  "v_mfma_f32_16x16x32_bf16 a[84:87], a[192:195], v[0:3], a[84:87]\n\t" \
  "ds_read_b64_tr_b16 a[220:221], v235 offset:8192\n\t" \
  "v_accvgpr_read_b32 v162, a66\n\t" \
  "v_mfma_f32_16x16x32_bf16 a[88:91], a[192:195], v[32:35], a[88:91]\n\t" \
  "ds_read_b64_tr_b16 a[222:223], v235 offset:12288\n\t" \
  "v_accvgpr_read_b32 v163, a67\n\t" \
  "v_mfma_f32_16x16x32_bf16 a[88:91], a[196:199], v[36:39], a[88:91]\n\t" \
  "v_accvgpr_read_b32 v164, a80\n\t" \
  "v_accvgpr_read_b32 v165, a81\n\t" \
  "v_mfma_f32_16x16x32_bf16 a[88:91], a[200:203], v[40:43], a[88:91]\n\t" \
  "v_accvgpr_read_b32 v166, a82\n\t" \
  "v_accvgpr_read_b32 v167, a83\n\t" \
  "v_mfma_f32_16x16x32_bf16 a[88:91], a[204:207], v[44:47], a[88:91]\n\t" \
  "s_waitcnt lgkmcnt(4)\n\t" \
  "v_xor_b32 v236, 0x200e0, v234\n\t" \
  "v_cvt_pk_bf16_f32 v168, v160, v161\n\t" \
  "v_mfma_f32_16x16x32_bf16 a[96:99], a[208:211], v[16:19], a[96:99]\n\t" \
  "ds_read_b64_tr_b16 a[192:193], v236 offset:0\n\t" \
  "v_cvt_pk_bf16_f32 v169, v162, v163\n\t" \
  "v_mfma_f32_16x16x32_bf16 a[96:99], a[212:215], v[20:23], a[96:99]\n\t" \
  "s_waitcnt lgkmcnt(1)\n\t" \
  "ds_read_b64_tr_b16 a[194:195], v236 offset:4096\n\t" \
  ".Lhs14:\n\ts_cbranch_execz .Lhs14n\n\t" \
  "v_cvt_pk_bf16_f32 v170, v164, v165\n\t" \
  "v_mfma_f32_16x16x32_bf16 a[96:99], a[216:219], v[24:27], a[96:99]\n\t" \
  "ds_read_b64_tr_b16 a[196:197], v236 offset:8192\n\t" \
  "v_cvt_pk_bf16_f32 v171, v166, v167\n\t" \
  "s_xor_b32 s76, s60, 2048\n\t" \
  "v_mfma_f32_16x16x32_bf16 a[96:99], a[220:223], v[28:31], a[96:99]\n\t" \
  "buffer_store_dwordx4 v[168:171], v242, %[ws], s76 offen sc1\n\t" \
  "v_mfma_f32_16x16x32_bf16 a[100:103], a[220:223], v[12:15], a[100:103]\n\t" \
  "ds_read_b64_tr_b16 a[198:199], v236 offset:12288\n\t" \
  "v_accvgpr_read_b32 v160, a68\n\t" \
  "v_mfma_f32_16x16x32_bf16 a[100:103], a[216:219], v[8:11], a[100:103]\n\t" \
  "v_xor_b32 v237, 0xe0, v234\n\t" \
  "v_accvgpr_read_b32 v161, a69\n\t" \
  "v_mfma_f32_16x16x32_bf16 a[100:103], a[212:215], v[4:7], a[100:103]\n\t" \
  "ds_read_b64_tr_b16 a[200:201], v237 offset:0\n\t" \
  "v_accvgpr_read_b32 v162, a70\n\t" \
  "v_accvgpr_read_b32 v163, a71\n\t" \
  "v_mfma_f32_16x16x32_bf16 a[100:103], a[208:211], v[0:3], a[100:103]\n\t" \
  "ds_read_b64_tr_b16 a[202:203], v237 offset:4096\n\t" \
  "v_accvgpr_read_b32 v164, a84\n\t" \
  "v_accvgpr_read_b32 v165, a85\n\t" \
  "v_mfma_f32_16x16x32_bf16 a[104:107], a[208:211], v[32:35], a[104:107]\n\t" \
  "ds_read_b64_tr_b16 a[204:205], v237 offset:8192\n\t" \
  "v_accvgpr_read_b32 v166, a86\n\t" \
  "v_accvgpr_read_b32 v167, a87\n\t" \
  "v_mfma_f32_16x16x32_bf16 a[104:107], a[212:215], v[36:39], a[104:107]\n\t" \
  "ds_read_b64_tr_b16 a[206:207], v237 offset:12288\n\t" \
  "v_xor_b32 v235, 0xc000, v234\n\t" \
  "v_cvt_pk_bf16_f32 v174, v160, v161\n\t" \
  "v_mfma_f32_16x16x32_bf16 a[104:107], a[216:219], v[40:43], a[104:107]\n\t" \
  "ds_read_b64_tr_b16 a[32:33], v235 offset:0\n\t" \
  "ds_read_b64_tr_b16 a[34:35], v235 offset:4096\n\t" \
  "ds_read_b64_tr_b16 a[48:49], v235 offset:8192\n\t" \
  "v_mfma_f32_16x16x32_bf16 a[104:107], a[220:223], v[44:47], a[104:107]\n\t" \
  "s_waitcnt lgkmcnt(7)\n\t" \
  "ds_read_b64_tr_b16 a[50:51], v235 offset:12288\n\t" \
  "ds_read_b64_tr_b16 a[36:37], v235 offset:16384\n\t" \
  "ds_read_b64_tr_b16 a[38:39], v235 offset:20480\n\t" \
  "v_mfma_f32_16x16x32_bf16 a[112:115], a[192:195], v[16:19], a[112:115]\n\t" \
  "ds_read_b64_tr_b16 a[52:53], v235 offset:24576\n\t" \
  "ds_read_b64_tr_b16 a[54:55], v235 offset:28672\n\t" \
  "v_xor_b32 v236, 0xc020, v234\n\t" \
  "v_mfma_f32_16x16x32_bf16 a[112:115], a[196:199], v[20:23], a[112:115]\n\t" \
  "s_waitcnt lgkmcnt(8)\n\t" \
  "ds_read_b64_tr_b16 a[0:1], v236 offset:0\n\t" \
  "ds_read_b64_tr_b16 a[2:3], v236 offset:4096\n\t" \
  "ds_read_b64_tr_b16 a[16:17], v236 offset:8192\n\t" \
  "v_mfma_f32_16x16x32_bf16 a[112:115], a[200:203], v[24:27], a[112:115]\n\t" \
  "ds_read_b64_tr_b16 a[18:19], v236 offset:12288\n\t" \
  "ds_read_b64_tr_b16 a[4:5], v236 offset:16384\n\t" \
  "ds_read_b64_tr_b16 a[6:7], v236 offset:20480\n\t" \
  "v_mfma_f32_16x16x32_bf16 a[112:115], a[204:207], v[28:31], a[112:115]\n\t" \
  "ds_read_b64_tr_b16 a[20:21], v236 offset:24576\n\t" \
  "ds_read_b64_tr_b16 a[22:23], v236 offset:28672\n\t" \
  "v_xor_b32 v237, 0xc040, v234\n\t" \
  "v_mfma_f32_16x16x32_bf16 a[116:119], a[204:207], v[12:15], a[116:119]\n\t" \
  "ds_read_b64_tr_b16 a[64:65], v237 offset:0\n\t" \
  "ds_read_b64_tr_b16 a[66:67], v237 offset:4096\n\t" \
  "v_cvt_pk_bf16_f32 v175, v162, v163\n\t" \
  "v_mfma_f32_16x16x32_bf16 a[116:119], a[200:203], v[8:11], a[116:119]\n\t" \
  "ds_read_b64_tr_b16 a[80:81], v237 offset:8192\n\t" \
  "v_cvt_pk_bf16_f32 v172, v164, v165\n\t" \
  "v_cvt_pk_bf16_f32 v173, v166, v167\n\t" \
  "v_mfma_f32_16x16x32_bf16 a[116:119], a[196:199], v[4:7], a[116:119]\n\t" \
  "s_xor_b32 s76, s61, 2048\n\t" \
  "ds_read_b64_tr_b16 a[82:83], v237 offset:12288\n\t" \
  "ds_read_b64_tr_b16 a[68:69], v237 offset:16384\n\t" \
  "ds_read_b64_tr_b16 a[70:71], v237 offset:20480\n\t" \
  "v_mfma_f32_16x16x32_bf16 a[116:119], a[192:195], v[0:3], a[116:119]\n\t" \
  "buffer_store_dwordx4 v[172:175], v242, %[ws], s76 offen sc1\n\t" \
  "ds_read_b64_tr_b16 a[84:85], v237 offset:24576\n\t" \
  "v_mfma_f32_16x16x32_bf16 a[120:123], a[192:195], v[32:35], a[120:123]\n\t" \
  "ds_read_b64_tr_b16 a[86:87], v237 offset:28672\n\t" \
  "v_xor_b32 v235, 0xc060, v234\n\t" \
  "v_mfma_f32_16x16x32_bf16 a[120:123], a[196:199], v[36:39], a[120:123]\n\t" \
  "ds_read_b64_tr_b16 a[208:209], v235 offset:0\n\t" \
  "ds_read_b64_tr_b16 a[210:211], v235 offset:4096\n\t" \
  "ds_read_b64_tr_b16 a[212:213], v235 offset:8192\n\t" \
  "v_mfma_f32_16x16x32_bf16 a[120:123], a[200:203], v[40:43], a[120:123]\n\t" \
  "ds_read_b64_tr_b16 a[214:215], v235 offset:12288\n\t" \
  "ds_read_b64_tr_b16 a[216:217], v235 offset:16384\n\t" \
  "ds_read_b64_tr_b16 a[218:219], v235 offset:20480\n\t" \
  "v_mfma_f32_16x16x32_bf16 a[120:123], a[204:207], v[44:47], a[120:123]\n\t" \
  "s_waitcnt vmcnt(17) lgkmcnt(15)\n\t" \
  "ds_read_b64_tr_b16 a[220:221], v235 offset:24576\n\t" \
  "ds_read_b64_tr_b16 a[222:223], v235 offset:28672\n\t" \
  "v_accvgpr_read_b32 v160, a96\n\t" \
  "v_mfma_f32_16x16x32_bf16 a[8:11], a[32:35], v[208:211], a[8:11]\n\t" \
  "v_accvgpr_read_b32 v161, a97\n\t" \
  "v_accvgpr_read_b32 v162, a98\n\t" \
  "v_accvgpr_read_b32 v163, a99\n\t" \
  "v_mfma_f32_16x16x32_bf16 a[8:11], a[48:51], v[212:215], a[8:11]\n\t" \
  "v_accvgpr_read_b32 v164, a112\n\t" \
  "v_accvgpr_read_b32 v165, a113\n\t" \
  "v_accvgpr_read_b32 v166, a114\n\t" \
  "v_mfma_f32_16x16x32_bf16 a[8:11], a[36:39], v[216:219], a[8:11]\n\t" \
  "v_xor_b32 v236, 0xc080, v234\n\t" \
  "v_accvgpr_read_b32 v167, a115\n\t" \
  "v_cvt_pk_bf16_f32 v168, v160, v161\n\t" \
  "v_mfma_f32_16x16x32_bf16 a[8:11], a[52:55], v[220:223], a[8:11]\n\t" \
  "ds_read_b64_tr_b16 v[32:33], v236 offset:0\n\t" \
  "v_cvt_pk_bf16_f32 v169, v162, v163\n\t" \
  "v_cvt_pk_bf16_f32 v170, v164, v165\n\t" \
  "v_mfma_f32_16x16x32_bf16 a[24:27], a[0:3], v[208:211], a[24:27]\n\t" \
  "ds_read_b64_tr_b16 v[34:35], v236 offset:4096\n\t" \
  "v_cvt_pk_bf16_f32 v171, v166, v167\n\t" \
  "s_xor_b32 s76, s60, 3072\n\t" \
  "v_mfma_f32_16x16x32_bf16 a[24:27], a[16:19], v[212:215], a[24:27]\n\t" \
  "s_waitcnt lgkmcnt(15)\n\t" \
  "buffer_store_dwordx4 v[168:171], v242, %[ws], s76 offen sc1\n\t" \
  "v_accvgpr_read_b32 v160, a100\n\t" \
  "v_mfma_f32_16x16x32_bf16 a[24:27], a[4:7], v[216:219], a[24:27]\n\t" \
  "ds_read_b64_tr_b16 v[36:37], v236 offset:8192\n\t" \
  "ds_read_b64_tr_b16 v[38:39], v236 offset:12288\n\t" \
  "v_accvgpr_read_b32 v161, a101\n\t" \
  "v_mfma_f32_16x16x32_bf16 a[24:27], a[20:23], v[220:223], a[24:27]\n\t" \
  "s_waitcnt lgkmcnt(15)\n\t" \
  "ds_read_b64_tr_b16 v[40:41], v236 offset:16384\n\t" \
  "v_accvgpr_read_b32 v162, a102\n\t" \
  "v_accvgpr_read_b32 v163, a103\n\t" \
  "v_mfma_f32_16x16x32_bf16 a[40:43], a[64:67], v[208:211], a[40:43]\n\t" \
  "ds_read_b64_tr_b16 v[42:43], v236 offset:20480\n\t" \
  "v_accvgpr_read_b32 v164, a116\n\t" \
  "v_accvgpr_read_b32 v165, a117\n\t" \
  "v_mfma_f32_16x16x32_bf16 a[40:43], a[80:83], v[212:215], a[40:43]\n\t" \
  "s_waitcnt lgkmcnt(14)\n\t" \
  "ds_read_b64_tr_b16 v[44:45], v236 offset:24576\n\t" \
  "v_accvgpr_read_b32 v166, a118\n\t" \
  "v_accvgpr_read_b32 v167, a119\n\t" \
  "v_mfma_f32_16x16x32_bf16 a[40:43], a[68:71], v[216:219], a[40:43]\n\t" \
  "ds_read_b64_tr_b16 v[46:47], v236 offset:28672\n\t" \
  "v_xor_b32 v237, 0xc0a0, v234\n\t" \
  "v_cvt_pk_bf16_f32 v174, v160, v161\n\t" \
  "v_mfma_f32_16x16x32_bf16 a[40:43], a[84:87], v[220:223], a[40:43]\n\t" \
  "s_waitcnt lgkmcnt(12)\n\t" \
  "ds_read_b64_tr_b16 a[192:193], v237 offset:0\n\t" \
  "ds_read_b64_tr_b16 a[194:195], v237 offset:4096\n\t" \
  "ds_read_b64_tr_b16 a[196:197], v237 offset:8192\n\t" \
  "v_mfma_f32_16x16x32_bf16 a[56:59], a[208:211], v[208:211], a[56:59]\n\t" \
  "s_add_u32 m0, %[m], 0\n\t" \
  "ds_read_b64_tr_b16 a[198:199], v237 offset:12288\n\t" \
  "ds_read_b64_tr_b16 a[200:201], v237 offset:16384\n\t" \
  "ds_read_b64_tr_b16 a[202:203], v237 offset:20480\n\t" \
  "v_mfma_f32_16x16x32_bf16 a[56:59], a[212:215], v[212:215], a[56:59]\n\t" \
  "s_waitcnt lgkmcnt(14)\n\t" \
  "ds_read_b64_tr_b16 a[204:205], v237 offset:24576\n\t" \
  "ds_read_b64_tr_b16 a[206:207], v237 offset:28672\n\t" \
  "v_xor_b32 v235, 0xc0c0, v234\n\t" \
  "v_mfma_f32_16x16x32_bf16 a[56:59], a[216:219], v[216:219], a[56:59]\n\t" \
  "ds_read_b64_tr_b16 v[0:1], v235 offset:0\n\t" \
  "ds_read_b64_tr_b16 v[2:3], v235 offset:4096\n\t" \
  "ds_read_b64_tr_b16 v[4:5], v235 offset:8192\n\t" \
  "v_mfma_f32_16x16x32_bf16 a[56:59], a[220:223], v[220:223], a[56:59]\n\t" \
  "s_waitcnt lgkmcnt(15)\n\t" \
  "s_barrier\n\t" \
  "v_mfma_f32_16x16x32_bf16 a[72:75], v[32:35], v[208:211], a[72:75]\n\t" \
  "s_xor_b32 s71, %[eso], 753664\n\t" \
  "ds_read_b64_tr_b16 v[6:7], v235 offset:12288\n\t" \
  "v_cvt_pk_bf16_f32 v175, v162, v163\n\t" \
  "v_cvt_pk_bf16_f32 v172, v164, v165\n\t" \
  "v_mfma_f32_16x16x32_bf16 a[72:75], v[36:39], v[212:215], a[72:75]\n\t" \
  "s_waitcnt lgkmcnt(12)\n\t" \
  "ds_read_b64_tr_b16 v[8:9], v235 offset:16384\n\t" \
  "v_cvt_pk_bf16_f32 v173, v166, v167\n\t" \
  "s_xor_b32 s76, s61, 3072\n\t" \
  "v_mfma_f32_16x16x32_bf16 a[72:75], v[40:43], v[216:219], a[72:75]\n\t" \
  "ds_read_b64_tr_b16 v[10:11], v235 offset:20480\n\t" \
  "ds_read_b64_tr_b16 v[12:13], v235 offset:24576\n\t" \
  "ds_read_b64_tr_b16 v[14:15], v235 offset:28672\n\t" \
  "v_mfma_f32_16x16x32_bf16 a[72:75], v[44:47], v[220:223], a[72:75]\n\t" \
  "s_waitcnt lgkmcnt(12)\n\t" \
  "v_xor_b32 v236, 0xc0e0, v234\n\t" \
  "v_mfma_f32_16x16x32_bf16 a[88:91], a[192:195], v[208:211], a[88:91]\n\t" \
  "ds_read_b64_tr_b16 v[16:17], v236 offset:0\n\t" \
  "ds_read_b64_tr_b16 v[18:19], v236 offset:4096\n\t" \
  "ds_read_b64_tr_b16 v[20:21], v236 offset:8192\n\t" \
  "v_mfma_f32_16x16x32_bf16 a[88:91], a[196:199], v[212:215], a[88:91]\n\t" \
  "s_waitcnt lgkmcnt(11)\n\t" \
  "buffer_store_dwordx4 v[172:175], v242, %[ws], s76 offen sc1\n\t" \
  "v_accvgpr_read_b32 v160, a8\n\t" \
  "v_mfma_f32_16x16x32_bf16 a[88:91], a[200:203], v[216:219], a[88:91]\n\t" \
  "ds_read_b64_tr_b16 v[22:23], v236 offset:12288\n\t" \
  "v_accvgpr_read_b32 v161, a9\n\t" \
  "v_accvgpr_read_b32 v162, a10\n\t" \
  "v_mfma_f32_16x16x32_bf16 a[88:91], a[204:207], v[220:223], a[88:91]\n\t" \
  "s_waitcnt lgkmcnt(8)\n\t" \
  "ds_read_b64_tr_b16 v[24:25], v236 offset:16384\n\t" \
  "v_accvgpr_read_b32 v163, a11\n\t" \
  "v_accvgpr_read_b32 v164, a24\n\t" \
  "v_mfma_f32_16x16x32_bf16 a[104:107], v[0:3], v[208:211], a[104:107]\n\t" \
  "ds_read_b64_tr_b16 v[26:27], v236 offset:20480\n\t" \
  "v_accvgpr_read_b32 v165, a25\n\t" \
  "v_accvgpr_read_b32 v166, a26\n\t" \
  "v_mfma_f32_16x16x32_bf16 a[104:107], v[4:7], v[212:215], a[104:107]\n\t" \
  "s_waitcnt lgkmcnt(6)\n\t" \
  "ds_read_b64_tr_b16 v[28:29], v236 offset:24576\n\t" \
  "v_accvgpr_read_b32 v167, a27\n\t" \
  "v_cvt_pk_bf16_f32 v168, v160, v161\n\t" \
  "v_mfma_f32_16x16x32_bf16 a[104:107], v[8:11], v[216:219], a[104:107]\n\t" \
  "ds_read_b64_tr_b16 v[30:31], v236 offset:28672\n\t" \
  "ds_read_b128 a[208:211], v242 offset:16384\n\t" \
  "v_cvt_pk_bf16_f32 v169, v162, v163\n\t" \
  "v_mfma_f32_16x16x32_bf16 a[104:107], v[12:15], v[220:223], a[104:107]\n\t" \
  "s_waitcnt lgkmcnt(5)\n\t" \
  "ds_read_b128 a[212:215], v242 offset:17408\n\t" \
  "v_cvt_pk_bf16_f32 v170, v164, v165\n\t" \
  "v_cvt_pk_bf16_f32 v171, v166, v167\n\t" \
  "v_mfma_f32_16x16x32_bf16 a[120:123], v[16:19], v[208:211], a[120:123]\n\t" \
  "s_xor_b32 s76, s62, 0\n\t" \
  "ds_read_b128 a[216:219], v242 offset:18432\n\t" \
  "ds_read_b128 a[220:223], v242 offset:19456\n\t" \
  "v_mfma_f32_16x16x32_bf16 a[120:123], v[20:23], v[212:215], a[120:123]\n\t" \
  "s_waitcnt lgkmcnt(4)\n\t" \
  "buffer_store_dwordx4 v[168:171], v242, %[ws], s76 offen sc1\n\t" \
  "v_accvgpr_read_b32 v160, a40\n\t" \
  "v_mfma_f32_16x16x32_bf16 a[120:123], v[24:27], v[216:219], a[120:123]\n\t" \
  "ds_read_b128 v[32:35], v242 offset:20480\n\t" \
  "v_accvgpr_read_b32 v161, a41\n\t" \
  "v_accvgpr_read_b32 v162, a42\n\t" \
  "v_mfma_f32_16x16x32_bf16 a[120:123], v[28:31], v[220:223], a[120:123]\n\t" \
  "s_waitcnt lgkmcnt(1)\n\t" \
  "ds_read_b128 v[36:39], v242 offset:21504\n\t" \
  "v_accvgpr_read_b32 v163, a43\n\t" \
  "v_accvgpr_read_b32 v164, a56\n\t" \
  STP("v_writelane_b32 %[stv], s94, 34\n\t") \
  STP("v_writelane_b32 %[stv], s92, 35\n\t") \
  STP("s_memrealtime s[94:95]\n\t") \
  STP("s_memtime s[92:93]\n\t") \
  "v_mfma_f32_16x16x32_bf16 v[96:99], v[64:67], a[208:211], v[96:99]\n\t" \
  "ds_read_b128 v[40:43], v242 offset:22528\n\t" \
  "v_accvgpr_read_b32 v165, a57\n\t" \
  "v_accvgpr_read_b32 v166, a58\n\t" \
  "v_mfma_f32_16x16x32_bf16 v[96:99], v[68:71], a[212:215], v[96:99]\n\t" \
  "ds_read_b128 v[44:47], v242 offset:23552\n\t" \
  "v_accvgpr_read_b32 v167, a59\n\t" \
  "v_cvt_pk_bf16_f32 v172, v160, v161\n\t" \
  "v_mfma_f32_16x16x32_bf16 v[96:99], v[72:75], a[216:219], v[96:99]\n\t" \
  "ds_read_b128 a[192:195], v242 offset:24576\n\t" \
  "v_cvt_pk_bf16_f32 v173, v162, v163\n\t" \
  "v_cvt_pk_bf16_f32 v174, v164, v165\n\t" \
  "v_mfma_f32_16x16x32_bf16 v[96:99], v[76:79], a[220:223], v[96:99]\n\t" \
  "ds_read_b128 a[196:199], v242 offset:25600\n\t" \
  "v_cvt_pk_bf16_f32 v175, v166, v167\n\t" \
  "s_xor_b32 s76, s62, 1024\n\t" \
  "v_mfma_f32_16x16x32_bf16 v[112:115], v[92:95], a[220:223], v[112:115]\n\t" \
  "buffer_store_dwordx4 v[172:175], v242, %[ws], s76 offen sc1\n\t" \
  "v_accvgpr_read_b32 v160, a72\n\t" \
  "v_mfma_f32_16x16x32_bf16 v[112:115], v[88:91], a[216:219], v[112:115]\n\t" \
  "ds_read_b128 a[200:203], v242 offset:26624\n\t" \
  "v_accvgpr_read_b32 v161, a73\n\t" \
  "v_accvgpr_read_b32 v162, a74\n\t" \
  "v_mfma_f32_16x16x32_bf16 v[112:115], v[84:87], a[212:215], v[112:115]\n\t" \
  "ds_read_b128 a[204:207], v242 offset:27648\n\t" \
  "v_accvgpr_read_b32 v163, a75\n\t" \
  "v_accvgpr_read_b32 v164, a88\n\t" \
  "v_mfma_f32_16x16x32_bf16 v[112:115], v[80:83], a[208:211], v[112:115]\n\t" \
  "s_waitcnt lgkmcnt(4)\n\t" \
  "v_accvgpr_read_b32 v165, a89\n\t" \
  "v_accvgpr_read_b32 v166, a90\n\t" \
  "v_accvgpr_read_b32 v167, a91\n\t" \
  "v_mfma_f32_16x16x32_bf16 v[100:103], v[64:67], v[32:35], v[100:103]\n\t" \
  "buffer_load_dwordx4 v242, %[ws], s71 offen offset:0 sc1 lds\n\t" \
  "v_cvt_pk_bf16_f32 v168, v160, v161\n\t" \
  "v_mfma_f32_16x16x32_bf16 v[100:103], v[68:71], v[36:39], v[100:103]\n\t" \
  "ds_read_b128 a[208:211], v242 offset:28672\n\t" \
  "v_cvt_pk_bf16_f32 v169, v162, v163\n\t" \
  "v_cvt_pk_bf16_f32 v170, v164, v165\n\t" \
  "v_mfma_f32_16x16x32_bf16 v[100:103], v[72:75], v[40:43], v[100:103]\n\t" \
  "ds_read_b128 a[212:215], v242 offset:29696\n\t" \
  "v_cvt_pk_bf16_f32 v171, v166, v167\n\t" \
  "s_xor_b32 s76, s62, 2048\n\t" \
  "v_mfma_f32_16x16x32_bf16 v[100:103], v[76:79], v[44:47], v[100:103]\n\t" \
  "buffer_store_dwordx4 v[168:171], v242, %[ws], s76 offen sc1\n\t" \
  "v_accvgpr_read_b32 v160, a104\n\t" \
  "v_mfma_f32_16x16x32_bf16 v[116:119], v[92:95], v[44:47], v[116:119]\n\t" \
  "s_mov_b32 s74, 0\n\t" \
  "ds_read_b128 a[216:219], v242 offset:30720\n\t" \
  "v_accvgpr_read_b32 v161, a105\n\t" \
  "v_accvgpr_read_b32 v162, a106\n\t" \
  "v_mfma_f32_16x16x32_bf16 v[116:119], v[88:91], v[40:43], v[116:119]\n\t" \
  "buffer_load_dwordx4 v242, %[ws], s71 offen offset:1024 sc1 lds\n\t" \
  "v_accvgpr_read_b32 v163, a107\n\t" \
  "v_mfma_f32_16x16x32_bf16 v[116:119], v[84:87], v[36:39], v[116:119]\n\t" \
  "ds_read_b128 a[220:223], v242 offset:31744\n\t" \
  "v_accvgpr_read_b32 v164, a120\n\t" \
  "v_accvgpr_read_b32 v165, a121\n\t" \
  "v_mfma_f32_16x16x32_bf16 v[116:119], v[80:83], v[32:35], v[116:119]\n\t" \
  "s_waitcnt lgkmcnt(4)\n\t" \
  "s_barrier\n\t" \
  "v_mfma_f32_16x16x32_bf16 v[104:107], v[64:67], a[192:195], v[104:107]\n\t" \
  "ds_read_b128 v[208:211], v242 offset:32768\n\t" \
  "v_accvgpr_read_b32 v166, a122\n\t" \
  "v_accvgpr_read_b32 v167, a123\n\t" \
  "v_mfma_f32_16x16x32_bf16 v[104:107], v[68:71], a[196:199], v[104:107]\n\t" \
  "ds_read_b128 v[212:215], v242 offset:33792\n\t" \
  "v_cvt_pk_bf16_f32 v172, v160, v161\n\t" \
  "v_cvt_pk_bf16_f32 v173, v162, v163\n\t" \
  "v_mfma_f32_16x16x32_bf16 v[104:107], v[72:75], a[200:203], v[104:107]\n\t" \
  "ds_read_b128 v[216:219], v242 offset:34816\n\t" \
  "v_cvt_pk_bf16_f32 v174, v164, v165\n\t" \
  "v_cvt_pk_bf16_f32 v175, v166, v167\n\t" \
  "v_mfma_f32_16x16x32_bf16 v[104:107], v[76:79], a[204:207], v[104:107]\n\t" \
  "s_xor_b32 s76, s62, 3072\n\t" \
  "ds_read_b128 v[220:223], v242 offset:35840\n\t" \
  "buffer_store_dwordx4 v[172:175], v242, %[ws], s76 offen sc1\n\t" \
  "v_mfma_f32_16x16x32_bf16 v[120:123], v[92:95], a[204:207], v[120:123]\n\t" \
  "v_mfma_f32_16x16x32_bf16 v[120:123], v[88:91], a[200:203], v[120:123]\n\t" \
  "buffer_load_dwordx4 v242, %[ws], s71 offen offset:2048 sc1 lds\n\t" \
  "v_mfma_f32_16x16x32_bf16 v[120:123], v[84:87], a[196:199], v[120:123]\n\t" \
  "v_mfma_f32_16x16x32_bf16 v[120:123], v[80:83], a[192:195], v[120:123]\n\t" \
  "s_waitcnt lgkmcnt(4)\n\t" \
  "buffer_load_dwordx4 v242, %[ws], s71 offen offset:3072 sc1 lds\n\t" \
  "s_add_u32 m0, %[m], 131072\n\t" \
  "v_mfma_f32_16x16x32_bf16 v[108:111], v[64:67], a[208:211], v[108:111]\n\t" \
  "s_add_u32 s71, %[xso], 0\n\t" \
  "ds_read_b128 a[192:195], v242 offset:36864\n\t" \
  "v_mfma_f32_16x16x32_bf16 v[108:111], v[68:71], a[212:215], v[108:111]\n\t" \
  "ds_read_b128 a[196:199], v242 offset:37888\n\t" \
  "v_mfma_f32_16x16x32_bf16 v[108:111], v[72:75], a[216:219], v[108:111]\n\t" \
  "ds_read_b128 a[200:203], v242 offset:38912\n\t" \
  "v_mfma_f32_16x16x32_bf16 v[108:111], v[76:79], a[220:223], v[108:111]\n\t" \
  "buffer_load_dwordx4 v248, %[sx], s71 offen offset:0 lds\n\t" \
  "s_add_u32 s71, %[xso], 7168\n\t" \
  "v_mfma_f32_16x16x32_bf16 v[124:127], v[92:95], a[220:223], v[124:127]\n\t" \
  "ds_read_b128 a[204:207], v242 offset:39936\n\t" \
  "v_mfma_f32_16x16x32_bf16 v[124:127], v[88:91], a[216:219], v[124:127]\n\t" \
  "v_mfma_f32_16x16x32_bf16 v[124:127], v[84:87], a[212:215], v[124:127]\n\t" \
  "v_xor_b32 v237, 0x14000, v234\n\t" \
  "v_mfma_f32_16x16x32_bf16 v[124:127], v[80:83], a[208:211], v[124:127]\n\t" \
  "s_waitcnt lgkmcnt(4)\n\t" \
  "buffer_load_dwordx4 v249, %[sx], s71 offen offset:1024 lds\n\t" \
  "s_add_u32 s71, %[xso], 14336\n\t" \
  "v_mfma_f32_16x16x32_bf16 v[96:99], a[160:163], v[208:211], v[96:99]\n\t" \
  "ds_read_b64_tr_b16 a[96:97], v237 offset:0\n\t" \
  "v_mfma_f32_16x16x32_bf16 v[96:99], a[164:167], v[212:215], v[96:99]\n\t" \
  "ds_read_b64_tr_b16 a[98:99], v237 offset:4096\n\t" \
  "v_mfma_f32_16x16x32_bf16 v[96:99], a[168:171], v[216:219], v[96:99]\n\t" \
  "s_waitcnt vmcnt(10)\n\t" \
  "buffer_store_dword v243, v225, %[ws], 0 offen sc1\n\t" \
  "v_mfma_f32_16x16x32_bf16 v[96:99], a[172:175], v[220:223], v[96:99]\n\t" \
  "ds_read_b64_tr_b16 a[112:113], v237 offset:8192\n\t" \
  "v_mfma_f32_16x16x32_bf16 v[112:115], a[188:191], v[220:223], v[112:115]\n\t" \
  "buffer_load_dwordx4 v250, %[sx], s71 offen offset:2048 lds\n\t" \
  "s_add_u32 s71, %[xso], 21504\n\t" \
  "v_mfma_f32_16x16x32_bf16 v[112:115], a[184:187], v[216:219], v[112:115]\n\t" \
  "ds_read_b64_tr_b16 a[114:115], v237 offset:12288\n\t" \
  "v_mfma_f32_16x16x32_bf16 v[112:115], a[180:183], v[212:215], v[112:115]\n\t" \
  "ds_read_b64_tr_b16 a[100:101], v237 offset:32768\n\t" \
  "v_mfma_f32_16x16x32_bf16 v[112:115], a[176:179], v[208:211], v[112:115]\n\t" \
  "s_waitcnt lgkmcnt(5)\n\t" \
  "ds_read_b64_tr_b16 a[102:103], v237 offset:36864\n\t" \
  "v_mfma_f32_16x16x32_bf16 v[100:103], a[160:163], a[192:195], v[100:103]\n\t" \
  "ds_read_b64_tr_b16 a[116:117], v237 offset:40960\n\t" \
  "v_mfma_f32_16x16x32_bf16 v[100:103], a[164:167], a[196:199], v[100:103]\n\t" \
  "buffer_load_dwordx4 v251, %[sx], s71 offen offset:3072 lds\n\t" \
  "s_add_u32 m0, %[m], 49152\n\t" \
  "v_mfma_f32_16x16x32_bf16 v[100:103], a[168:171], a[200:203], v[100:103]\n\t" \
  "s_xor_b32 s71, %[eso], 622592\n\t" \
  "ds_read_b64_tr_b16 a[118:119], v237 offset:45056\n\t" \
  "ds_read_b128 a[208:211], v242 offset:40960\n\t" \
  "ds_read_b128 a[212:215], v242 offset:41984\n\t" \
  "v_mfma_f32_16x16x32_bf16 v[100:103], a[172:175], a[204:207], v[100:103]\n\t" \
  "ds_read_b128 a[216:219], v242 offset:43008\n\t" \
  "ds_read_b128 a[220:223], v242 offset:44032\n\t" \
  "v_mfma_f32_16x16x32_bf16 v[116:119], a[188:191], a[204:207], v[116:119]\n\t" \
  "ds_read_b128 v[208:211], v242 offset:45056\n\t" \
  "v_mfma_f32_16x16x32_bf16 v[116:119], a[184:187], a[200:203], v[116:119]\n\t" \
  "buffer_load_dwordx4 v242, %[ws], s71 offen offset:0 sc1 lds\n\t" \
  "v_mfma_f32_16x16x32_bf16 v[116:119], a[180:183], a[196:199], v[116:119]\n\t" \
  "ds_read_b128 v[212:215], v242 offset:46080\n\t" \
  "ds_read_b128 v[216:219], v242 offset:47104\n\t" \
  "v_mfma_f32_16x16x32_bf16 v[116:119], a[176:179], a[192:195], v[116:119]\n\t" \
  "s_waitcnt lgkmcnt(3)\n\t" \
  "s_barrier\n\t" \
  "v_mfma_f32_16x16x32_bf16 v[104:107], a[160:163], a[208:211], v[104:107]\n\t" \
  "ds_read_b128 v[220:223], v242 offset:48128\n\t" \
  "v_xor_b32 v235, 0x14020, v234\n\t" \
  "v_mfma_f32_16x16x32_bf16 v[104:107], a[164:167], a[212:215], v[104:107]\n\t" \
  "buffer_load_dwordx4 v242, %[ws], s71 offen offset:1024 sc1 lds\n\t" \
  "ds_read_b64_tr_b16 a[64:65], v235 offset:0\n\t" \
  "v_mfma_f32_16x16x32_bf16 v[104:107], a[168:171], a[216:219], v[104:107]\n\t" \
  "ds_read_b64_tr_b16 a[66:67], v235 offset:4096\n\t" \
  "ds_read_b64_tr_b16 a[68:69], v235 offset:8192\n\t" \
  "ds_read_b64_tr_b16 a[70:71], v235 offset:12288\n\t" \
  "v_mfma_f32_16x16x32_bf16 v[104:107], a[172:175], a[220:223], v[104:107]\n\t" \
  "ds_read_b64_tr_b16 a[80:81], v235 offset:32768\n\t" \
  "ds_read_b64_tr_b16 a[82:83], v235 offset:36864\n\t" \
  "v_mfma_f32_16x16x32_bf16 v[120:123], a[188:191], a[220:223], v[120:123]\n\t" \
  "ds_read_b64_tr_b16 a[84:85], v235 offset:40960\n\t" \
  "ds_read_b64_tr_b16 a[86:87], v235 offset:45056\n\t" \
  "v_mfma_f32_16x16x32_bf16 v[120:123], a[184:187], a[216:219], v[120:123]\n\t" \
  "ds_read_b128 a[192:195], v230 offset:32768\n\t" \
  "ds_read_b128 a[196:199], v230 offset:33792\n\t" \
  "v_and_b32 v235, 16, v242\n\t" \
  "v_mfma_f32_16x16x32_bf16 v[120:123], a[180:183], a[212:215], v[120:123]\n\t" \
  "buffer_load_dwordx4 v242, %[ws], s71 offen offset:2048 sc1 lds\n\t" \
  "v_accvgpr_read_b32 v224, a224\n\t" \
  "v_mfma_f32_16x16x32_bf16 v[120:123], a[176:179], a[208:211], v[120:123]\n\t" \
  "s_waitcnt lgkmcnt(10)\n\t" \
  "ds_read_b128 a[200:203], v230 offset:34816\n\t" \
  "ds_read_b128 a[204:207], v230 offset:35840\n\t" \
  "v_mov_b32 v229, 0x3f80\n\t" \
  "v_mfma_f32_16x16x32_bf16 v[108:111], a[160:163], v[208:211], v[108:111]\n\t" \
  "ds_read_b128 v[0:3], v230 offset:36864\n\t" \
  "v_accvgpr_read_b32 v227, a228\n\t" \
  "v_lshlrev_b32 v229, v235, v229\n\t" \
  "v_mfma_f32_16x16x32_bf16 v[108:111], a[164:167], v[212:215], v[108:111]\n\t" \
  "s_mov_b32 s82, 0x300003\n\t" \
  "ds_read_b128 v[4:7], v230 offset:37888\n\t" \
  "v_perm_b32 v64, v227, v224, s79\n\t" \
  "v_mfma_f32_16x16x32_bf16 v[108:111], a[168:171], v[216:219], v[108:111]\n\t" \
  "buffer_load_dwordx4 v242, %[ws], s71 offen offset:3072 sc1 lds\n\t" \
  "s_add_u32 m0, %[m], 65536\n\t" \
  "v_mfma_f32_16x16x32_bf16 v[108:111], a[172:175], v[220:223], v[108:111]\n\t" \
  "s_xor_b32 s71, %[eso], 884736\n\t" \
  "s_mov_b32 s83, 0x30000300\n\t" \
  "ds_read_b128 v[8:11], v230 offset:38912\n\t" \
  "v_mfma_f32_16x16x32_bf16 v[124:127], a[188:191], v[220:223], v[124:127]\n\t" \
  "s_mov_b32 s84, 0xc0000c\n\t" \
  "ds_read_b128 v[12:15], v230 offset:39936\n\t" \
  "v_perm_b32 v68, v227, v224, s80\n\t" \
  "v_mfma_f32_16x16x32_bf16 v[124:127], a[184:187], v[216:219], v[124:127]\n\t" \
  "s_mov_b32 s85, 0xc0000c00\n\t" \
  "v_accvgpr_read_b32 v224, a225\n\t" \
  "v_cndmask_b32 v238, 0, v229, s[82:83]\n\t" \
  "v_mfma_f32_16x16x32_bf16 v[124:127], a[180:183], v[212:215], v[124:127]\n\t" \
  "v_cndmask_b32 v239, 0, v229, s[84:85]\n\t" \
  "v_accvgpr_read_b32 v227, a229\n\t" \
  "v_mfma_f32_16x16x32_bf16 v[124:127], a[176:179], v[208:211], v[124:127]\n\t" \
  "s_waitcnt lgkmcnt(4)\n\t" \
  "buffer_load_dwordx4 v242, %[ws], s71 offen offset:0 sc1 lds\n\t" \
  "v_mfma_f32_16x16x32_bf16 v[96:99], a[128:131], a[192:195], v[96:99]\n\t" \
  "ds_read_b128 v[208:211], v230 offset:40960\n\t" \
  "v_and_b32 v236, 15, v244\n\t" \
  "v_mfma_f32_16x16x32_bf16 v[96:99], a[132:135], a[196:199], v[96:99]\n\t" \
  "ds_read_b128 v[212:215], v230 offset:41984\n\t" \
  "v_lshrrev_b32 v229, 4, v244\n\t" \
  "v_mfma_f32_16x16x32_bf16 v[96:99], a[136:139], a[200:203], v[96:99]\n\t" \
  "ds_read_b128 v[216:219], v230 offset:43008\n\t" \
  "v_perm_b32 v65, v227, v224, s79\n\t" \
  "v_mfma_f32_16x16x32_bf16 v[96:99], a[140:143], a[204:207], v[96:99]\n\t" \
  "s_waitcnt vmcnt(12)\n\t" \
  "buffer_store_dword v243, v226, %[ws], 0 offen sc1\n\t" \
  "v_mfma_f32_16x16x32_bf16 v[112:115], a[156:159], a[204:207], v[112:115]\n\t" \
  "ds_read_b128 v[220:223], v230 offset:44032\n\t" \
  "v_lshlrev_b32 v235, 2, v236\n\t" \
  "v_mfma_f32_16x16x32_bf16 v[112:115], a[152:155], a[200:203], v[112:115]\n\t" \
  "buffer_load_dwordx4 v242, %[ws], s71 offen offset:1024 sc1 lds\n\t" \
  "v_mfma_f32_16x16x32_bf16 v[112:115], a[148:151], a[196:199], v[112:115]\n\t" \
  "v_perm_b32 v69, v227, v224, s80\n\t" \
  "v_mul_u32_u24 v236, 80, v236\n\t" \
  "v_mfma_f32_16x16x32_bf16 v[112:115], a[144:147], a[192:195], v[112:115]\n\t" \
  "s_waitcnt lgkmcnt(4)\n\t" \
  "v_lshlrev_b32 v237, 4, v229\n\t" \
  "v_accvgpr_read_b32 v224, a226\n\t" \
  "v_mfma_f32_16x16x32_bf16 v[100:103], a[128:131], v[0:3], v[100:103]\n\t" \
  "buffer_load_dwordx4 v242, %[ws], s71 offen offset:2048 sc1 lds\n\t" \
  "v_mfma_f32_16x16x32_bf16 v[100:103], a[132:135], v[4:7], v[100:103]\n\t" \
  "ds_read_b128 a[192:195], v230 offset:45056\n\t" \
  "v_add_u32 v237, s70, v237\n\t" \
  "v_mfma_f32_16x16x32_bf16 v[100:103], a[136:139], v[8:11], v[100:103]\n\t" \
  "ds_read_b128 a[196:199], v230 offset:46080\n\t" \
  "v_add_u32 v236, v237, v236\n\t" \
  "v_mfma_f32_16x16x32_bf16 v[100:103], a[140:143], v[12:15], v[100:103]\n\t" \
  "ds_read_b128 a[200:203], v230 offset:47104\n\t" \
  "v_accvgpr_read_b32 v227, a230\n\t" \
  "v_mfma_f32_16x16x32_bf16 v[116:119], a[156:159], v[12:15], v[116:119]\n\t" \
  "buffer_load_dwordx4 v242, %[ws], s71 offen offset:3072 sc1 lds\n\t" \
  "s_add_u32 m0, %[m], 16384\n\t" \
  "v_mfma_f32_16x16x32_bf16 v[116:119], a[152:155], v[8:11], v[116:119]\n\t" \
  "s_xor_b32 s71, %[eso], 229376\n\t" \
  "ds_read_b128 a[204:207], v230 offset:48128\n\t" \
  "ds_read_b128 a[208:211], v236 offset:960\n\t" \
  "v_mfma_f32_16x16x32_bf16 v[116:119], a[148:151], v[4:7], v[116:119]\n\t" \
  "ds_read_b128 a[212:215], v236 offset:640\n\t" \
  "v_perm_b32 v66, v227, v224, s79\n\t" \
  "v_mfma_f32_16x16x32_bf16 v[116:119], a[144:147], v[0:3], v[116:119]\n\t" \
  "s_waitcnt vmcnt(14) lgkmcnt(6)\n\t" \
  "s_barrier\n\t" \
  "v_mfma_f32_16x16x32_bf16 v[104:107], a[128:131], v[208:211], v[104:107]\n\t" \
  "buffer_load_dwordx2 v[32:33], v252, %[sd], s74 offen offset:0\n\t" \
  "v_mfma_f32_16x16x32_bf16 v[104:107], a[132:135], v[212:215], v[104:107]\n\t" \
  "ds_read_b128 a[160:163], v242 offset:0\n\t" \
  "v_perm_b32 v70, v227, v224, s80\n\t" \
  "v_mfma_f32_16x16x32_bf16 v[104:107], a[136:139], v[216:219], v[104:107]\n\t" \
  "ds_read_b128 a[164:167], v242 offset:1024\n\t" \
  "v_accvgpr_read_b32 v224, a227\n\t" \
  "v_mfma_f32_16x16x32_bf16 v[104:107], a[140:143], v[220:223], v[104:107]\n\t" \
  "ds_read_b128 a[168:171], v242 offset:2048\n\t" \
  "v_accvgpr_read_b32 v227, a231\n\t" \
  "v_mfma_f32_16x16x32_bf16 v[120:123], a[156:159], v[220:223], v[120:123]\n\t" \
  "buffer_load_dwordx2 v[34:35], v252, %[sd], s74 offen offset:2048\n\t" \
  "s_mov_b32 s74, 4096\n\t" \
  "v_mfma_f32_16x16x32_bf16 v[120:123], a[152:155], v[216:219], v[120:123]\n\t" \
  "ds_read_b128 a[172:175], v242 offset:3072\n\t" \
  "v_perm_b32 v67, v227, v224, s79\n\t" \
  "v_mfma_f32_16x16x32_bf16 v[120:123], a[148:151], v[212:215], v[120:123]\n\t" \
  "ds_read_b128 a[216:219], v236 offset:320\n\t" \
  "v_perm_b32 v71, v227, v224, s80\n\t" \
  "v_mfma_f32_16x16x32_bf16 v[120:123], a[144:147], v[208:211], v[120:123]\n\t" \
  "s_waitcnt lgkmcnt(7)\n\t" \
  "v_accvgpr_read_b32 v224, a232\n\t" \
  "v_accvgpr_read_b32 v227, a236\n\t" \
  "v_mfma_f32_16x16x32_bf16 v[108:111], a[128:131], a[192:195], v[108:111]\n\t" \
  "buffer_load_dwordx2 v[36:37], v252, %[sd], s74 offen offset:0\n\t" \
  "v_mfma_f32_16x16x32_bf16 v[108:111], a[132:135], a[196:199], v[108:111]\n\t" \
  "ds_read_b128 v[208:211], v242 offset:4096\n\t" \
  "v_perm_b32 v72, v227, v224, s79\n\t" \
  "v_mfma_f32_16x16x32_bf16 v[108:111], a[136:139], a[200:203], v[108:111]\n\t" \
  "ds_read_b128 v[212:215], v242 offset:5120\n\t" \
  "v_perm_b32 v76, v227, v224, s80\n\t" \
  "v_mfma_f32_16x16x32_bf16 v[108:111], a[140:143], a[204:207], v[108:111]\n\t" \
  "ds_read_b128 v[216:219], v242 offset:6144\n\t" \
  "v_accvgpr_read_b32 v224, a233\n\t" \
  "v_mfma_f32_16x16x32_bf16 v[124:127], a[156:159], a[204:207], v[124:127]\n\t" \
  "buffer_load_dwordx2 v[38:39], v252, %[sd], s74 offen offset:2048\n\t" \
  "s_mov_b32 s74, 8192\n\t" \
  "v_mfma_f32_16x16x32_bf16 v[124:127], a[152:155], a[200:203], v[124:127]\n\t" \
  "ds_read_b128 v[220:223], v242 offset:7168\n\t" \
  "ds_read_b128 a[220:223], v236 offset:0\n\t" \
  "buffer_load_dword v246, v235, %[ws], s68 offen sc1\n\t" \
  "v_mfma_f32_16x16x32_bf16 v[124:127], a[148:151], a[196:199], v[124:127]\n\t" \
  "v_accvgpr_read_b32 v227, a237\n\t" \
  "v_mfma_f32_16x16x32_bf16 v[124:127], a[144:147], a[192:195], v[124:127]\n\t" \
  "s_waitcnt lgkmcnt(6)\n\t" \
  "v_perm_b32 v73, v227, v224, s79\n\t" \
  "v_mfma_f32_16x16x32_bf16 v[96:99], v[128:131], a[160:163], v[96:99]\n\t" \
  "ds_read_b128 a[128:131], v242 offset:8192\n\t" \
  "v_mfma_f32_16x16x32_bf16 v[96:99], v[132:135], a[164:167], v[96:99]\n\t" \
  "buffer_load_dwordx2 v[40:41], v252, %[sd], s74 offen offset:0\n\t" \
  "v_mfma_f32_16x16x32_bf16 v[96:99], v[136:139], a[168:171], v[96:99]\n\t" \
  "ds_read_b128 a[132:135], v242 offset:9216\n\t" \
  "v_perm_b32 v77, v227, v224, s80\n\t" \
  "v_mfma_f32_16x16x32_bf16 v[96:99], v[140:143], a[172:175], v[96:99]\n\t" \
  "ds_read_b128 a[136:139], v242 offset:10240\n\t" \
  "v_mfma_f32_16x16x32_bf16 v[112:115], v[156:159], a[172:175], v[112:115]\n\t" \
  "ds_read_b128 a[140:143], v242 offset:11264\n\t" \
  "v_accvgpr_read_b32 v224, a234\n\t" \
  "v_mfma_f32_16x16x32_bf16 v[112:115], v[152:155], a[168:171], v[112:115]\n\t" \
  ".Lhs15:\n\ts_cbranch_execz .Lhs15n\n\t" \
  "buffer_load_dwordx2 v[42:43], v252, %[sd], s74 offen offset:2048\n\t" \
  "s_mov_b32 s74, 12288\n\t" \
  "v_mfma_f32_16x16x32_bf16 v[112:115], v[148:151], a[164:167], v[112:115]\n\t" \
  "v_mfma_f32_16x16x32_bf16 v[112:115], v[144:147], a[160:163], v[112:115]\n\t" \
  "s_waitcnt lgkmcnt(5)\n\t" \
  "v_accvgpr_read_b32 v227, a238\n\t" \
  "v_mfma_f32_16x16x32_bf16 v[100:103], v[128:131], v[208:211], v[100:103]\n\t" \
  "ds_read_b128 a[144:147], v242 offset:12288\n\t" \
  "v_accvgpr_write_b32 a192, 0\n\t" \
  "v_mfma_f32_16x16x32_bf16 v[100:103], v[132:135], v[212:215], v[100:103]\n\t" \
  "buffer_load_dwordx2 v[44:45], v252, %[sd], s74 offen offset:0\n\t" \
  "v_mfma_f32_16x16x32_bf16 v[100:103], v[136:139], v[216:219], v[100:103]\n\t" \
  "ds_read_b128 a[148:151], v242 offset:13312\n\t" \
  "v_accvgpr_write_b32 a193, 0\n\t" \
  "v_mfma_f32_16x16x32_bf16 v[100:103], v[140:143], v[220:223], v[100:103]\n\t" \
  "ds_read_b128 a[152:155], v242 offset:14336\n\t" \
  "v_accvgpr_write_b32 a194, v238\n\t" \
  "v_mfma_f32_16x16x32_bf16 v[116:119], v[156:159], v[220:223], v[116:119]\n\t" \
  "ds_read_b128 a[156:159], v242 offset:15360\n\t" \
  "v_accvgpr_write_b32 a195, v239\n\t" \
  "v_mfma_f32_16x16x32_bf16 v[116:119], v[152:155], v[216:219], v[116:119]\n\t" \
  "buffer_load_dwordx2 v[46:47], v252, %[sd], s74 offen offset:2048\n\t" \
  "v_mfma_f32_16x16x32_bf16 v[116:119], v[148:151], v[212:215], v[116:119]\n\t" \
  "v_accvgpr_write_b32 a196, v238\n\t" \
  "v_accvgpr_write_b32 a197, v239\n\t" \
  "v_mfma_f32_16x16x32_bf16 v[116:119], v[144:147], v[208:211], v[116:119]\n\t" \
  "s_waitcnt vmcnt(14) lgkmcnt(4)\n\t" \
  "s_barrier\n\t" \
  "v_mfma_f32_16x16x32_bf16 v[104:107], v[128:131], a[128:131], v[104:107]\n\t" \
  "ds_read_b128 v[16:19], v242 offset:49152\n\t" \
  "v_accvgpr_write_b32 a198, 0\n\t" \
  "v_mfma_f32_16x16x32_bf16 v[104:107], v[132:135], a[132:135], v[104:107]\n\t" \
  "ds_read_b128 v[20:23], v242 offset:50176\n\t" \
  "v_accvgpr_write_b32 a199, 0\n\t" \
  "v_mfma_f32_16x16x32_bf16 v[104:107], v[136:139], a[136:139], v[104:107]\n\t" \
  "buffer_load_dwordx4 v242, %[ws], s71 offen offset:0 sc1 lds\n\t" \
  "v_mfma_f32_16x16x32_bf16 v[104:107], v[140:143], a[140:143], v[104:107]\n\t" \
  "ds_read_b128 v[24:27], v242 offset:51200\n\t" \
  "ds_read_b64 v[208:209], v231 offset:0\n\t" \
  "v_mfma_f32_16x16x32_bf16 v[120:123], v[156:159], a[140:143], v[120:123]\n\t" \
  "ds_read_b128 v[28:31], v242 offset:52224\n\t" \
  "v_perm_b32 v74, v227, v224, s79\n\t" \
  "v_mfma_f32_16x16x32_bf16 v[120:123], v[152:155], a[136:139], v[120:123]\n\t" \
  "ds_read_b64 v[210:211], v231 offset:4096\n\t" \
  "v_perm_b32 v78, v227, v224, s80\n\t" \
  "v_mfma_f32_16x16x32_bf16 v[120:123], v[148:151], a[132:135], v[120:123]\n\t" \
  "buffer_load_dwordx4 v242, %[ws], s71 offen offset:1024 sc1 lds\n\t" \
  "v_mfma_f32_16x16x32_bf16 v[120:123], v[144:147], a[128:131], v[120:123]\n\t" \
  "s_waitcnt lgkmcnt(6)\n\t" \
  "ds_read_b64 v[212:213], v231 offset:8192\n\t" \
  "v_accvgpr_read_b32 v224, a235\n\t" \
  "v_mfma_f32_16x16x32_bf16 v[108:111], v[128:131], a[144:147], v[108:111]\n\t" \
  "ds_read_b128 a[8:11], v242 offset:53248\n\t" \
  "ds_read_b64 v[214:215], v231 offset:12288\n\t" \
  "v_mfma_f32_16x16x32_bf16 v[108:111], v[132:135], a[148:151], v[108:111]\n\t" \
  "ds_read_b128 a[24:27], v242 offset:54272\n\t" \
  "ds_read_b64 v[216:217], v233 offset:0\n\t" \
  "v_mfma_f32_16x16x32_bf16 v[108:111], v[136:139], a[152:155], v[108:111]\n\t" \
  "ds_read_b128 a[40:43], v242 offset:55296\n\t" \
  "v_accvgpr_read_b32 v227, a239\n\t" \
  "v_mfma_f32_16x16x32_bf16 v[108:111], v[140:143], a[156:159], v[108:111]\n\t" \
  "buffer_load_dwordx4 v242, %[ws], s71 offen offset:2048 sc1 lds\n\t" \
  "v_mfma_f32_16x16x32_bf16 v[124:127], v[156:159], a[156:159], v[124:127]\n\t" \
  "ds_read_b64 v[218:219], v233 offset:4096\n\t" \
  "ds_read_b64 v[220:221], v233 offset:8192\n\t" \
  "v_mfma_f32_16x16x32_bf16 v[124:127], v[152:155], a[152:155], v[124:127]\n\t" \
  "ds_read_b128 a[56:59], v242 offset:56320\n\t" \
  "v_perm_b32 v75, v227, v224, s79\n\t" \
  "ds_read_b64 v[222:223], v233 offset:12288\n\t" \
  "v_mfma_f32_16x16x32_bf16 v[124:127], v[148:151], a[148:151], v[124:127]\n\t" \
  "v_perm_b32 v79, v227, v224, s80\n\t" \
  "v_accvgpr_read_b32 v224, a240\n\t" \
  "v_mfma_f32_16x16x32_bf16 v[124:127], v[144:147], a[144:147], v[124:127]\n\t" \
  "s_waitcnt lgkmcnt(11)\n\t" \
  "buffer_load_dwordx4 v242, %[ws], s71 offen offset:3072 sc1 lds\n\t" \
  "v_mfma_f32_16x16x32_bf16 v[96:99], a[12:15], v[16:19], v[96:99]\n\t" \
  "ds_read_b128 v[128:131], v242 offset:57344\n\t" \
  "v_accvgpr_read_b32 v227, a244\n\t" \
  "v_mfma_f32_16x16x32_bf16 v[96:99], a[28:31], v[20:23], v[96:99]\n\t" \
  "buffer_load_dword v228, v235, %[ws], s68 offen sc1\n\t" \
  "v_mfma_f32_16x16x32_bf16 v[96:99], a[44:47], v[24:27], v[96:99]\n\t" \
  "ds_read_b128 v[132:135], v242 offset:58368\n\t" \
  "v_perm_b32 v80, v227, v224, s79\n\t" \
  "v_mfma_f32_16x16x32_bf16 v[96:99], a[60:63], v[28:31], v[96:99]\n\t" \
  "ds_read_b128 a[72:75], v242 offset:59392\n\t" \
  "v_perm_b32 v84, v227, v224, s80\n\t" \
  "s_waitcnt vmcnt(9)\n\t" \
  "v_cmp_ne_u32 vcc, %[tag], v246\n\t" \
  "s_and_b32 vcc_lo, vcc_lo, 0xff\n\t" \
  "s_or_b32 s81, s81, vcc_lo\n\t" \
  "v_mfma_f32_16x16x32_bf16 v[112:115], a[124:127], v[28:31], v[112:115]\n\t" \
  "ds_read_b128 a[88:91], v242 offset:60416\n\t" \
  "v_accvgpr_read_b32 v224, a241\n\t" \
  "v_mfma_f32_16x16x32_bf16 v[112:115], a[108:111], v[24:27], v[112:115]\n\t" \
  "s_and_b32 s72, s81, 15\n\t" \
  "s_cselect_b32 s72, -1, 0\n\t" \
  "v_lshl_or_b32 v247, s72, 31, v242\n\t" \
  "s_add_u32 s72, s63, 0\n\t" \
  "v_mfma_f32_16x16x32_bf16 v[112:115], a[92:95], v[20:23], v[112:115]\n\t" \
  "buffer_load_dwordx4 a[128:131], v247, %[ws], s72 offen sc1\n\t" \
  "v_mfma_f32_16x16x32_bf16 v[112:115], a[76:79], v[16:19], v[112:115]\n\t" \
  "s_waitcnt lgkmcnt(5)\n\t" \
  "s_add_u32 s72, s64, 32768\n\t" \
  "v_accvgpr_read_b32 v227, a245\n\t" \
  "v_mfma_f32_16x16x32_bf16 v[100:103], a[12:15], a[8:11], v[100:103]\n\t" \
  "ds_read_b128 a[104:107], v242 offset:61440\n\t" \
  "v_perm_b32 v81, v227, v224, s79\n\t" \
  "v_mfma_f32_16x16x32_bf16 v[100:103], a[28:31], a[24:27], v[100:103]\n\t" \
  "buffer_load_dwordx4 a[140:143], v247, %[ws], s72 offen sc1\n\t" \
  "v_mfma_f32_16x16x32_bf16 v[100:103], a[44:47], a[40:43], v[100:103]\n\t" \
  "s_add_u32 s72, s63, 65536\n\t" \
  "ds_read_b128 a[120:123], v242 offset:62464\n\t" \
  "v_perm_b32 v85, v227, v224, s80\n\t" \
  "v_mfma_f32_16x16x32_bf16 v[100:103], a[60:63], a[56:59], v[100:103]\n\t" \
  "ds_read_b128 a[176:179], v242 offset:63488\n\t" \
  "v_mfma_f32_16x16x32_bf16 v[116:119], a[124:127], a[56:59], v[116:119]\n\t" \
  "ds_read_b128 a[180:183], v242 offset:64512\n\t" \
  "v_accvgpr_read_b32 v224, a242\n\t" \
  "v_mfma_f32_16x16x32_bf16 v[116:119], a[108:111], a[40:43], v[116:119]\n\t" \
  "buffer_load_dwordx4 a[152:155], v247, %[ws], s72 offen sc1\n\t" \
  "s_add_u32 s72, s64, 98304\n\t" \
  "v_mfma_f32_16x16x32_bf16 v[116:119], a[92:95], a[24:27], v[116:119]\n\t" \
  "s_waitcnt lgkmcnt(8)\n\t" \
  "v_lshlrev_b32 v0, 16, v208\n\t" \
  "v_and_b32 v1, 0xffff0000, v208\n\t" \
  "v_mfma_f32_16x16x32_bf16 v[116:119], a[76:79], a[8:11], v[116:119]\n\t" \
  "s_waitcnt lgkmcnt(4)\n\t" \
  "s_barrier\n\t" \
  "v_mfma_f32_16x16x32_bf16 v[104:107], a[12:15], v[128:131], v[104:107]\n\t" \
  "ds_read_b128 a[184:187], v230 offset:0\n\t" \
  "v_lshlrev_b32 v2, 16, v209\n\t" \
  "v_mfma_f32_16x16x32_bf16 v[104:107], a[28:31], v[132:135], v[104:107]\n\t" \
  "ds_read_b128 a[188:191], v230 offset:1024\n\t" \
  "v_and_b32 v3, 0xffff0000, v209\n\t" \
  "v_mfma_f32_16x16x32_bf16 v[104:107], a[44:47], a[72:75], v[104:107]\n\t" \
  "buffer_load_dwordx4 a[164:167], v247, %[ws], s72 offen sc1\n\t" \
  "v_mfma_f32_16x16x32_bf16 v[104:107], a[60:63], a[88:91], v[104:107]\n\t" \
  "s_and_b32 s72, s81, 240\n\t" \
  "s_cselect_b32 s72, -1, 0\n\t" \
  "v_lshl_or_b32 v247, s72, 31, v242\n\t" \
  "s_add_u32 s72, s63, 4096\n\t" \
  "v_mfma_f32_16x16x32_bf16 v[120:123], a[124:127], a[88:91], v[120:123]\n\t" \
  "ds_read_b128 v[136:139], v230 offset:2048\n\t" \
  "v_lshlrev_b32 v4, 16, v210\n\t" \
  "v_mfma_f32_16x16x32_bf16 v[120:123], a[108:111], a[72:75], v[120:123]\n\t" \
  "ds_read_b128 v[140:143], v230 offset:3072\n\t" \
  "v_and_b32 v5, 0xffff0000, v210\n\t" \
  "v_mfma_f32_16x16x32_bf16 v[120:123], a[92:95], v[132:135], v[120:123]\n\t" \
  "buffer_load_dwordx4 a[132:135], v247, %[ws], s72 offen sc1\n\t" \
  "s_add_u32 s72, s64, 36864\n\t" \
  "v_mfma_f32_16x16x32_bf16 v[120:123], a[76:79], v[128:131], v[120:123]\n\t" \
  "s_waitcnt lgkmcnt(4)\n\t" \
  "v_lshlrev_b32 v6, 16, v211\n\t" \
  "v_and_b32 v7, 0xffff0000, v211\n\t" \
  "v_mfma_f32_16x16x32_bf16 v[108:111], a[12:15], a[104:107], v[108:111]\n\t" \
  "ds_read_b128 v[128:131], v230 offset:4096\n\t" \
  "v_accvgpr_read_b32 v227, a246\n\t" \
  "v_mfma_f32_16x16x32_bf16 v[108:111], a[28:31], a[120:123], v[108:111]\n\t" \
  "ds_read_b128 v[132:135], v230 offset:5120\n\t" \
  "v_lshlrev_b32 v8, 16, v212\n\t" \
  "v_mfma_f32_16x16x32_bf16 v[108:111], a[44:47], a[176:179], v[108:111]\n\t" \
  "ds_read_b128 v[144:147], v230 offset:6144\n\t" \
  "v_perm_b32 v82, v227, v224, s79\n\t" \
  "v_mfma_f32_16x16x32_bf16 v[108:111], a[60:63], a[180:183], v[108:111]\n\t" \
  "buffer_load_dwordx4 a[144:147], v247, %[ws], s72 offen sc1\n\t" \
  "s_add_u32 s72, s63, 69632\n\t" \
  "v_mfma_f32_16x16x32_bf16 v[124:127], a[124:127], a[180:183], v[124:127]\n\t" \
  "ds_read_b128 v[148:151], v230 offset:7168\n\t" \
  "v_and_b32 v9, 0xffff0000, v212\n\t" \
  "v_mfma_f32_16x16x32_bf16 v[124:127], a[108:111], a[176:179], v[124:127]\n\t" \
  "v_perm_b32 v86, v227, v224, s80\n\t" \
  "v_lshlrev_b32 v10, 16, v213\n\t" \
  "v_mfma_f32_16x16x32_bf16 v[124:127], a[92:95], a[120:123], v[124:127]\n\t" \
  "v_accvgpr_read_b32 v224, a243\n\t" \
  "v_and_b32 v11, 0xffff0000, v213\n\t" \
  "v_mfma_f32_16x16x32_bf16 v[124:127], a[76:79], a[104:107], v[124:127]\n\t" \
  "s_waitcnt lgkmcnt(4)\n\t" \
  "buffer_load_dwordx4 a[156:159], v247, %[ws], s72 offen sc1\n\t" \
  "s_add_u32 s72, s64, 102400\n\t" \
  "v_mfma_f32_16x16x32_bf16 v[96:99], a[96:99], a[184:187], v[96:99]\n\t" \
  "ds_read_b128 v[152:155], v230 offset:8192\n\t" \
  "v_lshlrev_b32 v12, 16, v214\n\t" \
  "v_mfma_f32_16x16x32_bf16 v[96:99], a[112:115], a[188:191], v[96:99]\n\t" \
  "ds_read_b128 v[156:159], v230 offset:9216\n\t" \
  "v_accvgpr_read_b32 v227, a247\n\t" \
  "v_mfma_f32_16x16x32_bf16 v[96:99], a[100:103], v[136:139], v[96:99]\n\t" \
  "ds_read_b128 a[8:11], v230 offset:10240\n\t" \
  "v_and_b32 v13, 0xffff0000, v214\n\t" \
  "v_mfma_f32_16x16x32_bf16 v[96:99], a[116:119], v[140:143], v[96:99]\n\t" \
  "buffer_load_dwordx4 a[168:171], v247, %[ws], s72 offen sc1\n\t" \
  "v_mfma_f32_16x16x32_bf16 v[112:115], a[84:87], v[140:143], v[112:115]\n\t" \
  "ds_read_b128 a[24:27], v230 offset:11264\n\t" \
  "v_perm_b32 v83, v227, v224, s79\n\t" \
  "v_mfma_f32_16x16x32_bf16 v[112:115], a[80:83], v[136:139], v[112:115]\n\t" \
  "v_lshlrev_b32 v14, 16, v215\n\t" \
  "v_perm_b32 v87, v227, v224, s80\n\t" \
  "v_mfma_f32_16x16x32_bf16 v[112:115], a[68:71], a[188:191], v[112:115]\n\t" \
  "v_and_b32 v15, 0xffff0000, v215\n\t" \
  "v_accvgpr_read_b32 v224, a248\n\t" \
  "v_mfma_f32_16x16x32_bf16 v[112:115], a[64:67], a[184:187], v[112:115]\n\t" \
  "s_waitcnt lgkmcnt(4)\n\t" \
  "v_lshlrev_b32 v16, 16, v216\n\t" \
  "v_and_b32 v17, 0xffff0000, v216\n\t" \
  "v_mfma_f32_16x16x32_bf16 v[100:103], a[96:99], v[128:131], v[100:103]\n\t" \
  "ds_read_b128 a[40:43], v230 offset:12288\n\t" \
  "v_accvgpr_read_b32 v227, a252\n\t" \
  "v_mfma_f32_16x16x32_bf16 v[100:103], a[112:115], v[132:135], v[100:103]\n\t" \
  "ds_read_b128 a[56:59], v230 offset:13312\n\t" \
  "v_lshlrev_b32 v18, 16, v217\n\t" \
  "v_mfma_f32_16x16x32_bf16 v[100:103], a[100:103], v[144:147], v[100:103]\n\t" \
  "ds_read_b128 a[72:75], v230 offset:14336\n\t" \
  "v_perm_b32 v88, v227, v224, s79\n\t" \
  "v_mfma_f32_16x16x32_bf16 v[100:103], a[116:119], v[148:151], v[100:103]\n\t" \
  "ds_read_b128 a[88:91], v230 offset:15360\n\t" \
  "v_and_b32 v19, 0xffff0000, v217\n\t" \
  "s_waitcnt vmcnt(8)\n\t" \
  "v_cmp_ne_u32 vcc, %[tag], v228\n\t" \
  "s_and_b32 vcc_lo, vcc_lo, 0xf00\n\t" \
  "s_or_b32 s81, s81, vcc_lo\n\t" \
  "v_mfma_f32_16x16x32_bf16 v[116:119], a[84:87], v[148:151], v[116:119]\n\t" \
  "s_and_b32 s72, s81, 3840\n\t" \
  "s_cselect_b32 s72, -1, 0\n\t" \
  "v_lshl_or_b32 v247, s72, 31, v242\n\t" \
  "s_add_u32 s72, s63, 16384\n\t" \
  "v_mfma_f32_16x16x32_bf16 v[116:119], a[80:83], v[144:147], v[116:119]\n\t" \
  "buffer_load_dwordx4 a[136:139], v247, %[ws], s72 offen sc1\n\t" \
  "v_mfma_f32_16x16x32_bf16 v[116:119], a[68:71], v[132:135], v[116:119]\n\t" \
  "s_add_u32 s72, s64, 49152\n\t" \
  "v_perm_b32 v92, v227, v224, s80\n\t" \
  "v_lshlrev_b32 v20, 16, v218\n\t" \
  "v_mfma_f32_16x16x32_bf16 v[116:119], a[64:67], v[128:131], v[116:119]\n\t" \
  "s_waitcnt lgkmcnt(4)\n\t" \
  "s_barrier\n\t" \
  "v_mfma_f32_16x16x32_bf16 v[104:107], a[96:99], v[152:155], v[104:107]\n\t" \
  "buffer_load_dwordx4 a[148:151], v247, %[ws], s72 offen sc1\n\t" \
  "v_mfma_f32_16x16x32_bf16 v[104:107], a[112:115], v[156:159], v[104:107]\n\t" \
  "s_add_u32 s72, s63, 81920\n\t" \
  "ds_read_b128 v[128:131], v242 offset:16384\n\t" \
  "v_accvgpr_read_b32 v224, a249\n\t" \
  "v_mfma_f32_16x16x32_bf16 v[104:107], a[100:103], a[8:11], v[104:107]\n\t" \
  "ds_read_b128 v[132:135], v242 offset:17408\n\t" \
  "v_and_b32 v21, 0xffff0000, v218\n\t" \
  "v_mfma_f32_16x16x32_bf16 v[104:107], a[116:119], a[24:27], v[104:107]\n\t" \
  "buffer_load_dwordx4 a[160:163], v247, %[ws], s72 offen sc1\n\t" \
  "v_mfma_f32_16x16x32_bf16 v[120:123], a[84:87], a[24:27], v[120:123]\n\t" \
  "s_add_u32 s72, s64, 114688\n\t" \
  "ds_read_b128 a[12:15], v242 offset:18432\n\t" \
  "v_lshlrev_b32 v22, 16, v219\n\t" \
  "v_mfma_f32_16x16x32_bf16 v[120:123], a[80:83], a[8:11], v[120:123]\n\t" \
  "ds_read_b128 a[28:31], v242 offset:19456\n\t" \
  "v_accvgpr_read_b32 v227, a253\n\t" \
  "v_mfma_f32_16x16x32_bf16 v[120:123], a[68:71], v[156:159], v[120:123]\n\t" \
  "v_and_b32 v23, 0xffff0000, v219\n\t" \
  "v_perm_b32 v89, v227, v224, s79\n\t" \
  "v_mfma_f32_16x16x32_bf16 v[120:123], a[64:67], v[152:155], v[120:123]\n\t" \
  "s_waitcnt lgkmcnt(4)\n\t" \
  "buffer_load_dwordx4 a[172:175], v247, %[ws], s72 offen sc1\n\t" \
  "v_mfma_f32_16x16x32_bf16 v[108:111], a[96:99], a[40:43], v[108:111]\n\t" \
  "ds_read_b128 a[44:47], v242 offset:20480\n\t" \
  "v_lshlrev_b32 v24, 16, v220\n\t" \
  "v_mfma_f32_16x16x32_bf16 v[108:111], a[112:115], a[56:59], v[108:111]\n\t" \
  "ds_read_b128 a[60:63], v242 offset:21504\n\t" \
  "v_perm_b32 v93, v227, v224, s80\n\t" \
  "v_mfma_f32_16x16x32_bf16 v[108:111], a[100:103], a[72:75], v[108:111]\n\t" \
  "ds_read_b128 a[76:79], v242 offset:22528\n\t" \
  "v_and_b32 v25, 0xffff0000, v220\n\t" \
  "v_mfma_f32_16x16x32_bf16 v[108:111], a[116:119], a[88:91], v[108:111]\n\t" \
  "ds_read_b128 a[92:95], v242 offset:23552\n\t" \
  "v_accvgpr_read_b32 v224, a250\n\t" \
  "v_mfma_f32_16x16x32_bf16 v[124:127], a[84:87], a[88:91], v[124:127]\n\t" \
  "v_lshlrev_b32 v26, 16, v221\n\t" \
  "v_and_b32 v27, 0xffff0000, v221\n\t" \
  "v_mfma_f32_16x16x32_bf16 v[124:127], a[80:83], a[72:75], v[124:127]\n\t" \
  "v_accvgpr_read_b32 v227, a254\n\t" \
  "v_lshlrev_b32 v28, 16, v222\n\t" \
  "v_mfma_f32_16x16x32_bf16 v[124:127], a[68:71], a[56:59], v[124:127]\n\t" \
  "v_perm_b32 v90, v227, v224, s79\n\t" \
  "v_and_b32 v29, 0xffff0000, v222\n\t" \
  "v_mfma_f32_16x16x32_bf16 v[124:127], a[64:67], a[40:43], v[124:127]\n\t" \
  "s_waitcnt lgkmcnt(4)\n\t" \
  "v_perm_b32 v94, v227, v224, s80\n\t" \
  "v_lshlrev_b32 v30, 16, v223\n\t" \
  "v_mfma_f32_16x16x32_bf16 v[96:99], a[32:35], v[128:131], v[96:99]\n\t" \
  "ds_read_b128 a[104:107], v242 offset:24576\n\t" \
  "v_accvgpr_read_b32 v224, a251\n\t" \
  "v_mfma_f32_16x16x32_bf16 v[96:99], a[48:51], v[132:135], v[96:99]\n\t" \
  "ds_read_b128 a[108:111], v242 offset:25600\n\t" \
  "v_and_b32 v31, 0xffff0000, v223\n\t" \
  "v_mfma_f32_16x16x32_bf16 v[96:99], a[36:39], a[12:15], v[96:99]\n\t" \
  "buffer_load_dword v246, v235, %[ws], s68 offen sc1\n\t" \
  "v_mfma_f32_16x16x32_bf16 v[96:99], a[52:55], a[28:31], v[96:99]\n\t" \
  "ds_read_b128 a[120:123], v242 offset:26624\n\t" \
  "v_accvgpr_read_b32 v227, a255\n\t" \
  "v_mfma_f32_16x16x32_bf16 v[112:115], a[20:23], a[28:31], v[112:115]\n\t" \
  "ds_read_b128 a[124:127], v242 offset:27648\n\t" \
  "v_perm_b32 v91, v227, v224, s79\n\t" \
  "v_mfma_f32_16x16x32_bf16 v[112:115], a[4:7], a[12:15], v[112:115]\n\t" \
  "v_mfma_f32_16x16x32_bf16 v[112:115], a[16:19], v[132:135], v[112:115]\n\t" \
  "v_perm_b32 v95, v227, v224, s80\n\t" \
  "v_mfma_f32_16x16x32_bf16 v[112:115], a[0:3], v[128:131], v[112:115]\n\t" \
  "s_waitcnt lgkmcnt(4)\n\t" \
  "v_mfma_f32_16x16x32_bf16 v[100:103], a[32:35], a[44:47], v[100:103]\n\t" \
  "ds_read_b128 a[176:179], v242 offset:28672\n\t" \
  "v_mfma_f32_16x16x32_bf16 v[100:103], a[48:51], a[60:63], v[100:103]\n\t" \
  "ds_read_b128 a[180:183], v242 offset:29696\n\t" \
  "v_mfma_f32_16x16x32_bf16 v[100:103], a[36:39], a[76:79], v[100:103]\n\t" \
  "ds_read_b128 a[184:187], v242 offset:30720\n\t" \
  "v_mfma_f32_16x16x32_bf16 v[100:103], a[52:55], a[92:95], v[100:103]\n\t" \
  "ds_read_b128 a[188:191], v242 offset:31744\n\t" \
  "v_mfma_f32_16x16x32_bf16 v[116:119], a[20:23], a[92:95], v[116:119]\n\t" \
  "v_mfma_f32_16x16x32_bf16 v[116:119], a[4:7], a[76:79], v[116:119]\n\t" \
  "v_mfma_f32_16x16x32_bf16 v[116:119], a[16:19], a[60:63], v[116:119]\n\t" \
  "v_mfma_f32_16x16x32_bf16 v[116:119], a[0:3], a[44:47], v[116:119]\n\t" \
  "s_waitcnt lgkmcnt(4)\n\t" \
  "v_mfma_f32_16x16x32_bf16 v[104:107], a[32:35], a[104:107], v[104:107]\n\t" \
  "ds_read_b128 v[128:131], v237 offset:2560\n\t" \
  "v_mfma_f32_16x16x32_bf16 v[104:107], a[48:51], a[108:111], v[104:107]\n\t" \
  "ds_read_b128 v[132:135], v237 offset:2624\n\t" \
  "v_mfma_f32_16x16x32_bf16 v[104:107], a[36:39], a[120:123], v[104:107]\n\t" \
  "v_mfma_f32_16x16x32_bf16 v[104:107], a[52:55], a[124:127], v[104:107]\n\t" \
  "v_mfma_f32_16x16x32_bf16 v[120:123], a[20:23], a[124:127], v[120:123]\n\t" \
  "v_mfma_f32_16x16x32_bf16 v[120:123], a[4:7], a[120:123], v[120:123]\n\t" \
  "v_mfma_f32_16x16x32_bf16 v[120:123], a[16:19], a[108:111], v[120:123]\n\t" \
  "v_mfma_f32_16x16x32_bf16 v[120:123], a[0:3], a[104:107], v[120:123]\n\t" \
  "s_waitcnt lgkmcnt(2)\n\t" \
  "v_mfma_f32_16x16x32_bf16 v[108:111], a[32:35], a[176:179], v[108:111]\n\t" \
  "v_mfma_f32_16x16x32_bf16 v[108:111], a[48:51], a[180:183], v[108:111]\n\t" \
  "v_mfma_f32_16x16x32_bf16 v[108:111], a[36:39], a[184:187], v[108:111]\n\t" \
  "v_mfma_f32_16x16x32_bf16 v[108:111], a[52:55], a[188:191], v[108:111]\n\t" \
  "v_mfma_f32_16x16x32_bf16 v[124:127], a[20:23], a[188:191], v[124:127]\n\t" \
  "v_mfma_f32_16x16x32_bf16 v[124:127], a[4:7], a[184:187], v[124:127]\n\t" \
  "v_mfma_f32_16x16x32_bf16 v[124:127], a[16:19], a[180:183], v[124:127]\n\t" \
  "v_mfma_f32_16x16x32_bf16 v[124:127], a[0:3], a[176:179], v[124:127]\n\t" \
  STP("v_writelane_b32 %[stv], s94, 36\n\t") \
  STP("v_writelane_b32 %[stv], s92, 37\n\t") \
  STP("s_memrealtime s[94:95]\n\t") \
  STP("s_memtime s[92:93]\n\t") \
  "s_cmp_lg_u32 s81, 0\n\t" \
  "s_cbranch_scc1 .Lop_s99\n\t" \
  ".Lop_d2:\n\t" \
  "s_waitcnt vmcnt(9)\n\t" \
  "v_mfma_f32_16x16x32_bf16 v[96:99], a[196:199], a[128:131], v[96:99]\n\t" \
  "v_mfma_f32_16x16x32_bf16 v[100:103], a[196:199], a[140:143], v[100:103]\n\t" \
  "v_mfma_f32_16x16x32_bf16 v[104:107], a[196:199], a[152:155], v[104:107]\n\t" \
  "v_mfma_f32_16x16x32_bf16 v[108:111], a[196:199], a[164:167], v[108:111]\n\t" \
  "s_waitcnt vmcnt(5)\n\t" \
  "v_mfma_f32_16x16x32_bf16 v[112:115], a[194:197], a[130:133], v[112:115]\n\t" \
  "v_mfma_f32_16x16x32_bf16 v[116:119], a[194:197], a[142:145], v[116:119]\n\t" \
  "v_mfma_f32_16x16x32_bf16 v[120:123], a[194:197], a[154:157], v[120:123]\n\t" \
  "v_mfma_f32_16x16x32_bf16 v[124:127], a[194:197], a[166:169], v[124:127]\n\t" \
  "s_waitcnt vmcnt(1)\n\t" \
  "v_mfma_f32_16x16x32_bf16 v[96:99], a[194:197], a[134:137], v[96:99]\n\t" \
  "v_mfma_f32_16x16x32_bf16 v[100:103], a[194:197], a[146:149], v[100:103]\n\t" \
  "v_mfma_f32_16x16x32_bf16 v[104:107], a[194:197], a[158:161], v[104:107]\n\t" \
  "v_mfma_f32_16x16x32_bf16 v[108:111], a[194:197], a[170:173], v[108:111]\n\t" \
  "v_mfma_f32_16x16x32_bf16 v[112:115], a[192:195], a[136:139], v[112:115]\n\t" \
  "v_mfma_f32_16x16x32_bf16 v[116:119], a[192:195], a[148:151], v[116:119]\n\t" \
  "v_mfma_f32_16x16x32_bf16 v[120:123], a[192:195], a[160:163], v[120:123]\n\t" \
  "v_mfma_f32_16x16x32_bf16 v[124:127], a[192:195], a[172:175], v[124:127]\n\t" \
  "s_waitcnt vmcnt(0) lgkmcnt(0)\n\t" \
  STP("v_writelane_b32 %[stv], s94, 38\n\t") \
  STP("v_writelane_b32 %[stv], s92, 39\n\t") \
  "s_nop 7\n\t" \
  ".subsection 1\n\t" \
  ".Lop_s0:\n\t" \
  "s_mov_b32 s78, 0\n\t" \
  ".Lop_r0:\n\t" \
  "s_sleep 2\n\t" \
  "buffer_load_dword v246, v245, %[ws], %[fw2] offen sc1\n\t" \
  "s_waitcnt vmcnt(0)\n\t" \
  "v_cmp_ne_u32 vcc, %[tag], v246\n\t" \
  "s_and_b32 vcc_lo, vcc_lo, 0x6\n\t" \
  "s_cbranch_scc0 .Lop_k0\n\t" \
  "s_add_u32 s78, s78, 1\n\t" \
  "s_cmp_lt_u32 s78, %[cap]\n\t" \
  "s_cbranch_scc1 .Lop_r0\n\t" \
  "buffer_store_dword v243, off, %[ws], 0 offset:256 sc1\n\t" \
  "s_branch .Lop_k0\n\t" \
  ".Lop_s1:\n\t" \
  "s_mov_b32 s78, 0\n\t" \
  ".Lop_r1:\n\t" \
  "s_sleep 2\n\t" \
  "buffer_load_dword v246, v245, %[ws], %[fw2] offen sc1\n\t" \
  "s_waitcnt vmcnt(0)\n\t" \
  "v_cmp_ne_u32 vcc, %[tag], v246\n\t" \
  "s_and_b32 vcc_lo, vcc_lo, 0xe00\n\t" \
  "s_cbranch_scc0 .Lop_k1\n\t" \
  "s_add_u32 s78, s78, 1\n\t" \
  "s_cmp_lt_u32 s78, %[cap]\n\t" \
  "s_cbranch_scc1 .Lop_r1\n\t" \
  "buffer_store_dword v243, off, %[ws], 0 offset:256 sc1\n\t" \
  "s_branch .Lop_k1\n\t" \
  ".Lop_s2:\n\t" \
  "s_mov_b32 s78, 0\n\t" \
  ".Lop_r2:\n\t" \
  "s_sleep 2\n\t" \
  "buffer_load_dword v246, v245, %[ws], %[fw2] offen sc1\n\t" \
  "s_waitcnt vmcnt(0)\n\t" \
  "v_cmp_ne_u32 vcc, %[tag], v246\n\t" \
  "s_and_b32 vcc_lo, vcc_lo, 0xfe00\n\t" \
  "s_cbranch_scc0 .Lop_k2\n\t" \
  "s_add_u32 s78, s78, 1\n\t" \
  "s_cmp_lt_u32 s78, %[cap]\n\t" \
  "s_cbranch_scc1 .Lop_r2\n\t" \
  "buffer_store_dword v243, off, %[ws], 0 offset:256 sc1\n\t" \
  "s_branch .Lop_k2\n\t" \
  ".Lop_s3:\n\t" \
  "s_mov_b32 s78, 0\n\t" \
  ".Lop_r3:\n\t" \
  "s_sleep 2\n\t" \
  "buffer_load_dword v246, v245, %[ws], %[fw2] offen sc1\n\t" \
  "s_waitcnt vmcnt(0)\n\t" \
  "v_cmp_ne_u32 vcc, %[tag], v246\n\t" \
  "s_and_b32 vcc_lo, vcc_lo, 0xfe0000\n\t" \
  "s_cbranch_scc0 .Lop_k3\n\t" \
  "s_add_u32 s78, s78, 1\n\t" \
  "s_cmp_lt_u32 s78, %[cap]\n\t" \
  "s_cbranch_scc1 .Lop_r3\n\t" \
  "buffer_store_dword v243, off, %[ws], 0 offset:256 sc1\n\t" \
  "s_branch .Lop_k3\n\t" \
  ".Lop_s4:\n\t" \
  "s_mov_b32 s78, 0\n\t" \
  ".Lop_r4:\n\t" \
  "s_sleep 4\n\t" \
  "s_add_u32 s75, s69, 0\n\t" \
  "buffer_load_dwordx4 v[208:211], v170, %[ws], s75 offen sc1\n\t" \
  "s_add_u32 s75, s69, 32768\n\t" \
  "buffer_load_dwordx4 v[212:215], v170, %[ws], s75 offen sc1\n\t" \
  "s_add_u32 s75, s69, 65536\n\t" \
  "buffer_load_dwordx4 v[216:219], v170, %[ws], s75 offen sc1\n\t" \
  "s_add_u32 s75, s69, 98304\n\t" \
  "buffer_load_dwordx4 v[220:223], v170, %[ws], s75 offen sc1\n\t" \
  "s_waitcnt vmcnt(0)\n\t" \
  "v_xor_b32 v246, v243, v209\n\t" \
  "v_bitop3_b32 v246, v246, v211, v243 bitop3:0xf6\n\t" \
  "v_bitop3_b32 v246, v246, v213, v243 bitop3:0xf6\n\t" \
  "v_bitop3_b32 v246, v246, v215, v243 bitop3:0xf6\n\t" \
  "v_bitop3_b32 v246, v246, v217, v243 bitop3:0xf6\n\t" \
  "v_bitop3_b32 v246, v246, v219, v243 bitop3:0xf6\n\t" \
  "v_bitop3_b32 v246, v246, v221, v243 bitop3:0xf6\n\t" \
  "v_bitop3_b32 v246, v246, v223, v243 bitop3:0xf6\n\t" \
  "v_cmp_ne_u32 vcc, 0, v246\n\t" \
  "s_cbranch_vccz .Lop_k4\n\t" \
  "s_add_u32 s78, s78, 1\n\t" \
  "s_cmp_lt_u32 s78, %[cap]\n\t" \
  "s_cbranch_scc1 .Lop_r4\n\t" \
  "buffer_store_dword v243, off, %[ws], 0 offset:256 sc1\n\t" \
  "s_branch .Lop_k4\n\t" \
  ".Lop_rd:\n\t" \
  "s_waitcnt vmcnt(0) lgkmcnt(0)\n\t" \
  "s_barrier\n\t" \
  "s_mov_b32 %[pm], 2\n\t" \
  "s_branch .Lop_end\n\t" \
  ".Lop_s99:\n\t" \
  "s_waitcnt vmcnt(0)\n\t" \
  "v_cmp_ne_u32 vcc, %[tag], v246\n\t" \
  "s_and_b32 vcc_lo, vcc_lo, 0xfff\n\t" \
  "s_cbranch_scc0 .Lop_k99\n\t" \
  "s_mov_b32 s78, 0\n\t" \
  ".Lop_r99:\n\t" \
  "buffer_load_dword v246, v235, %[ws], s68 offen sc1\n\t" \
  "s_waitcnt vmcnt(0)\n\t" \
  "v_cmp_ne_u32 vcc, %[tag], v246\n\t" \
  "s_and_b32 vcc_lo, vcc_lo, 0xfff\n\t" \
  "s_cbranch_scc0 .Lop_k99\n\t" \
  "s_add_u32 s78, s78, 1\n\t" \
  "s_cmp_lt_u32 s78, %[cap]\n\t" \
  "s_cbranch_scc1 .Lop_r99\n\t" \
  "buffer_store_dword v243, off, %[ws], 0 offset:256 sc1\n\t" \
  "s_branch .Lop_k99\n\t" \
  ".Lop_k99:\n\t" \
  "s_and_b32 s72, s81, 15\n\t" \
  "s_cbranch_scc0 .Lop_m0\n\t" \
  "s_add_u32 s72, s63, 0\n\t" \
  "buffer_load_dwordx4 a[128:131], v242, %[ws], s72 offen sc1\n\t" \
  "s_add_u32 s72, s64, 32768\n\t" \
  "buffer_load_dwordx4 a[140:143], v242, %[ws], s72 offen sc1\n\t" \
  "s_add_u32 s72, s63, 65536\n\t" \
  "buffer_load_dwordx4 a[152:155], v242, %[ws], s72 offen sc1\n\t" \
  "s_add_u32 s72, s64, 98304\n\t" \
  "buffer_load_dwordx4 a[164:167], v242, %[ws], s72 offen sc1\n\t" \
  ".Lop_m0:\n\t" \
  "s_and_b32 s72, s81, 240\n\t" \
  "s_cbranch_scc0 .Lop_m1\n\t" \
  "s_add_u32 s72, s63, 4096\n\t" \
  "buffer_load_dwordx4 a[132:135], v242, %[ws], s72 offen sc1\n\t" \
  "s_add_u32 s72, s64, 36864\n\t" \
  "buffer_load_dwordx4 a[144:147], v242, %[ws], s72 offen sc1\n\t" \
  "s_add_u32 s72, s63, 69632\n\t" \
  "buffer_load_dwordx4 a[156:159], v242, %[ws], s72 offen sc1\n\t" \
  "s_add_u32 s72, s64, 102400\n\t" \
  "buffer_load_dwordx4 a[168:171], v242, %[ws], s72 offen sc1\n\t" \
  ".Lop_m1:\n\t" \
  "s_and_b32 s72, s81, 3840\n\t" \
  "s_cbranch_scc0 .Lop_m2\n\t" \
  "s_add_u32 s72, s63, 16384\n\t" \
  "buffer_load_dwordx4 a[136:139], v242, %[ws], s72 offen sc1\n\t" \
  "s_add_u32 s72, s64, 49152\n\t" \
  "buffer_load_dwordx4 a[148:151], v242, %[ws], s72 offen sc1\n\t" \
  "s_add_u32 s72, s63, 81920\n\t" \
  "buffer_load_dwordx4 a[160:163], v242, %[ws], s72 offen sc1\n\t" \
  "s_add_u32 s72, s64, 114688\n\t" \
  "buffer_load_dwordx4 a[172:175], v242, %[ws], s72 offen sc1\n\t" \
  ".Lop_m2:\n\t" \
  "s_waitcnt vmcnt(0)\n\t" \
  "s_branch .Lop_d2\n\t" \
  ".Lfz_z:\n\t" \
  ".fill 128, 4, 0xBF800000\n\t" \
  ".subsection 0\n\t" \
  ".Lop_end:\n\t" \
  ""
#define OP_CLOB "v208", "v209", "v210", "v211", "v212", "v214", "v215", "v216", "v217", "v218", "v219", "v220", "v221", "v222", "v223", "v228", "v229", "v230", "v231", "v232", "v233", "v235", "v236", "v238", "v239", "v246", "v248", "v249", "v250", "v251", "s60", "s61", "s62", "s63", "s64", "s65", "s66", "s67", "s68", "s69", "s70", "s71", "s72", "s73", "s74", "s75", "s76", "s77", "s78", "s79", "s80", "s81", "s82", "s83", "s84", "s85", "s92", "s93", "s94", "s95", "vcc", "scc", "memory"
#define ST_ASM "s_nop 1\n\t" "v_mfma_f32_16x16x32_bf16 v[80:83], a[208:211], v[160:163], 0\n\t" "v_mfma_f32_16x16x32_bf16 v[80:83], a[212:215], v[164:167], v[80:83]\n\t" "v_mfma_f32_16x16x32_bf16 v[80:83], a[216:219], v[168:171], v[80:83]\n\t" "v_mfma_f32_16x16x32_bf16 v[80:83], a[220:223], v[172:175], v[80:83]\n\t" "v_dot2_f32_bf16 v64, v160, v160, 0\n\t" "v_dot2_f32_bf16 v65, v164, v164, 0\n\t" "v_dot2_f32_bf16 v66, v168, v168, 0\n\t" "v_dot2_f32_bf16 v67, v172, v172, 0\n\t" "v_dot2_f32_bf16 v64, v161, v161, v64\n\t" "v_dot2_f32_bf16 v65, v165, v165, v65\n\t" "v_dot2_f32_bf16 v66, v169, v169, v66\n\t" "v_dot2_f32_bf16 v67, v173, v173, v67\n\t" "v_dot2_f32_bf16 v64, v162, v162, v64\n\t" "v_dot2_f32_bf16 v65, v166, v166, v65\n\t" "v_dot2_f32_bf16 v66, v170, v170, v66\n\t" "v_dot2_f32_bf16 v67, v174, v174, v67\n\t" "v_dot2_f32_bf16 v64, v163, v163, v64\n\t" "v_dot2_f32_bf16 v65, v167, v167, v65\n\t" "v_dot2_f32_bf16 v66, v171, v171, v66\n\t" "v_dot2_f32_bf16 v67, v175, v175, v67\n\t" "s_nop 2\n\t"
static_assert(W_MODB == 2228224);
constexpr int GSLOT = 49152;
constexpr unsigned KRS = 256;
struct KVP { srd_t srdK, srdV; unsigned vkb[4], vvb[2], vksrc[4], vvsrc[2], sofk; };
struct H4 {
  srd_t srdW;
  unsigned voffX, voffW[4];
  unsigned xb1, wb[2][8];
  unsigned mX, mW, soX, rot;
  unsigned pfd, fo, fl, fvo;
  u32x32 q[4];
};
#define W4(a) "+v"(a[0]), "+v"(a[1]), "+v"(a[2]), "+v"(a[3])
#define W8(a) W4(a), "+v"(a[4]), "+v"(a[5]), "+v"(a[6]), "+v"(a[7])
constexpr unsigned kXT = 65536, kWT = 64 * QD * 2;
DI void w_dma4(unsigned m0, srd_t srd, const unsigned (&vo)[4], unsigned so) {
  asm volatile("s_mov_b32 m0, %0\n\ts_nop 0\n\tbuffer_load_dwordx4 %1, %5, %6 offen lds\n\tbuffer_load_dwordx4 %2, %5, %6 offen offset:1024 lds\n\t"
               "buffer_load_dwordx4 %3, %5, %6 offen offset:2048 lds\n\tbuffer_load_dwordx4 %4, %5, %6 offen offset:3072 lds"
               :: "s"(m0), "v"(vo[0]), "v"(vo[1]), "v"(vo[2]), "v"(vo[3]), "s"(srd), "s"(so) : "memory");
}
DI void w_voffs(unsigned (&vo)[4], int ldw, unsigned wid, unsigned lane, unsigned k0) {
#pragma unroll
  for (int j = 0; j < 4; ++j) {
    const unsigned k = (4 * wid + j) * 4 + (lane >> 4), c16 = lane & 15, sw = (k & 3) | (((k >> 3) & 1) << 2);
    vo[j] = (k ^ (32 * k0)) * ldw * 2 + ((((c16 >> 1) ^ sw) << 1) + (c16 & 1)) * 16 - 1024 * j;
  }
}
DI void h4_init_addr(H4& c, unsigned ldsbase, unsigned wid, unsigned lane, unsigned k0) {
  const unsigned r16 = lane & 15, g = lane >> 4, q = (lane >> 2) & 3, p = lane & 3;
  c.xb1 = ldsbase + GSLOT + wid * 8192 + k0 * 4096 + g * 1024 + r16 * 16;
#pragma unroll
  for (int sl = 0; sl < 2; ++sl)
#pragma unroll
    for (int nb = 0; nb < 8; ++nb) { c.wb[sl][nb] = ldsbase + 2 * sl * GSLOT + 32768 + (8 * g + q) * 256 + ((nb ^ (q | ((g & 1) << 2))) << 5) + p * 8; asm volatile("" : "+v"(c.wb[sl][nb])); }
}
#define FSTP(x)
#define FSTS(x)
#define FQ_STV
#define FQ_STC
#define FQ_ASM \
  "s_mov_b32 s98, 0xffff0000\n\t" \
  ".p2align 3\n\t" \
  "ds_read_b128 v[128:131], v208 offset:0\n\t" \
  "ds_read_b128 v[132:135], v208 offset:16\n\t" \
  "ds_read_b128 v[136:139], v208 offset:256\n\t" \
  "ds_read_b128 v[140:143], v208 offset:272\n\t" \
  "ds_read_b128 v[32:35], v204 offset:49152\n\t" \
  "ds_read_b128 v[36:39], v204 offset:51200\n\t" \
  "ds_read_b128 v[40:43], v204 offset:53248\n\t" \
  "ds_read_b128 v[44:47], v204 offset:55296\n\t" \
  "ds_read_b128 v[144:147], v208 offset:128\n\t" \
  "ds_read_b128 v[148:151], v208 offset:144\n\t" \
  "ds_read_b128 v[152:155], v208 offset:384\n\t" \
  "ds_read_b128 v[156:159], v208 offset:400\n\t" \
  "ds_read_b128 v[48:51], v205 offset:49152\n\t" \
  "ds_read_b128 v[52:55], v205 offset:51200\n\t" \
  "ds_read_b128 v[56:59], v205 offset:53248\n\t" \
  "ds_read_b128 v[60:63], v205 offset:55296\n\t" \
  "v_lshlrev_b32 v176, 16, v0\n\t" \
  "v_and_b32 v177, s98, v0\n\t" \
  "v_lshlrev_b32 v178, 16, v1\n\t" \
  "v_and_b32 v179, s98, v1\n\t" \
  "v_lshlrev_b32 v180, 16, v2\n\t" \
  "v_and_b32 v181, s98, v2\n\t" \
  "v_lshlrev_b32 v182, 16, v3\n\t" \
  "v_and_b32 v183, s98, v3\n\t" \
  "v_pk_fma_f32 v[176:177], v[176:177], v[160:161], v[162:163] op_sel_hi:[1,0,0]\n\t" \
  "v_pk_fma_f32 v[178:179], v[178:179], v[160:161], v[162:163] op_sel_hi:[1,0,0]\n\t" \
  "v_pk_fma_f32 v[180:181], v[180:181], v[160:161], v[162:163] op_sel_hi:[1,0,0]\n\t" \
  "v_pk_fma_f32 v[182:183], v[182:183], v[160:161], v[162:163] op_sel_hi:[1,0,0]\n\t" \
  "s_waitcnt lgkmcnt(12)\n\t" \
  "s_nop 0\n\t" \
  "s_xor_b32 s97, %[pso], 0\n\t" \
  ".p2align 3\n\t" \
  "v_pk_fma_f32 v[176:177], v[176:177], v[128:129], v[136:137]\n\t" \
  "v_pk_fma_f32 v[178:179], v[178:179], v[130:131], v[138:139]\n\t" \
  "v_pk_fma_f32 v[180:181], v[180:181], v[132:133], v[140:141]\n\t" \
  "v_pk_fma_f32 v[182:183], v[182:183], v[134:135], v[142:143]\n\t" \
  "v_cvt_pk_bf16_f32 v0, v176, v177\n\t" \
  "v_cvt_pk_bf16_f32 v1, v178, v179\n\t" \
  "v_cvt_pk_bf16_f32 v2, v180, v181\n\t" \
  "v_cvt_pk_bf16_f32 v3, v182, v183\n\t" \
  "buffer_store_dwordx4 v[0:3], v214, %[ws], s97 offen sc1\n\t" \
  "v_lshlrev_b32 v176, 16, v4\n\t" \
  "v_and_b32 v177, s98, v4\n\t" \
  "v_lshlrev_b32 v178, 16, v5\n\t" \
  "v_and_b32 v179, s98, v5\n\t" \
  "v_lshlrev_b32 v180, 16, v6\n\t" \
  "v_and_b32 v181, s98, v6\n\t" \
  "v_lshlrev_b32 v182, 16, v7\n\t" \
  "v_and_b32 v183, s98, v7\n\t" \
  "v_pk_fma_f32 v[176:177], v[176:177], v[164:165], v[166:167] op_sel_hi:[1,0,0]\n\t" \
  "v_pk_fma_f32 v[178:179], v[178:179], v[164:165], v[166:167] op_sel_hi:[1,0,0]\n\t" \
  "v_pk_fma_f32 v[180:181], v[180:181], v[164:165], v[166:167] op_sel_hi:[1,0,0]\n\t" \
  "v_pk_fma_f32 v[182:183], v[182:183], v[164:165], v[166:167] op_sel_hi:[1,0,0]\n\t" \
  "s_xor_b32 s97, %[pso], 256\n\t" \
  ".p2align 3\n\t" \
  "v_pk_fma_f32 v[176:177], v[176:177], v[128:129], v[136:137]\n\t" \
  "v_pk_fma_f32 v[178:179], v[178:179], v[130:131], v[138:139]\n\t" \
  "v_pk_fma_f32 v[180:181], v[180:181], v[132:133], v[140:141]\n\t" \
  "v_pk_fma_f32 v[182:183], v[182:183], v[134:135], v[142:143]\n\t" \
  "v_cvt_pk_bf16_f32 v4, v176, v177\n\t" \
  "v_cvt_pk_bf16_f32 v5, v178, v179\n\t" \
  "v_cvt_pk_bf16_f32 v6, v180, v181\n\t" \
  "v_cvt_pk_bf16_f32 v7, v182, v183\n\t" \
  "buffer_store_dwordx4 v[4:7], v214, %[ws], s97 offen sc1\n\t" \
  "v_lshlrev_b32 v176, 16, v8\n\t" \
  "v_and_b32 v177, s98, v8\n\t" \
  "v_lshlrev_b32 v178, 16, v9\n\t" \
  "v_and_b32 v179, s98, v9\n\t" \
  "v_lshlrev_b32 v180, 16, v10\n\t" \
  "v_and_b32 v181, s98, v10\n\t" \
  "v_lshlrev_b32 v182, 16, v11\n\t" \
  "v_and_b32 v183, s98, v11\n\t" \
  "v_pk_fma_f32 v[176:177], v[176:177], v[168:169], v[170:171] op_sel_hi:[1,0,0]\n\t" \
  "v_pk_fma_f32 v[178:179], v[178:179], v[168:169], v[170:171] op_sel_hi:[1,0,0]\n\t" \
  "v_pk_fma_f32 v[180:181], v[180:181], v[168:169], v[170:171] op_sel_hi:[1,0,0]\n\t" \
  "v_pk_fma_f32 v[182:183], v[182:183], v[168:169], v[170:171] op_sel_hi:[1,0,0]\n\t" \
  "s_xor_b32 s97, %[pso], 512\n\t" \
  ".p2align 3\n\t" \
  "v_pk_fma_f32 v[176:177], v[176:177], v[128:129], v[136:137]\n\t" \
  "v_pk_fma_f32 v[178:179], v[178:179], v[130:131], v[138:139]\n\t" \
  "v_pk_fma_f32 v[180:181], v[180:181], v[132:133], v[140:141]\n\t" \
  "v_pk_fma_f32 v[182:183], v[182:183], v[134:135], v[142:143]\n\t" \
  "v_cvt_pk_bf16_f32 v8, v176, v177\n\t" \
  "v_cvt_pk_bf16_f32 v9, v178, v179\n\t" \
  "v_cvt_pk_bf16_f32 v10, v180, v181\n\t" \
  "v_cvt_pk_bf16_f32 v11, v182, v183\n\t" \
  "buffer_store_dwordx4 v[8:11], v214, %[ws], s97 offen sc1\n\t" \
  "v_lshlrev_b32 v176, 16, v12\n\t" \
  "v_and_b32 v177, s98, v12\n\t" \
  "v_lshlrev_b32 v178, 16, v13\n\t" \
  "v_and_b32 v179, s98, v13\n\t" \
  "v_lshlrev_b32 v180, 16, v14\n\t" \
  "v_and_b32 v181, s98, v14\n\t" \
  "v_lshlrev_b32 v182, 16, v15\n\t" \
  "v_and_b32 v183, s98, v15\n\t" \
  "v_pk_fma_f32 v[176:177], v[176:177], v[172:173], v[174:175] op_sel_hi:[1,0,0]\n\t" \
  "v_pk_fma_f32 v[178:179], v[178:179], v[172:173], v[174:175] op_sel_hi:[1,0,0]\n\t" \
  "v_pk_fma_f32 v[180:181], v[180:181], v[172:173], v[174:175] op_sel_hi:[1,0,0]\n\t" \
  "v_pk_fma_f32 v[182:183], v[182:183], v[172:173], v[174:175] op_sel_hi:[1,0,0]\n\t" \
  "s_xor_b32 s97, %[pso], 768\n\t" \
  ".p2align 3\n\t" \
  "v_pk_fma_f32 v[176:177], v[176:177], v[128:129], v[136:137]\n\t" \
  "v_pk_fma_f32 v[178:179], v[178:179], v[130:131], v[138:139]\n\t" \
  "v_pk_fma_f32 v[180:181], v[180:181], v[132:133], v[140:141]\n\t" \
  "v_pk_fma_f32 v[182:183], v[182:183], v[134:135], v[142:143]\n\t" \
  "v_cvt_pk_bf16_f32 v12, v176, v177\n\t" \
  "v_cvt_pk_bf16_f32 v13, v178, v179\n\t" \
  "v_cvt_pk_bf16_f32 v14, v180, v181\n\t" \
  "v_cvt_pk_bf16_f32 v15, v182, v183\n\t" \
  "buffer_store_dwordx4 v[12:15], v214, %[ws], s97 offen sc1\n\t" \
  "ds_read_b128 v[128:131], v208 offset:512\n\t" \
  "ds_read_b128 v[132:135], v208 offset:528\n\t" \
  "ds_read_b128 v[136:139], v208 offset:768\n\t" \
  "ds_read_b128 v[140:143], v208 offset:784\n\t" \
  FSTP("s_memrealtime s[94:95]\n\t") \
  FSTP("s_memtime s[92:93]\n\t") \
  "v_lshlrev_b32 v176, 16, v16\n\t" \
  "v_and_b32 v177, s98, v16\n\t" \
  "v_lshlrev_b32 v178, 16, v17\n\t" \
  "v_and_b32 v179, s98, v17\n\t" \
  "v_lshlrev_b32 v180, 16, v18\n\t" \
  "v_and_b32 v181, s98, v18\n\t" \
  "v_lshlrev_b32 v182, 16, v19\n\t" \
  "v_and_b32 v183, s98, v19\n\t" \
  "v_pk_fma_f32 v[176:177], v[176:177], v[160:161], v[162:163] op_sel_hi:[1,0,0]\n\t" \
  "v_pk_fma_f32 v[178:179], v[178:179], v[160:161], v[162:163] op_sel_hi:[1,0,0]\n\t" \
  "v_pk_fma_f32 v[180:181], v[180:181], v[160:161], v[162:163] op_sel_hi:[1,0,0]\n\t" \
  "v_pk_fma_f32 v[182:183], v[182:183], v[160:161], v[162:163] op_sel_hi:[1,0,0]\n\t" \
  "s_waitcnt lgkmcnt(8)\n\t" \
  "s_nop 0\n\t" \
  "v_pk_fma_f32 v[176:177], v[176:177], v[144:145], v[152:153]\n\t" \
  "v_pk_fma_f32 v[178:179], v[178:179], v[146:147], v[154:155]\n\t" \
  "v_pk_fma_f32 v[180:181], v[180:181], v[148:149], v[156:157]\n\t" \
  "v_pk_fma_f32 v[182:183], v[182:183], v[150:151], v[158:159]\n\t" \
  "v_cvt_pk_bf16_f32 v16, v176, v177\n\t" \
  "v_cvt_pk_bf16_f32 v17, v178, v179\n\t" \
  "v_cvt_pk_bf16_f32 v18, v180, v181\n\t" \
  "v_cvt_pk_bf16_f32 v19, v182, v183\n\t" \
  "s_add_u32 m0, %[mx], 98304\n\t" \
  "s_xor_b32 s96, %[c17], 0\n\t" \
  ".p2align 3\n\t" \
  "s_add_u32 s96, s96, %[sl2]\n\t" \
  "buffer_load_dwordx4 v212, %[sx], s96 offen lds\n\t" \
  ".p2align 3\n\t" \
  "v_lshlrev_b32 v176, 16, v20\n\t" \
  "v_and_b32 v177, s98, v20\n\t" \
  "v_lshlrev_b32 v178, 16, v21\n\t" \
  "v_and_b32 v179, s98, v21\n\t" \
  "v_lshlrev_b32 v180, 16, v22\n\t" \
  "v_and_b32 v181, s98, v22\n\t" \
  "v_lshlrev_b32 v182, 16, v23\n\t" \
  "v_and_b32 v183, s98, v23\n\t" \
  "v_pk_fma_f32 v[176:177], v[176:177], v[164:165], v[166:167] op_sel_hi:[1,0,0]\n\t" \
  "v_pk_fma_f32 v[178:179], v[178:179], v[164:165], v[166:167] op_sel_hi:[1,0,0]\n\t" \
  "v_pk_fma_f32 v[180:181], v[180:181], v[164:165], v[166:167] op_sel_hi:[1,0,0]\n\t" \
  "v_pk_fma_f32 v[182:183], v[182:183], v[164:165], v[166:167] op_sel_hi:[1,0,0]\n\t" \
  "v_pk_fma_f32 v[176:177], v[176:177], v[144:145], v[152:153]\n\t" \
  "v_pk_fma_f32 v[178:179], v[178:179], v[146:147], v[154:155]\n\t" \
  "v_pk_fma_f32 v[180:181], v[180:181], v[148:149], v[156:157]\n\t" \
  "v_pk_fma_f32 v[182:183], v[182:183], v[150:151], v[158:159]\n\t" \
  "v_cvt_pk_bf16_f32 v20, v176, v177\n\t" \
  "v_cvt_pk_bf16_f32 v21, v178, v179\n\t" \
  "v_cvt_pk_bf16_f32 v22, v180, v181\n\t" \
  "v_cvt_pk_bf16_f32 v23, v182, v183\n\t" \
  "buffer_load_dwordx4 v213, %[sx], s96 offen offset:1024 lds\n\t" \
  ".p2align 3\n\t" \
  "v_lshlrev_b32 v176, 16, v24\n\t" \
  "v_and_b32 v177, s98, v24\n\t" \
  "v_lshlrev_b32 v178, 16, v25\n\t" \
  "v_and_b32 v179, s98, v25\n\t" \
  "v_lshlrev_b32 v180, 16, v26\n\t" \
  "v_and_b32 v181, s98, v26\n\t" \
  "v_lshlrev_b32 v182, 16, v27\n\t" \
  "v_and_b32 v183, s98, v27\n\t" \
  "v_pk_fma_f32 v[176:177], v[176:177], v[168:169], v[170:171] op_sel_hi:[1,0,0]\n\t" \
  "v_pk_fma_f32 v[178:179], v[178:179], v[168:169], v[170:171] op_sel_hi:[1,0,0]\n\t" \
  "v_pk_fma_f32 v[180:181], v[180:181], v[168:169], v[170:171] op_sel_hi:[1,0,0]\n\t" \
  "v_pk_fma_f32 v[182:183], v[182:183], v[168:169], v[170:171] op_sel_hi:[1,0,0]\n\t" \
  "v_pk_fma_f32 v[176:177], v[176:177], v[144:145], v[152:153]\n\t" \
  "v_pk_fma_f32 v[178:179], v[178:179], v[146:147], v[154:155]\n\t" \
  "v_pk_fma_f32 v[180:181], v[180:181], v[148:149], v[156:157]\n\t" \
  "v_pk_fma_f32 v[182:183], v[182:183], v[150:151], v[158:159]\n\t" \
  "v_cvt_pk_bf16_f32 v24, v176, v177\n\t" \
  "v_cvt_pk_bf16_f32 v25, v178, v179\n\t" \
  "v_cvt_pk_bf16_f32 v26, v180, v181\n\t" \
  "v_cvt_pk_bf16_f32 v27, v182, v183\n\t" \
  "s_add_u32 m0, %[mx], 100352\n\t" \
  "s_xor_b32 s96, %[c17], 131072\n\t" \
  ".p2align 3\n\t" \
  "s_add_u32 s96, s96, %[sl2]\n\t" \
  "buffer_load_dwordx4 v212, %[sx], s96 offen lds\n\t" \
  ".p2align 3\n\t" \
  "v_lshlrev_b32 v176, 16, v28\n\t" \
  "v_and_b32 v177, s98, v28\n\t" \
  "v_lshlrev_b32 v178, 16, v29\n\t" \
  "v_and_b32 v179, s98, v29\n\t" \
  "v_lshlrev_b32 v180, 16, v30\n\t" \
  "v_and_b32 v181, s98, v30\n\t" \
  "v_lshlrev_b32 v182, 16, v31\n\t" \
  "v_and_b32 v183, s98, v31\n\t" \
  "v_pk_fma_f32 v[176:177], v[176:177], v[172:173], v[174:175] op_sel_hi:[1,0,0]\n\t" \
  "v_pk_fma_f32 v[178:179], v[178:179], v[172:173], v[174:175] op_sel_hi:[1,0,0]\n\t" \
  "v_pk_fma_f32 v[180:181], v[180:181], v[172:173], v[174:175] op_sel_hi:[1,0,0]\n\t" \
  "v_pk_fma_f32 v[182:183], v[182:183], v[172:173], v[174:175] op_sel_hi:[1,0,0]\n\t" \
  "v_pk_fma_f32 v[176:177], v[176:177], v[144:145], v[152:153]\n\t" \
  "v_pk_fma_f32 v[178:179], v[178:179], v[146:147], v[154:155]\n\t" \
  "v_pk_fma_f32 v[180:181], v[180:181], v[148:149], v[156:157]\n\t" \
  "v_pk_fma_f32 v[182:183], v[182:183], v[150:151], v[158:159]\n\t" \
  "v_cvt_pk_bf16_f32 v28, v176, v177\n\t" \
  "v_cvt_pk_bf16_f32 v29, v178, v179\n\t" \
  "v_cvt_pk_bf16_f32 v30, v180, v181\n\t" \
  "v_cvt_pk_bf16_f32 v31, v182, v183\n\t" \
  "buffer_load_dwordx4 v213, %[sx], s96 offen offset:1024 lds\n\t" \
  ".p2align 3\n\t" \
  "ds_read_b128 v[144:147], v208 offset:640\n\t" \
  "ds_read_b128 v[148:151], v208 offset:656\n\t" \
  "ds_read_b128 v[152:155], v208 offset:896\n\t" \
  "ds_read_b128 v[156:159], v208 offset:912\n\t" \
  FSTP("v_writelane_b32 %[stv], s94, 28\n\t") \
  FSTP("v_writelane_b32 %[stv], s92, 29\n\t") \
  FSTP("s_memrealtime s[94:95]\n\t") \
  FSTP("s_memtime s[92:93]\n\t") \
  "v_lshlrev_b32 v176, 16, v32\n\t" \
  "v_and_b32 v177, s98, v32\n\t" \
  "v_lshlrev_b32 v178, 16, v33\n\t" \
  "v_and_b32 v179, s98, v33\n\t" \
  "v_lshlrev_b32 v180, 16, v34\n\t" \
  "v_and_b32 v181, s98, v34\n\t" \
  "v_lshlrev_b32 v182, 16, v35\n\t" \
  "v_and_b32 v183, s98, v35\n\t" \
  "v_pk_fma_f32 v[176:177], v[176:177], v[160:161], v[162:163] op_sel_hi:[1,0,0]\n\t" \
  "v_pk_fma_f32 v[178:179], v[178:179], v[160:161], v[162:163] op_sel_hi:[1,0,0]\n\t" \
  "v_pk_fma_f32 v[180:181], v[180:181], v[160:161], v[162:163] op_sel_hi:[1,0,0]\n\t" \
  "v_pk_fma_f32 v[182:183], v[182:183], v[160:161], v[162:163] op_sel_hi:[1,0,0]\n\t" \
  "s_waitcnt lgkmcnt(4)\n\t" \
  "s_nop 0\n\t" \
  "v_pk_fma_f32 v[176:177], v[176:177], v[128:129], v[136:137]\n\t" \
  "v_pk_fma_f32 v[178:179], v[178:179], v[130:131], v[138:139]\n\t" \
  "v_pk_fma_f32 v[180:181], v[180:181], v[132:133], v[140:141]\n\t" \
  "v_pk_fma_f32 v[182:183], v[182:183], v[134:135], v[142:143]\n\t" \
  "v_cvt_pk_bf16_f32 v32, v176, v177\n\t" \
  "v_cvt_pk_bf16_f32 v33, v178, v179\n\t" \
  "v_cvt_pk_bf16_f32 v34, v180, v181\n\t" \
  "v_cvt_pk_bf16_f32 v35, v182, v183\n\t" \
  "s_add_u32 m0, %[mx], 102400\n\t" \
  "s_xor_b32 s96, %[c17], 262144\n\t" \
  ".p2align 3\n\t" \
  "s_add_u32 s96, s96, %[sl2]\n\t" \
  "buffer_load_dwordx4 v212, %[sx], s96 offen lds\n\t" \
  ".p2align 3\n\t" \
  "v_lshlrev_b32 v176, 16, v36\n\t" \
  "v_and_b32 v177, s98, v36\n\t" \
  "v_lshlrev_b32 v178, 16, v37\n\t" \
  "v_and_b32 v179, s98, v37\n\t" \
  "v_lshlrev_b32 v180, 16, v38\n\t" \
  "v_and_b32 v181, s98, v38\n\t" \
  "v_lshlrev_b32 v182, 16, v39\n\t" \
  "v_and_b32 v183, s98, v39\n\t" \
  "v_pk_fma_f32 v[176:177], v[176:177], v[164:165], v[166:167] op_sel_hi:[1,0,0]\n\t" \
  "v_pk_fma_f32 v[178:179], v[178:179], v[164:165], v[166:167] op_sel_hi:[1,0,0]\n\t" \
  "v_pk_fma_f32 v[180:181], v[180:181], v[164:165], v[166:167] op_sel_hi:[1,0,0]\n\t" \
  "v_pk_fma_f32 v[182:183], v[182:183], v[164:165], v[166:167] op_sel_hi:[1,0,0]\n\t" \
  "v_pk_fma_f32 v[176:177], v[176:177], v[128:129], v[136:137]\n\t" \
  "v_pk_fma_f32 v[178:179], v[178:179], v[130:131], v[138:139]\n\t" \
  "v_pk_fma_f32 v[180:181], v[180:181], v[132:133], v[140:141]\n\t" \
  "v_pk_fma_f32 v[182:183], v[182:183], v[134:135], v[142:143]\n\t" \
  "v_cvt_pk_bf16_f32 v36, v176, v177\n\t" \
  "v_cvt_pk_bf16_f32 v37, v178, v179\n\t" \
  "v_cvt_pk_bf16_f32 v38, v180, v181\n\t" \
  "v_cvt_pk_bf16_f32 v39, v182, v183\n\t" \
  "buffer_load_dwordx4 v213, %[sx], s96 offen offset:1024 lds\n\t" \
  ".p2align 3\n\t" \
  "v_lshlrev_b32 v176, 16, v40\n\t" \
  "v_and_b32 v177, s98, v40\n\t" \
  "v_lshlrev_b32 v178, 16, v41\n\t" \
  "v_and_b32 v179, s98, v41\n\t" \
  "v_lshlrev_b32 v180, 16, v42\n\t" \
  "v_and_b32 v181, s98, v42\n\t" \
  "v_lshlrev_b32 v182, 16, v43\n\t" \
  "v_and_b32 v183, s98, v43\n\t" \
  "v_pk_fma_f32 v[176:177], v[176:177], v[168:169], v[170:171] op_sel_hi:[1,0,0]\n\t" \
  "v_pk_fma_f32 v[178:179], v[178:179], v[168:169], v[170:171] op_sel_hi:[1,0,0]\n\t" \
  "v_pk_fma_f32 v[180:181], v[180:181], v[168:169], v[170:171] op_sel_hi:[1,0,0]\n\t" \
  "v_pk_fma_f32 v[182:183], v[182:183], v[168:169], v[170:171] op_sel_hi:[1,0,0]\n\t" \
  "v_pk_fma_f32 v[176:177], v[176:177], v[128:129], v[136:137]\n\t" \
  "v_pk_fma_f32 v[178:179], v[178:179], v[130:131], v[138:139]\n\t" \
  "v_pk_fma_f32 v[180:181], v[180:181], v[132:133], v[140:141]\n\t" \
  "v_pk_fma_f32 v[182:183], v[182:183], v[134:135], v[142:143]\n\t" \
  "v_cvt_pk_bf16_f32 v40, v176, v177\n\t" \
  "v_cvt_pk_bf16_f32 v41, v178, v179\n\t" \
  "v_cvt_pk_bf16_f32 v42, v180, v181\n\t" \
  "v_cvt_pk_bf16_f32 v43, v182, v183\n\t" \
  "s_add_u32 m0, %[mx], 104448\n\t" \
  "s_xor_b32 s96, %[c17], 393216\n\t" \
  ".p2align 3\n\t" \
  "s_add_u32 s96, s96, %[sl2]\n\t" \
  "buffer_load_dwordx4 v212, %[sx], s96 offen lds\n\t" \
  ".p2align 3\n\t" \
  "v_lshlrev_b32 v176, 16, v44\n\t" \
  "v_and_b32 v177, s98, v44\n\t" \
  "v_lshlrev_b32 v178, 16, v45\n\t" \
  "v_and_b32 v179, s98, v45\n\t" \
  "v_lshlrev_b32 v180, 16, v46\n\t" \
  "v_and_b32 v181, s98, v46\n\t" \
  "v_lshlrev_b32 v182, 16, v47\n\t" \
  "v_and_b32 v183, s98, v47\n\t" \
  "v_pk_fma_f32 v[176:177], v[176:177], v[172:173], v[174:175] op_sel_hi:[1,0,0]\n\t" \
  "v_pk_fma_f32 v[178:179], v[178:179], v[172:173], v[174:175] op_sel_hi:[1,0,0]\n\t" \
  "v_pk_fma_f32 v[180:181], v[180:181], v[172:173], v[174:175] op_sel_hi:[1,0,0]\n\t" \
  "v_pk_fma_f32 v[182:183], v[182:183], v[172:173], v[174:175] op_sel_hi:[1,0,0]\n\t" \
  "v_pk_fma_f32 v[176:177], v[176:177], v[128:129], v[136:137]\n\t" \
  "v_pk_fma_f32 v[178:179], v[178:179], v[130:131], v[138:139]\n\t" \
  "v_pk_fma_f32 v[180:181], v[180:181], v[132:133], v[140:141]\n\t" \
  "v_pk_fma_f32 v[182:183], v[182:183], v[134:135], v[142:143]\n\t" \
  "v_cvt_pk_bf16_f32 v44, v176, v177\n\t" \
  "v_cvt_pk_bf16_f32 v45, v178, v179\n\t" \
  "v_cvt_pk_bf16_f32 v46, v180, v181\n\t" \
  "v_cvt_pk_bf16_f32 v47, v182, v183\n\t" \
  "buffer_load_dwordx4 v213, %[sx], s96 offen offset:1024 lds\n\t" \
  ".p2align 3\n\t" \
  "ds_read_b128 v[128:131], v208 offset:1024\n\t" \
  "ds_read_b128 v[132:135], v208 offset:1040\n\t" \
  "ds_read_b128 v[136:139], v208 offset:1280\n\t" \
  "ds_read_b128 v[140:143], v208 offset:1296\n\t" \
  FSTP("v_writelane_b32 %[stv], s94, 30\n\t") \
  FSTP("v_writelane_b32 %[stv], s92, 31\n\t") \
  FSTP("s_memrealtime s[94:95]\n\t") \
  FSTP("s_memtime s[92:93]\n\t") \
  "v_lshlrev_b32 v176, 16, v48\n\t" \
  "v_and_b32 v177, s98, v48\n\t" \
  "v_lshlrev_b32 v178, 16, v49\n\t" \
  "v_and_b32 v179, s98, v49\n\t" \
  "v_lshlrev_b32 v180, 16, v50\n\t" \
  "v_and_b32 v181, s98, v50\n\t" \
  "v_lshlrev_b32 v182, 16, v51\n\t" \
  "v_and_b32 v183, s98, v51\n\t" \
  "v_pk_fma_f32 v[176:177], v[176:177], v[160:161], v[162:163] op_sel_hi:[1,0,0]\n\t" \
  "v_pk_fma_f32 v[178:179], v[178:179], v[160:161], v[162:163] op_sel_hi:[1,0,0]\n\t" \
  "v_pk_fma_f32 v[180:181], v[180:181], v[160:161], v[162:163] op_sel_hi:[1,0,0]\n\t" \
  "v_pk_fma_f32 v[182:183], v[182:183], v[160:161], v[162:163] op_sel_hi:[1,0,0]\n\t" \
  "s_waitcnt lgkmcnt(4)\n\t" \
  "s_nop 0\n\t" \
  "v_pk_fma_f32 v[176:177], v[176:177], v[144:145], v[152:153]\n\t" \
  "v_pk_fma_f32 v[178:179], v[178:179], v[146:147], v[154:155]\n\t" \
  "v_pk_fma_f32 v[180:181], v[180:181], v[148:149], v[156:157]\n\t" \
  "v_pk_fma_f32 v[182:183], v[182:183], v[150:151], v[158:159]\n\t" \
  "v_cvt_pk_bf16_f32 v48, v176, v177\n\t" \
  "v_cvt_pk_bf16_f32 v49, v178, v179\n\t" \
  "v_cvt_pk_bf16_f32 v50, v180, v181\n\t" \
  "v_cvt_pk_bf16_f32 v51, v182, v183\n\t" \
  "s_add_u32 m0, %[mw], 49152\n\t" \
  "s_mov_b32 s96, %[w1]\n\t" \
  ".p2align 3\n\t" \
  "buffer_load_dwordx4 v200, %[sw], s96 offen offset:0 lds\n\t" \
  ".p2align 3\n\t" \
  "ds_read_b64_tr_b16 a[128:129], v184 offset:0\n\t" \
  "ds_read_b64_tr_b16 a[130:131], v184 offset:1024\n\t" \
  "ds_read_b64_tr_b16 a[132:133], v185 offset:0\n\t" \
  "ds_read_b64_tr_b16 a[134:135], v185 offset:1024\n\t" \
  "v_lshlrev_b32 v176, 16, v52\n\t" \
  "v_and_b32 v177, s98, v52\n\t" \
  "v_lshlrev_b32 v178, 16, v53\n\t" \
  "v_and_b32 v179, s98, v53\n\t" \
  "v_lshlrev_b32 v180, 16, v54\n\t" \
  "v_and_b32 v181, s98, v54\n\t" \
  "v_lshlrev_b32 v182, 16, v55\n\t" \
  "v_and_b32 v183, s98, v55\n\t" \
  "v_pk_fma_f32 v[176:177], v[176:177], v[164:165], v[166:167] op_sel_hi:[1,0,0]\n\t" \
  "v_pk_fma_f32 v[178:179], v[178:179], v[164:165], v[166:167] op_sel_hi:[1,0,0]\n\t" \
  "v_pk_fma_f32 v[180:181], v[180:181], v[164:165], v[166:167] op_sel_hi:[1,0,0]\n\t" \
  "v_pk_fma_f32 v[182:183], v[182:183], v[164:165], v[166:167] op_sel_hi:[1,0,0]\n\t" \
  "v_pk_fma_f32 v[176:177], v[176:177], v[144:145], v[152:153]\n\t" \
  "v_pk_fma_f32 v[178:179], v[178:179], v[146:147], v[154:155]\n\t" \
  "v_pk_fma_f32 v[180:181], v[180:181], v[148:149], v[156:157]\n\t" \
  "v_pk_fma_f32 v[182:183], v[182:183], v[150:151], v[158:159]\n\t" \
  "v_cvt_pk_bf16_f32 v52, v176, v177\n\t" \
  "v_cvt_pk_bf16_f32 v53, v178, v179\n\t" \
  "v_cvt_pk_bf16_f32 v54, v180, v181\n\t" \
  "v_cvt_pk_bf16_f32 v55, v182, v183\n\t" \
  "buffer_load_dwordx4 v201, %[sw], s96 offen offset:1024 lds\n\t" \
  ".p2align 3\n\t" \
  "ds_read_b64_tr_b16 a[136:137], v186 offset:0\n\t" \
  "ds_read_b64_tr_b16 a[138:139], v186 offset:1024\n\t" \
  "ds_read_b64_tr_b16 a[140:141], v187 offset:0\n\t" \
  "ds_read_b64_tr_b16 a[142:143], v187 offset:1024\n\t" \
  "v_lshlrev_b32 v176, 16, v56\n\t" \
  "v_and_b32 v177, s98, v56\n\t" \
  "v_lshlrev_b32 v178, 16, v57\n\t" \
  "v_and_b32 v179, s98, v57\n\t" \
  "v_lshlrev_b32 v180, 16, v58\n\t" \
  "v_and_b32 v181, s98, v58\n\t" \
  "v_lshlrev_b32 v182, 16, v59\n\t" \
  "v_and_b32 v183, s98, v59\n\t" \
  "v_pk_fma_f32 v[176:177], v[176:177], v[168:169], v[170:171] op_sel_hi:[1,0,0]\n\t" \
  "v_pk_fma_f32 v[178:179], v[178:179], v[168:169], v[170:171] op_sel_hi:[1,0,0]\n\t" \
  "v_pk_fma_f32 v[180:181], v[180:181], v[168:169], v[170:171] op_sel_hi:[1,0,0]\n\t" \
  "v_pk_fma_f32 v[182:183], v[182:183], v[168:169], v[170:171] op_sel_hi:[1,0,0]\n\t" \
  "v_pk_fma_f32 v[176:177], v[176:177], v[144:145], v[152:153]\n\t" \
  "v_pk_fma_f32 v[178:179], v[178:179], v[146:147], v[154:155]\n\t" \
  "v_pk_fma_f32 v[180:181], v[180:181], v[148:149], v[156:157]\n\t" \
  "v_pk_fma_f32 v[182:183], v[182:183], v[150:151], v[158:159]\n\t" \
  "v_cvt_pk_bf16_f32 v56, v176, v177\n\t" \
  "v_cvt_pk_bf16_f32 v57, v178, v179\n\t" \
  "v_cvt_pk_bf16_f32 v58, v180, v181\n\t" \
  "v_cvt_pk_bf16_f32 v59, v182, v183\n\t" \
  "buffer_load_dwordx4 v202, %[sw], s96 offen offset:2048 lds\n\t" \
  ".p2align 3\n\t" \
  "ds_read_b64_tr_b16 a[144:145], v188 offset:0\n\t" \
  "ds_read_b64_tr_b16 a[146:147], v188 offset:1024\n\t" \
  "ds_read_b64_tr_b16 a[148:149], v189 offset:0\n\t" \
  "ds_read_b64_tr_b16 a[150:151], v189 offset:1024\n\t" \
  "v_lshlrev_b32 v176, 16, v60\n\t" \
  "v_and_b32 v177, s98, v60\n\t" \
  "v_lshlrev_b32 v178, 16, v61\n\t" \
  "v_and_b32 v179, s98, v61\n\t" \
  "v_lshlrev_b32 v180, 16, v62\n\t" \
  "v_and_b32 v181, s98, v62\n\t" \
  "v_lshlrev_b32 v182, 16, v63\n\t" \
  "v_and_b32 v183, s98, v63\n\t" \
  "v_pk_fma_f32 v[176:177], v[176:177], v[172:173], v[174:175] op_sel_hi:[1,0,0]\n\t" \
  "v_pk_fma_f32 v[178:179], v[178:179], v[172:173], v[174:175] op_sel_hi:[1,0,0]\n\t" \
  "v_pk_fma_f32 v[180:181], v[180:181], v[172:173], v[174:175] op_sel_hi:[1,0,0]\n\t" \
  "v_pk_fma_f32 v[182:183], v[182:183], v[172:173], v[174:175] op_sel_hi:[1,0,0]\n\t" \
  "v_pk_fma_f32 v[176:177], v[176:177], v[144:145], v[152:153]\n\t" \
  "v_pk_fma_f32 v[178:179], v[178:179], v[146:147], v[154:155]\n\t" \
  "v_pk_fma_f32 v[180:181], v[180:181], v[148:149], v[156:157]\n\t" \
  "v_pk_fma_f32 v[182:183], v[182:183], v[150:151], v[158:159]\n\t" \
  "v_cvt_pk_bf16_f32 v60, v176, v177\n\t" \
  "v_cvt_pk_bf16_f32 v61, v178, v179\n\t" \
  "v_cvt_pk_bf16_f32 v62, v180, v181\n\t" \
  "v_cvt_pk_bf16_f32 v63, v182, v183\n\t" \
  "buffer_load_dwordx4 v203, %[sw], s96 offen offset:3072 lds\n\t" \
  ".p2align 3\n\t" \
  "ds_read_b64_tr_b16 a[152:153], v190 offset:0\n\t" \
  "ds_read_b64_tr_b16 a[154:155], v190 offset:1024\n\t" \
  "ds_read_b64_tr_b16 a[156:157], v191 offset:0\n\t" \
  "ds_read_b64_tr_b16 a[158:159], v191 offset:1024\n\t" \
  "ds_read_b128 v[144:147], v208 offset:1152\n\t" \
  "ds_read_b128 v[148:151], v208 offset:1168\n\t" \
  "ds_read_b128 v[152:155], v208 offset:1408\n\t" \
  "ds_read_b128 v[156:159], v208 offset:1424\n\t" \
  FSTP("v_writelane_b32 %[stv], s94, 32\n\t") \
  FSTP("v_writelane_b32 %[stv], s92, 33\n\t") \
  FSTP("s_memrealtime s[94:95]\n\t") \
  FSTP("s_memtime s[92:93]\n\t") \
  "s_waitcnt vmcnt(12)\n\t" \
  "s_nop 0\n\t" \
  "s_mov_b64 exec, 1\n\t" \
  "buffer_store_dword v211, off, %[ws], %[fw] sc1\n\t" \
  "s_mov_b64 exec, -1\n\t" \
  FSTP("v_writelane_b32 %[stv], s94, 34\n\t") \
  FSTP("v_writelane_b32 %[stv], s92, 35\n\t") \
  FSTP("s_memrealtime s[94:95]\n\t") \
  FSTP("s_memtime s[92:93]\n\t") \
  "s_waitcnt lgkmcnt(4)\n\t" \
  "v_mfma_f32_16x16x32_bf16 a[0:3], a[128:131], v[0:3], 0\n\t" \
  "s_add_u32 m0, %[mw], 98304\n\t" \
  ".Lhs0:\n\ts_cbranch_execz .Lhs0n\n\t" \
  "ds_read_b64_tr_b16 a[160:161], v184 offset:8192\n\t" \
  "v_mfma_f32_16x16x32_bf16 a[4:7], a[128:131], v[4:7], 0\n\t" \
  "s_mov_b32 s96, %[w2]\n\t" \
  "ds_read_b64_tr_b16 a[162:163], v184 offset:9216\n\t" \
  "v_mfma_f32_16x16x32_bf16 a[8:11], a[128:131], v[8:11], 0\n\t" \
  "buffer_load_dwordx4 v200, %[sw], s96 offen offset:0 lds\n\t" \
  "v_mfma_f32_16x16x32_bf16 a[12:15], a[128:131], v[12:15], 0\n\t" \
  "buffer_load_dwordx4 v201, %[sw], s96 offen offset:1024 lds\n\t" \
  "v_mfma_f32_16x16x32_bf16 a[28:31], a[132:135], v[12:15], 0\n\t" \
  "ds_read_b64_tr_b16 a[164:165], v185 offset:8192\n\t" \
  "v_mfma_f32_16x16x32_bf16 a[24:27], a[132:135], v[8:11], 0\n\t" \
  "ds_read_b64_tr_b16 a[166:167], v185 offset:9216\n\t" \
  "v_mfma_f32_16x16x32_bf16 a[20:23], a[132:135], v[4:7], 0\n\t" \
  "buffer_load_dwordx4 v202, %[sw], s96 offen offset:2048 lds\n\t" \
  "v_mfma_f32_16x16x32_bf16 a[16:19], a[132:135], v[0:3], 0\n\t" \
  "ds_read_b64_tr_b16 a[168:169], v186 offset:8192\n\t" \
  "v_mfma_f32_16x16x32_bf16 a[32:35], a[136:139], v[0:3], 0\n\t" \
  "s_waitcnt vmcnt(8)\n\t" \
  "ds_read_b64_tr_b16 a[170:171], v186 offset:9216\n\t" \
  "ds_read_b128 v[64:67], v206 offset:0\n\t" \
  "v_mfma_f32_16x16x32_bf16 a[36:39], a[136:139], v[4:7], 0\n\t" \
  "buffer_load_dwordx4 v203, %[sw], s96 offen offset:3072 lds\n\t" \
  "v_mfma_f32_16x16x32_bf16 a[40:43], a[136:139], v[8:11], 0\n\t" \
  "s_add_u32 m0, %[mx], 0\n\t" \
  "ds_read_b64_tr_b16 a[172:173], v187 offset:8192\n\t" \
  "ds_read_b128 v[68:71], v206 offset:2048\n\t" \
  "v_mfma_f32_16x16x32_bf16 a[44:47], a[136:139], v[12:15], 0\n\t" \
  "s_xor_b32 s96, %[c17], 0\n\t" \
  "ds_read_b64_tr_b16 a[174:175], v187 offset:9216\n\t" \
  "ds_read_b128 v[72:75], v206 offset:4096\n\t" \
  "v_mfma_f32_16x16x32_bf16 a[60:63], a[140:143], v[12:15], 0\n\t" \
  "s_add_u32 s96, s96, %[sl3]\n\t" \
  "ds_read_b64_tr_b16 a[176:177], v188 offset:8192\n\t" \
  "ds_read_b128 v[76:79], v206 offset:6144\n\t" \
  "s_waitcnt lgkmcnt(0)\n\t" \
  "v_lshlrev_b32 v176, 16, v64\n\t" \
  "v_and_b32 v177, s98, v64\n\t" \
  "v_mfma_f32_16x16x32_bf16 a[56:59], a[140:143], v[8:11], 0\n\t" \
  "buffer_load_dwordx4 v212, %[sx], s96 offen lds\n\t" \
  "v_lshlrev_b32 v178, 16, v65\n\t" \
  "v_and_b32 v179, s98, v65\n\t" \
  "v_lshlrev_b32 v180, 16, v66\n\t" \
  "v_mfma_f32_16x16x32_bf16 a[52:55], a[140:143], v[4:7], 0\n\t" \
  "ds_read_b64_tr_b16 a[178:179], v188 offset:9216\n\t" \
  "v_and_b32 v181, s98, v66\n\t" \
  "v_lshlrev_b32 v182, 16, v67\n\t" \
  "v_and_b32 v183, s98, v67\n\t" \
  "v_fma_f32 v176, v176, v160, v162\n\t" \
  "v_mfma_f32_16x16x32_bf16 a[48:51], a[140:143], v[0:3], 0\n\t" \
  "buffer_load_dwordx4 v213, %[sx], s96 offen offset:1024 lds\n\t" \
  "v_fma_f32 v177, v177, v160, v162\n\t" \
  "v_fma_f32 v178, v178, v160, v162\n\t" \
  "v_fma_f32 v179, v179, v160, v162\n\t" \
  "v_mfma_f32_16x16x32_bf16 a[64:67], a[144:147], v[0:3], 0\n\t" \
  "s_add_u32 m0, %[mx], 2048\n\t" \
  "ds_read_b64_tr_b16 a[180:181], v189 offset:8192\n\t" \
  "v_fma_f32 v180, v180, v160, v162\n\t" \
  "v_fma_f32 v181, v181, v160, v162\n\t" \
  "v_fma_f32 v182, v182, v160, v162\n\t" \
  "v_fma_f32 v183, v183, v160, v162\n\t" \
  "v_mfma_f32_16x16x32_bf16 a[68:71], a[144:147], v[4:7], 0\n\t" \
  "s_xor_b32 s96, %[c17], 131072\n\t" \
  "ds_read_b64_tr_b16 a[182:183], v189 offset:9216\n\t" \
  "v_fma_f32 v176, v176, v128, v136\n\t" \
  "v_fma_f32 v177, v177, v129, v137\n\t" \
  "v_fma_f32 v178, v178, v130, v138\n\t" \
  "v_fma_f32 v179, v179, v131, v139\n\t" \
  "v_mfma_f32_16x16x32_bf16 a[72:75], a[144:147], v[8:11], 0\n\t" \
  "s_add_u32 s96, s96, %[sl3]\n\t" \
  "buffer_load_dwordx4 v212, %[sx], s96 offen lds\n\t" \
  "v_fma_f32 v180, v180, v132, v140\n\t" \
  "v_fma_f32 v181, v181, v133, v141\n\t" \
  "v_mfma_f32_16x16x32_bf16 a[76:79], a[144:147], v[12:15], 0\n\t" \
  "ds_read_b64_tr_b16 a[184:185], v190 offset:8192\n\t" \
  "v_fma_f32 v182, v182, v134, v142\n\t" \
  "v_fma_f32 v183, v183, v135, v143\n\t" \
  "v_cvt_pk_bf16_f32 v64, v176, v177\n\t" \
  "v_cvt_pk_bf16_f32 v65, v178, v179\n\t" \
  "v_mfma_f32_16x16x32_bf16 a[92:95], a[148:151], v[12:15], 0\n\t" \
  "buffer_load_dwordx4 v213, %[sx], s96 offen offset:1024 lds\n\t" \
  "v_cvt_pk_bf16_f32 v66, v180, v181\n\t" \
  "v_cvt_pk_bf16_f32 v67, v182, v183\n\t" \
  "v_lshlrev_b32 v176, 16, v68\n\t" \
  "v_mfma_f32_16x16x32_bf16 a[88:91], a[148:151], v[8:11], 0\n\t" \
  "s_add_u32 m0, %[mx], 4096\n\t" \
  "ds_read_b64_tr_b16 a[186:187], v190 offset:9216\n\t" \
  "v_and_b32 v177, s98, v68\n\t" \
  "v_lshlrev_b32 v178, 16, v69\n\t" \
  "v_and_b32 v179, s98, v69\n\t" \
  "v_lshlrev_b32 v180, 16, v70\n\t" \
  "v_mfma_f32_16x16x32_bf16 a[84:87], a[148:151], v[4:7], 0\n\t" \
  "s_xor_b32 s96, %[c17], 262144\n\t" \
  "s_add_u32 s96, s96, %[sl3]\n\t" \
  "buffer_load_dwordx4 v212, %[sx], s96 offen lds\n\t" \
  "v_and_b32 v181, s98, v70\n\t" \
  "v_mfma_f32_16x16x32_bf16 a[80:83], a[148:151], v[0:3], 0\n\t" \
  "ds_read_b64_tr_b16 a[188:189], v191 offset:8192\n\t" \
  "v_lshlrev_b32 v182, 16, v71\n\t" \
  "v_and_b32 v183, s98, v71\n\t" \
  "v_fma_f32 v176, v176, v164, v166\n\t" \
  "v_fma_f32 v177, v177, v164, v166\n\t" \
  "v_mfma_f32_16x16x32_bf16 a[96:99], a[152:155], v[0:3], 0\n\t" \
  "buffer_load_dwordx4 v213, %[sx], s96 offen offset:1024 lds\n\t" \
  "v_fma_f32 v178, v178, v164, v166\n\t" \
  "v_fma_f32 v179, v179, v164, v166\n\t" \
  "v_fma_f32 v180, v180, v164, v166\n\t" \
  "v_mfma_f32_16x16x32_bf16 a[100:103], a[152:155], v[4:7], 0\n\t" \
  "s_add_u32 m0, %[mx], 6144\n\t" \
  "ds_read_b64_tr_b16 a[190:191], v191 offset:9216\n\t" \
  "v_fma_f32 v181, v181, v164, v166\n\t" \
  "v_fma_f32 v182, v182, v164, v166\n\t" \
  "v_fma_f32 v183, v183, v164, v166\n\t" \
  "v_fma_f32 v176, v176, v128, v136\n\t" \
  "v_mfma_f32_16x16x32_bf16 a[104:107], a[152:155], v[8:11], 0\n\t" \
  "s_xor_b32 s96, %[c17], 393216\n\t" \
  "s_add_u32 s96, s96, %[sl3]\n\t" \
  "buffer_load_dwordx4 v212, %[sx], s96 offen lds\n\t" \
  "v_fma_f32 v177, v177, v129, v137\n\t" \
  "v_mfma_f32_16x16x32_bf16 a[108:111], a[152:155], v[12:15], 0\n\t" \
  "v_fma_f32 v178, v178, v130, v138\n\t" \
  "v_fma_f32 v179, v179, v131, v139\n\t" \
  "v_fma_f32 v180, v180, v132, v140\n\t" \
  "v_fma_f32 v181, v181, v133, v141\n\t" \
  "v_fma_f32 v182, v182, v134, v142\n\t" \
  "v_mfma_f32_16x16x32_bf16 a[124:127], a[156:159], v[12:15], 0\n\t" \
  "buffer_load_dwordx4 v213, %[sx], s96 offen offset:1024 lds\n\t" \
  "v_fma_f32 v183, v183, v135, v143\n\t" \
  "v_cvt_pk_bf16_f32 v68, v176, v177\n\t" \
  "v_cvt_pk_bf16_f32 v69, v178, v179\n\t" \
  "v_mfma_f32_16x16x32_bf16 a[120:123], a[156:159], v[8:11], 0\n\t" \
  "v_cvt_pk_bf16_f32 v70, v180, v181\n\t" \
  "v_cvt_pk_bf16_f32 v71, v182, v183\n\t" \
  "v_lshlrev_b32 v176, 16, v72\n\t" \
  "v_and_b32 v177, s98, v72\n\t" \
  "v_mfma_f32_16x16x32_bf16 a[116:119], a[156:159], v[4:7], 0\n\t" \
  "v_lshlrev_b32 v178, 16, v73\n\t" \
  "v_and_b32 v179, s98, v73\n\t" \
  "v_lshlrev_b32 v180, 16, v74\n\t" \
  "v_and_b32 v181, s98, v74\n\t" \
  "v_lshlrev_b32 v182, 16, v75\n\t" \
  "v_and_b32 v183, s98, v75\n\t" \
  "v_mfma_f32_16x16x32_bf16 a[112:115], a[156:159], v[0:3], 0\n\t" \
  FSTP("v_writelane_b32 %[stv], s94, 36\n\t") \
  FSTP("v_writelane_b32 %[stv], s92, 37\n\t") \
  FSTP("s_memrealtime s[94:95]\n\t") \
  FSTP("s_memtime s[92:93]\n\t") \
  "s_waitcnt vmcnt(13) lgkmcnt(0)\n\t" \
  "s_barrier\n\t" \
  "v_mfma_f32_16x16x32_bf16 a[0:3], a[160:163], v[16:19], a[0:3]\n\t" \
  "s_add_u32 m0, %[mw], 0\n\t" \
  "ds_read_b64_tr_b16 a[128:129], v184 offset:49152\n\t" \
  "v_fma_f32 v176, v176, v168, v170\n\t" \
  "v_fma_f32 v177, v177, v168, v170\n\t" \
  "v_mfma_f32_16x16x32_bf16 a[4:7], a[160:163], v[20:23], a[4:7]\n\t" \
  "s_mov_b32 s96, %[w3]\n\t" \
  "ds_read_b64_tr_b16 a[130:131], v184 offset:50176\n\t" \
  "v_fma_f32 v178, v178, v168, v170\n\t" \
  "v_fma_f32 v179, v179, v168, v170\n\t" \
  "v_mfma_f32_16x16x32_bf16 a[8:11], a[160:163], v[24:27], a[8:11]\n\t" \
  "ds_read_b64_tr_b16 a[132:133], v185 offset:49152\n\t" \
  "v_fma_f32 v180, v180, v168, v170\n\t" \
  "v_fma_f32 v181, v181, v168, v170\n\t" \
  "v_mfma_f32_16x16x32_bf16 a[12:15], a[160:163], v[28:31], a[12:15]\n\t" \
  "ds_read_b64_tr_b16 a[134:135], v185 offset:50176\n\t" \
  "v_fma_f32 v182, v182, v168, v170\n\t" \
  "v_fma_f32 v183, v183, v168, v170\n\t" \
  "v_mfma_f32_16x16x32_bf16 a[28:31], a[164:167], v[28:31], a[28:31]\n\t" \
  "buffer_load_dwordx4 v200, %[sw], s96 offen offset:0 lds\n\t" \
  "v_fma_f32 v176, v176, v128, v136\n\t" \
  "v_mfma_f32_16x16x32_bf16 a[24:27], a[164:167], v[24:27], a[24:27]\n\t" \
  "ds_read_b64_tr_b16 a[136:137], v186 offset:49152\n\t" \
  "v_fma_f32 v177, v177, v129, v137\n\t" \
  "v_fma_f32 v178, v178, v130, v138\n\t" \
  "v_mfma_f32_16x16x32_bf16 a[20:23], a[164:167], v[20:23], a[20:23]\n\t" \
  "ds_read_b64_tr_b16 a[138:139], v186 offset:50176\n\t" \
  "v_fma_f32 v179, v179, v131, v139\n\t" \
  "v_fma_f32 v180, v180, v132, v140\n\t" \
  "v_mfma_f32_16x16x32_bf16 a[16:19], a[164:167], v[16:19], a[16:19]\n\t" \
  "buffer_load_dwordx4 v201, %[sw], s96 offen offset:1024 lds\n\t" \
  "v_fma_f32 v181, v181, v133, v141\n\t" \
  "v_mfma_f32_16x16x32_bf16 a[32:35], a[168:171], v[16:19], a[32:35]\n\t" \
  "ds_read_b64_tr_b16 a[140:141], v187 offset:49152\n\t" \
  "v_fma_f32 v182, v182, v134, v142\n\t" \
  "v_fma_f32 v183, v183, v135, v143\n\t" \
  "v_mfma_f32_16x16x32_bf16 a[36:39], a[168:171], v[20:23], a[36:39]\n\t" \
  "ds_read_b64_tr_b16 a[142:143], v187 offset:50176\n\t" \
  "v_cvt_pk_bf16_f32 v72, v176, v177\n\t" \
  "v_cvt_pk_bf16_f32 v73, v178, v179\n\t" \
  "v_mfma_f32_16x16x32_bf16 a[40:43], a[168:171], v[24:27], a[40:43]\n\t" \
  "ds_read_b64_tr_b16 a[144:145], v188 offset:49152\n\t" \
  "v_cvt_pk_bf16_f32 v74, v180, v181\n\t" \
  "v_cvt_pk_bf16_f32 v75, v182, v183\n\t" \
  "v_mfma_f32_16x16x32_bf16 a[44:47], a[168:171], v[28:31], a[44:47]\n\t" \
  "buffer_load_dwordx4 v202, %[sw], s96 offen offset:2048 lds\n\t" \
  "v_mfma_f32_16x16x32_bf16 a[60:63], a[172:175], v[28:31], a[60:63]\n\t" \
  "ds_read_b64_tr_b16 a[146:147], v188 offset:50176\n\t" \
  "ds_read_b128 v[80:83], v207 offset:0\n\t" \
  "v_mfma_f32_16x16x32_bf16 a[56:59], a[172:175], v[24:27], a[56:59]\n\t" \
  "ds_read_b64_tr_b16 a[148:149], v189 offset:49152\n\t" \
  "ds_read_b128 v[84:87], v207 offset:2048\n\t" \
  "v_mfma_f32_16x16x32_bf16 a[52:55], a[172:175], v[20:23], a[52:55]\n\t" \
  "ds_read_b64_tr_b16 a[150:151], v189 offset:50176\n\t" \
  "ds_read_b128 v[88:91], v207 offset:4096\n\t" \
  "v_mfma_f32_16x16x32_bf16 a[48:51], a[172:175], v[16:19], a[48:51]\n\t" \
  "ds_read_b64_tr_b16 a[152:153], v190 offset:49152\n\t" \
  "ds_read_b128 v[92:95], v207 offset:6144\n\t" \
  "v_lshlrev_b32 v176, 16, v76\n\t" \
  "v_and_b32 v177, s98, v76\n\t" \
  "v_mfma_f32_16x16x32_bf16 a[64:67], a[176:179], v[16:19], a[64:67]\n\t" \
  "buffer_load_dwordx4 v203, %[sw], s96 offen offset:3072 lds\n\t" \
  "v_lshlrev_b32 v178, 16, v77\n\t" \
  "v_and_b32 v179, s98, v77\n\t" \
  "v_mfma_f32_16x16x32_bf16 a[68:71], a[176:179], v[20:23], a[68:71]\n\t" \
  "ds_read_b64_tr_b16 a[154:155], v190 offset:50176\n\t" \
  "v_lshlrev_b32 v180, 16, v78\n\t" \
  "v_and_b32 v181, s98, v78\n\t" \
  "v_lshlrev_b32 v182, 16, v79\n\t" \
  "v_mfma_f32_16x16x32_bf16 a[72:75], a[176:179], v[24:27], a[72:75]\n\t" \
  "ds_read_b64_tr_b16 a[156:157], v191 offset:49152\n\t" \
  "v_and_b32 v183, s98, v79\n\t" \
  "v_fma_f32 v176, v176, v172, v174\n\t" \
  "v_fma_f32 v177, v177, v172, v174\n\t" \
  "v_mfma_f32_16x16x32_bf16 a[76:79], a[176:179], v[28:31], a[76:79]\n\t" \
  "ds_read_b64_tr_b16 a[158:159], v191 offset:50176\n\t" \
  "v_fma_f32 v178, v178, v172, v174\n\t" \
  "v_fma_f32 v179, v179, v172, v174\n\t" \
  "v_mfma_f32_16x16x32_bf16 a[92:95], a[180:183], v[28:31], a[92:95]\n\t" \
  "v_fma_f32 v180, v180, v172, v174\n\t" \
  "v_fma_f32 v181, v181, v172, v174\n\t" \
  "v_fma_f32 v182, v182, v172, v174\n\t" \
  "v_mfma_f32_16x16x32_bf16 a[88:91], a[180:183], v[24:27], a[88:91]\n\t" \
  "v_fma_f32 v183, v183, v172, v174\n\t" \
  "v_fma_f32 v176, v176, v128, v136\n\t" \
  "v_fma_f32 v177, v177, v129, v137\n\t" \
  "v_mfma_f32_16x16x32_bf16 a[84:87], a[180:183], v[20:23], a[84:87]\n\t" \
  "global_load_dword %[pf], v215, %[wq]\n\t" \
  "v_fma_f32 v178, v178, v130, v138\n\t" \
  "v_mfma_f32_16x16x32_bf16 a[80:83], a[180:183], v[16:19], a[80:83]\n\t" \
  "v_fma_f32 v179, v179, v131, v139\n\t" \
  "v_fma_f32 v180, v180, v132, v140\n\t" \
  "v_fma_f32 v181, v181, v133, v141\n\t" \
  "v_mfma_f32_16x16x32_bf16 a[96:99], a[184:187], v[16:19], a[96:99]\n\t" \
  "v_fma_f32 v182, v182, v134, v142\n\t" \
  "v_fma_f32 v183, v183, v135, v143\n\t" \
  "v_cvt_pk_bf16_f32 v76, v176, v177\n\t" \
  "v_mfma_f32_16x16x32_bf16 a[100:103], a[184:187], v[20:23], a[100:103]\n\t" \
  "v_cvt_pk_bf16_f32 v77, v178, v179\n\t" \
  "v_cvt_pk_bf16_f32 v78, v180, v181\n\t" \
  "v_cvt_pk_bf16_f32 v79, v182, v183\n\t" \
  "v_mfma_f32_16x16x32_bf16 a[104:107], a[184:187], v[24:27], a[104:107]\n\t" \
  "ds_read_b128 v[128:131], v208 offset:1536\n\t" \
  "ds_read_b128 v[132:135], v208 offset:1552\n\t" \
  "v_mfma_f32_16x16x32_bf16 a[108:111], a[184:187], v[28:31], a[108:111]\n\t" \
  "ds_read_b128 v[136:139], v208 offset:1792\n\t" \
  "ds_read_b128 v[140:143], v208 offset:1808\n\t" \
  "s_waitcnt lgkmcnt(7)\n\t" \
  "v_lshlrev_b32 v176, 16, v80\n\t" \
  "v_mfma_f32_16x16x32_bf16 a[124:127], a[188:191], v[28:31], a[124:127]\n\t" \
  "v_and_b32 v177, s98, v80\n\t" \
  "v_lshlrev_b32 v178, 16, v81\n\t" \
  "v_and_b32 v179, s98, v81\n\t" \
  "v_lshlrev_b32 v180, 16, v82\n\t" \
  "v_mfma_f32_16x16x32_bf16 a[120:123], a[188:191], v[24:27], a[120:123]\n\t" \
  "v_and_b32 v181, s98, v82\n\t" \
  "v_lshlrev_b32 v182, 16, v83\n\t" \
  "v_and_b32 v183, s98, v83\n\t" \
  "v_mfma_f32_16x16x32_bf16 a[116:119], a[188:191], v[20:23], a[116:119]\n\t" \
  "v_fma_f32 v176, v176, v160, v162\n\t" \
  "v_fma_f32 v177, v177, v160, v162\n\t" \
  "v_fma_f32 v178, v178, v160, v162\n\t" \
  "v_fma_f32 v179, v179, v160, v162\n\t" \
  "v_mfma_f32_16x16x32_bf16 a[112:115], a[188:191], v[16:19], a[112:115]\n\t" \
  FSTP("v_writelane_b32 %[stv], s94, 38\n\t") \
  FSTP("v_writelane_b32 %[stv], s92, 39\n\t") \
  FSTP("s_memrealtime s[94:95]\n\t") \
  FSTP("s_memtime s[92:93]\n\t") \
  "s_waitcnt lgkmcnt(4)\n\t" \
  "v_mfma_f32_16x16x32_bf16 a[0:3], a[128:131], v[32:35], a[0:3]\n\t" \
  "global_load_dword %[pf], v217, %[kv]\n\t" \
  "v_fma_f32 v180, v180, v160, v162\n\t" \
  "v_mfma_f32_16x16x32_bf16 a[4:7], a[128:131], v[36:39], a[4:7]\n\t" \
  "ds_read_b64_tr_b16 a[160:161], v184 offset:57344\n\t" \
  "v_fma_f32 v181, v181, v160, v162\n\t" \
  "v_fma_f32 v182, v182, v160, v162\n\t" \
  "v_mfma_f32_16x16x32_bf16 a[8:11], a[128:131], v[40:43], a[8:11]\n\t" \
  "ds_read_b64_tr_b16 a[162:163], v184 offset:58368\n\t" \
  "v_fma_f32 v183, v183, v160, v162\n\t" \
  "v_fma_f32 v176, v176, v144, v152\n\t" \
  "v_mfma_f32_16x16x32_bf16 a[12:15], a[128:131], v[44:47], a[12:15]\n\t" \
  "ds_read_b64_tr_b16 a[164:165], v185 offset:57344\n\t" \
  "v_fma_f32 v177, v177, v145, v153\n\t" \
  "v_fma_f32 v178, v178, v146, v154\n\t" \
  "v_mfma_f32_16x16x32_bf16 a[28:31], a[132:135], v[44:47], a[28:31]\n\t" \
  "ds_read_b64_tr_b16 a[166:167], v185 offset:58368\n\t" \
  "v_fma_f32 v179, v179, v147, v155\n\t" \
  "v_fma_f32 v180, v180, v148, v156\n\t" \
  "v_mfma_f32_16x16x32_bf16 a[24:27], a[132:135], v[40:43], a[24:27]\n\t" \
  "ds_read_b64_tr_b16 a[168:169], v186 offset:57344\n\t" \
  "v_fma_f32 v181, v181, v149, v157\n\t" \
  "v_fma_f32 v182, v182, v150, v158\n\t" \
  "v_mfma_f32_16x16x32_bf16 a[20:23], a[132:135], v[36:39], a[20:23]\n\t" \
  "ds_read_b64_tr_b16 a[170:171], v186 offset:58368\n\t" \
  "v_fma_f32 v183, v183, v151, v159\n\t" \
  "v_cvt_pk_bf16_f32 v80, v176, v177\n\t" \
  "v_mfma_f32_16x16x32_bf16 a[16:19], a[132:135], v[32:35], a[16:19]\n\t" \
  "ds_read_b64_tr_b16 a[172:173], v187 offset:57344\n\t" \
  "v_cvt_pk_bf16_f32 v81, v178, v179\n\t" \
  "v_cvt_pk_bf16_f32 v82, v180, v181\n\t" \
  "v_mfma_f32_16x16x32_bf16 a[32:35], a[136:139], v[32:35], a[32:35]\n\t" \
  "ds_read_b64_tr_b16 a[174:175], v187 offset:58368\n\t" \
  "v_cvt_pk_bf16_f32 v83, v182, v183\n\t" \
  "v_lshlrev_b32 v176, 16, v84\n\t" \
  "v_mfma_f32_16x16x32_bf16 a[36:39], a[136:139], v[36:39], a[36:39]\n\t" \
  "ds_read_b64_tr_b16 a[176:177], v188 offset:57344\n\t" \
  "v_and_b32 v177, s98, v84\n\t" \
  "v_lshlrev_b32 v178, 16, v85\n\t" \
  "v_mfma_f32_16x16x32_bf16 a[40:43], a[136:139], v[40:43], a[40:43]\n\t" \
  "ds_read_b64_tr_b16 a[178:179], v188 offset:58368\n\t" \
  "v_and_b32 v179, s98, v85\n\t" \
  "v_lshlrev_b32 v180, 16, v86\n\t" \
  "v_mfma_f32_16x16x32_bf16 a[44:47], a[136:139], v[44:47], a[44:47]\n\t" \
  "ds_read_b64_tr_b16 a[180:181], v189 offset:57344\n\t" \
  "v_and_b32 v181, s98, v86\n\t" \
  "v_lshlrev_b32 v182, 16, v87\n\t" \
  "v_mfma_f32_16x16x32_bf16 a[60:63], a[140:143], v[44:47], a[60:63]\n\t" \
  "ds_read_b64_tr_b16 a[182:183], v189 offset:58368\n\t" \
  "v_and_b32 v183, s98, v87\n\t" \
  "v_fma_f32 v176, v176, v164, v166\n\t" \
  "v_mfma_f32_16x16x32_bf16 a[56:59], a[140:143], v[40:43], a[56:59]\n\t" \
  "ds_read_b64_tr_b16 a[184:185], v190 offset:57344\n\t" \
  "v_fma_f32 v177, v177, v164, v166\n\t" \
  "v_fma_f32 v178, v178, v164, v166\n\t" \
  "v_mfma_f32_16x16x32_bf16 a[52:55], a[140:143], v[36:39], a[52:55]\n\t" \
  "buffer_store_short v218, v219, %[ws], 0 offen sc1\n\t" \
  "v_fma_f32 v179, v179, v164, v166\n\t" \
  "v_mfma_f32_16x16x32_bf16 a[48:51], a[140:143], v[32:35], a[48:51]\n\t" \
  "ds_read_b64_tr_b16 a[186:187], v190 offset:58368\n\t" \
  "v_fma_f32 v180, v180, v164, v166\n\t" \
  "v_fma_f32 v181, v181, v164, v166\n\t" \
  "v_mfma_f32_16x16x32_bf16 a[64:67], a[144:147], v[32:35], a[64:67]\n\t" \
  "ds_read_b64_tr_b16 a[188:189], v191 offset:57344\n\t" \
  "v_fma_f32 v182, v182, v164, v166\n\t" \
  "v_fma_f32 v183, v183, v164, v166\n\t" \
  "v_mfma_f32_16x16x32_bf16 a[68:71], a[144:147], v[36:39], a[68:71]\n\t" \
  "ds_read_b64_tr_b16 a[190:191], v191 offset:58368\n\t" \
  "v_fma_f32 v176, v176, v144, v152\n\t" \
  "v_fma_f32 v177, v177, v145, v153\n\t" \
  "v_mfma_f32_16x16x32_bf16 a[72:75], a[144:147], v[40:43], a[72:75]\n\t" \
  "v_fma_f32 v178, v178, v146, v154\n\t" \
  "v_fma_f32 v179, v179, v147, v155\n\t" \
  "v_fma_f32 v180, v180, v148, v156\n\t" \
  "v_mfma_f32_16x16x32_bf16 a[76:79], a[144:147], v[44:47], a[76:79]\n\t" \
  "v_fma_f32 v181, v181, v149, v157\n\t" \
  "v_fma_f32 v182, v182, v150, v158\n\t" \
  "v_fma_f32 v183, v183, v151, v159\n\t" \
  "v_mfma_f32_16x16x32_bf16 a[92:95], a[148:151], v[44:47], a[92:95]\n\t" \
  "v_cvt_pk_bf16_f32 v84, v176, v177\n\t" \
  "v_cvt_pk_bf16_f32 v85, v178, v179\n\t" \
  "v_cvt_pk_bf16_f32 v86, v180, v181\n\t" \
  "v_mfma_f32_16x16x32_bf16 a[88:91], a[148:151], v[40:43], a[88:91]\n\t" \
  "v_cvt_pk_bf16_f32 v87, v182, v183\n\t" \
  "v_lshlrev_b32 v176, 16, v88\n\t" \
  "v_and_b32 v177, s98, v88\n\t" \
  "v_mfma_f32_16x16x32_bf16 a[84:87], a[148:151], v[36:39], a[84:87]\n\t" \
  "v_lshlrev_b32 v178, 16, v89\n\t" \
  "v_and_b32 v179, s98, v89\n\t" \
  "v_lshlrev_b32 v180, 16, v90\n\t" \
  "v_mfma_f32_16x16x32_bf16 a[80:83], a[148:151], v[32:35], a[80:83]\n\t" \
  "v_and_b32 v181, s98, v90\n\t" \
  "v_lshlrev_b32 v182, 16, v91\n\t" \
  "v_and_b32 v183, s98, v91\n\t" \
  "v_mfma_f32_16x16x32_bf16 a[96:99], a[152:155], v[32:35], a[96:99]\n\t" \
  "v_fma_f32 v176, v176, v168, v170\n\t" \
  "v_fma_f32 v177, v177, v168, v170\n\t" \
  "v_fma_f32 v178, v178, v168, v170\n\t" \
  "v_mfma_f32_16x16x32_bf16 a[100:103], a[152:155], v[36:39], a[100:103]\n\t" \
  "v_fma_f32 v179, v179, v168, v170\n\t" \
  "v_fma_f32 v180, v180, v168, v170\n\t" \
  "v_fma_f32 v181, v181, v168, v170\n\t" \
  "v_mfma_f32_16x16x32_bf16 a[104:107], a[152:155], v[40:43], a[104:107]\n\t" \
  "v_fma_f32 v182, v182, v168, v170\n\t" \
  "v_fma_f32 v183, v183, v168, v170\n\t" \
  "v_mfma_f32_16x16x32_bf16 a[108:111], a[152:155], v[44:47], a[108:111]\n\t" \
  "v_fma_f32 v176, v176, v144, v152\n\t" \
  "v_fma_f32 v177, v177, v145, v153\n\t" \
  "v_mfma_f32_16x16x32_bf16 a[124:127], a[156:159], v[44:47], a[124:127]\n\t" \
  "v_fma_f32 v178, v178, v146, v154\n\t" \
  "v_fma_f32 v179, v179, v147, v155\n\t" \
  "v_mfma_f32_16x16x32_bf16 a[120:123], a[156:159], v[40:43], a[120:123]\n\t" \
  "v_fma_f32 v180, v180, v148, v156\n\t" \
  "v_fma_f32 v181, v181, v149, v157\n\t" \
  "v_mfma_f32_16x16x32_bf16 a[116:119], a[156:159], v[36:39], a[116:119]\n\t" \
  "v_fma_f32 v182, v182, v150, v158\n\t" \
  "v_fma_f32 v183, v183, v151, v159\n\t" \
  "v_mfma_f32_16x16x32_bf16 a[112:115], a[156:159], v[32:35], a[112:115]\n\t" \
  FSTP("v_writelane_b32 %[stv], s94, 40\n\t") \
  FSTP("v_writelane_b32 %[stv], s92, 41\n\t") \
  FSTP("s_memrealtime s[94:95]\n\t") \
  FSTP("s_memtime s[92:93]\n\t") \
  "s_waitcnt vmcnt(15) lgkmcnt(0)\n\t" \
  "s_barrier\n\t" \
  "v_mfma_f32_16x16x32_bf16 a[0:3], a[160:163], v[48:51], a[0:3]\n\t" \
  "buffer_load_dword %[fl], v210, %[ws], %[fo] offen sc1\n\t" \
  "v_cvt_pk_bf16_f32 v88, v176, v177\n\t" \
  "v_cvt_pk_bf16_f32 v89, v178, v179\n\t" \
  "v_mfma_f32_16x16x32_bf16 a[4:7], a[160:163], v[52:55], a[4:7]\n\t" \
  "s_add_u32 s96, %[rot19], 2097152\n\t" \
  "ds_read_b64_tr_b16 a[128:129], v192 offset:0\n\t" \
  "v_cvt_pk_bf16_f32 v90, v180, v181\n\t" \
  "v_cvt_pk_bf16_f32 v91, v182, v183\n\t" \
  "s_waitcnt vmcnt(8)\n\t" \
  "v_mfma_f32_16x16x32_bf16 a[8:11], a[160:163], v[56:59], a[8:11]\n\t" \
  "s_and_b32 s96, s96, 0x780000\n\t" \
  "ds_read_b64_tr_b16 a[130:131], v192 offset:1024\n\t" \
  "ds_read_b128 v[96:99], v204 offset:0\n\t" \
  "v_mfma_f32_16x16x32_bf16 a[12:15], a[160:163], v[60:63], a[12:15]\n\t" \
  "s_add_u32 m0, %[mw], 49152\n\t" \
  "ds_read_b64_tr_b16 a[132:133], v193 offset:0\n\t" \
  "ds_read_b128 v[100:103], v204 offset:2048\n\t" \
  "v_mfma_f32_16x16x32_bf16 a[28:31], a[164:167], v[60:63], a[28:31]\n\t" \
  "buffer_load_dwordx4 v200, %[sw], s96 offen offset:0 lds\n\t" \
  "v_mfma_f32_16x16x32_bf16 a[24:27], a[164:167], v[56:59], a[24:27]\n\t" \
  "ds_read_b64_tr_b16 a[134:135], v193 offset:1024\n\t" \
  "ds_read_b128 v[104:107], v204 offset:4096\n\t" \
  "v_mfma_f32_16x16x32_bf16 a[20:23], a[164:167], v[52:55], a[20:23]\n\t" \
  "ds_read_b64_tr_b16 a[136:137], v194 offset:0\n\t" \
  "ds_read_b128 v[108:111], v204 offset:6144\n\t" \
  "v_lshlrev_b32 v176, 16, v92\n\t" \
  "v_and_b32 v177, s98, v92\n\t" \
  "v_mfma_f32_16x16x32_bf16 a[16:19], a[164:167], v[48:51], a[16:19]\n\t" \
  "ds_read_b64_tr_b16 a[138:139], v194 offset:1024\n\t" \
  "v_lshlrev_b32 v178, 16, v93\n\t" \
  "v_and_b32 v179, s98, v93\n\t" \
  "v_lshlrev_b32 v180, 16, v94\n\t" \
  "v_mfma_f32_16x16x32_bf16 a[32:35], a[168:171], v[48:51], a[32:35]\n\t" \
  "ds_read_b64_tr_b16 a[140:141], v195 offset:0\n\t" \
  "v_and_b32 v181, s98, v94\n\t" \
  "v_lshlrev_b32 v182, 16, v95\n\t" \
  "v_and_b32 v183, s98, v95\n\t" \
  "v_mfma_f32_16x16x32_bf16 a[36:39], a[168:171], v[52:55], a[36:39]\n\t" \
  "buffer_load_dwordx4 v201, %[sw], s96 offen offset:1024 lds\n\t" \
  "v_fma_f32 v176, v176, v172, v174\n\t" \
  "v_fma_f32 v177, v177, v172, v174\n\t" \
  "v_mfma_f32_16x16x32_bf16 a[40:43], a[168:171], v[56:59], a[40:43]\n\t" \
  "ds_read_b64_tr_b16 a[142:143], v195 offset:1024\n\t" \
  "v_fma_f32 v178, v178, v172, v174\n\t" \
  "v_fma_f32 v179, v179, v172, v174\n\t" \
  "v_fma_f32 v180, v180, v172, v174\n\t" \
  "v_mfma_f32_16x16x32_bf16 a[44:47], a[168:171], v[60:63], a[44:47]\n\t" \
  "ds_read_b64_tr_b16 a[144:145], v196 offset:0\n\t" \
  "v_fma_f32 v181, v181, v172, v174\n\t" \
  "v_fma_f32 v182, v182, v172, v174\n\t" \
  "v_fma_f32 v183, v183, v172, v174\n\t" \
  "v_mfma_f32_16x16x32_bf16 a[60:63], a[172:175], v[60:63], a[60:63]\n\t" \
  "ds_read_b64_tr_b16 a[146:147], v196 offset:1024\n\t" \
  "v_fma_f32 v176, v176, v144, v152\n\t" \
  "v_fma_f32 v177, v177, v145, v153\n\t" \
  "v_fma_f32 v178, v178, v146, v154\n\t" \
  "v_mfma_f32_16x16x32_bf16 a[56:59], a[172:175], v[56:59], a[56:59]\n\t" \
  "ds_read_b64_tr_b16 a[148:149], v197 offset:0\n\t" \
  "v_fma_f32 v179, v179, v147, v155\n\t" \
  "v_fma_f32 v180, v180, v148, v156\n\t" \
  "v_fma_f32 v181, v181, v149, v157\n\t" \
  "v_mfma_f32_16x16x32_bf16 a[52:55], a[172:175], v[52:55], a[52:55]\n\t" \
  "buffer_load_dwordx4 v202, %[sw], s96 offen offset:2048 lds\n\t" \
  "v_fma_f32 v182, v182, v150, v158\n\t" \
  "v_mfma_f32_16x16x32_bf16 a[48:51], a[172:175], v[48:51], a[48:51]\n\t" \
  "ds_read_b64_tr_b16 a[150:151], v197 offset:1024\n\t" \
  "v_fma_f32 v183, v183, v151, v159\n\t" \
  "v_cvt_pk_bf16_f32 v92, v176, v177\n\t" \
  "v_mfma_f32_16x16x32_bf16 a[64:67], a[176:179], v[48:51], a[64:67]\n\t" \
  "ds_read_b64_tr_b16 a[152:153], v198 offset:0\n\t" \
  "v_cvt_pk_bf16_f32 v93, v178, v179\n\t" \
  "v_cvt_pk_bf16_f32 v94, v180, v181\n\t" \
  "v_mfma_f32_16x16x32_bf16 a[68:71], a[176:179], v[52:55], a[68:71]\n\t" \
  "ds_read_b64_tr_b16 a[154:155], v198 offset:1024\n\t" \
  "v_cvt_pk_bf16_f32 v95, v182, v183\n\t" \
  "v_mfma_f32_16x16x32_bf16 a[72:75], a[176:179], v[56:59], a[72:75]\n\t" \
  "buffer_load_dwordx4 v203, %[sw], s96 offen offset:3072 lds\n\t" \
  "v_mfma_f32_16x16x32_bf16 a[76:79], a[176:179], v[60:63], a[76:79]\n\t" \
  "ds_read_b64_tr_b16 a[156:157], v199 offset:0\n\t" \
  "ds_read_b128 v[144:147], v208 offset:1664\n\t" \
  "v_mfma_f32_16x16x32_bf16 a[92:95], a[180:183], v[60:63], a[92:95]\n\t" \
  "ds_read_b64_tr_b16 a[158:159], v199 offset:1024\n\t" \
  "ds_read_b128 v[148:151], v208 offset:1680\n\t" \
  "v_mfma_f32_16x16x32_bf16 a[88:91], a[180:183], v[56:59], a[88:91]\n\t" \
  "ds_read_b128 v[152:155], v208 offset:1920\n\t" \
  "ds_read_b128 v[156:159], v208 offset:1936\n\t" \
  "s_waitcnt lgkmcnt(15)\n\t" \
  "v_lshlrev_b32 v176, 16, v96\n\t" \
  "v_mfma_f32_16x16x32_bf16 a[84:87], a[180:183], v[52:55], a[84:87]\n\t" \
  "v_and_b32 v177, s98, v96\n\t" \
  "v_lshlrev_b32 v178, 16, v97\n\t" \
  "v_and_b32 v179, s98, v97\n\t" \
  "v_lshlrev_b32 v180, 16, v98\n\t" \
  "v_mfma_f32_16x16x32_bf16 a[80:83], a[180:183], v[48:51], a[80:83]\n\t" \
  "global_load_dword %[pf], v216, %[wq]\n\t" \
  "v_and_b32 v181, s98, v98\n\t" \
  "v_lshlrev_b32 v182, 16, v99\n\t" \
  "v_mfma_f32_16x16x32_bf16 a[96:99], a[184:187], v[48:51], a[96:99]\n\t" \
  "s_lshr_b32 s99, %[c17], 9\n\t" \
  "v_xor_b32 v248, s99, v214\n\t" \
  "v_xor_b32 v249, 256, v248\n\t" \
  "v_xor_b32 v250, 512, v248\n\t" \
  "v_xor_b32 v251, 768, v248\n\t" \
  "v_mfma_f32_16x16x32_bf16 a[100:103], a[184:187], v[52:55], a[100:103]\n\t" \
  "v_and_b32 v183, s98, v99\n\t" \
  "v_fma_f32 v176, v176, v160, v162\n\t" \
  "v_fma_f32 v177, v177, v160, v162\n\t" \
  "v_fma_f32 v178, v178, v160, v162\n\t" \
  "v_mfma_f32_16x16x32_bf16 a[104:107], a[184:187], v[56:59], a[104:107]\n\t" \
  "v_fma_f32 v179, v179, v160, v162\n\t" \
  "v_fma_f32 v180, v180, v160, v162\n\t" \
  "v_fma_f32 v181, v181, v160, v162\n\t" \
  "v_mfma_f32_16x16x32_bf16 a[108:111], a[184:187], v[60:63], a[108:111]\n\t" \
  "v_fma_f32 v182, v182, v160, v162\n\t" \
  "v_fma_f32 v183, v183, v160, v162\n\t" \
  "v_fma_f32 v176, v176, v128, v136\n\t" \
  "v_mfma_f32_16x16x32_bf16 a[124:127], a[188:191], v[60:63], a[124:127]\n\t" \
  "v_fma_f32 v177, v177, v129, v137\n\t" \
  "v_fma_f32 v178, v178, v130, v138\n\t" \
  "v_fma_f32 v179, v179, v131, v139\n\t" \
  "v_mfma_f32_16x16x32_bf16 a[120:123], a[188:191], v[56:59], a[120:123]\n\t" \
  "v_fma_f32 v180, v180, v132, v140\n\t" \
  "v_fma_f32 v181, v181, v133, v141\n\t" \
  "v_fma_f32 v182, v182, v134, v142\n\t" \
  "v_mfma_f32_16x16x32_bf16 a[116:119], a[188:191], v[52:55], a[116:119]\n\t" \
  "v_fma_f32 v183, v183, v135, v143\n\t" \
  "v_cvt_pk_bf16_f32 v96, v176, v177\n\t" \
  "v_cvt_pk_bf16_f32 v97, v178, v179\n\t" \
  "v_cvt_pk_bf16_f32 v98, v180, v181\n\t" \
  "v_mfma_f32_16x16x32_bf16 a[112:115], a[188:191], v[48:51], a[112:115]\n\t" \
  FSTP("v_writelane_b32 %[stv], s94, 42\n\t") \
  FSTP("v_writelane_b32 %[stv], s92, 43\n\t") \
  FSTP("s_memrealtime s[94:95]\n\t") \
  FSTP("s_memtime s[92:93]\n\t") \
  "s_waitcnt lgkmcnt(3)\n\t" \
  "v_mfma_f32_16x16x32_bf16 a[0:3], a[128:131], v[64:67], a[0:3]\n\t" \
  "s_waitcnt vmcnt(5)\n\t" \
  "v_cmp_ne_u32 vcc, v211, %[fl]\n\t" \
  "s_cbranch_vccnz .Lfq_s\n\t" \
  ".Lfq_k:\n\t" \
  "v_mfma_f32_16x16x32_bf16 a[4:7], a[128:131], v[68:71], a[4:7]\n\t" \
  "s_add_u32 s97, %[rot19], 2097152\n\t" \
  "ds_read_b64_tr_b16 a[160:161], v192 offset:8192\n\t" \
  "v_cvt_pk_bf16_f32 v99, v182, v183\n\t" \
  "v_lshlrev_b32 v176, 16, v100\n\t" \
  "v_and_b32 v177, s98, v100\n\t" \
  "v_mfma_f32_16x16x32_bf16 a[8:11], a[128:131], v[72:75], a[8:11]\n\t" \
  "s_bfe_u32 s97, s97, 0x140003\n\t" \
  "ds_read_b64_tr_b16 a[162:163], v192 offset:9216\n\t" \
  "v_lshlrev_b32 v178, 16, v101\n\t" \
  "v_and_b32 v179, s98, v101\n\t" \
  "v_lshlrev_b32 v180, 16, v102\n\t" \
  "v_mfma_f32_16x16x32_bf16 a[12:15], a[128:131], v[76:79], a[12:15]\n\t" \
  "s_add_u32 s97, s97, %[sox]\n\t" \
  "ds_read_b64_tr_b16 a[164:165], v193 offset:8192\n\t" \
  "v_and_b32 v181, s98, v102\n\t" \
  "v_lshlrev_b32 v182, 16, v103\n\t" \
  "v_and_b32 v183, s98, v103\n\t" \
  "v_mfma_f32_16x16x32_bf16 a[28:31], a[132:135], v[76:79], a[28:31]\n\t" \
  "buffer_load_dwordx4 a[192:195], v248, %[ws], s97 offen sc1\n\t" \
  "v_fma_f32 v176, v176, v164, v166\n\t" \
  "v_fma_f32 v177, v177, v164, v166\n\t" \
  "v_mfma_f32_16x16x32_bf16 a[24:27], a[132:135], v[72:75], a[24:27]\n\t" \
  "buffer_load_dwordx4 a[196:199], v249, %[ws], s97 offen sc1\n\t" \
  "v_fma_f32 v178, v178, v164, v166\n\t" \
  "v_fma_f32 v179, v179, v164, v166\n\t" \
  "v_mfma_f32_16x16x32_bf16 a[20:23], a[132:135], v[68:71], a[20:23]\n\t" \
  "ds_read_b64_tr_b16 a[166:167], v193 offset:9216\n\t" \
  "v_fma_f32 v180, v180, v164, v166\n\t" \
  "v_fma_f32 v181, v181, v164, v166\n\t" \
  "v_fma_f32 v182, v182, v164, v166\n\t" \
  "v_mfma_f32_16x16x32_bf16 a[16:19], a[132:135], v[64:67], a[16:19]\n\t" \
  "ds_read_b64_tr_b16 a[168:169], v194 offset:8192\n\t" \
  "v_fma_f32 v183, v183, v164, v166\n\t" \
  "v_fma_f32 v176, v176, v128, v136\n\t" \
  "v_fma_f32 v177, v177, v129, v137\n\t" \
  "v_mfma_f32_16x16x32_bf16 a[32:35], a[136:139], v[64:67], a[32:35]\n\t" \
  "buffer_load_dwordx4 a[200:203], v250, %[ws], s97 offen sc1\n\t" \
  "v_fma_f32 v178, v178, v130, v138\n\t" \
  ".Lhs1:\n\ts_cbranch_execz .Lhs1n\n\t" \
  "v_fma_f32 v179, v179, v131, v139\n\t" \
  "v_mfma_f32_16x16x32_bf16 a[36:39], a[136:139], v[68:71], a[36:39]\n\t" \
  "ds_read_b64_tr_b16 a[170:171], v194 offset:9216\n\t" \
  "v_fma_f32 v180, v180, v132, v140\n\t" \
  "v_fma_f32 v181, v181, v133, v141\n\t" \
  "v_mfma_f32_16x16x32_bf16 a[40:43], a[136:139], v[72:75], a[40:43]\n\t" \
  "ds_read_b64_tr_b16 a[172:173], v195 offset:8192\n\t" \
  "v_fma_f32 v182, v182, v134, v142\n\t" \
  "v_fma_f32 v183, v183, v135, v143\n\t" \
  "v_mfma_f32_16x16x32_bf16 a[44:47], a[136:139], v[76:79], a[44:47]\n\t" \
  "buffer_load_dwordx4 a[204:207], v251, %[ws], s97 offen sc1\n\t" \
  "v_cvt_pk_bf16_f32 v100, v176, v177\n\t" \
  "v_mfma_f32_16x16x32_bf16 a[60:63], a[140:143], v[76:79], a[60:63]\n\t" \
  "s_add_u32 s97, s97, %[dk]\n\t" \
  "ds_read_b64_tr_b16 a[174:175], v195 offset:9216\n\t" \
  "v_cvt_pk_bf16_f32 v101, v178, v179\n\t" \
  "v_cvt_pk_bf16_f32 v102, v180, v181\n\t" \
  "v_mfma_f32_16x16x32_bf16 a[56:59], a[140:143], v[72:75], a[56:59]\n\t" \
  "ds_read_b64_tr_b16 a[176:177], v196 offset:8192\n\t" \
  "v_cvt_pk_bf16_f32 v103, v182, v183\n\t" \
  "v_lshlrev_b32 v176, 16, v104\n\t" \
  "v_mfma_f32_16x16x32_bf16 a[52:55], a[140:143], v[68:71], a[52:55]\n\t" \
  "buffer_load_dwordx4 a[208:211], v248, %[ws], s97 offen sc1\n\t" \
  "v_and_b32 v177, s98, v104\n\t" \
  "v_mfma_f32_16x16x32_bf16 a[48:51], a[140:143], v[64:67], a[48:51]\n\t" \
  "ds_read_b64_tr_b16 a[178:179], v196 offset:9216\n\t" \
  "v_lshlrev_b32 v178, 16, v105\n\t" \
  "v_and_b32 v179, s98, v105\n\t" \
  "v_mfma_f32_16x16x32_bf16 a[64:67], a[144:147], v[64:67], a[64:67]\n\t" \
  "ds_read_b64_tr_b16 a[180:181], v197 offset:8192\n\t" \
  "v_lshlrev_b32 v180, 16, v106\n\t" \
  "v_and_b32 v181, s98, v106\n\t" \
  "v_mfma_f32_16x16x32_bf16 a[68:71], a[144:147], v[68:71], a[68:71]\n\t" \
  "buffer_load_dwordx4 a[212:215], v249, %[ws], s97 offen sc1\n\t" \
  "v_lshlrev_b32 v182, 16, v107\n\t" \
  "v_mfma_f32_16x16x32_bf16 a[72:75], a[144:147], v[72:75], a[72:75]\n\t" \
  "ds_read_b64_tr_b16 a[182:183], v197 offset:9216\n\t" \
  "v_and_b32 v183, s98, v107\n\t" \
  "v_fma_f32 v176, v176, v168, v170\n\t" \
  "v_mfma_f32_16x16x32_bf16 a[76:79], a[144:147], v[76:79], a[76:79]\n\t" \
  "ds_read_b64_tr_b16 a[184:185], v198 offset:8192\n\t" \
  "v_fma_f32 v177, v177, v168, v170\n\t" \
  "v_fma_f32 v178, v178, v168, v170\n\t" \
  "v_mfma_f32_16x16x32_bf16 a[92:95], a[148:151], v[76:79], a[92:95]\n\t" \
  "ds_read_b64_tr_b16 a[186:187], v198 offset:9216\n\t" \
  "v_fma_f32 v179, v179, v168, v170\n\t" \
  "v_fma_f32 v180, v180, v168, v170\n\t" \
  "v_mfma_f32_16x16x32_bf16 a[88:91], a[148:151], v[72:75], a[88:91]\n\t" \
  "buffer_load_dwordx4 a[216:219], v250, %[ws], s97 offen sc1\n\t" \
  "v_fma_f32 v181, v181, v168, v170\n\t" \
  "v_mfma_f32_16x16x32_bf16 a[84:87], a[148:151], v[68:71], a[84:87]\n\t" \
  "ds_read_b64_tr_b16 a[188:189], v199 offset:8192\n\t" \
  "v_fma_f32 v182, v182, v168, v170\n\t" \
  "v_fma_f32 v183, v183, v168, v170\n\t" \
  "v_mfma_f32_16x16x32_bf16 a[80:83], a[148:151], v[64:67], a[80:83]\n\t" \
  "ds_read_b64_tr_b16 a[190:191], v199 offset:9216\n\t" \
  "v_fma_f32 v176, v176, v128, v136\n\t" \
  "v_fma_f32 v177, v177, v129, v137\n\t" \
  "v_mfma_f32_16x16x32_bf16 a[96:99], a[152:155], v[64:67], a[96:99]\n\t" \
  "buffer_load_dwordx4 a[220:223], v251, %[ws], s97 offen sc1\n\t" \
  "v_fma_f32 v178, v178, v130, v138\n\t" \
  "v_mfma_f32_16x16x32_bf16 a[100:103], a[152:155], v[68:71], a[100:103]\n\t" \
  "v_fma_f32 v179, v179, v131, v139\n\t" \
  "v_fma_f32 v180, v180, v132, v140\n\t" \
  "v_fma_f32 v181, v181, v133, v141\n\t" \
  "v_mfma_f32_16x16x32_bf16 a[104:107], a[152:155], v[72:75], a[104:107]\n\t" \
  "v_fma_f32 v182, v182, v134, v142\n\t" \
  "v_fma_f32 v183, v183, v135, v143\n\t" \
  "v_cvt_pk_bf16_f32 v104, v176, v177\n\t" \
  "v_mfma_f32_16x16x32_bf16 a[108:111], a[152:155], v[76:79], a[108:111]\n\t" \
  "v_cvt_pk_bf16_f32 v105, v178, v179\n\t" \
  "v_cvt_pk_bf16_f32 v106, v180, v181\n\t" \
  "v_cvt_pk_bf16_f32 v107, v182, v183\n\t" \
  "v_mfma_f32_16x16x32_bf16 a[124:127], a[156:159], v[76:79], a[124:127]\n\t" \
  "ds_read_b128 v[112:115], v205 offset:0\n\t" \
  "ds_read_b128 v[116:119], v205 offset:2048\n\t" \
  "v_mfma_f32_16x16x32_bf16 a[120:123], a[156:159], v[72:75], a[120:123]\n\t" \
  "ds_read_b128 v[120:123], v205 offset:4096\n\t" \
  "ds_read_b128 v[124:127], v205 offset:6144\n\t" \
  "v_lshlrev_b32 v176, 16, v108\n\t" \
  "v_and_b32 v177, s98, v108\n\t" \
  "v_mfma_f32_16x16x32_bf16 a[116:119], a[156:159], v[68:71], a[116:119]\n\t" \
  "v_lshlrev_b32 v178, 16, v109\n\t" \
  "v_and_b32 v179, s98, v109\n\t" \
  "v_lshlrev_b32 v180, 16, v110\n\t" \
  "v_and_b32 v181, s98, v110\n\t" \
  "v_mfma_f32_16x16x32_bf16 a[112:115], a[156:159], v[64:67], a[112:115]\n\t" \
  FSTP("v_writelane_b32 %[stv], s94, 44\n\t") \
  FSTP("v_writelane_b32 %[stv], s92, 45\n\t") \
  FSTP("s_memrealtime s[94:95]\n\t") \
  FSTP("s_memtime s[92:93]\n\t") \
  "s_waitcnt lgkmcnt(4)\n\t" \
  "s_barrier\n\t" \
  "v_mfma_f32_16x16x32_bf16 a[0:3], a[160:163], v[80:83], a[0:3]\n\t" \
  "s_add_u32 s96, %[rot19], 2621440\n\t" \
  "ds_read_b64_tr_b16 a[128:129], v184 offset:0\n\t" \
  "v_lshlrev_b32 v182, 16, v111\n\t" \
  "v_and_b32 v183, s98, v111\n\t" \
  "v_fma_f32 v176, v176, v172, v174\n\t" \
  "v_mfma_f32_16x16x32_bf16 a[4:7], a[160:163], v[84:87], a[4:7]\n\t" \
  "s_and_b32 s96, s96, 0x780000\n\t" \
  "ds_read_b64_tr_b16 a[130:131], v184 offset:1024\n\t" \
  "v_fma_f32 v177, v177, v172, v174\n\t" \
  "v_fma_f32 v178, v178, v172, v174\n\t" \
  "v_fma_f32 v179, v179, v172, v174\n\t" \
  "v_mfma_f32_16x16x32_bf16 a[8:11], a[160:163], v[88:91], a[8:11]\n\t" \
  "s_add_u32 m0, %[mw], 98304\n\t" \
  "ds_read_b64_tr_b16 a[132:133], v185 offset:0\n\t" \
  "v_fma_f32 v180, v180, v172, v174\n\t" \
  "v_fma_f32 v181, v181, v172, v174\n\t" \
  "v_fma_f32 v182, v182, v172, v174\n\t" \
  "v_mfma_f32_16x16x32_bf16 a[12:15], a[160:163], v[92:95], a[12:15]\n\t" \
  "buffer_load_dwordx4 v200, %[sw], s96 offen offset:0 lds\n\t" \
  "v_fma_f32 v183, v183, v172, v174\n\t" \
  "v_fma_f32 v176, v176, v128, v136\n\t" \
  "v_mfma_f32_16x16x32_bf16 a[28:31], a[164:167], v[92:95], a[28:31]\n\t" \
  "buffer_load_dwordx4 v201, %[sw], s96 offen offset:1024 lds\n\t" \
  "v_fma_f32 v177, v177, v129, v137\n\t" \
  "v_fma_f32 v178, v178, v130, v138\n\t" \
  "v_mfma_f32_16x16x32_bf16 a[24:27], a[164:167], v[88:91], a[24:27]\n\t" \
  "ds_read_b64_tr_b16 a[134:135], v185 offset:1024\n\t" \
  "v_fma_f32 v179, v179, v131, v139\n\t" \
  "v_fma_f32 v180, v180, v132, v140\n\t" \
  "v_mfma_f32_16x16x32_bf16 a[20:23], a[164:167], v[84:87], a[20:23]\n\t" \
  "ds_read_b64_tr_b16 a[136:137], v186 offset:0\n\t" \
  "v_fma_f32 v181, v181, v133, v141\n\t" \
  "v_fma_f32 v182, v182, v134, v142\n\t" \
  "v_mfma_f32_16x16x32_bf16 a[16:19], a[164:167], v[80:83], a[16:19]\n\t" \
  "buffer_load_dwordx4 v202, %[sw], s96 offen offset:2048 lds\n\t" \
  "v_fma_f32 v183, v183, v135, v143\n\t" \
  "v_mfma_f32_16x16x32_bf16 a[32:35], a[168:171], v[80:83], a[32:35]\n\t" \
  "ds_read_b64_tr_b16 a[138:139], v186 offset:1024\n\t" \
  "v_cvt_pk_bf16_f32 v108, v176, v177\n\t" \
  "v_cvt_pk_bf16_f32 v109, v178, v179\n\t" \
  "v_mfma_f32_16x16x32_bf16 a[36:39], a[168:171], v[84:87], a[36:39]\n\t" \
  "ds_read_b64_tr_b16 a[140:141], v187 offset:0\n\t" \
  "v_cvt_pk_bf16_f32 v110, v180, v181\n\t" \
  "v_cvt_pk_bf16_f32 v111, v182, v183\n\t" \
  "s_waitcnt lgkmcnt(7)\n\t" \
  "v_mfma_f32_16x16x32_bf16 a[40:43], a[168:171], v[88:91], a[40:43]\n\t" \
  "buffer_load_dwordx4 v203, %[sw], s96 offen offset:3072 lds\n\t" \
  "v_lshlrev_b32 v176, 16, v112\n\t" \
  "v_mfma_f32_16x16x32_bf16 a[44:47], a[168:171], v[92:95], a[44:47]\n\t" \
  "s_add_u32 s97, %[rot19], 2621440\n\t" \
  "ds_read_b64_tr_b16 a[142:143], v187 offset:1024\n\t" \
  "v_and_b32 v177, s98, v112\n\t" \
  "v_lshlrev_b32 v178, 16, v113\n\t" \
  "v_mfma_f32_16x16x32_bf16 a[60:63], a[172:175], v[92:95], a[60:63]\n\t" \
  "s_bfe_u32 s97, s97, 0x140003\n\t" \
  "ds_read_b64_tr_b16 a[144:145], v188 offset:0\n\t" \
  "v_and_b32 v179, s98, v113\n\t" \
  "v_lshlrev_b32 v180, 16, v114\n\t" \
  "v_mfma_f32_16x16x32_bf16 a[56:59], a[172:175], v[88:91], a[56:59]\n\t" \
  "s_add_u32 s97, s97, %[sox]\n\t" \
  "ds_read_b64_tr_b16 a[146:147], v188 offset:1024\n\t" \
  "v_and_b32 v181, s98, v114\n\t" \
  "v_lshlrev_b32 v182, 16, v115\n\t" \
  "v_mfma_f32_16x16x32_bf16 a[52:55], a[172:175], v[84:87], a[52:55]\n\t" \
  "buffer_load_dwordx4 a[224:227], v248, %[ws], s97 offen sc1\n\t" \
  "v_and_b32 v183, s98, v115\n\t" \
  "v_mfma_f32_16x16x32_bf16 a[48:51], a[172:175], v[80:83], a[48:51]\n\t" \
  "ds_read_b64_tr_b16 a[148:149], v189 offset:0\n\t" \
  "v_fma_f32 v176, v176, v160, v162\n\t" \
  "v_fma_f32 v177, v177, v160, v162\n\t" \
  "v_mfma_f32_16x16x32_bf16 a[64:67], a[176:179], v[80:83], a[64:67]\n\t" \
  "ds_read_b64_tr_b16 a[150:151], v189 offset:1024\n\t" \
  "v_fma_f32 v178, v178, v160, v162\n\t" \
  "v_fma_f32 v179, v179, v160, v162\n\t" \
  "v_mfma_f32_16x16x32_bf16 a[68:71], a[176:179], v[84:87], a[68:71]\n\t" \
  "buffer_load_dwordx4 a[228:231], v249, %[ws], s97 offen sc1\n\t" \
  "v_fma_f32 v180, v180, v160, v162\n\t" \
  "v_mfma_f32_16x16x32_bf16 a[72:75], a[176:179], v[88:91], a[72:75]\n\t" \
  "ds_read_b64_tr_b16 a[152:153], v190 offset:0\n\t" \
  "v_fma_f32 v181, v181, v160, v162\n\t" \
  "v_fma_f32 v182, v182, v160, v162\n\t" \
  "v_mfma_f32_16x16x32_bf16 a[76:79], a[176:179], v[92:95], a[76:79]\n\t" \
  "ds_read_b64_tr_b16 a[154:155], v190 offset:1024\n\t" \
  "v_fma_f32 v183, v183, v160, v162\n\t" \
  "v_fma_f32 v176, v176, v144, v152\n\t" \
  "v_mfma_f32_16x16x32_bf16 a[92:95], a[180:183], v[92:95], a[92:95]\n\t" \
  "ds_read_b64_tr_b16 a[156:157], v191 offset:0\n\t" \
  "v_fma_f32 v177, v177, v145, v153\n\t" \
  "v_fma_f32 v178, v178, v146, v154\n\t" \
  "v_mfma_f32_16x16x32_bf16 a[88:91], a[180:183], v[88:91], a[88:91]\n\t" \
  "buffer_load_dwordx4 a[232:235], v250, %[ws], s97 offen sc1\n\t" \
  "v_fma_f32 v179, v179, v147, v155\n\t" \
  "v_mfma_f32_16x16x32_bf16 a[84:87], a[180:183], v[84:87], a[84:87]\n\t" \
  "ds_read_b64_tr_b16 a[158:159], v191 offset:1024\n\t" \
  "v_fma_f32 v180, v180, v148, v156\n\t" \
  "v_fma_f32 v181, v181, v149, v157\n\t" \
  "v_mfma_f32_16x16x32_bf16 a[80:83], a[180:183], v[80:83], a[80:83]\n\t" \
  "v_fma_f32 v182, v182, v150, v158\n\t" \
  "v_fma_f32 v183, v183, v151, v159\n\t" \
  "v_cvt_pk_bf16_f32 v112, v176, v177\n\t" \
  "v_mfma_f32_16x16x32_bf16 a[96:99], a[184:187], v[80:83], a[96:99]\n\t" \
  "buffer_load_dwordx4 a[236:239], v251, %[ws], s97 offen sc1\n\t" \
  "v_cvt_pk_bf16_f32 v113, v178, v179\n\t" \
  "v_mfma_f32_16x16x32_bf16 a[100:103], a[184:187], v[84:87], a[100:103]\n\t" \
  "v_cvt_pk_bf16_f32 v114, v180, v181\n\t" \
  "v_cvt_pk_bf16_f32 v115, v182, v183\n\t" \
  "v_lshlrev_b32 v176, 16, v116\n\t" \
  "v_mfma_f32_16x16x32_bf16 a[104:107], a[184:187], v[88:91], a[104:107]\n\t" \
  "v_and_b32 v177, s98, v116\n\t" \
  "v_lshlrev_b32 v178, 16, v117\n\t" \
  "v_and_b32 v179, s98, v117\n\t" \
  "v_mfma_f32_16x16x32_bf16 a[108:111], a[184:187], v[92:95], a[108:111]\n\t" \
  "v_lshlrev_b32 v180, 16, v118\n\t" \
  "v_and_b32 v181, s98, v118\n\t" \
  "v_lshlrev_b32 v182, 16, v119\n\t" \
  "v_mfma_f32_16x16x32_bf16 a[124:127], a[188:191], v[92:95], a[124:127]\n\t" \
  "v_and_b32 v183, s98, v119\n\t" \
  "v_fma_f32 v176, v176, v164, v166\n\t" \
  "v_fma_f32 v177, v177, v164, v166\n\t" \
  "v_mfma_f32_16x16x32_bf16 a[120:123], a[188:191], v[88:91], a[120:123]\n\t" \
  "v_fma_f32 v178, v178, v164, v166\n\t" \
  "v_fma_f32 v179, v179, v164, v166\n\t" \
  "v_fma_f32 v180, v180, v164, v166\n\t" \
  "v_mfma_f32_16x16x32_bf16 a[116:119], a[188:191], v[84:87], a[116:119]\n\t" \
  "v_fma_f32 v181, v181, v164, v166\n\t" \
  "v_fma_f32 v182, v182, v164, v166\n\t" \
  "v_fma_f32 v183, v183, v164, v166\n\t" \
  "v_fma_f32 v176, v176, v144, v152\n\t" \
  "v_mfma_f32_16x16x32_bf16 a[112:115], a[188:191], v[80:83], a[112:115]\n\t" \
  FSTP("v_writelane_b32 %[stv], s94, 46\n\t") \
  FSTP("v_writelane_b32 %[stv], s92, 47\n\t") \
  FSTP("s_memrealtime s[94:95]\n\t") \
  FSTP("s_memtime s[92:93]\n\t") \
  "s_waitcnt lgkmcnt(0)\n\t" \
  "v_mfma_f32_16x16x32_bf16 a[0:3], a[128:131], v[96:99], a[0:3]\n\t" \
  "s_add_u32 s96, %[rot19], 3145728\n\t" \
  "ds_read_b64_tr_b16 a[160:161], v184 offset:8192\n\t" \
  "v_fma_f32 v177, v177, v145, v153\n\t" \
  "v_fma_f32 v178, v178, v146, v154\n\t" \
  "v_fma_f32 v179, v179, v147, v155\n\t" \
  "v_mfma_f32_16x16x32_bf16 a[4:7], a[128:131], v[100:103], a[4:7]\n\t" \
  "s_and_b32 s96, s96, 0x780000\n\t" \
  "ds_read_b64_tr_b16 a[162:163], v184 offset:9216\n\t" \
  "v_fma_f32 v180, v180, v148, v156\n\t" \
  "v_fma_f32 v181, v181, v149, v157\n\t" \
  "v_fma_f32 v182, v182, v150, v158\n\t" \
  "v_mfma_f32_16x16x32_bf16 a[8:11], a[128:131], v[104:107], a[8:11]\n\t" \
  "s_add_u32 m0, %[mw], 16384\n\t" \
  "ds_read_b64_tr_b16 a[164:165], v185 offset:8192\n\t" \
  "v_fma_f32 v183, v183, v151, v159\n\t" \
  "v_cvt_pk_bf16_f32 v116, v176, v177\n\t" \
  "v_cvt_pk_bf16_f32 v117, v178, v179\n\t" \
  "v_mfma_f32_16x16x32_bf16 a[12:15], a[128:131], v[108:111], a[12:15]\n\t" \
  "buffer_load_dwordx4 v200, %[sw], s96 offen offset:0 lds\n\t" \
  "v_cvt_pk_bf16_f32 v118, v180, v181\n\t" \
  "v_mfma_f32_16x16x32_bf16 a[28:31], a[132:135], v[108:111], a[28:31]\n\t" \
  "ds_read_b64_tr_b16 a[166:167], v185 offset:9216\n\t" \
  "v_cvt_pk_bf16_f32 v119, v182, v183\n\t" \
  "v_lshlrev_b32 v176, 16, v120\n\t" \
  "v_mfma_f32_16x16x32_bf16 a[24:27], a[132:135], v[104:107], a[24:27]\n\t" \
  "ds_read_b64_tr_b16 a[168:169], v186 offset:8192\n\t" \
  "v_and_b32 v177, s98, v120\n\t" \
  "v_lshlrev_b32 v178, 16, v121\n\t" \
  "v_mfma_f32_16x16x32_bf16 a[20:23], a[132:135], v[100:103], a[20:23]\n\t" \
  "ds_read_b64_tr_b16 a[170:171], v186 offset:9216\n\t" \
  "v_and_b32 v179, s98, v121\n\t" \
  "v_lshlrev_b32 v180, 16, v122\n\t" \
  "v_mfma_f32_16x16x32_bf16 a[16:19], a[132:135], v[96:99], a[16:19]\n\t" \
  "buffer_load_dwordx4 v201, %[sw], s96 offen offset:1024 lds\n\t" \
  "v_and_b32 v181, s98, v122\n\t" \
  "v_mfma_f32_16x16x32_bf16 a[32:35], a[136:139], v[96:99], a[32:35]\n\t" \
  "ds_read_b64_tr_b16 a[172:173], v187 offset:8192\n\t" \
  "v_lshlrev_b32 v182, 16, v123\n\t" \
  "v_and_b32 v183, s98, v123\n\t" \
  "v_mfma_f32_16x16x32_bf16 a[36:39], a[136:139], v[100:103], a[36:39]\n\t" \
  "ds_read_b64_tr_b16 a[174:175], v187 offset:9216\n\t" \
  "v_fma_f32 v176, v176, v168, v170\n\t" \
  "v_fma_f32 v177, v177, v168, v170\n\t" \
  "v_mfma_f32_16x16x32_bf16 a[40:43], a[136:139], v[104:107], a[40:43]\n\t" \
  "ds_read_b64_tr_b16 a[176:177], v188 offset:8192\n\t" \
  "v_fma_f32 v178, v178, v168, v170\n\t" \
  "v_fma_f32 v179, v179, v168, v170\n\t" \
  "v_mfma_f32_16x16x32_bf16 a[44:47], a[136:139], v[108:111], a[44:47]\n\t" \
  "ds_read_b64_tr_b16 a[178:179], v188 offset:9216\n\t" \
  "v_fma_f32 v180, v180, v168, v170\n\t" \
  "v_fma_f32 v181, v181, v168, v170\n\t" \
  "v_mfma_f32_16x16x32_bf16 a[60:63], a[140:143], v[108:111], a[60:63]\n\t" \
  "ds_read_b64_tr_b16 a[180:181], v189 offset:8192\n\t" \
  "v_fma_f32 v182, v182, v168, v170\n\t" \
  "v_fma_f32 v183, v183, v168, v170\n\t" \
  "v_mfma_f32_16x16x32_bf16 a[56:59], a[140:143], v[104:107], a[56:59]\n\t" \
  "ds_read_b64_tr_b16 a[182:183], v189 offset:9216\n\t" \
  "v_fma_f32 v176, v176, v144, v152\n\t" \
  "v_fma_f32 v177, v177, v145, v153\n\t" \
  "v_mfma_f32_16x16x32_bf16 a[52:55], a[140:143], v[100:103], a[52:55]\n\t" \
  "buffer_load_dwordx4 v202, %[sw], s96 offen offset:2048 lds\n\t" \
  "v_fma_f32 v178, v178, v146, v154\n\t" \
  "v_mfma_f32_16x16x32_bf16 a[48:51], a[140:143], v[96:99], a[48:51]\n\t" \
  "ds_read_b64_tr_b16 a[184:185], v190 offset:8192\n\t" \
  "v_fma_f32 v179, v179, v147, v155\n\t" \
  "v_fma_f32 v180, v180, v148, v156\n\t" \
  "v_mfma_f32_16x16x32_bf16 a[64:67], a[144:147], v[96:99], a[64:67]\n\t" \
  "ds_read_b64_tr_b16 a[186:187], v190 offset:9216\n\t" \
  "v_fma_f32 v181, v181, v149, v157\n\t" \
  "v_fma_f32 v182, v182, v150, v158\n\t" \
  "v_mfma_f32_16x16x32_bf16 a[68:71], a[144:147], v[100:103], a[68:71]\n\t" \
  "ds_read_b64_tr_b16 a[188:189], v191 offset:8192\n\t" \
  "v_fma_f32 v183, v183, v151, v159\n\t" \
  "v_cvt_pk_bf16_f32 v120, v176, v177\n\t" \
  "v_mfma_f32_16x16x32_bf16 a[72:75], a[144:147], v[104:107], a[72:75]\n\t" \
  "ds_read_b64_tr_b16 a[190:191], v191 offset:9216\n\t" \
  "v_cvt_pk_bf16_f32 v121, v178, v179\n\t" \
  "v_cvt_pk_bf16_f32 v122, v180, v181\n\t" \
  "v_mfma_f32_16x16x32_bf16 a[76:79], a[144:147], v[108:111], a[76:79]\n\t" \
  "v_cvt_pk_bf16_f32 v123, v182, v183\n\t" \
  "v_lshlrev_b32 v176, 16, v124\n\t" \
  "v_and_b32 v177, s98, v124\n\t" \
  "v_mfma_f32_16x16x32_bf16 a[92:95], a[148:151], v[108:111], a[92:95]\n\t" \
  "v_lshlrev_b32 v178, 16, v125\n\t" \
  "v_and_b32 v179, s98, v125\n\t" \
  "v_lshlrev_b32 v180, 16, v126\n\t" \
  "v_mfma_f32_16x16x32_bf16 a[88:91], a[148:151], v[104:107], a[88:91]\n\t" \
  "buffer_load_dwordx4 v203, %[sw], s96 offen offset:3072 lds\n\t" \
  "v_and_b32 v181, s98, v126\n\t" \
  "v_mfma_f32_16x16x32_bf16 a[84:87], a[148:151], v[100:103], a[84:87]\n\t" \
  "v_lshlrev_b32 v182, 16, v127\n\t" \
  "v_and_b32 v183, s98, v127\n\t" \
  "v_fma_f32 v176, v176, v172, v174\n\t" \
  "v_mfma_f32_16x16x32_bf16 a[80:83], a[148:151], v[96:99], a[80:83]\n\t" \
  "v_fma_f32 v177, v177, v172, v174\n\t" \
  "v_fma_f32 v178, v178, v172, v174\n\t" \
  "v_fma_f32 v179, v179, v172, v174\n\t" \
  "v_mfma_f32_16x16x32_bf16 a[96:99], a[152:155], v[96:99], a[96:99]\n\t" \
  "v_mbcnt_lo_u32_b32 v217, -1, 0\n\t" \
  "v_mbcnt_hi_u32_b32 v217, -1, v217\n\t" \
  "v_cmp_gt_u32 vcc, 16, v217\n\t" \
  "v_mfma_f32_16x16x32_bf16 a[100:103], a[152:155], v[100:103], a[100:103]\n\t" \
  "v_lshlrev_b32 v217, 7, v217\n\t" \
  "v_bfrev_b32 v215, 1\n\t" \
  "v_cndmask_b32 v217, v215, v217, vcc\n\t" \
  "v_mfma_f32_16x16x32_bf16 a[104:107], a[152:155], v[104:107], a[104:107]\n\t" \
  "v_fma_f32 v180, v180, v172, v174\n\t" \
  "v_fma_f32 v181, v181, v172, v174\n\t" \
  "v_fma_f32 v182, v182, v172, v174\n\t" \
  "v_mfma_f32_16x16x32_bf16 a[108:111], a[152:155], v[108:111], a[108:111]\n\t" \
  "v_fma_f32 v183, v183, v172, v174\n\t" \
  "v_fma_f32 v176, v176, v144, v152\n\t" \
  "v_fma_f32 v177, v177, v145, v153\n\t" \
  "v_mfma_f32_16x16x32_bf16 a[124:127], a[156:159], v[108:111], a[124:127]\n\t" \
  "v_fma_f32 v178, v178, v146, v154\n\t" \
  "v_fma_f32 v179, v179, v147, v155\n\t" \
  "v_fma_f32 v180, v180, v148, v156\n\t" \
  "v_mfma_f32_16x16x32_bf16 a[120:123], a[156:159], v[104:107], a[120:123]\n\t" \
  "v_fma_f32 v181, v181, v149, v157\n\t" \
  "v_fma_f32 v182, v182, v150, v158\n\t" \
  "v_fma_f32 v183, v183, v151, v159\n\t" \
  "v_mfma_f32_16x16x32_bf16 a[116:119], a[156:159], v[100:103], a[116:119]\n\t" \
  "v_cvt_pk_bf16_f32 v124, v176, v177\n\t" \
  "v_cvt_pk_bf16_f32 v125, v178, v179\n\t" \
  "v_cvt_pk_bf16_f32 v126, v180, v181\n\t" \
  "v_cvt_pk_bf16_f32 v127, v182, v183\n\t" \
  "v_mfma_f32_16x16x32_bf16 a[112:115], a[156:159], v[96:99], a[112:115]\n\t" \
  FSTP("v_writelane_b32 %[stv], s94, 48\n\t") \
  FSTP("v_writelane_b32 %[stv], s92, 49\n\t") \
  FSTP("s_memrealtime s[94:95]\n\t") \
  FSTP("s_memtime s[92:93]\n\t") \
  "s_waitcnt vmcnt(21) lgkmcnt(0)\n\t" \
  "s_barrier\n\t" \
  "v_mfma_f32_16x16x32_bf16 a[0:3], a[160:163], v[112:115], a[0:3]\n\t" \
  "s_add_u32 s97, s97, %[dk]\n\t" \
  "ds_read_b64_tr_b16 a[128:129], v184 offset:49152\n\t" \
  "v_mfma_f32_16x16x32_bf16 a[4:7], a[160:163], v[116:119], a[4:7]\n\t" \
  "buffer_load_dwordx4 a[240:243], v248, %[ws], s97 offen sc1\n\t" \
  "v_mfma_f32_16x16x32_bf16 a[8:11], a[160:163], v[120:123], a[8:11]\n\t" \
  "ds_read_b64_tr_b16 a[130:131], v184 offset:50176\n\t" \
  "v_mfma_f32_16x16x32_bf16 a[12:15], a[160:163], v[124:127], a[12:15]\n\t" \
  "buffer_load_dwordx4 a[244:247], v249, %[ws], s97 offen sc1\n\t" \
  "v_mfma_f32_16x16x32_bf16 a[28:31], a[164:167], v[124:127], a[28:31]\n\t" \
  "ds_read_b64_tr_b16 a[132:133], v185 offset:49152\n\t" \
  "v_mfma_f32_16x16x32_bf16 a[24:27], a[164:167], v[120:123], a[24:27]\n\t" \
  "ds_read_b64_tr_b16 a[134:135], v185 offset:50176\n\t" \
  "v_mfma_f32_16x16x32_bf16 a[20:23], a[164:167], v[116:119], a[20:23]\n\t" \
  "buffer_load_dwordx4 a[248:251], v250, %[ws], s97 offen sc1\n\t" \
  "v_mfma_f32_16x16x32_bf16 a[16:19], a[164:167], v[112:115], a[16:19]\n\t" \
  "ds_read_b64_tr_b16 a[136:137], v186 offset:49152\n\t" \
  "v_mfma_f32_16x16x32_bf16 a[32:35], a[168:171], v[112:115], a[32:35]\n\t" \
  "ds_read_b64_tr_b16 a[138:139], v186 offset:50176\n\t" \
  "v_mfma_f32_16x16x32_bf16 a[36:39], a[168:171], v[116:119], a[36:39]\n\t" \
  "buffer_load_dwordx4 a[252:255], v251, %[ws], s97 offen sc1\n\t" \
  "v_mfma_f32_16x16x32_bf16 a[40:43], a[168:171], v[120:123], a[40:43]\n\t" \
  "s_add_u32 s96, %[rot19], 3670016\n\t" \
  "ds_read_b64_tr_b16 a[140:141], v187 offset:49152\n\t" \
  "v_mfma_f32_16x16x32_bf16 a[44:47], a[168:171], v[124:127], a[44:47]\n\t" \
  "s_and_b32 s96, s96, 0x780000\n\t" \
  "ds_read_b64_tr_b16 a[142:143], v187 offset:50176\n\t" \
  "v_mfma_f32_16x16x32_bf16 a[60:63], a[172:175], v[124:127], a[60:63]\n\t" \
  "s_add_u32 m0, %[mw], 32768\n\t" \
  "ds_read_b64_tr_b16 a[144:145], v188 offset:49152\n\t" \
  "v_mfma_f32_16x16x32_bf16 a[56:59], a[172:175], v[120:123], a[56:59]\n\t" \
  "buffer_load_dwordx4 v200, %[sw], s96 offen offset:0 lds\n\t" \
  "v_mfma_f32_16x16x32_bf16 a[52:55], a[172:175], v[116:119], a[52:55]\n\t" \
  "ds_read_b64_tr_b16 a[146:147], v188 offset:50176\n\t" \
  "v_mfma_f32_16x16x32_bf16 a[48:51], a[172:175], v[112:115], a[48:51]\n\t" \
  "ds_read_b64_tr_b16 a[148:149], v189 offset:49152\n\t" \
  "v_mfma_f32_16x16x32_bf16 a[64:67], a[176:179], v[112:115], a[64:67]\n\t" \
  "ds_read_b64_tr_b16 a[150:151], v189 offset:50176\n\t" \
  "v_mfma_f32_16x16x32_bf16 a[68:71], a[176:179], v[116:119], a[68:71]\n\t" \
  "buffer_load_dwordx4 v201, %[sw], s96 offen offset:1024 lds\n\t" \
  "v_mfma_f32_16x16x32_bf16 a[72:75], a[176:179], v[120:123], a[72:75]\n\t" \
  "ds_read_b64_tr_b16 a[152:153], v190 offset:49152\n\t" \
  "v_mfma_f32_16x16x32_bf16 a[76:79], a[176:179], v[124:127], a[76:79]\n\t" \
  "ds_read_b64_tr_b16 a[154:155], v190 offset:50176\n\t" \
  "v_mfma_f32_16x16x32_bf16 a[92:95], a[180:183], v[124:127], a[92:95]\n\t" \
  "ds_read_b64_tr_b16 a[156:157], v191 offset:49152\n\t" \
  "v_mfma_f32_16x16x32_bf16 a[88:91], a[180:183], v[120:123], a[88:91]\n\t" \
  "buffer_load_dwordx4 v202, %[sw], s96 offen offset:2048 lds\n\t" \
  "v_mfma_f32_16x16x32_bf16 a[84:87], a[180:183], v[116:119], a[84:87]\n\t" \
  "ds_read_b64_tr_b16 a[158:159], v191 offset:50176\n\t" \
  "v_mfma_f32_16x16x32_bf16 a[80:83], a[180:183], v[112:115], a[80:83]\n\t" \
  "v_mfma_f32_16x16x32_bf16 a[96:99], a[184:187], v[112:115], a[96:99]\n\t" \
  "v_mfma_f32_16x16x32_bf16 a[100:103], a[184:187], v[116:119], a[100:103]\n\t" \
  "buffer_load_dwordx4 v203, %[sw], s96 offen offset:3072 lds\n\t" \
  "v_mfma_f32_16x16x32_bf16 a[104:107], a[184:187], v[120:123], a[104:107]\n\t" \
  "s_add_u32 s99, %[pf1], 0\n\t" \
  "v_mfma_f32_16x16x32_bf16 a[108:111], a[184:187], v[124:127], a[108:111]\n\t" \
  "buffer_load_dword %[pf], v217, %[wo], s99 offen\n\t" \
  "v_mfma_f32_16x16x32_bf16 a[124:127], a[188:191], v[124:127], a[124:127]\n\t" \
  "v_mfma_f32_16x16x32_bf16 a[120:123], a[188:191], v[120:123], a[120:123]\n\t" \
  "v_mfma_f32_16x16x32_bf16 a[116:119], a[188:191], v[116:119], a[116:119]\n\t" \
  "v_mfma_f32_16x16x32_bf16 a[112:115], a[188:191], v[112:115], a[112:115]\n\t" \
  FSTP("v_writelane_b32 %[stv], s94, 50\n\t") \
  FSTP("v_writelane_b32 %[stv], s92, 51\n\t") \
  FSTP("s_memrealtime s[94:95]\n\t") \
  FSTP("s_memtime s[92:93]\n\t") \
  FSTS("s_memrealtime s[94:95]\n\t") \
  FSTS("s_waitcnt lgkmcnt(0)\n\t") \
  FSTS("v_writelane_b32 %[stv], s94, 14\n\t") \
  "s_waitcnt vmcnt(25) lgkmcnt(0)\n\t" \
  "v_mfma_f32_16x16x32_bf16 a[0:3], a[128:131], a[192:195], a[0:3]\n\t" \
  "s_add_u32 s96, %[rot19], 4194304\n\t" \
  "ds_read_b64_tr_b16 a[160:161], v184 offset:57344\n\t" \
  "v_mfma_f32_16x16x32_bf16 a[4:7], a[128:131], a[196:199], a[4:7]\n\t" \
  "s_and_b32 s96, s96, 0x780000\n\t" \
  "v_mfma_f32_16x16x32_bf16 a[8:11], a[128:131], a[200:203], a[8:11]\n\t" \
  "s_add_u32 m0, %[mw], 0\n\t" \
  "ds_read_b64_tr_b16 a[162:163], v184 offset:58368\n\t" \
  "v_mfma_f32_16x16x32_bf16 a[12:15], a[128:131], a[204:207], a[12:15]\n\t" \
  "buffer_load_dwordx4 v200, %[sw], s96 offen offset:0 lds\n\t" \
  "v_mfma_f32_16x16x32_bf16 a[28:31], a[132:135], a[204:207], a[28:31]\n\t" \
  "ds_read_b64_tr_b16 a[164:165], v185 offset:57344\n\t" \
  "v_mfma_f32_16x16x32_bf16 a[24:27], a[132:135], a[200:203], a[24:27]\n\t" \
  "ds_read_b64_tr_b16 a[166:167], v185 offset:58368\n\t" \
  "v_mfma_f32_16x16x32_bf16 a[20:23], a[132:135], a[196:199], a[20:23]\n\t" \
  "ds_read_b64_tr_b16 a[168:169], v186 offset:57344\n\t" \
  "v_mfma_f32_16x16x32_bf16 a[16:19], a[132:135], a[192:195], a[16:19]\n\t" \
  "buffer_load_dwordx4 v201, %[sw], s96 offen offset:1024 lds\n\t" \
  "v_mfma_f32_16x16x32_bf16 a[32:35], a[136:139], a[192:195], a[32:35]\n\t" \
  "ds_read_b64_tr_b16 a[170:171], v186 offset:58368\n\t" \
  "v_accvgpr_read_b32 v0, a0\n\t" \
  "v_accvgpr_read_b32 v1, a1\n\t" \
  "v_mfma_f32_16x16x32_bf16 a[36:39], a[136:139], a[196:199], a[36:39]\n\t" \
  "ds_read_b64_tr_b16 a[172:173], v187 offset:57344\n\t" \
  "v_accvgpr_read_b32 v2, a2\n\t" \
  "v_mfma_f32_16x16x32_bf16 a[40:43], a[136:139], a[200:203], a[40:43]\n\t" \
  "v_accvgpr_read_b32 v3, a3\n\t" \
  "v_accvgpr_read_b32 v32, a4\n\t" \
  "v_mfma_f32_16x16x32_bf16 a[44:47], a[136:139], a[204:207], a[44:47]\n\t" \
  "ds_read_b64_tr_b16 a[174:175], v187 offset:58368\n\t" \
  "v_accvgpr_read_b32 v33, a5\n\t" \
  "v_mfma_f32_16x16x32_bf16 a[60:63], a[140:143], a[204:207], a[60:63]\n\t" \
  "ds_read_b64_tr_b16 a[176:177], v188 offset:57344\n\t" \
  "v_accvgpr_read_b32 v34, a6\n\t" \
  "v_mfma_f32_16x16x32_bf16 a[56:59], a[140:143], a[200:203], a[56:59]\n\t" \
  "v_accvgpr_read_b32 v35, a7\n\t" \
  "v_accvgpr_read_b32 v64, a8\n\t" \
  "v_mfma_f32_16x16x32_bf16 a[52:55], a[140:143], a[196:199], a[52:55]\n\t" \
  "buffer_load_dwordx4 v202, %[sw], s96 offen offset:2048 lds\n\t" \
  "v_mfma_f32_16x16x32_bf16 a[48:51], a[140:143], a[192:195], a[48:51]\n\t" \
  "ds_read_b64_tr_b16 a[178:179], v188 offset:58368\n\t" \
  "v_accvgpr_read_b32 v65, a9\n\t" \
  "v_mfma_f32_16x16x32_bf16 a[64:67], a[144:147], a[192:195], a[64:67]\n\t" \
  "ds_read_b64_tr_b16 a[180:181], v189 offset:57344\n\t" \
  "v_accvgpr_read_b32 v66, a10\n\t" \
  "v_mfma_f32_16x16x32_bf16 a[68:71], a[144:147], a[196:199], a[68:71]\n\t" \
  "ds_read_b64_tr_b16 a[182:183], v189 offset:58368\n\t" \
  "v_accvgpr_read_b32 v67, a11\n\t" \
  "v_mfma_f32_16x16x32_bf16 a[72:75], a[144:147], a[200:203], a[72:75]\n\t" \
  "ds_read_b64_tr_b16 a[184:185], v190 offset:57344\n\t" \
  "v_accvgpr_read_b32 v96, a12\n\t" \
  "v_mfma_f32_16x16x32_bf16 a[76:79], a[144:147], a[204:207], a[76:79]\n\t" \
  "v_accvgpr_read_b32 v97, a13\n\t" \
  "v_accvgpr_read_b32 v98, a14\n\t" \
  "v_mfma_f32_16x16x32_bf16 a[92:95], a[148:151], a[204:207], a[92:95]\n\t" \
  "ds_read_b64_tr_b16 a[186:187], v190 offset:58368\n\t" \
  "v_accvgpr_read_b32 v99, a15\n\t" \
  "v_mfma_f32_16x16x32_bf16 a[88:91], a[148:151], a[200:203], a[88:91]\n\t" \
  "buffer_load_dwordx4 v203, %[sw], s96 offen offset:3072 lds\n\t" \
  "v_mfma_f32_16x16x32_bf16 a[84:87], a[148:151], a[196:199], a[84:87]\n\t" \
  "ds_read_b64_tr_b16 a[188:189], v191 offset:57344\n\t" \
  "v_accvgpr_read_b32 v100, a28\n\t" \
  "v_mfma_f32_16x16x32_bf16 a[80:83], a[148:151], a[192:195], a[80:83]\n\t" \
  "ds_read_b64_tr_b16 a[190:191], v191 offset:58368\n\t" \
  "v_accvgpr_read_b32 v101, a29\n\t" \
  "v_mfma_f32_16x16x32_bf16 a[96:99], a[152:155], a[192:195], a[96:99]\n\t" \
  "v_accvgpr_read_b32 v102, a30\n\t" \
  "v_accvgpr_read_b32 v103, a31\n\t" \
  "v_mfma_f32_16x16x32_bf16 a[100:103], a[152:155], a[196:199], a[100:103]\n\t" \
  "v_accvgpr_read_b32 v68, a24\n\t" \
  "v_accvgpr_read_b32 v69, a25\n\t" \
  "v_mfma_f32_16x16x32_bf16 a[104:107], a[152:155], a[200:203], a[104:107]\n\t" \
  "v_accvgpr_read_b32 v70, a26\n\t" \
  "v_accvgpr_read_b32 v71, a27\n\t" \
  "v_mfma_f32_16x16x32_bf16 a[108:111], a[152:155], a[204:207], a[108:111]\n\t" \
  "v_accvgpr_read_b32 v36, a20\n\t" \
  "v_accvgpr_read_b32 v37, a21\n\t" \
  "v_mfma_f32_16x16x32_bf16 a[124:127], a[156:159], a[204:207], a[124:127]\n\t" \
  "v_accvgpr_read_b32 v38, a22\n\t" \
  "v_accvgpr_read_b32 v39, a23\n\t" \
  "v_mfma_f32_16x16x32_bf16 a[120:123], a[156:159], a[200:203], a[120:123]\n\t" \
  "v_accvgpr_read_b32 v4, a16\n\t" \
  "v_accvgpr_read_b32 v5, a17\n\t" \
  "v_mfma_f32_16x16x32_bf16 a[116:119], a[156:159], a[196:199], a[116:119]\n\t" \
  "v_accvgpr_read_b32 v6, a18\n\t" \
  "v_accvgpr_read_b32 v7, a19\n\t" \
  "v_mfma_f32_16x16x32_bf16 a[112:115], a[156:159], a[192:195], a[112:115]\n\t" \
  "s_waitcnt vmcnt(21) lgkmcnt(0)\n\t" \
  "s_barrier\n\t" \
  "v_mfma_f32_16x16x32_bf16 a[32:35], a[168:171], a[208:211], a[32:35]\n\t" \
  "s_add_u32 s97, %[rot19], 3145728\n\t" \
  "ds_read_b64_tr_b16 a[128:129], v192 offset:0\n\t" \
  "v_mfma_f32_16x16x32_bf16 a[36:39], a[168:171], a[212:215], a[36:39]\n\t" \
  "s_bfe_u32 s97, s97, 0x140003\n\t" \
  "v_mfma_f32_16x16x32_bf16 a[40:43], a[168:171], a[216:219], a[40:43]\n\t" \
  "s_add_u32 s97, s97, %[sox]\n\t" \
  "ds_read_b64_tr_b16 a[130:131], v192 offset:1024\n\t" \
  "v_mfma_f32_16x16x32_bf16 a[44:47], a[168:171], a[220:223], a[44:47]\n\t" \
  "buffer_load_dwordx4 a[192:195], v248, %[ws], s97 offen sc1\n\t" \
  "v_mfma_f32_16x16x32_bf16 a[60:63], a[172:175], a[220:223], a[60:63]\n\t" \
  "buffer_load_dwordx4 a[196:199], v249, %[ws], s97 offen sc1\n\t" \
  "v_mfma_f32_16x16x32_bf16 a[56:59], a[172:175], a[216:219], a[56:59]\n\t" \
  "ds_read_b64_tr_b16 a[132:133], v193 offset:0\n\t" \
  "v_mfma_f32_16x16x32_bf16 a[52:55], a[172:175], a[212:215], a[52:55]\n\t" \
  "ds_read_b64_tr_b16 a[134:135], v193 offset:1024\n\t" \
  "v_mfma_f32_16x16x32_bf16 a[48:51], a[172:175], a[208:211], a[48:51]\n\t" \
  "buffer_load_dwordx4 a[200:203], v250, %[ws], s97 offen sc1\n\t" \
  "v_mfma_f32_16x16x32_bf16 a[64:67], a[176:179], a[208:211], a[64:67]\n\t" \
  "ds_read_b64_tr_b16 a[136:137], v194 offset:0\n\t" \
  "v_accvgpr_read_b32 v8, a32\n\t" \
  "v_accvgpr_read_b32 v9, a33\n\t" \
  "v_mfma_f32_16x16x32_bf16 a[68:71], a[176:179], a[212:215], a[68:71]\n\t" \
  "ds_read_b64_tr_b16 a[138:139], v194 offset:1024\n\t" \
  "v_accvgpr_read_b32 v10, a34\n\t" \
  "v_accvgpr_read_b32 v11, a35\n\t" \
  "v_mfma_f32_16x16x32_bf16 a[72:75], a[176:179], a[216:219], a[72:75]\n\t" \
  "ds_read_b64_tr_b16 a[140:141], v195 offset:0\n\t" \
  "v_accvgpr_read_b32 v40, a36\n\t" \
  "v_accvgpr_read_b32 v41, a37\n\t" \
  "v_mfma_f32_16x16x32_bf16 a[76:79], a[176:179], a[220:223], a[76:79]\n\t" \
  "buffer_load_dwordx4 a[204:207], v251, %[ws], s97 offen sc1\n\t" \
  "v_accvgpr_read_b32 v42, a38\n\t" \
  "v_mfma_f32_16x16x32_bf16 a[92:95], a[180:183], a[220:223], a[92:95]\n\t" \
  "s_add_u32 s96, %[rot19], 4718592\n\t" \
  "ds_read_b64_tr_b16 a[142:143], v195 offset:1024\n\t" \
  "v_accvgpr_read_b32 v43, a39\n\t" \
  "v_accvgpr_read_b32 v72, a40\n\t" \
  "v_mfma_f32_16x16x32_bf16 a[88:91], a[180:183], a[216:219], a[88:91]\n\t" \
  "s_and_b32 s96, s96, 0x780000\n\t" \
  "ds_read_b64_tr_b16 a[144:145], v196 offset:0\n\t" \
  "v_accvgpr_read_b32 v73, a41\n\t" \
  "v_accvgpr_read_b32 v74, a42\n\t" \
  ".Lhs2:\n\ts_cbranch_execz .Lhs2n\n\t" \
  "v_mfma_f32_16x16x32_bf16 a[84:87], a[180:183], a[212:215], a[84:87]\n\t" \
  "s_add_u32 m0, %[mw], 49152\n\t" \
  "ds_read_b64_tr_b16 a[146:147], v196 offset:1024\n\t" \
  "v_accvgpr_read_b32 v75, a43\n\t" \
  "v_accvgpr_read_b32 v104, a44\n\t" \
  "v_mfma_f32_16x16x32_bf16 a[80:83], a[180:183], a[208:211], a[80:83]\n\t" \
  "buffer_load_dwordx4 v200, %[sw], s96 offen offset:0 lds\n\t" \
  "v_accvgpr_read_b32 v105, a45\n\t" \
  "v_mfma_f32_16x16x32_bf16 a[96:99], a[184:187], a[208:211], a[96:99]\n\t" \
  "ds_read_b64_tr_b16 a[148:149], v197 offset:0\n\t" \
  "v_accvgpr_read_b32 v106, a46\n\t" \
  "v_mfma_f32_16x16x32_bf16 a[100:103], a[184:187], a[212:215], a[100:103]\n\t" \
  "ds_read_b64_tr_b16 a[150:151], v197 offset:1024\n\t" \
  "v_accvgpr_read_b32 v107, a47\n\t" \
  "v_mfma_f32_16x16x32_bf16 a[104:107], a[184:187], a[216:219], a[104:107]\n\t" \
  "ds_read_b64_tr_b16 a[152:153], v198 offset:0\n\t" \
  "v_accvgpr_read_b32 v108, a60\n\t" \
  "v_mfma_f32_16x16x32_bf16 a[108:111], a[184:187], a[220:223], a[108:111]\n\t" \
  "buffer_load_dwordx4 v201, %[sw], s96 offen offset:1024 lds\n\t" \
  "v_mfma_f32_16x16x32_bf16 a[124:127], a[188:191], a[220:223], a[124:127]\n\t" \
  "ds_read_b64_tr_b16 a[154:155], v198 offset:1024\n\t" \
  "v_accvgpr_read_b32 v109, a61\n\t" \
  "v_mfma_f32_16x16x32_bf16 a[120:123], a[188:191], a[216:219], a[120:123]\n\t" \
  "ds_read_b64_tr_b16 a[156:157], v199 offset:0\n\t" \
  "v_accvgpr_read_b32 v110, a62\n\t" \
  "v_mfma_f32_16x16x32_bf16 a[116:119], a[188:191], a[212:215], a[116:119]\n\t" \
  "v_accvgpr_read_b32 v111, a63\n\t" \
  "v_accvgpr_read_b32 v76, a56\n\t" \
  "v_mfma_f32_16x16x32_bf16 a[112:115], a[188:191], a[208:211], a[112:115]\n\t" \
  "buffer_load_dwordx4 v202, %[sw], s96 offen offset:2048 lds\n\t" \
  "v_mfma_f32_16x16x32_bf16 v[0:3], a[160:163], a[208:211], v[0:3]\n\t" \
  "ds_read_b64_tr_b16 a[158:159], v199 offset:1024\n\t" \
  "v_accvgpr_read_b32 v77, a57\n\t" \
  "v_mfma_f32_16x16x32_bf16 v[32:35], a[160:163], a[212:215], v[32:35]\n\t" \
  "v_accvgpr_read_b32 v78, a58\n\t" \
  "v_accvgpr_read_b32 v79, a59\n\t" \
  "v_mfma_f32_16x16x32_bf16 v[64:67], a[160:163], a[216:219], v[64:67]\n\t" \
  "v_accvgpr_read_b32 v44, a52\n\t" \
  "v_accvgpr_read_b32 v45, a53\n\t" \
  "v_mfma_f32_16x16x32_bf16 v[96:99], a[160:163], a[220:223], v[96:99]\n\t" \
  "buffer_load_dwordx4 v203, %[sw], s96 offen offset:3072 lds\n\t" \
  "v_mfma_f32_16x16x32_bf16 v[100:103], a[164:167], a[220:223], v[100:103]\n\t" \
  "v_accvgpr_read_b32 v46, a54\n\t" \
  "v_accvgpr_read_b32 v47, a55\n\t" \
  "v_mfma_f32_16x16x32_bf16 v[68:71], a[164:167], a[216:219], v[68:71]\n\t" \
  "v_accvgpr_read_b32 v12, a48\n\t" \
  "v_accvgpr_read_b32 v13, a49\n\t" \
  "v_mfma_f32_16x16x32_bf16 v[36:39], a[164:167], a[212:215], v[36:39]\n\t" \
  "v_accvgpr_read_b32 v14, a50\n\t" \
  "v_accvgpr_read_b32 v15, a51\n\t" \
  "v_mfma_f32_16x16x32_bf16 v[4:7], a[164:167], a[208:211], v[4:7]\n\t" \
  "s_waitcnt vmcnt(25) lgkmcnt(0)\n\t" \
  "v_mfma_f32_16x16x32_bf16 a[64:67], a[144:147], a[224:227], a[64:67]\n\t" \
  "ds_read_b64_tr_b16 a[160:161], v192 offset:8192\n\t" \
  "v_add_u32 v216, 3145728, v216\n\t" \
  "v_mfma_f32_16x16x32_bf16 a[68:71], a[144:147], a[228:231], a[68:71]\n\t" \
  "v_and_b32 v216, 0x7fffff, v216\n\t" \
  "v_mfma_f32_16x16x32_bf16 a[72:75], a[144:147], a[232:235], a[72:75]\n\t" \
  "global_load_dword %[pf], v216, %[wq]\n\t" \
  "v_mfma_f32_16x16x32_bf16 a[76:79], a[144:147], a[236:239], a[76:79]\n\t" \
  "s_add_u32 s97, s97, %[dk]\n\t" \
  "ds_read_b64_tr_b16 a[162:163], v192 offset:9216\n\t" \
  "v_mfma_f32_16x16x32_bf16 a[92:95], a[148:151], a[236:239], a[92:95]\n\t" \
  "ds_read_b64_tr_b16 a[164:165], v193 offset:8192\n\t" \
  "v_mfma_f32_16x16x32_bf16 a[88:91], a[148:151], a[232:235], a[88:91]\n\t" \
  "buffer_load_dwordx4 a[208:211], v248, %[ws], s97 offen sc1\n\t" \
  "v_mfma_f32_16x16x32_bf16 a[84:87], a[148:151], a[228:231], a[84:87]\n\t" \
  "ds_read_b64_tr_b16 a[166:167], v193 offset:9216\n\t" \
  "v_mfma_f32_16x16x32_bf16 a[80:83], a[148:151], a[224:227], a[80:83]\n\t" \
  "ds_read_b64_tr_b16 a[168:169], v194 offset:8192\n\t" \
  "v_mfma_f32_16x16x32_bf16 a[96:99], a[152:155], a[224:227], a[96:99]\n\t" \
  "ds_read_b64_tr_b16 a[170:171], v194 offset:9216\n\t" \
  "v_accvgpr_read_b32 v16, a64\n\t" \
  "v_accvgpr_read_b32 v17, a65\n\t" \
  "v_mfma_f32_16x16x32_bf16 a[100:103], a[152:155], a[228:231], a[100:103]\n\t" \
  "ds_read_b64_tr_b16 a[172:173], v195 offset:8192\n\t" \
  "v_accvgpr_read_b32 v18, a66\n\t" \
  "v_accvgpr_read_b32 v19, a67\n\t" \
  "v_mfma_f32_16x16x32_bf16 a[104:107], a[152:155], a[232:235], a[104:107]\n\t" \
  "v_accvgpr_read_b32 v48, a68\n\t" \
  "v_accvgpr_read_b32 v49, a69\n\t" \
  "v_accvgpr_read_b32 v50, a70\n\t" \
  "v_mfma_f32_16x16x32_bf16 a[108:111], a[152:155], a[236:239], a[108:111]\n\t" \
  "buffer_load_dwordx4 a[212:215], v249, %[ws], s97 offen sc1\n\t" \
  "v_mfma_f32_16x16x32_bf16 a[124:127], a[156:159], a[236:239], a[124:127]\n\t" \
  "ds_read_b64_tr_b16 a[174:175], v195 offset:9216\n\t" \
  "v_accvgpr_read_b32 v51, a71\n\t" \
  "v_mfma_f32_16x16x32_bf16 a[120:123], a[156:159], a[232:235], a[120:123]\n\t" \
  "ds_read_b64_tr_b16 a[176:177], v196 offset:8192\n\t" \
  "v_accvgpr_read_b32 v80, a72\n\t" \
  "v_mfma_f32_16x16x32_bf16 a[116:119], a[156:159], a[228:231], a[116:119]\n\t" \
  "ds_read_b64_tr_b16 a[178:179], v196 offset:9216\n\t" \
  "v_accvgpr_read_b32 v81, a73\n\t" \
  "v_mfma_f32_16x16x32_bf16 a[112:115], a[156:159], a[224:227], a[112:115]\n\t" \
  "ds_read_b64_tr_b16 a[180:181], v197 offset:8192\n\t" \
  "v_accvgpr_read_b32 v82, a74\n\t" \
  "v_mfma_f32_16x16x32_bf16 v[0:3], a[128:131], a[224:227], v[0:3]\n\t" \
  "buffer_load_dwordx4 a[216:219], v250, %[ws], s97 offen sc1\n\t" \
  "v_mfma_f32_16x16x32_bf16 v[32:35], a[128:131], a[228:231], v[32:35]\n\t" \
  "ds_read_b64_tr_b16 a[182:183], v197 offset:9216\n\t" \
  "v_accvgpr_read_b32 v83, a75\n\t" \
  "v_mfma_f32_16x16x32_bf16 v[64:67], a[128:131], a[232:235], v[64:67]\n\t" \
  "ds_read_b64_tr_b16 a[184:185], v198 offset:8192\n\t" \
  "v_accvgpr_read_b32 v112, a76\n\t" \
  "v_mfma_f32_16x16x32_bf16 v[96:99], a[128:131], a[236:239], v[96:99]\n\t" \
  "v_accvgpr_read_b32 v113, a77\n\t" \
  "v_accvgpr_read_b32 v114, a78\n\t" \
  "v_mfma_f32_16x16x32_bf16 v[100:103], a[132:135], a[236:239], v[100:103]\n\t" \
  "ds_read_b64_tr_b16 a[186:187], v198 offset:9216\n\t" \
  "v_accvgpr_read_b32 v115, a79\n\t" \
  "v_mfma_f32_16x16x32_bf16 v[68:71], a[132:135], a[232:235], v[68:71]\n\t" \
  "ds_read_b64_tr_b16 a[188:189], v199 offset:8192\n\t" \
  "v_accvgpr_read_b32 v116, a92\n\t" \
  "v_mfma_f32_16x16x32_bf16 v[36:39], a[132:135], a[228:231], v[36:39]\n\t" \
  "buffer_load_dwordx4 a[220:223], v251, %[ws], s97 offen sc1\n\t" \
  "v_mfma_f32_16x16x32_bf16 v[4:7], a[132:135], a[224:227], v[4:7]\n\t" \
  "ds_read_b64_tr_b16 a[190:191], v199 offset:9216\n\t" \
  "v_accvgpr_read_b32 v117, a93\n\t" \
  "v_mfma_f32_16x16x32_bf16 v[8:11], a[136:139], a[224:227], v[8:11]\n\t" \
  "v_accvgpr_read_b32 v118, a94\n\t" \
  "v_accvgpr_read_b32 v119, a95\n\t" \
  "v_mfma_f32_16x16x32_bf16 v[40:43], a[136:139], a[228:231], v[40:43]\n\t" \
  "v_accvgpr_read_b32 v84, a88\n\t" \
  "v_accvgpr_read_b32 v85, a89\n\t" \
  "v_mfma_f32_16x16x32_bf16 v[72:75], a[136:139], a[232:235], v[72:75]\n\t" \
  "v_accvgpr_read_b32 v86, a90\n\t" \
  "v_accvgpr_read_b32 v87, a91\n\t" \
  "v_mfma_f32_16x16x32_bf16 v[104:107], a[136:139], a[236:239], v[104:107]\n\t" \
  "v_accvgpr_read_b32 v52, a84\n\t" \
  "v_accvgpr_read_b32 v53, a85\n\t" \
  "v_mfma_f32_16x16x32_bf16 v[108:111], a[140:143], a[236:239], v[108:111]\n\t" \
  "v_accvgpr_read_b32 v54, a86\n\t" \
  "v_accvgpr_read_b32 v55, a87\n\t" \
  "v_mfma_f32_16x16x32_bf16 v[76:79], a[140:143], a[232:235], v[76:79]\n\t" \
  "v_accvgpr_read_b32 v20, a80\n\t" \
  "v_accvgpr_read_b32 v21, a81\n\t" \
  "v_mfma_f32_16x16x32_bf16 v[44:47], a[140:143], a[228:231], v[44:47]\n\t" \
  "v_accvgpr_read_b32 v22, a82\n\t" \
  "v_accvgpr_read_b32 v23, a83\n\t" \
  "v_mfma_f32_16x16x32_bf16 v[12:15], a[140:143], a[224:227], v[12:15]\n\t" \
  "s_waitcnt vmcnt(22) lgkmcnt(0)\n\t" \
  "s_barrier\n\t" \
  "v_mfma_f32_16x16x32_bf16 a[96:99], a[184:187], a[240:243], a[96:99]\n\t" \
  "s_add_u32 s97, %[rot19], 3670016\n\t" \
  "ds_read_b64_tr_b16 a[128:129], v184 offset:16384\n\t" \
  "v_mfma_f32_16x16x32_bf16 a[100:103], a[184:187], a[244:247], a[100:103]\n\t" \
  "s_bfe_u32 s97, s97, 0x140003\n\t" \
  "v_mfma_f32_16x16x32_bf16 a[104:107], a[184:187], a[248:251], a[104:107]\n\t" \
  "s_add_u32 s97, s97, %[sox]\n\t" \
  "ds_read_b64_tr_b16 a[130:131], v184 offset:17408\n\t" \
  "v_mfma_f32_16x16x32_bf16 a[108:111], a[184:187], a[252:255], a[108:111]\n\t" \
  "buffer_load_dwordx4 a[224:227], v248, %[ws], s97 offen sc1\n\t" \
  "v_mfma_f32_16x16x32_bf16 a[124:127], a[188:191], a[252:255], a[124:127]\n\t" \
  "buffer_load_dwordx4 a[228:231], v249, %[ws], s97 offen sc1\n\t" \
  "v_mfma_f32_16x16x32_bf16 a[120:123], a[188:191], a[248:251], a[120:123]\n\t" \
  "ds_read_b64_tr_b16 a[132:133], v185 offset:16384\n\t" \
  "v_mfma_f32_16x16x32_bf16 a[116:119], a[188:191], a[244:247], a[116:119]\n\t" \
  "ds_read_b64_tr_b16 a[134:135], v185 offset:17408\n\t" \
  "v_mfma_f32_16x16x32_bf16 a[112:115], a[188:191], a[240:243], a[112:115]\n\t" \
  "buffer_load_dwordx4 a[232:235], v250, %[ws], s97 offen sc1\n\t" \
  "v_mfma_f32_16x16x32_bf16 v[0:3], a[160:163], a[240:243], v[0:3]\n\t" \
  "ds_read_b64_tr_b16 a[136:137], v186 offset:16384\n\t" \
  "v_accvgpr_read_b32 v24, a96\n\t" \
  "v_accvgpr_read_b32 v25, a97\n\t" \
  "v_mfma_f32_16x16x32_bf16 v[32:35], a[160:163], a[244:247], v[32:35]\n\t" \
  "ds_read_b64_tr_b16 a[138:139], v186 offset:17408\n\t" \
  "v_accvgpr_read_b32 v26, a98\n\t" \
  "v_accvgpr_read_b32 v27, a99\n\t" \
  "v_mfma_f32_16x16x32_bf16 v[64:67], a[160:163], a[248:251], v[64:67]\n\t" \
  "ds_read_b64_tr_b16 a[140:141], v187 offset:16384\n\t" \
  "v_accvgpr_read_b32 v56, a100\n\t" \
  "v_accvgpr_read_b32 v57, a101\n\t" \
  "v_mfma_f32_16x16x32_bf16 v[96:99], a[160:163], a[252:255], v[96:99]\n\t" \
  "buffer_load_dwordx4 a[236:239], v251, %[ws], s97 offen sc1\n\t" \
  "v_accvgpr_read_b32 v58, a102\n\t" \
  "v_mfma_f32_16x16x32_bf16 v[100:103], a[164:167], a[252:255], v[100:103]\n\t" \
  "s_add_u32 s96, %[rot19], 5242880\n\t" \
  "ds_read_b64_tr_b16 a[142:143], v187 offset:17408\n\t" \
  "v_accvgpr_read_b32 v59, a103\n\t" \
  "v_accvgpr_read_b32 v88, a104\n\t" \
  "v_mfma_f32_16x16x32_bf16 v[68:71], a[164:167], a[248:251], v[68:71]\n\t" \
  "s_and_b32 s96, s96, 0x780000\n\t" \
  "ds_read_b64_tr_b16 a[144:145], v188 offset:16384\n\t" \
  "v_accvgpr_read_b32 v89, a105\n\t" \
  "v_accvgpr_read_b32 v90, a106\n\t" \
  "v_mfma_f32_16x16x32_bf16 v[36:39], a[164:167], a[244:247], v[36:39]\n\t" \
  "s_add_u32 m0, %[mw], 98304\n\t" \
  "ds_read_b64_tr_b16 a[146:147], v188 offset:17408\n\t" \
  "v_accvgpr_read_b32 v91, a107\n\t" \
  "v_accvgpr_read_b32 v120, a108\n\t" \
  "v_mfma_f32_16x16x32_bf16 v[4:7], a[164:167], a[240:243], v[4:7]\n\t" \
  "buffer_load_dwordx4 v200, %[sw], s96 offen offset:0 lds\n\t" \
  "v_accvgpr_read_b32 v121, a109\n\t" \
  "v_mfma_f32_16x16x32_bf16 v[8:11], a[168:171], a[240:243], v[8:11]\n\t" \
  "ds_read_b64_tr_b16 a[148:149], v189 offset:16384\n\t" \
  "v_accvgpr_read_b32 v122, a110\n\t" \
  "v_mfma_f32_16x16x32_bf16 v[40:43], a[168:171], a[244:247], v[40:43]\n\t" \
  "ds_read_b64_tr_b16 a[150:151], v189 offset:17408\n\t" \
  "v_accvgpr_read_b32 v123, a111\n\t" \
  "v_mfma_f32_16x16x32_bf16 v[72:75], a[168:171], a[248:251], v[72:75]\n\t" \
  "ds_read_b64_tr_b16 a[152:153], v190 offset:16384\n\t" \
  "v_accvgpr_read_b32 v124, a124\n\t" \
  "v_mfma_f32_16x16x32_bf16 v[104:107], a[168:171], a[252:255], v[104:107]\n\t" \
  "buffer_load_dwordx4 v201, %[sw], s96 offen offset:1024 lds\n\t" \
  "v_mfma_f32_16x16x32_bf16 v[108:111], a[172:175], a[252:255], v[108:111]\n\t" \
  "ds_read_b64_tr_b16 a[154:155], v190 offset:17408\n\t" \
  "v_accvgpr_read_b32 v125, a125\n\t" \
  "v_mfma_f32_16x16x32_bf16 v[76:79], a[172:175], a[248:251], v[76:79]\n\t" \
  "ds_read_b64_tr_b16 a[156:157], v191 offset:16384\n\t" \
  "v_accvgpr_read_b32 v126, a126\n\t" \
  "v_mfma_f32_16x16x32_bf16 v[44:47], a[172:175], a[244:247], v[44:47]\n\t" \
  "v_accvgpr_read_b32 v127, a127\n\t" \
  "v_accvgpr_read_b32 v92, a120\n\t" \
  "v_mfma_f32_16x16x32_bf16 v[12:15], a[172:175], a[240:243], v[12:15]\n\t" \
  "buffer_load_dwordx4 v202, %[sw], s96 offen offset:2048 lds\n\t" \
  "v_mfma_f32_16x16x32_bf16 v[16:19], a[176:179], a[240:243], v[16:19]\n\t" \
  "ds_read_b64_tr_b16 a[158:159], v191 offset:17408\n\t" \
  "v_accvgpr_read_b32 v93, a121\n\t" \
  "v_mfma_f32_16x16x32_bf16 v[48:51], a[176:179], a[244:247], v[48:51]\n\t" \
  "v_accvgpr_read_b32 v94, a122\n\t" \
  "v_accvgpr_read_b32 v95, a123\n\t" \
  "v_mfma_f32_16x16x32_bf16 v[80:83], a[176:179], a[248:251], v[80:83]\n\t" \
  "v_accvgpr_read_b32 v60, a116\n\t" \
  "v_accvgpr_read_b32 v61, a117\n\t" \
  "v_mfma_f32_16x16x32_bf16 v[112:115], a[176:179], a[252:255], v[112:115]\n\t" \
  "buffer_load_dwordx4 v203, %[sw], s96 offen offset:3072 lds\n\t" \
  "v_mfma_f32_16x16x32_bf16 v[116:119], a[180:183], a[252:255], v[116:119]\n\t" \
  "v_accvgpr_read_b32 v62, a118\n\t" \
  "v_accvgpr_read_b32 v63, a119\n\t" \
  "v_mfma_f32_16x16x32_bf16 v[84:87], a[180:183], a[248:251], v[84:87]\n\t" \
  "v_accvgpr_read_b32 v28, a112\n\t" \
  "v_accvgpr_read_b32 v29, a113\n\t" \
  "v_mfma_f32_16x16x32_bf16 v[52:55], a[180:183], a[244:247], v[52:55]\n\t" \
  "v_accvgpr_read_b32 v30, a114\n\t" \
  "v_accvgpr_read_b32 v31, a115\n\t" \
  "v_mfma_f32_16x16x32_bf16 v[20:23], a[180:183], a[240:243], v[20:23]\n\t" \
  "s_waitcnt vmcnt(17) lgkmcnt(0)\n\t" \
  "v_mfma_f32_16x16x32_bf16 v[0:3], a[128:131], a[192:195], v[0:3]\n\t" \
  "s_add_u32 s97, s97, %[dk]\n\t" \
  "ds_read_b64_tr_b16 a[160:161], v184 offset:24576\n\t" \
  "v_mfma_f32_16x16x32_bf16 v[32:35], a[128:131], a[196:199], v[32:35]\n\t" \
  "buffer_load_dwordx4 a[240:243], v248, %[ws], s97 offen sc1\n\t" \
  "v_mfma_f32_16x16x32_bf16 v[64:67], a[128:131], a[200:203], v[64:67]\n\t" \
  "ds_read_b64_tr_b16 a[162:163], v184 offset:25600\n\t" \
  "v_mfma_f32_16x16x32_bf16 v[96:99], a[128:131], a[204:207], v[96:99]\n\t" \
  "buffer_load_dwordx4 a[244:247], v249, %[ws], s97 offen sc1\n\t" \
  "v_mfma_f32_16x16x32_bf16 v[100:103], a[132:135], a[204:207], v[100:103]\n\t" \
  "ds_read_b64_tr_b16 a[164:165], v185 offset:24576\n\t" \
  "v_mfma_f32_16x16x32_bf16 v[68:71], a[132:135], a[200:203], v[68:71]\n\t" \
  "ds_read_b64_tr_b16 a[166:167], v185 offset:25600\n\t" \
  "v_mfma_f32_16x16x32_bf16 v[36:39], a[132:135], a[196:199], v[36:39]\n\t" \
  "ds_read_b64_tr_b16 a[168:169], v186 offset:24576\n\t" \
  "v_mfma_f32_16x16x32_bf16 v[4:7], a[132:135], a[192:195], v[4:7]\n\t" \
  "buffer_load_dwordx4 a[248:251], v250, %[ws], s97 offen sc1\n\t" \
  "v_mfma_f32_16x16x32_bf16 v[8:11], a[136:139], a[192:195], v[8:11]\n\t" \
  "ds_read_b64_tr_b16 a[170:171], v186 offset:25600\n\t" \
  "v_mfma_f32_16x16x32_bf16 v[40:43], a[136:139], a[196:199], v[40:43]\n\t" \
  "ds_read_b64_tr_b16 a[172:173], v187 offset:24576\n\t" \
  "v_mfma_f32_16x16x32_bf16 v[72:75], a[136:139], a[200:203], v[72:75]\n\t" \
  "v_mfma_f32_16x16x32_bf16 v[104:107], a[136:139], a[204:207], v[104:107]\n\t" \
  "buffer_load_dwordx4 a[252:255], v251, %[ws], s97 offen sc1\n\t" \
  "v_mfma_f32_16x16x32_bf16 v[108:111], a[140:143], a[204:207], v[108:111]\n\t" \
  "s_add_u32 s97, %[rot19], 4194304\n\t" \
  "ds_read_b64_tr_b16 a[174:175], v187 offset:25600\n\t" \
  "v_mfma_f32_16x16x32_bf16 v[76:79], a[140:143], a[200:203], v[76:79]\n\t" \
  "s_bfe_u32 s97, s97, 0x140003\n\t" \
  "ds_read_b64_tr_b16 a[176:177], v188 offset:24576\n\t" \
  "v_mfma_f32_16x16x32_bf16 v[44:47], a[140:143], a[196:199], v[44:47]\n\t" \
  "s_add_u32 s97, s97, %[sox]\n\t" \
  "ds_read_b64_tr_b16 a[178:179], v188 offset:25600\n\t" \
  "v_mfma_f32_16x16x32_bf16 v[12:15], a[140:143], a[192:195], v[12:15]\n\t" \
  "buffer_load_dwordx4 a[32:35], v248, %[ws], s97 offen sc1\n\t" \
  "v_mfma_f32_16x16x32_bf16 v[16:19], a[144:147], a[192:195], v[16:19]\n\t" \
  "ds_read_b64_tr_b16 a[180:181], v189 offset:24576\n\t" \
  "v_mfma_f32_16x16x32_bf16 v[48:51], a[144:147], a[196:199], v[48:51]\n\t" \
  "ds_read_b64_tr_b16 a[182:183], v189 offset:25600\n\t" \
  "v_mfma_f32_16x16x32_bf16 v[80:83], a[144:147], a[200:203], v[80:83]\n\t" \
  "ds_read_b64_tr_b16 a[184:185], v190 offset:24576\n\t" \
  "v_mfma_f32_16x16x32_bf16 v[112:115], a[144:147], a[204:207], v[112:115]\n\t" \
  "buffer_load_dwordx4 a[36:39], v249, %[ws], s97 offen sc1\n\t" \
  "v_mfma_f32_16x16x32_bf16 v[116:119], a[148:151], a[204:207], v[116:119]\n\t" \
  "ds_read_b64_tr_b16 a[186:187], v190 offset:25600\n\t" \
  "v_mfma_f32_16x16x32_bf16 v[84:87], a[148:151], a[200:203], v[84:87]\n\t" \
  "ds_read_b64_tr_b16 a[188:189], v191 offset:24576\n\t" \
  "v_mfma_f32_16x16x32_bf16 v[52:55], a[148:151], a[196:199], v[52:55]\n\t" \
  "v_mfma_f32_16x16x32_bf16 v[20:23], a[148:151], a[192:195], v[20:23]\n\t" \
  "buffer_load_dwordx4 a[40:43], v250, %[ws], s97 offen sc1\n\t" \
  "v_mfma_f32_16x16x32_bf16 v[24:27], a[152:155], a[192:195], v[24:27]\n\t" \
  "ds_read_b64_tr_b16 a[190:191], v191 offset:25600\n\t" \
  "v_mfma_f32_16x16x32_bf16 v[56:59], a[152:155], a[196:199], v[56:59]\n\t" \
  "v_mfma_f32_16x16x32_bf16 v[88:91], a[152:155], a[200:203], v[88:91]\n\t" \
  "v_mfma_f32_16x16x32_bf16 v[120:123], a[152:155], a[204:207], v[120:123]\n\t" \
  "buffer_load_dwordx4 a[44:47], v251, %[ws], s97 offen sc1\n\t" \
  "v_mfma_f32_16x16x32_bf16 v[124:127], a[156:159], a[204:207], v[124:127]\n\t" \
  "v_mfma_f32_16x16x32_bf16 v[92:95], a[156:159], a[200:203], v[92:95]\n\t" \
  "v_mfma_f32_16x16x32_bf16 v[60:63], a[156:159], a[196:199], v[60:63]\n\t" \
  "v_mfma_f32_16x16x32_bf16 v[28:31], a[156:159], a[192:195], v[28:31]\n\t" \
  "s_waitcnt vmcnt(16) lgkmcnt(0)\n\t" \
  "s_barrier\n\t" \
  "v_mfma_f32_16x16x32_bf16 v[0:3], a[160:163], a[208:211], v[0:3]\n\t" \
  "s_add_u32 s97, s97, %[dk]\n\t" \
  "ds_read_b64_tr_b16 a[128:129], v184 offset:32768\n\t" \
  "ds_read_b64_tr_b16 a[130:131], v184 offset:33792\n\t" \
  "v_mfma_f32_16x16x32_bf16 v[32:35], a[160:163], a[212:215], v[32:35]\n\t" \
  "buffer_load_dwordx4 a[48:51], v248, %[ws], s97 offen sc1\n\t" \
  "v_mfma_f32_16x16x32_bf16 v[64:67], a[160:163], a[216:219], v[64:67]\n\t" \
  "s_bfe_u32 %[hl], %[bid], 0x30003\n\t" \
  "ds_read_b64_tr_b16 a[132:133], v185 offset:32768\n\t" \
  "ds_read_b64_tr_b16 a[134:135], v185 offset:33792\n\t" \
  "v_mfma_f32_16x16x32_bf16 v[96:99], a[160:163], a[220:223], v[96:99]\n\t" \
  "buffer_load_dwordx4 a[52:55], v249, %[ws], s97 offen sc1\n\t" \
  "v_mfma_f32_16x16x32_bf16 v[100:103], a[164:167], a[220:223], v[100:103]\n\t" \
  "s_lshl_b32 %[hl18], %[hl], 18\n\t" \
  "ds_read_b64_tr_b16 a[136:137], v186 offset:32768\n\t" \
  "ds_read_b64_tr_b16 a[138:139], v186 offset:33792\n\t" \
  "v_mfma_f32_16x16x32_bf16 v[68:71], a[164:167], a[216:219], v[68:71]\n\t" \
  "s_lshr_b32 s98, %[bid], 3\n\t" \
  "ds_read_b64_tr_b16 a[140:141], v187 offset:32768\n\t" \
  "ds_read_b64_tr_b16 a[142:143], v187 offset:33792\n\t" \
  "v_mfma_f32_16x16x32_bf16 v[36:39], a[164:167], a[212:215], v[36:39]\n\t" \
  "s_lshl_b32 %[cto], %[wid], 5\n\t" \
  "ds_read_b64_tr_b16 a[144:145], v188 offset:32768\n\t" \
  "ds_read_b64_tr_b16 a[146:147], v188 offset:33792\n\t" \
  "v_mfma_f32_16x16x32_bf16 v[4:7], a[164:167], a[208:211], v[4:7]\n\t" \
  "buffer_load_dwordx4 a[56:59], v250, %[ws], s97 offen sc1\n\t" \
  "v_mfma_f32_16x16x32_bf16 v[8:11], a[168:171], a[208:211], v[8:11]\n\t" \
  "s_add_u32 %[cto], %[cto], s98\n\t" \
  "ds_read_b64_tr_b16 a[148:149], v189 offset:32768\n\t" \
  "ds_read_b64_tr_b16 a[150:151], v189 offset:33792\n\t" \
  "v_mfma_f32_16x16x32_bf16 v[40:43], a[168:171], a[212:215], v[40:43]\n\t" \
  "s_lshl_b32 %[cto], %[cto], 7\n\t" \
  "ds_read_b64_tr_b16 a[152:153], v190 offset:32768\n\t" \
  "ds_read_b64_tr_b16 a[154:155], v190 offset:33792\n\t" \
  "v_mfma_f32_16x16x32_bf16 v[72:75], a[168:171], a[216:219], v[72:75]\n\t" \
  "s_lshl2_add_u32 %[pf2], s98, %[wid]\n\t" \
  "ds_read_b64_tr_b16 a[156:157], v191 offset:32768\n\t" \
  "ds_read_b64_tr_b16 a[158:159], v191 offset:33792\n\t" \
  "v_mfma_f32_16x16x32_bf16 v[104:107], a[168:171], a[220:223], v[104:107]\n\t" \
  "buffer_load_dwordx4 a[60:63], v251, %[ws], s97 offen sc1\n\t" \
  "v_mfma_f32_16x16x32_bf16 v[108:111], a[172:175], a[220:223], v[108:111]\n\t" \
  "s_add_u32 s97, %[rot19], 4718592\n\t" \
  "ds_read_b64_tr_b16 a[64:65], v184 offset:40960\n\t" \
  "ds_read_b64_tr_b16 a[66:67], v184 offset:41984\n\t" \
  "v_mfma_f32_16x16x32_bf16 v[76:79], a[172:175], a[216:219], v[76:79]\n\t" \
  "s_bfe_u32 s97, s97, 0x140003\n\t" \
  "ds_read_b64_tr_b16 a[68:69], v185 offset:40960\n\t" \
  "ds_read_b64_tr_b16 a[70:71], v185 offset:41984\n\t" \
  "v_mfma_f32_16x16x32_bf16 v[44:47], a[172:175], a[212:215], v[44:47]\n\t" \
  "s_add_u32 s97, s97, %[sox]\n\t" \
  "v_mbcnt_lo_u32_b32 v233, -1, 0\n\t" \
  "v_mfma_f32_16x16x32_bf16 v[12:15], a[172:175], a[208:211], v[12:15]\n\t" \
  "buffer_load_dwordx4 a[96:99], v248, %[ws], s97 offen sc1\n\t" \
  "v_mfma_f32_16x16x32_bf16 v[16:19], a[176:179], a[208:211], v[16:19]\n\t" \
  "s_mul_i32 %[pf2], %[pf2], 98304\n\t" \
  "ds_read_b64_tr_b16 a[72:73], v186 offset:40960\n\t" \
  "ds_read_b64_tr_b16 a[74:75], v186 offset:41984\n\t" \
  "v_mfma_f32_16x16x32_bf16 v[48:51], a[176:179], a[212:215], v[48:51]\n\t" \
  "s_lshr_b32 s98, %[bid], 2\n\t" \
  "ds_read_b64_tr_b16 a[76:77], v187 offset:40960\n\t" \
  "ds_read_b64_tr_b16 a[78:79], v187 offset:41984\n\t" \
  "v_mfma_f32_16x16x32_bf16 v[80:83], a[176:179], a[216:219], v[80:83]\n\t" \
  "s_xor_b32 s98, s98, %[wid]\n\t" \
  "ds_read_b64_tr_b16 a[80:81], v188 offset:40960\n\t" \
  "v_mbcnt_hi_u32_b32 v233, -1, v233\n\t" \
  "v_mfma_f32_16x16x32_bf16 v[112:115], a[176:179], a[220:223], v[112:115]\n\t" \
  "buffer_load_dwordx4 a[100:103], v249, %[ws], s97 offen sc1\n\t" \
  "v_mfma_f32_16x16x32_bf16 v[116:119], a[180:183], a[220:223], v[116:119]\n\t" \
  "s_lshl_b32 s98, s98, 13\n\t" \
  "ds_read_b64_tr_b16 a[82:83], v188 offset:41984\n\t" \
  "ds_read_b64_tr_b16 a[84:85], v189 offset:40960\n\t" \
  "v_mfma_f32_16x16x32_bf16 v[84:87], a[180:183], a[216:219], v[84:87]\n\t" \
  "s_and_b32 %[po], s98, 0x2000\n\t" \
  "ds_read_b64_tr_b16 a[86:87], v189 offset:41984\n\t" \
  "v_lshrrev_b32 v129, 4, v233\n\t" \
  "v_mfma_f32_16x16x32_bf16 v[52:55], a[180:183], a[212:215], v[52:55]\n\t" \
  "s_lshr_b32 s98, %[bid], 6\n\t" \
  "ds_read_b64_tr_b16 a[88:89], v190 offset:40960\n\t" \
  "v_and_b32 v130, 15, v233\n\t" \
  "v_mfma_f32_16x16x32_bf16 v[20:23], a[180:183], a[208:211], v[20:23]\n\t" \
  "buffer_load_dwordx4 a[104:107], v250, %[ws], s97 offen sc1\n\t" \
  "v_mfma_f32_16x16x32_bf16 v[24:27], a[184:187], a[208:211], v[24:27]\n\t" \
  "s_lshl_b32 s98, s98, 22\n\t" \
  "ds_read_b64_tr_b16 a[90:91], v190 offset:41984\n\t" \
  "ds_read_b64_tr_b16 a[92:93], v191 offset:40960\n\t" \
  "v_mfma_f32_16x16x32_bf16 v[56:59], a[184:187], a[212:215], v[56:59]\n\t" \
  "s_add_u32 %[po], %[po], s98\n\t" \
  "ds_read_b64_tr_b16 a[94:95], v191 offset:41984\n\t" \
  "v_bfe_u32 v131, v233, 2, 2\n\t" \
  "v_mfma_f32_16x16x32_bf16 v[88:91], a[184:187], a[216:219], v[88:91]\n\t" \
  "s_lshl_b32 %[xcb], %[wid], 15\n\t" \
  "v_and_b32 v132, 3, v233\n\t" \
  "v_mfma_f32_16x16x32_bf16 v[120:123], a[184:187], a[220:223], v[120:123]\n\t" \
  "buffer_load_dwordx4 a[108:111], v251, %[ws], s97 offen sc1\n\t" \
  "v_mfma_f32_16x16x32_bf16 v[124:127], a[188:191], a[220:223], v[124:127]\n\t" \
  "s_add_u32 %[xcb], %[xcb], 0x1800000\n\t" \
  "v_cmp_lt_u32 vcc, 6, v233\n\t" \
  "v_mfma_f32_16x16x32_bf16 v[92:95], a[188:191], a[216:219], v[92:95]\n\t" \
  "s_add_u32 %[xcb], %[xcb], %[po]\n\t" \
  "v_mov_b32 v133, 6272\n\t" \
  "v_mfma_f32_16x16x32_bf16 v[60:63], a[188:191], a[212:215], v[60:63]\n\t" \
  "s_and_b32 s98, %[bid], 0x38\n\t" \
  "v_cndmask_b32 v133, 0, v133, vcc\n\t" \
  "v_mfma_f32_16x16x32_bf16 v[28:31], a[188:191], a[208:211], v[28:31]\n\t" \
  "s_waitcnt vmcnt(12) lgkmcnt(0)\n\t" \
  "s_barrier\n\t" \
  "v_mfma_f32_16x16x32_bf16 v[0:3], a[128:131], a[224:227], v[0:3]\n\t" \
  "s_add_u32 s97, s97, %[dk]\n\t" \
  "ds_read_b64_tr_b16 a[160:161], v184 offset:0\n\t" \
  "v_lshl_add_u32 v133, v233, 7, v133\n\t" \
  "v_mfma_f32_16x16x32_bf16 v[0:3], a[64:67], a[240:243], v[0:3]\n\t" \
  "buffer_load_dwordx4 a[112:115], v248, %[ws], s97 offen sc1\n\t" \
  "v_mfma_f32_16x16x32_bf16 v[32:35], a[64:67], a[244:247], v[32:35]\n\t" \
  "s_lshl_b32 s98, s98, 16\n\t" \
  "ds_read_b64_tr_b16 a[162:163], v184 offset:1024\n\t" \
  "v_cmp_gt_u32 vcc, 14, v233\n\t" \
  "v_mfma_f32_16x16x32_bf16 v[32:35], a[128:131], a[228:231], v[32:35]\n\t" \
  "buffer_load_dwordx4 a[116:119], v249, %[ws], s97 offen sc1\n\t" \
  "v_mfma_f32_16x16x32_bf16 v[64:67], a[128:131], a[232:235], v[64:67]\n\t" \
  "s_add_u32 %[xcb], %[xcb], s98\n\t" \
  "ds_read_b64_tr_b16 a[164:165], v185 offset:0\n\t" \
  "v_bfrev_b32 v247, 1\n\t" \
  "v_mfma_f32_16x16x32_bf16 v[64:67], a[64:67], a[248:251], v[64:67]\n\t" \
  "s_add_u32 %[otb], %[po], 0x800000\n\t" \
  "ds_read_b64_tr_b16 a[166:167], v185 offset:1024\n\t" \
  "v_cndmask_b32 v245, v247, v133, vcc\n\t" \
  "v_mfma_f32_16x16x32_bf16 v[96:99], a[64:67], a[252:255], v[96:99]\n\t" \
  "s_and_b32 s98, %[bid], 0x3\n\t" \
  "ds_read_b64_tr_b16 a[168:169], v186 offset:0\n\t" \
  "v_writelane_b32 v247, %[prb], 0\n\t" \
  "v_mfma_f32_16x16x32_bf16 v[96:99], a[128:131], a[236:239], v[96:99]\n\t" \
  "buffer_load_dwordx4 a[120:123], v250, %[ws], s97 offen sc1\n\t" \
  "v_mfma_f32_16x16x32_bf16 v[4:7], a[132:135], a[224:227], v[4:7]\n\t" \
  "s_lshl_b32 s98, s98, 20\n\t" \
  "ds_read_b64_tr_b16 a[170:171], v186 offset:1024\n\t" \
  "v_lshrrev_b32 v133, 1, v129\n\t" \
  "v_mfma_f32_16x16x32_bf16 v[4:7], a[68:71], a[240:243], v[4:7]\n\t" \
  "s_add_u32 %[otb], %[otb], s98\n\t" \
  "ds_read_b64_tr_b16 a[172:173], v187 offset:0\n\t" \
  "v_and_b32 v134, 1, v129\n\t" \
  "v_mfma_f32_16x16x32_bf16 v[36:39], a[68:71], a[244:247], v[36:39]\n\t" \
  "s_and_b32 s98, %[wid], 2\n\t" \
  "v_lshl_or_b32 v133, v134, 1, v133\n\t" \
  "v_mfma_f32_16x16x32_bf16 v[36:39], a[132:135], a[228:231], v[36:39]\n\t" \
  "buffer_load_dwordx4 a[124:127], v251, %[ws], s97 offen sc1\n\t" \
  "v_mfma_f32_16x16x32_bf16 v[68:71], a[132:135], a[232:235], v[68:71]\n\t" \
  "s_add_u32 s96, %[rot19], 5767168\n\t" \
  "ds_read_b64_tr_b16 a[174:175], v187 offset:1024\n\t" \
  "v_xor_b32 v133, v133, v130\n\t" \
  "v_mfma_f32_16x16x32_bf16 v[68:71], a[68:71], a[248:251], v[68:71]\n\t" \
  "s_and_b32 s96, s96, 0x780000\n\t" \
  "ds_read_b64_tr_b16 a[176:177], v188 offset:0\n\t" \
  "v_lshlrev_b32 v133, 4, v133\n\t" \
  "v_mfma_f32_16x16x32_bf16 v[100:103], a[68:71], a[252:255], v[100:103]\n\t" \
  "s_add_u32 m0, %[mw], 16384\n\t" \
  "ds_read_b64_tr_b16 a[178:179], v188 offset:1024\n\t" \
  "v_lshl_add_u32 v133, v130, 8, v133\n\t" \
  "v_mfma_f32_16x16x32_bf16 v[100:103], a[132:135], a[236:239], v[100:103]\n\t" \
  "buffer_load_dwordx4 v200, %[sw], s96 offen offset:0 lds\n\t" \
  "v_mfma_f32_16x16x32_bf16 v[8:11], a[136:139], a[224:227], v[8:11]\n\t" \
  "s_lshl_b32 s98, s98, 13\n\t" \
  "ds_read_b64_tr_b16 a[180:181], v189 offset:0\n\t" \
  "v_add_u32 v220, 49152, v133\n\t" \
  "v_mfma_f32_16x16x32_bf16 v[8:11], a[72:75], a[240:243], v[8:11]\n\t" \
  "s_add_u32 %[otb], %[otb], s98\n\t" \
  "ds_read_b64_tr_b16 a[182:183], v189 offset:1024\n\t" \
  "v_xor_b32 v221, 64, v220\n\t" \
  "v_mfma_f32_16x16x32_bf16 v[40:43], a[72:75], a[244:247], v[40:43]\n\t" \
  "s_and_b32 s98, %[wid], 1\n\t" \
  "ds_read_b64_tr_b16 a[184:185], v190 offset:0\n\t" \
  "v_xor_b32 v222, 128, v220\n\t" \
  "v_mfma_f32_16x16x32_bf16 v[40:43], a[136:139], a[228:231], v[40:43]\n\t" \
  "buffer_load_dwordx4 v201, %[sw], s96 offen offset:1024 lds\n\t" \
  "v_mfma_f32_16x16x32_bf16 v[72:75], a[136:139], a[232:235], v[72:75]\n\t" \
  "s_lshl_b32 s98, s98, 12\n\t" \
  "ds_read_b64_tr_b16 a[186:187], v190 offset:1024\n\t" \
  "v_xor_b32 v223, 192, v220\n\t" \
  "v_mfma_f32_16x16x32_bf16 v[72:75], a[72:75], a[248:251], v[72:75]\n\t" \
  "s_add_u32 %[otb], %[otb], s98\n\t" \
  "ds_read_b64_tr_b16 a[188:189], v191 offset:0\n\t" \
  "v_xor_b32 v133, v130, v129\n\t" \
  "v_mfma_f32_16x16x32_bf16 v[104:107], a[72:75], a[252:255], v[104:107]\n\t" \
  "s_lshr_b32 %[eso], %[c17], 2\n\t" \
  "v_lshlrev_b32 v133, 4, v133\n\t" \
  "v_mfma_f32_16x16x32_bf16 v[104:107], a[136:139], a[236:239], v[104:107]\n\t" \
  "buffer_load_dwordx4 v202, %[sw], s96 offen offset:2048 lds\n\t" \
  "v_mfma_f32_16x16x32_bf16 v[12:15], a[140:143], a[224:227], v[12:15]\n\t" \
  "s_add_u32 %[eso], %[eso], %[otb]\n\t" \
  "ds_read_b64_tr_b16 a[190:191], v191 offset:1024\n\t" \
  "v_lshl_add_u32 v224, v129, 8, v133\n\t" \
  "v_mfma_f32_16x16x32_bf16 v[12:15], a[76:79], a[240:243], v[12:15]\n\t" \
  "s_and_b32 s98, %[bid], 0x38\n\t" \
  "ds_read_b64_tr_b16 a[0:1], v184 offset:8192\n\t" \
  "v_xor_b32 v225, 64, v224\n\t" \
  "v_mfma_f32_16x16x32_bf16 v[44:47], a[76:79], a[244:247], v[44:47]\n\t" \
  "s_lshl_b32 s98, s98, 14\n\t" \
  "ds_read_b64_tr_b16 a[2:3], v184 offset:9216\n\t" \
  "v_xor_b32 v226, 128, v224\n\t" \
  "v_mfma_f32_16x16x32_bf16 v[44:47], a[140:143], a[228:231], v[44:47]\n\t" \
  "buffer_load_dwordx4 v203, %[sw], s96 offen offset:3072 lds\n\t" \
  "v_mfma_f32_16x16x32_bf16 v[76:79], a[140:143], a[232:235], v[76:79]\n\t" \
  "s_add_u32 s97, %[rot19], 5242880\n\t" \
  "ds_read_b64_tr_b16 a[4:5], v185 offset:8192\n\t" \
  "v_xor_b32 v227, 192, v224\n\t" \
  "v_mfma_f32_16x16x32_bf16 v[76:79], a[76:79], a[248:251], v[76:79]\n\t" \
  "s_bfe_u32 s97, s97, 0x140003\n\t" \
  "ds_read_b64_tr_b16 a[6:7], v185 offset:9216\n\t" \
  "v_lshlrev_b32 v133, 3, v132\n\t" \
  "v_mfma_f32_16x16x32_bf16 v[108:111], a[76:79], a[252:255], v[108:111]\n\t" \
  "s_add_u32 s97, s97, %[sox]\n\t" \
  "ds_read_b64_tr_b16 a[8:9], v186 offset:8192\n\t" \
  "v_lshl_add_u32 v133, v129, 10, v133\n\t" \
  "v_mfma_f32_16x16x32_bf16 v[108:111], a[140:143], a[236:239], v[108:111]\n\t" \
  "buffer_load_dwordx4 a[192:195], v248, %[ws], s97 offen sc1\n\t" \
  "v_mfma_f32_16x16x32_bf16 v[16:19], a[144:147], a[224:227], v[16:19]\n\t" \
  "s_add_u32 %[eso], %[eso], s98\n\t" \
  "ds_read_b64_tr_b16 a[10:11], v186 offset:9216\n\t" \
  "v_lshl_add_u32 v228, v131, 6, v133\n\t" \
  "v_mfma_f32_16x16x32_bf16 v[16:19], a[80:83], a[240:243], v[16:19]\n\t" \
  "s_lshr_b32 s98, %[bid], 1\n\t" \
  "ds_read_b64_tr_b16 a[12:13], v187 offset:8192\n\t" \
  "v_lshl_add_u32 v228, v134, 5, v228\n\t" \
  "v_mfma_f32_16x16x32_bf16 v[48:51], a[80:83], a[244:247], v[48:51]\n\t" \
  "s_xor_b32 s98, s98, %[wid]\n\t" \
  "v_add_u32 v228, 81920, v228\n\t" \
  "v_mfma_f32_16x16x32_bf16 v[48:51], a[144:147], a[228:231], v[48:51]\n\t" \
  "buffer_load_dwordx4 a[196:199], v249, %[ws], s97 offen sc1\n\t" \
  "v_mfma_f32_16x16x32_bf16 v[80:83], a[144:147], a[232:235], v[80:83]\n\t" \
  "s_lshl_b32 s98, s98, 13\n\t" \
  "ds_read_b64_tr_b16 a[14:15], v187 offset:9216\n\t" \
  "v_xor_b32 v229, 32, v228\n\t" \
  "v_mfma_f32_16x16x32_bf16 v[80:83], a[80:83], a[248:251], v[80:83]\n\t" \
  "s_and_b32 %[po], s98, 0x2000\n\t" \
  "ds_read_b64_tr_b16 a[16:17], v188 offset:8192\n\t" \
  "v_lshl_add_u32 v244, v131, 8, v133\n\t" \
  "v_mfma_f32_16x16x32_bf16 v[112:115], a[80:83], a[252:255], v[112:115]\n\t" \
  "s_lshr_b32 s98, %[bid], 5\n\t" \
  "ds_read_b64_tr_b16 a[18:19], v188 offset:9216\n\t" \
  "v_lshl_add_u32 v244, v131, 5, v244\n\t" \
  "v_mfma_f32_16x16x32_bf16 v[112:115], a[144:147], a[236:239], v[112:115]\n\t" \
  "buffer_load_dwordx4 a[200:203], v250, %[ws], s97 offen sc1\n\t" \
  "v_mfma_f32_16x16x32_bf16 v[20:23], a[148:151], a[224:227], v[20:23]\n\t" \
  "s_xor_b32 s98, s98, %[wid]\n\t" \
  "ds_read_b64_tr_b16 a[20:21], v189 offset:8192\n\t" \
  "v_lshl_add_u32 v244, v134, 7, v244\n\t" \
  "v_mfma_f32_16x16x32_bf16 v[20:23], a[84:87], a[240:243], v[20:23]\n\t" \
  "s_lshl_b32 s98, s98, 13\n\t" \
  "ds_read_b64_tr_b16 a[22:23], v189 offset:9216\n\t" \
  ".Lhs3:\n\ts_cbranch_execz .Lhs3n\n\t" \
  "v_lshlrev_b32 v133, 4, v132\n\t" \
  "v_mfma_f32_16x16x32_bf16 v[52:55], a[84:87], a[244:247], v[52:55]\n\t" \
  "s_and_b32 %[px], s98, 0x2000\n\t" \
  "ds_read_b64_tr_b16 a[24:25], v190 offset:8192\n\t" \
  "v_lshl_add_u32 v133, v129, 6, v133\n\t" \
  "v_mfma_f32_16x16x32_bf16 v[52:55], a[148:151], a[228:231], v[52:55]\n\t" \
  "buffer_load_dwordx4 a[204:207], v251, %[ws], s97 offen sc1\n\t" \
  "v_mfma_f32_16x16x32_bf16 v[84:87], a[148:151], a[232:235], v[84:87]\n\t" \
  "s_add_u32 s97, s97, %[dk]\n\t" \
  "ds_read_b64_tr_b16 a[26:27], v190 offset:9216\n\t" \
  "v_lshl_add_u32 v230, v131, 8, v133\n\t" \
  "v_mfma_f32_16x16x32_bf16 v[84:87], a[84:87], a[248:251], v[84:87]\n\t" \
  "s_lshl_b32 %[frb], %[wid], 2\n\t" \
  "ds_read_b64_tr_b16 a[28:29], v191 offset:8192\n\t" \
  "v_xor_b32 v231, 32, v230\n\t" \
  "v_mfma_f32_16x16x32_bf16 v[116:119], a[84:87], a[252:255], v[116:119]\n\t" \
  "s_add_u32 %[frb], %[frb], 0x8000\n\t" \
  "v_lshlrev_b32 v133, 1, v129\n\t" \
  "v_mfma_f32_16x16x32_bf16 v[116:119], a[148:151], a[236:239], v[116:119]\n\t" \
  "buffer_load_dwordx4 a[208:211], v248, %[ws], s97 offen sc1\n\t" \
  "v_mfma_f32_16x16x32_bf16 v[24:27], a[152:155], a[224:227], v[24:27]\n\t" \
  "s_and_b32 s98, %[bid], 0x80\n\t" \
  "ds_read_b64_tr_b16 a[30:31], v191 offset:9216\n\t" \
  "v_xor_b32 v133, v133, v130\n\t" \
  "v_mfma_f32_16x16x32_bf16 v[24:27], a[88:91], a[240:243], v[24:27]\n\t" \
  "s_lshl_b32 s98, s98, 7\n\t" \
  "v_lshlrev_b32 v133, 4, v133\n\t" \
  "v_mfma_f32_16x16x32_bf16 v[56:59], a[88:91], a[244:247], v[56:59]\n\t" \
  "s_add_u32 %[frb], %[frb], s98\n\t" \
  "v_lshl_add_u32 v240, v129, 11, v133\n\t" \
  "v_mfma_f32_16x16x32_bf16 v[56:59], a[152:155], a[228:231], v[56:59]\n\t" \
  "buffer_load_dwordx4 a[212:215], v249, %[ws], s97 offen sc1\n\t" \
  "v_mfma_f32_16x16x32_bf16 v[88:91], a[152:155], a[232:235], v[88:91]\n\t" \
  "s_and_b32 s98, %[bid], 0x4\n\t" \
  "v_xor_b32 v241, 0x80, v240\n\t" \
  "v_mfma_f32_16x16x32_bf16 v[88:91], a[88:91], a[248:251], v[88:91]\n\t" \
  "s_lshl_b32 s98, s98, 11\n\t" \
  "v_lshrrev_b32 v133, 1, v233\n\t" \
  "v_mfma_f32_16x16x32_bf16 v[120:123], a[88:91], a[252:255], v[120:123]\n\t" \
  "s_add_u32 %[frb], %[frb], s98\n\t" \
  "v_mul_u32_u24 v133, 57344, v133\n\t" \
  "v_mfma_f32_16x16x32_bf16 v[120:123], a[152:155], a[236:239], v[120:123]\n\t" \
  "buffer_load_dwordx4 a[216:219], v250, %[ws], s97 offen sc1\n\t" \
  "v_mfma_f32_16x16x32_bf16 v[28:31], a[156:159], a[224:227], v[28:31]\n\t" \
  "s_and_b32 s98, %[bid], 0x78\n\t" \
  "v_and_b32 v134, 1, v233\n\t" \
  "v_mfma_f32_16x16x32_bf16 v[28:31], a[92:95], a[240:243], v[28:31]\n\t" \
  "s_lshl_b32 s98, s98, 6\n\t" \
  "v_lshl_add_u32 v133, v134, 4, v133\n\t" \
  "v_mfma_f32_16x16x32_bf16 v[60:63], a[92:95], a[244:247], v[60:63]\n\t" \
  "s_add_u32 %[frb], %[frb], s98\n\t" \
  "v_or_b32 v232, %[idm], v133\n\t" \
  "v_mfma_f32_16x16x32_bf16 v[60:63], a[156:159], a[228:231], v[60:63]\n\t" \
  "buffer_load_dwordx4 a[220:223], v251, %[ws], s97 offen sc1\n\t" \
  "v_mfma_f32_16x16x32_bf16 v[92:95], a[156:159], a[232:235], v[92:95]\n\t" \
  "s_lshl_b32 %[fw2], %[wid], 7\n\t" \
  "v_mfma_f32_16x16x32_bf16 v[92:95], a[92:95], a[248:251], v[92:95]\n\t" \
  "s_add_u32 %[fw2], %[fw2], 0x20000\n\t" \
  "v_mfma_f32_16x16x32_bf16 v[124:127], a[92:95], a[252:255], v[124:127]\n\t" \
  "s_and_b32 s98, %[bid], 0xc0\n\t" \
  "v_mfma_f32_16x16x32_bf16 v[124:127], a[156:159], a[236:239], v[124:127]\n\t" \
  "s_waitcnt vmcnt(20) lgkmcnt(0)\n\t" \
  "s_barrier\n\t" \
  "v_mfma_f32_16x16x32_bf16 v[0:3], a[160:163], a[32:35], v[0:3]\n\t" \
  "s_add_u32 s96, %[rot19], 6291456\n\t" \
  "ds_read_b64_tr_b16 a[128:129], v184 offset:49152\n\t" \
  "v_mfma_f32_16x16x32_bf16 v[0:3], a[0:3], a[48:51], v[0:3]\n\t" \
  "s_and_b32 s96, s96, 0x780000\n\t" \
  "v_mfma_f32_16x16x32_bf16 v[32:35], a[0:3], a[52:55], v[32:35]\n\t" \
  "s_add_u32 m0, %[mw], 32768\n\t" \
  "ds_read_b64_tr_b16 a[130:131], v184 offset:50176\n\t" \
  "v_mfma_f32_16x16x32_bf16 v[32:35], a[160:163], a[36:39], v[32:35]\n\t" \
  "buffer_load_dwordx4 v200, %[sw], s96 offen offset:0 lds\n\t" \
  "v_mfma_f32_16x16x32_bf16 v[64:67], a[160:163], a[40:43], v[64:67]\n\t" \
  "buffer_load_dwordx4 v201, %[sw], s96 offen offset:1024 lds\n\t" \
  "v_mfma_f32_16x16x32_bf16 v[64:67], a[0:3], a[56:59], v[64:67]\n\t" \
  "s_lshl_b32 s98, s98, 9\n\t" \
  "ds_read_b64_tr_b16 a[132:133], v185 offset:49152\n\t" \
  "v_mfma_f32_16x16x32_bf16 v[96:99], a[0:3], a[60:63], v[96:99]\n\t" \
  "s_add_u32 %[fw2], %[fw2], s98\n\t" \
  "ds_read_b64_tr_b16 a[134:135], v185 offset:50176\n\t" \
  "v_mfma_f32_16x16x32_bf16 v[96:99], a[160:163], a[44:47], v[96:99]\n\t" \
  "s_and_b32 s98, %[bid], 0x2\n\t" \
  "ds_read_b64_tr_b16 a[136:137], v186 offset:49152\n\t" \
  "v_mfma_f32_16x16x32_bf16 v[4:7], a[164:167], a[32:35], v[4:7]\n\t" \
  "s_lshl_b32 s98, s98, 13\n\t" \
  "ds_read_b64_tr_b16 a[138:139], v186 offset:50176\n\t" \
  "v_mfma_f32_16x16x32_bf16 v[4:7], a[4:7], a[48:51], v[4:7]\n\t" \
  "buffer_load_dwordx4 v202, %[sw], s96 offen offset:2048 lds\n\t" \
  "v_mfma_f32_16x16x32_bf16 v[36:39], a[4:7], a[52:55], v[36:39]\n\t" \
  "s_add_u32 %[fw2], %[fw2], s98\n\t" \
  "ds_read_b64_tr_b16 a[140:141], v187 offset:49152\n\t" \
  "v_mfma_f32_16x16x32_bf16 v[36:39], a[164:167], a[36:39], v[36:39]\n\t" \
  "s_and_b32 s98, %[bid], 0x4\n\t" \
  "ds_read_b64_tr_b16 a[142:143], v187 offset:50176\n\t" \
  "v_mfma_f32_16x16x32_bf16 v[68:71], a[164:167], a[40:43], v[68:71]\n\t" \
  "s_lshl_b32 s98, s98, 11\n\t" \
  "ds_read_b64_tr_b16 a[144:145], v188 offset:49152\n\t" \
  "v_mfma_f32_16x16x32_bf16 v[68:71], a[4:7], a[56:59], v[68:71]\n\t" \
  "buffer_load_dwordx4 v203, %[sw], s96 offen offset:3072 lds\n\t" \
  "v_mfma_f32_16x16x32_bf16 v[100:103], a[4:7], a[60:63], v[100:103]\n\t" \
  "s_add_u32 s97, %[rot19], 5767168\n\t" \
  "ds_read_b64_tr_b16 a[146:147], v188 offset:50176\n\t" \
  "v_mfma_f32_16x16x32_bf16 v[100:103], a[164:167], a[44:47], v[100:103]\n\t" \
  "s_bfe_u32 s97, s97, 0x140003\n\t" \
  "ds_read_b64_tr_b16 a[148:149], v189 offset:49152\n\t" \
  "v_mfma_f32_16x16x32_bf16 v[8:11], a[168:171], a[32:35], v[8:11]\n\t" \
  "s_add_u32 s97, s97, %[sox]\n\t" \
  "v_mfma_f32_16x16x32_bf16 v[8:11], a[8:11], a[48:51], v[8:11]\n\t" \
  "s_add_u32 %[fw2], %[fw2], s98\n\t" \
  "ds_read_b64_tr_b16 a[150:151], v189 offset:50176\n\t" \
  "v_mfma_f32_16x16x32_bf16 v[40:43], a[8:11], a[52:55], v[40:43]\n\t" \
  "buffer_load_dwordx4 a[224:227], v248, %[ws], s97 offen sc1\n\t" \
  "v_mfma_f32_16x16x32_bf16 v[40:43], a[168:171], a[36:39], v[40:43]\n\t" \
  "s_and_b32 s98, %[bid], 0x1\n\t" \
  "ds_read_b64_tr_b16 a[152:153], v190 offset:49152\n\t" \
  "v_mfma_f32_16x16x32_bf16 v[72:75], a[168:171], a[40:43], v[72:75]\n\t" \
  "s_lshl_b32 s98, s98, 12\n\t" \
  "ds_read_b64_tr_b16 a[154:155], v190 offset:50176\n\t" \
  "v_mfma_f32_16x16x32_bf16 v[72:75], a[8:11], a[56:59], v[72:75]\n\t" \
  "s_add_u32 %[fw2], %[fw2], s98\n\t" \
  "ds_read_b64_tr_b16 a[156:157], v191 offset:49152\n\t" \
  "v_mfma_f32_16x16x32_bf16 v[104:107], a[8:11], a[60:63], v[104:107]\n\t" \
  "s_lshl2_add_u32 %[fw2], %[hl], %[fw2]\n\t" \
  "v_mfma_f32_16x16x32_bf16 v[104:107], a[168:171], a[44:47], v[104:107]\n\t" \
  "buffer_load_dwordx4 a[228:231], v249, %[ws], s97 offen sc1\n\t" \
  "v_mfma_f32_16x16x32_bf16 v[12:15], a[172:175], a[32:35], v[12:15]\n\t" \
  "s_lshl_b32 %[xso], %[wid], 15\n\t" \
  "ds_read_b64_tr_b16 a[158:159], v191 offset:50176\n\t" \
  "v_mfma_f32_16x16x32_bf16 v[12:15], a[12:15], a[48:51], v[12:15]\n\t" \
  "s_and_b32 s98, %[bid], 0x4\n\t" \
  "ds_read_b64_tr_b16 a[64:65], v184 offset:57344\n\t" \
  "v_mfma_f32_16x16x32_bf16 v[44:47], a[12:15], a[52:55], v[44:47]\n\t" \
  "s_lshl_b32 s98, s98, 19\n\t" \
  "ds_read_b64_tr_b16 a[66:67], v184 offset:58368\n\t" \
  "v_mfma_f32_16x16x32_bf16 v[44:47], a[172:175], a[36:39], v[44:47]\n\t" \
  "buffer_load_dwordx4 a[232:235], v250, %[ws], s97 offen sc1\n\t" \
  "v_mfma_f32_16x16x32_bf16 v[76:79], a[172:175], a[40:43], v[76:79]\n\t" \
  "s_add_u32 %[xso], %[xso], s98\n\t" \
  "ds_read_b64_tr_b16 a[68:69], v185 offset:57344\n\t" \
  "v_mfma_f32_16x16x32_bf16 v[76:79], a[12:15], a[56:59], v[76:79]\n\t" \
  "s_and_b32 s98, %[bid], 0xc0\n\t" \
  "ds_read_b64_tr_b16 a[70:71], v185 offset:58368\n\t" \
  "v_mfma_f32_16x16x32_bf16 v[108:111], a[12:15], a[60:63], v[108:111]\n\t" \
  "s_lshl_b32 s98, s98, 13\n\t" \
  "ds_read_b64_tr_b16 a[72:73], v186 offset:57344\n\t" \
  "v_mfma_f32_16x16x32_bf16 v[108:111], a[172:175], a[44:47], v[108:111]\n\t" \
  "s_add_u32 %[xso], %[xso], s98\n\t" \
  "v_mfma_f32_16x16x32_bf16 v[16:19], a[176:179], a[32:35], v[16:19]\n\t" \
  "buffer_load_dwordx4 a[236:239], v251, %[ws], s97 offen sc1\n\t" \
  "v_mfma_f32_16x16x32_bf16 v[16:19], a[16:19], a[48:51], v[16:19]\n\t" \
  "s_add_u32 s99, %[pf1], 2048\n\t" \
  "ds_read_b64_tr_b16 a[74:75], v186 offset:58368\n\t" \
  "v_mfma_f32_16x16x32_bf16 v[48:51], a[16:19], a[52:55], v[48:51]\n\t" \
  "s_and_b32 s98, %[bid], 0x3\n\t" \
  "ds_read_b64_tr_b16 a[76:77], v187 offset:57344\n\t" \
  "v_mfma_f32_16x16x32_bf16 v[48:51], a[176:179], a[36:39], v[48:51]\n\t" \
  "s_lshl_b32 s98, s98, 17\n\t" \
  "ds_read_b64_tr_b16 a[78:79], v187 offset:58368\n\t" \
  "v_mfma_f32_16x16x32_bf16 v[80:83], a[176:179], a[40:43], v[80:83]\n\t" \
  "buffer_load_dword %[pf], v217, %[wo], s99 offen\n\t" \
  "v_mfma_f32_16x16x32_bf16 v[80:83], a[16:19], a[56:59], v[80:83]\n\t" \
  "s_add_u32 s97, s97, %[dk]\n\t" \
  "ds_read_b64_tr_b16 a[80:81], v188 offset:57344\n\t" \
  "v_mfma_f32_16x16x32_bf16 v[112:115], a[16:19], a[60:63], v[112:115]\n\t" \
  "s_add_u32 %[xso], %[xso], s98\n\t" \
  "ds_read_b64_tr_b16 a[82:83], v188 offset:58368\n\t" \
  "v_mfma_f32_16x16x32_bf16 v[112:115], a[176:179], a[44:47], v[112:115]\n\t" \
  "s_and_b32 s98, %[bid], 0x38\n\t" \
  "ds_read_b64_tr_b16 a[84:85], v189 offset:57344\n\t" \
  "v_mfma_f32_16x16x32_bf16 v[20:23], a[180:183], a[32:35], v[20:23]\n\t" \
  "s_lshl_b32 s98, s98, 5\n\t" \
  "v_mfma_f32_16x16x32_bf16 v[20:23], a[20:23], a[48:51], v[20:23]\n\t" \
  "buffer_load_dwordx4 a[240:243], v248, %[ws], s97 offen sc1\n\t" \
  "v_mfma_f32_16x16x32_bf16 v[52:55], a[20:23], a[52:55], v[52:55]\n\t" \
  "s_add_u32 %[xso], %[xso], s98\n\t" \
  "ds_read_b64_tr_b16 a[86:87], v189 offset:58368\n\t" \
  "v_mfma_f32_16x16x32_bf16 v[52:55], a[180:183], a[36:39], v[52:55]\n\t" \
  "s_lshl_b32 %[ss], %[wid], 7\n\t" \
  "ds_read_b64_tr_b16 a[88:89], v190 offset:57344\n\t" \
  "v_mfma_f32_16x16x32_bf16 v[84:87], a[180:183], a[40:43], v[84:87]\n\t" \
  "s_add_u32 %[ss], %[ss], 0x240000\n\t" \
  "ds_read_b64_tr_b16 a[90:91], v190 offset:58368\n\t" \
  "v_mfma_f32_16x16x32_bf16 v[84:87], a[20:23], a[56:59], v[84:87]\n\t" \
  "s_and_b32 s98, %[bid], 0xff\n\t" \
  "ds_read_b64_tr_b16 a[92:93], v191 offset:57344\n\t" \
  "v_mfma_f32_16x16x32_bf16 v[116:119], a[20:23], a[60:63], v[116:119]\n\t" \
  "buffer_load_dwordx4 a[244:247], v249, %[ws], s97 offen sc1\n\t" \
  "v_mfma_f32_16x16x32_bf16 v[116:119], a[180:183], a[44:47], v[116:119]\n\t" \
  "s_lshl_b32 s98, s98, 9\n\t" \
  "ds_read_b64_tr_b16 a[94:95], v191 offset:58368\n\t" \
  "v_mfma_f32_16x16x32_bf16 v[24:27], a[184:187], a[32:35], v[24:27]\n\t" \
  "s_add_u32 %[ss], %[ss], s98\n\t" \
  "v_mfma_f32_16x16x32_bf16 v[24:27], a[24:27], a[48:51], v[24:27]\n\t" \
  "s_lshl_b32 %[mbp], %[wid], 7\n\t" \
  "v_mfma_f32_16x16x32_bf16 v[56:59], a[24:27], a[52:55], v[56:59]\n\t" \
  "buffer_load_dwordx4 a[248:251], v250, %[ws], s97 offen sc1\n\t" \
  "v_mfma_f32_16x16x32_bf16 v[56:59], a[184:187], a[36:39], v[56:59]\n\t" \
  "s_add_u32 %[mbp], %[mbp], 0x220000\n\t" \
  "v_mfma_f32_16x16x32_bf16 v[88:91], a[184:187], a[40:43], v[88:91]\n\t" \
  "s_and_b32 s98, %[bid], 0xc0\n\t" \
  "v_mfma_f32_16x16x32_bf16 v[88:91], a[24:27], a[56:59], v[88:91]\n\t" \
  "s_lshl_b32 s98, s98, 9\n\t" \
  "v_mfma_f32_16x16x32_bf16 v[120:123], a[24:27], a[60:63], v[120:123]\n\t" \
  "s_add_u32 %[mbp], %[mbp], s98\n\t" \
  "v_mfma_f32_16x16x32_bf16 v[120:123], a[184:187], a[44:47], v[120:123]\n\t" \
  "buffer_load_dwordx4 a[252:255], v251, %[ws], s97 offen sc1\n\t" \
  "v_mfma_f32_16x16x32_bf16 v[28:31], a[188:191], a[32:35], v[28:31]\n\t" \
  "s_and_b32 s98, %[bid], 0x3\n\t" \
  "v_mfma_f32_16x16x32_bf16 v[28:31], a[28:31], a[48:51], v[28:31]\n\t" \
  "s_lshl_b32 s98, s98, 13\n\t" \
  "v_mfma_f32_16x16x32_bf16 v[60:63], a[28:31], a[52:55], v[60:63]\n\t" \
  "s_add_u32 %[mbp], %[mbp], s98\n\t" \
  "v_mfma_f32_16x16x32_bf16 v[60:63], a[188:191], a[36:39], v[60:63]\n\t" \
  "s_and_b32 s98, %[bid], 0x4\n\t" \
  "v_mfma_f32_16x16x32_bf16 v[92:95], a[188:191], a[40:43], v[92:95]\n\t" \
  "s_lshl_b32 s98, s98, 10\n\t" \
  "v_mfma_f32_16x16x32_bf16 v[92:95], a[28:31], a[56:59], v[92:95]\n\t" \
  "s_add_u32 %[mbp], %[mbp], s98\n\t" \
  "v_mfma_f32_16x16x32_bf16 v[124:127], a[28:31], a[60:63], v[124:127]\n\t" \
  "s_and_b32 s98, %[bid], 0x38\n\t" \
  "v_mfma_f32_16x16x32_bf16 v[124:127], a[188:191], a[44:47], v[124:127]\n\t" \
  "s_waitcnt vmcnt(25) lgkmcnt(0)\n\t" \
  "s_barrier\n\t" \
  "v_mfma_f32_16x16x32_bf16 v[0:3], a[128:131], a[96:99], v[0:3]\n\t" \
  "s_add_u32 s96, %[rot19], 6815744\n\t" \
  "ds_read_b64_tr_b16 a[160:161], v192 offset:0\n\t" \
  "v_mfma_f32_16x16x32_bf16 v[0:3], a[64:67], a[112:115], v[0:3]\n\t" \
  "s_and_b32 s96, s96, 0x780000\n\t" \
  "v_mfma_f32_16x16x32_bf16 v[32:35], a[64:67], a[116:119], v[32:35]\n\t" \
  "s_add_u32 m0, %[mw], 49152\n\t" \
  "ds_read_b64_tr_b16 a[162:163], v192 offset:1024\n\t" \
  "v_mfma_f32_16x16x32_bf16 v[32:35], a[128:131], a[100:103], v[32:35]\n\t" \
  "buffer_load_dwordx4 v200, %[sw], s96 offen offset:0 lds\n\t" \
  "v_mfma_f32_16x16x32_bf16 v[64:67], a[128:131], a[104:107], v[64:67]\n\t" \
  "buffer_load_dwordx4 v201, %[sw], s96 offen offset:1024 lds\n\t" \
  "v_mfma_f32_16x16x32_bf16 v[64:67], a[64:67], a[120:123], v[64:67]\n\t" \
  "s_lshl_b32 s98, s98, 6\n\t" \
  "ds_read_b64_tr_b16 a[164:165], v193 offset:0\n\t" \
  "v_mfma_f32_16x16x32_bf16 v[96:99], a[64:67], a[124:127], v[96:99]\n\t" \
  "s_add_u32 %[mbp], %[mbp], s98\n\t" \
  "ds_read_b64_tr_b16 a[166:167], v193 offset:1024\n\t" \
  "v_mfma_f32_16x16x32_bf16 v[96:99], a[128:131], a[108:111], v[96:99]\n\t" \
  "s_lshr_b32 s98, %[bid], 6\n\t" \
  "ds_read_b64_tr_b16 a[168:169], v194 offset:0\n\t" \
  "v_mfma_f32_16x16x32_bf16 v[4:7], a[132:135], a[96:99], v[4:7]\n\t" \
  "s_lshl2_add_u32 %[gvo], s98, %[wid]\n\t" \
  "ds_read_b64_tr_b16 a[170:171], v194 offset:1024\n\t" \
  "v_mfma_f32_16x16x32_bf16 v[4:7], a[68:71], a[112:115], v[4:7]\n\t" \
  "buffer_load_dwordx4 v202, %[sw], s96 offen offset:2048 lds\n\t" \
  "v_mfma_f32_16x16x32_bf16 v[36:39], a[68:71], a[116:119], v[36:39]\n\t" \
  "s_mul_i32 %[gvo], %[gvo], 786432\n\t" \
  "ds_read_b64_tr_b16 a[172:173], v195 offset:0\n\t" \
  "v_mfma_f32_16x16x32_bf16 v[36:39], a[132:135], a[100:103], v[36:39]\n\t" \
  "s_and_b32 s98, %[bid], 0x38\n\t" \
  "ds_read_b64_tr_b16 a[174:175], v195 offset:1024\n\t" \
  "v_mfma_f32_16x16x32_bf16 v[68:71], a[132:135], a[104:107], v[68:71]\n\t" \
  "s_lshl_b32 s98, s98, 4\n\t" \
  "ds_read_b64_tr_b16 a[176:177], v196 offset:0\n\t" \
  "v_mfma_f32_16x16x32_bf16 v[68:71], a[68:71], a[120:123], v[68:71]\n\t" \
  "buffer_load_dwordx4 v203, %[sw], s96 offen offset:3072 lds\n\t" \
  "v_mfma_f32_16x16x32_bf16 v[100:103], a[68:71], a[124:127], v[100:103]\n\t" \
  "s_add_u32 s97, %[rot19], 6291456\n\t" \
  "ds_read_b64_tr_b16 a[178:179], v196 offset:1024\n\t" \
  "v_mfma_f32_16x16x32_bf16 v[100:103], a[132:135], a[108:111], v[100:103]\n\t" \
  "s_bfe_u32 s97, s97, 0x140003\n\t" \
  "ds_read_b64_tr_b16 a[180:181], v197 offset:0\n\t" \
  "v_mfma_f32_16x16x32_bf16 v[8:11], a[136:139], a[96:99], v[8:11]\n\t" \
  "s_add_u32 s97, s97, %[sox]\n\t" \
  "v_mfma_f32_16x16x32_bf16 v[8:11], a[72:75], a[112:115], v[8:11]\n\t" \
  "s_add_u32 %[gvo], %[gvo], s98\n\t" \
  "ds_read_b64_tr_b16 a[182:183], v197 offset:1024\n\t" \
  "v_mfma_f32_16x16x32_bf16 v[40:43], a[72:75], a[116:119], v[40:43]\n\t" \
  "buffer_load_dwordx4 a[32:35], v248, %[ws], s97 offen sc1\n\t" \
  "v_mfma_f32_16x16x32_bf16 v[40:43], a[136:139], a[100:103], v[40:43]\n\t" \
  "s_lshr_b32 s98, %[bid], 2\n\t" \
  "ds_read_b64_tr_b16 a[184:185], v198 offset:0\n\t" \
  "v_mfma_f32_16x16x32_bf16 v[72:75], a[136:139], a[104:107], v[72:75]\n\t" \
  "s_xor_b32 s98, s98, %[bid]\n\t" \
  "ds_read_b64_tr_b16 a[186:187], v198 offset:1024\n\t" \
  "v_mfma_f32_16x16x32_bf16 v[72:75], a[72:75], a[120:123], v[72:75]\n\t" \
  "s_and_b32 s98, s98, 4\n\t" \
  "ds_read_b64_tr_b16 a[188:189], v199 offset:0\n\t" \
  "v_mfma_f32_16x16x32_bf16 v[104:107], a[72:75], a[124:127], v[104:107]\n\t" \
  "s_lshl_b32 %[vo], s98, 4\n\t" \
  "v_mfma_f32_16x16x32_bf16 v[104:107], a[136:139], a[108:111], v[104:107]\n\t" \
  "buffer_load_dwordx4 a[36:39], v249, %[ws], s97 offen sc1\n\t" \
  "v_mfma_f32_16x16x32_bf16 v[12:15], a[140:143], a[96:99], v[12:15]\n\t" \
  "s_and_b32 s98, %[bid], 0x8\n\t" \
  "ds_read_b64_tr_b16 a[190:191], v199 offset:1024\n\t" \
  "v_mfma_f32_16x16x32_bf16 v[12:15], a[76:79], a[112:115], v[12:15]\n\t" \
  "s_lshl_b32 s98, s98, 2\n\t" \
  "ds_read_b64_tr_b16 a[0:1], v192 offset:8192\n\t" \
  "v_mfma_f32_16x16x32_bf16 v[44:47], a[76:79], a[116:119], v[44:47]\n\t" \
  "s_add_u32 %[vo], %[vo], s98\n\t" \
  "ds_read_b64_tr_b16 a[2:3], v192 offset:9216\n\t" \
  "v_mfma_f32_16x16x32_bf16 v[44:47], a[140:143], a[100:103], v[44:47]\n\t" \
  "buffer_load_dwordx4 a[40:43], v250, %[ws], s97 offen sc1\n\t" \
  "v_mfma_f32_16x16x32_bf16 v[76:79], a[140:143], a[104:107], v[76:79]\n\t" \
  "s_and_b32 s98, %[bid], 0x3\n\t" \
  "ds_read_b64_tr_b16 a[4:5], v193 offset:8192\n\t" \
  "v_mfma_f32_16x16x32_bf16 v[76:79], a[76:79], a[120:123], v[76:79]\n\t" \
  "s_lshl_b32 s98, s98, 3\n\t" \
  "ds_read_b64_tr_b16 a[6:7], v193 offset:9216\n\t" \
  "v_mfma_f32_16x16x32_bf16 v[108:111], a[76:79], a[124:127], v[108:111]\n\t" \
  "s_add_u32 %[vo], %[vo], s98\n\t" \
  "ds_read_b64_tr_b16 a[8:9], v194 offset:8192\n\t" \
  "v_mfma_f32_16x16x32_bf16 v[108:111], a[140:143], a[108:111], v[108:111]\n\t" \
  "s_and_b32 s98, %[bid], 0x4\n\t" \
  "v_mfma_f32_16x16x32_bf16 v[16:19], a[144:147], a[96:99], v[16:19]\n\t" \
  "buffer_load_dwordx4 a[44:47], v251, %[ws], s97 offen sc1\n\t" \
  "v_mfma_f32_16x16x32_bf16 v[16:19], a[80:83], a[112:115], v[16:19]\n\t" \
  "s_add_u32 s97, s97, %[dk]\n\t" \
  "ds_read_b64_tr_b16 a[10:11], v194 offset:9216\n\t" \
  "v_mfma_f32_16x16x32_bf16 v[48:51], a[80:83], a[116:119], v[48:51]\n\t" \
  "s_lshl_b32 s98, s98, 0\n\t" \
  "ds_read_b64_tr_b16 a[12:13], v195 offset:8192\n\t" \
  "v_mfma_f32_16x16x32_bf16 v[48:51], a[144:147], a[100:103], v[48:51]\n\t" \
  "s_add_u32 %[vo], %[vo], s98\n\t" \
  "ds_read_b64_tr_b16 a[14:15], v195 offset:9216\n\t" \
  "v_mfma_f32_16x16x32_bf16 v[80:83], a[144:147], a[104:107], v[80:83]\n\t" \
  "buffer_load_dwordx4 a[48:51], v248, %[ws], s97 offen sc1\n\t" \
  "v_mfma_f32_16x16x32_bf16 v[80:83], a[80:83], a[120:123], v[80:83]\n\t" \
  "s_add_u32 %[vo], %[vo], 0x2000\n\t" \
  "ds_read_b64_tr_b16 a[16:17], v196 offset:8192\n\t" \
  "v_mfma_f32_16x16x32_bf16 v[112:115], a[80:83], a[124:127], v[112:115]\n\t" \
  "ds_read_b64_tr_b16 a[18:19], v196 offset:9216\n\t" \
  "v_mfma_f32_16x16x32_bf16 v[112:115], a[144:147], a[108:111], v[112:115]\n\t" \
  "ds_read_b64_tr_b16 a[20:21], v197 offset:8192\n\t" \
  "v_mfma_f32_16x16x32_bf16 v[20:23], a[148:151], a[96:99], v[20:23]\n\t" \
  "s_cmp_lt_i32 %[prb], 0\n\t" \
  "s_cselect_b32 %[vo], %[prb], %[vo]\n\t" \
  "v_mfma_f32_16x16x32_bf16 v[20:23], a[84:87], a[112:115], v[20:23]\n\t" \
  "buffer_load_dwordx4 a[52:55], v249, %[ws], s97 offen sc1\n\t" \
  "v_mfma_f32_16x16x32_bf16 v[52:55], a[84:87], a[116:119], v[52:55]\n\t" \
  "ds_read_b64_tr_b16 a[22:23], v197 offset:9216\n\t" \
  "v_mfma_f32_16x16x32_bf16 v[52:55], a[148:151], a[100:103], v[52:55]\n\t" \
  "ds_read_b64_tr_b16 a[24:25], v198 offset:8192\n\t" \
  "v_mfma_f32_16x16x32_bf16 v[84:87], a[148:151], a[104:107], v[84:87]\n\t" \
  "ds_read_b64_tr_b16 a[26:27], v198 offset:9216\n\t" \
  "v_mfma_f32_16x16x32_bf16 v[84:87], a[84:87], a[120:123], v[84:87]\n\t" \
  "ds_read_b64_tr_b16 a[28:29], v199 offset:8192\n\t" \
  "v_mfma_f32_16x16x32_bf16 v[116:119], a[84:87], a[124:127], v[116:119]\n\t" \
  "buffer_load_dwordx4 a[56:59], v250, %[ws], s97 offen sc1\n\t" \
  "v_mfma_f32_16x16x32_bf16 v[116:119], a[148:151], a[108:111], v[116:119]\n\t" \
  "ds_read_b64_tr_b16 a[30:31], v199 offset:9216\n\t" \
  "v_mfma_f32_16x16x32_bf16 v[24:27], a[152:155], a[96:99], v[24:27]\n\t" \
  "v_mfma_f32_16x16x32_bf16 v[24:27], a[88:91], a[112:115], v[24:27]\n\t" \
  "v_mfma_f32_16x16x32_bf16 v[56:59], a[88:91], a[116:119], v[56:59]\n\t" \
  "buffer_load_dwordx4 a[60:63], v251, %[ws], s97 offen sc1\n\t" \
  "v_mfma_f32_16x16x32_bf16 v[56:59], a[152:155], a[100:103], v[56:59]\n\t" \
  "s_add_u32 s99, %[pf1], 4096\n\t" \
  "v_mfma_f32_16x16x32_bf16 v[88:91], a[152:155], a[104:107], v[88:91]\n\t" \
  "v_mfma_f32_16x16x32_bf16 v[88:91], a[88:91], a[120:123], v[88:91]\n\t" \
  "v_mfma_f32_16x16x32_bf16 v[120:123], a[88:91], a[124:127], v[120:123]\n\t" \
  "v_mfma_f32_16x16x32_bf16 v[120:123], a[152:155], a[108:111], v[120:123]\n\t" \
  "buffer_load_dword %[pf], v217, %[wo], s99 offen\n\t" \
  "v_mfma_f32_16x16x32_bf16 v[28:31], a[156:159], a[96:99], v[28:31]\n\t" \
  "v_mfma_f32_16x16x32_bf16 v[28:31], a[92:95], a[112:115], v[28:31]\n\t" \
  "v_mfma_f32_16x16x32_bf16 v[60:63], a[92:95], a[116:119], v[60:63]\n\t" \
  "v_mfma_f32_16x16x32_bf16 v[60:63], a[156:159], a[100:103], v[60:63]\n\t" \
  "v_mfma_f32_16x16x32_bf16 v[92:95], a[156:159], a[104:107], v[92:95]\n\t" \
  "v_mfma_f32_16x16x32_bf16 v[92:95], a[92:95], a[120:123], v[92:95]\n\t" \
  "v_mfma_f32_16x16x32_bf16 v[124:127], a[92:95], a[124:127], v[124:127]\n\t" \
  "v_mfma_f32_16x16x32_bf16 v[124:127], a[156:159], a[108:111], v[124:127]\n\t" \
  "s_waitcnt vmcnt(26) lgkmcnt(0)\n\t" \
  "s_barrier\n\t" \
  "v_mfma_f32_16x16x32_bf16 v[0:3], a[160:163], a[192:195], v[0:3]\n\t" \
  "s_add_u32 s96, %[rot19], 7340032\n\t" \
  "ds_read_b64_tr_b16 a[128:129], v184 offset:16384\n\t" \
  "v_mfma_f32_16x16x32_bf16 v[0:3], a[0:3], a[208:211], v[0:3]\n\t" \
  "s_and_b32 s96, s96, 0x780000\n\t" \
  "v_mfma_f32_16x16x32_bf16 v[32:35], a[0:3], a[212:215], v[32:35]\n\t" \
  "s_add_u32 m0, %[mw], 0\n\t" \
  "ds_read_b64_tr_b16 a[130:131], v184 offset:17408\n\t" \
  "v_mfma_f32_16x16x32_bf16 v[32:35], a[160:163], a[196:199], v[32:35]\n\t" \
  "buffer_load_dwordx4 v200, %[sw], s96 offen offset:0 lds\n\t" \
  "v_mfma_f32_16x16x32_bf16 v[64:67], a[160:163], a[200:203], v[64:67]\n\t" \
  "ds_read_b64_tr_b16 a[132:133], v185 offset:16384\n\t" \
  "v_mfma_f32_16x16x32_bf16 v[64:67], a[0:3], a[216:219], v[64:67]\n\t" \
  "buffer_load_dwordx4 v201, %[sw], s96 offen offset:1024 lds\n\t" \
  "v_mfma_f32_16x16x32_bf16 v[96:99], a[0:3], a[220:223], v[96:99]\n\t" \
  "ds_read_b64_tr_b16 a[134:135], v185 offset:17408\n\t" \
  "v_mfma_f32_16x16x32_bf16 v[96:99], a[160:163], a[204:207], v[96:99]\n\t" \
  "ds_read_b64_tr_b16 a[136:137], v186 offset:16384\n\t" \
  "v_mfma_f32_16x16x32_bf16 v[4:7], a[164:167], a[192:195], v[4:7]\n\t" \
  "ds_read_b64_tr_b16 a[138:139], v186 offset:17408\n\t" \
  "v_mfma_f32_16x16x32_bf16 v[4:7], a[4:7], a[208:211], v[4:7]\n\t" \
  "ds_read_b64_tr_b16 a[140:141], v187 offset:16384\n\t" \
  "v_mfma_f32_16x16x32_bf16 v[36:39], a[4:7], a[212:215], v[36:39]\n\t" \
  "buffer_load_dwordx4 v202, %[sw], s96 offen offset:2048 lds\n\t" \
  "v_mfma_f32_16x16x32_bf16 v[36:39], a[164:167], a[196:199], v[36:39]\n\t" \
  "ds_read_b64_tr_b16 a[142:143], v187 offset:17408\n\t" \
  "v_mfma_f32_16x16x32_bf16 v[68:71], a[164:167], a[200:203], v[68:71]\n\t" \
  "ds_read_b64_tr_b16 a[144:145], v188 offset:16384\n\t" \
  "v_mfma_f32_16x16x32_bf16 v[68:71], a[4:7], a[216:219], v[68:71]\n\t" \
  "v_mfma_f32_16x16x32_bf16 v[100:103], a[4:7], a[220:223], v[100:103]\n\t" \
  "ds_read_b64_tr_b16 a[146:147], v188 offset:17408\n\t" \
  "v_mfma_f32_16x16x32_bf16 v[100:103], a[164:167], a[204:207], v[100:103]\n\t" \
  "buffer_load_dwordx4 v203, %[sw], s96 offen offset:3072 lds\n\t" \
  "v_mfma_f32_16x16x32_bf16 v[8:11], a[168:171], a[192:195], v[8:11]\n\t" \
  "s_add_u32 s97, %[rot19], 6815744\n\t" \
  "ds_read_b64_tr_b16 a[148:149], v189 offset:16384\n\t" \
  "v_mfma_f32_16x16x32_bf16 v[8:11], a[8:11], a[208:211], v[8:11]\n\t" \
  "s_bfe_u32 s97, s97, 0x140003\n\t" \
  "ds_read_b64_tr_b16 a[150:151], v189 offset:17408\n\t" \
  "v_mfma_f32_16x16x32_bf16 v[40:43], a[8:11], a[212:215], v[40:43]\n\t" \
  "s_add_u32 s97, s97, %[sox]\n\t" \
  "ds_read_b64_tr_b16 a[152:153], v190 offset:16384\n\t" \
  "v_mfma_f32_16x16x32_bf16 v[40:43], a[168:171], a[196:199], v[40:43]\n\t" \
  "v_mfma_f32_16x16x32_bf16 v[72:75], a[168:171], a[200:203], v[72:75]\n\t" \
  "buffer_load_dwordx4 a[96:99], v248, %[ws], s97 offen sc1\n\t" \
  "v_mfma_f32_16x16x32_bf16 v[72:75], a[8:11], a[216:219], v[72:75]\n\t" \
  "ds_read_b64_tr_b16 a[154:155], v190 offset:17408\n\t" \
  "v_mfma_f32_16x16x32_bf16 v[104:107], a[8:11], a[220:223], v[104:107]\n\t" \
  "ds_read_b64_tr_b16 a[156:157], v191 offset:16384\n\t" \
  "v_mfma_f32_16x16x32_bf16 v[104:107], a[168:171], a[204:207], v[104:107]\n\t" \
  "ds_read_b64_tr_b16 a[158:159], v191 offset:17408\n\t" \
  "v_mfma_f32_16x16x32_bf16 v[12:15], a[172:175], a[192:195], v[12:15]\n\t" \
  "ds_read_b64_tr_b16 a[64:65], v184 offset:24576\n\t" \
  "v_mfma_f32_16x16x32_bf16 v[12:15], a[12:15], a[208:211], v[12:15]\n\t" \
  "buffer_load_dwordx4 a[100:103], v249, %[ws], s97 offen sc1\n\t" \
  "v_mfma_f32_16x16x32_bf16 v[44:47], a[12:15], a[212:215], v[44:47]\n\t" \
  "ds_read_b64_tr_b16 a[66:67], v184 offset:25600\n\t" \
  "v_mfma_f32_16x16x32_bf16 v[44:47], a[172:175], a[196:199], v[44:47]\n\t" \
  "ds_read_b64_tr_b16 a[68:69], v185 offset:24576\n\t" \
  "v_mfma_f32_16x16x32_bf16 v[76:79], a[172:175], a[200:203], v[76:79]\n\t" \
  "v_mfma_f32_16x16x32_bf16 v[76:79], a[12:15], a[216:219], v[76:79]\n\t" \
  "ds_read_b64_tr_b16 a[70:71], v185 offset:25600\n\t" \
  "v_mfma_f32_16x16x32_bf16 v[108:111], a[12:15], a[220:223], v[108:111]\n\t" \
  "buffer_load_dwordx4 a[104:107], v250, %[ws], s97 offen sc1\n\t" \
  "v_mfma_f32_16x16x32_bf16 v[108:111], a[172:175], a[204:207], v[108:111]\n\t" \
  "ds_read_b64_tr_b16 a[72:73], v186 offset:24576\n\t" \
  "v_mfma_f32_16x16x32_bf16 v[16:19], a[176:179], a[192:195], v[16:19]\n\t" \
  "ds_read_b64_tr_b16 a[74:75], v186 offset:25600\n\t" \
  "v_mfma_f32_16x16x32_bf16 v[16:19], a[16:19], a[208:211], v[16:19]\n\t" \
  "ds_read_b64_tr_b16 a[76:77], v187 offset:24576\n\t" \
  "v_mfma_f32_16x16x32_bf16 v[48:51], a[16:19], a[212:215], v[48:51]\n\t" \
  "v_mfma_f32_16x16x32_bf16 v[48:51], a[176:179], a[196:199], v[48:51]\n\t" \
  "buffer_load_dwordx4 a[108:111], v251, %[ws], s97 offen sc1\n\t" \
  "v_mfma_f32_16x16x32_bf16 v[80:83], a[176:179], a[200:203], v[80:83]\n\t" \
  "s_add_u32 s97, s97, %[dk]\n\t" \
  "ds_read_b64_tr_b16 a[78:79], v187 offset:25600\n\t" \
  "v_mfma_f32_16x16x32_bf16 v[80:83], a[16:19], a[216:219], v[80:83]\n\t" \
  "ds_read_b64_tr_b16 a[80:81], v188 offset:24576\n\t" \
  "v_mfma_f32_16x16x32_bf16 v[112:115], a[16:19], a[220:223], v[112:115]\n\t" \
  "ds_read_b64_tr_b16 a[82:83], v188 offset:25600\n\t" \
  "v_mfma_f32_16x16x32_bf16 v[112:115], a[176:179], a[204:207], v[112:115]\n\t" \
  "ds_read_b64_tr_b16 a[84:85], v189 offset:24576\n\t" \
  "v_mfma_f32_16x16x32_bf16 v[20:23], a[180:183], a[192:195], v[20:23]\n\t" \
  "buffer_load_dwordx4 a[112:115], v248, %[ws], s97 offen sc1\n\t" \
  "v_mfma_f32_16x16x32_bf16 v[20:23], a[20:23], a[208:211], v[20:23]\n\t" \
  "ds_read_b64_tr_b16 a[86:87], v189 offset:25600\n\t" \
  "v_mfma_f32_16x16x32_bf16 v[52:55], a[20:23], a[212:215], v[52:55]\n\t" \
  "ds_read_b64_tr_b16 a[88:89], v190 offset:24576\n\t" \
  "v_mfma_f32_16x16x32_bf16 v[52:55], a[180:183], a[196:199], v[52:55]\n\t" \
  "v_mfma_f32_16x16x32_bf16 v[84:87], a[180:183], a[200:203], v[84:87]\n\t" \
  "ds_read_b64_tr_b16 a[90:91], v190 offset:25600\n\t" \
  "v_mfma_f32_16x16x32_bf16 v[84:87], a[20:23], a[216:219], v[84:87]\n\t" \
  "buffer_load_dwordx4 a[116:119], v249, %[ws], s97 offen sc1\n\t" \
  "v_mfma_f32_16x16x32_bf16 v[116:119], a[20:23], a[220:223], v[116:119]\n\t" \
  "ds_read_b64_tr_b16 a[92:93], v191 offset:24576\n\t" \
  "v_mfma_f32_16x16x32_bf16 v[116:119], a[180:183], a[204:207], v[116:119]\n\t" \
  "ds_read_b64_tr_b16 a[94:95], v191 offset:25600\n\t" \
  "v_mfma_f32_16x16x32_bf16 v[24:27], a[184:187], a[192:195], v[24:27]\n\t" \
  "v_mfma_f32_16x16x32_bf16 v[24:27], a[24:27], a[208:211], v[24:27]\n\t" \
  "v_mfma_f32_16x16x32_bf16 v[56:59], a[24:27], a[212:215], v[56:59]\n\t" \
  "buffer_load_dwordx4 a[120:123], v250, %[ws], s97 offen sc1\n\t" \
  "v_mfma_f32_16x16x32_bf16 v[56:59], a[184:187], a[196:199], v[56:59]\n\t" \
  "v_mfma_f32_16x16x32_bf16 v[88:91], a[184:187], a[200:203], v[88:91]\n\t" \
  "v_mfma_f32_16x16x32_bf16 v[88:91], a[24:27], a[216:219], v[88:91]\n\t" \
  "v_mfma_f32_16x16x32_bf16 v[120:123], a[24:27], a[220:223], v[120:123]\n\t" \
  "v_mfma_f32_16x16x32_bf16 v[120:123], a[184:187], a[204:207], v[120:123]\n\t" \
  "buffer_load_dwordx4 a[124:127], v251, %[ws], s97 offen sc1\n\t" \
  "v_mfma_f32_16x16x32_bf16 v[28:31], a[188:191], a[192:195], v[28:31]\n\t" \
  "v_mfma_f32_16x16x32_bf16 v[28:31], a[28:31], a[208:211], v[28:31]\n\t" \
  "v_mfma_f32_16x16x32_bf16 v[60:63], a[28:31], a[212:215], v[60:63]\n\t" \
  "v_mfma_f32_16x16x32_bf16 v[60:63], a[188:191], a[196:199], v[60:63]\n\t" \
  "v_mfma_f32_16x16x32_bf16 v[92:95], a[188:191], a[200:203], v[92:95]\n\t" \
  "v_mfma_f32_16x16x32_bf16 v[92:95], a[28:31], a[216:219], v[92:95]\n\t" \
  "v_mfma_f32_16x16x32_bf16 v[124:127], a[28:31], a[220:223], v[124:127]\n\t" \
  "v_mfma_f32_16x16x32_bf16 v[124:127], a[188:191], a[204:207], v[124:127]\n\t" \
  "s_waitcnt vmcnt(25) lgkmcnt(0)\n\t" \
  "s_barrier\n\t" \
  "v_mfma_f32_16x16x32_bf16 v[0:3], a[128:131], a[224:227], v[0:3]\n\t" \
  "s_add_u32 s96, %[rot19], 7864320\n\t" \
  "ds_read_b64_tr_b16 a[160:161], v184 offset:32768\n\t" \
  "v_mfma_f32_16x16x32_bf16 v[0:3], a[64:67], a[240:243], v[0:3]\n\t" \
  "s_and_b32 s96, s96, 0x780000\n\t" \
  "v_mfma_f32_16x16x32_bf16 v[32:35], a[64:67], a[244:247], v[32:35]\n\t" \
  "s_add_u32 m0, %[mw], 98304\n\t" \
  "ds_read_b64_tr_b16 a[162:163], v184 offset:33792\n\t" \
  "v_mfma_f32_16x16x32_bf16 v[32:35], a[128:131], a[228:231], v[32:35]\n\t" \
  "buffer_load_dwordx4 v200, %[sw], s96 offen offset:0 lds\n\t" \
  "v_mfma_f32_16x16x32_bf16 v[64:67], a[128:131], a[232:235], v[64:67]\n\t" \
  "buffer_load_dwordx4 v201, %[sw], s96 offen offset:1024 lds\n\t" \
  "v_mfma_f32_16x16x32_bf16 v[64:67], a[64:67], a[248:251], v[64:67]\n\t" \
  "ds_read_b64_tr_b16 a[164:165], v185 offset:32768\n\t" \
  "v_mfma_f32_16x16x32_bf16 v[96:99], a[64:67], a[252:255], v[96:99]\n\t" \
  "ds_read_b64_tr_b16 a[166:167], v185 offset:33792\n\t" \
  "v_mfma_f32_16x16x32_bf16 v[96:99], a[128:131], a[236:239], v[96:99]\n\t" \
  "ds_read_b64_tr_b16 a[168:169], v186 offset:32768\n\t" \
  "v_mfma_f32_16x16x32_bf16 v[4:7], a[132:135], a[224:227], v[4:7]\n\t" \
  "ds_read_b64_tr_b16 a[170:171], v186 offset:33792\n\t" \
  "v_mfma_f32_16x16x32_bf16 v[4:7], a[68:71], a[240:243], v[4:7]\n\t" \
  "buffer_load_dwordx4 v202, %[sw], s96 offen offset:2048 lds\n\t" \
  "v_mfma_f32_16x16x32_bf16 v[36:39], a[68:71], a[244:247], v[36:39]\n\t" \
  "ds_read_b64_tr_b16 a[172:173], v187 offset:32768\n\t" \
  "v_mfma_f32_16x16x32_bf16 v[36:39], a[132:135], a[228:231], v[36:39]\n\t" \
  "ds_read_b64_tr_b16 a[174:175], v187 offset:33792\n\t" \
  "v_mfma_f32_16x16x32_bf16 v[68:71], a[132:135], a[232:235], v[68:71]\n\t" \
  "ds_read_b64_tr_b16 a[176:177], v188 offset:32768\n\t" \
  "v_mfma_f32_16x16x32_bf16 v[68:71], a[68:71], a[248:251], v[68:71]\n\t" \
  "buffer_load_dwordx4 v203, %[sw], s96 offen offset:3072 lds\n\t" \
  "v_mfma_f32_16x16x32_bf16 v[100:103], a[68:71], a[252:255], v[100:103]\n\t" \
  "s_add_u32 s97, %[rot19], 7340032\n\t" \
  "ds_read_b64_tr_b16 a[178:179], v188 offset:33792\n\t" \
  "v_mfma_f32_16x16x32_bf16 v[100:103], a[132:135], a[236:239], v[100:103]\n\t" \
  "s_bfe_u32 s97, s97, 0x140003\n\t" \
  "ds_read_b64_tr_b16 a[180:181], v189 offset:32768\n\t" \
  "v_mfma_f32_16x16x32_bf16 v[8:11], a[136:139], a[224:227], v[8:11]\n\t" \
  "s_add_u32 s97, s97, %[sox]\n\t" \
  "v_mfma_f32_16x16x32_bf16 v[8:11], a[72:75], a[240:243], v[8:11]\n\t" \
  "ds_read_b64_tr_b16 a[182:183], v189 offset:33792\n\t" \
  "v_mfma_f32_16x16x32_bf16 v[40:43], a[72:75], a[244:247], v[40:43]\n\t" \
  "buffer_load_dwordx4 a[192:195], v248, %[ws], s97 offen sc1\n\t" \
  "v_mfma_f32_16x16x32_bf16 v[40:43], a[136:139], a[228:231], v[40:43]\n\t" \
  "ds_read_b64_tr_b16 a[184:185], v190 offset:32768\n\t" \
  "v_mfma_f32_16x16x32_bf16 v[72:75], a[136:139], a[232:235], v[72:75]\n\t" \
  "ds_read_b64_tr_b16 a[186:187], v190 offset:33792\n\t" \
  "v_mfma_f32_16x16x32_bf16 v[72:75], a[72:75], a[248:251], v[72:75]\n\t" \
  "ds_read_b64_tr_b16 a[188:189], v191 offset:32768\n\t" \
  "v_mfma_f32_16x16x32_bf16 v[104:107], a[72:75], a[252:255], v[104:107]\n\t" \
  "v_mfma_f32_16x16x32_bf16 v[104:107], a[136:139], a[236:239], v[104:107]\n\t" \
  "buffer_load_dwordx4 a[196:199], v249, %[ws], s97 offen sc1\n\t" \
  "v_mfma_f32_16x16x32_bf16 v[12:15], a[140:143], a[224:227], v[12:15]\n\t" \
  "ds_read_b64_tr_b16 a[190:191], v191 offset:33792\n\t" \
  "v_mfma_f32_16x16x32_bf16 v[12:15], a[76:79], a[240:243], v[12:15]\n\t" \
  "ds_read_b64_tr_b16 a[0:1], v184 offset:40960\n\t" \
  "v_mfma_f32_16x16x32_bf16 v[44:47], a[76:79], a[244:247], v[44:47]\n\t" \
  "ds_read_b64_tr_b16 a[2:3], v184 offset:41984\n\t" \
  "v_mfma_f32_16x16x32_bf16 v[44:47], a[140:143], a[228:231], v[44:47]\n\t" \
  "buffer_load_dwordx4 a[200:203], v250, %[ws], s97 offen sc1\n\t" \
  "v_mfma_f32_16x16x32_bf16 v[76:79], a[140:143], a[232:235], v[76:79]\n\t" \
  "ds_read_b64_tr_b16 a[4:5], v185 offset:40960\n\t" \
  "v_mfma_f32_16x16x32_bf16 v[76:79], a[76:79], a[248:251], v[76:79]\n\t" \
  "ds_read_b64_tr_b16 a[6:7], v185 offset:41984\n\t" \
  "v_mfma_f32_16x16x32_bf16 v[108:111], a[76:79], a[252:255], v[108:111]\n\t" \
  "ds_read_b64_tr_b16 a[8:9], v186 offset:40960\n\t" \
  "v_mfma_f32_16x16x32_bf16 v[108:111], a[140:143], a[236:239], v[108:111]\n\t" \
  "v_mfma_f32_16x16x32_bf16 v[16:19], a[144:147], a[224:227], v[16:19]\n\t" \
  "buffer_load_dwordx4 a[204:207], v251, %[ws], s97 offen sc1\n\t" \
  "v_mfma_f32_16x16x32_bf16 v[16:19], a[80:83], a[240:243], v[16:19]\n\t" \
  "s_add_u32 s99, %[pf1], 6144\n\t" \
  "ds_read_b64_tr_b16 a[10:11], v186 offset:41984\n\t" \
  "v_mfma_f32_16x16x32_bf16 v[48:51], a[80:83], a[244:247], v[48:51]\n\t" \
  "ds_read_b64_tr_b16 a[12:13], v187 offset:40960\n\t" \
  "v_mfma_f32_16x16x32_bf16 v[48:51], a[144:147], a[228:231], v[48:51]\n\t" \
  ".Lhs4:\n\ts_cbranch_execz .Lhs4n\n\t" \
  "ds_read_b64_tr_b16 a[14:15], v187 offset:41984\n\t" \
  "v_mfma_f32_16x16x32_bf16 v[80:83], a[144:147], a[232:235], v[80:83]\n\t" \
  "buffer_load_dword %[pf], v217, %[wo], s99 offen\n\t" \
  "v_mfma_f32_16x16x32_bf16 v[80:83], a[80:83], a[248:251], v[80:83]\n\t" \
  "s_add_u32 s97, s97, %[dk]\n\t" \
  "ds_read_b64_tr_b16 a[16:17], v188 offset:40960\n\t" \
  "v_mfma_f32_16x16x32_bf16 v[112:115], a[80:83], a[252:255], v[112:115]\n\t" \
  "ds_read_b64_tr_b16 a[18:19], v188 offset:41984\n\t" \
  "v_mfma_f32_16x16x32_bf16 v[112:115], a[144:147], a[236:239], v[112:115]\n\t" \
  "ds_read_b64_tr_b16 a[20:21], v189 offset:40960\n\t" \
  "v_mfma_f32_16x16x32_bf16 v[20:23], a[148:151], a[224:227], v[20:23]\n\t" \
  "v_mfma_f32_16x16x32_bf16 v[20:23], a[84:87], a[240:243], v[20:23]\n\t" \
  "buffer_load_dwordx4 a[208:211], v248, %[ws], s97 offen sc1\n\t" \
  "v_mfma_f32_16x16x32_bf16 v[52:55], a[84:87], a[244:247], v[52:55]\n\t" \
  "ds_read_b64_tr_b16 a[22:23], v189 offset:41984\n\t" \
  "v_mfma_f32_16x16x32_bf16 v[52:55], a[148:151], a[228:231], v[52:55]\n\t" \
  "ds_read_b64_tr_b16 a[24:25], v190 offset:40960\n\t" \
  "v_mfma_f32_16x16x32_bf16 v[84:87], a[148:151], a[232:235], v[84:87]\n\t" \
  "ds_read_b64_tr_b16 a[26:27], v190 offset:41984\n\t" \
  "v_mfma_f32_16x16x32_bf16 v[84:87], a[84:87], a[248:251], v[84:87]\n\t" \
  "ds_read_b64_tr_b16 a[28:29], v191 offset:40960\n\t" \
  "v_mfma_f32_16x16x32_bf16 v[116:119], a[84:87], a[252:255], v[116:119]\n\t" \
  "buffer_load_dwordx4 a[212:215], v249, %[ws], s97 offen sc1\n\t" \
  "v_mfma_f32_16x16x32_bf16 v[116:119], a[148:151], a[236:239], v[116:119]\n\t" \
  "ds_read_b64_tr_b16 a[30:31], v191 offset:41984\n\t" \
  "v_mfma_f32_16x16x32_bf16 v[24:27], a[152:155], a[224:227], v[24:27]\n\t" \
  "v_mfma_f32_16x16x32_bf16 v[24:27], a[88:91], a[240:243], v[24:27]\n\t" \
  "v_mfma_f32_16x16x32_bf16 v[56:59], a[88:91], a[244:247], v[56:59]\n\t" \
  "buffer_load_dwordx4 a[216:219], v250, %[ws], s97 offen sc1\n\t" \
  "v_mfma_f32_16x16x32_bf16 v[56:59], a[152:155], a[228:231], v[56:59]\n\t" \
  "v_mfma_f32_16x16x32_bf16 v[88:91], a[152:155], a[232:235], v[88:91]\n\t" \
  "v_mfma_f32_16x16x32_bf16 v[88:91], a[88:91], a[248:251], v[88:91]\n\t" \
  "v_mfma_f32_16x16x32_bf16 v[120:123], a[88:91], a[252:255], v[120:123]\n\t" \
  "v_mfma_f32_16x16x32_bf16 v[120:123], a[152:155], a[236:239], v[120:123]\n\t" \
  "buffer_load_dwordx4 a[220:223], v251, %[ws], s97 offen sc1\n\t" \
  "v_mfma_f32_16x16x32_bf16 v[28:31], a[156:159], a[224:227], v[28:31]\n\t" \
  "v_mfma_f32_16x16x32_bf16 v[28:31], a[92:95], a[240:243], v[28:31]\n\t" \
  "v_mfma_f32_16x16x32_bf16 v[60:63], a[92:95], a[244:247], v[60:63]\n\t" \
  "v_mfma_f32_16x16x32_bf16 v[60:63], a[156:159], a[228:231], v[60:63]\n\t" \
  "v_mfma_f32_16x16x32_bf16 v[92:95], a[156:159], a[232:235], v[92:95]\n\t" \
  "v_mfma_f32_16x16x32_bf16 v[92:95], a[92:95], a[248:251], v[92:95]\n\t" \
  "v_mfma_f32_16x16x32_bf16 v[124:127], a[92:95], a[252:255], v[124:127]\n\t" \
  "v_mfma_f32_16x16x32_bf16 v[124:127], a[156:159], a[236:239], v[124:127]\n\t" \
  "s_waitcnt vmcnt(26) lgkmcnt(0)\n\t" \
  "s_barrier\n\t" \
  "v_mfma_f32_16x16x32_bf16 v[0:3], a[160:163], a[32:35], v[0:3]\n\t" \
  "s_add_u32 s97, %[rot19], 7864320\n\t" \
  "ds_read_b64_tr_b16 a[128:129], v184 offset:49152\n\t" \
  "v_mfma_f32_16x16x32_bf16 v[0:3], a[0:3], a[48:51], v[0:3]\n\t" \
  "s_bfe_u32 s97, s97, 0x140003\n\t" \
  "v_mfma_f32_16x16x32_bf16 v[32:35], a[0:3], a[52:55], v[32:35]\n\t" \
  "s_add_u32 s97, s97, %[sox]\n\t" \
  "ds_read_b64_tr_b16 a[130:131], v184 offset:50176\n\t" \
  "v_mfma_f32_16x16x32_bf16 v[32:35], a[160:163], a[36:39], v[32:35]\n\t" \
  "buffer_load_dwordx4 a[224:227], v248, %[ws], s97 offen sc1\n\t" \
  "v_mfma_f32_16x16x32_bf16 v[64:67], a[160:163], a[40:43], v[64:67]\n\t" \
  "ds_read_b64_tr_b16 a[132:133], v185 offset:49152\n\t" \
  "v_mfma_f32_16x16x32_bf16 v[64:67], a[0:3], a[56:59], v[64:67]\n\t" \
  "ds_read_b64_tr_b16 a[134:135], v185 offset:50176\n\t" \
  "v_mfma_f32_16x16x32_bf16 v[96:99], a[0:3], a[60:63], v[96:99]\n\t" \
  "buffer_load_dwordx4 a[228:231], v249, %[ws], s97 offen sc1\n\t" \
  "v_mfma_f32_16x16x32_bf16 v[96:99], a[160:163], a[44:47], v[96:99]\n\t" \
  "ds_read_b64_tr_b16 a[136:137], v186 offset:49152\n\t" \
  "v_mfma_f32_16x16x32_bf16 v[4:7], a[164:167], a[32:35], v[4:7]\n\t" \
  "ds_read_b64_tr_b16 a[138:139], v186 offset:50176\n\t" \
  "v_mfma_f32_16x16x32_bf16 v[4:7], a[4:7], a[48:51], v[4:7]\n\t" \
  "ds_read_b64_tr_b16 a[140:141], v187 offset:49152\n\t" \
  "v_mfma_f32_16x16x32_bf16 v[36:39], a[4:7], a[52:55], v[36:39]\n\t" \
  "v_mfma_f32_16x16x32_bf16 v[36:39], a[164:167], a[36:39], v[36:39]\n\t" \
  "ds_read_b64_tr_b16 a[142:143], v187 offset:50176\n\t" \
  "v_mfma_f32_16x16x32_bf16 v[68:71], a[164:167], a[40:43], v[68:71]\n\t" \
  "buffer_load_dwordx4 a[232:235], v250, %[ws], s97 offen sc1\n\t" \
  "v_mfma_f32_16x16x32_bf16 v[68:71], a[4:7], a[56:59], v[68:71]\n\t" \
  "ds_read_b64_tr_b16 a[144:145], v188 offset:49152\n\t" \
  "v_mfma_f32_16x16x32_bf16 v[100:103], a[4:7], a[60:63], v[100:103]\n\t" \
  "ds_read_b64_tr_b16 a[146:147], v188 offset:50176\n\t" \
  "v_mfma_f32_16x16x32_bf16 v[100:103], a[164:167], a[44:47], v[100:103]\n\t" \
  "ds_read_b64_tr_b16 a[148:149], v189 offset:49152\n\t" \
  "v_mfma_f32_16x16x32_bf16 v[8:11], a[168:171], a[32:35], v[8:11]\n\t" \
  "v_mfma_f32_16x16x32_bf16 v[8:11], a[8:11], a[48:51], v[8:11]\n\t" \
  "ds_read_b64_tr_b16 a[150:151], v189 offset:50176\n\t" \
  "v_mfma_f32_16x16x32_bf16 v[40:43], a[8:11], a[52:55], v[40:43]\n\t" \
  "buffer_load_dwordx4 a[236:239], v251, %[ws], s97 offen sc1\n\t" \
  "v_mfma_f32_16x16x32_bf16 v[40:43], a[168:171], a[36:39], v[40:43]\n\t" \
  "ds_read_b64_tr_b16 a[152:153], v190 offset:49152\n\t" \
  "v_mfma_f32_16x16x32_bf16 v[72:75], a[168:171], a[40:43], v[72:75]\n\t" \
  "s_add_u32 s97, s97, %[dk]\n\t" \
  "ds_read_b64_tr_b16 a[154:155], v190 offset:50176\n\t" \
  "v_mfma_f32_16x16x32_bf16 v[72:75], a[8:11], a[56:59], v[72:75]\n\t" \
  "ds_read_b64_tr_b16 a[156:157], v191 offset:49152\n\t" \
  "v_mfma_f32_16x16x32_bf16 v[104:107], a[8:11], a[60:63], v[104:107]\n\t" \
  "v_mfma_f32_16x16x32_bf16 v[104:107], a[168:171], a[44:47], v[104:107]\n\t" \
  "ds_read_b64_tr_b16 a[158:159], v191 offset:50176\n\t" \
  "v_mfma_f32_16x16x32_bf16 v[12:15], a[172:175], a[32:35], v[12:15]\n\t" \
  "buffer_load_dwordx4 a[240:243], v248, %[ws], s97 offen sc1\n\t" \
  "v_mfma_f32_16x16x32_bf16 v[12:15], a[12:15], a[48:51], v[12:15]\n\t" \
  "ds_read_b64_tr_b16 a[64:65], v184 offset:57344\n\t" \
  "v_mfma_f32_16x16x32_bf16 v[44:47], a[12:15], a[52:55], v[44:47]\n\t" \
  "ds_read_b64_tr_b16 a[66:67], v184 offset:58368\n\t" \
  "v_mfma_f32_16x16x32_bf16 v[44:47], a[172:175], a[36:39], v[44:47]\n\t" \
  "ds_read_b64_tr_b16 a[68:69], v185 offset:57344\n\t" \
  "v_mfma_f32_16x16x32_bf16 v[76:79], a[172:175], a[40:43], v[76:79]\n\t" \
  "v_mfma_f32_16x16x32_bf16 v[76:79], a[12:15], a[56:59], v[76:79]\n\t" \
  "ds_read_b64_tr_b16 a[70:71], v185 offset:58368\n\t" \
  "v_mfma_f32_16x16x32_bf16 v[108:111], a[12:15], a[60:63], v[108:111]\n\t" \
  "buffer_load_dwordx4 a[244:247], v249, %[ws], s97 offen sc1\n\t" \
  "v_mfma_f32_16x16x32_bf16 v[108:111], a[172:175], a[44:47], v[108:111]\n\t" \
  "ds_read_b64_tr_b16 a[72:73], v186 offset:57344\n\t" \
  "v_mfma_f32_16x16x32_bf16 v[16:19], a[176:179], a[32:35], v[16:19]\n\t" \
  "ds_read_b64_tr_b16 a[74:75], v186 offset:58368\n\t" \
  "v_mfma_f32_16x16x32_bf16 v[16:19], a[16:19], a[48:51], v[16:19]\n\t" \
  "ds_read_b64_tr_b16 a[76:77], v187 offset:57344\n\t" \
  "v_mfma_f32_16x16x32_bf16 v[48:51], a[16:19], a[52:55], v[48:51]\n\t" \
  "v_mfma_f32_16x16x32_bf16 v[48:51], a[176:179], a[36:39], v[48:51]\n\t" \
  "ds_read_b64_tr_b16 a[78:79], v187 offset:58368\n\t" \
  "v_mfma_f32_16x16x32_bf16 v[80:83], a[176:179], a[40:43], v[80:83]\n\t" \
  "buffer_load_dwordx4 a[248:251], v250, %[ws], s97 offen sc1\n\t" \
  "v_mfma_f32_16x16x32_bf16 v[80:83], a[16:19], a[56:59], v[80:83]\n\t" \
  "ds_read_b64_tr_b16 a[80:81], v188 offset:57344\n\t" \
  "v_mfma_f32_16x16x32_bf16 v[112:115], a[16:19], a[60:63], v[112:115]\n\t" \
  "ds_read_b64_tr_b16 a[82:83], v188 offset:58368\n\t" \
  "v_mfma_f32_16x16x32_bf16 v[112:115], a[176:179], a[44:47], v[112:115]\n\t" \
  "ds_read_b64_tr_b16 a[84:85], v189 offset:57344\n\t" \
  "v_mfma_f32_16x16x32_bf16 v[20:23], a[180:183], a[32:35], v[20:23]\n\t" \
  "v_mfma_f32_16x16x32_bf16 v[20:23], a[20:23], a[48:51], v[20:23]\n\t" \
  "ds_read_b64_tr_b16 a[86:87], v189 offset:58368\n\t" \
  "v_mfma_f32_16x16x32_bf16 v[52:55], a[20:23], a[52:55], v[52:55]\n\t" \
  "buffer_load_dwordx4 a[252:255], v251, %[ws], s97 offen sc1\n\t" \
  "v_mfma_f32_16x16x32_bf16 v[52:55], a[180:183], a[36:39], v[52:55]\n\t" \
  "ds_read_b64_tr_b16 a[88:89], v190 offset:57344\n\t" \
  "v_mfma_f32_16x16x32_bf16 v[84:87], a[180:183], a[40:43], v[84:87]\n\t" \
  "ds_read_b64_tr_b16 a[90:91], v190 offset:58368\n\t" \
  "v_mfma_f32_16x16x32_bf16 v[84:87], a[20:23], a[56:59], v[84:87]\n\t" \
  "ds_read_b64_tr_b16 a[92:93], v191 offset:57344\n\t" \
  "v_mfma_f32_16x16x32_bf16 v[116:119], a[20:23], a[60:63], v[116:119]\n\t" \
  "v_mfma_f32_16x16x32_bf16 v[116:119], a[180:183], a[44:47], v[116:119]\n\t" \
  "ds_read_b64_tr_b16 a[94:95], v191 offset:58368\n\t" \
  "v_mfma_f32_16x16x32_bf16 v[24:27], a[184:187], a[32:35], v[24:27]\n\t" \
  "buffer_load_dword %[pf], v217, %[ws], %[pto] offen sc1\n\t" \
  "v_mfma_f32_16x16x32_bf16 v[24:27], a[24:27], a[48:51], v[24:27]\n\t" \
  "v_mfma_f32_16x16x32_bf16 v[56:59], a[24:27], a[52:55], v[56:59]\n\t" \
  "s_add_u32 s99, %[pf4], 0\n\t" \
  "v_mfma_f32_16x16x32_bf16 v[56:59], a[184:187], a[36:39], v[56:59]\n\t" \
  "v_mfma_f32_16x16x32_bf16 v[88:91], a[184:187], a[40:43], v[88:91]\n\t" \
  "v_mfma_f32_16x16x32_bf16 v[88:91], a[24:27], a[56:59], v[88:91]\n\t" \
  "v_mfma_f32_16x16x32_bf16 v[120:123], a[24:27], a[60:63], v[120:123]\n\t" \
  "buffer_load_dword %[pf], v245, %[sU], s99 offen\n\t" \
  "v_mfma_f32_16x16x32_bf16 v[120:123], a[184:187], a[44:47], v[120:123]\n\t" \
  "v_mfma_f32_16x16x32_bf16 v[28:31], a[188:191], a[32:35], v[28:31]\n\t" \
  "v_mfma_f32_16x16x32_bf16 v[28:31], a[28:31], a[48:51], v[28:31]\n\t" \
  "v_mfma_f32_16x16x32_bf16 v[60:63], a[28:31], a[52:55], v[60:63]\n\t" \
  "v_mfma_f32_16x16x32_bf16 v[60:63], a[188:191], a[36:39], v[60:63]\n\t" \
  "v_mfma_f32_16x16x32_bf16 v[92:95], a[188:191], a[40:43], v[92:95]\n\t" \
  "v_mfma_f32_16x16x32_bf16 v[92:95], a[28:31], a[56:59], v[92:95]\n\t" \
  "v_mfma_f32_16x16x32_bf16 v[124:127], a[28:31], a[60:63], v[124:127]\n\t" \
  "v_mfma_f32_16x16x32_bf16 v[124:127], a[188:191], a[44:47], v[124:127]\n\t" \
  "s_waitcnt vmcnt(23) lgkmcnt(0)\n\t" \
  "s_barrier\n\t" \
  "v_mfma_f32_16x16x32_bf16 v[0:3], a[128:131], a[96:99], v[0:3]\n\t" \
  "s_add_u32 s99, %[pf4], 14336\n\t" \
  "ds_read_b64_tr_b16 a[160:161], v184 offset:0\n\t" \
  "v_mfma_f32_16x16x32_bf16 v[0:3], a[64:67], a[112:115], v[0:3]\n\t" \
  "buffer_load_dword %[pf], v245, %[sU], s99 offen\n\t" \
  "v_mfma_f32_16x16x32_bf16 v[32:35], a[64:67], a[116:119], v[32:35]\n\t" \
  "ds_read_b64_tr_b16 a[162:163], v184 offset:1024\n\t" \
  "v_mfma_f32_16x16x32_bf16 v[32:35], a[128:131], a[100:103], v[32:35]\n\t" \
  "ds_read_b64_tr_b16 a[164:165], v185 offset:0\n\t" \
  "v_mfma_f32_16x16x32_bf16 v[64:67], a[128:131], a[104:107], v[64:67]\n\t" \
  "s_add_u32 m0, %[mw], 16384\n\t" \
  "v_mfma_f32_16x16x32_bf16 v[64:67], a[64:67], a[120:123], v[64:67]\n\t" \
  "s_add_u32 s96, %[sofk], 0\n\t" \
  "ds_read_b64_tr_b16 a[166:167], v185 offset:1024\n\t" \
  "v_mfma_f32_16x16x32_bf16 v[96:99], a[64:67], a[124:127], v[96:99]\n\t" \
  "ds_read_b64_tr_b16 a[168:169], v186 offset:0\n\t" \
  "v_mfma_f32_16x16x32_bf16 v[96:99], a[128:131], a[108:111], v[96:99]\n\t" \
  "v_mfma_f32_16x16x32_bf16 v[4:7], a[132:135], a[96:99], v[4:7]\n\t" \
  "buffer_load_dwordx4 v224, %[sK], s96 offen offset:0 lds\n\t" \
  "v_mfma_f32_16x16x32_bf16 v[4:7], a[68:71], a[112:115], v[4:7]\n\t" \
  "ds_read_b64_tr_b16 a[170:171], v186 offset:1024\n\t" \
  "v_mfma_f32_16x16x32_bf16 v[36:39], a[68:71], a[116:119], v[36:39]\n\t" \
  "ds_read_b64_tr_b16 a[172:173], v187 offset:0\n\t" \
  "v_mfma_f32_16x16x32_bf16 v[36:39], a[132:135], a[100:103], v[36:39]\n\t" \
  "ds_read_b64_tr_b16 a[174:175], v187 offset:1024\n\t" \
  "v_mfma_f32_16x16x32_bf16 v[68:71], a[132:135], a[104:107], v[68:71]\n\t" \
  "ds_read_b64_tr_b16 a[176:177], v188 offset:0\n\t" \
  "v_mfma_f32_16x16x32_bf16 v[68:71], a[68:71], a[120:123], v[68:71]\n\t" \
  "v_mfma_f32_16x16x32_bf16 v[100:103], a[68:71], a[124:127], v[100:103]\n\t" \
  "ds_read_b64_tr_b16 a[178:179], v188 offset:1024\n\t" \
  "v_mfma_f32_16x16x32_bf16 v[100:103], a[132:135], a[108:111], v[100:103]\n\t" \
  "ds_read_b64_tr_b16 a[180:181], v189 offset:0\n\t" \
  "v_mfma_f32_16x16x32_bf16 v[8:11], a[136:139], a[96:99], v[8:11]\n\t" \
  "v_mfma_f32_16x16x32_bf16 v[8:11], a[72:75], a[112:115], v[8:11]\n\t" \
  "buffer_load_dwordx4 v225, %[sK], s96 offen offset:1024 lds\n\t" \
  "v_mfma_f32_16x16x32_bf16 v[40:43], a[72:75], a[116:119], v[40:43]\n\t" \
  "ds_read_b64_tr_b16 a[182:183], v189 offset:1024\n\t" \
  "v_mfma_f32_16x16x32_bf16 v[40:43], a[136:139], a[100:103], v[40:43]\n\t" \
  "ds_read_b64_tr_b16 a[184:185], v190 offset:0\n\t" \
  "v_mfma_f32_16x16x32_bf16 v[72:75], a[136:139], a[104:107], v[72:75]\n\t" \
  "ds_read_b64_tr_b16 a[186:187], v190 offset:1024\n\t" \
  "v_mfma_f32_16x16x32_bf16 v[72:75], a[72:75], a[120:123], v[72:75]\n\t" \
  "ds_read_b64_tr_b16 a[188:189], v191 offset:0\n\t" \
  "v_mfma_f32_16x16x32_bf16 v[104:107], a[72:75], a[124:127], v[104:107]\n\t" \
  "v_mfma_f32_16x16x32_bf16 v[104:107], a[136:139], a[108:111], v[104:107]\n\t" \
  "ds_read_b64_tr_b16 a[190:191], v191 offset:1024\n\t" \
  "v_mfma_f32_16x16x32_bf16 v[12:15], a[140:143], a[96:99], v[12:15]\n\t" \
  "ds_read_b64_tr_b16 a[0:1], v184 offset:8192\n\t" \
  "v_mfma_f32_16x16x32_bf16 v[12:15], a[76:79], a[112:115], v[12:15]\n\t" \
  "buffer_load_dwordx4 v226, %[sK], s96 offen offset:2048 lds\n\t" \
  "v_mfma_f32_16x16x32_bf16 v[44:47], a[76:79], a[116:119], v[44:47]\n\t" \
  "ds_read_b64_tr_b16 a[2:3], v184 offset:9216\n\t" \
  "v_mfma_f32_16x16x32_bf16 v[44:47], a[140:143], a[100:103], v[44:47]\n\t" \
  "ds_read_b64_tr_b16 a[4:5], v185 offset:8192\n\t" \
  "v_mfma_f32_16x16x32_bf16 v[76:79], a[140:143], a[104:107], v[76:79]\n\t" \
  "v_mfma_f32_16x16x32_bf16 v[76:79], a[76:79], a[120:123], v[76:79]\n\t" \
  "ds_read_b64_tr_b16 a[6:7], v185 offset:9216\n\t" \
  "v_mfma_f32_16x16x32_bf16 v[108:111], a[76:79], a[124:127], v[108:111]\n\t" \
  "ds_read_b64_tr_b16 a[8:9], v186 offset:8192\n\t" \
  "v_mfma_f32_16x16x32_bf16 v[108:111], a[140:143], a[108:111], v[108:111]\n\t" \
  "v_mfma_f32_16x16x32_bf16 v[16:19], a[144:147], a[96:99], v[16:19]\n\t" \
  "ds_read_b64_tr_b16 a[10:11], v186 offset:9216\n\t" \
  "v_mfma_f32_16x16x32_bf16 v[16:19], a[80:83], a[112:115], v[16:19]\n\t" \
  "ds_read_b64_tr_b16 a[12:13], v187 offset:8192\n\t" \
  "v_mfma_f32_16x16x32_bf16 v[48:51], a[80:83], a[116:119], v[48:51]\n\t" \
  "buffer_load_dwordx4 v227, %[sK], s96 offen offset:3072 lds\n\t" \
  "v_mfma_f32_16x16x32_bf16 v[48:51], a[144:147], a[100:103], v[48:51]\n\t" \
  "ds_read_b64_tr_b16 a[14:15], v187 offset:9216\n\t" \
  "v_mfma_f32_16x16x32_bf16 v[80:83], a[144:147], a[104:107], v[80:83]\n\t" \
  "ds_read_b64_tr_b16 a[16:17], v188 offset:8192\n\t" \
  "v_mfma_f32_16x16x32_bf16 v[80:83], a[80:83], a[120:123], v[80:83]\n\t" \
  "v_mfma_f32_16x16x32_bf16 v[112:115], a[80:83], a[124:127], v[112:115]\n\t" \
  "s_add_u32 m0, %[mw], 49152\n\t" \
  "ds_read_b64_tr_b16 a[18:19], v188 offset:9216\n\t" \
  "v_mfma_f32_16x16x32_bf16 v[112:115], a[144:147], a[108:111], v[112:115]\n\t" \
  "s_add_u32 s96, %[sofk], 0\n\t" \
  "ds_read_b64_tr_b16 a[20:21], v189 offset:8192\n\t" \
  "v_mfma_f32_16x16x32_bf16 v[20:23], a[148:151], a[96:99], v[20:23]\n\t" \
  "v_mfma_f32_16x16x32_bf16 v[20:23], a[84:87], a[112:115], v[20:23]\n\t" \
  "ds_read_b64_tr_b16 a[22:23], v189 offset:9216\n\t" \
  "v_mfma_f32_16x16x32_bf16 v[52:55], a[84:87], a[116:119], v[52:55]\n\t" \
  "buffer_load_dwordx4 v230, %[sV], s96 offen offset:0 lds\n\t" \
  "v_mfma_f32_16x16x32_bf16 v[52:55], a[148:151], a[100:103], v[52:55]\n\t" \
  "ds_read_b64_tr_b16 a[24:25], v190 offset:8192\n\t" \
  "v_mfma_f32_16x16x32_bf16 v[84:87], a[148:151], a[104:107], v[84:87]\n\t" \
  "ds_read_b64_tr_b16 a[26:27], v190 offset:9216\n\t" \
  "v_mfma_f32_16x16x32_bf16 v[84:87], a[84:87], a[120:123], v[84:87]\n\t" \
  "ds_read_b64_tr_b16 a[28:29], v191 offset:8192\n\t" \
  "v_mfma_f32_16x16x32_bf16 v[116:119], a[84:87], a[124:127], v[116:119]\n\t" \
  "v_mfma_f32_16x16x32_bf16 v[116:119], a[148:151], a[108:111], v[116:119]\n\t" \
  "ds_read_b64_tr_b16 a[30:31], v191 offset:9216\n\t" \
  "v_mfma_f32_16x16x32_bf16 v[24:27], a[152:155], a[96:99], v[24:27]\n\t" \
  "v_mfma_f32_16x16x32_bf16 v[24:27], a[88:91], a[112:115], v[24:27]\n\t" \
  "v_mfma_f32_16x16x32_bf16 v[56:59], a[88:91], a[116:119], v[56:59]\n\t" \
  "v_mfma_f32_16x16x32_bf16 v[56:59], a[152:155], a[100:103], v[56:59]\n\t" \
  "buffer_load_dwordx4 v231, %[sV], s96 offen offset:1024 lds\n\t" \
  "v_mfma_f32_16x16x32_bf16 v[88:91], a[152:155], a[104:107], v[88:91]\n\t" \
  "v_mfma_f32_16x16x32_bf16 v[88:91], a[88:91], a[120:123], v[88:91]\n\t" \
  "v_mfma_f32_16x16x32_bf16 v[120:123], a[88:91], a[124:127], v[120:123]\n\t" \
  "v_mfma_f32_16x16x32_bf16 v[120:123], a[152:155], a[108:111], v[120:123]\n\t" \
  "v_mfma_f32_16x16x32_bf16 v[28:31], a[156:159], a[96:99], v[28:31]\n\t" \
  "v_mfma_f32_16x16x32_bf16 v[28:31], a[92:95], a[112:115], v[28:31]\n\t" \
  "v_mfma_f32_16x16x32_bf16 v[60:63], a[92:95], a[116:119], v[60:63]\n\t" \
  "v_mfma_f32_16x16x32_bf16 v[60:63], a[156:159], a[100:103], v[60:63]\n\t" \
  "v_mfma_f32_16x16x32_bf16 v[92:95], a[156:159], a[104:107], v[92:95]\n\t" \
  "v_mfma_f32_16x16x32_bf16 v[92:95], a[92:95], a[120:123], v[92:95]\n\t" \
  "v_mfma_f32_16x16x32_bf16 v[124:127], a[92:95], a[124:127], v[124:127]\n\t" \
  "v_mfma_f32_16x16x32_bf16 v[124:127], a[156:159], a[108:111], v[124:127]\n\t" \
  "s_waitcnt vmcnt(17) lgkmcnt(0)\n\t" \
  "s_barrier\n\t" \
  "v_mfma_f32_16x16x32_bf16 v[0:3], a[160:163], a[192:195], v[0:3]\n\t" \
  "buffer_load_dwordx4 v230, %[sV], s96 offen offset:2048 lds\n\t" \
  "v_mfma_f32_16x16x32_bf16 v[0:3], a[0:3], a[208:211], v[0:3]\n\t" \
  "ds_read_b64_tr_b16 a[128:129], v192 offset:0\n\t" \
  "v_mfma_f32_16x16x32_bf16 v[32:35], a[0:3], a[212:215], v[32:35]\n\t" \
  "ds_read_b64_tr_b16 a[130:131], v192 offset:1024\n\t" \
  "v_mfma_f32_16x16x32_bf16 v[32:35], a[160:163], a[196:199], v[32:35]\n\t" \
  "ds_read_b64_tr_b16 a[132:133], v193 offset:0\n\t" \
  "v_mfma_f32_16x16x32_bf16 v[64:67], a[160:163], a[200:203], v[64:67]\n\t" \
  "v_mfma_f32_16x16x32_bf16 v[64:67], a[0:3], a[216:219], v[64:67]\n\t" \
  "ds_read_b64_tr_b16 a[134:135], v193 offset:1024\n\t" \
  "v_mfma_f32_16x16x32_bf16 v[96:99], a[0:3], a[220:223], v[96:99]\n\t" \
  "ds_read_b64_tr_b16 a[136:137], v194 offset:0\n\t" \
  "v_mfma_f32_16x16x32_bf16 v[96:99], a[160:163], a[204:207], v[96:99]\n\t" \
  "buffer_load_dwordx4 v231, %[sV], s96 offen offset:3072 lds\n\t" \
  "v_mfma_f32_16x16x32_bf16 v[4:7], a[164:167], a[192:195], v[4:7]\n\t" \
  "ds_read_b64_tr_b16 a[138:139], v194 offset:1024\n\t" \
  "v_mfma_f32_16x16x32_bf16 v[4:7], a[4:7], a[208:211], v[4:7]\n\t" \
  "ds_read_b64_tr_b16 a[140:141], v195 offset:0\n\t" \
  "v_mfma_f32_16x16x32_bf16 v[36:39], a[4:7], a[212:215], v[36:39]\n\t" \
  "v_mfma_f32_16x16x32_bf16 v[36:39], a[164:167], a[196:199], v[36:39]\n\t" \
  "s_add_u32 m0, %[mw], 32768\n\t" \
  "ds_read_b64_tr_b16 a[142:143], v195 offset:1024\n\t" \
  "v_mfma_f32_16x16x32_bf16 v[68:71], a[164:167], a[200:203], v[68:71]\n\t" \
  "s_add_u32 s96, %[sofk], 16384\n\t" \
  "ds_read_b64_tr_b16 a[144:145], v196 offset:0\n\t" \
  "v_mfma_f32_16x16x32_bf16 v[68:71], a[4:7], a[216:219], v[68:71]\n\t" \
  "v_mfma_f32_16x16x32_bf16 v[100:103], a[4:7], a[220:223], v[100:103]\n\t" \
  "ds_read_b64_tr_b16 a[146:147], v196 offset:1024\n\t" \
  "v_mfma_f32_16x16x32_bf16 v[100:103], a[164:167], a[204:207], v[100:103]\n\t" \
  "buffer_load_dwordx4 v224, %[sK], s96 offen offset:0 lds\n\t" \
  "v_mfma_f32_16x16x32_bf16 v[8:11], a[168:171], a[192:195], v[8:11]\n\t" \
  "ds_read_b64_tr_b16 a[148:149], v197 offset:0\n\t" \
  "v_mfma_f32_16x16x32_bf16 v[8:11], a[8:11], a[208:211], v[8:11]\n\t" \
  "ds_read_b64_tr_b16 a[150:151], v197 offset:1024\n\t" \
  "v_mfma_f32_16x16x32_bf16 v[40:43], a[8:11], a[212:215], v[40:43]\n\t" \
  "ds_read_b64_tr_b16 a[152:153], v198 offset:0\n\t" \
  "v_mfma_f32_16x16x32_bf16 v[40:43], a[168:171], a[196:199], v[40:43]\n\t" \
  "v_mfma_f32_16x16x32_bf16 v[72:75], a[168:171], a[200:203], v[72:75]\n\t" \
  "ds_read_b64_tr_b16 a[154:155], v198 offset:1024\n\t" \
  "v_mfma_f32_16x16x32_bf16 v[72:75], a[8:11], a[216:219], v[72:75]\n\t" \
  "ds_read_b64_tr_b16 a[156:157], v199 offset:0\n\t" \
  "v_mfma_f32_16x16x32_bf16 v[104:107], a[8:11], a[220:223], v[104:107]\n\t" \
  "buffer_load_dwordx4 v225, %[sK], s96 offen offset:1024 lds\n\t" \
  "v_mfma_f32_16x16x32_bf16 v[104:107], a[168:171], a[204:207], v[104:107]\n\t" \
  "ds_read_b64_tr_b16 a[158:159], v199 offset:1024\n\t" \
  "v_mfma_f32_16x16x32_bf16 v[12:15], a[172:175], a[192:195], v[12:15]\n\t" \
  "ds_read_b64_tr_b16 a[64:65], v192 offset:8192\n\t" \
  "v_mfma_f32_16x16x32_bf16 v[12:15], a[12:15], a[208:211], v[12:15]\n\t" \
  "v_mfma_f32_16x16x32_bf16 v[44:47], a[12:15], a[212:215], v[44:47]\n\t" \
  "ds_read_b64_tr_b16 a[66:67], v192 offset:9216\n\t" \
  "v_mfma_f32_16x16x32_bf16 v[44:47], a[172:175], a[196:199], v[44:47]\n\t" \
  "ds_read_b64_tr_b16 a[68:69], v193 offset:8192\n\t" \
  "v_mfma_f32_16x16x32_bf16 v[76:79], a[172:175], a[200:203], v[76:79]\n\t" \
  "v_mfma_f32_16x16x32_bf16 v[76:79], a[12:15], a[216:219], v[76:79]\n\t" \
  "ds_read_b64_tr_b16 a[70:71], v193 offset:9216\n\t" \
  "v_mfma_f32_16x16x32_bf16 v[108:111], a[12:15], a[220:223], v[108:111]\n\t" \
  "buffer_load_dwordx4 v226, %[sK], s96 offen offset:2048 lds\n\t" \
  "v_mfma_f32_16x16x32_bf16 v[108:111], a[172:175], a[204:207], v[108:111]\n\t" \
  "ds_read_b64_tr_b16 a[72:73], v194 offset:8192\n\t" \
  "v_mfma_f32_16x16x32_bf16 v[16:19], a[176:179], a[192:195], v[16:19]\n\t" \
  "ds_read_b64_tr_b16 a[74:75], v194 offset:9216\n\t" \
  "v_mfma_f32_16x16x32_bf16 v[16:19], a[16:19], a[208:211], v[16:19]\n\t" \
  "ds_read_b64_tr_b16 a[76:77], v195 offset:8192\n\t" \
  "v_mfma_f32_16x16x32_bf16 v[48:51], a[16:19], a[212:215], v[48:51]\n\t" \
  "v_mfma_f32_16x16x32_bf16 v[48:51], a[176:179], a[196:199], v[48:51]\n\t" \
  "ds_read_b64_tr_b16 a[78:79], v195 offset:9216\n\t" \
  "v_mfma_f32_16x16x32_bf16 v[80:83], a[176:179], a[200:203], v[80:83]\n\t" \
  "ds_read_b64_tr_b16 a[80:81], v196 offset:8192\n\t" \
  "v_mfma_f32_16x16x32_bf16 v[80:83], a[16:19], a[216:219], v[80:83]\n\t" \
  "buffer_load_dwordx4 v227, %[sK], s96 offen offset:3072 lds\n\t" \
  "v_mfma_f32_16x16x32_bf16 v[112:115], a[16:19], a[220:223], v[112:115]\n\t" \
  "ds_read_b64_tr_b16 a[82:83], v196 offset:9216\n\t" \
  "v_mfma_f32_16x16x32_bf16 v[112:115], a[176:179], a[204:207], v[112:115]\n\t" \
  "ds_read_b64_tr_b16 a[84:85], v197 offset:8192\n\t" \
  "v_mfma_f32_16x16x32_bf16 v[20:23], a[180:183], a[192:195], v[20:23]\n\t" \
  "v_mfma_f32_16x16x32_bf16 v[20:23], a[20:23], a[208:211], v[20:23]\n\t" \
  "s_add_u32 s99, %[pf4], 28672\n\t" \
  "ds_read_b64_tr_b16 a[86:87], v197 offset:9216\n\t" \
  "v_mfma_f32_16x16x32_bf16 v[52:55], a[20:23], a[212:215], v[52:55]\n\t" \
  "ds_read_b64_tr_b16 a[88:89], v198 offset:8192\n\t" \
  "v_mfma_f32_16x16x32_bf16 v[52:55], a[180:183], a[196:199], v[52:55]\n\t" \
  "v_mfma_f32_16x16x32_bf16 v[84:87], a[180:183], a[200:203], v[84:87]\n\t" \
  "ds_read_b64_tr_b16 a[90:91], v198 offset:9216\n\t" \
  "v_mfma_f32_16x16x32_bf16 v[84:87], a[20:23], a[216:219], v[84:87]\n\t" \
  "buffer_load_dword %[pf], v245, %[sU], s99 offen\n\t" \
  "v_mfma_f32_16x16x32_bf16 v[116:119], a[20:23], a[220:223], v[116:119]\n\t" \
  "ds_read_b64_tr_b16 a[92:93], v199 offset:8192\n\t" \
  "v_mfma_f32_16x16x32_bf16 v[116:119], a[180:183], a[204:207], v[116:119]\n\t" \
  "ds_read_b64_tr_b16 a[94:95], v199 offset:9216\n\t" \
  "v_mfma_f32_16x16x32_bf16 v[24:27], a[184:187], a[192:195], v[24:27]\n\t" \
  "s_add_u32 s99, %[pf4], 43008\n\t" \
  "v_mfma_f32_16x16x32_bf16 v[24:27], a[24:27], a[208:211], v[24:27]\n\t" \
  "v_mfma_f32_16x16x32_bf16 v[56:59], a[24:27], a[212:215], v[56:59]\n\t" \
  "v_mfma_f32_16x16x32_bf16 v[56:59], a[184:187], a[196:199], v[56:59]\n\t" \
  "v_mfma_f32_16x16x32_bf16 v[88:91], a[184:187], a[200:203], v[88:91]\n\t" \
  "buffer_load_dword %[pf], v245, %[sU], s99 offen\n\t" \
  "v_mfma_f32_16x16x32_bf16 v[88:91], a[24:27], a[216:219], v[88:91]\n\t" \
  "v_mfma_f32_16x16x32_bf16 v[120:123], a[24:27], a[220:223], v[120:123]\n\t" \
  "v_mfma_f32_16x16x32_bf16 v[120:123], a[184:187], a[204:207], v[120:123]\n\t" \
  "v_mfma_f32_16x16x32_bf16 v[28:31], a[188:191], a[192:195], v[28:31]\n\t" \
  "v_mfma_f32_16x16x32_bf16 v[28:31], a[28:31], a[208:211], v[28:31]\n\t" \
  "v_mfma_f32_16x16x32_bf16 v[60:63], a[28:31], a[212:215], v[60:63]\n\t" \
  "v_mfma_f32_16x16x32_bf16 v[60:63], a[188:191], a[196:199], v[60:63]\n\t" \
  "v_mfma_f32_16x16x32_bf16 v[92:95], a[188:191], a[200:203], v[92:95]\n\t" \
  "v_mfma_f32_16x16x32_bf16 v[92:95], a[28:31], a[216:219], v[92:95]\n\t" \
  "v_mfma_f32_16x16x32_bf16 v[124:127], a[28:31], a[220:223], v[124:127]\n\t" \
  "v_mfma_f32_16x16x32_bf16 v[124:127], a[188:191], a[204:207], v[124:127]\n\t" \
  "s_waitcnt vmcnt(17)\n\t" \
  "s_waitcnt lgkmcnt(0)\n\t" \
  FSTP("s_waitcnt lgkmcnt(0)\n\t") \
  FSTP("v_writelane_b32 %[stv], s94, 52\n\t") \
  FSTP("v_writelane_b32 %[stv], s92, 53\n\t") \
  ".subsection 1\n\t" \
  ".Lfq_s:\n\t" \
  "s_mov_b32 s97, 0\n\t" \
  ".Lfq_r:\n\t" \
  "buffer_load_dword %[fl], v210, %[ws], %[fo] offen sc1\n\t" \
  "s_waitcnt vmcnt(0)\n\t" \
  "v_cmp_ne_u32 vcc, v211, %[fl]\n\t" \
  "s_cbranch_vccz .Lfq_k\n\t" \
  "s_add_u32 s97, s97, 1\n\t" \
  "s_cmp_lt_u32 s97, " FQ_CAP "\n\t" \
  "s_cbranch_scc1 .Lfq_r\n\t" \
  "buffer_store_dword v211, off, %[ws], 0 offset:256 sc1\n\t" \
  "s_branch .Lfq_k\n\t" \
  ".subsection 0\n\t"
#define FQ_CL "memory", "scc", "vcc", "s96", "s97", "s98", "s99", "v128", "v129", "v130", "v131", "v132", "v133", "v134", "v135", "v136", "v137", "v138", "v139", "v140", "v141", "v142", "v143", "v144", "v145", "v146", "v147", "v148", "v149", "v150", "v151", "v152", "v153", "v154", "v155", "v156", "v157", "v158", "v159", "v176", "v177", "v178", "v179", "v180", "v181", "v182", "v183", "v249", "v250", "v251" FQ_STC
#define FQ_X2E 0
#define FQ_W1L 1
#define FQ_W2E 0
static_assert(W_VOTE == 8192 && W_FLAGR == 32768 && W_FLAGO == 131072 && W_MODB == 2228224 && W_SIL == 2359296 && W_OT == 8388608 && W_XCH == 25165824);
#define FQ_EV 0
#define FQ_EB 0
#define FQ_CTO
#define FQ_XD 1
#define STS(x)
#define AT_STV
#define STA(x)
#define AT_ASM \
  "s_cmp_eq_u32 %[pm], 0\n\t" \
  "s_cbranch_scc1 .Lsf_go\n\t" \
  "v_mfma_f32_16x16x32_bf16 v[28:31], a[156:159], a[224:227], v[28:31]\n\t" \
  "v_mov_b32 v216, 0x3f803f80\n\t" \
  "v_lshlrev_b32 v204, 7, v233\n\t" \
  "v_mfma_f32_16x16x32_bf16 v[28:31], a[92:95], a[240:243], v[28:31]\n\t" \
  "v_cmp_gt_u32 vcc, 16, v233\n\t" \
  "v_mov_b32 v217, 0x3f803f80\n\t" \
  "v_mfma_f32_16x16x32_bf16 v[24:27], a[152:155], a[224:227], v[24:27]\n\t" \
  "v_lshrrev_b32 v202, 3, v233\n\t" \
  "v_mov_b32 v218, 0x3f803f80\n\t" \
  "v_mfma_f32_16x16x32_bf16 v[24:27], a[88:91], a[240:243], v[24:27]\n\t" \
  "v_mul_u32_u24 v202, 11264, v202\n\t" \
  "v_mov_b32 v219, 0x3f803f80\n\t" \
  "v_mfma_f32_16x16x32_bf16 v[20:23], a[148:151], a[224:227], v[20:23]\n\t" \
  "v_cvt_pk_bf16_f32 v31, v30, v31\n\t" \
  "v_add_u32 v202, v202, v204\n\t" \
  "v_mfma_f32_16x16x32_bf16 v[20:23], a[84:87], a[240:243], v[20:23]\n\t" \
  "v_cvt_pk_bf16_f32 v30, v28, v29\n\t" \
  "v_lshlrev_b32 v242, 4, v233\n\t" \
  "v_mfma_f32_16x16x32_bf16 v[16:19], a[144:147], a[224:227], v[16:19]\n\t" \
  "s_mov_b32 s99, 0\n\t" \
  "v_bfrev_b32 v195, 1\n\t" \
  "v_cvt_pk_bf16_f32 v28, v24, v25\n\t" \
  "v_mfma_f32_16x16x32_bf16 v[16:19], a[80:83], a[240:243], v[16:19]\n\t" \
  "v_cvt_pk_bf16_f32 v29, v26, v27\n\t" \
  "v_add_u32 v237, 65536, v228\n\t" \
  "v_mfma_f32_16x16x32_bf16 v[12:15], a[140:143], a[224:227], v[12:15]\n\t" \
  "v_cndmask_b32 v204, v195, v204, vcc\n\t" \
  "v_cndmask_b32 v202, v195, v202, vcc\n\t" \
  "v_mfma_f32_16x16x32_bf16 v[12:15], a[76:79], a[240:243], v[12:15]\n\t" \
  "v_cvt_pk_bf16_f32 v27, v22, v23\n\t" \
  "v_add_u32 v238, 65536, v229\n\t" \
  "v_mfma_f32_16x16x32_bf16 v[8:11], a[136:139], a[224:227], v[8:11]\n\t" \
  "s_mov_b32 %[na], 0\n\t" \
  "v_permlane16_swap_b32 v28, v30\n\t" \
  "v_mfma_f32_16x16x32_bf16 v[8:11], a[72:75], a[240:243], v[8:11]\n\t" \
  "v_cvt_pk_bf16_f32 v26, v20, v21\n\t" \
  "v_cvt_pk_bf16_f32 v24, v16, v17\n\t" \
  "v_mfma_f32_16x16x32_bf16 v[4:7], a[132:135], a[224:227], v[4:7]\n\t" \
  "v_permlane16_swap_b32 v29, v31\n\t" \
  "v_mfma_f32_16x16x32_bf16 v[4:7], a[68:71], a[240:243], v[4:7]\n\t" \
  "v_cvt_pk_bf16_f32 v25, v18, v19\n\t" \
  "v_cvt_pk_bf16_f32 v23, v14, v15\n\t" \
  "v_mfma_f32_16x16x32_bf16 v[0:3], a[128:131], a[224:227], v[0:3]\n\t" \
  "v_cvt_pk_bf16_f32 v22, v12, v13\n\t" \
  "v_cvt_pk_bf16_f32 v20, v8, v9\n\t" \
  "v_mfma_f32_16x16x32_bf16 v[0:3], a[64:67], a[240:243], v[0:3]\n\t" \
  "s_waitcnt vmcnt(2)\n\t" \
  "s_barrier\n\t" \
  "v_mfma_f32_16x16x32_bf16 v[60:63], a[156:159], a[228:231], v[60:63]\n\t" \
  "ds_read_b128 a[0:3], v220 offset:0\n\t" \
  "v_cvt_pk_bf16_f32 v21, v10, v11\n\t" \
  "v_mfma_f32_16x16x32_bf16 v[60:63], a[92:95], a[244:247], v[60:63]\n\t" \
  "ds_read_b128 a[4:7], v221 offset:0\n\t" \
  "v_cvt_pk_bf16_f32 v19, v6, v7\n\t" \
  "v_mfma_f32_16x16x32_bf16 v[56:59], a[152:155], a[228:231], v[56:59]\n\t" \
  "s_add_u32 m0, %[m], 0\n\t" \
  "ds_read_b128 a[8:11], v222 offset:0\n\t" \
  "v_cvt_pk_bf16_f32 v18, v4, v5\n\t" \
  "v_mfma_f32_16x16x32_bf16 v[56:59], a[88:91], a[244:247], v[56:59]\n\t" \
  "s_xor_b32 s97, %[wob], 0\n\t" \
  "ds_read_b128 a[12:15], v223 offset:0\n\t" \
  "v_cvt_pk_bf16_f32 v16, v0, v1\n\t" \
  "v_mfma_f32_16x16x32_bf16 v[52:55], a[148:151], a[228:231], v[52:55]\n\t" \
  "ds_read_b128 a[16:19], v220 offset:4096\n\t" \
  "v_cvt_pk_bf16_f32 v17, v2, v3\n\t" \
  "v_mfma_f32_16x16x32_bf16 v[52:55], a[84:87], a[244:247], v[52:55]\n\t" \
  "ds_read_b128 a[20:23], v221 offset:4096\n\t" \
  "v_cvt_pk_bf16_f32 v63, v62, v63\n\t" \
  "v_mfma_f32_16x16x32_bf16 v[48:51], a[144:147], a[228:231], v[48:51]\n\t" \
  "ds_read_b128 a[24:27], v222 offset:4096\n\t" \
  "v_cvt_pk_bf16_f32 v62, v60, v61\n\t" \
  "v_mfma_f32_16x16x32_bf16 v[48:51], a[80:83], a[244:247], v[48:51]\n\t" \
  "ds_read_b128 a[28:31], v223 offset:4096\n\t" \
  "v_cvt_pk_bf16_f32 v60, v56, v57\n\t" \
  "v_mfma_f32_16x16x32_bf16 v[44:47], a[140:143], a[228:231], v[44:47]\n\t" \
  "ds_read_b128 a[32:35], v220 offset:8192\n\t" \
  "v_cvt_pk_bf16_f32 v61, v58, v59\n\t" \
  "v_mfma_f32_16x16x32_bf16 v[44:47], a[76:79], a[244:247], v[44:47]\n\t" \
  "buffer_load_dwordx4 v240, %[wo], s97 offen offset:0 lds\n\t" \
  "v_mfma_f32_16x16x32_bf16 v[40:43], a[136:139], a[228:231], v[40:43]\n\t" \
  "s_add_u32 s97, s97, 7168\n\t" \
  "ds_read_b128 a[36:39], v221 offset:8192\n\t" \
  "v_cvt_pk_bf16_f32 v59, v54, v55\n\t" \
  "v_mfma_f32_16x16x32_bf16 v[40:43], a[72:75], a[244:247], v[40:43]\n\t" \
  "ds_read_b128 a[40:43], v222 offset:8192\n\t" \
  "v_cvt_pk_bf16_f32 v58, v52, v53\n\t" \
  "v_mfma_f32_16x16x32_bf16 v[36:39], a[132:135], a[228:231], v[36:39]\n\t" \
  "ds_read_b128 a[44:47], v223 offset:8192\n\t" \
  "v_cvt_pk_bf16_f32 v56, v48, v49\n\t" \
  "v_mfma_f32_16x16x32_bf16 v[36:39], a[68:71], a[244:247], v[36:39]\n\t" \
  "buffer_load_dwordx4 v241, %[wo], s97 offen offset:1024 lds\n\t" \
  "v_mfma_f32_16x16x32_bf16 v[32:35], a[128:131], a[228:231], v[32:35]\n\t" \
  "s_add_u32 s97, s97, 7168\n\t" \
  "ds_read_b128 a[48:51], v220 offset:12288\n\t" \
  "v_cvt_pk_bf16_f32 v57, v50, v51\n\t" \
  "v_mfma_f32_16x16x32_bf16 v[32:35], a[64:67], a[244:247], v[32:35]\n\t" \
  "ds_read_b128 a[52:55], v221 offset:12288\n\t" \
  "v_cvt_pk_bf16_f32 v55, v46, v47\n\t" \
  "v_mfma_f32_16x16x32_bf16 v[92:95], a[156:159], a[232:235], v[92:95]\n\t" \
  "buffer_load_dwordx4 v240, %[wo], s97 offen offset:2048 lds\n\t" \
  "v_mfma_f32_16x16x32_bf16 v[92:95], a[92:95], a[248:251], v[92:95]\n\t" \
  "s_add_u32 s97, s97, 7168\n\t" \
  "ds_read_b128 a[56:59], v222 offset:12288\n\t" \
  "v_cvt_pk_bf16_f32 v54, v44, v45\n\t" \
  "v_mfma_f32_16x16x32_bf16 v[88:91], a[152:155], a[232:235], v[88:91]\n\t" \
  "ds_read_b128 a[60:63], v223 offset:12288\n\t" \
  "v_cvt_pk_bf16_f32 v52, v40, v41\n\t" \
  "v_mfma_f32_16x16x32_bf16 v[88:91], a[88:91], a[248:251], v[88:91]\n\t" \
  "v_permlane16_swap_b32 v24, v26\n\t" \
  "v_mfma_f32_16x16x32_bf16 v[84:87], a[148:151], a[232:235], v[84:87]\n\t" \
  "buffer_load_dwordx4 v241, %[wo], s97 offen offset:3072 lds\n\t" \
  "v_mfma_f32_16x16x32_bf16 v[84:87], a[84:87], a[248:251], v[84:87]\n\t" \
  "s_add_u32 m0, %[m], 16384\n\t" \
  "v_cvt_pk_bf16_f32 v53, v42, v43\n\t" \
  "v_cvt_pk_bf16_f32 v51, v38, v39\n\t" \
  "v_mfma_f32_16x16x32_bf16 v[80:83], a[144:147], a[232:235], v[80:83]\n\t" \
  "s_xor_b32 s97, %[wob], 131072\n\t" \
  "v_permlane16_swap_b32 v25, v27\n\t" \
  "v_mfma_f32_16x16x32_bf16 v[80:83], a[80:83], a[248:251], v[80:83]\n\t" \
  "v_cvt_pk_bf16_f32 v50, v36, v37\n\t" \
  "v_cvt_pk_bf16_f32 v48, v32, v33\n\t" \
  "v_mfma_f32_16x16x32_bf16 v[76:79], a[140:143], a[232:235], v[76:79]\n\t" \
  "v_permlane16_swap_b32 v20, v22\n\t" \
  "v_mfma_f32_16x16x32_bf16 v[76:79], a[76:79], a[248:251], v[76:79]\n\t" \
  "v_cvt_pk_bf16_f32 v49, v34, v35\n\t" \
  "v_cvt_pk_bf16_f32 v95, v94, v95\n\t" \
  "v_mfma_f32_16x16x32_bf16 v[72:75], a[136:139], a[232:235], v[72:75]\n\t" \
  "v_permlane16_swap_b32 v21, v23\n\t" \
  "v_mfma_f32_16x16x32_bf16 v[72:75], a[72:75], a[248:251], v[72:75]\n\t" \
  "v_cvt_pk_bf16_f32 v94, v92, v93\n\t" \
  "v_cvt_pk_bf16_f32 v92, v88, v89\n\t" \
  "v_mfma_f32_16x16x32_bf16 v[68:71], a[132:135], a[232:235], v[68:71]\n\t" \
  "v_permlane16_swap_b32 v16, v18\n\t" \
  "v_mfma_f32_16x16x32_bf16 v[68:71], a[68:71], a[248:251], v[68:71]\n\t" \
  "v_cvt_pk_bf16_f32 v93, v90, v91\n\t" \
  "v_cvt_pk_bf16_f32 v91, v86, v87\n\t" \
  "v_mfma_f32_16x16x32_bf16 v[64:67], a[128:131], a[232:235], v[64:67]\n\t" \
  "buffer_load_dwordx4 v240, %[wo], s97 offen offset:0 lds\n\t" \
  "v_mfma_f32_16x16x32_bf16 v[64:67], a[64:67], a[248:251], v[64:67]\n\t" \
  "s_add_u32 s97, s97, 7168\n\t" \
  "v_permlane16_swap_b32 v17, v19\n\t" \
  "v_mfma_f32_16x16x32_bf16 v[124:127], a[156:159], a[236:239], v[124:127]\n\t" \
  "v_cvt_pk_bf16_f32 v90, v84, v85\n\t" \
  "v_cvt_pk_bf16_f32 v88, v80, v81\n\t" \
  "v_mfma_f32_16x16x32_bf16 v[124:127], a[92:95], a[252:255], v[124:127]\n\t" \
  "v_permlane16_swap_b32 v60, v62\n\t" \
  "v_mfma_f32_16x16x32_bf16 v[120:123], a[152:155], a[236:239], v[120:123]\n\t" \
  "buffer_load_dwordx4 v241, %[wo], s97 offen offset:1024 lds\n\t" \
  "v_mfma_f32_16x16x32_bf16 v[120:123], a[88:91], a[252:255], v[120:123]\n\t" \
  "s_add_u32 s97, s97, 7168\n\t" \
  "v_cvt_pk_bf16_f32 v89, v82, v83\n\t" \
  ".Lhs5:\n\ts_cbranch_execz .Lhs5n\n\t" \
  "v_cvt_pk_bf16_f32 v87, v78, v79\n\t" \
  "v_mfma_f32_16x16x32_bf16 v[116:119], a[148:151], a[236:239], v[116:119]\n\t" \
  "v_permlane16_swap_b32 v61, v63\n\t" \
  "v_mfma_f32_16x16x32_bf16 v[116:119], a[84:87], a[252:255], v[116:119]\n\t" \
  "buffer_load_dwordx4 v240, %[wo], s97 offen offset:2048 lds\n\t" \
  "v_mfma_f32_16x16x32_bf16 v[112:115], a[144:147], a[236:239], v[112:115]\n\t" \
  "s_add_u32 s97, s97, 7168\n\t" \
  "v_cvt_pk_bf16_f32 v86, v76, v77\n\t" \
  "v_cvt_pk_bf16_f32 v84, v72, v73\n\t" \
  "v_mfma_f32_16x16x32_bf16 v[112:115], a[80:83], a[252:255], v[112:115]\n\t" \
  "v_permlane16_swap_b32 v56, v58\n\t" \
  "v_mfma_f32_16x16x32_bf16 v[108:111], a[140:143], a[236:239], v[108:111]\n\t" \
  "v_cvt_pk_bf16_f32 v85, v74, v75\n\t" \
  "v_cvt_pk_bf16_f32 v83, v70, v71\n\t" \
  "v_mfma_f32_16x16x32_bf16 v[108:111], a[76:79], a[252:255], v[108:111]\n\t" \
  "buffer_load_dwordx4 v241, %[wo], s97 offen offset:3072 lds\n\t" \
  "v_mfma_f32_16x16x32_bf16 v[104:107], a[136:139], a[236:239], v[104:107]\n\t" \
  "v_permlane16_swap_b32 v57, v59\n\t" \
  "v_mfma_f32_16x16x32_bf16 v[104:107], a[72:75], a[252:255], v[104:107]\n\t" \
  "v_cvt_pk_bf16_f32 v82, v68, v69\n\t" \
  "v_cvt_pk_bf16_f32 v80, v64, v65\n\t" \
  "v_mfma_f32_16x16x32_bf16 v[100:103], a[132:135], a[236:239], v[100:103]\n\t" \
  "v_permlane16_swap_b32 v52, v54\n\t" \
  "v_mfma_f32_16x16x32_bf16 v[100:103], a[68:71], a[252:255], v[100:103]\n\t" \
  "buffer_load_dword %[pfd], v247, %[ws], 0 offen sc1\n\t" \
  "v_mfma_f32_16x16x32_bf16 v[96:99], a[128:131], a[236:239], v[96:99]\n\t" \
  "s_add_u32 s98, %[pf4], 0\n\t" \
  "v_cvt_pk_bf16_f32 v81, v66, v67\n\t" \
  "v_cvt_pk_bf16_f32 v127, v126, v127\n\t" \
  "v_mfma_f32_16x16x32_bf16 v[96:99], a[64:67], a[252:255], v[96:99]\n\t" \
  "s_waitcnt lgkmcnt(0)\n\t" \
  "s_barrier\n\t" \
  STS("s_memrealtime s[94:95]\n\t") \
  STS("s_memtime s[92:93]\n\t") \
  "v_mfma_f32_16x16x32_bf16 v[0:3], a[0:3], v[16:19], 0\n\t" \
  "s_add_u32 m0, %[m], 49152\n\t" \
  "buffer_load_dword %[pfd], v245, %[sG], s98 offen\n\t" \
  "v_mfma_f32_16x16x32_bf16 v[0:3], a[4:7], v[20:23], v[0:3]\n\t" \
  "s_add_u32 s96, %[sofk], 32768\n\t" \
  "ds_read_b128 a[192:195], v220 offset:16384\n\t" \
  "v_cvt_pk_bf16_f32 v126, v124, v125\n\t" \
  "v_mfma_f32_16x16x32_bf16 v[0:3], a[8:11], v[24:27], v[0:3]\n\t" \
  "ds_read_b128 a[196:199], v221 offset:16384\n\t" \
  "v_cvt_pk_bf16_f32 v124, v120, v121\n\t" \
  "v_mfma_f32_16x16x32_bf16 v[0:3], a[12:15], v[28:31], v[0:3]\n\t" \
  "ds_read_b128 a[200:203], v222 offset:16384\n\t" \
  "v_cvt_pk_bf16_f32 v125, v122, v123\n\t" \
  "v_permlane16_swap_b32 v53, v55\n\t" \
  "v_permlane16_swap_b32 v48, v50\n\t" \
  "v_permlane16_swap_b32 v49, v51\n\t" \
  "s_nop 1\n\t" \
  "v_mfma_f32_16x16x32_bf16 v[32:35], a[0:3], v[48:51], 0\n\t" \
  "ds_read_b128 a[204:207], v223 offset:16384\n\t" \
  "v_max3_f32 v194, v0, v1, v2\n\t" \
  "v_mfma_f32_16x16x32_bf16 v[32:35], a[4:7], v[52:55], v[32:35]\n\t" \
  "ds_read_b128 a[208:211], v220 offset:20480\n\t" \
  "v_max_f32 v194, v194, v3\n\t" \
  "v_mfma_f32_16x16x32_bf16 v[32:35], a[8:11], v[56:59], v[32:35]\n\t" \
  "ds_read_b128 a[212:215], v221 offset:20480\n\t" \
  "v_mov_b32 v195, v194\n\t" \
  "v_mfma_f32_16x16x32_bf16 v[32:35], a[12:15], v[60:63], v[32:35]\n\t" \
  "ds_read_b128 a[216:219], v222 offset:20480\n\t" \
  "v_cvt_pk_bf16_f32 v123, v118, v119\n\t" \
  "v_permlane32_swap_b32 v194, v195\n\t" \
  "v_max_f32 v194, v194, v195\n\t" \
  "v_mov_b32 v195, v194\n\t" \
  "s_nop 1\n\t" \
  "v_permlane16_swap_b32 v194, v195\n\t" \
  "v_max_f32 v194, v194, v195\n\t" \
  "v_sub_f32 v176, 0xc2c80000, v194\n\t" \
  "v_sub_f32 v177, 0xc2c80000, v194\n\t" \
  "v_sub_f32 v178, 0xc2c80000, v194\n\t" \
  "v_sub_f32 v179, 0xc2c80000, v194\n\t" \
  "s_nop 1\n\t" \
  "v_mfma_f32_16x16x32_bf16 v[4:7], a[16:19], v[16:19], v[176:179]\n\t" \
  "ds_read_b128 a[220:223], v223 offset:20480\n\t" \
  "v_add_f32 v0, v0, v176\n\t" \
  "v_mfma_f32_16x16x32_bf16 v[4:7], a[20:23], v[20:23], v[4:7]\n\t" \
  "ds_read_b128 a[224:227], v220 offset:24576\n\t" \
  "v_add_f32 v1, v1, v176\n\t" \
  "v_mfma_f32_16x16x32_bf16 v[4:7], a[24:27], v[24:27], v[4:7]\n\t" \
  "ds_read_b128 a[228:231], v221 offset:24576\n\t" \
  "v_add_f32 v2, v2, v176\n\t" \
  "v_mfma_f32_16x16x32_bf16 v[4:7], a[28:31], v[28:31], v[4:7]\n\t" \
  "v_add_f32 v3, v3, v176\n\t" \
  "v_max3_f32 v194, v32, v33, v34\n\t" \
  "v_mfma_f32_16x16x32_bf16 v[8:11], a[32:35], v[16:19], v[176:179]\n\t" \
  STS("v_writelane_b32 %[stv], s94, 18\n\t") \
  STS("v_writelane_b32 %[stv], s92, 19\n\t") \
  "ds_read_b128 a[232:235], v222 offset:24576\n\t" \
  "v_max_f32 v194, v194, v35\n\t" \
  "v_mfma_f32_16x16x32_bf16 v[8:11], a[36:39], v[20:23], v[8:11]\n\t" \
  "v_mov_b32 v195, v194\n\t" \
  "v_cvt_pk_bf16_f32 v122, v116, v117\n\t" \
  "v_mfma_f32_16x16x32_bf16 v[8:11], a[40:43], v[24:27], v[8:11]\n\t" \
  "v_permlane32_swap_b32 v194, v195\n\t" \
  "v_mfma_f32_16x16x32_bf16 v[8:11], a[44:47], v[28:31], v[8:11]\n\t" \
  "ds_read_b128 a[236:239], v223 offset:24576\n\t" \
  "v_max_f32 v194, v194, v195\n\t" \
  "v_mfma_f32_16x16x32_bf16 v[12:15], a[48:51], v[16:19], v[176:179]\n\t" \
  "ds_read_b128 a[240:243], v220 offset:28672\n\t" \
  "v_mov_b32 v195, v194\n\t" \
  "v_mfma_f32_16x16x32_bf16 v[12:15], a[52:55], v[20:23], v[12:15]\n\t" \
  "buffer_load_dwordx4 v224, %[sK], s96 offen offset:0 lds\n\t" \
  "v_mfma_f32_16x16x32_bf16 v[12:15], a[56:59], v[24:27], v[12:15]\n\t" \
  "v_permlane16_swap_b32 v194, v195\n\t" \
  "v_mfma_f32_16x16x32_bf16 v[12:15], a[60:63], v[28:31], v[12:15]\n\t" \
  "ds_read_b128 a[244:247], v221 offset:28672\n\t" \
  "v_max_f32 v194, v194, v195\n\t" \
  "v_permlane16_swap_b32 v92, v94\n\t" \
  "v_permlane16_swap_b32 v93, v95\n\t" \
  "v_permlane16_swap_b32 v88, v90\n\t" \
  "v_permlane16_swap_b32 v89, v91\n\t" \
  "v_permlane16_swap_b32 v84, v86\n\t" \
  "v_permlane16_swap_b32 v85, v87\n\t" \
  "v_permlane16_swap_b32 v80, v82\n\t" \
  "v_permlane16_swap_b32 v81, v83\n\t" \
  "s_nop 1\n\t" \
  "v_mfma_f32_16x16x32_bf16 v[64:67], a[0:3], v[80:83], 0\n\t" \
  "ds_read_b128 a[248:251], v222 offset:28672\n\t" \
  "v_sub_f32 v180, 0xc2c80000, v194\n\t" \
  "v_mfma_f32_16x16x32_bf16 v[64:67], a[4:7], v[84:87], v[64:67]\n\t" \
  "v_sub_f32 v181, 0xc2c80000, v194\n\t" \
  "v_sub_f32 v182, 0xc2c80000, v194\n\t" \
  "v_mfma_f32_16x16x32_bf16 v[64:67], a[8:11], v[88:91], v[64:67]\n\t" \
  "ds_read_b128 a[252:255], v223 offset:28672\n\t" \
  "v_sub_f32 v183, 0xc2c80000, v194\n\t" \
  "v_mfma_f32_16x16x32_bf16 v[64:67], a[12:15], v[92:95], v[64:67]\n\t" \
  "v_add_f32 v32, v32, v180\n\t" \
  "v_add_f32 v33, v33, v180\n\t" \
  "v_mfma_f32_16x16x32_bf16 v[36:39], a[16:19], v[48:51], v[180:183]\n\t" \
  "v_add_f32 v34, v34, v180\n\t" \
  "v_add_f32 v35, v35, v180\n\t" \
  "v_mfma_f32_16x16x32_bf16 v[36:39], a[20:23], v[52:55], v[36:39]\n\t" \
  "buffer_load_dwordx4 v225, %[sK], s96 offen offset:1024 lds\n\t" \
  "v_mfma_f32_16x16x32_bf16 v[36:39], a[24:27], v[56:59], v[36:39]\n\t" \
  "v_max3_f32 v194, v64, v65, v66\n\t" \
  "v_max_f32 v194, v194, v67\n\t" \
  "v_mfma_f32_16x16x32_bf16 v[36:39], a[28:31], v[60:63], v[36:39]\n\t" \
  "v_mov_b32 v195, v194\n\t" \
  "v_cvt_pk_bf16_f32 v120, v112, v113\n\t" \
  "v_mfma_f32_16x16x32_bf16 v[40:43], a[32:35], v[48:51], v[180:183]\n\t" \
  "v_permlane32_swap_b32 v194, v195\n\t" \
  "v_mfma_f32_16x16x32_bf16 v[40:43], a[36:39], v[52:55], v[40:43]\n\t" \
  "v_max_f32 v194, v194, v195\n\t" \
  "v_mov_b32 v195, v194\n\t" \
  "v_mfma_f32_16x16x32_bf16 v[40:43], a[40:43], v[56:59], v[40:43]\n\t" \
  "buffer_load_dwordx4 v226, %[sK], s96 offen offset:2048 lds\n\t" \
  "v_mfma_f32_16x16x32_bf16 v[40:43], a[44:47], v[60:63], v[40:43]\n\t" \
  "v_permlane16_swap_b32 v194, v195\n\t" \
  "v_mfma_f32_16x16x32_bf16 v[44:47], a[48:51], v[48:51], v[180:183]\n\t" \
  "v_max_f32 v194, v194, v195\n\t" \
  "v_sub_f32 v184, 0xc2c80000, v194\n\t" \
  "v_mfma_f32_16x16x32_bf16 v[44:47], a[52:55], v[52:55], v[44:47]\n\t" \
  "v_sub_f32 v185, 0xc2c80000, v194\n\t" \
  "v_sub_f32 v186, 0xc2c80000, v194\n\t" \
  "v_mfma_f32_16x16x32_bf16 v[44:47], a[56:59], v[56:59], v[44:47]\n\t" \
  "v_sub_f32 v187, 0xc2c80000, v194\n\t" \
  "v_add_f32 v64, v64, v184\n\t" \
  "v_mfma_f32_16x16x32_bf16 v[44:47], a[60:63], v[60:63], v[44:47]\n\t" \
  "ds_read_b64_tr_b16 a[128:129], v228 offset:0\n\t" \
  "v_add_f32 v65, v65, v184\n\t" \
  "v_cvt_pk_bf16_f32 v121, v114, v115\n\t" \
  "v_cvt_pk_bf16_f32 v119, v110, v111\n\t" \
  "v_cvt_pk_bf16_f32 v118, v108, v109\n\t" \
  "v_cvt_pk_bf16_f32 v116, v104, v105\n\t" \
  "v_cvt_pk_bf16_f32 v117, v106, v107\n\t" \
  "v_cvt_pk_bf16_f32 v115, v102, v103\n\t" \
  "v_cvt_pk_bf16_f32 v114, v100, v101\n\t" \
  "v_cvt_pk_bf16_f32 v112, v96, v97\n\t" \
  "v_cvt_pk_bf16_f32 v113, v98, v99\n\t" \
  "v_permlane16_swap_b32 v124, v126\n\t" \
  "v_permlane16_swap_b32 v125, v127\n\t" \
  "v_permlane16_swap_b32 v120, v122\n\t" \
  "v_permlane16_swap_b32 v121, v123\n\t" \
  "v_permlane16_swap_b32 v116, v118\n\t" \
  "v_permlane16_swap_b32 v117, v119\n\t" \
  "v_permlane16_swap_b32 v112, v114\n\t" \
  "v_permlane16_swap_b32 v113, v115\n\t" \
  "s_nop 1\n\t" \
  "v_mfma_f32_16x16x32_bf16 v[96:99], a[0:3], v[112:115], 0\n\t" \
  "ds_read_b64_tr_b16 a[130:131], v228 offset:4096\n\t" \
  "v_add_f32 v66, v66, v184\n\t" \
  "v_mfma_f32_16x16x32_bf16 v[96:99], a[4:7], v[116:119], v[96:99]\n\t" \
  "ds_read_b64_tr_b16 a[132:133], v228 offset:8192\n\t" \
  "v_add_f32 v67, v67, v184\n\t" \
  "v_mfma_f32_16x16x32_bf16 v[96:99], a[8:11], v[120:123], v[96:99]\n\t" \
  "buffer_load_dwordx4 v227, %[sK], s96 offen offset:3072 lds\n\t" \
  "v_mfma_f32_16x16x32_bf16 v[96:99], a[12:15], v[124:127], v[96:99]\n\t" \
  "s_add_u32 m0, %[m], 147456\n\t" \
  "v_exp_f32 v0, v0\n\t" \
  "v_mfma_f32_16x16x32_bf16 v[68:71], a[16:19], v[80:83], v[184:187]\n\t" \
  "s_add_u32 s96, %[sofk], 16384\n\t" \
  "v_exp_f32 v1, v1\n\t" \
  "v_mfma_f32_16x16x32_bf16 v[68:71], a[20:23], v[84:87], v[68:71]\n\t" \
  "v_exp_f32 v2, v2\n\t" \
  "v_mfma_f32_16x16x32_bf16 v[68:71], a[24:27], v[88:91], v[68:71]\n\t" \
  "ds_read_b64_tr_b16 a[134:135], v228 offset:12288\n\t" \
  "v_max3_f32 v194, v96, v97, v98\n\t" \
  "v_mfma_f32_16x16x32_bf16 v[68:71], a[28:31], v[92:95], v[68:71]\n\t" \
  "ds_read_b64_tr_b16 a[136:137], v229 offset:0\n\t" \
  "v_max_f32 v194, v194, v99\n\t" \
  "v_mfma_f32_16x16x32_bf16 v[72:75], a[32:35], v[80:83], v[184:187]\n\t" \
  "ds_read_b64_tr_b16 a[138:139], v229 offset:4096\n\t" \
  "v_mov_b32 v195, v194\n\t" \
  "v_mfma_f32_16x16x32_bf16 v[72:75], a[36:39], v[84:87], v[72:75]\n\t" \
  "v_exp_f32 v3, v3\n\t" \
  "v_mfma_f32_16x16x32_bf16 v[72:75], a[40:43], v[88:91], v[72:75]\n\t" \
  "v_permlane32_swap_b32 v194, v195\n\t" \
  "v_mfma_f32_16x16x32_bf16 v[72:75], a[44:47], v[92:95], v[72:75]\n\t" \
  "ds_read_b64_tr_b16 a[140:141], v229 offset:8192\n\t" \
  "v_max_f32 v194, v194, v195\n\t" \
  "v_mfma_f32_16x16x32_bf16 v[76:79], a[48:51], v[80:83], v[184:187]\n\t" \
  "ds_read_b64_tr_b16 a[142:143], v229 offset:12288\n\t" \
  "v_mov_b32 v195, v194\n\t" \
  "v_mfma_f32_16x16x32_bf16 v[76:79], a[52:55], v[84:87], v[76:79]\n\t" \
  "buffer_load_dwordx4 v230, %[sV], s96 offen offset:0 lds\n\t" \
  "v_mfma_f32_16x16x32_bf16 v[76:79], a[56:59], v[88:91], v[76:79]\n\t" \
  "v_permlane16_swap_b32 v194, v195\n\t" \
  "v_mfma_f32_16x16x32_bf16 v[76:79], a[60:63], v[92:95], v[76:79]\n\t" \
  "ds_read_b64_tr_b16 a[144:145], v228 offset:256\n\t" \
  "v_max_f32 v194, v194, v195\n\t" \
  "v_sub_f32 v188, 0xc2c80000, v194\n\t" \
  "v_sub_f32 v189, 0xc2c80000, v194\n\t" \
  "v_sub_f32 v190, 0xc2c80000, v194\n\t" \
  "v_sub_f32 v191, 0xc2c80000, v194\n\t" \
  "s_nop 1\n\t" \
  "v_mfma_f32_16x16x32_bf16 v[100:103], a[16:19], v[112:115], v[188:191]\n\t" \
  "ds_read_b64_tr_b16 a[146:147], v228 offset:4352\n\t" \
  "v_add_f32 v96, v96, v188\n\t" \
  "v_mfma_f32_16x16x32_bf16 v[100:103], a[20:23], v[116:119], v[100:103]\n\t" \
  "ds_read_b64_tr_b16 a[148:149], v228 offset:8448\n\t" \
  "v_add_f32 v97, v97, v188\n\t" \
  "v_mfma_f32_16x16x32_bf16 v[100:103], a[24:27], v[120:123], v[100:103]\n\t" \
  "v_add_f32 v98, v98, v188\n\t" \
  "v_add_f32 v99, v99, v188\n\t" \
  "v_mfma_f32_16x16x32_bf16 v[100:103], a[28:31], v[124:127], v[100:103]\n\t" \
  "buffer_load_dwordx4 v231, %[sV], s96 offen offset:1024 lds\n\t" \
  "v_mfma_f32_16x16x32_bf16 v[104:107], a[32:35], v[112:115], v[188:191]\n\t" \
  "ds_read_b64_tr_b16 a[150:151], v228 offset:12544\n\t" \
  "v_cvt_pk_bf16_f32 v128, v0, v1\n\t" \
  "v_mfma_f32_16x16x32_bf16 v[104:107], a[36:39], v[116:119], v[104:107]\n\t" \
  "v_exp_f32 v4, v4\n\t" \
  "v_mfma_f32_16x16x32_bf16 v[104:107], a[40:43], v[120:123], v[104:107]\n\t" \
  "v_exp_f32 v5, v5\n\t" \
  "v_mfma_f32_16x16x32_bf16 v[104:107], a[44:47], v[124:127], v[104:107]\n\t" \
  "v_exp_f32 v6, v6\n\t" \
  "v_mfma_f32_16x16x32_bf16 v[108:111], a[48:51], v[112:115], v[188:191]\n\t" \
  "buffer_load_dwordx4 v230, %[sV], s96 offen offset:2048 lds\n\t" \
  "v_mfma_f32_16x16x32_bf16 v[108:111], a[52:55], v[116:119], v[108:111]\n\t" \
  "v_exp_f32 v7, v7\n\t" \
  "v_mfma_f32_16x16x32_bf16 v[108:111], a[56:59], v[120:123], v[108:111]\n\t" \
  "ds_read_b64_tr_b16 a[152:153], v229 offset:256\n\t" \
  "v_cvt_pk_bf16_f32 v129, v2, v3\n\t" \
  "v_mfma_f32_16x16x32_bf16 v[108:111], a[60:63], v[124:127], v[108:111]\n\t" \
  "s_waitcnt lgkmcnt(13)\n\t" \
  "v_exp_f32 v8, v8\n\t" \
  STA("s_memrealtime s[94:95]\n\t") \
  STA("s_memtime s[92:93]\n\t") \
  "v_mfma_f32_16x16x32_bf16 v[0:3], a[192:195], v[16:19], v[176:179]\n\t" \
  "v_exp_f32 v9, v9\n\t" \
  "v_mfma_f32_16x16x32_bf16 v[0:3], a[196:199], v[20:23], v[0:3]\n\t" \
  "ds_read_b64_tr_b16 a[154:155], v229 offset:4352\n\t" \
  "v_cvt_pk_bf16_f32 v130, v4, v5\n\t" \
  "v_mfma_f32_16x16x32_bf16 v[0:3], a[200:203], v[24:27], v[0:3]\n\t" \
  "buffer_load_dwordx4 v231, %[sV], s96 offen offset:3072 lds\n\t" \
  "v_mfma_f32_16x16x32_bf16 v[0:3], a[204:207], v[28:31], v[0:3]\n\t" \
  "ds_read_b64_tr_b16 a[156:157], v229 offset:8448\n\t" \
  "v_cvt_pk_bf16_f32 v131, v6, v7\n\t" \
  "v_mfma_f32_16x16x32_bf16 v[4:7], a[220:223], v[28:31], v[176:179]\n\t" \
  "v_exp_f32 v10, v10\n\t" \
  "v_mfma_f32_16x16x32_bf16 v[4:7], a[216:219], v[24:27], v[4:7]\n\t" \
  "s_add_u32 s98, %[pf4], 14336\n\t" \
  "v_exp_f32 v11, v11\n\t" \
  "v_mfma_f32_16x16x32_bf16 v[4:7], a[212:215], v[20:23], v[4:7]\n\t" \
  "v_exp_f32 v12, v12\n\t" \
  "v_mfma_f32_16x16x32_bf16 v[4:7], a[208:211], v[16:19], v[4:7]\n\t" \
  "v_exp_f32 v13, v13\n\t" \
  "v_cvt_pk_bf16_f32 v132, v8, v9\n\t" \
  "v_cvt_pk_bf16_f32 v133, v10, v11\n\t" \
  "v_mfma_f32_16x16x32_bf16 v[8:11], a[224:227], v[16:19], v[176:179]\n\t" \
  "v_exp_f32 v14, v14\n\t" \
  "v_mfma_f32_16x16x32_bf16 v[8:11], a[228:231], v[20:23], v[8:11]\n\t" \
  "v_exp_f32 v15, v15\n\t" \
  "v_mfma_f32_16x16x32_bf16 v[8:11], a[232:235], v[24:27], v[8:11]\n\t" \
  "buffer_load_dword %[pfd], v245, %[sG], s98 offen\n\t" \
  "v_mfma_f32_16x16x32_bf16 v[8:11], a[236:239], v[28:31], v[8:11]\n\t" \
  "v_exp_f32 v32, v32\n\t" \
  "v_cvt_pk_bf16_f32 v134, v12, v13\n\t" \
  "v_cvt_pk_bf16_f32 v135, v14, v15\n\t" \
  "v_mfma_f32_16x16x32_bf16 v[12:15], a[252:255], v[28:31], v[176:179]\n\t" \
  STA("v_writelane_b32 %[stv], s94, 26\n\t") \
  STA("v_writelane_b32 %[stv], s92, 27\n\t") \
  "v_exp_f32 v33, v33\n\t" \
  "v_mfma_f32_16x16x32_bf16 v[12:15], a[248:251], v[24:27], v[12:15]\n\t" \
  "v_exp_f32 v34, v34\n\t" \
  "v_mfma_f32_16x16x32_bf16 v[12:15], a[244:247], v[20:23], v[12:15]\n\t" \
  "v_exp_f32 v35, v35\n\t" \
  "v_mfma_f32_16x16x32_bf16 v[12:15], a[240:243], v[16:19], v[12:15]\n\t" \
  "ds_read_b64_tr_b16 a[158:159], v229 offset:12544\n\t" \
  "v_cvt_pk_bf16_f32 v136, v32, v33\n\t" \
  "v_cvt_pk_bf16_f32 v137, v34, v35\n\t" \
  "v_mfma_f32_16x16x32_bf16 v[32:35], a[192:195], v[48:51], v[180:183]\n\t" \
  "v_exp_f32 v36, v36\n\t" \
  "v_mfma_f32_16x16x32_bf16 v[32:35], a[196:199], v[52:55], v[32:35]\n\t" \
  "v_exp_f32 v37, v37\n\t" \
  "v_mfma_f32_16x16x32_bf16 v[32:35], a[200:203], v[56:59], v[32:35]\n\t" \
  "ds_read_b64_tr_b16 a[160:161], v228 offset:512\n\t" \
  "v_cvt_pk_bf16_f32 v138, v36, v37\n\t" \
  "v_exp_f32 v38, v38\n\t" \
  "v_mfma_f32_16x16x32_bf16 v[32:35], a[204:207], v[60:63], v[32:35]\n\t" \
  "s_add_u32 s98, %[pf4], 28672\n\t" \
  "v_exp_f32 v39, v39\n\t" \
  "v_lshl_add_u32 v207, v233, 14, %[cto]\n\t" \
  "v_min_u32 v207, .Lfz_z-.Lat_t0-4, v207\n\t" \
  "v_cvt_pk_bf16_f32 v139, v38, v39\n\t" \
  "v_mfma_f32_16x16x32_bf16 v[36:39], a[220:223], v[60:63], v[180:183]\n\t" \
  "v_exp_f32 v40, v40\n\t" \
  "v_mfma_f32_16x16x32_bf16 v[36:39], a[216:219], v[56:59], v[36:39]\n\t" \
  "buffer_load_dword %[pfd], v245, %[sG], s98 offen\n\t" \
  "v_exp_f32 v41, v41\n\t" \
  "s_getpc_b64 s[96:97]\n\t" \
  ".Lat_t0:\n\t" \
  "global_load_dword %[pfd], v207, s[96:97]\n\t" \
  "v_mfma_f32_16x16x32_bf16 v[36:39], a[212:215], v[52:55], v[36:39]\n\t" \
  "ds_read_b64_tr_b16 a[162:163], v228 offset:4608\n\t" \
  "v_cvt_pk_bf16_f32 v140, v40, v41\n\t" \
  "v_exp_f32 v42, v42\n\t" \
  "v_mfma_f32_16x16x32_bf16 v[36:39], a[208:211], v[48:51], v[36:39]\n\t" \
  "ds_read_b64_tr_b16 a[164:165], v228 offset:8704\n\t" \
  "ds_read_b64_tr_b16 a[166:167], v228 offset:12800\n\t" \
  "v_exp_f32 v43, v43\n\t" \
  "s_nop 0\n\t" \
  "v_cvt_pk_bf16_f32 v141, v42, v43\n\t" \
  "v_mfma_f32_16x16x32_bf16 v[40:43], a[224:227], v[48:51], v[180:183]\n\t" \
  "v_exp_f32 v44, v44\n\t" \
  "v_mfma_f32_16x16x32_bf16 v[40:43], a[228:231], v[52:55], v[40:43]\n\t" \
  "v_exp_f32 v45, v45\n\t" \
  "v_mfma_f32_16x16x32_bf16 v[40:43], a[232:235], v[56:59], v[40:43]\n\t" \
  "v_exp_f32 v46, v46\n\t" \
  "v_mfma_f32_16x16x32_bf16 v[40:43], a[236:239], v[60:63], v[40:43]\n\t" \
  "v_exp_f32 v47, v47\n\t" \
  "v_cvt_pk_bf16_f32 v142, v44, v45\n\t" \
  "v_cvt_pk_bf16_f32 v143, v46, v47\n\t" \
  "v_mfma_f32_16x16x32_bf16 v[44:47], a[252:255], v[60:63], v[180:183]\n\t" \
  "v_exp_f32 v64, v64\n\t" \
  "v_mfma_f32_16x16x32_bf16 v[44:47], a[248:251], v[56:59], v[44:47]\n\t" \
  "ds_read_b64_tr_b16 a[168:169], v229 offset:512\n\t" \
  "ds_read_b64_tr_b16 a[170:171], v229 offset:4608\n\t" \
  "v_exp_f32 v65, v65\n\t" \
  "v_mfma_f32_16x16x32_bf16 v[44:47], a[244:247], v[52:55], v[44:47]\n\t" \
  "v_exp_f32 v66, v66\n\t" \
  "v_mfma_f32_16x16x32_bf16 v[44:47], a[240:243], v[48:51], v[44:47]\n\t" \
  "v_exp_f32 v67, v67\n\t" \
  "v_cvt_pk_bf16_f32 v144, v64, v65\n\t" \
  "v_cvt_pk_bf16_f32 v145, v66, v67\n\t" \
  "v_mfma_f32_16x16x32_bf16 v[64:67], a[192:195], v[80:83], v[184:187]\n\t" \
  "v_exp_f32 v68, v68\n\t" \
  "v_mfma_f32_16x16x32_bf16 v[64:67], a[196:199], v[84:87], v[64:67]\n\t" \
  "v_exp_f32 v69, v69\n\t" \
  "v_mfma_f32_16x16x32_bf16 v[64:67], a[200:203], v[88:91], v[64:67]\n\t" \
  "ds_read_b64_tr_b16 a[172:173], v229 offset:8704\n\t" \
  "v_cvt_pk_bf16_f32 v146, v68, v69\n\t" \
  "v_exp_f32 v70, v70\n\t" \
  "v_mfma_f32_16x16x32_bf16 v[64:67], a[204:207], v[92:95], v[64:67]\n\t" \
  "v_exp_f32 v71, v71\n\t" \
  "s_nop 0\n\t" \
  "v_cvt_pk_bf16_f32 v147, v70, v71\n\t" \
  "v_mfma_f32_16x16x32_bf16 v[68:71], a[220:223], v[92:95], v[184:187]\n\t" \
  "v_exp_f32 v72, v72\n\t" \
  "v_mfma_f32_16x16x32_bf16 v[68:71], a[216:219], v[88:91], v[68:71]\n\t" \
  "ds_read_b64_tr_b16 a[174:175], v229 offset:12800\n\t" \
  "ds_read_b64_tr_b16 a[176:177], v228 offset:768\n\t" \
  "v_exp_f32 v73, v73\n\t" \
  "v_mfma_f32_16x16x32_bf16 v[68:71], a[212:215], v[84:87], v[68:71]\n\t" \
  "v_exp_f32 v74, v74\n\t" \
  "v_mfma_f32_16x16x32_bf16 v[68:71], a[208:211], v[80:83], v[68:71]\n\t" \
  "v_exp_f32 v75, v75\n\t" \
  "v_cvt_pk_bf16_f32 v148, v72, v73\n\t" \
  "v_cvt_pk_bf16_f32 v149, v74, v75\n\t" \
  "v_mfma_f32_16x16x32_bf16 v[72:75], a[224:227], v[80:83], v[184:187]\n\t" \
  "v_exp_f32 v76, v76\n\t" \
  "v_mfma_f32_16x16x32_bf16 v[72:75], a[228:231], v[84:87], v[72:75]\n\t" \
  "v_exp_f32 v77, v77\n\t" \
  "v_mfma_f32_16x16x32_bf16 v[72:75], a[232:235], v[88:91], v[72:75]\n\t" \
  "ds_read_b64_tr_b16 a[178:179], v228 offset:4864\n\t" \
  "v_cvt_pk_bf16_f32 v150, v76, v77\n\t" \
  "v_exp_f32 v78, v78\n\t" \
  "v_mfma_f32_16x16x32_bf16 v[72:75], a[236:239], v[92:95], v[72:75]\n\t" \
  "v_exp_f32 v79, v79\n\t" \
  "s_nop 0\n\t" \
  "v_cvt_pk_bf16_f32 v151, v78, v79\n\t" \
  "v_mfma_f32_16x16x32_bf16 v[76:79], a[252:255], v[92:95], v[184:187]\n\t" \
  "v_exp_f32 v96, v96\n\t" \
  "v_mfma_f32_16x16x32_bf16 v[76:79], a[248:251], v[88:91], v[76:79]\n\t" \
  "ds_read_b64_tr_b16 a[180:181], v228 offset:8960\n\t" \
  "ds_read_b64_tr_b16 a[182:183], v228 offset:13056\n\t" \
  "v_exp_f32 v97, v97\n\t" \
  "v_mfma_f32_16x16x32_bf16 v[76:79], a[244:247], v[84:87], v[76:79]\n\t" \
  "v_exp_f32 v98, v98\n\t" \
  "v_mfma_f32_16x16x32_bf16 v[76:79], a[240:243], v[80:83], v[76:79]\n\t" \
  "v_exp_f32 v99, v99\n\t" \
  "v_cvt_pk_bf16_f32 v152, v96, v97\n\t" \
  "v_cvt_pk_bf16_f32 v153, v98, v99\n\t" \
  "v_mfma_f32_16x16x32_bf16 v[96:99], a[192:195], v[112:115], v[188:191]\n\t" \
  "v_exp_f32 v100, v100\n\t" \
  "v_mfma_f32_16x16x32_bf16 v[96:99], a[196:199], v[116:119], v[96:99]\n\t" \
  "v_exp_f32 v101, v101\n\t" \
  "v_mfma_f32_16x16x32_bf16 v[96:99], a[200:203], v[120:123], v[96:99]\n\t" \
  "ds_read_b64_tr_b16 a[184:185], v229 offset:768\n\t" \
  "v_cvt_pk_bf16_f32 v154, v100, v101\n\t" \
  "v_exp_f32 v102, v102\n\t" \
  "v_mfma_f32_16x16x32_bf16 v[96:99], a[204:207], v[124:127], v[96:99]\n\t" \
  "v_exp_f32 v103, v103\n\t" \
  "s_nop 0\n\t" \
  "v_cvt_pk_bf16_f32 v155, v102, v103\n\t" \
  "v_mfma_f32_16x16x32_bf16 v[100:103], a[220:223], v[124:127], v[188:191]\n\t" \
  "v_exp_f32 v104, v104\n\t" \
  "v_mfma_f32_16x16x32_bf16 v[100:103], a[216:219], v[120:123], v[100:103]\n\t" \
  "ds_read_b64_tr_b16 a[186:187], v229 offset:4864\n\t" \
  "ds_read_b64_tr_b16 a[188:189], v229 offset:8960\n\t" \
  "v_exp_f32 v105, v105\n\t" \
  "v_mfma_f32_16x16x32_bf16 v[100:103], a[212:215], v[116:119], v[100:103]\n\t" \
  "v_exp_f32 v106, v106\n\t" \
  "v_mfma_f32_16x16x32_bf16 v[100:103], a[208:211], v[112:115], v[100:103]\n\t" \
  "v_exp_f32 v107, v107\n\t" \
  "v_cvt_pk_bf16_f32 v156, v104, v105\n\t" \
  "v_cvt_pk_bf16_f32 v157, v106, v107\n\t" \
  "v_mfma_f32_16x16x32_bf16 v[104:107], a[224:227], v[112:115], v[188:191]\n\t" \
  "v_exp_f32 v108, v108\n\t" \
  "v_mfma_f32_16x16x32_bf16 v[104:107], a[228:231], v[116:119], v[104:107]\n\t" \
  "v_exp_f32 v109, v109\n\t" \
  "v_mfma_f32_16x16x32_bf16 v[104:107], a[232:235], v[120:123], v[104:107]\n\t" \
  "ds_read_b64_tr_b16 a[190:191], v229 offset:13056\n\t" \
  "v_cvt_pk_bf16_f32 v158, v108, v109\n\t" \
  "v_exp_f32 v110, v110\n\t" \
  "v_mfma_f32_16x16x32_bf16 v[104:107], a[236:239], v[124:127], v[104:107]\n\t" \
  "s_add_u32 s98, %[pf4], 43008\n\t" \
  "v_exp_f32 v111, v111\n\t" \
  "s_nop 0\n\t" \
  "v_cvt_pk_bf16_f32 v159, v110, v111\n\t" \
  "v_mfma_f32_16x16x32_bf16 v[108:111], a[252:255], v[124:127], v[188:191]\n\t" \
  "buffer_load_dword %[pfd], v245, %[sG], s98 offen\n\t" \
  "v_mfma_f32_16x16x32_bf16 v[108:111], a[248:251], v[120:123], v[108:111]\n\t" \
  "v_exp_f32 v0, v0\n\t" \
  "v_mfma_f32_16x16x32_bf16 v[108:111], a[244:247], v[116:119], v[108:111]\n\t" \
  "v_exp_f32 v1, v1\n\t" \
  "v_mfma_f32_16x16x32_bf16 v[108:111], a[240:243], v[112:115], v[108:111]\n\t" \
  "s_waitcnt vmcnt(4) lgkmcnt(0)\n\t" \
  "s_barrier\n\t" \
  STA("s_memrealtime s[94:95]\n\t") \
  STA("s_memtime s[92:93]\n\t") \
  "v_mfma_f32_16x16x32_bf16 a[0:3], a[128:131], v[128:131], 0\n\t" \
  "v_exp_f32 v2, v2\n\t" \
  "v_mfma_f32_16x16x32_bf16 a[0:3], a[132:135], v[132:135], a[0:3]\n\t" \
  "v_exp_f32 v3, v3\n\t" \
  "v_mfma_f32_16x16x32_bf16 a[16:19], a[136:139], v[128:131], 0\n\t" \
  "ds_read_b128 a[192:195], v220 offset:0\n\t" \
  "ds_read_b128 a[196:199], v221 offset:0\n\t" \
  "v_mfma_f32_16x16x32_bf16 a[16:19], a[140:143], v[132:135], a[16:19]\n\t" \
  "v_exp_f32 v4, v4\n\t" \
  "v_mfma_f32_16x16x32_bf16 a[32:35], a[144:147], v[128:131], 0\n\t" \
  "v_exp_f32 v5, v5\n\t" \
  "v_mfma_f32_16x16x32_bf16 a[32:35], a[148:151], v[132:135], a[32:35]\n\t" \
  "v_exp_f32 v6, v6\n\t" \
  "v_mfma_f32_16x16x32_bf16 a[48:51], a[152:155], v[128:131], 0\n\t" \
  "v_exp_f32 v7, v7\n\t" \
  "v_mfma_f32_16x16x32_bf16 a[48:51], a[156:159], v[132:135], a[48:51]\n\t" \
  "v_exp_f32 v8, v8\n\t" \
  "v_mfma_f32_16x16x32_bf16 a[64:67], a[160:163], v[128:131], 0\n\t" \
  "v_exp_f32 v9, v9\n\t" \
  "v_mfma_f32_16x16x32_bf16 a[64:67], a[164:167], v[132:135], a[64:67]\n\t" \
  "v_exp_f32 v10, v10\n\t" \
  "v_mfma_f32_16x16x32_bf16 a[80:83], a[168:171], v[128:131], 0\n\t" \
  "ds_read_b128 a[200:203], v222 offset:0\n\t" \
  "ds_read_b128 a[204:207], v223 offset:0\n\t" \
  "v_mfma_f32_16x16x32_bf16 a[80:83], a[172:175], v[132:135], a[80:83]\n\t" \
  "v_exp_f32 v11, v11\n\t" \
  "v_mfma_f32_16x16x32_bf16 a[96:99], a[176:179], v[128:131], 0\n\t" \
  STA("v_writelane_b32 %[stv], s94, 28\n\t") \
  STA("v_writelane_b32 %[stv], s92, 29\n\t") \
  "v_exp_f32 v12, v12\n\t" \
  "v_mfma_f32_16x16x32_bf16 a[96:99], a[180:183], v[132:135], a[96:99]\n\t" \
  "v_exp_f32 v13, v13\n\t" \
  "v_mfma_f32_16x16x32_bf16 a[112:115], a[184:187], v[128:131], 0\n\t" \
  "v_exp_f32 v14, v14\n\t" \
  "v_mfma_f32_16x16x32_bf16 a[112:115], a[188:191], v[132:135], a[112:115]\n\t" \
  "v_exp_f32 v15, v15\n\t" \
  "v_mfma_f32_16x16x32_bf16 v[160:163], v[216:219], v[128:131], 0\n\t" \
  "v_exp_f32 v32, v32\n\t" \
  "v_mfma_f32_16x16x32_bf16 v[160:163], v[216:219], v[132:135], v[160:163]\n\t" \
  "ds_read_b128 a[208:211], v220 offset:4096\n\t" \
  "v_cvt_pk_bf16_f32 v128, v0, v1\n\t" \
  "v_mfma_f32_16x16x32_bf16 a[4:7], a[128:131], v[136:139], 0\n\t" \
  "v_exp_f32 v33, v33\n\t" \
  "v_mfma_f32_16x16x32_bf16 a[4:7], a[132:135], v[140:143], a[4:7]\n\t" \
  "ds_read_b128 a[212:215], v221 offset:4096\n\t" \
  "v_cvt_pk_bf16_f32 v129, v2, v3\n\t" \
  "v_mfma_f32_16x16x32_bf16 a[20:23], a[136:139], v[136:139], 0\n\t" \
  "s_add_u32 m0, %[m], 65536\n\t" \
  "v_exp_f32 v34, v34\n\t" \
  "v_mfma_f32_16x16x32_bf16 a[20:23], a[140:143], v[140:143], a[20:23]\n\t" \
  "s_add_u32 s96, %[sofk], 49152\n\t" \
  "v_exp_f32 v35, v35\n\t" \
  "v_mfma_f32_16x16x32_bf16 a[36:39], a[144:147], v[136:139], 0\n\t" \
  "v_exp_f32 v36, v36\n\t" \
  "v_mfma_f32_16x16x32_bf16 a[36:39], a[148:151], v[140:143], a[36:39]\n\t" \
  "ds_read_b128 a[216:219], v222 offset:4096\n\t" \
  "v_cvt_pk_bf16_f32 v130, v4, v5\n\t" \
  "v_mfma_f32_16x16x32_bf16 a[52:55], a[152:155], v[136:139], 0\n\t" \
  "v_exp_f32 v37, v37\n\t" \
  "v_mfma_f32_16x16x32_bf16 a[52:55], a[156:159], v[140:143], a[52:55]\n\t" \
  "v_exp_f32 v38, v38\n\t" \
  "v_mfma_f32_16x16x32_bf16 a[68:71], a[160:163], v[136:139], 0\n\t" \
  "v_exp_f32 v39, v39\n\t" \
  "v_mfma_f32_16x16x32_bf16 a[68:71], a[164:167], v[140:143], a[68:71]\n\t" \
  "buffer_load_dwordx4 v224, %[sK], s96 offen offset:0 lds\n\t" \
  "v_mfma_f32_16x16x32_bf16 a[84:87], a[168:171], v[136:139], 0\n\t" \
  "ds_read_b128 a[220:223], v223 offset:4096\n\t" \
  "v_cvt_pk_bf16_f32 v131, v6, v7\n\t" \
  "v_mfma_f32_16x16x32_bf16 a[84:87], a[172:175], v[140:143], a[84:87]\n\t" \
  "v_exp_f32 v40, v40\n\t" \
  "v_mfma_f32_16x16x32_bf16 a[100:103], a[176:179], v[136:139], 0\n\t" \
  "ds_read_b128 a[224:227], v220 offset:8192\n\t" \
  "v_cvt_pk_bf16_f32 v132, v8, v9\n\t" \
  "v_mfma_f32_16x16x32_bf16 a[100:103], a[180:183], v[140:143], a[100:103]\n\t" \
  "buffer_load_dwordx4 v225, %[sK], s96 offen offset:1024 lds\n\t" \
  "v_mfma_f32_16x16x32_bf16 a[116:119], a[184:187], v[136:139], 0\n\t" \
  "v_exp_f32 v41, v41\n\t" \
  "v_mfma_f32_16x16x32_bf16 a[116:119], a[188:191], v[140:143], a[116:119]\n\t" \
  "v_exp_f32 v42, v42\n\t" \
  "v_mfma_f32_16x16x32_bf16 v[164:167], v[216:219], v[136:139], 0\n\t" \
  "buffer_load_dwordx4 v226, %[sK], s96 offen offset:2048 lds\n\t" \
  "v_mfma_f32_16x16x32_bf16 v[164:167], v[216:219], v[140:143], v[164:167]\n\t" \
  "ds_read_b128 a[228:231], v221 offset:8192\n\t" \
  "v_cvt_pk_bf16_f32 v133, v10, v11\n\t" \
  "v_mfma_f32_16x16x32_bf16 a[8:11], a[128:131], v[144:147], 0\n\t" \
  "v_exp_f32 v43, v43\n\t" \
  "v_mfma_f32_16x16x32_bf16 a[8:11], a[132:135], v[148:151], a[8:11]\n\t" \
  "v_exp_f32 v44, v44\n\t" \
  "v_mfma_f32_16x16x32_bf16 a[24:27], a[136:139], v[144:147], 0\n\t" \
  "buffer_load_dwordx4 v227, %[sK], s96 offen offset:3072 lds\n\t" \
  "v_mfma_f32_16x16x32_bf16 a[24:27], a[140:143], v[148:151], a[24:27]\n\t" \
  "s_add_u32 m0, %[m], 81920\n\t" \
  "ds_read_b128 a[232:235], v222 offset:8192\n\t" \
  "v_cvt_pk_bf16_f32 v134, v12, v13\n\t" \
  "v_mfma_f32_16x16x32_bf16 a[40:43], a[144:147], v[144:147], 0\n\t" \
  "s_add_u32 s96, %[sofk], 32768\n\t" \
  "v_exp_f32 v45, v45\n\t" \
  "v_mfma_f32_16x16x32_bf16 a[40:43], a[148:151], v[148:151], a[40:43]\n\t" \
  "v_exp_f32 v46, v46\n\t" \
  "v_mfma_f32_16x16x32_bf16 a[56:59], a[152:155], v[144:147], 0\n\t" \
  "ds_read_b128 a[236:239], v223 offset:8192\n\t" \
  "v_cvt_pk_bf16_f32 v135, v14, v15\n\t" \
  "v_mfma_f32_16x16x32_bf16 a[56:59], a[156:159], v[148:151], a[56:59]\n\t" \
  "v_exp_f32 v47, v47\n\t" \
  "v_mfma_f32_16x16x32_bf16 a[72:75], a[160:163], v[144:147], 0\n\t" \
  "v_exp_f32 v64, v64\n\t" \
  "v_mfma_f32_16x16x32_bf16 a[72:75], a[164:167], v[148:151], a[72:75]\n\t" \
  "ds_read_b128 a[240:243], v220 offset:12288\n\t" \
  "v_cvt_pk_bf16_f32 v136, v32, v33\n\t" \
  "v_mfma_f32_16x16x32_bf16 a[88:91], a[168:171], v[144:147], 0\n\t" \
  "v_exp_f32 v65, v65\n\t" \
  "v_mfma_f32_16x16x32_bf16 a[88:91], a[172:175], v[148:151], a[88:91]\n\t" \
  "v_exp_f32 v66, v66\n\t" \
  "v_mfma_f32_16x16x32_bf16 a[104:107], a[176:179], v[144:147], 0\n\t" \
  "buffer_load_dwordx4 v230, %[sV], s96 offen offset:0 lds\n\t" \
  "v_mfma_f32_16x16x32_bf16 a[104:107], a[180:183], v[148:151], a[104:107]\n\t" \
  "ds_read_b128 a[244:247], v221 offset:12288\n\t" \
  "v_cvt_pk_bf16_f32 v137, v34, v35\n\t" \
  "v_mfma_f32_16x16x32_bf16 a[120:123], a[184:187], v[144:147], 0\n\t" \
  "v_exp_f32 v67, v67\n\t" \
  "v_mfma_f32_16x16x32_bf16 a[120:123], a[188:191], v[148:151], a[120:123]\n\t" \
  "buffer_load_dwordx4 v231, %[sV], s96 offen offset:1024 lds\n\t" \
  "v_mfma_f32_16x16x32_bf16 v[168:171], v[216:219], v[144:147], 0\n\t" \
  "v_exp_f32 v68, v68\n\t" \
  "v_mfma_f32_16x16x32_bf16 v[168:171], v[216:219], v[148:151], v[168:171]\n\t" \
  "ds_read_b128 a[248:251], v222 offset:12288\n\t" \
  "v_cvt_pk_bf16_f32 v138, v36, v37\n\t" \
  "v_mfma_f32_16x16x32_bf16 a[12:15], a[128:131], v[152:155], 0\n\t" \
  "v_exp_f32 v69, v69\n\t" \
  "v_mfma_f32_16x16x32_bf16 a[12:15], a[132:135], v[156:159], a[12:15]\n\t" \
  "buffer_load_dwordx4 v230, %[sV], s96 offen offset:2048 lds\n\t" \
  "v_mfma_f32_16x16x32_bf16 a[28:31], a[136:139], v[152:155], 0\n\t" \
  "v_exp_f32 v70, v70\n\t" \
  "v_mfma_f32_16x16x32_bf16 a[28:31], a[140:143], v[156:159], a[28:31]\n\t" \
  "ds_read_b128 a[252:255], v223 offset:12288\n\t" \
  "v_cvt_pk_bf16_f32 v139, v38, v39\n\t" \
  "v_mfma_f32_16x16x32_bf16 a[44:47], a[144:147], v[152:155], 0\n\t" \
  "buffer_load_dwordx4 v231, %[sV], s96 offen offset:3072 lds\n\t" \
  "v_mfma_f32_16x16x32_bf16 a[44:47], a[148:151], v[156:159], a[44:47]\n\t" \
  "v_exp_f32 v71, v71\n\t" \
  "v_mfma_f32_16x16x32_bf16 a[60:63], a[152:155], v[152:155], 0\n\t" \
  "v_exp_f32 v72, v72\n\t" \
  "v_mfma_f32_16x16x32_bf16 a[60:63], a[156:159], v[156:159], a[60:63]\n\t" \
  "v_exp_f32 v73, v73\n\t" \
  "v_mfma_f32_16x16x32_bf16 a[76:79], a[160:163], v[152:155], 0\n\t" \
  "v_cvt_pk_bf16_f32 v140, v40, v41\n\t" \
  "v_cvt_pk_bf16_f32 v141, v42, v43\n\t" \
  "v_mfma_f32_16x16x32_bf16 a[76:79], a[164:167], v[156:159], a[76:79]\n\t" \
  "s_add_u32 m0, %[m], 114688\n\t" \
  "s_mov_b32 s96, 0\n\t" \
  "buffer_load_dwordx4 v232, %[sU], s96 offen offset:0 lds\n\t" \
  "v_mfma_f32_16x16x32_bf16 a[92:95], a[168:171], v[152:155], 0\n\t" \
  "s_mov_b32 s96, 6144\n\t" \
  "v_exp_f32 v74, v74\n\t" \
  "v_mfma_f32_16x16x32_bf16 a[92:95], a[172:175], v[156:159], a[92:95]\n\t" \
  "v_exp_f32 v75, v75\n\t" \
  "v_mfma_f32_16x16x32_bf16 a[108:111], a[176:179], v[152:155], 0\n\t" \
  "v_exp_f32 v76, v76\n\t" \
  "v_mfma_f32_16x16x32_bf16 a[108:111], a[180:183], v[156:159], a[108:111]\n\t" \
  "buffer_load_dwordx4 v232, %[sU], s96 offen offset:1024 lds\n\t" \
  "v_mfma_f32_16x16x32_bf16 a[124:127], a[184:187], v[152:155], 0\n\t" \
  "v_exp_f32 v77, v77\n\t" \
  "v_mfma_f32_16x16x32_bf16 a[124:127], a[188:191], v[156:159], a[124:127]\n\t" \
  "v_exp_f32 v78, v78\n\t" \
  "v_mfma_f32_16x16x32_bf16 v[172:175], v[216:219], v[152:155], 0\n\t" \
  "v_exp_f32 v79, v79\n\t" \
  "v_mfma_f32_16x16x32_bf16 v[172:175], v[216:219], v[156:159], v[172:175]\n\t" \
  "s_waitcnt lgkmcnt(0)\n\t" \
  "v_exp_f32 v96, v96\n\t" \
  STA("s_memrealtime s[94:95]\n\t") \
  STA("s_memtime s[92:93]\n\t") \
  "v_mfma_f32_16x16x32_bf16 v[0:3], a[192:195], v[16:19], v[176:179]\n\t" \
  "ds_read_b64_tr_b16 a[128:129], v237 offset:0\n\t" \
  "v_cvt_pk_bf16_f32 v142, v44, v45\n\t" \
  ".Lhs6:\n\ts_cbranch_execz .Lhs6n\n\t" \
  "v_mfma_f32_16x16x32_bf16 v[0:3], a[196:199], v[20:23], v[0:3]\n\t" \
  "v_exp_f32 v97, v97\n\t" \
  "v_mfma_f32_16x16x32_bf16 v[0:3], a[200:203], v[24:27], v[0:3]\n\t" \
  "ds_read_b64_tr_b16 a[130:131], v237 offset:4096\n\t" \
  "v_cvt_pk_bf16_f32 v143, v46, v47\n\t" \
  "v_mfma_f32_16x16x32_bf16 v[0:3], a[204:207], v[28:31], v[0:3]\n\t" \
  "v_exp_f32 v98, v98\n\t" \
  "v_mfma_f32_16x16x32_bf16 v[4:7], a[220:223], v[28:31], v[176:179]\n\t" \
  "ds_read_b64_tr_b16 a[132:133], v237 offset:8192\n\t" \
  "v_cvt_pk_bf16_f32 v144, v64, v65\n\t" \
  "v_mfma_f32_16x16x32_bf16 v[4:7], a[216:219], v[24:27], v[4:7]\n\t" \
  "v_exp_f32 v99, v99\n\t" \
  "v_mfma_f32_16x16x32_bf16 v[4:7], a[212:215], v[20:23], v[4:7]\n\t" \
  "ds_read_b64_tr_b16 a[134:135], v237 offset:12288\n\t" \
  "v_cvt_pk_bf16_f32 v145, v66, v67\n\t" \
  "v_mfma_f32_16x16x32_bf16 v[4:7], a[208:211], v[16:19], v[4:7]\n\t" \
  "ds_read_b64_tr_b16 a[136:137], v238 offset:0\n\t" \
  "v_cvt_pk_bf16_f32 v146, v68, v69\n\t" \
  "v_mfma_f32_16x16x32_bf16 v[8:11], a[224:227], v[16:19], v[176:179]\n\t" \
  "v_exp_f32 v100, v100\n\t" \
  "v_mfma_f32_16x16x32_bf16 v[8:11], a[228:231], v[20:23], v[8:11]\n\t" \
  "ds_read_b64_tr_b16 a[138:139], v238 offset:4096\n\t" \
  "v_cvt_pk_bf16_f32 v147, v70, v71\n\t" \
  "v_mfma_f32_16x16x32_bf16 v[8:11], a[232:235], v[24:27], v[8:11]\n\t" \
  "v_exp_f32 v101, v101\n\t" \
  "v_mfma_f32_16x16x32_bf16 v[8:11], a[236:239], v[28:31], v[8:11]\n\t" \
  "ds_read_b64_tr_b16 a[140:141], v238 offset:8192\n\t" \
  "v_cvt_pk_bf16_f32 v148, v72, v73\n\t" \
  "v_mfma_f32_16x16x32_bf16 v[12:15], a[252:255], v[28:31], v[176:179]\n\t" \
  STA("v_writelane_b32 %[stv], s94, 30\n\t") \
  STA("v_writelane_b32 %[stv], s92, 31\n\t") \
  "v_exp_f32 v102, v102\n\t" \
  "v_mfma_f32_16x16x32_bf16 v[12:15], a[248:251], v[24:27], v[12:15]\n\t" \
  "ds_read_b64_tr_b16 a[142:143], v238 offset:12288\n\t" \
  "v_cvt_pk_bf16_f32 v149, v74, v75\n\t" \
  "v_mfma_f32_16x16x32_bf16 v[12:15], a[244:247], v[20:23], v[12:15]\n\t" \
  "ds_read_b64_tr_b16 a[144:145], v237 offset:256\n\t" \
  "v_cvt_pk_bf16_f32 v150, v76, v77\n\t" \
  "v_mfma_f32_16x16x32_bf16 v[12:15], a[240:243], v[16:19], v[12:15]\n\t" \
  "v_exp_f32 v103, v103\n\t" \
  "v_mfma_f32_16x16x32_bf16 v[32:35], a[192:195], v[48:51], v[180:183]\n\t" \
  "ds_read_b64_tr_b16 a[146:147], v237 offset:4352\n\t" \
  "v_cvt_pk_bf16_f32 v151, v78, v79\n\t" \
  "v_mfma_f32_16x16x32_bf16 v[32:35], a[196:199], v[52:55], v[32:35]\n\t" \
  "v_exp_f32 v104, v104\n\t" \
  "v_mfma_f32_16x16x32_bf16 v[32:35], a[200:203], v[56:59], v[32:35]\n\t" \
  "ds_read_b64_tr_b16 a[148:149], v237 offset:8448\n\t" \
  "v_cvt_pk_bf16_f32 v152, v96, v97\n\t" \
  "v_mfma_f32_16x16x32_bf16 v[32:35], a[204:207], v[60:63], v[32:35]\n\t" \
  "v_exp_f32 v105, v105\n\t" \
  "v_mfma_f32_16x16x32_bf16 v[36:39], a[220:223], v[60:63], v[180:183]\n\t" \
  "ds_read_b64_tr_b16 a[150:151], v237 offset:12544\n\t" \
  "v_cvt_pk_bf16_f32 v153, v98, v99\n\t" \
  "v_mfma_f32_16x16x32_bf16 v[36:39], a[216:219], v[56:59], v[36:39]\n\t" \
  "ds_read_b64_tr_b16 a[152:153], v238 offset:256\n\t" \
  "v_cvt_pk_bf16_f32 v154, v100, v101\n\t" \
  "v_mfma_f32_16x16x32_bf16 v[36:39], a[212:215], v[52:55], v[36:39]\n\t" \
  "s_add_u32 m0, %[m], 114688\n\t" \
  "s_mov_b32 s96, 12288\n\t" \
  "ds_read_b64_tr_b16 a[154:155], v238 offset:4352\n\t" \
  "v_mfma_f32_16x16x32_bf16 v[36:39], a[208:211], v[48:51], v[36:39]\n\t" \
  "buffer_load_dwordx4 v232, %[sU], s96 offen offset:2048 lds\n\t" \
  "v_mfma_f32_16x16x32_bf16 v[40:43], a[224:227], v[48:51], v[180:183]\n\t" \
  "s_mov_b32 s96, 18432\n\t" \
  "v_exp_f32 v106, v106\n\t" \
  "v_mfma_f32_16x16x32_bf16 v[40:43], a[228:231], v[52:55], v[40:43]\n\t" \
  "ds_read_b64_tr_b16 a[156:157], v238 offset:8448\n\t" \
  "v_cvt_pk_bf16_f32 v155, v102, v103\n\t" \
  "v_mfma_f32_16x16x32_bf16 v[40:43], a[232:235], v[56:59], v[40:43]\n\t" \
  "v_exp_f32 v107, v107\n\t" \
  "v_mfma_f32_16x16x32_bf16 v[40:43], a[236:239], v[60:63], v[40:43]\n\t" \
  "ds_read_b64_tr_b16 a[158:159], v238 offset:12544\n\t" \
  "v_cvt_pk_bf16_f32 v156, v104, v105\n\t" \
  "v_mfma_f32_16x16x32_bf16 v[44:47], a[252:255], v[60:63], v[180:183]\n\t" \
  "ds_read_b64_tr_b16 a[160:161], v237 offset:512\n\t" \
  "v_cvt_pk_bf16_f32 v157, v106, v107\n\t" \
  "v_mfma_f32_16x16x32_bf16 v[44:47], a[248:251], v[56:59], v[44:47]\n\t" \
  "v_exp_f32 v108, v108\n\t" \
  "v_mfma_f32_16x16x32_bf16 v[44:47], a[244:247], v[52:55], v[44:47]\n\t" \
  "buffer_load_dwordx4 v232, %[sU], s96 offen offset:3072 lds\n\t" \
  "v_mfma_f32_16x16x32_bf16 v[44:47], a[240:243], v[48:51], v[44:47]\n\t" \
  "v_exp_f32 v109, v109\n\t" \
  "v_mfma_f32_16x16x32_bf16 v[64:67], a[192:195], v[80:83], v[184:187]\n\t" \
  "ds_read_b64_tr_b16 a[162:163], v237 offset:4608\n\t" \
  "v_cvt_pk_bf16_f32 v158, v108, v109\n\t" \
  "v_mfma_f32_16x16x32_bf16 v[64:67], a[196:199], v[84:87], v[64:67]\n\t" \
  "v_exp_f32 v110, v110\n\t" \
  "v_mfma_f32_16x16x32_bf16 v[64:67], a[200:203], v[88:91], v[64:67]\n\t" \
  "ds_read_b64_tr_b16 a[164:165], v237 offset:8704\n\t" \
  "ds_read_b64_tr_b16 a[166:167], v237 offset:12800\n\t" \
  "v_mfma_f32_16x16x32_bf16 v[64:67], a[204:207], v[92:95], v[64:67]\n\t" \
  "v_exp_f32 v111, v111\n\t" \
  "v_mfma_f32_16x16x32_bf16 v[68:71], a[220:223], v[92:95], v[184:187]\n\t" \
  "ds_read_b64_tr_b16 a[168:169], v238 offset:512\n\t" \
  "v_cvt_pk_bf16_f32 v159, v110, v111\n\t" \
  "v_mfma_f32_16x16x32_bf16 v[68:71], a[216:219], v[88:91], v[68:71]\n\t" \
  "v_exp_f32 v0, v0\n\t" \
  "v_mfma_f32_16x16x32_bf16 v[68:71], a[212:215], v[84:87], v[68:71]\n\t" \
  "v_exp_f32 v1, v1\n\t" \
  "v_mfma_f32_16x16x32_bf16 v[68:71], a[208:211], v[80:83], v[68:71]\n\t" \
  "ds_read_b64_tr_b16 a[170:171], v238 offset:4608\n\t" \
  "ds_read_b64_tr_b16 a[172:173], v238 offset:8704\n\t" \
  "v_mfma_f32_16x16x32_bf16 v[72:75], a[224:227], v[80:83], v[184:187]\n\t" \
  "s_add_u32 m0, %[m], 131072\n\t" \
  "s_mov_b32 s96, 28672\n\t" \
  "buffer_load_dwordx4 v232, %[sU], s96 offen offset:0 lds\n\t" \
  "v_mfma_f32_16x16x32_bf16 v[72:75], a[228:231], v[84:87], v[72:75]\n\t" \
  "s_mov_b32 s96, 34816\n\t" \
  "v_exp_f32 v2, v2\n\t" \
  "v_mfma_f32_16x16x32_bf16 v[72:75], a[232:235], v[88:91], v[72:75]\n\t" \
  "v_exp_f32 v3, v3\n\t" \
  "v_mfma_f32_16x16x32_bf16 v[72:75], a[236:239], v[92:95], v[72:75]\n\t" \
  "ds_read_b64_tr_b16 a[174:175], v238 offset:12800\n\t" \
  "ds_read_b64_tr_b16 a[176:177], v237 offset:768\n\t" \
  "v_mfma_f32_16x16x32_bf16 v[76:79], a[252:255], v[92:95], v[184:187]\n\t" \
  "v_exp_f32 v4, v4\n\t" \
  "v_mfma_f32_16x16x32_bf16 v[76:79], a[248:251], v[88:91], v[76:79]\n\t" \
  "v_exp_f32 v5, v5\n\t" \
  "v_mfma_f32_16x16x32_bf16 v[76:79], a[244:247], v[84:87], v[76:79]\n\t" \
  "ds_read_b64_tr_b16 a[178:179], v237 offset:4864\n\t" \
  "ds_read_b64_tr_b16 a[180:181], v237 offset:8960\n\t" \
  "v_mfma_f32_16x16x32_bf16 v[76:79], a[240:243], v[80:83], v[76:79]\n\t" \
  "v_exp_f32 v6, v6\n\t" \
  "v_mfma_f32_16x16x32_bf16 v[96:99], a[192:195], v[112:115], v[188:191]\n\t" \
  "v_exp_f32 v7, v7\n\t" \
  "v_mfma_f32_16x16x32_bf16 v[96:99], a[196:199], v[116:119], v[96:99]\n\t" \
  "v_exp_f32 v8, v8\n\t" \
  "v_mfma_f32_16x16x32_bf16 v[96:99], a[200:203], v[120:123], v[96:99]\n\t" \
  "buffer_load_dwordx4 v232, %[sU], s96 offen offset:1024 lds\n\t" \
  "v_mfma_f32_16x16x32_bf16 v[96:99], a[204:207], v[124:127], v[96:99]\n\t" \
  "ds_read_b64_tr_b16 a[182:183], v237 offset:13056\n\t" \
  "ds_read_b64_tr_b16 a[184:185], v238 offset:768\n\t" \
  "v_mfma_f32_16x16x32_bf16 v[100:103], a[220:223], v[124:127], v[188:191]\n\t" \
  "v_exp_f32 v9, v9\n\t" \
  "v_mfma_f32_16x16x32_bf16 v[100:103], a[216:219], v[120:123], v[100:103]\n\t" \
  "ds_read_b64_tr_b16 a[186:187], v238 offset:4864\n\t" \
  "ds_read_b64_tr_b16 a[188:189], v238 offset:8960\n\t" \
  "v_mfma_f32_16x16x32_bf16 v[100:103], a[212:215], v[116:119], v[100:103]\n\t" \
  "v_exp_f32 v10, v10\n\t" \
  "v_mfma_f32_16x16x32_bf16 v[100:103], a[208:211], v[112:115], v[100:103]\n\t" \
  "v_exp_f32 v11, v11\n\t" \
  "v_mfma_f32_16x16x32_bf16 v[104:107], a[224:227], v[112:115], v[188:191]\n\t" \
  "v_exp_f32 v12, v12\n\t" \
  "v_mfma_f32_16x16x32_bf16 v[104:107], a[228:231], v[116:119], v[104:107]\n\t" \
  "ds_read_b64_tr_b16 a[190:191], v238 offset:13056\n\t" \
  "v_mfma_f32_16x16x32_bf16 v[104:107], a[232:235], v[120:123], v[104:107]\n\t" \
  "v_exp_f32 v13, v13\n\t" \
  "v_mfma_f32_16x16x32_bf16 v[104:107], a[236:239], v[124:127], v[104:107]\n\t" \
  "v_exp_f32 v14, v14\n\t" \
  "v_mfma_f32_16x16x32_bf16 v[108:111], a[252:255], v[124:127], v[188:191]\n\t" \
  "v_exp_f32 v15, v15\n\t" \
  "v_mfma_f32_16x16x32_bf16 v[108:111], a[248:251], v[120:123], v[108:111]\n\t" \
  "v_exp_f32 v32, v32\n\t" \
  "v_mfma_f32_16x16x32_bf16 v[108:111], a[244:247], v[116:119], v[108:111]\n\t" \
  "v_exp_f32 v33, v33\n\t" \
  "v_mfma_f32_16x16x32_bf16 v[108:111], a[240:243], v[112:115], v[108:111]\n\t" \
  "s_waitcnt vmcnt(6) lgkmcnt(0)\n\t" \
  "s_barrier\n\t" \
  STA("s_memrealtime s[94:95]\n\t") \
  STA("s_memtime s[92:93]\n\t") \
  "v_mfma_f32_16x16x32_bf16 a[0:3], a[128:131], v[128:131], a[0:3]\n\t" \
  "v_exp_f32 v34, v34\n\t" \
  "v_mfma_f32_16x16x32_bf16 a[0:3], a[132:135], v[132:135], a[0:3]\n\t" \
  "v_exp_f32 v35, v35\n\t" \
  "v_mfma_f32_16x16x32_bf16 a[16:19], a[136:139], v[128:131], a[16:19]\n\t" \
  "ds_read_b128 a[192:195], v220 offset:16384\n\t" \
  "ds_read_b128 a[196:199], v221 offset:16384\n\t" \
  "v_mfma_f32_16x16x32_bf16 a[16:19], a[140:143], v[132:135], a[16:19]\n\t" \
  "v_exp_f32 v36, v36\n\t" \
  "v_mfma_f32_16x16x32_bf16 a[32:35], a[144:147], v[128:131], a[32:35]\n\t" \
  "v_exp_f32 v37, v37\n\t" \
  "v_mfma_f32_16x16x32_bf16 a[32:35], a[148:151], v[132:135], a[32:35]\n\t" \
  "v_exp_f32 v38, v38\n\t" \
  "v_mfma_f32_16x16x32_bf16 a[48:51], a[152:155], v[128:131], a[48:51]\n\t" \
  "v_exp_f32 v39, v39\n\t" \
  "v_mfma_f32_16x16x32_bf16 a[48:51], a[156:159], v[132:135], a[48:51]\n\t" \
  "v_exp_f32 v40, v40\n\t" \
  "v_mfma_f32_16x16x32_bf16 a[64:67], a[160:163], v[128:131], a[64:67]\n\t" \
  "v_exp_f32 v41, v41\n\t" \
  "v_mfma_f32_16x16x32_bf16 a[64:67], a[164:167], v[132:135], a[64:67]\n\t" \
  "v_exp_f32 v42, v42\n\t" \
  "v_mfma_f32_16x16x32_bf16 a[80:83], a[168:171], v[128:131], a[80:83]\n\t" \
  "ds_read_b128 a[200:203], v222 offset:16384\n\t" \
  "ds_read_b128 a[204:207], v223 offset:16384\n\t" \
  "v_mfma_f32_16x16x32_bf16 a[80:83], a[172:175], v[132:135], a[80:83]\n\t" \
  "v_exp_f32 v43, v43\n\t" \
  "v_mfma_f32_16x16x32_bf16 a[96:99], a[176:179], v[128:131], a[96:99]\n\t" \
  STA("v_writelane_b32 %[stv], s94, 32\n\t") \
  STA("v_writelane_b32 %[stv], s92, 33\n\t") \
  "buffer_store_dword v233, v247, %[ws], 0 offen sc1\n\t" \
  "v_mfma_f32_16x16x32_bf16 a[96:99], a[180:183], v[132:135], a[96:99]\n\t" \
  "v_exp_f32 v44, v44\n\t" \
  "v_mfma_f32_16x16x32_bf16 a[112:115], a[184:187], v[128:131], a[112:115]\n\t" \
  "v_exp_f32 v45, v45\n\t" \
  "v_mfma_f32_16x16x32_bf16 a[112:115], a[188:191], v[132:135], a[112:115]\n\t" \
  "v_exp_f32 v46, v46\n\t" \
  "v_mfma_f32_16x16x32_bf16 v[160:163], v[216:219], v[128:131], v[160:163]\n\t" \
  "v_exp_f32 v47, v47\n\t" \
  "v_mfma_f32_16x16x32_bf16 v[160:163], v[216:219], v[132:135], v[160:163]\n\t" \
  "ds_read_b128 a[208:211], v220 offset:20480\n\t" \
  "v_cvt_pk_bf16_f32 v128, v0, v1\n\t" \
  "v_mfma_f32_16x16x32_bf16 a[4:7], a[128:131], v[136:139], a[4:7]\n\t" \
  "v_exp_f32 v64, v64\n\t" \
  "v_mfma_f32_16x16x32_bf16 a[4:7], a[132:135], v[140:143], a[4:7]\n\t" \
  "ds_read_b128 a[212:215], v221 offset:20480\n\t" \
  "v_cvt_pk_bf16_f32 v129, v2, v3\n\t" \
  "v_mfma_f32_16x16x32_bf16 a[20:23], a[136:139], v[136:139], a[20:23]\n\t" \
  "s_add_u32 m0, %[m], 49152\n\t" \
  "v_exp_f32 v65, v65\n\t" \
  "v_mfma_f32_16x16x32_bf16 a[20:23], a[140:143], v[140:143], a[20:23]\n\t" \
  "s_add_u32 s96, %[sofk], 65536\n\t" \
  "v_exp_f32 v66, v66\n\t" \
  "v_mfma_f32_16x16x32_bf16 a[36:39], a[144:147], v[136:139], a[36:39]\n\t" \
  "v_exp_f32 v67, v67\n\t" \
  "v_mfma_f32_16x16x32_bf16 a[36:39], a[148:151], v[140:143], a[36:39]\n\t" \
  "ds_read_b128 a[216:219], v222 offset:20480\n\t" \
  "v_cvt_pk_bf16_f32 v130, v4, v5\n\t" \
  "v_mfma_f32_16x16x32_bf16 a[52:55], a[152:155], v[136:139], a[52:55]\n\t" \
  "v_exp_f32 v68, v68\n\t" \
  "v_mfma_f32_16x16x32_bf16 a[52:55], a[156:159], v[140:143], a[52:55]\n\t" \
  "v_exp_f32 v69, v69\n\t" \
  "v_mfma_f32_16x16x32_bf16 a[68:71], a[160:163], v[136:139], a[68:71]\n\t" \
  "v_exp_f32 v70, v70\n\t" \
  "v_mfma_f32_16x16x32_bf16 a[68:71], a[164:167], v[140:143], a[68:71]\n\t" \
  "buffer_load_dwordx4 v224, %[sK], s96 offen offset:0 lds\n\t" \
  "v_mfma_f32_16x16x32_bf16 a[84:87], a[168:171], v[136:139], a[84:87]\n\t" \
  "ds_read_b128 a[220:223], v223 offset:20480\n\t" \
  "v_cvt_pk_bf16_f32 v131, v6, v7\n\t" \
  "v_mfma_f32_16x16x32_bf16 a[84:87], a[172:175], v[140:143], a[84:87]\n\t" \
  "v_exp_f32 v71, v71\n\t" \
  "v_mfma_f32_16x16x32_bf16 a[100:103], a[176:179], v[136:139], a[100:103]\n\t" \
  "ds_read_b128 a[224:227], v220 offset:24576\n\t" \
  "v_cvt_pk_bf16_f32 v132, v8, v9\n\t" \
  "v_mfma_f32_16x16x32_bf16 a[100:103], a[180:183], v[140:143], a[100:103]\n\t" \
  "buffer_load_dwordx4 v225, %[sK], s96 offen offset:1024 lds\n\t" \
  "v_mfma_f32_16x16x32_bf16 a[116:119], a[184:187], v[136:139], a[116:119]\n\t" \
  "v_exp_f32 v72, v72\n\t" \
  "v_mfma_f32_16x16x32_bf16 a[116:119], a[188:191], v[140:143], a[116:119]\n\t" \
  "v_exp_f32 v73, v73\n\t" \
  "v_mfma_f32_16x16x32_bf16 v[164:167], v[216:219], v[136:139], v[164:167]\n\t" \
  "buffer_load_dwordx4 v226, %[sK], s96 offen offset:2048 lds\n\t" \
  "v_mfma_f32_16x16x32_bf16 v[164:167], v[216:219], v[140:143], v[164:167]\n\t" \
  "ds_read_b128 a[228:231], v221 offset:24576\n\t" \
  "v_cvt_pk_bf16_f32 v133, v10, v11\n\t" \
  "v_mfma_f32_16x16x32_bf16 a[8:11], a[128:131], v[144:147], a[8:11]\n\t" \
  "v_exp_f32 v74, v74\n\t" \
  "v_mfma_f32_16x16x32_bf16 a[8:11], a[132:135], v[148:151], a[8:11]\n\t" \
  "v_exp_f32 v75, v75\n\t" \
  "v_mfma_f32_16x16x32_bf16 a[24:27], a[136:139], v[144:147], a[24:27]\n\t" \
  "buffer_load_dwordx4 v227, %[sK], s96 offen offset:3072 lds\n\t" \
  "v_mfma_f32_16x16x32_bf16 a[24:27], a[140:143], v[148:151], a[24:27]\n\t" \
  "s_add_u32 m0, %[m], 147456\n\t" \
  "ds_read_b128 a[232:235], v222 offset:24576\n\t" \
  "v_cvt_pk_bf16_f32 v134, v12, v13\n\t" \
  "v_mfma_f32_16x16x32_bf16 a[40:43], a[144:147], v[144:147], a[40:43]\n\t" \
  "s_add_u32 s96, %[sofk], 49152\n\t" \
  "v_exp_f32 v76, v76\n\t" \
  "v_mfma_f32_16x16x32_bf16 a[40:43], a[148:151], v[148:151], a[40:43]\n\t" \
  "v_exp_f32 v77, v77\n\t" \
  "v_mfma_f32_16x16x32_bf16 a[56:59], a[152:155], v[144:147], a[56:59]\n\t" \
  "ds_read_b128 a[236:239], v223 offset:24576\n\t" \
  "v_cvt_pk_bf16_f32 v135, v14, v15\n\t" \
  "v_mfma_f32_16x16x32_bf16 a[56:59], a[156:159], v[148:151], a[56:59]\n\t" \
  "v_exp_f32 v78, v78\n\t" \
  "v_mfma_f32_16x16x32_bf16 a[72:75], a[160:163], v[144:147], a[72:75]\n\t" \
  "v_exp_f32 v79, v79\n\t" \
  "v_mfma_f32_16x16x32_bf16 a[72:75], a[164:167], v[148:151], a[72:75]\n\t" \
  "ds_read_b128 a[240:243], v220 offset:28672\n\t" \
  "v_cvt_pk_bf16_f32 v136, v32, v33\n\t" \
  "v_mfma_f32_16x16x32_bf16 a[88:91], a[168:171], v[144:147], a[88:91]\n\t" \
  "v_exp_f32 v96, v96\n\t" \
  "v_mfma_f32_16x16x32_bf16 a[88:91], a[172:175], v[148:151], a[88:91]\n\t" \
  "v_exp_f32 v97, v97\n\t" \
  "v_mfma_f32_16x16x32_bf16 a[104:107], a[176:179], v[144:147], a[104:107]\n\t" \
  "buffer_load_dwordx4 v230, %[sV], s96 offen offset:0 lds\n\t" \
  "v_mfma_f32_16x16x32_bf16 a[104:107], a[180:183], v[148:151], a[104:107]\n\t" \
  "ds_read_b128 a[244:247], v221 offset:28672\n\t" \
  "v_cvt_pk_bf16_f32 v137, v34, v35\n\t" \
  "v_mfma_f32_16x16x32_bf16 a[120:123], a[184:187], v[144:147], a[120:123]\n\t" \
  "v_exp_f32 v98, v98\n\t" \
  "v_mfma_f32_16x16x32_bf16 a[120:123], a[188:191], v[148:151], a[120:123]\n\t" \
  "buffer_load_dwordx4 v231, %[sV], s96 offen offset:1024 lds\n\t" \
  "v_mfma_f32_16x16x32_bf16 v[168:171], v[216:219], v[144:147], v[168:171]\n\t" \
  "v_exp_f32 v99, v99\n\t" \
  "v_mfma_f32_16x16x32_bf16 v[168:171], v[216:219], v[148:151], v[168:171]\n\t" \
  "ds_read_b128 a[248:251], v222 offset:28672\n\t" \
  "v_cvt_pk_bf16_f32 v138, v36, v37\n\t" \
  "v_mfma_f32_16x16x32_bf16 a[12:15], a[128:131], v[152:155], a[12:15]\n\t" \
  "v_exp_f32 v100, v100\n\t" \
  "v_mfma_f32_16x16x32_bf16 a[12:15], a[132:135], v[156:159], a[12:15]\n\t" \
  "buffer_load_dwordx4 v230, %[sV], s96 offen offset:2048 lds\n\t" \
  "v_mfma_f32_16x16x32_bf16 a[28:31], a[136:139], v[152:155], a[28:31]\n\t" \
  "v_exp_f32 v101, v101\n\t" \
  "v_mfma_f32_16x16x32_bf16 a[28:31], a[140:143], v[156:159], a[28:31]\n\t" \
  "ds_read_b128 a[252:255], v223 offset:28672\n\t" \
  "v_cvt_pk_bf16_f32 v139, v38, v39\n\t" \
  "v_mfma_f32_16x16x32_bf16 a[44:47], a[144:147], v[152:155], a[44:47]\n\t" \
  "buffer_load_dwordx4 v231, %[sV], s96 offen offset:3072 lds\n\t" \
  "v_mfma_f32_16x16x32_bf16 a[44:47], a[148:151], v[156:159], a[44:47]\n\t" \
  "v_exp_f32 v102, v102\n\t" \
  "v_mfma_f32_16x16x32_bf16 a[60:63], a[152:155], v[152:155], a[60:63]\n\t" \
  "v_exp_f32 v103, v103\n\t" \
  "v_mfma_f32_16x16x32_bf16 a[60:63], a[156:159], v[156:159], a[60:63]\n\t" \
  "v_exp_f32 v104, v104\n\t" \
  "v_mfma_f32_16x16x32_bf16 a[76:79], a[160:163], v[152:155], a[76:79]\n\t" \
  "v_cvt_pk_bf16_f32 v140, v40, v41\n\t" \
  "v_cvt_pk_bf16_f32 v141, v42, v43\n\t" \
  "v_mfma_f32_16x16x32_bf16 a[76:79], a[164:167], v[156:159], a[76:79]\n\t" \
  "s_add_u32 m0, %[m], 131072\n\t" \
  "s_mov_b32 s96, 40960\n\t" \
  "buffer_load_dwordx4 v232, %[sU], s96 offen offset:2048 lds\n\t" \
  "v_mfma_f32_16x16x32_bf16 a[92:95], a[168:171], v[152:155], a[92:95]\n\t" \
  "s_mov_b32 s96, 47104\n\t" \
  "v_exp_f32 v105, v105\n\t" \
  "v_mfma_f32_16x16x32_bf16 a[92:95], a[172:175], v[156:159], a[92:95]\n\t" \
  "v_exp_f32 v106, v106\n\t" \
  "v_mfma_f32_16x16x32_bf16 a[108:111], a[176:179], v[152:155], a[108:111]\n\t" \
  "v_exp_f32 v107, v107\n\t" \
  "v_mfma_f32_16x16x32_bf16 a[108:111], a[180:183], v[156:159], a[108:111]\n\t" \
  "buffer_load_dwordx4 v232, %[sU], s96 offen offset:3072 lds\n\t" \
  "v_mfma_f32_16x16x32_bf16 a[124:127], a[184:187], v[152:155], a[124:127]\n\t" \
  "v_exp_f32 v108, v108\n\t" \
  "v_mfma_f32_16x16x32_bf16 a[124:127], a[188:191], v[156:159], a[124:127]\n\t" \
  "v_exp_f32 v109, v109\n\t" \
  "v_mfma_f32_16x16x32_bf16 v[172:175], v[216:219], v[152:155], v[172:175]\n\t" \
  "v_cvt_pk_bf16_f32 v142, v44, v45\n\t" \
  "v_cvt_pk_bf16_f32 v143, v46, v47\n\t" \
  "v_mfma_f32_16x16x32_bf16 v[172:175], v[216:219], v[156:159], v[172:175]\n\t" \
  "s_waitcnt lgkmcnt(0)\n\t" \
  "s_mul_i32 s98, %[sofk], 5\n\t" \
  "s_xor_b32 s98, s98, 0\n\t" \
  "s_lshr_b32 s98, s98, 1\n\t" \
  "v_cvt_pk_bf16_f32 v144, v64, v65\n\t" \
  STA("s_memrealtime s[94:95]\n\t") \
  STA("s_memtime s[92:93]\n\t") \
  "v_mfma_f32_16x16x32_bf16 v[0:3], a[192:195], v[16:19], v[176:179]\n\t" \
  "buffer_load_dword %[pfd], v204, %[sD], s98 offen\n\t" \
  "v_mfma_f32_16x16x32_bf16 v[0:3], a[196:199], v[20:23], v[0:3]\n\t" \
  "ds_read_b64_tr_b16 a[128:129], v228 offset:0\n\t" \
  "v_cvt_pk_bf16_f32 v145, v66, v67\n\t" \
  "v_mfma_f32_16x16x32_bf16 v[0:3], a[200:203], v[24:27], v[0:3]\n\t" \
  "ds_read_b64_tr_b16 a[130:131], v228 offset:4096\n\t" \
  "v_cvt_pk_bf16_f32 v146, v68, v69\n\t" \
  "v_mfma_f32_16x16x32_bf16 v[0:3], a[204:207], v[28:31], v[0:3]\n\t" \
  "v_exp_f32 v110, v110\n\t" \
  "v_mfma_f32_16x16x32_bf16 v[4:7], a[220:223], v[28:31], v[176:179]\n\t" \
  "ds_read_b64_tr_b16 a[132:133], v228 offset:8192\n\t" \
  "v_cvt_pk_bf16_f32 v147, v70, v71\n\t" \
  "v_mfma_f32_16x16x32_bf16 v[4:7], a[216:219], v[24:27], v[4:7]\n\t" \
  "v_exp_f32 v111, v111\n\t" \
  "v_mfma_f32_16x16x32_bf16 v[4:7], a[212:215], v[20:23], v[4:7]\n\t" \
  "ds_read_b64_tr_b16 a[134:135], v228 offset:12288\n\t" \
  "v_cvt_pk_bf16_f32 v148, v72, v73\n\t" \
  "v_mfma_f32_16x16x32_bf16 v[4:7], a[208:211], v[16:19], v[4:7]\n\t" \
  "ds_read_b64_tr_b16 a[136:137], v229 offset:0\n\t" \
  "v_cvt_pk_bf16_f32 v149, v74, v75\n\t" \
  "v_mfma_f32_16x16x32_bf16 v[8:11], a[224:227], v[16:19], v[176:179]\n\t" \
  "v_exp_f32 v0, v0\n\t" \
  "v_mfma_f32_16x16x32_bf16 v[8:11], a[228:231], v[20:23], v[8:11]\n\t" \
  "ds_read_b64_tr_b16 a[138:139], v229 offset:4096\n\t" \
  "v_cvt_pk_bf16_f32 v150, v76, v77\n\t" \
  "v_mfma_f32_16x16x32_bf16 v[8:11], a[232:235], v[24:27], v[8:11]\n\t" \
  "v_exp_f32 v1, v1\n\t" \
  "v_mfma_f32_16x16x32_bf16 v[8:11], a[236:239], v[28:31], v[8:11]\n\t" \
  "ds_read_b64_tr_b16 a[140:141], v229 offset:8192\n\t" \
  "v_cvt_pk_bf16_f32 v151, v78, v79\n\t" \
  "v_mfma_f32_16x16x32_bf16 v[12:15], a[252:255], v[28:31], v[176:179]\n\t" \
  STA("v_writelane_b32 %[stv], s94, 34\n\t") \
  STA("v_writelane_b32 %[stv], s92, 35\n\t") \
  "s_mul_i32 s98, %[sofk], 5\n\t" \
  "s_xor_b32 s98, s98, 4096\n\t" \
  "s_lshr_b32 s98, s98, 1\n\t" \
  "v_cvt_pk_bf16_f32 v152, v96, v97\n\t" \
  "v_mfma_f32_16x16x32_bf16 v[12:15], a[248:251], v[24:27], v[12:15]\n\t" \
  "buffer_load_dword %[pfd], v204, %[sD], s98 offen\n\t" \
  "v_mfma_f32_16x16x32_bf16 v[12:15], a[244:247], v[20:23], v[12:15]\n\t" \
  "ds_read_b64_tr_b16 a[142:143], v229 offset:12288\n\t" \
  "v_cvt_pk_bf16_f32 v153, v98, v99\n\t" \
  "v_mfma_f32_16x16x32_bf16 v[12:15], a[240:243], v[16:19], v[12:15]\n\t" \
  "ds_read_b64_tr_b16 a[144:145], v228 offset:256\n\t" \
  "v_cvt_pk_bf16_f32 v154, v100, v101\n\t" \
  "v_mfma_f32_16x16x32_bf16 v[32:35], a[192:195], v[48:51], v[180:183]\n\t" \
  "ds_read_b64_tr_b16 a[146:147], v228 offset:4352\n\t" \
  "v_cvt_pk_bf16_f32 v155, v102, v103\n\t" \
  "v_mfma_f32_16x16x32_bf16 v[32:35], a[196:199], v[52:55], v[32:35]\n\t" \
  "v_exp_f32 v2, v2\n\t" \
  "v_mfma_f32_16x16x32_bf16 v[32:35], a[200:203], v[56:59], v[32:35]\n\t" \
  "ds_read_b64_tr_b16 a[148:149], v228 offset:8448\n\t" \
  "v_cvt_pk_bf16_f32 v156, v104, v105\n\t" \
  "v_mfma_f32_16x16x32_bf16 v[32:35], a[204:207], v[60:63], v[32:35]\n\t" \
  "v_exp_f32 v3, v3\n\t" \
  "v_mfma_f32_16x16x32_bf16 v[36:39], a[220:223], v[60:63], v[180:183]\n\t" \
  "ds_read_b64_tr_b16 a[150:151], v228 offset:12544\n\t" \
  "v_cvt_pk_bf16_f32 v157, v106, v107\n\t" \
  "v_mfma_f32_16x16x32_bf16 v[36:39], a[216:219], v[56:59], v[36:39]\n\t" \
  "ds_read_b64_tr_b16 a[152:153], v229 offset:256\n\t" \
  "v_cvt_pk_bf16_f32 v158, v108, v109\n\t" \
  "v_mfma_f32_16x16x32_bf16 v[36:39], a[212:215], v[52:55], v[36:39]\n\t" \
  "v_exp_f32 v4, v4\n\t" \
  "v_mfma_f32_16x16x32_bf16 v[36:39], a[208:211], v[48:51], v[36:39]\n\t" \
  "ds_read_b64_tr_b16 a[154:155], v229 offset:4352\n\t" \
  "v_cvt_pk_bf16_f32 v159, v110, v111\n\t" \
  "v_mfma_f32_16x16x32_bf16 v[40:43], a[224:227], v[48:51], v[180:183]\n\t" \
  "v_exp_f32 v5, v5\n\t" \
  "v_mfma_f32_16x16x32_bf16 v[40:43], a[228:231], v[52:55], v[40:43]\n\t" \
  "v_exp_f32 v6, v6\n\t" \
  "v_mfma_f32_16x16x32_bf16 v[40:43], a[232:235], v[56:59], v[40:43]\n\t" \
  "v_exp_f32 v7, v7\n\t" \
  "v_mfma_f32_16x16x32_bf16 v[40:43], a[236:239], v[60:63], v[40:43]\n\t" \
  "ds_read_b64_tr_b16 a[156:157], v229 offset:8448\n\t" \
  "ds_read_b64_tr_b16 a[158:159], v229 offset:12544\n\t" \
  "v_mfma_f32_16x16x32_bf16 v[44:47], a[252:255], v[60:63], v[180:183]\n\t" \
  "v_exp_f32 v8, v8\n\t" \
  "v_mfma_f32_16x16x32_bf16 v[44:47], a[248:251], v[56:59], v[44:47]\n\t" \
  "v_exp_f32 v9, v9\n\t" \
  "v_mfma_f32_16x16x32_bf16 v[44:47], a[244:247], v[52:55], v[44:47]\n\t" \
  "ds_read_b64_tr_b16 a[160:161], v228 offset:512\n\t" \
  "ds_read_b64_tr_b16 a[162:163], v228 offset:4608\n\t" \
  "v_mfma_f32_16x16x32_bf16 v[44:47], a[240:243], v[48:51], v[44:47]\n\t" \
  "v_exp_f32 v10, v10\n\t" \
  "v_mfma_f32_16x16x32_bf16 v[64:67], a[192:195], v[80:83], v[184:187]\n\t" \
  "buffer_load_dword %[pfd], v247, %[ws], 0 offen sc1\n\t" \
  "v_mfma_f32_16x16x32_bf16 v[64:67], a[196:199], v[84:87], v[64:67]\n\t" \
  "v_exp_f32 v11, v11\n\t" \
  "v_mfma_f32_16x16x32_bf16 v[64:67], a[200:203], v[88:91], v[64:67]\n\t" \
  "ds_read_b64_tr_b16 a[164:165], v228 offset:8704\n\t" \
  "ds_read_b64_tr_b16 a[166:167], v228 offset:12800\n\t" \
  "v_mfma_f32_16x16x32_bf16 v[64:67], a[204:207], v[92:95], v[64:67]\n\t" \
  "v_exp_f32 v12, v12\n\t" \
  "v_mfma_f32_16x16x32_bf16 v[68:71], a[220:223], v[92:95], v[184:187]\n\t" \
  "v_exp_f32 v13, v13\n\t" \
  "v_mfma_f32_16x16x32_bf16 v[68:71], a[216:219], v[88:91], v[68:71]\n\t" \
  "ds_read_b64_tr_b16 a[168:169], v229 offset:512\n\t" \
  "ds_read_b64_tr_b16 a[170:171], v229 offset:4608\n\t" \
  "v_mfma_f32_16x16x32_bf16 v[68:71], a[212:215], v[84:87], v[68:71]\n\t" \
  "v_exp_f32 v14, v14\n\t" \
  "v_mfma_f32_16x16x32_bf16 v[68:71], a[208:211], v[80:83], v[68:71]\n\t" \
  "v_exp_f32 v15, v15\n\t" \
  "v_mfma_f32_16x16x32_bf16 v[72:75], a[224:227], v[80:83], v[184:187]\n\t" \
  "v_exp_f32 v32, v32\n\t" \
  "v_mfma_f32_16x16x32_bf16 v[72:75], a[228:231], v[84:87], v[72:75]\n\t" \
  "ds_read_b64_tr_b16 a[172:173], v229 offset:8704\n\t" \
  "ds_read_b64_tr_b16 a[174:175], v229 offset:12800\n\t" \
  "v_mfma_f32_16x16x32_bf16 v[72:75], a[232:235], v[88:91], v[72:75]\n\t" \
  "v_exp_f32 v33, v33\n\t" \
  "v_mfma_f32_16x16x32_bf16 v[72:75], a[236:239], v[92:95], v[72:75]\n\t" \
  "v_exp_f32 v34, v34\n\t" \
  "v_mfma_f32_16x16x32_bf16 v[76:79], a[252:255], v[92:95], v[184:187]\n\t" \
  "ds_read_b64_tr_b16 a[176:177], v228 offset:768\n\t" \
  "ds_read_b64_tr_b16 a[178:179], v228 offset:4864\n\t" \
  "v_mfma_f32_16x16x32_bf16 v[76:79], a[248:251], v[88:91], v[76:79]\n\t" \
  "v_exp_f32 v35, v35\n\t" \
  "v_mfma_f32_16x16x32_bf16 v[76:79], a[244:247], v[84:87], v[76:79]\n\t" \
  "v_exp_f32 v36, v36\n\t" \
  "v_mfma_f32_16x16x32_bf16 v[76:79], a[240:243], v[80:83], v[76:79]\n\t" \
  "v_exp_f32 v37, v37\n\t" \
  "v_mfma_f32_16x16x32_bf16 v[96:99], a[192:195], v[112:115], v[188:191]\n\t" \
  "ds_read_b64_tr_b16 a[180:181], v228 offset:8960\n\t" \
  "ds_read_b64_tr_b16 a[182:183], v228 offset:13056\n\t" \
  "v_mfma_f32_16x16x32_bf16 v[96:99], a[196:199], v[116:119], v[96:99]\n\t" \
  "v_exp_f32 v38, v38\n\t" \
  "v_mfma_f32_16x16x32_bf16 v[96:99], a[200:203], v[120:123], v[96:99]\n\t" \
  "v_exp_f32 v39, v39\n\t" \
  "v_mfma_f32_16x16x32_bf16 v[96:99], a[204:207], v[124:127], v[96:99]\n\t" \
  "ds_read_b64_tr_b16 a[184:185], v229 offset:768\n\t" \
  "ds_read_b64_tr_b16 a[186:187], v229 offset:4864\n\t" \
  "v_mfma_f32_16x16x32_bf16 v[100:103], a[220:223], v[124:127], v[188:191]\n\t" \
  "v_exp_f32 v40, v40\n\t" \
  "v_mfma_f32_16x16x32_bf16 v[100:103], a[216:219], v[120:123], v[100:103]\n\t" \
  "v_exp_f32 v41, v41\n\t" \
  "v_mfma_f32_16x16x32_bf16 v[100:103], a[212:215], v[116:119], v[100:103]\n\t" \
  "v_exp_f32 v42, v42\n\t" \
  "v_mfma_f32_16x16x32_bf16 v[100:103], a[208:211], v[112:115], v[100:103]\n\t" \
  "ds_read_b64_tr_b16 a[188:189], v229 offset:8960\n\t" \
  "ds_read_b64_tr_b16 a[190:191], v229 offset:13056\n\t" \
  "v_mfma_f32_16x16x32_bf16 v[104:107], a[224:227], v[112:115], v[188:191]\n\t" \
  "v_exp_f32 v43, v43\n\t" \
  "v_mfma_f32_16x16x32_bf16 v[104:107], a[228:231], v[116:119], v[104:107]\n\t" \
  "v_exp_f32 v44, v44\n\t" \
  "v_mfma_f32_16x16x32_bf16 v[104:107], a[232:235], v[120:123], v[104:107]\n\t" \
  "s_getreg_b32 s98, hwreg(HW_REG_IB_STS, 0, 4)\n\t" \
  "s_cmp_lg_u32 s98, 0\n\t" \
  "s_addc_u32 %[na], %[na], 0\n\t" \
  "v_mfma_f32_16x16x32_bf16 v[104:107], a[236:239], v[124:127], v[104:107]\n\t" \
  "v_exp_f32 v45, v45\n\t" \
  "v_mfma_f32_16x16x32_bf16 v[108:111], a[252:255], v[124:127], v[188:191]\n\t" \
  "v_exp_f32 v46, v46\n\t" \
  "v_mfma_f32_16x16x32_bf16 v[108:111], a[248:251], v[120:123], v[108:111]\n\t" \
  "s_getreg_b32 s98, hwreg(HW_REG_IB_STS, 0, 4)\n\t" \
  "s_cmp_lg_u32 s98, 0\n\t" \
  "s_addc_u32 %[na], %[na], 0\n\t" \
  "v_mfma_f32_16x16x32_bf16 v[108:111], a[244:247], v[116:119], v[108:111]\n\t" \
  "v_exp_f32 v47, v47\n\t" \
  "v_mfma_f32_16x16x32_bf16 v[108:111], a[240:243], v[112:115], v[108:111]\n\t" \
  "s_waitcnt vmcnt(5) lgkmcnt(0)\n\t" \
  "s_barrier\n\t" \
  STA("s_memrealtime s[94:95]\n\t") \
  STA("s_memtime s[92:93]\n\t") \
  "v_mfma_f32_16x16x32_bf16 a[0:3], a[128:131], v[128:131], a[0:3]\n\t" \
  "v_exp_f32 v64, v64\n\t" \
  "v_mfma_f32_16x16x32_bf16 a[0:3], a[132:135], v[132:135], a[0:3]\n\t" \
  "v_exp_f32 v65, v65\n\t" \
  "v_mfma_f32_16x16x32_bf16 a[16:19], a[136:139], v[128:131], a[16:19]\n\t" \
  "ds_read_b128 a[192:195], v220 offset:0\n\t" \
  "ds_read_b128 a[196:199], v221 offset:0\n\t" \
  "v_mfma_f32_16x16x32_bf16 a[16:19], a[140:143], v[132:135], a[16:19]\n\t" \
  "v_exp_f32 v66, v66\n\t" \
  "v_mfma_f32_16x16x32_bf16 a[32:35], a[144:147], v[128:131], a[32:35]\n\t" \
  "v_exp_f32 v67, v67\n\t" \
  "v_mfma_f32_16x16x32_bf16 a[32:35], a[148:151], v[132:135], a[32:35]\n\t" \
  "v_exp_f32 v68, v68\n\t" \
  "v_mfma_f32_16x16x32_bf16 a[48:51], a[152:155], v[128:131], a[48:51]\n\t" \
  STS("v_writelane_b32 %[stv], %[na], 58\n\t") \
  "s_cmp_ge_u32 %[na], 1\n\t" \
  "s_cselect_b32 s98, 0x4000, 0\n\t" \
  "s_or_b32 s98, s98, %[tag]\n\t" \
  "s_cmp_ge_u32 %[na], 2\n\t" \
  "s_cselect_b32 %[na], 2.0, 0\n\t" \
  "s_or_b32 s98, s98, %[na]\n\t" \
  "v_mov_b32 v209, s98\n\t" \
  "v_mfma_f32_16x16x32_bf16 a[48:51], a[156:159], v[132:135], a[48:51]\n\t" \
  "v_exp_f32 v69, v69\n\t" \
  "v_mfma_f32_16x16x32_bf16 a[64:67], a[160:163], v[128:131], a[64:67]\n\t" \
  "ds_read_b128 a[200:203], v222 offset:0\n\t" \
  "ds_read_b128 a[204:207], v223 offset:0\n\t" \
  "v_mfma_f32_16x16x32_bf16 a[64:67], a[164:167], v[132:135], a[64:67]\n\t" \
  "v_exp_f32 v70, v70\n\t" \
  "v_mfma_f32_16x16x32_bf16 a[80:83], a[168:171], v[128:131], a[80:83]\n\t" \
  "ds_read_b128 a[208:211], v220 offset:4096\n\t" \
  "ds_read_b128 a[212:215], v221 offset:4096\n\t" \
  "v_mfma_f32_16x16x32_bf16 a[80:83], a[172:175], v[132:135], a[80:83]\n\t" \
  "v_bfrev_b32 v205, 1\n\t" \
  "v_writelane_b32 v205, %[vo], 0\n\t" \
  "v_mfma_f32_16x16x32_bf16 a[96:99], a[176:179], v[128:131], a[96:99]\n\t" \
  STA("v_writelane_b32 %[stv], s94, 36\n\t") \
  STA("v_writelane_b32 %[stv], s92, 37\n\t") \
  "buffer_store_dword v209, v205, %[ws], 0 offen sc1\n\t" \
  "v_mfma_f32_16x16x32_bf16 a[96:99], a[180:183], v[132:135], a[96:99]\n\t" \
  "v_exp_f32 v71, v71\n\t" \
  "v_mfma_f32_16x16x32_bf16 a[112:115], a[184:187], v[128:131], a[112:115]\n\t" \
  "v_exp_f32 v72, v72\n\t" \
  "v_mfma_f32_16x16x32_bf16 a[112:115], a[188:191], v[132:135], a[112:115]\n\t" \
  "v_exp_f32 v73, v73\n\t" \
  "v_mfma_f32_16x16x32_bf16 v[160:163], v[216:219], v[128:131], v[160:163]\n\t" \
  "v_exp_f32 v74, v74\n\t" \
  "v_mfma_f32_16x16x32_bf16 v[160:163], v[216:219], v[132:135], v[160:163]\n\t" \
  "v_exp_f32 v75, v75\n\t" \
  "v_mfma_f32_16x16x32_bf16 a[4:7], a[128:131], v[136:139], a[4:7]\n\t" \
  "v_exp_f32 v76, v76\n\t" \
  "v_mfma_f32_16x16x32_bf16 a[4:7], a[132:135], v[140:143], a[4:7]\n\t" \
  "v_exp_f32 v77, v77\n\t" \
  "v_mfma_f32_16x16x32_bf16 a[20:23], a[136:139], v[136:139], a[20:23]\n\t" \
  "s_add_u32 m0, %[m], 65536\n\t" \
  "v_exp_f32 v78, v78\n\t" \
  "v_mfma_f32_16x16x32_bf16 a[20:23], a[140:143], v[140:143], a[20:23]\n\t" \
  "s_add_u32 s96, %[sofk], 81920\n\t" \
  "v_exp_f32 v79, v79\n\t" \
  "v_mfma_f32_16x16x32_bf16 a[36:39], a[144:147], v[136:139], a[36:39]\n\t" \
  "v_exp_f32 v96, v96\n\t" \
  "v_mfma_f32_16x16x32_bf16 a[36:39], a[148:151], v[140:143], a[36:39]\n\t" \
  "ds_read_b128 a[216:219], v222 offset:4096\n\t" \
  "v_cvt_pk_bf16_f32 v128, v0, v1\n\t" \
  "v_mfma_f32_16x16x32_bf16 a[52:55], a[152:155], v[136:139], a[52:55]\n\t" \
  "v_exp_f32 v97, v97\n\t" \
  "v_mfma_f32_16x16x32_bf16 a[52:55], a[156:159], v[140:143], a[52:55]\n\t" \
  "v_exp_f32 v98, v98\n\t" \
  "v_mfma_f32_16x16x32_bf16 a[68:71], a[160:163], v[136:139], a[68:71]\n\t" \
  "v_exp_f32 v99, v99\n\t" \
  "v_mfma_f32_16x16x32_bf16 a[68:71], a[164:167], v[140:143], a[68:71]\n\t" \
  "buffer_load_dwordx4 v224, %[sK], s96 offen offset:0 lds\n\t" \
  "v_mfma_f32_16x16x32_bf16 a[84:87], a[168:171], v[136:139], a[84:87]\n\t" \
  "ds_read_b128 a[220:223], v223 offset:4096\n\t" \
  "v_cvt_pk_bf16_f32 v129, v2, v3\n\t" \
  "v_mfma_f32_16x16x32_bf16 a[84:87], a[172:175], v[140:143], a[84:87]\n\t" \
  "v_exp_f32 v100, v100\n\t" \
  "v_mfma_f32_16x16x32_bf16 a[100:103], a[176:179], v[136:139], a[100:103]\n\t" \
  "ds_read_b128 a[224:227], v220 offset:8192\n\t" \
  "v_cvt_pk_bf16_f32 v130, v4, v5\n\t" \
  "v_mfma_f32_16x16x32_bf16 a[100:103], a[180:183], v[140:143], a[100:103]\n\t" \
  "buffer_load_dwordx4 v225, %[sK], s96 offen offset:1024 lds\n\t" \
  "v_mfma_f32_16x16x32_bf16 a[116:119], a[184:187], v[136:139], a[116:119]\n\t" \
  "v_exp_f32 v101, v101\n\t" \
  "v_mfma_f32_16x16x32_bf16 a[116:119], a[188:191], v[140:143], a[116:119]\n\t" \
  "v_exp_f32 v102, v102\n\t" \
  "v_mfma_f32_16x16x32_bf16 v[164:167], v[216:219], v[136:139], v[164:167]\n\t" \
  "buffer_load_dwordx4 v226, %[sK], s96 offen offset:2048 lds\n\t" \
  "v_mfma_f32_16x16x32_bf16 v[164:167], v[216:219], v[140:143], v[164:167]\n\t" \
  "ds_read_b128 a[228:231], v221 offset:8192\n\t" \
  "v_cvt_pk_bf16_f32 v131, v6, v7\n\t" \
  "v_mfma_f32_16x16x32_bf16 a[8:11], a[128:131], v[144:147], a[8:11]\n\t" \
  "v_exp_f32 v103, v103\n\t" \
  "v_mfma_f32_16x16x32_bf16 a[8:11], a[132:135], v[148:151], a[8:11]\n\t" \
  "v_exp_f32 v104, v104\n\t" \
  "v_mfma_f32_16x16x32_bf16 a[24:27], a[136:139], v[144:147], a[24:27]\n\t" \
  "buffer_load_dwordx4 v227, %[sK], s96 offen offset:3072 lds\n\t" \
  "v_mfma_f32_16x16x32_bf16 a[24:27], a[140:143], v[148:151], a[24:27]\n\t" \
  "s_add_u32 m0, %[m], 81920\n\t" \
  "ds_read_b128 a[232:235], v222 offset:8192\n\t" \
  "v_cvt_pk_bf16_f32 v132, v8, v9\n\t" \
  "v_mfma_f32_16x16x32_bf16 a[40:43], a[144:147], v[144:147], a[40:43]\n\t" \
  "s_add_u32 s96, %[sofk], 65536\n\t" \
  "v_exp_f32 v105, v105\n\t" \
  "v_mfma_f32_16x16x32_bf16 a[40:43], a[148:151], v[148:151], a[40:43]\n\t" \
  "v_exp_f32 v106, v106\n\t" \
  "v_mfma_f32_16x16x32_bf16 a[56:59], a[152:155], v[144:147], a[56:59]\n\t" \
  "ds_read_b128 a[236:239], v223 offset:8192\n\t" \
  "v_cvt_pk_bf16_f32 v133, v10, v11\n\t" \
  ".Lhs7:\n\ts_cbranch_execz .Lhs7n\n\t" \
  "v_mfma_f32_16x16x32_bf16 a[56:59], a[156:159], v[148:151], a[56:59]\n\t" \
  "v_exp_f32 v107, v107\n\t" \
  "v_mfma_f32_16x16x32_bf16 a[72:75], a[160:163], v[144:147], a[72:75]\n\t" \
  "v_exp_f32 v108, v108\n\t" \
  "v_mfma_f32_16x16x32_bf16 a[72:75], a[164:167], v[148:151], a[72:75]\n\t" \
  "ds_read_b128 a[240:243], v220 offset:12288\n\t" \
  "v_cvt_pk_bf16_f32 v134, v12, v13\n\t" \
  "v_mfma_f32_16x16x32_bf16 a[88:91], a[168:171], v[144:147], a[88:91]\n\t" \
  "v_exp_f32 v109, v109\n\t" \
  "v_mfma_f32_16x16x32_bf16 a[88:91], a[172:175], v[148:151], a[88:91]\n\t" \
  "v_exp_f32 v110, v110\n\t" \
  "v_mfma_f32_16x16x32_bf16 a[104:107], a[176:179], v[144:147], a[104:107]\n\t" \
  "buffer_load_dwordx4 v230, %[sV], s96 offen offset:0 lds\n\t" \
  "v_mfma_f32_16x16x32_bf16 a[104:107], a[180:183], v[148:151], a[104:107]\n\t" \
  "ds_read_b128 a[244:247], v221 offset:12288\n\t" \
  "v_cvt_pk_bf16_f32 v135, v14, v15\n\t" \
  "v_mfma_f32_16x16x32_bf16 a[120:123], a[184:187], v[144:147], a[120:123]\n\t" \
  "v_exp_f32 v111, v111\n\t" \
  "v_mfma_f32_16x16x32_bf16 a[120:123], a[188:191], v[148:151], a[120:123]\n\t" \
  "buffer_load_dwordx4 v231, %[sV], s96 offen offset:1024 lds\n\t" \
  "v_mfma_f32_16x16x32_bf16 v[168:171], v[216:219], v[144:147], v[168:171]\n\t" \
  "ds_read_b128 a[248:251], v222 offset:12288\n\t" \
  "v_cvt_pk_bf16_f32 v136, v32, v33\n\t" \
  "v_mfma_f32_16x16x32_bf16 v[168:171], v[216:219], v[148:151], v[168:171]\n\t" \
  "ds_read_b128 a[252:255], v223 offset:12288\n\t" \
  "v_cvt_pk_bf16_f32 v137, v34, v35\n\t" \
  "v_mfma_f32_16x16x32_bf16 a[12:15], a[128:131], v[152:155], a[12:15]\n\t" \
  "v_cvt_pk_bf16_f32 v138, v36, v37\n\t" \
  "v_cvt_pk_bf16_f32 v139, v38, v39\n\t" \
  "v_mfma_f32_16x16x32_bf16 a[12:15], a[132:135], v[156:159], a[12:15]\n\t" \
  "buffer_load_dwordx4 v230, %[sV], s96 offen offset:2048 lds\n\t" \
  "v_mfma_f32_16x16x32_bf16 a[28:31], a[136:139], v[152:155], a[28:31]\n\t" \
  "v_cvt_pk_bf16_f32 v140, v40, v41\n\t" \
  "v_cvt_pk_bf16_f32 v141, v42, v43\n\t" \
  "v_mfma_f32_16x16x32_bf16 a[28:31], a[140:143], v[156:159], a[28:31]\n\t" \
  "v_cvt_pk_bf16_f32 v142, v44, v45\n\t" \
  "v_cvt_pk_bf16_f32 v143, v46, v47\n\t" \
  "v_mfma_f32_16x16x32_bf16 a[44:47], a[144:147], v[152:155], a[44:47]\n\t" \
  "buffer_load_dwordx4 v231, %[sV], s96 offen offset:3072 lds\n\t" \
  "v_mfma_f32_16x16x32_bf16 a[44:47], a[148:151], v[156:159], a[44:47]\n\t" \
  "v_cvt_pk_bf16_f32 v144, v64, v65\n\t" \
  "v_cvt_pk_bf16_f32 v145, v66, v67\n\t" \
  "v_mfma_f32_16x16x32_bf16 a[60:63], a[152:155], v[152:155], a[60:63]\n\t" \
  "v_cvt_pk_bf16_f32 v146, v68, v69\n\t" \
  "v_cvt_pk_bf16_f32 v147, v70, v71\n\t" \
  "v_mfma_f32_16x16x32_bf16 a[60:63], a[156:159], v[156:159], a[60:63]\n\t" \
  "s_add_u32 m0, %[m], 32768\n\t" \
  "s_mov_b32 s96, 0\n\t" \
  "v_cvt_pk_bf16_f32 v148, v72, v73\n\t" \
  "v_mfma_f32_16x16x32_bf16 a[76:79], a[160:163], v[152:155], a[76:79]\n\t" \
  "v_cvt_pk_bf16_f32 v149, v74, v75\n\t" \
  "v_cvt_pk_bf16_f32 v150, v76, v77\n\t" \
  "v_mfma_f32_16x16x32_bf16 a[76:79], a[164:167], v[156:159], a[76:79]\n\t" \
  "buffer_load_dwordx4 v232, %[sG], s96 offen offset:0 lds\n\t" \
  "v_mfma_f32_16x16x32_bf16 a[92:95], a[168:171], v[152:155], a[92:95]\n\t" \
  "s_mov_b32 s96, 6144\n\t" \
  "v_cvt_pk_bf16_f32 v151, v78, v79\n\t" \
  "v_mfma_f32_16x16x32_bf16 a[92:95], a[172:175], v[156:159], a[92:95]\n\t" \
  "v_mfma_f32_16x16x32_bf16 a[108:111], a[176:179], v[152:155], a[108:111]\n\t" \
  "v_mfma_f32_16x16x32_bf16 a[108:111], a[180:183], v[156:159], a[108:111]\n\t" \
  "buffer_load_dwordx4 v232, %[sG], s96 offen offset:1024 lds\n\t" \
  "v_mfma_f32_16x16x32_bf16 a[124:127], a[184:187], v[152:155], a[124:127]\n\t" \
  "v_mfma_f32_16x16x32_bf16 a[124:127], a[188:191], v[156:159], a[124:127]\n\t" \
  "s_add_u32 s98, %[pf2], 0\n\t" \
  "v_mfma_f32_16x16x32_bf16 v[172:175], v[216:219], v[152:155], v[172:175]\n\t" \
  "buffer_load_dword %[pfd], v202, %[sM], s98 offen\n\t" \
  "v_mfma_f32_16x16x32_bf16 v[172:175], v[216:219], v[156:159], v[172:175]\n\t" \
  "s_waitcnt lgkmcnt(0)\n\t" \
  "v_cvt_pk_bf16_f32 v152, v96, v97\n\t" \
  "v_cvt_pk_bf16_f32 v153, v98, v99\n\t" \
  STA("s_memrealtime s[94:95]\n\t") \
  STA("s_memtime s[92:93]\n\t") \
  "v_mfma_f32_16x16x32_bf16 v[0:3], a[192:195], v[16:19], v[176:179]\n\t" \
  "ds_read_b64_tr_b16 a[128:129], v237 offset:0\n\t" \
  "v_cvt_pk_bf16_f32 v154, v100, v101\n\t" \
  "v_mfma_f32_16x16x32_bf16 v[0:3], a[196:199], v[20:23], v[0:3]\n\t" \
  "ds_read_b64_tr_b16 a[130:131], v237 offset:4096\n\t" \
  "v_cvt_pk_bf16_f32 v155, v102, v103\n\t" \
  "v_mfma_f32_16x16x32_bf16 v[0:3], a[200:203], v[24:27], v[0:3]\n\t" \
  "ds_read_b64_tr_b16 a[132:133], v237 offset:8192\n\t" \
  "v_cvt_pk_bf16_f32 v156, v104, v105\n\t" \
  "v_mfma_f32_16x16x32_bf16 v[0:3], a[204:207], v[28:31], v[0:3]\n\t" \
  "ds_read_b64_tr_b16 a[134:135], v237 offset:12288\n\t" \
  "v_cvt_pk_bf16_f32 v157, v106, v107\n\t" \
  "v_mfma_f32_16x16x32_bf16 v[4:7], a[220:223], v[28:31], v[176:179]\n\t" \
  "ds_read_b64_tr_b16 a[136:137], v238 offset:0\n\t" \
  "v_cvt_pk_bf16_f32 v158, v108, v109\n\t" \
  "v_mfma_f32_16x16x32_bf16 v[4:7], a[216:219], v[24:27], v[4:7]\n\t" \
  "ds_read_b64_tr_b16 a[138:139], v238 offset:4096\n\t" \
  "v_cvt_pk_bf16_f32 v159, v110, v111\n\t" \
  "v_mfma_f32_16x16x32_bf16 v[4:7], a[212:215], v[20:23], v[4:7]\n\t" \
  "v_exp_f32 v0, v0\n\t" \
  "v_mfma_f32_16x16x32_bf16 v[4:7], a[208:211], v[16:19], v[4:7]\n\t" \
  "v_exp_f32 v1, v1\n\t" \
  "v_mfma_f32_16x16x32_bf16 v[8:11], a[224:227], v[16:19], v[176:179]\n\t" \
  "v_exp_f32 v2, v2\n\t" \
  "v_mfma_f32_16x16x32_bf16 v[8:11], a[228:231], v[20:23], v[8:11]\n\t" \
  "v_exp_f32 v3, v3\n\t" \
  "v_mfma_f32_16x16x32_bf16 v[8:11], a[232:235], v[24:27], v[8:11]\n\t" \
  "v_mfma_f32_16x16x32_bf16 v[8:11], a[236:239], v[28:31], v[8:11]\n\t" \
  "s_add_u32 s98, %[pf2], 24576\n\t" \
  "v_exp_f32 v4, v4\n\t" \
  "v_mfma_f32_16x16x32_bf16 v[12:15], a[252:255], v[28:31], v[176:179]\n\t" \
  STA("v_writelane_b32 %[stv], s94, 38\n\t") \
  STA("v_writelane_b32 %[stv], s92, 39\n\t") \
  "buffer_load_dword %[pfd], v202, %[sM], s98 offen\n\t" \
  "v_mfma_f32_16x16x32_bf16 v[12:15], a[248:251], v[24:27], v[12:15]\n\t" \
  "ds_read_b64_tr_b16 a[140:141], v238 offset:8192\n\t" \
  "ds_read_b64_tr_b16 a[142:143], v238 offset:12288\n\t" \
  "v_mfma_f32_16x16x32_bf16 v[12:15], a[244:247], v[20:23], v[12:15]\n\t" \
  "v_exp_f32 v5, v5\n\t" \
  "v_mfma_f32_16x16x32_bf16 v[12:15], a[240:243], v[16:19], v[12:15]\n\t" \
  "v_exp_f32 v6, v6\n\t" \
  "v_mfma_f32_16x16x32_bf16 v[32:35], a[192:195], v[48:51], v[180:183]\n\t" \
  "ds_read_b64_tr_b16 a[144:145], v237 offset:256\n\t" \
  "ds_read_b64_tr_b16 a[146:147], v237 offset:4352\n\t" \
  "v_mfma_f32_16x16x32_bf16 v[32:35], a[196:199], v[52:55], v[32:35]\n\t" \
  "v_exp_f32 v7, v7\n\t" \
  "v_mfma_f32_16x16x32_bf16 v[32:35], a[200:203], v[56:59], v[32:35]\n\t" \
  "v_exp_f32 v8, v8\n\t" \
  "v_mfma_f32_16x16x32_bf16 v[32:35], a[204:207], v[60:63], v[32:35]\n\t" \
  "v_exp_f32 v9, v9\n\t" \
  "v_mfma_f32_16x16x32_bf16 v[36:39], a[220:223], v[60:63], v[180:183]\n\t" \
  "ds_read_b64_tr_b16 a[148:149], v237 offset:8448\n\t" \
  "ds_read_b64_tr_b16 a[150:151], v237 offset:12544\n\t" \
  "v_mfma_f32_16x16x32_bf16 v[36:39], a[216:219], v[56:59], v[36:39]\n\t" \
  "v_exp_f32 v10, v10\n\t" \
  "v_mfma_f32_16x16x32_bf16 v[36:39], a[212:215], v[52:55], v[36:39]\n\t" \
  "v_exp_f32 v11, v11\n\t" \
  "v_mfma_f32_16x16x32_bf16 v[36:39], a[208:211], v[48:51], v[36:39]\n\t" \
  "ds_read_b64_tr_b16 a[152:153], v238 offset:256\n\t" \
  "ds_read_b64_tr_b16 a[154:155], v238 offset:4352\n\t" \
  "v_mfma_f32_16x16x32_bf16 v[40:43], a[224:227], v[48:51], v[180:183]\n\t" \
  "v_exp_f32 v12, v12\n\t" \
  "v_mfma_f32_16x16x32_bf16 v[40:43], a[228:231], v[52:55], v[40:43]\n\t" \
  "v_exp_f32 v13, v13\n\t" \
  "v_mfma_f32_16x16x32_bf16 v[40:43], a[232:235], v[56:59], v[40:43]\n\t" \
  "v_exp_f32 v14, v14\n\t" \
  "v_mfma_f32_16x16x32_bf16 v[40:43], a[236:239], v[60:63], v[40:43]\n\t" \
  "ds_read_b64_tr_b16 a[156:157], v238 offset:8448\n\t" \
  "ds_read_b64_tr_b16 a[158:159], v238 offset:12544\n\t" \
  "v_mfma_f32_16x16x32_bf16 v[44:47], a[252:255], v[60:63], v[180:183]\n\t" \
  "v_exp_f32 v15, v15\n\t" \
  "v_mfma_f32_16x16x32_bf16 v[44:47], a[248:251], v[56:59], v[44:47]\n\t" \
  "v_exp_f32 v32, v32\n\t" \
  "v_mfma_f32_16x16x32_bf16 v[44:47], a[244:247], v[52:55], v[44:47]\n\t" \
  "ds_read_b64_tr_b16 a[160:161], v237 offset:512\n\t" \
  "ds_read_b64_tr_b16 a[162:163], v237 offset:4608\n\t" \
  "v_mfma_f32_16x16x32_bf16 v[44:47], a[240:243], v[48:51], v[44:47]\n\t" \
  "v_exp_f32 v33, v33\n\t" \
  "v_mfma_f32_16x16x32_bf16 v[64:67], a[192:195], v[80:83], v[184:187]\n\t" \
  "v_exp_f32 v34, v34\n\t" \
  "v_mfma_f32_16x16x32_bf16 v[64:67], a[196:199], v[84:87], v[64:67]\n\t" \
  "v_exp_f32 v35, v35\n\t" \
  "v_mfma_f32_16x16x32_bf16 v[64:67], a[200:203], v[88:91], v[64:67]\n\t" \
  "ds_read_b64_tr_b16 a[164:165], v237 offset:8704\n\t" \
  "ds_read_b64_tr_b16 a[166:167], v237 offset:12800\n\t" \
  "v_mfma_f32_16x16x32_bf16 v[64:67], a[204:207], v[92:95], v[64:67]\n\t" \
  "v_exp_f32 v36, v36\n\t" \
  "v_mfma_f32_16x16x32_bf16 v[68:71], a[220:223], v[92:95], v[184:187]\n\t" \
  "v_exp_f32 v37, v37\n\t" \
  "v_mfma_f32_16x16x32_bf16 v[68:71], a[216:219], v[88:91], v[68:71]\n\t" \
  "ds_read_b64_tr_b16 a[168:169], v238 offset:512\n\t" \
  "ds_read_b64_tr_b16 a[170:171], v238 offset:4608\n\t" \
  "v_mfma_f32_16x16x32_bf16 v[68:71], a[212:215], v[84:87], v[68:71]\n\t" \
  "v_exp_f32 v38, v38\n\t" \
  "v_mfma_f32_16x16x32_bf16 v[68:71], a[208:211], v[80:83], v[68:71]\n\t" \
  "v_exp_f32 v39, v39\n\t" \
  "v_mfma_f32_16x16x32_bf16 v[72:75], a[224:227], v[80:83], v[184:187]\n\t" \
  "v_exp_f32 v40, v40\n\t" \
  "v_mfma_f32_16x16x32_bf16 v[72:75], a[228:231], v[84:87], v[72:75]\n\t" \
  "ds_read_b64_tr_b16 a[172:173], v238 offset:8704\n\t" \
  "ds_read_b64_tr_b16 a[174:175], v238 offset:12800\n\t" \
  "v_mfma_f32_16x16x32_bf16 v[72:75], a[232:235], v[88:91], v[72:75]\n\t" \
  "v_exp_f32 v41, v41\n\t" \
  "v_mfma_f32_16x16x32_bf16 v[72:75], a[236:239], v[92:95], v[72:75]\n\t" \
  "v_exp_f32 v42, v42\n\t" \
  "v_mfma_f32_16x16x32_bf16 v[76:79], a[252:255], v[92:95], v[184:187]\n\t" \
  "ds_read_b64_tr_b16 a[176:177], v237 offset:768\n\t" \
  "ds_read_b64_tr_b16 a[178:179], v237 offset:4864\n\t" \
  "v_mfma_f32_16x16x32_bf16 v[76:79], a[248:251], v[88:91], v[76:79]\n\t" \
  "v_exp_f32 v43, v43\n\t" \
  "v_mfma_f32_16x16x32_bf16 v[76:79], a[244:247], v[84:87], v[76:79]\n\t" \
  "v_exp_f32 v44, v44\n\t" \
  "v_mfma_f32_16x16x32_bf16 v[76:79], a[240:243], v[80:83], v[76:79]\n\t" \
  "v_exp_f32 v45, v45\n\t" \
  "v_mfma_f32_16x16x32_bf16 v[96:99], a[192:195], v[112:115], v[188:191]\n\t" \
  "ds_read_b64_tr_b16 a[180:181], v237 offset:8960\n\t" \
  "ds_read_b64_tr_b16 a[182:183], v237 offset:13056\n\t" \
  "v_mfma_f32_16x16x32_bf16 v[96:99], a[196:199], v[116:119], v[96:99]\n\t" \
  "v_exp_f32 v46, v46\n\t" \
  "v_mfma_f32_16x16x32_bf16 v[96:99], a[200:203], v[120:123], v[96:99]\n\t" \
  "v_exp_f32 v47, v47\n\t" \
  "v_mfma_f32_16x16x32_bf16 v[96:99], a[204:207], v[124:127], v[96:99]\n\t" \
  "ds_read_b64_tr_b16 a[184:185], v238 offset:768\n\t" \
  "ds_read_b64_tr_b16 a[186:187], v238 offset:4864\n\t" \
  "v_mfma_f32_16x16x32_bf16 v[100:103], a[220:223], v[124:127], v[188:191]\n\t" \
  "v_exp_f32 v64, v64\n\t" \
  "v_mfma_f32_16x16x32_bf16 v[100:103], a[216:219], v[120:123], v[100:103]\n\t" \
  "v_exp_f32 v65, v65\n\t" \
  "v_mfma_f32_16x16x32_bf16 v[100:103], a[212:215], v[116:119], v[100:103]\n\t" \
  "v_exp_f32 v66, v66\n\t" \
  "v_mfma_f32_16x16x32_bf16 v[100:103], a[208:211], v[112:115], v[100:103]\n\t" \
  "ds_read_b64_tr_b16 a[188:189], v238 offset:8960\n\t" \
  "ds_read_b64_tr_b16 a[190:191], v238 offset:13056\n\t" \
  "v_mfma_f32_16x16x32_bf16 v[104:107], a[224:227], v[112:115], v[188:191]\n\t" \
  "v_exp_f32 v67, v67\n\t" \
  "v_mfma_f32_16x16x32_bf16 v[104:107], a[228:231], v[116:119], v[104:107]\n\t" \
  "v_exp_f32 v68, v68\n\t" \
  "v_mfma_f32_16x16x32_bf16 v[104:107], a[232:235], v[120:123], v[104:107]\n\t" \
  "v_exp_f32 v69, v69\n\t" \
  "v_mfma_f32_16x16x32_bf16 v[104:107], a[236:239], v[124:127], v[104:107]\n\t" \
  "v_exp_f32 v70, v70\n\t" \
  "v_mfma_f32_16x16x32_bf16 v[108:111], a[252:255], v[124:127], v[188:191]\n\t" \
  "v_exp_f32 v71, v71\n\t" \
  "v_mfma_f32_16x16x32_bf16 v[108:111], a[248:251], v[120:123], v[108:111]\n\t" \
  "v_exp_f32 v72, v72\n\t" \
  "v_mfma_f32_16x16x32_bf16 v[108:111], a[244:247], v[116:119], v[108:111]\n\t" \
  "v_exp_f32 v73, v73\n\t" \
  "v_mfma_f32_16x16x32_bf16 v[108:111], a[240:243], v[112:115], v[108:111]\n\t" \
  "s_waitcnt vmcnt(4) lgkmcnt(0)\n\t" \
  "s_barrier\n\t" \
  STA("s_memrealtime s[94:95]\n\t") \
  STA("s_memtime s[92:93]\n\t") \
  "v_mfma_f32_16x16x32_bf16 a[0:3], a[128:131], v[128:131], a[0:3]\n\t" \
  "v_exp_f32 v74, v74\n\t" \
  "v_mfma_f32_16x16x32_bf16 a[0:3], a[132:135], v[132:135], a[0:3]\n\t" \
  "v_exp_f32 v75, v75\n\t" \
  "v_mfma_f32_16x16x32_bf16 a[16:19], a[136:139], v[128:131], a[16:19]\n\t" \
  "ds_read_b128 a[192:195], v220 offset:16384\n\t" \
  "ds_read_b128 a[196:199], v221 offset:16384\n\t" \
  "v_mfma_f32_16x16x32_bf16 a[16:19], a[140:143], v[132:135], a[16:19]\n\t" \
  "v_exp_f32 v76, v76\n\t" \
  "v_mfma_f32_16x16x32_bf16 a[32:35], a[144:147], v[128:131], a[32:35]\n\t" \
  "s_lshr_b32 s98, %[sofk], 6\n\t" \
  "v_xor_b32 v234, s98, v244\n\t" \
  "v_add_u32 v213, %[m], v242\n\t" \
  "v_mfma_f32_16x16x32_bf16 a[32:35], a[148:151], v[132:135], a[32:35]\n\t" \
  "v_exp_f32 v77, v77\n\t" \
  "v_mfma_f32_16x16x32_bf16 a[48:51], a[152:155], v[128:131], a[48:51]\n\t" \
  "v_exp_f32 v78, v78\n\t" \
  "v_mfma_f32_16x16x32_bf16 a[48:51], a[156:159], v[132:135], a[48:51]\n\t" \
  "v_exp_f32 v79, v79\n\t" \
  "v_mfma_f32_16x16x32_bf16 a[64:67], a[160:163], v[128:131], a[64:67]\n\t" \
  "ds_read_b128 a[200:203], v222 offset:16384\n\t" \
  "v_add_u32 v215, 98304, v213\n\t" \
  "v_mfma_f32_16x16x32_bf16 a[64:67], a[164:167], v[132:135], a[64:67]\n\t" \
  "v_exp_f32 v96, v96\n\t" \
  "v_mfma_f32_16x16x32_bf16 a[80:83], a[168:171], v[128:131], a[80:83]\n\t" \
  "v_exp_f32 v97, v97\n\t" \
  "v_mfma_f32_16x16x32_bf16 a[80:83], a[172:175], v[132:135], a[80:83]\n\t" \
  "v_exp_f32 v98, v98\n\t" \
  "v_mfma_f32_16x16x32_bf16 a[96:99], a[176:179], v[128:131], a[96:99]\n\t" \
  STA("v_writelane_b32 %[stv], s94, 40\n\t") \
  STA("v_writelane_b32 %[stv], s92, 41\n\t") \
  "ds_read_b128 a[204:207], v223 offset:16384\n\t" \
  "v_mov_b32 v243, %[tag]\n\t" \
  "v_mfma_f32_16x16x32_bf16 a[96:99], a[180:183], v[132:135], a[96:99]\n\t" \
  "v_exp_f32 v99, v99\n\t" \
  "v_mfma_f32_16x16x32_bf16 a[112:115], a[184:187], v[128:131], a[112:115]\n\t" \
  "ds_read_b128 a[208:211], v220 offset:20480\n\t" \
  "v_lshrrev_b32 v254, 3, v233\n\t" \
  "v_mfma_f32_16x16x32_bf16 a[112:115], a[188:191], v[132:135], a[112:115]\n\t" \
  "ds_read_b128 a[212:215], v221 offset:20480\n\t" \
  "v_min_u32 v254, 1, v254\n\t" \
  "v_mfma_f32_16x16x32_bf16 v[160:163], v[216:219], v[128:131], v[160:163]\n\t" \
  "ds_read_b128 a[216:219], v222 offset:20480\n\t" \
  "v_lshlrev_b32 v254, 31, v254\n\t" \
  "v_mfma_f32_16x16x32_bf16 v[160:163], v[216:219], v[132:135], v[160:163]\n\t" \
  "ds_read_b128 a[220:223], v223 offset:20480\n\t" \
  "v_lshl_or_b32 v254, v233, 9, v254\n\t" \
  "v_mfma_f32_16x16x32_bf16 a[4:7], a[128:131], v[136:139], a[4:7]\n\t" \
  "s_lshr_b32 m0, %[hl18], 18\n\t" \
  "s_brev_b32 s98, 1\n\t" \
  "v_writelane_b32 v254, s98, m0\n\t" \
  "v_mfma_f32_16x16x32_bf16 a[4:7], a[132:135], v[140:143], a[4:7]\n\t" \
  "s_lshr_b32 s98, %[hl18], 18\n\t" \
  "v_xor_b32 v245, s98, v233\n\t" \
  "v_and_b32 v245, 31, v245\n\t" \
  "s_lshr_b32 s98, %[hl18], 18\n\t" \
  "s_mul_i32 s98, s98, 127\n\t" \
  "v_add_lshl_u32 v245, v245, s98, 2\n\t" \
  "v_mfma_f32_16x16x32_bf16 a[20:23], a[136:139], v[136:139], a[20:23]\n\t" \
  "s_add_u32 m0, %[m], 49152\n\t" \
  "v_exp_f32 v100, v100\n\t" \
  "v_mfma_f32_16x16x32_bf16 a[20:23], a[140:143], v[140:143], a[20:23]\n\t" \
  "s_add_u32 s96, %[sofk], 98304\n\t" \
  "v_exp_f32 v101, v101\n\t" \
  "v_mfma_f32_16x16x32_bf16 a[36:39], a[144:147], v[136:139], a[36:39]\n\t" \
  "v_exp_f32 v102, v102\n\t" \
  "v_mfma_f32_16x16x32_bf16 a[36:39], a[148:151], v[140:143], a[36:39]\n\t" \
  "v_exp_f32 v103, v103\n\t" \
  "v_mfma_f32_16x16x32_bf16 a[52:55], a[152:155], v[136:139], a[52:55]\n\t" \
  "v_exp_f32 v104, v104\n\t" \
  "v_mfma_f32_16x16x32_bf16 a[52:55], a[156:159], v[140:143], a[52:55]\n\t" \
  "v_exp_f32 v105, v105\n\t" \
  "v_mfma_f32_16x16x32_bf16 a[68:71], a[160:163], v[136:139], a[68:71]\n\t" \
  "v_exp_f32 v106, v106\n\t" \
  "v_mfma_f32_16x16x32_bf16 a[68:71], a[164:167], v[140:143], a[68:71]\n\t" \
  "buffer_load_dwordx4 v224, %[sK], s96 offen offset:0 lds\n\t" \
  "v_mfma_f32_16x16x32_bf16 a[84:87], a[168:171], v[136:139], a[84:87]\n\t" \
  "ds_read_b128 a[224:227], v220 offset:24576\n\t" \
  "v_cvt_pk_bf16_f32 v128, v0, v1\n\t" \
  "v_mfma_f32_16x16x32_bf16 a[84:87], a[172:175], v[140:143], a[84:87]\n\t" \
  "v_exp_f32 v107, v107\n\t" \
  "v_mfma_f32_16x16x32_bf16 a[100:103], a[176:179], v[136:139], a[100:103]\n\t" \
  "v_exp_f32 v108, v108\n\t" \
  "v_mfma_f32_16x16x32_bf16 a[100:103], a[180:183], v[140:143], a[100:103]\n\t" \
  "buffer_load_dwordx4 v225, %[sK], s96 offen offset:1024 lds\n\t" \
  "v_mfma_f32_16x16x32_bf16 a[116:119], a[184:187], v[136:139], a[116:119]\n\t" \
  "v_exp_f32 v109, v109\n\t" \
  "v_mfma_f32_16x16x32_bf16 a[116:119], a[188:191], v[140:143], a[116:119]\n\t" \
  "v_exp_f32 v110, v110\n\t" \
  "v_mfma_f32_16x16x32_bf16 v[164:167], v[216:219], v[136:139], v[164:167]\n\t" \
  "buffer_load_dwordx4 v226, %[sK], s96 offen offset:2048 lds\n\t" \
  "v_mfma_f32_16x16x32_bf16 v[164:167], v[216:219], v[140:143], v[164:167]\n\t" \
  "ds_read_b128 a[228:231], v221 offset:24576\n\t" \
  "v_cvt_pk_bf16_f32 v129, v2, v3\n\t" \
  "v_mfma_f32_16x16x32_bf16 a[8:11], a[128:131], v[144:147], a[8:11]\n\t" \
  "v_exp_f32 v111, v111\n\t" \
  "v_mfma_f32_16x16x32_bf16 a[8:11], a[132:135], v[148:151], a[8:11]\n\t" \
  "ds_read_b128 a[232:235], v222 offset:24576\n\t" \
  "v_cvt_pk_bf16_f32 v130, v4, v5\n\t" \
  "v_mfma_f32_16x16x32_bf16 a[24:27], a[136:139], v[144:147], a[24:27]\n\t" \
  "buffer_load_dwordx4 v227, %[sK], s96 offen offset:3072 lds\n\t" \
  "v_mfma_f32_16x16x32_bf16 a[24:27], a[140:143], v[148:151], a[24:27]\n\t" \
  "s_add_u32 m0, %[m], 147456\n\t" \
  "ds_read_b128 a[236:239], v223 offset:24576\n\t" \
  "v_cvt_pk_bf16_f32 v131, v6, v7\n\t" \
  "v_mfma_f32_16x16x32_bf16 a[40:43], a[144:147], v[144:147], a[40:43]\n\t" \
  "s_add_u32 s96, %[sofk], 81920\n\t" \
  "ds_read_b128 a[240:243], v220 offset:28672\n\t" \
  "v_cvt_pk_bf16_f32 v132, v8, v9\n\t" \
  "v_mfma_f32_16x16x32_bf16 a[40:43], a[148:151], v[148:151], a[40:43]\n\t" \
  "ds_read_b128 a[244:247], v221 offset:28672\n\t" \
  "v_cvt_pk_bf16_f32 v133, v10, v11\n\t" \
  "v_mfma_f32_16x16x32_bf16 a[56:59], a[152:155], v[144:147], a[56:59]\n\t" \
  "v_cvt_pk_bf16_f32 v134, v12, v13\n\t" \
  "v_cvt_pk_bf16_f32 v135, v14, v15\n\t" \
  "v_mfma_f32_16x16x32_bf16 a[56:59], a[156:159], v[148:151], a[56:59]\n\t" \
  "ds_read_b128 a[248:251], v222 offset:28672\n\t" \
  "v_cvt_pk_bf16_f32 v136, v32, v33\n\t" \
  "v_mfma_f32_16x16x32_bf16 a[72:75], a[160:163], v[144:147], a[72:75]\n\t" \
  "v_cvt_pk_bf16_f32 v137, v34, v35\n\t" \
  "v_cvt_pk_bf16_f32 v138, v36, v37\n\t" \
  "v_mfma_f32_16x16x32_bf16 a[72:75], a[164:167], v[148:151], a[72:75]\n\t" \
  "v_cvt_pk_bf16_f32 v139, v38, v39\n\t" \
  "v_cvt_pk_bf16_f32 v140, v40, v41\n\t" \
  "v_mfma_f32_16x16x32_bf16 a[88:91], a[168:171], v[144:147], a[88:91]\n\t" \
  "v_cvt_pk_bf16_f32 v141, v42, v43\n\t" \
  "v_cvt_pk_bf16_f32 v142, v44, v45\n\t" \
  "v_mfma_f32_16x16x32_bf16 a[88:91], a[172:175], v[148:151], a[88:91]\n\t" \
  "ds_read_b128 a[252:255], v223 offset:28672\n\t" \
  "v_cvt_pk_bf16_f32 v143, v46, v47\n\t" \
  "v_mfma_f32_16x16x32_bf16 a[104:107], a[176:179], v[144:147], a[104:107]\n\t" \
  "buffer_load_dwordx4 v230, %[sV], s96 offen offset:0 lds\n\t" \
  "v_mfma_f32_16x16x32_bf16 a[104:107], a[180:183], v[148:151], a[104:107]\n\t" \
  "v_mfma_f32_16x16x32_bf16 a[120:123], a[184:187], v[144:147], a[120:123]\n\t" \
  "v_mfma_f32_16x16x32_bf16 a[120:123], a[188:191], v[148:151], a[120:123]\n\t" \
  "buffer_load_dwordx4 v231, %[sV], s96 offen offset:1024 lds\n\t" \
  "v_mfma_f32_16x16x32_bf16 v[168:171], v[216:219], v[144:147], v[168:171]\n\t" \
  "v_mfma_f32_16x16x32_bf16 v[168:171], v[216:219], v[148:151], v[168:171]\n\t" \
  "v_cvt_pk_bf16_f32 v144, v64, v65\n\t" \
  "v_cvt_pk_bf16_f32 v145, v66, v67\n\t" \
  "v_mfma_f32_16x16x32_bf16 a[12:15], a[128:131], v[152:155], a[12:15]\n\t" \
  "v_cvt_pk_bf16_f32 v146, v68, v69\n\t" \
  "v_cvt_pk_bf16_f32 v147, v70, v71\n\t" \
  "v_mfma_f32_16x16x32_bf16 a[12:15], a[132:135], v[156:159], a[12:15]\n\t" \
  "buffer_load_dwordx4 v230, %[sV], s96 offen offset:2048 lds\n\t" \
  "v_mfma_f32_16x16x32_bf16 a[28:31], a[136:139], v[152:155], a[28:31]\n\t" \
  "v_cvt_pk_bf16_f32 v148, v72, v73\n\t" \
  "v_cvt_pk_bf16_f32 v149, v74, v75\n\t" \
  "v_mfma_f32_16x16x32_bf16 a[28:31], a[140:143], v[156:159], a[28:31]\n\t" \
  "v_cvt_pk_bf16_f32 v150, v76, v77\n\t" \
  "v_cvt_pk_bf16_f32 v151, v78, v79\n\t" \
  "v_mfma_f32_16x16x32_bf16 a[44:47], a[144:147], v[152:155], a[44:47]\n\t" \
  "buffer_load_dwordx4 v231, %[sV], s96 offen offset:3072 lds\n\t" \
  "v_mfma_f32_16x16x32_bf16 a[44:47], a[148:151], v[156:159], a[44:47]\n\t" \
  "v_mfma_f32_16x16x32_bf16 a[60:63], a[152:155], v[152:155], a[60:63]\n\t" \
  "s_add_u32 m0, %[m], 32768\n\t" \
  "s_mov_b32 s96, 12288\n\t" \
  "v_mfma_f32_16x16x32_bf16 a[60:63], a[156:159], v[156:159], a[60:63]\n\t" \
  "v_mfma_f32_16x16x32_bf16 a[76:79], a[160:163], v[152:155], a[76:79]\n\t" \
  "v_mfma_f32_16x16x32_bf16 a[76:79], a[164:167], v[156:159], a[76:79]\n\t" \
  "buffer_load_dwordx4 v232, %[sG], s96 offen offset:2048 lds\n\t" \
  "v_mfma_f32_16x16x32_bf16 a[92:95], a[168:171], v[152:155], a[92:95]\n\t" \
  "s_mov_b32 s96, 18432\n\t" \
  "v_mfma_f32_16x16x32_bf16 a[92:95], a[172:175], v[156:159], a[92:95]\n\t" \
  "v_mfma_f32_16x16x32_bf16 a[108:111], a[176:179], v[152:155], a[108:111]\n\t" \
  "v_mfma_f32_16x16x32_bf16 a[108:111], a[180:183], v[156:159], a[108:111]\n\t" \
  "buffer_load_dwordx4 v232, %[sG], s96 offen offset:3072 lds\n\t" \
  "v_mfma_f32_16x16x32_bf16 a[124:127], a[184:187], v[152:155], a[124:127]\n\t" \
  "v_mfma_f32_16x16x32_bf16 a[124:127], a[188:191], v[156:159], a[124:127]\n\t" \
  "s_add_u32 s98, %[pf2], 49152\n\t" \
  "v_mfma_f32_16x16x32_bf16 v[172:175], v[216:219], v[152:155], v[172:175]\n\t" \
  "buffer_load_dword %[pfd], v202, %[sM], s98 offen\n\t" \
  "v_mfma_f32_16x16x32_bf16 v[172:175], v[216:219], v[156:159], v[172:175]\n\t" \
  "s_waitcnt lgkmcnt(0)\n\t" \
  "v_cvt_pk_bf16_f32 v152, v96, v97\n\t" \
  "v_cvt_pk_bf16_f32 v153, v98, v99\n\t" \
  STA("s_memrealtime s[94:95]\n\t") \
  STA("s_memtime s[92:93]\n\t") \
  "v_mfma_f32_16x16x32_bf16 v[0:3], a[192:195], v[16:19], v[176:179]\n\t" \
  "ds_read_b64_tr_b16 a[128:129], v228 offset:0\n\t" \
  "v_cvt_pk_bf16_f32 v154, v100, v101\n\t" \
  "v_mfma_f32_16x16x32_bf16 v[0:3], a[196:199], v[20:23], v[0:3]\n\t" \
  "ds_read_b64_tr_b16 a[130:131], v228 offset:4096\n\t" \
  "v_cvt_pk_bf16_f32 v155, v102, v103\n\t" \
  "v_mfma_f32_16x16x32_bf16 v[0:3], a[200:203], v[24:27], v[0:3]\n\t" \
  "ds_read_b64_tr_b16 a[132:133], v228 offset:8192\n\t" \
  "v_lshrrev_b32 v252, 3, v233\n\t" \
  "v_mfma_f32_16x16x32_bf16 v[0:3], a[204:207], v[28:31], v[0:3]\n\t" \
  "ds_read_b64_tr_b16 a[134:135], v228 offset:12288\n\t" \
  "v_cvt_pk_bf16_f32 v156, v104, v105\n\t" \
  "v_mfma_f32_16x16x32_bf16 v[4:7], a[220:223], v[28:31], v[176:179]\n\t" \
  "ds_read_b64_tr_b16 a[136:137], v229 offset:0\n\t" \
  "v_cvt_pk_bf16_f32 v157, v106, v107\n\t" \
  "v_mfma_f32_16x16x32_bf16 v[4:7], a[216:219], v[24:27], v[4:7]\n\t" \
  "ds_read_b64_tr_b16 a[138:139], v229 offset:4096\n\t" \
  "v_cvt_pk_bf16_f32 v158, v108, v109\n\t" \
  "v_mfma_f32_16x16x32_bf16 v[4:7], a[212:215], v[20:23], v[4:7]\n\t" \
  "v_exp_f32 v0, v0\n\t" \
  "v_mfma_f32_16x16x32_bf16 v[4:7], a[208:211], v[16:19], v[4:7]\n\t" \
  "v_exp_f32 v1, v1\n\t" \
  "v_mfma_f32_16x16x32_bf16 v[8:11], a[224:227], v[16:19], v[176:179]\n\t" \
  "v_exp_f32 v2, v2\n\t" \
  "v_mfma_f32_16x16x32_bf16 v[8:11], a[228:231], v[20:23], v[8:11]\n\t" \
  "v_exp_f32 v3, v3\n\t" \
  "v_mfma_f32_16x16x32_bf16 v[8:11], a[232:235], v[24:27], v[8:11]\n\t" \
  "ds_read_b64_tr_b16 a[140:141], v229 offset:8192\n\t" \
  "v_mul_u32_u24 v247, 98176, v252\n\t" \
  "v_mfma_f32_16x16x32_bf16 v[8:11], a[236:239], v[28:31], v[8:11]\n\t" \
  "s_add_u32 s98, %[pf2], 73728\n\t" \
  "v_exp_f32 v4, v4\n\t" \
  "v_mfma_f32_16x16x32_bf16 v[12:15], a[252:255], v[28:31], v[176:179]\n\t" \
  STA("v_writelane_b32 %[stv], s94, 42\n\t") \
  STA("v_writelane_b32 %[stv], s92, 43\n\t") \
  "buffer_load_dword %[pfd], v202, %[sM], s98 offen\n\t" \
  "v_mfma_f32_16x16x32_bf16 v[12:15], a[248:251], v[24:27], v[12:15]\n\t" \
  "ds_read_b64_tr_b16 a[142:143], v229 offset:12288\n\t" \
  "v_cvt_pk_bf16_f32 v159, v110, v111\n\t" \
  "v_mfma_f32_16x16x32_bf16 v[12:15], a[244:247], v[20:23], v[12:15]\n\t" \
  "ds_read_b64_tr_b16 a[144:145], v228 offset:256\n\t" \
  "v_lshl_add_u32 v247, v233, 4, v247\n\t" \
  "v_mfma_f32_16x16x32_bf16 v[12:15], a[240:243], v[16:19], v[12:15]\n\t" \
  "v_exp_f32 v5, v5\n\t" \
  "v_mfma_f32_16x16x32_bf16 v[32:35], a[192:195], v[48:51], v[180:183]\n\t" \
  "ds_read_b64_tr_b16 a[146:147], v228 offset:4352\n\t" \
  "v_lshlrev_b32 v252, 4, v252\n\t" \
  "v_mfma_f32_16x16x32_bf16 v[32:35], a[196:199], v[52:55], v[32:35]\n\t" \
  "v_exp_f32 v6, v6\n\t" \
  "v_mfma_f32_16x16x32_bf16 v[32:35], a[200:203], v[56:59], v[32:35]\n\t" \
  "v_exp_f32 v7, v7\n\t" \
  "v_mfma_f32_16x16x32_bf16 v[32:35], a[204:207], v[60:63], v[32:35]\n\t" \
  "v_exp_f32 v8, v8\n\t" \
  "v_mfma_f32_16x16x32_bf16 v[36:39], a[220:223], v[60:63], v[180:183]\n\t" \
  "ds_read_b64_tr_b16 a[148:149], v228 offset:8448\n\t" \
  "ds_read_b64_tr_b16 a[150:151], v228 offset:12544\n\t" \
  "v_mfma_f32_16x16x32_bf16 v[36:39], a[216:219], v[56:59], v[36:39]\n\t" \
  "v_exp_f32 v9, v9\n\t" \
  "v_mfma_f32_16x16x32_bf16 v[36:39], a[212:215], v[52:55], v[36:39]\n\t" \
  "v_exp_f32 v10, v10\n\t" \
  "v_mfma_f32_16x16x32_bf16 v[36:39], a[208:211], v[48:51], v[36:39]\n\t" \
  "ds_read_b64_tr_b16 a[152:153], v229 offset:256\n\t" \
  "ds_read_b64_tr_b16 a[154:155], v229 offset:4352\n\t" \
  "v_mfma_f32_16x16x32_bf16 v[40:43], a[224:227], v[48:51], v[180:183]\n\t" \
  "v_exp_f32 v11, v11\n\t" \
  "v_mfma_f32_16x16x32_bf16 v[40:43], a[228:231], v[52:55], v[40:43]\n\t" \
  "v_exp_f32 v12, v12\n\t" \
  "v_mfma_f32_16x16x32_bf16 v[40:43], a[232:235], v[56:59], v[40:43]\n\t" \
  "v_exp_f32 v13, v13\n\t" \
  "v_mfma_f32_16x16x32_bf16 v[40:43], a[236:239], v[60:63], v[40:43]\n\t" \
  "ds_read_b64_tr_b16 a[156:157], v229 offset:8448\n\t" \
  "ds_read_b64_tr_b16 a[158:159], v229 offset:12544\n\t" \
  "v_mfma_f32_16x16x32_bf16 v[44:47], a[252:255], v[60:63], v[180:183]\n\t" \
  "v_exp_f32 v14, v14\n\t" \
  "v_mfma_f32_16x16x32_bf16 v[44:47], a[248:251], v[56:59], v[44:47]\n\t" \
  "v_exp_f32 v15, v15\n\t" \
  "v_mfma_f32_16x16x32_bf16 v[44:47], a[244:247], v[52:55], v[44:47]\n\t" \
  "ds_read_b64_tr_b16 a[160:161], v228 offset:512\n\t" \
  "ds_read_b64_tr_b16 a[162:163], v228 offset:4608\n\t" \
  "v_mfma_f32_16x16x32_bf16 v[44:47], a[240:243], v[48:51], v[44:47]\n\t" \
  "v_exp_f32 v32, v32\n\t" \
  "v_mfma_f32_16x16x32_bf16 v[64:67], a[192:195], v[80:83], v[184:187]\n\t" \
  "v_exp_f32 v33, v33\n\t" \
  "v_mfma_f32_16x16x32_bf16 v[64:67], a[196:199], v[84:87], v[64:67]\n\t" \
  "v_exp_f32 v34, v34\n\t" \
  "v_mfma_f32_16x16x32_bf16 v[64:67], a[200:203], v[88:91], v[64:67]\n\t" \
  "ds_read_b64_tr_b16 a[164:165], v228 offset:8704\n\t" \
  "ds_read_b64_tr_b16 a[166:167], v228 offset:12800\n\t" \
  "v_mfma_f32_16x16x32_bf16 v[64:67], a[204:207], v[92:95], v[64:67]\n\t" \
  "v_exp_f32 v35, v35\n\t" \
  "v_mfma_f32_16x16x32_bf16 v[68:71], a[220:223], v[92:95], v[184:187]\n\t" \
  "v_exp_f32 v36, v36\n\t" \
  "v_mfma_f32_16x16x32_bf16 v[68:71], a[216:219], v[88:91], v[68:71]\n\t" \
  "ds_read_b64_tr_b16 a[168:169], v229 offset:512\n\t" \
  "ds_read_b64_tr_b16 a[170:171], v229 offset:4608\n\t" \
  "v_mfma_f32_16x16x32_bf16 v[68:71], a[212:215], v[84:87], v[68:71]\n\t" \
  "v_exp_f32 v37, v37\n\t" \
  "v_mfma_f32_16x16x32_bf16 v[68:71], a[208:211], v[80:83], v[68:71]\n\t" \
  "v_exp_f32 v38, v38\n\t" \
  "v_mfma_f32_16x16x32_bf16 v[72:75], a[224:227], v[80:83], v[184:187]\n\t" \
  "v_exp_f32 v39, v39\n\t" \
  "v_mfma_f32_16x16x32_bf16 v[72:75], a[228:231], v[84:87], v[72:75]\n\t" \
  "ds_read_b64_tr_b16 a[172:173], v229 offset:8704\n\t" \
  "ds_read_b64_tr_b16 a[174:175], v229 offset:12800\n\t" \
  "v_mfma_f32_16x16x32_bf16 v[72:75], a[232:235], v[88:91], v[72:75]\n\t" \
  "v_exp_f32 v40, v40\n\t" \
  "v_mfma_f32_16x16x32_bf16 v[72:75], a[236:239], v[92:95], v[72:75]\n\t" \
  "v_exp_f32 v41, v41\n\t" \
  "v_mfma_f32_16x16x32_bf16 v[76:79], a[252:255], v[92:95], v[184:187]\n\t" \
  "ds_read_b64_tr_b16 a[176:177], v228 offset:768\n\t" \
  "ds_read_b64_tr_b16 a[178:179], v228 offset:4864\n\t" \
  "v_mfma_f32_16x16x32_bf16 v[76:79], a[248:251], v[88:91], v[76:79]\n\t" \
  "v_exp_f32 v42, v42\n\t" \
  "v_mfma_f32_16x16x32_bf16 v[76:79], a[244:247], v[84:87], v[76:79]\n\t" \
  "v_exp_f32 v43, v43\n\t" \
  "v_mfma_f32_16x16x32_bf16 v[76:79], a[240:243], v[80:83], v[76:79]\n\t" \
  "v_exp_f32 v44, v44\n\t" \
  "v_mfma_f32_16x16x32_bf16 v[96:99], a[192:195], v[112:115], v[188:191]\n\t" \
  "ds_read_b64_tr_b16 a[180:181], v228 offset:8960\n\t" \
  "ds_read_b64_tr_b16 a[182:183], v228 offset:13056\n\t" \
  "v_mfma_f32_16x16x32_bf16 v[96:99], a[196:199], v[116:119], v[96:99]\n\t" \
  "v_exp_f32 v45, v45\n\t" \
  "v_mfma_f32_16x16x32_bf16 v[96:99], a[200:203], v[120:123], v[96:99]\n\t" \
  "v_exp_f32 v46, v46\n\t" \
  "v_mfma_f32_16x16x32_bf16 v[96:99], a[204:207], v[124:127], v[96:99]\n\t" \
  "ds_read_b64_tr_b16 a[184:185], v229 offset:768\n\t" \
  "ds_read_b64_tr_b16 a[186:187], v229 offset:4864\n\t" \
  "v_mfma_f32_16x16x32_bf16 v[100:103], a[220:223], v[124:127], v[188:191]\n\t" \
  "v_exp_f32 v47, v47\n\t" \
  "v_mfma_f32_16x16x32_bf16 v[100:103], a[216:219], v[120:123], v[100:103]\n\t" \
  "v_exp_f32 v64, v64\n\t" \
  "v_mfma_f32_16x16x32_bf16 v[100:103], a[212:215], v[116:119], v[100:103]\n\t" \
  "v_exp_f32 v65, v65\n\t" \
  "v_mfma_f32_16x16x32_bf16 v[100:103], a[208:211], v[112:115], v[100:103]\n\t" \
  "ds_read_b64_tr_b16 a[188:189], v229 offset:8960\n\t" \
  "ds_read_b64_tr_b16 a[190:191], v229 offset:13056\n\t" \
  "v_mfma_f32_16x16x32_bf16 v[104:107], a[224:227], v[112:115], v[188:191]\n\t" \
  "v_exp_f32 v66, v66\n\t" \
  "v_mfma_f32_16x16x32_bf16 v[104:107], a[228:231], v[116:119], v[104:107]\n\t" \
  "v_exp_f32 v67, v67\n\t" \
  "v_mfma_f32_16x16x32_bf16 v[104:107], a[232:235], v[120:123], v[104:107]\n\t" \
  "v_exp_f32 v68, v68\n\t" \
  "v_mfma_f32_16x16x32_bf16 v[104:107], a[236:239], v[124:127], v[104:107]\n\t" \
  "v_exp_f32 v69, v69\n\t" \
  "v_mfma_f32_16x16x32_bf16 v[108:111], a[252:255], v[124:127], v[188:191]\n\t" \
  "v_exp_f32 v70, v70\n\t" \
  "v_mfma_f32_16x16x32_bf16 v[108:111], a[248:251], v[120:123], v[108:111]\n\t" \
  "v_exp_f32 v71, v71\n\t" \
  "v_mfma_f32_16x16x32_bf16 v[108:111], a[244:247], v[116:119], v[108:111]\n\t" \
  "v_exp_f32 v72, v72\n\t" \
  "v_mfma_f32_16x16x32_bf16 v[108:111], a[240:243], v[112:115], v[108:111]\n\t" \
  "s_waitcnt vmcnt(4) lgkmcnt(0)\n\t" \
  "s_barrier\n\t" \
  STA("s_memrealtime s[94:95]\n\t") \
  STA("s_memtime s[92:93]\n\t") \
  "v_mfma_f32_16x16x32_bf16 a[0:3], a[128:131], v[128:131], a[0:3]\n\t" \
  "v_exp_f32 v73, v73\n\t" \
  "v_mfma_f32_16x16x32_bf16 a[0:3], a[132:135], v[132:135], a[0:3]\n\t" \
  "v_exp_f32 v74, v74\n\t" \
  "v_mfma_f32_16x16x32_bf16 a[16:19], a[136:139], v[128:131], a[16:19]\n\t" \
  "ds_read_b128 a[192:195], v220 offset:0\n\t" \
  "ds_read_b128 a[196:199], v221 offset:0\n\t" \
  "v_mfma_f32_16x16x32_bf16 a[16:19], a[140:143], v[132:135], a[16:19]\n\t" \
  "v_exp_f32 v75, v75\n\t" \
  "v_mfma_f32_16x16x32_bf16 a[32:35], a[144:147], v[128:131], a[32:35]\n\t" \
  "v_exp_f32 v76, v76\n\t" \
  "v_mfma_f32_16x16x32_bf16 a[32:35], a[148:151], v[132:135], a[32:35]\n\t" \
  "v_exp_f32 v77, v77\n\t" \
  "v_mfma_f32_16x16x32_bf16 a[48:51], a[152:155], v[128:131], a[48:51]\n\t" \
  "v_exp_f32 v78, v78\n\t" \
  "v_mfma_f32_16x16x32_bf16 a[48:51], a[156:159], v[132:135], a[48:51]\n\t" \
  "v_exp_f32 v79, v79\n\t" \
  "v_mfma_f32_16x16x32_bf16 a[64:67], a[160:163], v[128:131], a[64:67]\n\t" \
  "v_exp_f32 v96, v96\n\t" \
  "v_mfma_f32_16x16x32_bf16 a[64:67], a[164:167], v[132:135], a[64:67]\n\t" \
  "v_exp_f32 v97, v97\n\t" \
  "v_mfma_f32_16x16x32_bf16 a[80:83], a[168:171], v[128:131], a[80:83]\n\t" \
  "ds_read_b128 a[200:203], v222 offset:0\n\t" \
  "ds_read_b128 a[204:207], v223 offset:0\n\t" \
  "v_mfma_f32_16x16x32_bf16 a[80:83], a[172:175], v[132:135], a[80:83]\n\t" \
  "v_exp_f32 v98, v98\n\t" \
  "v_mfma_f32_16x16x32_bf16 a[96:99], a[176:179], v[128:131], a[96:99]\n\t" \
  STA("v_writelane_b32 %[stv], s94, 44\n\t") \
  STA("v_writelane_b32 %[stv], s92, 45\n\t") \
  "v_exp_f32 v99, v99\n\t" \
  "v_mfma_f32_16x16x32_bf16 a[96:99], a[180:183], v[132:135], a[96:99]\n\t" \
  "v_exp_f32 v100, v100\n\t" \
  "v_mfma_f32_16x16x32_bf16 a[112:115], a[184:187], v[128:131], a[112:115]\n\t" \
  "v_exp_f32 v101, v101\n\t" \
  "v_mfma_f32_16x16x32_bf16 a[112:115], a[188:191], v[132:135], a[112:115]\n\t" \
  "v_exp_f32 v102, v102\n\t" \
  "v_mfma_f32_16x16x32_bf16 v[160:163], v[216:219], v[128:131], v[160:163]\n\t" \
  "v_exp_f32 v103, v103\n\t" \
  "v_mfma_f32_16x16x32_bf16 v[160:163], v[216:219], v[132:135], v[160:163]\n\t" \
  "ds_read_b128 a[208:211], v220 offset:4096\n\t" \
  "v_cvt_pk_bf16_f32 v128, v0, v1\n\t" \
  "v_mfma_f32_16x16x32_bf16 a[4:7], a[128:131], v[136:139], a[4:7]\n\t" \
  "v_exp_f32 v104, v104\n\t" \
  "v_mfma_f32_16x16x32_bf16 a[4:7], a[132:135], v[140:143], a[4:7]\n\t" \
  "ds_read_b128 a[212:215], v221 offset:4096\n\t" \
  ".Lhs8:\n\ts_cbranch_execz .Lhs8n\n\t" \
  "v_cvt_pk_bf16_f32 v129, v2, v3\n\t" \
  "v_mfma_f32_16x16x32_bf16 a[20:23], a[136:139], v[136:139], a[20:23]\n\t" \
  "s_add_u32 m0, %[m], 65536\n\t" \
  "v_exp_f32 v105, v105\n\t" \
  "v_mfma_f32_16x16x32_bf16 a[20:23], a[140:143], v[140:143], a[20:23]\n\t" \
  "s_add_u32 s96, %[sofk], 114688\n\t" \
  "v_exp_f32 v106, v106\n\t" \
  "v_mfma_f32_16x16x32_bf16 a[36:39], a[144:147], v[136:139], a[36:39]\n\t" \
  "v_exp_f32 v107, v107\n\t" \
  "v_mfma_f32_16x16x32_bf16 a[36:39], a[148:151], v[140:143], a[36:39]\n\t" \
  "ds_read_b128 a[216:219], v222 offset:4096\n\t" \
  "v_cvt_pk_bf16_f32 v130, v4, v5\n\t" \
  "v_mfma_f32_16x16x32_bf16 a[52:55], a[152:155], v[136:139], a[52:55]\n\t" \
  "v_exp_f32 v108, v108\n\t" \
  "v_mfma_f32_16x16x32_bf16 a[52:55], a[156:159], v[140:143], a[52:55]\n\t" \
  "v_exp_f32 v109, v109\n\t" \
  "v_mfma_f32_16x16x32_bf16 a[68:71], a[160:163], v[136:139], a[68:71]\n\t" \
  "v_exp_f32 v110, v110\n\t" \
  "v_mfma_f32_16x16x32_bf16 a[68:71], a[164:167], v[140:143], a[68:71]\n\t" \
  "buffer_load_dwordx4 v224, %[sK], s96 offen offset:0 lds\n\t" \
  "v_mfma_f32_16x16x32_bf16 a[84:87], a[168:171], v[136:139], a[84:87]\n\t" \
  "ds_read_b128 a[220:223], v223 offset:4096\n\t" \
  "v_cvt_pk_bf16_f32 v131, v6, v7\n\t" \
  "v_mfma_f32_16x16x32_bf16 a[84:87], a[172:175], v[140:143], a[84:87]\n\t" \
  "v_exp_f32 v111, v111\n\t" \
  "v_mfma_f32_16x16x32_bf16 a[100:103], a[176:179], v[136:139], a[100:103]\n\t" \
  "ds_read_b128 a[224:227], v220 offset:8192\n\t" \
  "v_cvt_pk_bf16_f32 v132, v8, v9\n\t" \
  "v_mfma_f32_16x16x32_bf16 a[100:103], a[180:183], v[140:143], a[100:103]\n\t" \
  "buffer_load_dwordx4 v225, %[sK], s96 offen offset:1024 lds\n\t" \
  "v_mfma_f32_16x16x32_bf16 a[116:119], a[184:187], v[136:139], a[116:119]\n\t" \
  "ds_read_b128 a[228:231], v221 offset:8192\n\t" \
  "v_cvt_pk_bf16_f32 v133, v10, v11\n\t" \
  "v_mfma_f32_16x16x32_bf16 a[116:119], a[188:191], v[140:143], a[116:119]\n\t" \
  "ds_read_b128 a[232:235], v222 offset:8192\n\t" \
  "v_cvt_pk_bf16_f32 v134, v12, v13\n\t" \
  "v_mfma_f32_16x16x32_bf16 v[164:167], v[216:219], v[136:139], v[164:167]\n\t" \
  "buffer_load_dwordx4 v226, %[sK], s96 offen offset:2048 lds\n\t" \
  "v_mfma_f32_16x16x32_bf16 v[164:167], v[216:219], v[140:143], v[164:167]\n\t" \
  "ds_read_b128 a[236:239], v223 offset:8192\n\t" \
  "v_cvt_pk_bf16_f32 v135, v14, v15\n\t" \
  "v_mfma_f32_16x16x32_bf16 a[8:11], a[128:131], v[144:147], a[8:11]\n\t" \
  "ds_read_b128 a[240:243], v220 offset:12288\n\t" \
  "v_cvt_pk_bf16_f32 v136, v32, v33\n\t" \
  "v_mfma_f32_16x16x32_bf16 a[8:11], a[132:135], v[148:151], a[8:11]\n\t" \
  "v_cvt_pk_bf16_f32 v137, v34, v35\n\t" \
  "v_cvt_pk_bf16_f32 v138, v36, v37\n\t" \
  "v_mfma_f32_16x16x32_bf16 a[24:27], a[136:139], v[144:147], a[24:27]\n\t" \
  "buffer_load_dwordx4 v227, %[sK], s96 offen offset:3072 lds\n\t" \
  "v_mfma_f32_16x16x32_bf16 a[24:27], a[140:143], v[148:151], a[24:27]\n\t" \
  "s_add_u32 m0, %[m], 81920\n\t" \
  "ds_read_b128 a[244:247], v221 offset:12288\n\t" \
  "v_cvt_pk_bf16_f32 v139, v38, v39\n\t" \
  "v_mfma_f32_16x16x32_bf16 a[40:43], a[144:147], v[144:147], a[40:43]\n\t" \
  "s_add_u32 s96, %[sofk], 98304\n\t" \
  "v_cvt_pk_bf16_f32 v140, v40, v41\n\t" \
  "v_cvt_pk_bf16_f32 v141, v42, v43\n\t" \
  "v_mfma_f32_16x16x32_bf16 a[40:43], a[148:151], v[148:151], a[40:43]\n\t" \
  "v_cvt_pk_bf16_f32 v142, v44, v45\n\t" \
  "v_cvt_pk_bf16_f32 v143, v46, v47\n\t" \
  "v_mfma_f32_16x16x32_bf16 a[56:59], a[152:155], v[144:147], a[56:59]\n\t" \
  "v_mfma_f32_16x16x32_bf16 a[56:59], a[156:159], v[148:151], a[56:59]\n\t" \
  "v_mfma_f32_16x16x32_bf16 a[72:75], a[160:163], v[144:147], a[72:75]\n\t" \
  "v_mfma_f32_16x16x32_bf16 a[72:75], a[164:167], v[148:151], a[72:75]\n\t" \
  "v_mfma_f32_16x16x32_bf16 a[88:91], a[168:171], v[144:147], a[88:91]\n\t" \
  "v_mfma_f32_16x16x32_bf16 a[88:91], a[172:175], v[148:151], a[88:91]\n\t" \
  "v_mfma_f32_16x16x32_bf16 a[104:107], a[176:179], v[144:147], a[104:107]\n\t" \
  "buffer_load_dwordx4 v230, %[sV], s96 offen offset:0 lds\n\t" \
  "v_mfma_f32_16x16x32_bf16 a[104:107], a[180:183], v[148:151], a[104:107]\n\t" \
  "v_mfma_f32_16x16x32_bf16 a[120:123], a[184:187], v[144:147], a[120:123]\n\t" \
  "v_mfma_f32_16x16x32_bf16 a[120:123], a[188:191], v[148:151], a[120:123]\n\t" \
  "buffer_load_dwordx4 v231, %[sV], s96 offen offset:1024 lds\n\t" \
  "v_mfma_f32_16x16x32_bf16 v[168:171], v[216:219], v[144:147], v[168:171]\n\t" \
  "v_mfma_f32_16x16x32_bf16 v[168:171], v[216:219], v[148:151], v[168:171]\n\t" \
  "ds_read_b128 a[248:251], v222 offset:12288\n\t" \
  "v_cvt_pk_bf16_f32 v144, v64, v65\n\t" \
  "v_mfma_f32_16x16x32_bf16 a[12:15], a[128:131], v[152:155], a[12:15]\n\t" \
  "ds_read_b128 a[252:255], v223 offset:12288\n\t" \
  "v_cvt_pk_bf16_f32 v145, v66, v67\n\t" \
  "v_mfma_f32_16x16x32_bf16 a[12:15], a[132:135], v[156:159], a[12:15]\n\t" \
  "buffer_load_dwordx4 v230, %[sV], s96 offen offset:2048 lds\n\t" \
  "v_mfma_f32_16x16x32_bf16 a[28:31], a[136:139], v[152:155], a[28:31]\n\t" \
  "v_cvt_pk_bf16_f32 v146, v68, v69\n\t" \
  "v_cvt_pk_bf16_f32 v147, v70, v71\n\t" \
  "v_mfma_f32_16x16x32_bf16 a[28:31], a[140:143], v[156:159], a[28:31]\n\t" \
  "v_cvt_pk_bf16_f32 v148, v72, v73\n\t" \
  "v_cvt_pk_bf16_f32 v149, v74, v75\n\t" \
  "v_mfma_f32_16x16x32_bf16 a[44:47], a[144:147], v[152:155], a[44:47]\n\t" \
  "buffer_load_dwordx4 v231, %[sV], s96 offen offset:3072 lds\n\t" \
  "v_mfma_f32_16x16x32_bf16 a[44:47], a[148:151], v[156:159], a[44:47]\n\t" \
  "v_cvt_pk_bf16_f32 v150, v76, v77\n\t" \
  "v_cvt_pk_bf16_f32 v151, v78, v79\n\t" \
  "v_mfma_f32_16x16x32_bf16 a[60:63], a[152:155], v[152:155], a[60:63]\n\t" \
  "s_add_u32 m0, %[m], 98304\n\t" \
  "s_mov_b32 s96, 28672\n\t" \
  "v_mfma_f32_16x16x32_bf16 a[60:63], a[156:159], v[156:159], a[60:63]\n\t" \
  "v_mfma_f32_16x16x32_bf16 a[76:79], a[160:163], v[152:155], a[76:79]\n\t" \
  "v_mfma_f32_16x16x32_bf16 a[76:79], a[164:167], v[156:159], a[76:79]\n\t" \
  "buffer_load_dwordx4 v232, %[sG], s96 offen offset:0 lds\n\t" \
  "v_mfma_f32_16x16x32_bf16 a[92:95], a[168:171], v[152:155], a[92:95]\n\t" \
  "v_lshrrev_b32 v205, 1, v233\n\t" \
  "v_lshlrev_b32 v205, 2, v205\n\t" \
  "v_mfma_f32_16x16x32_bf16 a[92:95], a[172:175], v[156:159], a[92:95]\n\t" \
  "v_add_u32 v205, 8192, v205\n\t" \
  "v_mfma_f32_16x16x32_bf16 a[108:111], a[176:179], v[152:155], a[108:111]\n\t" \
  "buffer_load_dword v209, v205, %[ws], 0 offen sc1\n\t" \
  "v_mfma_f32_16x16x32_bf16 a[108:111], a[180:183], v[156:159], a[108:111]\n\t" \
  "s_mov_b32 s96, 34816\n\t" \
  "v_mfma_f32_16x16x32_bf16 a[124:127], a[184:187], v[152:155], a[124:127]\n\t" \
  "buffer_load_dwordx4 v232, %[sG], s96 offen offset:1024 lds\n\t" \
  "v_mfma_f32_16x16x32_bf16 a[124:127], a[188:191], v[156:159], a[124:127]\n\t" \
  "s_mul_i32 s98, %[sofk], 5\n\t" \
  "s_xor_b32 s98, s98, 8192\n\t" \
  "s_lshr_b32 s98, s98, 1\n\t" \
  "v_mfma_f32_16x16x32_bf16 v[172:175], v[216:219], v[152:155], v[172:175]\n\t" \
  "buffer_load_dword %[pfd], v204, %[sD], s98 offen\n\t" \
  "v_mfma_f32_16x16x32_bf16 v[172:175], v[216:219], v[156:159], v[172:175]\n\t" \
  "s_waitcnt lgkmcnt(0)\n\t" \
  "v_cvt_pk_bf16_f32 v152, v96, v97\n\t" \
  "v_cvt_pk_bf16_f32 v153, v98, v99\n\t" \
  STA("s_memrealtime s[94:95]\n\t") \
  STA("s_memtime s[92:93]\n\t") \
  "v_mfma_f32_16x16x32_bf16 v[0:3], a[192:195], v[16:19], v[176:179]\n\t" \
  "ds_read_b64_tr_b16 a[128:129], v237 offset:0\n\t" \
  "v_cvt_pk_bf16_f32 v154, v100, v101\n\t" \
  "v_mfma_f32_16x16x32_bf16 v[0:3], a[196:199], v[20:23], v[0:3]\n\t" \
  "ds_read_b64_tr_b16 a[130:131], v237 offset:4096\n\t" \
  "v_cvt_pk_bf16_f32 v155, v102, v103\n\t" \
  "v_mfma_f32_16x16x32_bf16 v[0:3], a[200:203], v[24:27], v[0:3]\n\t" \
  "ds_read_b64_tr_b16 a[132:133], v237 offset:8192\n\t" \
  "v_cvt_pk_bf16_f32 v156, v104, v105\n\t" \
  "v_mfma_f32_16x16x32_bf16 v[0:3], a[204:207], v[28:31], v[0:3]\n\t" \
  "ds_read_b64_tr_b16 a[134:135], v237 offset:12288\n\t" \
  "v_cvt_pk_bf16_f32 v157, v106, v107\n\t" \
  "v_mfma_f32_16x16x32_bf16 v[4:7], a[220:223], v[28:31], v[176:179]\n\t" \
  "ds_read_b64_tr_b16 a[136:137], v238 offset:0\n\t" \
  "v_cvt_pk_bf16_f32 v158, v108, v109\n\t" \
  "v_mfma_f32_16x16x32_bf16 v[4:7], a[216:219], v[24:27], v[4:7]\n\t" \
  "ds_read_b64_tr_b16 a[138:139], v238 offset:4096\n\t" \
  "v_cvt_pk_bf16_f32 v159, v110, v111\n\t" \
  "v_mfma_f32_16x16x32_bf16 v[4:7], a[212:215], v[20:23], v[4:7]\n\t" \
  "v_exp_f32 v0, v0\n\t" \
  "v_mfma_f32_16x16x32_bf16 v[4:7], a[208:211], v[16:19], v[4:7]\n\t" \
  "v_exp_f32 v1, v1\n\t" \
  "v_mfma_f32_16x16x32_bf16 v[8:11], a[224:227], v[16:19], v[176:179]\n\t" \
  "v_exp_f32 v2, v2\n\t" \
  "v_mfma_f32_16x16x32_bf16 v[8:11], a[228:231], v[20:23], v[8:11]\n\t" \
  "v_exp_f32 v3, v3\n\t" \
  "v_mfma_f32_16x16x32_bf16 v[8:11], a[232:235], v[24:27], v[8:11]\n\t" \
  "ds_read_b64_tr_b16 a[140:141], v238 offset:8192\n\t" \
  "ds_read_b64_tr_b16 a[142:143], v238 offset:12288\n\t" \
  "v_mfma_f32_16x16x32_bf16 v[8:11], a[236:239], v[28:31], v[8:11]\n\t" \
  "v_exp_f32 v4, v4\n\t" \
  "v_mfma_f32_16x16x32_bf16 v[12:15], a[252:255], v[28:31], v[176:179]\n\t" \
  STA("v_writelane_b32 %[stv], s94, 46\n\t") \
  STA("v_writelane_b32 %[stv], s92, 47\n\t") \
  "ds_read_b64_tr_b16 a[144:145], v237 offset:256\n\t" \
  "ds_read_b64_tr_b16 a[146:147], v237 offset:4352\n\t" \
  "v_mfma_f32_16x16x32_bf16 v[12:15], a[248:251], v[24:27], v[12:15]\n\t" \
  "ds_read_b64_tr_b16 a[148:149], v237 offset:8448\n\t" \
  "ds_read_b64_tr_b16 a[150:151], v237 offset:12544\n\t" \
  "v_lshl_add_u32 v207, v233, 14, %[cto]\n\t" \
  "v_min_u32 v207, .Lfz_z-.Lat_t1-4, v207\n\t" \
  "v_mfma_f32_16x16x32_bf16 v[12:15], a[244:247], v[20:23], v[12:15]\n\t" \
  "s_getpc_b64 s[96:97]\n\t" \
  ".Lat_t1:\n\t" \
  "global_load_dword %[pfd], v207, s[96:97]\n\t" \
  "v_mfma_f32_16x16x32_bf16 v[12:15], a[240:243], v[16:19], v[12:15]\n\t" \
  "s_mul_i32 s98, %[sofk], 5\n\t" \
  "s_xor_b32 s98, s98, 12288\n\t" \
  "s_lshr_b32 s98, s98, 1\n\t" \
  "v_mfma_f32_16x16x32_bf16 v[32:35], a[192:195], v[48:51], v[180:183]\n\t" \
  "buffer_load_dword %[pfd], v204, %[sD], s98 offen\n\t" \
  "v_mfma_f32_16x16x32_bf16 v[32:35], a[196:199], v[52:55], v[32:35]\n\t" \
  "v_exp_f32 v5, v5\n\t" \
  "v_mfma_f32_16x16x32_bf16 v[32:35], a[200:203], v[56:59], v[32:35]\n\t" \
  "v_exp_f32 v6, v6\n\t" \
  "v_mfma_f32_16x16x32_bf16 v[32:35], a[204:207], v[60:63], v[32:35]\n\t" \
  "v_exp_f32 v7, v7\n\t" \
  "v_mfma_f32_16x16x32_bf16 v[36:39], a[220:223], v[60:63], v[180:183]\n\t" \
  "v_exp_f32 v8, v8\n\t" \
  "v_mfma_f32_16x16x32_bf16 v[36:39], a[216:219], v[56:59], v[36:39]\n\t" \
  "v_exp_f32 v9, v9\n\t" \
  "v_mfma_f32_16x16x32_bf16 v[36:39], a[212:215], v[52:55], v[36:39]\n\t" \
  "v_exp_f32 v10, v10\n\t" \
  "v_mfma_f32_16x16x32_bf16 v[36:39], a[208:211], v[48:51], v[36:39]\n\t" \
  "ds_read_b64_tr_b16 a[152:153], v238 offset:256\n\t" \
  "ds_read_b64_tr_b16 a[154:155], v238 offset:4352\n\t" \
  "v_mfma_f32_16x16x32_bf16 v[40:43], a[224:227], v[48:51], v[180:183]\n\t" \
  "v_exp_f32 v11, v11\n\t" \
  "v_mfma_f32_16x16x32_bf16 v[40:43], a[228:231], v[52:55], v[40:43]\n\t" \
  "v_exp_f32 v12, v12\n\t" \
  "v_mfma_f32_16x16x32_bf16 v[40:43], a[232:235], v[56:59], v[40:43]\n\t" \
  "v_exp_f32 v13, v13\n\t" \
  "v_mfma_f32_16x16x32_bf16 v[40:43], a[236:239], v[60:63], v[40:43]\n\t" \
  "ds_read_b64_tr_b16 a[156:157], v238 offset:8448\n\t" \
  "ds_read_b64_tr_b16 a[158:159], v238 offset:12544\n\t" \
  "v_mfma_f32_16x16x32_bf16 v[44:47], a[252:255], v[60:63], v[180:183]\n\t" \
  "v_exp_f32 v14, v14\n\t" \
  "v_mfma_f32_16x16x32_bf16 v[44:47], a[248:251], v[56:59], v[44:47]\n\t" \
  "v_exp_f32 v15, v15\n\t" \
  "v_mfma_f32_16x16x32_bf16 v[44:47], a[244:247], v[52:55], v[44:47]\n\t" \
  "ds_read_b64_tr_b16 a[160:161], v237 offset:512\n\t" \
  "ds_read_b64_tr_b16 a[162:163], v237 offset:4608\n\t" \
  "v_mfma_f32_16x16x32_bf16 v[44:47], a[240:243], v[48:51], v[44:47]\n\t" \
  "v_exp_f32 v32, v32\n\t" \
  "v_mfma_f32_16x16x32_bf16 v[64:67], a[192:195], v[80:83], v[184:187]\n\t" \
  "v_exp_f32 v33, v33\n\t" \
  "v_mfma_f32_16x16x32_bf16 v[64:67], a[196:199], v[84:87], v[64:67]\n\t" \
  "v_exp_f32 v34, v34\n\t" \
  "v_mfma_f32_16x16x32_bf16 v[64:67], a[200:203], v[88:91], v[64:67]\n\t" \
  "ds_read_b64_tr_b16 a[164:165], v237 offset:8704\n\t" \
  "ds_read_b64_tr_b16 a[166:167], v237 offset:12800\n\t" \
  "v_mfma_f32_16x16x32_bf16 v[64:67], a[204:207], v[92:95], v[64:67]\n\t" \
  "v_exp_f32 v35, v35\n\t" \
  "v_mfma_f32_16x16x32_bf16 v[68:71], a[220:223], v[92:95], v[184:187]\n\t" \
  "v_exp_f32 v36, v36\n\t" \
  "v_mfma_f32_16x16x32_bf16 v[68:71], a[216:219], v[88:91], v[68:71]\n\t" \
  "ds_read_b64_tr_b16 a[168:169], v238 offset:512\n\t" \
  "ds_read_b64_tr_b16 a[170:171], v238 offset:4608\n\t" \
  "v_mfma_f32_16x16x32_bf16 v[68:71], a[212:215], v[84:87], v[68:71]\n\t" \
  "v_exp_f32 v37, v37\n\t" \
  "v_mfma_f32_16x16x32_bf16 v[68:71], a[208:211], v[80:83], v[68:71]\n\t" \
  "v_exp_f32 v38, v38\n\t" \
  "v_mfma_f32_16x16x32_bf16 v[72:75], a[224:227], v[80:83], v[184:187]\n\t" \
  "v_exp_f32 v39, v39\n\t" \
  "v_mfma_f32_16x16x32_bf16 v[72:75], a[228:231], v[84:87], v[72:75]\n\t" \
  "ds_read_b64_tr_b16 a[172:173], v238 offset:8704\n\t" \
  "ds_read_b64_tr_b16 a[174:175], v238 offset:12800\n\t" \
  "v_mfma_f32_16x16x32_bf16 v[72:75], a[232:235], v[88:91], v[72:75]\n\t" \
  "v_exp_f32 v40, v40\n\t" \
  "v_mfma_f32_16x16x32_bf16 v[72:75], a[236:239], v[92:95], v[72:75]\n\t" \
  "v_exp_f32 v41, v41\n\t" \
  "v_mfma_f32_16x16x32_bf16 v[76:79], a[252:255], v[92:95], v[184:187]\n\t" \
  "ds_read_b64_tr_b16 a[176:177], v237 offset:768\n\t" \
  "ds_read_b64_tr_b16 a[178:179], v237 offset:4864\n\t" \
  "v_mfma_f32_16x16x32_bf16 v[76:79], a[248:251], v[88:91], v[76:79]\n\t" \
  "v_exp_f32 v42, v42\n\t" \
  "v_mfma_f32_16x16x32_bf16 v[76:79], a[244:247], v[84:87], v[76:79]\n\t" \
  "v_exp_f32 v43, v43\n\t" \
  "v_mfma_f32_16x16x32_bf16 v[76:79], a[240:243], v[80:83], v[76:79]\n\t" \
  "v_exp_f32 v44, v44\n\t" \
  "v_mfma_f32_16x16x32_bf16 v[96:99], a[192:195], v[112:115], v[188:191]\n\t" \
  "ds_read_b64_tr_b16 a[180:181], v237 offset:8960\n\t" \
  "ds_read_b64_tr_b16 a[182:183], v237 offset:13056\n\t" \
  "v_mfma_f32_16x16x32_bf16 v[96:99], a[196:199], v[116:119], v[96:99]\n\t" \
  "v_exp_f32 v45, v45\n\t" \
  "v_mfma_f32_16x16x32_bf16 v[96:99], a[200:203], v[120:123], v[96:99]\n\t" \
  "v_exp_f32 v46, v46\n\t" \
  "v_mfma_f32_16x16x32_bf16 v[96:99], a[204:207], v[124:127], v[96:99]\n\t" \
  "ds_read_b64_tr_b16 a[184:185], v238 offset:768\n\t" \
  "ds_read_b64_tr_b16 a[186:187], v238 offset:4864\n\t" \
  "v_mfma_f32_16x16x32_bf16 v[100:103], a[220:223], v[124:127], v[188:191]\n\t" \
  "v_exp_f32 v47, v47\n\t" \
  "v_mfma_f32_16x16x32_bf16 v[100:103], a[216:219], v[120:123], v[100:103]\n\t" \
  "v_exp_f32 v64, v64\n\t" \
  "v_mfma_f32_16x16x32_bf16 v[100:103], a[212:215], v[116:119], v[100:103]\n\t" \
  "v_exp_f32 v65, v65\n\t" \
  "v_mfma_f32_16x16x32_bf16 v[100:103], a[208:211], v[112:115], v[100:103]\n\t" \
  "ds_read_b64_tr_b16 a[188:189], v238 offset:8960\n\t" \
  "ds_read_b64_tr_b16 a[190:191], v238 offset:13056\n\t" \
  "v_mfma_f32_16x16x32_bf16 v[104:107], a[224:227], v[112:115], v[188:191]\n\t" \
  "v_exp_f32 v66, v66\n\t" \
  "v_mfma_f32_16x16x32_bf16 v[104:107], a[228:231], v[116:119], v[104:107]\n\t" \
  "v_exp_f32 v67, v67\n\t" \
  "v_mfma_f32_16x16x32_bf16 v[104:107], a[232:235], v[120:123], v[104:107]\n\t" \
  "v_exp_f32 v68, v68\n\t" \
  "v_mfma_f32_16x16x32_bf16 v[104:107], a[236:239], v[124:127], v[104:107]\n\t" \
  "v_exp_f32 v69, v69\n\t" \
  "v_mfma_f32_16x16x32_bf16 v[108:111], a[252:255], v[124:127], v[188:191]\n\t" \
  "v_exp_f32 v70, v70\n\t" \
  "v_mfma_f32_16x16x32_bf16 v[108:111], a[248:251], v[120:123], v[108:111]\n\t" \
  "v_exp_f32 v71, v71\n\t" \
  "v_mfma_f32_16x16x32_bf16 v[108:111], a[244:247], v[116:119], v[108:111]\n\t" \
  "v_exp_f32 v72, v72\n\t" \
  "v_mfma_f32_16x16x32_bf16 v[108:111], a[240:243], v[112:115], v[108:111]\n\t" \
  "s_waitcnt vmcnt(6) lgkmcnt(0)\n\t" \
  "s_barrier\n\t" \
  STA("s_memrealtime s[94:95]\n\t") \
  STA("s_memtime s[92:93]\n\t") \
  "v_mfma_f32_16x16x32_bf16 a[0:3], a[128:131], v[128:131], a[0:3]\n\t" \
  "v_exp_f32 v73, v73\n\t" \
  "v_mfma_f32_16x16x32_bf16 a[0:3], a[132:135], v[132:135], a[0:3]\n\t" \
  "v_exp_f32 v74, v74\n\t" \
  "v_mfma_f32_16x16x32_bf16 a[16:19], a[136:139], v[128:131], a[16:19]\n\t" \
  "ds_read_b128 a[192:195], v220 offset:16384\n\t" \
  "ds_read_b128 a[196:199], v221 offset:16384\n\t" \
  "v_mfma_f32_16x16x32_bf16 a[16:19], a[140:143], v[132:135], a[16:19]\n\t" \
  "s_waitcnt vmcnt(4)\n\t" \
  "v_and_b32 v206, 0xbfffbfff, v209\n\t" \
  "v_cmp_ne_u32_e64 s[96:97], %[tag], v206\n\t" \
  "s_cmp_lg_u64 s[96:97], 0\n\t" \
  "s_cbranch_scc1 .Lat_v\n\t" \
  ".Lat_w:\n\t" \
  "v_mfma_f32_16x16x32_bf16 a[32:35], a[144:147], v[128:131], a[32:35]\n\t" \
  "v_exp_f32 v75, v75\n\t" \
  "v_mfma_f32_16x16x32_bf16 a[32:35], a[148:151], v[132:135], a[32:35]\n\t" \
  "v_exp_f32 v76, v76\n\t" \
  "v_mfma_f32_16x16x32_bf16 a[48:51], a[152:155], v[128:131], a[48:51]\n\t" \
  "ds_read_b128 a[200:203], v222 offset:16384\n\t" \
  "ds_read_b128 a[204:207], v223 offset:16384\n\t" \
  "v_mfma_f32_16x16x32_bf16 a[48:51], a[156:159], v[132:135], a[48:51]\n\t" \
  "ds_read_b128 a[208:211], v220 offset:20480\n\t" \
  "v_and_b32 v206, 1, v233\n\t" \
  "v_lshlrev_b32 v206, 4, v206\n\t" \
  "v_mfma_f32_16x16x32_bf16 a[64:67], a[160:163], v[128:131], a[64:67]\n\t" \
  "v_lshrrev_b32 v206, v206, v209\n\t" \
  "v_and_b32 v206, 0x4000, v206\n\t" \
  "v_mfma_f32_16x16x32_bf16 a[64:67], a[164:167], v[132:135], a[64:67]\n\t" \
  "v_cmp_ne_u32_e64 s[96:97], 0, v206\n\t" \
  STS("s_nop 4\n\t") \
  STS("v_writelane_b32 %[stv], s96, 60\n\t") \
  STS("v_writelane_b32 %[stv], s97, 61\n\t") \
  "s_bcnt1_i32_b32 s98, s96\n\t" \
  "s_bcnt1_i32_b32 %[na], s97\n\t" \
  "s_cmp_gt_u32 s98, %[na]\n\t" \
  "s_cselect_b32 %[nb], 0x2000, 0\n\t" \
  "s_cselect_b32 s98, s96, s97\n\t" \
  "s_cselect_b32 s96, s97, s96\n\t" \
  "v_mfma_f32_16x16x32_bf16 a[80:83], a[168:171], v[128:131], a[80:83]\n\t" \
  "s_lshr_b32 s97, s98, 16\n\t" \
  "s_bcnt1_i32_b32 s97, s97\n\t" \
  "s_lshr_b32 s96, s96, 16\n\t" \
  "s_bcnt1_i32_b32 s96, s96\n\t" \
  "s_add_u32 s96, s96, 7\n\t" \
  "s_cmp_ge_u32 s97, s96\n\t" \
  "s_cselect_b32 %[na], -1, 0\n\t" \
  "s_xor_b32 s98, %[po], %[nb]\n\t" \
  "s_and_b32 s98, s98, %[na]\n\t" \
  "s_xor_b32 %[eso], %[eso], s98\n\t" \
  STS("v_writelane_b32 %[stv], %[na], 57\n\t") \
  STS("v_writelane_b32 %[stv], %[nb], 59\n\t") \
  "v_mfma_f32_16x16x32_bf16 a[80:83], a[172:175], v[132:135], a[80:83]\n\t" \
  "v_exp_f32 v77, v77\n\t" \
  "v_mfma_f32_16x16x32_bf16 a[96:99], a[176:179], v[128:131], a[96:99]\n\t" \
  STA("v_writelane_b32 %[stv], s94, 48\n\t") \
  STA("v_writelane_b32 %[stv], s92, 49\n\t") \
  "v_exp_f32 v78, v78\n\t" \
  "v_mfma_f32_16x16x32_bf16 a[96:99], a[180:183], v[132:135], a[96:99]\n\t" \
  "s_xor_b32 s98, %[px], %[nb]\n\t" \
  "s_and_b32 s98, s98, %[na]\n\t" \
  "s_xor_b32 %[xcb], %[xcb], s98\n\t" \
  "s_xor_b32 %[frb], %[frb], %[nb]\n\t" \
  "s_xor_b32 %[fw2], %[fw2], %[nb]\n\t" \
  "v_mfma_f32_16x16x32_bf16 a[112:115], a[184:187], v[128:131], a[112:115]\n\t" \
  "v_exp_f32 v79, v79\n\t" \
  "v_mfma_f32_16x16x32_bf16 a[112:115], a[188:191], v[132:135], a[112:115]\n\t" \
  "v_exp_f32 v96, v96\n\t" \
  "v_mfma_f32_16x16x32_bf16 v[160:163], v[216:219], v[128:131], v[160:163]\n\t" \
  "v_exp_f32 v97, v97\n\t" \
  "v_mfma_f32_16x16x32_bf16 v[160:163], v[216:219], v[132:135], v[160:163]\n\t" \
  "v_exp_f32 v98, v98\n\t" \
  "v_mfma_f32_16x16x32_bf16 a[4:7], a[128:131], v[136:139], a[4:7]\n\t" \
  "v_exp_f32 v99, v99\n\t" \
  "v_mfma_f32_16x16x32_bf16 a[4:7], a[132:135], v[140:143], a[4:7]\n\t" \
  "ds_read_b128 a[212:215], v221 offset:20480\n\t" \
  "v_cvt_pk_bf16_f32 v128, v0, v1\n\t" \
  "v_mfma_f32_16x16x32_bf16 a[20:23], a[136:139], v[136:139], a[20:23]\n\t" \
  "s_add_u32 m0, %[m], 147456\n\t" \
  "v_exp_f32 v100, v100\n\t" \
  "v_mfma_f32_16x16x32_bf16 a[20:23], a[140:143], v[140:143], a[20:23]\n\t" \
  "s_add_u32 s96, %[sofk], 114688\n\t" \
  "v_exp_f32 v101, v101\n\t" \
  "v_mfma_f32_16x16x32_bf16 a[36:39], a[144:147], v[136:139], a[36:39]\n\t" \
  "v_exp_f32 v102, v102\n\t" \
  "v_mfma_f32_16x16x32_bf16 a[36:39], a[148:151], v[140:143], a[36:39]\n\t" \
  "ds_read_b128 a[216:219], v222 offset:20480\n\t" \
  "v_cvt_pk_bf16_f32 v129, v2, v3\n\t" \
  "v_mfma_f32_16x16x32_bf16 a[52:55], a[152:155], v[136:139], a[52:55]\n\t" \
  "v_exp_f32 v103, v103\n\t" \
  "v_mfma_f32_16x16x32_bf16 a[52:55], a[156:159], v[140:143], a[52:55]\n\t" \
  "v_exp_f32 v104, v104\n\t" \
  "v_mfma_f32_16x16x32_bf16 a[68:71], a[160:163], v[136:139], a[68:71]\n\t" \
  "v_exp_f32 v105, v105\n\t" \
  "v_mfma_f32_16x16x32_bf16 a[68:71], a[164:167], v[140:143], a[68:71]\n\t" \
  "ds_read_b128 a[220:223], v223 offset:20480\n\t" \
  "v_cvt_pk_bf16_f32 v130, v4, v5\n\t" \
  "v_mfma_f32_16x16x32_bf16 a[84:87], a[168:171], v[136:139], a[84:87]\n\t" \
  "buffer_load_dwordx4 v230, %[sV], s96 offen offset:0 lds\n\t" \
  "v_mfma_f32_16x16x32_bf16 a[84:87], a[172:175], v[140:143], a[84:87]\n\t" \
  "v_exp_f32 v106, v106\n\t" \
  "v_mfma_f32_16x16x32_bf16 a[100:103], a[176:179], v[136:139], a[100:103]\n\t" \
  "ds_read_b128 a[224:227], v220 offset:24576\n\t" \
  "v_cvt_pk_bf16_f32 v131, v6, v7\n\t" \
  "v_mfma_f32_16x16x32_bf16 a[100:103], a[180:183], v[140:143], a[100:103]\n\t" \
  "v_exp_f32 v107, v107\n\t" \
  "v_mfma_f32_16x16x32_bf16 a[116:119], a[184:187], v[136:139], a[116:119]\n\t" \
  "buffer_load_dwordx4 v231, %[sV], s96 offen offset:1024 lds\n\t" \
  "v_mfma_f32_16x16x32_bf16 a[116:119], a[188:191], v[140:143], a[116:119]\n\t" \
  "v_exp_f32 v108, v108\n\t" \
  "v_mfma_f32_16x16x32_bf16 v[164:167], v[216:219], v[136:139], v[164:167]\n\t" \
  "ds_read_b128 a[228:231], v221 offset:24576\n\t" \
  "v_cvt_pk_bf16_f32 v132, v8, v9\n\t" \
  "v_mfma_f32_16x16x32_bf16 v[164:167], v[216:219], v[140:143], v[164:167]\n\t" \
  "v_exp_f32 v109, v109\n\t" \
  "v_mfma_f32_16x16x32_bf16 a[8:11], a[128:131], v[144:147], a[8:11]\n\t" \
  "buffer_load_dwordx4 v230, %[sV], s96 offen offset:2048 lds\n\t" \
  "v_mfma_f32_16x16x32_bf16 a[8:11], a[132:135], v[148:151], a[8:11]\n\t" \
  "v_exp_f32 v110, v110\n\t" \
  "v_mfma_f32_16x16x32_bf16 a[24:27], a[136:139], v[144:147], a[24:27]\n\t" \
  "ds_read_b128 a[232:235], v222 offset:24576\n\t" \
  "v_cvt_pk_bf16_f32 v133, v10, v11\n\t" \
  "v_mfma_f32_16x16x32_bf16 a[24:27], a[140:143], v[148:151], a[24:27]\n\t" \
  "v_exp_f32 v111, v111\n\t" \
  "v_mfma_f32_16x16x32_bf16 a[40:43], a[144:147], v[144:147], a[40:43]\n\t" \
  "buffer_load_dwordx4 v231, %[sV], s96 offen offset:3072 lds\n\t" \
  "v_mfma_f32_16x16x32_bf16 a[40:43], a[148:151], v[148:151], a[40:43]\n\t" \
  "s_add_u32 m0, %[m], 49152\n\t" \
  "ds_read_b128 a[236:239], v223 offset:24576\n\t" \
  "v_cvt_pk_bf16_f32 v134, v12, v13\n\t" \
  "v_mfma_f32_16x16x32_bf16 a[56:59], a[152:155], v[144:147], a[56:59]\n\t" \
  "s_xor_b32 s97, %[wob], 262144\n\t" \
  "ds_read_b128 a[240:243], v220 offset:28672\n\t" \
  "v_cvt_pk_bf16_f32 v135, v14, v15\n\t" \
  "v_mfma_f32_16x16x32_bf16 a[56:59], a[156:159], v[148:151], a[56:59]\n\t" \
  "ds_read_b128 a[244:247], v221 offset:28672\n\t" \
  "v_cvt_pk_bf16_f32 v136, v32, v33\n\t" \
  "v_mfma_f32_16x16x32_bf16 a[72:75], a[160:163], v[144:147], a[72:75]\n\t" \
  "ds_read_b128 a[248:251], v222 offset:28672\n\t" \
  "v_cvt_pk_bf16_f32 v137, v34, v35\n\t" \
  "v_mfma_f32_16x16x32_bf16 a[72:75], a[164:167], v[148:151], a[72:75]\n\t" \
  "v_cvt_pk_bf16_f32 v138, v36, v37\n\t" \
  "v_cvt_pk_bf16_f32 v139, v38, v39\n\t" \
  "v_mfma_f32_16x16x32_bf16 a[88:91], a[168:171], v[144:147], a[88:91]\n\t" \
  "v_cvt_pk_bf16_f32 v140, v40, v41\n\t" \
  "v_cvt_pk_bf16_f32 v141, v42, v43\n\t" \
  "v_mfma_f32_16x16x32_bf16 a[88:91], a[172:175], v[148:151], a[88:91]\n\t" \
  "ds_read_b128 a[252:255], v223 offset:28672\n\t" \
  "v_cvt_pk_bf16_f32 v142, v44, v45\n\t" \
  "v_mfma_f32_16x16x32_bf16 a[104:107], a[176:179], v[144:147], a[104:107]\n\t" \
  "v_cvt_pk_bf16_f32 v143, v46, v47\n\t" \
  "v_mfma_f32_16x16x32_bf16 a[104:107], a[180:183], v[148:151], a[104:107]\n\t" \
  "v_mfma_f32_16x16x32_bf16 a[120:123], a[184:187], v[144:147], a[120:123]\n\t" \
  "v_mfma_f32_16x16x32_bf16 a[120:123], a[188:191], v[148:151], a[120:123]\n\t" \
  "v_mfma_f32_16x16x32_bf16 v[168:171], v[216:219], v[144:147], v[168:171]\n\t" \
  "buffer_load_dwordx4 v240, %[wo], s97 offen offset:0 lds\n\t" \
  "v_mfma_f32_16x16x32_bf16 v[168:171], v[216:219], v[148:151], v[168:171]\n\t" \
  "s_add_u32 s97, s97, 7168\n\t" \
  "v_cvt_pk_bf16_f32 v144, v64, v65\n\t" \
  "v_cvt_pk_bf16_f32 v145, v66, v67\n\t" \
  "v_mfma_f32_16x16x32_bf16 a[12:15], a[128:131], v[152:155], a[12:15]\n\t" \
  "v_cvt_pk_bf16_f32 v146, v68, v69\n\t" \
  "v_cvt_pk_bf16_f32 v147, v70, v71\n\t" \
  "v_mfma_f32_16x16x32_bf16 a[12:15], a[132:135], v[156:159], a[12:15]\n\t" \
  "v_cvt_pk_bf16_f32 v148, v72, v73\n\t" \
  "v_cvt_pk_bf16_f32 v149, v74, v75\n\t" \
  "v_mfma_f32_16x16x32_bf16 a[28:31], a[136:139], v[152:155], a[28:31]\n\t" \
  "buffer_load_dwordx4 v241, %[wo], s97 offen offset:1024 lds\n\t" \
  "v_mfma_f32_16x16x32_bf16 a[28:31], a[140:143], v[156:159], a[28:31]\n\t" \
  "s_add_u32 s97, s97, 7168\n\t" \
  "v_cvt_pk_bf16_f32 v150, v76, v77\n\t" \
  "v_cvt_pk_bf16_f32 v151, v78, v79\n\t" \
  "v_mfma_f32_16x16x32_bf16 a[44:47], a[144:147], v[152:155], a[44:47]\n\t" \
  "v_mfma_f32_16x16x32_bf16 a[44:47], a[148:151], v[156:159], a[44:47]\n\t" \
  "v_mfma_f32_16x16x32_bf16 a[60:63], a[152:155], v[152:155], a[60:63]\n\t" \
  "buffer_load_dwordx4 v240, %[wo], s97 offen offset:2048 lds\n\t" \
  "v_mfma_f32_16x16x32_bf16 a[60:63], a[156:159], v[156:159], a[60:63]\n\t" \
  "s_add_u32 s97, s97, 7168\n\t" \
  "v_mfma_f32_16x16x32_bf16 a[76:79], a[160:163], v[152:155], a[76:79]\n\t" \
  "v_mfma_f32_16x16x32_bf16 a[76:79], a[164:167], v[156:159], a[76:79]\n\t" \
  "v_mfma_f32_16x16x32_bf16 a[92:95], a[168:171], v[152:155], a[92:95]\n\t" \
  "buffer_load_dwordx4 v241, %[wo], s97 offen offset:3072 lds\n\t" \
  "v_mfma_f32_16x16x32_bf16 a[92:95], a[172:175], v[156:159], a[92:95]\n\t" \
  "v_mfma_f32_16x16x32_bf16 a[108:111], a[176:179], v[152:155], a[108:111]\n\t" \
  "v_mfma_f32_16x16x32_bf16 a[108:111], a[180:183], v[156:159], a[108:111]\n\t" \
  "v_mfma_f32_16x16x32_bf16 a[124:127], a[184:187], v[152:155], a[124:127]\n\t" \
  "v_mfma_f32_16x16x32_bf16 a[124:127], a[188:191], v[156:159], a[124:127]\n\t" \
  "v_mfma_f32_16x16x32_bf16 v[172:175], v[216:219], v[152:155], v[172:175]\n\t" \
  "v_mfma_f32_16x16x32_bf16 v[172:175], v[216:219], v[156:159], v[172:175]\n\t" \
  "s_waitcnt lgkmcnt(0)\n\t" \
  "s_barrier\n\t" \
  STA("s_memrealtime s[94:95]\n\t") \
  STA("s_memtime s[92:93]\n\t") \
  "v_mfma_f32_16x16x32_bf16 v[0:3], a[192:195], v[16:19], v[176:179]\n\t" \
  "ds_read_b64_tr_b16 a[128:129], v228 offset:0\n\t" \
  "v_cvt_pk_bf16_f32 v152, v96, v97\n\t" \
  "v_mfma_f32_16x16x32_bf16 v[0:3], a[196:199], v[20:23], v[0:3]\n\t" \
  "ds_read_b64_tr_b16 a[130:131], v228 offset:4096\n\t" \
  "v_cvt_pk_bf16_f32 v153, v98, v99\n\t" \
  "v_mfma_f32_16x16x32_bf16 v[0:3], a[200:203], v[24:27], v[0:3]\n\t" \
  "ds_read_b64_tr_b16 a[132:133], v228 offset:8192\n\t" \
  "v_cvt_pk_bf16_f32 v154, v100, v101\n\t" \
  "v_mfma_f32_16x16x32_bf16 v[0:3], a[204:207], v[28:31], v[0:3]\n\t" \
  "ds_read_b64_tr_b16 a[134:135], v228 offset:12288\n\t" \
  "v_cvt_pk_bf16_f32 v155, v102, v103\n\t" \
  "v_mfma_f32_16x16x32_bf16 v[4:7], a[220:223], v[28:31], v[176:179]\n\t" \
  "s_add_u32 m0, %[m], 65536\n\t" \
  "ds_read_b64_tr_b16 a[136:137], v229 offset:0\n\t" \
  "v_cvt_pk_bf16_f32 v156, v104, v105\n\t" \
  "v_mfma_f32_16x16x32_bf16 v[4:7], a[216:219], v[24:27], v[4:7]\n\t" \
  "s_xor_b32 s97, %[wob], 393216\n\t" \
  "v_exp_f32 v0, v0\n\t" \
  "v_mfma_f32_16x16x32_bf16 v[4:7], a[212:215], v[20:23], v[4:7]\n\t" \
  "v_exp_f32 v1, v1\n\t" \
  "v_mfma_f32_16x16x32_bf16 v[4:7], a[208:211], v[16:19], v[4:7]\n\t" \
  "v_exp_f32 v2, v2\n\t" \
  "v_mfma_f32_16x16x32_bf16 v[8:11], a[224:227], v[16:19], v[176:179]\n\t" \
  "v_exp_f32 v3, v3\n\t" \
  "v_mfma_f32_16x16x32_bf16 v[8:11], a[228:231], v[20:23], v[8:11]\n\t" \
  "ds_read_b64_tr_b16 a[138:139], v229 offset:4096\n\t" \
  "v_cvt_pk_bf16_f32 v157, v106, v107\n\t" \
  "v_mfma_f32_16x16x32_bf16 v[8:11], a[232:235], v[24:27], v[8:11]\n\t" \
  "ds_read_b64_tr_b16 a[140:141], v229 offset:8192\n\t" \
  "v_cvt_pk_bf16_f32 v158, v108, v109\n\t" \
  "v_mfma_f32_16x16x32_bf16 v[8:11], a[236:239], v[28:31], v[8:11]\n\t" \
  "v_exp_f32 v4, v4\n\t" \
  "v_mfma_f32_16x16x32_bf16 v[12:15], a[252:255], v[28:31], v[176:179]\n\t" \
  STA("v_writelane_b32 %[stv], s94, 50\n\t") \
  STA("v_writelane_b32 %[stv], s92, 51\n\t") \
  "buffer_load_dwordx4 v240, %[wo], s97 offen offset:0 lds\n\t" \
  "v_mfma_f32_16x16x32_bf16 v[12:15], a[248:251], v[24:27], v[12:15]\n\t" \
  "s_add_u32 s97, s97, 7168\n\t" \
  "ds_read_b64_tr_b16 a[142:143], v229 offset:12288\n\t" \
  "v_cvt_pk_bf16_f32 v159, v110, v111\n\t" \
  "v_mfma_f32_16x16x32_bf16 v[12:15], a[244:247], v[20:23], v[12:15]\n\t" \
  "v_exp_f32 v5, v5\n\t" \
  "v_mfma_f32_16x16x32_bf16 v[12:15], a[240:243], v[16:19], v[12:15]\n\t" \
  "v_exp_f32 v6, v6\n\t" \
  "v_mfma_f32_16x16x32_bf16 v[32:35], a[192:195], v[48:51], v[180:183]\n\t" \
  "buffer_load_dwordx4 v241, %[wo], s97 offen offset:1024 lds\n\t" \
  "v_mfma_f32_16x16x32_bf16 v[32:35], a[196:199], v[52:55], v[32:35]\n\t" \
  "s_add_u32 s97, s97, 7168\n\t" \
  "ds_read_b64_tr_b16 a[144:145], v228 offset:256\n\t" \
  "ds_read_b64_tr_b16 a[146:147], v228 offset:4352\n\t" \
  "v_mfma_f32_16x16x32_bf16 v[32:35], a[200:203], v[56:59], v[32:35]\n\t" \
  "v_exp_f32 v7, v7\n\t" \
  "v_mfma_f32_16x16x32_bf16 v[32:35], a[204:207], v[60:63], v[32:35]\n\t" \
  "v_exp_f32 v8, v8\n\t" \
  "v_mfma_f32_16x16x32_bf16 v[36:39], a[220:223], v[60:63], v[180:183]\n\t" \
  "buffer_load_dwordx4 v240, %[wo], s97 offen offset:2048 lds\n\t" \
  "v_mfma_f32_16x16x32_bf16 v[36:39], a[216:219], v[56:59], v[36:39]\n\t" \
  "s_add_u32 s97, s97, 7168\n\t" \
  "ds_read_b64_tr_b16 a[148:149], v228 offset:8448\n\t" \
  "ds_read_b64_tr_b16 a[150:151], v228 offset:12544\n\t" \
  "v_mfma_f32_16x16x32_bf16 v[36:39], a[212:215], v[52:55], v[36:39]\n\t" \
  "v_exp_f32 v9, v9\n\t" \
  "v_mfma_f32_16x16x32_bf16 v[36:39], a[208:211], v[48:51], v[36:39]\n\t" \
  "ds_read_b64_tr_b16 a[152:153], v229 offset:256\n\t" \
  "ds_read_b64_tr_b16 a[154:155], v229 offset:4352\n\t" \
  "v_mfma_f32_16x16x32_bf16 v[40:43], a[224:227], v[48:51], v[180:183]\n\t" \
  "buffer_load_dwordx4 v241, %[wo], s97 offen offset:3072 lds\n\t" \
  "v_mfma_f32_16x16x32_bf16 v[40:43], a[228:231], v[52:55], v[40:43]\n\t" \
  "v_exp_f32 v10, v10\n\t" \
  "v_mfma_f32_16x16x32_bf16 v[40:43], a[232:235], v[56:59], v[40:43]\n\t" \
  "v_exp_f32 v11, v11\n\t" \
  "v_mfma_f32_16x16x32_bf16 v[40:43], a[236:239], v[60:63], v[40:43]\n\t" \
  "ds_read_b64_tr_b16 a[156:157], v229 offset:8448\n\t" \
  "ds_read_b64_tr_b16 a[158:159], v229 offset:12544\n\t" \
  "v_mfma_f32_16x16x32_bf16 v[44:47], a[252:255], v[60:63], v[180:183]\n\t" \
  "v_exp_f32 v12, v12\n\t" \
  "v_mfma_f32_16x16x32_bf16 v[44:47], a[248:251], v[56:59], v[44:47]\n\t" \
  "v_exp_f32 v13, v13\n\t" \
  "v_mfma_f32_16x16x32_bf16 v[44:47], a[244:247], v[52:55], v[44:47]\n\t" \
  "ds_read_b64_tr_b16 a[160:161], v228 offset:512\n\t" \
  "ds_read_b64_tr_b16 a[162:163], v228 offset:4608\n\t" \
  "v_mfma_f32_16x16x32_bf16 v[44:47], a[240:243], v[48:51], v[44:47]\n\t" \
  "s_add_u32 m0, %[m], 98304\n\t" \
  "s_mov_b32 s96, 40960\n\t" \
  "buffer_load_dwordx4 v232, %[sG], s96 offen offset:2048 lds\n\t" \
  "v_mfma_f32_16x16x32_bf16 v[64:67], a[192:195], v[80:83], v[184:187]\n\t" \
  "v_exp_f32 v14, v14\n\t" \
  "v_mfma_f32_16x16x32_bf16 v[64:67], a[196:199], v[84:87], v[64:67]\n\t" \
  "v_exp_f32 v15, v15\n\t" \
  "v_mfma_f32_16x16x32_bf16 v[64:67], a[200:203], v[88:91], v[64:67]\n\t" \
  "s_mov_b32 s96, 47104\n\t" \
  "ds_read_b64_tr_b16 a[164:165], v228 offset:8704\n\t" \
  "ds_read_b64_tr_b16 a[166:167], v228 offset:12800\n\t" \
  "v_mfma_f32_16x16x32_bf16 v[64:67], a[204:207], v[92:95], v[64:67]\n\t" \
  "v_exp_f32 v32, v32\n\t" \
  "v_mfma_f32_16x16x32_bf16 v[68:71], a[220:223], v[92:95], v[184:187]\n\t" \
  "v_exp_f32 v33, v33\n\t" \
  "v_mfma_f32_16x16x32_bf16 v[68:71], a[216:219], v[88:91], v[68:71]\n\t" \
  "ds_read_b64_tr_b16 a[168:169], v229 offset:512\n\t" \
  "ds_read_b64_tr_b16 a[170:171], v229 offset:4608\n\t" \
  "v_mfma_f32_16x16x32_bf16 v[68:71], a[212:215], v[84:87], v[68:71]\n\t" \
  "v_exp_f32 v34, v34\n\t" \
  "v_mfma_f32_16x16x32_bf16 v[68:71], a[208:211], v[80:83], v[68:71]\n\t" \
  "v_exp_f32 v35, v35\n\t" \
  "v_mfma_f32_16x16x32_bf16 v[72:75], a[224:227], v[80:83], v[184:187]\n\t" \
  "v_exp_f32 v36, v36\n\t" \
  "v_mfma_f32_16x16x32_bf16 v[72:75], a[228:231], v[84:87], v[72:75]\n\t" \
  "ds_read_b64_tr_b16 a[172:173], v229 offset:8704\n\t" \
  "ds_read_b64_tr_b16 a[174:175], v229 offset:12800\n\t" \
  "v_mfma_f32_16x16x32_bf16 v[72:75], a[232:235], v[88:91], v[72:75]\n\t" \
  "v_exp_f32 v37, v37\n\t" \
  "v_mfma_f32_16x16x32_bf16 v[72:75], a[236:239], v[92:95], v[72:75]\n\t" \
  "v_exp_f32 v38, v38\n\t" \
  "v_mfma_f32_16x16x32_bf16 v[76:79], a[252:255], v[92:95], v[184:187]\n\t" \
  "ds_read_b64_tr_b16 a[176:177], v228 offset:768\n\t" \
  "ds_read_b64_tr_b16 a[178:179], v228 offset:4864\n\t" \
  "v_mfma_f32_16x16x32_bf16 v[76:79], a[248:251], v[88:91], v[76:79]\n\t" \
  "v_exp_f32 v39, v39\n\t" \
  "v_mfma_f32_16x16x32_bf16 v[76:79], a[244:247], v[84:87], v[76:79]\n\t" \
  "buffer_load_dwordx4 v232, %[sG], s96 offen offset:3072 lds\n\t" \
  "v_mfma_f32_16x16x32_bf16 v[76:79], a[240:243], v[80:83], v[76:79]\n\t" \
  "v_exp_f32 v40, v40\n\t" \
  "v_mfma_f32_16x16x32_bf16 v[96:99], a[192:195], v[112:115], v[188:191]\n\t" \
  "ds_read_b64_tr_b16 a[180:181], v228 offset:8960\n\t" \
  ".Lhs9:\n\ts_cbranch_execz .Lhs9n\n\t" \
  "ds_read_b64_tr_b16 a[182:183], v228 offset:13056\n\t" \
  "v_mfma_f32_16x16x32_bf16 v[96:99], a[196:199], v[116:119], v[96:99]\n\t" \
  "v_exp_f32 v41, v41\n\t" \
  "v_mfma_f32_16x16x32_bf16 v[96:99], a[200:203], v[120:123], v[96:99]\n\t" \
  "v_exp_f32 v42, v42\n\t" \
  "v_mfma_f32_16x16x32_bf16 v[96:99], a[204:207], v[124:127], v[96:99]\n\t" \
  "ds_read_b64_tr_b16 a[184:185], v229 offset:768\n\t" \
  "ds_read_b64_tr_b16 a[186:187], v229 offset:4864\n\t" \
  "v_mfma_f32_16x16x32_bf16 v[100:103], a[220:223], v[124:127], v[188:191]\n\t" \
  "v_exp_f32 v43, v43\n\t" \
  "v_mfma_f32_16x16x32_bf16 v[100:103], a[216:219], v[120:123], v[100:103]\n\t" \
  "v_exp_f32 v44, v44\n\t" \
  "v_mfma_f32_16x16x32_bf16 v[100:103], a[212:215], v[116:119], v[100:103]\n\t" \
  "v_exp_f32 v45, v45\n\t" \
  "v_mfma_f32_16x16x32_bf16 v[100:103], a[208:211], v[112:115], v[100:103]\n\t" \
  "ds_read_b64_tr_b16 a[188:189], v229 offset:8960\n\t" \
  "ds_read_b64_tr_b16 a[190:191], v229 offset:13056\n\t" \
  "v_mfma_f32_16x16x32_bf16 v[104:107], a[224:227], v[112:115], v[188:191]\n\t" \
  "v_exp_f32 v46, v46\n\t" \
  "v_mfma_f32_16x16x32_bf16 v[104:107], a[228:231], v[116:119], v[104:107]\n\t" \
  "v_exp_f32 v47, v47\n\t" \
  "v_mfma_f32_16x16x32_bf16 v[104:107], a[232:235], v[120:123], v[104:107]\n\t" \
  "v_exp_f32 v64, v64\n\t" \
  "v_mfma_f32_16x16x32_bf16 v[104:107], a[236:239], v[124:127], v[104:107]\n\t" \
  "v_exp_f32 v65, v65\n\t" \
  "v_mfma_f32_16x16x32_bf16 v[108:111], a[252:255], v[124:127], v[188:191]\n\t" \
  "v_exp_f32 v66, v66\n\t" \
  "v_mfma_f32_16x16x32_bf16 v[108:111], a[248:251], v[120:123], v[108:111]\n\t" \
  "v_exp_f32 v67, v67\n\t" \
  "v_mfma_f32_16x16x32_bf16 v[108:111], a[244:247], v[116:119], v[108:111]\n\t" \
  "v_exp_f32 v68, v68\n\t" \
  "v_mfma_f32_16x16x32_bf16 v[108:111], a[240:243], v[112:115], v[108:111]\n\t" \
  "s_waitcnt vmcnt(6) lgkmcnt(0)\n\t" \
  "s_barrier\n\t" \
  STA("s_memrealtime s[94:95]\n\t") \
  STA("s_memtime s[92:93]\n\t") \
  "v_mfma_f32_16x16x32_bf16 a[0:3], a[128:131], v[128:131], a[0:3]\n\t" \
  "s_add_u32 m0, %[m], 81920\n\t" \
  "v_exp_f32 v69, v69\n\t" \
  "v_mfma_f32_16x16x32_bf16 a[0:3], a[132:135], v[132:135], a[0:3]\n\t" \
  "s_xor_b32 s97, %[wob], 524288\n\t" \
  "v_exp_f32 v70, v70\n\t" \
  "v_mfma_f32_16x16x32_bf16 a[16:19], a[136:139], v[128:131], a[16:19]\n\t" \
  "ds_read_b64_tr_b16 a[192:193], v237 offset:0\n\t" \
  "ds_read_b64_tr_b16 a[194:195], v237 offset:4096\n\t" \
  "v_mfma_f32_16x16x32_bf16 a[16:19], a[140:143], v[132:135], a[16:19]\n\t" \
  "v_exp_f32 v71, v71\n\t" \
  "v_mfma_f32_16x16x32_bf16 a[32:35], a[144:147], v[128:131], a[32:35]\n\t" \
  "ds_read_b128 v[176:179], v215 offset:16384\n\t" \
  "ds_read_b64_tr_b16 a[196:197], v237 offset:8192\n\t" \
  "v_mfma_f32_16x16x32_bf16 a[32:35], a[148:151], v[132:135], a[32:35]\n\t" \
  "v_exp_f32 v72, v72\n\t" \
  "v_mfma_f32_16x16x32_bf16 a[48:51], a[152:155], v[128:131], a[48:51]\n\t" \
  "buffer_load_dwordx4 v240, %[wo], s97 offen offset:0 lds\n\t" \
  "v_mfma_f32_16x16x32_bf16 a[48:51], a[156:159], v[132:135], a[48:51]\n\t" \
  "s_add_u32 s97, s97, 7168\n\t" \
  "ds_read_b128 v[180:183], v215 offset:17408\n\t" \
  "ds_read_b64_tr_b16 a[198:199], v237 offset:12288\n\t" \
  "v_mfma_f32_16x16x32_bf16 a[64:67], a[160:163], v[128:131], a[64:67]\n\t" \
  "buffer_load_dwordx4 v241, %[wo], s97 offen offset:1024 lds\n\t" \
  "v_mfma_f32_16x16x32_bf16 a[64:67], a[164:167], v[132:135], a[64:67]\n\t" \
  "s_add_u32 s97, s97, 7168\n\t" \
  "ds_read_b64_tr_b16 a[200:201], v238 offset:0\n\t" \
  "ds_read_b128 v[184:187], v215 offset:18432\n\t" \
  "v_mfma_f32_16x16x32_bf16 a[80:83], a[168:171], v[128:131], a[80:83]\n\t" \
  "ds_read_b64_tr_b16 a[202:203], v238 offset:4096\n\t" \
  "ds_read_b128 v[188:191], v215 offset:19456\n\t" \
  "v_mfma_f32_16x16x32_bf16 a[80:83], a[172:175], v[132:135], a[80:83]\n\t" \
  "ds_read_b64_tr_b16 a[204:205], v238 offset:8192\n\t" \
  "ds_read_b128 v[192:195], v215 offset:32768\n\t" \
  "v_mfma_f32_16x16x32_bf16 a[96:99], a[176:179], v[128:131], a[96:99]\n\t" \
  STA("v_writelane_b32 %[stv], s94, 52\n\t") \
  STA("v_writelane_b32 %[stv], s92, 53\n\t") \
  "buffer_load_dwordx4 v240, %[wo], s97 offen offset:2048 lds\n\t" \
  "v_mfma_f32_16x16x32_bf16 a[96:99], a[180:183], v[132:135], a[96:99]\n\t" \
  "s_add_u32 s97, s97, 7168\n\t" \
  "ds_read_b64_tr_b16 a[206:207], v238 offset:12288\n\t" \
  "ds_read_b128 v[196:199], v215 offset:33792\n\t" \
  "v_mfma_f32_16x16x32_bf16 a[112:115], a[184:187], v[128:131], a[112:115]\n\t" \
  "ds_read_b64_tr_b16 a[208:209], v237 offset:256\n\t" \
  "ds_read_b128 v[200:203], v215 offset:34816\n\t" \
  "v_mfma_f32_16x16x32_bf16 a[112:115], a[188:191], v[132:135], a[112:115]\n\t" \
  "ds_read_b64_tr_b16 a[210:211], v237 offset:4352\n\t" \
  "ds_read_b64_tr_b16 a[212:213], v237 offset:8448\n\t" \
  "v_mfma_f32_16x16x32_bf16 v[160:163], v[216:219], v[128:131], v[160:163]\n\t" \
  "buffer_load_dwordx4 v241, %[wo], s97 offen offset:3072 lds\n\t" \
  "v_mfma_f32_16x16x32_bf16 v[160:163], v[216:219], v[132:135], v[160:163]\n\t" \
  "ds_read_b128 v[204:207], v215 offset:35840\n\t" \
  "v_cvt_pk_bf16_f32 v128, v0, v1\n\t" \
  "v_mfma_f32_16x16x32_bf16 a[4:7], a[128:131], v[136:139], a[4:7]\n\t" \
  "ds_read_b64_tr_b16 a[214:215], v237 offset:12544\n\t" \
  "v_cvt_pk_bf16_f32 v129, v2, v3\n\t" \
  "v_mfma_f32_16x16x32_bf16 a[4:7], a[132:135], v[140:143], a[4:7]\n\t" \
  "ds_read_b64_tr_b16 a[216:217], v238 offset:256\n\t" \
  "v_cvt_pk_bf16_f32 v130, v4, v5\n\t" \
  "v_mfma_f32_16x16x32_bf16 a[20:23], a[136:139], v[136:139], a[20:23]\n\t" \
  "s_add_u32 m0, %[m], 114688\n\t" \
  "ds_read_b64_tr_b16 a[218:219], v238 offset:4352\n\t" \
  "v_cvt_pk_bf16_f32 v131, v6, v7\n\t" \
  "v_mfma_f32_16x16x32_bf16 a[20:23], a[140:143], v[140:143], a[20:23]\n\t" \
  "s_xor_b32 s97, %[wob], 786432\n\t" \
  "ds_read_b64_tr_b16 a[220:221], v238 offset:8448\n\t" \
  "v_cvt_pk_bf16_f32 v132, v8, v9\n\t" \
  "v_mfma_f32_16x16x32_bf16 a[36:39], a[144:147], v[136:139], a[36:39]\n\t" \
  "s_waitcnt lgkmcnt(13)\n\t" \
  "ds_read_b64_tr_b16 a[222:223], v238 offset:12544\n\t" \
  "v_cvt_pk_bf16_f32 v133, v10, v11\n\t" \
  "v_mfma_f32_16x16x32_bf16 a[36:39], a[148:151], v[140:143], a[36:39]\n\t" \
  "ds_read_b64_tr_b16 a[224:225], v237 offset:512\n\t" \
  "v_cvt_pk_bf16_f32 v134, v12, v13\n\t" \
  "v_mfma_f32_16x16x32_bf16 a[52:55], a[152:155], v[136:139], a[52:55]\n\t" \
  "ds_read_b64_tr_b16 a[226:227], v237 offset:4608\n\t" \
  "v_cvt_pk_bf16_f32 v135, v14, v15\n\t" \
  "v_mfma_f32_16x16x32_bf16 a[52:55], a[156:159], v[140:143], a[52:55]\n\t" \
  "ds_read_b64_tr_b16 a[228:229], v237 offset:8704\n\t" \
  "ds_read_b64_tr_b16 a[230:231], v237 offset:12800\n\t" \
  "v_mfma_f32_16x16x32_bf16 a[68:71], a[160:163], v[136:139], a[68:71]\n\t" \
  "v_exp_f32 v73, v73\n\t" \
  "v_mfma_f32_16x16x32_bf16 a[68:71], a[164:167], v[140:143], a[68:71]\n\t" \
  "ds_read_b64_tr_b16 a[232:233], v238 offset:512\n\t" \
  "ds_read_b64_tr_b16 a[234:235], v238 offset:4608\n\t" \
  "v_mfma_f32_16x16x32_bf16 a[84:87], a[168:171], v[136:139], a[84:87]\n\t" \
  "buffer_load_dwordx4 v240, %[wo], s97 offen offset:0 lds\n\t" \
  "v_mfma_f32_16x16x32_bf16 a[84:87], a[172:175], v[140:143], a[84:87]\n\t" \
  "s_add_u32 s97, s97, 7168\n\t" \
  "v_exp_f32 v74, v74\n\t" \
  "v_mfma_f32_16x16x32_bf16 a[100:103], a[176:179], v[136:139], a[100:103]\n\t" \
  "ds_read_b64_tr_b16 a[236:237], v238 offset:8704\n\t" \
  "ds_read_b64_tr_b16 a[238:239], v238 offset:12800\n\t" \
  "v_mfma_f32_16x16x32_bf16 a[100:103], a[180:183], v[140:143], a[100:103]\n\t" \
  "buffer_load_dwordx4 v241, %[wo], s97 offen offset:1024 lds\n\t" \
  "v_mfma_f32_16x16x32_bf16 a[116:119], a[184:187], v[136:139], a[116:119]\n\t" \
  "s_add_u32 s97, s97, 7168\n\t" \
  "ds_read_b64_tr_b16 a[240:241], v237 offset:768\n\t" \
  "ds_read_b64_tr_b16 a[242:243], v237 offset:4864\n\t" \
  "v_mfma_f32_16x16x32_bf16 a[116:119], a[188:191], v[140:143], a[116:119]\n\t" \
  "v_exp_f32 v75, v75\n\t" \
  "v_mfma_f32_16x16x32_bf16 v[164:167], v[216:219], v[136:139], v[164:167]\n\t" \
  "buffer_load_dwordx4 v240, %[wo], s97 offen offset:2048 lds\n\t" \
  "v_mfma_f32_16x16x32_bf16 v[164:167], v[216:219], v[140:143], v[164:167]\n\t" \
  "s_add_u32 s97, s97, 7168\n\t" \
  "ds_read_b64_tr_b16 a[244:245], v237 offset:8960\n\t" \
  "v_cvt_pk_bf16_f32 v136, v32, v33\n\t" \
  "v_mfma_f32_16x16x32_bf16 a[0:3], a[192:195], v[128:131], a[0:3]\n\t" \
  "ds_read_b64_tr_b16 a[246:247], v237 offset:13056\n\t" \
  "v_cvt_pk_bf16_f32 v137, v34, v35\n\t" \
  "v_mfma_f32_16x16x32_bf16 a[0:3], a[196:199], v[132:135], a[0:3]\n\t" \
  "ds_read_b64_tr_b16 a[248:249], v238 offset:768\n\t" \
  "v_cvt_pk_bf16_f32 v138, v36, v37\n\t" \
  "v_mfma_f32_16x16x32_bf16 v[160:163], v[216:219], v[128:131], v[160:163]\n\t" \
  "buffer_load_dwordx4 v241, %[wo], s97 offen offset:3072 lds\n\t" \
  "v_mfma_f32_16x16x32_bf16 v[160:163], v[216:219], v[132:135], v[160:163]\n\t" \
  "s_add_u32 m0, %[m], 131072\n\t" \
  "ds_read_b64_tr_b16 a[250:251], v238 offset:4864\n\t" \
  "v_cvt_pk_bf16_f32 v139, v38, v39\n\t" \
  "v_mfma_f32_16x16x32_bf16 a[16:19], a[200:203], v[128:131], a[16:19]\n\t" \
  "s_waitcnt lgkmcnt(15)\n\t" \
  "ds_read_b64_tr_b16 a[252:253], v238 offset:8960\n\t" \
  "v_cvt_pk_bf16_f32 v140, v40, v41\n\t" \
  "v_mfma_f32_16x16x32_bf16 a[16:19], a[204:207], v[132:135], a[16:19]\n\t" \
  "s_xor_b32 s97, %[wob], 917504\n\t" \
  "ds_read_b64_tr_b16 a[254:255], v238 offset:13056\n\t" \
  "v_cvt_pk_bf16_f32 v141, v42, v43\n\t" \
  "v_xor_b32 v235, 64, v234\n\t" \
  "v_mfma_f32_16x16x32_bf16 a[32:35], a[208:211], v[128:131], a[32:35]\n\t" \
  "v_exp_f32 v76, v76\n\t" \
  "v_cvt_pk_bf16_f32 v142, v44, v45\n\t" \
  "v_mfma_f32_16x16x32_bf16 a[32:35], a[212:215], v[132:135], a[32:35]\n\t" \
  "ds_read_b64_tr_b16 v[48:49], v235 offset:0\n\t" \
  "v_cvt_pk_bf16_f32 v143, v46, v47\n\t" \
  "v_mfma_f32_16x16x32_bf16 a[48:51], a[216:219], v[128:131], a[48:51]\n\t" \
  "s_waitcnt lgkmcnt(15)\n\t" \
  "v_exp_f32 v77, v77\n\t" \
  "v_mfma_f32_16x16x32_bf16 a[48:51], a[220:223], v[132:135], a[48:51]\n\t" \
  "ds_read_b64_tr_b16 v[50:51], v235 offset:4096\n\t" \
  "ds_read_b64_tr_b16 v[52:53], v235 offset:8192\n\t" \
  "v_mfma_f32_16x16x32_bf16 a[64:67], a[224:227], v[128:131], a[64:67]\n\t" \
  "s_waitcnt lgkmcnt(15)\n\t" \
  "buffer_load_dwordx4 v240, %[wo], s97 offen offset:0 lds\n\t" \
  "v_mfma_f32_16x16x32_bf16 a[64:67], a[228:231], v[132:135], a[64:67]\n\t" \
  "s_waitcnt lgkmcnt(13)\n\t" \
  "v_rcp_f32 v208, v160\n\t" \
  "v_mfma_f32_16x16x32_bf16 a[80:83], a[232:235], v[128:131], a[80:83]\n\t" \
  "s_waitcnt lgkmcnt(11)\n\t" \
  "v_cmp_lt_f32 vcc, 0x5f800000, v160\n\t" \
  "s_cbranch_vccnz .Lat_x0\n\t" \
  ".Lat_y0:\n\t" \
  "v_accvgpr_read_b32 v16, a0\n\t" \
  "v_mfma_f32_16x16x32_bf16 a[80:83], a[236:239], v[132:135], a[80:83]\n\t" \
  "s_waitcnt lgkmcnt(9)\n\t" \
  "ds_read_b64_tr_b16 v[54:55], v235 offset:12288\n\t" \
  "v_accvgpr_read_b32 v17, a1\n\t" \
  "v_accvgpr_read_b32 v18, a2\n\t" \
  "v_mfma_f32_16x16x32_bf16 a[96:99], a[240:243], v[128:131], a[96:99]\n\t" \
  "s_waitcnt lgkmcnt(8)\n\t" \
  "v_accvgpr_read_b32 v19, a3\n\t" \
  "v_accvgpr_read_b32 v20, a16\n\t" \
  "v_accvgpr_read_b32 v21, a17\n\t" \
  "v_accvgpr_read_b32 v22, a18\n\t" \
  "v_accvgpr_read_b32 v23, a19\n\t" \
  "v_pk_mul_f32 v[16:17], v[16:17], v[208:209] op_sel_hi:[1,0]\n\t" \
  "v_pk_mul_f32 v[18:19], v[18:19], v[208:209] op_sel_hi:[1,0]\n\t" \
  "v_pk_mul_f32 v[20:21], v[20:21], v[208:209] op_sel_hi:[1,0]\n\t" \
  "v_pk_mul_f32 v[22:23], v[22:23], v[208:209] op_sel_hi:[1,0]\n\t" \
  "v_cvt_pk_bf16_f32 v0, v16, v17\n\t" \
  "v_mfma_f32_16x16x32_bf16 a[96:99], a[244:247], v[132:135], a[96:99]\n\t" \
  "s_waitcnt lgkmcnt(6)\n\t" \
  "ds_read_b64_tr_b16 v[56:57], v235 offset:16384\n\t" \
  "v_cvt_pk_bf16_f32 v1, v18, v19\n\t" \
  "v_cvt_pk_bf16_f32 v2, v20, v21\n\t" \
  "v_cvt_pk_bf16_f32 v3, v22, v23\n\t" \
  "v_mfma_f32_16x16x32_bf16 a[112:115], a[248:251], v[128:131], a[112:115]\n\t" \
  "s_waitcnt lgkmcnt(5)\n\t" \
  "ds_read_b64_tr_b16 v[58:59], v235 offset:20480\n\t" \
  "v_max3_f32 v209, |v0|, |v1|, |v2|\n\t" \
  "v_mfma_f32_16x16x32_bf16 a[112:115], a[252:255], v[132:135], a[112:115]\n\t" \
  "s_waitcnt vmcnt(11)\n\t" \
  "s_barrier\n\t" \
  "v_mfma_f32_16x16x32_bf16 a[8:11], a[128:131], v[144:147], a[8:11]\n\t" \
  "s_add_u32 s97, s97, 7168\n\t" \
  "buffer_load_dwordx4 v241, %[wo], s97 offen offset:1024 lds\n\t" \
  "v_mfma_f32_16x16x32_bf16 a[8:11], a[132:135], v[148:151], a[8:11]\n\t" \
  "s_add_u32 s97, s97, 7168\n\t" \
  "v_cmp_lt_f32 vcc, 0x33800000, v209\n\t" \
  "s_cbranch_vccz .Lat_x4\n\t" \
  ".Lat_y4:\n\t" \
  "v_accvgpr_read_b32 v24, a32\n\t" \
  "v_mfma_f32_16x16x32_bf16 a[24:27], a[136:139], v[144:147], a[24:27]\n\t" \
  "buffer_load_dwordx4 v240, %[wo], s97 offen offset:2048 lds\n\t" \
  "v_accvgpr_read_b32 v25, a33\n\t" \
  "v_accvgpr_read_b32 v26, a34\n\t" \
  "v_mfma_f32_16x16x32_bf16 a[24:27], a[140:143], v[148:151], a[24:27]\n\t" \
  "s_add_u32 s97, s97, 7168\n\t" \
  "v_accvgpr_read_b32 v27, a35\n\t" \
  "v_accvgpr_read_b32 v28, a48\n\t" \
  "v_accvgpr_read_b32 v29, a49\n\t" \
  "v_accvgpr_read_b32 v30, a50\n\t" \
  "v_accvgpr_read_b32 v31, a51\n\t" \
  "v_pk_mul_f32 v[24:25], v[24:25], v[208:209] op_sel_hi:[1,0]\n\t" \
  "v_pk_mul_f32 v[26:27], v[26:27], v[208:209] op_sel_hi:[1,0]\n\t" \
  "v_pk_mul_f32 v[28:29], v[28:29], v[208:209] op_sel_hi:[1,0]\n\t" \
  "v_pk_mul_f32 v[30:31], v[30:31], v[208:209] op_sel_hi:[1,0]\n\t" \
  "v_cvt_pk_bf16_f32 v4, v24, v25\n\t" \
  "v_mfma_f32_16x16x32_bf16 a[40:43], a[144:147], v[144:147], a[40:43]\n\t" \
  "s_xor_b32 s98, %[eso], 0\n\t" \
  "ds_read_b64_tr_b16 v[60:61], v235 offset:24576\n\t" \
  "v_cvt_pk_bf16_f32 v5, v26, v27\n\t" \
  "v_cvt_pk_bf16_f32 v6, v28, v29\n\t" \
  "v_cvt_pk_bf16_f32 v7, v30, v31\n\t" \
  "v_mfma_f32_16x16x32_bf16 a[40:43], a[148:151], v[148:151], a[40:43]\n\t" \
  "buffer_load_dwordx4 v241, %[wo], s97 offen offset:3072 lds\n\t" \
  "v_accvgpr_read_b32 v16, a64\n\t" \
  "v_accvgpr_read_b32 v17, a65\n\t" \
  "v_accvgpr_read_b32 v18, a66\n\t" \
  "v_mfma_f32_16x16x32_bf16 a[56:59], a[152:155], v[144:147], a[56:59]\n\t" \
  "v_exp_f32 v78, v78\n\t" \
  "v_mfma_f32_16x16x32_bf16 a[56:59], a[156:159], v[148:151], a[56:59]\n\t" \
  "buffer_store_dwordx4 v[0:3], v242, %[ws], s98 offen offset:0 sc1\n\t" \
  "v_mfma_f32_16x16x32_bf16 a[72:75], a[160:163], v[144:147], a[72:75]\n\t" \
  "v_accvgpr_read_b32 v19, a67\n\t" \
  "v_accvgpr_read_b32 v20, a80\n\t" \
  "v_accvgpr_read_b32 v21, a81\n\t" \
  "v_accvgpr_read_b32 v22, a82\n\t" \
  "v_accvgpr_read_b32 v23, a83\n\t" \
  "v_pk_mul_f32 v[16:17], v[16:17], v[208:209] op_sel_hi:[1,0]\n\t" \
  "v_pk_mul_f32 v[18:19], v[18:19], v[208:209] op_sel_hi:[1,0]\n\t" \
  "v_pk_mul_f32 v[20:21], v[20:21], v[208:209] op_sel_hi:[1,0]\n\t" \
  "v_pk_mul_f32 v[22:23], v[22:23], v[208:209] op_sel_hi:[1,0]\n\t" \
  "v_cvt_pk_bf16_f32 v8, v16, v17\n\t" \
  "v_mfma_f32_16x16x32_bf16 a[72:75], a[164:167], v[148:151], a[72:75]\n\t" \
  "s_xor_b32 s98, %[eso], 0\n\t" \
  "ds_read_b64_tr_b16 v[62:63], v235 offset:28672\n\t" \
  "v_xor_b32 v236, 96, v234\n\t" \
  "v_mfma_f32_16x16x32_bf16 a[88:91], a[168:171], v[144:147], a[88:91]\n\t" \
  "buffer_store_dwordx4 v[4:7], v242, %[ws], s98 offen offset:1024 sc1\n\t" \
  "v_mfma_f32_16x16x32_bf16 a[88:91], a[172:175], v[148:151], a[88:91]\n\t" \
  "ds_read_b64_tr_b16 v[80:81], v236 offset:0\n\t" \
  "v_cvt_pk_bf16_f32 v9, v18, v19\n\t" \
  "v_cvt_pk_bf16_f32 v10, v20, v21\n\t" \
  "v_cvt_pk_bf16_f32 v11, v22, v23\n\t" \
  "v_mfma_f32_16x16x32_bf16 a[104:107], a[176:179], v[144:147], a[104:107]\n\t" \
  "ds_read_b64_tr_b16 v[82:83], v236 offset:4096\n\t" \
  "v_accvgpr_read_b32 v24, a96\n\t" \
  "v_mfma_f32_16x16x32_bf16 a[104:107], a[180:183], v[148:151], a[104:107]\n\t" \
  "ds_read_b64_tr_b16 v[84:85], v236 offset:8192\n\t" \
  "v_accvgpr_read_b32 v25, a97\n\t" \
  "v_accvgpr_read_b32 v26, a98\n\t" \
  "v_mfma_f32_16x16x32_bf16 a[120:123], a[184:187], v[144:147], a[120:123]\n\t" \
  "v_exp_f32 v79, v79\n\t" \
  "v_mfma_f32_16x16x32_bf16 a[120:123], a[188:191], v[148:151], a[120:123]\n\t" \
  "v_accvgpr_read_b32 v27, a99\n\t" \
  "v_accvgpr_read_b32 v28, a112\n\t" \
  "v_accvgpr_read_b32 v29, a113\n\t" \
  "v_accvgpr_read_b32 v30, a114\n\t" \
  "v_accvgpr_read_b32 v31, a115\n\t" \
  "v_pk_mul_f32 v[24:25], v[24:25], v[208:209] op_sel_hi:[1,0]\n\t" \
  "v_pk_mul_f32 v[26:27], v[26:27], v[208:209] op_sel_hi:[1,0]\n\t" \
  "v_pk_mul_f32 v[28:29], v[28:29], v[208:209] op_sel_hi:[1,0]\n\t" \
  "v_pk_mul_f32 v[30:31], v[30:31], v[208:209] op_sel_hi:[1,0]\n\t" \
  "v_cvt_pk_bf16_f32 v12, v24, v25\n\t" \
  "v_mfma_f32_16x16x32_bf16 v[168:171], v[216:219], v[144:147], v[168:171]\n\t" \
  "v_exp_f32 v96, v96\n\t" \
  "v_mfma_f32_16x16x32_bf16 v[168:171], v[216:219], v[148:151], v[168:171]\n\t" \
  "s_xor_b32 s98, %[eso], 0\n\t" \
  "v_exp_f32 v97, v97\n\t" \
  STS("s_memrealtime s[94:95]\n\t") \
  STS("s_memtime s[92:93]\n\t") \
  "v_mfma_f32_16x16x32_bf16 a[4:7], a[192:195], v[136:139], a[4:7]\n\t" \
  "buffer_store_dwordx4 v[8:11], v242, %[ws], s98 offen offset:2048 sc1\n\t" \
  "v_cvt_pk_bf16_f32 v144, v64, v65\n\t" \
  "v_mfma_f32_16x16x32_bf16 a[4:7], a[196:199], v[140:143], a[4:7]\n\t" \
  "ds_read_b64_tr_b16 v[86:87], v236 offset:12288\n\t" \
  "v_cvt_pk_bf16_f32 v13, v26, v27\n\t" \
  "v_cvt_pk_bf16_f32 v14, v28, v29\n\t" \
  "v_cvt_pk_bf16_f32 v15, v30, v31\n\t" \
  "v_cvt_pk_bf16_f32 v145, v66, v67\n\t" \
  "v_mfma_f32_16x16x32_bf16 v[164:167], v[216:219], v[136:139], v[164:167]\n\t" \
  "v_exp_f32 v98, v98\n\t" \
  "v_cvt_pk_bf16_f32 v146, v68, v69\n\t" \
  "v_mfma_f32_16x16x32_bf16 v[164:167], v[216:219], v[140:143], v[164:167]\n\t" \
  "ds_read_b64_tr_b16 v[88:89], v236 offset:16384\n\t" \
  "v_cvt_pk_bf16_f32 v147, v70, v71\n\t" \
  "v_mfma_f32_16x16x32_bf16 a[20:23], a[200:203], v[136:139], a[20:23]\n\t" \
  "v_exp_f32 v99, v99\n\t" \
  "v_cvt_pk_bf16_f32 v148, v72, v73\n\t" \
  "v_mfma_f32_16x16x32_bf16 a[20:23], a[204:207], v[140:143], a[20:23]\n\t" \
  "v_exp_f32 v100, v100\n\t" \
  "v_cvt_pk_bf16_f32 v149, v74, v75\n\t" \
  "v_mfma_f32_16x16x32_bf16 a[36:39], a[208:211], v[136:139], a[36:39]\n\t" \
  "ds_read_b64_tr_b16 v[90:91], v236 offset:20480\n\t" \
  "v_cvt_pk_bf16_f32 v150, v76, v77\n\t" \
  "v_mfma_f32_16x16x32_bf16 a[36:39], a[212:215], v[140:143], a[36:39]\n\t" \
  "s_xor_b32 s98, %[eso], 0\n\t" \
  "v_exp_f32 v101, v101\n\t" \
  "v_cvt_pk_bf16_f32 v151, v78, v79\n\t" \
  "v_mfma_f32_16x16x32_bf16 a[52:55], a[216:219], v[136:139], a[52:55]\n\t" \
  "buffer_store_dwordx4 v[12:15], v242, %[ws], s98 offen offset:3072 sc1\n\t" \
  "v_mfma_f32_16x16x32_bf16 a[52:55], a[220:223], v[140:143], a[52:55]\n\t" \
  "ds_read_b64_tr_b16 v[92:93], v236 offset:24576\n\t" \
  "ds_read_b64_tr_b16 v[94:95], v236 offset:28672\n\t" \
  "v_mfma_f32_16x16x32_bf16 a[68:71], a[224:227], v[136:139], a[68:71]\n\t" \
  "v_rcp_f32 v210, v164\n\t" \
  "v_mfma_f32_16x16x32_bf16 a[68:71], a[228:231], v[140:143], a[68:71]\n\t" \
  "v_cmp_lt_f32 vcc, 0x5f800000, v164\n\t" \
  "s_cbranch_vccnz .Lat_x1\n\t" \
  ".Lat_y1:\n\t" \
  "v_accvgpr_read_b32 v16, a4\n\t" \
  "v_mfma_f32_16x16x32_bf16 a[84:87], a[232:235], v[136:139], a[84:87]\n\t" \
  STS("v_writelane_b32 %[stv], s94, 20\n\t") \
  STS("v_writelane_b32 %[stv], s92, 21\n\t") \
  "v_accvgpr_read_b32 v17, a5\n\t" \
  "v_accvgpr_read_b32 v18, a6\n\t" \
  "v_mfma_f32_16x16x32_bf16 a[84:87], a[236:239], v[140:143], a[84:87]\n\t" \
  "v_accvgpr_read_b32 v19, a7\n\t" \
  "v_accvgpr_read_b32 v20, a20\n\t" \
  "v_accvgpr_read_b32 v21, a21\n\t" \
  "v_accvgpr_read_b32 v22, a22\n\t" \
  "v_accvgpr_read_b32 v23, a23\n\t" \
  "v_pk_mul_f32 v[16:17], v[16:17], v[210:211] op_sel_hi:[1,0]\n\t" \
  "v_pk_mul_f32 v[18:19], v[18:19], v[210:211] op_sel_hi:[1,0]\n\t" \
  "v_pk_mul_f32 v[20:21], v[20:21], v[210:211] op_sel_hi:[1,0]\n\t" \
  "v_pk_mul_f32 v[22:23], v[22:23], v[210:211] op_sel_hi:[1,0]\n\t" \
  "v_cvt_pk_bf16_f32 v32, v16, v17\n\t" \
  "v_mfma_f32_16x16x32_bf16 a[100:103], a[240:243], v[136:139], a[100:103]\n\t" \
  "v_xor_b32 v235, 128, v234\n\t" \
  "v_cvt_pk_bf16_f32 v33, v18, v19\n\t" \
  "v_cvt_pk_bf16_f32 v34, v20, v21\n\t" \
  "v_cvt_pk_bf16_f32 v35, v22, v23\n\t" \
  "v_mfma_f32_16x16x32_bf16 a[100:103], a[244:247], v[140:143], a[100:103]\n\t" \
  "s_xor_b32 s98, %[eso], 32768\n\t" \
  "ds_read_b64_tr_b16 v[112:113], v235 offset:0\n\t" \
  "v_accvgpr_read_b32 v24, a36\n\t" \
  "v_mfma_f32_16x16x32_bf16 a[116:119], a[248:251], v[136:139], a[116:119]\n\t" \
  "buffer_store_dwordx4 v[32:35], v242, %[ws], s98 offen offset:0 sc1\n\t" \
  "v_accvgpr_read_b32 v25, a37\n\t" \
  "v_accvgpr_read_b32 v26, a38\n\t" \
  "v_mfma_f32_16x16x32_bf16 a[116:119], a[252:255], v[140:143], a[116:119]\n\t" \
  "v_exp_f32 v102, v102\n\t" \
  "v_mfma_f32_16x16x32_bf16 a[12:15], a[128:131], v[152:155], a[12:15]\n\t" \
  "v_accvgpr_read_b32 v27, a39\n\t" \
  "v_accvgpr_read_b32 v28, a52\n\t" \
  "v_accvgpr_read_b32 v29, a53\n\t" \
  "v_accvgpr_read_b32 v30, a54\n\t" \
  "v_accvgpr_read_b32 v31, a55\n\t" \
  "v_pk_mul_f32 v[24:25], v[24:25], v[210:211] op_sel_hi:[1,0]\n\t" \
  "v_pk_mul_f32 v[26:27], v[26:27], v[210:211] op_sel_hi:[1,0]\n\t" \
  "v_pk_mul_f32 v[28:29], v[28:29], v[210:211] op_sel_hi:[1,0]\n\t" \
  "v_pk_mul_f32 v[30:31], v[30:31], v[210:211] op_sel_hi:[1,0]\n\t" \
  "v_cvt_pk_bf16_f32 v36, v24, v25\n\t" \
  "v_mfma_f32_16x16x32_bf16 a[12:15], a[132:135], v[156:159], a[12:15]\n\t" \
  "ds_read_b64_tr_b16 v[114:115], v235 offset:4096\n\t" \
  "v_cvt_pk_bf16_f32 v37, v26, v27\n\t" \
  "v_mfma_f32_16x16x32_bf16 a[28:31], a[136:139], v[152:155], a[28:31]\n\t" \
  "v_exp_f32 v103, v103\n\t" \
  "v_mfma_f32_16x16x32_bf16 a[28:31], a[140:143], v[156:159], a[28:31]\n\t" \
  "ds_read_b64_tr_b16 v[116:117], v235 offset:8192\n\t" \
  "v_cvt_pk_bf16_f32 v38, v28, v29\n\t" \
  "v_mfma_f32_16x16x32_bf16 a[44:47], a[144:147], v[152:155], a[44:47]\n\t" \
  "ds_read_b64_tr_b16 v[118:119], v235 offset:12288\n\t" \
  "v_cvt_pk_bf16_f32 v39, v30, v31\n\t" \
  "v_mfma_f32_16x16x32_bf16 a[44:47], a[148:151], v[156:159], a[44:47]\n\t" \
  "s_xor_b32 s98, %[eso], 32768\n\t" \
  "ds_read_b64_tr_b16 v[120:121], v235 offset:16384\n\t" \
  "v_accvgpr_read_b32 v16, a68\n\t" \
  "v_accvgpr_read_b32 v17, a69\n\t" \
  "v_accvgpr_read_b32 v18, a70\n\t" \
  "v_mfma_f32_16x16x32_bf16 a[60:63], a[152:155], v[152:155], a[60:63]\n\t" \
  "buffer_store_dwordx4 v[36:39], v242, %[ws], s98 offen offset:1024 sc1\n\t" \
  "v_mfma_f32_16x16x32_bf16 a[60:63], a[156:159], v[156:159], a[60:63]\n\t" \
  "v_accvgpr_read_b32 v19, a71\n\t" \
  "v_accvgpr_read_b32 v20, a84\n\t" \
  "v_accvgpr_read_b32 v21, a85\n\t" \
  "v_accvgpr_read_b32 v22, a86\n\t" \
  "v_accvgpr_read_b32 v23, a87\n\t" \
  "v_pk_mul_f32 v[16:17], v[16:17], v[210:211] op_sel_hi:[1,0]\n\t" \
  "v_pk_mul_f32 v[18:19], v[18:19], v[210:211] op_sel_hi:[1,0]\n\t" \
  "v_pk_mul_f32 v[20:21], v[20:21], v[210:211] op_sel_hi:[1,0]\n\t" \
  "v_pk_mul_f32 v[22:23], v[22:23], v[210:211] op_sel_hi:[1,0]\n\t" \
  "v_cvt_pk_bf16_f32 v40, v16, v17\n\t" \
  "v_mfma_f32_16x16x32_bf16 a[76:79], a[160:163], v[152:155], a[76:79]\n\t" \
  "v_exp_f32 v104, v104\n\t" \
  "v_mfma_f32_16x16x32_bf16 a[76:79], a[164:167], v[156:159], a[76:79]\n\t" \
  "v_exp_f32 v105, v105\n\t" \
  "v_mfma_f32_16x16x32_bf16 a[92:95], a[168:171], v[152:155], a[92:95]\n\t" \
  "ds_read_b64_tr_b16 v[122:123], v235 offset:20480\n\t" \
  "v_cvt_pk_bf16_f32 v41, v18, v19\n\t" \
  "v_mfma_f32_16x16x32_bf16 a[92:95], a[172:175], v[156:159], a[92:95]\n\t" \
  "ds_read_b64_tr_b16 v[124:125], v235 offset:24576\n\t" \
  "v_cvt_pk_bf16_f32 v42, v20, v21\n\t" \
  "v_cvt_pk_bf16_f32 v43, v22, v23\n\t" \
  "v_mfma_f32_16x16x32_bf16 a[108:111], a[176:179], v[152:155], a[108:111]\n\t" \
  "ds_read_b64_tr_b16 v[126:127], v235 offset:28672\n\t" \
  "v_accvgpr_read_b32 v24, a100\n\t" \
  "v_mfma_f32_16x16x32_bf16 a[108:111], a[180:183], v[156:159], a[108:111]\n\t" \
  "s_xor_b32 s98, %[eso], 32768\n\t" \
  "v_accvgpr_read_b32 v25, a101\n\t" \
  "v_accvgpr_read_b32 v26, a102\n\t" \
  "v_mfma_f32_16x16x32_bf16 a[124:127], a[184:187], v[152:155], a[124:127]\n\t" \
  "buffer_store_dwordx4 v[40:43], v242, %[ws], s98 offen offset:2048 sc1\n\t" \
  "v_mfma_f32_16x16x32_bf16 a[124:127], a[188:191], v[156:159], a[124:127]\n\t" \
  "v_accvgpr_read_b32 v27, a103\n\t" \
  "v_accvgpr_read_b32 v28, a116\n\t" \
  "v_accvgpr_read_b32 v29, a117\n\t" \
  "v_accvgpr_read_b32 v30, a118\n\t" \
  "v_accvgpr_read_b32 v31, a119\n\t" \
  "v_pk_mul_f32 v[24:25], v[24:25], v[210:211] op_sel_hi:[1,0]\n\t" \
  "v_pk_mul_f32 v[26:27], v[26:27], v[210:211] op_sel_hi:[1,0]\n\t" \
  "v_pk_mul_f32 v[28:29], v[28:29], v[210:211] op_sel_hi:[1,0]\n\t" \
  "v_pk_mul_f32 v[30:31], v[30:31], v[210:211] op_sel_hi:[1,0]\n\t" \
  "v_cvt_pk_bf16_f32 v44, v24, v25\n\t" \
  "v_mfma_f32_16x16x32_bf16 v[172:175], v[216:219], v[152:155], v[172:175]\n\t" \
  "v_exp_f32 v106, v106\n\t" \
  "v_mfma_f32_16x16x32_bf16 v[172:175], v[216:219], v[156:159], v[172:175]\n\t" \
  "ds_read_b64_tr_b16 a[128:129], v234 offset:0\n\t" \
  "v_cvt_pk_bf16_f32 v152, v96, v97\n\t" \
  "v_mfma_f32_16x16x32_bf16 a[8:11], a[192:195], v[144:147], a[8:11]\n\t" \
  "ds_read_b64_tr_b16 a[130:131], v234 offset:4096\n\t" \
  "v_cvt_pk_bf16_f32 v153, v98, v99\n\t" \
  "v_mfma_f32_16x16x32_bf16 a[8:11], a[196:199], v[148:151], a[8:11]\n\t" \
  "ds_read_b64_tr_b16 a[132:133], v234 offset:8192\n\t" \
  "v_cvt_pk_bf16_f32 v45, v26, v27\n\t" \
  "v_cvt_pk_bf16_f32 v46, v28, v29\n\t" \
  "v_cvt_pk_bf16_f32 v47, v30, v31\n\t" \
  "v_cvt_pk_bf16_f32 v154, v100, v101\n\t" \
  "v_mfma_f32_16x16x32_bf16 v[168:171], v[216:219], v[144:147], v[168:171]\n\t" \
  "v_exp_f32 v107, v107\n\t" \
  "v_cvt_pk_bf16_f32 v155, v102, v103\n\t" \
  "v_mfma_f32_16x16x32_bf16 v[168:171], v[216:219], v[148:151], v[168:171]\n\t" \
  "s_xor_b32 s98, %[eso], 32768\n\t" \
  "ds_read_b64_tr_b16 a[134:135], v234 offset:12288\n\t" \
  "v_cvt_pk_bf16_f32 v156, v104, v105\n\t" \
  "v_mfma_f32_16x16x32_bf16 a[24:27], a[200:203], v[144:147], a[24:27]\n\t" \
  "buffer_store_dwordx4 v[44:47], v242, %[ws], s98 offen offset:3072 sc1\n\t" \
  "v_cvt_pk_bf16_f32 v157, v106, v107\n\t" \
  "v_mfma_f32_16x16x32_bf16 a[24:27], a[204:207], v[148:151], a[24:27]\n\t" \
  "v_exp_f32 v108, v108\n\t" \
  "v_mfma_f32_16x16x32_bf16 a[40:43], a[208:211], v[144:147], a[40:43]\n\t" \
  "ds_read_b64_tr_b16 a[136:137], v234 offset:16384\n\t" \
  "ds_read_b64_tr_b16 a[138:139], v234 offset:20480\n\t" \
  "v_mfma_f32_16x16x32_bf16 a[40:43], a[212:215], v[148:151], a[40:43]\n\t" \
  "v_exp_f32 v109, v109\n\t" \
  "v_mfma_f32_16x16x32_bf16 a[56:59], a[216:219], v[144:147], a[56:59]\n\t" \
  "ds_read_b64_tr_b16 a[140:141], v234 offset:24576\n\t" \
  "v_cvt_pk_bf16_f32 v158, v108, v109\n\t" \
  "v_mfma_f32_16x16x32_bf16 a[56:59], a[220:223], v[148:151], a[56:59]\n\t" \
  "ds_read_b64_tr_b16 a[142:143], v234 offset:28672\n\t" \
  "v_xor_b32 v236, 32, v234\n\t" \
  "v_mfma_f32_16x16x32_bf16 a[72:75], a[224:227], v[144:147], a[72:75]\n\t" \
  "ds_read_b64_tr_b16 a[144:145], v236 offset:0\n\t" \
  "ds_read_b64_tr_b16 a[146:147], v236 offset:4096\n\t" \
  "v_mfma_f32_16x16x32_bf16 a[72:75], a[228:231], v[148:151], a[72:75]\n\t" \
  "v_rcp_f32 v212, v168\n\t" \
  "v_mfma_f32_16x16x32_bf16 a[88:91], a[232:235], v[144:147], a[88:91]\n\t" \
  "ds_read_b64_tr_b16 a[148:149], v236 offset:8192\n\t" \
  "ds_read_b64_tr_b16 a[150:151], v236 offset:12288\n\t" \
  "v_mfma_f32_16x16x32_bf16 a[88:91], a[236:239], v[148:151], a[88:91]\n\t" \
  "v_cmp_lt_f32 vcc, 0x5f800000, v168\n\t" \
  "s_cbranch_vccnz .Lat_x2\n\t" \
  ".Lat_y2:\n\t" \
  "v_accvgpr_read_b32 v16, a8\n\t" \
  "v_mfma_f32_16x16x32_bf16 a[104:107], a[240:243], v[144:147], a[104:107]\n\t" \
  "ds_read_b64_tr_b16 a[152:153], v236 offset:16384\n\t" \
  "v_accvgpr_read_b32 v17, a9\n\t" \
  "v_accvgpr_read_b32 v18, a10\n\t" \
  "v_mfma_f32_16x16x32_bf16 a[104:107], a[244:247], v[148:151], a[104:107]\n\t" \
  "v_exp_f32 v110, v110\n\t" \
  "v_mfma_f32_16x16x32_bf16 a[120:123], a[248:251], v[144:147], a[120:123]\n\t" \
  "v_exp_f32 v111, v111\n\t" \
  "v_mfma_f32_16x16x32_bf16 a[120:123], a[252:255], v[148:151], a[120:123]\n\t" \
  "v_accvgpr_read_b32 v19, a11\n\t" \
  "v_accvgpr_read_b32 v20, a24\n\t" \
  "v_accvgpr_read_b32 v21, a25\n\t" \
  "v_accvgpr_read_b32 v22, a26\n\t" \
  "v_accvgpr_read_b32 v23, a27\n\t" \
  "v_pk_mul_f32 v[16:17], v[16:17], v[212:213] op_sel_hi:[1,0]\n\t" \
  "v_pk_mul_f32 v[18:19], v[18:19], v[212:213] op_sel_hi:[1,0]\n\t" \
  "v_pk_mul_f32 v[20:21], v[20:21], v[212:213] op_sel_hi:[1,0]\n\t" \
  "v_pk_mul_f32 v[22:23], v[22:23], v[212:213] op_sel_hi:[1,0]\n\t" \
  "v_cvt_pk_bf16_f32 v64, v16, v17\n\t" \
  "v_mfma_f32_16x16x32_bf16 a[12:15], a[192:195], v[152:155], a[12:15]\n\t" \
  "ds_read_b64_tr_b16 a[154:155], v236 offset:20480\n\t" \
  "v_cvt_pk_bf16_f32 v65, v18, v19\n\t" \
  "v_cvt_pk_bf16_f32 v66, v20, v21\n\t" \
  "v_cvt_pk_bf16_f32 v67, v22, v23\n\t" \
  "v_accvgpr_read_b32 v24, a40\n\t" \
  "v_cvt_pk_bf16_f32 v159, v110, v111\n\t" \
  "s_nop 1\n\t" \
  "v_mfma_f32_16x16x32_bf16 a[12:15], a[196:199], v[156:159], a[12:15]\n\t" \
  "s_xor_b32 s98, %[eso], 65536\n\t" \
  "ds_read_b64_tr_b16 a[156:157], v236 offset:24576\n\t" \
  "v_accvgpr_read_b32 v25, a41\n\t" \
  "v_accvgpr_read_b32 v26, a42\n\t" \
  "v_mfma_f32_16x16x32_bf16 v[172:175], v[216:219], v[152:155], v[172:175]\n\t" \
  "buffer_store_dwordx4 v[64:67], v242, %[ws], s98 offen offset:0 sc1\n\t" \
  "v_mfma_f32_16x16x32_bf16 v[172:175], v[216:219], v[156:159], v[172:175]\n\t" \
  "v_accvgpr_read_b32 v27, a43\n\t" \
  "v_accvgpr_read_b32 v28, a56\n\t" \
  "v_accvgpr_read_b32 v29, a57\n\t" \
  "v_accvgpr_read_b32 v30, a58\n\t" \
  "v_accvgpr_read_b32 v31, a59\n\t" \
  "v_pk_mul_f32 v[24:25], v[24:25], v[212:213] op_sel_hi:[1,0]\n\t" \
  "v_pk_mul_f32 v[26:27], v[26:27], v[212:213] op_sel_hi:[1,0]\n\t" \
  "v_pk_mul_f32 v[28:29], v[28:29], v[212:213] op_sel_hi:[1,0]\n\t" \
  "v_pk_mul_f32 v[30:31], v[30:31], v[212:213] op_sel_hi:[1,0]\n\t" \
  "v_cvt_pk_bf16_f32 v68, v24, v25\n\t" \
  "v_mfma_f32_16x16x32_bf16 a[28:31], a[200:203], v[152:155], a[28:31]\n\t" \
  "ds_read_b64_tr_b16 a[158:159], v236 offset:28672\n\t" \
  "v_xor_b32 v235, 192, v234\n\t" \
  "v_cvt_pk_bf16_f32 v69, v26, v27\n\t" \
  "v_cvt_pk_bf16_f32 v70, v28, v29\n\t" \
  "v_cvt_pk_bf16_f32 v71, v30, v31\n\t" \
  "v_mfma_f32_16x16x32_bf16 a[28:31], a[204:207], v[156:159], a[28:31]\n\t" \
  "s_xor_b32 s98, %[eso], 65536\n\t" \
  "ds_read_b64_tr_b16 a[160:161], v235 offset:0\n\t" \
  "v_accvgpr_read_b32 v16, a72\n\t" \
  "v_accvgpr_read_b32 v17, a73\n\t" \
  "v_accvgpr_read_b32 v18, a74\n\t" \
  ".Lhs10:\n\ts_cbranch_execz .Lhs10n\n\t" \
  "v_mfma_f32_16x16x32_bf16 a[44:47], a[208:211], v[152:155], a[44:47]\n\t" \
  "buffer_store_dwordx4 v[68:71], v242, %[ws], s98 offen offset:1024 sc1\n\t" \
  "v_mfma_f32_16x16x32_bf16 a[44:47], a[212:215], v[156:159], a[44:47]\n\t" \
  "v_accvgpr_read_b32 v19, a75\n\t" \
  "v_accvgpr_read_b32 v20, a88\n\t" \
  "v_accvgpr_read_b32 v21, a89\n\t" \
  "v_accvgpr_read_b32 v22, a90\n\t" \
  "v_accvgpr_read_b32 v23, a91\n\t" \
  "v_pk_mul_f32 v[16:17], v[16:17], v[212:213] op_sel_hi:[1,0]\n\t" \
  "v_pk_mul_f32 v[18:19], v[18:19], v[212:213] op_sel_hi:[1,0]\n\t" \
  "v_pk_mul_f32 v[20:21], v[20:21], v[212:213] op_sel_hi:[1,0]\n\t" \
  "v_pk_mul_f32 v[22:23], v[22:23], v[212:213] op_sel_hi:[1,0]\n\t" \
  "v_cvt_pk_bf16_f32 v72, v16, v17\n\t" \
  "v_mfma_f32_16x16x32_bf16 a[60:63], a[216:219], v[152:155], a[60:63]\n\t" \
  "ds_read_b64_tr_b16 a[162:163], v235 offset:4096\n\t" \
  "v_cvt_pk_bf16_f32 v73, v18, v19\n\t" \
  "v_mfma_f32_16x16x32_bf16 a[60:63], a[220:223], v[156:159], a[60:63]\n\t" \
  "ds_read_b64_tr_b16 a[164:165], v235 offset:8192\n\t" \
  "v_cvt_pk_bf16_f32 v74, v20, v21\n\t" \
  "v_mfma_f32_16x16x32_bf16 a[76:79], a[224:227], v[152:155], a[76:79]\n\t" \
  "ds_read_b64_tr_b16 a[166:167], v235 offset:12288\n\t" \
  "v_cvt_pk_bf16_f32 v75, v22, v23\n\t" \
  "v_mfma_f32_16x16x32_bf16 a[76:79], a[228:231], v[156:159], a[76:79]\n\t" \
  "s_xor_b32 s98, %[eso], 65536\n\t" \
  "ds_read_b64_tr_b16 a[168:169], v235 offset:16384\n\t" \
  "v_accvgpr_read_b32 v24, a104\n\t" \
  "v_mfma_f32_16x16x32_bf16 a[92:95], a[232:235], v[152:155], a[92:95]\n\t" \
  "buffer_store_dwordx4 v[72:75], v242, %[ws], s98 offen offset:2048 sc1\n\t" \
  "v_mfma_f32_16x16x32_bf16 a[92:95], a[236:239], v[156:159], a[92:95]\n\t" \
  "ds_read_b64_tr_b16 a[170:171], v235 offset:20480\n\t" \
  "v_accvgpr_read_b32 v25, a105\n\t" \
  "v_accvgpr_read_b32 v26, a106\n\t" \
  "v_mfma_f32_16x16x32_bf16 a[108:111], a[240:243], v[152:155], a[108:111]\n\t" \
  "ds_read_b64_tr_b16 a[172:173], v235 offset:24576\n\t" \
  "ds_read_b64_tr_b16 a[174:175], v235 offset:28672\n\t" \
  "v_xor_b32 v236, 224, v234\n\t" \
  "v_mfma_f32_16x16x32_bf16 a[108:111], a[244:247], v[156:159], a[108:111]\n\t" \
  "v_accvgpr_read_b32 v27, a107\n\t" \
  "v_accvgpr_read_b32 v28, a120\n\t" \
  "v_accvgpr_read_b32 v29, a121\n\t" \
  "v_accvgpr_read_b32 v30, a122\n\t" \
  "v_accvgpr_read_b32 v31, a123\n\t" \
  "v_pk_mul_f32 v[24:25], v[24:25], v[212:213] op_sel_hi:[1,0]\n\t" \
  "v_pk_mul_f32 v[26:27], v[26:27], v[212:213] op_sel_hi:[1,0]\n\t" \
  "v_pk_mul_f32 v[28:29], v[28:29], v[212:213] op_sel_hi:[1,0]\n\t" \
  "v_pk_mul_f32 v[30:31], v[30:31], v[212:213] op_sel_hi:[1,0]\n\t" \
  "v_cvt_pk_bf16_f32 v76, v24, v25\n\t" \
  "v_mfma_f32_16x16x32_bf16 a[124:127], a[248:251], v[152:155], a[124:127]\n\t" \
  "ds_read_b64_tr_b16 a[176:177], v236 offset:0\n\t" \
  "v_cvt_pk_bf16_f32 v77, v26, v27\n\t" \
  "v_mfma_f32_16x16x32_bf16 a[124:127], a[252:255], v[156:159], a[124:127]\n\t" \
  "s_waitcnt lgkmcnt(15)\n\t" \
  "ds_read_b64_tr_b16 a[178:179], v236 offset:4096\n\t" \
  "v_cvt_pk_bf16_f32 v78, v28, v29\n\t" \
  "v_mfma_f32_16x16x32_bf16 a[32:35], v[48:51], v[0:3], 0\n\t" \
  "ds_read_b64_tr_b16 a[180:181], v236 offset:8192\n\t" \
  "v_cvt_pk_bf16_f32 v79, v30, v31\n\t" \
  "v_mfma_f32_16x16x32_bf16 a[32:35], v[52:55], v[4:7], a[32:35]\n\t" \
  "s_xor_b32 s98, %[eso], 65536\n\t" \
  "ds_read_b64_tr_b16 a[182:183], v236 offset:12288\n\t" \
  "ds_read_b64_tr_b16 a[184:185], v236 offset:16384\n\t" \
  "v_mfma_f32_16x16x32_bf16 a[32:35], v[56:59], v[8:11], a[32:35]\n\t" \
  "buffer_store_dwordx4 v[76:79], v242, %[ws], s98 offen offset:3072 sc1\n\t" \
  "v_mfma_f32_16x16x32_bf16 a[32:35], v[60:63], v[12:15], a[32:35]\n\t" \
  "ds_read_b64_tr_b16 a[186:187], v236 offset:20480\n\t" \
  "ds_read_b64_tr_b16 a[188:189], v236 offset:24576\n\t" \
  "v_mfma_f32_16x16x32_bf16 a[48:51], v[92:95], v[12:15], 0\n\t" \
  "ds_read_b64_tr_b16 a[190:191], v236 offset:28672\n\t" \
  "v_xor_b32 v236, 160, v234\n\t" \
  "v_mfma_f32_16x16x32_bf16 a[48:51], v[88:91], v[8:11], a[48:51]\n\t" \
  "ds_read_b64_tr_b16 v[96:97], v236 offset:0\n\t" \
  "ds_read_b64_tr_b16 v[98:99], v236 offset:4096\n\t" \
  "v_mfma_f32_16x16x32_bf16 a[48:51], v[84:87], v[4:7], a[48:51]\n\t" \
  "v_rcp_f32 v214, v172\n\t" \
  "v_mfma_f32_16x16x32_bf16 a[48:51], v[80:83], v[0:3], a[48:51]\n\t" \
  "v_cmp_lt_f32 vcc, 0x5f800000, v172\n\t" \
  "s_cbranch_vccnz .Lat_x3\n\t" \
  ".Lat_y3:\n\t" \
  "v_mfma_f32_16x16x32_bf16 a[64:67], v[112:115], v[0:3], 0\n\t" \
  "ds_read_b64_tr_b16 v[100:101], v236 offset:8192\n\t" \
  "v_accvgpr_read_b32 v16, a12\n\t" \
  "v_mfma_f32_16x16x32_bf16 a[64:67], v[116:119], v[4:7], a[64:67]\n\t" \
  "ds_read_b64_tr_b16 v[102:103], v236 offset:12288\n\t" \
  "v_accvgpr_read_b32 v17, a13\n\t" \
  "v_mfma_f32_16x16x32_bf16 a[64:67], v[120:123], v[8:11], a[64:67]\n\t" \
  "ds_read_b64_tr_b16 v[104:105], v236 offset:16384\n\t" \
  "v_accvgpr_read_b32 v18, a14\n\t" \
  "v_mfma_f32_16x16x32_bf16 a[64:67], v[124:127], v[12:15], a[64:67]\n\t" \
  "ds_read_b64_tr_b16 v[106:107], v236 offset:20480\n\t" \
  "v_accvgpr_read_b32 v19, a15\n\t" \
  "v_mfma_f32_16x16x32_bf16 a[0:3], a[140:143], v[12:15], 0\n\t" \
  "ds_read_b64_tr_b16 v[108:109], v236 offset:24576\n\t" \
  "v_accvgpr_read_b32 v20, a28\n\t" \
  "v_mfma_f32_16x16x32_bf16 a[0:3], a[136:139], v[8:11], a[0:3]\n\t" \
  "ds_read_b64_tr_b16 v[110:111], v236 offset:28672\n\t" \
  "v_accvgpr_read_b32 v21, a29\n\t" \
  "v_mfma_f32_16x16x32_bf16 a[0:3], a[132:135], v[4:7], a[0:3]\n\t" \
  "v_accvgpr_read_b32 v22, a30\n\t" \
  "v_accvgpr_read_b32 v23, a31\n\t" \
  "v_mfma_f32_16x16x32_bf16 a[0:3], a[128:131], v[0:3], a[0:3]\n\t" \
  "v_mul_f32 v16, v16, v214\n\t" \
  "v_mul_f32 v17, v17, v214\n\t" \
  "v_mfma_f32_16x16x32_bf16 a[16:19], a[144:147], v[0:3], 0\n\t" \
  "s_waitcnt lgkmcnt(15)\n\t" \
  "v_mul_f32 v18, v18, v214\n\t" \
  "v_mul_f32 v19, v19, v214\n\t" \
  "v_mfma_f32_16x16x32_bf16 a[16:19], a[148:151], v[4:7], a[16:19]\n\t" \
  "v_mul_f32 v20, v20, v214\n\t" \
  "v_mul_f32 v21, v21, v214\n\t" \
  "v_mfma_f32_16x16x32_bf16 a[16:19], a[152:155], v[8:11], a[16:19]\n\t" \
  "v_mul_f32 v22, v22, v214\n\t" \
  "v_mul_f32 v23, v23, v214\n\t" \
  "v_mfma_f32_16x16x32_bf16 a[16:19], a[156:159], v[12:15], a[16:19]\n\t" \
  "v_cvt_pk_bf16_f32 v160, v16, v17\n\t" \
  "v_cvt_pk_bf16_f32 v161, v18, v19\n\t" \
  "v_mfma_f32_16x16x32_bf16 a[96:99], a[172:175], v[12:15], 0\n\t" \
  "buffer_load_dwordx4 v[224:227], v252, %[ws], %[ss] offen sc1\n\t" \
  "v_mfma_f32_16x16x32_bf16 a[96:99], a[168:171], v[8:11], a[96:99]\n\t" \
  "v_cvt_pk_bf16_f32 v162, v20, v21\n\t" \
  "v_cvt_pk_bf16_f32 v163, v22, v23\n\t" \
  "v_mfma_f32_16x16x32_bf16 a[96:99], a[164:167], v[4:7], a[96:99]\n\t" \
  "s_waitcnt vmcnt(25)\n\t" \
  "ds_read_b128 a[224:227], v213 offset:32768\n\t" \
  "ds_read_b128 a[228:231], v213 offset:33792\n\t" \
  "v_mfma_f32_16x16x32_bf16 a[96:99], a[160:163], v[0:3], a[96:99]\n\t" \
  "s_waitcnt lgkmcnt(15)\n\t" \
  "ds_read_b128 a[232:235], v213 offset:34816\n\t" \
  "ds_read_b128 a[236:239], v213 offset:35840\n\t" \
  "v_mfma_f32_16x16x32_bf16 a[112:115], a[176:179], v[0:3], 0\n\t" \
  "s_waitcnt lgkmcnt(15)\n\t" \
  "buffer_load_dwordx4 v[128:131], v247, %[sM], %[gvo] offen\n\t" \
  "v_mfma_f32_16x16x32_bf16 a[112:115], a[180:183], v[4:7], a[112:115]\n\t" \
  "s_waitcnt lgkmcnt(14)\n\t" \
  "ds_read_b128 a[240:243], v215 offset:0\n\t" \
  "ds_read_b128 a[244:247], v215 offset:1024\n\t" \
  "v_mfma_f32_16x16x32_bf16 a[112:115], a[184:187], v[8:11], a[112:115]\n\t" \
  "s_waitcnt lgkmcnt(14)\n\t" \
  "ds_read_b128 a[248:251], v215 offset:2048\n\t" \
  "ds_read_b128 a[252:255], v215 offset:3072\n\t" \
  "v_mfma_f32_16x16x32_bf16 a[112:115], a[188:191], v[12:15], a[112:115]\n\t" \
  "s_waitcnt lgkmcnt(8)\n\t" \
  "s_waitcnt lgkmcnt(4)\n\t" \
  "ds_write_b128 v213, v[160:163] offset:32768\n\t" \
  "v_mfma_f32_16x16x32_bf16 a[80:83], v[108:111], v[12:15], 0\n\t" \
  "s_add_u32 s98, %[gvo], 12288\n\t" \
  "v_accvgpr_read_b32 v24, a44\n\t" \
  "v_accvgpr_read_b32 v25, a45\n\t" \
  "v_mfma_f32_16x16x32_bf16 a[80:83], v[104:107], v[8:11], a[80:83]\n\t" \
  "buffer_load_dwordx4 v[132:135], v247, %[sM], s98 offen\n\t" \
  "v_mfma_f32_16x16x32_bf16 a[80:83], v[100:103], v[4:7], a[80:83]\n\t" \
  "s_add_u32 s98, %[gvo], 24576\n\t" \
  "v_accvgpr_read_b32 v26, a46\n\t" \
  "v_accvgpr_read_b32 v27, a47\n\t" \
  "v_mfma_f32_16x16x32_bf16 a[80:83], v[96:99], v[0:3], a[80:83]\n\t" \
  "v_accvgpr_read_b32 v28, a60\n\t" \
  "v_accvgpr_read_b32 v29, a61\n\t" \
  "v_mfma_f32_16x16x32_bf16 a[36:39], v[48:51], v[32:35], 0\n\t" \
  "buffer_load_dwordx4 v[136:139], v247, %[sM], s98 offen\n\t" \
  "v_mfma_f32_16x16x32_bf16 a[36:39], v[52:55], v[36:39], a[36:39]\n\t" \
  "s_add_u32 s98, %[gvo], 36864\n\t" \
  "v_accvgpr_read_b32 v30, a62\n\t" \
  "v_accvgpr_read_b32 v31, a63\n\t" \
  "v_mfma_f32_16x16x32_bf16 a[36:39], v[56:59], v[40:43], a[36:39]\n\t" \
  "v_mul_f32 v24, v24, v214\n\t" \
  "v_mul_f32 v25, v25, v214\n\t" \
  "v_mfma_f32_16x16x32_bf16 a[36:39], v[60:63], v[44:47], a[36:39]\n\t" \
  "v_mul_f32 v26, v26, v214\n\t" \
  "v_mul_f32 v27, v27, v214\n\t" \
  "s_waitcnt vmcnt(8)\n\t" \
  "buffer_store_dword v243, v254, %[ws], %[fw2] offen offset:0 sc1\n\t" \
  "v_mfma_f32_16x16x32_bf16 a[52:55], v[92:95], v[44:47], 0\n\t" \
  "buffer_load_dwordx4 v[140:143], v247, %[sM], s98 offen\n\t" \
  "v_mfma_f32_16x16x32_bf16 a[52:55], v[88:91], v[40:43], a[52:55]\n\t" \
  "s_add_u32 s98, %[gvo], 49152\n\t" \
  "v_mul_f32 v28, v28, v214\n\t" \
  "v_mul_f32 v29, v29, v214\n\t" \
  "v_mfma_f32_16x16x32_bf16 a[52:55], v[84:87], v[36:39], a[52:55]\n\t" \
  "v_mul_f32 v30, v30, v214\n\t" \
  "v_mul_f32 v31, v31, v214\n\t" \
  "v_mfma_f32_16x16x32_bf16 a[52:55], v[80:83], v[32:35], a[52:55]\n\t" \
  "v_cvt_pk_bf16_f32 v164, v24, v25\n\t" \
  "v_cvt_pk_bf16_f32 v165, v26, v27\n\t" \
  "v_mfma_f32_16x16x32_bf16 a[68:71], v[112:115], v[32:35], 0\n\t" \
  "buffer_load_dwordx4 v[144:147], v247, %[sM], s98 offen\n\t" \
  "v_mfma_f32_16x16x32_bf16 a[68:71], v[116:119], v[36:39], a[68:71]\n\t" \
  "s_add_u32 s98, %[gvo], 61440\n\t" \
  "v_cvt_pk_bf16_f32 v166, v28, v29\n\t" \
  "v_cvt_pk_bf16_f32 v167, v30, v31\n\t" \
  "v_mfma_f32_16x16x32_bf16 a[68:71], v[120:123], v[40:43], a[68:71]\n\t" \
  "ds_write_b128 v213, v[164:167] offset:33792\n\t" \
  "v_mfma_f32_16x16x32_bf16 a[68:71], v[124:127], v[44:47], a[68:71]\n\t" \
  "v_accvgpr_read_b32 v16, a76\n\t" \
  "v_accvgpr_read_b32 v17, a77\n\t" \
  "v_mfma_f32_16x16x32_bf16 a[4:7], a[140:143], v[44:47], 0\n\t" \
  "buffer_load_dwordx4 v[148:151], v247, %[sM], s98 offen\n\t" \
  "v_mfma_f32_16x16x32_bf16 a[4:7], a[136:139], v[40:43], a[4:7]\n\t" \
  "s_add_u32 s98, %[gvo], 73728\n\t" \
  "v_accvgpr_read_b32 v18, a78\n\t" \
  "v_accvgpr_read_b32 v19, a79\n\t" \
  "v_mfma_f32_16x16x32_bf16 a[4:7], a[132:135], v[36:39], a[4:7]\n\t" \
  "v_accvgpr_read_b32 v20, a92\n\t" \
  "v_accvgpr_read_b32 v21, a93\n\t" \
  "v_mfma_f32_16x16x32_bf16 a[4:7], a[128:131], v[32:35], a[4:7]\n\t" \
  "v_accvgpr_read_b32 v22, a94\n\t" \
  "v_accvgpr_read_b32 v23, a95\n\t" \
  "v_mfma_f32_16x16x32_bf16 a[20:23], a[144:147], v[32:35], 0\n\t" \
  "buffer_load_dwordx4 v[152:155], v247, %[sM], s98 offen\n\t" \
  "v_mfma_f32_16x16x32_bf16 a[20:23], a[148:151], v[36:39], a[20:23]\n\t" \
  "s_add_u32 s98, %[gvo], 86016\n\t" \
  "v_mul_f32 v16, v16, v214\n\t" \
  "v_mul_f32 v17, v17, v214\n\t" \
  "v_mfma_f32_16x16x32_bf16 a[20:23], a[152:155], v[40:43], a[20:23]\n\t" \
  "v_mul_f32 v18, v18, v214\n\t" \
  "v_mul_f32 v19, v19, v214\n\t" \
  "v_mfma_f32_16x16x32_bf16 a[20:23], a[156:159], v[44:47], a[20:23]\n\t" \
  "v_mul_f32 v20, v20, v214\n\t" \
  "v_mul_f32 v21, v21, v214\n\t" \
  "v_mfma_f32_16x16x32_bf16 a[100:103], a[172:175], v[44:47], 0\n\t" \
  "buffer_load_dwordx4 v[156:159], v247, %[sM], s98 offen\n\t" \
  "v_mfma_f32_16x16x32_bf16 a[100:103], a[168:171], v[40:43], a[100:103]\n\t" \
  "v_mul_f32 v22, v22, v214\n\t" \
  "v_mul_f32 v23, v23, v214\n\t" \
  "v_mfma_f32_16x16x32_bf16 a[100:103], a[164:167], v[36:39], a[100:103]\n\t" \
  "v_cvt_pk_bf16_f32 v168, v16, v17\n\t" \
  "v_cvt_pk_bf16_f32 v169, v18, v19\n\t" \
  "v_mfma_f32_16x16x32_bf16 a[100:103], a[160:163], v[32:35], a[100:103]\n\t" \
  "v_cvt_pk_bf16_f32 v170, v20, v21\n\t" \
  "v_cvt_pk_bf16_f32 v171, v22, v23\n\t" \
  "v_mfma_f32_16x16x32_bf16 a[116:119], a[176:179], v[32:35], 0\n\t" \
  "ds_write_b128 v213, v[168:171] offset:34816\n\t" \
  "v_mfma_f32_16x16x32_bf16 a[116:119], a[180:183], v[36:39], a[116:119]\n\t" \
  "v_accvgpr_read_b32 v24, a108\n\t" \
  "v_accvgpr_read_b32 v25, a109\n\t" \
  "v_mfma_f32_16x16x32_bf16 a[116:119], a[184:187], v[40:43], a[116:119]\n\t" \
  "v_accvgpr_read_b32 v26, a110\n\t" \
  "v_accvgpr_read_b32 v27, a111\n\t" \
  "v_mfma_f32_16x16x32_bf16 a[116:119], a[188:191], v[44:47], a[116:119]\n\t" \
  "v_accvgpr_read_b32 v28, a124\n\t" \
  "v_accvgpr_read_b32 v29, a125\n\t" \
  "v_mfma_f32_16x16x32_bf16 a[84:87], v[108:111], v[44:47], 0\n\t" \
  "s_add_u32 m0, %[m], 147456\n\t" \
  "v_accvgpr_read_b32 v30, a126\n\t" \
  "v_accvgpr_read_b32 v31, a127\n\t" \
  "v_mfma_f32_16x16x32_bf16 a[84:87], v[104:107], v[40:43], a[84:87]\n\t" \
  "s_xor_b32 s97, %[wob], 655360\n\t" \
  "v_mul_f32 v24, v24, v214\n\t" \
  "v_mul_f32 v25, v25, v214\n\t" \
  "v_mfma_f32_16x16x32_bf16 a[84:87], v[100:103], v[36:39], a[84:87]\n\t" \
  "v_mul_f32 v26, v26, v214\n\t" \
  "v_mul_f32 v27, v27, v214\n\t" \
  "v_mfma_f32_16x16x32_bf16 a[84:87], v[96:99], v[32:35], a[84:87]\n\t" \
  "v_mul_f32 v28, v28, v214\n\t" \
  "v_mul_f32 v29, v29, v214\n\t" \
  "v_mfma_f32_16x16x32_bf16 a[40:43], v[48:51], v[64:67], 0\n\t" \
  "v_mul_f32 v30, v30, v214\n\t" \
  "v_mul_f32 v31, v31, v214\n\t" \
  "v_mfma_f32_16x16x32_bf16 a[40:43], v[52:55], v[68:71], a[40:43]\n\t" \
  "v_cvt_pk_bf16_f32 v172, v24, v25\n\t" \
  "v_cvt_pk_bf16_f32 v173, v26, v27\n\t" \
  "v_mfma_f32_16x16x32_bf16 a[40:43], v[56:59], v[72:75], a[40:43]\n\t" \
  "buffer_load_dwordx4 v240, %[wo], s97 offen offset:0 lds\n\t" \
  "v_mfma_f32_16x16x32_bf16 a[40:43], v[60:63], v[76:79], a[40:43]\n\t" \
  "s_add_u32 s97, s97, 7168\n\t" \
  "v_cvt_pk_bf16_f32 v174, v28, v29\n\t" \
  "v_cvt_pk_bf16_f32 v175, v30, v31\n\t" \
  "v_mfma_f32_16x16x32_bf16 a[56:59], v[92:95], v[76:79], 0\n\t" \
  "ds_write_b128 v213, v[172:175] offset:35840\n\t" \
  "v_mfma_f32_16x16x32_bf16 a[56:59], v[88:91], v[72:75], a[56:59]\n\t" \
  "buffer_load_dwordx4 v241, %[wo], s97 offen offset:1024 lds\n\t" \
  "v_mfma_f32_16x16x32_bf16 a[56:59], v[84:87], v[68:71], a[56:59]\n\t" \
  "s_add_u32 s97, s97, 7168\n\t" \
  "v_mfma_f32_16x16x32_bf16 a[56:59], v[80:83], v[64:67], a[56:59]\n\t" \
  "v_mfma_f32_16x16x32_bf16 a[72:75], v[112:115], v[64:67], 0\n\t" \
  "buffer_load_dwordx4 v240, %[wo], s97 offen offset:2048 lds\n\t" \
  "v_mfma_f32_16x16x32_bf16 a[72:75], v[116:119], v[68:71], a[72:75]\n\t" \
  "s_add_u32 s97, s97, 7168\n\t" \
  "v_mfma_f32_16x16x32_bf16 a[72:75], v[120:123], v[72:75], a[72:75]\n\t" \
  "v_mfma_f32_16x16x32_bf16 a[72:75], v[124:127], v[76:79], a[72:75]\n\t" \
  "buffer_load_dwordx4 v241, %[wo], s97 offen offset:3072 lds\n\t" \
  STS("s_memrealtime s[94:95]\n\t") \
  STS("s_memtime s[92:93]\n\t") \
  STS("s_waitcnt lgkmcnt(0)\n\t") \
  STS("v_writelane_b32 %[stv], s94, 22\n\t") \
  STS("v_writelane_b32 %[stv], s92, 23\n\t") \
  ".subsection 1\n\t" \
  "s_endpgm\n\t" \
  ".Lat_v:\n\t" \
  "s_mov_b32 s98, 0\n\t" \
  ".Lat_v1:\n\t" \
  "s_sleep 4\n\t" \
  "buffer_load_dword v209, v205, %[ws], 0 offen sc1\n\t" \
  "s_waitcnt vmcnt(0)\n\t" \
  "v_and_b32 v206, 0xbfffbfff, v209\n\t" \
  "v_cmp_ne_u32_e64 s[96:97], %[tag], v206\n\t" \
  "s_cmp_lg_u64 s[96:97], 0\n\t" \
  "s_cbranch_scc0 .Lat_w\n\t" \
  "s_add_u32 s98, s98, 1\n\t" \
  "s_cmp_lt_u32 s98, %[cap]\n\t" \
  "s_cbranch_scc1 .Lat_v1\n\t" \
  "v_mov_b32 v206, %[tag]\n\t" \
  "buffer_store_dword v206, off, %[ws], 0 offset:256 sc1\n\t" \
  "s_branch .Lat_w\n\t" \
  ".Lat_x0:\n\t" \
  "v_lshlrev_b32 v209, 12, v233\n\t" \
  "v_bfrev_b32 v254, 1\n\t" \
  "s_mov_b64 exec, 15\n\t" \
  "ds_write_b32 v209, v243 offset:49152\n\t" \
  "s_mov_b64 exec, -1\n\t" \
  "s_waitcnt lgkmcnt(0)\n\t" \
  "s_branch .Lat_y0\n\t" \
  ".Lat_x4:\n\t" \
  "v_lshlrev_b32 v209, 12, v233\n\t" \
  "v_bfrev_b32 v254, 1\n\t" \
  "s_mov_b64 exec, 15\n\t" \
  "ds_write_b32 v209, v243 offset:49152\n\t" \
  "s_mov_b64 exec, -1\n\t" \
  "s_waitcnt lgkmcnt(0)\n\t" \
  "s_branch .Lat_y4\n\t" \
  ".Lat_x1:\n\t" \
  "v_lshlrev_b32 v209, 12, v233\n\t" \
  "v_bfrev_b32 v254, 1\n\t" \
  "s_mov_b64 exec, 15\n\t" \
  "ds_write_b32 v209, v243 offset:49152\n\t" \
  "s_mov_b64 exec, -1\n\t" \
  "s_waitcnt lgkmcnt(0)\n\t" \
  "s_branch .Lat_y1\n\t" \
  ".Lat_x2:\n\t" \
  "v_lshlrev_b32 v209, 12, v233\n\t" \
  "v_bfrev_b32 v254, 1\n\t" \
  "s_mov_b64 exec, 15\n\t" \
  "ds_write_b32 v209, v243 offset:49152\n\t" \
  "s_mov_b64 exec, -1\n\t" \
  "s_waitcnt lgkmcnt(0)\n\t" \
  "s_branch .Lat_y2\n\t" \
  ".Lat_x3:\n\t" \
  "v_lshlrev_b32 v209, 12, v233\n\t" \
  "v_bfrev_b32 v254, 1\n\t" \
  "s_mov_b64 exec, 15\n\t" \
  "ds_write_b32 v209, v243 offset:49152\n\t" \
  "s_mov_b64 exec, -1\n\t" \
  "s_waitcnt lgkmcnt(0)\n\t" \
  "s_branch .Lat_y3\n\t" \
  ".subsection 2\n\t" \
  ".Lsf_go:\n\t" \
  "v_mfma_f32_16x16x32_bf16 v[28:31], a[156:159], a[224:227], v[28:31]\n\t" \
  "v_mov_b32 v216, 0x3f803f80\n\t" \
  "v_lshlrev_b32 v204, 7, v233\n\t" \
  "v_mfma_f32_16x16x32_bf16 v[28:31], a[92:95], a[240:243], v[28:31]\n\t" \
  "v_cmp_gt_u32 vcc, 16, v233\n\t" \
  "v_lshrrev_b32 v202, 3, v233\n\t" \
  "v_mfma_f32_16x16x32_bf16 v[24:27], a[152:155], a[224:227], v[24:27]\n\t" \
  "v_mul_u32_u24 v202, 11264, v202\n\t" \
  "v_add_u32 v202, v202, v204\n\t" \
  "v_mfma_f32_16x16x32_bf16 v[24:27], a[88:91], a[240:243], v[24:27]\n\t" \
  "v_bfrev_b32 v195, 1\n\t" \
  "v_mov_b32 v217, 0x3f803f80\n\t" \
  "v_mfma_f32_16x16x32_bf16 v[20:23], a[148:151], a[224:227], v[20:23]\n\t" \
  "v_cvt_pk_bf16_f32 v31, v30, v31\n\t" \
  "v_cvt_pk_bf16_f32 v30, v28, v29\n\t" \
  "v_mfma_f32_16x16x32_bf16 v[20:23], a[84:87], a[240:243], v[20:23]\n\t" \
  "v_cndmask_b32 v204, v195, v204, vcc\n\t" \
  "v_cndmask_b32 v202, v195, v202, vcc\n\t" \
  "v_mfma_f32_16x16x32_bf16 v[16:19], a[144:147], a[224:227], v[16:19]\n\t" \
  "v_cvt_pk_bf16_f32 v28, v24, v25\n\t" \
  "v_cvt_pk_bf16_f32 v29, v26, v27\n\t" \
  "v_mfma_f32_16x16x32_bf16 v[16:19], a[80:83], a[240:243], v[16:19]\n\t" \
  "v_permlane16_swap_b32 v28, v30\n\t" \
  "v_permlane16_swap_b32 v29, v31\n\t" \
  "v_mfma_f32_16x16x32_bf16 v[12:15], a[140:143], a[224:227], v[12:15]\n\t" \
  "v_cvt_pk_bf16_f32 v27, v22, v23\n\t" \
  "v_cvt_pk_bf16_f32 v26, v20, v21\n\t" \
  "v_mfma_f32_16x16x32_bf16 v[12:15], a[76:79], a[240:243], v[12:15]\n\t" \
  "v_mov_b32 v218, 0x3f803f80\n\t" \
  "v_mov_b32 v219, 0x3f803f80\n\t" \
  "v_mfma_f32_16x16x32_bf16 v[8:11], a[136:139], a[224:227], v[8:11]\n\t" \
  "v_cvt_pk_bf16_f32 v24, v16, v17\n\t" \
  "v_cvt_pk_bf16_f32 v25, v18, v19\n\t" \
  "v_mfma_f32_16x16x32_bf16 v[8:11], a[72:75], a[240:243], v[8:11]\n\t" \
  "v_permlane16_swap_b32 v24, v26\n\t" \
  "v_permlane16_swap_b32 v25, v27\n\t" \
  "v_mfma_f32_16x16x32_bf16 v[4:7], a[132:135], a[224:227], v[4:7]\n\t" \
  "v_cvt_pk_bf16_f32 v23, v14, v15\n\t" \
  "v_cvt_pk_bf16_f32 v22, v12, v13\n\t" \
  "v_mfma_f32_16x16x32_bf16 v[4:7], a[68:71], a[240:243], v[4:7]\n\t" \
  "v_mov_b32 v196, 1.0\n\t" \
  "v_mov_b32 v197, 1.0\n\t" \
  "v_mfma_f32_16x16x32_bf16 v[0:3], a[128:131], a[224:227], v[0:3]\n\t" \
  "v_cvt_pk_bf16_f32 v20, v8, v9\n\t" \
  "v_cvt_pk_bf16_f32 v21, v10, v11\n\t" \
  "v_mfma_f32_16x16x32_bf16 v[0:3], a[64:67], a[240:243], v[0:3]\n\t" \
  "v_permlane16_swap_b32 v20, v22\n\t" \
  "v_permlane16_swap_b32 v21, v23\n\t" \
  "s_waitcnt vmcnt(2)\n\t" \
  "s_barrier\n\t" \
  "v_mfma_f32_16x16x32_bf16 v[60:63], a[156:159], a[228:231], v[60:63]\n\t" \
  "v_cvt_pk_bf16_f32 v19, v6, v7\n\t" \
  "v_cvt_pk_bf16_f32 v18, v4, v5\n\t" \
  "v_mfma_f32_16x16x32_bf16 v[60:63], a[92:95], a[244:247], v[60:63]\n\t" \
  "v_cvt_pk_bf16_f32 v16, v0, v1\n\t" \
  "v_cvt_pk_bf16_f32 v17, v2, v3\n\t" \
  "v_mfma_f32_16x16x32_bf16 v[56:59], a[152:155], a[228:231], v[56:59]\n\t" \
  "v_permlane16_swap_b32 v16, v18\n\t" \
  "v_permlane16_swap_b32 v17, v19\n\t" \
  "v_mfma_f32_16x16x32_bf16 v[56:59], a[88:91], a[244:247], v[56:59]\n\t" \
  "v_mov_b32 v198, 1.0\n\t" \
  "v_mov_b32 v199, 1.0\n\t" \
  "v_mfma_f32_16x16x32_bf16 v[52:55], a[148:151], a[228:231], v[52:55]\n\t" \
  "v_cvt_pk_bf16_f32 v63, v62, v63\n\t" \
  "v_cvt_pk_bf16_f32 v62, v60, v61\n\t" \
  "v_mfma_f32_16x16x32_bf16 v[52:55], a[84:87], a[244:247], v[52:55]\n\t" \
  "v_lshlrev_b32 v242, 4, v233\n\t" \
  "s_mov_b32 s99, 0\n\t" \
  "v_mfma_f32_16x16x32_bf16 v[48:51], a[144:147], a[228:231], v[48:51]\n\t" \
  "v_cvt_pk_bf16_f32 v60, v56, v57\n\t" \
  "v_cvt_pk_bf16_f32 v61, v58, v59\n\t" \
  "v_mfma_f32_16x16x32_bf16 v[48:51], a[80:83], a[244:247], v[48:51]\n\t" \
  "v_permlane16_swap_b32 v60, v62\n\t" \
  "v_permlane16_swap_b32 v61, v63\n\t" \
  "v_mfma_f32_16x16x32_bf16 v[44:47], a[140:143], a[228:231], v[44:47]\n\t" \
  "v_cvt_pk_bf16_f32 v59, v54, v55\n\t" \
  "v_cvt_pk_bf16_f32 v58, v52, v53\n\t" \
  "v_mfma_f32_16x16x32_bf16 v[44:47], a[76:79], a[244:247], v[44:47]\n\t" \
  "v_add_u32 v237, 65536, v228\n\t" \
  "v_add_u32 v238, 65536, v229\n\t" \
  "v_mfma_f32_16x16x32_bf16 v[40:43], a[136:139], a[228:231], v[40:43]\n\t" \
  "v_cvt_pk_bf16_f32 v56, v48, v49\n\t" \
  "v_cvt_pk_bf16_f32 v57, v50, v51\n\t" \
  "v_mfma_f32_16x16x32_bf16 v[40:43], a[72:75], a[244:247], v[40:43]\n\t" \
  "v_permlane16_swap_b32 v56, v58\n\t" \
  "v_permlane16_swap_b32 v57, v59\n\t" \
  "v_mfma_f32_16x16x32_bf16 v[36:39], a[132:135], a[228:231], v[36:39]\n\t" \
  "v_cvt_pk_bf16_f32 v55, v46, v47\n\t" \
  "v_cvt_pk_bf16_f32 v54, v44, v45\n\t" \
  "v_mfma_f32_16x16x32_bf16 v[36:39], a[68:71], a[244:247], v[36:39]\n\t" \
  "ds_read_b128 a[0:3], v220 offset:0\n\t" \
  "ds_read_b128 a[4:7], v221 offset:0\n\t" \
  "v_mfma_f32_16x16x32_bf16 v[32:35], a[128:131], a[228:231], v[32:35]\n\t" \
  "v_cvt_pk_bf16_f32 v52, v40, v41\n\t" \
  "v_cvt_pk_bf16_f32 v53, v42, v43\n\t" \
  "v_mfma_f32_16x16x32_bf16 v[32:35], a[64:67], a[244:247], v[32:35]\n\t" \
  "v_permlane16_swap_b32 v52, v54\n\t" \
  "v_permlane16_swap_b32 v53, v55\n\t" \
  "v_mfma_f32_16x16x32_bf16 v[92:95], a[156:159], a[232:235], v[92:95]\n\t" \
  "v_cvt_pk_bf16_f32 v51, v38, v39\n\t" \
  "v_cvt_pk_bf16_f32 v50, v36, v37\n\t" \
  "v_mfma_f32_16x16x32_bf16 v[92:95], a[92:95], a[248:251], v[92:95]\n\t" \
  "s_add_u32 m0, %[m], 0\n\t" \
  "ds_read_b128 a[8:11], v222 offset:0\n\t" \
  "v_mfma_f32_16x16x32_bf16 v[88:91], a[152:155], a[232:235], v[88:91]\n\t" \
  "v_cvt_pk_bf16_f32 v48, v32, v33\n\t" \
  "v_cvt_pk_bf16_f32 v49, v34, v35\n\t" \
  "v_mfma_f32_16x16x32_bf16 v[88:91], a[88:91], a[248:251], v[88:91]\n\t" \
  "v_permlane16_swap_b32 v48, v50\n\t" \
  "v_permlane16_swap_b32 v49, v51\n\t" \
  "v_mfma_f32_16x16x32_bf16 v[84:87], a[148:151], a[232:235], v[84:87]\n\t" \
  "v_cvt_pk_bf16_f32 v95, v94, v95\n\t" \
  "v_cvt_pk_bf16_f32 v94, v92, v93\n\t" \
  "v_mfma_f32_16x16x32_bf16 v[84:87], a[84:87], a[248:251], v[84:87]\n\t" \
  "ds_read_b128 a[12:15], v223 offset:0\n\t" \
  "s_xor_b32 s97, %[wob], 0\n\t" \
  "v_mfma_f32_16x16x32_bf16 v[80:83], a[144:147], a[232:235], v[80:83]\n\t" \
  "v_cvt_pk_bf16_f32 v92, v88, v89\n\t" \
  "v_cvt_pk_bf16_f32 v93, v90, v91\n\t" \
  "v_mfma_f32_16x16x32_bf16 v[80:83], a[80:83], a[248:251], v[80:83]\n\t" \
  "v_permlane16_swap_b32 v92, v94\n\t" \
  "v_permlane16_swap_b32 v93, v95\n\t" \
  "v_mfma_f32_16x16x32_bf16 v[76:79], a[140:143], a[232:235], v[76:79]\n\t" \
  "v_cvt_pk_bf16_f32 v91, v86, v87\n\t" \
  "v_cvt_pk_bf16_f32 v90, v84, v85\n\t" \
  "v_mfma_f32_16x16x32_bf16 v[76:79], a[76:79], a[248:251], v[76:79]\n\t" \
  "ds_read_b128 a[16:19], v220 offset:4096\n\t" \
  "ds_read_b128 a[20:23], v221 offset:4096\n\t" \
  "v_mfma_f32_16x16x32_bf16 v[72:75], a[136:139], a[232:235], v[72:75]\n\t" \
  "v_cvt_pk_bf16_f32 v88, v80, v81\n\t" \
  "v_cvt_pk_bf16_f32 v89, v82, v83\n\t" \
  "v_mfma_f32_16x16x32_bf16 v[72:75], a[72:75], a[248:251], v[72:75]\n\t" \
  "v_permlane16_swap_b32 v88, v90\n\t" \
  "v_permlane16_swap_b32 v89, v91\n\t" \
  "v_mfma_f32_16x16x32_bf16 v[68:71], a[132:135], a[232:235], v[68:71]\n\t" \
  "v_cvt_pk_bf16_f32 v87, v78, v79\n\t" \
  "v_cvt_pk_bf16_f32 v86, v76, v77\n\t" \
  "v_mfma_f32_16x16x32_bf16 v[68:71], a[68:71], a[248:251], v[68:71]\n\t" \
  "ds_read_b128 a[24:27], v222 offset:4096\n\t" \
  "buffer_load_dwordx4 v240, %[wo], s97 offen offset:0 lds\n\t" \
  "v_mfma_f32_16x16x32_bf16 v[64:67], a[128:131], a[232:235], v[64:67]\n\t" \
  "v_cvt_pk_bf16_f32 v84, v72, v73\n\t" \
  "v_cvt_pk_bf16_f32 v85, v74, v75\n\t" \
  "v_mfma_f32_16x16x32_bf16 v[64:67], a[64:67], a[248:251], v[64:67]\n\t" \
  "v_permlane16_swap_b32 v84, v86\n\t" \
  "v_permlane16_swap_b32 v85, v87\n\t" \
  "v_mfma_f32_16x16x32_bf16 v[124:127], a[156:159], a[236:239], v[124:127]\n\t" \
  "v_cvt_pk_bf16_f32 v83, v70, v71\n\t" \
  "v_cvt_pk_bf16_f32 v82, v68, v69\n\t" \
  "v_mfma_f32_16x16x32_bf16 v[124:127], a[92:95], a[252:255], v[124:127]\n\t" \
  "ds_read_b128 a[28:31], v223 offset:4096\n\t" \
  "ds_read_b128 a[32:35], v220 offset:8192\n\t" \
  "v_mfma_f32_16x16x32_bf16 v[120:123], a[152:155], a[236:239], v[120:123]\n\t" \
  "v_cvt_pk_bf16_f32 v80, v64, v65\n\t" \
  "v_cvt_pk_bf16_f32 v81, v66, v67\n\t" \
  "v_mfma_f32_16x16x32_bf16 v[120:123], a[88:91], a[252:255], v[120:123]\n\t" \
  "v_permlane16_swap_b32 v80, v82\n\t" \
  "v_permlane16_swap_b32 v81, v83\n\t" \
  "v_mfma_f32_16x16x32_bf16 v[116:119], a[148:151], a[236:239], v[116:119]\n\t" \
  "v_cvt_pk_bf16_f32 v127, v126, v127\n\t" \
  "v_cvt_pk_bf16_f32 v126, v124, v125\n\t" \
  "v_mfma_f32_16x16x32_bf16 v[116:119], a[84:87], a[252:255], v[116:119]\n\t" \
  "s_add_u32 s97, s97, 7168\n\t" \
  "buffer_load_dwordx4 v241, %[wo], s97 offen offset:1024 lds\n\t" \
  "v_mfma_f32_16x16x32_bf16 v[112:115], a[144:147], a[236:239], v[112:115]\n\t" \
  "v_cvt_pk_bf16_f32 v124, v120, v121\n\t" \
  "v_cvt_pk_bf16_f32 v125, v122, v123\n\t" \
  "v_mfma_f32_16x16x32_bf16 v[112:115], a[80:83], a[252:255], v[112:115]\n\t" \
  "v_permlane16_swap_b32 v124, v126\n\t" \
  "v_permlane16_swap_b32 v125, v127\n\t" \
  "ds_read_b128 a[36:39], v221 offset:8192\n\t" \
  "ds_read_b128 a[40:43], v222 offset:8192\n\t" \
  "ds_read_b128 a[44:47], v223 offset:8192\n\t" \
  "ds_read_b128 a[48:51], v220 offset:12288\n\t" \
  "ds_read_b128 a[52:55], v221 offset:12288\n\t" \
  "ds_read_b128 a[56:59], v222 offset:12288\n\t" \
  "ds_read_b128 a[60:63], v223 offset:12288\n\t" \
  "v_mfma_f32_16x16x32_bf16 v[108:111], a[140:143], a[236:239], v[108:111]\n\t" \
  "v_cvt_pk_bf16_f32 v123, v118, v119\n\t" \
  "v_cvt_pk_bf16_f32 v122, v116, v117\n\t" \
  "v_mfma_f32_16x16x32_bf16 v[108:111], a[76:79], a[252:255], v[108:111]\n\t" \
  "v_cvt_pk_bf16_f32 v120, v112, v113\n\t" \
  "v_cvt_pk_bf16_f32 v121, v114, v115\n\t" \
  "v_mfma_f32_16x16x32_bf16 v[104:107], a[136:139], a[236:239], v[104:107]\n\t" \
  "v_permlane16_swap_b32 v120, v122\n\t" \
  "v_permlane16_swap_b32 v121, v123\n\t" \
  "v_mfma_f32_16x16x32_bf16 v[104:107], a[72:75], a[252:255], v[104:107]\n\t" \
  "s_add_u32 s97, s97, 7168\n\t" \
  "buffer_load_dwordx4 v240, %[wo], s97 offen offset:2048 lds\n\t" \
  "v_mfma_f32_16x16x32_bf16 v[100:103], a[132:135], a[236:239], v[100:103]\n\t" \
  "v_cvt_pk_bf16_f32 v119, v110, v111\n\t" \
  "v_cvt_pk_bf16_f32 v118, v108, v109\n\t" \
  "v_mfma_f32_16x16x32_bf16 v[100:103], a[68:71], a[252:255], v[100:103]\n\t" \
  "s_add_u32 s97, s97, 7168\n\t" \
  "buffer_load_dwordx4 v241, %[wo], s97 offen offset:3072 lds\n\t" \
  "v_mfma_f32_16x16x32_bf16 v[96:99], a[128:131], a[236:239], v[96:99]\n\t" \
  "v_cvt_pk_bf16_f32 v116, v104, v105\n\t" \
  "v_cvt_pk_bf16_f32 v117, v106, v107\n\t" \
  "v_mfma_f32_16x16x32_bf16 v[96:99], a[64:67], a[252:255], v[96:99]\n\t" \
  "v_permlane16_swap_b32 v116, v118\n\t" \
  "v_permlane16_swap_b32 v117, v119\n\t" \
  "s_add_u32 m0, %[m], 16384\n\t" \
  "s_xor_b32 s97, %[wob], 131072\n\t" \
  "buffer_load_dwordx4 v240, %[wo], s97 offen offset:0 lds\n\t" \
  "s_add_u32 s97, s97, 7168\n\t" \
  "buffer_load_dwordx4 v241, %[wo], s97 offen offset:1024 lds\n\t" \
  "s_add_u32 s97, s97, 7168\n\t" \
  "buffer_load_dwordx4 v240, %[wo], s97 offen offset:2048 lds\n\t" \
  "s_add_u32 s97, s97, 7168\n\t" \
  "buffer_load_dwordx4 v241, %[wo], s97 offen offset:3072 lds\n\t" \
  "s_waitcnt lgkmcnt(0)\n\t" \
  "s_barrier\n\t" \
  "v_mfma_f32_16x16x32_bf16 v[0:3], a[0:3], v[16:19], 0\n\t" \
  "v_cvt_pk_bf16_f32 v115, v102, v103\n\t" \
  "v_cvt_pk_bf16_f32 v114, v100, v101\n\t" \
  "v_mfma_f32_16x16x32_bf16 v[0:3], a[4:7], v[20:23], v[0:3]\n\t" \
  "v_cvt_pk_bf16_f32 v112, v96, v97\n\t" \
  "v_cvt_pk_bf16_f32 v113, v98, v99\n\t" \
  "v_mfma_f32_16x16x32_bf16 v[0:3], a[8:11], v[24:27], v[0:3]\n\t" \
  "v_permlane16_swap_b32 v112, v114\n\t" \
  "v_permlane16_swap_b32 v113, v115\n\t" \
  "v_mfma_f32_16x16x32_bf16 v[0:3], a[12:15], v[28:31], v[0:3]\n\t" \
  "s_add_u32 s98, %[pf4], 0\n\t" \
  "buffer_load_dword %[pfd], v245, %[sG], s98 offen\n\t" \
  "v_mfma_f32_16x16x32_bf16 v[32:35], a[0:3], v[48:51], 0\n\t" \
  "ds_read_b128 a[192:195], v220 offset:16384\n\t" \
  "s_add_u32 m0, %[m], 49152\n\t" \
  "v_mfma_f32_16x16x32_bf16 v[32:35], a[4:7], v[52:55], v[32:35]\n\t" \
  "ds_read_b128 a[196:199], v221 offset:16384\n\t" \
  "ds_read_b128 a[200:203], v222 offset:16384\n\t" \
  "v_mfma_f32_16x16x32_bf16 v[32:35], a[8:11], v[56:59], v[32:35]\n\t" \
  "v_max3_f32 v194, v0, v1, v2\n\t" \
  "v_max_f32 v194, v194, v3\n\t" \
  "v_mfma_f32_16x16x32_bf16 v[32:35], a[12:15], v[60:63], v[32:35]\n\t" \
  "v_mov_b32 v195, v194\n\t" \
  "s_add_u32 s96, %[sofk], 32768\n\t" \
  "s_nop 0\n\t" \
  "v_permlane32_swap_b32 v194, v195\n\t" \
  "v_max_f32 v194, v194, v195\n\t" \
  "v_mov_b32 v195, v194\n\t" \
  "s_nop 1\n\t" \
  "v_permlane16_swap_b32 v194, v195\n\t" \
  "v_max_f32 v194, v194, v195\n\t" \
  "v_sub_f32 v176, 0xc2000000, v194\n\t" \
  "v_sub_f32 v177, 0xc2000000, v194\n\t" \
  "v_sub_f32 v178, 0xc2000000, v194\n\t" \
  "v_sub_f32 v179, 0xc2000000, v194\n\t" \
  "s_nop 1\n\t" \
  "v_mfma_f32_16x16x32_bf16 v[4:7], a[16:19], v[16:19], v[176:179]\n\t" \
  "v_add_f32 v0, v0, v176\n\t" \
  "v_add_f32 v1, v1, v176\n\t" \
  "v_mfma_f32_16x16x32_bf16 v[4:7], a[20:23], v[20:23], v[4:7]\n\t" \
  "v_add_f32 v2, v2, v176\n\t" \
  "v_add_f32 v3, v3, v176\n\t" \
  "v_mfma_f32_16x16x32_bf16 v[4:7], a[24:27], v[24:27], v[4:7]\n\t" \
  "ds_read_b128 a[204:207], v223 offset:16384\n\t" \
  "v_max3_f32 v194, v32, v33, v34\n\t" \
  "v_mfma_f32_16x16x32_bf16 v[4:7], a[28:31], v[28:31], v[4:7]\n\t" \
  "v_max_f32 v194, v194, v35\n\t" \
  "v_mov_b32 v195, v194\n\t" \
  "v_mfma_f32_16x16x32_bf16 v[8:11], a[32:35], v[16:19], v[176:179]\n\t" \
  "ds_read_b128 a[208:211], v220 offset:20480\n\t" \
  "v_permlane32_swap_b32 v194, v195\n\t" \
  "v_mfma_f32_16x16x32_bf16 v[8:11], a[36:39], v[20:23], v[8:11]\n\t" \
  "v_max_f32 v194, v194, v195\n\t" \
  "v_mov_b32 v195, v194\n\t" \
  "v_mfma_f32_16x16x32_bf16 v[8:11], a[40:43], v[24:27], v[8:11]\n\t" \
  "ds_read_b128 a[212:215], v221 offset:20480\n\t" \
  "v_permlane16_swap_b32 v194, v195\n\t" \
  "v_mfma_f32_16x16x32_bf16 v[8:11], a[44:47], v[28:31], v[8:11]\n\t" \
  "v_max_f32 v194, v194, v195\n\t" \
  "v_sub_f32 v180, 0xc2000000, v194\n\t" \
  "v_mfma_f32_16x16x32_bf16 v[12:15], a[48:51], v[16:19], v[176:179]\n\t" \
  "v_sub_f32 v181, 0xc2000000, v194\n\t" \
  "v_sub_f32 v182, 0xc2000000, v194\n\t" \
  "v_mfma_f32_16x16x32_bf16 v[12:15], a[52:55], v[20:23], v[12:15]\n\t" \
  "v_sub_f32 v183, 0xc2000000, v194\n\t" \
  "v_add_f32 v32, v32, v180\n\t" \
  "v_mfma_f32_16x16x32_bf16 v[12:15], a[56:59], v[24:27], v[12:15]\n\t" \
  "v_add_f32 v33, v33, v180\n\t" \
  "v_add_f32 v34, v34, v180\n\t" \
  "v_mfma_f32_16x16x32_bf16 v[12:15], a[60:63], v[28:31], v[12:15]\n\t" \
  "v_add_f32 v35, v35, v180\n\t" \
  "v_max3_f32 v192, v4, v5, v6\n\t" \
  "v_mfma_f32_16x16x32_bf16 v[64:67], a[0:3], v[80:83], 0\n\t" \
  "buffer_load_dwordx4 v224, %[sK], s96 offen offset:0 lds\n\t" \
  "ds_read_b128 a[216:219], v222 offset:20480\n\t" \
  "v_mfma_f32_16x16x32_bf16 v[64:67], a[4:7], v[84:87], v[64:67]\n\t" \
  "ds_read_b128 a[220:223], v223 offset:20480\n\t" \
  "v_max3_f32 v203, v7, v8, v9\n\t" \
  "v_mfma_f32_16x16x32_bf16 v[64:67], a[8:11], v[88:91], v[64:67]\n\t" \
  "v_max3_f32 v192, v192, v10, v11\n\t" \
  "ds_read_b128 a[224:227], v220 offset:24576\n\t" \
  "v_mfma_f32_16x16x32_bf16 v[64:67], a[12:15], v[92:95], v[64:67]\n\t" \
  "buffer_load_dwordx4 v225, %[sK], s96 offen offset:1024 lds\n\t" \
  "ds_read_b128 a[228:231], v221 offset:24576\n\t" \
  "v_mfma_f32_16x16x32_bf16 v[36:39], a[16:19], v[48:51], v[180:183]\n\t" \
  "v_max3_f32 v203, v203, v12, v13\n\t" \
  "v_max3_f32 v192, v192, v14, v15\n\t" \
  "v_mfma_f32_16x16x32_bf16 v[36:39], a[20:23], v[52:55], v[36:39]\n\t" \
  "v_max_f32 v192, v192, v203\n\t" \
  "ds_read_b128 a[232:235], v222 offset:24576\n\t" \
  "v_mfma_f32_16x16x32_bf16 v[36:39], a[24:27], v[56:59], v[36:39]\n\t" \
  "v_cmp_lt_f32 vcc, 0x42800000, v192\n\t" \
  "v_max3_f32 v194, v64, v65, v66\n\t" \
  "v_mfma_f32_16x16x32_bf16 v[36:39], a[28:31], v[60:63], v[36:39]\n\t" \
  "s_cbranch_vccnz .Lsf_c00\n\t" \
  ".Lsf_r00:\n\t" \
  "v_max_f32 v194, v194, v67\n\t" \
  "v_mfma_f32_16x16x32_bf16 v[40:43], a[32:35], v[48:51], v[180:183]\n\t" \
  "v_mov_b32 v195, v194\n\t" \
  "ds_read_b128 a[236:239], v223 offset:24576\n\t" \
  "v_mfma_f32_16x16x32_bf16 v[40:43], a[36:39], v[52:55], v[40:43]\n\t" \
  "v_permlane32_swap_b32 v194, v195\n\t" \
  "v_max_f32 v194, v194, v195\n\t" \
  "v_mfma_f32_16x16x32_bf16 v[40:43], a[40:43], v[56:59], v[40:43]\n\t" \
  "v_mov_b32 v195, v194\n\t" \
  "buffer_load_dwordx4 v226, %[sK], s96 offen offset:2048 lds\n\t" \
  "v_mfma_f32_16x16x32_bf16 v[40:43], a[44:47], v[60:63], v[40:43]\n\t" \
  "v_permlane16_swap_b32 v194, v195\n\t" \
  "v_max_f32 v194, v194, v195\n\t" \
  "v_mfma_f32_16x16x32_bf16 v[44:47], a[48:51], v[48:51], v[180:183]\n\t" \
  "v_sub_f32 v184, 0xc2000000, v194\n\t" \
  "v_sub_f32 v185, 0xc2000000, v194\n\t" \
  "v_mfma_f32_16x16x32_bf16 v[44:47], a[52:55], v[52:55], v[44:47]\n\t" \
  "v_sub_f32 v186, 0xc2000000, v194\n\t" \
  "v_sub_f32 v187, 0xc2000000, v194\n\t" \
  "v_mfma_f32_16x16x32_bf16 v[44:47], a[56:59], v[56:59], v[44:47]\n\t" \
  "v_add_f32 v64, v64, v184\n\t" \
  "v_add_f32 v65, v65, v184\n\t" \
  "v_mfma_f32_16x16x32_bf16 v[44:47], a[60:63], v[60:63], v[44:47]\n\t" \
  "v_add_f32 v66, v66, v184\n\t" \
  "v_add_f32 v67, v67, v184\n\t" \
  "v_mfma_f32_16x16x32_bf16 v[96:99], a[0:3], v[112:115], 0\n\t" \
  "ds_read_b128 a[240:243], v220 offset:28672\n\t" \
  "ds_read_b128 a[244:247], v221 offset:28672\n\t" \
  "v_mfma_f32_16x16x32_bf16 v[96:99], a[4:7], v[116:119], v[96:99]\n\t" \
  "v_max3_f32 v192, v36, v37, v38\n\t" \
  "ds_read_b128 a[248:251], v222 offset:28672\n\t" \
  "v_mfma_f32_16x16x32_bf16 v[96:99], a[8:11], v[120:123], v[96:99]\n\t" \
  "buffer_load_dwordx4 v227, %[sK], s96 offen offset:3072 lds\n\t" \
  "ds_read_b128 a[252:255], v223 offset:28672\n\t" \
  "v_mfma_f32_16x16x32_bf16 v[96:99], a[12:15], v[124:127], v[96:99]\n\t" \
  "v_max3_f32 v203, v39, v40, v41\n\t" \
  "v_max3_f32 v192, v192, v42, v43\n\t" \
  "v_mfma_f32_16x16x32_bf16 v[68:71], a[16:19], v[80:83], v[184:187]\n\t" \
  "ds_read_b64_tr_b16 a[128:129], v228 offset:0\n\t" \
  "v_max3_f32 v203, v203, v44, v45\n\t" \
  "v_mfma_f32_16x16x32_bf16 v[68:71], a[20:23], v[84:87], v[68:71]\n\t" \
  "v_max3_f32 v192, v192, v46, v47\n\t" \
  "v_max_f32 v192, v192, v203\n\t" \
  "v_mfma_f32_16x16x32_bf16 v[68:71], a[24:27], v[88:91], v[68:71]\n\t" \
  "v_cmp_lt_f32 vcc, 0x42800000, v192\n\t" \
  "s_add_u32 m0, %[m], 147456\n\t" \
  "v_mfma_f32_16x16x32_bf16 v[68:71], a[28:31], v[92:95], v[68:71]\n\t" \
  "s_cbranch_vccnz .Lsf_c01\n\t" \
  ".Lsf_r01:\n\t" \
  "ds_read_b64_tr_b16 a[130:131], v228 offset:4096\n\t" \
  "v_mfma_f32_16x16x32_bf16 v[72:75], a[32:35], v[80:83], v[184:187]\n\t" \
  "v_max3_f32 v194, v96, v97, v98\n\t" \
  "v_max_f32 v194, v194, v99\n\t" \
  "v_mfma_f32_16x16x32_bf16 v[72:75], a[36:39], v[84:87], v[72:75]\n\t" \
  "v_mov_b32 v195, v194\n\t" \
  "ds_read_b64_tr_b16 a[132:133], v228 offset:8192\n\t" \
  "v_mfma_f32_16x16x32_bf16 v[72:75], a[40:43], v[88:91], v[72:75]\n\t" \
  "v_permlane32_swap_b32 v194, v195\n\t" \
  "v_max_f32 v194, v194, v195\n\t" \
  "v_mfma_f32_16x16x32_bf16 v[72:75], a[44:47], v[92:95], v[72:75]\n\t" \
  "v_mov_b32 v195, v194\n\t" \
  "s_add_u32 s96, %[sofk], 16384\n\t" \
  "v_mfma_f32_16x16x32_bf16 v[76:79], a[48:51], v[80:83], v[184:187]\n\t" \
  "v_permlane16_swap_b32 v194, v195\n\t" \
  "v_max_f32 v194, v194, v195\n\t" \
  "v_mfma_f32_16x16x32_bf16 v[76:79], a[52:55], v[84:87], v[76:79]\n\t" \
  "v_sub_f32 v188, 0xc2000000, v194\n\t" \
  "v_sub_f32 v189, 0xc2000000, v194\n\t" \
  "v_mfma_f32_16x16x32_bf16 v[76:79], a[56:59], v[88:91], v[76:79]\n\t" \
  "v_sub_f32 v190, 0xc2000000, v194\n\t" \
  "v_sub_f32 v191, 0xc2000000, v194\n\t" \
  "v_mfma_f32_16x16x32_bf16 v[76:79], a[60:63], v[92:95], v[76:79]\n\t" \
  "v_add_f32 v96, v96, v188\n\t" \
  "v_add_f32 v97, v97, v188\n\t" \
  "v_mfma_f32_16x16x32_bf16 v[100:103], a[16:19], v[112:115], v[188:191]\n\t" \
  "v_add_f32 v98, v98, v188\n\t" \
  "v_add_f32 v99, v99, v188\n\t" \
  "v_mfma_f32_16x16x32_bf16 v[100:103], a[20:23], v[116:119], v[100:103]\n\t" \
  "ds_read_b64_tr_b16 a[134:135], v228 offset:12288\n\t" \
  "v_max3_f32 v192, v68, v69, v70\n\t" \
  "v_mfma_f32_16x16x32_bf16 v[100:103], a[24:27], v[120:123], v[100:103]\n\t" \
  "ds_read_b64_tr_b16 a[136:137], v229 offset:0\n\t" \
  "v_max3_f32 v203, v71, v72, v73\n\t" \
  "v_mfma_f32_16x16x32_bf16 v[100:103], a[28:31], v[124:127], v[100:103]\n\t" \
  "v_max3_f32 v192, v192, v74, v75\n\t" \
  "buffer_load_dwordx4 v230, %[sV], s96 offen offset:0 lds\n\t" \
  "v_mfma_f32_16x16x32_bf16 v[104:107], a[32:35], v[112:115], v[188:191]\n\t" \
  "ds_read_b64_tr_b16 a[138:139], v229 offset:4096\n\t" \
  "ds_read_b64_tr_b16 a[140:141], v229 offset:8192\n\t" \
  "v_mfma_f32_16x16x32_bf16 v[104:107], a[36:39], v[116:119], v[104:107]\n\t" \
  "v_max3_f32 v203, v203, v76, v77\n\t" \
  "v_max3_f32 v192, v192, v78, v79\n\t" \
  "v_mfma_f32_16x16x32_bf16 v[104:107], a[40:43], v[120:123], v[104:107]\n\t" \
  "v_max_f32 v192, v192, v203\n\t" \
  "ds_read_b64_tr_b16 a[142:143], v229 offset:12288\n\t" \
  "v_mfma_f32_16x16x32_bf16 v[104:107], a[44:47], v[124:127], v[104:107]\n\t" \
  "v_cmp_lt_f32 vcc, 0x42800000, v192\n\t" \
  "buffer_load_dwordx4 v231, %[sV], s96 offen offset:1024 lds\n\t" \
  "v_mfma_f32_16x16x32_bf16 v[108:111], a[48:51], v[112:115], v[188:191]\n\t" \
  "s_cbranch_vccnz .Lsf_c02\n\t" \
  ".Lsf_r02:\n\t" \
  "v_max3_f32 v192, v100, v101, v102\n\t" \
  "v_mfma_f32_16x16x32_bf16 v[108:111], a[52:55], v[116:119], v[108:111]\n\t" \
  "ds_read_b64_tr_b16 a[144:145], v228 offset:256\n\t" \
  "ds_read_b64_tr_b16 a[146:147], v228 offset:4352\n\t" \
  "v_mfma_f32_16x16x32_bf16 v[108:111], a[56:59], v[120:123], v[108:111]\n\t" \
  "v_max3_f32 v203, v103, v104, v105\n\t" \
  "v_max3_f32 v192, v192, v106, v107\n\t" \
  "v_mfma_f32_16x16x32_bf16 v[108:111], a[60:63], v[124:127], v[108:111]\n\t" \
  "buffer_load_dwordx4 v230, %[sV], s96 offen offset:2048 lds\n\t" \
  "ds_read_b64_tr_b16 a[148:149], v228 offset:8448\n\t" \
  "v_exp_f32 v0, v0\n\t" \
  "v_exp_f32 v1, v1\n\t" \
  "v_exp_f32 v2, v2\n\t" \
  "v_exp_f32 v3, v3\n\t" \
  "v_cvt_pk_bf16_f32 v128, v0, v1\n\t" \
  "v_cvt_pk_bf16_f32 v129, v2, v3\n\t" \
  "s_waitcnt lgkmcnt(11)\n\t" \
  "v_mfma_f32_16x16x32_bf16 v[0:3], a[192:195], v[16:19], v[176:179]\n\t" \
  "v_exp_f32 v4, v4\n\t" \
  "v_mfma_f32_16x16x32_bf16 v[0:3], a[196:199], v[20:23], v[0:3]\n\t" \
  "v_max3_f32 v203, v203, v108, v109\n\t" \
  "v_max3_f32 v192, v192, v110, v111\n\t" \
  "v_mfma_f32_16x16x32_bf16 v[0:3], a[200:203], v[24:27], v[0:3]\n\t" \
  "v_max_f32 v192, v192, v203\n\t" \
  "ds_read_b64_tr_b16 a[150:151], v228 offset:12544\n\t" \
  "v_mfma_f32_16x16x32_bf16 v[0:3], a[204:207], v[28:31], v[0:3]\n\t" \
  "v_cmp_lt_f32 vcc, 0x42800000, v192\n\t" \
  "buffer_load_dwordx4 v231, %[sV], s96 offen offset:3072 lds\n\t" \
  "v_exp_f32 v5, v5\n\t" \
  "v_exp_f32 v6, v6\n\t" \
  "v_exp_f32 v7, v7\n\t" \
  "v_cvt_pk_bf16_f32 v130, v4, v5\n\t" \
  "v_cvt_pk_bf16_f32 v131, v6, v7\n\t" \
  "v_mfma_f32_16x16x32_bf16 v[4:7], a[220:223], v[28:31], v[176:179]\n\t" \
  "s_cbranch_vccnz .Lsf_c03\n\t" \
  ".Lsf_r03:\n\t" \
  "ds_read_b64_tr_b16 a[152:153], v229 offset:256\n\t" \
  "v_mfma_f32_16x16x32_bf16 v[4:7], a[216:219], v[24:27], v[4:7]\n\t" \
  "s_cmp_lg_u32 s99, 0\n\t" \
  "s_cbranch_scc1 .Lsf_p0\n\t" \
  ".Lsf_q0:\n\t" \
  "v_mfma_f32_16x16x32_bf16 v[4:7], a[212:215], v[20:23], v[4:7]\n\t" \
  "v_max3_f32 v192, v0, v1, v2\n\t" \
  "ds_read_b64_tr_b16 a[154:155], v229 offset:4352\n\t" \
  "v_mfma_f32_16x16x32_bf16 v[4:7], a[208:211], v[16:19], v[4:7]\n\t" \
  "v_exp_f32 v8, v8\n\t" \
  "v_exp_f32 v9, v9\n\t" \
  "v_exp_f32 v10, v10\n\t" \
  "v_exp_f32 v11, v11\n\t" \
  "v_cvt_pk_bf16_f32 v132, v8, v9\n\t" \
  "v_cvt_pk_bf16_f32 v133, v10, v11\n\t" \
  "v_mfma_f32_16x16x32_bf16 v[8:11], a[224:227], v[16:19], v[176:179]\n\t" \
  "s_add_u32 s98, %[pf4], 14336\n\t" \
  "buffer_load_dword %[pfd], v245, %[sG], s98 offen\n\t" \
  "v_mfma_f32_16x16x32_bf16 v[8:11], a[228:231], v[20:23], v[8:11]\n\t" \
  "v_max3_f32 v203, v3, v4, v5\n\t" \
  "v_max3_f32 v192, v192, v6, v7\n\t" \
  "v_mfma_f32_16x16x32_bf16 v[8:11], a[232:235], v[24:27], v[8:11]\n\t" \
  "v_exp_f32 v12, v12\n\t" \
  "v_mfma_f32_16x16x32_bf16 v[8:11], a[236:239], v[28:31], v[8:11]\n\t" \
  "ds_read_b64_tr_b16 a[156:157], v229 offset:8448\n\t" \
  "ds_read_b64_tr_b16 a[158:159], v229 offset:12544\n\t" \
  "v_exp_f32 v13, v13\n\t" \
  "v_exp_f32 v14, v14\n\t" \
  "v_exp_f32 v15, v15\n\t" \
  "v_cvt_pk_bf16_f32 v134, v12, v13\n\t" \
  "v_cvt_pk_bf16_f32 v135, v14, v15\n\t" \
  "v_mfma_f32_16x16x32_bf16 v[12:15], a[252:255], v[28:31], v[176:179]\n\t" \
  "v_exp_f32 v32, v32\n\t" \
  "v_mfma_f32_16x16x32_bf16 v[12:15], a[248:251], v[24:27], v[12:15]\n\t" \
  "v_exp_f32 v33, v33\n\t" \
  "v_mfma_f32_16x16x32_bf16 v[12:15], a[244:247], v[20:23], v[12:15]\n\t" \
  "v_cvt_pk_bf16_f32 v136, v32, v33\n\t" \
  "v_max3_f32 v203, v203, v8, v9\n\t" \
  "v_mfma_f32_16x16x32_bf16 v[12:15], a[240:243], v[16:19], v[12:15]\n\t" \
  "v_max3_f32 v192, v192, v10, v11\n\t" \
  "ds_read_b64_tr_b16 a[160:161], v228 offset:512\n\t" \
  "v_exp_f32 v34, v34\n\t" \
  "v_exp_f32 v35, v35\n\t" \
  "s_nop 0\n\t" \
  "v_cvt_pk_bf16_f32 v137, v34, v35\n\t" \
  "v_mfma_f32_16x16x32_bf16 v[32:35], a[192:195], v[48:51], v[180:183]\n\t" \
  "v_exp_f32 v36, v36\n\t" \
  "v_mfma_f32_16x16x32_bf16 v[32:35], a[196:199], v[52:55], v[32:35]\n\t" \
  "v_max3_f32 v203, v203, v12, v13\n\t" \
  "v_max3_f32 v192, v192, v14, v15\n\t" \
  "v_mfma_f32_16x16x32_bf16 v[32:35], a[200:203], v[56:59], v[32:35]\n\t" \
  "v_max_f32 v192, v192, v203\n\t" \
  "ds_read_b64_tr_b16 a[162:163], v228 offset:4608\n\t" \
  "v_mfma_f32_16x16x32_bf16 v[32:35], a[204:207], v[60:63], v[32:35]\n\t" \
  "v_cmp_lt_f32 vcc, 0x42800000, v192\n\t" \
  "ds_read_b64_tr_b16 a[164:165], v228 offset:8704\n\t" \
  "v_exp_f32 v37, v37\n\t" \
  "v_exp_f32 v38, v38\n\t" \
  "v_exp_f32 v39, v39\n\t" \
  "v_cvt_pk_bf16_f32 v138, v36, v37\n\t" \
  "v_cvt_pk_bf16_f32 v139, v38, v39\n\t" \
  "v_mfma_f32_16x16x32_bf16 v[36:39], a[220:223], v[60:63], v[180:183]\n\t" \
  "s_cbranch_vccnz .Lsf_c10\n\t" \
  ".Lsf_r10:\n\t" \
  "v_max3_f32 v192, v32, v33, v34\n\t" \
  "v_mfma_f32_16x16x32_bf16 v[36:39], a[216:219], v[56:59], v[36:39]\n\t" \
  "v_exp_f32 v40, v40\n\t" \
  "v_mfma_f32_16x16x32_bf16 v[36:39], a[212:215], v[52:55], v[36:39]\n\t" \
  "s_add_u32 s98, %[pf4], 28672\n\t" \
  "buffer_load_dword %[pfd], v245, %[sG], s98 offen\n\t" \
  "v_mfma_f32_16x16x32_bf16 v[36:39], a[208:211], v[48:51], v[36:39]\n\t" \
  "v_exp_f32 v41, v41\n\t" \
  "v_exp_f32 v42, v42\n\t" \
  "v_exp_f32 v43, v43\n\t" \
  "v_cvt_pk_bf16_f32 v140, v40, v41\n\t" \
  "v_cvt_pk_bf16_f32 v141, v42, v43\n\t" \
  "v_mfma_f32_16x16x32_bf16 v[40:43], a[224:227], v[48:51], v[180:183]\n\t" \
  "ds_read_b64_tr_b16 a[166:167], v228 offset:12800\n\t" \
  "ds_read_b64_tr_b16 a[168:169], v229 offset:512\n\t" \
  "v_mfma_f32_16x16x32_bf16 v[40:43], a[228:231], v[52:55], v[40:43]\n\t" \
  "v_exp_f32 v44, v44\n\t" \
  "v_mfma_f32_16x16x32_bf16 v[40:43], a[232:235], v[56:59], v[40:43]\n\t" \
  "v_max3_f32 v203, v35, v36, v37\n\t" \
  "v_max3_f32 v192, v192, v38, v39\n\t" \
  "v_mfma_f32_16x16x32_bf16 v[40:43], a[236:239], v[60:63], v[40:43]\n\t" \
  "v_exp_f32 v45, v45\n\t" \
  "v_exp_f32 v46, v46\n\t" \
  "v_exp_f32 v47, v47\n\t" \
  "v_cvt_pk_bf16_f32 v142, v44, v45\n\t" \
  "v_cvt_pk_bf16_f32 v143, v46, v47\n\t" \
  "v_mfma_f32_16x16x32_bf16 v[44:47], a[252:255], v[60:63], v[180:183]\n\t" \
  "v_exp_f32 v64, v64\n\t" \
  "v_mfma_f32_16x16x32_bf16 v[44:47], a[248:251], v[56:59], v[44:47]\n\t" \
  "ds_read_b64_tr_b16 a[170:171], v229 offset:4608\n\t" \
  "v_max3_f32 v203, v203, v40, v41\n\t" \
  "v_mfma_f32_16x16x32_bf16 v[44:47], a[244:247], v[52:55], v[44:47]\n\t" \
  "v_max3_f32 v192, v192, v42, v43\n\t" \
  "ds_read_b64_tr_b16 a[172:173], v229 offset:8704\n\t" \
  "v_mfma_f32_16x16x32_bf16 v[44:47], a[240:243], v[48:51], v[44:47]\n\t" \
  "v_exp_f32 v65, v65\n\t" \
  "v_exp_f32 v66, v66\n\t" \
  "v_exp_f32 v67, v67\n\t" \
  "v_cvt_pk_bf16_f32 v144, v64, v65\n\t" \
  "v_cvt_pk_bf16_f32 v145, v66, v67\n\t" \
  "v_mfma_f32_16x16x32_bf16 v[64:67], a[192:195], v[80:83], v[184:187]\n\t" \
  "v_exp_f32 v68, v68\n\t" \
  "v_mfma_f32_16x16x32_bf16 v[64:67], a[196:199], v[84:87], v[64:67]\n\t" \
  "v_max3_f32 v203, v203, v44, v45\n\t" \
  "v_max3_f32 v192, v192, v46, v47\n\t" \
  "v_mfma_f32_16x16x32_bf16 v[64:67], a[200:203], v[88:91], v[64:67]\n\t" \
  "v_max_f32 v192, v192, v203\n\t" \
  "ds_read_b64_tr_b16 a[174:175], v229 offset:12800\n\t" \
  "v_mfma_f32_16x16x32_bf16 v[64:67], a[204:207], v[92:95], v[64:67]\n\t" \
  "v_cmp_lt_f32 vcc, 0x42800000, v192\n\t" \
  "ds_read_b64_tr_b16 a[176:177], v228 offset:768\n\t" \
  "v_exp_f32 v69, v69\n\t" \
  "v_exp_f32 v70, v70\n\t" \
  "v_exp_f32 v71, v71\n\t" \
  "v_cvt_pk_bf16_f32 v146, v68, v69\n\t" \
  "v_cvt_pk_bf16_f32 v147, v70, v71\n\t" \
  "v_mfma_f32_16x16x32_bf16 v[68:71], a[220:223], v[92:95], v[184:187]\n\t" \
  "s_cbranch_vccnz .Lsf_c11\n\t" \
  ".Lsf_r11:\n\t" \
  "v_max3_f32 v192, v64, v65, v66\n\t" \
  "v_mfma_f32_16x16x32_bf16 v[68:71], a[216:219], v[88:91], v[68:71]\n\t" \
  "v_exp_f32 v72, v72\n\t" \
  "v_mfma_f32_16x16x32_bf16 v[68:71], a[212:215], v[84:87], v[68:71]\n\t" \
  "v_exp_f32 v73, v73\n\t" \
  "v_mfma_f32_16x16x32_bf16 v[68:71], a[208:211], v[80:83], v[68:71]\n\t" \
  "v_cvt_pk_bf16_f32 v148, v72, v73\n\t" \
  "ds_read_b64_tr_b16 a[178:179], v228 offset:4864\n\t" \
  "v_exp_f32 v74, v74\n\t" \
  "v_exp_f32 v75, v75\n\t" \
  "s_nop 0\n\t" \
  "v_cvt_pk_bf16_f32 v149, v74, v75\n\t" \
  "v_mfma_f32_16x16x32_bf16 v[72:75], a[224:227], v[80:83], v[184:187]\n\t" \
  "v_exp_f32 v76, v76\n\t" \
  "v_mfma_f32_16x16x32_bf16 v[72:75], a[228:231], v[84:87], v[72:75]\n\t" \
  "v_max3_f32 v203, v67, v68, v69\n\t" \
  "v_max3_f32 v192, v192, v70, v71\n\t" \
  "v_mfma_f32_16x16x32_bf16 v[72:75], a[232:235], v[88:91], v[72:75]\n\t" \
  "v_exp_f32 v77, v77\n\t" \
  "v_mfma_f32_16x16x32_bf16 v[72:75], a[236:239], v[92:95], v[72:75]\n\t" \
  "v_cvt_pk_bf16_f32 v150, v76, v77\n\t" \
  "ds_read_b64_tr_b16 a[180:181], v228 offset:8960\n\t" \
  "v_exp_f32 v78, v78\n\t" \
  "v_exp_f32 v79, v79\n\t" \
  "s_nop 0\n\t" \
  "v_cvt_pk_bf16_f32 v151, v78, v79\n\t" \
  "v_mfma_f32_16x16x32_bf16 v[76:79], a[252:255], v[92:95], v[184:187]\n\t" \
  "v_exp_f32 v96, v96\n\t" \
  "v_mfma_f32_16x16x32_bf16 v[76:79], a[248:251], v[88:91], v[76:79]\n\t" \
  "ds_read_b64_tr_b16 a[182:183], v228 offset:13056\n\t" \
  "v_max3_f32 v203, v203, v72, v73\n\t" \
  "v_mfma_f32_16x16x32_bf16 v[76:79], a[244:247], v[84:87], v[76:79]\n\t" \
  "v_max3_f32 v192, v192, v74, v75\n\t" \
  "ds_read_b64_tr_b16 a[184:185], v229 offset:768\n\t" \
  "v_mfma_f32_16x16x32_bf16 v[76:79], a[240:243], v[80:83], v[76:79]\n\t" \
  "v_exp_f32 v97, v97\n\t" \
  "v_exp_f32 v98, v98\n\t" \
  "v_exp_f32 v99, v99\n\t" \
  "v_cvt_pk_bf16_f32 v152, v96, v97\n\t" \
  "v_cvt_pk_bf16_f32 v153, v98, v99\n\t" \
  "v_mfma_f32_16x16x32_bf16 v[96:99], a[192:195], v[112:115], v[188:191]\n\t" \
  "v_exp_f32 v100, v100\n\t" \
  "v_mfma_f32_16x16x32_bf16 v[96:99], a[196:199], v[116:119], v[96:99]\n\t" \
  "v_max3_f32 v203, v203, v76, v77\n\t" \
  "v_max3_f32 v192, v192, v78, v79\n\t" \
  "v_mfma_f32_16x16x32_bf16 v[96:99], a[200:203], v[120:123], v[96:99]\n\t" \
  "v_max_f32 v192, v192, v203\n\t" \
  "ds_read_b64_tr_b16 a[186:187], v229 offset:4864\n\t" \
  "v_mfma_f32_16x16x32_bf16 v[96:99], a[204:207], v[124:127], v[96:99]\n\t" \
  "v_cmp_lt_f32 vcc, 0x42800000, v192\n\t" \
  "ds_read_b64_tr_b16 a[188:189], v229 offset:8960\n\t" \
  "v_exp_f32 v101, v101\n\t" \
  "v_exp_f32 v102, v102\n\t" \
  "v_exp_f32 v103, v103\n\t" \
  "v_cvt_pk_bf16_f32 v154, v100, v101\n\t" \
  "v_cvt_pk_bf16_f32 v155, v102, v103\n\t" \
  "v_mfma_f32_16x16x32_bf16 v[100:103], a[220:223], v[124:127], v[188:191]\n\t" \
  "s_cbranch_vccnz .Lsf_c12\n\t" \
  ".Lsf_r12:\n\t" \
  "v_max3_f32 v192, v96, v97, v98\n\t" \
  "v_mfma_f32_16x16x32_bf16 v[100:103], a[216:219], v[120:123], v[100:103]\n\t" \
  "v_exp_f32 v104, v104\n\t" \
  "v_mfma_f32_16x16x32_bf16 v[100:103], a[212:215], v[116:119], v[100:103]\n\t" \
  "v_exp_f32 v105, v105\n\t" \
  "v_mfma_f32_16x16x32_bf16 v[100:103], a[208:211], v[112:115], v[100:103]\n\t" \
  "v_cvt_pk_bf16_f32 v156, v104, v105\n\t" \
  "ds_read_b64_tr_b16 a[190:191], v229 offset:13056\n\t" \
  "v_exp_f32 v106, v106\n\t" \
  "v_exp_f32 v107, v107\n\t" \
  "s_nop 0\n\t" \
  "v_cvt_pk_bf16_f32 v157, v106, v107\n\t" \
  "v_mfma_f32_16x16x32_bf16 v[104:107], a[224:227], v[112:115], v[188:191]\n\t" \
  "v_exp_f32 v108, v108\n\t" \
  "v_mfma_f32_16x16x32_bf16 v[104:107], a[228:231], v[116:119], v[104:107]\n\t" \
  "v_exp_f32 v109, v109\n\t" \
  "v_mfma_f32_16x16x32_bf16 v[104:107], a[232:235], v[120:123], v[104:107]\n\t" \
  "v_cvt_pk_bf16_f32 v158, v108, v109\n\t" \
  "v_max3_f32 v203, v99, v100, v101\n\t" \
  "v_mfma_f32_16x16x32_bf16 v[104:107], a[236:239], v[124:127], v[104:107]\n\t" \
  "v_max3_f32 v192, v192, v102, v103\n\t" \
  "v_exp_f32 v110, v110\n\t" \
  "v_exp_f32 v111, v111\n\t" \
  "s_nop 0\n\t" \
  "v_cvt_pk_bf16_f32 v159, v110, v111\n\t" \
  "v_mfma_f32_16x16x32_bf16 v[108:111], a[252:255], v[124:127], v[188:191]\n\t" \
  "v_exp_f32 v0, v0\n\t" \
  "v_mfma_f32_16x16x32_bf16 v[108:111], a[248:251], v[120:123], v[108:111]\n\t" \
  "v_max3_f32 v203, v203, v104, v105\n\t" \
  "v_max3_f32 v192, v192, v106, v107\n\t" \
  "v_mfma_f32_16x16x32_bf16 v[108:111], a[244:247], v[116:119], v[108:111]\n\t" \
  "s_add_u32 s98, %[pf4], 43008\n\t" \
  "buffer_load_dword %[pfd], v245, %[sG], s98 offen\n\t" \
  "v_mfma_f32_16x16x32_bf16 v[108:111], a[240:243], v[112:115], v[108:111]\n\t" \
  "v_exp_f32 v1, v1\n\t" \
  "s_waitcnt vmcnt(3) lgkmcnt(0)\n\t" \
  "s_barrier\n\t" \
  "v_mfma_f32_16x16x32_bf16 a[0:3], a[128:131], v[128:131], 0\n\t" \
  "v_exp_f32 v2, v2\n\t" \
  "v_mfma_f32_16x16x32_bf16 a[0:3], a[132:135], v[132:135], a[0:3]\n\t" \
  "v_exp_f32 v3, v3\n\t" \
  "v_mfma_f32_16x16x32_bf16 a[16:19], a[136:139], v[128:131], 0\n\t" \
  "ds_read_b128 a[192:195], v220 offset:0\n\t" \
  "v_max3_f32 v203, v203, v108, v109\n\t" \
  "v_mfma_f32_16x16x32_bf16 a[16:19], a[140:143], v[132:135], a[16:19]\n\t" \
  "v_max3_f32 v192, v192, v110, v111\n\t" \
  "v_max_f32 v192, v192, v203\n\t" \
  "v_mfma_f32_16x16x32_bf16 a[32:35], a[144:147], v[128:131], 0\n\t" \
  "v_cmp_lt_f32 vcc, 0x42800000, v192\n\t" \
  "ds_read_b128 a[196:199], v221 offset:0\n\t" \
  "v_mfma_f32_16x16x32_bf16 a[32:35], a[148:151], v[132:135], a[32:35]\n\t" \
  "s_cbranch_vccnz .Lsf_c13\n\t" \
  ".Lsf_r13:\n\t" \
  "ds_read_b128 a[200:203], v222 offset:0\n\t" \
  "v_mfma_f32_16x16x32_bf16 a[48:51], a[152:155], v[128:131], 0\n\t" \
  "v_exp_f32 v4, v4\n\t" \
  "v_mfma_f32_16x16x32_bf16 a[48:51], a[156:159], v[132:135], a[48:51]\n\t" \
  "v_exp_f32 v5, v5\n\t" \
  "v_mfma_f32_16x16x32_bf16 a[64:67], a[160:163], v[128:131], 0\n\t" \
  "v_exp_f32 v6, v6\n\t" \
  "v_mfma_f32_16x16x32_bf16 a[64:67], a[164:167], v[132:135], a[64:67]\n\t" \
  "v_exp_f32 v7, v7\n\t" \
  "v_mfma_f32_16x16x32_bf16 a[80:83], a[168:171], v[128:131], 0\n\t" \
  "v_exp_f32 v8, v8\n\t" \
  "v_mfma_f32_16x16x32_bf16 a[80:83], a[172:175], v[132:135], a[80:83]\n\t" \
  "v_exp_f32 v9, v9\n\t" \
  "v_mfma_f32_16x16x32_bf16 a[96:99], a[176:179], v[128:131], 0\n\t" \
  "v_exp_f32 v10, v10\n\t" \
  "v_mfma_f32_16x16x32_bf16 a[96:99], a[180:183], v[132:135], a[96:99]\n\t" \
  "v_exp_f32 v11, v11\n\t" \
  "v_mfma_f32_16x16x32_bf16 a[112:115], a[184:187], v[128:131], 0\n\t" \
  "ds_read_b128 a[204:207], v223 offset:0\n\t" \
  "ds_read_b128 a[208:211], v220 offset:4096\n\t" \
  "v_mfma_f32_16x16x32_bf16 a[112:115], a[188:191], v[132:135], a[112:115]\n\t" \
  "v_exp_f32 v12, v12\n\t" \
  "v_mfma_f32_16x16x32_bf16 v[160:163], v[216:219], v[128:131], 0\n\t" \
  "v_exp_f32 v13, v13\n\t" \
  "v_mfma_f32_16x16x32_bf16 v[160:163], v[216:219], v[132:135], v[160:163]\n\t" \
  "v_cvt_pk_bf16_f32 v128, v0, v1\n\t" \
  "v_cvt_pk_bf16_f32 v129, v2, v3\n\t" \
  "v_mfma_f32_16x16x32_bf16 a[4:7], a[128:131], v[136:139], 0\n\t" \
  "v_cvt_pk_bf16_f32 v130, v4, v5\n\t" \
  "v_cvt_pk_bf16_f32 v131, v6, v7\n\t" \
  "v_mfma_f32_16x16x32_bf16 a[4:7], a[132:135], v[140:143], a[4:7]\n\t" \
  "v_cvt_pk_bf16_f32 v132, v8, v9\n\t" \
  "v_cvt_pk_bf16_f32 v133, v10, v11\n\t" \
  "v_mfma_f32_16x16x32_bf16 a[20:23], a[136:139], v[136:139], 0\n\t" \
  "v_cvt_pk_bf16_f32 v134, v12, v13\n\t" \
  "ds_read_b128 a[212:215], v221 offset:4096\n\t" \
  "v_mfma_f32_16x16x32_bf16 a[20:23], a[140:143], v[140:143], a[20:23]\n\t" \
  "v_exp_f32 v14, v14\n\t" \
  "v_mfma_f32_16x16x32_bf16 a[36:39], a[144:147], v[136:139], 0\n\t" \
  "s_add_u32 m0, %[m], 65536\n\t" \
  "ds_read_b128 a[216:219], v222 offset:4096\n\t" \
  "v_mfma_f32_16x16x32_bf16 a[36:39], a[148:151], v[140:143], a[36:39]\n\t" \
  "v_exp_f32 v15, v15\n\t" \
  "v_mfma_f32_16x16x32_bf16 a[52:55], a[152:155], v[136:139], 0\n\t" \
  "v_cvt_pk_bf16_f32 v135, v14, v15\n\t" \
  "s_add_u32 s96, %[sofk], 49152\n\t" \
  "v_mfma_f32_16x16x32_bf16 a[52:55], a[156:159], v[140:143], a[52:55]\n\t" \
  "v_exp_f32 v32, v32\n\t" \
  "v_mfma_f32_16x16x32_bf16 a[68:71], a[160:163], v[136:139], 0\n\t" \
  "v_exp_f32 v33, v33\n\t" \
  "v_mfma_f32_16x16x32_bf16 a[68:71], a[164:167], v[140:143], a[68:71]\n\t" \
  "ds_read_b128 a[220:223], v223 offset:4096\n\t" \
  "buffer_load_dwordx4 v224, %[sK], s96 offen offset:0 lds\n\t" \
  "v_mfma_f32_16x16x32_bf16 a[84:87], a[168:171], v[136:139], 0\n\t" \
  "v_exp_f32 v34, v34\n\t" \
  "v_mfma_f32_16x16x32_bf16 a[84:87], a[172:175], v[140:143], a[84:87]\n\t" \
  "v_exp_f32 v35, v35\n\t" \
  "v_mfma_f32_16x16x32_bf16 a[100:103], a[176:179], v[136:139], 0\n\t" \
  "v_exp_f32 v36, v36\n\t" \
  "v_mfma_f32_16x16x32_bf16 a[100:103], a[180:183], v[140:143], a[100:103]\n\t" \
  "ds_read_b128 a[224:227], v220 offset:8192\n\t" \
  "buffer_load_dwordx4 v225, %[sK], s96 offen offset:1024 lds\n\t" \
  "v_mfma_f32_16x16x32_bf16 a[116:119], a[184:187], v[136:139], 0\n\t" \
  "v_exp_f32 v37, v37\n\t" \
  "v_mfma_f32_16x16x32_bf16 a[116:119], a[188:191], v[140:143], a[116:119]\n\t" \
  "v_exp_f32 v38, v38\n\t" \
  "v_mfma_f32_16x16x32_bf16 v[164:167], v[216:219], v[136:139], 0\n\t" \
  "buffer_load_dwordx4 v226, %[sK], s96 offen offset:2048 lds\n\t" \
  "ds_read_b128 a[228:231], v221 offset:8192\n\t" \
  "v_mfma_f32_16x16x32_bf16 v[164:167], v[216:219], v[140:143], v[164:167]\n\t" \
  "v_cvt_pk_bf16_f32 v136, v32, v33\n\t" \
  "v_cvt_pk_bf16_f32 v137, v34, v35\n\t" \
  "v_mfma_f32_16x16x32_bf16 a[8:11], a[128:131], v[144:147], 0\n\t" \
  "v_cvt_pk_bf16_f32 v138, v36, v37\n\t" \
  "buffer_load_dwordx4 v227, %[sK], s96 offen offset:3072 lds\n\t" \
  "v_mfma_f32_16x16x32_bf16 a[8:11], a[132:135], v[148:151], a[8:11]\n\t" \
  "v_exp_f32 v39, v39\n\t" \
  "v_mfma_f32_16x16x32_bf16 a[24:27], a[136:139], v[144:147], 0\n\t" \
  "v_cvt_pk_bf16_f32 v139, v38, v39\n\t" \
  "ds_read_b128 a[232:235], v222 offset:8192\n\t" \
  "v_mfma_f32_16x16x32_bf16 a[24:27], a[140:143], v[148:151], a[24:27]\n\t" \
  "v_exp_f32 v40, v40\n\t" \
  "v_mfma_f32_16x16x32_bf16 a[40:43], a[144:147], v[144:147], 0\n\t" \
  "v_exp_f32 v41, v41\n\t" \
  "v_mfma_f32_16x16x32_bf16 a[40:43], a[148:151], v[148:151], a[40:43]\n\t" \
  "v_cvt_pk_bf16_f32 v140, v40, v41\n\t" \
  "s_add_u32 m0, %[m], 81920\n\t" \
  "v_mfma_f32_16x16x32_bf16 a[56:59], a[152:155], v[144:147], 0\n\t" \
  "v_exp_f32 v42, v42\n\t" \
  "v_mfma_f32_16x16x32_bf16 a[56:59], a[156:159], v[148:151], a[56:59]\n\t" \
  "v_exp_f32 v43, v43\n\t" \
  "v_mfma_f32_16x16x32_bf16 a[72:75], a[160:163], v[144:147], 0\n\t" \
  "v_cvt_pk_bf16_f32 v141, v42, v43\n\t" \
  "ds_read_b128 a[236:239], v223 offset:8192\n\t" \
  "v_mfma_f32_16x16x32_bf16 a[72:75], a[164:167], v[148:151], a[72:75]\n\t" \
  "s_add_u32 s96, %[sofk], 32768\n\t" \
  "ds_read_b128 a[240:243], v220 offset:12288\n\t" \
  "v_mfma_f32_16x16x32_bf16 a[88:91], a[168:171], v[144:147], 0\n\t" \
  "v_exp_f32 v44, v44\n\t" \
  "v_mfma_f32_16x16x32_bf16 a[88:91], a[172:175], v[148:151], a[88:91]\n\t" \
  "v_exp_f32 v45, v45\n\t" \
  "v_mfma_f32_16x16x32_bf16 a[104:107], a[176:179], v[144:147], 0\n\t" \
  "v_cvt_pk_bf16_f32 v142, v44, v45\n\t" \
  "buffer_load_dwordx4 v230, %[sV], s96 offen offset:0 lds\n\t" \
  "v_mfma_f32_16x16x32_bf16 a[104:107], a[180:183], v[148:151], a[104:107]\n\t" \
  "v_exp_f32 v46, v46\n\t" \
  "v_mfma_f32_16x16x32_bf16 a[120:123], a[184:187], v[144:147], 0\n\t" \
  "v_exp_f32 v47, v47\n\t" \
  "v_mfma_f32_16x16x32_bf16 a[120:123], a[188:191], v[148:151], a[120:123]\n\t" \
  "v_cvt_pk_bf16_f32 v143, v46, v47\n\t" \
  "ds_read_b128 a[244:247], v221 offset:12288\n\t" \
  "v_mfma_f32_16x16x32_bf16 v[168:171], v[216:219], v[144:147], 0\n\t" \
  "v_exp_f32 v64, v64\n\t" \
  "v_mfma_f32_16x16x32_bf16 v[168:171], v[216:219], v[148:151], v[168:171]\n\t" \
  "buffer_load_dwordx4 v231, %[sV], s96 offen offset:1024 lds\n\t" \
  "ds_read_b128 a[248:251], v222 offset:12288\n\t" \
  "v_mfma_f32_16x16x32_bf16 a[12:15], a[128:131], v[152:155], 0\n\t" \
  "v_exp_f32 v65, v65\n\t" \
  "v_mfma_f32_16x16x32_bf16 a[12:15], a[132:135], v[156:159], a[12:15]\n\t" \
  "v_cvt_pk_bf16_f32 v144, v64, v65\n\t" \
  "buffer_load_dwordx4 v230, %[sV], s96 offen offset:2048 lds\n\t" \
  "v_mfma_f32_16x16x32_bf16 a[28:31], a[136:139], v[152:155], 0\n\t" \
  "v_exp_f32 v66, v66\n\t" \
  "v_mfma_f32_16x16x32_bf16 a[28:31], a[140:143], v[156:159], a[28:31]\n\t" \
  "v_exp_f32 v67, v67\n\t" \
  "v_mfma_f32_16x16x32_bf16 a[44:47], a[144:147], v[152:155], 0\n\t" \
  "v_cvt_pk_bf16_f32 v145, v66, v67\n\t" \
  "ds_read_b128 a[252:255], v223 offset:12288\n\t" \
  "v_mfma_f32_16x16x32_bf16 a[44:47], a[148:151], v[156:159], a[44:47]\n\t" \
  "v_exp_f32 v68, v68\n\t" \
  "v_mfma_f32_16x16x32_bf16 a[60:63], a[152:155], v[152:155], 0\n\t" \
  "buffer_load_dwordx4 v231, %[sV], s96 offen offset:3072 lds\n\t" \
  "v_mfma_f32_16x16x32_bf16 a[60:63], a[156:159], v[156:159], a[60:63]\n\t" \
  "v_exp_f32 v69, v69\n\t" \
  "v_mfma_f32_16x16x32_bf16 a[76:79], a[160:163], v[152:155], 0\n\t" \
  "v_cvt_pk_bf16_f32 v146, v68, v69\n\t" \
  "v_mfma_f32_16x16x32_bf16 a[76:79], a[164:167], v[156:159], a[76:79]\n\t" \
  "v_exp_f32 v70, v70\n\t" \
  "v_mfma_f32_16x16x32_bf16 a[92:95], a[168:171], v[152:155], 0\n\t" \
  "s_add_u32 m0, %[m], 114688\n\t" \
  "s_mov_b32 s96, 0\n\t" \
  "buffer_load_dwordx4 v232, %[sU], s96 offen offset:0 lds\n\t" \
  "v_mfma_f32_16x16x32_bf16 a[92:95], a[172:175], v[156:159], a[92:95]\n\t" \
  "v_exp_f32 v71, v71\n\t" \
  "v_mfma_f32_16x16x32_bf16 a[108:111], a[176:179], v[152:155], 0\n\t" \
  "v_cvt_pk_bf16_f32 v147, v70, v71\n\t" \
  "v_mfma_f32_16x16x32_bf16 a[108:111], a[180:183], v[156:159], a[108:111]\n\t" \
  "s_mov_b32 s96, 6144\n\t" \
  "buffer_load_dwordx4 v232, %[sU], s96 offen offset:1024 lds\n\t" \
  "v_mfma_f32_16x16x32_bf16 a[124:127], a[184:187], v[152:155], 0\n\t" \
  "v_exp_f32 v72, v72\n\t" \
  "v_mfma_f32_16x16x32_bf16 a[124:127], a[188:191], v[156:159], a[124:127]\n\t" \
  "v_exp_f32 v73, v73\n\t" \
  "v_mfma_f32_16x16x32_bf16 v[172:175], v[216:219], v[152:155], 0\n\t" \
  "v_cvt_pk_bf16_f32 v148, v72, v73\n\t" \
  "v_mfma_f32_16x16x32_bf16 v[172:175], v[216:219], v[156:159], v[172:175]\n\t" \
  "v_exp_f32 v74, v74\n\t" \
  "s_cmp_lg_u32 s99, 0\n\t" \
  "s_cbranch_scc1 .Lsf_p1\n\t" \
  ".Lsf_q1:\n\t" \
  "s_waitcnt lgkmcnt(0)\n\t" \
  "v_mfma_f32_16x16x32_bf16 v[0:3], a[192:195], v[16:19], v[176:179]\n\t" \
  "v_exp_f32 v75, v75\n\t" \
  "v_mfma_f32_16x16x32_bf16 v[0:3], a[196:199], v[20:23], v[0:3]\n\t" \
  "v_cvt_pk_bf16_f32 v149, v74, v75\n\t" \
  "ds_read_b64_tr_b16 a[128:129], v237 offset:0\n\t" \
  "v_mfma_f32_16x16x32_bf16 v[0:3], a[200:203], v[24:27], v[0:3]\n\t" \
  "v_exp_f32 v76, v76\n\t" \
  "v_mfma_f32_16x16x32_bf16 v[0:3], a[204:207], v[28:31], v[0:3]\n\t" \
  "v_exp_f32 v77, v77\n\t" \
  "v_mfma_f32_16x16x32_bf16 v[4:7], a[220:223], v[28:31], v[176:179]\n\t" \
  "v_cvt_pk_bf16_f32 v150, v76, v77\n\t" \
  "ds_read_b64_tr_b16 a[130:131], v237 offset:4096\n\t" \
  "v_mfma_f32_16x16x32_bf16 v[4:7], a[216:219], v[24:27], v[4:7]\n\t" \
  "v_exp_f32 v78, v78\n\t" \
  "v_mfma_f32_16x16x32_bf16 v[4:7], a[212:215], v[20:23], v[4:7]\n\t" \
  "ds_read_b64_tr_b16 a[132:133], v237 offset:8192\n\t" \
  "v_max3_f32 v192, v0, v1, v2\n\t" \
  "v_mfma_f32_16x16x32_bf16 v[4:7], a[208:211], v[16:19], v[4:7]\n\t" \
  "v_exp_f32 v79, v79\n\t" \
  "v_mfma_f32_16x16x32_bf16 v[8:11], a[224:227], v[16:19], v[176:179]\n\t" \
  "v_cvt_pk_bf16_f32 v151, v78, v79\n\t" \
  "ds_read_b64_tr_b16 a[134:135], v237 offset:12288\n\t" \
  "v_mfma_f32_16x16x32_bf16 v[8:11], a[228:231], v[20:23], v[8:11]\n\t" \
  "ds_read_b64_tr_b16 a[136:137], v238 offset:0\n\t" \
  "ds_read_b64_tr_b16 a[138:139], v238 offset:4096\n\t" \
  "v_mfma_f32_16x16x32_bf16 v[8:11], a[232:235], v[24:27], v[8:11]\n\t" \
  "v_max3_f32 v203, v3, v4, v5\n\t" \
  "v_max3_f32 v192, v192, v6, v7\n\t" \
  "v_mfma_f32_16x16x32_bf16 v[8:11], a[236:239], v[28:31], v[8:11]\n\t" \
  "v_exp_f32 v96, v96\n\t" \
  "v_mfma_f32_16x16x32_bf16 v[12:15], a[252:255], v[28:31], v[176:179]\n\t" \
  "v_exp_f32 v97, v97\n\t" \
  "v_mfma_f32_16x16x32_bf16 v[12:15], a[248:251], v[24:27], v[12:15]\n\t" \
  "v_cvt_pk_bf16_f32 v152, v96, v97\n\t" \
  "ds_read_b64_tr_b16 a[140:141], v238 offset:8192\n\t" \
  "v_mfma_f32_16x16x32_bf16 v[12:15], a[244:247], v[20:23], v[12:15]\n\t" \
  "v_exp_f32 v98, v98\n\t" \
  "v_mfma_f32_16x16x32_bf16 v[12:15], a[240:243], v[16:19], v[12:15]\n\t" \
  "v_max3_f32 v203, v203, v8, v9\n\t" \
  "v_max3_f32 v192, v192, v10, v11\n\t" \
  "v_mfma_f32_16x16x32_bf16 v[32:35], a[192:195], v[48:51], v[180:183]\n\t" \
  "ds_read_b64_tr_b16 a[142:143], v238 offset:12288\n\t" \
  "ds_read_b64_tr_b16 a[144:145], v237 offset:256\n\t" \
  "v_mfma_f32_16x16x32_bf16 v[32:35], a[196:199], v[52:55], v[32:35]\n\t" \
  "v_exp_f32 v99, v99\n\t" \
  "v_mfma_f32_16x16x32_bf16 v[32:35], a[200:203], v[56:59], v[32:35]\n\t" \
  "v_cvt_pk_bf16_f32 v153, v98, v99\n\t" \
  "ds_read_b64_tr_b16 a[146:147], v237 offset:4352\n\t" \
  "v_mfma_f32_16x16x32_bf16 v[32:35], a[204:207], v[60:63], v[32:35]\n\t" \
  "v_max3_f32 v203, v203, v12, v13\n\t" \
  "v_max3_f32 v192, v192, v14, v15\n\t" \
  "v_mfma_f32_16x16x32_bf16 v[36:39], a[220:223], v[60:63], v[180:183]\n\t" \
  "v_max_f32 v192, v192, v203\n\t" \
  "ds_read_b64_tr_b16 a[148:149], v237 offset:8448\n\t" \
  "v_mfma_f32_16x16x32_bf16 v[36:39], a[216:219], v[56:59], v[36:39]\n\t" \
  "v_cmp_lt_f32 vcc, 0x42800000, v192\n\t" \
  "ds_read_b64_tr_b16 a[150:151], v237 offset:12544\n\t" \
  "v_mfma_f32_16x16x32_bf16 v[36:39], a[212:215], v[52:55], v[36:39]\n\t" \
  "s_cbranch_vccnz .Lsf_c20\n\t" \
  ".Lsf_r20:\n\t" \
  "v_max3_f32 v192, v32, v33, v34\n\t" \
  "v_mfma_f32_16x16x32_bf16 v[36:39], a[208:211], v[48:51], v[36:39]\n\t" \
  "s_add_u32 m0, %[m], 114688\n\t" \
  "s_mov_b32 s96, 12288\n\t" \
  "buffer_load_dwordx4 v232, %[sU], s96 offen offset:2048 lds\n\t" \
  "v_mfma_f32_16x16x32_bf16 v[40:43], a[224:227], v[48:51], v[180:183]\n\t" \
  "ds_read_b64_tr_b16 a[152:153], v238 offset:256\n\t" \
  "ds_read_b64_tr_b16 a[154:155], v238 offset:4352\n\t" \
  "v_mfma_f32_16x16x32_bf16 v[40:43], a[228:231], v[52:55], v[40:43]\n\t" \
  "v_exp_f32 v100, v100\n\t" \
  "v_mfma_f32_16x16x32_bf16 v[40:43], a[232:235], v[56:59], v[40:43]\n\t" \
  "v_max3_f32 v203, v35, v36, v37\n\t" \
  "v_max3_f32 v192, v192, v38, v39\n\t" \
  "v_mfma_f32_16x16x32_bf16 v[40:43], a[236:239], v[60:63], v[40:43]\n\t" \
  "ds_read_b64_tr_b16 a[156:157], v238 offset:8448\n\t" \
  "ds_read_b64_tr_b16 a[158:159], v238 offset:12544\n\t" \
  "v_mfma_f32_16x16x32_bf16 v[44:47], a[252:255], v[60:63], v[180:183]\n\t" \
  "v_exp_f32 v101, v101\n\t" \
  "v_mfma_f32_16x16x32_bf16 v[44:47], a[248:251], v[56:59], v[44:47]\n\t" \
  "v_cvt_pk_bf16_f32 v154, v100, v101\n\t" \
  "ds_read_b64_tr_b16 a[160:161], v237 offset:512\n\t" \
  "v_mfma_f32_16x16x32_bf16 v[44:47], a[244:247], v[52:55], v[44:47]\n\t" \
  "v_max3_f32 v203, v203, v40, v41\n\t" \
  "v_max3_f32 v192, v192, v42, v43\n\t" \
  "v_mfma_f32_16x16x32_bf16 v[44:47], a[240:243], v[48:51], v[44:47]\n\t" \
  "ds_read_b64_tr_b16 a[162:163], v237 offset:4608\n\t" \
  "ds_read_b64_tr_b16 a[164:165], v237 offset:8704\n\t" \
  "v_mfma_f32_16x16x32_bf16 v[64:67], a[192:195], v[80:83], v[184:187]\n\t" \
  "s_mov_b32 s96, 18432\n\t" \
  "buffer_load_dwordx4 v232, %[sU], s96 offen offset:3072 lds\n\t" \
  "v_mfma_f32_16x16x32_bf16 v[64:67], a[196:199], v[84:87], v[64:67]\n\t" \
  "v_exp_f32 v102, v102\n\t" \
  "v_mfma_f32_16x16x32_bf16 v[64:67], a[200:203], v[88:91], v[64:67]\n\t" \
  "v_max3_f32 v203, v203, v44, v45\n\t" \
  "v_max3_f32 v192, v192, v46, v47\n\t" \
  "v_mfma_f32_16x16x32_bf16 v[64:67], a[204:207], v[92:95], v[64:67]\n\t" \
  "v_max_f32 v192, v192, v203\n\t" \
  "ds_read_b64_tr_b16 a[166:167], v237 offset:12800\n\t" \
  "v_mfma_f32_16x16x32_bf16 v[68:71], a[220:223], v[92:95], v[184:187]\n\t" \
  "v_cmp_lt_f32 vcc, 0x42800000, v192\n\t" \
  "ds_read_b64_tr_b16 a[168:169], v238 offset:512\n\t" \
  "v_mfma_f32_16x16x32_bf16 v[68:71], a[216:219], v[88:91], v[68:71]\n\t" \
  "s_cbranch_vccnz .Lsf_c21\n\t" \
  ".Lsf_r21:\n\t" \
  "ds_read_b64_tr_b16 a[170:171], v238 offset:4608\n\t" \
  "v_mfma_f32_16x16x32_bf16 v[68:71], a[212:215], v[84:87], v[68:71]\n\t" \
  "v_max3_f32 v192, v64, v65, v66\n\t" \
  "ds_read_b64_tr_b16 a[172:173], v238 offset:8704\n\t" \
  "v_mfma_f32_16x16x32_bf16 v[68:71], a[208:211], v[80:83], v[68:71]\n\t" \
  "v_exp_f32 v103, v103\n\t" \
  "v_mfma_f32_16x16x32_bf16 v[72:75], a[224:227], v[80:83], v[184:187]\n\t" \
  "v_cvt_pk_bf16_f32 v155, v102, v103\n\t" \
  "ds_read_b64_tr_b16 a[174:175], v238 offset:12800\n\t" \
  "v_mfma_f32_16x16x32_bf16 v[72:75], a[228:231], v[84:87], v[72:75]\n\t" \
  "v_exp_f32 v104, v104\n\t" \
  "v_mfma_f32_16x16x32_bf16 v[72:75], a[232:235], v[88:91], v[72:75]\n\t" \
  "s_add_u32 m0, %[m], 131072\n\t" \
  "s_mov_b32 s96, 28672\n\t" \
  "buffer_load_dwordx4 v232, %[sU], s96 offen offset:0 lds\n\t" \
  "v_mfma_f32_16x16x32_bf16 v[72:75], a[236:239], v[92:95], v[72:75]\n\t" \
  "v_max3_f32 v203, v67, v68, v69\n\t" \
  "v_max3_f32 v192, v192, v70, v71\n\t" \
  "v_mfma_f32_16x16x32_bf16 v[76:79], a[252:255], v[92:95], v[184:187]\n\t" \
  "ds_read_b64_tr_b16 a[176:177], v237 offset:768\n\t" \
  "ds_read_b64_tr_b16 a[178:179], v237 offset:4864\n\t" \
  "v_mfma_f32_16x16x32_bf16 v[76:79], a[248:251], v[88:91], v[76:79]\n\t" \
  "v_exp_f32 v105, v105\n\t" \
  "v_mfma_f32_16x16x32_bf16 v[76:79], a[244:247], v[84:87], v[76:79]\n\t" \
  "v_cvt_pk_bf16_f32 v156, v104, v105\n\t" \
  "v_max3_f32 v203, v203, v72, v73\n\t" \
  "v_mfma_f32_16x16x32_bf16 v[76:79], a[240:243], v[80:83], v[76:79]\n\t" \
  "v_max3_f32 v192, v192, v74, v75\n\t" \
  "ds_read_b64_tr_b16 a[180:181], v237 offset:8960\n\t" \
  "v_mfma_f32_16x16x32_bf16 v[96:99], a[192:195], v[112:115], v[188:191]\n\t" \
  "v_exp_f32 v106, v106\n\t" \
  "v_mfma_f32_16x16x32_bf16 v[96:99], a[196:199], v[116:119], v[96:99]\n\t" \
  "v_exp_f32 v107, v107\n\t" \
  "v_mfma_f32_16x16x32_bf16 v[96:99], a[200:203], v[120:123], v[96:99]\n\t" \
  "v_cvt_pk_bf16_f32 v157, v106, v107\n\t" \
  "ds_read_b64_tr_b16 a[182:183], v237 offset:13056\n\t" \
  "v_mfma_f32_16x16x32_bf16 v[96:99], a[204:207], v[124:127], v[96:99]\n\t" \
  "v_max3_f32 v203, v203, v76, v77\n\t" \
  "v_max3_f32 v192, v192, v78, v79\n\t" \
  "v_mfma_f32_16x16x32_bf16 v[100:103], a[220:223], v[124:127], v[188:191]\n\t" \
  "v_max_f32 v192, v192, v203\n\t" \
  "ds_read_b64_tr_b16 a[184:185], v238 offset:768\n\t" \
  "v_mfma_f32_16x16x32_bf16 v[100:103], a[216:219], v[120:123], v[100:103]\n\t" \
  "v_cmp_lt_f32 vcc, 0x42800000, v192\n\t" \
  "ds_read_b64_tr_b16 a[186:187], v238 offset:4864\n\t" \
  "v_mfma_f32_16x16x32_bf16 v[100:103], a[212:215], v[116:119], v[100:103]\n\t" \
  "s_cbranch_vccnz .Lsf_c22\n\t" \
  ".Lsf_r22:\n\t" \
  "v_max3_f32 v192, v96, v97, v98\n\t" \
  "v_mfma_f32_16x16x32_bf16 v[100:103], a[208:211], v[112:115], v[100:103]\n\t" \
  "s_mov_b32 s96, 34816\n\t" \
  "buffer_load_dwordx4 v232, %[sU], s96 offen offset:1024 lds\n\t" \
  "v_mfma_f32_16x16x32_bf16 v[104:107], a[224:227], v[112:115], v[188:191]\n\t" \
  "ds_read_b64_tr_b16 a[188:189], v238 offset:8960\n\t" \
  "ds_read_b64_tr_b16 a[190:191], v238 offset:13056\n\t" \
  "v_mfma_f32_16x16x32_bf16 v[104:107], a[228:231], v[116:119], v[104:107]\n\t" \
  "v_exp_f32 v108, v108\n\t" \
  "v_mfma_f32_16x16x32_bf16 v[104:107], a[232:235], v[120:123], v[104:107]\n\t" \
  "v_max3_f32 v203, v99, v100, v101\n\t" \
  "v_max3_f32 v192, v192, v102, v103\n\t" \
  "v_mfma_f32_16x16x32_bf16 v[104:107], a[236:239], v[124:127], v[104:107]\n\t" \
  "v_exp_f32 v109, v109\n\t" \
  "v_exp_f32 v110, v110\n\t" \
  "v_exp_f32 v111, v111\n\t" \
  "v_cvt_pk_bf16_f32 v158, v108, v109\n\t" \
  "v_cvt_pk_bf16_f32 v159, v110, v111\n\t" \
  "v_mfma_f32_16x16x32_bf16 v[108:111], a[252:255], v[124:127], v[188:191]\n\t" \
  "v_exp_f32 v0, v0\n\t" \
  "v_mfma_f32_16x16x32_bf16 v[108:111], a[248:251], v[120:123], v[108:111]\n\t" \
  "v_exp_f32 v1, v1\n\t" \
  "v_mfma_f32_16x16x32_bf16 v[108:111], a[244:247], v[116:119], v[108:111]\n\t" \
  "v_max3_f32 v203, v203, v104, v105\n\t" \
  "v_max3_f32 v192, v192, v106, v107\n\t" \
  "v_mfma_f32_16x16x32_bf16 v[108:111], a[240:243], v[112:115], v[108:111]\n\t" \
  "v_exp_f32 v2, v2\n\t" \
  "s_waitcnt vmcnt(6) lgkmcnt(0)\n\t" \
  "s_barrier\n\t" \
  "v_mfma_f32_16x16x32_bf16 a[0:3], a[128:131], v[128:131], a[0:3]\n\t" \
  "v_exp_f32 v3, v3\n\t" \
  "v_mfma_f32_16x16x32_bf16 a[0:3], a[132:135], v[132:135], a[0:3]\n\t" \
  "v_exp_f32 v4, v4\n\t" \
  "v_mfma_f32_16x16x32_bf16 a[16:19], a[136:139], v[128:131], a[16:19]\n\t" \
  "ds_read_b128 a[192:195], v220 offset:16384\n\t" \
  "v_max3_f32 v203, v203, v108, v109\n\t" \
  "v_mfma_f32_16x16x32_bf16 a[16:19], a[140:143], v[132:135], a[16:19]\n\t" \
  "v_max3_f32 v192, v192, v110, v111\n\t" \
  "v_max_f32 v192, v192, v203\n\t" \
  "v_mfma_f32_16x16x32_bf16 a[32:35], a[144:147], v[128:131], a[32:35]\n\t" \
  "v_cmp_lt_f32 vcc, 0x42800000, v192\n\t" \
  "ds_read_b128 a[196:199], v221 offset:16384\n\t" \
  "v_mfma_f32_16x16x32_bf16 a[32:35], a[148:151], v[132:135], a[32:35]\n\t" \
  "s_cbranch_vccnz .Lsf_c23\n\t" \
  ".Lsf_r23:\n\t" \
  "ds_read_b128 a[200:203], v222 offset:16384\n\t" \
  "v_mfma_f32_16x16x32_bf16 a[48:51], a[152:155], v[128:131], a[48:51]\n\t" \
  "v_exp_f32 v5, v5\n\t" \
  "v_mfma_f32_16x16x32_bf16 a[48:51], a[156:159], v[132:135], a[48:51]\n\t" \
  "v_exp_f32 v6, v6\n\t" \
  "v_mfma_f32_16x16x32_bf16 a[64:67], a[160:163], v[128:131], a[64:67]\n\t" \
  "v_exp_f32 v7, v7\n\t" \
  "v_mfma_f32_16x16x32_bf16 a[64:67], a[164:167], v[132:135], a[64:67]\n\t" \
  "v_exp_f32 v8, v8\n\t" \
  "v_mfma_f32_16x16x32_bf16 a[80:83], a[168:171], v[128:131], a[80:83]\n\t" \
  "v_exp_f32 v9, v9\n\t" \
  "v_mfma_f32_16x16x32_bf16 a[80:83], a[172:175], v[132:135], a[80:83]\n\t" \
  "v_exp_f32 v10, v10\n\t" \
  "v_mfma_f32_16x16x32_bf16 a[96:99], a[176:179], v[128:131], a[96:99]\n\t" \
  "v_exp_f32 v11, v11\n\t" \
  "v_mfma_f32_16x16x32_bf16 a[96:99], a[180:183], v[132:135], a[96:99]\n\t" \
  "v_exp_f32 v12, v12\n\t" \
  "v_mfma_f32_16x16x32_bf16 a[112:115], a[184:187], v[128:131], a[112:115]\n\t" \
  "ds_read_b128 a[204:207], v223 offset:16384\n\t" \
  "ds_read_b128 a[208:211], v220 offset:20480\n\t" \
  "v_mfma_f32_16x16x32_bf16 a[112:115], a[188:191], v[132:135], a[112:115]\n\t" \
  "v_exp_f32 v13, v13\n\t" \
  "v_mfma_f32_16x16x32_bf16 v[160:163], v[216:219], v[128:131], v[160:163]\n\t" \
  "v_exp_f32 v14, v14\n\t" \
  "v_mfma_f32_16x16x32_bf16 v[160:163], v[216:219], v[132:135], v[160:163]\n\t" \
  "v_cvt_pk_bf16_f32 v128, v0, v1\n\t" \
  "v_cvt_pk_bf16_f32 v129, v2, v3\n\t" \
  "v_mfma_f32_16x16x32_bf16 a[4:7], a[128:131], v[136:139], a[4:7]\n\t" \
  "v_cvt_pk_bf16_f32 v130, v4, v5\n\t" \
  "v_cvt_pk_bf16_f32 v131, v6, v7\n\t" \
  "v_mfma_f32_16x16x32_bf16 a[4:7], a[132:135], v[140:143], a[4:7]\n\t" \
  "v_cvt_pk_bf16_f32 v132, v8, v9\n\t" \
  "v_cvt_pk_bf16_f32 v133, v10, v11\n\t" \
  "v_mfma_f32_16x16x32_bf16 a[20:23], a[136:139], v[136:139], a[20:23]\n\t" \
  "v_cvt_pk_bf16_f32 v134, v12, v13\n\t" \
  "ds_read_b128 a[212:215], v221 offset:20480\n\t" \
  "v_mfma_f32_16x16x32_bf16 a[20:23], a[140:143], v[140:143], a[20:23]\n\t" \
  "v_exp_f32 v15, v15\n\t" \
  "v_mfma_f32_16x16x32_bf16 a[36:39], a[144:147], v[136:139], a[36:39]\n\t" \
  "v_cvt_pk_bf16_f32 v135, v14, v15\n\t" \
  "s_add_u32 m0, %[m], 49152\n\t" \
  "v_mfma_f32_16x16x32_bf16 a[36:39], a[148:151], v[140:143], a[36:39]\n\t" \
  "v_exp_f32 v32, v32\n\t" \
  "v_mfma_f32_16x16x32_bf16 a[52:55], a[152:155], v[136:139], a[52:55]\n\t" \
  "ds_read_b128 a[216:219], v222 offset:20480\n\t" \
  "s_add_u32 s96, %[sofk], 65536\n\t" \
  "v_mfma_f32_16x16x32_bf16 a[52:55], a[156:159], v[140:143], a[52:55]\n\t" \
  "v_exp_f32 v33, v33\n\t" \
  "v_mfma_f32_16x16x32_bf16 a[68:71], a[160:163], v[136:139], a[68:71]\n\t" \
  "v_exp_f32 v34, v34\n\t" \
  "v_mfma_f32_16x16x32_bf16 a[68:71], a[164:167], v[140:143], a[68:71]\n\t" \
  "ds_read_b128 a[220:223], v223 offset:20480\n\t" \
  "buffer_load_dwordx4 v224, %[sK], s96 offen offset:0 lds\n\t" \
  "v_mfma_f32_16x16x32_bf16 a[84:87], a[168:171], v[136:139], a[84:87]\n\t" \
  "v_exp_f32 v35, v35\n\t" \
  "v_mfma_f32_16x16x32_bf16 a[84:87], a[172:175], v[140:143], a[84:87]\n\t" \
  "v_exp_f32 v36, v36\n\t" \
  "v_mfma_f32_16x16x32_bf16 a[100:103], a[176:179], v[136:139], a[100:103]\n\t" \
  "v_exp_f32 v37, v37\n\t" \
  "v_mfma_f32_16x16x32_bf16 a[100:103], a[180:183], v[140:143], a[100:103]\n\t" \
  "ds_read_b128 a[224:227], v220 offset:24576\n\t" \
  "buffer_load_dwordx4 v225, %[sK], s96 offen offset:1024 lds\n\t" \
  "v_mfma_f32_16x16x32_bf16 a[116:119], a[184:187], v[136:139], a[116:119]\n\t" \
  "v_exp_f32 v38, v38\n\t" \
  "v_mfma_f32_16x16x32_bf16 a[116:119], a[188:191], v[140:143], a[116:119]\n\t" \
  "v_exp_f32 v39, v39\n\t" \
  "v_mfma_f32_16x16x32_bf16 v[164:167], v[216:219], v[136:139], v[164:167]\n\t" \
  "buffer_load_dwordx4 v226, %[sK], s96 offen offset:2048 lds\n\t" \
  "ds_read_b128 a[228:231], v221 offset:24576\n\t" \
  "v_mfma_f32_16x16x32_bf16 v[164:167], v[216:219], v[140:143], v[164:167]\n\t" \
  "v_cvt_pk_bf16_f32 v136, v32, v33\n\t" \
  "v_cvt_pk_bf16_f32 v137, v34, v35\n\t" \
  "v_mfma_f32_16x16x32_bf16 a[8:11], a[128:131], v[144:147], a[8:11]\n\t" \
  "v_cvt_pk_bf16_f32 v138, v36, v37\n\t" \
  "v_cvt_pk_bf16_f32 v139, v38, v39\n\t" \
  "v_mfma_f32_16x16x32_bf16 a[8:11], a[132:135], v[148:151], a[8:11]\n\t" \
  "v_exp_f32 v40, v40\n\t" \
  "v_mfma_f32_16x16x32_bf16 a[24:27], a[136:139], v[144:147], a[24:27]\n\t" \
  "buffer_load_dwordx4 v227, %[sK], s96 offen offset:3072 lds\n\t" \
  "ds_read_b128 a[232:235], v222 offset:24576\n\t" \
  "v_mfma_f32_16x16x32_bf16 a[24:27], a[140:143], v[148:151], a[24:27]\n\t" \
  "v_exp_f32 v41, v41\n\t" \
  "v_mfma_f32_16x16x32_bf16 a[40:43], a[144:147], v[144:147], a[40:43]\n\t" \
  "v_cvt_pk_bf16_f32 v140, v40, v41\n\t" \
  "s_add_u32 m0, %[m], 147456\n\t" \
  "v_mfma_f32_16x16x32_bf16 a[40:43], a[148:151], v[148:151], a[40:43]\n\t" \
  "v_exp_f32 v42, v42\n\t" \
  "v_mfma_f32_16x16x32_bf16 a[56:59], a[152:155], v[144:147], a[56:59]\n\t" \
  "v_exp_f32 v43, v43\n\t" \
  "v_mfma_f32_16x16x32_bf16 a[56:59], a[156:159], v[148:151], a[56:59]\n\t" \
  "v_cvt_pk_bf16_f32 v141, v42, v43\n\t" \
  "ds_read_b128 a[236:239], v223 offset:24576\n\t" \
  "v_mfma_f32_16x16x32_bf16 a[72:75], a[160:163], v[144:147], a[72:75]\n\t" \
  "v_exp_f32 v44, v44\n\t" \
  "v_mfma_f32_16x16x32_bf16 a[72:75], a[164:167], v[148:151], a[72:75]\n\t" \
  "s_add_u32 s96, %[sofk], 49152\n\t" \
  "ds_read_b128 a[240:243], v220 offset:28672\n\t" \
  "v_mfma_f32_16x16x32_bf16 a[88:91], a[168:171], v[144:147], a[88:91]\n\t" \
  "v_exp_f32 v45, v45\n\t" \
  "v_mfma_f32_16x16x32_bf16 a[88:91], a[172:175], v[148:151], a[88:91]\n\t" \
  "v_cvt_pk_bf16_f32 v142, v44, v45\n\t" \
  "buffer_load_dwordx4 v230, %[sV], s96 offen offset:0 lds\n\t" \
  "v_mfma_f32_16x16x32_bf16 a[104:107], a[176:179], v[144:147], a[104:107]\n\t" \
  "v_exp_f32 v46, v46\n\t" \
  "v_mfma_f32_16x16x32_bf16 a[104:107], a[180:183], v[148:151], a[104:107]\n\t" \
  "v_exp_f32 v47, v47\n\t" \
  "v_mfma_f32_16x16x32_bf16 a[120:123], a[184:187], v[144:147], a[120:123]\n\t" \
  "v_cvt_pk_bf16_f32 v143, v46, v47\n\t" \
  "ds_read_b128 a[244:247], v221 offset:28672\n\t" \
  "v_mfma_f32_16x16x32_bf16 a[120:123], a[188:191], v[148:151], a[120:123]\n\t" \
  "v_exp_f32 v64, v64\n\t" \
  "v_mfma_f32_16x16x32_bf16 v[168:171], v[216:219], v[144:147], v[168:171]\n\t" \
  "v_exp_f32 v65, v65\n\t" \
  "v_mfma_f32_16x16x32_bf16 v[168:171], v[216:219], v[148:151], v[168:171]\n\t" \
  "v_cvt_pk_bf16_f32 v144, v64, v65\n\t" \
  "buffer_load_dwordx4 v231, %[sV], s96 offen offset:1024 lds\n\t" \
  "v_mfma_f32_16x16x32_bf16 a[12:15], a[128:131], v[152:155], a[12:15]\n\t" \
  "v_exp_f32 v66, v66\n\t" \
  "v_mfma_f32_16x16x32_bf16 a[12:15], a[132:135], v[156:159], a[12:15]\n\t" \
  "ds_read_b128 a[248:251], v222 offset:28672\n\t" \
  "buffer_load_dwordx4 v230, %[sV], s96 offen offset:2048 lds\n\t" \
  "v_mfma_f32_16x16x32_bf16 a[28:31], a[136:139], v[152:155], a[28:31]\n\t" \
  "v_exp_f32 v67, v67\n\t" \
  "v_mfma_f32_16x16x32_bf16 a[28:31], a[140:143], v[156:159], a[28:31]\n\t" \
  "v_cvt_pk_bf16_f32 v145, v66, v67\n\t" \
  "ds_read_b128 a[252:255], v223 offset:28672\n\t" \
  "v_mfma_f32_16x16x32_bf16 a[44:47], a[144:147], v[152:155], a[44:47]\n\t" \
  "v_exp_f32 v68, v68\n\t" \
  "v_mfma_f32_16x16x32_bf16 a[44:47], a[148:151], v[156:159], a[44:47]\n\t" \
  "v_exp_f32 v69, v69\n\t" \
  "v_mfma_f32_16x16x32_bf16 a[60:63], a[152:155], v[152:155], a[60:63]\n\t" \
  "v_cvt_pk_bf16_f32 v146, v68, v69\n\t" \
  "buffer_load_dwordx4 v231, %[sV], s96 offen offset:3072 lds\n\t" \
  "v_mfma_f32_16x16x32_bf16 a[60:63], a[156:159], v[156:159], a[60:63]\n\t" \
  "v_exp_f32 v70, v70\n\t" \
  "v_mfma_f32_16x16x32_bf16 a[76:79], a[160:163], v[152:155], a[76:79]\n\t" \
  "v_exp_f32 v71, v71\n\t" \
  "v_mfma_f32_16x16x32_bf16 a[76:79], a[164:167], v[156:159], a[76:79]\n\t" \
  "v_cvt_pk_bf16_f32 v147, v70, v71\n\t" \
  "v_mfma_f32_16x16x32_bf16 a[92:95], a[168:171], v[152:155], a[92:95]\n\t" \
  "s_add_u32 m0, %[m], 131072\n\t" \
  "s_mov_b32 s96, 40960\n\t" \
  "buffer_load_dwordx4 v232, %[sU], s96 offen offset:2048 lds\n\t" \
  "v_mfma_f32_16x16x32_bf16 a[92:95], a[172:175], v[156:159], a[92:95]\n\t" \
  "v_exp_f32 v72, v72\n\t" \
  "v_mfma_f32_16x16x32_bf16 a[108:111], a[176:179], v[152:155], a[108:111]\n\t" \
  "v_exp_f32 v73, v73\n\t" \
  "v_mfma_f32_16x16x32_bf16 a[108:111], a[180:183], v[156:159], a[108:111]\n\t" \
  "v_cvt_pk_bf16_f32 v148, v72, v73\n\t" \
  "v_mfma_f32_16x16x32_bf16 a[124:127], a[184:187], v[152:155], a[124:127]\n\t" \
  "s_mov_b32 s96, 47104\n\t" \
  "buffer_load_dwordx4 v232, %[sU], s96 offen offset:3072 lds\n\t" \
  "v_mfma_f32_16x16x32_bf16 a[124:127], a[188:191], v[156:159], a[124:127]\n\t" \
  "v_exp_f32 v74, v74\n\t" \
  "v_mfma_f32_16x16x32_bf16 v[172:175], v[216:219], v[152:155], v[172:175]\n\t" \
  "v_exp_f32 v75, v75\n\t" \
  "v_mfma_f32_16x16x32_bf16 v[172:175], v[216:219], v[156:159], v[172:175]\n\t" \
  "v_cvt_pk_bf16_f32 v149, v74, v75\n\t" \
  "s_cmp_lg_u32 s99, 0\n\t" \
  "s_cbranch_scc1 .Lsf_p2\n\t" \
  ".Lsf_q2:\n\t" \
  "s_waitcnt lgkmcnt(0)\n\t" \
  "v_mfma_f32_16x16x32_bf16 v[0:3], a[192:195], v[16:19], v[176:179]\n\t" \
  "s_mul_i32 s98, %[sofk], 5\n\t" \
  "s_xor_b32 s98, s98, 0\n\t" \
  "s_lshr_b32 s98, s98, 1\n\t" \
  "buffer_load_dword %[pfd], v204, %[sD], s98 offen\n\t" \
  "v_mfma_f32_16x16x32_bf16 v[0:3], a[196:199], v[20:23], v[0:3]\n\t" \
  "v_exp_f32 v76, v76\n\t" \
  "v_mfma_f32_16x16x32_bf16 v[0:3], a[200:203], v[24:27], v[0:3]\n\t" \
  "ds_read_b64_tr_b16 a[128:129], v228 offset:0\n\t" \
  "ds_read_b64_tr_b16 a[130:131], v228 offset:4096\n\t" \
  "v_mfma_f32_16x16x32_bf16 v[0:3], a[204:207], v[28:31], v[0:3]\n\t" \
  "v_exp_f32 v77, v77\n\t" \
  "v_mfma_f32_16x16x32_bf16 v[4:7], a[220:223], v[28:31], v[176:179]\n\t" \
  "v_cvt_pk_bf16_f32 v150, v76, v77\n\t" \
  "ds_read_b64_tr_b16 a[132:133], v228 offset:8192\n\t" \
  "v_mfma_f32_16x16x32_bf16 v[4:7], a[216:219], v[24:27], v[4:7]\n\t" \
  "v_exp_f32 v78, v78\n\t" \
  "v_mfma_f32_16x16x32_bf16 v[4:7], a[212:215], v[20:23], v[4:7]\n\t" \
  "v_exp_f32 v79, v79\n\t" \
  "v_mfma_f32_16x16x32_bf16 v[4:7], a[208:211], v[16:19], v[4:7]\n\t" \
  "v_cvt_pk_bf16_f32 v151, v78, v79\n\t" \
  "v_max3_f32 v192, v0, v1, v2\n\t" \
  "v_mfma_f32_16x16x32_bf16 v[8:11], a[224:227], v[16:19], v[176:179]\n\t" \
  "ds_read_b64_tr_b16 a[134:135], v228 offset:12288\n\t" \
  "ds_read_b64_tr_b16 a[136:137], v229 offset:0\n\t" \
  "v_mfma_f32_16x16x32_bf16 v[8:11], a[228:231], v[20:23], v[8:11]\n\t" \
  "v_exp_f32 v96, v96\n\t" \
  "v_mfma_f32_16x16x32_bf16 v[8:11], a[232:235], v[24:27], v[8:11]\n\t" \
  "v_max3_f32 v203, v3, v4, v5\n\t" \
  "v_max3_f32 v192, v192, v6, v7\n\t" \
  "v_mfma_f32_16x16x32_bf16 v[8:11], a[236:239], v[28:31], v[8:11]\n\t" \
  "ds_read_b64_tr_b16 a[138:139], v229 offset:4096\n\t" \
  "ds_read_b64_tr_b16 a[140:141], v229 offset:8192\n\t" \
  "v_mfma_f32_16x16x32_bf16 v[12:15], a[252:255], v[28:31], v[176:179]\n\t" \
  "v_exp_f32 v97, v97\n\t" \
  "v_mfma_f32_16x16x32_bf16 v[12:15], a[248:251], v[24:27], v[12:15]\n\t" \
  "v_cvt_pk_bf16_f32 v152, v96, v97\n\t" \
  "ds_read_b64_tr_b16 a[142:143], v229 offset:12288\n\t" \
  "v_mfma_f32_16x16x32_bf16 v[12:15], a[244:247], v[20:23], v[12:15]\n\t" \
  "v_max3_f32 v203, v203, v8, v9\n\t" \
  "v_max3_f32 v192, v192, v10, v11\n\t" \
  "v_mfma_f32_16x16x32_bf16 v[12:15], a[240:243], v[16:19], v[12:15]\n\t" \
  "s_mul_i32 s98, %[sofk], 5\n\t" \
  "s_xor_b32 s98, s98, 4096\n\t" \
  "s_lshr_b32 s98, s98, 1\n\t" \
  "buffer_load_dword %[pfd], v204, %[sD], s98 offen\n\t" \
  "v_mfma_f32_16x16x32_bf16 v[32:35], a[192:195], v[48:51], v[180:183]\n\t" \
  "ds_read_b64_tr_b16 a[144:145], v228 offset:256\n\t" \
  "ds_read_b64_tr_b16 a[146:147], v228 offset:4352\n\t" \
  "v_mfma_f32_16x16x32_bf16 v[32:35], a[196:199], v[52:55], v[32:35]\n\t" \
  "v_exp_f32 v98, v98\n\t" \
  "v_mfma_f32_16x16x32_bf16 v[32:35], a[200:203], v[56:59], v[32:35]\n\t" \
  "v_max3_f32 v203, v203, v12, v13\n\t" \
  "v_max3_f32 v192, v192, v14, v15\n\t" \
  "v_mfma_f32_16x16x32_bf16 v[32:35], a[204:207], v[60:63], v[32:35]\n\t" \
  "v_max_f32 v192, v192, v203\n\t" \
  "ds_read_b64_tr_b16 a[148:149], v228 offset:8448\n\t" \
  "v_mfma_f32_16x16x32_bf16 v[36:39], a[220:223], v[60:63], v[180:183]\n\t" \
  "v_cmp_lt_f32 vcc, 0x42800000, v192\n\t" \
  "ds_read_b64_tr_b16 a[150:151], v228 offset:12544\n\t" \
  "v_mfma_f32_16x16x32_bf16 v[36:39], a[216:219], v[56:59], v[36:39]\n\t" \
  "s_cbranch_vccnz .Lsf_c30\n\t" \
  ".Lsf_r30:\n\t" \
  "ds_read_b64_tr_b16 a[152:153], v229 offset:256\n\t" \
  "v_mfma_f32_16x16x32_bf16 v[36:39], a[212:215], v[52:55], v[36:39]\n\t" \
  "v_max3_f32 v192, v32, v33, v34\n\t" \
  "ds_read_b64_tr_b16 a[154:155], v229 offset:4352\n\t" \
  "v_mfma_f32_16x16x32_bf16 v[36:39], a[208:211], v[48:51], v[36:39]\n\t" \
  "v_exp_f32 v99, v99\n\t" \
  "v_mfma_f32_16x16x32_bf16 v[40:43], a[224:227], v[48:51], v[180:183]\n\t" \
  "v_cvt_pk_bf16_f32 v153, v98, v99\n\t" \
  "ds_read_b64_tr_b16 a[156:157], v229 offset:8448\n\t" \
  "v_mfma_f32_16x16x32_bf16 v[40:43], a[228:231], v[52:55], v[40:43]\n\t" \
  "v_exp_f32 v100, v100\n\t" \
  "v_mfma_f32_16x16x32_bf16 v[40:43], a[232:235], v[56:59], v[40:43]\n\t" \
  "v_exp_f32 v101, v101\n\t" \
  "v_mfma_f32_16x16x32_bf16 v[40:43], a[236:239], v[60:63], v[40:43]\n\t" \
  "v_cvt_pk_bf16_f32 v154, v100, v101\n\t" \
  "v_max3_f32 v203, v35, v36, v37\n\t" \
  "v_mfma_f32_16x16x32_bf16 v[44:47], a[252:255], v[60:63], v[180:183]\n\t" \
  "v_max3_f32 v192, v192, v38, v39\n\t" \
  "ds_read_b64_tr_b16 a[158:159], v229 offset:12544\n\t" \
  "v_mfma_f32_16x16x32_bf16 v[44:47], a[248:251], v[56:59], v[44:47]\n\t" \
  "v_exp_f32 v102, v102\n\t" \
  "v_mfma_f32_16x16x32_bf16 v[44:47], a[244:247], v[52:55], v[44:47]\n\t" \
  "ds_read_b64_tr_b16 a[160:161], v228 offset:512\n\t" \
  "v_max3_f32 v203, v203, v40, v41\n\t" \
  "v_mfma_f32_16x16x32_bf16 v[44:47], a[240:243], v[48:51], v[44:47]\n\t" \
  "v_max3_f32 v192, v192, v42, v43\n\t" \
  "ds_read_b64_tr_b16 a[162:163], v228 offset:4608\n\t" \
  "v_mfma_f32_16x16x32_bf16 v[64:67], a[192:195], v[80:83], v[184:187]\n\t" \
  "v_exp_f32 v103, v103\n\t" \
  "v_mfma_f32_16x16x32_bf16 v[64:67], a[196:199], v[84:87], v[64:67]\n\t" \
  "v_cvt_pk_bf16_f32 v155, v102, v103\n\t" \
  "ds_read_b64_tr_b16 a[164:165], v228 offset:8704\n\t" \
  "v_mfma_f32_16x16x32_bf16 v[64:67], a[200:203], v[88:91], v[64:67]\n\t" \
  "v_max3_f32 v203, v203, v44, v45\n\t" \
  "v_max3_f32 v192, v192, v46, v47\n\t" \
  "v_mfma_f32_16x16x32_bf16 v[64:67], a[204:207], v[92:95], v[64:67]\n\t" \
  "v_max_f32 v192, v192, v203\n\t" \
  "ds_read_b64_tr_b16 a[166:167], v228 offset:12800\n\t" \
  "v_mfma_f32_16x16x32_bf16 v[68:71], a[220:223], v[92:95], v[184:187]\n\t" \
  "v_cmp_lt_f32 vcc, 0x42800000, v192\n\t" \
  "ds_read_b64_tr_b16 a[168:169], v229 offset:512\n\t" \
  "v_mfma_f32_16x16x32_bf16 v[68:71], a[216:219], v[88:91], v[68:71]\n\t" \
  "s_cbranch_vccnz .Lsf_c31\n\t" \
  ".Lsf_r31:\n\t" \
  "ds_read_b64_tr_b16 a[170:171], v229 offset:4608\n\t" \
  "v_mfma_f32_16x16x32_bf16 v[68:71], a[212:215], v[84:87], v[68:71]\n\t" \
  "v_max3_f32 v192, v64, v65, v66\n\t" \
  "ds_read_b64_tr_b16 a[172:173], v229 offset:8704\n\t" \
  "v_mfma_f32_16x16x32_bf16 v[68:71], a[208:211], v[80:83], v[68:71]\n\t" \
  "v_exp_f32 v104, v104\n\t" \
  "v_mfma_f32_16x16x32_bf16 v[72:75], a[224:227], v[80:83], v[184:187]\n\t" \
  "v_exp_f32 v105, v105\n\t" \
  "v_mfma_f32_16x16x32_bf16 v[72:75], a[228:231], v[84:87], v[72:75]\n\t" \
  "v_cvt_pk_bf16_f32 v156, v104, v105\n\t" \
  "ds_read_b64_tr_b16 a[174:175], v229 offset:12800\n\t" \
  "v_mfma_f32_16x16x32_bf16 v[72:75], a[232:235], v[88:91], v[72:75]\n\t" \
  "v_exp_f32 v106, v106\n\t" \
  "v_mfma_f32_16x16x32_bf16 v[72:75], a[236:239], v[92:95], v[72:75]\n\t" \
  "v_max3_f32 v203, v67, v68, v69\n\t" \
  "v_max3_f32 v192, v192, v70, v71\n\t" \
  "v_mfma_f32_16x16x32_bf16 v[76:79], a[252:255], v[92:95], v[184:187]\n\t" \
  "ds_read_b64_tr_b16 a[176:177], v228 offset:768\n\t" \
  "ds_read_b64_tr_b16 a[178:179], v228 offset:4864\n\t" \
  "v_mfma_f32_16x16x32_bf16 v[76:79], a[248:251], v[88:91], v[76:79]\n\t" \
  "v_exp_f32 v107, v107\n\t" \
  "v_mfma_f32_16x16x32_bf16 v[76:79], a[244:247], v[84:87], v[76:79]\n\t" \
  "v_cvt_pk_bf16_f32 v157, v106, v107\n\t" \
  "v_max3_f32 v203, v203, v72, v73\n\t" \
  "v_mfma_f32_16x16x32_bf16 v[76:79], a[240:243], v[80:83], v[76:79]\n\t" \
  "v_max3_f32 v192, v192, v74, v75\n\t" \
  "ds_read_b64_tr_b16 a[180:181], v228 offset:8960\n\t" \
  "v_mfma_f32_16x16x32_bf16 v[96:99], a[192:195], v[112:115], v[188:191]\n\t" \
  "v_exp_f32 v108, v108\n\t" \
  "v_mfma_f32_16x16x32_bf16 v[96:99], a[196:199], v[116:119], v[96:99]\n\t" \
  "v_exp_f32 v109, v109\n\t" \
  "v_mfma_f32_16x16x32_bf16 v[96:99], a[200:203], v[120:123], v[96:99]\n\t" \
  "v_cvt_pk_bf16_f32 v158, v108, v109\n\t" \
  "ds_read_b64_tr_b16 a[182:183], v228 offset:13056\n\t" \
  "v_mfma_f32_16x16x32_bf16 v[96:99], a[204:207], v[124:127], v[96:99]\n\t" \
  "v_max3_f32 v203, v203, v76, v77\n\t" \
  "v_max3_f32 v192, v192, v78, v79\n\t" \
  "v_mfma_f32_16x16x32_bf16 v[100:103], a[220:223], v[124:127], v[188:191]\n\t" \
  "v_max_f32 v192, v192, v203\n\t" \
  "ds_read_b64_tr_b16 a[184:185], v229 offset:768\n\t" \
  "v_mfma_f32_16x16x32_bf16 v[100:103], a[216:219], v[120:123], v[100:103]\n\t" \
  "v_cmp_lt_f32 vcc, 0x42800000, v192\n\t" \
  "ds_read_b64_tr_b16 a[186:187], v229 offset:4864\n\t" \
  "v_mfma_f32_16x16x32_bf16 v[100:103], a[212:215], v[116:119], v[100:103]\n\t" \
  "s_cbranch_vccnz .Lsf_c32\n\t" \
  ".Lsf_r32:\n\t" \
  "v_max3_f32 v192, v96, v97, v98\n\t" \
  "v_mfma_f32_16x16x32_bf16 v[100:103], a[208:211], v[112:115], v[100:103]\n\t" \
  "ds_read_b64_tr_b16 a[188:189], v229 offset:8960\n\t" \
  "ds_read_b64_tr_b16 a[190:191], v229 offset:13056\n\t" \
  "v_mfma_f32_16x16x32_bf16 v[104:107], a[224:227], v[112:115], v[188:191]\n\t" \
  "v_exp_f32 v110, v110\n\t" \
  "v_mfma_f32_16x16x32_bf16 v[104:107], a[228:231], v[116:119], v[104:107]\n\t" \
  "v_exp_f32 v111, v111\n\t" \
  "v_mfma_f32_16x16x32_bf16 v[104:107], a[232:235], v[120:123], v[104:107]\n\t" \
  "v_cvt_pk_bf16_f32 v159, v110, v111\n\t" \
  "v_max3_f32 v203, v99, v100, v101\n\t" \
  "v_mfma_f32_16x16x32_bf16 v[104:107], a[236:239], v[124:127], v[104:107]\n\t" \
  "v_max3_f32 v192, v192, v102, v103\n\t" \
  "v_mfma_f32_16x16x32_bf16 v[108:111], a[252:255], v[124:127], v[188:191]\n\t" \
  "v_exp_f32 v0, v0\n\t" \
  "v_mfma_f32_16x16x32_bf16 v[108:111], a[248:251], v[120:123], v[108:111]\n\t" \
  "v_exp_f32 v1, v1\n\t" \
  "v_mfma_f32_16x16x32_bf16 v[108:111], a[244:247], v[116:119], v[108:111]\n\t" \
  "v_exp_f32 v2, v2\n\t" \
  "v_mfma_f32_16x16x32_bf16 v[108:111], a[240:243], v[112:115], v[108:111]\n\t" \
  "v_max3_f32 v203, v203, v104, v105\n\t" \
  "v_max3_f32 v192, v192, v106, v107\n\t" \
  "s_waitcnt vmcnt(4) lgkmcnt(0)\n\t" \
  "s_barrier\n\t" \
  "v_mfma_f32_16x16x32_bf16 a[0:3], a[128:131], v[128:131], a[0:3]\n\t" \
  "v_exp_f32 v3, v3\n\t" \
  "v_mfma_f32_16x16x32_bf16 a[0:3], a[132:135], v[132:135], a[0:3]\n\t" \
  "v_exp_f32 v4, v4\n\t" \
  "v_mfma_f32_16x16x32_bf16 a[16:19], a[136:139], v[128:131], a[16:19]\n\t" \
  "ds_read_b128 a[192:195], v220 offset:0\n\t" \
  "v_max3_f32 v203, v203, v108, v109\n\t" \
  "v_mfma_f32_16x16x32_bf16 a[16:19], a[140:143], v[132:135], a[16:19]\n\t" \
  "v_max3_f32 v192, v192, v110, v111\n\t" \
  "v_max_f32 v192, v192, v203\n\t" \
  "v_mfma_f32_16x16x32_bf16 a[32:35], a[144:147], v[128:131], a[32:35]\n\t" \
  "v_cmp_lt_f32 vcc, 0x42800000, v192\n\t" \
  "ds_read_b128 a[196:199], v221 offset:0\n\t" \
  "v_mfma_f32_16x16x32_bf16 a[32:35], a[148:151], v[132:135], a[32:35]\n\t" \
  "s_cbranch_vccnz .Lsf_c33\n\t" \
  ".Lsf_r33:\n\t" \
  "ds_read_b128 a[200:203], v222 offset:0\n\t" \
  "v_mfma_f32_16x16x32_bf16 a[48:51], a[152:155], v[128:131], a[48:51]\n\t" \
  "v_exp_f32 v5, v5\n\t" \
  "v_mfma_f32_16x16x32_bf16 a[48:51], a[156:159], v[132:135], a[48:51]\n\t" \
  "v_exp_f32 v6, v6\n\t" \
  "v_mfma_f32_16x16x32_bf16 a[64:67], a[160:163], v[128:131], a[64:67]\n\t" \
  "v_exp_f32 v7, v7\n\t" \
  "v_mfma_f32_16x16x32_bf16 a[64:67], a[164:167], v[132:135], a[64:67]\n\t" \
  "v_exp_f32 v8, v8\n\t" \
  "v_mfma_f32_16x16x32_bf16 a[80:83], a[168:171], v[128:131], a[80:83]\n\t" \
  "v_exp_f32 v9, v9\n\t" \
  "v_mfma_f32_16x16x32_bf16 a[80:83], a[172:175], v[132:135], a[80:83]\n\t" \
  "v_exp_f32 v10, v10\n\t" \
  "v_mfma_f32_16x16x32_bf16 a[96:99], a[176:179], v[128:131], a[96:99]\n\t" \
  "v_exp_f32 v11, v11\n\t" \
  "v_mfma_f32_16x16x32_bf16 a[96:99], a[180:183], v[132:135], a[96:99]\n\t" \
  "v_exp_f32 v12, v12\n\t" \
  "v_mfma_f32_16x16x32_bf16 a[112:115], a[184:187], v[128:131], a[112:115]\n\t" \
  "ds_read_b128 a[204:207], v223 offset:0\n\t" \
  "ds_read_b128 a[208:211], v220 offset:4096\n\t" \
  "v_mfma_f32_16x16x32_bf16 a[112:115], a[188:191], v[132:135], a[112:115]\n\t" \
  "v_exp_f32 v13, v13\n\t" \
  "v_mfma_f32_16x16x32_bf16 v[160:163], v[216:219], v[128:131], v[160:163]\n\t" \
  "v_exp_f32 v14, v14\n\t" \
  "v_mfma_f32_16x16x32_bf16 v[160:163], v[216:219], v[132:135], v[160:163]\n\t" \
  "v_cvt_pk_bf16_f32 v128, v0, v1\n\t" \
  "v_cvt_pk_bf16_f32 v129, v2, v3\n\t" \
  "v_mfma_f32_16x16x32_bf16 a[4:7], a[128:131], v[136:139], a[4:7]\n\t" \
  "v_cvt_pk_bf16_f32 v130, v4, v5\n\t" \
  "v_cvt_pk_bf16_f32 v131, v6, v7\n\t" \
  "v_mfma_f32_16x16x32_bf16 a[4:7], a[132:135], v[140:143], a[4:7]\n\t" \
  "v_cvt_pk_bf16_f32 v132, v8, v9\n\t" \
  "v_cvt_pk_bf16_f32 v133, v10, v11\n\t" \
  "v_mfma_f32_16x16x32_bf16 a[20:23], a[136:139], v[136:139], a[20:23]\n\t" \
  "v_cvt_pk_bf16_f32 v134, v12, v13\n\t" \
  "ds_read_b128 a[212:215], v221 offset:4096\n\t" \
  "v_mfma_f32_16x16x32_bf16 a[20:23], a[140:143], v[140:143], a[20:23]\n\t" \
  "v_exp_f32 v15, v15\n\t" \
  "v_mfma_f32_16x16x32_bf16 a[36:39], a[144:147], v[136:139], a[36:39]\n\t" \
  "v_cvt_pk_bf16_f32 v135, v14, v15\n\t" \
  "s_add_u32 m0, %[m], 65536\n\t" \
  "v_mfma_f32_16x16x32_bf16 a[36:39], a[148:151], v[140:143], a[36:39]\n\t" \
  "v_exp_f32 v32, v32\n\t" \
  "v_mfma_f32_16x16x32_bf16 a[52:55], a[152:155], v[136:139], a[52:55]\n\t" \
  "ds_read_b128 a[216:219], v222 offset:4096\n\t" \
  "s_add_u32 s96, %[sofk], 81920\n\t" \
  "v_mfma_f32_16x16x32_bf16 a[52:55], a[156:159], v[140:143], a[52:55]\n\t" \
  "v_exp_f32 v33, v33\n\t" \
  "v_mfma_f32_16x16x32_bf16 a[68:71], a[160:163], v[136:139], a[68:71]\n\t" \
  "v_exp_f32 v34, v34\n\t" \
  "v_mfma_f32_16x16x32_bf16 a[68:71], a[164:167], v[140:143], a[68:71]\n\t" \
  "ds_read_b128 a[220:223], v223 offset:4096\n\t" \
  "buffer_load_dwordx4 v224, %[sK], s96 offen offset:0 lds\n\t" \
  "v_mfma_f32_16x16x32_bf16 a[84:87], a[168:171], v[136:139], a[84:87]\n\t" \
  "v_exp_f32 v35, v35\n\t" \
  "v_mfma_f32_16x16x32_bf16 a[84:87], a[172:175], v[140:143], a[84:87]\n\t" \
  "v_exp_f32 v36, v36\n\t" \
  "v_mfma_f32_16x16x32_bf16 a[100:103], a[176:179], v[136:139], a[100:103]\n\t" \
  "v_exp_f32 v37, v37\n\t" \
  "v_mfma_f32_16x16x32_bf16 a[100:103], a[180:183], v[140:143], a[100:103]\n\t" \
  "ds_read_b128 a[224:227], v220 offset:8192\n\t" \
  "buffer_load_dwordx4 v225, %[sK], s96 offen offset:1024 lds\n\t" \
  "v_mfma_f32_16x16x32_bf16 a[116:119], a[184:187], v[136:139], a[116:119]\n\t" \
  "v_exp_f32 v38, v38\n\t" \
  "v_mfma_f32_16x16x32_bf16 a[116:119], a[188:191], v[140:143], a[116:119]\n\t" \
  "v_exp_f32 v39, v39\n\t" \
  "v_mfma_f32_16x16x32_bf16 v[164:167], v[216:219], v[136:139], v[164:167]\n\t" \
  "buffer_load_dwordx4 v226, %[sK], s96 offen offset:2048 lds\n\t" \
  "ds_read_b128 a[228:231], v221 offset:8192\n\t" \
  "v_mfma_f32_16x16x32_bf16 v[164:167], v[216:219], v[140:143], v[164:167]\n\t" \
  "v_cvt_pk_bf16_f32 v136, v32, v33\n\t" \
  "v_cvt_pk_bf16_f32 v137, v34, v35\n\t" \
  "v_mfma_f32_16x16x32_bf16 a[8:11], a[128:131], v[144:147], a[8:11]\n\t" \
  "v_cvt_pk_bf16_f32 v138, v36, v37\n\t" \
  "v_cvt_pk_bf16_f32 v139, v38, v39\n\t" \
  "v_mfma_f32_16x16x32_bf16 a[8:11], a[132:135], v[148:151], a[8:11]\n\t" \
  "v_exp_f32 v40, v40\n\t" \
  "v_mfma_f32_16x16x32_bf16 a[24:27], a[136:139], v[144:147], a[24:27]\n\t" \
  "buffer_load_dwordx4 v227, %[sK], s96 offen offset:3072 lds\n\t" \
  "ds_read_b128 a[232:235], v222 offset:8192\n\t" \
  "v_mfma_f32_16x16x32_bf16 a[24:27], a[140:143], v[148:151], a[24:27]\n\t" \
  "v_exp_f32 v41, v41\n\t" \
  "v_mfma_f32_16x16x32_bf16 a[40:43], a[144:147], v[144:147], a[40:43]\n\t" \
  "v_cvt_pk_bf16_f32 v140, v40, v41\n\t" \
  "s_add_u32 m0, %[m], 81920\n\t" \
  "v_mfma_f32_16x16x32_bf16 a[40:43], a[148:151], v[148:151], a[40:43]\n\t" \
  "v_exp_f32 v42, v42\n\t" \
  "v_mfma_f32_16x16x32_bf16 a[56:59], a[152:155], v[144:147], a[56:59]\n\t" \
  "v_exp_f32 v43, v43\n\t" \
  "v_mfma_f32_16x16x32_bf16 a[56:59], a[156:159], v[148:151], a[56:59]\n\t" \
  "v_cvt_pk_bf16_f32 v141, v42, v43\n\t" \
  "ds_read_b128 a[236:239], v223 offset:8192\n\t" \
  "v_mfma_f32_16x16x32_bf16 a[72:75], a[160:163], v[144:147], a[72:75]\n\t" \
  "v_exp_f32 v44, v44\n\t" \
  "v_mfma_f32_16x16x32_bf16 a[72:75], a[164:167], v[148:151], a[72:75]\n\t" \
  "s_add_u32 s96, %[sofk], 65536\n\t" \
  "ds_read_b128 a[240:243], v220 offset:12288\n\t" \
  "v_mfma_f32_16x16x32_bf16 a[88:91], a[168:171], v[144:147], a[88:91]\n\t" \
  "v_exp_f32 v45, v45\n\t" \
  "v_mfma_f32_16x16x32_bf16 a[88:91], a[172:175], v[148:151], a[88:91]\n\t" \
  "v_cvt_pk_bf16_f32 v142, v44, v45\n\t" \
  "buffer_load_dwordx4 v230, %[sV], s96 offen offset:0 lds\n\t" \
  "v_mfma_f32_16x16x32_bf16 a[104:107], a[176:179], v[144:147], a[104:107]\n\t" \
  "v_exp_f32 v46, v46\n\t" \
  "v_mfma_f32_16x16x32_bf16 a[104:107], a[180:183], v[148:151], a[104:107]\n\t" \
  "v_exp_f32 v47, v47\n\t" \
  "v_mfma_f32_16x16x32_bf16 a[120:123], a[184:187], v[144:147], a[120:123]\n\t" \
  "v_cvt_pk_bf16_f32 v143, v46, v47\n\t" \
  "ds_read_b128 a[244:247], v221 offset:12288\n\t" \
  "v_mfma_f32_16x16x32_bf16 a[120:123], a[188:191], v[148:151], a[120:123]\n\t" \
  "v_exp_f32 v64, v64\n\t" \
  "v_mfma_f32_16x16x32_bf16 v[168:171], v[216:219], v[144:147], v[168:171]\n\t" \
  "v_exp_f32 v65, v65\n\t" \
  "v_mfma_f32_16x16x32_bf16 v[168:171], v[216:219], v[148:151], v[168:171]\n\t" \
  "v_cvt_pk_bf16_f32 v144, v64, v65\n\t" \
  "buffer_load_dwordx4 v231, %[sV], s96 offen offset:1024 lds\n\t" \
  "v_mfma_f32_16x16x32_bf16 a[12:15], a[128:131], v[152:155], a[12:15]\n\t" \
  "v_exp_f32 v66, v66\n\t" \
  "v_mfma_f32_16x16x32_bf16 a[12:15], a[132:135], v[156:159], a[12:15]\n\t" \
  "ds_read_b128 a[248:251], v222 offset:12288\n\t" \
  "buffer_load_dwordx4 v230, %[sV], s96 offen offset:2048 lds\n\t" \
  "v_mfma_f32_16x16x32_bf16 a[28:31], a[136:139], v[152:155], a[28:31]\n\t" \
  "v_exp_f32 v67, v67\n\t" \
  "v_mfma_f32_16x16x32_bf16 a[28:31], a[140:143], v[156:159], a[28:31]\n\t" \
  "v_cvt_pk_bf16_f32 v145, v66, v67\n\t" \
  "ds_read_b128 a[252:255], v223 offset:12288\n\t" \
  "v_mfma_f32_16x16x32_bf16 a[44:47], a[144:147], v[152:155], a[44:47]\n\t" \
  "v_exp_f32 v68, v68\n\t" \
  "v_mfma_f32_16x16x32_bf16 a[44:47], a[148:151], v[156:159], a[44:47]\n\t" \
  "v_exp_f32 v69, v69\n\t" \
  "v_mfma_f32_16x16x32_bf16 a[60:63], a[152:155], v[152:155], a[60:63]\n\t" \
  "v_cvt_pk_bf16_f32 v146, v68, v69\n\t" \
  "buffer_load_dwordx4 v231, %[sV], s96 offen offset:3072 lds\n\t" \
  "v_mfma_f32_16x16x32_bf16 a[60:63], a[156:159], v[156:159], a[60:63]\n\t" \
  "v_exp_f32 v70, v70\n\t" \
  "v_mfma_f32_16x16x32_bf16 a[76:79], a[160:163], v[152:155], a[76:79]\n\t" \
  "v_exp_f32 v71, v71\n\t" \
  "v_mfma_f32_16x16x32_bf16 a[76:79], a[164:167], v[156:159], a[76:79]\n\t" \
  "v_cvt_pk_bf16_f32 v147, v70, v71\n\t" \
  "v_mfma_f32_16x16x32_bf16 a[92:95], a[168:171], v[152:155], a[92:95]\n\t" \
  "s_add_u32 m0, %[m], 32768\n\t" \
  "s_mov_b32 s96, 0\n\t" \
  "buffer_load_dwordx4 v232, %[sG], s96 offen offset:0 lds\n\t" \
  "v_mfma_f32_16x16x32_bf16 a[92:95], a[172:175], v[156:159], a[92:95]\n\t" \
  "v_exp_f32 v72, v72\n\t" \
  "v_mfma_f32_16x16x32_bf16 a[108:111], a[176:179], v[152:155], a[108:111]\n\t" \
  "v_exp_f32 v73, v73\n\t" \
  "v_mfma_f32_16x16x32_bf16 a[108:111], a[180:183], v[156:159], a[108:111]\n\t" \
  "v_cvt_pk_bf16_f32 v148, v72, v73\n\t" \
  "v_mfma_f32_16x16x32_bf16 a[124:127], a[184:187], v[152:155], a[124:127]\n\t" \
  "s_mov_b32 s96, 6144\n\t" \
  "buffer_load_dwordx4 v232, %[sG], s96 offen offset:1024 lds\n\t" \
  "v_mfma_f32_16x16x32_bf16 a[124:127], a[188:191], v[156:159], a[124:127]\n\t" \
  "v_exp_f32 v74, v74\n\t" \
  "v_mfma_f32_16x16x32_bf16 v[172:175], v[216:219], v[152:155], v[172:175]\n\t" \
  "v_exp_f32 v75, v75\n\t" \
  "v_mfma_f32_16x16x32_bf16 v[172:175], v[216:219], v[156:159], v[172:175]\n\t" \
  "v_cvt_pk_bf16_f32 v149, v74, v75\n\t" \
  "s_cmp_lg_u32 s99, 0\n\t" \
  "s_cbranch_scc1 .Lsf_p3\n\t" \
  ".Lsf_q3:\n\t" \
  "s_waitcnt lgkmcnt(0)\n\t" \
  "v_mfma_f32_16x16x32_bf16 v[0:3], a[192:195], v[16:19], v[176:179]\n\t" \
  "s_add_u32 s98, %[pf2], 0\n\t" \
  "buffer_load_dword %[pfd], v202, %[sM], s98 offen\n\t" \
  "v_mfma_f32_16x16x32_bf16 v[0:3], a[196:199], v[20:23], v[0:3]\n\t" \
  "v_exp_f32 v76, v76\n\t" \
  "v_mfma_f32_16x16x32_bf16 v[0:3], a[200:203], v[24:27], v[0:3]\n\t" \
  "ds_read_b64_tr_b16 a[128:129], v237 offset:0\n\t" \
  "ds_read_b64_tr_b16 a[130:131], v237 offset:4096\n\t" \
  "v_mfma_f32_16x16x32_bf16 v[0:3], a[204:207], v[28:31], v[0:3]\n\t" \
  "v_exp_f32 v77, v77\n\t" \
  "v_mfma_f32_16x16x32_bf16 v[4:7], a[220:223], v[28:31], v[176:179]\n\t" \
  "v_cvt_pk_bf16_f32 v150, v76, v77\n\t" \
  "ds_read_b64_tr_b16 a[132:133], v237 offset:8192\n\t" \
  "v_mfma_f32_16x16x32_bf16 v[4:7], a[216:219], v[24:27], v[4:7]\n\t" \
  "v_exp_f32 v78, v78\n\t" \
  "v_mfma_f32_16x16x32_bf16 v[4:7], a[212:215], v[20:23], v[4:7]\n\t" \
  "v_exp_f32 v79, v79\n\t" \
  "v_mfma_f32_16x16x32_bf16 v[4:7], a[208:211], v[16:19], v[4:7]\n\t" \
  "v_cvt_pk_bf16_f32 v151, v78, v79\n\t" \
  "v_max3_f32 v192, v0, v1, v2\n\t" \
  "v_mfma_f32_16x16x32_bf16 v[8:11], a[224:227], v[16:19], v[176:179]\n\t" \
  "ds_read_b64_tr_b16 a[134:135], v237 offset:12288\n\t" \
  "ds_read_b64_tr_b16 a[136:137], v238 offset:0\n\t" \
  "v_mfma_f32_16x16x32_bf16 v[8:11], a[228:231], v[20:23], v[8:11]\n\t" \
  "v_exp_f32 v96, v96\n\t" \
  "v_mfma_f32_16x16x32_bf16 v[8:11], a[232:235], v[24:27], v[8:11]\n\t" \
  "v_max3_f32 v203, v3, v4, v5\n\t" \
  "v_max3_f32 v192, v192, v6, v7\n\t" \
  "v_mfma_f32_16x16x32_bf16 v[8:11], a[236:239], v[28:31], v[8:11]\n\t" \
  "ds_read_b64_tr_b16 a[138:139], v238 offset:4096\n\t" \
  "ds_read_b64_tr_b16 a[140:141], v238 offset:8192\n\t" \
  "v_mfma_f32_16x16x32_bf16 v[12:15], a[252:255], v[28:31], v[176:179]\n\t" \
  "v_exp_f32 v97, v97\n\t" \
  "v_mfma_f32_16x16x32_bf16 v[12:15], a[248:251], v[24:27], v[12:15]\n\t" \
  "v_cvt_pk_bf16_f32 v152, v96, v97\n\t" \
  "ds_read_b64_tr_b16 a[142:143], v238 offset:12288\n\t" \
  "v_mfma_f32_16x16x32_bf16 v[12:15], a[244:247], v[20:23], v[12:15]\n\t" \
  "v_max3_f32 v203, v203, v8, v9\n\t" \
  "v_max3_f32 v192, v192, v10, v11\n\t" \
  "v_mfma_f32_16x16x32_bf16 v[12:15], a[240:243], v[16:19], v[12:15]\n\t" \
  "s_add_u32 s98, %[pf2], 24576\n\t" \
  "buffer_load_dword %[pfd], v202, %[sM], s98 offen\n\t" \
  "v_mfma_f32_16x16x32_bf16 v[32:35], a[192:195], v[48:51], v[180:183]\n\t" \
  "ds_read_b64_tr_b16 a[144:145], v237 offset:256\n\t" \
  "ds_read_b64_tr_b16 a[146:147], v237 offset:4352\n\t" \
  "v_mfma_f32_16x16x32_bf16 v[32:35], a[196:199], v[52:55], v[32:35]\n\t" \
  "v_exp_f32 v98, v98\n\t" \
  "v_mfma_f32_16x16x32_bf16 v[32:35], a[200:203], v[56:59], v[32:35]\n\t" \
  "v_max3_f32 v203, v203, v12, v13\n\t" \
  "v_max3_f32 v192, v192, v14, v15\n\t" \
  "v_mfma_f32_16x16x32_bf16 v[32:35], a[204:207], v[60:63], v[32:35]\n\t" \
  "v_max_f32 v192, v192, v203\n\t" \
  "ds_read_b64_tr_b16 a[148:149], v237 offset:8448\n\t" \
  "v_mfma_f32_16x16x32_bf16 v[36:39], a[220:223], v[60:63], v[180:183]\n\t" \
  "v_cmp_lt_f32 vcc, 0x42800000, v192\n\t" \
  "ds_read_b64_tr_b16 a[150:151], v237 offset:12544\n\t" \
  "v_mfma_f32_16x16x32_bf16 v[36:39], a[216:219], v[56:59], v[36:39]\n\t" \
  "s_cbranch_vccnz .Lsf_c40\n\t" \
  ".Lsf_r40:\n\t" \
  "ds_read_b64_tr_b16 a[152:153], v238 offset:256\n\t" \
  "v_mfma_f32_16x16x32_bf16 v[36:39], a[212:215], v[52:55], v[36:39]\n\t" \
  "v_max3_f32 v192, v32, v33, v34\n\t" \
  "ds_read_b64_tr_b16 a[154:155], v238 offset:4352\n\t" \
  "v_mfma_f32_16x16x32_bf16 v[36:39], a[208:211], v[48:51], v[36:39]\n\t" \
  "v_exp_f32 v99, v99\n\t" \
  "v_mfma_f32_16x16x32_bf16 v[40:43], a[224:227], v[48:51], v[180:183]\n\t" \
  "v_cvt_pk_bf16_f32 v153, v98, v99\n\t" \
  "ds_read_b64_tr_b16 a[156:157], v238 offset:8448\n\t" \
  "v_mfma_f32_16x16x32_bf16 v[40:43], a[228:231], v[52:55], v[40:43]\n\t" \
  "v_exp_f32 v100, v100\n\t" \
  "v_mfma_f32_16x16x32_bf16 v[40:43], a[232:235], v[56:59], v[40:43]\n\t" \
  "v_exp_f32 v101, v101\n\t" \
  "v_mfma_f32_16x16x32_bf16 v[40:43], a[236:239], v[60:63], v[40:43]\n\t" \
  "v_cvt_pk_bf16_f32 v154, v100, v101\n\t" \
  "v_max3_f32 v203, v35, v36, v37\n\t" \
  "v_mfma_f32_16x16x32_bf16 v[44:47], a[252:255], v[60:63], v[180:183]\n\t" \
  "v_max3_f32 v192, v192, v38, v39\n\t" \
  "ds_read_b64_tr_b16 a[158:159], v238 offset:12544\n\t" \
  "v_mfma_f32_16x16x32_bf16 v[44:47], a[248:251], v[56:59], v[44:47]\n\t" \
  "v_exp_f32 v102, v102\n\t" \
  "v_mfma_f32_16x16x32_bf16 v[44:47], a[244:247], v[52:55], v[44:47]\n\t" \
  "ds_read_b64_tr_b16 a[160:161], v237 offset:512\n\t" \
  "v_max3_f32 v203, v203, v40, v41\n\t" \
  "v_mfma_f32_16x16x32_bf16 v[44:47], a[240:243], v[48:51], v[44:47]\n\t" \
  "v_max3_f32 v192, v192, v42, v43\n\t" \
  "ds_read_b64_tr_b16 a[162:163], v237 offset:4608\n\t" \
  "v_mfma_f32_16x16x32_bf16 v[64:67], a[192:195], v[80:83], v[184:187]\n\t" \
  "v_exp_f32 v103, v103\n\t" \
  "v_mfma_f32_16x16x32_bf16 v[64:67], a[196:199], v[84:87], v[64:67]\n\t" \
  "v_cvt_pk_bf16_f32 v155, v102, v103\n\t" \
  "ds_read_b64_tr_b16 a[164:165], v237 offset:8704\n\t" \
  "v_mfma_f32_16x16x32_bf16 v[64:67], a[200:203], v[88:91], v[64:67]\n\t" \
  "v_max3_f32 v203, v203, v44, v45\n\t" \
  "v_max3_f32 v192, v192, v46, v47\n\t" \
  "v_mfma_f32_16x16x32_bf16 v[64:67], a[204:207], v[92:95], v[64:67]\n\t" \
  "v_max_f32 v192, v192, v203\n\t" \
  "ds_read_b64_tr_b16 a[166:167], v237 offset:12800\n\t" \
  "v_mfma_f32_16x16x32_bf16 v[68:71], a[220:223], v[92:95], v[184:187]\n\t" \
  "v_cmp_lt_f32 vcc, 0x42800000, v192\n\t" \
  "ds_read_b64_tr_b16 a[168:169], v238 offset:512\n\t" \
  "v_mfma_f32_16x16x32_bf16 v[68:71], a[216:219], v[88:91], v[68:71]\n\t" \
  "s_cbranch_vccnz .Lsf_c41\n\t" \
  ".Lsf_r41:\n\t" \
  "ds_read_b64_tr_b16 a[170:171], v238 offset:4608\n\t" \
  "v_mfma_f32_16x16x32_bf16 v[68:71], a[212:215], v[84:87], v[68:71]\n\t" \
  "v_max3_f32 v192, v64, v65, v66\n\t" \
  "ds_read_b64_tr_b16 a[172:173], v238 offset:8704\n\t" \
  "v_mfma_f32_16x16x32_bf16 v[68:71], a[208:211], v[80:83], v[68:71]\n\t" \
  "v_exp_f32 v104, v104\n\t" \
  "v_mfma_f32_16x16x32_bf16 v[72:75], a[224:227], v[80:83], v[184:187]\n\t" \
  "v_exp_f32 v105, v105\n\t" \
  "v_mfma_f32_16x16x32_bf16 v[72:75], a[228:231], v[84:87], v[72:75]\n\t" \
  "v_cvt_pk_bf16_f32 v156, v104, v105\n\t" \
  "ds_read_b64_tr_b16 a[174:175], v238 offset:12800\n\t" \
  "v_mfma_f32_16x16x32_bf16 v[72:75], a[232:235], v[88:91], v[72:75]\n\t" \
  "v_exp_f32 v106, v106\n\t" \
  "v_mfma_f32_16x16x32_bf16 v[72:75], a[236:239], v[92:95], v[72:75]\n\t" \
  "v_max3_f32 v203, v67, v68, v69\n\t" \
  "v_max3_f32 v192, v192, v70, v71\n\t" \
  "v_mfma_f32_16x16x32_bf16 v[76:79], a[252:255], v[92:95], v[184:187]\n\t" \
  "ds_read_b64_tr_b16 a[176:177], v237 offset:768\n\t" \
  "ds_read_b64_tr_b16 a[178:179], v237 offset:4864\n\t" \
  "v_mfma_f32_16x16x32_bf16 v[76:79], a[248:251], v[88:91], v[76:79]\n\t" \
  "v_exp_f32 v107, v107\n\t" \
  "v_mfma_f32_16x16x32_bf16 v[76:79], a[244:247], v[84:87], v[76:79]\n\t" \
  "v_cvt_pk_bf16_f32 v157, v106, v107\n\t" \
  "v_max3_f32 v203, v203, v72, v73\n\t" \
  "v_mfma_f32_16x16x32_bf16 v[76:79], a[240:243], v[80:83], v[76:79]\n\t" \
  "v_max3_f32 v192, v192, v74, v75\n\t" \
  "ds_read_b64_tr_b16 a[180:181], v237 offset:8960\n\t" \
  "v_mfma_f32_16x16x32_bf16 v[96:99], a[192:195], v[112:115], v[188:191]\n\t" \
  "v_exp_f32 v108, v108\n\t" \
  "v_mfma_f32_16x16x32_bf16 v[96:99], a[196:199], v[116:119], v[96:99]\n\t" \
  "v_exp_f32 v109, v109\n\t" \
  "v_mfma_f32_16x16x32_bf16 v[96:99], a[200:203], v[120:123], v[96:99]\n\t" \
  "v_cvt_pk_bf16_f32 v158, v108, v109\n\t" \
  "ds_read_b64_tr_b16 a[182:183], v237 offset:13056\n\t" \
  "v_mfma_f32_16x16x32_bf16 v[96:99], a[204:207], v[124:127], v[96:99]\n\t" \
  "v_max3_f32 v203, v203, v76, v77\n\t" \
  "v_max3_f32 v192, v192, v78, v79\n\t" \
  "v_mfma_f32_16x16x32_bf16 v[100:103], a[220:223], v[124:127], v[188:191]\n\t" \
  "v_max_f32 v192, v192, v203\n\t" \
  "ds_read_b64_tr_b16 a[184:185], v238 offset:768\n\t" \
  "v_mfma_f32_16x16x32_bf16 v[100:103], a[216:219], v[120:123], v[100:103]\n\t" \
  "v_cmp_lt_f32 vcc, 0x42800000, v192\n\t" \
  "ds_read_b64_tr_b16 a[186:187], v238 offset:4864\n\t" \
  "v_mfma_f32_16x16x32_bf16 v[100:103], a[212:215], v[116:119], v[100:103]\n\t" \
  "s_cbranch_vccnz .Lsf_c42\n\t" \
  ".Lsf_r42:\n\t" \
  "v_max3_f32 v192, v96, v97, v98\n\t" \
  "v_mfma_f32_16x16x32_bf16 v[100:103], a[208:211], v[112:115], v[100:103]\n\t" \
  "ds_read_b64_tr_b16 a[188:189], v238 offset:8960\n\t" \
  "ds_read_b64_tr_b16 a[190:191], v238 offset:13056\n\t" \
  "v_mfma_f32_16x16x32_bf16 v[104:107], a[224:227], v[112:115], v[188:191]\n\t" \
  "v_exp_f32 v110, v110\n\t" \
  "v_mfma_f32_16x16x32_bf16 v[104:107], a[228:231], v[116:119], v[104:107]\n\t" \
  "v_exp_f32 v111, v111\n\t" \
  "v_mfma_f32_16x16x32_bf16 v[104:107], a[232:235], v[120:123], v[104:107]\n\t" \
  "v_cvt_pk_bf16_f32 v159, v110, v111\n\t" \
  "v_max3_f32 v203, v99, v100, v101\n\t" \
  "v_mfma_f32_16x16x32_bf16 v[104:107], a[236:239], v[124:127], v[104:107]\n\t" \
  "v_max3_f32 v192, v192, v102, v103\n\t" \
  "v_mfma_f32_16x16x32_bf16 v[108:111], a[252:255], v[124:127], v[188:191]\n\t" \
  "v_exp_f32 v0, v0\n\t" \
  "v_mfma_f32_16x16x32_bf16 v[108:111], a[248:251], v[120:123], v[108:111]\n\t" \
  "v_exp_f32 v1, v1\n\t" \
  "v_mfma_f32_16x16x32_bf16 v[108:111], a[244:247], v[116:119], v[108:111]\n\t" \
  "v_exp_f32 v2, v2\n\t" \
  "v_mfma_f32_16x16x32_bf16 v[108:111], a[240:243], v[112:115], v[108:111]\n\t" \
  "v_max3_f32 v203, v203, v104, v105\n\t" \
  "v_max3_f32 v192, v192, v106, v107\n\t" \
  "s_waitcnt vmcnt(4) lgkmcnt(0)\n\t" \
  "s_barrier\n\t" \
  "v_mfma_f32_16x16x32_bf16 a[0:3], a[128:131], v[128:131], a[0:3]\n\t" \
  "v_exp_f32 v3, v3\n\t" \
  "v_mfma_f32_16x16x32_bf16 a[0:3], a[132:135], v[132:135], a[0:3]\n\t" \
  "v_exp_f32 v4, v4\n\t" \
  "v_mfma_f32_16x16x32_bf16 a[16:19], a[136:139], v[128:131], a[16:19]\n\t" \
  "ds_read_b128 a[192:195], v220 offset:16384\n\t" \
  "v_max3_f32 v203, v203, v108, v109\n\t" \
  "v_mfma_f32_16x16x32_bf16 a[16:19], a[140:143], v[132:135], a[16:19]\n\t" \
  "v_max3_f32 v192, v192, v110, v111\n\t" \
  "v_max_f32 v192, v192, v203\n\t" \
  "v_mfma_f32_16x16x32_bf16 a[32:35], a[144:147], v[128:131], a[32:35]\n\t" \
  "v_cmp_lt_f32 vcc, 0x42800000, v192\n\t" \
  "ds_read_b128 a[196:199], v221 offset:16384\n\t" \
  "v_mfma_f32_16x16x32_bf16 a[32:35], a[148:151], v[132:135], a[32:35]\n\t" \
  "s_cbranch_vccnz .Lsf_c43\n\t" \
  ".Lsf_r43:\n\t" \
  "ds_read_b128 a[200:203], v222 offset:16384\n\t" \
  "v_mfma_f32_16x16x32_bf16 a[48:51], a[152:155], v[128:131], a[48:51]\n\t" \
  "s_lshr_b32 s98, %[sofk], 6\n\t" \
  "v_xor_b32 v234, s98, v244\n\t" \
  "v_mfma_f32_16x16x32_bf16 a[48:51], a[156:159], v[132:135], a[48:51]\n\t" \
  "v_add_u32 v213, %[m], v242\n\t" \
  "v_add_u32 v215, 98304, v213\n\t" \
  "v_mfma_f32_16x16x32_bf16 a[64:67], a[160:163], v[128:131], a[64:67]\n\t" \
  "v_mov_b32 v243, %[tag]\n\t" \
  "v_lshrrev_b32 v254, 3, v233\n\t" \
  "v_mfma_f32_16x16x32_bf16 a[64:67], a[164:167], v[132:135], a[64:67]\n\t" \
  "v_exp_f32 v5, v5\n\t" \
  "v_mfma_f32_16x16x32_bf16 a[80:83], a[168:171], v[128:131], a[80:83]\n\t" \
  "v_exp_f32 v6, v6\n\t" \
  "v_mfma_f32_16x16x32_bf16 a[80:83], a[172:175], v[132:135], a[80:83]\n\t" \
  "v_min_u32 v254, 1, v254\n\t" \
  "v_lshlrev_b32 v254, 31, v254\n\t" \
  "v_mfma_f32_16x16x32_bf16 a[96:99], a[176:179], v[128:131], a[96:99]\n\t" \
  "ds_read_b128 a[204:207], v223 offset:16384\n\t" \
  "v_lshl_or_b32 v254, v233, 9, v254\n\t" \
  "v_mfma_f32_16x16x32_bf16 a[96:99], a[180:183], v[132:135], a[96:99]\n\t" \
  "v_exp_f32 v7, v7\n\t" \
  "v_mfma_f32_16x16x32_bf16 a[112:115], a[184:187], v[128:131], a[112:115]\n\t" \
  "v_exp_f32 v8, v8\n\t" \
  "v_mfma_f32_16x16x32_bf16 a[112:115], a[188:191], v[132:135], a[112:115]\n\t" \
  "s_lshr_b32 m0, %[hl18], 18\n\t" \
  "s_brev_b32 s98, 1\n\t" \
  "v_writelane_b32 v254, s98, m0\n\t" \
  "v_mfma_f32_16x16x32_bf16 v[160:163], v[216:219], v[128:131], v[160:163]\n\t" \
  "s_lshr_b32 s98, %[hl18], 18\n\t" \
  "v_xor_b32 v245, s98, v233\n\t" \
  "v_mfma_f32_16x16x32_bf16 v[160:163], v[216:219], v[132:135], v[160:163]\n\t" \
  "v_cvt_pk_bf16_f32 v128, v0, v1\n\t" \
  "v_cvt_pk_bf16_f32 v129, v2, v3\n\t" \
  "v_mfma_f32_16x16x32_bf16 a[4:7], a[128:131], v[136:139], a[4:7]\n\t" \
  "v_cvt_pk_bf16_f32 v130, v4, v5\n\t" \
  "v_cvt_pk_bf16_f32 v131, v6, v7\n\t" \
  "v_mfma_f32_16x16x32_bf16 a[4:7], a[132:135], v[140:143], a[4:7]\n\t" \
  "ds_read_b128 a[208:211], v220 offset:20480\n\t" \
  "v_and_b32 v245, 31, v245\n\t" \
  "s_lshr_b32 s98, %[hl18], 18\n\t" \
  "s_mul_i32 s98, s98, 127\n\t" \
  "v_add_lshl_u32 v245, v245, s98, 2\n\t" \
  "v_mfma_f32_16x16x32_bf16 a[20:23], a[136:139], v[136:139], a[20:23]\n\t" \
  "ds_read_b128 a[212:215], v221 offset:20480\n\t" \
  "s_add_u32 m0, %[m], 49152\n\t" \
  "v_mfma_f32_16x16x32_bf16 a[20:23], a[140:143], v[140:143], a[20:23]\n\t" \
  "v_exp_f32 v9, v9\n\t" \
  "v_mfma_f32_16x16x32_bf16 a[36:39], a[144:147], v[136:139], a[36:39]\n\t" \
  "v_cvt_pk_bf16_f32 v132, v8, v9\n\t" \
  "ds_read_b128 a[216:219], v222 offset:20480\n\t" \
  "v_mfma_f32_16x16x32_bf16 a[36:39], a[148:151], v[140:143], a[36:39]\n\t" \
  "v_exp_f32 v10, v10\n\t" \
  "v_mfma_f32_16x16x32_bf16 a[52:55], a[152:155], v[136:139], a[52:55]\n\t" \
  "v_exp_f32 v11, v11\n\t" \
  "v_mfma_f32_16x16x32_bf16 a[52:55], a[156:159], v[140:143], a[52:55]\n\t" \
  "v_cvt_pk_bf16_f32 v133, v10, v11\n\t" \
  "s_add_u32 s96, %[sofk], 98304\n\t" \
  "v_mfma_f32_16x16x32_bf16 a[68:71], a[160:163], v[136:139], a[68:71]\n\t" \
  "v_exp_f32 v12, v12\n\t" \
  "v_mfma_f32_16x16x32_bf16 a[68:71], a[164:167], v[140:143], a[68:71]\n\t" \
  "ds_read_b128 a[220:223], v223 offset:20480\n\t" \
  "buffer_load_dwordx4 v224, %[sK], s96 offen offset:0 lds\n\t" \
  "v_mfma_f32_16x16x32_bf16 a[84:87], a[168:171], v[136:139], a[84:87]\n\t" \
  "v_exp_f32 v13, v13\n\t" \
  "v_mfma_f32_16x16x32_bf16 a[84:87], a[172:175], v[140:143], a[84:87]\n\t" \
  "v_cvt_pk_bf16_f32 v134, v12, v13\n\t" \
  "ds_read_b128 a[224:227], v220 offset:24576\n\t" \
  "v_mfma_f32_16x16x32_bf16 a[100:103], a[176:179], v[136:139], a[100:103]\n\t" \
  "v_exp_f32 v14, v14\n\t" \
  "v_mfma_f32_16x16x32_bf16 a[100:103], a[180:183], v[140:143], a[100:103]\n\t" \
  "v_exp_f32 v15, v15\n\t" \
  "v_mfma_f32_16x16x32_bf16 a[116:119], a[184:187], v[136:139], a[116:119]\n\t" \
  "v_cvt_pk_bf16_f32 v135, v14, v15\n\t" \
  "buffer_load_dwordx4 v225, %[sK], s96 offen offset:1024 lds\n\t" \
  "v_mfma_f32_16x16x32_bf16 a[116:119], a[188:191], v[140:143], a[116:119]\n\t" \
  "v_exp_f32 v32, v32\n\t" \
  "v_mfma_f32_16x16x32_bf16 v[164:167], v[216:219], v[136:139], v[164:167]\n\t" \
  "buffer_load_dwordx4 v226, %[sK], s96 offen offset:2048 lds\n\t" \
  "ds_read_b128 a[228:231], v221 offset:24576\n\t" \
  "v_mfma_f32_16x16x32_bf16 v[164:167], v[216:219], v[140:143], v[164:167]\n\t" \
  "v_exp_f32 v33, v33\n\t" \
  "v_mfma_f32_16x16x32_bf16 a[8:11], a[128:131], v[144:147], a[8:11]\n\t" \
  "v_cvt_pk_bf16_f32 v136, v32, v33\n\t" \
  "buffer_load_dwordx4 v227, %[sK], s96 offen offset:3072 lds\n\t" \
  "v_mfma_f32_16x16x32_bf16 a[8:11], a[132:135], v[148:151], a[8:11]\n\t" \
  "v_exp_f32 v34, v34\n\t" \
  "v_mfma_f32_16x16x32_bf16 a[24:27], a[136:139], v[144:147], a[24:27]\n\t" \
  "v_exp_f32 v35, v35\n\t" \
  "v_mfma_f32_16x16x32_bf16 a[24:27], a[140:143], v[148:151], a[24:27]\n\t" \
  "v_cvt_pk_bf16_f32 v137, v34, v35\n\t" \
  "ds_read_b128 a[232:235], v222 offset:24576\n\t" \
  "v_mfma_f32_16x16x32_bf16 a[40:43], a[144:147], v[144:147], a[40:43]\n\t" \
  "v_exp_f32 v36, v36\n\t" \
  "v_mfma_f32_16x16x32_bf16 a[40:43], a[148:151], v[148:151], a[40:43]\n\t" \
  "v_exp_f32 v37, v37\n\t" \
  "v_mfma_f32_16x16x32_bf16 a[56:59], a[152:155], v[144:147], a[56:59]\n\t" \
  "v_cvt_pk_bf16_f32 v138, v36, v37\n\t" \
  "s_add_u32 m0, %[m], 147456\n\t" \
  "v_mfma_f32_16x16x32_bf16 a[56:59], a[156:159], v[148:151], a[56:59]\n\t" \
  "v_exp_f32 v38, v38\n\t" \
  "v_mfma_f32_16x16x32_bf16 a[72:75], a[160:163], v[144:147], a[72:75]\n\t" \
  "ds_read_b128 a[236:239], v223 offset:24576\n\t" \
  "s_add_u32 s96, %[sofk], 81920\n\t" \
  "v_mfma_f32_16x16x32_bf16 a[72:75], a[164:167], v[148:151], a[72:75]\n\t" \
  "v_exp_f32 v39, v39\n\t" \
  "v_mfma_f32_16x16x32_bf16 a[88:91], a[168:171], v[144:147], a[88:91]\n\t" \
  "v_cvt_pk_bf16_f32 v139, v38, v39\n\t" \
  "ds_read_b128 a[240:243], v220 offset:28672\n\t" \
  "v_mfma_f32_16x16x32_bf16 a[88:91], a[172:175], v[148:151], a[88:91]\n\t" \
  "v_exp_f32 v40, v40\n\t" \
  "v_mfma_f32_16x16x32_bf16 a[104:107], a[176:179], v[144:147], a[104:107]\n\t" \
  "v_exp_f32 v41, v41\n\t" \
  "v_mfma_f32_16x16x32_bf16 a[104:107], a[180:183], v[148:151], a[104:107]\n\t" \
  "v_cvt_pk_bf16_f32 v140, v40, v41\n\t" \
  "buffer_load_dwordx4 v230, %[sV], s96 offen offset:0 lds\n\t" \
  "v_mfma_f32_16x16x32_bf16 a[120:123], a[184:187], v[144:147], a[120:123]\n\t" \
  "v_exp_f32 v42, v42\n\t" \
  "v_mfma_f32_16x16x32_bf16 a[120:123], a[188:191], v[148:151], a[120:123]\n\t" \
  "ds_read_b128 a[244:247], v221 offset:28672\n\t" \
  "buffer_load_dwordx4 v231, %[sV], s96 offen offset:1024 lds\n\t" \
  "v_mfma_f32_16x16x32_bf16 v[168:171], v[216:219], v[144:147], v[168:171]\n\t" \
  "v_exp_f32 v43, v43\n\t" \
  "v_mfma_f32_16x16x32_bf16 v[168:171], v[216:219], v[148:151], v[168:171]\n\t" \
  "v_cvt_pk_bf16_f32 v141, v42, v43\n\t" \
  "ds_read_b128 a[248:251], v222 offset:28672\n\t" \
  "v_mfma_f32_16x16x32_bf16 a[12:15], a[128:131], v[152:155], a[12:15]\n\t" \
  "v_exp_f32 v44, v44\n\t" \
  "v_mfma_f32_16x16x32_bf16 a[12:15], a[132:135], v[156:159], a[12:15]\n\t" \
  "v_exp_f32 v45, v45\n\t" \
  "v_mfma_f32_16x16x32_bf16 a[28:31], a[136:139], v[152:155], a[28:31]\n\t" \
  "v_cvt_pk_bf16_f32 v142, v44, v45\n\t" \
  "buffer_load_dwordx4 v230, %[sV], s96 offen offset:2048 lds\n\t" \
  "v_mfma_f32_16x16x32_bf16 a[28:31], a[140:143], v[156:159], a[28:31]\n\t" \
  "v_exp_f32 v46, v46\n\t" \
  "v_mfma_f32_16x16x32_bf16 a[44:47], a[144:147], v[152:155], a[44:47]\n\t" \
  "ds_read_b128 a[252:255], v223 offset:28672\n\t" \
  "buffer_load_dwordx4 v231, %[sV], s96 offen offset:3072 lds\n\t" \
  "v_mfma_f32_16x16x32_bf16 a[44:47], a[148:151], v[156:159], a[44:47]\n\t" \
  "v_exp_f32 v47, v47\n\t" \
  "v_mfma_f32_16x16x32_bf16 a[60:63], a[152:155], v[152:155], a[60:63]\n\t" \
  "v_cvt_pk_bf16_f32 v143, v46, v47\n\t" \
  "v_mfma_f32_16x16x32_bf16 a[60:63], a[156:159], v[156:159], a[60:63]\n\t" \
  "v_exp_f32 v64, v64\n\t" \
  "v_mfma_f32_16x16x32_bf16 a[76:79], a[160:163], v[152:155], a[76:79]\n\t" \
  "v_exp_f32 v65, v65\n\t" \
  "v_mfma_f32_16x16x32_bf16 a[76:79], a[164:167], v[156:159], a[76:79]\n\t" \
  "v_cvt_pk_bf16_f32 v144, v64, v65\n\t" \
  "v_mfma_f32_16x16x32_bf16 a[92:95], a[168:171], v[152:155], a[92:95]\n\t" \
  "s_add_u32 m0, %[m], 32768\n\t" \
  "s_mov_b32 s96, 12288\n\t" \
  "buffer_load_dwordx4 v232, %[sG], s96 offen offset:2048 lds\n\t" \
  "v_mfma_f32_16x16x32_bf16 a[92:95], a[172:175], v[156:159], a[92:95]\n\t" \
  "v_exp_f32 v66, v66\n\t" \
  "v_mfma_f32_16x16x32_bf16 a[108:111], a[176:179], v[152:155], a[108:111]\n\t" \
  "v_exp_f32 v67, v67\n\t" \
  "v_mfma_f32_16x16x32_bf16 a[108:111], a[180:183], v[156:159], a[108:111]\n\t" \
  "v_cvt_pk_bf16_f32 v145, v66, v67\n\t" \
  "v_mfma_f32_16x16x32_bf16 a[124:127], a[184:187], v[152:155], a[124:127]\n\t" \
  "s_mov_b32 s96, 18432\n\t" \
  "buffer_load_dwordx4 v232, %[sG], s96 offen offset:3072 lds\n\t" \
  "v_mfma_f32_16x16x32_bf16 a[124:127], a[188:191], v[156:159], a[124:127]\n\t" \
  "v_exp_f32 v68, v68\n\t" \
  "v_mfma_f32_16x16x32_bf16 v[172:175], v[216:219], v[152:155], v[172:175]\n\t" \
  "v_exp_f32 v69, v69\n\t" \
  "v_mfma_f32_16x16x32_bf16 v[172:175], v[216:219], v[156:159], v[172:175]\n\t" \
  "v_cvt_pk_bf16_f32 v146, v68, v69\n\t" \
  "s_cmp_lg_u32 s99, 0\n\t" \
  "s_cbranch_scc1 .Lsf_p4\n\t" \
  ".Lsf_q4:\n\t" \
  "s_waitcnt lgkmcnt(0)\n\t" \
  "v_mfma_f32_16x16x32_bf16 v[0:3], a[192:195], v[16:19], v[176:179]\n\t" \
  "s_add_u32 s98, %[pf2], 49152\n\t" \
  "buffer_load_dword %[pfd], v202, %[sM], s98 offen\n\t" \
  "v_mfma_f32_16x16x32_bf16 v[0:3], a[196:199], v[20:23], v[0:3]\n\t" \
  "ds_read_b64_tr_b16 a[128:129], v228 offset:0\n\t" \
  "v_lshrrev_b32 v252, 3, v233\n\t" \
  "v_mfma_f32_16x16x32_bf16 v[0:3], a[200:203], v[24:27], v[0:3]\n\t" \
  "v_exp_f32 v70, v70\n\t" \
  "v_mfma_f32_16x16x32_bf16 v[0:3], a[204:207], v[28:31], v[0:3]\n\t" \
  "v_exp_f32 v71, v71\n\t" \
  "v_mfma_f32_16x16x32_bf16 v[4:7], a[220:223], v[28:31], v[176:179]\n\t" \
  "v_cvt_pk_bf16_f32 v147, v70, v71\n\t" \
  "ds_read_b64_tr_b16 a[130:131], v228 offset:4096\n\t" \
  "v_mfma_f32_16x16x32_bf16 v[4:7], a[216:219], v[24:27], v[4:7]\n\t" \
  "ds_read_b64_tr_b16 a[132:133], v228 offset:8192\n\t" \
  "v_mul_u32_u24 v247, 98176, v252\n\t" \
  "v_mfma_f32_16x16x32_bf16 v[4:7], a[212:215], v[20:23], v[4:7]\n\t" \
  "v_max3_f32 v192, v0, v1, v2\n\t" \
  "ds_read_b64_tr_b16 a[134:135], v228 offset:12288\n\t" \
  "v_mfma_f32_16x16x32_bf16 v[4:7], a[208:211], v[16:19], v[4:7]\n\t" \
  "v_exp_f32 v72, v72\n\t" \
  "v_mfma_f32_16x16x32_bf16 v[8:11], a[224:227], v[16:19], v[176:179]\n\t" \
  "ds_read_b64_tr_b16 a[136:137], v229 offset:0\n\t" \
  "v_lshl_add_u32 v247, v233, 4, v247\n\t" \
  "v_mfma_f32_16x16x32_bf16 v[8:11], a[228:231], v[20:23], v[8:11]\n\t" \
  "v_exp_f32 v73, v73\n\t" \
  "v_mfma_f32_16x16x32_bf16 v[8:11], a[232:235], v[24:27], v[8:11]\n\t" \
  "v_cvt_pk_bf16_f32 v148, v72, v73\n\t" \
  "v_max3_f32 v203, v3, v4, v5\n\t" \
  "v_mfma_f32_16x16x32_bf16 v[8:11], a[236:239], v[28:31], v[8:11]\n\t" \
  "v_max3_f32 v192, v192, v6, v7\n\t" \
  "ds_read_b64_tr_b16 a[138:139], v229 offset:4096\n\t" \
  "v_mfma_f32_16x16x32_bf16 v[12:15], a[252:255], v[28:31], v[176:179]\n\t" \
  "ds_read_b64_tr_b16 a[140:141], v229 offset:8192\n\t" \
  "v_lshlrev_b32 v252, 4, v252\n\t" \
  "v_mfma_f32_16x16x32_bf16 v[12:15], a[248:251], v[24:27], v[12:15]\n\t" \
  "s_add_u32 s98, %[pf2], 73728\n\t" \
  "buffer_load_dword %[pfd], v202, %[sM], s98 offen\n\t" \
  "v_mfma_f32_16x16x32_bf16 v[12:15], a[244:247], v[20:23], v[12:15]\n\t" \
  "v_max3_f32 v203, v203, v8, v9\n\t" \
  "v_max3_f32 v192, v192, v10, v11\n\t" \
  "v_mfma_f32_16x16x32_bf16 v[12:15], a[240:243], v[16:19], v[12:15]\n\t" \
  "ds_read_b64_tr_b16 a[142:143], v229 offset:12288\n\t" \
  "ds_read_b64_tr_b16 a[144:145], v228 offset:256\n\t" \
  "v_mfma_f32_16x16x32_bf16 v[32:35], a[192:195], v[48:51], v[180:183]\n\t" \
  "v_exp_f32 v74, v74\n\t" \
  "v_mfma_f32_16x16x32_bf16 v[32:35], a[196:199], v[52:55], v[32:35]\n\t" \
  "v_exp_f32 v75, v75\n\t" \
  "v_mfma_f32_16x16x32_bf16 v[32:35], a[200:203], v[56:59], v[32:35]\n\t" \
  "v_cvt_pk_bf16_f32 v149, v74, v75\n\t" \
  "ds_read_b64_tr_b16 a[146:147], v228 offset:4352\n\t" \
  "v_mfma_f32_16x16x32_bf16 v[32:35], a[204:207], v[60:63], v[32:35]\n\t" \
  "v_max3_f32 v203, v203, v12, v13\n\t" \
  "v_max3_f32 v192, v192, v14, v15\n\t" \
  "v_mfma_f32_16x16x32_bf16 v[36:39], a[220:223], v[60:63], v[180:183]\n\t" \
  "v_max_f32 v192, v192, v203\n\t" \
  "ds_read_b64_tr_b16 a[148:149], v228 offset:8448\n\t" \
  "v_mfma_f32_16x16x32_bf16 v[36:39], a[216:219], v[56:59], v[36:39]\n\t" \
  "v_cmp_lt_f32 vcc, 0x42800000, v192\n\t" \
  "ds_read_b64_tr_b16 a[150:151], v228 offset:12544\n\t" \
  "v_mfma_f32_16x16x32_bf16 v[36:39], a[212:215], v[52:55], v[36:39]\n\t" \
  "s_cbranch_vccnz .Lsf_c50\n\t" \
  ".Lsf_r50:\n\t" \
  "v_max3_f32 v192, v32, v33, v34\n\t" \
  "v_mfma_f32_16x16x32_bf16 v[36:39], a[208:211], v[48:51], v[36:39]\n\t" \
  "ds_read_b64_tr_b16 a[152:153], v229 offset:256\n\t" \
  "ds_read_b64_tr_b16 a[154:155], v229 offset:4352\n\t" \
  "v_mfma_f32_16x16x32_bf16 v[40:43], a[224:227], v[48:51], v[180:183]\n\t" \
  "v_exp_f32 v76, v76\n\t" \
  "v_mfma_f32_16x16x32_bf16 v[40:43], a[228:231], v[52:55], v[40:43]\n\t" \
  "v_exp_f32 v77, v77\n\t" \
  "v_mfma_f32_16x16x32_bf16 v[40:43], a[232:235], v[56:59], v[40:43]\n\t" \
  "v_cvt_pk_bf16_f32 v150, v76, v77\n\t" \
  "v_max3_f32 v203, v35, v36, v37\n\t" \
  "v_mfma_f32_16x16x32_bf16 v[40:43], a[236:239], v[60:63], v[40:43]\n\t" \
  "v_max3_f32 v192, v192, v38, v39\n\t" \
  "ds_read_b64_tr_b16 a[156:157], v229 offset:8448\n\t" \
  "v_mfma_f32_16x16x32_bf16 v[44:47], a[252:255], v[60:63], v[180:183]\n\t" \
  "v_exp_f32 v78, v78\n\t" \
  "v_mfma_f32_16x16x32_bf16 v[44:47], a[248:251], v[56:59], v[44:47]\n\t" \
  "ds_read_b64_tr_b16 a[158:159], v229 offset:12544\n\t" \
  "ds_read_b64_tr_b16 a[160:161], v228 offset:512\n\t" \
  "v_mfma_f32_16x16x32_bf16 v[44:47], a[244:247], v[52:55], v[44:47]\n\t" \
  "v_max3_f32 v203, v203, v40, v41\n\t" \
  "v_max3_f32 v192, v192, v42, v43\n\t" \
  "v_mfma_f32_16x16x32_bf16 v[44:47], a[240:243], v[48:51], v[44:47]\n\t" \
  "v_exp_f32 v79, v79\n\t" \
  "v_mfma_f32_16x16x32_bf16 v[64:67], a[192:195], v[80:83], v[184:187]\n\t" \
  "v_cvt_pk_bf16_f32 v151, v78, v79\n\t" \
  "ds_read_b64_tr_b16 a[162:163], v228 offset:4608\n\t" \
  "v_mfma_f32_16x16x32_bf16 v[64:67], a[196:199], v[84:87], v[64:67]\n\t" \
  "v_exp_f32 v96, v96\n\t" \
  "v_mfma_f32_16x16x32_bf16 v[64:67], a[200:203], v[88:91], v[64:67]\n\t" \
  "ds_read_b64_tr_b16 a[164:165], v228 offset:8704\n\t" \
  "v_max3_f32 v203, v203, v44, v45\n\t" \
  "v_mfma_f32_16x16x32_bf16 v[64:67], a[204:207], v[92:95], v[64:67]\n\t" \
  "v_max3_f32 v192, v192, v46, v47\n\t" \
  "v_max_f32 v192, v192, v203\n\t" \
  "v_mfma_f32_16x16x32_bf16 v[68:71], a[220:223], v[92:95], v[184:187]\n\t" \
  "v_cmp_lt_f32 vcc, 0x42800000, v192\n\t" \
  "ds_read_b64_tr_b16 a[166:167], v228 offset:12800\n\t" \
  "v_mfma_f32_16x16x32_bf16 v[68:71], a[216:219], v[88:91], v[68:71]\n\t" \
  "s_cbranch_vccnz .Lsf_c51\n\t" \
  ".Lsf_r51:\n\t" \
  "ds_read_b64_tr_b16 a[168:169], v229 offset:512\n\t" \
  "v_mfma_f32_16x16x32_bf16 v[68:71], a[212:215], v[84:87], v[68:71]\n\t" \
  "v_max3_f32 v192, v64, v65, v66\n\t" \
  "ds_read_b64_tr_b16 a[170:171], v229 offset:4608\n\t" \
  "v_mfma_f32_16x16x32_bf16 v[68:71], a[208:211], v[80:83], v[68:71]\n\t" \
  "v_exp_f32 v97, v97\n\t" \
  "v_mfma_f32_16x16x32_bf16 v[72:75], a[224:227], v[80:83], v[184:187]\n\t" \
  "v_cvt_pk_bf16_f32 v152, v96, v97\n\t" \
  "ds_read_b64_tr_b16 a[172:173], v229 offset:8704\n\t" \
  "v_mfma_f32_16x16x32_bf16 v[72:75], a[228:231], v[84:87], v[72:75]\n\t" \
  "v_exp_f32 v98, v98\n\t" \
  "v_mfma_f32_16x16x32_bf16 v[72:75], a[232:235], v[88:91], v[72:75]\n\t" \
  "v_exp_f32 v99, v99\n\t" \
  "v_mfma_f32_16x16x32_bf16 v[72:75], a[236:239], v[92:95], v[72:75]\n\t" \
  "v_cvt_pk_bf16_f32 v153, v98, v99\n\t" \
  "v_max3_f32 v203, v67, v68, v69\n\t" \
  "v_mfma_f32_16x16x32_bf16 v[76:79], a[252:255], v[92:95], v[184:187]\n\t" \
  "v_max3_f32 v192, v192, v70, v71\n\t" \
  "ds_read_b64_tr_b16 a[174:175], v229 offset:12800\n\t" \
  "v_mfma_f32_16x16x32_bf16 v[76:79], a[248:251], v[88:91], v[76:79]\n\t" \
  "ds_read_b64_tr_b16 a[176:177], v228 offset:768\n\t" \
  "ds_read_b64_tr_b16 a[178:179], v228 offset:4864\n\t" \
  "v_mfma_f32_16x16x32_bf16 v[76:79], a[244:247], v[84:87], v[76:79]\n\t" \
  "v_max3_f32 v203, v203, v72, v73\n\t" \
  "v_max3_f32 v192, v192, v74, v75\n\t" \
  "v_mfma_f32_16x16x32_bf16 v[76:79], a[240:243], v[80:83], v[76:79]\n\t" \
  "v_exp_f32 v100, v100\n\t" \
  "v_mfma_f32_16x16x32_bf16 v[96:99], a[192:195], v[112:115], v[188:191]\n\t" \
  "ds_read_b64_tr_b16 a[180:181], v228 offset:8960\n\t" \
  "ds_read_b64_tr_b16 a[182:183], v228 offset:13056\n\t" \
  "v_mfma_f32_16x16x32_bf16 v[96:99], a[196:199], v[116:119], v[96:99]\n\t" \
  "v_exp_f32 v101, v101\n\t" \
  "v_mfma_f32_16x16x32_bf16 v[96:99], a[200:203], v[120:123], v[96:99]\n\t" \
  "v_cvt_pk_bf16_f32 v154, v100, v101\n\t" \
  "v_max3_f32 v203, v203, v76, v77\n\t" \
  "v_mfma_f32_16x16x32_bf16 v[96:99], a[204:207], v[124:127], v[96:99]\n\t" \
  "v_max3_f32 v192, v192, v78, v79\n\t" \
  "v_max_f32 v192, v192, v203\n\t" \
  "v_exp_f32 v102, v102\n\t" \
  "v_exp_f32 v103, v103\n\t" \
  "s_nop 0\n\t" \
  "v_cvt_pk_bf16_f32 v155, v102, v103\n\t" \
  "v_mfma_f32_16x16x32_bf16 v[100:103], a[220:223], v[124:127], v[188:191]\n\t" \
  "v_cmp_lt_f32 vcc, 0x42800000, v192\n\t" \
  "ds_read_b64_tr_b16 a[184:185], v229 offset:768\n\t" \
  "v_mfma_f32_16x16x32_bf16 v[100:103], a[216:219], v[120:123], v[100:103]\n\t" \
  "s_cbranch_vccnz .Lsf_c52\n\t" \
  ".Lsf_r52:\n\t" \
  "ds_read_b64_tr_b16 a[186:187], v229 offset:4864\n\t" \
  "v_mfma_f32_16x16x32_bf16 v[100:103], a[212:215], v[116:119], v[100:103]\n\t" \
  "v_max3_f32 v192, v96, v97, v98\n\t" \
  "ds_read_b64_tr_b16 a[188:189], v229 offset:8960\n\t" \
  "v_mfma_f32_16x16x32_bf16 v[100:103], a[208:211], v[112:115], v[100:103]\n\t" \
  "v_exp_f32 v104, v104\n\t" \
  "v_exp_f32 v105, v105\n\t" \
  "v_exp_f32 v106, v106\n\t" \
  "v_exp_f32 v107, v107\n\t" \
  "v_cvt_pk_bf16_f32 v156, v104, v105\n\t" \
  "v_cvt_pk_bf16_f32 v157, v106, v107\n\t" \
  "v_mfma_f32_16x16x32_bf16 v[104:107], a[224:227], v[112:115], v[188:191]\n\t" \
  "v_exp_f32 v108, v108\n\t" \
  "v_mfma_f32_16x16x32_bf16 v[104:107], a[228:231], v[116:119], v[104:107]\n\t" \
  "ds_read_b64_tr_b16 a[190:191], v229 offset:13056\n\t" \
  "v_max3_f32 v203, v99, v100, v101\n\t" \
  "v_mfma_f32_16x16x32_bf16 v[104:107], a[232:235], v[120:123], v[104:107]\n\t" \
  "v_max3_f32 v192, v192, v102, v103\n\t" \
  "v_mfma_f32_16x16x32_bf16 v[104:107], a[236:239], v[124:127], v[104:107]\n\t" \
  "v_exp_f32 v109, v109\n\t" \
  "v_exp_f32 v110, v110\n\t" \
  "v_exp_f32 v111, v111\n\t" \
  "v_cvt_pk_bf16_f32 v158, v108, v109\n\t" \
  "v_cvt_pk_bf16_f32 v159, v110, v111\n\t" \
  "v_mfma_f32_16x16x32_bf16 v[108:111], a[252:255], v[124:127], v[188:191]\n\t" \
  "v_exp_f32 v0, v0\n\t" \
  "v_mfma_f32_16x16x32_bf16 v[108:111], a[248:251], v[120:123], v[108:111]\n\t" \
  "v_exp_f32 v1, v1\n\t" \
  "v_mfma_f32_16x16x32_bf16 v[108:111], a[244:247], v[116:119], v[108:111]\n\t" \
  "v_max3_f32 v203, v203, v104, v105\n\t" \
  "v_max3_f32 v192, v192, v106, v107\n\t" \
  "v_mfma_f32_16x16x32_bf16 v[108:111], a[240:243], v[112:115], v[108:111]\n\t" \
  "v_exp_f32 v2, v2\n\t" \
  "s_waitcnt vmcnt(4) lgkmcnt(0)\n\t" \
  "s_barrier\n\t" \
  "v_mfma_f32_16x16x32_bf16 a[0:3], a[128:131], v[128:131], a[0:3]\n\t" \
  "v_exp_f32 v3, v3\n\t" \
  "v_mfma_f32_16x16x32_bf16 a[0:3], a[132:135], v[132:135], a[0:3]\n\t" \
  "v_exp_f32 v4, v4\n\t" \
  "v_mfma_f32_16x16x32_bf16 a[16:19], a[136:139], v[128:131], a[16:19]\n\t" \
  "ds_read_b128 a[192:195], v220 offset:0\n\t" \
  "v_max3_f32 v203, v203, v108, v109\n\t" \
  "v_mfma_f32_16x16x32_bf16 a[16:19], a[140:143], v[132:135], a[16:19]\n\t" \
  "v_max3_f32 v192, v192, v110, v111\n\t" \
  "v_max_f32 v192, v192, v203\n\t" \
  "v_mfma_f32_16x16x32_bf16 a[32:35], a[144:147], v[128:131], a[32:35]\n\t" \
  "v_cmp_lt_f32 vcc, 0x42800000, v192\n\t" \
  "ds_read_b128 a[196:199], v221 offset:0\n\t" \
  "v_mfma_f32_16x16x32_bf16 a[32:35], a[148:151], v[132:135], a[32:35]\n\t" \
  "s_cbranch_vccnz .Lsf_c53\n\t" \
  ".Lsf_r53:\n\t" \
  "ds_read_b128 a[200:203], v222 offset:0\n\t" \
  "v_mfma_f32_16x16x32_bf16 a[48:51], a[152:155], v[128:131], a[48:51]\n\t" \
  "v_exp_f32 v5, v5\n\t" \
  "v_mfma_f32_16x16x32_bf16 a[48:51], a[156:159], v[132:135], a[48:51]\n\t" \
  "v_exp_f32 v6, v6\n\t" \
  "v_mfma_f32_16x16x32_bf16 a[64:67], a[160:163], v[128:131], a[64:67]\n\t" \
  "v_exp_f32 v7, v7\n\t" \
  "v_mfma_f32_16x16x32_bf16 a[64:67], a[164:167], v[132:135], a[64:67]\n\t" \
  "v_exp_f32 v8, v8\n\t" \
  "v_mfma_f32_16x16x32_bf16 a[80:83], a[168:171], v[128:131], a[80:83]\n\t" \
  "v_exp_f32 v9, v9\n\t" \
  "v_mfma_f32_16x16x32_bf16 a[80:83], a[172:175], v[132:135], a[80:83]\n\t" \
  "v_exp_f32 v10, v10\n\t" \
  "v_mfma_f32_16x16x32_bf16 a[96:99], a[176:179], v[128:131], a[96:99]\n\t" \
  "v_exp_f32 v11, v11\n\t" \
  "v_mfma_f32_16x16x32_bf16 a[96:99], a[180:183], v[132:135], a[96:99]\n\t" \
  "v_exp_f32 v12, v12\n\t" \
  "v_mfma_f32_16x16x32_bf16 a[112:115], a[184:187], v[128:131], a[112:115]\n\t" \
  "ds_read_b128 a[204:207], v223 offset:0\n\t" \
  "ds_read_b128 a[208:211], v220 offset:4096\n\t" \
  "v_mfma_f32_16x16x32_bf16 a[112:115], a[188:191], v[132:135], a[112:115]\n\t" \
  "v_exp_f32 v13, v13\n\t" \
  "v_mfma_f32_16x16x32_bf16 v[160:163], v[216:219], v[128:131], v[160:163]\n\t" \
  "v_exp_f32 v14, v14\n\t" \
  "v_mfma_f32_16x16x32_bf16 v[160:163], v[216:219], v[132:135], v[160:163]\n\t" \
  "v_cvt_pk_bf16_f32 v128, v0, v1\n\t" \
  "v_cvt_pk_bf16_f32 v129, v2, v3\n\t" \
  "v_mfma_f32_16x16x32_bf16 a[4:7], a[128:131], v[136:139], a[4:7]\n\t" \
  "v_cvt_pk_bf16_f32 v130, v4, v5\n\t" \
  "v_cvt_pk_bf16_f32 v131, v6, v7\n\t" \
  "v_mfma_f32_16x16x32_bf16 a[4:7], a[132:135], v[140:143], a[4:7]\n\t" \
  "v_cvt_pk_bf16_f32 v132, v8, v9\n\t" \
  "v_cvt_pk_bf16_f32 v133, v10, v11\n\t" \
  "v_mfma_f32_16x16x32_bf16 a[20:23], a[136:139], v[136:139], a[20:23]\n\t" \
  "v_cvt_pk_bf16_f32 v134, v12, v13\n\t" \
  "ds_read_b128 a[212:215], v221 offset:4096\n\t" \
  "v_mfma_f32_16x16x32_bf16 a[20:23], a[140:143], v[140:143], a[20:23]\n\t" \
  "v_exp_f32 v15, v15\n\t" \
  "v_mfma_f32_16x16x32_bf16 a[36:39], a[144:147], v[136:139], a[36:39]\n\t" \
  "v_cvt_pk_bf16_f32 v135, v14, v15\n\t" \
  "s_add_u32 m0, %[m], 65536\n\t" \
  "v_mfma_f32_16x16x32_bf16 a[36:39], a[148:151], v[140:143], a[36:39]\n\t" \
  "v_exp_f32 v32, v32\n\t" \
  "v_mfma_f32_16x16x32_bf16 a[52:55], a[152:155], v[136:139], a[52:55]\n\t" \
  "ds_read_b128 a[216:219], v222 offset:4096\n\t" \
  "s_add_u32 s96, %[sofk], 114688\n\t" \
  "v_mfma_f32_16x16x32_bf16 a[52:55], a[156:159], v[140:143], a[52:55]\n\t" \
  "v_exp_f32 v33, v33\n\t" \
  "v_mfma_f32_16x16x32_bf16 a[68:71], a[160:163], v[136:139], a[68:71]\n\t" \
  "v_exp_f32 v34, v34\n\t" \
  "v_mfma_f32_16x16x32_bf16 a[68:71], a[164:167], v[140:143], a[68:71]\n\t" \
  "ds_read_b128 a[220:223], v223 offset:4096\n\t" \
  "buffer_load_dwordx4 v224, %[sK], s96 offen offset:0 lds\n\t" \
  "v_mfma_f32_16x16x32_bf16 a[84:87], a[168:171], v[136:139], a[84:87]\n\t" \
  "v_exp_f32 v35, v35\n\t" \
  "v_mfma_f32_16x16x32_bf16 a[84:87], a[172:175], v[140:143], a[84:87]\n\t" \
  "v_exp_f32 v36, v36\n\t" \
  "v_mfma_f32_16x16x32_bf16 a[100:103], a[176:179], v[136:139], a[100:103]\n\t" \
  "v_exp_f32 v37, v37\n\t" \
  "v_mfma_f32_16x16x32_bf16 a[100:103], a[180:183], v[140:143], a[100:103]\n\t" \
  "ds_read_b128 a[224:227], v220 offset:8192\n\t" \
  "buffer_load_dwordx4 v225, %[sK], s96 offen offset:1024 lds\n\t" \
  "v_mfma_f32_16x16x32_bf16 a[116:119], a[184:187], v[136:139], a[116:119]\n\t" \
  "v_exp_f32 v38, v38\n\t" \
  "v_mfma_f32_16x16x32_bf16 a[116:119], a[188:191], v[140:143], a[116:119]\n\t" \
  "v_exp_f32 v39, v39\n\t" \
  "v_mfma_f32_16x16x32_bf16 v[164:167], v[216:219], v[136:139], v[164:167]\n\t" \
  "buffer_load_dwordx4 v226, %[sK], s96 offen offset:2048 lds\n\t" \
  "ds_read_b128 a[228:231], v221 offset:8192\n\t" \
  "v_mfma_f32_16x16x32_bf16 v[164:167], v[216:219], v[140:143], v[164:167]\n\t" \
  "v_cvt_pk_bf16_f32 v136, v32, v33\n\t" \
  "v_cvt_pk_bf16_f32 v137, v34, v35\n\t" \
  "v_mfma_f32_16x16x32_bf16 a[8:11], a[128:131], v[144:147], a[8:11]\n\t" \
  "v_cvt_pk_bf16_f32 v138, v36, v37\n\t" \
  "v_cvt_pk_bf16_f32 v139, v38, v39\n\t" \
  "v_mfma_f32_16x16x32_bf16 a[8:11], a[132:135], v[148:151], a[8:11]\n\t" \
  "v_exp_f32 v40, v40\n\t" \
  "v_mfma_f32_16x16x32_bf16 a[24:27], a[136:139], v[144:147], a[24:27]\n\t" \
  "buffer_load_dwordx4 v227, %[sK], s96 offen offset:3072 lds\n\t" \
  "ds_read_b128 a[232:235], v222 offset:8192\n\t" \
  "v_mfma_f32_16x16x32_bf16 a[24:27], a[140:143], v[148:151], a[24:27]\n\t" \
  "v_exp_f32 v41, v41\n\t" \
  "v_mfma_f32_16x16x32_bf16 a[40:43], a[144:147], v[144:147], a[40:43]\n\t" \
  "v_cvt_pk_bf16_f32 v140, v40, v41\n\t" \
  "s_add_u32 m0, %[m], 81920\n\t" \
  "v_mfma_f32_16x16x32_bf16 a[40:43], a[148:151], v[148:151], a[40:43]\n\t" \
  "v_exp_f32 v42, v42\n\t" \
  "v_mfma_f32_16x16x32_bf16 a[56:59], a[152:155], v[144:147], a[56:59]\n\t" \
  "v_exp_f32 v43, v43\n\t" \
  "v_mfma_f32_16x16x32_bf16 a[56:59], a[156:159], v[148:151], a[56:59]\n\t" \
  "v_cvt_pk_bf16_f32 v141, v42, v43\n\t" \
  "ds_read_b128 a[236:239], v223 offset:8192\n\t" \
  "v_mfma_f32_16x16x32_bf16 a[72:75], a[160:163], v[144:147], a[72:75]\n\t" \
  "v_exp_f32 v44, v44\n\t" \
  "v_mfma_f32_16x16x32_bf16 a[72:75], a[164:167], v[148:151], a[72:75]\n\t" \
  "s_add_u32 s96, %[sofk], 98304\n\t" \
  "ds_read_b128 a[240:243], v220 offset:12288\n\t" \
  "v_mfma_f32_16x16x32_bf16 a[88:91], a[168:171], v[144:147], a[88:91]\n\t" \
  "v_exp_f32 v45, v45\n\t" \
  "v_mfma_f32_16x16x32_bf16 a[88:91], a[172:175], v[148:151], a[88:91]\n\t" \
  "v_cvt_pk_bf16_f32 v142, v44, v45\n\t" \
  "buffer_load_dwordx4 v230, %[sV], s96 offen offset:0 lds\n\t" \
  "v_mfma_f32_16x16x32_bf16 a[104:107], a[176:179], v[144:147], a[104:107]\n\t" \
  "v_exp_f32 v46, v46\n\t" \
  "v_mfma_f32_16x16x32_bf16 a[104:107], a[180:183], v[148:151], a[104:107]\n\t" \
  "v_exp_f32 v47, v47\n\t" \
  "v_mfma_f32_16x16x32_bf16 a[120:123], a[184:187], v[144:147], a[120:123]\n\t" \
  "v_cvt_pk_bf16_f32 v143, v46, v47\n\t" \
  "ds_read_b128 a[244:247], v221 offset:12288\n\t" \
  "v_mfma_f32_16x16x32_bf16 a[120:123], a[188:191], v[148:151], a[120:123]\n\t" \
  "v_exp_f32 v64, v64\n\t" \
  "v_mfma_f32_16x16x32_bf16 v[168:171], v[216:219], v[144:147], v[168:171]\n\t" \
  "v_exp_f32 v65, v65\n\t" \
  "v_mfma_f32_16x16x32_bf16 v[168:171], v[216:219], v[148:151], v[168:171]\n\t" \
  "v_cvt_pk_bf16_f32 v144, v64, v65\n\t" \
  "buffer_load_dwordx4 v231, %[sV], s96 offen offset:1024 lds\n\t" \
  "v_mfma_f32_16x16x32_bf16 a[12:15], a[128:131], v[152:155], a[12:15]\n\t" \
  "v_exp_f32 v66, v66\n\t" \
  "v_mfma_f32_16x16x32_bf16 a[12:15], a[132:135], v[156:159], a[12:15]\n\t" \
  "ds_read_b128 a[248:251], v222 offset:12288\n\t" \
  "buffer_load_dwordx4 v230, %[sV], s96 offen offset:2048 lds\n\t" \
  "v_mfma_f32_16x16x32_bf16 a[28:31], a[136:139], v[152:155], a[28:31]\n\t" \
  "v_exp_f32 v67, v67\n\t" \
  "v_mfma_f32_16x16x32_bf16 a[28:31], a[140:143], v[156:159], a[28:31]\n\t" \
  "v_cvt_pk_bf16_f32 v145, v66, v67\n\t" \
  "ds_read_b128 a[252:255], v223 offset:12288\n\t" \
  "v_mfma_f32_16x16x32_bf16 a[44:47], a[144:147], v[152:155], a[44:47]\n\t" \
  "v_exp_f32 v68, v68\n\t" \
  "v_mfma_f32_16x16x32_bf16 a[44:47], a[148:151], v[156:159], a[44:47]\n\t" \
  "v_exp_f32 v69, v69\n\t" \
  "v_mfma_f32_16x16x32_bf16 a[60:63], a[152:155], v[152:155], a[60:63]\n\t" \
  "v_cvt_pk_bf16_f32 v146, v68, v69\n\t" \
  "buffer_load_dwordx4 v231, %[sV], s96 offen offset:3072 lds\n\t" \
  "v_mfma_f32_16x16x32_bf16 a[60:63], a[156:159], v[156:159], a[60:63]\n\t" \
  "v_exp_f32 v70, v70\n\t" \
  "v_mfma_f32_16x16x32_bf16 a[76:79], a[160:163], v[152:155], a[76:79]\n\t" \
  "v_exp_f32 v71, v71\n\t" \
  "v_mfma_f32_16x16x32_bf16 a[76:79], a[164:167], v[156:159], a[76:79]\n\t" \
  "v_cvt_pk_bf16_f32 v147, v70, v71\n\t" \
  "v_mfma_f32_16x16x32_bf16 a[92:95], a[168:171], v[152:155], a[92:95]\n\t" \
  "s_add_u32 m0, %[m], 98304\n\t" \
  "s_mov_b32 s96, 28672\n\t" \
  "buffer_load_dwordx4 v232, %[sG], s96 offen offset:0 lds\n\t" \
  "v_mfma_f32_16x16x32_bf16 a[92:95], a[172:175], v[156:159], a[92:95]\n\t" \
  "v_lshrrev_b32 v205, 1, v233\n\t" \
  "v_lshlrev_b32 v205, 2, v205\n\t" \
  "v_mfma_f32_16x16x32_bf16 a[108:111], a[176:179], v[152:155], a[108:111]\n\t" \
  "v_add_u32 v205, 8192, v205\n\t" \
  "buffer_load_dword v209, v205, %[ws], 0 offen sc1\n\t" \
  "v_mfma_f32_16x16x32_bf16 a[108:111], a[180:183], v[156:159], a[108:111]\n\t" \
  "s_mov_b32 s96, 34816\n\t" \
  "buffer_load_dwordx4 v232, %[sG], s96 offen offset:1024 lds\n\t" \
  "v_mfma_f32_16x16x32_bf16 a[124:127], a[184:187], v[152:155], a[124:127]\n\t" \
  "v_exp_f32 v72, v72\n\t" \
  "v_mfma_f32_16x16x32_bf16 a[124:127], a[188:191], v[156:159], a[124:127]\n\t" \
  "v_exp_f32 v73, v73\n\t" \
  "v_mfma_f32_16x16x32_bf16 v[172:175], v[216:219], v[152:155], v[172:175]\n\t" \
  "v_cvt_pk_bf16_f32 v148, v72, v73\n\t" \
  "v_mfma_f32_16x16x32_bf16 v[172:175], v[216:219], v[156:159], v[172:175]\n\t" \
  "v_exp_f32 v74, v74\n\t" \
  "s_cmp_lg_u32 s99, 0\n\t" \
  "s_cbranch_scc1 .Lsf_p5\n\t" \
  ".Lsf_q5:\n\t" \
  "s_waitcnt lgkmcnt(0)\n\t" \
  "v_mfma_f32_16x16x32_bf16 v[0:3], a[192:195], v[16:19], v[176:179]\n\t" \
  "s_mul_i32 s98, %[sofk], 5\n\t" \
  "s_xor_b32 s98, s98, 8192\n\t" \
  "s_lshr_b32 s98, s98, 1\n\t" \
  "buffer_load_dword %[pfd], v204, %[sD], s98 offen\n\t" \
  "v_mfma_f32_16x16x32_bf16 v[0:3], a[196:199], v[20:23], v[0:3]\n\t" \
  "v_exp_f32 v75, v75\n\t" \
  "v_mfma_f32_16x16x32_bf16 v[0:3], a[200:203], v[24:27], v[0:3]\n\t" \
  "v_cvt_pk_bf16_f32 v149, v74, v75\n\t" \
  "ds_read_b64_tr_b16 a[128:129], v237 offset:0\n\t" \
  "v_mfma_f32_16x16x32_bf16 v[0:3], a[204:207], v[28:31], v[0:3]\n\t" \
  "v_exp_f32 v76, v76\n\t" \
  "v_mfma_f32_16x16x32_bf16 v[4:7], a[220:223], v[28:31], v[176:179]\n\t" \
  "ds_read_b64_tr_b16 a[130:131], v237 offset:4096\n\t" \
  "ds_read_b64_tr_b16 a[132:133], v237 offset:8192\n\t" \
  "v_mfma_f32_16x16x32_bf16 v[4:7], a[216:219], v[24:27], v[4:7]\n\t" \
  "v_exp_f32 v77, v77\n\t" \
  "v_mfma_f32_16x16x32_bf16 v[4:7], a[212:215], v[20:23], v[4:7]\n\t" \
  "v_cvt_pk_bf16_f32 v150, v76, v77\n\t" \
  "v_max3_f32 v192, v0, v1, v2\n\t" \
  "v_mfma_f32_16x16x32_bf16 v[4:7], a[208:211], v[16:19], v[4:7]\n\t" \
  "v_exp_f32 v78, v78\n\t" \
  "v_mfma_f32_16x16x32_bf16 v[8:11], a[224:227], v[16:19], v[176:179]\n\t" \
  "ds_read_b64_tr_b16 a[134:135], v237 offset:12288\n\t" \
  "ds_read_b64_tr_b16 a[136:137], v238 offset:0\n\t" \
  "v_mfma_f32_16x16x32_bf16 v[8:11], a[228:231], v[20:23], v[8:11]\n\t" \
  "v_exp_f32 v79, v79\n\t" \
  "v_mfma_f32_16x16x32_bf16 v[8:11], a[232:235], v[24:27], v[8:11]\n\t" \
  "v_cvt_pk_bf16_f32 v151, v78, v79\n\t" \
  "v_max3_f32 v203, v3, v4, v5\n\t" \
  "v_mfma_f32_16x16x32_bf16 v[8:11], a[236:239], v[28:31], v[8:11]\n\t" \
  "v_max3_f32 v192, v192, v6, v7\n\t" \
  "ds_read_b64_tr_b16 a[138:139], v238 offset:4096\n\t" \
  "v_mfma_f32_16x16x32_bf16 v[12:15], a[252:255], v[28:31], v[176:179]\n\t" \
  "ds_read_b64_tr_b16 a[140:141], v238 offset:8192\n\t" \
  "ds_read_b64_tr_b16 a[142:143], v238 offset:12288\n\t" \
  "v_mfma_f32_16x16x32_bf16 v[12:15], a[248:251], v[24:27], v[12:15]\n\t" \
  "v_exp_f32 v96, v96\n\t" \
  "v_mfma_f32_16x16x32_bf16 v[12:15], a[244:247], v[20:23], v[12:15]\n\t" \
  "v_max3_f32 v203, v203, v8, v9\n\t" \
  "v_max3_f32 v192, v192, v10, v11\n\t" \
  "v_mfma_f32_16x16x32_bf16 v[12:15], a[240:243], v[16:19], v[12:15]\n\t" \
  "s_mul_i32 s98, %[sofk], 5\n\t" \
  "s_xor_b32 s98, s98, 12288\n\t" \
  "s_lshr_b32 s98, s98, 1\n\t" \
  "buffer_load_dword %[pfd], v204, %[sD], s98 offen\n\t" \
  "v_mfma_f32_16x16x32_bf16 v[32:35], a[192:195], v[48:51], v[180:183]\n\t" \
  "ds_read_b64_tr_b16 a[144:145], v237 offset:256\n\t" \
  "ds_read_b64_tr_b16 a[146:147], v237 offset:4352\n\t" \
  "v_mfma_f32_16x16x32_bf16 v[32:35], a[196:199], v[52:55], v[32:35]\n\t" \
  "v_exp_f32 v97, v97\n\t" \
  "v_mfma_f32_16x16x32_bf16 v[32:35], a[200:203], v[56:59], v[32:35]\n\t" \
  "v_cvt_pk_bf16_f32 v152, v96, v97\n\t" \
  "v_max3_f32 v203, v203, v12, v13\n\t" \
  "v_mfma_f32_16x16x32_bf16 v[32:35], a[204:207], v[60:63], v[32:35]\n\t" \
  "v_max3_f32 v192, v192, v14, v15\n\t" \
  "v_max_f32 v192, v192, v203\n\t" \
  "v_mfma_f32_16x16x32_bf16 v[36:39], a[220:223], v[60:63], v[180:183]\n\t" \
  "v_cmp_lt_f32 vcc, 0x42800000, v192\n\t" \
  "ds_read_b64_tr_b16 a[148:149], v237 offset:8448\n\t" \
  "v_mfma_f32_16x16x32_bf16 v[36:39], a[216:219], v[56:59], v[36:39]\n\t" \
  "s_cbranch_vccnz .Lsf_c60\n\t" \
  ".Lsf_r60:\n\t" \
  "ds_read_b64_tr_b16 a[150:151], v237 offset:12544\n\t" \
  "v_mfma_f32_16x16x32_bf16 v[36:39], a[212:215], v[52:55], v[36:39]\n\t" \
  "v_max3_f32 v192, v32, v33, v34\n\t" \
  "ds_read_b64_tr_b16 a[152:153], v238 offset:256\n\t" \
  "v_mfma_f32_16x16x32_bf16 v[36:39], a[208:211], v[48:51], v[36:39]\n\t" \
  "v_exp_f32 v98, v98\n\t" \
  "v_mfma_f32_16x16x32_bf16 v[40:43], a[224:227], v[48:51], v[180:183]\n\t" \
  "v_exp_f32 v99, v99\n\t" \
  "v_mfma_f32_16x16x32_bf16 v[40:43], a[228:231], v[52:55], v[40:43]\n\t" \
  "v_cvt_pk_bf16_f32 v153, v98, v99\n\t" \
  "ds_read_b64_tr_b16 a[154:155], v238 offset:4352\n\t" \
  "v_mfma_f32_16x16x32_bf16 v[40:43], a[232:235], v[56:59], v[40:43]\n\t" \
  "v_exp_f32 v100, v100\n\t" \
  "v_mfma_f32_16x16x32_bf16 v[40:43], a[236:239], v[60:63], v[40:43]\n\t" \
  "v_max3_f32 v203, v35, v36, v37\n\t" \
  "v_max3_f32 v192, v192, v38, v39\n\t" \
  "v_mfma_f32_16x16x32_bf16 v[44:47], a[252:255], v[60:63], v[180:183]\n\t" \
  "ds_read_b64_tr_b16 a[156:157], v238 offset:8448\n\t" \
  "ds_read_b64_tr_b16 a[158:159], v238 offset:12544\n\t" \
  "v_mfma_f32_16x16x32_bf16 v[44:47], a[248:251], v[56:59], v[44:47]\n\t" \
  "v_exp_f32 v101, v101\n\t" \
  "v_mfma_f32_16x16x32_bf16 v[44:47], a[244:247], v[52:55], v[44:47]\n\t" \
  "v_cvt_pk_bf16_f32 v154, v100, v101\n\t" \
  "ds_read_b64_tr_b16 a[160:161], v237 offset:512\n\t" \
  "v_mfma_f32_16x16x32_bf16 v[44:47], a[240:243], v[48:51], v[44:47]\n\t" \
  "v_max3_f32 v203, v203, v40, v41\n\t" \
  "v_max3_f32 v192, v192, v42, v43\n\t" \
  "v_mfma_f32_16x16x32_bf16 v[64:67], a[192:195], v[80:83], v[184:187]\n\t" \
  "ds_read_b64_tr_b16 a[162:163], v237 offset:4608\n\t" \
  "ds_read_b64_tr_b16 a[164:165], v237 offset:8704\n\t" \
  "v_mfma_f32_16x16x32_bf16 v[64:67], a[196:199], v[84:87], v[64:67]\n\t" \
  "v_exp_f32 v102, v102\n\t" \
  "v_mfma_f32_16x16x32_bf16 v[64:67], a[200:203], v[88:91], v[64:67]\n\t" \
  "v_max3_f32 v203, v203, v44, v45\n\t" \
  "v_max3_f32 v192, v192, v46, v47\n\t" \
  "v_mfma_f32_16x16x32_bf16 v[64:67], a[204:207], v[92:95], v[64:67]\n\t" \
  "v_max_f32 v192, v192, v203\n\t" \
  "ds_read_b64_tr_b16 a[166:167], v237 offset:12800\n\t" \
  "v_mfma_f32_16x16x32_bf16 v[68:71], a[220:223], v[92:95], v[184:187]\n\t" \
  "v_cmp_lt_f32 vcc, 0x42800000, v192\n\t" \
  "ds_read_b64_tr_b16 a[168:169], v238 offset:512\n\t" \
  "v_mfma_f32_16x16x32_bf16 v[68:71], a[216:219], v[88:91], v[68:71]\n\t" \
  "s_cbranch_vccnz .Lsf_c61\n\t" \
  ".Lsf_r61:\n\t" \
  "ds_read_b64_tr_b16 a[170:171], v238 offset:4608\n\t" \
  "v_mfma_f32_16x16x32_bf16 v[68:71], a[212:215], v[84:87], v[68:71]\n\t" \
  "v_max3_f32 v192, v64, v65, v66\n\t" \
  "ds_read_b64_tr_b16 a[172:173], v238 offset:8704\n\t" \
  "v_mfma_f32_16x16x32_bf16 v[68:71], a[208:211], v[80:83], v[68:71]\n\t" \
  "v_exp_f32 v103, v103\n\t" \
  "v_mfma_f32_16x16x32_bf16 v[72:75], a[224:227], v[80:83], v[184:187]\n\t" \
  "v_cvt_pk_bf16_f32 v155, v102, v103\n\t" \
  "ds_read_b64_tr_b16 a[174:175], v238 offset:12800\n\t" \
  "v_mfma_f32_16x16x32_bf16 v[72:75], a[228:231], v[84:87], v[72:75]\n\t" \
  "v_exp_f32 v104, v104\n\t" \
  "v_mfma_f32_16x16x32_bf16 v[72:75], a[232:235], v[88:91], v[72:75]\n\t" \
  "v_exp_f32 v105, v105\n\t" \
  "v_mfma_f32_16x16x32_bf16 v[72:75], a[236:239], v[92:95], v[72:75]\n\t" \
  "v_cvt_pk_bf16_f32 v156, v104, v105\n\t" \
  "v_max3_f32 v203, v67, v68, v69\n\t" \
  "v_mfma_f32_16x16x32_bf16 v[76:79], a[252:255], v[92:95], v[184:187]\n\t" \
  "v_max3_f32 v192, v192, v70, v71\n\t" \
  "ds_read_b64_tr_b16 a[176:177], v237 offset:768\n\t" \
  "v_mfma_f32_16x16x32_bf16 v[76:79], a[248:251], v[88:91], v[76:79]\n\t" \
  "v_exp_f32 v106, v106\n\t" \
  "v_mfma_f32_16x16x32_bf16 v[76:79], a[244:247], v[84:87], v[76:79]\n\t" \
  "ds_read_b64_tr_b16 a[178:179], v237 offset:4864\n\t" \
  "v_max3_f32 v203, v203, v72, v73\n\t" \
  "v_mfma_f32_16x16x32_bf16 v[76:79], a[240:243], v[80:83], v[76:79]\n\t" \
  "v_max3_f32 v192, v192, v74, v75\n\t" \
  "ds_read_b64_tr_b16 a[180:181], v237 offset:8960\n\t" \
  "v_mfma_f32_16x16x32_bf16 v[96:99], a[192:195], v[112:115], v[188:191]\n\t" \
  "v_exp_f32 v107, v107\n\t" \
  "v_mfma_f32_16x16x32_bf16 v[96:99], a[196:199], v[116:119], v[96:99]\n\t" \
  "v_cvt_pk_bf16_f32 v157, v106, v107\n\t" \
  "ds_read_b64_tr_b16 a[182:183], v237 offset:13056\n\t" \
  "v_mfma_f32_16x16x32_bf16 v[96:99], a[200:203], v[120:123], v[96:99]\n\t" \
  "v_max3_f32 v203, v203, v76, v77\n\t" \
  "v_max3_f32 v192, v192, v78, v79\n\t" \
  "v_mfma_f32_16x16x32_bf16 v[96:99], a[204:207], v[124:127], v[96:99]\n\t" \
  "v_max_f32 v192, v192, v203\n\t" \
  "ds_read_b64_tr_b16 a[184:185], v238 offset:768\n\t" \
  "v_mfma_f32_16x16x32_bf16 v[100:103], a[220:223], v[124:127], v[188:191]\n\t" \
  "v_cmp_lt_f32 vcc, 0x42800000, v192\n\t" \
  "ds_read_b64_tr_b16 a[186:187], v238 offset:4864\n\t" \
  "v_mfma_f32_16x16x32_bf16 v[100:103], a[216:219], v[120:123], v[100:103]\n\t" \
  "s_cbranch_vccnz .Lsf_c62\n\t" \
  ".Lsf_r62:\n\t" \
  "ds_read_b64_tr_b16 a[188:189], v238 offset:8960\n\t" \
  "v_mfma_f32_16x16x32_bf16 v[100:103], a[212:215], v[116:119], v[100:103]\n\t" \
  "v_max3_f32 v192, v96, v97, v98\n\t" \
  "ds_read_b64_tr_b16 a[190:191], v238 offset:13056\n\t" \
  "v_mfma_f32_16x16x32_bf16 v[100:103], a[208:211], v[112:115], v[100:103]\n\t" \
  "v_exp_f32 v108, v108\n\t" \
  "v_mfma_f32_16x16x32_bf16 v[104:107], a[224:227], v[112:115], v[188:191]\n\t" \
  "v_exp_f32 v109, v109\n\t" \
  "v_mfma_f32_16x16x32_bf16 v[104:107], a[228:231], v[116:119], v[104:107]\n\t" \
  "v_cvt_pk_bf16_f32 v158, v108, v109\n\t" \
  "v_mfma_f32_16x16x32_bf16 v[104:107], a[232:235], v[120:123], v[104:107]\n\t" \
  "v_exp_f32 v110, v110\n\t" \
  "v_mfma_f32_16x16x32_bf16 v[104:107], a[236:239], v[124:127], v[104:107]\n\t" \
  "v_max3_f32 v203, v99, v100, v101\n\t" \
  "v_max3_f32 v192, v192, v102, v103\n\t" \
  "v_exp_f32 v111, v111\n\t" \
  "s_nop 0\n\t" \
  "v_cvt_pk_bf16_f32 v159, v110, v111\n\t" \
  "v_mfma_f32_16x16x32_bf16 v[108:111], a[252:255], v[124:127], v[188:191]\n\t" \
  "v_exp_f32 v0, v0\n\t" \
  "v_mfma_f32_16x16x32_bf16 v[108:111], a[248:251], v[120:123], v[108:111]\n\t" \
  "v_exp_f32 v1, v1\n\t" \
  "v_mfma_f32_16x16x32_bf16 v[108:111], a[244:247], v[116:119], v[108:111]\n\t" \
  "v_max3_f32 v203, v203, v104, v105\n\t" \
  "v_max3_f32 v192, v192, v106, v107\n\t" \
  "v_mfma_f32_16x16x32_bf16 v[108:111], a[240:243], v[112:115], v[108:111]\n\t" \
  "v_exp_f32 v2, v2\n\t" \
  "s_waitcnt vmcnt(5) lgkmcnt(0)\n\t" \
  "s_barrier\n\t" \
  "v_mfma_f32_16x16x32_bf16 a[0:3], a[128:131], v[128:131], a[0:3]\n\t" \
  "v_exp_f32 v3, v3\n\t" \
  "v_mfma_f32_16x16x32_bf16 a[0:3], a[132:135], v[132:135], a[0:3]\n\t" \
  "ds_read_b128 a[192:195], v220 offset:16384\n\t" \
  "ds_read_b128 a[196:199], v221 offset:16384\n\t" \
  "v_mfma_f32_16x16x32_bf16 a[16:19], a[136:139], v[128:131], a[16:19]\n\t" \
  "v_max3_f32 v203, v203, v108, v109\n\t" \
  "v_max3_f32 v192, v192, v110, v111\n\t" \
  "v_mfma_f32_16x16x32_bf16 a[16:19], a[140:143], v[132:135], a[16:19]\n\t" \
  "v_max_f32 v192, v192, v203\n\t" \
  "ds_read_b128 a[200:203], v222 offset:16384\n\t" \
  "v_mfma_f32_16x16x32_bf16 a[32:35], a[144:147], v[128:131], a[32:35]\n\t" \
  "v_cmp_lt_f32 vcc, 0x42800000, v192\n\t" \
  "ds_read_b128 a[204:207], v223 offset:16384\n\t" \
  "v_mfma_f32_16x16x32_bf16 a[32:35], a[148:151], v[132:135], a[32:35]\n\t" \
  "s_cbranch_vccnz .Lsf_c63\n\t" \
  ".Lsf_r63:\n\t" \
  "ds_read_b128 a[208:211], v220 offset:20480\n\t" \
  "v_mfma_f32_16x16x32_bf16 a[48:51], a[152:155], v[128:131], a[48:51]\n\t" \
  "s_waitcnt vmcnt(3)\n\t" \
  "v_and_b32 v206, 0xbfffbfff, v209\n\t" \
  "v_cmp_ne_u32_e64 s[96:97], %[tag], v206\n\t" \
  "s_cmp_lg_u64 s[96:97], 0\n\t" \
  "s_cbranch_scc1 .Lsf_v\n\t" \
  ".Lsf_w:\n\t" \
  "v_mfma_f32_16x16x32_bf16 a[48:51], a[156:159], v[132:135], a[48:51]\n\t" \
  "v_and_b32 v206, 1, v233\n\t" \
  "v_lshlrev_b32 v206, 4, v206\n\t" \
  "v_mfma_f32_16x16x32_bf16 a[64:67], a[160:163], v[128:131], a[64:67]\n\t" \
  "v_lshrrev_b32 v206, v206, v209\n\t" \
  "v_and_b32 v206, 0x4000, v206\n\t" \
  "v_mfma_f32_16x16x32_bf16 a[64:67], a[164:167], v[132:135], a[64:67]\n\t" \
  "v_cmp_ne_u32_e64 s[96:97], 0, v206\n\t" \
  "s_bcnt1_i32_b32 s98, s96\n\t" \
  "s_bcnt1_i32_b32 %[na], s97\n\t" \
  "s_cmp_gt_u32 s98, %[na]\n\t" \
  "s_cselect_b32 %[nb], 0x2000, 0\n\t" \
  "s_cselect_b32 s98, s96, s97\n\t" \
  "s_cselect_b32 s96, s97, s96\n\t" \
  "v_mfma_f32_16x16x32_bf16 a[80:83], a[168:171], v[128:131], a[80:83]\n\t" \
  "s_lshr_b32 s97, s98, 16\n\t" \
  "s_bcnt1_i32_b32 s97, s97\n\t" \
  "v_mfma_f32_16x16x32_bf16 a[80:83], a[172:175], v[132:135], a[80:83]\n\t" \
  "s_lshr_b32 s96, s96, 16\n\t" \
  "s_bcnt1_i32_b32 s96, s96\n\t" \
  "v_mfma_f32_16x16x32_bf16 a[96:99], a[176:179], v[128:131], a[96:99]\n\t" \
  "s_add_u32 s96, s96, 7\n\t" \
  "s_cmp_ge_u32 s97, s96\n\t" \
  "s_cselect_b32 %[na], -1, 0\n\t" \
  "v_mfma_f32_16x16x32_bf16 a[96:99], a[180:183], v[132:135], a[96:99]\n\t" \
  "s_xor_b32 s98, %[po], %[nb]\n\t" \
  "s_and_b32 s98, s98, %[na]\n\t" \
  "s_xor_b32 %[eso], %[eso], s98\n\t" \
  "v_mfma_f32_16x16x32_bf16 a[112:115], a[184:187], v[128:131], a[112:115]\n\t" \
  "s_xor_b32 s98, %[px], %[nb]\n\t" \
  "s_and_b32 s98, s98, %[na]\n\t" \
  "s_xor_b32 %[xcb], %[xcb], s98\n\t" \
  "v_mfma_f32_16x16x32_bf16 a[112:115], a[188:191], v[132:135], a[112:115]\n\t" \
  "s_xor_b32 %[frb], %[frb], %[nb]\n\t" \
  "s_xor_b32 %[fw2], %[fw2], %[nb]\n\t" \
  "v_mfma_f32_16x16x32_bf16 v[160:163], v[216:219], v[128:131], v[160:163]\n\t" \
  "v_exp_f32 v4, v4\n\t" \
  "v_mfma_f32_16x16x32_bf16 v[160:163], v[216:219], v[132:135], v[160:163]\n\t" \
  "v_cvt_pk_bf16_f32 v128, v0, v1\n\t" \
  "v_cvt_pk_bf16_f32 v129, v2, v3\n\t" \
  "v_mfma_f32_16x16x32_bf16 a[4:7], a[128:131], v[136:139], a[4:7]\n\t" \
  "v_exp_f32 v5, v5\n\t" \
  "v_mfma_f32_16x16x32_bf16 a[4:7], a[132:135], v[140:143], a[4:7]\n\t" \
  "v_cvt_pk_bf16_f32 v130, v4, v5\n\t" \
  "ds_read_b128 a[212:215], v221 offset:20480\n\t" \
  "v_mfma_f32_16x16x32_bf16 a[20:23], a[136:139], v[136:139], a[20:23]\n\t" \
  "v_exp_f32 v6, v6\n\t" \
  "v_mfma_f32_16x16x32_bf16 a[20:23], a[140:143], v[140:143], a[20:23]\n\t" \
  "v_exp_f32 v7, v7\n\t" \
  "v_mfma_f32_16x16x32_bf16 a[36:39], a[144:147], v[136:139], a[36:39]\n\t" \
  "v_cvt_pk_bf16_f32 v131, v6, v7\n\t" \
  "s_add_u32 m0, %[m], 147456\n\t" \
  "v_mfma_f32_16x16x32_bf16 a[36:39], a[148:151], v[140:143], a[36:39]\n\t" \
  "v_exp_f32 v8, v8\n\t" \
  "v_mfma_f32_16x16x32_bf16 a[52:55], a[152:155], v[136:139], a[52:55]\n\t" \
  "ds_read_b128 a[216:219], v222 offset:20480\n\t" \
  "s_add_u32 s96, %[sofk], 114688\n\t" \
  "v_mfma_f32_16x16x32_bf16 a[52:55], a[156:159], v[140:143], a[52:55]\n\t" \
  "v_exp_f32 v9, v9\n\t" \
  "v_mfma_f32_16x16x32_bf16 a[68:71], a[160:163], v[136:139], a[68:71]\n\t" \
  "v_cvt_pk_bf16_f32 v132, v8, v9\n\t" \
  "ds_read_b128 a[220:223], v223 offset:20480\n\t" \
  "v_mfma_f32_16x16x32_bf16 a[68:71], a[164:167], v[140:143], a[68:71]\n\t" \
  "v_exp_f32 v10, v10\n\t" \
  "v_mfma_f32_16x16x32_bf16 a[84:87], a[168:171], v[136:139], a[84:87]\n\t" \
  "v_exp_f32 v11, v11\n\t" \
  "v_mfma_f32_16x16x32_bf16 a[84:87], a[172:175], v[140:143], a[84:87]\n\t" \
  "v_cvt_pk_bf16_f32 v133, v10, v11\n\t" \
  "buffer_load_dwordx4 v230, %[sV], s96 offen offset:0 lds\n\t" \
  "v_mfma_f32_16x16x32_bf16 a[100:103], a[176:179], v[136:139], a[100:103]\n\t" \
  "v_exp_f32 v12, v12\n\t" \
  "v_mfma_f32_16x16x32_bf16 a[100:103], a[180:183], v[140:143], a[100:103]\n\t" \
  "v_exp_f32 v13, v13\n\t" \
  "v_mfma_f32_16x16x32_bf16 a[116:119], a[184:187], v[136:139], a[116:119]\n\t" \
  "v_cvt_pk_bf16_f32 v134, v12, v13\n\t" \
  "ds_read_b128 a[224:227], v220 offset:24576\n\t" \
  "v_mfma_f32_16x16x32_bf16 a[116:119], a[188:191], v[140:143], a[116:119]\n\t" \
  "v_exp_f32 v14, v14\n\t" \
  "v_mfma_f32_16x16x32_bf16 v[164:167], v[216:219], v[136:139], v[164:167]\n\t" \
  "buffer_load_dwordx4 v231, %[sV], s96 offen offset:1024 lds\n\t" \
  "ds_read_b128 a[228:231], v221 offset:24576\n\t" \
  "v_mfma_f32_16x16x32_bf16 v[164:167], v[216:219], v[140:143], v[164:167]\n\t" \
  "v_exp_f32 v15, v15\n\t" \
  "v_mfma_f32_16x16x32_bf16 a[8:11], a[128:131], v[144:147], a[8:11]\n\t" \
  "v_cvt_pk_bf16_f32 v135, v14, v15\n\t" \
  "buffer_load_dwordx4 v230, %[sV], s96 offen offset:2048 lds\n\t" \
  "v_mfma_f32_16x16x32_bf16 a[8:11], a[132:135], v[148:151], a[8:11]\n\t" \
  "v_exp_f32 v32, v32\n\t" \
  "v_mfma_f32_16x16x32_bf16 a[24:27], a[136:139], v[144:147], a[24:27]\n\t" \
  "v_exp_f32 v33, v33\n\t" \
  "v_mfma_f32_16x16x32_bf16 a[24:27], a[140:143], v[148:151], a[24:27]\n\t" \
  "v_cvt_pk_bf16_f32 v136, v32, v33\n\t" \
  "ds_read_b128 a[232:235], v222 offset:24576\n\t" \
  "v_mfma_f32_16x16x32_bf16 a[40:43], a[144:147], v[144:147], a[40:43]\n\t" \
  "v_exp_f32 v34, v34\n\t" \
  "v_mfma_f32_16x16x32_bf16 a[40:43], a[148:151], v[148:151], a[40:43]\n\t" \
  "v_exp_f32 v35, v35\n\t" \
  "v_mfma_f32_16x16x32_bf16 a[56:59], a[152:155], v[144:147], a[56:59]\n\t" \
  "v_cvt_pk_bf16_f32 v137, v34, v35\n\t" \
  "buffer_load_dwordx4 v231, %[sV], s96 offen offset:3072 lds\n\t" \
  "v_mfma_f32_16x16x32_bf16 a[56:59], a[156:159], v[148:151], a[56:59]\n\t" \
  "v_exp_f32 v36, v36\n\t" \
  "v_mfma_f32_16x16x32_bf16 a[72:75], a[160:163], v[144:147], a[72:75]\n\t" \
  "ds_read_b128 a[236:239], v223 offset:24576\n\t" \
  "s_add_u32 m0, %[m], 49152\n\t" \
  "v_mfma_f32_16x16x32_bf16 a[72:75], a[164:167], v[148:151], a[72:75]\n\t" \
  "v_exp_f32 v37, v37\n\t" \
  "v_mfma_f32_16x16x32_bf16 a[88:91], a[168:171], v[144:147], a[88:91]\n\t" \
  "v_cvt_pk_bf16_f32 v138, v36, v37\n\t" \
  "ds_read_b128 a[240:243], v220 offset:28672\n\t" \
  "v_mfma_f32_16x16x32_bf16 a[88:91], a[172:175], v[148:151], a[88:91]\n\t" \
  "v_exp_f32 v38, v38\n\t" \
  "v_mfma_f32_16x16x32_bf16 a[104:107], a[176:179], v[144:147], a[104:107]\n\t" \
  "v_exp_f32 v39, v39\n\t" \
  "v_mfma_f32_16x16x32_bf16 a[104:107], a[180:183], v[148:151], a[104:107]\n\t" \
  "v_cvt_pk_bf16_f32 v139, v38, v39\n\t" \
  "s_xor_b32 s97, %[wob], 262144\n\t" \
  "v_mfma_f32_16x16x32_bf16 a[120:123], a[184:187], v[144:147], a[120:123]\n\t" \
  "v_exp_f32 v40, v40\n\t" \
  "v_mfma_f32_16x16x32_bf16 a[120:123], a[188:191], v[148:151], a[120:123]\n\t" \
  "ds_read_b128 a[244:247], v221 offset:28672\n\t" \
  "buffer_load_dwordx4 v240, %[wo], s97 offen offset:0 lds\n\t" \
  "v_mfma_f32_16x16x32_bf16 v[168:171], v[216:219], v[144:147], v[168:171]\n\t" \
  "v_exp_f32 v41, v41\n\t" \
  "v_mfma_f32_16x16x32_bf16 v[168:171], v[216:219], v[148:151], v[168:171]\n\t" \
  "v_cvt_pk_bf16_f32 v140, v40, v41\n\t" \
  "ds_read_b128 a[248:251], v222 offset:28672\n\t" \
  "v_mfma_f32_16x16x32_bf16 a[12:15], a[128:131], v[152:155], a[12:15]\n\t" \
  "v_exp_f32 v42, v42\n\t" \
  "v_mfma_f32_16x16x32_bf16 a[12:15], a[132:135], v[156:159], a[12:15]\n\t" \
  "v_exp_f32 v43, v43\n\t" \
  "v_mfma_f32_16x16x32_bf16 a[28:31], a[136:139], v[152:155], a[28:31]\n\t" \
  "v_cvt_pk_bf16_f32 v141, v42, v43\n\t" \
  "ds_read_b128 a[252:255], v223 offset:28672\n\t" \
  "v_mfma_f32_16x16x32_bf16 a[28:31], a[140:143], v[156:159], a[28:31]\n\t" \
  "v_exp_f32 v44, v44\n\t" \
  "v_mfma_f32_16x16x32_bf16 a[44:47], a[144:147], v[152:155], a[44:47]\n\t" \
  "s_add_u32 s97, s97, 7168\n\t" \
  "buffer_load_dwordx4 v241, %[wo], s97 offen offset:1024 lds\n\t" \
  "v_mfma_f32_16x16x32_bf16 a[44:47], a[148:151], v[156:159], a[44:47]\n\t" \
  "v_exp_f32 v45, v45\n\t" \
  "v_mfma_f32_16x16x32_bf16 a[60:63], a[152:155], v[152:155], a[60:63]\n\t" \
  "v_cvt_pk_bf16_f32 v142, v44, v45\n\t" \
  "v_mfma_f32_16x16x32_bf16 a[60:63], a[156:159], v[156:159], a[60:63]\n\t" \
  "v_exp_f32 v46, v46\n\t" \
  "v_mfma_f32_16x16x32_bf16 a[76:79], a[160:163], v[152:155], a[76:79]\n\t" \
  "s_add_u32 s97, s97, 7168\n\t" \
  "buffer_load_dwordx4 v240, %[wo], s97 offen offset:2048 lds\n\t" \
  "v_mfma_f32_16x16x32_bf16 a[76:79], a[164:167], v[156:159], a[76:79]\n\t" \
  "v_exp_f32 v47, v47\n\t" \
  "v_mfma_f32_16x16x32_bf16 a[92:95], a[168:171], v[152:155], a[92:95]\n\t" \
  "v_cvt_pk_bf16_f32 v143, v46, v47\n\t" \
  "v_mfma_f32_16x16x32_bf16 a[92:95], a[172:175], v[156:159], a[92:95]\n\t" \
  "v_exp_f32 v64, v64\n\t" \
  "v_mfma_f32_16x16x32_bf16 a[108:111], a[176:179], v[152:155], a[108:111]\n\t" \
  "s_add_u32 s97, s97, 7168\n\t" \
  "buffer_load_dwordx4 v241, %[wo], s97 offen offset:3072 lds\n\t" \
  "v_mfma_f32_16x16x32_bf16 a[108:111], a[180:183], v[156:159], a[108:111]\n\t" \
  "v_exp_f32 v65, v65\n\t" \
  "v_mfma_f32_16x16x32_bf16 a[124:127], a[184:187], v[152:155], a[124:127]\n\t" \
  "v_cvt_pk_bf16_f32 v144, v64, v65\n\t" \
  "v_mfma_f32_16x16x32_bf16 a[124:127], a[188:191], v[156:159], a[124:127]\n\t" \
  "v_exp_f32 v66, v66\n\t" \
  "v_mfma_f32_16x16x32_bf16 v[172:175], v[216:219], v[152:155], v[172:175]\n\t" \
  "v_exp_f32 v67, v67\n\t" \
  "v_mfma_f32_16x16x32_bf16 v[172:175], v[216:219], v[156:159], v[172:175]\n\t" \
  "v_cvt_pk_bf16_f32 v145, v66, v67\n\t" \
  "s_cmp_lg_u32 s99, 0\n\t" \
  "s_cbranch_scc1 .Lsf_p6\n\t" \
  ".Lsf_q6:\n\t" \
  "s_waitcnt lgkmcnt(0)\n\t" \
  "s_barrier\n\t" \
  "v_mfma_f32_16x16x32_bf16 v[0:3], a[192:195], v[16:19], v[176:179]\n\t" \
  "v_exp_f32 v68, v68\n\t" \
  "v_mfma_f32_16x16x32_bf16 v[0:3], a[196:199], v[20:23], v[0:3]\n\t" \
  "v_exp_f32 v69, v69\n\t" \
  "v_mfma_f32_16x16x32_bf16 v[0:3], a[200:203], v[24:27], v[0:3]\n\t" \
  "v_cvt_pk_bf16_f32 v146, v68, v69\n\t" \
  "ds_read_b64_tr_b16 a[128:129], v228 offset:0\n\t" \
  "v_mfma_f32_16x16x32_bf16 v[0:3], a[204:207], v[28:31], v[0:3]\n\t" \
  "v_exp_f32 v70, v70\n\t" \
  "v_mfma_f32_16x16x32_bf16 v[4:7], a[220:223], v[28:31], v[176:179]\n\t" \
  "ds_read_b64_tr_b16 a[130:131], v228 offset:4096\n\t" \
  "ds_read_b64_tr_b16 a[132:133], v228 offset:8192\n\t" \
  "v_mfma_f32_16x16x32_bf16 v[4:7], a[216:219], v[24:27], v[4:7]\n\t" \
  "v_exp_f32 v71, v71\n\t" \
  "v_mfma_f32_16x16x32_bf16 v[4:7], a[212:215], v[20:23], v[4:7]\n\t" \
  "v_cvt_pk_bf16_f32 v147, v70, v71\n\t" \
  "v_max3_f32 v192, v0, v1, v2\n\t" \
  "v_mfma_f32_16x16x32_bf16 v[4:7], a[208:211], v[16:19], v[4:7]\n\t" \
  "s_add_u32 m0, %[m], 65536\n\t" \
  "ds_read_b64_tr_b16 a[134:135], v228 offset:12288\n\t" \
  "v_mfma_f32_16x16x32_bf16 v[8:11], a[224:227], v[16:19], v[176:179]\n\t" \
  "ds_read_b64_tr_b16 a[136:137], v229 offset:0\n\t" \
  "s_xor_b32 s97, %[wob], 393216\n\t" \
  "v_mfma_f32_16x16x32_bf16 v[8:11], a[228:231], v[20:23], v[8:11]\n\t" \
  "v_exp_f32 v72, v72\n\t" \
  "v_mfma_f32_16x16x32_bf16 v[8:11], a[232:235], v[24:27], v[8:11]\n\t" \
  "v_max3_f32 v203, v3, v4, v5\n\t" \
  "v_max3_f32 v192, v192, v6, v7\n\t" \
  "v_mfma_f32_16x16x32_bf16 v[8:11], a[236:239], v[28:31], v[8:11]\n\t" \
  "ds_read_b64_tr_b16 a[138:139], v229 offset:4096\n\t" \
  "ds_read_b64_tr_b16 a[140:141], v229 offset:8192\n\t" \
  "v_mfma_f32_16x16x32_bf16 v[12:15], a[252:255], v[28:31], v[176:179]\n\t" \
  "v_exp_f32 v73, v73\n\t" \
  "v_mfma_f32_16x16x32_bf16 v[12:15], a[248:251], v[24:27], v[12:15]\n\t" \
  "v_cvt_pk_bf16_f32 v148, v72, v73\n\t" \
  "buffer_load_dwordx4 v240, %[wo], s97 offen offset:0 lds\n\t" \
  "v_mfma_f32_16x16x32_bf16 v[12:15], a[244:247], v[20:23], v[12:15]\n\t" \
  "v_max3_f32 v203, v203, v8, v9\n\t" \
  "v_max3_f32 v192, v192, v10, v11\n\t" \
  "v_mfma_f32_16x16x32_bf16 v[12:15], a[240:243], v[16:19], v[12:15]\n\t" \
  "ds_read_b64_tr_b16 a[142:143], v229 offset:12288\n\t" \
  "ds_read_b64_tr_b16 a[144:145], v228 offset:256\n\t" \
  "v_mfma_f32_16x16x32_bf16 v[32:35], a[192:195], v[48:51], v[180:183]\n\t" \
  "ds_read_b64_tr_b16 a[146:147], v228 offset:4352\n\t" \
  "ds_read_b64_tr_b16 a[148:149], v228 offset:8448\n\t" \
  "v_mfma_f32_16x16x32_bf16 v[32:35], a[196:199], v[52:55], v[32:35]\n\t" \
  "v_exp_f32 v74, v74\n\t" \
  "v_mfma_f32_16x16x32_bf16 v[32:35], a[200:203], v[56:59], v[32:35]\n\t" \
  "v_max3_f32 v203, v203, v12, v13\n\t" \
  "v_max3_f32 v192, v192, v14, v15\n\t" \
  "v_mfma_f32_16x16x32_bf16 v[32:35], a[204:207], v[60:63], v[32:35]\n\t" \
  "v_max_f32 v192, v192, v203\n\t" \
  "ds_read_b64_tr_b16 a[150:151], v228 offset:12544\n\t" \
  "v_mfma_f32_16x16x32_bf16 v[36:39], a[220:223], v[60:63], v[180:183]\n\t" \
  "v_cmp_lt_f32 vcc, 0x42800000, v192\n\t" \
  "ds_read_b64_tr_b16 a[152:153], v229 offset:256\n\t" \
  "v_mfma_f32_16x16x32_bf16 v[36:39], a[216:219], v[56:59], v[36:39]\n\t" \
  "s_cbranch_vccnz .Lsf_c70\n\t" \
  ".Lsf_r70:\n\t" \
  "ds_read_b64_tr_b16 a[154:155], v229 offset:4352\n\t" \
  "v_mfma_f32_16x16x32_bf16 v[36:39], a[212:215], v[52:55], v[36:39]\n\t" \
  "s_add_u32 s97, s97, 7168\n\t" \
  "buffer_load_dwordx4 v241, %[wo], s97 offen offset:1024 lds\n\t" \
  "v_mfma_f32_16x16x32_bf16 v[36:39], a[208:211], v[48:51], v[36:39]\n\t" \
  "v_max3_f32 v192, v32, v33, v34\n\t" \
  "ds_read_b64_tr_b16 a[156:157], v229 offset:8448\n\t" \
  "v_mfma_f32_16x16x32_bf16 v[40:43], a[224:227], v[48:51], v[180:183]\n\t" \
  "s_add_u32 s97, s97, 7168\n\t" \
  "buffer_load_dwordx4 v240, %[wo], s97 offen offset:2048 lds\n\t" \
  "v_mfma_f32_16x16x32_bf16 v[40:43], a[228:231], v[52:55], v[40:43]\n\t" \
  "v_exp_f32 v75, v75\n\t" \
  "v_mfma_f32_16x16x32_bf16 v[40:43], a[232:235], v[56:59], v[40:43]\n\t" \
  "v_cvt_pk_bf16_f32 v149, v74, v75\n\t" \
  "v_max3_f32 v203, v35, v36, v37\n\t" \
  "v_mfma_f32_16x16x32_bf16 v[40:43], a[236:239], v[60:63], v[40:43]\n\t" \
  "v_max3_f32 v192, v192, v38, v39\n\t" \
  "ds_read_b64_tr_b16 a[158:159], v229 offset:12544\n\t" \
  "v_mfma_f32_16x16x32_bf16 v[44:47], a[252:255], v[60:63], v[180:183]\n\t" \
  "s_add_u32 s97, s97, 7168\n\t" \
  "buffer_load_dwordx4 v241, %[wo], s97 offen offset:3072 lds\n\t" \
  "v_mfma_f32_16x16x32_bf16 v[44:47], a[248:251], v[56:59], v[44:47]\n\t" \
  "v_exp_f32 v76, v76\n\t" \
  "v_mfma_f32_16x16x32_bf16 v[44:47], a[244:247], v[52:55], v[44:47]\n\t" \
  "ds_read_b64_tr_b16 a[160:161], v228 offset:512\n\t" \
  "v_max3_f32 v203, v203, v40, v41\n\t" \
  "v_mfma_f32_16x16x32_bf16 v[44:47], a[240:243], v[48:51], v[44:47]\n\t" \
  "v_max3_f32 v192, v192, v42, v43\n\t" \
  "ds_read_b64_tr_b16 a[162:163], v228 offset:4608\n\t" \
  "v_mfma_f32_16x16x32_bf16 v[64:67], a[192:195], v[80:83], v[184:187]\n\t" \
  "s_add_u32 m0, %[m], 98304\n\t" \
  "s_mov_b32 s96, 40960\n\t" \
  "buffer_load_dwordx4 v232, %[sG], s96 offen offset:2048 lds\n\t" \
  "v_mfma_f32_16x16x32_bf16 v[64:67], a[196:199], v[84:87], v[64:67]\n\t" \
  "v_exp_f32 v77, v77\n\t" \
  "v_mfma_f32_16x16x32_bf16 v[64:67], a[200:203], v[88:91], v[64:67]\n\t" \
  "v_cvt_pk_bf16_f32 v150, v76, v77\n\t" \
  "ds_read_b64_tr_b16 a[164:165], v228 offset:8704\n\t" \
  "v_mfma_f32_16x16x32_bf16 v[64:67], a[204:207], v[92:95], v[64:67]\n\t" \
  "v_max3_f32 v203, v203, v44, v45\n\t" \
  "v_max3_f32 v192, v192, v46, v47\n\t" \
  "v_mfma_f32_16x16x32_bf16 v[68:71], a[220:223], v[92:95], v[184:187]\n\t" \
  "v_max_f32 v192, v192, v203\n\t" \
  "ds_read_b64_tr_b16 a[166:167], v228 offset:12800\n\t" \
  "v_mfma_f32_16x16x32_bf16 v[68:71], a[216:219], v[88:91], v[68:71]\n\t" \
  "v_cmp_lt_f32 vcc, 0x42800000, v192\n\t" \
  "ds_read_b64_tr_b16 a[168:169], v229 offset:512\n\t" \
  "v_mfma_f32_16x16x32_bf16 v[68:71], a[212:215], v[84:87], v[68:71]\n\t" \
  "s_cbranch_vccnz .Lsf_c71\n\t" \
  ".Lsf_r71:\n\t" \
  "v_max3_f32 v192, v64, v65, v66\n\t" \
  "v_mfma_f32_16x16x32_bf16 v[68:71], a[208:211], v[80:83], v[68:71]\n\t" \
  "ds_read_b64_tr_b16 a[170:171], v229 offset:4608\n\t" \
  "ds_read_b64_tr_b16 a[172:173], v229 offset:8704\n\t" \
  "v_mfma_f32_16x16x32_bf16 v[72:75], a[224:227], v[80:83], v[184:187]\n\t" \
  "v_exp_f32 v78, v78\n\t" \
  "v_mfma_f32_16x16x32_bf16 v[72:75], a[228:231], v[84:87], v[72:75]\n\t" \
  "v_exp_f32 v79, v79\n\t" \
  "v_mfma_f32_16x16x32_bf16 v[72:75], a[232:235], v[88:91], v[72:75]\n\t" \
  "v_cvt_pk_bf16_f32 v151, v78, v79\n\t" \
  "v_max3_f32 v203, v67, v68, v69\n\t" \
  "v_mfma_f32_16x16x32_bf16 v[72:75], a[236:239], v[92:95], v[72:75]\n\t" \
  "v_max3_f32 v192, v192, v70, v71\n\t" \
  "ds_read_b64_tr_b16 a[174:175], v229 offset:12800\n\t" \
  "v_mfma_f32_16x16x32_bf16 v[76:79], a[252:255], v[92:95], v[184:187]\n\t" \
  "ds_read_b64_tr_b16 a[176:177], v228 offset:768\n\t" \
  "ds_read_b64_tr_b16 a[178:179], v228 offset:4864\n\t" \
  "v_mfma_f32_16x16x32_bf16 v[76:79], a[248:251], v[88:91], v[76:79]\n\t" \
  "v_exp_f32 v96, v96\n\t" \
  "v_mfma_f32_16x16x32_bf16 v[76:79], a[244:247], v[84:87], v[76:79]\n\t" \
  "v_max3_f32 v203, v203, v72, v73\n\t" \
  "v_max3_f32 v192, v192, v74, v75\n\t" \
  "v_mfma_f32_16x16x32_bf16 v[76:79], a[240:243], v[80:83], v[76:79]\n\t" \
  "ds_read_b64_tr_b16 a[180:181], v228 offset:8960\n\t" \
  "ds_read_b64_tr_b16 a[182:183], v228 offset:13056\n\t" \
  "v_exp_f32 v97, v97\n\t" \
  "v_exp_f32 v98, v98\n\t" \
  "v_exp_f32 v99, v99\n\t" \
  "v_cvt_pk_bf16_f32 v152, v96, v97\n\t" \
  "v_cvt_pk_bf16_f32 v153, v98, v99\n\t" \
  "v_mfma_f32_16x16x32_bf16 v[96:99], a[192:195], v[112:115], v[188:191]\n\t" \
  "s_mov_b32 s96, 47104\n\t" \
  "buffer_load_dwordx4 v232, %[sG], s96 offen offset:3072 lds\n\t" \
  "v_mfma_f32_16x16x32_bf16 v[96:99], a[196:199], v[116:119], v[96:99]\n\t" \
  "v_max3_f32 v203, v203, v76, v77\n\t" \
  "v_max3_f32 v192, v192, v78, v79\n\t" \
  "v_mfma_f32_16x16x32_bf16 v[96:99], a[200:203], v[120:123], v[96:99]\n\t" \
  "v_max_f32 v192, v192, v203\n\t" \
  "ds_read_b64_tr_b16 a[184:185], v229 offset:768\n\t" \
  "v_mfma_f32_16x16x32_bf16 v[96:99], a[204:207], v[124:127], v[96:99]\n\t" \
  "v_cmp_lt_f32 vcc, 0x42800000, v192\n\t" \
  "ds_read_b64_tr_b16 a[186:187], v229 offset:4864\n\t" \
  "v_exp_f32 v100, v100\n\t" \
  "v_exp_f32 v101, v101\n\t" \
  "v_exp_f32 v102, v102\n\t" \
  "v_exp_f32 v103, v103\n\t" \
  "v_cvt_pk_bf16_f32 v154, v100, v101\n\t" \
  "v_cvt_pk_bf16_f32 v155, v102, v103\n\t" \
  "v_mfma_f32_16x16x32_bf16 v[100:103], a[220:223], v[124:127], v[188:191]\n\t" \
  "s_cbranch_vccnz .Lsf_c72\n\t" \
  ".Lsf_r72:\n\t" \
  "v_max3_f32 v192, v96, v97, v98\n\t" \
  "v_mfma_f32_16x16x32_bf16 v[100:103], a[216:219], v[120:123], v[100:103]\n\t" \
  "v_exp_f32 v104, v104\n\t" \
  "v_mfma_f32_16x16x32_bf16 v[100:103], a[212:215], v[116:119], v[100:103]\n\t" \
  "v_exp_f32 v105, v105\n\t" \
  "v_mfma_f32_16x16x32_bf16 v[100:103], a[208:211], v[112:115], v[100:103]\n\t" \
  "v_cvt_pk_bf16_f32 v156, v104, v105\n\t" \
  "ds_read_b64_tr_b16 a[188:189], v229 offset:8960\n\t" \
  "v_exp_f32 v106, v106\n\t" \
  "v_exp_f32 v107, v107\n\t" \
  "s_nop 0\n\t" \
  "v_cvt_pk_bf16_f32 v157, v106, v107\n\t" \
  "v_mfma_f32_16x16x32_bf16 v[104:107], a[224:227], v[112:115], v[188:191]\n\t" \
  "v_exp_f32 v108, v108\n\t" \
  "v_mfma_f32_16x16x32_bf16 v[104:107], a[228:231], v[116:119], v[104:107]\n\t" \
  "ds_read_b64_tr_b16 a[190:191], v229 offset:13056\n\t" \
  "v_max3_f32 v203, v99, v100, v101\n\t" \
  "v_mfma_f32_16x16x32_bf16 v[104:107], a[232:235], v[120:123], v[104:107]\n\t" \
  "v_max3_f32 v192, v192, v102, v103\n\t" \
  "v_mfma_f32_16x16x32_bf16 v[104:107], a[236:239], v[124:127], v[104:107]\n\t" \
  "v_exp_f32 v109, v109\n\t" \
  "v_exp_f32 v110, v110\n\t" \
  "v_exp_f32 v111, v111\n\t" \
  "v_cvt_pk_bf16_f32 v158, v108, v109\n\t" \
  "v_cvt_pk_bf16_f32 v159, v110, v111\n\t" \
  "v_mfma_f32_16x16x32_bf16 v[108:111], a[252:255], v[124:127], v[188:191]\n\t" \
  "v_exp_f32 v0, v0\n\t" \
  "v_mfma_f32_16x16x32_bf16 v[108:111], a[248:251], v[120:123], v[108:111]\n\t" \
  "v_exp_f32 v1, v1\n\t" \
  "v_mfma_f32_16x16x32_bf16 v[108:111], a[244:247], v[116:119], v[108:111]\n\t" \
  "v_max3_f32 v203, v203, v104, v105\n\t" \
  "v_max3_f32 v192, v192, v106, v107\n\t" \
  "v_mfma_f32_16x16x32_bf16 v[108:111], a[240:243], v[112:115], v[108:111]\n\t" \
  "v_exp_f32 v2, v2\n\t" \
  "s_waitcnt vmcnt(6) lgkmcnt(0)\n\t" \
  "s_barrier\n\t" \
  "v_mfma_f32_16x16x32_bf16 a[0:3], a[128:131], v[128:131], a[0:3]\n\t" \
  "ds_read_b64_tr_b16 a[192:193], v237 offset:0\n\t" \
  "s_add_u32 m0, %[m], 81920\n\t" \
  "v_mfma_f32_16x16x32_bf16 a[0:3], a[132:135], v[132:135], a[0:3]\n\t" \
  "v_exp_f32 v3, v3\n\t" \
  "v_mfma_f32_16x16x32_bf16 a[16:19], a[136:139], v[128:131], a[16:19]\n\t" \
  "v_max3_f32 v203, v203, v108, v109\n\t" \
  "v_max3_f32 v192, v192, v110, v111\n\t" \
  "v_mfma_f32_16x16x32_bf16 a[16:19], a[140:143], v[132:135], a[16:19]\n\t" \
  "v_max_f32 v192, v192, v203\n\t" \
  "ds_read_b64_tr_b16 a[194:195], v237 offset:4096\n\t" \
  "v_mfma_f32_16x16x32_bf16 a[32:35], a[144:147], v[128:131], a[32:35]\n\t" \
  "v_cmp_lt_f32 vcc, 0x42800000, v192\n\t" \
  "ds_read_b64_tr_b16 a[196:197], v237 offset:8192\n\t" \
  "v_mfma_f32_16x16x32_bf16 a[32:35], a[148:151], v[132:135], a[32:35]\n\t" \
  "s_cbranch_vccnz .Lsf_c73\n\t" \
  ".Lsf_r73:\n\t" \
  "s_xor_b32 s97, %[wob], 524288\n\t" \
  "v_mfma_f32_16x16x32_bf16 a[48:51], a[152:155], v[128:131], a[48:51]\n\t" \
  "ds_read_b64_tr_b16 a[198:199], v237 offset:12288\n\t" \
  "ds_read_b64_tr_b16 a[200:201], v238 offset:0\n\t" \
  "v_mfma_f32_16x16x32_bf16 a[48:51], a[156:159], v[132:135], a[48:51]\n\t" \
  "buffer_load_dwordx4 v240, %[wo], s97 offen offset:0 lds\n\t" \
  "ds_read_b64_tr_b16 a[202:203], v238 offset:4096\n\t" \
  "v_mfma_f32_16x16x32_bf16 a[64:67], a[160:163], v[128:131], a[64:67]\n\t" \
  "ds_read_b64_tr_b16 a[204:205], v238 offset:8192\n\t" \
  "ds_read_b64_tr_b16 a[206:207], v238 offset:12288\n\t" \
  "v_mfma_f32_16x16x32_bf16 a[64:67], a[164:167], v[132:135], a[64:67]\n\t" \
  "v_exp_f32 v4, v4\n\t" \
  "v_mfma_f32_16x16x32_bf16 a[80:83], a[168:171], v[128:131], a[80:83]\n\t" \
  "s_add_u32 s97, s97, 7168\n\t" \
  "buffer_load_dwordx4 v241, %[wo], s97 offen offset:1024 lds\n\t" \
  "v_mfma_f32_16x16x32_bf16 a[80:83], a[172:175], v[132:135], a[80:83]\n\t" \
  "ds_read_b64_tr_b16 a[208:209], v237 offset:256\n\t" \
  "ds_read_b64_tr_b16 a[210:211], v237 offset:4352\n\t" \
  "v_mfma_f32_16x16x32_bf16 a[96:99], a[176:179], v[128:131], a[96:99]\n\t" \
  "v_exp_f32 v5, v5\n\t" \
  "v_mfma_f32_16x16x32_bf16 a[96:99], a[180:183], v[132:135], a[96:99]\n\t" \
  "s_add_u32 s97, s97, 7168\n\t" \
  "buffer_load_dwordx4 v240, %[wo], s97 offen offset:2048 lds\n\t" \
  "v_mfma_f32_16x16x32_bf16 a[112:115], a[184:187], v[128:131], a[112:115]\n\t" \
  "ds_read_b64_tr_b16 a[212:213], v237 offset:8448\n\t" \
  "ds_read_b64_tr_b16 a[214:215], v237 offset:12544\n\t" \
  "v_mfma_f32_16x16x32_bf16 a[112:115], a[188:191], v[132:135], a[112:115]\n\t" \
  "s_add_u32 s97, s97, 7168\n\t" \
  "buffer_load_dwordx4 v241, %[wo], s97 offen offset:3072 lds\n\t" \
  "v_mfma_f32_16x16x32_bf16 v[160:163], v[216:219], v[128:131], v[160:163]\n\t" \
  "v_exp_f32 v6, v6\n\t" \
  "v_mfma_f32_16x16x32_bf16 v[160:163], v[216:219], v[132:135], v[160:163]\n\t" \
  "v_cvt_pk_bf16_f32 v128, v0, v1\n\t" \
  "v_cvt_pk_bf16_f32 v129, v2, v3\n\t" \
  "v_mfma_f32_16x16x32_bf16 a[4:7], a[128:131], v[136:139], a[4:7]\n\t" \
  "v_cvt_pk_bf16_f32 v130, v4, v5\n\t" \
  "ds_read_b64_tr_b16 a[216:217], v238 offset:256\n\t" \
  "v_mfma_f32_16x16x32_bf16 a[4:7], a[132:135], v[140:143], a[4:7]\n\t" \
  "ds_read_b64_tr_b16 a[218:219], v238 offset:4352\n\t" \
  "ds_read_b64_tr_b16 a[220:221], v238 offset:8448\n\t" \
  "v_mfma_f32_16x16x32_bf16 a[20:23], a[136:139], v[136:139], a[20:23]\n\t" \
  "ds_read_b64_tr_b16 a[222:223], v238 offset:12544\n\t" \
  "ds_read_b64_tr_b16 a[224:225], v237 offset:512\n\t" \
  "v_mfma_f32_16x16x32_bf16 a[20:23], a[140:143], v[140:143], a[20:23]\n\t" \
  "v_exp_f32 v7, v7\n\t" \
  "v_mfma_f32_16x16x32_bf16 a[36:39], a[144:147], v[136:139], a[36:39]\n\t" \
  "v_cvt_pk_bf16_f32 v131, v6, v7\n\t" \
  "s_add_u32 m0, %[m], 114688\n\t" \
  "v_mfma_f32_16x16x32_bf16 a[36:39], a[148:151], v[140:143], a[36:39]\n\t" \
  "ds_read_b64_tr_b16 a[226:227], v237 offset:4608\n\t" \
  "ds_read_b64_tr_b16 a[228:229], v237 offset:8704\n\t" \
  "v_mfma_f32_16x16x32_bf16 a[52:55], a[152:155], v[136:139], a[52:55]\n\t" \
  "s_xor_b32 s97, %[wob], 786432\n\t" \
  "ds_read_b64_tr_b16 a[230:231], v237 offset:12800\n\t" \
  "v_mfma_f32_16x16x32_bf16 a[52:55], a[156:159], v[140:143], a[52:55]\n\t" \
  "v_exp_f32 v8, v8\n\t" \
  "v_mfma_f32_16x16x32_bf16 a[68:71], a[160:163], v[136:139], a[68:71]\n\t" \
  "v_exp_f32 v9, v9\n\t" \
  "v_mfma_f32_16x16x32_bf16 a[68:71], a[164:167], v[140:143], a[68:71]\n\t" \
  "v_cvt_pk_bf16_f32 v132, v8, v9\n\t" \
  "ds_read_b64_tr_b16 a[232:233], v238 offset:512\n\t" \
  "v_mfma_f32_16x16x32_bf16 a[84:87], a[168:171], v[136:139], a[84:87]\n\t" \
  "v_exp_f32 v10, v10\n\t" \
  "v_mfma_f32_16x16x32_bf16 a[84:87], a[172:175], v[140:143], a[84:87]\n\t" \
  "ds_read_b64_tr_b16 a[234:235], v238 offset:4608\n\t" \
  "ds_read_b64_tr_b16 a[236:237], v238 offset:8704\n\t" \
  "v_mfma_f32_16x16x32_bf16 a[100:103], a[176:179], v[136:139], a[100:103]\n\t" \
  "v_exp_f32 v11, v11\n\t" \
  "v_mfma_f32_16x16x32_bf16 a[100:103], a[180:183], v[140:143], a[100:103]\n\t" \
  "v_cvt_pk_bf16_f32 v133, v10, v11\n\t" \
  "ds_read_b64_tr_b16 a[238:239], v238 offset:12800\n\t" \
  "v_mfma_f32_16x16x32_bf16 a[116:119], a[184:187], v[136:139], a[116:119]\n\t" \
  "ds_read_b64_tr_b16 a[240:241], v237 offset:768\n\t" \
  "ds_read_b64_tr_b16 a[242:243], v237 offset:4864\n\t" \
  "v_mfma_f32_16x16x32_bf16 a[116:119], a[188:191], v[140:143], a[116:119]\n\t" \
  "v_exp_f32 v12, v12\n\t" \
  "v_mfma_f32_16x16x32_bf16 v[164:167], v[216:219], v[136:139], v[164:167]\n\t" \
  "v_exp_f32 v13, v13\n\t" \
  "v_mfma_f32_16x16x32_bf16 v[164:167], v[216:219], v[140:143], v[164:167]\n\t" \
  "v_cvt_pk_bf16_f32 v134, v12, v13\n\t" \
  "ds_read_b64_tr_b16 a[244:245], v237 offset:8960\n\t" \
  "v_mfma_f32_16x16x32_bf16 a[8:11], a[128:131], v[144:147], a[8:11]\n\t" \
  "ds_read_b64_tr_b16 a[246:247], v237 offset:13056\n\t" \
  "ds_read_b64_tr_b16 a[248:249], v238 offset:768\n\t" \
  "v_mfma_f32_16x16x32_bf16 a[8:11], a[132:135], v[148:151], a[8:11]\n\t" \
  "v_exp_f32 v14, v14\n\t" \
  "v_mfma_f32_16x16x32_bf16 a[24:27], a[136:139], v[144:147], a[24:27]\n\t" \
  "v_exp_f32 v15, v15\n\t" \
  "v_mfma_f32_16x16x32_bf16 a[24:27], a[140:143], v[148:151], a[24:27]\n\t" \
  "v_cvt_pk_bf16_f32 v135, v14, v15\n\t" \
  "ds_read_b64_tr_b16 a[250:251], v238 offset:4864\n\t" \
  "v_mfma_f32_16x16x32_bf16 a[40:43], a[144:147], v[144:147], a[40:43]\n\t" \
  "ds_read_b64_tr_b16 a[252:253], v238 offset:8960\n\t" \
  "ds_read_b64_tr_b16 a[254:255], v238 offset:13056\n\t" \
  "v_mfma_f32_16x16x32_bf16 a[40:43], a[148:151], v[148:151], a[40:43]\n\t" \
  "v_exp_f32 v32, v32\n\t" \
  "v_mfma_f32_16x16x32_bf16 a[56:59], a[152:155], v[144:147], a[56:59]\n\t" \
  "v_xor_b32 v235, 64, v234\n\t" \
  "ds_read_b64_tr_b16 v[48:49], v235 offset:0\n\t" \
  "v_mfma_f32_16x16x32_bf16 a[56:59], a[156:159], v[148:151], a[56:59]\n\t" \
  "v_exp_f32 v33, v33\n\t" \
  "v_mfma_f32_16x16x32_bf16 a[72:75], a[160:163], v[144:147], a[72:75]\n\t" \
  "v_cvt_pk_bf16_f32 v136, v32, v33\n\t" \
  "ds_read_b64_tr_b16 v[50:51], v235 offset:4096\n\t" \
  "v_mfma_f32_16x16x32_bf16 a[72:75], a[164:167], v[148:151], a[72:75]\n\t" \
  "v_exp_f32 v34, v34\n\t" \
  "v_mfma_f32_16x16x32_bf16 a[88:91], a[168:171], v[144:147], a[88:91]\n\t" \
  "v_exp_f32 v35, v35\n\t" \
  "v_mfma_f32_16x16x32_bf16 a[88:91], a[172:175], v[148:151], a[88:91]\n\t" \
  "v_cvt_pk_bf16_f32 v137, v34, v35\n\t" \
  "ds_read_b64_tr_b16 v[52:53], v235 offset:8192\n\t" \
  "v_mfma_f32_16x16x32_bf16 a[104:107], a[176:179], v[144:147], a[104:107]\n\t" \
  "v_exp_f32 v36, v36\n\t" \
  "v_mfma_f32_16x16x32_bf16 a[104:107], a[180:183], v[148:151], a[104:107]\n\t" \
  "v_exp_f32 v37, v37\n\t" \
  "v_mfma_f32_16x16x32_bf16 a[120:123], a[184:187], v[144:147], a[120:123]\n\t" \
  "v_cvt_pk_bf16_f32 v138, v36, v37\n\t" \
  "ds_read_b64_tr_b16 v[54:55], v235 offset:12288\n\t" \
  "v_mfma_f32_16x16x32_bf16 a[120:123], a[188:191], v[148:151], a[120:123]\n\t" \
  "v_exp_f32 v38, v38\n\t" \
  "v_mfma_f32_16x16x32_bf16 v[168:171], v[216:219], v[144:147], v[168:171]\n\t" \
  "v_exp_f32 v39, v39\n\t" \
  "v_mfma_f32_16x16x32_bf16 v[168:171], v[216:219], v[148:151], v[168:171]\n\t" \
  "v_cvt_pk_bf16_f32 v139, v38, v39\n\t" \
  "ds_read_b64_tr_b16 v[56:57], v235 offset:16384\n\t" \
  "v_mfma_f32_16x16x32_bf16 a[12:15], a[128:131], v[152:155], a[12:15]\n\t" \
  "v_exp_f32 v40, v40\n\t" \
  "v_mfma_f32_16x16x32_bf16 a[12:15], a[132:135], v[156:159], a[12:15]\n\t" \
  "v_exp_f32 v41, v41\n\t" \
  "v_mfma_f32_16x16x32_bf16 a[28:31], a[136:139], v[152:155], a[28:31]\n\t" \
  "v_cvt_pk_bf16_f32 v140, v40, v41\n\t" \
  "ds_read_b64_tr_b16 v[58:59], v235 offset:20480\n\t" \
  "v_mfma_f32_16x16x32_bf16 a[28:31], a[140:143], v[156:159], a[28:31]\n\t" \
  "v_exp_f32 v42, v42\n\t" \
  "v_mfma_f32_16x16x32_bf16 a[44:47], a[144:147], v[152:155], a[44:47]\n\t" \
  "ds_read_b64_tr_b16 v[60:61], v235 offset:24576\n\t" \
  "ds_read_b64_tr_b16 v[62:63], v235 offset:28672\n\t" \
  "v_mfma_f32_16x16x32_bf16 a[44:47], a[148:151], v[156:159], a[44:47]\n\t" \
  "v_exp_f32 v43, v43\n\t" \
  "v_mfma_f32_16x16x32_bf16 a[60:63], a[152:155], v[152:155], a[60:63]\n\t" \
  "v_cvt_pk_bf16_f32 v141, v42, v43\n\t" \
  "v_xor_b32 v236, 96, v234\n\t" \
  "v_mfma_f32_16x16x32_bf16 a[60:63], a[156:159], v[156:159], a[60:63]\n\t" \
  "v_exp_f32 v44, v44\n\t" \
  "v_mfma_f32_16x16x32_bf16 a[76:79], a[160:163], v[152:155], a[76:79]\n\t" \
  "v_exp_f32 v45, v45\n\t" \
  "v_mfma_f32_16x16x32_bf16 a[76:79], a[164:167], v[156:159], a[76:79]\n\t" \
  "v_cvt_pk_bf16_f32 v142, v44, v45\n\t" \
  "ds_read_b64_tr_b16 v[80:81], v236 offset:0\n\t" \
  "v_mfma_f32_16x16x32_bf16 a[92:95], a[168:171], v[152:155], a[92:95]\n\t" \
  "v_exp_f32 v46, v46\n\t" \
  "v_mfma_f32_16x16x32_bf16 a[92:95], a[172:175], v[156:159], a[92:95]\n\t" \
  "v_exp_f32 v47, v47\n\t" \
  "v_mfma_f32_16x16x32_bf16 a[108:111], a[176:179], v[152:155], a[108:111]\n\t" \
  "v_cvt_pk_bf16_f32 v143, v46, v47\n\t" \
  "ds_read_b64_tr_b16 v[82:83], v236 offset:4096\n\t" \
  "v_mfma_f32_16x16x32_bf16 a[108:111], a[180:183], v[156:159], a[108:111]\n\t" \
  "v_exp_f32 v64, v64\n\t" \
  "v_mfma_f32_16x16x32_bf16 a[124:127], a[184:187], v[152:155], a[124:127]\n\t" \
  "v_exp_f32 v65, v65\n\t" \
  "v_mfma_f32_16x16x32_bf16 a[124:127], a[188:191], v[156:159], a[124:127]\n\t" \
  "v_cvt_pk_bf16_f32 v144, v64, v65\n\t" \
  "ds_read_b64_tr_b16 v[84:85], v236 offset:8192\n\t" \
  "v_mfma_f32_16x16x32_bf16 v[172:175], v[216:219], v[152:155], v[172:175]\n\t" \
  "v_exp_f32 v66, v66\n\t" \
  "v_mfma_f32_16x16x32_bf16 v[172:175], v[216:219], v[156:159], v[172:175]\n\t" \
  "ds_read_b64_tr_b16 v[86:87], v236 offset:12288\n\t" \
  "ds_read_b64_tr_b16 v[88:89], v236 offset:16384\n\t" \
  "s_cmp_lg_u32 s99, 0\n\t" \
  "s_cbranch_scc1 .Lsf_p7\n\t" \
  ".Lsf_q7:\n\t" \
  "s_waitcnt lgkmcnt(15)\n\t" \
  "v_mfma_f32_16x16x32_bf16 a[0:3], a[192:195], v[128:131], a[0:3]\n\t" \
  "v_exp_f32 v67, v67\n\t" \
  "v_mfma_f32_16x16x32_bf16 a[0:3], a[196:199], v[132:135], a[0:3]\n\t" \
  "v_cvt_pk_bf16_f32 v145, v66, v67\n\t" \
  "ds_read_b128 v[176:179], v215 offset:16384\n\t" \
  "v_mfma_f32_16x16x32_bf16 v[160:163], v[216:219], v[128:131], v[160:163]\n\t" \
  "v_exp_f32 v68, v68\n\t" \
  "v_mfma_f32_16x16x32_bf16 v[160:163], v[216:219], v[132:135], v[160:163]\n\t" \
  "ds_read_b64_tr_b16 v[90:91], v236 offset:20480\n\t" \
  "ds_read_b128 v[180:183], v215 offset:17408\n\t" \
  "v_mfma_f32_16x16x32_bf16 a[16:19], a[200:203], v[128:131], a[16:19]\n\t" \
  "ds_read_b64_tr_b16 v[92:93], v236 offset:24576\n\t" \
  "ds_read_b128 v[184:187], v215 offset:18432\n\t" \
  "v_mfma_f32_16x16x32_bf16 a[16:19], a[204:207], v[132:135], a[16:19]\n\t" \
  "v_exp_f32 v69, v69\n\t" \
  "v_mfma_f32_16x16x32_bf16 a[32:35], a[208:211], v[128:131], a[32:35]\n\t" \
  "v_cvt_pk_bf16_f32 v146, v68, v69\n\t" \
  "ds_read_b128 v[188:191], v215 offset:19456\n\t" \
  "v_mfma_f32_16x16x32_bf16 a[32:35], a[212:215], v[132:135], a[32:35]\n\t" \
  "s_waitcnt lgkmcnt(0)\n\t" \
  "buffer_load_dwordx4 v240, %[wo], s97 offen offset:0 lds\n\t" \
  "v_mfma_f32_16x16x32_bf16 a[48:51], a[216:219], v[128:131], a[48:51]\n\t" \
  "s_add_u32 s97, s97, 7168\n\t" \
  "buffer_load_dwordx4 v241, %[wo], s97 offen offset:1024 lds\n\t" \
  "v_mfma_f32_16x16x32_bf16 a[48:51], a[220:223], v[132:135], a[48:51]\n\t" \
  "s_add_u32 s97, s97, 7168\n\t" \
  "buffer_load_dwordx4 v240, %[wo], s97 offen offset:2048 lds\n\t" \
  "v_mfma_f32_16x16x32_bf16 a[64:67], a[224:227], v[128:131], a[64:67]\n\t" \
  "s_add_u32 s97, s97, 7168\n\t" \
  "buffer_load_dwordx4 v241, %[wo], s97 offen offset:3072 lds\n\t" \
  "v_mfma_f32_16x16x32_bf16 a[64:67], a[228:231], v[132:135], a[64:67]\n\t" \
  "s_add_u32 m0, %[m], 131072\n\t" \
  "s_xor_b32 s97, %[wob], 917504\n\t" \
  "v_mfma_f32_16x16x32_bf16 a[80:83], a[232:235], v[128:131], a[80:83]\n\t" \
  "ds_read_b64_tr_b16 v[94:95], v236 offset:28672\n\t" \
  "ds_read_b128 v[192:195], v215 offset:32768\n\t" \
  "v_mfma_f32_16x16x32_bf16 a[80:83], a[236:239], v[132:135], a[80:83]\n\t" \
  "v_rcp_f32 v208, v160\n\t" \
  "v_mfma_f32_16x16x32_bf16 a[96:99], a[240:243], v[128:131], a[96:99]\n\t" \
  "v_accvgpr_read_b32 v16, a0\n\t" \
  "v_accvgpr_read_b32 v17, a1\n\t" \
  "v_mfma_f32_16x16x32_bf16 a[96:99], a[244:247], v[132:135], a[96:99]\n\t" \
  "v_accvgpr_read_b32 v18, a2\n\t" \
  "v_accvgpr_read_b32 v19, a3\n\t" \
  "v_mfma_f32_16x16x32_bf16 a[112:115], a[248:251], v[128:131], a[112:115]\n\t" \
  "v_accvgpr_read_b32 v20, a16\n\t" \
  "v_accvgpr_read_b32 v21, a17\n\t" \
  "v_mfma_f32_16x16x32_bf16 a[112:115], a[252:255], v[132:135], a[112:115]\n\t" \
  "v_accvgpr_read_b32 v22, a18\n\t" \
  "v_accvgpr_read_b32 v23, a19\n\t" \
  "s_waitcnt vmcnt(10)\n\t" \
  "s_barrier\n\t" \
  "v_mfma_f32_16x16x32_bf16 a[4:7], a[192:195], v[136:139], a[4:7]\n\t" \
  "v_xor_b32 v235, 128, v234\n\t" \
  "ds_read_b128 v[196:199], v215 offset:33792\n\t" \
  "v_mfma_f32_16x16x32_bf16 a[4:7], a[196:199], v[140:143], a[4:7]\n\t" \
  "v_mul_f32 v16, v16, v208\n\t" \
  "v_mul_f32 v17, v17, v208\n\t" \
  "v_mfma_f32_16x16x32_bf16 v[164:167], v[216:219], v[136:139], v[164:167]\n\t" \
  "v_mul_f32 v18, v18, v208\n\t" \
  "v_mul_f32 v19, v19, v208\n\t" \
  "v_mfma_f32_16x16x32_bf16 v[164:167], v[216:219], v[140:143], v[164:167]\n\t" \
  "v_mul_f32 v20, v20, v208\n\t" \
  "v_mul_f32 v21, v21, v208\n\t" \
  "v_mfma_f32_16x16x32_bf16 a[20:23], a[200:203], v[136:139], a[20:23]\n\t" \
  "v_mul_f32 v22, v22, v208\n\t" \
  "v_mul_f32 v23, v23, v208\n\t" \
  "v_mfma_f32_16x16x32_bf16 a[20:23], a[204:207], v[140:143], a[20:23]\n\t" \
  "ds_read_b128 v[200:203], v215 offset:34816\n\t" \
  "ds_read_b64_tr_b16 v[112:113], v235 offset:0\n\t" \
  "v_mfma_f32_16x16x32_bf16 a[36:39], a[208:211], v[136:139], a[36:39]\n\t" \
  "ds_read_b128 v[204:207], v215 offset:35840\n\t" \
  "s_waitcnt lgkmcnt(0)\n\t" \
  "v_mfma_f32_16x16x32_bf16 a[36:39], a[212:215], v[140:143], a[36:39]\n\t" \
  "buffer_load_dwordx4 v240, %[wo], s97 offen offset:0 lds\n\t" \
  "ds_read_b64_tr_b16 v[114:115], v235 offset:4096\n\t" \
  "v_mfma_f32_16x16x32_bf16 a[52:55], a[216:219], v[136:139], a[52:55]\n\t" \
  "s_add_u32 s97, s97, 7168\n\t" \
  "buffer_load_dwordx4 v241, %[wo], s97 offen offset:1024 lds\n\t" \
  "v_mfma_f32_16x16x32_bf16 a[52:55], a[220:223], v[140:143], a[52:55]\n\t" \
  "s_add_u32 s97, s97, 7168\n\t" \
  "buffer_load_dwordx4 v240, %[wo], s97 offen offset:2048 lds\n\t" \
  "v_mfma_f32_16x16x32_bf16 a[68:71], a[224:227], v[136:139], a[68:71]\n\t" \
  "s_add_u32 s97, s97, 7168\n\t" \
  "buffer_load_dwordx4 v241, %[wo], s97 offen offset:3072 lds\n\t" \
  "v_mfma_f32_16x16x32_bf16 a[68:71], a[228:231], v[140:143], a[68:71]\n\t" \
  "v_cvt_pk_bf16_f32 v0, v16, v17\n\t" \
  "v_cvt_pk_bf16_f32 v1, v18, v19\n\t" \
  "v_mfma_f32_16x16x32_bf16 a[84:87], a[232:235], v[136:139], a[84:87]\n\t" \
  "v_cvt_pk_bf16_f32 v2, v20, v21\n\t" \
  "v_cvt_pk_bf16_f32 v3, v22, v23\n\t" \
  "v_mfma_f32_16x16x32_bf16 a[84:87], a[236:239], v[140:143], a[84:87]\n\t" \
  "s_xor_b32 s98, %[eso], 0\n\t" \
  "buffer_store_dwordx4 v[0:3], v242, %[ws], s98 offen offset:0 sc1\n\t" \
  "v_mfma_f32_16x16x32_bf16 a[100:103], a[240:243], v[136:139], a[100:103]\n\t" \
  "v_accvgpr_read_b32 v24, a32\n\t" \
  "v_accvgpr_read_b32 v25, a33\n\t" \
  "v_mfma_f32_16x16x32_bf16 a[100:103], a[244:247], v[140:143], a[100:103]\n\t" \
  "v_accvgpr_read_b32 v26, a34\n\t" \
  "v_accvgpr_read_b32 v27, a35\n\t" \
  "v_mfma_f32_16x16x32_bf16 a[116:119], a[248:251], v[136:139], a[116:119]\n\t" \
  "v_accvgpr_read_b32 v28, a48\n\t" \
  "v_accvgpr_read_b32 v29, a49\n\t" \
  "v_mfma_f32_16x16x32_bf16 a[116:119], a[252:255], v[140:143], a[116:119]\n\t" \
  "v_accvgpr_read_b32 v30, a50\n\t" \
  "v_accvgpr_read_b32 v31, a51\n\t" \
  "v_exp_f32 v70, v70\n\t" \
  "v_exp_f32 v71, v71\n\t" \
  "s_nop 0\n\t" \
  "v_cvt_pk_bf16_f32 v147, v70, v71\n\t" \
  "s_nop 1\n\t" \
  "v_mfma_f32_16x16x32_bf16 a[8:11], a[192:195], v[144:147], a[8:11]\n\t" \
  "ds_read_b64_tr_b16 v[116:117], v235 offset:8192\n\t" \
  "ds_read_b64_tr_b16 v[118:119], v235 offset:12288\n\t" \
  "v_exp_f32 v72, v72\n\t" \
  "v_exp_f32 v73, v73\n\t" \
  "v_exp_f32 v74, v74\n\t" \
  "v_exp_f32 v75, v75\n\t" \
  "v_exp_f32 v76, v76\n\t" \
  "v_exp_f32 v77, v77\n\t" \
  "v_exp_f32 v78, v78\n\t" \
  "v_exp_f32 v79, v79\n\t" \
  "v_cvt_pk_bf16_f32 v148, v72, v73\n\t" \
  "v_cvt_pk_bf16_f32 v149, v74, v75\n\t" \
  "v_cvt_pk_bf16_f32 v150, v76, v77\n\t" \
  "v_cvt_pk_bf16_f32 v151, v78, v79\n\t" \
  "s_nop 1\n\t" \
  "v_mfma_f32_16x16x32_bf16 a[8:11], a[196:199], v[148:151], a[8:11]\n\t" \
  "v_mul_f32 v24, v24, v208\n\t" \
  "v_mul_f32 v25, v25, v208\n\t" \
  "v_mfma_f32_16x16x32_bf16 v[168:171], v[216:219], v[144:147], v[168:171]\n\t" \
  "v_mul_f32 v26, v26, v208\n\t" \
  "v_mul_f32 v27, v27, v208\n\t" \
  "v_mfma_f32_16x16x32_bf16 v[168:171], v[216:219], v[148:151], v[168:171]\n\t" \
  "v_mul_f32 v28, v28, v208\n\t" \
  "v_mul_f32 v29, v29, v208\n\t" \
  "v_mfma_f32_16x16x32_bf16 a[24:27], a[200:203], v[144:147], a[24:27]\n\t" \
  "v_mul_f32 v30, v30, v208\n\t" \
  "v_mul_f32 v31, v31, v208\n\t" \
  "v_mfma_f32_16x16x32_bf16 a[24:27], a[204:207], v[148:151], a[24:27]\n\t" \
  "ds_read_b64_tr_b16 v[120:121], v235 offset:16384\n\t" \
  "v_cvt_pk_bf16_f32 v4, v24, v25\n\t" \
  "v_mfma_f32_16x16x32_bf16 a[40:43], a[208:211], v[144:147], a[40:43]\n\t" \
  "v_cvt_pk_bf16_f32 v5, v26, v27\n\t" \
  "v_cvt_pk_bf16_f32 v6, v28, v29\n\t" \
  "v_mfma_f32_16x16x32_bf16 a[40:43], a[212:215], v[148:151], a[40:43]\n\t" \
  "v_cvt_pk_bf16_f32 v7, v30, v31\n\t" \
  "ds_read_b64_tr_b16 v[122:123], v235 offset:20480\n\t" \
  "v_mfma_f32_16x16x32_bf16 a[56:59], a[216:219], v[144:147], a[56:59]\n\t" \
  "s_xor_b32 s98, %[eso], 0\n\t" \
  "buffer_store_dwordx4 v[4:7], v242, %[ws], s98 offen offset:1024 sc1\n\t" \
  "v_mfma_f32_16x16x32_bf16 a[56:59], a[220:223], v[148:151], a[56:59]\n\t" \
  "v_accvgpr_read_b32 v16, a64\n\t" \
  "v_accvgpr_read_b32 v17, a65\n\t" \
  "v_mfma_f32_16x16x32_bf16 a[72:75], a[224:227], v[144:147], a[72:75]\n\t" \
  "v_accvgpr_read_b32 v18, a66\n\t" \
  "v_accvgpr_read_b32 v19, a67\n\t" \
  "v_mfma_f32_16x16x32_bf16 a[72:75], a[228:231], v[148:151], a[72:75]\n\t" \
  "v_accvgpr_read_b32 v20, a80\n\t" \
  "v_accvgpr_read_b32 v21, a81\n\t" \
  "v_mfma_f32_16x16x32_bf16 a[88:91], a[232:235], v[144:147], a[88:91]\n\t" \
  "v_accvgpr_read_b32 v22, a82\n\t" \
  "v_accvgpr_read_b32 v23, a83\n\t" \
  "v_mfma_f32_16x16x32_bf16 a[88:91], a[236:239], v[148:151], a[88:91]\n\t" \
  "ds_read_b64_tr_b16 v[124:125], v235 offset:24576\n\t" \
  "v_mul_f32 v16, v16, v208\n\t" \
  "v_mfma_f32_16x16x32_bf16 a[104:107], a[240:243], v[144:147], a[104:107]\n\t" \
  "v_mul_f32 v17, v17, v208\n\t" \
  "v_mul_f32 v18, v18, v208\n\t" \
  "v_mfma_f32_16x16x32_bf16 a[104:107], a[244:247], v[148:151], a[104:107]\n\t" \
  "v_mul_f32 v19, v19, v208\n\t" \
  "v_mul_f32 v20, v20, v208\n\t" \
  "v_mfma_f32_16x16x32_bf16 a[120:123], a[248:251], v[144:147], a[120:123]\n\t" \
  "v_mul_f32 v21, v21, v208\n\t" \
  "v_mul_f32 v22, v22, v208\n\t" \
  "v_mfma_f32_16x16x32_bf16 a[120:123], a[252:255], v[148:151], a[120:123]\n\t" \
  "v_mul_f32 v23, v23, v208\n\t" \
  "ds_read_b64_tr_b16 v[126:127], v235 offset:28672\n\t" \
  "v_exp_f32 v96, v96\n\t" \
  "v_exp_f32 v97, v97\n\t" \
  "v_exp_f32 v98, v98\n\t" \
  "v_exp_f32 v99, v99\n\t" \
  "v_exp_f32 v100, v100\n\t" \
  "v_exp_f32 v101, v101\n\t" \
  "v_exp_f32 v102, v102\n\t" \
  "v_exp_f32 v103, v103\n\t" \
  "v_cvt_pk_bf16_f32 v152, v96, v97\n\t" \
  "v_cvt_pk_bf16_f32 v153, v98, v99\n\t" \
  "v_cvt_pk_bf16_f32 v154, v100, v101\n\t" \
  "v_cvt_pk_bf16_f32 v155, v102, v103\n\t" \
  "s_nop 1\n\t" \
  "v_mfma_f32_16x16x32_bf16 a[12:15], a[192:195], v[152:155], a[12:15]\n\t" \
  "v_cvt_pk_bf16_f32 v8, v16, v17\n\t" \
  "v_cvt_pk_bf16_f32 v9, v18, v19\n\t" \
  "v_exp_f32 v104, v104\n\t" \
  "v_exp_f32 v105, v105\n\t" \
  "v_exp_f32 v106, v106\n\t" \
  "v_exp_f32 v107, v107\n\t" \
  "v_exp_f32 v108, v108\n\t" \
  "v_exp_f32 v109, v109\n\t" \
  "v_exp_f32 v110, v110\n\t" \
  "v_exp_f32 v111, v111\n\t" \
  "v_cvt_pk_bf16_f32 v156, v104, v105\n\t" \
  "v_cvt_pk_bf16_f32 v157, v106, v107\n\t" \
  "v_cvt_pk_bf16_f32 v158, v108, v109\n\t" \
  "v_cvt_pk_bf16_f32 v159, v110, v111\n\t" \
  "s_nop 1\n\t" \
  "v_mfma_f32_16x16x32_bf16 a[12:15], a[196:199], v[156:159], a[12:15]\n\t" \
  "v_cvt_pk_bf16_f32 v10, v20, v21\n\t" \
  "v_cvt_pk_bf16_f32 v11, v22, v23\n\t" \
  "v_mfma_f32_16x16x32_bf16 v[172:175], v[216:219], v[152:155], v[172:175]\n\t" \
  "s_xor_b32 s98, %[eso], 0\n\t" \
  "buffer_store_dwordx4 v[8:11], v242, %[ws], s98 offen offset:2048 sc1\n\t" \
  "v_mfma_f32_16x16x32_bf16 v[172:175], v[216:219], v[156:159], v[172:175]\n\t" \
  "ds_read_b64_tr_b16 a[128:129], v234 offset:0\n\t" \
  "ds_read_b64_tr_b16 a[130:131], v234 offset:4096\n\t" \
  "v_mfma_f32_16x16x32_bf16 a[28:31], a[200:203], v[152:155], a[28:31]\n\t" \
  "v_accvgpr_read_b32 v24, a96\n\t" \
  "v_accvgpr_read_b32 v25, a97\n\t" \
  "v_mfma_f32_16x16x32_bf16 a[28:31], a[204:207], v[156:159], a[28:31]\n\t" \
  "v_accvgpr_read_b32 v26, a98\n\t" \
  "v_accvgpr_read_b32 v27, a99\n\t" \
  "v_mfma_f32_16x16x32_bf16 a[44:47], a[208:211], v[152:155], a[44:47]\n\t" \
  "v_accvgpr_read_b32 v28, a112\n\t" \
  "v_accvgpr_read_b32 v29, a113\n\t" \
  "v_mfma_f32_16x16x32_bf16 a[44:47], a[212:215], v[156:159], a[44:47]\n\t" \
  "v_accvgpr_read_b32 v30, a114\n\t" \
  "v_accvgpr_read_b32 v31, a115\n\t" \
  "v_mfma_f32_16x16x32_bf16 a[60:63], a[216:219], v[152:155], a[60:63]\n\t" \
  "ds_read_b64_tr_b16 a[132:133], v234 offset:8192\n\t" \
  "ds_read_b64_tr_b16 a[134:135], v234 offset:12288\n\t" \
  "v_mfma_f32_16x16x32_bf16 a[60:63], a[220:223], v[156:159], a[60:63]\n\t" \
  "v_mul_f32 v24, v24, v208\n\t" \
  "v_mul_f32 v25, v25, v208\n\t" \
  "v_mfma_f32_16x16x32_bf16 a[76:79], a[224:227], v[152:155], a[76:79]\n\t" \
  "v_mul_f32 v26, v26, v208\n\t" \
  "v_mul_f32 v27, v27, v208\n\t" \
  "v_mfma_f32_16x16x32_bf16 a[76:79], a[228:231], v[156:159], a[76:79]\n\t" \
  "v_mul_f32 v28, v28, v208\n\t" \
  "v_mul_f32 v29, v29, v208\n\t" \
  "v_mfma_f32_16x16x32_bf16 a[92:95], a[232:235], v[152:155], a[92:95]\n\t" \
  "v_mul_f32 v30, v30, v208\n\t" \
  "v_mul_f32 v31, v31, v208\n\t" \
  "v_mfma_f32_16x16x32_bf16 a[92:95], a[236:239], v[156:159], a[92:95]\n\t" \
  "ds_read_b64_tr_b16 a[136:137], v234 offset:16384\n\t" \
  "ds_read_b64_tr_b16 a[138:139], v234 offset:20480\n\t" \
  "v_mfma_f32_16x16x32_bf16 a[108:111], a[240:243], v[152:155], a[108:111]\n\t" \
  "v_cvt_pk_bf16_f32 v12, v24, v25\n\t" \
  "v_cvt_pk_bf16_f32 v13, v26, v27\n\t" \
  "v_mfma_f32_16x16x32_bf16 a[108:111], a[244:247], v[156:159], a[108:111]\n\t" \
  "v_cvt_pk_bf16_f32 v14, v28, v29\n\t" \
  "v_cvt_pk_bf16_f32 v15, v30, v31\n\t" \
  "v_mfma_f32_16x16x32_bf16 a[124:127], a[248:251], v[152:155], a[124:127]\n\t" \
  "s_xor_b32 s98, %[eso], 0\n\t" \
  "buffer_store_dwordx4 v[12:15], v242, %[ws], s98 offen offset:3072 sc1\n\t" \
  "v_mfma_f32_16x16x32_bf16 a[124:127], a[252:255], v[156:159], a[124:127]\n\t" \
  "ds_read_b64_tr_b16 a[140:141], v234 offset:24576\n\t" \
  "ds_read_b64_tr_b16 a[142:143], v234 offset:28672\n\t" \
  "v_mfma_f32_16x16x32_bf16 a[32:35], v[48:51], v[0:3], 0\n\t" \
  "v_rcp_f32 v210, v164\n\t" \
  "v_mfma_f32_16x16x32_bf16 a[32:35], v[52:55], v[4:7], a[32:35]\n\t" \
  "v_accvgpr_read_b32 v16, a4\n\t" \
  "v_accvgpr_read_b32 v17, a5\n\t" \
  "v_mfma_f32_16x16x32_bf16 a[32:35], v[56:59], v[8:11], a[32:35]\n\t" \
  "v_accvgpr_read_b32 v18, a6\n\t" \
  "v_accvgpr_read_b32 v19, a7\n\t" \
  "v_mfma_f32_16x16x32_bf16 a[32:35], v[60:63], v[12:15], a[32:35]\n\t" \
  "v_accvgpr_read_b32 v20, a20\n\t" \
  "v_accvgpr_read_b32 v21, a21\n\t" \
  "v_mfma_f32_16x16x32_bf16 a[48:51], v[92:95], v[12:15], 0\n\t" \
  "v_accvgpr_read_b32 v22, a22\n\t" \
  "v_accvgpr_read_b32 v23, a23\n\t" \
  "v_mfma_f32_16x16x32_bf16 a[48:51], v[88:91], v[8:11], a[48:51]\n\t" \
  "v_xor_b32 v236, 32, v234\n\t" \
  "ds_read_b64_tr_b16 a[144:145], v236 offset:0\n\t" \
  "v_mfma_f32_16x16x32_bf16 a[48:51], v[84:87], v[4:7], a[48:51]\n\t" \
  "v_mul_f32 v16, v16, v210\n\t" \
  "v_mul_f32 v17, v17, v210\n\t" \
  "v_mfma_f32_16x16x32_bf16 a[48:51], v[80:83], v[0:3], a[48:51]\n\t" \
  "v_mul_f32 v18, v18, v210\n\t" \
  "v_mul_f32 v19, v19, v210\n\t" \
  "s_waitcnt lgkmcnt(15)\n\t" \
  "v_mfma_f32_16x16x32_bf16 a[64:67], v[112:115], v[0:3], 0\n\t" \
  "v_mul_f32 v20, v20, v210\n\t" \
  "v_mul_f32 v21, v21, v210\n\t" \
  "s_waitcnt lgkmcnt(13)\n\t" \
  "v_mfma_f32_16x16x32_bf16 a[64:67], v[116:119], v[4:7], a[64:67]\n\t" \
  "v_mul_f32 v22, v22, v210\n\t" \
  "v_mul_f32 v23, v23, v210\n\t" \
  "s_waitcnt lgkmcnt(11)\n\t" \
  "v_mfma_f32_16x16x32_bf16 a[64:67], v[120:123], v[8:11], a[64:67]\n\t" \
  "ds_read_b64_tr_b16 a[146:147], v236 offset:4096\n\t" \
  "ds_read_b64_tr_b16 a[148:149], v236 offset:8192\n\t" \
  "s_waitcnt lgkmcnt(11)\n\t" \
  "v_mfma_f32_16x16x32_bf16 a[64:67], v[124:127], v[12:15], a[64:67]\n\t" \
  "v_cvt_pk_bf16_f32 v32, v16, v17\n\t" \
  "v_cvt_pk_bf16_f32 v33, v18, v19\n\t" \
  "ds_read_b64_tr_b16 a[150:151], v236 offset:12288\n\t" \
  "ds_read_b64_tr_b16 a[152:153], v236 offset:16384\n\t" \
  "ds_read_b64_tr_b16 a[154:155], v236 offset:20480\n\t" \
  "ds_read_b64_tr_b16 a[156:157], v236 offset:24576\n\t" \
  "ds_read_b64_tr_b16 a[158:159], v236 offset:28672\n\t" \
  "s_waitcnt lgkmcnt(8)\n\t" \
  "v_mfma_f32_16x16x32_bf16 a[0:3], a[140:143], v[12:15], 0\n\t" \
  "v_cvt_pk_bf16_f32 v34, v20, v21\n\t" \
  "v_cvt_pk_bf16_f32 v35, v22, v23\n\t" \
  "v_mfma_f32_16x16x32_bf16 a[0:3], a[136:139], v[8:11], a[0:3]\n\t" \
  "s_xor_b32 s98, %[eso], 32768\n\t" \
  "buffer_store_dwordx4 v[32:35], v242, %[ws], s98 offen offset:0 sc1\n\t" \
  "v_mfma_f32_16x16x32_bf16 a[0:3], a[132:135], v[4:7], a[0:3]\n\t" \
  "v_accvgpr_read_b32 v24, a36\n\t" \
  "v_accvgpr_read_b32 v25, a37\n\t" \
  "v_mfma_f32_16x16x32_bf16 a[0:3], a[128:131], v[0:3], a[0:3]\n\t" \
  "v_accvgpr_read_b32 v26, a38\n\t" \
  "v_accvgpr_read_b32 v27, a39\n\t" \
  "v_xor_b32 v235, 192, v234\n\t" \
  "ds_read_b64_tr_b16 a[160:161], v235 offset:0\n\t" \
  "ds_read_b64_tr_b16 a[162:163], v235 offset:4096\n\t" \
  "ds_read_b64_tr_b16 a[164:165], v235 offset:8192\n\t" \
  "ds_read_b64_tr_b16 a[166:167], v235 offset:12288\n\t" \
  "ds_read_b64_tr_b16 a[168:169], v235 offset:16384\n\t" \
  "ds_read_b64_tr_b16 a[170:171], v235 offset:20480\n\t" \
  "ds_read_b64_tr_b16 a[172:173], v235 offset:24576\n\t" \
  "ds_read_b64_tr_b16 a[174:175], v235 offset:28672\n\t" \
  "s_waitcnt lgkmcnt(14)\n\t" \
  "v_mfma_f32_16x16x32_bf16 a[16:19], a[144:147], v[0:3], 0\n\t" \
  "v_accvgpr_read_b32 v28, a52\n\t" \
  "v_accvgpr_read_b32 v29, a53\n\t" \
  "s_waitcnt lgkmcnt(12)\n\t" \
  "v_mfma_f32_16x16x32_bf16 a[16:19], a[148:151], v[4:7], a[16:19]\n\t" \
  "v_accvgpr_read_b32 v30, a54\n\t" \
  "v_accvgpr_read_b32 v31, a55\n\t" \
  "s_waitcnt lgkmcnt(10)\n\t" \
  "v_mfma_f32_16x16x32_bf16 a[16:19], a[152:155], v[8:11], a[16:19]\n\t" \
  "v_mul_f32 v24, v24, v210\n\t" \
  "v_mul_f32 v25, v25, v210\n\t" \
  "s_waitcnt lgkmcnt(8)\n\t" \
  "v_mfma_f32_16x16x32_bf16 a[16:19], a[156:159], v[12:15], a[16:19]\n\t" \
  "v_mul_f32 v26, v26, v210\n\t" \
  "v_mul_f32 v27, v27, v210\n\t" \
  "v_xor_b32 v236, 224, v234\n\t" \
  "ds_read_b64_tr_b16 a[176:177], v236 offset:0\n\t" \
  "ds_read_b64_tr_b16 a[178:179], v236 offset:4096\n\t" \
  "ds_read_b64_tr_b16 a[180:181], v236 offset:8192\n\t" \
  "ds_read_b64_tr_b16 a[182:183], v236 offset:12288\n\t" \
  "ds_read_b64_tr_b16 a[184:185], v236 offset:16384\n\t" \
  "ds_read_b64_tr_b16 a[186:187], v236 offset:20480\n\t" \
  "ds_read_b64_tr_b16 a[188:189], v236 offset:24576\n\t" \
  "ds_read_b64_tr_b16 a[190:191], v236 offset:28672\n\t" \
  "s_waitcnt lgkmcnt(8)\n\t" \
  "v_mfma_f32_16x16x32_bf16 a[96:99], a[172:175], v[12:15], 0\n\t" \
  "v_mul_f32 v28, v28, v210\n\t" \
  "v_mul_f32 v29, v29, v210\n\t" \
  "v_mfma_f32_16x16x32_bf16 a[96:99], a[168:171], v[8:11], a[96:99]\n\t" \
  "v_mul_f32 v30, v30, v210\n\t" \
  "v_mul_f32 v31, v31, v210\n\t" \
  "v_mfma_f32_16x16x32_bf16 a[96:99], a[164:167], v[4:7], a[96:99]\n\t" \
  "v_cvt_pk_bf16_f32 v36, v24, v25\n\t" \
  "v_cvt_pk_bf16_f32 v37, v26, v27\n\t" \
  "v_mfma_f32_16x16x32_bf16 a[96:99], a[160:163], v[0:3], a[96:99]\n\t" \
  "v_cvt_pk_bf16_f32 v38, v28, v29\n\t" \
  "v_cvt_pk_bf16_f32 v39, v30, v31\n\t" \
  "v_xor_b32 v236, 160, v234\n\t" \
  "ds_read_b64_tr_b16 v[96:97], v236 offset:0\n\t" \
  "ds_read_b64_tr_b16 v[98:99], v236 offset:4096\n\t" \
  "ds_read_b64_tr_b16 v[100:101], v236 offset:8192\n\t" \
  "ds_read_b64_tr_b16 v[102:103], v236 offset:12288\n\t" \
  "ds_read_b64_tr_b16 v[104:105], v236 offset:16384\n\t" \
  "ds_read_b64_tr_b16 v[106:107], v236 offset:20480\n\t" \
  "ds_read_b64_tr_b16 v[108:109], v236 offset:24576\n\t" \
  "ds_read_b64_tr_b16 v[110:111], v236 offset:28672\n\t" \
  "s_waitcnt lgkmcnt(14)\n\t" \
  "v_mfma_f32_16x16x32_bf16 a[112:115], a[176:179], v[0:3], 0\n\t" \
  "s_xor_b32 s98, %[eso], 32768\n\t" \
  "buffer_store_dwordx4 v[36:39], v242, %[ws], s98 offen offset:1024 sc1\n\t" \
  "s_waitcnt lgkmcnt(12)\n\t" \
  "v_mfma_f32_16x16x32_bf16 a[112:115], a[180:183], v[4:7], a[112:115]\n\t" \
  "v_accvgpr_read_b32 v16, a68\n\t" \
  "v_accvgpr_read_b32 v17, a69\n\t" \
  "s_waitcnt lgkmcnt(10)\n\t" \
  "v_mfma_f32_16x16x32_bf16 a[112:115], a[184:187], v[8:11], a[112:115]\n\t" \
  "v_accvgpr_read_b32 v18, a70\n\t" \
  "v_accvgpr_read_b32 v19, a71\n\t" \
  "s_waitcnt lgkmcnt(8)\n\t" \
  "v_mfma_f32_16x16x32_bf16 a[112:115], a[188:191], v[12:15], a[112:115]\n\t" \
  "v_accvgpr_read_b32 v20, a84\n\t" \
  "v_accvgpr_read_b32 v21, a85\n\t" \
  "s_waitcnt lgkmcnt(0)\n\t" \
  "v_mfma_f32_16x16x32_bf16 a[80:83], v[108:111], v[12:15], 0\n\t" \
  "v_accvgpr_read_b32 v22, a86\n\t" \
  "v_accvgpr_read_b32 v23, a87\n\t" \
  "v_mfma_f32_16x16x32_bf16 a[80:83], v[104:107], v[8:11], a[80:83]\n\t" \
  "v_mul_f32 v16, v16, v210\n\t" \
  "v_mul_f32 v17, v17, v210\n\t" \
  "v_mfma_f32_16x16x32_bf16 a[80:83], v[100:103], v[4:7], a[80:83]\n\t" \
  "v_mul_f32 v18, v18, v210\n\t" \
  "v_mul_f32 v19, v19, v210\n\t" \
  "v_mfma_f32_16x16x32_bf16 a[80:83], v[96:99], v[0:3], a[80:83]\n\t" \
  "v_mul_f32 v20, v20, v210\n\t" \
  "v_mul_f32 v21, v21, v210\n\t" \
  "v_mul_f32 v22, v22, v210\n\t" \
  "v_mul_f32 v23, v23, v210\n\t" \
  "v_cvt_pk_bf16_f32 v40, v16, v17\n\t" \
  "v_cvt_pk_bf16_f32 v41, v18, v19\n\t" \
  "v_cvt_pk_bf16_f32 v42, v20, v21\n\t" \
  "v_cvt_pk_bf16_f32 v43, v22, v23\n\t" \
  "v_accvgpr_read_b32 v24, a100\n\t" \
  "v_accvgpr_read_b32 v25, a101\n\t" \
  "v_accvgpr_read_b32 v26, a102\n\t" \
  "v_accvgpr_read_b32 v27, a103\n\t" \
  "v_accvgpr_read_b32 v28, a116\n\t" \
  "v_accvgpr_read_b32 v29, a117\n\t" \
  "v_accvgpr_read_b32 v30, a118\n\t" \
  "v_accvgpr_read_b32 v31, a119\n\t" \
  "v_mul_f32 v24, v24, v210\n\t" \
  "v_mul_f32 v25, v25, v210\n\t" \
  "v_mul_f32 v26, v26, v210\n\t" \
  "v_mul_f32 v27, v27, v210\n\t" \
  "v_mul_f32 v28, v28, v210\n\t" \
  "v_mul_f32 v29, v29, v210\n\t" \
  "v_mul_f32 v30, v30, v210\n\t" \
  "v_mul_f32 v31, v31, v210\n\t" \
  "v_cvt_pk_bf16_f32 v44, v24, v25\n\t" \
  "v_cvt_pk_bf16_f32 v45, v26, v27\n\t" \
  "v_cvt_pk_bf16_f32 v46, v28, v29\n\t" \
  "v_cvt_pk_bf16_f32 v47, v30, v31\n\t" \
  "v_mfma_f32_16x16x32_bf16 a[36:39], v[48:51], v[32:35], 0\n\t" \
  "s_xor_b32 s98, %[eso], 32768\n\t" \
  "buffer_store_dwordx4 v[40:43], v242, %[ws], s98 offen offset:2048 sc1\n\t" \
  "v_mfma_f32_16x16x32_bf16 a[36:39], v[52:55], v[36:39], a[36:39]\n\t" \
  "s_xor_b32 s98, %[eso], 32768\n\t" \
  "buffer_store_dwordx4 v[44:47], v242, %[ws], s98 offen offset:3072 sc1\n\t" \
  "v_mfma_f32_16x16x32_bf16 a[36:39], v[56:59], v[40:43], a[36:39]\n\t" \
  "v_rcp_f32 v212, v168\n\t" \
  "v_mfma_f32_16x16x32_bf16 a[36:39], v[60:63], v[44:47], a[36:39]\n\t" \
  "v_accvgpr_read_b32 v16, a8\n\t" \
  "v_accvgpr_read_b32 v17, a9\n\t" \
  "s_waitcnt vmcnt(0)\n\t" \
  "buffer_store_dword v243, v254, %[ws], %[fw2] offen offset:0 sc1\n\t" \
  "v_mfma_f32_16x16x32_bf16 a[52:55], v[92:95], v[44:47], 0\n\t" \
  "v_accvgpr_read_b32 v18, a10\n\t" \
  "v_accvgpr_read_b32 v19, a11\n\t" \
  "v_mfma_f32_16x16x32_bf16 a[52:55], v[88:91], v[40:43], a[52:55]\n\t" \
  "v_accvgpr_read_b32 v20, a24\n\t" \
  "v_accvgpr_read_b32 v21, a25\n\t" \
  "v_mfma_f32_16x16x32_bf16 a[52:55], v[84:87], v[36:39], a[52:55]\n\t" \
  "v_accvgpr_read_b32 v22, a26\n\t" \
  "v_accvgpr_read_b32 v23, a27\n\t" \
  "v_mfma_f32_16x16x32_bf16 a[52:55], v[80:83], v[32:35], a[52:55]\n\t" \
  "v_mul_f32 v16, v16, v212\n\t" \
  "v_mul_f32 v17, v17, v212\n\t" \
  "ds_read_b128 a[224:227], v213 offset:32768\n\t" \
  "ds_read_b128 a[228:231], v213 offset:33792\n\t" \
  "ds_read_b128 a[232:235], v213 offset:34816\n\t" \
  "ds_read_b128 a[236:239], v213 offset:35840\n\t" \
  "ds_read_b128 a[240:243], v215 offset:0\n\t" \
  "ds_read_b128 a[244:247], v215 offset:1024\n\t" \
  "ds_read_b128 a[248:251], v215 offset:2048\n\t" \
  "ds_read_b128 a[252:255], v215 offset:3072\n\t" \
  "v_mfma_f32_16x16x32_bf16 a[68:71], v[112:115], v[32:35], 0\n\t" \
  "v_mul_f32 v18, v18, v212\n\t" \
  "v_mul_f32 v19, v19, v212\n\t" \
  "v_mfma_f32_16x16x32_bf16 a[68:71], v[116:119], v[36:39], a[68:71]\n\t" \
  "v_mul_f32 v20, v20, v212\n\t" \
  "v_mul_f32 v21, v21, v212\n\t" \
  "v_mfma_f32_16x16x32_bf16 a[68:71], v[120:123], v[40:43], a[68:71]\n\t" \
  "v_mul_f32 v22, v22, v212\n\t" \
  "v_mul_f32 v23, v23, v212\n\t" \
  "v_mfma_f32_16x16x32_bf16 a[68:71], v[124:127], v[44:47], a[68:71]\n\t" \
  "v_cvt_pk_bf16_f32 v64, v16, v17\n\t" \
  "v_cvt_pk_bf16_f32 v65, v18, v19\n\t" \
  "v_mfma_f32_16x16x32_bf16 a[4:7], a[140:143], v[44:47], 0\n\t" \
  "v_cvt_pk_bf16_f32 v66, v20, v21\n\t" \
  "v_cvt_pk_bf16_f32 v67, v22, v23\n\t" \
  "v_mfma_f32_16x16x32_bf16 a[4:7], a[136:139], v[40:43], a[4:7]\n\t" \
  "s_xor_b32 s98, %[eso], 65536\n\t" \
  "buffer_store_dwordx4 v[64:67], v242, %[ws], s98 offen offset:0 sc1\n\t" \
  "v_mfma_f32_16x16x32_bf16 a[4:7], a[132:135], v[36:39], a[4:7]\n\t" \
  "v_accvgpr_read_b32 v24, a40\n\t" \
  "v_accvgpr_read_b32 v25, a41\n\t" \
  "v_mfma_f32_16x16x32_bf16 a[4:7], a[128:131], v[32:35], a[4:7]\n\t" \
  "v_accvgpr_read_b32 v26, a42\n\t" \
  "v_accvgpr_read_b32 v27, a43\n\t" \
  "v_mfma_f32_16x16x32_bf16 a[20:23], a[144:147], v[32:35], 0\n\t" \
  "v_accvgpr_read_b32 v28, a56\n\t" \
  "v_accvgpr_read_b32 v29, a57\n\t" \
  "v_mfma_f32_16x16x32_bf16 a[20:23], a[148:151], v[36:39], a[20:23]\n\t" \
  "v_accvgpr_read_b32 v30, a58\n\t" \
  "v_accvgpr_read_b32 v31, a59\n\t" \
  "v_mfma_f32_16x16x32_bf16 a[20:23], a[152:155], v[40:43], a[20:23]\n\t" \
  "v_mul_f32 v24, v24, v212\n\t" \
  "v_mul_f32 v25, v25, v212\n\t" \
  "v_mfma_f32_16x16x32_bf16 a[20:23], a[156:159], v[44:47], a[20:23]\n\t" \
  "v_mul_f32 v26, v26, v212\n\t" \
  "v_mul_f32 v27, v27, v212\n\t" \
  "v_mfma_f32_16x16x32_bf16 a[100:103], a[172:175], v[44:47], 0\n\t" \
  "v_mul_f32 v28, v28, v212\n\t" \
  "v_mul_f32 v29, v29, v212\n\t" \
  "v_mfma_f32_16x16x32_bf16 a[100:103], a[168:171], v[40:43], a[100:103]\n\t" \
  "v_mul_f32 v30, v30, v212\n\t" \
  "v_mul_f32 v31, v31, v212\n\t" \
  "v_mfma_f32_16x16x32_bf16 a[100:103], a[164:167], v[36:39], a[100:103]\n\t" \
  "v_cvt_pk_bf16_f32 v68, v24, v25\n\t" \
  "v_cvt_pk_bf16_f32 v69, v26, v27\n\t" \
  "v_mfma_f32_16x16x32_bf16 a[100:103], a[160:163], v[32:35], a[100:103]\n\t" \
  "v_cvt_pk_bf16_f32 v70, v28, v29\n\t" \
  "v_cvt_pk_bf16_f32 v71, v30, v31\n\t" \
  "v_mfma_f32_16x16x32_bf16 a[116:119], a[176:179], v[32:35], 0\n\t" \
  "s_xor_b32 s98, %[eso], 65536\n\t" \
  "buffer_store_dwordx4 v[68:71], v242, %[ws], s98 offen offset:1024 sc1\n\t" \
  "v_mfma_f32_16x16x32_bf16 a[116:119], a[180:183], v[36:39], a[116:119]\n\t" \
  "v_accvgpr_read_b32 v16, a72\n\t" \
  "v_accvgpr_read_b32 v17, a73\n\t" \
  "v_mfma_f32_16x16x32_bf16 a[116:119], a[184:187], v[40:43], a[116:119]\n\t" \
  "v_accvgpr_read_b32 v18, a74\n\t" \
  "v_accvgpr_read_b32 v19, a75\n\t" \
  "v_mfma_f32_16x16x32_bf16 a[116:119], a[188:191], v[44:47], a[116:119]\n\t" \
  "v_accvgpr_read_b32 v20, a88\n\t" \
  "v_accvgpr_read_b32 v21, a89\n\t" \
  "v_mfma_f32_16x16x32_bf16 a[84:87], v[108:111], v[44:47], 0\n\t" \
  "v_accvgpr_read_b32 v22, a90\n\t" \
  "v_accvgpr_read_b32 v23, a91\n\t" \
  "v_mfma_f32_16x16x32_bf16 a[84:87], v[104:107], v[40:43], a[84:87]\n\t" \
  "v_mul_f32 v16, v16, v212\n\t" \
  "v_mul_f32 v17, v17, v212\n\t" \
  "v_mfma_f32_16x16x32_bf16 a[84:87], v[100:103], v[36:39], a[84:87]\n\t" \
  "v_mul_f32 v18, v18, v212\n\t" \
  "v_mul_f32 v19, v19, v212\n\t" \
  "v_mfma_f32_16x16x32_bf16 a[84:87], v[96:99], v[32:35], a[84:87]\n\t" \
  "v_mul_f32 v20, v20, v212\n\t" \
  "v_mul_f32 v21, v21, v212\n\t" \
  "v_mul_f32 v22, v22, v212\n\t" \
  "v_mul_f32 v23, v23, v212\n\t" \
  "v_cvt_pk_bf16_f32 v72, v16, v17\n\t" \
  "v_cvt_pk_bf16_f32 v73, v18, v19\n\t" \
  "v_cvt_pk_bf16_f32 v74, v20, v21\n\t" \
  "v_cvt_pk_bf16_f32 v75, v22, v23\n\t" \
  "v_accvgpr_read_b32 v24, a104\n\t" \
  "v_accvgpr_read_b32 v25, a105\n\t" \
  "v_accvgpr_read_b32 v26, a106\n\t" \
  "v_accvgpr_read_b32 v27, a107\n\t" \
  "v_accvgpr_read_b32 v28, a120\n\t" \
  "v_accvgpr_read_b32 v29, a121\n\t" \
  "v_accvgpr_read_b32 v30, a122\n\t" \
  "v_accvgpr_read_b32 v31, a123\n\t" \
  "v_mul_f32 v24, v24, v212\n\t" \
  "v_mul_f32 v25, v25, v212\n\t" \
  "v_mul_f32 v26, v26, v212\n\t" \
  "v_mul_f32 v27, v27, v212\n\t" \
  "v_mul_f32 v28, v28, v212\n\t" \
  "v_mul_f32 v29, v29, v212\n\t" \
  "v_mul_f32 v30, v30, v212\n\t" \
  "v_mul_f32 v31, v31, v212\n\t" \
  "v_cvt_pk_bf16_f32 v76, v24, v25\n\t" \
  "v_cvt_pk_bf16_f32 v77, v26, v27\n\t" \
  "v_cvt_pk_bf16_f32 v78, v28, v29\n\t" \
  "v_cvt_pk_bf16_f32 v79, v30, v31\n\t" \
  "v_mfma_f32_16x16x32_bf16 a[40:43], v[48:51], v[64:67], 0\n\t" \
  "s_xor_b32 s98, %[eso], 65536\n\t" \
  "buffer_store_dwordx4 v[72:75], v242, %[ws], s98 offen offset:2048 sc1\n\t" \
  "v_mfma_f32_16x16x32_bf16 a[40:43], v[52:55], v[68:71], a[40:43]\n\t" \
  "s_xor_b32 s98, %[eso], 65536\n\t" \
  "buffer_store_dwordx4 v[76:79], v242, %[ws], s98 offen offset:3072 sc1\n\t" \
  "v_mfma_f32_16x16x32_bf16 a[40:43], v[56:59], v[72:75], a[40:43]\n\t" \
  "buffer_load_dwordx4 v[224:227], v252, %[ws], %[ss] offen sc1\n\t" \
  "buffer_load_dwordx4 v[128:131], v247, %[sM], %[gvo] offen\n\t" \
  "v_mfma_f32_16x16x32_bf16 a[40:43], v[60:63], v[76:79], a[40:43]\n\t" \
  "v_rcp_f32 v214, v172\n\t" \
  "v_mfma_f32_16x16x32_bf16 a[56:59], v[92:95], v[76:79], 0\n\t" \
  "v_accvgpr_read_b32 v16, a12\n\t" \
  "v_accvgpr_read_b32 v17, a13\n\t" \
  "v_mfma_f32_16x16x32_bf16 a[56:59], v[88:91], v[72:75], a[56:59]\n\t" \
  "v_accvgpr_read_b32 v18, a14\n\t" \
  "v_accvgpr_read_b32 v19, a15\n\t" \
  "v_mfma_f32_16x16x32_bf16 a[56:59], v[84:87], v[68:71], a[56:59]\n\t" \
  "v_accvgpr_read_b32 v20, a28\n\t" \
  "v_accvgpr_read_b32 v21, a29\n\t" \
  "v_mfma_f32_16x16x32_bf16 a[56:59], v[80:83], v[64:67], a[56:59]\n\t" \
  "v_accvgpr_read_b32 v22, a30\n\t" \
  "v_accvgpr_read_b32 v23, a31\n\t" \
  "v_mfma_f32_16x16x32_bf16 a[72:75], v[112:115], v[64:67], 0\n\t" \
  "v_mul_f32 v16, v16, v214\n\t" \
  "v_mul_f32 v17, v17, v214\n\t" \
  "v_mul_f32 v18, v18, v214\n\t" \
  "v_mul_f32 v19, v19, v214\n\t" \
  "v_mul_f32 v20, v20, v214\n\t" \
  "v_mul_f32 v21, v21, v214\n\t" \
  "v_mul_f32 v22, v22, v214\n\t" \
  "v_mul_f32 v23, v23, v214\n\t" \
  "v_cvt_pk_bf16_f32 v160, v16, v17\n\t" \
  "v_cvt_pk_bf16_f32 v161, v18, v19\n\t" \
  "v_cvt_pk_bf16_f32 v162, v20, v21\n\t" \
  "v_cvt_pk_bf16_f32 v163, v22, v23\n\t" \
  "s_waitcnt lgkmcnt(4)\n\t" \
  "ds_write_b128 v213, v[160:163] offset:32768\n\t" \
  "v_accvgpr_read_b32 v24, a44\n\t" \
  "v_accvgpr_read_b32 v25, a45\n\t" \
  "v_accvgpr_read_b32 v26, a46\n\t" \
  "v_accvgpr_read_b32 v27, a47\n\t" \
  "v_accvgpr_read_b32 v28, a60\n\t" \
  "v_accvgpr_read_b32 v29, a61\n\t" \
  "v_accvgpr_read_b32 v30, a62\n\t" \
  "v_accvgpr_read_b32 v31, a63\n\t" \
  "v_mul_f32 v24, v24, v214\n\t" \
  "v_mul_f32 v25, v25, v214\n\t" \
  "v_mul_f32 v26, v26, v214\n\t" \
  "v_mul_f32 v27, v27, v214\n\t" \
  "v_mul_f32 v28, v28, v214\n\t" \
  "v_mul_f32 v29, v29, v214\n\t" \
  "v_mul_f32 v30, v30, v214\n\t" \
  "v_mul_f32 v31, v31, v214\n\t" \
  "v_cvt_pk_bf16_f32 v164, v24, v25\n\t" \
  "v_cvt_pk_bf16_f32 v165, v26, v27\n\t" \
  "v_cvt_pk_bf16_f32 v166, v28, v29\n\t" \
  "v_cvt_pk_bf16_f32 v167, v30, v31\n\t" \
  "ds_write_b128 v213, v[164:167] offset:33792\n\t" \
  "v_accvgpr_read_b32 v16, a76\n\t" \
  "v_accvgpr_read_b32 v17, a77\n\t" \
  "v_accvgpr_read_b32 v18, a78\n\t" \
  "v_accvgpr_read_b32 v19, a79\n\t" \
  "v_accvgpr_read_b32 v20, a92\n\t" \
  "v_accvgpr_read_b32 v21, a93\n\t" \
  "v_accvgpr_read_b32 v22, a94\n\t" \
  "v_accvgpr_read_b32 v23, a95\n\t" \
  "v_mul_f32 v16, v16, v214\n\t" \
  "v_mul_f32 v17, v17, v214\n\t" \
  "v_mul_f32 v18, v18, v214\n\t" \
  "v_mul_f32 v19, v19, v214\n\t" \
  "v_mul_f32 v20, v20, v214\n\t" \
  "v_mul_f32 v21, v21, v214\n\t" \
  "v_mul_f32 v22, v22, v214\n\t" \
  "v_mul_f32 v23, v23, v214\n\t" \
  "v_cvt_pk_bf16_f32 v168, v16, v17\n\t" \
  "v_cvt_pk_bf16_f32 v169, v18, v19\n\t" \
  "v_cvt_pk_bf16_f32 v170, v20, v21\n\t" \
  "v_cvt_pk_bf16_f32 v171, v22, v23\n\t" \
  "ds_write_b128 v213, v[168:171] offset:34816\n\t" \
  "v_accvgpr_read_b32 v24, a108\n\t" \
  "v_accvgpr_read_b32 v25, a109\n\t" \
  "v_accvgpr_read_b32 v26, a110\n\t" \
  "v_accvgpr_read_b32 v27, a111\n\t" \
  "v_accvgpr_read_b32 v28, a124\n\t" \
  "v_accvgpr_read_b32 v29, a125\n\t" \
  "v_accvgpr_read_b32 v30, a126\n\t" \
  "v_accvgpr_read_b32 v31, a127\n\t" \
  "v_mul_f32 v24, v24, v214\n\t" \
  "v_mul_f32 v25, v25, v214\n\t" \
  "v_mul_f32 v26, v26, v214\n\t" \
  "v_mul_f32 v27, v27, v214\n\t" \
  "v_mul_f32 v28, v28, v214\n\t" \
  "v_mul_f32 v29, v29, v214\n\t" \
  "v_mul_f32 v30, v30, v214\n\t" \
  "v_mul_f32 v31, v31, v214\n\t" \
  "v_cvt_pk_bf16_f32 v172, v24, v25\n\t" \
  "v_cvt_pk_bf16_f32 v173, v26, v27\n\t" \
  "v_cvt_pk_bf16_f32 v174, v28, v29\n\t" \
  "v_cvt_pk_bf16_f32 v175, v30, v31\n\t" \
  "ds_write_b128 v213, v[172:175] offset:35840\n\t" \
  "v_mfma_f32_16x16x32_bf16 a[72:75], v[116:119], v[68:71], a[72:75]\n\t" \
  "s_add_u32 s98, %[gvo], 12288\n\t" \
  "buffer_load_dwordx4 v[132:135], v247, %[sM], s98 offen\n\t" \
  "v_mfma_f32_16x16x32_bf16 a[72:75], v[120:123], v[72:75], a[72:75]\n\t" \
  "s_add_u32 s98, %[gvo], 24576\n\t" \
  "buffer_load_dwordx4 v[136:139], v247, %[sM], s98 offen\n\t" \
  "v_mfma_f32_16x16x32_bf16 a[72:75], v[124:127], v[76:79], a[72:75]\n\t" \
  "s_add_u32 s98, %[gvo], 36864\n\t" \
  "buffer_load_dwordx4 v[140:143], v247, %[sM], s98 offen\n\t" \
  "s_add_u32 m0, %[m], 147456\n\t" \
  "s_xor_b32 s97, %[wob], 655360\n\t" \
  "buffer_load_dwordx4 v240, %[wo], s97 offen offset:0 lds\n\t" \
  "s_add_u32 s97, s97, 7168\n\t" \
  "buffer_load_dwordx4 v241, %[wo], s97 offen offset:1024 lds\n\t" \
  "s_add_u32 s97, s97, 7168\n\t" \
  "buffer_load_dwordx4 v240, %[wo], s97 offen offset:2048 lds\n\t" \
  "s_add_u32 s97, s97, 7168\n\t" \
  "buffer_load_dwordx4 v241, %[wo], s97 offen offset:3072 lds\n\t" \
  "s_add_u32 s98, %[gvo], 49152\n\t" \
  "buffer_load_dwordx4 v[144:147], v247, %[sM], s98 offen\n\t" \
  "s_add_u32 s98, %[gvo], 61440\n\t" \
  "buffer_load_dwordx4 v[148:151], v247, %[sM], s98 offen\n\t" \
  "s_add_u32 s98, %[gvo], 73728\n\t" \
  "buffer_load_dwordx4 v[152:155], v247, %[sM], s98 offen\n\t" \
  "s_add_u32 s98, %[gvo], 86016\n\t" \
  "buffer_load_dwordx4 v[156:159], v247, %[sM], s98 offen\n\t" \
  "s_waitcnt vmcnt(0)\n\t" \
  "s_branch .Lat_end\n\t" \
  ".Lsf_c00:\n\t" \
  "s_mov_b32 vcc_lo, 0\n\t" \
  "s_branch .Lsf_s0\n\t" \
  ".Lsf_c10:\n\t" \
  "s_mov_b32 vcc_lo, 1\n\t" \
  "s_branch .Lsf_s0\n\t" \
  ".Lsf_c20:\n\t" \
  "s_mov_b32 vcc_lo, 2\n\t" \
  "s_branch .Lsf_s0\n\t" \
  ".Lsf_c30:\n\t" \
  "s_mov_b32 vcc_lo, 3\n\t" \
  "s_branch .Lsf_s0\n\t" \
  ".Lsf_c40:\n\t" \
  "s_mov_b32 vcc_lo, 4\n\t" \
  "s_branch .Lsf_s0\n\t" \
  ".Lsf_c50:\n\t" \
  "s_mov_b32 vcc_lo, 5\n\t" \
  "s_branch .Lsf_s0\n\t" \
  ".Lsf_c60:\n\t" \
  "s_mov_b32 vcc_lo, 6\n\t" \
  "s_branch .Lsf_s0\n\t" \
  ".Lsf_c70:\n\t" \
  "s_mov_b32 vcc_lo, 7\n\t" \
  "s_branch .Lsf_s0\n\t" \
  ".Lsf_s0:\n\t" \
  "s_nop 7\n\t" \
  "s_nop 1\n\t" \
  "v_mov_b32 v193, v192\n\t" \
  "s_nop 1\n\t" \
  "v_permlane32_swap_b32 v192, v193\n\t" \
  "s_nop 1\n\t" \
  "v_max_f32 v192, v192, v193\n\t" \
  "s_nop 1\n\t" \
  "v_mov_b32 v193, v192\n\t" \
  "s_nop 1\n\t" \
  "v_permlane16_swap_b32 v192, v193\n\t" \
  "s_nop 1\n\t" \
  "v_max_f32 v192, v192, v193\n\t" \
  "v_max_f32 v192, 0, v192\n\t" \
  "v_sub_f32 v0, v0, v192\n\t" \
  "v_sub_f32 v1, v1, v192\n\t" \
  "v_sub_f32 v2, v2, v192\n\t" \
  "v_sub_f32 v3, v3, v192\n\t" \
  "v_sub_f32 v4, v4, v192\n\t" \
  "v_sub_f32 v5, v5, v192\n\t" \
  "v_sub_f32 v6, v6, v192\n\t" \
  "v_sub_f32 v7, v7, v192\n\t" \
  "v_sub_f32 v8, v8, v192\n\t" \
  "v_sub_f32 v9, v9, v192\n\t" \
  "v_sub_f32 v10, v10, v192\n\t" \
  "v_sub_f32 v11, v11, v192\n\t" \
  "v_sub_f32 v12, v12, v192\n\t" \
  "v_sub_f32 v13, v13, v192\n\t" \
  "v_sub_f32 v14, v14, v192\n\t" \
  "v_sub_f32 v15, v15, v192\n\t" \
  "v_sub_f32 v176, v176, v192\n\t" \
  "v_sub_f32 v177, v177, v192\n\t" \
  "v_sub_f32 v178, v178, v192\n\t" \
  "v_sub_f32 v179, v179, v192\n\t" \
  "v_exp_f32 v196, -v192\n\t" \
  "s_mov_b32 s99, 1\n\t" \
  "s_nop 1\n\t" \
  "s_cselect_b32 vcc_hi, 1, 0\n\t" \
  "s_cmp_eq_u32 vcc_lo, 0\n\t" \
  "s_cbranch_scc1 .Lsf_b00\n\t" \
  "s_cmp_eq_u32 vcc_lo, 1\n\t" \
  "s_cbranch_scc1 .Lsf_b10\n\t" \
  "s_cmp_eq_u32 vcc_lo, 2\n\t" \
  "s_cbranch_scc1 .Lsf_b20\n\t" \
  "s_cmp_eq_u32 vcc_lo, 3\n\t" \
  "s_cbranch_scc1 .Lsf_b30\n\t" \
  "s_cmp_eq_u32 vcc_lo, 4\n\t" \
  "s_cbranch_scc1 .Lsf_b40\n\t" \
  "s_cmp_eq_u32 vcc_lo, 5\n\t" \
  "s_cbranch_scc1 .Lsf_b50\n\t" \
  "s_cmp_eq_u32 vcc_lo, 6\n\t" \
  "s_cbranch_scc1 .Lsf_b60\n\t" \
  "s_cmp_eq_u32 vcc_lo, 7\n\t" \
  "s_cbranch_scc1 .Lsf_b70\n\t" \
  ".Lsf_b00:\n\t" \
  "s_cmp_lg_u32 vcc_hi, 0\n\t" \
  "s_branch .Lsf_r00\n\t" \
  ".Lsf_b10:\n\t" \
  "s_cmp_lg_u32 vcc_hi, 0\n\t" \
  "s_branch .Lsf_r10\n\t" \
  ".Lsf_b20:\n\t" \
  "s_cmp_lg_u32 vcc_hi, 0\n\t" \
  "s_branch .Lsf_r20\n\t" \
  ".Lsf_b30:\n\t" \
  "s_cmp_lg_u32 vcc_hi, 0\n\t" \
  "s_branch .Lsf_r30\n\t" \
  ".Lsf_b40:\n\t" \
  "s_cmp_lg_u32 vcc_hi, 0\n\t" \
  "s_branch .Lsf_r40\n\t" \
  ".Lsf_b50:\n\t" \
  "s_cmp_lg_u32 vcc_hi, 0\n\t" \
  "s_branch .Lsf_r50\n\t" \
  ".Lsf_b60:\n\t" \
  "s_cmp_lg_u32 vcc_hi, 0\n\t" \
  "s_branch .Lsf_r60\n\t" \
  ".Lsf_b70:\n\t" \
  "s_cmp_lg_u32 vcc_hi, 0\n\t" \
  "s_branch .Lsf_r70\n\t" \
  ".Lsf_c01:\n\t" \
  "s_mov_b32 vcc_lo, 0\n\t" \
  "s_branch .Lsf_s1\n\t" \
  ".Lsf_c11:\n\t" \
  "s_mov_b32 vcc_lo, 1\n\t" \
  "s_branch .Lsf_s1\n\t" \
  ".Lsf_c21:\n\t" \
  "s_mov_b32 vcc_lo, 2\n\t" \
  "s_branch .Lsf_s1\n\t" \
  ".Lsf_c31:\n\t" \
  "s_mov_b32 vcc_lo, 3\n\t" \
  "s_branch .Lsf_s1\n\t" \
  ".Lsf_c41:\n\t" \
  "s_mov_b32 vcc_lo, 4\n\t" \
  "s_branch .Lsf_s1\n\t" \
  ".Lsf_c51:\n\t" \
  "s_mov_b32 vcc_lo, 5\n\t" \
  "s_branch .Lsf_s1\n\t" \
  ".Lsf_c61:\n\t" \
  "s_mov_b32 vcc_lo, 6\n\t" \
  "s_branch .Lsf_s1\n\t" \
  ".Lsf_c71:\n\t" \
  "s_mov_b32 vcc_lo, 7\n\t" \
  "s_branch .Lsf_s1\n\t" \
  ".Lsf_s1:\n\t" \
  "s_nop 7\n\t" \
  "s_nop 1\n\t" \
  "v_mov_b32 v193, v192\n\t" \
  "s_nop 1\n\t" \
  "v_permlane32_swap_b32 v192, v193\n\t" \
  "s_nop 1\n\t" \
  "v_max_f32 v192, v192, v193\n\t" \
  "s_nop 1\n\t" \
  "v_mov_b32 v193, v192\n\t" \
  "s_nop 1\n\t" \
  "v_permlane16_swap_b32 v192, v193\n\t" \
  "s_nop 1\n\t" \
  "v_max_f32 v192, v192, v193\n\t" \
  "v_max_f32 v192, 0, v192\n\t" \
  "v_sub_f32 v32, v32, v192\n\t" \
  "v_sub_f32 v33, v33, v192\n\t" \
  "v_sub_f32 v34, v34, v192\n\t" \
  "v_sub_f32 v35, v35, v192\n\t" \
  "v_sub_f32 v36, v36, v192\n\t" \
  "v_sub_f32 v37, v37, v192\n\t" \
  "v_sub_f32 v38, v38, v192\n\t" \
  "v_sub_f32 v39, v39, v192\n\t" \
  "v_sub_f32 v40, v40, v192\n\t" \
  "v_sub_f32 v41, v41, v192\n\t" \
  "v_sub_f32 v42, v42, v192\n\t" \
  "v_sub_f32 v43, v43, v192\n\t" \
  "v_sub_f32 v44, v44, v192\n\t" \
  "v_sub_f32 v45, v45, v192\n\t" \
  "v_sub_f32 v46, v46, v192\n\t" \
  "v_sub_f32 v47, v47, v192\n\t" \
  "v_sub_f32 v180, v180, v192\n\t" \
  "v_sub_f32 v181, v181, v192\n\t" \
  "v_sub_f32 v182, v182, v192\n\t" \
  "v_sub_f32 v183, v183, v192\n\t" \
  "v_exp_f32 v197, -v192\n\t" \
  "s_mov_b32 s99, 1\n\t" \
  "s_nop 1\n\t" \
  "s_cselect_b32 vcc_hi, 1, 0\n\t" \
  "s_cmp_eq_u32 vcc_lo, 0\n\t" \
  "s_cbranch_scc1 .Lsf_b01\n\t" \
  "s_cmp_eq_u32 vcc_lo, 1\n\t" \
  "s_cbranch_scc1 .Lsf_b11\n\t" \
  "s_cmp_eq_u32 vcc_lo, 2\n\t" \
  "s_cbranch_scc1 .Lsf_b21\n\t" \
  "s_cmp_eq_u32 vcc_lo, 3\n\t" \
  "s_cbranch_scc1 .Lsf_b31\n\t" \
  "s_cmp_eq_u32 vcc_lo, 4\n\t" \
  "s_cbranch_scc1 .Lsf_b41\n\t" \
  "s_cmp_eq_u32 vcc_lo, 5\n\t" \
  "s_cbranch_scc1 .Lsf_b51\n\t" \
  "s_cmp_eq_u32 vcc_lo, 6\n\t" \
  "s_cbranch_scc1 .Lsf_b61\n\t" \
  "s_cmp_eq_u32 vcc_lo, 7\n\t" \
  "s_cbranch_scc1 .Lsf_b71\n\t" \
  ".Lsf_b01:\n\t" \
  "s_cmp_lg_u32 vcc_hi, 0\n\t" \
  "s_branch .Lsf_r01\n\t" \
  ".Lsf_b11:\n\t" \
  "s_cmp_lg_u32 vcc_hi, 0\n\t" \
  "s_branch .Lsf_r11\n\t" \
  ".Lsf_b21:\n\t" \
  "s_cmp_lg_u32 vcc_hi, 0\n\t" \
  "s_branch .Lsf_r21\n\t" \
  ".Lsf_b31:\n\t" \
  "s_cmp_lg_u32 vcc_hi, 0\n\t" \
  "s_branch .Lsf_r31\n\t" \
  ".Lsf_b41:\n\t" \
  "s_cmp_lg_u32 vcc_hi, 0\n\t" \
  "s_branch .Lsf_r41\n\t" \
  ".Lsf_b51:\n\t" \
  "s_cmp_lg_u32 vcc_hi, 0\n\t" \
  "s_branch .Lsf_r51\n\t" \
  ".Lsf_b61:\n\t" \
  "s_cmp_lg_u32 vcc_hi, 0\n\t" \
  "s_branch .Lsf_r61\n\t" \
  ".Lsf_b71:\n\t" \
  "s_cmp_lg_u32 vcc_hi, 0\n\t" \
  "s_branch .Lsf_r71\n\t" \
  ".Lsf_c02:\n\t" \
  "s_mov_b32 vcc_lo, 0\n\t" \
  "s_branch .Lsf_s2\n\t" \
  ".Lsf_c12:\n\t" \
  "s_mov_b32 vcc_lo, 1\n\t" \
  "s_branch .Lsf_s2\n\t" \
  ".Lsf_c22:\n\t" \
  "s_mov_b32 vcc_lo, 2\n\t" \
  "s_branch .Lsf_s2\n\t" \
  ".Lsf_c32:\n\t" \
  "s_mov_b32 vcc_lo, 3\n\t" \
  "s_branch .Lsf_s2\n\t" \
  ".Lsf_c42:\n\t" \
  "s_mov_b32 vcc_lo, 4\n\t" \
  "s_branch .Lsf_s2\n\t" \
  ".Lsf_c52:\n\t" \
  "s_mov_b32 vcc_lo, 5\n\t" \
  "s_branch .Lsf_s2\n\t" \
  ".Lsf_c62:\n\t" \
  "s_mov_b32 vcc_lo, 6\n\t" \
  "s_branch .Lsf_s2\n\t" \
  ".Lsf_c72:\n\t" \
  "s_mov_b32 vcc_lo, 7\n\t" \
  "s_branch .Lsf_s2\n\t" \
  ".Lsf_s2:\n\t" \
  "s_nop 7\n\t" \
  "s_nop 1\n\t" \
  "v_mov_b32 v193, v192\n\t" \
  "s_nop 1\n\t" \
  "v_permlane32_swap_b32 v192, v193\n\t" \
  "s_nop 1\n\t" \
  "v_max_f32 v192, v192, v193\n\t" \
  "s_nop 1\n\t" \
  "v_mov_b32 v193, v192\n\t" \
  "s_nop 1\n\t" \
  "v_permlane16_swap_b32 v192, v193\n\t" \
  "s_nop 1\n\t" \
  "v_max_f32 v192, v192, v193\n\t" \
  "v_max_f32 v192, 0, v192\n\t" \
  "v_sub_f32 v64, v64, v192\n\t" \
  "v_sub_f32 v65, v65, v192\n\t" \
  "v_sub_f32 v66, v66, v192\n\t" \
  "v_sub_f32 v67, v67, v192\n\t" \
  "v_sub_f32 v68, v68, v192\n\t" \
  "v_sub_f32 v69, v69, v192\n\t" \
  "v_sub_f32 v70, v70, v192\n\t" \
  "v_sub_f32 v71, v71, v192\n\t" \
  "v_sub_f32 v72, v72, v192\n\t" \
  "v_sub_f32 v73, v73, v192\n\t" \
  "v_sub_f32 v74, v74, v192\n\t" \
  "v_sub_f32 v75, v75, v192\n\t" \
  "v_sub_f32 v76, v76, v192\n\t" \
  "v_sub_f32 v77, v77, v192\n\t" \
  "v_sub_f32 v78, v78, v192\n\t" \
  "v_sub_f32 v79, v79, v192\n\t" \
  "v_sub_f32 v184, v184, v192\n\t" \
  "v_sub_f32 v185, v185, v192\n\t" \
  "v_sub_f32 v186, v186, v192\n\t" \
  "v_sub_f32 v187, v187, v192\n\t" \
  "v_exp_f32 v198, -v192\n\t" \
  "s_mov_b32 s99, 1\n\t" \
  "s_nop 1\n\t" \
  "s_cselect_b32 vcc_hi, 1, 0\n\t" \
  "s_cmp_eq_u32 vcc_lo, 0\n\t" \
  "s_cbranch_scc1 .Lsf_b02\n\t" \
  "s_cmp_eq_u32 vcc_lo, 1\n\t" \
  "s_cbranch_scc1 .Lsf_b12\n\t" \
  "s_cmp_eq_u32 vcc_lo, 2\n\t" \
  "s_cbranch_scc1 .Lsf_b22\n\t" \
  "s_cmp_eq_u32 vcc_lo, 3\n\t" \
  "s_cbranch_scc1 .Lsf_b32\n\t" \
  "s_cmp_eq_u32 vcc_lo, 4\n\t" \
  "s_cbranch_scc1 .Lsf_b42\n\t" \
  "s_cmp_eq_u32 vcc_lo, 5\n\t" \
  "s_cbranch_scc1 .Lsf_b52\n\t" \
  "s_cmp_eq_u32 vcc_lo, 6\n\t" \
  "s_cbranch_scc1 .Lsf_b62\n\t" \
  "s_cmp_eq_u32 vcc_lo, 7\n\t" \
  "s_cbranch_scc1 .Lsf_b72\n\t" \
  ".Lsf_b02:\n\t" \
  "s_cmp_lg_u32 vcc_hi, 0\n\t" \
  "s_branch .Lsf_r02\n\t" \
  ".Lsf_b12:\n\t" \
  "s_cmp_lg_u32 vcc_hi, 0\n\t" \
  "s_branch .Lsf_r12\n\t" \
  ".Lsf_b22:\n\t" \
  "s_cmp_lg_u32 vcc_hi, 0\n\t" \
  "s_branch .Lsf_r22\n\t" \
  ".Lsf_b32:\n\t" \
  "s_cmp_lg_u32 vcc_hi, 0\n\t" \
  "s_branch .Lsf_r32\n\t" \
  ".Lsf_b42:\n\t" \
  "s_cmp_lg_u32 vcc_hi, 0\n\t" \
  "s_branch .Lsf_r42\n\t" \
  ".Lsf_b52:\n\t" \
  "s_cmp_lg_u32 vcc_hi, 0\n\t" \
  "s_branch .Lsf_r52\n\t" \
  ".Lsf_b62:\n\t" \
  "s_cmp_lg_u32 vcc_hi, 0\n\t" \
  "s_branch .Lsf_r62\n\t" \
  ".Lsf_b72:\n\t" \
  "s_cmp_lg_u32 vcc_hi, 0\n\t" \
  "s_branch .Lsf_r72\n\t" \
  ".Lsf_c03:\n\t" \
  "s_mov_b32 vcc_lo, 0\n\t" \
  "s_branch .Lsf_s3\n\t" \
  ".Lsf_c13:\n\t" \
  "s_mov_b32 vcc_lo, 1\n\t" \
  "s_branch .Lsf_s3\n\t" \
  ".Lsf_c23:\n\t" \
  "s_mov_b32 vcc_lo, 2\n\t" \
  "s_branch .Lsf_s3\n\t" \
  ".Lsf_c33:\n\t" \
  "s_mov_b32 vcc_lo, 3\n\t" \
  "s_branch .Lsf_s3\n\t" \
  ".Lsf_c43:\n\t" \
  "s_mov_b32 vcc_lo, 4\n\t" \
  "s_branch .Lsf_s3\n\t" \
  ".Lsf_c53:\n\t" \
  "s_mov_b32 vcc_lo, 5\n\t" \
  "s_branch .Lsf_s3\n\t" \
  ".Lsf_c63:\n\t" \
  "s_mov_b32 vcc_lo, 6\n\t" \
  "s_branch .Lsf_s3\n\t" \
  ".Lsf_c73:\n\t" \
  "s_mov_b32 vcc_lo, 7\n\t" \
  "s_branch .Lsf_s3\n\t" \
  ".Lsf_s3:\n\t" \
  "s_nop 7\n\t" \
  "s_nop 1\n\t" \
  "v_mov_b32 v193, v192\n\t" \
  "s_nop 1\n\t" \
  "v_permlane32_swap_b32 v192, v193\n\t" \
  "s_nop 1\n\t" \
  "v_max_f32 v192, v192, v193\n\t" \
  "s_nop 1\n\t" \
  "v_mov_b32 v193, v192\n\t" \
  "s_nop 1\n\t" \
  "v_permlane16_swap_b32 v192, v193\n\t" \
  "s_nop 1\n\t" \
  "v_max_f32 v192, v192, v193\n\t" \
  "v_max_f32 v192, 0, v192\n\t" \
  "v_sub_f32 v96, v96, v192\n\t" \
  "v_sub_f32 v97, v97, v192\n\t" \
  "v_sub_f32 v98, v98, v192\n\t" \
  "v_sub_f32 v99, v99, v192\n\t" \
  "v_sub_f32 v100, v100, v192\n\t" \
  "v_sub_f32 v101, v101, v192\n\t" \
  "v_sub_f32 v102, v102, v192\n\t" \
  "v_sub_f32 v103, v103, v192\n\t" \
  "v_sub_f32 v104, v104, v192\n\t" \
  "v_sub_f32 v105, v105, v192\n\t" \
  "v_sub_f32 v106, v106, v192\n\t" \
  "v_sub_f32 v107, v107, v192\n\t" \
  "v_sub_f32 v108, v108, v192\n\t" \
  "v_sub_f32 v109, v109, v192\n\t" \
  "v_sub_f32 v110, v110, v192\n\t" \
  "v_sub_f32 v111, v111, v192\n\t" \
  "v_sub_f32 v188, v188, v192\n\t" \
  "v_sub_f32 v189, v189, v192\n\t" \
  "v_sub_f32 v190, v190, v192\n\t" \
  "v_sub_f32 v191, v191, v192\n\t" \
  "v_exp_f32 v199, -v192\n\t" \
  "s_mov_b32 s99, 1\n\t" \
  "s_nop 1\n\t" \
  "s_cselect_b32 vcc_hi, 1, 0\n\t" \
  "s_cmp_eq_u32 vcc_lo, 0\n\t" \
  "s_cbranch_scc1 .Lsf_b03\n\t" \
  "s_cmp_eq_u32 vcc_lo, 1\n\t" \
  "s_cbranch_scc1 .Lsf_b13\n\t" \
  "s_cmp_eq_u32 vcc_lo, 2\n\t" \
  "s_cbranch_scc1 .Lsf_b23\n\t" \
  "s_cmp_eq_u32 vcc_lo, 3\n\t" \
  "s_cbranch_scc1 .Lsf_b33\n\t" \
  "s_cmp_eq_u32 vcc_lo, 4\n\t" \
  "s_cbranch_scc1 .Lsf_b43\n\t" \
  "s_cmp_eq_u32 vcc_lo, 5\n\t" \
  "s_cbranch_scc1 .Lsf_b53\n\t" \
  "s_cmp_eq_u32 vcc_lo, 6\n\t" \
  "s_cbranch_scc1 .Lsf_b63\n\t" \
  "s_cmp_eq_u32 vcc_lo, 7\n\t" \
  "s_cbranch_scc1 .Lsf_b73\n\t" \
  ".Lsf_b03:\n\t" \
  "s_cmp_lg_u32 vcc_hi, 0\n\t" \
  "s_branch .Lsf_r03\n\t" \
  ".Lsf_b13:\n\t" \
  "s_cmp_lg_u32 vcc_hi, 0\n\t" \
  "s_branch .Lsf_r13\n\t" \
  ".Lsf_b23:\n\t" \
  "s_cmp_lg_u32 vcc_hi, 0\n\t" \
  "s_branch .Lsf_r23\n\t" \
  ".Lsf_b33:\n\t" \
  "s_cmp_lg_u32 vcc_hi, 0\n\t" \
  "s_branch .Lsf_r33\n\t" \
  ".Lsf_b43:\n\t" \
  "s_cmp_lg_u32 vcc_hi, 0\n\t" \
  "s_branch .Lsf_r43\n\t" \
  ".Lsf_b53:\n\t" \
  "s_cmp_lg_u32 vcc_hi, 0\n\t" \
  "s_branch .Lsf_r53\n\t" \
  ".Lsf_b63:\n\t" \
  "s_cmp_lg_u32 vcc_hi, 0\n\t" \
  "s_branch .Lsf_r63\n\t" \
  ".Lsf_b73:\n\t" \
  "s_cmp_lg_u32 vcc_hi, 0\n\t" \
  "s_branch .Lsf_r73\n\t" \
  ".Lsf_p0:\n\t" \
  "s_mov_b32 vcc_lo, 0\n\t" \
  "s_branch .Lsf_rs\n\t" \
  ".Lsf_p1:\n\t" \
  "s_mov_b32 vcc_lo, 1\n\t" \
  "s_branch .Lsf_rs\n\t" \
  ".Lsf_p2:\n\t" \
  "s_mov_b32 vcc_lo, 2\n\t" \
  "s_branch .Lsf_rs\n\t" \
  ".Lsf_p3:\n\t" \
  "s_mov_b32 vcc_lo, 3\n\t" \
  "s_branch .Lsf_rs\n\t" \
  ".Lsf_p4:\n\t" \
  "s_mov_b32 vcc_lo, 4\n\t" \
  "s_branch .Lsf_rs\n\t" \
  ".Lsf_p5:\n\t" \
  "s_mov_b32 vcc_lo, 5\n\t" \
  "s_branch .Lsf_rs\n\t" \
  ".Lsf_p6:\n\t" \
  "s_mov_b32 vcc_lo, 6\n\t" \
  "s_branch .Lsf_rs\n\t" \
  ".Lsf_p7:\n\t" \
  "s_mov_b32 vcc_lo, 7\n\t" \
  "s_branch .Lsf_rs\n\t" \
  ".Lsf_rs:\n\t" \
  "s_nop 7\n\t" \
  "s_nop 7\n\t" \
  "v_accvgpr_read_b32 v193, a0\n\t" \
  "v_mul_f32 v193, v196, v193\n\t" \
  "v_accvgpr_write_b32 a0, v193\n\t" \
  "v_accvgpr_read_b32 v193, a1\n\t" \
  "v_mul_f32 v193, v196, v193\n\t" \
  "v_accvgpr_write_b32 a1, v193\n\t" \
  "v_accvgpr_read_b32 v193, a2\n\t" \
  "v_mul_f32 v193, v196, v193\n\t" \
  "v_accvgpr_write_b32 a2, v193\n\t" \
  "v_accvgpr_read_b32 v193, a3\n\t" \
  "v_mul_f32 v193, v196, v193\n\t" \
  "v_accvgpr_write_b32 a3, v193\n\t" \
  "v_accvgpr_read_b32 v193, a4\n\t" \
  "v_mul_f32 v193, v197, v193\n\t" \
  "v_accvgpr_write_b32 a4, v193\n\t" \
  "v_accvgpr_read_b32 v193, a5\n\t" \
  "v_mul_f32 v193, v197, v193\n\t" \
  "v_accvgpr_write_b32 a5, v193\n\t" \
  "v_accvgpr_read_b32 v193, a6\n\t" \
  "v_mul_f32 v193, v197, v193\n\t" \
  "v_accvgpr_write_b32 a6, v193\n\t" \
  "v_accvgpr_read_b32 v193, a7\n\t" \
  "v_mul_f32 v193, v197, v193\n\t" \
  "v_accvgpr_write_b32 a7, v193\n\t" \
  "v_accvgpr_read_b32 v193, a8\n\t" \
  "v_mul_f32 v193, v198, v193\n\t" \
  "v_accvgpr_write_b32 a8, v193\n\t" \
  "v_accvgpr_read_b32 v193, a9\n\t" \
  "v_mul_f32 v193, v198, v193\n\t" \
  "v_accvgpr_write_b32 a9, v193\n\t" \
  "v_accvgpr_read_b32 v193, a10\n\t" \
  "v_mul_f32 v193, v198, v193\n\t" \
  "v_accvgpr_write_b32 a10, v193\n\t" \
  "v_accvgpr_read_b32 v193, a11\n\t" \
  "v_mul_f32 v193, v198, v193\n\t" \
  "v_accvgpr_write_b32 a11, v193\n\t" \
  "v_accvgpr_read_b32 v193, a12\n\t" \
  "v_mul_f32 v193, v199, v193\n\t" \
  "v_accvgpr_write_b32 a12, v193\n\t" \
  "v_accvgpr_read_b32 v193, a13\n\t" \
  "v_mul_f32 v193, v199, v193\n\t" \
  "v_accvgpr_write_b32 a13, v193\n\t" \
  "v_accvgpr_read_b32 v193, a14\n\t" \
  "v_mul_f32 v193, v199, v193\n\t" \
  "v_accvgpr_write_b32 a14, v193\n\t" \
  "v_accvgpr_read_b32 v193, a15\n\t" \
  "v_mul_f32 v193, v199, v193\n\t" \
  "v_accvgpr_write_b32 a15, v193\n\t" \
  "v_accvgpr_read_b32 v193, a16\n\t" \
  "v_mul_f32 v193, v196, v193\n\t" \
  "v_accvgpr_write_b32 a16, v193\n\t" \
  "v_accvgpr_read_b32 v193, a17\n\t" \
  "v_mul_f32 v193, v196, v193\n\t" \
  "v_accvgpr_write_b32 a17, v193\n\t" \
  "v_accvgpr_read_b32 v193, a18\n\t" \
  "v_mul_f32 v193, v196, v193\n\t" \
  "v_accvgpr_write_b32 a18, v193\n\t" \
  "v_accvgpr_read_b32 v193, a19\n\t" \
  "v_mul_f32 v193, v196, v193\n\t" \
  "v_accvgpr_write_b32 a19, v193\n\t" \
  "v_accvgpr_read_b32 v193, a20\n\t" \
  "v_mul_f32 v193, v197, v193\n\t" \
  "v_accvgpr_write_b32 a20, v193\n\t" \
  "v_accvgpr_read_b32 v193, a21\n\t" \
  "v_mul_f32 v193, v197, v193\n\t" \
  "v_accvgpr_write_b32 a21, v193\n\t" \
  "v_accvgpr_read_b32 v193, a22\n\t" \
  "v_mul_f32 v193, v197, v193\n\t" \
  "v_accvgpr_write_b32 a22, v193\n\t" \
  "v_accvgpr_read_b32 v193, a23\n\t" \
  "v_mul_f32 v193, v197, v193\n\t" \
  "v_accvgpr_write_b32 a23, v193\n\t" \
  "v_accvgpr_read_b32 v193, a24\n\t" \
  "v_mul_f32 v193, v198, v193\n\t" \
  "v_accvgpr_write_b32 a24, v193\n\t" \
  "v_accvgpr_read_b32 v193, a25\n\t" \
  "v_mul_f32 v193, v198, v193\n\t" \
  "v_accvgpr_write_b32 a25, v193\n\t" \
  "v_accvgpr_read_b32 v193, a26\n\t" \
  "v_mul_f32 v193, v198, v193\n\t" \
  "v_accvgpr_write_b32 a26, v193\n\t" \
  "v_accvgpr_read_b32 v193, a27\n\t" \
  "v_mul_f32 v193, v198, v193\n\t" \
  "v_accvgpr_write_b32 a27, v193\n\t" \
  "v_accvgpr_read_b32 v193, a28\n\t" \
  "v_mul_f32 v193, v199, v193\n\t" \
  "v_accvgpr_write_b32 a28, v193\n\t" \
  "v_accvgpr_read_b32 v193, a29\n\t" \
  "v_mul_f32 v193, v199, v193\n\t" \
  "v_accvgpr_write_b32 a29, v193\n\t" \
  "v_accvgpr_read_b32 v193, a30\n\t" \
  "v_mul_f32 v193, v199, v193\n\t" \
  "v_accvgpr_write_b32 a30, v193\n\t" \
  "v_accvgpr_read_b32 v193, a31\n\t" \
  "v_mul_f32 v193, v199, v193\n\t" \
  "v_accvgpr_write_b32 a31, v193\n\t" \
  "v_accvgpr_read_b32 v193, a32\n\t" \
  "v_mul_f32 v193, v196, v193\n\t" \
  "v_accvgpr_write_b32 a32, v193\n\t" \
  "v_accvgpr_read_b32 v193, a33\n\t" \
  "v_mul_f32 v193, v196, v193\n\t" \
  "v_accvgpr_write_b32 a33, v193\n\t" \
  "v_accvgpr_read_b32 v193, a34\n\t" \
  "v_mul_f32 v193, v196, v193\n\t" \
  "v_accvgpr_write_b32 a34, v193\n\t" \
  "v_accvgpr_read_b32 v193, a35\n\t" \
  "v_mul_f32 v193, v196, v193\n\t" \
  "v_accvgpr_write_b32 a35, v193\n\t" \
  "v_accvgpr_read_b32 v193, a36\n\t" \
  "v_mul_f32 v193, v197, v193\n\t" \
  "v_accvgpr_write_b32 a36, v193\n\t" \
  "v_accvgpr_read_b32 v193, a37\n\t" \
  "v_mul_f32 v193, v197, v193\n\t" \
  "v_accvgpr_write_b32 a37, v193\n\t" \
  "v_accvgpr_read_b32 v193, a38\n\t" \
  "v_mul_f32 v193, v197, v193\n\t" \
  "v_accvgpr_write_b32 a38, v193\n\t" \
  "v_accvgpr_read_b32 v193, a39\n\t" \
  "v_mul_f32 v193, v197, v193\n\t" \
  "v_accvgpr_write_b32 a39, v193\n\t" \
  "v_accvgpr_read_b32 v193, a40\n\t" \
  "v_mul_f32 v193, v198, v193\n\t" \
  "v_accvgpr_write_b32 a40, v193\n\t" \
  "v_accvgpr_read_b32 v193, a41\n\t" \
  "v_mul_f32 v193, v198, v193\n\t" \
  "v_accvgpr_write_b32 a41, v193\n\t" \
  "v_accvgpr_read_b32 v193, a42\n\t" \
  "v_mul_f32 v193, v198, v193\n\t" \
  "v_accvgpr_write_b32 a42, v193\n\t" \
  "v_accvgpr_read_b32 v193, a43\n\t" \
  "v_mul_f32 v193, v198, v193\n\t" \
  "v_accvgpr_write_b32 a43, v193\n\t" \
  "v_accvgpr_read_b32 v193, a44\n\t" \
  "v_mul_f32 v193, v199, v193\n\t" \
  "v_accvgpr_write_b32 a44, v193\n\t" \
  "v_accvgpr_read_b32 v193, a45\n\t" \
  "v_mul_f32 v193, v199, v193\n\t" \
  "v_accvgpr_write_b32 a45, v193\n\t" \
  "v_accvgpr_read_b32 v193, a46\n\t" \
  "v_mul_f32 v193, v199, v193\n\t" \
  "v_accvgpr_write_b32 a46, v193\n\t" \
  "v_accvgpr_read_b32 v193, a47\n\t" \
  "v_mul_f32 v193, v199, v193\n\t" \
  "v_accvgpr_write_b32 a47, v193\n\t" \
  "v_accvgpr_read_b32 v193, a48\n\t" \
  "v_mul_f32 v193, v196, v193\n\t" \
  "v_accvgpr_write_b32 a48, v193\n\t" \
  "v_accvgpr_read_b32 v193, a49\n\t" \
  "v_mul_f32 v193, v196, v193\n\t" \
  "v_accvgpr_write_b32 a49, v193\n\t" \
  "v_accvgpr_read_b32 v193, a50\n\t" \
  "v_mul_f32 v193, v196, v193\n\t" \
  "v_accvgpr_write_b32 a50, v193\n\t" \
  "v_accvgpr_read_b32 v193, a51\n\t" \
  "v_mul_f32 v193, v196, v193\n\t" \
  "v_accvgpr_write_b32 a51, v193\n\t" \
  "v_accvgpr_read_b32 v193, a52\n\t" \
  "v_mul_f32 v193, v197, v193\n\t" \
  "v_accvgpr_write_b32 a52, v193\n\t" \
  "v_accvgpr_read_b32 v193, a53\n\t" \
  "v_mul_f32 v193, v197, v193\n\t" \
  "v_accvgpr_write_b32 a53, v193\n\t" \
  "v_accvgpr_read_b32 v193, a54\n\t" \
  "v_mul_f32 v193, v197, v193\n\t" \
  "v_accvgpr_write_b32 a54, v193\n\t" \
  "v_accvgpr_read_b32 v193, a55\n\t" \
  "v_mul_f32 v193, v197, v193\n\t" \
  "v_accvgpr_write_b32 a55, v193\n\t" \
  "v_accvgpr_read_b32 v193, a56\n\t" \
  "v_mul_f32 v193, v198, v193\n\t" \
  "v_accvgpr_write_b32 a56, v193\n\t" \
  "v_accvgpr_read_b32 v193, a57\n\t" \
  "v_mul_f32 v193, v198, v193\n\t" \
  "v_accvgpr_write_b32 a57, v193\n\t" \
  "v_accvgpr_read_b32 v193, a58\n\t" \
  "v_mul_f32 v193, v198, v193\n\t" \
  "v_accvgpr_write_b32 a58, v193\n\t" \
  "v_accvgpr_read_b32 v193, a59\n\t" \
  "v_mul_f32 v193, v198, v193\n\t" \
  "v_accvgpr_write_b32 a59, v193\n\t" \
  "v_accvgpr_read_b32 v193, a60\n\t" \
  "v_mul_f32 v193, v199, v193\n\t" \
  "v_accvgpr_write_b32 a60, v193\n\t" \
  "v_accvgpr_read_b32 v193, a61\n\t" \
  "v_mul_f32 v193, v199, v193\n\t" \
  "v_accvgpr_write_b32 a61, v193\n\t" \
  "v_accvgpr_read_b32 v193, a62\n\t" \
  "v_mul_f32 v193, v199, v193\n\t" \
  "v_accvgpr_write_b32 a62, v193\n\t" \
  "v_accvgpr_read_b32 v193, a63\n\t" \
  "v_mul_f32 v193, v199, v193\n\t" \
  "v_accvgpr_write_b32 a63, v193\n\t" \
  "v_accvgpr_read_b32 v193, a64\n\t" \
  "v_mul_f32 v193, v196, v193\n\t" \
  "v_accvgpr_write_b32 a64, v193\n\t" \
  "v_accvgpr_read_b32 v193, a65\n\t" \
  "v_mul_f32 v193, v196, v193\n\t" \
  "v_accvgpr_write_b32 a65, v193\n\t" \
  "v_accvgpr_read_b32 v193, a66\n\t" \
  "v_mul_f32 v193, v196, v193\n\t" \
  "v_accvgpr_write_b32 a66, v193\n\t" \
  "v_accvgpr_read_b32 v193, a67\n\t" \
  "v_mul_f32 v193, v196, v193\n\t" \
  "v_accvgpr_write_b32 a67, v193\n\t" \
  "v_accvgpr_read_b32 v193, a68\n\t" \
  "v_mul_f32 v193, v197, v193\n\t" \
  "v_accvgpr_write_b32 a68, v193\n\t" \
  "v_accvgpr_read_b32 v193, a69\n\t" \
  "v_mul_f32 v193, v197, v193\n\t" \
  "v_accvgpr_write_b32 a69, v193\n\t" \
  "v_accvgpr_read_b32 v193, a70\n\t" \
  "v_mul_f32 v193, v197, v193\n\t" \
  "v_accvgpr_write_b32 a70, v193\n\t" \
  "v_accvgpr_read_b32 v193, a71\n\t" \
  "v_mul_f32 v193, v197, v193\n\t" \
  "v_accvgpr_write_b32 a71, v193\n\t" \
  "v_accvgpr_read_b32 v193, a72\n\t" \
  "v_mul_f32 v193, v198, v193\n\t" \
  "v_accvgpr_write_b32 a72, v193\n\t" \
  "v_accvgpr_read_b32 v193, a73\n\t" \
  "v_mul_f32 v193, v198, v193\n\t" \
  "v_accvgpr_write_b32 a73, v193\n\t" \
  "v_accvgpr_read_b32 v193, a74\n\t" \
  "v_mul_f32 v193, v198, v193\n\t" \
  "v_accvgpr_write_b32 a74, v193\n\t" \
  "v_accvgpr_read_b32 v193, a75\n\t" \
  "v_mul_f32 v193, v198, v193\n\t" \
  "v_accvgpr_write_b32 a75, v193\n\t" \
  "v_accvgpr_read_b32 v193, a76\n\t" \
  "v_mul_f32 v193, v199, v193\n\t" \
  "v_accvgpr_write_b32 a76, v193\n\t" \
  "v_accvgpr_read_b32 v193, a77\n\t" \
  "v_mul_f32 v193, v199, v193\n\t" \
  "v_accvgpr_write_b32 a77, v193\n\t" \
  "v_accvgpr_read_b32 v193, a78\n\t" \
  "v_mul_f32 v193, v199, v193\n\t" \
  "v_accvgpr_write_b32 a78, v193\n\t" \
  "v_accvgpr_read_b32 v193, a79\n\t" \
  "v_mul_f32 v193, v199, v193\n\t" \
  "v_accvgpr_write_b32 a79, v193\n\t" \
  "v_accvgpr_read_b32 v193, a80\n\t" \
  "v_mul_f32 v193, v196, v193\n\t" \
  "v_accvgpr_write_b32 a80, v193\n\t" \
  "v_accvgpr_read_b32 v193, a81\n\t" \
  "v_mul_f32 v193, v196, v193\n\t" \
  "v_accvgpr_write_b32 a81, v193\n\t" \
  "v_accvgpr_read_b32 v193, a82\n\t" \
  "v_mul_f32 v193, v196, v193\n\t" \
  "v_accvgpr_write_b32 a82, v193\n\t" \
  "v_accvgpr_read_b32 v193, a83\n\t" \
  "v_mul_f32 v193, v196, v193\n\t" \
  "v_accvgpr_write_b32 a83, v193\n\t" \
  "v_accvgpr_read_b32 v193, a84\n\t" \
  "v_mul_f32 v193, v197, v193\n\t" \
  "v_accvgpr_write_b32 a84, v193\n\t" \
  "v_accvgpr_read_b32 v193, a85\n\t" \
  "v_mul_f32 v193, v197, v193\n\t" \
  "v_accvgpr_write_b32 a85, v193\n\t" \
  "v_accvgpr_read_b32 v193, a86\n\t" \
  "v_mul_f32 v193, v197, v193\n\t" \
  "v_accvgpr_write_b32 a86, v193\n\t" \
  "v_accvgpr_read_b32 v193, a87\n\t" \
  "v_mul_f32 v193, v197, v193\n\t" \
  "v_accvgpr_write_b32 a87, v193\n\t" \
  "v_accvgpr_read_b32 v193, a88\n\t" \
  "v_mul_f32 v193, v198, v193\n\t" \
  "v_accvgpr_write_b32 a88, v193\n\t" \
  "v_accvgpr_read_b32 v193, a89\n\t" \
  "v_mul_f32 v193, v198, v193\n\t" \
  "v_accvgpr_write_b32 a89, v193\n\t" \
  "v_accvgpr_read_b32 v193, a90\n\t" \
  "v_mul_f32 v193, v198, v193\n\t" \
  "v_accvgpr_write_b32 a90, v193\n\t" \
  "v_accvgpr_read_b32 v193, a91\n\t" \
  "v_mul_f32 v193, v198, v193\n\t" \
  "v_accvgpr_write_b32 a91, v193\n\t" \
  "v_accvgpr_read_b32 v193, a92\n\t" \
  "v_mul_f32 v193, v199, v193\n\t" \
  "v_accvgpr_write_b32 a92, v193\n\t" \
  "v_accvgpr_read_b32 v193, a93\n\t" \
  "v_mul_f32 v193, v199, v193\n\t" \
  "v_accvgpr_write_b32 a93, v193\n\t" \
  "v_accvgpr_read_b32 v193, a94\n\t" \
  "v_mul_f32 v193, v199, v193\n\t" \
  "v_accvgpr_write_b32 a94, v193\n\t" \
  "v_accvgpr_read_b32 v193, a95\n\t" \
  "v_mul_f32 v193, v199, v193\n\t" \
  "v_accvgpr_write_b32 a95, v193\n\t" \
  "v_accvgpr_read_b32 v193, a96\n\t" \
  "v_mul_f32 v193, v196, v193\n\t" \
  "v_accvgpr_write_b32 a96, v193\n\t" \
  "v_accvgpr_read_b32 v193, a97\n\t" \
  "v_mul_f32 v193, v196, v193\n\t" \
  "v_accvgpr_write_b32 a97, v193\n\t" \
  "v_accvgpr_read_b32 v193, a98\n\t" \
  "v_mul_f32 v193, v196, v193\n\t" \
  "v_accvgpr_write_b32 a98, v193\n\t" \
  "v_accvgpr_read_b32 v193, a99\n\t" \
  "v_mul_f32 v193, v196, v193\n\t" \
  "v_accvgpr_write_b32 a99, v193\n\t" \
  "v_accvgpr_read_b32 v193, a100\n\t" \
  "v_mul_f32 v193, v197, v193\n\t" \
  "v_accvgpr_write_b32 a100, v193\n\t" \
  "v_accvgpr_read_b32 v193, a101\n\t" \
  "v_mul_f32 v193, v197, v193\n\t" \
  "v_accvgpr_write_b32 a101, v193\n\t" \
  "v_accvgpr_read_b32 v193, a102\n\t" \
  "v_mul_f32 v193, v197, v193\n\t" \
  "v_accvgpr_write_b32 a102, v193\n\t" \
  "v_accvgpr_read_b32 v193, a103\n\t" \
  "v_mul_f32 v193, v197, v193\n\t" \
  "v_accvgpr_write_b32 a103, v193\n\t" \
  "v_accvgpr_read_b32 v193, a104\n\t" \
  "v_mul_f32 v193, v198, v193\n\t" \
  "v_accvgpr_write_b32 a104, v193\n\t" \
  "v_accvgpr_read_b32 v193, a105\n\t" \
  "v_mul_f32 v193, v198, v193\n\t" \
  "v_accvgpr_write_b32 a105, v193\n\t" \
  "v_accvgpr_read_b32 v193, a106\n\t" \
  "v_mul_f32 v193, v198, v193\n\t" \
  "v_accvgpr_write_b32 a106, v193\n\t" \
  "v_accvgpr_read_b32 v193, a107\n\t" \
  "v_mul_f32 v193, v198, v193\n\t" \
  "v_accvgpr_write_b32 a107, v193\n\t" \
  "v_accvgpr_read_b32 v193, a108\n\t" \
  "v_mul_f32 v193, v199, v193\n\t" \
  "v_accvgpr_write_b32 a108, v193\n\t" \
  "v_accvgpr_read_b32 v193, a109\n\t" \
  "v_mul_f32 v193, v199, v193\n\t" \
  "v_accvgpr_write_b32 a109, v193\n\t" \
  "v_accvgpr_read_b32 v193, a110\n\t" \
  "v_mul_f32 v193, v199, v193\n\t" \
  "v_accvgpr_write_b32 a110, v193\n\t" \
  "v_accvgpr_read_b32 v193, a111\n\t" \
  "v_mul_f32 v193, v199, v193\n\t" \
  "v_accvgpr_write_b32 a111, v193\n\t" \
  "v_accvgpr_read_b32 v193, a112\n\t" \
  "v_mul_f32 v193, v196, v193\n\t" \
  "v_accvgpr_write_b32 a112, v193\n\t" \
  "v_accvgpr_read_b32 v193, a113\n\t" \
  "v_mul_f32 v193, v196, v193\n\t" \
  "v_accvgpr_write_b32 a113, v193\n\t" \
  "v_accvgpr_read_b32 v193, a114\n\t" \
  "v_mul_f32 v193, v196, v193\n\t" \
  "v_accvgpr_write_b32 a114, v193\n\t" \
  "v_accvgpr_read_b32 v193, a115\n\t" \
  "v_mul_f32 v193, v196, v193\n\t" \
  "v_accvgpr_write_b32 a115, v193\n\t" \
  "v_accvgpr_read_b32 v193, a116\n\t" \
  "v_mul_f32 v193, v197, v193\n\t" \
  "v_accvgpr_write_b32 a116, v193\n\t" \
  "v_accvgpr_read_b32 v193, a117\n\t" \
  "v_mul_f32 v193, v197, v193\n\t" \
  "v_accvgpr_write_b32 a117, v193\n\t" \
  "v_accvgpr_read_b32 v193, a118\n\t" \
  "v_mul_f32 v193, v197, v193\n\t" \
  "v_accvgpr_write_b32 a118, v193\n\t" \
  "v_accvgpr_read_b32 v193, a119\n\t" \
  "v_mul_f32 v193, v197, v193\n\t" \
  "v_accvgpr_write_b32 a119, v193\n\t" \
  "v_accvgpr_read_b32 v193, a120\n\t" \
  "v_mul_f32 v193, v198, v193\n\t" \
  "v_accvgpr_write_b32 a120, v193\n\t" \
  "v_accvgpr_read_b32 v193, a121\n\t" \
  "v_mul_f32 v193, v198, v193\n\t" \
  "v_accvgpr_write_b32 a121, v193\n\t" \
  "v_accvgpr_read_b32 v193, a122\n\t" \
  "v_mul_f32 v193, v198, v193\n\t" \
  "v_accvgpr_write_b32 a122, v193\n\t" \
  "v_accvgpr_read_b32 v193, a123\n\t" \
  "v_mul_f32 v193, v198, v193\n\t" \
  "v_accvgpr_write_b32 a123, v193\n\t" \
  "v_accvgpr_read_b32 v193, a124\n\t" \
  "v_mul_f32 v193, v199, v193\n\t" \
  "v_accvgpr_write_b32 a124, v193\n\t" \
  "v_accvgpr_read_b32 v193, a125\n\t" \
  "v_mul_f32 v193, v199, v193\n\t" \
  "v_accvgpr_write_b32 a125, v193\n\t" \
  "v_accvgpr_read_b32 v193, a126\n\t" \
  "v_mul_f32 v193, v199, v193\n\t" \
  "v_accvgpr_write_b32 a126, v193\n\t" \
  "v_accvgpr_read_b32 v193, a127\n\t" \
  "v_mul_f32 v193, v199, v193\n\t" \
  "v_accvgpr_write_b32 a127, v193\n\t" \
  "v_mul_f32 v160, v196, v160\n\t" \
  "v_mul_f32 v161, v196, v161\n\t" \
  "v_mul_f32 v162, v196, v162\n\t" \
  "v_mul_f32 v163, v196, v163\n\t" \
  "v_mov_b32 v196, 1.0\n\t" \
  "v_mul_f32 v164, v197, v164\n\t" \
  "v_mul_f32 v165, v197, v165\n\t" \
  "v_mul_f32 v166, v197, v166\n\t" \
  "v_mul_f32 v167, v197, v167\n\t" \
  "v_mov_b32 v197, 1.0\n\t" \
  "v_mul_f32 v168, v198, v168\n\t" \
  "v_mul_f32 v169, v198, v169\n\t" \
  "v_mul_f32 v170, v198, v170\n\t" \
  "v_mul_f32 v171, v198, v171\n\t" \
  "v_mov_b32 v198, 1.0\n\t" \
  "v_mul_f32 v172, v199, v172\n\t" \
  "v_mul_f32 v173, v199, v173\n\t" \
  "v_mul_f32 v174, v199, v174\n\t" \
  "v_mul_f32 v175, v199, v175\n\t" \
  "v_mov_b32 v199, 1.0\n\t" \
  "s_mov_b32 s99, 0\n\t" \
  "s_nop 1\n\t" \
  "s_cmp_eq_u32 vcc_lo, 0\n\t" \
  "s_cbranch_scc1 .Lsf_q0\n\t" \
  "s_cmp_eq_u32 vcc_lo, 1\n\t" \
  "s_cbranch_scc1 .Lsf_q1\n\t" \
  "s_cmp_eq_u32 vcc_lo, 2\n\t" \
  "s_cbranch_scc1 .Lsf_q2\n\t" \
  "s_cmp_eq_u32 vcc_lo, 3\n\t" \
  "s_cbranch_scc1 .Lsf_q3\n\t" \
  "s_cmp_eq_u32 vcc_lo, 4\n\t" \
  "s_cbranch_scc1 .Lsf_q4\n\t" \
  "s_cmp_eq_u32 vcc_lo, 5\n\t" \
  "s_cbranch_scc1 .Lsf_q5\n\t" \
  "s_cmp_eq_u32 vcc_lo, 6\n\t" \
  "s_cbranch_scc1 .Lsf_q6\n\t" \
  "s_cmp_eq_u32 vcc_lo, 7\n\t" \
  "s_cbranch_scc1 .Lsf_q7\n\t" \
  "s_endpgm\n\t" \
  ".Lsf_v:\n\t" \
  "s_mov_b32 s98, 0\n\t" \
  ".Lsf_v1:\n\t" \
  "s_sleep 4\n\t" \
  "buffer_load_dword v209, v205, %[ws], 0 offen sc1\n\t" \
  "s_waitcnt vmcnt(0)\n\t" \
  "v_and_b32 v206, 0xbfffbfff, v209\n\t" \
  "v_cmp_ne_u32_e64 s[96:97], %[tag], v206\n\t" \
  "s_cmp_lg_u64 s[96:97], 0\n\t" \
  "s_cbranch_scc0 .Lsf_w\n\t" \
  "s_add_u32 s98, s98, 1\n\t" \
  "s_cmp_lt_u32 s98, %[cap]\n\t" \
  "s_cbranch_scc1 .Lsf_v1\n\t" \
  "v_mov_b32 v206, %[tag]\n\t" \
  "buffer_store_dword v206, off, %[ws], 0 offset:256 sc1\n\t" \
  "s_branch .Lsf_w\n\t" \
  ".subsection 0\n\t" \
  ".Lat_end:\n\t" \
  ""
#define AT_CLOB "v208", "v209", "v210", "v211", "v212", "v214", "v215", "v216", "v217", "v218", "v219", "v235", "v236", "v237", "v238", "s92", "s93", "s94", "s95", "s96", "s97", "s98", "s99", "vcc", "scc", "memory"
#define AT_PF1
#define AT_NU 1
#define AT_BN 0
#define AT_XF "+{v[0:15]}"
#define AT_WF "+{v[16:47]}"
#define TL_ASM \
  "v_pk_fma_f32 v[0:1], v[128:129], v[96:97], v[0:1]\n\t" \
  "v_pk_fma_f32 v[2:3], v[130:131], v[98:99], v[2:3]\n\t" \
  "v_pk_fma_f32 v[16:17], v[132:133], v[112:113], v[16:17]\n\t" \
  "v_pk_fma_f32 v[18:19], v[134:135], v[114:115], v[18:19]\n\t" \
  "v_pk_fma_f32 v[4:5], v[128:129], v[100:101], v[4:5]\n\t" \
  "v_pk_fma_f32 v[6:7], v[130:131], v[102:103], v[6:7]\n\t" \
  "v_pk_fma_f32 v[20:21], v[132:133], v[116:117], v[20:21]\n\t" \
  "v_pk_fma_f32 v[22:23], v[134:135], v[118:119], v[22:23]\n\t" \
  "v_cvt_pk_bf16_f32 v160, v0, v1\n\t" \
  "v_cvt_pk_bf16_f32 v161, v2, v3\n\t" \
  "v_cvt_pk_bf16_f32 v162, v16, v17\n\t" \
  "v_cvt_pk_bf16_f32 v163, v18, v19\n\t" \
  "v_pk_fma_f32 v[8:9], v[128:129], v[104:105], v[8:9]\n\t" \
  "v_pk_fma_f32 v[10:11], v[130:131], v[106:107], v[10:11]\n\t" \
  "v_pk_fma_f32 v[24:25], v[132:133], v[120:121], v[24:25]\n\t" \
  "v_pk_fma_f32 v[26:27], v[134:135], v[122:123], v[26:27]\n\t" \
  "v_cvt_pk_bf16_f32 v164, v4, v5\n\t" \
  "v_cvt_pk_bf16_f32 v165, v6, v7\n\t" \
  "v_cvt_pk_bf16_f32 v166, v20, v21\n\t" \
  "v_cvt_pk_bf16_f32 v167, v22, v23\n\t" \
  "v_pk_fma_f32 v[12:13], v[128:129], v[108:109], v[12:13]\n\t" \
  "v_pk_fma_f32 v[14:15], v[130:131], v[110:111], v[14:15]\n\t" \
  "v_pk_fma_f32 v[28:29], v[132:133], v[124:125], v[28:29]\n\t" \
  "v_pk_fma_f32 v[30:31], v[134:135], v[126:127], v[30:31]\n\t" \
  "v_cvt_pk_bf16_f32 v168, v8, v9\n\t" \
  "v_cvt_pk_bf16_f32 v169, v10, v11\n\t" \
  "v_cvt_pk_bf16_f32 v170, v24, v25\n\t" \
  "v_cvt_pk_bf16_f32 v171, v26, v27\n\t" \
  "v_cvt_pk_bf16_f32 v172, v12, v13\n\t" \
  "v_cvt_pk_bf16_f32 v173, v14, v15\n\t" \
  "v_cvt_pk_bf16_f32 v174, v28, v29\n\t" \
  "v_cvt_pk_bf16_f32 v175, v30, v31\n\t" \
  "v_dot2_f32_bf16 v152, v160, v160, 0\n\t" \
  "v_dot2_f32_bf16 v153, v164, v164, 0\n\t" \
  "v_dot2_f32_bf16 v154, v168, v168, 0\n\t" \
  "v_dot2_f32_bf16 v155, v172, v172, 0\n\t" \
  "v_dot2_f32_bf16 v152, v161, v161, v152\n\t" \
  "v_dot2_f32_bf16 v153, v165, v165, v153\n\t" \
  "v_dot2_f32_bf16 v154, v169, v169, v154\n\t" \
  "v_dot2_f32_bf16 v155, v173, v173, v155\n\t" \
  "v_dot2_f32_bf16 v152, v162, v162, v152\n\t" \
  "v_dot2_f32_bf16 v153, v166, v166, v153\n\t" \
  "v_dot2_f32_bf16 v154, v170, v170, v154\n\t" \
  "v_dot2_f32_bf16 v155, v174, v174, v155\n\t" \
  "v_dot2_f32_bf16 v152, v163, v163, v152\n\t" \
  "v_dot2_f32_bf16 v153, v167, v167, v153\n\t" \
  "v_dot2_f32_bf16 v154, v171, v171, v154\n\t" \
  "v_dot2_f32_bf16 v155, v175, v175, v155\n\t" \
  "v_mfma_f32_16x16x32_bf16 v[156:159], a[208:211], v[160:163], 0\n\t" \
  "s_lshl_b32 s64, %[wid], 10\n\t" \
  "v_add_u32 v234, s64, v242\n\t" \
  "v_mov_b32 v250, s87\n\t" \
  "v_mfma_f32_16x16x32_bf16 v[156:159], a[212:215], v[164:167], v[156:159]\n\t" \
  "s_lshl_b32 s61, %[bid], 11\n\t" \
  "v_permlane32_swap_b32 v152, v154\n\t" \
  "v_mfma_f32_16x16x32_bf16 v[156:159], a[216:219], v[168:171], v[156:159]\n\t" \
  "s_and_b32 s61, s61, 0x2000\n\t" \
  "v_permlane32_swap_b32 v153, v155\n\t" \
  "v_mfma_f32_16x16x32_bf16 v[156:159], a[220:223], v[172:175], v[156:159]\n\t" \
  "s_xor_b32 s61, s61, %[nb]\n\t" \
  "v_add_f32 v152, v152, v154\n\t" \
  "v_add_f32 v153, v153, v155\n\t" \
  "v_writelane_b32 v250, s88, 1\n\t" \
  "s_and_b32 s80, %[bid], 3\n\t" \
  "v_permlane16_swap_b32 v152, v153\n\t" \
  "s_lshr_b32 s60, %[bid], 6\n\t" \
  "s_lshl2_add_u32 s60, s60, s80\n\t" \
  "v_add_f32 v158, v157, v158\n\t" \
  "v_add_f32 v157, v152, v153\n\t" \
  "v_mov_b32 v159, v250\n\t" \
  "ds_write_b128 v234, v[156:159]\n\t" \
  "s_lshl_b32 s62, s60, 15\n\t" \
  "s_add_u32 s62, s62, s61\n\t" \
  "s_add_u32 s62, s62, 524288\n\t" \
  "s_and_b32 s63, %[bid], 0x20\n\t" \
  "s_lshl_b32 s63, s63, 9\n\t" \
  "s_add_u32 s77, s62, s63\n\t" \
  "s_and_b32 s63, %[bid], 0x18\n\t" \
  "s_lshl_b32 s63, s63, 8\n\t" \
  "s_add_u32 s77, s77, s63\n\t" \
  "s_add_u32 s77, s77, s64\n\t" \
  "v_lshl_add_u32 v248, %[wid], 3, v242\n\t" \
  "v_mov_b32 v217, v243\n\t" \
  "v_mov_b32 v219, v243\n\t" \
  "s_cmp_gt_u32 %[wid], 1\n\t" \
  "s_waitcnt lgkmcnt(0)\n\t" \
  "s_barrier\n\t" \
  "s_cbranch_scc1 .Ltl_np\n\t" \
  "ds_read2st64_b64 v[208:211], v248 offset1:2\n\t" \
  "ds_read2st64_b64 v[212:215], v248 offset0:4 offset1:6\n\t" \
  "s_waitcnt lgkmcnt(1)\n\t" \
  "v_pk_add_f32 v[208:209], v[208:209], v[210:211]\n\t" \
  "s_waitcnt lgkmcnt(0)\n\t" \
  "v_pk_add_f32 v[212:213], v[212:213], v[214:215]\n\t" \
  "s_nop 0\n\t" \
  "v_add_f32 v216, v208, v212\n\t" \
  "v_add_f32 v218, v209, v213\n\t" \
  "buffer_store_dwordx4 v[216:219], v242, %[ws], s77 offen sc1\n\t" \
  ".Ltl_np:\n\t" \
  STP("s_memrealtime s[90:91]\n\t") \
  STP("s_memtime s[82:83]\n\t") \
  STP("s_waitcnt lgkmcnt(0)\n\t") \
  STP("s_or_b32 s90, s90, 1\n\t") \
  STP("v_writelane_b32 %[stv], s90, 40\n\t") \
  STP("v_writelane_b32 %[stv], s82, 41\n\t") \
  "s_and_b32 s63, %[wid], 2\n\t" \
  "s_lshl_b32 s63, s63, 13\n\t" \
  "s_add_u32 s62, s62, s63\n\t" \
  "s_and_b32 s63, %[wid], 1\n\t" \
  "s_lshl_b32 s63, s63, 12\n\t" \
  "s_add_u32 s62, s62, s63\n\t" \
  "ds_read_b128 v[128:131], v237 offset:2688\n\t" \
  "ds_read_b128 v[132:135], v237 offset:2752\n\t" \
  "ds_read_b128 v[136:139], v237 offset:2816\n\t" \
  "ds_read_b128 v[140:143], v237 offset:2880\n\t" \
  "ds_read_b128 v[144:147], v237 offset:2944\n\t" \
  "ds_read_b128 v[148:151], v237 offset:3008\n\t" \
  "s_mov_b32 s75, 0x05040100\n\t" \
  "s_mov_b32 s76, 0x07060302\n\t" \
  "s_mov_b32 s72, 0\n\t" \
  "s_mov_b32 s81, 0xffff0000\n\t" \
  "s_bfe_u32 s79, %[bid], 0x30003\n\t" \
  "s_and_b32 s63, %[bid], 0x4\n\t" \
  "s_lshl_b32 s63, s63, 2\n\t" \
  "s_add_u32 s60, s60, s63\n\t" \
  "v_lshlrev_b32 v0, 16, v160\n\t" \
  "v_and_b32 v1, 0xffff0000, v160\n\t" \
  "v_lshlrev_b32 v2, 16, v161\n\t" \
  "v_and_b32 v3, 0xffff0000, v161\n\t" \
  "v_lshlrev_b32 v16, 16, v162\n\t" \
  "v_and_b32 v17, 0xffff0000, v162\n\t" \
  "v_lshlrev_b32 v18, 16, v163\n\t" \
  "v_and_b32 v19, 0xffff0000, v163\n\t" \
  "v_lshlrev_b32 v4, 16, v164\n\t" \
  "v_and_b32 v5, 0xffff0000, v164\n\t" \
  "v_lshlrev_b32 v6, 16, v165\n\t" \
  "v_and_b32 v7, 0xffff0000, v165\n\t" \
  "v_lshlrev_b32 v20, 16, v166\n\t" \
  "v_and_b32 v21, 0xffff0000, v166\n\t" \
  "v_lshlrev_b32 v22, 16, v167\n\t" \
  "v_and_b32 v23, 0xffff0000, v167\n\t" \
  "v_lshlrev_b32 v8, 16, v168\n\t" \
  "v_and_b32 v9, 0xffff0000, v168\n\t" \
  "v_lshlrev_b32 v10, 16, v169\n\t" \
  "v_and_b32 v11, 0xffff0000, v169\n\t" \
  "v_lshlrev_b32 v24, 16, v170\n\t" \
  "v_and_b32 v25, 0xffff0000, v170\n\t" \
  "v_lshlrev_b32 v26, 16, v171\n\t" \
  "v_and_b32 v27, 0xffff0000, v171\n\t" \
  "v_lshlrev_b32 v12, 16, v172\n\t" \
  "v_and_b32 v13, 0xffff0000, v172\n\t" \
  "buffer_load_dwordx4 v[208:211], v242, %[ws], s62 offen offset:0 sc1\n\t" \
  "buffer_load_dwordx4 v[212:215], v242, %[ws], s62 offen offset:1024 sc1\n\t" \
  "buffer_load_dwordx4 v[216:219], v242, %[ws], s62 offen offset:2048 sc1\n\t" \
  "buffer_load_dwordx4 v[220:223], v242, %[ws], s62 offen offset:3072 sc1\n\t" \
  "s_branch .Lht0\n\t" \
  ".p2align 12\n\t" \
  ".Lht0:\n\t" \
  "s_cbranch_execz .Lht0n\n\t" \
  "v_lshlrev_b32 v14, 16, v173\n\t" \
  "v_and_b32 v15, 0xffff0000, v173\n\t" \
  "v_lshlrev_b32 v28, 16, v174\n\t" \
  "v_and_b32 v29, 0xffff0000, v174\n\t" \
  "v_lshlrev_b32 v30, 16, v175\n\t" \
  "v_and_b32 v31, 0xffff0000, v175\n\t" \
  "s_lshl_b32 s63, s60, 6\n\t" \
  "s_sub_u32 s63, 0x7ff, s63\n\t" \
  "v_sub_u32 v248, s63, v244\n\t" \
  "v_and_b32 v246, 31, v244\n\t" \
  "s_and_b32 s63, %[bid], 0x18\n\t" \
  "s_lshl_b32 s63, s63, 7\n\t" \
  "s_add_u32 s65, s61, s63\n\t" \
  "s_and_b32 s63, %[bid], 0x3\n\t" \
  "s_lshl_b32 s63, s63, 8\n\t" \
  "s_add_u32 s65, s65, s63\n\t" \
  "s_add_u32 s65, s65, 65536\n\t" \
  "v_lshl_add_u32 v235, v246, 3, s65\n\t" \
  "s_lshl_b32 s68, 1, s60\n\t" \
  "s_lshl2_add_u32 s65, s79, %[wid]\n\t" \
  "s_lshl_b32 s63, s65, 13\n\t" \
  "s_and_b32 s63, s63, 0x2000\n\t" \
  "s_xor_b32 s63, s63, %[nb]\n\t" \
  "s_lshr_b32 s65, s65, 1\n\t" \
  "s_lshl_b32 s65, s65, 8\n\t" \
  "s_add_u32 s65, s65, s63\n\t" \
  "s_lshl3_add_u32 s65, s60, s65\n\t" \
  "s_add_u32 s65, s65, 65536\n\t" \
  "v_bfrev_b32 v249, 1\n\t" \
  "s_nop 0\n\t" \
  "v_writelane_b32 v249, 0, 63\n\t" \
  "v_mov_b32 v159, v243\n\t" \
  "s_add_u32 s63, s61, 262144\n\t" \
  "v_and_b32 v236, -32, v234\n\t" \
  "v_add_u32 v236, s63, v236\n\t" \
  "v_mov_b32 v113, v243\n\t" \
  "v_mov_b32 v115, v243\n\t" \
  "v_mov_b32 v117, v243\n\t" \
  "v_mov_b32 v119, v243\n\t" \
  "v_mov_b32 v121, v243\n\t" \
  "v_mov_b32 v123, v243\n\t" \
  "v_mov_b32 v125, v243\n\t" \
  "v_mov_b32 v127, v243\n\t" \
  "v_and_b32 v239, 48, v244\n\t" \
  "v_lshl_add_u32 v239, %[wid], 7, v239\n\t" \
  "v_and_b32 v157, 15, v244\n\t" \
  "v_bfrev_b32 v156, 1\n\t" \
  "s_lshl_b32 s66, s60, 13\n\t" \
  "s_and_b32 s66, s66, 0x3c000\n\t" \
  "s_and_b32 s63, %[bid], 0x1\n\t" \
  "s_lshl_b32 s63, s63, 12\n\t" \
  "s_add_u32 s66, s66, s63\n\t" \
  "s_and_b32 s63, %[bid], 0x38\n\t" \
  "s_lshl_b32 s63, s63, 6\n\t" \
  "s_add_u32 s66, s66, s63\n\t" \
  "s_add_u32 s66, s66, 262144\n\t" \
  "s_xor_b32 s77, s66, 0x2000\n\t" \
  "s_lshl_b32 s67, s60, 12\n\t" \
  "s_and_b32 s67, s67, 0x1c000\n\t" \
  "s_and_b32 s63, %[bid], 0x20\n\t" \
  "s_lshl_b32 s63, s63, 8\n\t" \
  "s_add_u32 s67, s67, s63\n\t" \
  "s_xor_b32 s67, s67, %[nb]\n\t" \
  "s_and_b32 s63, %[bid], 0x3\n\t" \
  "s_lshl_b32 s63, s63, 11\n\t" \
  "s_add_u32 s67, s67, s63\n\t" \
  "s_and_b32 s63, %[bid], 0x18\n\t" \
  "s_lshl_b32 s63, s63, 6\n\t" \
  "s_add_u32 s67, s67, s63\n\t" \
  "s_add_u32 s67, s67, 1966080\n\t" \
  "v_perm_b32 v240, v34, v32, s75\n\t" \
  "v_perm_b32 v34, v34, v32, s76\n\t" \
  "v_perm_b32 v32, v35, v33, s75\n\t" \
  "v_perm_b32 v35, v35, v33, s76\n\t" \
  "v_perm_b32 v33, v38, v36, s75\n\t" \
  "v_perm_b32 v38, v38, v36, s76\n\t" \
  "v_perm_b32 v36, v39, v37, s75\n\t" \
  "v_perm_b32 v39, v39, v37, s76\n\t" \
  "v_perm_b32 v37, v42, v40, s75\n\t" \
  "v_perm_b32 v42, v42, v40, s76\n\t" \
  "v_perm_b32 v40, v43, v41, s75\n\t" \
  "v_perm_b32 v43, v43, v41, s76\n\t" \
  "v_perm_b32 v41, v46, v44, s75\n\t" \
  "v_perm_b32 v46, v46, v44, s76\n\t" \
  "v_perm_b32 v44, v47, v45, s75\n\t" \
  "v_perm_b32 v47, v47, v45, s76\n\t" \
  "v_perm_b32 v45, v50, v48, s75\n\t" \
  "v_perm_b32 v50, v50, v48, s76\n\t" \
  "v_perm_b32 v48, v51, v49, s75\n\t" \
  "v_perm_b32 v51, v51, v49, s76\n\t" \
  "v_perm_b32 v49, v54, v52, s75\n\t" \
  "v_perm_b32 v54, v54, v52, s76\n\t" \
  "v_perm_b32 v52, v55, v53, s75\n\t" \
  "v_perm_b32 v55, v55, v53, s76\n\t" \
  "v_perm_b32 v53, v58, v56, s75\n\t" \
  "v_perm_b32 v58, v58, v56, s76\n\t" \
  "v_perm_b32 v56, v59, v57, s75\n\t" \
  "v_perm_b32 v59, v59, v57, s76\n\t" \
  "v_perm_b32 v57, v62, v60, s75\n\t" \
  "v_perm_b32 v62, v62, v60, s76\n\t" \
  "v_perm_b32 v60, v63, v61, s75\n\t" \
  "v_perm_b32 v63, v63, v61, s76\n\t" \
  "s_waitcnt vmcnt(0)\n\t" \
  ".Ltl_c0:\n\t" \
  "v_xor_b32 v152, %[tag], v209\n\t" \
  "v_bitop3_b32 v152, v152, v211, %[tag] bitop3:0xf6\n\t" \
  "v_bitop3_b32 v152, v152, v213, %[tag] bitop3:0xf6\n\t" \
  "v_bitop3_b32 v152, v152, v215, %[tag] bitop3:0xf6\n\t" \
  "v_bitop3_b32 v152, v152, v217, %[tag] bitop3:0xf6\n\t" \
  "v_bitop3_b32 v152, v152, v219, %[tag] bitop3:0xf6\n\t" \
  "v_bitop3_b32 v152, v152, v221, %[tag] bitop3:0xf6\n\t" \
  "v_bitop3_b32 v152, v152, v223, %[tag] bitop3:0xf6\n\t" \
  "v_cmp_ne_u32 vcc, 0, v152\n\t" \
  "v_add_f32 v152, v208, v216\n\t" \
  "v_add_f32 v153, v210, v218\n\t" \
  "v_add_f32 v154, v212, v220\n\t" \
  "v_add_f32 v155, v214, v222\n\t" \
  "s_cbranch_vccnz .Ltl_s0\n\t" \
  ".Ltl_k0:\n\t" \
  "ds_write_b128 v234, v[152:155] offset:4096\n\t" \
  "buffer_load_dwordx2 v[216:217], v235, %[ws], 0 offen sc1\n\t" \
  "s_waitcnt lgkmcnt(0)\n\t" \
  "s_barrier\n\t" \
  STP("s_memrealtime s[90:91]\n\t") \
  STP("s_memtime s[82:83]\n\t") \
  STP("s_waitcnt lgkmcnt(0)\n\t") \
  STP("s_or_b32 s90, s90, 1\n\t") \
  STP("v_writelane_b32 %[stv], s90, 42\n\t") \
  STP("v_writelane_b32 %[stv], s82, 43\n\t") \
  "ds_read_b128 v[96:99], v242 offset:4096\n\t" \
  "ds_read_b128 v[100:103], v242 offset:5120\n\t" \
  "ds_read_b128 v[104:107], v242 offset:6144\n\t" \
  "ds_read_b128 v[108:111], v242 offset:7168\n\t" \
  "s_waitcnt lgkmcnt(2)\n\t" \
  "v_pk_add_f32 v[96:97], v[96:97], v[100:101]\n\t" \
  "v_pk_add_f32 v[98:99], v[98:99], v[102:103]\n\t" \
  "s_waitcnt lgkmcnt(0)\n\t" \
  "v_pk_add_f32 v[104:105], v[104:105], v[108:109]\n\t" \
  "v_pk_add_f32 v[106:107], v[106:107], v[110:111]\n\t" \
  "v_pk_add_f32 v[96:97], v[96:97], v[104:105]\n\t" \
  "v_pk_add_f32 v[98:99], v[98:99], v[106:107]\n\t" \
  "v_mul_f32 v96, 0x3a800000, v96\n\t" \
  "v_mul_f32 v97, 0x3a800000, v97\n\t" \
  "v_readlane_b32 s87, v99, 0\n\t" \
  "v_fma_f32 v97, -v96, v96, v97\n\t" \
  "v_readlane_b32 s88, v99, 1\n\t" \
  "v_max_f32 v97, 0, v97\n\t" \
  "v_add_f32 v97, 0x3727c5ac, v97\n\t" \
  "v_rsq_f32 v208, v97\n\t" \
  "v_fma_f32 v98, -v96, s87, v98\n\t" \
  "v_fma_f32 v98, v208, v98, s88\n\t" \
  "v_cvt_pk_bf16_f32 v98, v98, v98\n\t" \
  "v_ashrrev_i32 v99, 31, v98\n\t" \
  "v_or_b32 v99, 0x80000000, v99\n\t" \
  "v_xor_b32 v98, v98, v99\n\t" \
  "v_and_or_b32 v158, v98, s81, v248\n\t" \
  "v_mul_f32 v209, v208, -v96\n\t" \
  "v_mov_b32 v212, v208\n\t" \
  "v_max_u32_dpp v158, v158, v158 quad_perm:[1,0,3,2] row_mask:0xf bank_mask:0xf\n\t" \
  "v_mov_b32 v213, v209\n\t" \
  "v_permlane32_swap_b32 v208, v212\n\t" \
  "v_max_u32_dpp v158, v158, v158 quad_perm:[2,3,0,1] row_mask:0xf bank_mask:0xf\n\t" \
  "v_permlane32_swap_b32 v209, v213\n\t" \
  "v_mov_b32 v210, v208\n\t" \
  "v_max_u32_dpp v158, v158, v158 row_half_mirror row_mask:0xf bank_mask:0xf\n\t" \
  "v_mov_b32 v211, v209\n\t" \
  "v_mov_b32 v214, v212\n\t" \
  "v_max_u32_dpp v158, v158, v158 row_mirror row_mask:0xf bank_mask:0xf\n\t" \
  "v_mov_b32 v215, v213\n\t" \
  "v_permlane16_swap_b32 v208, v210\n\t" \
  "v_max_u32_dpp v158, v158, v158 row_bcast:15 row_mask:0xa bank_mask:0xf\n\t" \
  "v_permlane16_swap_b32 v209, v211\n\t" \
  "v_permlane16_swap_b32 v212, v214\n\t" \
  "v_max_u32_dpp v158, v158, v158 row_bcast:31 row_mask:0xc bank_mask:0xf\n\t" \
  "buffer_store_dwordx2 v[158:159], v249, %[ws], s65 offen sc1\n\t" \
  "v_readlane_b32 s69, v158, 63\n\t" \
  STP("s_memrealtime s[90:91]\n\t") \
  STP("s_memtime s[82:83]\n\t") \
  STP("s_waitcnt lgkmcnt(0)\n\t") \
  STP("s_or_b32 s90, s90, 1\n\t") \
  STP("v_writelane_b32 %[stv], s90, 44\n\t") \
  STP("v_writelane_b32 %[stv], s82, 45\n\t") \
  "v_permlane16_swap_b32 v213, v215\n\t" \
  "s_andn2_b32 s73, 63, s69\n\t" \
  "s_waitcnt vmcnt(1)\n\t" \
  "v_cmp_eq_u32 vcc, %[tag], v217\n\t" \
  "s_mov_b32 s74, 0x100\n\t" \
  "s_nop 0\n\t" \
  "v_cndmask_b32 v152, 0, v216, vcc\n\t" \
  "s_bcnt1_i32_b32 s78, vcc_lo\n\t" \
  "v_cmp_lt_u32 vcc, s69, v152\n\t" \
  "s_cbranch_vccz .Ltl_cd\n\t" \
  ".Ltl_ce:\n\t" \
  "v_max_u32_dpp v152, v152, v152 quad_perm:[1,0,3,2] row_mask:0xf bank_mask:0xf\n\t" \
  "s_lshl_b32 s63, s60, 17\n\t" \
  "s_lshl_b32 s64, s79, 8\n\t" \
  "v_max_u32_dpp v152, v152, v152 quad_perm:[2,3,0,1] row_mask:0xf bank_mask:0xf\n\t" \
  "s_add_u32 s63, s63, s64\n\t" \
  "s_lshl_b32 s64, %[wid], 6\n\t" \
  "v_max_u32_dpp v152, v152, v152 row_half_mirror row_mask:0xf bank_mask:0xf\n\t" \
  "s_add_u32 s63, s63, s64\n\t" \
  "v_lshl_add_u32 v238, v157, 11, s63\n\t" \
  "v_max_u32_dpp v152, v152, v152 row_mirror row_mask:0xf bank_mask:0xf\n\t" \
  "s_nop 1\n\t" \
  "v_max_u32_dpp v152, v152, v152 row_bcast:15 row_mask:0xa bank_mask:0xf\n\t" \
  "s_nop 0\n\t" \
  "v_readlane_b32 s70, v152, 31\n\t" \
  "s_max_u32 s70, s70, s69\n\t" \
  "s_andn2_b32 s63, 0x780, s70\n\t" \
  "s_lshl_b32 s63, s63, 7\n\t" \
  "s_andn2_b32 s71, 64, s70\n\t" \
  "s_lshl_b32 s71, s71, 6\n\t" \
  "s_or_b32 s71, s71, s63\n\t" \
  "buffer_load_dwordx2 v[216:217], v235, %[ws], 0 offen sc1\n\t" \
  "buffer_load_dwordx4 v[218:221], v236, %[ws], s71 offen sc1\n\t" \
  "buffer_load_dwordx4 v[222:225], v236, %[ws], s71 offen offset:16 sc1\n\t" \
  "v_bfe_u32 v153, v244, 4, 1\n\t" \
  "v_lshl_add_u32 v238, v153, 5, v238\n\t" \
  "v_lshrrev_b32 v154, 5, v244\n\t" \
  "v_lshl_add_u32 v238, v154, 4, v238\n\t" \
  "s_mov_b64 s[96:97], %[out]\n\t" \
  "s_and_b32 s97, s97, 0xffff\n\t" \
  "s_mov_b32 s98, 0x400000\n\t" \
  "s_mov_b32 s99, 0x20000\n\t" \
  "s_lshr_b32 s64, %[bid], 3\n\t" \
  "s_and_b32 s63, s64, 27\n\t" \
  "s_cmp_lt_u32 s64, 24\n\t" \
  "s_cselect_b32 s64, s64, s63\n\t" \
  "s_mul_i32 s63, s80, 56\n\t" \
  "s_add_u32 s64, s64, s63\n\t" \
  "s_and_b32 s63, %[bid], 4\n\t" \
  "s_mul_i32 s63, s63, 7\n\t" \
  "s_add_u32 s64, s64, s63\n\t" \
  "s_lshl_b32 s63, %[wid], 12\n\t" \
  "s_and_b32 s63, s63, 0x2000\n\t" \
  "s_xor_b32 s85, s63, %[nb]\n\t" \
  "s_lshr_b32 s63, s64, 2\n\t" \
  "s_lshl_b32 s63, s63, 14\n\t" \
  "s_add_u32 s85, s85, s63\n\t" \
  "s_and_b32 s63, s64, 3\n\t" \
  "s_lshl_b32 s63, s63, 11\n\t" \
  "s_add_u32 s85, s85, s63\n\t" \
  "s_add_u32 s85, s85, 1048576\n\t" \
  "v_and_b32 v247, 0x7f0, v234\n\t" \
  "v_lshlrev_b32 v245, 6, v154\n\t" \
  "v_lshl_add_u32 v245, v153, 4, v245\n\t" \
  "v_and_b32 v153, 1, v244\n\t" \
  "v_lshl_add_u32 v245, v153, 5, v245\n\t" \
  "v_lshl_add_u32 v245, %[wid], 7, v245\n\t" \
  "v_and_b32 v153, 14, v244\n\t" \
  "v_lshl_add_u32 v245, v153, 8, v245\n\t" \
  "v_lshlrev_b32 v246, 2, v246\n\t" \
  "v_add_u32 v248, 0x80, v246\n\t" \
  "s_mov_b32 s72, 0\n\t" \
  "s_lshr_b32 s62, %[bid], 5\n\t" \
  "s_bfe_u32 s63, %[bid], 0x10002\n\t" \
  "s_sub_u32 s62, s62, s63\n\t" \
  "s_cmp_ge_u32 s78, 0\n\t" \
  "s_cselect_b32 s84, 1, 0\n\t" \
  "s_cmp_eq_u32 s62, 6\n\t" \
  "s_cselect_b32 s84, 0, s84\n\t" \
  "s_cmp_lt_u32 s78, 33\n\t" \
  "s_cselect_b32 s78, 0, 0x80000000\n\t" \
  "s_cmp_lg_u32 s84, 0\n\t" \
  "s_cbranch_scc1 .Lht1\n\t" \
  ".Ltl_h2:\n\t" \
  "v_pk_fma_f32 v[112:113], v[0:1], v[208:209], v[208:209] op_sel:[0,0,1] op_sel_hi:[1,0,1]\n\t" \
  "v_pk_fma_f32 v[114:115], v[2:3], v[208:209], v[208:209] op_sel:[0,0,1] op_sel_hi:[1,0,1]\n\t" \
  "v_pk_fma_f32 v[116:117], v[16:17], v[208:209], v[208:209] op_sel:[0,0,1] op_sel_hi:[1,0,1]\n\t" \
  "v_pk_fma_f32 v[118:119], v[18:19], v[208:209], v[208:209] op_sel:[0,0,1] op_sel_hi:[1,0,1]\n\t" \
  "v_pk_fma_f32 v[112:113], v[112:113], v[136:137], v[128:129]\n\t" \
  "v_pk_fma_f32 v[114:115], v[114:115], v[138:139], v[130:131]\n\t" \
  "v_pk_fma_f32 v[116:117], v[116:117], v[140:141], v[132:133]\n\t" \
  "v_pk_fma_f32 v[118:119], v[118:119], v[142:143], v[134:135]\n\t" \
  "v_cvt_pk_bf16_f32 v96, v112, v113\n\t" \
  "v_cvt_pk_bf16_f32 v97, v114, v115\n\t" \
  "v_cvt_pk_bf16_f32 v98, v116, v117\n\t" \
  "v_cvt_pk_bf16_f32 v99, v118, v119\n\t" \
  "v_pk_fma_f32 v[120:121], v[4:5], v[210:211], v[210:211] op_sel:[0,0,1] op_sel_hi:[1,0,1]\n\t" \
  "v_pk_fma_f32 v[122:123], v[6:7], v[210:211], v[210:211] op_sel:[0,0,1] op_sel_hi:[1,0,1]\n\t" \
  "v_pk_fma_f32 v[124:125], v[20:21], v[210:211], v[210:211] op_sel:[0,0,1] op_sel_hi:[1,0,1]\n\t" \
  "v_pk_fma_f32 v[126:127], v[22:23], v[210:211], v[210:211] op_sel:[0,0,1] op_sel_hi:[1,0,1]\n\t" \
  "v_pk_fma_f32 v[120:121], v[120:121], v[136:137], v[128:129]\n\t" \
  "v_pk_fma_f32 v[122:123], v[122:123], v[138:139], v[130:131]\n\t" \
  "v_pk_fma_f32 v[124:125], v[124:125], v[140:141], v[132:133]\n\t" \
  "v_pk_fma_f32 v[126:127], v[126:127], v[142:143], v[134:135]\n\t" \
  "v_cvt_pk_bf16_f32 v100, v120, v121\n\t" \
  "v_cvt_pk_bf16_f32 v101, v122, v123\n\t" \
  "v_cvt_pk_bf16_f32 v102, v124, v125\n\t" \
  "v_cvt_pk_bf16_f32 v103, v126, v127\n\t" \
  "v_pk_fma_f32 v[112:113], v[8:9], v[212:213], v[212:213] op_sel:[0,0,1] op_sel_hi:[1,0,1]\n\t" \
  "v_pk_fma_f32 v[114:115], v[10:11], v[212:213], v[212:213] op_sel:[0,0,1] op_sel_hi:[1,0,1]\n\t" \
  "v_pk_fma_f32 v[116:117], v[24:25], v[212:213], v[212:213] op_sel:[0,0,1] op_sel_hi:[1,0,1]\n\t" \
  "v_pk_fma_f32 v[118:119], v[26:27], v[212:213], v[212:213] op_sel:[0,0,1] op_sel_hi:[1,0,1]\n\t" \
  "v_pk_fma_f32 v[112:113], v[112:113], v[136:137], v[128:129]\n\t" \
  "v_pk_fma_f32 v[114:115], v[114:115], v[138:139], v[130:131]\n\t" \
  "v_pk_fma_f32 v[116:117], v[116:117], v[140:141], v[132:133]\n\t" \
  "v_pk_fma_f32 v[118:119], v[118:119], v[142:143], v[134:135]\n\t" \
  "v_cvt_pk_bf16_f32 v104, v112, v113\n\t" \
  "v_cvt_pk_bf16_f32 v105, v114, v115\n\t" \
  "v_cvt_pk_bf16_f32 v106, v116, v117\n\t" \
  "v_cvt_pk_bf16_f32 v107, v118, v119\n\t" \
  "v_pk_fma_f32 v[120:121], v[12:13], v[214:215], v[214:215] op_sel:[0,0,1] op_sel_hi:[1,0,1]\n\t" \
  "v_pk_fma_f32 v[122:123], v[14:15], v[214:215], v[214:215] op_sel:[0,0,1] op_sel_hi:[1,0,1]\n\t" \
  "v_pk_fma_f32 v[124:125], v[28:29], v[214:215], v[214:215] op_sel:[0,0,1] op_sel_hi:[1,0,1]\n\t" \
  "v_pk_fma_f32 v[126:127], v[30:31], v[214:215], v[214:215] op_sel:[0,0,1] op_sel_hi:[1,0,1]\n\t" \
  "v_pk_fma_f32 v[120:121], v[120:121], v[136:137], v[128:129]\n\t" \
  "v_pk_fma_f32 v[122:123], v[122:123], v[138:139], v[130:131]\n\t" \
  "v_pk_fma_f32 v[124:125], v[124:125], v[140:141], v[132:133]\n\t" \
  "v_pk_fma_f32 v[126:127], v[126:127], v[142:143], v[134:135]\n\t" \
  "v_cvt_pk_bf16_f32 v108, v120, v121\n\t" \
  "v_cvt_pk_bf16_f32 v109, v122, v123\n\t" \
  "v_cvt_pk_bf16_f32 v110, v124, v125\n\t" \
  "v_cvt_pk_bf16_f32 v111, v126, v127\n\t" \
  "v_sub_u32 v152, s74, v157\n\t" \
  "v_bfrev_b32 v155, 1\n\t" \
  "v_cmp_eq_u32 vcc, 0, v152\n\t" \
  "v_lshlrev_b32 v160, 16, v96\n\t" \
  "v_and_b32 v161, 0xffff0000, v96\n\t" \
  "v_lshlrev_b32 v162, 16, v97\n\t" \
  "v_and_b32 v163, 0xffff0000, v97\n\t" \
  "v_lshlrev_b32 v164, 16, v98\n\t" \
  "v_and_b32 v165, 0xffff0000, v98\n\t" \
  "v_lshlrev_b32 v166, 16, v99\n\t" \
  "v_and_b32 v167, 0xffff0000, v99\n\t" \
  "v_pk_fma_f32 v[160:161], v[144:145], v[160:161], v[0:1]\n\t" \
  "v_pk_fma_f32 v[162:163], v[146:147], v[162:163], v[2:3]\n\t" \
  "v_pk_fma_f32 v[164:165], v[148:149], v[164:165], v[16:17]\n\t" \
  "v_pk_fma_f32 v[166:167], v[150:151], v[166:167], v[18:19]\n\t" \
  "v_cndmask_b32 v228, v238, v155, vcc\n\t" \
  "v_cvt_pk_bf16_f32 v112, v160, v161\n\t" \
  "v_cvt_pk_bf16_f32 v114, v164, v165\n\t" \
  "v_cvt_pk_bf16_f32 v113, v162, v163\n\t" \
  "v_cvt_pk_bf16_f32 v115, v166, v167\n\t" \
  "v_or_b32 v232, s78, v228\n\t" \
  "s_mov_b32 s64, 0\n\t" \
  "v_permlane16_swap_b32 v112, v114\n\t" \
  "v_permlane16_swap_b32 v113, v115\n\t" \
  "buffer_store_dwordx4 v[112:115], v232, s[96:99], s64 offen sc0 sc1\n\t" \
  "v_cmp_eq_u32 vcc, 16, v152\n\t" \
  "v_lshlrev_b32 v160, 16, v100\n\t" \
  "v_and_b32 v161, 0xffff0000, v100\n\t" \
  "v_lshlrev_b32 v162, 16, v101\n\t" \
  "v_and_b32 v163, 0xffff0000, v101\n\t" \
  "v_lshlrev_b32 v164, 16, v102\n\t" \
  "v_and_b32 v165, 0xffff0000, v102\n\t" \
  "v_lshlrev_b32 v166, 16, v103\n\t" \
  "v_and_b32 v167, 0xffff0000, v103\n\t" \
  "v_pk_fma_f32 v[160:161], v[144:145], v[160:161], v[4:5]\n\t" \
  "v_pk_fma_f32 v[162:163], v[146:147], v[162:163], v[6:7]\n\t" \
  "v_pk_fma_f32 v[164:165], v[148:149], v[164:165], v[20:21]\n\t" \
  "v_pk_fma_f32 v[166:167], v[150:151], v[166:167], v[22:23]\n\t" \
  "v_cndmask_b32 v229, v238, v155, vcc\n\t" \
  "v_cvt_pk_bf16_f32 v116, v160, v161\n\t" \
  "v_cvt_pk_bf16_f32 v118, v164, v165\n\t" \
  "v_cvt_pk_bf16_f32 v117, v162, v163\n\t" \
  "v_cvt_pk_bf16_f32 v119, v166, v167\n\t" \
  "v_or_b32 v232, s78, v229\n\t" \
  "s_mov_b32 s64, 32768\n\t" \
  "v_permlane16_swap_b32 v116, v118\n\t" \
  "v_permlane16_swap_b32 v117, v119\n\t" \
  "buffer_store_dwordx4 v[116:119], v232, s[96:99], s64 offen sc0 sc1\n\t" \
  "v_cmp_eq_u32 vcc, 32, v152\n\t" \
  "v_lshlrev_b32 v160, 16, v104\n\t" \
  "v_and_b32 v161, 0xffff0000, v104\n\t" \
  "v_lshlrev_b32 v162, 16, v105\n\t" \
  "v_and_b32 v163, 0xffff0000, v105\n\t" \
  "v_lshlrev_b32 v164, 16, v106\n\t" \
  "v_and_b32 v165, 0xffff0000, v106\n\t" \
  "v_lshlrev_b32 v166, 16, v107\n\t" \
  "v_and_b32 v167, 0xffff0000, v107\n\t" \
  "v_pk_fma_f32 v[160:161], v[144:145], v[160:161], v[8:9]\n\t" \
  "v_pk_fma_f32 v[162:163], v[146:147], v[162:163], v[10:11]\n\t" \
  "v_pk_fma_f32 v[164:165], v[148:149], v[164:165], v[24:25]\n\t" \
  "v_pk_fma_f32 v[166:167], v[150:151], v[166:167], v[26:27]\n\t" \
  "v_cndmask_b32 v230, v238, v155, vcc\n\t" \
  "v_cvt_pk_bf16_f32 v120, v160, v161\n\t" \
  "v_cvt_pk_bf16_f32 v122, v164, v165\n\t" \
  "v_cvt_pk_bf16_f32 v121, v162, v163\n\t" \
  "v_cvt_pk_bf16_f32 v123, v166, v167\n\t" \
  "v_or_b32 v232, s78, v230\n\t" \
  "s_mov_b32 s64, 65536\n\t" \
  "v_permlane16_swap_b32 v120, v122\n\t" \
  "v_permlane16_swap_b32 v121, v123\n\t" \
  "buffer_store_dwordx4 v[120:123], v232, s[96:99], s64 offen sc0 sc1\n\t" \
  "v_cmp_eq_u32 vcc, 48, v152\n\t" \
  "v_lshlrev_b32 v160, 16, v108\n\t" \
  "v_and_b32 v161, 0xffff0000, v108\n\t" \
  "v_lshlrev_b32 v162, 16, v109\n\t" \
  "v_and_b32 v163, 0xffff0000, v109\n\t" \
  "v_lshlrev_b32 v164, 16, v110\n\t" \
  "v_and_b32 v165, 0xffff0000, v110\n\t" \
  "v_lshlrev_b32 v166, 16, v111\n\t" \
  "v_and_b32 v167, 0xffff0000, v111\n\t" \
  "v_pk_fma_f32 v[160:161], v[144:145], v[160:161], v[12:13]\n\t" \
  "v_pk_fma_f32 v[162:163], v[146:147], v[162:163], v[14:15]\n\t" \
  "v_pk_fma_f32 v[164:165], v[148:149], v[164:165], v[28:29]\n\t" \
  "v_pk_fma_f32 v[166:167], v[150:151], v[166:167], v[30:31]\n\t" \
  "v_cndmask_b32 v231, v238, v155, vcc\n\t" \
  "v_cvt_pk_bf16_f32 v124, v160, v161\n\t" \
  "v_cvt_pk_bf16_f32 v126, v164, v165\n\t" \
  "v_cvt_pk_bf16_f32 v125, v162, v163\n\t" \
  "v_cvt_pk_bf16_f32 v127, v166, v167\n\t" \
  "v_or_b32 v232, s78, v231\n\t" \
  "s_mov_b32 s64, 98304\n\t" \
  "v_permlane16_swap_b32 v124, v126\n\t" \
  "v_permlane16_swap_b32 v125, v127\n\t" \
  "buffer_store_dwordx4 v[124:127], v232, s[96:99], s64 offen sc0 sc1\n\t" \
  STP("s_memrealtime s[90:91]\n\t") \
  STP("s_memtime s[82:83]\n\t") \
  STP("s_waitcnt lgkmcnt(0)\n\t") \
  STP("s_or_b32 s90, s90, 1\n\t") \
  STP("v_writelane_b32 %[stv], s90, 46\n\t") \
  STP("v_writelane_b32 %[stv], s82, 47\n\t") \
  "s_cmp_lg_u32 s84, 0\n\t" \
  "s_cbranch_scc1 .Ltl_oe\n\t" \
  "s_waitcnt vmcnt(4)\n\t" \
  "s_branch .Ltl_c1\n\t" \
  ".Ltl_s0:\n\t" \
  "s_sleep 1\n\t" \
  "buffer_load_dwordx4 v[208:211], v242, %[ws], s62 offen offset:0 sc1\n\t" \
  "buffer_load_dwordx4 v[212:215], v242, %[ws], s62 offen offset:1024 sc1\n\t" \
  "buffer_load_dwordx4 v[216:219], v242, %[ws], s62 offen offset:2048 sc1\n\t" \
  "buffer_load_dwordx4 v[220:223], v242, %[ws], s62 offen offset:3072 sc1\n\t" \
  "s_add_u32 s72, s72, 1\n\t" \
  "s_cmp_lt_u32 s72, %[cap]\n\t" \
  "s_waitcnt vmcnt(0)\n\t" \
  "s_cbranch_scc1 .Ltl_c0\n\t" \
  "buffer_store_dword v243, off, %[ws], 0 offset:256 sc1\n\t" \
  "s_branch .Ltl_k0\n\t" \
  ".Ltl_cd:\n\t" \
  "s_mov_b32 s74, s73\n\t" \
  "s_and_b32 s63, s73, 15\n\t" \
  "v_cmp_eq_u32 vcc, s63, v157\n\t" \
  "s_cmp_lt_u32 s73, 32\n\t" \
  "s_cbranch_scc1 .Ltl_d0\n\t" \
  "s_cmp_lt_u32 s73, 48\n\t" \
  "s_cbranch_scc1 .Ltl_e2\n\t" \
  ".Ltl_e3:\n\t" \
  "v_pk_fma_f32 v[96:97], v[12:13], v[214:215], v[214:215] op_sel:[0,0,1] op_sel_hi:[1,0,1]\n\t" \
  "v_pk_fma_f32 v[98:99], v[14:15], v[214:215], v[214:215] op_sel:[0,0,1] op_sel_hi:[1,0,1]\n\t" \
  "v_pk_fma_f32 v[100:101], v[28:29], v[214:215], v[214:215] op_sel:[0,0,1] op_sel_hi:[1,0,1]\n\t" \
  "v_pk_fma_f32 v[102:103], v[30:31], v[214:215], v[214:215] op_sel:[0,0,1] op_sel_hi:[1,0,1]\n\t" \
  "v_mov_b32 v120, v172\n\t" \
  "v_mov_b32 v122, v173\n\t" \
  "v_mov_b32 v124, v174\n\t" \
  "v_mov_b32 v126, v175\n\t" \
  "s_branch .Ltl_cs\n\t" \
  ".Ltl_e2:\n\t" \
  "v_pk_fma_f32 v[96:97], v[8:9], v[212:213], v[212:213] op_sel:[0,0,1] op_sel_hi:[1,0,1]\n\t" \
  "v_pk_fma_f32 v[98:99], v[10:11], v[212:213], v[212:213] op_sel:[0,0,1] op_sel_hi:[1,0,1]\n\t" \
  "v_pk_fma_f32 v[100:101], v[24:25], v[212:213], v[212:213] op_sel:[0,0,1] op_sel_hi:[1,0,1]\n\t" \
  "v_pk_fma_f32 v[102:103], v[26:27], v[212:213], v[212:213] op_sel:[0,0,1] op_sel_hi:[1,0,1]\n\t" \
  "v_mov_b32 v120, v168\n\t" \
  "v_mov_b32 v122, v169\n\t" \
  "v_mov_b32 v124, v170\n\t" \
  "v_mov_b32 v126, v171\n\t" \
  "s_branch .Ltl_cs\n\t" \
  ".Ltl_d0:\n\t" \
  "s_cmp_lt_u32 s73, 16\n\t" \
  "s_cbranch_scc1 .Ltl_e0\n\t" \
  ".Ltl_e1:\n\t" \
  "v_pk_fma_f32 v[96:97], v[4:5], v[210:211], v[210:211] op_sel:[0,0,1] op_sel_hi:[1,0,1]\n\t" \
  "v_pk_fma_f32 v[98:99], v[6:7], v[210:211], v[210:211] op_sel:[0,0,1] op_sel_hi:[1,0,1]\n\t" \
  "v_pk_fma_f32 v[100:101], v[20:21], v[210:211], v[210:211] op_sel:[0,0,1] op_sel_hi:[1,0,1]\n\t" \
  "v_pk_fma_f32 v[102:103], v[22:23], v[210:211], v[210:211] op_sel:[0,0,1] op_sel_hi:[1,0,1]\n\t" \
  "v_mov_b32 v120, v164\n\t" \
  "v_mov_b32 v122, v165\n\t" \
  "v_mov_b32 v124, v166\n\t" \
  "v_mov_b32 v126, v167\n\t" \
  "s_branch .Ltl_cs\n\t" \
  ".Ltl_e0:\n\t" \
  "v_pk_fma_f32 v[96:97], v[0:1], v[208:209], v[208:209] op_sel:[0,0,1] op_sel_hi:[1,0,1]\n\t" \
  "v_pk_fma_f32 v[98:99], v[2:3], v[208:209], v[208:209] op_sel:[0,0,1] op_sel_hi:[1,0,1]\n\t" \
  "v_pk_fma_f32 v[100:101], v[16:17], v[208:209], v[208:209] op_sel:[0,0,1] op_sel_hi:[1,0,1]\n\t" \
  "v_pk_fma_f32 v[102:103], v[18:19], v[208:209], v[208:209] op_sel:[0,0,1] op_sel_hi:[1,0,1]\n\t" \
  "v_mov_b32 v120, v160\n\t" \
  "v_mov_b32 v122, v161\n\t" \
  "v_mov_b32 v124, v162\n\t" \
  "v_mov_b32 v126, v163\n\t" \
  ".Ltl_cs:\n\t" \
  "v_pk_fma_f32 v[96:97], v[96:97], v[136:137], v[128:129]\n\t" \
  "v_pk_fma_f32 v[98:99], v[98:99], v[138:139], v[130:131]\n\t" \
  "v_pk_fma_f32 v[100:101], v[100:101], v[140:141], v[132:133]\n\t" \
  "v_pk_fma_f32 v[102:103], v[102:103], v[142:143], v[134:135]\n\t" \
  "v_cndmask_b32 v239, v156, v239, vcc\n\t" \
  "v_cvt_pk_bf16_f32 v112, v96, v97\n\t" \
  "v_cvt_pk_bf16_f32 v114, v98, v99\n\t" \
  "v_cvt_pk_bf16_f32 v116, v100, v101\n\t" \
  "v_cvt_pk_bf16_f32 v118, v102, v103\n\t" \
  "buffer_store_dwordx4 v[112:115], v239, %[ws], s66 offen offset:0 sc1\n\t" \
  "buffer_store_dwordx4 v[116:119], v239, %[ws], s66 offen offset:64 sc1\n\t" \
  "buffer_store_dwordx4 v[112:115], v239, %[ws], s77 offen offset:0 sc1\n\t" \
  "buffer_store_dwordx4 v[116:119], v239, %[ws], s77 offen offset:64 sc1\n\t" \
  "buffer_store_dwordx4 v[120:123], v239, %[ws], s67 offen offset:0 sc1\n\t" \
  "buffer_store_dwordx4 v[124:127], v239, %[ws], s67 offen offset:64 sc1\n\t" \
  "s_branch .Ltl_ce\n\t" \
  ".p2align 12\n\t" \
  ".Lht1:\n\t" \
  "s_cbranch_execz .Lht1n\n\t" \
  "s_waitcnt vmcnt(0)\n\t" \
  ".Ltl_c1:\n\t" \
  "v_cmp_eq_u32 vcc, %[tag], v217\n\t" \
  "v_xor_b32 v153, %[tag], v219\n\t" \
  "v_bitop3_b32 v153, v153, v221, %[tag] bitop3:0xf6\n\t" \
  "v_cndmask_b32 v152, 0, v216, vcc\n\t" \
  "v_bitop3_b32 v153, v153, v223, %[tag] bitop3:0xf6\n\t" \
  "v_bitop3_b32 v153, v153, v225, %[tag] bitop3:0xf6\n\t" \
  "v_max_u32_dpp v152, v152, v152 quad_perm:[1,0,3,2] row_mask:0xf bank_mask:0xf\n\t" \
  "s_or_b32 s77, vcc_lo, s68\n\t" \
  "v_cmp_ne_u32 vcc, 0, v153\n\t" \
  "v_max_u32_dpp v152, v152, v152 quad_perm:[2,3,0,1] row_mask:0xf bank_mask:0xf\n\t" \
  "s_nop 1\n\t" \
  "v_max_u32_dpp v152, v152, v152 row_half_mirror row_mask:0xf bank_mask:0xf\n\t" \
  "s_nop 1\n\t" \
  "v_max_u32_dpp v152, v152, v152 row_mirror row_mask:0xf bank_mask:0xf\n\t" \
  "s_nop 1\n\t" \
  "v_max_u32_dpp v152, v152, v152 row_bcast:15 row_mask:0xa bank_mask:0xf\n\t" \
  "s_nop 0\n\t" \
  "v_readlane_b32 s70, v152, 31\n\t" \
  "s_max_u32 s70, s70, s69\n\t" \
  "s_andn2_b32 s63, 0x780, s70\n\t" \
  "s_lshl_b32 s63, s63, 7\n\t" \
  "s_andn2_b32 s64, 64, s70\n\t" \
  "s_lshl_b32 s64, s64, 6\n\t" \
  "s_or_b32 s64, s64, s63\n\t" \
  "s_xor_b32 s63, s64, s71\n\t" \
  "s_mov_b32 s71, s64\n\t" \
  "s_orn2_b32 s63, s63, s77\n\t" \
  "s_or_b32 vcc_lo, vcc_lo, vcc_hi\n\t" \
  "s_or_b32 s63, s63, vcc_lo\n\t" \
  "s_cbranch_scc1 .Ltl_s1\n\t" \
  ".Ltl_k1:\n\t" \
  STP("s_memrealtime s[90:91]\n\t") \
  STP("s_memtime s[82:83]\n\t") \
  STP("s_waitcnt lgkmcnt(0)\n\t") \
  STP("s_or_b32 s90, s90, 1\n\t") \
  STP("v_writelane_b32 %[stv], s90, 48\n\t") \
  STP("v_writelane_b32 %[stv], s82, 49\n\t") \
  "s_cmp_eq_u32 s62, 6\n\t" \
  "s_cbranch_scc1 .Ltl_nf\n\t" \
  "v_dot2_f32_bf16 v96, v64, v218, 0\n\t" \
  "v_dot2_f32_bf16 v104, v241, v218, 0\n\t" \
  "v_dot2_f32_bf16 v97, v68, v218, 0\n\t" \
  "v_dot2_f32_bf16 v105, v180, v218, 0\n\t" \
  "v_dot2_f32_bf16 v98, v65, v218, 0\n\t" \
  "v_dot2_f32_bf16 v106, v176, v218, 0\n\t" \
  "v_dot2_f32_bf16 v99, v69, v218, 0\n\t" \
  "v_dot2_f32_bf16 v107, v181, v218, 0\n\t" \
  "v_dot2_f32_bf16 v100, v66, v218, 0\n\t" \
  "v_dot2_f32_bf16 v108, v177, v218, 0\n\t" \
  "v_dot2_f32_bf16 v101, v70, v218, 0\n\t" \
  "v_dot2_f32_bf16 v109, v182, v218, 0\n\t" \
  "v_dot2_f32_bf16 v102, v67, v218, 0\n\t" \
  "v_dot2_f32_bf16 v110, v178, v218, 0\n\t" \
  "v_dot2_f32_bf16 v103, v71, v218, 0\n\t" \
  "v_dot2_f32_bf16 v111, v183, v218, 0\n\t" \
  "v_dot2_f32_bf16 v96, v72, v220, v96\n\t" \
  "v_dot2_f32_bf16 v104, v179, v220, v104\n\t" \
  "v_dot2_f32_bf16 v97, v76, v220, v97\n\t" \
  "v_dot2_f32_bf16 v105, v188, v220, v105\n\t" \
  "v_dot2_f32_bf16 v98, v73, v220, v98\n\t" \
  "v_dot2_f32_bf16 v106, v184, v220, v106\n\t" \
  "v_dot2_f32_bf16 v99, v77, v220, v99\n\t" \
  "v_dot2_f32_bf16 v107, v189, v220, v107\n\t" \
  "v_dot2_f32_bf16 v100, v74, v220, v100\n\t" \
  "v_dot2_f32_bf16 v108, v185, v220, v108\n\t" \
  "v_dot2_f32_bf16 v101, v78, v220, v101\n\t" \
  "v_dot2_f32_bf16 v109, v190, v220, v109\n\t" \
  "v_dot2_f32_bf16 v102, v75, v220, v102\n\t" \
  "v_dot2_f32_bf16 v110, v186, v220, v110\n\t" \
  "v_dot2_f32_bf16 v103, v79, v220, v103\n\t" \
  "v_dot2_f32_bf16 v111, v191, v220, v111\n\t" \
  "v_dot2_f32_bf16 v96, v80, v222, v96\n\t" \
  "v_dot2_f32_bf16 v104, v187, v222, v104\n\t" \
  "v_dot2_f32_bf16 v97, v84, v222, v97\n\t" \
  "v_dot2_f32_bf16 v105, v196, v222, v105\n\t" \
  "v_dot2_f32_bf16 v98, v81, v222, v98\n\t" \
  "v_dot2_f32_bf16 v106, v192, v222, v106\n\t" \
  "v_dot2_f32_bf16 v99, v85, v222, v99\n\t" \
  "v_dot2_f32_bf16 v107, v197, v222, v107\n\t" \
  "v_dot2_f32_bf16 v100, v82, v222, v100\n\t" \
  "v_dot2_f32_bf16 v108, v193, v222, v108\n\t" \
  "v_dot2_f32_bf16 v101, v86, v222, v101\n\t" \
  "v_dot2_f32_bf16 v109, v198, v222, v109\n\t" \
  "v_dot2_f32_bf16 v102, v83, v222, v102\n\t" \
  "v_dot2_f32_bf16 v110, v194, v222, v110\n\t" \
  "v_dot2_f32_bf16 v103, v87, v222, v103\n\t" \
  "v_dot2_f32_bf16 v111, v199, v222, v111\n\t" \
  "v_dot2_f32_bf16 v96, v88, v224, v96\n\t" \
  "v_dot2_f32_bf16 v104, v195, v224, v104\n\t" \
  "v_dot2_f32_bf16 v97, v92, v224, v97\n\t" \
  "v_dot2_f32_bf16 v105, v204, v224, v105\n\t" \
  "v_dot2_f32_bf16 v98, v89, v224, v98\n\t" \
  "v_dot2_f32_bf16 v106, v200, v224, v106\n\t" \
  "v_dot2_f32_bf16 v99, v93, v224, v99\n\t" \
  "v_dot2_f32_bf16 v107, v205, v224, v107\n\t" \
  "v_dot2_f32_bf16 v100, v90, v224, v100\n\t" \
  "v_dot2_f32_bf16 v108, v201, v224, v108\n\t" \
  "v_dot2_f32_bf16 v101, v94, v224, v101\n\t" \
  "v_dot2_f32_bf16 v109, v206, v224, v109\n\t" \
  "v_dot2_f32_bf16 v102, v91, v224, v102\n\t" \
  "v_dot2_f32_bf16 v110, v202, v224, v110\n\t" \
  "v_dot2_f32_bf16 v103, v95, v224, v103\n\t" \
  "v_dot2_f32_bf16 v111, v207, v224, v111\n\t" \
  "v_permlane32_swap_b32 v96, v104\n\t" \
  "v_permlane32_swap_b32 v97, v105\n\t" \
  "v_permlane32_swap_b32 v98, v106\n\t" \
  "v_permlane32_swap_b32 v99, v107\n\t" \
  "v_permlane32_swap_b32 v100, v108\n\t" \
  "v_permlane32_swap_b32 v101, v109\n\t" \
  "v_permlane32_swap_b32 v102, v110\n\t" \
  "v_permlane32_swap_b32 v103, v111\n\t" \
  "v_pk_add_f32 v[96:97], v[96:97], v[104:105]\n\t" \
  "v_pk_add_f32 v[100:101], v[100:101], v[108:109]\n\t" \
  "v_pk_add_f32 v[98:99], v[98:99], v[106:107]\n\t" \
  "v_pk_add_f32 v[102:103], v[102:103], v[110:111]\n\t" \
  "v_permlane16_swap_b32 v96, v100\n\t" \
  "v_permlane16_swap_b32 v97, v101\n\t" \
  "v_permlane16_swap_b32 v98, v102\n\t" \
  "v_permlane16_swap_b32 v99, v103\n\t" \
  "v_add_f32 v96, v96, v100\n\t" \
  "v_add_f32 v97, v97, v101\n\t" \
  "v_add_f32 v98, v98, v102\n\t" \
  "v_add_f32 v99, v99, v103\n\t" \
  "v_add_f32_dpp v96, v96, v96 row_ror:8 row_mask:0xf bank_mask:0xf\n\t" \
  "v_add_f32_dpp v97, v97, v97 row_ror:8 row_mask:0xf bank_mask:0xf\n\t" \
  "v_add_f32_dpp v98, v98, v98 row_ror:8 row_mask:0xf bank_mask:0xf\n\t" \
  "v_add_f32_dpp v99, v99, v99 row_ror:8 row_mask:0xf bank_mask:0xf\n\t" \
  "v_add_f32_dpp v96, v96, v96 row_ror:4 row_mask:0xf bank_mask:0xf\n\t" \
  "v_add_f32_dpp v97, v97, v97 row_ror:4 row_mask:0xf bank_mask:0xf\n\t" \
  "v_add_f32_dpp v98, v98, v98 row_ror:4 row_mask:0xf bank_mask:0xf\n\t" \
  "v_add_f32_dpp v99, v99, v99 row_ror:4 row_mask:0xf bank_mask:0xf\n\t" \
  "v_add_f32_dpp v96, v96, v96 row_ror:2 row_mask:0xf bank_mask:0xf\n\t" \
  "v_add_f32_dpp v97, v97, v97 row_ror:2 row_mask:0xf bank_mask:0xf\n\t" \
  "v_add_f32_dpp v98, v98, v98 row_ror:2 row_mask:0xf bank_mask:0xf\n\t" \
  "v_add_f32_dpp v99, v99, v99 row_ror:2 row_mask:0xf bank_mask:0xf\n\t" \
  "ds_write_b128 v245, v[96:99] offset:8192\n\t" \
  "v_mov_b32 v169, v243\n\t" \
  "v_mov_b32 v171, v243\n\t" \
  "s_waitcnt lgkmcnt(0)\n\t" \
  "s_barrier\n\t" \
  "ds_read2st64_b32 v[96:97], v246 offset0:32 offset1:33\n\t" \
  "ds_read2st64_b32 v[98:99], v248 offset0:32 offset1:33\n\t" \
  "s_waitcnt lgkmcnt(0)\n\t" \
  "v_pk_add_f32 v[96:97], v[96:97], v[98:99]\n\t" \
  "s_nop 0\n\t" \
  "v_add_f32 v96, v96, v97\n\t" \
  "v_cvt_pk_bf16_f32 v96, v96, v96\n\t" \
  "v_lshlrev_b32 v97, 16, v96\n\t" \
  "v_lshlrev_b32 v96, 16, v96\n\t" \
  "s_nop 1\n\t" \
  "v_permlane16_swap_b32 v96, v97\n\t" \
  "v_mul_f32 v98, 0xbfb8aa3b, v96\n\t" \
  "v_exp_f32 v98, v98\n\t" \
  "s_nop 0\n\t" \
  "v_add_f32 v98, 1.0, v98\n\t" \
  "v_rcp_f32 v98, v98\n\t" \
  "s_nop 0\n\t" \
  "v_mul_f32 v96, v98, v96\n\t" \
  "v_cvt_pk_bf16_f32 v96, v96, v96\n\t" \
  "v_lshlrev_b32 v96, 16, v96\n\t" \
  "v_mul_f32 v96, v97, v96\n\t" \
  "s_nop 1\n\t" \
  "v_mov_b32_dpp v97, v96 quad_perm:[1,0,3,2] row_mask:0xf bank_mask:0xf\n\t" \
  "v_cvt_pk_bf16_f32 v96, v96, v97\n\t" \
  "s_nop 0\n\t" \
  "v_readlane_b32 s75, v96, 0\n\t" \
  "v_readlane_b32 s76, v96, 2\n\t" \
  "v_readlane_b32 s63, v96, 4\n\t" \
  "v_readlane_b32 s64, v96, 6\n\t" \
  "v_readlane_b32 s65, v96, 8\n\t" \
  "v_readlane_b32 s77, v96, 10\n\t" \
  "v_readlane_b32 s87, v96, 12\n\t" \
  "v_readlane_b32 s88, v96, 14\n\t" \
  "v_dot2_f32_bf16 v100, v240, s75, 0\n\t" \
  "v_dot2_f32_bf16 v101, v34, s75, 0\n\t" \
  "v_dot2_f32_bf16 v102, v32, s75, 0\n\t" \
  "v_dot2_f32_bf16 v103, v35, s75, 0\n\t" \
  "v_dot2_f32_bf16 v100, v33, s76, v100\n\t" \
  "v_dot2_f32_bf16 v101, v38, s76, v101\n\t" \
  "v_dot2_f32_bf16 v102, v36, s76, v102\n\t" \
  "v_dot2_f32_bf16 v103, v39, s76, v103\n\t" \
  "v_dot2_f32_bf16 v100, v37, s63, v100\n\t" \
  "v_dot2_f32_bf16 v101, v42, s63, v101\n\t" \
  "v_dot2_f32_bf16 v102, v40, s63, v102\n\t" \
  "v_dot2_f32_bf16 v103, v43, s63, v103\n\t" \
  "v_dot2_f32_bf16 v100, v41, s64, v100\n\t" \
  "v_dot2_f32_bf16 v101, v46, s64, v101\n\t" \
  "v_dot2_f32_bf16 v102, v44, s64, v102\n\t" \
  "v_dot2_f32_bf16 v103, v47, s64, v103\n\t" \
  "v_dot2_f32_bf16 v100, v45, s65, v100\n\t" \
  "v_dot2_f32_bf16 v101, v50, s65, v101\n\t" \
  "v_dot2_f32_bf16 v102, v48, s65, v102\n\t" \
  "v_dot2_f32_bf16 v103, v51, s65, v103\n\t" \
  "v_dot2_f32_bf16 v100, v49, s77, v100\n\t" \
  "v_dot2_f32_bf16 v101, v54, s77, v101\n\t" \
  "v_dot2_f32_bf16 v102, v52, s77, v102\n\t" \
  "v_dot2_f32_bf16 v103, v55, s77, v103\n\t" \
  "v_dot2_f32_bf16 v100, v53, s87, v100\n\t" \
  "v_dot2_f32_bf16 v101, v58, s87, v101\n\t" \
  "v_dot2_f32_bf16 v102, v56, s87, v102\n\t" \
  "v_dot2_f32_bf16 v103, v59, s87, v103\n\t" \
  "v_dot2_f32_bf16 v100, v57, s88, v100\n\t" \
  "v_dot2_f32_bf16 v101, v62, s88, v101\n\t" \
  "v_dot2_f32_bf16 v102, v60, s88, v102\n\t" \
  "v_dot2_f32_bf16 v103, v63, s88, v103\n\t" \
  "s_nop 0\n\t" \
  "v_cvt_pk_bf16_f32 v168, v100, v101\n\t" \
  "s_nop 0\n\t" \
  "v_cvt_pk_bf16_f32 v170, v102, v103\n\t" \
  "buffer_store_dwordx4 v[168:171], v247, %[ws], s85 offen sc1\n\t" \
  ".Ltl_nf:\n\t" \
  STP("s_memrealtime s[90:91]\n\t") \
  STP("s_memtime s[82:83]\n\t") \
  STP("s_waitcnt lgkmcnt(0)\n\t") \
  STP("s_or_b32 s90, s90, 1\n\t") \
  STP("v_writelane_b32 %[stv], s90, 50\n\t") \
  STP("v_writelane_b32 %[stv], s82, 51\n\t") \
  "s_cmp_lg_u32 s84, 0\n\t" \
  "s_cbranch_scc1 .Ltl_df\n\t" \
  "s_cmp_lg_u32 s78, 0\n\t" \
  "s_cbranch_scc1 .Ltl_o1\n\t" \
  "s_cmp_lt_u32 s74, 64\n\t" \
  "s_cbranch_scc1 .Ltl_o1\n\t" \
  ".Ltl_oe:\n\t" \
  "s_or_b32 s63, %[bid], %[wid]\n\t" \
  "s_cbranch_scc0 .Ltl_ep\n\t" \
  ".Ltl_ee:\n\t" \
  "s_cmp_eq_u32 s62, 6\n\t" \
  "s_cbranch_scc0 .Ltl_end\n\t" \
  "s_lshl_b32 s77, s79, 9\n\t" \
  "s_lshl_b32 s63, s80, 7\n\t" \
  "s_add_u32 s77, s77, s63\n\t" \
  "v_lshl_add_u32 v37, v157, 3, s77\n\t" \
  "s_add_u32 s63, s71, s61\n\t" \
  "s_add_u32 s63, s63, 262144\n\t" \
  "v_add_u32 v33, s63, v37\n\t" \
  "s_andn2_b32 s63, 0x700, s70\n\t" \
  "s_lshl_b32 s63, s63, 6\n\t" \
  "s_andn2_b32 s64, 0xc0, s70\n\t" \
  "s_lshl_b32 s64, s64, 5\n\t" \
  "s_add_u32 s63, s63, s64\n\t" \
  "s_add_u32 s63, s63, s61\n\t" \
  "s_add_u32 s63, s63, 1966080\n\t" \
  "v_and_b32 v34, 0x7ff, v37\n\t" \
  "v_add_u32 v34, s63, v34\n\t" \
  "s_and_b32 s77, s77, 0x7ff\n\t" \
  "s_add_u32 s77, s77, s61\n\t" \
  "s_lshl_b32 s63, %[wid], 15\n\t" \
  "s_add_u32 s77, s77, s63\n\t" \
  "v_and_b32 v36, 0x70, v242\n\t" \
  "v_lshl_add_u32 v32, v154, 14, v36\n\t" \
  "v_and_b32 v35, 24, v244\n\t" \
  "v_lshl_add_u32 v32, v35, 8, v32\n\t" \
  "s_add_u32 s60, s77, 1048576\n\t" \
  "s_add_u32 s65, s77, 1179648\n\t" \
  "s_add_u32 s66, s77, 1310720\n\t" \
  "s_add_u32 s67, s77, 1441792\n\t" \
  "s_add_u32 s68, s77, 1572864\n\t" \
  "s_add_u32 s73, s77, 1703936\n\t" \
  "s_add_u32 s74, s77, 1835008\n\t" \
  "s_lshl_b32 s63, s80, 12\n\t" \
  "s_add_u32 s63, s63, 147456\n\t" \
  "v_lshl_add_u32 v40, v157, 3, s63\n\t" \
  "ds_read_b64 v[40:41], v40 offset:2944\n\t" \
  "s_ashr_i32 s63, s70, 31\n\t" \
  "s_orn2_b32 s63, 0x80000000, s63\n\t" \
  "s_xor_b32 s63, s63, s70\n\t" \
  "s_and_b32 s63, s63, 0xffff0000\n\t" \
  "v_mov_b32 v38, 0xbfb8aa3b\n\t" \
  "v_mul_f32 v38, s63, v38\n\t" \
  "v_exp_f32 v38, v38\n\t" \
  "v_lshrrev_b32 v37, 1, v37\n\t" \
  "v_add_f32 v38, 1.0, v38\n\t" \
  "v_rcp_f32 v38, v38\n\t" \
  "s_andn2_b32 s64, 0x7ff, s70\n\t" \
  "s_lshl_b32 s64, s64, 11\n\t" \
  "v_cvt_pk_bf16_f32 v38, v38, v38\n\t" \
  "v_lshlrev_b32 v38, 16, v38\n\t" \
  "v_lshrrev_b32 v153, 4, v244\n\t" \
  "v_lshl_add_u32 v36, v153, 2, v36\n\t" \
  "v_lshl_add_u32 v36, %[wid], 7, v36\n\t" \
  "v_and_b32 v35, 8, v244\n\t" \
  "v_lshl_add_u32 v36, v35, 7, v36\n\t" \
  "v_lshlrev_b32 v35, 3, v157\n\t" \
  "v_add_u32 v35, 0x3000, v35\n\t" \
  "v_min_u32 v153, 1, v153\n\t" \
  "v_lshl_or_b32 v37, v153, 31, v37\n\t" \
  "s_mov_b32 s75, 0x3f80\n\t" \
  "s_mov_b32 s76, 0x3f800000\n\t" \
  "s_mov_b32 s72, 0\n\t" \
  "buffer_load_dwordx2 v[42:43], v33, %[ws], 0 offen sc1\n\t" \
  "buffer_load_dwordx2 v[44:45], v34, %[ws], 0 offen sc1\n\t" \
  "buffer_load_dwordx4 v[0:3], v32, %[ws], s60 offen sc1\n\t" \
  "buffer_load_dwordx4 v[4:7], v32, %[ws], s65 offen sc1\n\t" \
  "buffer_load_dwordx4 v[8:11], v32, %[ws], s66 offen sc1\n\t" \
  "buffer_load_dwordx4 v[12:15], v32, %[ws], s67 offen sc1\n\t" \
  "buffer_load_dwordx4 v[16:19], v32, %[ws], s68 offen sc1\n\t" \
  "buffer_load_dwordx4 v[20:23], v32, %[ws], s73 offen sc1\n\t" \
  "buffer_load_dwordx4 v[24:27], v32, %[ws], s74 offen sc1\n\t" \
  "s_waitcnt vmcnt(0)\n\t" \
  ".Ltl_c2:\n\t" \
  "v_xor_b32 v152, %[tag], v43\n\t" \
  "v_bitop3_b32 v152, v152, v45, %[tag] bitop3:0xf6\n\t" \
  "v_bitop3_b32 v152, v152, v1, %[tag] bitop3:0xf6\n\t" \
  "v_bitop3_b32 v152, v152, v3, %[tag] bitop3:0xf6\n\t" \
  "v_bitop3_b32 v152, v152, v5, %[tag] bitop3:0xf6\n\t" \
  "v_bitop3_b32 v152, v152, v7, %[tag] bitop3:0xf6\n\t" \
  "v_bitop3_b32 v152, v152, v9, %[tag] bitop3:0xf6\n\t" \
  "v_bitop3_b32 v152, v152, v11, %[tag] bitop3:0xf6\n\t" \
  "v_bitop3_b32 v152, v152, v13, %[tag] bitop3:0xf6\n\t" \
  "v_bitop3_b32 v152, v152, v15, %[tag] bitop3:0xf6\n\t" \
  "v_bitop3_b32 v152, v152, v17, %[tag] bitop3:0xf6\n\t" \
  "v_bitop3_b32 v152, v152, v19, %[tag] bitop3:0xf6\n\t" \
  "v_bitop3_b32 v152, v152, v21, %[tag] bitop3:0xf6\n\t" \
  "v_bitop3_b32 v152, v152, v23, %[tag] bitop3:0xf6\n\t" \
  "v_bitop3_b32 v152, v152, v25, %[tag] bitop3:0xf6\n\t" \
  "v_bitop3_b32 v152, v152, v27, %[tag] bitop3:0xf6\n\t" \
  "v_cmp_ne_u32 vcc, 0, v152\n\t" \
  "s_cbranch_vccnz .Ltl_s2\n\t" \
  ".Ltl_k2:\n\t" \
  "v_dot2_f32_bf16 v48, v0, s75, 0\n\t" \
  "v_dot2_f32_bf16 v49, v0, s76, 0\n\t" \
  "v_dot2_f32_bf16 v50, v2, s75, 0\n\t" \
  "v_dot2_f32_bf16 v51, v2, s76, 0\n\t" \
  "v_dot2_f32_bf16 v48, v4, s75, v48\n\t" \
  "v_dot2_f32_bf16 v49, v4, s76, v49\n\t" \
  "v_dot2_f32_bf16 v50, v6, s75, v50\n\t" \
  "v_dot2_f32_bf16 v51, v6, s76, v51\n\t" \
  "v_dot2_f32_bf16 v48, v8, s75, v48\n\t" \
  "v_dot2_f32_bf16 v49, v8, s76, v49\n\t" \
  "v_dot2_f32_bf16 v50, v10, s75, v50\n\t" \
  "v_dot2_f32_bf16 v51, v10, s76, v51\n\t" \
  "v_dot2_f32_bf16 v48, v12, s75, v48\n\t" \
  "v_dot2_f32_bf16 v49, v12, s76, v49\n\t" \
  "v_dot2_f32_bf16 v50, v14, s75, v50\n\t" \
  "v_dot2_f32_bf16 v51, v14, s76, v51\n\t" \
  "v_dot2_f32_bf16 v48, v16, s75, v48\n\t" \
  "v_dot2_f32_bf16 v49, v16, s76, v49\n\t" \
  "v_dot2_f32_bf16 v50, v18, s75, v50\n\t" \
  "v_dot2_f32_bf16 v51, v18, s76, v51\n\t" \
  "v_dot2_f32_bf16 v48, v20, s75, v48\n\t" \
  "v_dot2_f32_bf16 v49, v20, s76, v49\n\t" \
  "v_dot2_f32_bf16 v50, v22, s75, v50\n\t" \
  "v_dot2_f32_bf16 v51, v22, s76, v51\n\t" \
  "v_dot2_f32_bf16 v48, v24, s75, v48\n\t" \
  "v_dot2_f32_bf16 v49, v24, s76, v49\n\t" \
  "v_dot2_f32_bf16 v50, v26, s75, v50\n\t" \
  "v_dot2_f32_bf16 v51, v26, s76, v51\n\t" \
  "s_nop 1\n\t" \
  "v_permlane32_swap_b32 v48, v50\n\t" \
  "v_permlane32_swap_b32 v49, v51\n\t" \
  "v_add_f32 v48, v48, v50\n\t" \
  "v_add_f32 v49, v49, v51\n\t" \
  "v_and_b32 v43, 0xffff0000, v42\n\t" \
  "v_lshlrev_b32 v42, 16, v42\n\t" \
  "v_permlane16_swap_b32 v48, v49\n\t" \
  "v_add_f32 v48, v48, v49\n\t" \
  "v_and_b32 v45, 0xffff0000, v44\n\t" \
  "v_lshlrev_b32 v44, 16, v44\n\t" \
  "v_add_f32_dpp v48, v48, v48 row_ror:8 row_mask:0xf bank_mask:0xf\n\t" \
  "ds_write_b32 v36, v48 offset:12288\n\t" \
  "s_cmp_lg_u32 %[wid], 0\n\t" \
  "s_waitcnt lgkmcnt(0)\n\t" \
  "s_barrier\n\t" \
  "s_cbranch_scc1 .Ltl_end\n\t" \
  "ds_read2_b64 v[0:3], v35 offset1:16\n\t" \
  "ds_read2_b64 v[4:7], v35 offset0:32 offset1:48\n\t" \
  "s_waitcnt lgkmcnt(1)\n\t" \
  "v_pk_add_f32 v[0:1], v[0:1], v[2:3]\n\t" \
  "s_waitcnt lgkmcnt(0)\n\t" \
  "v_pk_add_f32 v[4:5], v[4:5], v[6:7]\n\t" \
  "s_nop 0\n\t" \
  "v_pk_add_f32 v[0:1], v[0:1], v[4:5]\n\t" \
  "s_nop 0\n\t" \
  "v_cvt_pk_bf16_f32 v0, v0, v1\n\t" \
  "v_and_b32 v1, 0xffff0000, v0\n\t" \
  "v_lshlrev_b32 v0, 16, v0\n\t" \
  "v_mul_f32 v0, v38, v0\n\t" \
  "v_mul_f32 v1, v38, v1\n\t" \
  "v_cvt_pk_bf16_f32 v0, v0, v1\n\t" \
  "v_and_b32 v1, 0xffff0000, v0\n\t" \
  "v_lshlrev_b32 v0, 16, v0\n\t" \
  "v_add_f32 v0, v0, v42\n\t" \
  "v_add_f32 v1, v1, v43\n\t" \
  "v_cvt_pk_bf16_f32 v0, v0, v1\n\t" \
  "v_and_b32 v1, 0xffff0000, v0\n\t" \
  "v_lshlrev_b32 v0, 16, v0\n\t" \
  "v_mul_f32 v0, v0, v40\n\t" \
  "v_mul_f32 v1, v1, v41\n\t" \
  "v_cvt_pk_bf16_f32 v0, v0, v1\n\t" \
  "v_and_b32 v1, 0xffff0000, v0\n\t" \
  "v_lshlrev_b32 v0, 16, v0\n\t" \
  "v_add_f32 v0, v0, v44\n\t" \
  "v_add_f32 v1, v1, v45\n\t" \
  "v_cvt_pk_bf16_f32 v0, v0, v1\n\t" \
  "buffer_store_dword v0, v37, s[96:99], s64 offen sc0 sc1\n\t" \
  "s_branch .Ltl_end\n\t" \
  ".Ltl_s1:\n\t" \
  "s_sleep 1\n\t" \
  "buffer_load_dwordx2 v[216:217], v235, %[ws], 0 offen sc1\n\t" \
  "buffer_load_dwordx4 v[218:221], v236, %[ws], s71 offen sc1\n\t" \
  "buffer_load_dwordx4 v[222:225], v236, %[ws], s71 offen offset:16 sc1\n\t" \
  "s_add_u32 s72, s72, 1\n\t" \
  "s_cmp_lt_u32 s72, %[cap]\n\t" \
  "s_waitcnt vmcnt(0)\n\t" \
  "s_cbranch_scc1 .Ltl_c1\n\t" \
  "buffer_store_dword v243, off, %[ws], 0 offset:256 sc1\n\t" \
  "s_branch .Ltl_k1\n\t" \
  ".Ltl_o1:\n\t" \
  "s_cmp_eq_u32 s70, s69\n\t" \
  "s_cselect_b32 s63, 0x80000000, 0\n\t" \
  "s_xor_b32 s77, s78, 0x80000000\n\t" \
  "v_or_b32 v152, s63, v238\n\t" \
  "v_or_b32 v153, s77, v238\n\t" \
  "v_cmp_gt_i32 vcc, 0, v228\n\t" \
  "s_mov_b32 s64, 0\n\t" \
  "s_nop 0\n\t" \
  "v_cndmask_b32 v228, v153, v152, vcc\n\t" \
  "buffer_store_dwordx4 v[112:115], v228, s[96:99], s64 offen sc0 sc1\n\t" \
  "v_cmp_gt_i32 vcc, 0, v229\n\t" \
  "s_mov_b32 s64, 32768\n\t" \
  "s_nop 0\n\t" \
  "v_cndmask_b32 v229, v153, v152, vcc\n\t" \
  "buffer_store_dwordx4 v[116:119], v229, s[96:99], s64 offen sc0 sc1\n\t" \
  "v_cmp_gt_i32 vcc, 0, v230\n\t" \
  "s_mov_b32 s64, 65536\n\t" \
  "s_nop 0\n\t" \
  "v_cndmask_b32 v230, v153, v152, vcc\n\t" \
  "buffer_store_dwordx4 v[120:123], v230, s[96:99], s64 offen sc0 sc1\n\t" \
  "v_cmp_gt_i32 vcc, 0, v231\n\t" \
  "s_mov_b32 s64, 98304\n\t" \
  "s_nop 0\n\t" \
  "v_cndmask_b32 v231, v153, v152, vcc\n\t" \
  "buffer_store_dwordx4 v[124:127], v231, s[96:99], s64 offen sc0 sc1\n\t" \
  "s_branch .Ltl_oe\n\t" \
  ".Ltl_df:\n\t" \
  "s_mov_b32 s78, 0\n\t" \
  "s_cmp_eq_u32 s70, s69\n\t" \
  "s_cselect_b32 s74, s74, 0x100\n\t" \
  "s_branch .Ltl_h2\n\t" \
  ".Ltl_ep:\n\t" \
  "s_mov_b32 s63, 2490368\n\t" \
  "buffer_store_dword v243, v249, %[ws], s63 offen sc1\n\t" \
  "s_branch .Ltl_ee\n\t" \
  ".Ltl_s2:\n\t" \
  "s_sleep 1\n\t" \
  "buffer_load_dwordx2 v[42:43], v33, %[ws], 0 offen sc1\n\t" \
  "buffer_load_dwordx2 v[44:45], v34, %[ws], 0 offen sc1\n\t" \
  "buffer_load_dwordx4 v[0:3], v32, %[ws], s60 offen sc1\n\t" \
  "buffer_load_dwordx4 v[4:7], v32, %[ws], s65 offen sc1\n\t" \
  "buffer_load_dwordx4 v[8:11], v32, %[ws], s66 offen sc1\n\t" \
  "buffer_load_dwordx4 v[12:15], v32, %[ws], s67 offen sc1\n\t" \
  "buffer_load_dwordx4 v[16:19], v32, %[ws], s68 offen sc1\n\t" \
  "buffer_load_dwordx4 v[20:23], v32, %[ws], s73 offen sc1\n\t" \
  "buffer_load_dwordx4 v[24:27], v32, %[ws], s74 offen sc1\n\t" \
  "s_add_u32 s72, s72, 1\n\t" \
  "s_cmp_lt_u32 s72, %[cap]\n\t" \
  "s_waitcnt vmcnt(0)\n\t" \
  "s_cbranch_scc1 .Ltl_c2\n\t" \
  "buffer_store_dword v243, off, %[ws], 0 offset:256 sc1\n\t" \
  "s_branch .Ltl_k2\n\t" \
  ".Ltl_end:\n\t" \
  ""
#define TL_CLOB "v208", "v209", "v210", "v211", "v212", "v213", "v214", "v215", "v216", "v217", "v218", "v219", "v220", "v221", "v222", "v223", "v224", "v225", "v226", "v227", "v228", "v229", "v230", "v231", "v232", "v233", "v234", "v235", "v236", "v238", "v239", "v245", "v246", "v247", "v248", "v249", "v250", "s60", "s61", "s62", "s63", "s64", "s65", "s66", "s67", "s68", "s69", "s70", "s71", "s72", "s73", "s74", "s75", "s76", "s77", "s78", "s79", "s80", "s81", "s82", "s83", "s84", "s85", "s90", "s91", "s96", "s97", "s98", "s99", "vcc", "scc", "memory"
static_assert(W_EPOCH == 2490368 && W_GKEY == 65536 && W_CANDH == 262144 && W_STAT == 524288 && W_YG == 1048576 && W_CANDX == 1966080);
struct WoP { srd_t srd; unsigned wv[2], m, hl, base, wa; };
struct W4C {
  KVP kv;
  unsigned pf1, pf2, pf4, cto, po, px, vo, eso, hl, hl18, frb, xcb, xso, ss, mbp, gvo, l16, gvm, vs, c3;
  WoP wo; u16* out;
  srd_t sG, sU, sD, sX, sR, sM;
  unsigned gv, wa0, fw2, tagv, fbv, tlb, pre, otb, na, gra, fl; u32x32 q[4], up; f32x32 wm; u32x16 o3;
};
struct Ptrs { const u16 *wo, *w_r, *wg, *wu, *wd; u16* out; };
DI void attn_setup(const Ctx& k, W4C& c, const u16* kc, const u16* vc, const u16* pm, srd_t srdx, const Ptrs& a) {
  const unsigned wid = k.wid, fb = k.xcd * 28 + ffn_j(k);
  c.kv.sofk = KRS * 16u * wid;
  const size_t kvoff = ((size_t)k.kvh * SKV + (SKV - WIN)) * DH;
  c.kv.srdK = mk_srd(kc + kvoff, WIN * DH * 2); c.kv.srdV = mk_srd(vc + kvoff, WIN * DH * 2);
  const unsigned go = wid * 256 * FF + fb * 16, nG = (DM * FF - go) * 2;
  c.sG = mk_srd(a.wg + go, nG); c.sU = mk_srd(a.wu + go, nG);
  c.sM = mk_srd(pm + 2048 + k.xcd * 512, DM * 6 * DM * 2 - 4096 - k.xcd * 1024);
  c.wo.srd = mk_srd(a.wo, QD * DM * 2); c.sX = srdx;
  c.sD = mk_srd(a.wd + (size_t)fb * 16 * DM, ffn_idle(k) ? 0u : 32768u); c.sR = mk_srd(a.w_r, DM * 2);
  c.out = a.out;
  asm volatile("" : "+s"(c.out), "+s"(c.kv.srdK), "+s"(c.kv.srdV), "+s"(c.sG), "+s"(c.sU), "+s"(c.sM), "+s"(c.wo.srd), "+s"(c.sD), "+s"(c.sR));
  c.wo.m = wid * 4096; c.wo.base = (k.kvh * 1024 + k.hl * 128 + wid * 16) * (DM * 2) + k.hl * 256;
  c.pf1 = (k.kvh * 1024 + k.j * 32 + wid * 8) * (DM * 2) + (k.xcd & 1) * 8192;
  c.pf4 = k.j * (8 * FF * 2) - ffn_j(k) * 32;
}
DI void attention(Ctx& k, H4& g, W4C& c STAMP_ARG) {
  unsigned nb;
  asm volatile(AT_ASM
               : "+{v[0:31]}"(g.q[0]), "+{v[32:63]}"(g.q[1]), "+{v[64:95]}"(g.q[2]), "+{v[96:127]}"(g.q[3]), [pfd] "+{v246}"(g.pfd), "=&{v[128:159]}"(c.wm), "=&{v[160:175]}"(c.o3), "+{v224}"(c.kv.vksrc[0]), "+{v225}"(c.kv.vksrc[1]), "+{v226}"(c.kv.vksrc[2]), "+{v227}"(c.kv.vksrc[3]), "=&{v[176:207]}"(c.up),
                 "=&{v234}"(c.wo.wa), "=&{v213}"(c.gra), "+{v245}"(g.fvo), "=&{v243}"(c.tagv), "=&{v254}"(c.fbv),
                 "=&{v242}"(c.l16), "+{v247}"(c.tlb), "=&{v252}"(c.vs), [na] "=&s"(c.na), [nb] "=&s"(nb), [eso] "+s"(c.eso), [xcb] "+s"(c.xcb), [frb] "+s"(c.frb), [fw2] "+s"(c.fw2) AT_STV
               : "{v220}"(c.kv.vkb[0]), "{v221}"(c.kv.vkb[1]), "{v222}"(c.kv.vkb[2]), "{v223}"(c.kv.vkb[3]),
                 "{v228}"(c.kv.vvb[0]), "{v229}"(c.kv.vvb[1]), "{v230}"(c.kv.vvsrc[0]), "{v231}"(c.kv.vvsrc[1]), "{v232}"(c.gv), "{v233}"(c.gvm), "{v240}"(c.wo.wv[0]), "{v241}"(c.wo.wv[1]), "{v244}"(c.wa0), "{v248}"(c.c3),
                 [ws] "s"(k.ws), [sK] "s"(c.kv.srdK), [sV] "s"(c.kv.srdV), [sG] "s"(c.sG), [sU] "s"(c.sU), [wo] "s"(c.wo.srd), [sM] "s"(c.sM), [sD] "s"(c.sD), [sR] "s"(c.sR),
                 [m] "s"(c.wo.m), [sofk] "s"(c.wo.m), [hl18] "s"(c.hl18), [wob] "s"(c.wo.base), [pf2] "s"(c.pf2), AT_PF1 [pf4] "s"(c.pf4),
                 [tag] "s"(k.tag), [cap] "i"(kSpinCap), [cto] "s"(c.cto), [po] "s"(c.po), [px] "s"(c.px), [vo] "s"(c.vo), [pm] "s"(k.pm), [ss] "s"(c.ss), [gvo] "s"(c.gvo)
               : AT_CLOB, ACLOB_LO, ACLOB_HI);
  k.nb = nb; c.fl = g.fvo;
#pragma unroll
  for (int i = 0; i < 4; ++i) c.q[i] = g.q[i];
  GAP();
}
constexpr unsigned SCR = 9 * UNIT;
#define STAMPF(I) do { } while (0)
#define FQ_STR2(x) #x
#define FQ_STR(x) FQ_STR2(x)
#define FQ_CAP FQ_STR(SPINCAP)
DI bool tags2(const u32x4& a, unsigned tag) { return (a[1] == tag) & (a[3] == tag); }
DI void bcast4(float v, float (&o)[4]) {
  auto a = __builtin_amdgcn_permlane32_swap(__float_as_uint(v), __float_as_uint(v), false, false);
  auto b = __builtin_amdgcn_permlane16_swap(a[0], a[0], false, false);
  auto c = __builtin_amdgcn_permlane16_swap(a[1], a[1], false, false);
  o[0] = __uint_as_float(b[0]); o[1] = __uint_as_float(b[1]); o[2] = __uint_as_float(c[0]); o[3] = __uint_as_float(c[1]);
}
DI void x_raw2(unsigned m0, srd_t srd, unsigned v0, unsigned v1, unsigned so) {
  asm volatile("s_mov_b32 m0, %0\n\ts_nop 0\n\tbuffer_load_dwordx4 %1, %3, %4 offen lds\n\tbuffer_load_dwordx4 %2, %3, %4 offen offset:1024 lds"
               :: "s"(m0), "v"(v0), "v"(v1), "s"(srd), "s"(so) : "memory");
}
DI void w_touch2(H4& g, const u16* wq, unsigned base, unsigned t) {
  touch(g.pfd, wq, base + ((g.rot + t) & 15u) * kWT);
}
constexpr unsigned TB0 = 6, TBN = 6;
#define SE 0
#define F1 0
#define FT 0
#define MK 0
#define PGA 0
#define WT 0
#define LT 0
#define EL 0
#define PT0 0
#define XH 0
#define S3 0
#define X1L 0
#define PBK 1
#define IL 1
#define PPOS 2
DI void front(Ctx& k, H4& g, W4C& c STAMP_ARG) {
  argp_t ap = args();
  const u16 *px = ap->x, *kc = ap->kc, *vc = ap->vc, *wqp = ap->wq, *pc_ = ap->c, *pm = ap->w_mod; char* pw = ap->ws;
  GAP();
  const unsigned lane = lane_id(), wid = k.wid, tid = wid * 64 + lane;
  g.pfd = 0;
  {
    unsigned long long pc; unsigned len;
    asm volatile("s_getpc_b64 %0\n.Lfz_a:\n\ts_mov_b32 %1, .Lfz_z-.Lfz_a-4" : "=s"(pc), "=s"(len));
    touch(g.pfd, (const void*)pc, min(((lane * 4 + wid) * 32 + k.j) * 128, len));
  }
  GAP();
  k.ws = mk_srd((char*)(((uintptr_t)pw + 0x1fffff) & ~(uintptr_t)0x1fffff), W_END);
  const srd_t srdx = mk_srd(px, S * DM * 2), srdm = mk_srd(pm, DM * 6 * DM * 2), srdc = mk_srd(pc_, DM * 2);
  asm volatile("" : "+s"(kc), "+s"(vc));
  const unsigned head = k.kvh * 8 + k.hl, row64 = k.rblk * 256 + k.kvh * 64, rot = k.kvh * 4, u0 = k.hl >> 1, kt = rot + ((u0 + wid) & 3), k0 = k.hl & 1, cq = k.kvh ^ 3;
  g.srdW = mk_srd(wqp + head * DH, (DM * QD - head * DH) * 2); g.rot = rot;
  u32x32 xa;
  const unsigned st1 = W_ST1 + (k.rblk << 12);
  u32x4 xs[4], wm[4];
  const unsigned cbk = (k.xcd & 1) * 32 + k.j, kp = k.kvh;
  const unsigned s3 = S3 && (k.j & 14) == 6, od = s3 & k.j, rg = tid >> (2 + s3), cm = 3 + 4 * s3;
  u32x2 cv = ald8<0>(srdc, rg * 8, kp * 512 + od * 256);
  { const unsigned vo = rg * (4 * 6 * DM * 2) + (tid & cm) * 16, so = kp * (256 * 6 * DM * 2) + cbk * 64 + od * (128 * 6 * DM * 2 - 64);
#pragma unroll
    for (int i = 0; i < 4; ++i) wm[i] = ald16<0>(srdm, vo, so + i * (6 * DM * 2)); }
  unsigned E = ald4<0, true>(k.ws, EL == 0 || lane == 0 ? W_EPOCH : lane == 1 ? W_HT + ((k.rblk & 3) << 20) : lane == 2 ? st1 + (k.kvh * 8 + k.hl) * 128 : W_FLAGH + (k.rblk * 4 + wid) * 128, 0u);
  { const unsigned so = (row64 + k.hl * 8 + 2 * wid) * (DM * 2);
    xs[0] = ald16<0>(srdx, lane * 16, so); xs[1] = ald16<1024>(srdx, lane * 16, so); xs[2] = ald16<2048>(srdx, lane * 16, so); xs[3] = ald16<3072>(srdx, lane * 16, so); }
  unsigned vx = (lane >> 3) * (DM * 2) + (((lane & 7) ^ (lane >> 4)) << 4), vx1 = (vx ^ 64) + 15360, sl[4];
  g.mX = k.ldsb + wid * 8192;
#pragma unroll
  for (int u = 0; u < 4; ++u) sl[u] = (k.rblk * 256 + wid * 16) * (DM * 2) + (rot + ((u0 + u) & 3)) * 128;
#define XRAW(u, rb) x_raw2(g.mX + (u) * GSLOT + (rb) * 2048, srdx, vx, vx1, sl[u] + (((rb) ^ cq) << 17))
  const unsigned hx = XH ? 2 * k0 : 0;
  if (!(LT & 2)) { XRAW(0, hx); XRAW(0, hx + 1); }
  const unsigned tw2 = (k.j * 2 + (wid >> 1)) * (QD * 2) + k.kvh * 2048 + (lane & 7) * 256 + (wid & 1) * 128;
  if (!(LT & 1)) w_touch2(g, wqp, tw2, (lane >> 3) & 3);
  constexpr int NL = (LT & 1) + 2 * (LT & 2);
  GAP();
  g.mW = k.ldsb + 32768 + wid * 4096;
#define X1() do { XRAW(1, 0); XRAW(1, 1); XRAW(1, 2); XRAW(1, 3); } while (0)
#define X01() do { XRAW(0, 2 - hx); XRAW(0, 3 - hx); if (!X1L) X1(); } while (0)
#define X2() do { XRAW(2, 0); XRAW(2, 1); XRAW(2, 2); XRAW(2, 3); } while (0)
  const unsigned tb = k.ldsb + SCR + 1024;
  u32x16 wbs; u32x8 vw, ma; u32x4 mb; const u16* kvt; unsigned pso, fw, w2, w3, gso, gpo;
  auto setup_w = [&]() {
    w_voffs(g.voffW, QD, wid, lane, k0);
    gso = st1 + (wid * 16 + (lane & 15)) * 16; gpo = W_MODA + ((lane >> 5) * 1024 + kt * 64 + (lane & 31) * 2) * 8;
    asm volatile("" : W4(g.voffW), "+v"(gso), "+v"(gpo));
    GAP();
  };
  auto setup_b = [&]() {
    g.voffX = wid * 8192 + ((lane >> 4) ^ cq) * 256 + (lane & 15) * 16; g.soX = W_HT + ((k.rblk & 3) << 20) + (((k.xcd ^ wid) & 1) << 13) + wid * 8192; g.fo = W_FLAGH + (k.rblk * 4 + wid) * 128;
    h4_init_addr(g, k.ldsb, wid, lane, k0);
    const unsigned xr = g.mX + (lane & 15) * 128 + (((4 * k0 + (lane >> 4)) ^ ((lane >> 1) & 7)) << 4);
    vw = u32x8{g.voffW[0], g.voffW[1], g.voffW[2], g.voffW[3], xr, xr ^ 64, xr + 2 * GSLOT, (xr ^ 64) + 2 * GSLOT};
#pragma unroll
    for (int i = 0; i < 8; ++i) { wbs[i] = g.wb[0][i]; wbs[8 + i] = g.wb[1][i]; }
    ma = u32x8{tb + (lane >> 4) * 32, g.voffX, (lane & 31) * 4, 0u, vx, vx1, g.xb1,
               WT == 1 ? tw2 + ((rot + 4 + (k.xcd & 1) + 2 * min(lane >> 3, 5u)) & 15u) * kWT : WT == 2 ? tw2 + ((rot + 4 + (k.xcd & 1) + 2 * min(lane >> 3, 2u)) & 15u) * kWT
                       : tw2 - (lane & 7) * 256 + (1 + 4 * (lane & 1)) * 256 + ((rot + 4 + min(lane >> 1, 11u)) & 15u) * kWT};
    mb = u32x4{WT == 2 ? tw2 + ((rot + 10 + (k.xcd & 1) + 2 * min(lane >> 3, 2u)) & 15u) * kWT : (tw2 & ~(((lane & 3) == 1) ? 256u : 0u)) + ((rot + TB0 + min(lane >> 3, TBN - 1u)) & 15u) * kWT, ((k.j & 15) * 64 + wid * 16 + (lane & 15)) * 128, 0u, W_SIL + k.bid * 512 + tid * 2};
    kvt = ((k.j & 16) ? vc : kc) + ((size_t)k.kvh * SKV + (SKV - WIN)) * DH;
    pso = g.soX + (rot + u0) * kXT + cq * 256 - GSLOT - k.ldsb; fw = g.fo + head * 4; w2 = (rot + ((u0 + 2) & 3)) * kWT; w3 = (rot + ((u0 + 3) & 3)) * kWT;
    asm volatile("" : "+{v[184:199]}"(wbs), "+{v[200:207]}"(vw), "+{v[208:215]}"(ma), "+{v[216:219]}"(mb), "+s"(kvt), "+s"(pso), "+s"(fw), "+s"(w2), "+s"(w3));
    GAP();
  };
  if constexpr (SE & 2) { setup_w(); setup_b(); }
  unsigned tag;
  HOP_GO(2, wid);
  unsigned la = k.ldsb + SCR + wid * 256 + (4 * (lane & 7) + 2 * (lane >> 5) + ((lane >> 4) & 1)) * 8 + (lane & 8) * 512, lr = k.ldsb + SCR + lane * 8;
  unsigned pa_ = (W_MODA + od * (W_MODC - W_MODA - 256) + (kp * 2048 + cbk * 32 + lane * 2) * 8) | (lane < (16u << s3) ? 0u : 0x80000000u);
  unsigned ps_ = (st1 + (k.kvh * 64 + k.hl * 8 + 2 * wid + (lane >> 5)) * 16) | ((lane & 31) == 0 ? 0u : 0x80000000u);
  asm volatile("" : "+v"(la), "+v"(lr), "+v"(pa_), "+v"(ps_));
  GAP();
  asm volatile("s_waitcnt vmcnt(%1)" : "+v"(cv) : "i"(14 + PT0 - NL) : "memory");
  unsigned sc[2];
#pragma unroll
  for (int i = 0; i < 2; ++i) { const float a = lo16f(cv[i]), b = hi16f(cv[i]); sc[i] = cvtpk(a * __builtin_amdgcn_rcpf(1.f + __expf(-a)), b * __builtin_amdgcn_rcpf(1.f + __expf(-b))); }
  asm volatile("" : "+v"(sc[0]), "+v"(sc[1]));
  GAP();
  STAMPF(27);
  asm volatile("s_waitcnt vmcnt(%4)" : W4(wm) : "i"(10 + PT0 - NL) : "memory");
  STAMP(1);
  if (LT & 2) { XRAW(0, 0); XRAW(0, 1); }
  if (LT & 1) w_touch2(g, wqp, tw2, (lane >> 3) & 3);
  GAP();
  {
    float a[8];
#pragma unroll
    for (int d = 0; d < 4; ++d) {
      a[2 * d] = dot2(plo(wm[2][d], wm[3][d]), sc[1], dot2z(plo(wm[0][d], wm[1][d]), sc[0]));
      a[2 * d + 1] = dot2(phi(wm[2][d], wm[3][d]), sc[1], dot2z(phi(wm[0][d], wm[1][d]), sc[0]));
    }
    float c0 = fold16(fold32(a[0], a[4]), fold32(a[2], a[6])), c1 = fold16(fold32(a[1], a[5]), fold32(a[3], a[7]));
    c0 += dpp_f<0x128>(c0); c1 += dpp_f<0x128>(c1);
    if (S3) { const float kf = s3 ? 0.f : 1.f; c0 = fmaf(dpp_f<0x124>(c0), kf, c0); c1 = fmaf(dpp_f<0x124>(c1), kf, c1); }
    else    { c0 += dpp_f<0x124>(c0); c1 += dpp_f<0x124>(c1); }
    lds_st(la, f32x2{c0, c1});
    asm volatile("s_waitcnt vmcnt(%1)" : "+v"(E) : "i"(9 + PT0) : "memory");
    tag = ((rfl(E) | TAGM) + TAGC) & ~TAGM;
    k.tag = tag;
    unsigned tv = tag;
    asm volatile("s_waitcnt lgkmcnt(0)\n\ts_barrier" : "+v"(tv) :: "memory");
    if (wid == 0) {
      const f32x2 s = (lds_ld<f32x2>(lr) + lds_ld<f32x2>(lr + 256)) + (lds_ld<f32x2>(lr + 512) + lds_ld<f32x2>(lr + 768));
      st16(k.ws, pa_, u32x4{__float_as_uint(s[0]), tv, __float_as_uint(s[1]), tv});
    }
  }
  GAP();
  asm volatile("s_waitcnt vmcnt(%5)\n\ts_cmp_eq_u32 %4, 0\n\ts_cbranch_scc1 .Lxs%=\n\ts_waitcnt vmcnt(%6)\n.Lxs%=:" : W4(xs) : "s"(wid), "i"(6 + PT0), "i"(5 + PT0) : "memory", "scc");
  STAMP(2);
  {
    float sm[2], sq[2];
#pragma unroll
    for (int r = 0; r < 2; ++r) { sm[r] = dot2z(xs[2 * r][0], 0x3f803f80u); sq[r] = dot2z(xs[2 * r][0], xs[2 * r][0]); }
#pragma unroll
    for (int e = 1; e < 8; ++e)
#pragma unroll
      for (int r = 0; r < 2; ++r) { const unsigned w = xs[2 * r + (e >> 2)][e & 3]; sm[r] = dot2(w, 0x3f803f80u, sm[r]); sq[r] = dot2(w, w, sq[r]); }
    const float z = sum_row16(fold16(fold32(sm[0], sm[1]), fold32(sq[0], sq[1]))) * (1.f / DM);
    auto zz = __builtin_amdgcn_permlane16_swap(__float_as_uint(z), __float_as_uint(z), false, false);
    const float mean = __uint_as_float(zz[0]);
    const float rstd = __builtin_amdgcn_rsqf(fmaxf(fmaf(-mean, mean, __uint_as_float(zz[1])), 0.f) + 1e-5f);
    st16(k.ws, ps_, u32x4{__float_as_uint(rstd), tag, __float_as_uint(-mean * rstd), tag});
  }
  GAP();
  STAMP(3);
  Ptrs pa;
  { argp_t aq = args(); pa = Ptrs{aq->wo, aq->w_r, aq->wg, aq->wu, aq->wd, aq->out}; }
  if (IL) { XRAW(0, 2 - hx); XRAW(0, 3 - hx); if (IL & 2) XRAW(1, 0); GAP(); } else X01();
  unsigned cb;
  asm volatile("buffer_load_ushort %0, %1, %2, %3 offen" : "=v"(cb) : "v"(tid * 2), "s"(srdc), "s"((k.j >> 3) * 512) : "memory");
  if constexpr (!(SE & 2)) setup_w();
  if (IL) { if (!(IL & 2)) XRAW(1, 0); XRAW(1, 1); if (IL & 2) XRAW(1, 2); } else w_dma4(g.mW, g.srdW, g.voffW, (rot + u0) * kWT);
#define W1() w_dma4(g.mW + GSLOT, g.srdW, g.voffW, (rot + ((u0 + 1) & 3)) * kWT)
#define W2() w_dma4(g.mW + 2 * GSLOT, g.srdW, g.voffW, (rot + ((u0 + 2) & 3)) * kWT)
  if constexpr (FQ_W1L == 0) W1();
  if constexpr (FQ_X2E == 2) X2();
  if constexpr (FQ_W2E == 2) W2();
  u32x4 st[4], p[4], p2[4];
  const bool k3 = S3 && (kt & 7) == 3;
  const unsigned g2 = gpo | (k3 ? 0u : 0x80000000u);
#define POLL() do { \
    asm volatile("buffer_load_dwordx4 %0, %4, %5, %6 offen sc1\n\tbuffer_load_dwordx4 %1, %4, %5, %7 offen sc1\n\tbuffer_load_dwordx4 %2, %4, %5, %8 offen sc1\n\tbuffer_load_dwordx4 %3, %4, %5, %9 offen sc1" \
                 : "=&{v[160:163]}"(st[0]), "=&{v[164:167]}"(st[1]), "=&{v[168:171]}"(st[2]), "=&{v[172:175]}"(st[3]) : "v"(gso), "s"(k.ws), "s"(cq << 10), "s"((cq ^ 1) << 10), "s"((cq ^ 2) << 10), "s"((cq ^ 3) << 10) : "memory"); \
    asm volatile("buffer_load_dwordx4 %0, %4, %5, 0 offen sc1\n\tbuffer_load_dwordx4 %1, %4, %5, %6 offen sc1\n\tbuffer_load_dwordx4 %2, %4, %5, %7 offen sc1\n\tbuffer_load_dwordx4 %3, %4, %5, %8 offen sc1" \
                 : "=&{v[128:131]}"(p[0]), "=&{v[132:135]}"(p[1]), "=&{v[136:139]}"(p[2]), "=&{v[140:143]}"(p[3]) : "v"(gpo), "s"(k.ws), "s"(16384u), "s"(32768u), "s"(49152u) : "memory"); \
    if (S3) asm volatile("buffer_load_dwordx4 %0, %4, %5, %6 offen sc1\n\tbuffer_load_dwordx4 %1, %4, %5, %7 offen sc1\n\tbuffer_load_dwordx4 %2, %4, %5, %8 offen sc1\n\tbuffer_load_dwordx4 %3, %4, %5, %9 offen sc1" \
                 : "=&{v[144:147]}"(p2[0]), "=&{v[148:151]}"(p2[1]), "=&{v[152:155]}"(p2[2]), "=&{v[156:159]}"(p2[3]) : "v"(g2), "s"(k.ws), "s"(W_MODC - W_MODA), "s"(W_MODC - W_MODA + 16384u), "s"(W_MODC - W_MODA + 32768u), "s"(W_MODC - W_MODA + 49152u) : "memory"); \
    GAP(); } while (0)
#define POLL_WAIT(N) asm volatile("s_waitcnt vmcnt(%12)" : "+{v[160:163]}"(st[0]), "+{v[164:167]}"(st[1]), "+{v[168:171]}"(st[2]), "+{v[172:175]}"(st[3]), "+{v[128:131]}"(p[0]), "+{v[132:135]}"(p[1]), "+{v[136:139]}"(p[2]), \
                                  "+{v[140:143]}"(p[3]), "+{v[144:147]}"(p2[0]), "+{v[148:151]}"(p2[1]), "+{v[152:155]}"(p2[2]), "+{v[156:159]}"(p2[3]) : "i"(N) : "memory")
  GAP();
  if (PBK) { HOP_FRONT(); }
  if constexpr (PPOS == 0) POLL();
  if constexpr (!(SE & 2)) setup_b();
  if constexpr (PPOS == 2 && !IL) POLL();
  if (X1L) X1();
  if (IL) { if (!(IL & 2)) XRAW(1, 2); XRAW(1, 3); if (IL & 2) w_dma4(g.mW, g.srdW, g.voffW, (rot + u0) * kWT); GAP(); }
  attn_setup(k, c, kc, vc, pm, srdx, pa);
  GAP();
  if (IL && !(IL & 2)) { w_dma4(g.mW, g.srdW, g.voffW, (rot + u0) * kWT); GAP(); }
  if (IL & 4) POLL();
  ma[3] = tag;
  static_assert(W_XCH == W_OT + (16u << 20));
  const unsigned vp = k.xcd * 2 + ((k.j >> 1) & 1);
  unsigned o_rot = rot << 19, o_sox = g.soX - GSLOT - k.ldsb, o_dk = 4096u - k0 * 8192u, o_c17 = cq << 17, o_w1 = (rot + ((u0 + 1) & 3)) * kWT, o_idm = ffn_idle(k) ? 0x80000000u : 0u;
  unsigned o_prb = ((k.j & 1) ? W_OT + (vp << 21) : W_PROBE + ((vp & 1) << 13) + vp * 128) | (ffn_idle(k) && wid == 0 ? 0u : 0x80000000u), o_pto = (blockIdx.x * 4 + wid) * 2048;
  unsigned ta = tb + wid * 512 + (((lane ^ 32) * 8) ^ (k0 * 128)); float one = lane >= 32 ? 1.f : 0.f;
  asm volatile("" : "+{v[208:215]}"(ma), "+s"(o_rot), "+s"(o_sox), "+s"(o_dk), "+s"(o_c17), "+s"(o_w1), "+s"(o_idm), "+s"(o_prb), "+s"(o_pto), "+s"(sl[2]), "+s"(sl[3]), "+s"(c.pf1), "+s"(c.pf4), "+s"(c.wo.base),
                    "+s"(k.pm), "+v"(ta), "+v"(one));
  GAP();
  if (IL && !(IL & 4)) POLL();
  if constexpr (FQ_W1L == 2) W1();
  if constexpr (FQ_X2E == 1) X2();
  if constexpr (FQ_W2E == 1) W2();
  GAP();
  STAMPF(5);
  HOP_GO(0, wid);
  if (!PBK) { HOP_FRONT(); }
  STAMPF(6);
  {
    POLL_WAIT((X1L ? 8 : 0) + (FQ_X2E == 1 ? 8 : 0) + (FQ_W1L == 2 ? 4 : 0) + (FQ_W2E == 1 ? 4 : 0));
    asm volatile("ds_read_b128 v[0:3], %3\n\tds_read_b128 v[4:7], %3 offset:2048\n\tds_read_b128 v[8:11], %3 offset:4096\n\tds_read_b128 v[12:15], %3 offset:6144\n\t"
                 "ds_read_b128 v[16:19], %4\n\tds_read_b128 v[20:23], %4 offset:2048\n\tds_read_b128 v[24:27], %4 offset:4096\n\tds_read_b128 v[28:31], %4 offset:6144"
                 : "=&{v[0:31]}"(xa), "+v"(g.pfd), "+v"(cb) : "v"(vw[4]), "v"(vw[5]) : "memory");
    STAMPF(7);
    static_assert(W_MODC - W_MODA == 0x60000);
#define FR_C2
#define FR_L3
    unsigned bad, sp = 0;
    asm volatile(".Lfr_c:\n\t" "v_xor_b32 %[b], %[t], v161\n\t" "v_bitop3_b32 %[b], %[b], v163, %[t] bitop3:0xf6\n\t" "v_bitop3_b32 %[b], %[b], v165, %[t] bitop3:0xf6\n\t" "v_bitop3_b32 %[b], %[b], v167, %[t] bitop3:0xf6\n\t" "v_bitop3_b32 %[b], %[b], v169, %[t] bitop3:0xf6\n\t" "v_bitop3_b32 %[b], %[b], v171, %[t] bitop3:0xf6\n\t" "v_bitop3_b32 %[b], %[b], v173, %[t] bitop3:0xf6\n\t" "v_bitop3_b32 %[b], %[b], v175, %[t] bitop3:0xf6\n\t" "v_bitop3_b32 %[b], %[b], v129, %[t] bitop3:0xf6\n\t" "v_bitop3_b32 %[b], %[b], v131, %[t] bitop3:0xf6\n\t" "v_bitop3_b32 %[b], %[b], v133, %[t] bitop3:0xf6\n\t" "v_bitop3_b32 %[b], %[b], v135, %[t] bitop3:0xf6\n\t" "v_bitop3_b32 %[b], %[b], v137, %[t] bitop3:0xf6\n\t" "v_bitop3_b32 %[b], %[b], v139, %[t] bitop3:0xf6\n\t" "v_bitop3_b32 %[b], %[b], v141, %[t] bitop3:0xf6\n\t" "v_bitop3_b32 %[b], %[b], v143, %[t] bitop3:0xf6\n\t" FR_C2 "v_cmp_ne_u32 vcc, 0, %[b]\n\ts_cbranch_vccnz .Lfr_s\n.Lfr_k:\n\t.subsection 1\n.Lfr_s:\n\ts_sleep 1\n\t" "buffer_load_dwordx4 v[160:163], %[gs], %[ws], %[q0] offen sc1\n\t" "buffer_load_dwordx4 v[164:167], %[gs], %[ws], %[q1] offen sc1\n\t" "buffer_load_dwordx4 v[168:171], %[gs], %[ws], %[q2] offen sc1\n\t" "buffer_load_dwordx4 v[172:175], %[gs], %[ws], %[q3] offen sc1\n\t" "s_mov_b32 vcc_lo, 0\n\t" "buffer_load_dwordx4 v[128:131], %[gp], %[ws], vcc_lo offen sc1\n\t" "s_mov_b32 vcc_lo, 16384\n\t" "buffer_load_dwordx4 v[132:135], %[gp], %[ws], vcc_lo offen sc1\n\t" "s_mov_b32 vcc_lo, 32768\n\t" "buffer_load_dwordx4 v[136:139], %[gp], %[ws], vcc_lo offen sc1\n\t" "s_mov_b32 vcc_lo, 49152\n\t" "buffer_load_dwordx4 v[140:143], %[gp], %[ws], vcc_lo offen sc1\n\t" FR_L3
                 "s_add_u32 %[sp], %[sp], 1\n\ts_cmp_lt_u32 %[sp], %[cap]\n\ts_waitcnt vmcnt(0)\n\ts_cbranch_scc1 .Lfr_c\n\tbuffer_store_dword v211, off, %[ws], 0 offset:256 sc1\n\ts_branch .Lfr_k\n\t.subsection 0"
                 : "+{v[160:163]}"(st[0]), "+{v[164:167]}"(st[1]), "+{v[168:171]}"(st[2]), "+{v[172:175]}"(st[3]), "+{v[128:131]}"(p[0]), "+{v[132:135]}"(p[1]), "+{v[136:139]}"(p[2]), "+{v[140:143]}"(p[3]),
                   "+{v[144:147]}"(p2[0]), "+{v[148:151]}"(p2[1]), "+{v[152:155]}"(p2[2]), "+{v[156:159]}"(p2[3]), [b] "=&v"(bad), [sp] "+s"(sp)
                 : [t] "s"(tag), [t2] "s"(k3 ? tag : 0u), [gs] "v"(gso), [gp] "v"(gpo), [g2] "v"(g2), [ws] "s"(k.ws), [q0] "s"(cq << 10), [q1] "s"((cq ^ 1) << 10), [q2] "s"((cq ^ 2) << 10), [q3] "s"((cq ^ 3) << 10),
                   [cap] "i"(kSpinCap), "{v[208:215]}"(ma) : "vcc", "scc", "memory");
    STAMP(4);
    f32x2 m[4];
#pragma unroll
    for (int i = 0; i < 4; ++i) { m[i] = f32x2{__uint_as_float(p[i][0]), __uint_as_float(p[i][2])}; if (S3) m[i] += f32x2{__uint_as_float(p2[i][0]), __uint_as_float(p2[i][2])}; }
    f32x2 s = (m[0] + m[1]) + (m[2] + m[3]);
    s = up2(cvtpk(s[0], s[1])) + one;
    lds_st(ta, up2(cvtpk(s[0], s[1])) * QSCALE);
    GAP();
    mb[2] = __float_as_uint(silu_bf(lo16f(cb))) >> 16;
    asm volatile("" : "+{v[216:219]}"(mb));
    STAMPF(12);
    BARRIER_LGKM();
    STAMPF(13);
  }
  g.q[0] = xa;
  asm volatile(FQ_ASM : "+{v[0:31]}"(g.q[0]), "=&{v[32:63]}"(g.q[1]), "=&{v[64:95]}"(g.q[2]), "=&{v[96:127]}"(g.q[3]), [fl] "=&v"(g.fl), [pf] "+{v246}"(g.pfd), "+{v[200:207]}"(vw), "+{v[208:215]}"(ma), "+{v[216:219]}"(mb),
                 "=&{v220}"(c.kv.vkb[0]), "=&{v221}"(c.kv.vkb[1]), "=&{v222}"(c.kv.vkb[2]), "=&{v223}"(c.kv.vkb[3]), "=&{v224}"(c.kv.vksrc[0]), "=&{v225}"(c.kv.vksrc[1]), "=&{v226}"(c.kv.vksrc[2]), "=&{v227}"(c.kv.vksrc[3]),
                 "=&{v228}"(c.kv.vvb[0]), "=&{v229}"(c.kv.vvb[1]), "=&{v230}"(c.kv.vvsrc[0]), "=&{v231}"(c.kv.vvsrc[1]), "=&{v232}"(c.gv), "=&{v240}"(c.wo.wv[0]), "=&{v241}"(c.wo.wv[1]), "=&{v244}"(c.wa0),
                 "=&{v245}"(g.fvo), "=&{v247}"(c.tlb), "=&{v233}"(c.gvm), "=&{v248}"(c.c3),
                 [hl] "=&s"(c.hl), [hl18] "=&s"(c.hl18), [cto] "=&s"(c.cto), [pf2] "=&s"(c.pf2), [po] "=&s"(c.po), [px] "=&s"(c.px), [vo] "=&s"(c.vo), [eso] "=&s"(c.eso), [otb] "=&s"(c.otb),
                 [xcb] "=&s"(c.xcb), [frb] "=&s"(c.frb), [fw2] "=&s"(c.fw2), [xso] "=&s"(c.xso), [ss] "=&s"(c.ss), [mbp] "=&s"(c.mbp), [gvo] "=&s"(c.gvo) FQ_STV
               : "{v[160:163]}"(st[0]), "{v[164:167]}"(st[1]), "{v[168:171]}"(st[2]), "{v[172:175]}"(st[3]), "{v[184:199]}"(wbs),
                 [ws] "s"(k.ws), [sw] "s"(g.srdW), [sx] "s"(srdx), [sK] "s"(c.kv.srdK), [sV] "s"(c.kv.srdV), [sG] "s"(c.sG), [sU] "s"(c.sU), [mw] "s"(g.mW), [mx] "s"(g.mX),
                 [rot19] "s"(o_rot), [w2] "s"(w2), [w3] "s"(w3), [sox] "s"(o_sox), [dk] "s"(o_dk), [fo] "s"(g.fo), [fw] "s"(fw), [pso] "s"(pso), [c17] "s"(o_c17), [sl2] "s"(sl[2]), [w1] "s"(o_w1),
                 [sl3] "s"(sl[3]), [wq] "s"(wqp), [kv] "s"(kvt), [sofk] "s"(c.wo.m), [pf4] "s"(c.pf4), [pf1] "s"(c.pf1), [wo] "s"(c.wo.srd),
                 [bid] "s"(k.bid), [wid] "s"(wid), [idm] "s"(o_idm), [prb] "s"(o_prb), [pto] "s"(o_pto)
               : FQ_CL, ACLOB_LO, ACLOB_HI);
  GAP();
}
DI void oproj(Ctx& k, W4C& c STAMP_ARG) {
  const unsigned wid = k.wid, tag = k.tag; unsigned gad, ln;
  u32x32 UP = c.up, DN = c.q[1]; f32x32 A3 = __builtin_bit_cast(f32x32, c.q[3]), XT = __builtin_bit_cast(f32x32, c.q[0]); float cww, bbw;
  asm volatile(OP_ASM
               : "+{v[176:207]}"(UP), "+{v[160:175]}"(c.o3), "+{v[32:63]}"(DN), "+{v[96:127]}"(A3), "+{v[0:31]}"(XT), "+{v[64:95]}"(c.q[2]), "+{v[128:159]}"(c.wm), "+{v224}"(c.kv.vksrc[0]), "+{v225}"(c.kv.vksrc[1]), "+{v226}"(c.kv.vksrc[2]), "+{v227}"(c.kv.vksrc[3]), "=&{s87}"(cww), "=&{s88}"(bbw),
                 "=&{v237}"(gad), "=&{v244}"(ln), "+{v213}"(c.gra), "+{v240}"(c.wo.wv[0]), "+{v241}"(c.wo.wv[1]), "+{v247}"(c.tlb), "+{v252}"(c.vs), [pm] "+s"(k.pm)
               : "{v234}"(c.wo.wa), "{v242}"(c.l16), "{v243}"(c.tagv), "{v245}"(c.fl), "{v254}"(c.fbv),
                 [ws] "s"(k.ws), [wo] "s"(c.wo.srd), [sd] "s"(c.sD), [sx] "s"(c.sX), [sr] "s"(c.sR),
                 [tag] "s"(tag), [wid] "s"(wid), [hl] "s"(c.hl), [eso] "s"(c.eso), [na] "s"(c.na), [frb] "s"(c.frb), [xcb] "s"(c.xcb), [xso] "s"(c.xso),
                 [m] "s"(c.wo.m), [wob] "s"(c.wo.base), [ss] "s"(c.ss), [mbp] "s"(c.mbp), [cap] "i"(kSpinCap), [fw2] "s"(c.fw2)
               : OP_CLOB, ACLOB_LO, ACLOB_HI);
  if (__builtin_expect(k.pm == 2, 0)) {
    asm volatile("s_getpc_b64 vcc\n.Lrs_a:\n\ts_sub_u32 vcc_lo, vcc_lo, .Lrs_a-.Lfz_a\n\ts_subb_u32 vcc_hi, vcc_hi, 0\n\ts_and_b32 vcc_lo, vcc_lo, 0xfffff000\n\ts_setpc_b64 vcc"
                 :: "{s[0:1]}"(__builtin_amdgcn_kernarg_segment_ptr()), "{s2}"(blockIdx.x), "{v0}"((wid * 64 + lane_id()) | 0x10000u) : "vcc", "scc", "memory");
    __builtin_unreachable();
  }
  asm volatile(TL_ASM
               : "+{v[176:207]}"(UP), "+{v[32:63]}"(DN), "+{v[96:127]}"(A3), "+{v[0:31]}"(XT), "+{v[64:95]}"(c.q[2]), "+{v[128:159]}"(c.wm), "+{v[160:175]}"(c.o3), "+{s87}"(cww), "+{s88}"(bbw), "+{v240}"(c.wo.wv[0])
               : "{v237}"(gad), "{v241}"(c.wo.wv[1]), "{v242}"(c.l16), "{v243}"(c.tagv), "{v244}"(ln),
                 [ws] "s"(k.ws), [tag] "s"(tag), [wid] "s"(wid), [bid] "s"(k.bid), [nb] "s"(k.nb), [out] "s"(c.out), [cap] "i"(kSpinCap)
               : TL_CLOB);
  STAMPO(26);
}
#define PERM 0
__global__ __launch_bounds__(256, 1) __attribute__((aligned(4096))) void k_fused(Args a) {
  Ctx k;
  const unsigned bid = PERM ? ((blockIdx.x << 3) | (blockIdx.x >> 5)) & 255u : blockIdx.x;
  unsigned t0;
  asm volatile("v_readfirstlane_b32 %0, %1" : "=s"(t0) : "v"(threadIdx.x));
  const unsigned wid = (t0 >> 6) & 3;
  k.pm = 1 - (t0 >> 16);
  k.wid = wid; k.bid = bid; k.xcd = ((bid & 3) << 1) | ((bid >> 2) & 1); k.j = bid >> 3; k.kvh = k.xcd >> 1; k.hl = k.j & 7; k.rblk = (k.xcd & 1) * 4 + (k.j >> 3);
  k.cs = (k.xcd & 1) << 13; k.nb = 0;
  STAMP_DECL;
  STAMP(0);
#define SPINTO(i) do { } while (0)
  W4C c;
  SPINTO(0);
  {
    H4 g;
    front(k, g, c STAMP_PASS);
    STAMP(8);
    SPINTO(1);
    attention(k, g, c STAMP_PASS);
  }
  SPINTO(2);
  oproj(k, c STAMP_PASS);
  SPINTO(3);
  STAMP_FLUSH();
  asm volatile(HOP_DEFS);
}

extern "C" void kernel_launch(void* const* d_in, const int* in_sizes, int n_in, void* d_out, int out_size, void* d_ws, size_t ws_size,
                              hipStream_t stream) {
  Args a;
  a.x = (const u16*)d_in[0]; a.kc = (const u16*)d_in[1]; a.vc = (const u16*)d_in[2]; a.wq = (const u16*)d_in[3]; a.wo = (const u16*)d_in[4];
  a.w_r = (const u16*)d_in[6]; a.wg = (const u16*)d_in[7]; a.wu = (const u16*)d_in[8]; a.wd = (const u16*)d_in[9]; a.c = (const u16*)d_in[10];
  a.w_mod = (const u16*)d_in[11]; a.out = (u16*)d_out; a.ws = (char*)d_ws;
  (void)hipFuncSetAttribute((const void*)k_fused, hipFuncAttributeMaxDynamicSharedMemorySize, 163840);
  k_fused<<<256, 256, 163840, stream>>>(a);
}
```

```cpp
#include <hip/hip_runtime.h>
#include <hip/hip_bf16.h>
#include <stdint.h>
#include <utility>

typedef unsigned short u16;
using bf16x8 = __attribute__((ext_vector_type(8))) short;
using s16x4  = __attribute__((ext_vector_type(4))) short;
using f32x16 = __attribute__((ext_vector_type(16))) float;
using u32x16 = __attribute__((ext_vector_type(16))) unsigned;
using f32x4  = __attribute__((ext_vector_type(4))) float;
using f32x2  = __attribute__((ext_vector_type(2))) float;
using u32x4  = __attribute__((ext_vector_type(4))) unsigned;
using u32x2  = __attribute__((ext_vector_type(2))) unsigned;
using u32x8 = __attribute__((ext_vector_type(8))) unsigned;
using f32x8 = __attribute__((ext_vector_type(8))) float;
using f32x32 = __attribute__((ext_vector_type(32))) float;
using u32x32 = __attribute__((ext_vector_type(32))) unsigned;
typedef __bf16 bf16x2_t __attribute__((ext_vector_type(2)));
#define DI __device__ __forceinline__
#define NTMASK 0
#define NT_SEL(bit) ((NTMASK >> (bit)) & 1)
#if NT_SEL(0)
#define NTX " nt"
#else
#define NTX ""
#endif
#if NT_SEL(1)
#define NTM " nt"
#else
#define NTM ""
#endif
#if NT_SEL(2)
#define NTQ " nt"
#else
#define NTQ ""
#endif
#if NT_SEL(3)
#define NTO " nt"
#else
#define NTO ""
#endif
#if NT_SEL(4)
#define NTG " nt"
#else
#define NTG ""
#endif
#if NT_SEL(5)
#define NTD " nt"
#else
#define NTD ""
#endif
#if NT_SEL(6)
#define NTT " nt"
#else
#define NTT ""
#endif
#if NT_SEL(7)
#define NTF " nt"
#else
#define NTF ""
#endif

constexpr int S = 2048, DM = 1024, HQ = 32, HKV = 4, DH = 128, SKV = 2048, WIN = 512, FF = 3584;
constexpr int QD = HQ * DH;
constexpr float QSCALE = 0.12751743082f;

DI float bf2f(u16 v) { return __uint_as_float(((unsigned)v) << 16); }
DI u16 f2bf(float x) { unsigned u = __float_as_uint(x); u += 0x7fffu + ((u >> 16) & 1u); return (u16)(u >> 16); }
DI unsigned cvtpk(float lo, float hi) { f32x2 v = {lo, hi}; bf16x2_t b = __builtin_convertvector(v, bf16x2_t); return __builtin_bit_cast(unsigned, b); }
DI float lo16f(unsigned w) { return __uint_as_float(w << 16); }
DI float hi16f(unsigned w) { return __uint_as_float(w & 0xffff0000u); }
DI float rbf(float x) { return lo16f(cvtpk(x, x)); }
DI f32x2 up2(unsigned w) { return f32x2{lo16f(w), hi16f(w)}; }
DI float dot2(unsigned a, unsigned b, float c) { return __builtin_amdgcn_fdot2_f32_bf16(__builtin_bit_cast(bf16x2_t, a), __builtin_bit_cast(bf16x2_t, b), c, false); }
DI float dot2z(unsigned a, unsigned b) { return __builtin_amdgcn_fdot2_f32_bf16(__builtin_bit_cast(bf16x2_t, a), __builtin_bit_cast(bf16x2_t, b), 0.f, false); }
DI unsigned plo(unsigned a, unsigned b) { return __builtin_amdgcn_perm(b, a, 0x05040100u); }
DI unsigned phi(unsigned a, unsigned b) { return __builtin_amdgcn_perm(b, a, 0x07060302u); }
DI unsigned rfl(unsigned x) { return __builtin_amdgcn_readfirstlane(x); }
template <int CTRL> DI float dpp_f(float v) { return __int_as_float(__builtin_amdgcn_update_dpp(0, __float_as_int(v), CTRL, 0xf, 0xf, true)); }
template <int CTRL> DI unsigned dpp_u(unsigned v) { return (unsigned)__builtin_amdgcn_update_dpp(0, (int)v, CTRL, 0xf, 0xf, true); }
DI float fold16(float a, float b) { auto r = __builtin_amdgcn_permlane16_swap(__float_as_uint(a), __float_as_uint(b), false, false); return __uint_as_float(r[0]) + __uint_as_float(r[1]); }
DI float fold32(float a, float b) { auto r = __builtin_amdgcn_permlane32_swap(__float_as_uint(a), __float_as_uint(b), false, false); return __uint_as_float(r[0]) + __uint_as_float(r[1]); }
DI float sum16(float v) { return fold16(v, v); }
DI float sum32(float v) { return fold32(v, v); }
DI float sum_row16(float v) { v += dpp_f<0xB1>(v); v += dpp_f<0x4E>(v); v += dpp_f<0x141>(v); v += dpp_f<0x140>(v); return v; }
DI float wave_sum(float v) { return sum32(sum16(sum_row16(v))); }
DI unsigned wave_maxu(unsigned v) {
  v = max(v, dpp_u<0xB1>(v)); v = max(v, dpp_u<0x4E>(v)); v = max(v, dpp_u<0x141>(v)); v = max(v, dpp_u<0x140>(v));
  { auto r = __builtin_amdgcn_permlane16_swap(v, v, false, false); v = max(r[0], r[1]); }
  { auto r = __builtin_amdgcn_permlane32_swap(v, v, false, false); v = max(r[0], r[1]); }
  return v;
}
#define GAP() __builtin_amdgcn_sched_barrier(0)
template <int N> using iseq = std::make_integer_sequence<int, N>;
template <int... Is> using ilist = std::integer_sequence<int, Is...>;
typedef __amdgpu_buffer_rsrc_t srd_t;
#define SRD3 0x00020000
DI srd_t mk_srd(const void* p, unsigned bytes) { return __builtin_amdgcn_make_buffer_rsrc((void*)p, (short)0, (int)bytes, SRD3); }
DI unsigned lane_id() { unsigned l; asm volatile("v_mbcnt_lo_u32_b32 %0, -1, 0\n\tv_mbcnt_hi_u32_b32 %0, -1, %0" : "=v"(l)); return l; }
struct Args { const u16 *x, *kc, *vc, *wq, *wo, *w_r, *wg, *wu, *wd, *c, *w_mod; u16* out; char* ws; };
typedef const __attribute__((address_space(4))) Args* argp_t;
DI argp_t args() { argp_t p = (argp_t)__builtin_amdgcn_kernarg_segment_ptr(); asm volatile("" : "+s"(p)); return p; }
#define BARRIER_LGKM() do { asm volatile("s_waitcnt lgkmcnt(0)\n\ts_barrier" ::: "memory"); } while (0)

constexpr unsigned W_EPOCH = 0x260000, W_TMO = 256;
constexpr unsigned W_FLAGH = 0x261000;
constexpr unsigned W_VOTE = 0x2000, W_PROBE = 0x4000;
constexpr unsigned W_GKEY = 0x10000;
constexpr unsigned W_FLAGR = 0x8000;
constexpr unsigned W_FLAGO = 0x20000;
constexpr unsigned W_CANDH = 0x40000;
constexpr unsigned W_STAT = 0x80000;
constexpr unsigned W_YG = 0x100000;
constexpr unsigned W_CANDX = 0x1E0000;
constexpr unsigned W_ST1 = 0x200000;
constexpr unsigned W_MODA = 0x210000;
constexpr unsigned W_MODC = 0x270000;
constexpr unsigned W_MODB = 0x220000;
constexpr unsigned W_SIL = 0x240000;
constexpr unsigned W_HT = 4u << 20;
constexpr unsigned W_OT = 8u << 20;
constexpr unsigned W_XCH = 24u << 20;
constexpr unsigned W_END = 40u << 20;
constexpr unsigned TAGM = 0x40004000u, TAGC = 0x9E3739B9u;
#define SPINCAP 262144
constexpr unsigned kSpinCap = SPINCAP;
#define SL0 1
#define SL1 1
#define SL2 1
#define SL3 1
template <int SL = 1> DI bool spin_fail(unsigned& spins, srd_t ws, unsigned tag) {
  __builtin_amdgcn_s_sleep(SL);
  if (++spins > kSpinCap) { __builtin_amdgcn_raw_buffer_store_b32(tag, ws, W_TMO, 0, 16); return true; }
  return (spins & 255u) == 0u && rfl(__builtin_amdgcn_raw_buffer_load_b32(ws, W_TMO, 0, 16)) == tag;
}
DI u32x4 ld16(srd_t ws, unsigned off) { return __builtin_amdgcn_raw_buffer_load_b128(ws, off, 0, 16); }
DI u32x2 ld8(srd_t ws, unsigned off) { return __builtin_amdgcn_raw_buffer_load_b64(ws, off, 0, 16); }
DI void st16(srd_t ws, unsigned off, u32x4 v) { __builtin_amdgcn_raw_buffer_store_b128(v, ws, off, 0, 16); }
DI void st8(srd_t ws, unsigned off, u32x2 v) { __builtin_amdgcn_raw_buffer_store_b64(v, ws, off, 0, 16); }
DI void st4(srd_t ws, unsigned off, unsigned v) { __builtin_amdgcn_raw_buffer_store_b32(v, ws, off, 0, 16); }
DI void wait_flags(srd_t ws, unsigned off, unsigned tag) {
  const unsigned o = off + (lane_id() & 31u) * 4u;
  for (unsigned spins = 0;;) {
    const unsigned f = __builtin_amdgcn_raw_buffer_load_b32(ws, o, 0, 16);
    if (__all(f == tag)) break;
    if (spin_fail(spins, ws, tag)) break;
  }
  asm volatile("" ::: "memory");
}
template <int OFF, bool DEV = false> DI u32x4 ald16(srd_t s, unsigned vo, unsigned so) {
  u32x4 r;
  if constexpr (DEV) asm volatile("buffer_load_dwordx4 %0, %1, %2, %3 offen offset:%4 sc1" : "=v"(r) : "v"(vo), "s"(s), "s"(so), "i"(OFF) : "memory");
  else               asm volatile("buffer_load_dwordx4 %0, %1, %2, %3 offen offset:%4" NTF : "=v"(r) : "v"(vo), "s"(s), "s"(so), "i"(OFF) : "memory");
  return r;
}
template <int OFF> DI u32x2 ald8(srd_t s, unsigned vo, unsigned so) {
  u32x2 r; asm volatile("buffer_load_dwordx2 %0, %1, %2, %3 offen offset:%4" : "=v"(r) : "v"(vo), "s"(s), "s"(so), "i"(OFF) : "memory"); return r;
}
DI u32x2 ald8d(srd_t s, unsigned vo) { u32x2 r; asm volatile("buffer_load_dwordx2 %0, %1, %2, 0 offen sc1" : "=v"(r) : "v"(vo), "s"(s) : "memory"); return r; }
template <int OFF, bool DEV = false> DI unsigned ald4(srd_t s, unsigned vo, unsigned so) {
  unsigned r;
  if constexpr (DEV) asm volatile("buffer_load_dword %0, %1, %2, %3 offen offset:%4 sc1" : "=v"(r) : "v"(vo), "s"(s), "s"(so), "i"(OFF) : "memory");
  else               asm volatile("buffer_load_dword %0, %1, %2, %3 offen offset:%4" : "=v"(r) : "v"(vo), "s"(s), "s"(so), "i"(OFF) : "memory");
  return r;
}
DI void touch(unsigned& d, const void* base, unsigned vo) { asm volatile("global_load_dword %0, %1, %2" : "+v"(d) : "v"(vo), "s"(base) : "memory"); }
DI void touchq(unsigned& d, const void* base, unsigned vo) { asm volatile("global_load_dword %0, %1, %2" NTQ : "+v"(d) : "v"(vo), "s"(base) : "memory"); }
template <class T> DI T lds_ld(unsigned a) { return *reinterpret_cast<const __attribute__((address_space(3))) T*>(a); }
template <class T> DI void lds_st(unsigned a, T v) { *reinterpret_cast<__attribute__((address_space(3))) T*>(a) = v; }
DI float silu_bf(float v) { return rbf(v * __builtin_amdgcn_rcpf(1.f + __expf(-v))); }
struct Ctx { static constexpr unsigned ldsb = 0; unsigned pm, bid, wid, xcd, j, kvh, hl, rblk, tag, cs, nb; srd_t ws; };
#define TAIL2 1
DI unsigned ffn_j(const Ctx& k) { return TAIL2 ? (k.j < 24 ? k.j : 24 + (k.j & 3)) : min(k.j, 27u); }
DI bool ffn_idle(const Ctx& k) { return TAIL2 ? (k.j >> 2) == 6 + (k.xcd & 1) : k.j >= 28; }
constexpr unsigned UNIT = 16384;
#define STAMPA(I) do { } while (0)
#define STAMPO(I) do { } while (0)
#define STAMP_DECL
#define STAMP(I) do { } while (0)
#define STAMP_FLUSH() do { } while (0)
#define STAMP_ARG
#define STAMP_PASS
#define ACLOB_LO "a0","a1","a2","a3","a4","a5","a6","a7","a8","a9","a10","a11","a12","a13","a14","a15","a16","a17","a18","a19","a20","a21","a22","a23","a24","a25","a26","a27","a28","a29","a30","a31","a32","a33","a34","a35","a36","a37","a38","a39","a40","a41","a42","a43","a44","a45","a46","a47","a48","a49","a50","a51","a52","a53","a54","a55","a56","a57","a58","a59","a60","a61","a62","a63","a64","a65","a66","a67","a68","a69","a70","a71","a72","a73","a74","a75","a76","a77","a78","a79","a80","a81","a82","a83","a84","a85","a86","a87","a88","a89","a90","a91","a92","a93","a94","a95","a96","a97","a98","a99","a100","a101","a102","a103","a104","a105","a106","a107","a108","a109","a110","a111","a112","a113","a114","a115","a116","a117","a118","a119","a120","a121","a122","a123","a124","a125","a126","a127"
#define ACLOB_HI "a128","a129","a130","a131","a132","a133","a134","a135","a136","a137","a138","a139","a140","a141","a142","a143","a144","a145","a146","a147","a148","a149","a150","a151","a152","a153","a154","a155","a156","a157","a158","a159","a160","a161","a162","a163","a164","a165","a166","a167","a168","a169","a170","a171","a172","a173","a174","a175","a176","a177","a178","a179","a180","a181","a182","a183","a184","a185","a186","a187","a188","a189","a190","a191","a192","a193","a194","a195","a196","a197","a198","a199","a200","a201","a202","a203","a204","a205","a206","a207","a208","a209","a210","a211","a212","a213","a214","a215","a216","a217","a218","a219","a220","a221","a222","a223","a224","a225","a226","a227","a228","a229","a230","a231","a232","a233","a234","a235","a236","a237","a238","a239","a240","a241","a242","a243","a244","a245","a246","a247","a248","a249","a250","a251","a252","a253","a254","a255"
#define HOP_DEFS ".set .Lhc00, .Lhs0\n\t.set .Lhs0n, .Lhs4\n\t.set .Lhs4n, .Lhs8\n\t.set .Lhs8n, .Lhr0\n\t.set .Lhc01, .Lhs1\n\t.set .Lhs1n, .Lhs5\n\t.set .Lhs5n, .Lhs9\n\t.set .Lhs9n, .Lhr0\n\t.set .Lhc02, .Lhs2\n\t.set .Lhs2n, .Lhs6\n\t.set .Lhs6n, .Lhs10\n\t.set .Lhs10n, .Lhr0\n\t.set .Lhc03, .Lhs3\n\t.set .Lhs3n, .Lhs7\n\t.set .Lhs7n, .Lhr0\n\t.set .Lhc10, .Lhs11\n\t.set .Lhs11n, .Lhs15\n\t.set .Lhs15n, .Lhr1\n\t.set .Lhc11, .Lhs12\n\t.set .Lhs12n, .Lht0\n\t.set .Lht0n, .Lhr1\n\t.set .Lhc12, .Lhs13\n\t.set .Lhs13n, .Lht1\n\t.set .Lht1n, .Lhr1\n\t.set .Lhc13, .Lhs14\n\t.set .Lhs14n, .Lhr1\n\t.set .Lhc20, .Lhf\n\t.set .Lhfn, .Lhr2\n\t.set .Lhc21, .Lhr2\n\t.set .Lhc22, .Lhr2\n\t.set .Lhc23, .Lhr2"
#define HOP_PAGE(n) asm volatile("s_branch .Lht" #n "\n\t.p2align 12\n.Lht" #n ":\n\ts_cbranch_execz .Lht" #n "n")
#define PAGE_ALIGN() asm volatile("s_branch .Lpa%=\n\t.p2align 12\n.Lpa%=:" ::: "memory")
#define HOP_FRONT() asm volatile("s_branch .Lhf\n\t.p2align 12\n.Lhf:\n\ts_cbranch_execz .Lhfn")
#define HOP_GO(g, w) asm volatile("s_mov_b64 exec, 0\n\ts_cmp_eq_u32 %0, 0\n\ts_cbranch_scc1 .Lhc" #g "0\n\ts_cmp_eq_u32 %0, 1\n\ts_cbranch_scc1 .Lhc" #g "1\n\ts_cmp_eq_u32 %0, 2\n\ts_cbranch_scc1 .Lhc" #g "2\n\ts_branch .Lhc" #g "3\n.Lhr" #g ":\n\ts_mov_b64 exec, -1" :: "s"(w) : "scc", "memory")
#define STP(x)
#define OP_ASM \
  "s_waitcnt lgkmcnt(0)\n\t" \
  "s_barrier\n\t" \
  STP("s_memrealtime s[94:95]\n\t") \
  STP("s_memtime s[92:93]\n\t") \
  "ds_read_b32 v228, v213 offset:49152\n\t" \
  "v_mfma_f32_16x16x32_bf16 a[8:11], a[140:143], v[76:79], 0\n\t" \
  "v_add_u32 v238, 14336, v240\n\t" \
  "v_add_u32 v239, 21504, v241\n\t" \
  "v_mfma_f32_16x16x32_bf16 a[8:11], a[136:139], v[72:75], a[8:11]\n\t" \
  "ds_read_b128 a[12:15], v242 offset:32768\n\t" \
  "v_add_u32 v241, 7168, v241\n\t" \
  "v_mfma_f32_16x16x32_bf16 a[8:11], a[132:135], v[68:71], a[8:11]\n\t" \
  "s_mov_b32 s79, 0x05040100\n\t" \
  "ds_read_b128 a[28:31], v242 offset:33792\n\t" \
  "s_mov_b32 s80, 0x07060302\n\t" \
  "v_mfma_f32_16x16x32_bf16 a[8:11], a[128:131], v[64:67], a[8:11]\n\t" \
  "ds_read_b128 a[44:47], v242 offset:34816\n\t" \
  "v_mfma_f32_16x16x32_bf16 a[24:27], a[144:147], v[64:67], 0\n\t" \
  "ds_read_b128 a[60:63], v242 offset:35840\n\t" \
  "v_mfma_f32_16x16x32_bf16 a[24:27], a[148:151], v[68:71], a[24:27]\n\t" \
  "v_mfma_f32_16x16x32_bf16 a[24:27], a[152:155], v[72:75], a[24:27]\n\t" \
  "s_waitcnt vmcnt(14)\n\t" \
  "buffer_store_dword v243, v254, %[ws], %[fw2] offen offset:32 sc1\n\t" \
  "v_mfma_f32_16x16x32_bf16 a[24:27], a[156:159], v[76:79], a[24:27]\n\t" \
  "ds_read_b128 a[76:79], v242 offset:36864\n\t" \
  "v_mfma_f32_16x16x32_bf16 a[104:107], a[172:175], v[76:79], 0\n\t" \
  "ds_read_b128 a[92:95], v242 offset:37888\n\t" \
  "v_mfma_f32_16x16x32_bf16 a[104:107], a[168:171], v[72:75], a[104:107]\n\t" \
  "ds_read_b128 a[108:111], v242 offset:38912\n\t" \
  "s_waitcnt lgkmcnt(7)\n\t" \
  "v_cmp_eq_u32 vcc, %[tag], v228\n\t" \
  "s_and_b32 vcc_lo, vcc_lo, %[pm]\n\t" \
  "s_cbranch_scc1 .Lop_rd\n\t" \
  "v_mfma_f32_16x16x32_bf16 a[104:107], a[164:167], v[68:71], a[104:107]\n\t" \
  "ds_read_b128 a[124:127], v242 offset:39936\n\t" \
  "v_mfma_f32_16x16x32_bf16 a[104:107], a[160:163], v[64:67], a[104:107]\n\t" \
  "ds_read_b128 a[208:211], v242 offset:40960\n\t" \
  "v_mfma_f32_16x16x32_bf16 a[120:123], a[176:179], v[64:67], 0\n\t" \
  "buffer_load_dword v246, v245, %[ws], %[fw2] offen sc1\n\t" \
  "v_mfma_f32_16x16x32_bf16 a[120:123], a[180:183], v[68:71], a[120:123]\n\t" \
  "ds_read_b128 a[212:215], v242 offset:41984\n\t" \
  "v_mfma_f32_16x16x32_bf16 a[120:123], a[184:187], v[72:75], a[120:123]\n\t" \
  "ds_read_b128 a[216:219], v242 offset:43008\n\t" \
  "v_mfma_f32_16x16x32_bf16 a[120:123], a[188:191], v[76:79], a[120:123]\n\t" \
  "s_xor_b32 s73, %[eso], 131072\n\t" \
  "ds_read_b128 a[220:223], v242 offset:44032\n\t" \
  "v_mfma_f32_16x16x32_bf16 a[88:91], v[108:111], v[76:79], 0\n\t" \
  "s_add_u32 m0, %[m], 0\n\t" \
  "ds_read_b128 a[192:195], v242 offset:45056\n\t" \
  "v_mfma_f32_16x16x32_bf16 a[88:91], v[104:107], v[72:75], a[88:91]\n\t" \
  "ds_read_b128 a[196:199], v242 offset:46080\n\t" \
  "v_mfma_f32_16x16x32_bf16 a[88:91], v[100:103], v[68:71], a[88:91]\n\t" \
  "ds_read_b128 a[200:203], v242 offset:47104\n\t" \
  "ds_read_b128 a[204:207], v242 offset:48128\n\t" \
  "v_mfma_f32_16x16x32_bf16 a[88:91], v[96:99], v[64:67], a[88:91]\n\t" \
  "s_waitcnt lgkmcnt(12)\n\t" \
  "s_mov_b64 exec, 0\n\t" \
  "s_cmp_eq_u32 %[wid], 0\n\t" \
  "s_cbranch_scc1 .Lhc10\n\t" \
  "s_cmp_eq_u32 %[wid], 1\n\t" \
  "s_cbranch_scc1 .Lhc11\n\t" \
  "s_cmp_eq_u32 %[wid], 2\n\t" \
  "s_cbranch_scc1 .Lhc12\n\t" \
  "s_branch .Lhc13\n\t" \
  ".Lhr1:\n\t" \
  "s_mov_b64 exec, -1\n\t" \
  "s_waitcnt vmcnt(6)\n\t" \
  "s_waitcnt vmcnt(0)\n\t" \
  "v_cmp_ne_u32 vcc, %[tag], v246\n\t" \
  "s_and_b32 vcc_lo, vcc_lo, 0x6\n\t" \
  "s_cbranch_scc1 .Lop_s0\n\t" \
  ".Lop_k0:\n\t" \
  "buffer_load_dwordx4 v[16:19], v242, %[ws], s73 offen offset:0 sc1\n\t" \
  "v_perm_b32 v248, v132, v128, s79\n\t" \
  "v_perm_b32 v128, v132, v128, s80\n\t" \
  "v_dot2_f32_bf16 v132, v248, v224, 0\n\t" \
  "v_dot2_f32_bf16 v128, v128, v224, 0\n\t" \
  "v_perm_b32 v249, v133, v129, s79\n\t" \
  "v_perm_b32 v129, v133, v129, s80\n\t" \
  "v_dot2_f32_bf16 v133, v249, v224, 0\n\t" \
  "v_dot2_f32_bf16 v129, v129, v224, 0\n\t" \
  "buffer_load_dwordx4 v[20:23], v242, %[ws], s73 offen offset:1024 sc1\n\t" \
  "v_perm_b32 v248, v134, v130, s79\n\t" \
  "v_perm_b32 v130, v134, v130, s80\n\t" \
  "v_dot2_f32_bf16 v134, v248, v224, 0\n\t" \
  "v_dot2_f32_bf16 v130, v130, v224, 0\n\t" \
  "v_perm_b32 v249, v135, v131, s79\n\t" \
  "v_perm_b32 v131, v135, v131, s80\n\t" \
  "v_dot2_f32_bf16 v135, v249, v224, 0\n\t" \
  "v_dot2_f32_bf16 v131, v131, v224, 0\n\t" \
  "buffer_load_dwordx4 v[24:27], v242, %[ws], s73 offen offset:2048 sc1\n\t" \
  "v_perm_b32 v248, v140, v136, s79\n\t" \
  "v_perm_b32 v136, v140, v136, s80\n\t" \
  "v_dot2_f32_bf16 v132, v248, v225, v132\n\t" \
  "v_dot2_f32_bf16 v128, v136, v225, v128\n\t" \
  "v_perm_b32 v249, v141, v137, s79\n\t" \
  "v_perm_b32 v137, v141, v137, s80\n\t" \
  "v_dot2_f32_bf16 v133, v249, v225, v133\n\t" \
  "v_dot2_f32_bf16 v129, v137, v225, v129\n\t" \
  "buffer_load_dwordx4 v[28:31], v242, %[ws], s73 offen offset:3072 sc1\n\t" \
  "v_perm_b32 v248, v142, v138, s79\n\t" \
  "v_perm_b32 v138, v142, v138, s80\n\t" \
  "v_dot2_f32_bf16 v134, v248, v225, v134\n\t" \
  "v_dot2_f32_bf16 v130, v138, v225, v130\n\t" \
  "v_perm_b32 v249, v143, v139, s79\n\t" \
  "v_perm_b32 v139, v143, v139, s80\n\t" \
  "v_dot2_f32_bf16 v135, v249, v225, v135\n\t" \
  "v_dot2_f32_bf16 v131, v139, v225, v131\n\t" \
  "s_xor_b32 s73, %[eso], 163840\n\t" \
  "buffer_load_dwordx4 v[0:3], v242, %[ws], s73 offen offset:0 sc1\n\t" \
  "v_perm_b32 v248, v148, v144, s79\n\t" \
  "v_perm_b32 v144, v148, v144, s80\n\t" \
  "v_dot2_f32_bf16 v132, v248, v226, v132\n\t" \
  "v_dot2_f32_bf16 v128, v144, v226, v128\n\t" \
  "v_perm_b32 v249, v149, v145, s79\n\t" \
  "v_perm_b32 v145, v149, v145, s80\n\t" \
  "v_dot2_f32_bf16 v133, v249, v226, v133\n\t" \
  "v_dot2_f32_bf16 v129, v145, v226, v129\n\t" \
  "buffer_load_dwordx4 v[4:7], v242, %[ws], s73 offen offset:1024 sc1\n\t" \
  "v_perm_b32 v248, v150, v146, s79\n\t" \
  "v_perm_b32 v146, v150, v146, s80\n\t" \
  "v_dot2_f32_bf16 v134, v248, v226, v134\n\t" \
  "v_dot2_f32_bf16 v130, v146, v226, v130\n\t" \
  "v_perm_b32 v249, v151, v147, s79\n\t" \
  "v_perm_b32 v147, v151, v147, s80\n\t" \
  "v_dot2_f32_bf16 v135, v249, v226, v135\n\t" \
  "v_dot2_f32_bf16 v131, v147, v226, v131\n\t" \
  "buffer_load_dwordx4 v[8:11], v242, %[ws], s73 offen offset:2048 sc1\n\t" \
  "v_perm_b32 v248, v156, v152, s79\n\t" \
  "v_perm_b32 v152, v156, v152, s80\n\t" \
  "v_dot2_f32_bf16 v132, v248, v227, v132\n\t" \
  "v_dot2_f32_bf16 v128, v152, v227, v128\n\t" \
  "v_perm_b32 v249, v157, v153, s79\n\t" \
  "v_perm_b32 v153, v157, v153, s80\n\t" \
  "v_dot2_f32_bf16 v133, v249, v227, v133\n\t" \
  "v_dot2_f32_bf16 v129, v153, v227, v129\n\t" \
  "buffer_load_dwordx4 v[12:15], v242, %[ws], s73 offen offset:3072 sc1\n\t" \
  "v_perm_b32 v248, v158, v154, s79\n\t" \
  "v_perm_b32 v154, v158, v154, s80\n\t" \
  "v_dot2_f32_bf16 v134, v248, v227, v134\n\t" \
  "v_dot2_f32_bf16 v130, v154, v227, v130\n\t" \
  "v_perm_b32 v249, v159, v155, s79\n\t" \
  "v_perm_b32 v155, v159, v155, s80\n\t" \
  "v_dot2_f32_bf16 v135, v249, v227, v135\n\t" \
  "v_dot2_f32_bf16 v131, v155, v227, v131\n\t" \
  "v_mfma_f32_16x16x32_bf16 v[96:99], a[128:131], a[12:15], 0\n\t" \
  "s_xor_b32 s73, %[eso], 262144\n\t" \
  "s_xor_b32 s71, %[wob], 1048576\n\t" \
  "v_mfma_f32_16x16x32_bf16 v[96:99], a[132:135], a[28:31], v[96:99]\n\t" \
  "v_mfma_f32_16x16x32_bf16 v[96:99], a[136:139], a[44:47], v[96:99]\n\t" \
  "v_mfma_f32_16x16x32_bf16 v[96:99], a[140:143], a[60:63], v[96:99]\n\t" \
  "v_mfma_f32_16x16x32_bf16 v[112:115], a[156:159], a[60:63], 0\n\t" \
  "buffer_load_dwordx4 v[48:51], v242, %[ws], s73 offen offset:0 sc1\n\t" \
  "v_mfma_f32_16x16x32_bf16 v[112:115], a[152:155], a[44:47], v[112:115]\n\t" \
  "v_mfma_f32_16x16x32_bf16 v[112:115], a[148:151], a[28:31], v[112:115]\n\t" \
  "v_mfma_f32_16x16x32_bf16 v[112:115], a[144:147], a[12:15], v[112:115]\n\t" \
  "s_waitcnt lgkmcnt(8)\n\t" \
  "v_mfma_f32_16x16x32_bf16 v[100:103], a[128:131], a[76:79], 0\n\t" \
  "buffer_load_dwordx4 v[52:55], v242, %[ws], s73 offen offset:1024 sc1\n\t" \
  "v_mfma_f32_16x16x32_bf16 v[100:103], a[132:135], a[92:95], v[100:103]\n\t" \
  "v_mfma_f32_16x16x32_bf16 v[100:103], a[136:139], a[108:111], v[100:103]\n\t" \
  "v_mfma_f32_16x16x32_bf16 v[100:103], a[140:143], a[124:127], v[100:103]\n\t" \
  "v_mfma_f32_16x16x32_bf16 v[116:119], a[156:159], a[124:127], 0\n\t" \
  "v_mfma_f32_16x16x32_bf16 v[116:119], a[152:155], a[108:111], v[116:119]\n\t" \
  "buffer_load_dwordx4 v[56:59], v242, %[ws], s73 offen offset:2048 sc1\n\t" \
  "v_mfma_f32_16x16x32_bf16 v[116:119], a[148:151], a[92:95], v[116:119]\n\t" \
  "v_mfma_f32_16x16x32_bf16 v[116:119], a[144:147], a[76:79], v[116:119]\n\t" \
  "s_waitcnt lgkmcnt(4)\n\t" \
  "v_mfma_f32_16x16x32_bf16 v[104:107], a[128:131], a[208:211], 0\n\t" \
  "v_xor_b32 v236, 0xc000, v234\n\t" \
  "v_mfma_f32_16x16x32_bf16 v[104:107], a[132:135], a[212:215], v[104:107]\n\t" \
  "buffer_load_dwordx4 v[60:63], v242, %[ws], s73 offen offset:3072 sc1\n\t" \
  "s_xor_b32 s73, %[eso], 294912\n\t" \
  "v_mfma_f32_16x16x32_bf16 v[104:107], a[136:139], a[216:219], v[104:107]\n\t" \
  "ds_read_b64_tr_b16 a[160:161], v236 offset:0\n\t" \
  "v_mfma_f32_16x16x32_bf16 v[104:107], a[140:143], a[220:223], v[104:107]\n\t" \
  "ds_read_b64_tr_b16 a[162:163], v236 offset:4096\n\t" \
  "v_mfma_f32_16x16x32_bf16 v[120:123], a[156:159], a[220:223], 0\n\t" \
  "ds_read_b64_tr_b16 a[164:165], v236 offset:8192\n\t" \
  "v_mfma_f32_16x16x32_bf16 v[120:123], a[152:155], a[216:219], v[120:123]\n\t" \
  "ds_read_b64_tr_b16 a[166:167], v236 offset:12288\n\t" \
  "v_mfma_f32_16x16x32_bf16 v[120:123], a[148:151], a[212:215], v[120:123]\n\t" \
  "buffer_load_dwordx4 v[80:83], v242, %[ws], s73 offen offset:0 sc1\n\t" \
  "v_mfma_f32_16x16x32_bf16 v[120:123], a[144:147], a[208:211], v[120:123]\n\t" \
  "s_waitcnt lgkmcnt(4)\n\t" \
  "ds_read_b64_tr_b16 a[168:169], v236 offset:16384\n\t" \
  "v_mfma_f32_16x16x32_bf16 v[108:111], a[128:131], a[192:195], 0\n\t" \
  "ds_read_b64_tr_b16 a[170:171], v236 offset:20480\n\t" \
  "v_mfma_f32_16x16x32_bf16 v[108:111], a[132:135], a[196:199], v[108:111]\n\t" \
  "ds_read_b64_tr_b16 a[172:173], v236 offset:24576\n\t" \
  "v_mfma_f32_16x16x32_bf16 v[108:111], a[136:139], a[200:203], v[108:111]\n\t" \
  "buffer_load_dwordx4 v[84:87], v242, %[ws], s73 offen offset:1024 sc1\n\t" \
  "v_mfma_f32_16x16x32_bf16 v[108:111], a[140:143], a[204:207], v[108:111]\n\t" \
  "ds_read_b64_tr_b16 a[174:175], v236 offset:28672\n\t" \
  "v_xor_b32 v237, 0xc020, v234\n\t" \
  "v_mfma_f32_16x16x32_bf16 v[124:127], a[156:159], a[204:207], 0\n\t" \
  "ds_read_b64_tr_b16 a[176:177], v237 offset:0\n\t" \
  "v_mfma_f32_16x16x32_bf16 v[124:127], a[152:155], a[200:203], v[124:127]\n\t" \
  "ds_read_b64_tr_b16 a[178:179], v237 offset:4096\n\t" \
  "v_mfma_f32_16x16x32_bf16 v[124:127], a[148:151], a[196:199], v[124:127]\n\t" \
  "buffer_load_dwordx4 v[88:91], v242, %[ws], s73 offen offset:2048 sc1\n\t" \
  "v_mfma_f32_16x16x32_bf16 v[124:127], a[144:147], a[192:195], v[124:127]\n\t" \
  "s_waitcnt vmcnt(11) lgkmcnt(6)\n\t" \
  "ds_read_b64_tr_b16 a[180:181], v237 offset:8192\n\t" \
  "ds_read_b64_tr_b16 a[182:183], v237 offset:12288\n\t" \
  "buffer_load_dword v246, v245, %[ws], %[fw2] offen sc1\n\t" \
  STP("v_writelane_b32 %[stv], s94, 24\n\t") \
  STP("v_writelane_b32 %[stv], s92, 25\n\t") \
  STP("s_memrealtime s[94:95]\n\t") \
  STP("s_memtime s[92:93]\n\t") \
  "v_mfma_f32_16x16x32_bf16 a[0:3], a[160:163], v[16:19], a[0:3]\n\t" \
  "ds_read_b64_tr_b16 a[184:185], v237 offset:16384\n\t" \
  "v_mfma_f32_16x16x32_bf16 a[0:3], a[164:167], v[20:23], a[0:3]\n\t" \
  "s_waitcnt lgkmcnt(5)\n\t" \
  "ds_read_b64_tr_b16 a[186:187], v237 offset:20480\n\t" \
  "v_mfma_f32_16x16x32_bf16 a[0:3], a[168:171], v[24:27], a[0:3]\n\t" \
  "ds_read_b64_tr_b16 a[188:189], v237 offset:24576\n\t" \
  "v_mfma_f32_16x16x32_bf16 a[0:3], a[172:175], v[28:31], a[0:3]\n\t" \
  "s_waitcnt vmcnt(8)\n\t" \
  "ds_read_b64_tr_b16 a[190:191], v237 offset:28672\n\t" \
  "v_xor_b32 v235, 0xc040, v234\n\t" \
  "v_mfma_f32_16x16x32_bf16 a[4:7], a[172:175], v[12:15], a[4:7]\n\t" \
  "buffer_load_dwordx4 v[92:95], v242, %[ws], s73 offen offset:3072 sc1\n\t" \
  "v_mfma_f32_16x16x32_bf16 a[4:7], a[168:171], v[8:11], a[4:7]\n\t" \
  "ds_read_b64_tr_b16 a[128:129], v235 offset:0\n\t" \
  "ds_read_b64_tr_b16 a[130:131], v235 offset:4096\n\t" \
  "v_mfma_f32_16x16x32_bf16 a[4:7], a[164:167], v[4:7], a[4:7]\n\t" \
  "ds_read_b64_tr_b16 a[132:133], v235 offset:8192\n\t" \
  "v_mfma_f32_16x16x32_bf16 a[4:7], a[160:163], v[0:3], a[4:7]\n\t" \
  "s_waitcnt lgkmcnt(7)\n\t" \
  "ds_read_b64_tr_b16 a[134:135], v235 offset:12288\n\t" \
  "v_mfma_f32_16x16x32_bf16 a[16:19], a[176:179], v[16:19], a[16:19]\n\t" \
  "buffer_load_dwordx4 v240, %[wo], s71 offen offset:0 lds\n\t" \
  "v_mfma_f32_16x16x32_bf16 a[16:19], a[180:183], v[20:23], a[16:19]\n\t" \
  "s_waitcnt lgkmcnt(4)\n\t" \
  "ds_read_b64_tr_b16 a[136:137], v235 offset:16384\n\t" \
  "ds_read_b64_tr_b16 a[138:139], v235 offset:20480\n\t" \
  "v_mfma_f32_16x16x32_bf16 a[16:19], a[184:187], v[24:27], a[16:19]\n\t" \
  "ds_read_b64_tr_b16 a[140:141], v235 offset:24576\n\t" \
  "v_mfma_f32_16x16x32_bf16 a[16:19], a[188:191], v[28:31], a[16:19]\n\t" \
  "ds_read_b64_tr_b16 a[142:143], v235 offset:28672\n\t" \
  "v_xor_b32 v236, 0xc060, v234\n\t" \
  "v_mfma_f32_16x16x32_bf16 a[20:23], a[188:191], v[12:15], a[20:23]\n\t" \
  "ds_read_b64_tr_b16 a[160:161], v236 offset:0\n\t" \
  "v_mfma_f32_16x16x32_bf16 a[20:23], a[184:187], v[8:11], a[20:23]\n\t" \
  "buffer_load_dwordx4 v241, %[wo], s71 offen offset:1024 lds\n\t" \
  "v_mfma_f32_16x16x32_bf16 a[20:23], a[180:183], v[4:7], a[20:23]\n\t" \
  "ds_read_b64_tr_b16 a[162:163], v236 offset:4096\n\t" \
  "ds_read_b64_tr_b16 a[164:165], v236 offset:8192\n\t" \
  "v_mfma_f32_16x16x32_bf16 a[20:23], a[176:179], v[0:3], a[20:23]\n\t" \
  "s_waitcnt lgkmcnt(7)\n\t" \
  "ds_read_b64_tr_b16 a[166:167], v236 offset:12288\n\t" \
  "v_mfma_f32_16x16x32_bf16 a[32:35], a[128:131], v[16:19], a[32:35]\n\t" \
  "s_xor_b32 s73, %[eso], 327680\n\t" \
  "ds_read_b64_tr_b16 a[168:169], v236 offset:16384\n\t" \
  "v_mfma_f32_16x16x32_bf16 a[32:35], a[132:135], v[20:23], a[32:35]\n\t" \
  "s_waitcnt lgkmcnt(5)\n\t" \
  "buffer_load_dwordx4 v238, %[wo], s71 offen offset:2048 lds\n\t" \
  "v_mfma_f32_16x16x32_bf16 a[32:35], a[136:139], v[24:27], a[32:35]\n\t" \
  "ds_read_b64_tr_b16 a[170:171], v236 offset:20480\n\t" \
  "ds_read_b64_tr_b16 a[172:173], v236 offset:24576\n\t" \
  "v_mfma_f32_16x16x32_bf16 a[32:35], a[140:143], v[28:31], a[32:35]\n\t" \
  "ds_read_b64_tr_b16 a[174:175], v236 offset:28672\n\t" \
  "v_xor_b32 v237, 0xc080, v234\n\t" \
  "v_mfma_f32_16x16x32_bf16 a[36:39], a[140:143], v[12:15], a[36:39]\n\t" \
  "ds_read_b64_tr_b16 a[176:177], v237 offset:0\n\t" \
  "v_mfma_f32_16x16x32_bf16 a[36:39], a[136:139], v[8:11], a[36:39]\n\t" \
  "ds_read_b64_tr_b16 a[178:179], v237 offset:4096\n\t" \
  "v_mfma_f32_16x16x32_bf16 a[36:39], a[132:135], v[4:7], a[36:39]\n\t" \
  "buffer_load_dwordx4 v239, %[wo], s71 offen offset:3072 lds\n\t" \
  "s_add_u32 m0, %[m], 16384\n\t" \
  "v_mfma_f32_16x16x32_bf16 a[36:39], a[128:131], v[0:3], a[36:39]\n\t" \
  "s_waitcnt lgkmcnt(6)\n\t" \
  "s_xor_b32 s71, %[wob], 1179648\n\t" \
  "ds_read_b64_tr_b16 a[180:181], v237 offset:8192\n\t" \
  "v_mfma_f32_16x16x32_bf16 a[48:51], a[160:163], v[16:19], a[48:51]\n\t" \
  "ds_read_b64_tr_b16 a[182:183], v237 offset:12288\n\t" \
  ".Lhs11:\n\ts_cbranch_execz .Lhs11n\n\t" \
  "ds_read_b64_tr_b16 a[184:185], v237 offset:16384\n\t" \
  "v_mfma_f32_16x16x32_bf16 a[48:51], a[164:167], v[20:23], a[48:51]\n\t" \
  "s_waitcnt lgkmcnt(5)\n\t" \
  "ds_read_b64_tr_b16 a[186:187], v237 offset:20480\n\t" \
  "s_waitcnt vmcnt(5)\n\t" \
  "v_cmp_ne_u32 vcc, %[tag], v246\n\t" \
  "s_and_b32 vcc_lo, vcc_lo, 0xe00\n\t" \
  "s_cbranch_scc1 .Lop_s1\n\t" \
  ".Lop_k1:\n\t" \
  "v_mfma_f32_16x16x32_bf16 a[48:51], a[168:171], v[24:27], a[48:51]\n\t" \
  "buffer_load_dwordx4 v[64:67], v242, %[ws], s73 offen offset:0 sc1\n\t" \
  "v_mfma_f32_16x16x32_bf16 a[48:51], a[172:175], v[28:31], a[48:51]\n\t" \
  "ds_read_b64_tr_b16 a[188:189], v237 offset:24576\n\t" \
  "ds_read_b64_tr_b16 a[190:191], v237 offset:28672\n\t" \
  "v_mfma_f32_16x16x32_bf16 a[52:55], a[172:175], v[12:15], a[52:55]\n\t" \
  "v_xor_b32 v235, 0xc0a0, v234\n\t" \
  "v_mfma_f32_16x16x32_bf16 a[52:55], a[168:171], v[8:11], a[52:55]\n\t" \
  "ds_read_b64_tr_b16 a[144:145], v235 offset:0\n\t" \
  "ds_read_b64_tr_b16 a[146:147], v235 offset:4096\n\t" \
  "v_mfma_f32_16x16x32_bf16 a[52:55], a[164:167], v[4:7], a[52:55]\n\t" \
  "buffer_load_dwordx4 v[68:71], v242, %[ws], s73 offen offset:1024 sc1\n\t" \
  "v_mfma_f32_16x16x32_bf16 a[52:55], a[160:163], v[0:3], a[52:55]\n\t" \
  "s_waitcnt lgkmcnt(6)\n\t" \
  "ds_read_b64_tr_b16 a[148:149], v235 offset:8192\n\t" \
  "ds_read_b64_tr_b16 a[150:151], v235 offset:12288\n\t" \
  "v_mfma_f32_16x16x32_bf16 a[64:67], a[176:179], v[16:19], a[64:67]\n\t" \
  "ds_read_b64_tr_b16 a[152:153], v235 offset:16384\n\t" \
  "v_mfma_f32_16x16x32_bf16 a[64:67], a[180:183], v[20:23], a[64:67]\n\t" \
  "s_waitcnt lgkmcnt(5)\n\t" \
  "ds_read_b64_tr_b16 a[154:155], v235 offset:20480\n\t" \
  "v_mfma_f32_16x16x32_bf16 a[64:67], a[184:187], v[24:27], a[64:67]\n\t" \
  "ds_read_b64_tr_b16 a[156:157], v235 offset:24576\n\t" \
  "v_mfma_f32_16x16x32_bf16 a[64:67], a[188:191], v[28:31], a[64:67]\n\t" \
  "buffer_load_dwordx4 v[72:75], v242, %[ws], s73 offen offset:2048 sc1\n\t" \
  "v_mfma_f32_16x16x32_bf16 a[68:71], a[188:191], v[12:15], a[68:71]\n\t" \
  "ds_read_b64_tr_b16 a[158:159], v235 offset:28672\n\t" \
  "v_xor_b32 v236, 0xc0c0, v234\n\t" \
  "v_mfma_f32_16x16x32_bf16 a[68:71], a[184:187], v[8:11], a[68:71]\n\t" \
  "ds_read_b64_tr_b16 a[128:129], v236 offset:0\n\t" \
  "ds_read_b64_tr_b16 a[130:131], v236 offset:4096\n\t" \
  "v_mfma_f32_16x16x32_bf16 a[68:71], a[180:183], v[4:7], a[68:71]\n\t" \
  "ds_read_b64_tr_b16 a[132:133], v236 offset:8192\n\t" \
  "v_mfma_f32_16x16x32_bf16 a[68:71], a[176:179], v[0:3], a[68:71]\n\t" \
  "s_waitcnt lgkmcnt(7)\n\t" \
  "s_barrier\n\t" \
  "v_mfma_f32_16x16x32_bf16 a[80:83], a[144:147], v[16:19], a[80:83]\n\t" \
  "buffer_load_dwordx4 v[76:79], v242, %[ws], s73 offen offset:3072 sc1\n\t" \
  "v_mfma_f32_16x16x32_bf16 a[80:83], a[148:151], v[20:23], a[80:83]\n\t" \
  "s_waitcnt lgkmcnt(3)\n\t" \
  "ds_read_b64_tr_b16 a[134:135], v236 offset:12288\n\t" \
  "ds_read_b64_tr_b16 a[136:137], v236 offset:16384\n\t" \
  "v_mfma_f32_16x16x32_bf16 a[80:83], a[152:155], v[24:27], a[80:83]\n\t" \
  "ds_read_b64_tr_b16 a[138:139], v236 offset:20480\n\t" \
  "ds_read_b64_tr_b16 a[140:141], v236 offset:24576\n\t" \
  "v_mfma_f32_16x16x32_bf16 a[80:83], a[156:159], v[28:31], a[80:83]\n\t" \
  "ds_read_b64_tr_b16 a[142:143], v236 offset:28672\n\t" \
  "v_xor_b32 v237, 0xc0e0, v234\n\t" \
  "v_mfma_f32_16x16x32_bf16 a[84:87], a[156:159], v[12:15], a[84:87]\n\t" \
  "buffer_load_dwordx4 v240, %[wo], s71 offen offset:0 lds\n\t" \
  "v_mfma_f32_16x16x32_bf16 a[84:87], a[152:155], v[8:11], a[84:87]\n\t" \
  "ds_read_b64_tr_b16 a[12:13], v237 offset:0\n\t" \
  "ds_read_b64_tr_b16 a[14:15], v237 offset:4096\n\t" \
  "v_mfma_f32_16x16x32_bf16 a[84:87], a[148:151], v[4:7], a[84:87]\n\t" \
  "ds_read_b64_tr_b16 a[28:29], v237 offset:8192\n\t" \
  "v_mfma_f32_16x16x32_bf16 a[84:87], a[144:147], v[0:3], a[84:87]\n\t" \
  "s_waitcnt lgkmcnt(7)\n\t" \
  "ds_read_b64_tr_b16 a[30:31], v237 offset:12288\n\t" \
  "v_mfma_f32_16x16x32_bf16 a[96:99], a[128:131], v[16:19], a[96:99]\n\t" \
  "buffer_load_dwordx4 v241, %[wo], s71 offen offset:1024 lds\n\t" \
  "v_mfma_f32_16x16x32_bf16 a[96:99], a[132:135], v[20:23], a[96:99]\n\t" \
  "s_waitcnt lgkmcnt(4)\n\t" \
  "ds_read_b64_tr_b16 a[44:45], v237 offset:16384\n\t" \
  "ds_read_b64_tr_b16 a[46:47], v237 offset:20480\n\t" \
  "v_mfma_f32_16x16x32_bf16 a[96:99], a[136:139], v[24:27], a[96:99]\n\t" \
  "ds_read_b64_tr_b16 a[60:61], v237 offset:24576\n\t" \
  "v_lshrrev_b32 v244, 4, v242\n\t" \
  "v_mfma_f32_16x16x32_bf16 a[96:99], a[140:143], v[28:31], a[96:99]\n\t" \
  "s_lshl_b32 s77, %[hl], 2\n\t" \
  "ds_read_b64_tr_b16 a[62:63], v237 offset:28672\n\t" \
  "v_xor_b32 v235, 0x14000, v234\n\t" \
  "v_mfma_f32_16x16x32_bf16 a[100:103], a[140:143], v[12:15], a[100:103]\n\t" \
  "buffer_load_dwordx4 v238, %[wo], s71 offen offset:2048 lds\n\t" \
  "v_mfma_f32_16x16x32_bf16 a[100:103], a[136:139], v[8:11], a[100:103]\n\t" \
  "s_add_u32 s77, s77, %[wid]\n\t" \
  "ds_read_b64_tr_b16 a[160:161], v235 offset:0\n\t" \
  "ds_read_b64_tr_b16 a[162:163], v235 offset:4096\n\t" \
  "v_mfma_f32_16x16x32_bf16 a[100:103], a[132:135], v[4:7], a[100:103]\n\t" \
  "s_lshl_b32 s77, s77, 6\n\t" \
  "ds_read_b64_tr_b16 a[164:165], v235 offset:8192\n\t" \
  "v_add_u32 v230, 0x10000, v242\n\t" \
  "v_mfma_f32_16x16x32_bf16 a[100:103], a[128:131], v[0:3], a[100:103]\n\t" \
  "s_waitcnt lgkmcnt(7)\n\t" \
  "ds_read_b64_tr_b16 a[166:167], v235 offset:12288\n\t" \
  "v_xor_b32 v236, 0x24000, v234\n\t" \
  "v_mfma_f32_16x16x32_bf16 a[112:115], a[12:15], v[16:19], a[112:115]\n\t" \
  "ds_read_b64_tr_b16 a[168:169], v236 offset:0\n\t" \
  "v_add_u32 v232, 0x20000, v242\n\t" \
  "v_mfma_f32_16x16x32_bf16 a[112:115], a[28:31], v[20:23], a[112:115]\n\t" \
  "s_waitcnt lgkmcnt(5)\n\t" \
  "buffer_load_dwordx4 v239, %[wo], s71 offen offset:3072 lds\n\t" \
  "s_add_u32 m0, %[m], 98304\n\t" \
  "v_mfma_f32_16x16x32_bf16 a[112:115], a[44:47], v[24:27], a[112:115]\n\t" \
  "s_xor_b32 s71, %[wob], 1310720\n\t" \
  "ds_read_b64_tr_b16 a[170:171], v236 offset:4096\n\t" \
  "ds_read_b64_tr_b16 a[172:173], v236 offset:8192\n\t" \
  "v_mfma_f32_16x16x32_bf16 a[112:115], a[60:63], v[28:31], a[112:115]\n\t" \
  "s_xor_b32 s73, %[eso], 393216\n\t" \
  "ds_read_b64_tr_b16 a[174:175], v236 offset:12288\n\t" \
  "s_add_u32 s70, %[m], 147456\n\t" \
  "v_mfma_f32_16x16x32_bf16 a[116:119], a[60:63], v[12:15], a[116:119]\n\t" \
  "s_lshl_b32 s69, s77, 2\n\t" \
  "v_xor_b32 v237, 0x14020, v234\n\t" \
  "s_add_u32 s69, s69, 2228224\n\t" \
  "v_mfma_f32_16x16x32_bf16 a[116:119], a[44:47], v[8:11], a[116:119]\n\t" \
  "buffer_load_dwordx4 v240, %[wo], s71 offen offset:0 lds\n\t" \
  "v_mfma_f32_16x16x32_bf16 a[116:119], a[28:31], v[4:7], a[116:119]\n\t" \
  "s_bfe_u32 s68, %[eso], 0x2000f\n\t" \
  "ds_read_b64_tr_b16 a[176:177], v237 offset:0\n\t" \
  "v_mfma_f32_16x16x32_bf16 a[116:119], a[12:15], v[0:3], a[116:119]\n\t" \
  "s_waitcnt lgkmcnt(5)\n\t" \
  "ds_read_b64_tr_b16 a[178:179], v237 offset:4096\n\t" \
  "s_lshl_b32 s60, s68, 17\n\t" \
  STP("v_writelane_b32 %[stv], s94, 26\n\t") \
  STP("v_writelane_b32 %[stv], s92, 27\n\t") \
  STP("s_memrealtime s[94:95]\n\t") \
  STP("s_memtime s[92:93]\n\t") \
  "v_mfma_f32_16x16x32_bf16 a[0:3], a[160:163], v[48:51], a[0:3]\n\t" \
  "ds_read_b64_tr_b16 a[180:181], v237 offset:8192\n\t" \
  "v_mfma_f32_16x16x32_bf16 a[0:3], a[164:167], v[52:55], a[0:3]\n\t" \
  "s_waitcnt lgkmcnt(3)\n\t" \
  "ds_read_b64_tr_b16 a[182:183], v237 offset:12288\n\t" \
  "v_xor_b32 v235, 0x24020, v234\n\t" \
  "v_mfma_f32_16x16x32_bf16 a[0:3], a[168:171], v[56:59], a[0:3]\n\t" \
  "buffer_load_dwordx4 v[16:19], v242, %[ws], s73 offen offset:0 sc1\n\t" \
  "v_mfma_f32_16x16x32_bf16 a[0:3], a[172:175], v[60:63], a[0:3]\n\t" \
  "s_waitcnt vmcnt(14)\n\t" \
  "ds_read_b64_tr_b16 a[184:185], v235 offset:0\n\t" \
  "s_add_u32 s60, s60, %[xcb]\n\t" \
  "v_mfma_f32_16x16x32_bf16 a[4:7], a[172:175], v[92:95], a[4:7]\n\t" \
  "s_lshl_b32 s63, %[wid], 10\n\t" \
  "ds_read_b64_tr_b16 a[186:187], v235 offset:4096\n\t" \
  "v_and_b32 v250, 7, v244\n\t" \
  "v_mfma_f32_16x16x32_bf16 a[4:7], a[168:171], v[88:91], a[4:7]\n\t" \
  "s_add_u32 s60, s60, s63\n\t" \
  "ds_read_b64_tr_b16 a[188:189], v235 offset:8192\n\t" \
  "v_lshlrev_b32 v250, 5, v250\n\t" \
  "v_mfma_f32_16x16x32_bf16 a[4:7], a[164:167], v[84:87], a[4:7]\n\t" \
  "buffer_load_dwordx4 v[20:23], v242, %[ws], s73 offen offset:1024 sc1\n\t" \
  "v_mfma_f32_16x16x32_bf16 a[4:7], a[160:163], v[80:83], a[4:7]\n\t" \
  "s_waitcnt vmcnt(7)\n\t" \
  "ds_read_b64_tr_b16 a[190:191], v235 offset:12288\n\t" \
  "v_lshrrev_b32 v251, 4, v244\n\t" \
  "v_mfma_f32_16x16x32_bf16 a[8:11], a[160:163], v[64:67], a[8:11]\n\t" \
  "s_xor_b32 s61, s60, 135168\n\t" \
  "v_xor_b32 v236, 0x14040, v234\n\t" \
  "v_lshl_add_u32 v250, v251, 3, v250\n\t" \
  "v_mfma_f32_16x16x32_bf16 a[8:11], a[164:167], v[68:71], a[8:11]\n\t" \
  "ds_read_b64_tr_b16 a[144:145], v236 offset:0\n\t" \
  "v_mfma_f32_16x16x32_bf16 a[8:11], a[168:171], v[72:75], a[8:11]\n\t" \
  "buffer_load_dwordx4 v[24:27], v242, %[ws], s73 offen offset:2048 sc1\n\t" \
  "v_mfma_f32_16x16x32_bf16 a[8:11], a[172:175], v[76:79], a[8:11]\n\t" \
  "s_waitcnt lgkmcnt(5)\n\t" \
  "ds_read_b64_tr_b16 a[146:147], v236 offset:4096\n\t" \
  "s_xor_b32 s62, s60, 278528\n\t" \
  "v_mfma_f32_16x16x32_bf16 a[16:19], a[176:179], v[48:51], a[16:19]\n\t" \
  "s_lshl_b32 s65, s68, 7\n\t" \
  "ds_read_b64_tr_b16 a[148:149], v236 offset:8192\n\t" \
  "v_mfma_f32_16x16x32_bf16 a[16:19], a[180:183], v[52:55], a[16:19]\n\t" \
  "s_waitcnt lgkmcnt(3)\n\t" \
  "ds_read_b64_tr_b16 a[150:151], v236 offset:12288\n\t" \
  "v_xor_b32 v237, 0x24040, v234\n\t" \
  "v_mfma_f32_16x16x32_bf16 a[16:19], a[184:187], v[56:59], a[16:19]\n\t" \
  "s_add_u32 s65, s65, %[frb]\n\t" \
  "ds_read_b64_tr_b16 a[152:153], v237 offset:0\n\t" \
  "v_mfma_f32_16x16x32_bf16 a[16:19], a[188:191], v[60:63], a[16:19]\n\t" \
  "buffer_load_dwordx4 v[28:31], v242, %[ws], s73 offen offset:3072 sc1\n\t" \
  "s_xor_b32 s73, %[eso], 425984\n\t" \
  "v_mfma_f32_16x16x32_bf16 a[20:23], a[188:191], v[92:95], a[20:23]\n\t" \
  "s_xor_b32 s66, s65, 144\n\t" \
  "ds_read_b64_tr_b16 a[154:155], v237 offset:4096\n\t" \
  "v_mfma_f32_16x16x32_bf16 a[20:23], a[184:187], v[88:91], a[20:23]\n\t" \
  "ds_read_b64_tr_b16 a[156:157], v237 offset:8192\n\t" \
  "v_mfma_f32_16x16x32_bf16 a[20:23], a[180:183], v[84:87], a[20:23]\n\t" \
  "ds_read_b64_tr_b16 a[158:159], v237 offset:12288\n\t" \
  "v_mfma_f32_16x16x32_bf16 a[20:23], a[176:179], v[80:83], a[20:23]\n\t" \
  "buffer_load_dwordx4 v[0:3], v242, %[ws], s73 offen offset:0 sc1\n\t" \
  "v_mfma_f32_16x16x32_bf16 a[24:27], a[176:179], v[64:67], a[24:27]\n\t" \
  "s_xor_b32 s67, s65, 288\n\t" \
  "v_xor_b32 v235, 0x14060, v234\n\t" \
  "v_mfma_f32_16x16x32_bf16 a[24:27], a[180:183], v[68:71], a[24:27]\n\t" \
  "s_xor_b32 s68, s65, 384\n\t" \
  "ds_read_b64_tr_b16 a[128:129], v235 offset:0\n\t" \
  "v_mfma_f32_16x16x32_bf16 a[24:27], a[184:187], v[72:75], a[24:27]\n\t" \
  "s_lshr_b32 s75, %[m], 4\n\t" \
  "s_bitset1_b32 s75, 17\n\t" \
  "v_add_u32 v250, s75, v250\n\t" \
  "v_mfma_f32_16x16x32_bf16 a[24:27], a[188:191], v[76:79], a[24:27]\n\t" \
  "s_waitcnt lgkmcnt(5)\n\t" \
  "ds_read_b64_tr_b16 a[130:131], v235 offset:4096\n\t" \
  "s_andn2_b32 s68, s68, 12\n\t" \
  "v_mfma_f32_16x16x32_bf16 a[32:35], a[144:147], v[48:51], a[32:35]\n\t" \
  "buffer_load_dwordx4 v[4:7], v242, %[ws], s73 offen offset:1024 sc1\n\t" \
  "v_mfma_f32_16x16x32_bf16 a[32:35], a[148:151], v[52:55], a[32:35]\n\t" \
  "s_waitcnt lgkmcnt(2)\n\t" \
  "ds_read_b64_tr_b16 a[132:133], v235 offset:8192\n\t" \
  "s_andn2_b32 s64, 0x2000, %[na]\n\t" \
  "v_mfma_f32_16x16x32_bf16 a[32:35], a[152:155], v[56:59], a[32:35]\n\t" \
  "v_permlane32_swap_b32 v132, v134\n\t" \
  "v_mfma_f32_16x16x32_bf16 a[32:35], a[156:159], v[60:63], a[32:35]\n\t" \
  "ds_read_b64_tr_b16 a[134:135], v235 offset:12288\n\t" \
  "v_xor_b32 v236, 0x24060, v234\n\t" \
  "v_mfma_f32_16x16x32_bf16 a[36:39], a[156:159], v[92:95], a[36:39]\n\t" \
  "buffer_load_dwordx4 v[8:11], v242, %[ws], s73 offen offset:2048 sc1\n\t" \
  "v_mfma_f32_16x16x32_bf16 a[36:39], a[152:155], v[88:91], a[36:39]\n\t" \
  "s_xor_b32 s63, s60, 393216\n\t" \
  "ds_read_b64_tr_b16 a[136:137], v236 offset:0\n\t" \
  "ds_read_b64_tr_b16 a[138:139], v236 offset:4096\n\t" \
  "v_mfma_f32_16x16x32_bf16 a[36:39], a[148:151], v[84:87], a[36:39]\n\t" \
  "s_andn2_b32 s63, s63, 98304\n\t" \
  "v_permlane32_swap_b32 v128, v130\n\t" \
  "v_mfma_f32_16x16x32_bf16 a[36:39], a[144:147], v[80:83], a[36:39]\n\t" \
  "ds_read_b64_tr_b16 a[140:141], v236 offset:8192\n\t" \
  "ds_read_b64_tr_b16 a[142:143], v236 offset:12288\n\t" \
  "v_mfma_f32_16x16x32_bf16 a[40:43], a[144:147], v[64:67], a[40:43]\n\t" \
  "buffer_load_dwordx4 v[12:15], v242, %[ws], s73 offen offset:3072 sc1\n\t" \
  "s_xor_b32 s73, %[eso], 458752\n\t" \
  "v_mfma_f32_16x16x32_bf16 a[40:43], a[148:151], v[68:71], a[40:43]\n\t" \
  "v_permlane32_swap_b32 v133, v135\n\t" \
  "v_mfma_f32_16x16x32_bf16 a[40:43], a[152:155], v[72:75], a[40:43]\n\t" \
  "v_permlane32_swap_b32 v129, v131\n\t" \
  "v_mfma_f32_16x16x32_bf16 a[40:43], a[156:159], v[76:79], a[40:43]\n\t" \
  "s_waitcnt lgkmcnt(4)\n\t" \
  "v_xor_b32 v237, 0x14080, v234\n\t" \
  "v_add_f32 v132, v132, v134\n\t" \
  "v_mfma_f32_16x16x32_bf16 a[48:51], a[128:131], v[48:51], a[48:51]\n\t" \
  "ds_read_b64_tr_b16 a[76:77], v237 offset:0\n\t" \
  "v_add_f32 v128, v128, v130\n\t" \
  "v_mfma_f32_16x16x32_bf16 a[48:51], a[132:135], v[52:55], a[48:51]\n\t" \
  "s_waitcnt lgkmcnt(1)\n\t" \
  "buffer_load_dwordx4 v[32:35], v242, %[ws], s73 offen offset:0 sc1\n\t" \
  "v_mfma_f32_16x16x32_bf16 a[48:51], a[136:139], v[56:59], a[48:51]\n\t" \
  "ds_read_b64_tr_b16 a[78:79], v237 offset:4096\n\t" \
  "ds_read_b64_tr_b16 a[92:93], v237 offset:8192\n\t" \
  "v_mfma_f32_16x16x32_bf16 a[48:51], a[140:143], v[60:63], a[48:51]\n\t" \
  "s_barrier\n\t" \
  "v_mfma_f32_16x16x32_bf16 a[52:55], a[140:143], v[92:95], a[52:55]\n\t" \
  "ds_read_b64_tr_b16 a[94:95], v237 offset:12288\n\t" \
  "v_xor_b32 v235, 0x24080, v234\n\t" \
  "v_mfma_f32_16x16x32_bf16 a[52:55], a[136:139], v[88:91], a[52:55]\n\t" \
  "buffer_load_dwordx4 v[36:39], v242, %[ws], s73 offen offset:1024 sc1\n\t" \
  "buffer_load_dword v246, v245, %[ws], %[fw2] offen sc1\n\t" \
  "v_mfma_f32_16x16x32_bf16 a[52:55], a[132:135], v[84:87], a[52:55]\n\t" \
  "ds_read_b64_tr_b16 a[108:109], v235 offset:0\n\t" \
  "ds_read_b64_tr_b16 a[110:111], v235 offset:4096\n\t" \
  "v_add_f32 v133, v133, v135\n\t" \
  "v_mfma_f32_16x16x32_bf16 a[52:55], a[128:131], v[80:83], a[52:55]\n\t" \
  "ds_read_b64_tr_b16 a[124:125], v235 offset:8192\n\t" \
  "ds_read_b64_tr_b16 a[126:127], v235 offset:12288\n\t" \
  "v_mfma_f32_16x16x32_bf16 a[56:59], a[128:131], v[64:67], a[56:59]\n\t" \
  "s_bitcmp1_b32 %[wid], 0\n\t" \
  "s_cselect_b32 s72, s64, 0\n\t" \
  "s_xor_b32 s63, s63, s72\n\t" \
  "v_mfma_f32_16x16x32_bf16 a[56:59], a[132:135], v[68:71], a[56:59]\n\t" \
  "s_xor_b32 s64, s64, s63\n\t" \
  "v_add_f32 v129, v129, v131\n\t" \
  "v_xor_b32 v236, 0x140a0, v234\n\t" \
  "v_mfma_f32_16x16x32_bf16 a[56:59], a[136:139], v[72:75], a[56:59]\n\t" \
  "ds_read_b64_tr_b16 a[144:145], v236 offset:0\n\t" \
  "v_mfma_f32_16x16x32_bf16 a[56:59], a[140:143], v[76:79], a[56:59]\n\t" \
  "s_waitcnt lgkmcnt(5)\n\t" \
  "buffer_load_dwordx4 v[40:43], v242, %[ws], s73 offen offset:2048 sc1\n\t" \
  "v_mfma_f32_16x16x32_bf16 a[64:67], a[76:79], v[48:51], a[64:67]\n\t" \
  "ds_read_b64_tr_b16 a[146:147], v236 offset:4096\n\t" \
  "v_permlane16_swap_b32 v132, v133\n\t" \
  "v_mfma_f32_16x16x32_bf16 a[64:67], a[92:95], v[52:55], a[64:67]\n\t" \
  "s_waitcnt lgkmcnt(2)\n\t" \
  "ds_read_b64_tr_b16 a[148:149], v236 offset:8192\n\t" \
  "v_mfma_f32_16x16x32_bf16 a[64:67], a[108:111], v[56:59], a[64:67]\n\t" \
  "ds_read_b64_tr_b16 a[150:151], v236 offset:12288\n\t" \
  "v_xor_b32 v237, 0x240a0, v234\n\t" \
  "v_mfma_f32_16x16x32_bf16 a[64:67], a[124:127], v[60:63], a[64:67]\n\t" \
  "buffer_load_dwordx4 v[44:47], v242, %[ws], s73 offen offset:3072 sc1\n\t" \
  "v_mfma_f32_16x16x32_bf16 a[68:71], a[124:127], v[92:95], a[68:71]\n\t" \
  "ds_read_b64_tr_b16 a[152:153], v237 offset:0\n\t" \
  "v_permlane16_swap_b32 v128, v129\n\t" \
  "v_mfma_f32_16x16x32_bf16 a[68:71], a[108:111], v[88:91], a[68:71]\n\t" \
  "ds_read_b64_tr_b16 a[154:155], v237 offset:4096\n\t" \
  "v_add_f32 v132, v132, v133\n\t" \
  "v_mfma_f32_16x16x32_bf16 a[68:71], a[92:95], v[84:87], a[68:71]\n\t" \
  "ds_read_b64_tr_b16 a[156:157], v237 offset:8192\n\t" \
  "v_add_f32 v128, v128, v129\n\t" \
  "v_mfma_f32_16x16x32_bf16 a[68:71], a[76:79], v[80:83], a[68:71]\n\t" \
  "ds_read_b64_tr_b16 a[158:159], v237 offset:12288\n\t" \
  "v_add_f32_dpp v248, v132, v132 row_ror:8 row_mask:0xf bank_mask:0xf\n\t" \
  "v_mfma_f32_16x16x32_bf16 a[72:75], a[76:79], v[64:67], a[72:75]\n\t" \
  "buffer_load_dwordx4 v241, %[wo], s71 offen offset:1024 lds\n\t" \
  "v_mfma_f32_16x16x32_bf16 a[72:75], a[92:95], v[68:71], a[72:75]\n\t" \
  "v_xor_b32 v235, 0x140c0, v234\n\t" \
  "v_add_f32_dpp v249, v128, v128 row_ror:8 row_mask:0xf bank_mask:0xf\n\t" \
  "v_mfma_f32_16x16x32_bf16 a[72:75], a[108:111], v[72:75], a[72:75]\n\t" \
  "ds_read_b64_tr_b16 a[12:13], v235 offset:0\n\t" \
  "v_mfma_f32_16x16x32_bf16 a[72:75], a[124:127], v[76:79], a[72:75]\n\t" \
  "s_waitcnt lgkmcnt(5)\n\t" \
  "ds_read_b64_tr_b16 a[14:15], v235 offset:4096\n\t" \
  "v_mfma_f32_16x16x32_bf16 a[80:83], a[144:147], v[48:51], a[80:83]\n\t" \
  "buffer_load_dwordx4 v238, %[wo], s71 offen offset:2048 lds\n\t" \
  "v_mfma_f32_16x16x32_bf16 a[80:83], a[148:151], v[52:55], a[80:83]\n\t" \
  "s_waitcnt lgkmcnt(2)\n\t" \
  "ds_read_b64_tr_b16 a[28:29], v235 offset:8192\n\t" \
  "v_mfma_f32_16x16x32_bf16 a[80:83], a[152:155], v[56:59], a[80:83]\n\t" \
  "s_xor_b32 s74, %[eso], 98304\n\t" \
  "ds_read_b64_tr_b16 a[30:31], v235 offset:12288\n\t" \
  "v_xor_b32 v236, 0x240c0, v234\n\t" \
  "v_mfma_f32_16x16x32_bf16 a[80:83], a[156:159], v[60:63], a[80:83]\n\t" \
  "ds_read_b64_tr_b16 a[44:45], v236 offset:0\n\t" \
  "v_mfma_f32_16x16x32_bf16 a[84:87], a[156:159], v[92:95], a[84:87]\n\t" \
  "ds_read_b64_tr_b16 a[46:47], v236 offset:4096\n\t" \
  "v_mfma_f32_16x16x32_bf16 a[84:87], a[152:155], v[88:91], a[84:87]\n\t" \
  "buffer_load_dwordx4 v239, %[wo], s71 offen offset:3072 lds\n\t" \
  "s_add_u32 m0, %[m], 32768\n\t" \
  "v_mfma_f32_16x16x32_bf16 a[84:87], a[148:151], v[84:87], a[84:87]\n\t" \
  "s_xor_b32 s71, %[wob], 1441792\n\t" \
  "ds_read_b64_tr_b16 a[60:61], v236 offset:8192\n\t" \
  "v_mfma_f32_16x16x32_bf16 a[84:87], a[144:147], v[80:83], a[84:87]\n\t" \
  "buffer_store_dwordx4 v[160:163], v242, %[ws], s74 offen offset:0 sc1\n\t" \
  "v_mfma_f32_16x16x32_bf16 a[88:91], a[144:147], v[64:67], a[88:91]\n\t" \
  "s_xor_b32 s74, %[eso], 98304\n\t" \
  "ds_read_b64_tr_b16 a[62:63], v236 offset:12288\n\t" \
  "v_xor_b32 v237, 0x140e0, v234\n\t" \
  "v_mfma_f32_16x16x32_bf16 a[88:91], a[148:151], v[68:71], a[88:91]\n\t" \
  "ds_read_b64_tr_b16 a[76:77], v237 offset:0\n\t" \
  "v_mfma_f32_16x16x32_bf16 a[88:91], a[152:155], v[72:75], a[88:91]\n\t" \
  "ds_read_b64_tr_b16 a[78:79], v237 offset:4096\n\t" \
  "v_mfma_f32_16x16x32_bf16 a[88:91], a[156:159], v[76:79], a[88:91]\n\t" \
  "s_waitcnt lgkmcnt(6)\n\t" \
  "buffer_load_dwordx4 v240, %[wo], s71 offen offset:0 lds\n\t" \
  "v_mfma_f32_16x16x32_bf16 a[96:99], a[12:15], v[48:51], a[96:99]\n\t" \
  "ds_read_b64_tr_b16 a[92:93], v237 offset:8192\n\t" \
  "v_mfma_f32_16x16x32_bf16 a[96:99], a[28:31], v[52:55], a[96:99]\n\t" \
  "s_waitcnt lgkmcnt(3)\n\t" \
  "buffer_store_dwordx4 v[164:167], v242, %[ws], s74 offen offset:1024 sc1\n\t" \
  "v_mfma_f32_16x16x32_bf16 a[96:99], a[44:47], v[56:59], a[96:99]\n\t" \
  "s_xor_b32 s74, %[eso], 98304\n\t" \
  "ds_read_b64_tr_b16 a[94:95], v237 offset:12288\n\t" \
  "v_xor_b32 v235, 0x240e0, v234\n\t" \
  "v_mfma_f32_16x16x32_bf16 a[96:99], a[60:63], v[60:63], a[96:99]\n\t" \
  "ds_read_b64_tr_b16 a[108:109], v235 offset:0\n\t" \
  "v_mfma_f32_16x16x32_bf16 a[100:103], a[60:63], v[92:95], a[100:103]\n\t" \
  "ds_read_b64_tr_b16 a[110:111], v235 offset:4096\n\t" \
  "v_mfma_f32_16x16x32_bf16 a[100:103], a[44:47], v[88:91], a[100:103]\n\t" \
  "buffer_load_dwordx4 v241, %[wo], s71 offen offset:1024 lds\n\t" \
  "v_mfma_f32_16x16x32_bf16 a[100:103], a[28:31], v[84:87], a[100:103]\n\t" \
  "ds_read_b64_tr_b16 a[124:125], v235 offset:8192\n\t" \
  "v_mfma_f32_16x16x32_bf16 a[100:103], a[12:15], v[80:83], a[100:103]\n\t" \
  "buffer_store_dwordx4 v[168:171], v242, %[ws], s74 offen offset:2048 sc1\n\t" \
  "s_waitcnt vmcnt(10)\n\t" \
  "v_cmp_ne_u32 vcc, %[tag], v246\n\t" \
  "s_and_b32 vcc_lo, vcc_lo, 0xfe00\n\t" \
  "s_cbranch_scc1 .Lop_s2\n\t" \
  ".Lop_k2:\n\t" \
  "v_mfma_f32_16x16x32_bf16 a[104:107], a[12:15], v[64:67], a[104:107]\n\t" \
  "s_xor_b32 s74, %[eso], 98304\n\t" \
  "ds_read_b64_tr_b16 a[126:127], v235 offset:12288\n\t" \
  "v_xor_b32 v236, 0x1c000, v234\n\t" \
  "v_mfma_f32_16x16x32_bf16 a[104:107], a[28:31], v[68:71], a[104:107]\n\t" \
  "ds_read_b64_tr_b16 a[128:129], v236 offset:0\n\t" \
  "v_mfma_f32_16x16x32_bf16 a[104:107], a[44:47], v[72:75], a[104:107]\n\t" \
  "ds_read_b64_tr_b16 a[130:131], v236 offset:4096\n\t" \
  "v_mfma_f32_16x16x32_bf16 a[104:107], a[60:63], v[76:79], a[104:107]\n\t" \
  "s_waitcnt lgkmcnt(6)\n\t" \
  "ds_read_b64_tr_b16 a[132:133], v236 offset:8192\n\t" \
  "v_mfma_f32_16x16x32_bf16 a[112:115], a[76:79], v[48:51], a[112:115]\n\t" \
  "buffer_store_dwordx4 v[172:175], v242, %[ws], s74 offen offset:3072 sc1\n\t" \
  "v_mfma_f32_16x16x32_bf16 a[112:115], a[92:95], v[52:55], a[112:115]\n\t" \
  "s_waitcnt lgkmcnt(3)\n\t" \
  "ds_read_b64_tr_b16 a[134:135], v236 offset:12288\n\t" \
  "v_mfma_f32_16x16x32_bf16 a[112:115], a[108:111], v[56:59], a[112:115]\n\t" \
  "buffer_load_dwordx4 v238, %[wo], s71 offen offset:2048 lds\n\t" \
  "v_mfma_f32_16x16x32_bf16 a[112:115], a[124:127], v[60:63], a[112:115]\n\t" \
  "ds_read_b64_tr_b16 a[136:137], v236 offset:16384\n\t" \
  "v_mfma_f32_16x16x32_bf16 a[116:119], a[124:127], v[92:95], a[116:119]\n\t" \
  "ds_read_b64_tr_b16 a[138:139], v236 offset:20480\n\t" \
  "v_mfma_f32_16x16x32_bf16 a[116:119], a[108:111], v[88:91], a[116:119]\n\t" \
  "ds_read_b64_tr_b16 a[140:141], v236 offset:24576\n\t" \
  "v_mfma_f32_16x16x32_bf16 a[116:119], a[92:95], v[84:87], a[116:119]\n\t" \
  "buffer_load_dwordx4 v239, %[wo], s71 offen offset:3072 lds\n\t" \
  "v_mfma_f32_16x16x32_bf16 a[116:119], a[76:79], v[80:83], a[116:119]\n\t" \
  "s_xor_b32 s73, %[eso], 524288\n\t" \
  "ds_read_b64_tr_b16 a[142:143], v236 offset:28672\n\t" \
  "v_mfma_f32_16x16x32_bf16 a[120:123], a[76:79], v[64:67], a[120:123]\n\t" \
  "v_xor_b32 v237, 0x1c020, v234\n\t" \
  "v_mfma_f32_16x16x32_bf16 a[120:123], a[92:95], v[68:71], a[120:123]\n\t" \
  "ds_read_b64_tr_b16 a[144:145], v237 offset:0\n\t" \
  "v_mfma_f32_16x16x32_bf16 a[120:123], a[108:111], v[72:75], a[120:123]\n\t" \
  "ds_read_b64_tr_b16 a[146:147], v237 offset:4096\n\t" \
  "v_mfma_f32_16x16x32_bf16 a[120:123], a[124:127], v[76:79], a[120:123]\n\t" \
  "s_waitcnt lgkmcnt(6)\n\t" \
  "buffer_load_dwordx4 v[48:51], v242, %[ws], s73 offen offset:0 sc1\n\t" \
  STP("v_writelane_b32 %[stv], s94, 28\n\t") \
  STP("v_writelane_b32 %[stv], s92, 29\n\t") \
  STP("s_memrealtime s[94:95]\n\t") \
  STP("s_memtime s[92:93]\n\t") \
  "v_mfma_f32_16x16x32_bf16 a[0:3], a[128:131], v[16:19], a[0:3]\n\t" \
  "ds_read_b64_tr_b16 a[148:149], v237 offset:8192\n\t" \
  "v_mfma_f32_16x16x32_bf16 a[0:3], a[132:135], v[20:23], a[0:3]\n\t" \
  "s_waitcnt lgkmcnt(3)\n\t" \
  "ds_read_b64_tr_b16 a[150:151], v237 offset:12288\n\t" \
  "v_mfma_f32_16x16x32_bf16 a[0:3], a[136:139], v[24:27], a[0:3]\n\t" \
  "ds_read_b64_tr_b16 a[152:153], v237 offset:16384\n\t" \
  "v_mfma_f32_16x16x32_bf16 a[0:3], a[140:143], v[28:31], a[0:3]\n\t" \
  "buffer_load_dwordx4 v[52:55], v242, %[ws], s73 offen offset:1024 sc1\n\t" \
  "v_mfma_f32_16x16x32_bf16 a[4:7], a[140:143], v[12:15], a[4:7]\n\t" \
  "ds_read_b64_tr_b16 a[154:155], v237 offset:20480\n\t" \
  "v_mfma_f32_16x16x32_bf16 a[4:7], a[136:139], v[8:11], a[4:7]\n\t" \
  "ds_read_b64_tr_b16 a[156:157], v237 offset:24576\n\t" \
  "v_mfma_f32_16x16x32_bf16 a[4:7], a[132:135], v[4:7], a[4:7]\n\t" \
  "ds_read_b64_tr_b16 a[158:159], v237 offset:28672\n\t" \
  "v_mfma_f32_16x16x32_bf16 a[4:7], a[128:131], v[0:3], a[4:7]\n\t" \
  "s_waitcnt vmcnt(13)\n\t" \
  "buffer_load_dwordx4 v[56:59], v242, %[ws], s73 offen offset:2048 sc1\n\t" \
  "v_mfma_f32_16x16x32_bf16 a[8:11], a[128:131], v[32:35], a[8:11]\n\t" \
  "v_xor_b32 v235, 0x1c040, v234\n\t" \
  "v_mfma_f32_16x16x32_bf16 a[8:11], a[132:135], v[36:39], a[8:11]\n\t" \
  "ds_read_b64_tr_b16 a[12:13], v235 offset:0\n\t" \
  "v_mfma_f32_16x16x32_bf16 a[8:11], a[136:139], v[40:43], a[8:11]\n\t" \
  "ds_read_b64_tr_b16 a[14:15], v235 offset:4096\n\t" \
  "v_mfma_f32_16x16x32_bf16 a[8:11], a[140:143], v[44:47], a[8:11]\n\t" \
  "s_waitcnt lgkmcnt(6)\n\t" \
  "ds_read_b64_tr_b16 a[28:29], v235 offset:8192\n\t" \
  "v_mfma_f32_16x16x32_bf16 a[16:19], a[144:147], v[16:19], a[16:19]\n\t" \
  "buffer_load_dwordx4 v[60:63], v242, %[ws], s73 offen offset:3072 sc1\n\t" \
  "s_xor_b32 s73, %[eso], 557056\n\t" \
  "v_mfma_f32_16x16x32_bf16 a[16:19], a[148:151], v[20:23], a[16:19]\n\t" \
  "s_waitcnt lgkmcnt(3)\n\t" \
  "ds_read_b64_tr_b16 a[30:31], v235 offset:12288\n\t" \
  "v_mfma_f32_16x16x32_bf16 a[16:19], a[152:155], v[24:27], a[16:19]\n\t" \
  "ds_read_b64_tr_b16 a[44:45], v235 offset:16384\n\t" \
  "v_mfma_f32_16x16x32_bf16 a[16:19], a[156:159], v[28:31], a[16:19]\n\t" \
  "ds_read_b64_tr_b16 a[46:47], v235 offset:20480\n\t" \
  "v_mfma_f32_16x16x32_bf16 a[20:23], a[156:159], v[12:15], a[20:23]\n\t" \
  "buffer_load_dwordx4 v[80:83], v242, %[ws], s73 offen offset:0 sc1\n\t" \
  "v_mfma_f32_16x16x32_bf16 a[20:23], a[152:155], v[8:11], a[20:23]\n\t" \
  "ds_read_b64_tr_b16 a[60:61], v235 offset:24576\n\t" \
  "v_mfma_f32_16x16x32_bf16 a[20:23], a[148:151], v[4:7], a[20:23]\n\t" \
  "ds_read_b64_tr_b16 a[62:63], v235 offset:28672\n\t" \
  "v_mfma_f32_16x16x32_bf16 a[20:23], a[144:147], v[0:3], a[20:23]\n\t" \
  "v_mfma_f32_16x16x32_bf16 a[24:27], a[144:147], v[32:35], a[24:27]\n\t" \
  "v_xor_b32 v236, 0x1c060, v234\n\t" \
  "v_mfma_f32_16x16x32_bf16 a[24:27], a[148:151], v[36:39], a[24:27]\n\t" \
  "buffer_load_dwordx4 v[84:87], v242, %[ws], s73 offen offset:1024 sc1\n\t" \
  "v_mfma_f32_16x16x32_bf16 a[24:27], a[152:155], v[40:43], a[24:27]\n\t" \
  "ds_read_b64_tr_b16 a[76:77], v236 offset:0\n\t" \
  "v_mfma_f32_16x16x32_bf16 a[24:27], a[156:159], v[44:47], a[24:27]\n\t" \
  "s_waitcnt lgkmcnt(5)\n\t" \
  "ds_read_b64_tr_b16 a[78:79], v236 offset:4096\n\t" \
  "v_mfma_f32_16x16x32_bf16 a[32:35], a[12:15], v[16:19], a[32:35]\n\t" \
  "ds_read_b64_tr_b16 a[92:93], v236 offset:8192\n\t" \
  "v_mfma_f32_16x16x32_bf16 a[32:35], a[28:31], v[20:23], a[32:35]\n\t" \
  "s_waitcnt lgkmcnt(3)\n\t" \
  "buffer_load_dwordx4 v[88:91], v242, %[ws], s73 offen offset:2048 sc1\n\t" \
  "v_mfma_f32_16x16x32_bf16 a[32:35], a[44:47], v[24:27], a[32:35]\n\t" \
  "ds_read_b64_tr_b16 a[94:95], v236 offset:12288\n\t" \
  "v_mfma_f32_16x16x32_bf16 a[32:35], a[60:63], v[28:31], a[32:35]\n\t" \
  "ds_read_b64_tr_b16 a[108:109], v236 offset:16384\n\t" \
  "v_mfma_f32_16x16x32_bf16 a[36:39], a[60:63], v[12:15], a[36:39]\n\t" \
  "ds_read_b64_tr_b16 a[110:111], v236 offset:20480\n\t" \
  "v_mfma_f32_16x16x32_bf16 a[36:39], a[44:47], v[8:11], a[36:39]\n\t" \
  "buffer_load_dwordx4 v[92:95], v242, %[ws], s73 offen offset:3072 sc1\n\t" \
  "s_xor_b32 s73, %[eso], 589824\n\t" \
  "v_mfma_f32_16x16x32_bf16 a[36:39], a[28:31], v[4:7], a[36:39]\n\t" \
  "ds_read_b64_tr_b16 a[124:125], v236 offset:24576\n\t" \
  "v_mfma_f32_16x16x32_bf16 a[36:39], a[12:15], v[0:3], a[36:39]\n\t" \
  "ds_read_b64_tr_b16 a[126:127], v236 offset:28672\n\t" \
  "v_mfma_f32_16x16x32_bf16 a[40:43], a[12:15], v[32:35], a[40:43]\n\t" \
  "v_xor_b32 v237, 0x1c080, v234\n\t" \
  "v_mfma_f32_16x16x32_bf16 a[40:43], a[28:31], v[36:39], a[40:43]\n\t" \
  "ds_read_b64_tr_b16 v[128:129], v237 offset:0\n\t" \
  "v_mfma_f32_16x16x32_bf16 a[40:43], a[44:47], v[40:43], a[40:43]\n\t" \
  "buffer_load_dwordx4 v[64:67], v242, %[ws], s73 offen offset:0 sc1\n\t" \
  "v_mfma_f32_16x16x32_bf16 a[40:43], a[60:63], v[44:47], a[40:43]\n\t" \
  "s_waitcnt lgkmcnt(5)\n\t" \
  "ds_read_b64_tr_b16 v[130:131], v237 offset:4096\n\t" \
  "v_mfma_f32_16x16x32_bf16 a[48:51], a[76:79], v[16:19], a[48:51]\n\t" \
  "s_add_u32 m0, %[m], 81920\n\t" \
  "ds_read_b64_tr_b16 v[132:133], v237 offset:8192\n\t" \
  "v_mfma_f32_16x16x32_bf16 a[48:51], a[92:95], v[20:23], a[48:51]\n\t" \
  "s_waitcnt lgkmcnt(3)\n\t" \
  "ds_read_b64_tr_b16 v[134:135], v237 offset:12288\n\t" \
  "v_mfma_f32_16x16x32_bf16 a[48:51], a[108:111], v[24:27], a[48:51]\n\t" \
  "buffer_load_dwordx4 v[68:71], v242, %[ws], s73 offen offset:1024 sc1\n\t" \
  "v_mfma_f32_16x16x32_bf16 a[48:51], a[124:127], v[28:31], a[48:51]\n\t" \
  "s_barrier\n\t" \
  "v_mfma_f32_16x16x32_bf16 a[52:55], a[124:127], v[12:15], a[52:55]\n\t" \
  "s_xor_b32 s71, %[wob], 1572864\n\t" \
  "ds_write_b64 v250, v[248:249] offset:19456\n\t" \
  "v_mfma_f32_16x16x32_bf16 a[52:55], a[108:111], v[8:11], a[52:55]\n\t" \
  "ds_read_b64_tr_b16 v[136:137], v237 offset:16384\n\t" \
  "ds_read_b64_tr_b16 v[138:139], v237 offset:20480\n\t" \
  "v_mfma_f32_16x16x32_bf16 a[52:55], a[92:95], v[4:7], a[52:55]\n\t" \
  "ds_read_b64_tr_b16 v[140:141], v237 offset:24576\n\t" \
  "v_mfma_f32_16x16x32_bf16 a[52:55], a[76:79], v[0:3], a[52:55]\n\t" \
  "buffer_load_dwordx4 v[72:75], v242, %[ws], s73 offen offset:2048 sc1\n\t" \
  "v_mfma_f32_16x16x32_bf16 a[56:59], a[76:79], v[32:35], a[56:59]\n\t" \
  "ds_read_b64_tr_b16 v[142:143], v237 offset:28672\n\t" \
  "v_xor_b32 v235, 0x1c0a0, v234\n\t" \
  "v_mfma_f32_16x16x32_bf16 a[56:59], a[92:95], v[36:39], a[56:59]\n\t" \
  "ds_read_b64_tr_b16 a[12:13], v235 offset:0\n\t" \
  "v_mfma_f32_16x16x32_bf16 a[56:59], a[108:111], v[40:43], a[56:59]\n\t" \
  "ds_read_b64_tr_b16 a[14:15], v235 offset:4096\n\t" \
  "v_mfma_f32_16x16x32_bf16 a[56:59], a[124:127], v[44:47], a[56:59]\n\t" \
  "s_waitcnt lgkmcnt(7)\n\t" \
  "buffer_load_dwordx4 v[76:79], v242, %[ws], s73 offen offset:3072 sc1\n\t" \
  "v_mfma_f32_16x16x32_bf16 a[64:67], v[128:131], v[16:19], a[64:67]\n\t" \
  "ds_read_b64_tr_b16 a[28:29], v235 offset:8192\n\t" \
  "v_mfma_f32_16x16x32_bf16 a[64:67], v[132:135], v[20:23], a[64:67]\n\t" \
  "s_waitcnt lgkmcnt(3)\n\t" \
  "ds_read_b64_tr_b16 a[30:31], v235 offset:12288\n\t" \
  "v_mfma_f32_16x16x32_bf16 a[64:67], v[136:139], v[24:27], a[64:67]\n\t" \
  "ds_read_b64_tr_b16 a[44:45], v235 offset:16384\n\t" \
  "v_mfma_f32_16x16x32_bf16 a[64:67], v[140:143], v[28:31], a[64:67]\n\t" \
  "buffer_load_dwordx4 v240, %[wo], s71 offen offset:0 lds\n\t" \
  "v_mfma_f32_16x16x32_bf16 a[68:71], v[140:143], v[12:15], a[68:71]\n\t" \
  "ds_read_b64_tr_b16 a[46:47], v235 offset:20480\n\t" \
  "v_mfma_f32_16x16x32_bf16 a[68:71], v[136:139], v[8:11], a[68:71]\n\t" \
  "ds_read_b64_tr_b16 a[60:61], v235 offset:24576\n\t" \
  "v_mfma_f32_16x16x32_bf16 a[68:71], v[132:135], v[4:7], a[68:71]\n\t" \
  "ds_read_b64_tr_b16 a[62:63], v235 offset:28672\n\t" \
  "v_mfma_f32_16x16x32_bf16 a[68:71], v[128:131], v[0:3], a[68:71]\n\t" \
  "v_mfma_f32_16x16x32_bf16 a[72:75], v[128:131], v[32:35], a[72:75]\n\t" \
  "buffer_load_dwordx4 v241, %[wo], s71 offen offset:1024 lds\n\t" \
  "v_mfma_f32_16x16x32_bf16 a[72:75], v[132:135], v[36:39], a[72:75]\n\t" \
  "v_xor_b32 v236, 0x1c0c0, v234\n\t" \
  "v_mfma_f32_16x16x32_bf16 a[72:75], v[136:139], v[40:43], a[72:75]\n\t" \
  "ds_read_b64_tr_b16 v[144:145], v236 offset:0\n\t" \
  "v_mfma_f32_16x16x32_bf16 a[72:75], v[140:143], v[44:47], a[72:75]\n\t" \
  "s_waitcnt lgkmcnt(5)\n\t" \
  "ds_read_b64_tr_b16 v[146:147], v236 offset:4096\n\t" \
  "v_mfma_f32_16x16x32_bf16 a[80:83], a[12:15], v[16:19], a[80:83]\n\t" \
  "buffer_load_dwordx4 v238, %[wo], s71 offen offset:2048 lds\n\t" \
  "v_mfma_f32_16x16x32_bf16 a[80:83], a[28:31], v[20:23], a[80:83]\n\t" \
  "s_waitcnt lgkmcnt(2)\n\t" \
  "ds_read_b64_tr_b16 v[148:149], v236 offset:8192\n\t" \
  "v_mfma_f32_16x16x32_bf16 a[80:83], a[44:47], v[24:27], a[80:83]\n\t" \
  "ds_read_b64_tr_b16 v[150:151], v236 offset:12288\n\t" \
  "v_mfma_f32_16x16x32_bf16 a[80:83], a[60:63], v[28:31], a[80:83]\n\t" \
  "s_barrier\n\t" \
  "v_mfma_f32_16x16x32_bf16 a[84:87], a[60:63], v[12:15], a[84:87]\n\t" \
  "s_waitcnt vmcnt(17)\n\t" \
  "buffer_store_dword v243, v254, %[ws], %[fw2] offen offset:64 sc1\n\t" \
  "v_mfma_f32_16x16x32_bf16 a[84:87], a[44:47], v[8:11], a[84:87]\n\t" \
  "ds_read_b64_tr_b16 v[152:153], v236 offset:16384\n\t" \
  "ds_read_b64_tr_b16 v[154:155], v236 offset:20480\n\t" \
  "v_mfma_f32_16x16x32_bf16 a[84:87], a[28:31], v[4:7], a[84:87]\n\t" \
  "buffer_load_dwordx4 v239, %[wo], s71 offen offset:3072 lds\n\t" \
  "v_mfma_f32_16x16x32_bf16 a[84:87], a[12:15], v[0:3], a[84:87]\n\t" \
  "ds_read_b64_tr_b16 v[156:157], v236 offset:24576\n\t" \
  "ds_read_b64_tr_b16 v[158:159], v236 offset:28672\n\t" \
  "v_mfma_f32_16x16x32_bf16 a[88:91], a[12:15], v[32:35], a[88:91]\n\t" \
  "v_and_b32 v250, 3, v244\n\t" \
  "v_xor_b32 v237, 0x1c0e0, v234\n\t" \
  "v_mfma_f32_16x16x32_bf16 a[88:91], a[28:31], v[36:39], a[88:91]\n\t" \
  "ds_read_b64_tr_b16 v[128:129], v237 offset:0\n\t" \
  "v_bfe_u32 v251, v244, 4, 1\n\t" \
  "v_mfma_f32_16x16x32_bf16 a[88:91], a[44:47], v[40:43], a[88:91]\n\t" \
  "ds_read_b64_tr_b16 v[130:131], v237 offset:4096\n\t" \
  ".Lhs12:\n\ts_cbranch_execz .Lhs12n\n\t" \
  "v_lshl_add_u32 v250, v251, 2, v250\n\t" \
  "v_mfma_f32_16x16x32_bf16 a[88:91], a[60:63], v[44:47], a[88:91]\n\t" \
  "s_waitcnt lgkmcnt(6)\n\t" \
  "ds_read_b64_tr_b16 v[132:133], v237 offset:8192\n\t" \
  "v_bfe_u32 v251, v244, 2, 2\n\t" \
  "v_mfma_f32_16x16x32_bf16 a[96:99], v[144:147], v[16:19], a[96:99]\n\t" \
  "ds_read_b64_tr_b16 v[134:135], v237 offset:12288\n\t" \
  "v_lshlrev_b32 v251, 8, v251\n\t" \
  "v_mfma_f32_16x16x32_bf16 a[96:99], v[148:151], v[20:23], a[96:99]\n\t" \
  "s_waitcnt lgkmcnt(4)\n\t" \
  "ds_read_b64_tr_b16 v[136:137], v237 offset:16384\n\t" \
  "v_lshl_add_u32 v251, v250, 3, v251\n\t" \
  "v_mfma_f32_16x16x32_bf16 a[96:99], v[152:155], v[24:27], a[96:99]\n\t" \
  "ds_read_b64_tr_b16 v[138:139], v237 offset:20480\n\t" \
  "v_mfma_f32_16x16x32_bf16 a[96:99], v[156:159], v[28:31], a[96:99]\n\t" \
  "ds_read_b64_tr_b16 v[140:141], v237 offset:24576\n\t" \
  "v_mfma_f32_16x16x32_bf16 a[100:103], v[156:159], v[12:15], a[100:103]\n\t" \
  "ds_read_b64_tr_b16 v[142:143], v237 offset:28672\n\t" \
  "v_mfma_f32_16x16x32_bf16 a[100:103], v[152:155], v[8:11], a[100:103]\n\t" \
  "s_lshr_b32 s75, %[m], 6\n\t" \
  "s_bitset1_b32 s75, 17\n\t" \
  "v_add_u32 v251, s75, v251\n\t" \
  "v_mfma_f32_16x16x32_bf16 a[100:103], v[148:151], v[4:7], a[100:103]\n\t" \
  "ds_read_b64 v[224:225], v251 offset:19456\n\t" \
  "v_lshlrev_b32 v228, 4, v250\n\t" \
  "v_mfma_f32_16x16x32_bf16 a[100:103], v[144:147], v[0:3], a[100:103]\n\t" \
  "v_mov_b32 v249, %[tag]\n\t" \
  "v_mov_b32 v251, %[tag]\n\t" \
  "v_mfma_f32_16x16x32_bf16 a[104:107], v[144:147], v[32:35], a[104:107]\n\t" \
  "s_mov_b32 s82, 0x000f000f\n\t" \
  "v_xor_b32 v235, 0x0, v234\n\t" \
  "s_mov_b32 s83, 0\n\t" \
  "v_mfma_f32_16x16x32_bf16 a[104:107], v[148:151], v[36:39], a[104:107]\n\t" \
  "ds_read_b64_tr_b16 a[12:13], v235 offset:0\n\t" \
  "v_mfma_f32_16x16x32_bf16 a[104:107], v[152:155], v[40:43], a[104:107]\n\t" \
  "ds_read_b64_tr_b16 a[14:15], v235 offset:4096\n\t" \
  "v_mfma_f32_16x16x32_bf16 a[104:107], v[156:159], v[44:47], a[104:107]\n\t" \
  "s_waitcnt lgkmcnt(7)\n\t" \
  "ds_read_b64_tr_b16 a[28:29], v235 offset:8192\n\t" \
  "v_mfma_f32_16x16x32_bf16 a[112:115], v[128:131], v[16:19], a[112:115]\n\t" \
  "ds_read_b64_tr_b16 a[30:31], v235 offset:12288\n\t" \
  "v_mfma_f32_16x16x32_bf16 a[112:115], v[132:135], v[20:23], a[112:115]\n\t" \
  "s_waitcnt lgkmcnt(5)\n\t" \
  "ds_read_b64_tr_b16 a[44:45], v235 offset:16384\n\t" \
  "v_mfma_f32_16x16x32_bf16 a[112:115], v[136:139], v[24:27], a[112:115]\n\t" \
  "s_waitcnt lgkmcnt(5)\n\t" \
  "v_add_f32_dpp v226, v224, v224 row_ror:4 row_mask:0xf bank_mask:0xf\n\t" \
  "v_add_f32_dpp v227, v225, v225 row_ror:4 row_mask:0xf bank_mask:0xf\n\t" \
  "v_mfma_f32_16x16x32_bf16 a[112:115], v[140:143], v[28:31], a[112:115]\n\t" \
  "ds_read_b64_tr_b16 a[46:47], v235 offset:20480\n\t" \
  "v_add_f32_dpp v248, v226, v226 row_ror:8 row_mask:0xf bank_mask:0xf\n\t" \
  "v_mfma_f32_16x16x32_bf16 a[116:119], v[140:143], v[12:15], a[116:119]\n\t" \
  "ds_read_b64_tr_b16 a[60:61], v235 offset:24576\n\t" \
  "v_add_f32_dpp v250, v227, v227 row_ror:8 row_mask:0xf bank_mask:0xf\n\t" \
  "v_mfma_f32_16x16x32_bf16 a[116:119], v[136:139], v[8:11], a[116:119]\n\t" \
  "s_mov_b64 exec, s[82:83]\n\t" \
  "buffer_store_dwordx4 v[248:251], v228, %[ws], %[mbp] offen sc1\n\t" \
  "s_mov_b64 exec, -1\n\t" \
  "v_mfma_f32_16x16x32_bf16 a[116:119], v[132:135], v[4:7], a[116:119]\n\t" \
  "ds_read_b64_tr_b16 a[62:63], v235 offset:28672\n\t" \
  "v_mfma_f32_16x16x32_bf16 a[116:119], v[128:131], v[0:3], a[116:119]\n\t" \
  "s_xor_b32 s73, %[eso], 655360\n\t" \
  "v_mfma_f32_16x16x32_bf16 a[120:123], v[128:131], v[32:35], a[120:123]\n\t" \
  "v_xor_b32 v236, 0x20, v234\n\t" \
  "v_mfma_f32_16x16x32_bf16 a[120:123], v[132:135], v[36:39], a[120:123]\n\t" \
  "ds_read_b64_tr_b16 a[76:77], v236 offset:0\n\t" \
  "v_mfma_f32_16x16x32_bf16 a[120:123], v[136:139], v[40:43], a[120:123]\n\t" \
  "ds_read_b64_tr_b16 a[78:79], v236 offset:4096\n\t" \
  "v_mov_b32 v160, 0\n\t" \
  "v_mfma_f32_16x16x32_bf16 a[120:123], v[140:143], v[44:47], a[120:123]\n\t" \
  "s_waitcnt vmcnt(14) lgkmcnt(6)\n\t" \
  "buffer_load_dwordx4 v[16:19], v242, %[ws], s73 offen offset:0 sc1\n\t" \
  "v_mfma_f32_16x16x32_bf16 a[0:3], a[12:15], v[48:51], a[0:3]\n\t" \
  "ds_read_b64_tr_b16 a[92:93], v236 offset:8192\n\t" \
  "v_mov_b32 v161, 0\n\t" \
  "v_mfma_f32_16x16x32_bf16 a[0:3], a[28:31], v[52:55], a[0:3]\n\t" \
  "s_waitcnt lgkmcnt(3)\n\t" \
  "buffer_load_dwordx4 v[20:23], v242, %[ws], s73 offen offset:1024 sc1\n\t" \
  "v_mfma_f32_16x16x32_bf16 a[0:3], a[44:47], v[56:59], a[0:3]\n\t" \
  "ds_read_b64_tr_b16 a[94:95], v236 offset:12288\n\t" \
  "v_mov_b32 v162, 0\n\t" \
  "v_mfma_f32_16x16x32_bf16 a[0:3], a[60:63], v[60:63], a[0:3]\n\t" \
  "s_waitcnt vmcnt(12)\n\t" \
  "ds_read_b64_tr_b16 a[108:109], v236 offset:16384\n\t" \
  "v_mov_b32 v163, 0\n\t" \
  "v_mfma_f32_16x16x32_bf16 a[4:7], a[60:63], v[92:95], a[4:7]\n\t" \
  "ds_read_b64_tr_b16 a[110:111], v236 offset:20480\n\t" \
  "v_mov_b32 v164, 0x3f803f80\n\t" \
  "v_mfma_f32_16x16x32_bf16 a[4:7], a[44:47], v[88:91], a[4:7]\n\t" \
  "buffer_load_dwordx4 v[24:27], v242, %[ws], s73 offen offset:2048 sc1\n\t" \
  "v_mfma_f32_16x16x32_bf16 a[4:7], a[28:31], v[84:87], a[4:7]\n\t" \
  "ds_read_b64_tr_b16 a[124:125], v236 offset:24576\n\t" \
  "v_add_u32 v165, s70, v242\n\t" \
  "v_mfma_f32_16x16x32_bf16 a[4:7], a[12:15], v[80:83], a[4:7]\n\t" \
  "s_waitcnt vmcnt(9)\n\t" \
  "ds_read_b64_tr_b16 a[126:127], v236 offset:28672\n\t" \
  "v_mfma_f32_16x16x32_bf16 a[8:11], a[12:15], v[64:67], a[8:11]\n\t" \
  "v_xor_b32 v237, 0x40, v234\n\t" \
  "v_mfma_f32_16x16x32_bf16 a[8:11], a[28:31], v[68:71], a[8:11]\n\t" \
  "buffer_load_dwordx4 v[28:31], v242, %[ws], s73 offen offset:3072 sc1\n\t" \
  "s_xor_b32 s73, %[eso], 688128\n\t" \
  "v_mfma_f32_16x16x32_bf16 a[8:11], a[44:47], v[72:75], a[8:11]\n\t" \
  "ds_read_b64_tr_b16 v[144:145], v237 offset:0\n\t" \
  "v_mfma_f32_16x16x32_bf16 a[8:11], a[60:63], v[76:79], a[8:11]\n\t" \
  "s_waitcnt lgkmcnt(5)\n\t" \
  "ds_read_b64_tr_b16 v[146:147], v237 offset:4096\n\t" \
  "v_mfma_f32_16x16x32_bf16 a[16:19], a[76:79], v[48:51], a[16:19]\n\t" \
  "ds_read_b64_tr_b16 v[148:149], v237 offset:8192\n\t" \
  "v_mfma_f32_16x16x32_bf16 a[16:19], a[92:95], v[52:55], a[16:19]\n\t" \
  "s_waitcnt lgkmcnt(3)\n\t" \
  "ds_read_b64_tr_b16 v[150:151], v237 offset:12288\n\t" \
  "v_mfma_f32_16x16x32_bf16 a[16:19], a[108:111], v[56:59], a[16:19]\n\t" \
  "buffer_load_dwordx4 v[0:3], v242, %[ws], s73 offen offset:0 sc1\n\t" \
  "v_mfma_f32_16x16x32_bf16 a[16:19], a[124:127], v[60:63], a[16:19]\n\t" \
  "ds_read_b64_tr_b16 v[152:153], v237 offset:16384\n\t" \
  "ds_write_b128 v165, v[160:163]\n\t" \
  "v_mfma_f32_16x16x32_bf16 a[20:23], a[124:127], v[92:95], a[20:23]\n\t" \
  "ds_read_b64_tr_b16 v[154:155], v237 offset:20480\n\t" \
  "v_and_b32 v171, 15, v244\n\t" \
  "v_mfma_f32_16x16x32_bf16 a[20:23], a[108:111], v[88:91], a[20:23]\n\t" \
  "ds_read_b64_tr_b16 v[156:157], v237 offset:24576\n\t" \
  "v_lshrrev_b32 v172, 4, v244\n\t" \
  "v_mfma_f32_16x16x32_bf16 a[20:23], a[92:95], v[84:87], a[20:23]\n\t" \
  "buffer_load_dwordx4 v[4:7], v242, %[ws], s73 offen offset:1024 sc1\n\t" \
  "v_mfma_f32_16x16x32_bf16 a[20:23], a[76:79], v[80:83], a[20:23]\n\t" \
  "ds_read_b64_tr_b16 v[158:159], v237 offset:28672\n\t" \
  "v_lshlrev_b32 v170, 13, v172\n\t" \
  "v_mfma_f32_16x16x32_bf16 a[24:27], a[76:79], v[64:67], a[24:27]\n\t" \
  "v_xor_b32 v235, 0x60, v234\n\t" \
  "v_lshl_add_u32 v170, v171, 4, v170\n\t" \
  "v_mfma_f32_16x16x32_bf16 a[24:27], a[92:95], v[68:71], a[24:27]\n\t" \
  "ds_read_b64_tr_b16 v[128:129], v235 offset:0\n\t" \
  "v_lshlrev_b32 v174, 7, v172\n\t" \
  "v_mfma_f32_16x16x32_bf16 a[24:27], a[108:111], v[72:75], a[24:27]\n\t" \
  "buffer_load_dwordx4 v[8:11], v242, %[ws], s73 offen offset:2048 sc1\n\t" \
  "v_mfma_f32_16x16x32_bf16 a[24:27], a[124:127], v[76:79], a[24:27]\n\t" \
  "s_waitcnt lgkmcnt(6)\n\t" \
  "ds_read_b64_tr_b16 v[130:131], v235 offset:4096\n\t" \
  "v_lshl_add_u32 v174, v171, 3, v174\n\t" \
  "v_mfma_f32_16x16x32_bf16 a[32:35], v[144:147], v[48:51], a[32:35]\n\t" \
  "ds_read_b64_tr_b16 v[132:133], v235 offset:8192\n\t" \
  "v_add_u32 v174, s70, v174\n\t" \
  "v_mfma_f32_16x16x32_bf16 a[32:35], v[148:151], v[52:55], a[32:35]\n\t" \
  "s_waitcnt lgkmcnt(3)\n\t" \
  "ds_read_b64_tr_b16 v[134:135], v235 offset:12288\n\t" \
  "v_mfma_f32_16x16x32_bf16 a[32:35], v[152:155], v[56:59], a[32:35]\n\t" \
  "ds_read_b64_tr_b16 v[136:137], v235 offset:16384\n\t" \
  "v_mfma_f32_16x16x32_bf16 a[32:35], v[156:159], v[60:63], a[32:35]\n\t" \
  "buffer_load_dwordx4 v[12:15], v242, %[ws], s73 offen offset:3072 sc1\n\t" \
  "s_xor_b32 s73, %[eso], 720896\n\t" \
  "v_mfma_f32_16x16x32_bf16 a[36:39], v[156:159], v[92:95], a[36:39]\n\t" \
  "ds_read_b64_tr_b16 v[138:139], v235 offset:20480\n\t" \
  "v_mfma_f32_16x16x32_bf16 a[36:39], v[152:155], v[88:91], a[36:39]\n\t" \
  "ds_read_b64_tr_b16 v[140:141], v235 offset:24576\n\t" \
  "v_mfma_f32_16x16x32_bf16 a[36:39], v[148:151], v[84:87], a[36:39]\n\t" \
  "ds_read_b64_tr_b16 v[142:143], v235 offset:28672\n\t" \
  "ds_write_b128 v165, v[160:163] offset:1024\n\t" \
  "v_mfma_f32_16x16x32_bf16 a[36:39], v[144:147], v[80:83], a[36:39]\n\t" \
  "buffer_load_dwordx4 v[32:35], v242, %[ws], s73 offen offset:0 sc1\n\t" \
  "v_mfma_f32_16x16x32_bf16 a[40:43], v[144:147], v[64:67], a[40:43]\n\t" \
  "v_lshlrev_b32 v171, 2, v171\n\t" \
  "v_xor_b32 v236, 0x80, v234\n\t" \
  "v_mfma_f32_16x16x32_bf16 a[40:43], v[148:151], v[68:71], a[40:43]\n\t" \
  "v_cmp_eq_u32 vcc, 2, v172\n\t" \
  "s_nop 1\n\t" \
  "v_cndmask_b32 v172, 0, 1.0, vcc\n\t" \
  "v_mfma_f32_16x16x32_bf16 a[40:43], v[152:155], v[72:75], a[40:43]\n\t" \
  "ds_read_b64_tr_b16 a[208:209], v236 offset:0\n\t" \
  "v_mfma_f32_16x16x32_bf16 a[40:43], v[156:159], v[76:79], a[40:43]\n\t" \
  "s_waitcnt lgkmcnt(6)\n\t" \
  "ds_read_b64_tr_b16 a[210:211], v236 offset:4096\n\t" \
  "v_mfma_f32_16x16x32_bf16 a[48:51], v[128:131], v[48:51], a[48:51]\n\t" \
  "s_add_u32 m0, %[m], 114688\n\t" \
  "buffer_load_dwordx4 v[36:39], v242, %[ws], s73 offen offset:1024 sc1\n\t" \
  "v_mfma_f32_16x16x32_bf16 a[48:51], v[132:135], v[52:55], a[48:51]\n\t" \
  "s_waitcnt lgkmcnt(3)\n\t" \
  "ds_read_b64_tr_b16 a[212:213], v236 offset:8192\n\t" \
  "ds_write_b128 v165, v[160:163] offset:1536\n\t" \
  "v_mfma_f32_16x16x32_bf16 a[48:51], v[136:139], v[56:59], a[48:51]\n\t" \
  "ds_read_b64_tr_b16 a[214:215], v236 offset:12288\n\t" \
  "s_waitcnt lgkmcnt(1)\n\t" \
  "v_bfe_u32 v160, v244, 3, 1\n\t" \
  "v_mfma_f32_16x16x32_bf16 a[48:51], v[140:143], v[60:63], a[48:51]\n\t" \
  "s_barrier\n\t" \
  "v_mfma_f32_16x16x32_bf16 a[52:55], v[140:143], v[92:95], a[52:55]\n\t" \
  "buffer_load_dwordx4 v[40:43], v242, %[ws], s73 offen offset:2048 sc1\n\t" \
  "v_bfe_u32 v173, v244, 1, 2\n\t" \
  "v_mfma_f32_16x16x32_bf16 a[52:55], v[136:139], v[88:91], a[52:55]\n\t" \
  "s_xor_b32 s71, %[wob], 1703936\n\t" \
  "ds_read_b64_tr_b16 a[216:217], v236 offset:16384\n\t" \
  "ds_read_b64_tr_b16 a[218:219], v236 offset:20480\n\t" \
  "v_mfma_f32_16x16x32_bf16 a[52:55], v[132:135], v[84:87], a[52:55]\n\t" \
  "ds_read_b64_tr_b16 a[220:221], v236 offset:24576\n\t" \
  "v_lshlrev_b32 v173, 4, v173\n\t" \
  "v_mfma_f32_16x16x32_bf16 a[52:55], v[128:131], v[80:83], a[52:55]\n\t" \
  "ds_read_b64_tr_b16 a[222:223], v236 offset:28672\n\t" \
  "v_lshl_add_u32 v173, v160, 3, v173\n\t" \
  "v_mfma_f32_16x16x32_bf16 a[56:59], v[128:131], v[64:67], a[56:59]\n\t" \
  "buffer_load_dwordx4 v[44:47], v242, %[ws], s73 offen offset:3072 sc1\n\t" \
  "v_mfma_f32_16x16x32_bf16 a[56:59], v[132:135], v[68:71], a[56:59]\n\t" \
  "v_xor_b32 v237, 0xa0, v234\n\t" \
  "v_and_b32 v160, 1, v244\n\t" \
  "v_mfma_f32_16x16x32_bf16 a[56:59], v[136:139], v[72:75], a[56:59]\n\t" \
  "ds_read_b64_tr_b16 v[144:145], v237 offset:0\n\t" \
  "v_lshl_add_u32 v173, v160, 2, v173\n\t" \
  "v_mfma_f32_16x16x32_bf16 a[56:59], v[140:143], v[76:79], a[56:59]\n\t" \
  "s_waitcnt lgkmcnt(5)\n\t" \
  "ds_read_b64_tr_b16 v[146:147], v237 offset:4096\n\t" \
  "v_add_u32 v173, s70, v173\n\t" \
  "v_mfma_f32_16x16x32_bf16 a[64:67], a[208:211], v[48:51], a[64:67]\n\t" \
  "ds_read_b64_tr_b16 v[148:149], v237 offset:8192\n\t" \
  "v_mfma_f32_16x16x32_bf16 a[64:67], a[212:215], v[52:55], a[64:67]\n\t" \
  "s_waitcnt lgkmcnt(3)\n\t" \
  "buffer_load_dwordx4 v240, %[wo], s71 offen offset:0 lds\n\t" \
  "v_mfma_f32_16x16x32_bf16 a[64:67], a[216:219], v[56:59], a[64:67]\n\t" \
  "ds_read_b64_tr_b16 v[150:151], v237 offset:12288\n\t" \
  "v_mfma_f32_16x16x32_bf16 a[64:67], a[220:223], v[60:63], a[64:67]\n\t" \
  "ds_read_b64_tr_b16 v[152:153], v237 offset:16384\n\t" \
  "v_mfma_f32_16x16x32_bf16 a[68:71], a[220:223], v[92:95], a[68:71]\n\t" \
  "ds_read_b64_tr_b16 v[154:155], v237 offset:20480\n\t" \
  "v_mfma_f32_16x16x32_bf16 a[68:71], a[216:219], v[88:91], a[68:71]\n\t" \
  "buffer_load_dwordx4 v241, %[wo], s71 offen offset:1024 lds\n\t" \
  "v_mfma_f32_16x16x32_bf16 a[68:71], a[212:215], v[84:87], a[68:71]\n\t" \
  "s_mov_b64 exec, 0xffff\n\t" \
  "ds_write_b32 v173, v164 offset:960\n\t" \
  "s_mov_b64 exec, -1\n\t" \
  "v_mfma_f32_16x16x32_bf16 a[68:71], a[208:211], v[80:83], a[68:71]\n\t" \
  "ds_read_b64_tr_b16 v[156:157], v237 offset:24576\n\t" \
  "ds_read_b64_tr_b16 v[158:159], v237 offset:28672\n\t" \
  "v_lshrrev_b32 v229, 4, v244\n\t" \
  "v_mfma_f32_16x16x32_bf16 a[72:75], a[208:211], v[64:67], a[72:75]\n\t" \
  "v_and_b32 v231, 15, v244\n\t" \
  "v_xor_b32 v248, v231, v229\n\t" \
  "v_mfma_f32_16x16x32_bf16 a[72:75], a[212:215], v[68:71], a[72:75]\n\t" \
  "v_lshlrev_b32 v248, 4, v248\n\t" \
  "v_xor_b32 v235, 0xc0, v234\n\t" \
  "v_mfma_f32_16x16x32_bf16 a[72:75], a[216:219], v[72:75], a[72:75]\n\t" \
  "buffer_load_dwordx4 v238, %[wo], s71 offen offset:2048 lds\n\t" \
  "v_lshl_add_u32 v248, v229, 11, v248\n\t" \
  "v_mfma_f32_16x16x32_bf16 a[72:75], a[220:223], v[76:79], a[72:75]\n\t" \
  "s_waitcnt lgkmcnt(5)\n\t" \
  "ds_read_b64_tr_b16 a[192:193], v235 offset:0\n\t" \
  "v_xor_b32 v249, 64, v248\n\t" \
  "v_mfma_f32_16x16x32_bf16 a[80:83], v[144:147], v[48:51], a[80:83]\n\t" \
  "ds_read_b64_tr_b16 a[194:195], v235 offset:4096\n\t" \
  "v_xor_b32 v250, 0x80, v248\n\t" \
  "v_mfma_f32_16x16x32_bf16 a[80:83], v[148:151], v[52:55], a[80:83]\n\t" \
  "s_waitcnt lgkmcnt(2)\n\t" \
  "ds_read_b64_tr_b16 a[196:197], v235 offset:8192\n\t" \
  "v_xor_b32 v251, 0xc0, v248\n\t" \
  "v_mfma_f32_16x16x32_bf16 a[80:83], v[152:155], v[56:59], a[80:83]\n\t" \
  "buffer_load_dwordx4 v239, %[wo], s71 offen offset:3072 lds\n\t" \
  "s_add_u32 m0, %[m], 131072\n\t" \
  "v_mfma_f32_16x16x32_bf16 a[80:83], v[156:159], v[60:63], a[80:83]\n\t" \
  "s_xor_b32 s71, %[wob], 1835008\n\t" \
  "ds_read_b64_tr_b16 a[198:199], v235 offset:12288\n\t" \
  "v_lshrrev_b32 v227, 5, v244\n\t" \
  "s_lshl_b32 s75, %[wid], 2\n\t" \
  "v_mfma_f32_16x16x32_bf16 a[84:87], v[156:159], v[92:95], a[84:87]\n\t" \
  "ds_read_b64_tr_b16 a[200:201], v235 offset:16384\n\t" \
  "v_add_u32 v227, s75, v227\n\t" \
  "v_mfma_f32_16x16x32_bf16 a[84:87], v[152:155], v[88:91], a[84:87]\n\t" \
  "ds_read_b64_tr_b16 a[202:203], v235 offset:20480\n\t" \
  "v_xor_b32 v227, v227, v231\n\t" \
  "v_mfma_f32_16x16x32_bf16 a[84:87], v[148:151], v[84:87], a[84:87]\n\t" \
  "buffer_load_dwordx4 v240, %[wo], s71 offen offset:0 lds\n\t" \
  "v_mfma_f32_16x16x32_bf16 a[84:87], v[144:147], v[80:83], a[84:87]\n\t" \
  "ds_read_b64_tr_b16 a[204:205], v235 offset:24576\n\t" \
  "ds_read_b64_tr_b16 a[206:207], v235 offset:28672\n\t" \
  "v_mfma_f32_16x16x32_bf16 a[88:91], v[144:147], v[64:67], a[88:91]\n\t" \
  "v_xor_b32 v236, 0xe0, v234\n\t" \
  "v_lshlrev_b32 v227, 4, v227\n\t" \
  "v_mfma_f32_16x16x32_bf16 a[88:91], v[148:151], v[68:71], a[88:91]\n\t" \
  "ds_read_b64_tr_b16 a[208:209], v236 offset:0\n\t" \
  "v_lshl_add_u32 v227, v231, 8, v227\n\t" \
  "v_mfma_f32_16x16x32_bf16 a[88:91], v[152:155], v[72:75], a[88:91]\n\t" \
  "ds_read_b64_tr_b16 a[210:211], v236 offset:4096\n\t" \
  "v_and_b32 v229, 1, v229\n\t" \
  "v_mfma_f32_16x16x32_bf16 a[88:91], v[156:159], v[76:79], a[88:91]\n\t" \
  "s_waitcnt lgkmcnt(6)\n\t" \
  "buffer_load_dwordx4 v241, %[wo], s71 offen offset:1024 lds\n\t" \
  "v_mfma_f32_16x16x32_bf16 a[96:99], a[192:195], v[48:51], a[96:99]\n\t" \
  "ds_read_b64_tr_b16 a[212:213], v236 offset:8192\n\t" \
  "v_lshl_add_u32 v227, v229, 3, v227\n\t" \
  "v_mfma_f32_16x16x32_bf16 a[96:99], a[196:199], v[52:55], a[96:99]\n\t" \
  "s_waitcnt lgkmcnt(3)\n\t" \
  "ds_read_b64_tr_b16 a[214:215], v236 offset:12288\n\t" \
  "v_add_u32 v231, 131072, v227\n\t" \
  "v_mfma_f32_16x16x32_bf16 a[96:99], a[200:203], v[56:59], a[96:99]\n\t" \
  "ds_read_b64_tr_b16 a[216:217], v236 offset:16384\n\t" \
  "v_xor_b32 v233, 32, v231\n\t" \
  "v_mfma_f32_16x16x32_bf16 a[96:99], a[204:207], v[60:63], a[96:99]\n\t" \
  "buffer_load_dwordx4 v238, %[wo], s71 offen offset:2048 lds\n\t" \
  "v_mfma_f32_16x16x32_bf16 a[100:103], a[204:207], v[92:95], a[100:103]\n\t" \
  "s_lshl_b32 s75, %[wid], 9\n\t" \
  "ds_read_b64_tr_b16 a[218:219], v236 offset:20480\n\t" \
  "v_mfma_f32_16x16x32_bf16 a[100:103], a[200:203], v[88:91], a[100:103]\n\t" \
  "ds_read_b64_tr_b16 a[220:221], v236 offset:24576\n\t" \
  "v_lshl_add_u32 v252, v244, 3, s75\n\t" \
  "v_mfma_f32_16x16x32_bf16 a[100:103], a[196:199], v[84:87], a[100:103]\n\t" \
  "ds_read_b64_tr_b16 a[222:223], v236 offset:28672\n\t" \
  "v_mfma_f32_16x16x32_bf16 a[100:103], a[192:195], v[80:83], a[100:103]\n\t" \
  "v_mfma_f32_16x16x32_bf16 a[104:107], a[192:195], v[64:67], a[104:107]\n\t" \
  "buffer_load_dwordx4 v239, %[wo], s71 offen offset:3072 lds\n\t" \
  "v_mfma_f32_16x16x32_bf16 a[104:107], a[196:199], v[68:71], a[104:107]\n\t" \
  "v_xor_b32 v237, 0x18000, v234\n\t" \
  "v_mfma_f32_16x16x32_bf16 a[104:107], a[200:203], v[72:75], a[104:107]\n\t" \
  "ds_read_b64_tr_b16 v[128:129], v237 offset:0\n\t" \
  "v_mfma_f32_16x16x32_bf16 a[104:107], a[204:207], v[76:79], a[104:107]\n\t" \
  "s_waitcnt lgkmcnt(5)\n\t" \
  "ds_read_b64_tr_b16 v[130:131], v237 offset:4096\n\t" \
  "v_mfma_f32_16x16x32_bf16 a[112:115], a[208:211], v[48:51], a[112:115]\n\t" \
  "ds_read_b64_tr_b16 v[132:133], v237 offset:8192\n\t" \
  "v_mfma_f32_16x16x32_bf16 a[112:115], a[212:215], v[52:55], a[112:115]\n\t" \
  "s_waitcnt lgkmcnt(3)\n\t" \
  "ds_read_b64_tr_b16 v[134:135], v237 offset:12288\n\t" \
  "v_xor_b32 v235, 0x8000, v234\n\t" \
  "v_mfma_f32_16x16x32_bf16 a[112:115], a[216:219], v[56:59], a[112:115]\n\t" \
  "ds_read_b64_tr_b16 v[136:137], v235 offset:0\n\t" \
  "v_mfma_f32_16x16x32_bf16 a[112:115], a[220:223], v[60:63], a[112:115]\n\t" \
  "ds_read_b64_tr_b16 v[138:139], v235 offset:4096\n\t" \
  "v_mfma_f32_16x16x32_bf16 a[116:119], a[220:223], v[92:95], a[116:119]\n\t" \
  "ds_read_b64_tr_b16 v[140:141], v235 offset:8192\n\t" \
  "v_mfma_f32_16x16x32_bf16 a[116:119], a[216:219], v[88:91], a[116:119]\n\t" \
  "ds_read_b64_tr_b16 v[142:143], v235 offset:12288\n\t" \
  "v_mfma_f32_16x16x32_bf16 a[116:119], a[212:215], v[84:87], a[116:119]\n\t" \
  "v_mfma_f32_16x16x32_bf16 a[116:119], a[208:211], v[80:83], a[116:119]\n\t" \
  "s_xor_b32 s73, %[eso], 786432\n\t" \
  "v_mfma_f32_16x16x32_bf16 a[120:123], a[208:211], v[64:67], a[120:123]\n\t" \
  "v_xor_b32 v236, 0x18020, v234\n\t" \
  "v_mfma_f32_16x16x32_bf16 a[120:123], a[212:215], v[68:71], a[120:123]\n\t" \
  "ds_read_b64_tr_b16 v[144:145], v236 offset:0\n\t" \
  "v_mfma_f32_16x16x32_bf16 a[120:123], a[216:219], v[72:75], a[120:123]\n\t" \
  "ds_read_b64_tr_b16 v[146:147], v236 offset:4096\n\t" \
  "v_mfma_f32_16x16x32_bf16 a[120:123], a[220:223], v[76:79], a[120:123]\n\t" \
  "s_waitcnt vmcnt(16) lgkmcnt(6)\n\t" \
  "buffer_load_dwordx4 v[48:51], v242, %[ws], s73 offen offset:0 sc1\n\t" \
  "v_mfma_f32_16x16x32_bf16 a[0:3], v[128:131], v[16:19], a[0:3]\n\t" \
  "ds_read_b64_tr_b16 v[148:149], v236 offset:8192\n\t" \
  "v_mfma_f32_16x16x32_bf16 a[0:3], v[132:135], v[20:23], a[0:3]\n\t" \
  "s_waitcnt lgkmcnt(3)\n\t" \
  "buffer_load_dwordx4 v[52:55], v242, %[ws], s73 offen offset:1024 sc1\n\t" \
  "v_mfma_f32_16x16x32_bf16 a[0:3], v[136:139], v[24:27], a[0:3]\n\t" \
  "ds_read_b64_tr_b16 v[150:151], v236 offset:12288\n\t" \
  "v_xor_b32 v237, 0x8020, v234\n\t" \
  "v_mfma_f32_16x16x32_bf16 a[0:3], v[140:143], v[28:31], a[0:3]\n\t" \
  "s_waitcnt vmcnt(14)\n\t" \
  "ds_read_b64_tr_b16 v[152:153], v237 offset:0\n\t" \
  "v_mfma_f32_16x16x32_bf16 a[4:7], v[140:143], v[12:15], a[4:7]\n\t" \
  "ds_read_b64_tr_b16 v[154:155], v237 offset:4096\n\t" \
  "v_mfma_f32_16x16x32_bf16 a[4:7], v[136:139], v[8:11], a[4:7]\n\t" \
  "buffer_load_dwordx4 v[56:59], v242, %[ws], s73 offen offset:2048 sc1\n\t" \
  "v_mfma_f32_16x16x32_bf16 a[4:7], v[132:135], v[4:7], a[4:7]\n\t" \
  "ds_read_b64_tr_b16 v[156:157], v237 offset:8192\n\t" \
  "v_mfma_f32_16x16x32_bf16 a[4:7], v[128:131], v[0:3], a[4:7]\n\t" \
  "s_waitcnt vmcnt(11)\n\t" \
  "ds_read_b64_tr_b16 v[158:159], v237 offset:12288\n\t" \
  "v_mfma_f32_16x16x32_bf16 a[8:11], v[128:131], v[32:35], a[8:11]\n\t" \
  "v_xor_b32 v235, 0x18040, v234\n\t" \
  "v_mfma_f32_16x16x32_bf16 a[8:11], v[132:135], v[36:39], a[8:11]\n\t" \
  "buffer_load_dwordx4 v[60:63], v242, %[ws], s73 offen offset:3072 sc1\n\t" \
  "s_xor_b32 s73, %[eso], 819200\n\t" \
  "v_mfma_f32_16x16x32_bf16 a[8:11], v[136:139], v[40:43], a[8:11]\n\t" \
  "ds_read_b64_tr_b16 a[208:209], v235 offset:0\n\t" \
  "v_mfma_f32_16x16x32_bf16 a[8:11], v[140:143], v[44:47], a[8:11]\n\t" \
  "s_waitcnt lgkmcnt(5)\n\t" \
  "ds_read_b64_tr_b16 a[210:211], v235 offset:4096\n\t" \
  "buffer_load_dword v246, v245, %[ws], %[fw2] offen sc1\n\t" \
  "v_mfma_f32_16x16x32_bf16 a[16:19], v[144:147], v[16:19], a[16:19]\n\t" \
  "ds_read_b64_tr_b16 a[212:213], v235 offset:8192\n\t" \
  "v_mfma_f32_16x16x32_bf16 a[16:19], v[148:151], v[20:23], a[16:19]\n\t" \
  "s_waitcnt lgkmcnt(3)\n\t" \
  "ds_read_b64_tr_b16 a[214:215], v235 offset:12288\n\t" \
  "v_xor_b32 v236, 0x8040, v234\n\t" \
  "v_mfma_f32_16x16x32_bf16 a[16:19], v[152:155], v[24:27], a[16:19]\n\t" \
  "ds_read_b64_tr_b16 a[216:217], v236 offset:0\n\t" \
  "v_mfma_f32_16x16x32_bf16 a[16:19], v[156:159], v[28:31], a[16:19]\n\t" \
  "buffer_load_dwordx4 v[80:83], v242, %[ws], s73 offen offset:0 sc1\n\t" \
  "v_mfma_f32_16x16x32_bf16 a[20:23], v[156:159], v[12:15], a[20:23]\n\t" \
  "ds_read_b64_tr_b16 a[218:219], v236 offset:4096\n\t" \
  "v_mfma_f32_16x16x32_bf16 a[20:23], v[152:155], v[8:11], a[20:23]\n\t" \
  "ds_read_b64_tr_b16 a[220:221], v236 offset:8192\n\t" \
  "v_mfma_f32_16x16x32_bf16 a[20:23], v[148:151], v[4:7], a[20:23]\n\t" \
  "ds_read_b64_tr_b16 a[222:223], v236 offset:12288\n\t" \
  "v_mfma_f32_16x16x32_bf16 a[20:23], v[144:147], v[0:3], a[20:23]\n\t" \
  "buffer_load_dwordx4 v[84:87], v242, %[ws], s73 offen offset:1024 sc1\n\t" \
  "v_mfma_f32_16x16x32_bf16 a[24:27], v[144:147], v[32:35], a[24:27]\n\t" \
  "v_xor_b32 v237, 0x18060, v234\n\t" \
  "v_mfma_f32_16x16x32_bf16 a[24:27], v[148:151], v[36:39], a[24:27]\n\t" \
  "ds_read_b64_tr_b16 a[192:193], v237 offset:0\n\t" \
  "v_mfma_f32_16x16x32_bf16 a[24:27], v[152:155], v[40:43], a[24:27]\n\t" \
  "ds_read_b64_tr_b16 a[194:195], v237 offset:4096\n\t" \
  "v_mfma_f32_16x16x32_bf16 a[24:27], v[156:159], v[44:47], a[24:27]\n\t" \
  "s_waitcnt lgkmcnt(6)\n\t" \
  "ds_read_b64_tr_b16 a[196:197], v237 offset:8192\n\t" \
  "v_mfma_f32_16x16x32_bf16 a[32:35], a[208:211], v[16:19], a[32:35]\n\t" \
  "buffer_load_dwordx4 v[88:91], v242, %[ws], s73 offen offset:2048 sc1\n\t" \
  "v_mfma_f32_16x16x32_bf16 a[32:35], a[212:215], v[20:23], a[32:35]\n\t" \
  "s_waitcnt lgkmcnt(3)\n\t" \
  "ds_read_b64_tr_b16 a[198:199], v237 offset:12288\n\t" \
  "v_xor_b32 v235, 0x8060, v234\n\t" \
  "v_mfma_f32_16x16x32_bf16 a[32:35], a[216:219], v[24:27], a[32:35]\n\t" \
  "ds_read_b64_tr_b16 a[200:201], v235 offset:0\n\t" \
  "v_mfma_f32_16x16x32_bf16 a[32:35], a[220:223], v[28:31], a[32:35]\n\t" \
  "ds_read_b64_tr_b16 a[202:203], v235 offset:4096\n\t" \
  "v_mfma_f32_16x16x32_bf16 a[36:39], a[220:223], v[12:15], a[36:39]\n\t" \
  "buffer_load_dwordx4 v[92:95], v242, %[ws], s73 offen offset:3072 sc1\n\t" \
  "s_xor_b32 s73, %[eso], 851968\n\t" \
  "v_mfma_f32_16x16x32_bf16 a[36:39], a[216:219], v[8:11], a[36:39]\n\t" \
  "ds_read_b64_tr_b16 a[204:205], v235 offset:8192\n\t" \
  "v_mfma_f32_16x16x32_bf16 a[36:39], a[212:215], v[4:7], a[36:39]\n\t" \
  "ds_read_b64_tr_b16 a[206:207], v235 offset:12288\n\t" \
  "v_mfma_f32_16x16x32_bf16 a[36:39], a[208:211], v[0:3], a[36:39]\n\t" \
  "v_mfma_f32_16x16x32_bf16 a[40:43], a[208:211], v[32:35], a[40:43]\n\t" \
  "buffer_load_dwordx4 v[64:67], v242, %[ws], s73 offen offset:0 sc1\n\t" \
  "v_mfma_f32_16x16x32_bf16 a[40:43], a[212:215], v[36:39], a[40:43]\n\t" \
  "v_mfma_f32_16x16x32_bf16 a[40:43], a[216:219], v[40:43], a[40:43]\n\t" \
  "v_mfma_f32_16x16x32_bf16 a[40:43], a[220:223], v[44:47], a[40:43]\n\t" \
  "s_waitcnt lgkmcnt(4)\n\t" \
  "v_xor_b32 v236, 0x18080, v234\n\t" \
  "v_mfma_f32_16x16x32_bf16 a[48:51], a[192:195], v[16:19], a[48:51]\n\t" \
  "s_add_u32 m0, %[m], 0\n\t" \
  "ds_read_b64_tr_b16 a[208:209], v236 offset:0\n\t" \
  "v_mfma_f32_16x16x32_bf16 a[48:51], a[196:199], v[20:23], a[48:51]\n\t" \
  "s_waitcnt lgkmcnt(1)\n\t" \
  "buffer_load_dwordx4 v[68:71], v242, %[ws], s73 offen offset:1024 sc1\n\t" \
  "v_mfma_f32_16x16x32_bf16 a[48:51], a[200:203], v[24:27], a[48:51]\n\t" \
  "ds_read_b64_tr_b16 a[210:211], v236 offset:4096\n\t" \
  "v_mfma_f32_16x16x32_bf16 a[48:51], a[204:207], v[28:31], a[48:51]\n\t" \
  "s_waitcnt vmcnt(15)\n\t" \
  "s_barrier\n\t" \
  "v_mfma_f32_16x16x32_bf16 a[52:55], a[204:207], v[12:15], a[52:55]\n\t" \
  "s_xor_b32 s71, %[wob], 1966080\n\t" \
  "ds_read_b64_tr_b16 a[212:213], v236 offset:8192\n\t" \
  "v_mfma_f32_16x16x32_bf16 a[52:55], a[200:203], v[8:11], a[52:55]\n\t" \
  "buffer_load_dwordx4 v[72:75], v242, %[ws], s73 offen offset:2048 sc1\n\t" \
  "v_mfma_f32_16x16x32_bf16 a[52:55], a[196:199], v[4:7], a[52:55]\n\t" \
  "ds_read_b64_tr_b16 a[214:215], v236 offset:12288\n\t" \
  "v_xor_b32 v237, 0x8080, v234\n\t" \
  "v_mfma_f32_16x16x32_bf16 a[52:55], a[192:195], v[0:3], a[52:55]\n\t" \
  "ds_read_b64_tr_b16 a[216:217], v237 offset:0\n\t" \
  "v_mfma_f32_16x16x32_bf16 a[56:59], a[192:195], v[32:35], a[56:59]\n\t" \
  "ds_read_b64_tr_b16 a[218:219], v237 offset:4096\n\t" \
  "v_mfma_f32_16x16x32_bf16 a[56:59], a[196:199], v[36:39], a[56:59]\n\t" \
  "ds_read_b64_tr_b16 a[220:221], v237 offset:8192\n\t" \
  "v_mfma_f32_16x16x32_bf16 a[56:59], a[200:203], v[40:43], a[56:59]\n\t" \
  "buffer_load_dwordx4 v[76:79], v242, %[ws], s73 offen offset:3072 sc1\n\t" \
  "ds_read_b64_tr_b16 a[222:223], v237 offset:12288\n\t" \
  "v_mfma_f32_16x16x32_bf16 a[56:59], a[204:207], v[44:47], a[56:59]\n\t" \
  "s_waitcnt lgkmcnt(4)\n\t" \
  "v_xor_b32 v235, 0x180a0, v234\n\t" \
  "v_mfma_f32_16x16x32_bf16 a[64:67], a[208:211], v[16:19], a[64:67]\n\t" \
  "s_add_u32 s75, s69, 0\n\t" \
  "ds_read_b64_tr_b16 a[192:193], v235 offset:0\n\t" \
  "v_mfma_f32_16x16x32_bf16 a[64:67], a[212:215], v[20:23], a[64:67]\n\t" \
  "s_waitcnt lgkmcnt(1)\n\t" \
  "s_waitcnt vmcnt(8)\n\t" \
  "v_cmp_ne_u32 vcc, %[tag], v246\n\t" \
  "s_and_b32 vcc_lo, vcc_lo, 0xfe0000\n\t" \
  "s_cbranch_scc1 .Lop_s3\n\t" \
  ".Lop_k3:\n\t" \
  "v_mfma_f32_16x16x32_bf16 a[64:67], a[216:219], v[24:27], a[64:67]\n\t" \
  "buffer_load_dwordx4 v240, %[wo], s71 offen offset:0 lds\n\t" \
  "v_mfma_f32_16x16x32_bf16 a[64:67], a[220:223], v[28:31], a[64:67]\n\t" \
  "ds_read_b64_tr_b16 a[194:195], v235 offset:4096\n\t" \
  "v_mfma_f32_16x16x32_bf16 a[68:71], a[220:223], v[12:15], a[68:71]\n\t" \
  "ds_read_b64_tr_b16 a[196:197], v235 offset:8192\n\t" \
  "v_mfma_f32_16x16x32_bf16 a[68:71], a[216:219], v[8:11], a[68:71]\n\t" \
  "ds_read_b64_tr_b16 a[198:199], v235 offset:12288\n\t" \
  "v_xor_b32 v236, 0x80a0, v234\n\t" \
  "v_mfma_f32_16x16x32_bf16 a[68:71], a[212:215], v[4:7], a[68:71]\n\t" \
  "buffer_load_dwordx4 v[208:211], v170, %[ws], s75 offen sc1\n\t" \
  "v_mfma_f32_16x16x32_bf16 a[68:71], a[208:211], v[0:3], a[68:71]\n\t" \
  "s_add_u32 s75, s69, 32768\n\t" \
  "ds_read_b64_tr_b16 a[200:201], v236 offset:0\n\t" \
  "v_mfma_f32_16x16x32_bf16 a[72:75], a[208:211], v[32:35], a[72:75]\n\t" \
  "ds_read_b64_tr_b16 a[202:203], v236 offset:4096\n\t" \
  "v_mfma_f32_16x16x32_bf16 a[72:75], a[212:215], v[36:39], a[72:75]\n\t" \
  "ds_read_b64_tr_b16 a[204:205], v236 offset:8192\n\t" \
  "v_mfma_f32_16x16x32_bf16 a[72:75], a[216:219], v[40:43], a[72:75]\n\t" \
  "ds_read_b64_tr_b16 a[206:207], v236 offset:12288\n\t" \
  "v_mfma_f32_16x16x32_bf16 a[72:75], a[220:223], v[44:47], a[72:75]\n\t" \
  "s_waitcnt lgkmcnt(4)\n\t" \
  "buffer_load_dwordx4 v[212:215], v170, %[ws], s75 offen sc1\n\t" \
  "v_mfma_f32_16x16x32_bf16 a[80:83], a[192:195], v[16:19], a[80:83]\n\t" \
  "s_add_u32 s75, s69, 65536\n\t" \
  "v_xor_b32 v237, 0x180c0, v234\n\t" \
  "v_mfma_f32_16x16x32_bf16 a[80:83], a[196:199], v[20:23], a[80:83]\n\t" \
  "s_waitcnt lgkmcnt(0)\n\t" \
  "ds_read_b64_tr_b16 a[208:209], v237 offset:0\n\t" \
  "v_mfma_f32_16x16x32_bf16 a[80:83], a[200:203], v[24:27], a[80:83]\n\t" \
  "ds_read_b64_tr_b16 a[210:211], v237 offset:4096\n\t" \
  "v_mfma_f32_16x16x32_bf16 a[80:83], a[204:207], v[28:31], a[80:83]\n\t" \
  "buffer_load_dwordx4 v[216:219], v170, %[ws], s75 offen sc1\n\t" \
  "v_mfma_f32_16x16x32_bf16 a[84:87], a[204:207], v[12:15], a[84:87]\n\t" \
  "s_add_u32 s75, s69, 98304\n\t" \
  "ds_read_b64_tr_b16 a[212:213], v237 offset:8192\n\t" \
  "v_mfma_f32_16x16x32_bf16 a[84:87], a[200:203], v[8:11], a[84:87]\n\t" \
  "ds_read_b64_tr_b16 a[214:215], v237 offset:12288\n\t" \
  "v_xor_b32 v235, 0x80c0, v234\n\t" \
  "v_mfma_f32_16x16x32_bf16 a[84:87], a[196:199], v[4:7], a[84:87]\n\t" \
  "ds_read_b64_tr_b16 a[216:217], v235 offset:0\n\t" \
  "v_mfma_f32_16x16x32_bf16 a[84:87], a[192:195], v[0:3], a[84:87]\n\t" \
  "ds_read_b64_tr_b16 a[218:219], v235 offset:4096\n\t" \
  "v_mfma_f32_16x16x32_bf16 a[88:91], a[192:195], v[32:35], a[88:91]\n\t" \
  "buffer_load_dwordx4 v[220:223], v170, %[ws], s75 offen sc1\n\t" \
  "v_mfma_f32_16x16x32_bf16 a[88:91], a[196:199], v[36:39], a[88:91]\n\t" \
  "ds_read_b64_tr_b16 a[220:221], v235 offset:8192\n\t" \
  "v_mfma_f32_16x16x32_bf16 a[88:91], a[200:203], v[40:43], a[88:91]\n\t" \
  "ds_read_b64_tr_b16 a[222:223], v235 offset:12288\n\t" \
  "v_mfma_f32_16x16x32_bf16 a[88:91], a[204:207], v[44:47], a[88:91]\n\t" \
  "s_waitcnt lgkmcnt(4)\n\t" \
  "v_xor_b32 v236, 0x180e0, v234\n\t" \
  "v_mfma_f32_16x16x32_bf16 a[96:99], a[208:211], v[16:19], a[96:99]\n\t" \
  "buffer_load_dwordx4 v241, %[wo], s71 offen offset:1024 lds\n\t" \
  "v_mfma_f32_16x16x32_bf16 a[96:99], a[212:215], v[20:23], a[96:99]\n\t" \
  "s_waitcnt lgkmcnt(0)\n\t" \
  "ds_read_b64_tr_b16 a[192:193], v236 offset:0\n\t" \
  "v_mfma_f32_16x16x32_bf16 a[96:99], a[216:219], v[24:27], a[96:99]\n\t" \
  "ds_read_b64_tr_b16 a[194:195], v236 offset:4096\n\t" \
  "v_mfma_f32_16x16x32_bf16 a[96:99], a[220:223], v[28:31], a[96:99]\n\t" \
  "ds_read_b64_tr_b16 a[196:197], v236 offset:8192\n\t" \
  "v_mfma_f32_16x16x32_bf16 a[100:103], a[220:223], v[12:15], a[100:103]\n\t" \
  "buffer_load_dwordx4 v238, %[wo], s71 offen offset:2048 lds\n\t" \
  "v_mfma_f32_16x16x32_bf16 a[100:103], a[216:219], v[8:11], a[100:103]\n\t" \
  "ds_read_b64_tr_b16 a[198:199], v236 offset:12288\n\t" \
  "v_xor_b32 v237, 0x80e0, v234\n\t" \
  "v_mfma_f32_16x16x32_bf16 a[100:103], a[212:215], v[4:7], a[100:103]\n\t" \
  "ds_read_b64_tr_b16 a[200:201], v237 offset:0\n\t" \
  "v_mfma_f32_16x16x32_bf16 a[100:103], a[208:211], v[0:3], a[100:103]\n\t" \
  "ds_read_b64_tr_b16 a[202:203], v237 offset:4096\n\t" \
  "v_mfma_f32_16x16x32_bf16 a[104:107], a[208:211], v[32:35], a[104:107]\n\t" \
  "ds_read_b64_tr_b16 a[204:205], v237 offset:8192\n\t" \
  "v_mfma_f32_16x16x32_bf16 a[104:107], a[212:215], v[36:39], a[104:107]\n\t" \
  "buffer_load_dwordx4 v239, %[wo], s71 offen offset:3072 lds\n\t" \
  "s_add_u32 m0, %[m], 16384\n\t" \
  "buffer_load_dword v224, v171, %[sr], s77 offen\n\t" \
  "v_mfma_f32_16x16x32_bf16 a[104:107], a[216:219], v[40:43], a[104:107]\n\t" \
  "s_xor_b32 s71, %[eso], 1015808\n\t" \
  "ds_read_b64_tr_b16 a[206:207], v237 offset:12288\n\t" \
  "v_mfma_f32_16x16x32_bf16 a[104:107], a[220:223], v[44:47], a[104:107]\n\t" \
  "s_waitcnt lgkmcnt(4)\n\t" \
  "v_xor_b32 v235, 0x14000, v234\n\t" \
  "v_mfma_f32_16x16x32_bf16 a[112:115], a[192:195], v[16:19], a[112:115]\n\t" \
  "ds_read_b64_tr_b16 a[208:209], v235 offset:0\n\t" \
  "v_mfma_f32_16x16x32_bf16 a[112:115], a[196:199], v[20:23], a[112:115]\n\t" \
  "s_waitcnt lgkmcnt(1)\n\t" \
  "ds_read_b64_tr_b16 a[210:211], v235 offset:4096\n\t" \
  "v_mfma_f32_16x16x32_bf16 a[112:115], a[200:203], v[24:27], a[112:115]\n\t" \
  "ds_read_b64_tr_b16 a[212:213], v235 offset:8192\n\t" \
  "v_mfma_f32_16x16x32_bf16 a[112:115], a[204:207], v[28:31], a[112:115]\n\t" \
  "buffer_load_dwordx4 v242, %[ws], s71 offen offset:0 sc1 lds\n\t" \
  "v_mfma_f32_16x16x32_bf16 a[116:119], a[204:207], v[12:15], a[116:119]\n\t" \
  "ds_read_b64_tr_b16 a[214:215], v235 offset:12288\n\t" \
  "v_mfma_f32_16x16x32_bf16 a[116:119], a[200:203], v[8:11], a[116:119]\n\t" \
  "ds_read_b64_tr_b16 a[216:217], v235 offset:32768\n\t" \
  "v_mfma_f32_16x16x32_bf16 a[116:119], a[196:199], v[4:7], a[116:119]\n\t" \
  "ds_read_b64_tr_b16 a[218:219], v235 offset:36864\n\t" \
  "v_mfma_f32_16x16x32_bf16 a[116:119], a[192:195], v[0:3], a[116:119]\n\t" \
  ".Lhs13:\n\ts_cbranch_execz .Lhs13n\n\t" \
  "buffer_load_dwordx4 v242, %[ws], s71 offen offset:1024 sc1 lds\n\t" \
  "v_mfma_f32_16x16x32_bf16 a[120:123], a[192:195], v[32:35], a[120:123]\n\t" \
  "s_xor_b32 s73, %[eso], 917504\n\t" \
  "ds_read_b64_tr_b16 a[220:221], v235 offset:40960\n\t" \
  "v_mfma_f32_16x16x32_bf16 a[120:123], a[196:199], v[36:39], a[120:123]\n\t" \
  "ds_read_b64_tr_b16 a[222:223], v235 offset:45056\n\t" \
  "v_mfma_f32_16x16x32_bf16 a[120:123], a[200:203], v[40:43], a[120:123]\n\t" \
  "v_mfma_f32_16x16x32_bf16 a[120:123], a[204:207], v[44:47], a[120:123]\n\t" \
  "s_waitcnt lgkmcnt(4)\n\t" \
  "buffer_load_dwordx4 v[16:19], v242, %[ws], s73 offen offset:0 sc1\n\t" \
  STP("v_writelane_b32 %[stv], s94, 30\n\t") \
  STP("v_writelane_b32 %[stv], s92, 31\n\t") \
  STP("s_memrealtime s[94:95]\n\t") \
  STP("s_memtime s[92:93]\n\t") \
  "v_mfma_f32_16x16x32_bf16 a[0:3], a[208:211], v[48:51], a[0:3]\n\t" \
  "v_xor_b32 v236, 0x14020, v234\n\t" \
  "v_perm_b32 v241, v180, v176, s79\n\t" \
  "v_mfma_f32_16x16x32_bf16 a[0:3], a[212:215], v[52:55], a[0:3]\n\t" \
  "s_waitcnt lgkmcnt(0)\n\t" \
  "ds_read_b64_tr_b16 a[192:193], v236 offset:0\n\t" \
  "v_perm_b32 v180, v180, v176, s80\n\t" \
  "v_mfma_f32_16x16x32_bf16 a[0:3], a[216:219], v[56:59], a[0:3]\n\t" \
  "ds_read_b64_tr_b16 a[194:195], v236 offset:4096\n\t" \
  "v_perm_b32 v176, v181, v177, s79\n\t" \
  "v_mfma_f32_16x16x32_bf16 a[0:3], a[220:223], v[60:63], a[0:3]\n\t" \
  "s_waitcnt vmcnt(16)\n\t" \
  "ds_read_b64_tr_b16 a[196:197], v236 offset:8192\n\t" \
  "v_perm_b32 v181, v181, v177, s80\n\t" \
  "v_mfma_f32_16x16x32_bf16 a[4:7], a[220:223], v[92:95], a[4:7]\n\t" \
  "buffer_load_dwordx4 v[20:23], v242, %[ws], s73 offen offset:1024 sc1\n\t" \
  "v_mfma_f32_16x16x32_bf16 a[4:7], a[216:219], v[88:91], a[4:7]\n\t" \
  "ds_read_b64_tr_b16 a[198:199], v236 offset:12288\n\t" \
  "v_perm_b32 v177, v182, v178, s79\n\t" \
  "v_mfma_f32_16x16x32_bf16 a[4:7], a[212:215], v[84:87], a[4:7]\n\t" \
  "ds_read_b64_tr_b16 a[200:201], v236 offset:32768\n\t" \
  "v_mfma_f32_16x16x32_bf16 a[4:7], a[208:211], v[80:83], a[4:7]\n\t" \
  "s_waitcnt vmcnt(13)\n\t" \
  "ds_read_b64_tr_b16 a[202:203], v236 offset:36864\n\t" \
  "v_mfma_f32_16x16x32_bf16 a[8:11], a[208:211], v[64:67], a[8:11]\n\t" \
  "buffer_load_dwordx4 v[24:27], v242, %[ws], s73 offen offset:2048 sc1\n\t" \
  "v_mfma_f32_16x16x32_bf16 a[8:11], a[212:215], v[68:71], a[8:11]\n\t" \
  "ds_read_b64_tr_b16 a[204:205], v236 offset:40960\n\t" \
  "v_perm_b32 v182, v182, v178, s80\n\t" \
  "v_mfma_f32_16x16x32_bf16 a[8:11], a[216:219], v[72:75], a[8:11]\n\t" \
  "ds_read_b64_tr_b16 a[206:207], v236 offset:45056\n\t" \
  "v_mfma_f32_16x16x32_bf16 a[8:11], a[220:223], v[76:79], a[8:11]\n\t" \
  "s_waitcnt lgkmcnt(4)\n\t" \
  "v_xor_b32 v237, 0x14040, v234\n\t" \
  "v_perm_b32 v178, v183, v179, s79\n\t" \
  "v_mfma_f32_16x16x32_bf16 a[16:19], a[192:195], v[48:51], a[16:19]\n\t" \
  "ds_read_b64_tr_b16 a[208:209], v237 offset:0\n\t" \
  "v_mfma_f32_16x16x32_bf16 a[16:19], a[196:199], v[52:55], a[16:19]\n\t" \
  "s_waitcnt lgkmcnt(1)\n\t" \
  "buffer_load_dwordx4 v[28:31], v242, %[ws], s73 offen offset:3072 sc1\n\t" \
  "s_xor_b32 s73, %[eso], 950272\n\t" \
  "v_mfma_f32_16x16x32_bf16 a[16:19], a[200:203], v[56:59], a[16:19]\n\t" \
  "ds_read_b64_tr_b16 a[210:211], v237 offset:4096\n\t" \
  "v_perm_b32 v183, v183, v179, s80\n\t" \
  "v_mfma_f32_16x16x32_bf16 a[16:19], a[204:207], v[60:63], a[16:19]\n\t" \
  "ds_read_b64_tr_b16 a[212:213], v237 offset:8192\n\t" \
  "v_mfma_f32_16x16x32_bf16 a[20:23], a[204:207], v[92:95], a[20:23]\n\t" \
  "ds_read_b64_tr_b16 a[214:215], v237 offset:12288\n\t" \
  "v_bfrev_b32 v225, 1\n\t" \
  "v_mfma_f32_16x16x32_bf16 a[20:23], a[200:203], v[88:91], a[20:23]\n\t" \
  "buffer_load_dwordx4 v[0:3], v242, %[ws], s73 offen offset:0 sc1\n\t" \
  "v_mfma_f32_16x16x32_bf16 a[20:23], a[196:199], v[84:87], a[20:23]\n\t" \
  "ds_read_b64_tr_b16 a[216:217], v237 offset:32768\n\t" \
  "v_bfrev_b32 v226, 1\n\t" \
  "v_mfma_f32_16x16x32_bf16 a[20:23], a[192:195], v[80:83], a[20:23]\n\t" \
  "ds_read_b64_tr_b16 a[218:219], v237 offset:36864\n\t" \
  "v_writelane_b32 v225, s65, 0\n\t" \
  "v_mfma_f32_16x16x32_bf16 a[24:27], a[192:195], v[64:67], a[24:27]\n\t" \
  "ds_read_b64_tr_b16 a[220:221], v237 offset:40960\n\t" \
  "v_perm_b32 v179, v188, v184, s79\n\t" \
  "v_mfma_f32_16x16x32_bf16 a[24:27], a[196:199], v[68:71], a[24:27]\n\t" \
  "buffer_load_dwordx4 v[4:7], v242, %[ws], s73 offen offset:1024 sc1\n\t" \
  "v_mfma_f32_16x16x32_bf16 a[24:27], a[200:203], v[72:75], a[24:27]\n\t" \
  "ds_read_b64_tr_b16 a[222:223], v237 offset:45056\n\t" \
  "v_writelane_b32 v225, s66, 1\n\t" \
  "v_mfma_f32_16x16x32_bf16 a[24:27], a[204:207], v[76:79], a[24:27]\n\t" \
  "s_waitcnt lgkmcnt(4)\n\t" \
  "s_waitcnt vmcnt(8)\n\t" \
  "v_xor_b32 v246, v243, v209\n\t" \
  "v_bitop3_b32 v246, v246, v211, v243 bitop3:0xf6\n\t" \
  "v_mfma_f32_16x16x32_bf16 a[32:35], a[208:211], v[48:51], a[32:35]\n\t" \
  "v_bitop3_b32 v246, v246, v213, v243 bitop3:0xf6\n\t" \
  "v_xor_b32 v235, 0x14060, v234\n\t" \
  "v_mfma_f32_16x16x32_bf16 a[32:35], a[212:215], v[52:55], a[32:35]\n\t" \
  "s_waitcnt lgkmcnt(0)\n\t" \
  "ds_read_b64_tr_b16 a[192:193], v235 offset:0\n\t" \
  "v_bitop3_b32 v246, v246, v215, v243 bitop3:0xf6\n\t" \
  "v_mfma_f32_16x16x32_bf16 a[32:35], a[216:219], v[56:59], a[32:35]\n\t" \
  "buffer_load_dwordx4 v[8:11], v242, %[ws], s73 offen offset:2048 sc1\n\t" \
  "v_mfma_f32_16x16x32_bf16 a[32:35], a[220:223], v[60:63], a[32:35]\n\t" \
  "ds_read_b64_tr_b16 a[194:195], v235 offset:4096\n\t" \
  "v_bitop3_b32 v246, v246, v217, v243 bitop3:0xf6\n\t" \
  "v_mfma_f32_16x16x32_bf16 a[36:39], a[220:223], v[92:95], a[36:39]\n\t" \
  "ds_read_b64_tr_b16 a[196:197], v235 offset:8192\n\t" \
  "v_bitop3_b32 v246, v246, v219, v243 bitop3:0xf6\n\t" \
  "v_mfma_f32_16x16x32_bf16 a[36:39], a[216:219], v[88:91], a[36:39]\n\t" \
  "ds_read_b64_tr_b16 a[198:199], v235 offset:12288\n\t" \
  "v_bitop3_b32 v246, v246, v221, v243 bitop3:0xf6\n\t" \
  "v_mfma_f32_16x16x32_bf16 a[36:39], a[212:215], v[84:87], a[36:39]\n\t" \
  "buffer_load_dwordx4 v[12:15], v242, %[ws], s73 offen offset:3072 sc1\n\t" \
  "s_xor_b32 s73, %[eso], 983040\n\t" \
  "v_mfma_f32_16x16x32_bf16 a[36:39], a[208:211], v[80:83], a[36:39]\n\t" \
  "ds_read_b64_tr_b16 a[200:201], v235 offset:32768\n\t" \
  "v_bitop3_b32 v246, v246, v223, v243 bitop3:0xf6\n\t" \
  "v_mfma_f32_16x16x32_bf16 a[40:43], a[208:211], v[64:67], a[40:43]\n\t" \
  "ds_read_b64_tr_b16 a[202:203], v235 offset:36864\n\t" \
  "v_perm_b32 v188, v188, v184, s80\n\t" \
  "v_mfma_f32_16x16x32_bf16 a[40:43], a[212:215], v[68:71], a[40:43]\n\t" \
  "ds_read_b64_tr_b16 a[204:205], v235 offset:40960\n\t" \
  "v_writelane_b32 v226, s67, 2\n\t" \
  "v_mfma_f32_16x16x32_bf16 a[40:43], a[216:219], v[72:75], a[40:43]\n\t" \
  "s_mov_b32 s81, 0\n\t" \
  "ds_read_b64_tr_b16 a[206:207], v235 offset:45056\n\t" \
  "v_perm_b32 v184, v189, v185, s79\n\t" \
  "v_mfma_f32_16x16x32_bf16 a[40:43], a[220:223], v[76:79], a[40:43]\n\t" \
  "s_waitcnt lgkmcnt(4)\n\t" \
  "buffer_load_dwordx4 v[32:35], v242, %[ws], s73 offen offset:0 sc1\n\t" \
  "v_mfma_f32_16x16x32_bf16 a[48:51], a[192:195], v[48:51], a[48:51]\n\t" \
  "v_cmp_ne_u32 vcc, 0, v246\n\t" \
  "s_cbranch_vccnz .Lop_s4\n\t" \
  ".Lop_k4:\n\t" \
  "v_mfma_f32_16x16x32_bf16 a[48:51], a[196:199], v[52:55], a[48:51]\n\t" \
  "s_waitcnt lgkmcnt(0)\n\t" \
  "v_add_f32 v208, v208, v212\n\t" \
  "v_add_f32 v216, v216, v220\n\t" \
  "v_add_f32 v210, v210, v214\n\t" \
  "v_mfma_f32_16x16x32_bf16 a[48:51], a[200:203], v[56:59], a[48:51]\n\t" \
  "v_xor_b32 v236, 0x14080, v234\n\t" \
  "v_add_f32 v218, v218, v222\n\t" \
  "v_add_f32 v208, v208, v216\n\t" \
  "v_mfma_f32_16x16x32_bf16 a[48:51], a[204:207], v[60:63], a[48:51]\n\t" \
  "s_barrier\n\t" \
  "v_mfma_f32_16x16x32_bf16 a[52:55], a[204:207], v[92:95], a[52:55]\n\t" \
  "buffer_load_dwordx4 v[36:39], v242, %[ws], s73 offen offset:1024 sc1\n\t" \
  "v_add_f32 v210, v210, v218\n\t" \
  "v_mfma_f32_16x16x32_bf16 a[52:55], a[200:203], v[88:91], a[52:55]\n\t" \
  "ds_read_b64_tr_b16 a[208:209], v236 offset:0\n\t" \
  "ds_read_b64_tr_b16 a[210:211], v236 offset:4096\n\t" \
  "v_perm_b32 v189, v189, v185, s80\n\t" \
  "v_mfma_f32_16x16x32_bf16 a[52:55], a[196:199], v[84:87], a[52:55]\n\t" \
  "ds_read_b64_tr_b16 a[212:213], v236 offset:8192\n\t" \
  "ds_read_b64_tr_b16 a[214:215], v236 offset:12288\n\t" \
  "v_lshlrev_b32 v163, 16, v224\n\t" \
  "v_mfma_f32_16x16x32_bf16 a[52:55], a[192:195], v[80:83], a[52:55]\n\t" \
  "buffer_load_dwordx4 v[40:43], v242, %[ws], s73 offen offset:2048 sc1\n\t" \
  "v_and_b32 v164, 0xffff0000, v224\n\t" \
  "v_mfma_f32_16x16x32_bf16 a[56:59], a[192:195], v[64:67], a[56:59]\n\t" \
  "ds_read_b64_tr_b16 a[216:217], v236 offset:32768\n\t" \
  "ds_read_b64_tr_b16 a[218:219], v236 offset:36864\n\t" \
  "v_perm_b32 v185, v190, v186, s79\n\t" \
  "v_mfma_f32_16x16x32_bf16 a[56:59], a[196:199], v[68:71], a[56:59]\n\t" \
  "ds_read_b64_tr_b16 a[220:221], v236 offset:40960\n\t" \
  "v_cvt_pk_bf16_f32 v162, v208, v210\n\t" \
  "v_lshlrev_b32 v160, 16, v162\n\t" \
  "v_mfma_f32_16x16x32_bf16 a[56:59], a[200:203], v[72:75], a[56:59]\n\t" \
  "ds_read_b64_tr_b16 a[222:223], v236 offset:45056\n\t" \
  "v_and_b32 v161, 0xffff0000, v162\n\t" \
  "v_perm_b32 v190, v190, v186, s80\n\t" \
  "v_mfma_f32_16x16x32_bf16 a[56:59], a[204:207], v[76:79], a[56:59]\n\t" \
  "s_waitcnt lgkmcnt(4)\n\t" \
  "v_add_f32 v160, v160, v172\n\t" \
  "v_add_f32 v161, v161, v172\n\t" \
  "v_mfma_f32_16x16x32_bf16 a[64:67], a[208:211], v[48:51], a[64:67]\n\t" \
  "buffer_load_dwordx4 v[44:47], v242, %[ws], s73 offen offset:3072 sc1\n\t" \
  "v_mfma_f32_16x16x32_bf16 a[64:67], a[212:215], v[52:55], a[64:67]\n\t" \
  "s_waitcnt lgkmcnt(0)\n\t" \
  "v_xor_b32 v237, 0x140a0, v234\n\t" \
  "v_perm_b32 v186, v191, v187, s79\n\t" \
  "v_cvt_pk_bf16_f32 v162, v160, v161\n\t" \
  "v_mfma_f32_16x16x32_bf16 a[64:67], a[216:219], v[56:59], a[64:67]\n\t" \
  "ds_read_b64_tr_b16 a[192:193], v237 offset:0\n\t" \
  "v_lshlrev_b32 v160, 16, v162\n\t" \
  "v_mfma_f32_16x16x32_bf16 a[64:67], a[220:223], v[60:63], a[64:67]\n\t" \
  "ds_read_b64_tr_b16 a[194:195], v237 offset:4096\n\t" \
  "v_and_b32 v161, 0xffff0000, v162\n\t" \
  "v_mfma_f32_16x16x32_bf16 a[68:71], a[220:223], v[92:95], a[68:71]\n\t" \
  "buffer_load_dwordx4 v242, %[ws], s71 offen offset:2048 sc1 lds\n\t" \
  "v_perm_b32 v191, v191, v187, s80\n\t" \
  "v_mfma_f32_16x16x32_bf16 a[68:71], a[216:219], v[88:91], a[68:71]\n\t" \
  "ds_read_b64_tr_b16 a[196:197], v237 offset:8192\n\t" \
  "v_perm_b32 v187, v196, v192, s79\n\t" \
  "v_mfma_f32_16x16x32_bf16 a[68:71], a[212:215], v[84:87], a[68:71]\n\t" \
  "ds_read_b64_tr_b16 a[198:199], v237 offset:12288\n\t" \
  "v_perm_b32 v196, v196, v192, s80\n\t" \
  "v_mfma_f32_16x16x32_bf16 a[68:71], a[208:211], v[80:83], a[68:71]\n\t" \
  "ds_read_b64_tr_b16 a[200:201], v237 offset:32768\n\t" \
  "v_perm_b32 v192, v197, v193, s79\n\t" \
  "v_mfma_f32_16x16x32_bf16 a[72:75], a[208:211], v[64:67], a[72:75]\n\t" \
  "ds_write_b64 v174, v[160:161] offset:2560\n\t" \
  "v_mul_f32 v166, v160, v163\n\t" \
  "v_mfma_f32_16x16x32_bf16 a[72:75], a[212:215], v[68:71], a[72:75]\n\t" \
  "buffer_load_dwordx4 v242, %[ws], s71 offen offset:3072 sc1 lds\n\t" \
  "s_add_u32 m0, %[m], 32768\n\t" \
  "v_mfma_f32_16x16x32_bf16 a[72:75], a[216:219], v[72:75], a[72:75]\n\t" \
  "s_xor_b32 s71, %[eso], 360448\n\t" \
  "ds_read_b64_tr_b16 a[202:203], v237 offset:36864\n\t" \
  "ds_read_b64_tr_b16 a[204:205], v237 offset:40960\n\t" \
  "v_mul_f32 v167, v161, v164\n\t" \
  "ds_read_b64_tr_b16 a[206:207], v237 offset:45056\n\t" \
  "v_mfma_f32_16x16x32_bf16 a[72:75], a[220:223], v[76:79], a[72:75]\n\t" \
  "s_waitcnt lgkmcnt(5)\n\t" \
  "v_cvt_pk_bf16_f32 v168, v166, v167\n\t" \
  "v_add_f32 v162, v166, v167\n\t" \
  "v_mfma_f32_16x16x32_bf16 a[80:83], a[192:195], v[48:51], a[80:83]\n\t" \
  "v_lshlrev_b32 v163, 16, v168\n\t" \
  "v_xor_b32 v235, 0x140c0, v234\n\t" \
  "v_mfma_f32_16x16x32_bf16 a[80:83], a[196:199], v[52:55], a[80:83]\n\t" \
  "s_waitcnt lgkmcnt(0)\n\t" \
  "buffer_load_dwordx4 v242, %[ws], s71 offen offset:0 sc1 lds\n\t" \
  "v_and_b32 v164, 0xffff0000, v168\n\t" \
  "v_mfma_f32_16x16x32_bf16 a[80:83], a[200:203], v[56:59], a[80:83]\n\t" \
  "ds_read_b64_tr_b16 a[208:209], v235 offset:0\n\t" \
  "v_add_f32_dpp v162, v162, v162 row_ror:8 row_mask:0xf bank_mask:0xf\n\t" \
  "v_mfma_f32_16x16x32_bf16 a[80:83], a[204:207], v[60:63], a[80:83]\n\t" \
  "ds_read_b64_tr_b16 a[210:211], v235 offset:4096\n\t" \
  "v_sub_f32 v166, v166, v163\n\t" \
  "v_mfma_f32_16x16x32_bf16 a[84:87], a[204:207], v[92:95], a[84:87]\n\t" \
  "ds_read_b64_tr_b16 a[212:213], v235 offset:8192\n\t" \
  "v_sub_f32 v167, v167, v164\n\t" \
  "v_mfma_f32_16x16x32_bf16 a[84:87], a[200:203], v[88:91], a[84:87]\n\t" \
  "buffer_load_dwordx4 v242, %[ws], s71 offen offset:1024 sc1 lds\n\t" \
  "v_perm_b32 v197, v197, v193, s80\n\t" \
  "v_mfma_f32_16x16x32_bf16 a[84:87], a[196:199], v[84:87], a[84:87]\n\t" \
  "ds_read_b64_tr_b16 a[214:215], v235 offset:12288\n\t" \
  "v_add_f32_dpp v162, v162, v162 row_ror:4 row_mask:0xf bank_mask:0xf\n\t" \
  "v_mfma_f32_16x16x32_bf16 a[84:87], a[192:195], v[80:83], a[84:87]\n\t" \
  "ds_read_b64_tr_b16 a[216:217], v235 offset:32768\n\t" \
  "v_cvt_pk_bf16_f32 v169, v166, v167\n\t" \
  "v_mfma_f32_16x16x32_bf16 a[88:91], a[192:195], v[64:67], a[88:91]\n\t" \
  "s_mov_b32 s82, 0\n\t" \
  "ds_read_b64_tr_b16 a[218:219], v235 offset:36864\n\t" \
  "v_perm_b32 v193, v198, v194, s79\n\t" \
  "v_mfma_f32_16x16x32_bf16 a[88:91], a[196:199], v[68:71], a[88:91]\n\t" \
  "ds_read_b64_tr_b16 a[220:221], v235 offset:40960\n\t" \
  "v_add_f32_dpp v162, v162, v162 row_ror:2 row_mask:0xf bank_mask:0xf\n\t" \
  "v_mfma_f32_16x16x32_bf16 a[88:91], a[200:203], v[72:75], a[88:91]\n\t" \
  "buffer_load_dwordx4 v242, %[ws], s71 offen offset:2048 sc1 lds\n\t" \
  "ds_read_b64_tr_b16 a[222:223], v235 offset:45056\n\t" \
  "v_mfma_f32_16x16x32_bf16 a[88:91], a[204:207], v[76:79], a[88:91]\n\t" \
  "s_waitcnt lgkmcnt(4)\n\t" \
  "v_perm_b32 v198, v198, v194, s80\n\t" \
  "s_mov_b32 s83, 0xffff\n\t" \
  "v_mfma_f32_16x16x32_bf16 a[96:99], a[208:211], v[48:51], a[96:99]\n\t" \
  "v_add_f32_dpp v162, v162, v162 row_ror:1 row_mask:0xf bank_mask:0xf\n\t" \
  "v_xor_b32 v236, 0x140e0, v234\n\t" \
  "v_mfma_f32_16x16x32_bf16 a[96:99], a[212:215], v[52:55], a[96:99]\n\t" \
  "s_waitcnt lgkmcnt(0)\n\t" \
  "ds_read_b64_tr_b16 a[192:193], v236 offset:0\n\t" \
  "v_perm_b32 v194, v199, v195, s79\n\t" \
  "v_mfma_f32_16x16x32_bf16 a[96:99], a[216:219], v[56:59], a[96:99]\n\t" \
  "buffer_load_dwordx4 v242, %[ws], s71 offen offset:3072 sc1 lds\n\t" \
  "s_add_u32 m0, %[m], 98304\n\t" \
  "v_mfma_f32_16x16x32_bf16 a[96:99], a[220:223], v[60:63], a[96:99]\n\t" \
  "s_xor_b32 s71, %[eso], 491520\n\t" \
  "s_mov_b64 exec, s[82:83]\n\t" \
  "ds_write_b32 v173, v168 offset:1040\n\t" \
  "ds_write_b32 v173, v169 offset:1120\n\t" \
  "s_mov_b64 exec, -1\n\t" \
  "v_mfma_f32_16x16x32_bf16 a[100:103], a[220:223], v[92:95], a[100:103]\n\t" \
  "ds_read_b64_tr_b16 a[194:195], v236 offset:4096\n\t" \
  "v_readlane_b32 s87, v162, 32\n\t" \
  "v_readlane_b32 s88, v162, 16\n\t" \
  "v_mfma_f32_16x16x32_bf16 a[100:103], a[216:219], v[88:91], a[100:103]\n\t" \
  "ds_read_b64_tr_b16 a[196:197], v236 offset:8192\n\t" \
  "v_perm_b32 v199, v199, v195, s80\n\t" \
  "v_mfma_f32_16x16x32_bf16 a[100:103], a[212:215], v[84:87], a[100:103]\n\t" \
  "s_xor_b32 s73, %[eso], 196608\n\t" \
  "ds_read_b64_tr_b16 a[198:199], v236 offset:12288\n\t" \
  "v_perm_b32 v195, v204, v200, s79\n\t" \
  "v_mfma_f32_16x16x32_bf16 a[100:103], a[208:211], v[80:83], a[100:103]\n\t" \
  "buffer_load_dwordx4 v242, %[ws], s71 offen offset:0 sc1 lds\n\t" \
  "v_mfma_f32_16x16x32_bf16 a[104:107], a[208:211], v[64:67], a[104:107]\n\t" \
  "ds_read_b64_tr_b16 a[200:201], v236 offset:32768\n\t" \
  "ds_read_b64_tr_b16 a[202:203], v236 offset:36864\n\t" \
  "v_mfma_f32_16x16x32_bf16 a[104:107], a[212:215], v[68:71], a[104:107]\n\t" \
  "ds_read_b64_tr_b16 a[204:205], v236 offset:40960\n\t" \
  "v_perm_b32 v204, v204, v200, s80\n\t" \
  "v_mfma_f32_16x16x32_bf16 a[104:107], a[216:219], v[72:75], a[104:107]\n\t" \
  "ds_read_b64_tr_b16 a[206:207], v236 offset:45056\n\t" \
  "v_perm_b32 v200, v205, v201, s79\n\t" \
  "v_mfma_f32_16x16x32_bf16 a[104:107], a[220:223], v[76:79], a[104:107]\n\t" \
  "s_waitcnt lgkmcnt(4)\n\t" \
  "buffer_load_dwordx4 v[208:211], v242, %[ws], s73 offen offset:0 sc1\n\t" \
  "v_mfma_f32_16x16x32_bf16 a[112:115], a[192:195], v[48:51], a[112:115]\n\t" \
  "v_xor_b32 v237, 0x20040, v234\n\t" \
  "v_perm_b32 v205, v205, v201, s80\n\t" \
  "v_mfma_f32_16x16x32_bf16 a[112:115], a[196:199], v[52:55], a[112:115]\n\t" \
  "s_waitcnt lgkmcnt(0)\n\t" \
  "ds_read_b64_tr_b16 a[208:209], v237 offset:0\n\t" \
  "v_perm_b32 v201, v206, v202, s79\n\t" \
  "v_mfma_f32_16x16x32_bf16 a[112:115], a[200:203], v[56:59], a[112:115]\n\t" \
  "ds_read_b64_tr_b16 a[210:211], v237 offset:4096\n\t" \
  "v_perm_b32 v206, v206, v202, s80\n\t" \
  "v_mfma_f32_16x16x32_bf16 a[112:115], a[204:207], v[60:63], a[112:115]\n\t" \
  "buffer_load_dwordx4 v[212:215], v242, %[ws], s73 offen offset:1024 sc1\n\t" \
  "v_mfma_f32_16x16x32_bf16 a[116:119], a[204:207], v[92:95], a[116:119]\n\t" \
  "ds_read_b64_tr_b16 a[212:213], v237 offset:8192\n\t" \
  "v_perm_b32 v202, v207, v203, s79\n\t" \
  "v_mfma_f32_16x16x32_bf16 a[116:119], a[200:203], v[88:91], a[116:119]\n\t" \
  "ds_read_b64_tr_b16 a[214:215], v237 offset:12288\n\t" \
  "v_xor_b32 v235, 0x40, v234\n\t" \
  "v_mfma_f32_16x16x32_bf16 a[116:119], a[196:199], v[84:87], a[116:119]\n\t" \
  "ds_read_b64_tr_b16 a[216:217], v235 offset:0\n\t" \
  "v_perm_b32 v207, v207, v203, s80\n\t" \
  "v_mfma_f32_16x16x32_bf16 a[116:119], a[192:195], v[80:83], a[116:119]\n\t" \
  "ds_read_b64_tr_b16 a[218:219], v235 offset:4096\n\t" \
  "v_mfma_f32_16x16x32_bf16 a[120:123], a[192:195], v[64:67], a[120:123]\n\t" \
  "buffer_load_dwordx4 v[216:219], v242, %[ws], s73 offen offset:2048 sc1\n\t" \
  "v_mfma_f32_16x16x32_bf16 a[120:123], a[196:199], v[68:71], a[120:123]\n\t" \
  "s_mov_b32 s74, 16384\n\t" \
  "ds_read_b64_tr_b16 a[220:221], v235 offset:8192\n\t" \
  "v_mfma_f32_16x16x32_bf16 a[120:123], a[200:203], v[72:75], a[120:123]\n\t" \
  "ds_read_b64_tr_b16 a[222:223], v235 offset:12288\n\t" \
  "v_mfma_f32_16x16x32_bf16 a[120:123], a[204:207], v[76:79], a[120:123]\n\t" \
  "s_waitcnt vmcnt(18) lgkmcnt(4)\n\t" \
  "v_xor_b32 v236, 0x20060, v234\n\t" \
  STP("v_writelane_b32 %[stv], s94, 32\n\t") \
  STP("v_writelane_b32 %[stv], s92, 33\n\t") \
  STP("s_memrealtime s[94:95]\n\t") \
  STP("s_memtime s[92:93]\n\t") \
  "v_mfma_f32_16x16x32_bf16 a[32:35], a[208:211], v[16:19], a[32:35]\n\t" \
  "buffer_load_dwordx4 v[220:223], v242, %[ws], s73 offen offset:3072 sc1\n\t" \
  "v_mfma_f32_16x16x32_bf16 a[32:35], a[212:215], v[20:23], a[32:35]\n\t" \
  "s_waitcnt lgkmcnt(0)\n\t" \
  "ds_read_b64_tr_b16 a[192:193], v236 offset:0\n\t" \
  "v_mfma_f32_16x16x32_bf16 a[32:35], a[216:219], v[24:27], a[32:35]\n\t" \
  "ds_read_b64_tr_b16 a[194:195], v236 offset:4096\n\t" \
  "v_mfma_f32_16x16x32_bf16 a[32:35], a[220:223], v[28:31], a[32:35]\n\t" \
  "s_waitcnt vmcnt(15)\n\t" \
  "ds_read_b64_tr_b16 a[196:197], v236 offset:8192\n\t" \
  "v_mfma_f32_16x16x32_bf16 a[36:39], a[220:223], v[12:15], a[36:39]\n\t" \
  "ds_read_b64_tr_b16 a[198:199], v236 offset:12288\n\t" \
  "v_xor_b32 v237, 0x60, v234\n\t" \
  "v_mfma_f32_16x16x32_bf16 a[36:39], a[216:219], v[8:11], a[36:39]\n\t" \
  "buffer_load_dwordx4 v242, %[ws], s71 offen offset:1024 sc1 lds\n\t" \
  "v_mfma_f32_16x16x32_bf16 a[36:39], a[212:215], v[4:7], a[36:39]\n\t" \
  "ds_read_b64_tr_b16 a[200:201], v237 offset:0\n\t" \
  "v_mfma_f32_16x16x32_bf16 a[36:39], a[208:211], v[0:3], a[36:39]\n\t" \
  "s_waitcnt vmcnt(12)\n\t" \
  "ds_read_b64_tr_b16 a[202:203], v237 offset:4096\n\t" \
  "v_mfma_f32_16x16x32_bf16 a[40:43], a[208:211], v[32:35], a[40:43]\n\t" \
  "ds_read_b64_tr_b16 a[204:205], v237 offset:8192\n\t" \
  "v_mfma_f32_16x16x32_bf16 a[40:43], a[212:215], v[36:39], a[40:43]\n\t" \
  "buffer_load_dwordx4 v242, %[ws], s71 offen offset:2048 sc1 lds\n\t" \
  "v_mfma_f32_16x16x32_bf16 a[40:43], a[216:219], v[40:43], a[40:43]\n\t" \
  "ds_read_b64_tr_b16 a[206:207], v237 offset:12288\n\t" \
  "v_xor_b32 v235, 0x20000, v234\n\t" \
  "v_mfma_f32_16x16x32_bf16 a[40:43], a[220:223], v[44:47], a[40:43]\n\t" \
  "s_waitcnt lgkmcnt(4)\n\t" \
  "ds_read_b64_tr_b16 v[64:65], v235 offset:0\n\t" \
  "v_mfma_f32_16x16x32_bf16 a[48:51], a[192:195], v[16:19], a[48:51]\n\t" \
  "ds_read_b64_tr_b16 v[66:67], v235 offset:4096\n\t" \
  "v_mfma_f32_16x16x32_bf16 a[48:51], a[196:199], v[20:23], a[48:51]\n\t" \
  "s_waitcnt lgkmcnt(2)\n\t" \
  "buffer_load_dwordx4 v242, %[ws], s71 offen offset:3072 sc1 lds\n\t" \
  "v_mfma_f32_16x16x32_bf16 a[48:51], a[200:203], v[24:27], a[48:51]\n\t" \
  "ds_read_b64_tr_b16 v[68:69], v235 offset:8192\n\t" \
  "v_mfma_f32_16x16x32_bf16 a[48:51], a[204:207], v[28:31], a[48:51]\n\t" \
  "ds_read_b64_tr_b16 v[70:71], v235 offset:12288\n\t" \
  "v_xor_b32 v236, 0x0, v234\n\t" \
  "v_mfma_f32_16x16x32_bf16 a[52:55], a[204:207], v[12:15], a[52:55]\n\t" \
  "ds_read_b64_tr_b16 v[72:73], v236 offset:0\n\t" \
  "v_accvgpr_read_b32 v160, a32\n\t" \
  "v_mfma_f32_16x16x32_bf16 a[52:55], a[200:203], v[8:11], a[52:55]\n\t" \
  "buffer_load_dwordx2 v[48:49], v252, %[sd], s74 offen offset:0\n\t" \
  "v_mfma_f32_16x16x32_bf16 a[52:55], a[196:199], v[4:7], a[52:55]\n\t" \
  "ds_read_b64_tr_b16 v[74:75], v236 offset:4096\n\t" \
  "ds_read_b64_tr_b16 v[76:77], v236 offset:8192\n\t" \
  "v_mfma_f32_16x16x32_bf16 a[52:55], a[192:195], v[0:3], a[52:55]\n\t" \
  "ds_read_b64_tr_b16 v[78:79], v236 offset:12288\n\t" \
  "v_accvgpr_read_b32 v161, a33\n\t" \
  "v_mfma_f32_16x16x32_bf16 a[56:59], a[192:195], v[32:35], a[56:59]\n\t" \
  "v_xor_b32 v237, 0x20020, v234\n\t" \
  "v_accvgpr_read_b32 v162, a34\n\t" \
  "v_mfma_f32_16x16x32_bf16 a[56:59], a[196:199], v[36:39], a[56:59]\n\t" \
  "buffer_load_dwordx2 v[50:51], v252, %[sd], s74 offen offset:2048\n\t" \
  "s_mov_b32 s74, 20480\n\t" \
  "v_mfma_f32_16x16x32_bf16 a[56:59], a[200:203], v[40:43], a[56:59]\n\t" \
  "ds_read_b64_tr_b16 v[80:81], v237 offset:0\n\t" \
  "v_accvgpr_read_b32 v163, a35\n\t" \
  "v_mfma_f32_16x16x32_bf16 a[56:59], a[204:207], v[44:47], a[56:59]\n\t" \
  "s_waitcnt lgkmcnt(5)\n\t" \
  "ds_read_b64_tr_b16 v[82:83], v237 offset:4096\n\t" \
  "v_accvgpr_read_b32 v164, a48\n\t" \
  "v_mfma_f32_16x16x32_bf16 a[0:3], v[64:67], v[16:19], a[0:3]\n\t" \
  "ds_read_b64_tr_b16 v[84:85], v237 offset:8192\n\t" \
  "v_accvgpr_read_b32 v165, a49\n\t" \
  "v_mfma_f32_16x16x32_bf16 a[0:3], v[68:71], v[20:23], a[0:3]\n\t" \
  "s_waitcnt lgkmcnt(3)\n\t" \
  "buffer_load_dwordx2 v[52:53], v252, %[sd], s74 offen offset:0\n\t" \
  "v_accvgpr_read_b32 v166, a50\n\t" \
  "v_mfma_f32_16x16x32_bf16 a[0:3], v[72:75], v[24:27], a[0:3]\n\t" \
  "ds_read_b64_tr_b16 v[86:87], v237 offset:12288\n\t" \
  "v_accvgpr_read_b32 v167, a51\n\t" \
  "v_cvt_pk_bf16_f32 v168, v160, v161\n\t" \
  "v_mfma_f32_16x16x32_bf16 a[0:3], v[76:79], v[28:31], a[0:3]\n\t" \
  "v_xor_b32 v235, 0x20, v234\n\t" \
  "v_cvt_pk_bf16_f32 v169, v162, v163\n\t" \
  "v_cvt_pk_bf16_f32 v170, v164, v165\n\t" \
  "v_mfma_f32_16x16x32_bf16 a[4:7], v[76:79], v[12:15], a[4:7]\n\t" \
  "ds_read_b64_tr_b16 v[88:89], v235 offset:0\n\t" \
  "v_cvt_pk_bf16_f32 v171, v166, v167\n\t" \
  "s_xor_b32 s76, s60, 1024\n\t" \
  "v_mfma_f32_16x16x32_bf16 a[4:7], v[72:75], v[8:11], a[4:7]\n\t" \
  "buffer_store_dwordx4 v[168:171], v242, %[ws], s76 offen sc1\n\t" \
  "v_accvgpr_read_b32 v160, a36\n\t" \
  "v_mfma_f32_16x16x32_bf16 a[4:7], v[68:71], v[4:7], a[4:7]\n\t" \
  "ds_read_b64_tr_b16 v[90:91], v235 offset:4096\n\t" \
  "ds_read_b64_tr_b16 v[92:93], v235 offset:8192\n\t" \
  "v_accvgpr_read_b32 v161, a37\n\t" \
  "v_mfma_f32_16x16x32_bf16 a[4:7], v[64:67], v[0:3], a[4:7]\n\t" \
  "buffer_load_dwordx2 v[54:55], v252, %[sd], s74 offen offset:2048\n\t" \
  "s_mov_b32 s74, 24576\n\t" \
  "v_mfma_f32_16x16x32_bf16 a[8:11], v[64:67], v[32:35], a[8:11]\n\t" \
  "ds_read_b64_tr_b16 v[94:95], v235 offset:12288\n\t" \
  "v_accvgpr_read_b32 v162, a38\n\t" \
  "v_accvgpr_read_b32 v163, a39\n\t" \
  "v_mfma_f32_16x16x32_bf16 a[8:11], v[68:71], v[36:39], a[8:11]\n\t" \
  "v_accvgpr_read_b32 v164, a52\n\t" \
  "v_accvgpr_read_b32 v165, a53\n\t" \
  "v_accvgpr_read_b32 v166, a54\n\t" \
  "v_mfma_f32_16x16x32_bf16 a[8:11], v[72:75], v[40:43], a[8:11]\n\t" \
  "v_accvgpr_read_b32 v167, a55\n\t" \
  "v_cvt_pk_bf16_f32 v174, v160, v161\n\t" \
  "v_cvt_pk_bf16_f32 v175, v162, v163\n\t" \
  "v_mfma_f32_16x16x32_bf16 a[8:11], v[76:79], v[44:47], a[8:11]\n\t" \
  "s_waitcnt lgkmcnt(4)\n\t" \
  "buffer_load_dwordx2 v[56:57], v252, %[sd], s74 offen offset:0\n\t" \
  "v_cvt_pk_bf16_f32 v172, v164, v165\n\t" \
  "v_mfma_f32_16x16x32_bf16 a[16:19], v[80:83], v[16:19], a[16:19]\n\t" \
  "v_xor_b32 v236, 0x20080, v234\n\t" \
  "v_cvt_pk_bf16_f32 v173, v166, v167\n\t" \
  "s_xor_b32 s76, s61, 1024\n\t" \
  "v_mfma_f32_16x16x32_bf16 a[16:19], v[84:87], v[20:23], a[16:19]\n\t" \
  "s_waitcnt lgkmcnt(0)\n\t" \
  "buffer_store_dwordx4 v[172:175], v242, %[ws], s76 offen sc1\n\t" \
  "v_mfma_f32_16x16x32_bf16 a[16:19], v[88:91], v[24:27], a[16:19]\n\t" \
  "ds_read_b64_tr_b16 a[208:209], v236 offset:0\n\t" \
  "ds_read_b64_tr_b16 a[210:211], v236 offset:4096\n\t" \
  "v_mfma_f32_16x16x32_bf16 a[16:19], v[92:95], v[28:31], a[16:19]\n\t" \
  "s_barrier\n\t" \
  "v_mfma_f32_16x16x32_bf16 a[20:23], v[92:95], v[12:15], a[20:23]\n\t" \
  "ds_read_b64_tr_b16 a[212:213], v236 offset:8192\n\t" \
  "ds_read_b64_tr_b16 a[214:215], v236 offset:12288\n\t" \
  "v_mfma_f32_16x16x32_bf16 a[20:23], v[88:91], v[8:11], a[20:23]\n\t" \
  "buffer_load_dwordx2 v[58:59], v252, %[sd], s74 offen offset:2048\n\t" \
  "s_mov_b32 s74, 28672\n\t" \
  "v_mfma_f32_16x16x32_bf16 a[20:23], v[84:87], v[4:7], a[20:23]\n\t" \
  "v_xor_b32 v237, 0x80, v234\n\t" \
  "v_accvgpr_read_b32 v160, a0\n\t" \
  "v_mfma_f32_16x16x32_bf16 a[20:23], v[80:83], v[0:3], a[20:23]\n\t" \
  "ds_read_b64_tr_b16 a[216:217], v237 offset:0\n\t" \
  "ds_read_b64_tr_b16 a[218:219], v237 offset:4096\n\t" \
  "v_mfma_f32_16x16x32_bf16 a[24:27], v[80:83], v[32:35], a[24:27]\n\t" \
  "ds_read_b64_tr_b16 a[220:221], v237 offset:8192\n\t" \
  "ds_read_b64_tr_b16 a[222:223], v237 offset:12288\n\t" \
  "v_mfma_f32_16x16x32_bf16 a[24:27], v[84:87], v[36:39], a[24:27]\n\t" \
  "buffer_load_dwordx2 v[60:61], v252, %[sd], s74 offen offset:0\n\t" \
  "v_mfma_f32_16x16x32_bf16 a[24:27], v[88:91], v[40:43], a[24:27]\n\t" \
  "v_xor_b32 v235, 0x200a0, v234\n\t" \
  "v_accvgpr_read_b32 v161, a1\n\t" \
  "v_mfma_f32_16x16x32_bf16 a[24:27], v[92:95], v[44:47], a[24:27]\n\t" \
  "s_waitcnt lgkmcnt(4)\n\t" \
  "ds_read_b64_tr_b16 a[192:193], v235 offset:0\n\t" \
  "v_accvgpr_read_b32 v162, a2\n\t" \
  "v_accvgpr_read_b32 v163, a3\n\t" \
  "v_mfma_f32_16x16x32_bf16 a[64:67], a[208:211], v[16:19], a[64:67]\n\t" \
  "ds_read_b64_tr_b16 a[194:195], v235 offset:4096\n\t" \
  "v_accvgpr_read_b32 v164, a16\n\t" \
  "v_accvgpr_read_b32 v165, a17\n\t" \
  "v_mfma_f32_16x16x32_bf16 a[64:67], a[212:215], v[20:23], a[64:67]\n\t" \
  "s_waitcnt lgkmcnt(2)\n\t" \
  "buffer_load_dwordx2 v[62:63], v252, %[sd], s74 offen offset:2048\n\t" \
  "v_accvgpr_read_b32 v166, a18\n\t" \
  "v_mfma_f32_16x16x32_bf16 a[64:67], a[216:219], v[24:27], a[64:67]\n\t" \
  "ds_read_b64_tr_b16 a[196:197], v235 offset:8192\n\t" \
  "v_accvgpr_read_b32 v167, a19\n\t" \
  "v_cvt_pk_bf16_f32 v168, v160, v161\n\t" \
  "v_mfma_f32_16x16x32_bf16 a[64:67], a[220:223], v[28:31], a[64:67]\n\t" \
  "ds_read_b64_tr_b16 a[198:199], v235 offset:12288\n\t" \
  "v_xor_b32 v236, 0xa0, v234\n\t" \
  "v_cvt_pk_bf16_f32 v169, v162, v163\n\t" \
  "v_mfma_f32_16x16x32_bf16 a[68:71], a[220:223], v[12:15], a[68:71]\n\t" \
  "ds_read_b64_tr_b16 a[200:201], v236 offset:0\n\t" \
  "v_cvt_pk_bf16_f32 v170, v164, v165\n\t" \
  "v_cvt_pk_bf16_f32 v171, v166, v167\n\t" \
  "v_mfma_f32_16x16x32_bf16 a[68:71], a[216:219], v[8:11], a[68:71]\n\t" \
  "s_xor_b32 s76, s60, 0\n\t" \
  "ds_read_b64_tr_b16 a[202:203], v236 offset:4096\n\t" \
  "buffer_store_dwordx4 v[168:171], v242, %[ws], s76 offen sc1\n\t" \
  "v_mfma_f32_16x16x32_bf16 a[68:71], a[212:215], v[4:7], a[68:71]\n\t" \
  "ds_read_b64_tr_b16 a[204:205], v236 offset:8192\n\t" \
  "v_accvgpr_read_b32 v160, a4\n\t" \
  "v_accvgpr_read_b32 v161, a5\n\t" \
  "v_mfma_f32_16x16x32_bf16 a[68:71], a[208:211], v[0:3], a[68:71]\n\t" \
  "ds_read_b64_tr_b16 a[206:207], v236 offset:12288\n\t" \
  "v_accvgpr_read_b32 v162, a6\n\t" \
  "v_accvgpr_read_b32 v163, a7\n\t" \
  "v_mfma_f32_16x16x32_bf16 a[72:75], a[208:211], v[32:35], a[72:75]\n\t" \
  "v_accvgpr_read_b32 v164, a20\n\t" \
  "v_accvgpr_read_b32 v165, a21\n\t" \
  "v_accvgpr_read_b32 v166, a22\n\t" \
  "v_mfma_f32_16x16x32_bf16 a[72:75], a[212:215], v[36:39], a[72:75]\n\t" \
  "v_accvgpr_read_b32 v167, a23\n\t" \
  "v_cvt_pk_bf16_f32 v174, v160, v161\n\t" \
  "v_cvt_pk_bf16_f32 v175, v162, v163\n\t" \
  "v_mfma_f32_16x16x32_bf16 a[72:75], a[216:219], v[40:43], a[72:75]\n\t" \
  "v_cvt_pk_bf16_f32 v172, v164, v165\n\t" \
  "v_cvt_pk_bf16_f32 v173, v166, v167\n\t" \
  "s_xor_b32 s76, s61, 0\n\t" \
  "v_mfma_f32_16x16x32_bf16 a[72:75], a[220:223], v[44:47], a[72:75]\n\t" \
  "s_waitcnt lgkmcnt(4)\n\t" \
  "buffer_store_dwordx4 v[172:175], v242, %[ws], s76 offen sc1\n\t" \
  "v_mfma_f32_16x16x32_bf16 a[80:83], a[192:195], v[16:19], a[80:83]\n\t" \
  "v_xor_b32 v237, 0x200c0, v234\n\t" \
  "v_mfma_f32_16x16x32_bf16 a[80:83], a[196:199], v[20:23], a[80:83]\n\t" \
  "s_waitcnt lgkmcnt(0)\n\t" \
  "ds_read_b64_tr_b16 a[208:209], v237 offset:0\n\t" \
  "v_mfma_f32_16x16x32_bf16 a[80:83], a[200:203], v[24:27], a[80:83]\n\t" \
  "ds_read_b64_tr_b16 a[210:211], v237 offset:4096\n\t" \
  "v_mfma_f32_16x16x32_bf16 a[80:83], a[204:207], v[28:31], a[80:83]\n\t" \
  "ds_read_b64_tr_b16 a[212:213], v237 offset:8192\n\t" \
  "v_mfma_f32_16x16x32_bf16 a[84:87], a[204:207], v[12:15], a[84:87]\n\t" \
  "ds_read_b64_tr_b16 a[214:215], v237 offset:12288\n\t" \
  "v_xor_b32 v235, 0xc0, v234\n\t" \
  "v_mfma_f32_16x16x32_bf16 a[84:87], a[200:203], v[8:11], a[84:87]\n\t" \
  "ds_read_b64_tr_b16 a[216:217], v235 offset:0\n\t" \
  "v_accvgpr_read_b32 v160, a64\n\t" \
  "v_mfma_f32_16x16x32_bf16 a[84:87], a[196:199], v[4:7], a[84:87]\n\t" \
  "ds_read_b64_tr_b16 a[218:219], v235 offset:4096\n\t" \
  "v_accvgpr_read_b32 v161, a65\n\t" \
  "v_mfma_f32_16x16x32_bf16 a[84:87], a[192:195], v[0:3], a[84:87]\n\t" \
  "ds_read_b64_tr_b16 a[220:221], v235 offset:8192\n\t" \
  "v_accvgpr_read_b32 v162, a66\n\t" \
  "v_mfma_f32_16x16x32_bf16 a[88:91], a[192:195], v[32:35], a[88:91]\n\t" \
  "ds_read_b64_tr_b16 a[222:223], v235 offset:12288\n\t" \
  "v_accvgpr_read_b32 v163, a67\n\t" \
  "v_mfma_f32_16x16x32_bf16 a[88:91], a[196:199], v[36:39], a[88:91]\n\t" \
  "v_accvgpr_read_b32 v164, a80\n\t" \
  "v_accvgpr_read_b32 v165, a81\n\t" \
  "v_mfma_f32_16x16x32_bf16 a[88:91], a[200:203], v[40:43], a[88:91]\n\t" \
  "v_accvgpr_read_b32 v166, a82\n\t" \
  "v_accvgpr_read_b32 v167, a83\n\t" \
  "v_mfma_f32_16x16x32_bf16 a[88:91], a[204:207], v[44:47], a[88:91]\n\t" \
  "s_waitcnt lgkmcnt(4)\n\t" \
  "v_xor_b32 v236, 0x200e0, v234\n\t" \
  "v_cvt_pk_bf16_f32 v168, v160, v161\n\t" \
  "v_mfma_f32_16x16x32_bf16 a[96:99], a[208:211], v[16:19], a[96:99]\n\t" \
  "ds_read_b64_tr_b16 a[192:193], v236 offset:0\n\t" \
  "v_cvt_pk_bf16_f32 v169, v162, v163\n\t" \
  ".Lhs14:\n\ts_cbranch_execz .Lhs14n\n\t" \
  "v_mfma_f32_16x16x32_bf16 a[96:99], a[212:215], v[20:23], a[96:99]\n\t" \
  "s_waitcnt lgkmcnt(1)\n\t" \
  "ds_read_b64_tr_b16 a[194:195], v236 offset:4096\n\t" \
  "v_cvt_pk_bf16_f32 v170, v164, v165\n\t" \
  "v_mfma_f32_16x16x32_bf16 a[96:99], a[216:219], v[24:27], a[96:99]\n\t" \
  "ds_read_b64_tr_b16 a[196:197], v236 offset:8192\n\t" \
  "v_cvt_pk_bf16_f32 v171, v166, v167\n\t" \
  "s_xor_b32 s76, s60, 2048\n\t" \
  "v_mfma_f32_16x16x32_bf16 a[96:99], a[220:223], v[28:31], a[96:99]\n\t" \
  "buffer_store_dwordx4 v[168:171], v242, %[ws], s76 offen sc1\n\t" \
  "v_mfma_f32_16x16x32_bf16 a[100:103], a[220:223], v[12:15], a[100:103]\n\t" \
  "ds_read_b64_tr_b16 a[198:199], v236 offset:12288\n\t" \
  "v_accvgpr_read_b32 v160, a68\n\t" \
  "v_mfma_f32_16x16x32_bf16 a[100:103], a[216:219], v[8:11], a[100:103]\n\t" \
  "v_xor_b32 v237, 0xe0, v234\n\t" \
  "v_accvgpr_read_b32 v161, a69\n\t" \
  "v_mfma_f32_16x16x32_bf16 a[100:103], a[212:215], v[4:7], a[100:103]\n\t" \
  "ds_read_b64_tr_b16 a[200:201], v237 offset:0\n\t" \
  "v_accvgpr_read_b32 v162, a70\n\t" \
  "v_accvgpr_read_b32 v163, a71\n\t" \
  "v_mfma_f32_16x16x32_bf16 a[100:103], a[208:211], v[0:3], a[100:103]\n\t" \
  "ds_read_b64_tr_b16 a[202:203], v237 offset:4096\n\t" \
  "v_accvgpr_read_b32 v164, a84\n\t" \
  "v_accvgpr_read_b32 v165, a85\n\t" \
  "v_mfma_f32_16x16x32_bf16 a[104:107], a[208:211], v[32:35], a[104:107]\n\t" \
  "ds_read_b64_tr_b16 a[204:205], v237 offset:8192\n\t" \
  "v_accvgpr_read_b32 v166, a86\n\t" \
  "v_accvgpr_read_b32 v167, a87\n\t" \
  "v_mfma_f32_16x16x32_bf16 a[104:107], a[212:215], v[36:39], a[104:107]\n\t" \
  "ds_read_b64_tr_b16 a[206:207], v237 offset:12288\n\t" \
  "v_xor_b32 v235, 0xc000, v234\n\t" \
  "v_cvt_pk_bf16_f32 v174, v160, v161\n\t" \
  "v_mfma_f32_16x16x32_bf16 a[104:107], a[216:219], v[40:43], a[104:107]\n\t" \
  "ds_read_b64_tr_b16 a[32:33], v235 offset:0\n\t" \
  "ds_read_b64_tr_b16 a[34:35], v235 offset:4096\n\t" \
  "ds_read_b64_tr_b16 a[48:49], v235 offset:8192\n\t" \
  "v_mfma_f32_16x16x32_bf16 a[104:107], a[220:223], v[44:47], a[104:107]\n\t" \
  "s_waitcnt lgkmcnt(7)\n\t" \
  "ds_read_b64_tr_b16 a[50:51], v235 offset:12288\n\t" \
  "ds_read_b64_tr_b16 a[36:37], v235 offset:16384\n\t" \
  "ds_read_b64_tr_b16 a[38:39], v235 offset:20480\n\t" \
  "v_mfma_f32_16x16x32_bf16 a[112:115], a[192:195], v[16:19], a[112:115]\n\t" \
  "ds_read_b64_tr_b16 a[52:53], v235 offset:24576\n\t" \
  "ds_read_b64_tr_b16 a[54:55], v235 offset:28672\n\t" \
  "v_xor_b32 v236, 0xc020, v234\n\t" \
  "v_mfma_f32_16x16x32_bf16 a[112:115], a[196:199], v[20:23], a[112:115]\n\t" \
  "s_waitcnt lgkmcnt(8)\n\t" \
  "ds_read_b64_tr_b16 a[0:1], v236 offset:0\n\t" \
  "ds_read_b64_tr_b16 a[2:3], v236 offset:4096\n\t" \
  "ds_read_b64_tr_b16 a[16:17], v236 offset:8192\n\t" \
  "v_mfma_f32_16x16x32_bf16 a[112:115], a[200:203], v[24:27], a[112:115]\n\t" \
  "ds_read_b64_tr_b16 a[18:19], v236 offset:12288\n\t" \
  "ds_read_b64_tr_b16 a[4:5], v236 offset:16384\n\t" \
  "ds_read_b64_tr_b16 a[6:7], v236 offset:20480\n\t" \
  "v_mfma_f32_16x16x32_bf16 a[112:115], a[204:207], v[28:31], a[112:115]\n\t" \
  "ds_read_b64_tr_b16 a[20:21], v236 offset:24576\n\t" \
  "ds_read_b64_tr_b16 a[22:23], v236 offset:28672\n\t" \
  "v_xor_b32 v237, 0xc040, v234\n\t" \
  "v_mfma_f32_16x16x32_bf16 a[116:119], a[204:207], v[12:15], a[116:119]\n\t" \
  "ds_read_b64_tr_b16 a[64:65], v237 offset:0\n\t" \
  "ds_read_b64_tr_b16 a[66:67], v237 offset:4096\n\t" \
  "v_cvt_pk_bf16_f32 v175, v162, v163\n\t" \
  "v_mfma_f32_16x16x32_bf16 a[116:119], a[200:203], v[8:11], a[116:119]\n\t" \
  "ds_read_b64_tr_b16 a[80:81], v237 offset:8192\n\t" \
  "v_cvt_pk_bf16_f32 v172, v164, v165\n\t" \
  "v_cvt_pk_bf16_f32 v173, v166, v167\n\t" \
  "v_mfma_f32_16x16x32_bf16 a[116:119], a[196:199], v[4:7], a[116:119]\n\t" \
  "s_xor_b32 s76, s61, 2048\n\t" \
  "ds_read_b64_tr_b16 a[82:83], v237 offset:12288\n\t" \
  "ds_read_b64_tr_b16 a[68:69], v237 offset:16384\n\t" \
  "ds_read_b64_tr_b16 a[70:71], v237 offset:20480\n\t" \
  "v_mfma_f32_16x16x32_bf16 a[116:119], a[192:195], v[0:3], a[116:119]\n\t" \
  "buffer_store_dwordx4 v[172:175], v242, %[ws], s76 offen sc1\n\t" \
  "ds_read_b64_tr_b16 a[84:85], v237 offset:24576\n\t" \
  "v_mfma_f32_16x16x32_bf16 a[120:123], a[192:195], v[32:35], a[120:123]\n\t" \
  "ds_read_b64_tr_b16 a[86:87], v237 offset:28672\n\t" \
  "v_xor_b32 v235, 0xc060, v234\n\t" \
  "v_mfma_f32_16x16x32_bf16 a[120:123], a[196:199], v[36:39], a[120:123]\n\t" \
  "ds_read_b64_tr_b16 a[208:209], v235 offset:0\n\t" \
  "ds_read_b64_tr_b16 a[210:211], v235 offset:4096\n\t" \
  "ds_read_b64_tr_b16 a[212:213], v235 offset:8192\n\t" \
  "v_mfma_f32_16x16x32_bf16 a[120:123], a[200:203], v[40:43], a[120:123]\n\t" \
  "ds_read_b64_tr_b16 a[214:215], v235 offset:12288\n\t" \
  "ds_read_b64_tr_b16 a[216:217], v235 offset:16384\n\t" \
  "ds_read_b64_tr_b16 a[218:219], v235 offset:20480\n\t" \
  "v_mfma_f32_16x16x32_bf16 a[120:123], a[204:207], v[44:47], a[120:123]\n\t" \
  "s_waitcnt vmcnt(17) lgkmcnt(15)\n\t" \
  "ds_read_b64_tr_b16 a[220:221], v235 offset:24576\n\t" \
  "ds_read_b64_tr_b16 a[222:223], v235 offset:28672\n\t" \
  "v_accvgpr_read_b32 v160, a96\n\t" \
  "v_mfma_f32_16x16x32_bf16 a[8:11], a[32:35], v[208:211], a[8:11]\n\t" \
  "v_accvgpr_read_b32 v161, a97\n\t" \
  "v_accvgpr_read_b32 v162, a98\n\t" \
  "v_accvgpr_read_b32 v163, a99\n\t" \
  "v_mfma_f32_16x16x32_bf16 a[8:11], a[48:51], v[212:215], a[8:11]\n\t" \
  "v_accvgpr_read_b32 v164, a112\n\t" \
  "v_accvgpr_read_b32 v165, a113\n\t" \
  "v_accvgpr_read_b32 v166, a114\n\t" \
  "v_mfma_f32_16x16x32_bf16 a[8:11], a[36:39], v[216:219], a[8:11]\n\t" \
  "v_xor_b32 v236, 0xc080, v234\n\t" \
  "v_accvgpr_read_b32 v167, a115\n\t" \
  "v_cvt_pk_bf16_f32 v168, v160, v161\n\t" \
  "v_mfma_f32_16x16x32_bf16 a[8:11], a[52:55], v[220:223], a[8:11]\n\t" \
  "ds_read_b64_tr_b16 v[32:33], v236 offset:0\n\t" \
  "v_cvt_pk_bf16_f32 v169, v162, v163\n\t" \
  "v_cvt_pk_bf16_f32 v170, v164, v165\n\t" \
  "v_mfma_f32_16x16x32_bf16 a[24:27], a[0:3], v[208:211], a[24:27]\n\t" \
  "ds_read_b64_tr_b16 v[34:35], v236 offset:4096\n\t" \
  "v_cvt_pk_bf16_f32 v171, v166, v167\n\t" \
  "s_xor_b32 s76, s60, 3072\n\t" \
  "v_mfma_f32_16x16x32_bf16 a[24:27], a[16:19], v[212:215], a[24:27]\n\t" \
  "s_waitcnt lgkmcnt(15)\n\t" \
  "buffer_store_dwordx4 v[168:171], v242, %[ws], s76 offen sc1\n\t" \
  "v_accvgpr_read_b32 v160, a100\n\t" \
  "v_mfma_f32_16x16x32_bf16 a[24:27], a[4:7], v[216:219], a[24:27]\n\t" \
  "ds_read_b64_tr_b16 v[36:37], v236 offset:8192\n\t" \
  "ds_read_b64_tr_b16 v[38:39], v236 offset:12288\n\t" \
  "v_accvgpr_read_b32 v161, a101\n\t" \
  "v_mfma_f32_16x16x32_bf16 a[24:27], a[20:23], v[220:223], a[24:27]\n\t" \
  "s_waitcnt lgkmcnt(15)\n\t" \
  "ds_read_b64_tr_b16 v[40:41], v236 offset:16384\n\t" \
  "v_accvgpr_read_b32 v162, a102\n\t" \
  "v_accvgpr_read_b32 v163, a103\n\t" \
  "v_mfma_f32_16x16x32_bf16 a[40:43], a[64:67], v[208:211], a[40:43]\n\t" \
  "ds_read_b64_tr_b16 v[42:43], v236 offset:20480\n\t" \
  "v_accvgpr_read_b32 v164, a116\n\t" \
  "v_accvgpr_read_b32 v165, a117\n\t" \
  "v_mfma_f32_16x16x32_bf16 a[40:43], a[80:83], v[212:215], a[40:43]\n\t" \
  "s_waitcnt lgkmcnt(14)\n\t" \
  "ds_read_b64_tr_b16 v[44:45], v236 offset:24576\n\t" \
  "v_accvgpr_read_b32 v166, a118\n\t" \
  "v_accvgpr_read_b32 v167, a119\n\t" \
  "v_mfma_f32_16x16x32_bf16 a[40:43], a[68:71], v[216:219], a[40:43]\n\t" \
  "ds_read_b64_tr_b16 v[46:47], v236 offset:28672\n\t" \
  "v_xor_b32 v237, 0xc0a0, v234\n\t" \
  "v_cvt_pk_bf16_f32 v174, v160, v161\n\t" \
  "v_mfma_f32_16x16x32_bf16 a[40:43], a[84:87], v[220:223], a[40:43]\n\t" \
  "s_waitcnt lgkmcnt(12)\n\t" \
  "ds_read_b64_tr_b16 a[192:193], v237 offset:0\n\t" \
  "ds_read_b64_tr_b16 a[194:195], v237 offset:4096\n\t" \
  "ds_read_b64_tr_b16 a[196:197], v237 offset:8192\n\t" \
  "v_mfma_f32_16x16x32_bf16 a[56:59], a[208:211], v[208:211], a[56:59]\n\t" \
  "s_add_u32 m0, %[m], 0\n\t" \
  "ds_read_b64_tr_b16 a[198:199], v237 offset:12288\n\t" \
  "ds_read_b64_tr_b16 a[200:201], v237 offset:16384\n\t" \
  "ds_read_b64_tr_b16 a[202:203], v237 offset:20480\n\t" \
  "v_mfma_f32_16x16x32_bf16 a[56:59], a[212:215], v[212:215], a[56:59]\n\t" \
  "s_waitcnt lgkmcnt(14)\n\t" \
  "ds_read_b64_tr_b16 a[204:205], v237 offset:24576\n\t" \
  "ds_read_b64_tr_b16 a[206:207], v237 offset:28672\n\t" \
  "v_xor_b32 v235, 0xc0c0, v234\n\t" \
  "v_mfma_f32_16x16x32_bf16 a[56:59], a[216:219], v[216:219], a[56:59]\n\t" \
  "ds_read_b64_tr_b16 v[0:1], v235 offset:0\n\t" \
  "ds_read_b64_tr_b16 v[2:3], v235 offset:4096\n\t" \
  "ds_read_b64_tr_b16 v[4:5], v235 offset:8192\n\t" \
  "v_mfma_f32_16x16x32_bf16 a[56:59], a[220:223], v[220:223], a[56:59]\n\t" \
  "s_waitcnt lgkmcnt(15)\n\t" \
  "s_barrier\n\t" \
  "v_mfma_f32_16x16x32_bf16 a[72:75], v[32:35], v[208:211], a[72:75]\n\t" \
  "s_xor_b32 s71, %[eso], 753664\n\t" \
  "ds_read_b64_tr_b16 v[6:7], v235 offset:12288\n\t" \
  "v_cvt_pk_bf16_f32 v175, v162, v163\n\t" \
  "v_cvt_pk_bf16_f32 v172, v164, v165\n\t" \
  "v_mfma_f32_16x16x32_bf16 a[72:75], v[36:39], v[212:215], a[72:75]\n\t" \
  "s_waitcnt lgkmcnt(12)\n\t" \
  "ds_read_b64_tr_b16 v[8:9], v235 offset:16384\n\t" \
  "v_cvt_pk_bf16_f32 v173, v166, v167\n\t" \
  "s_xor_b32 s76, s61, 3072\n\t" \
  "v_mfma_f32_16x16x32_bf16 a[72:75], v[40:43], v[216:219], a[72:75]\n\t" \
  "ds_read_b64_tr_b16 v[10:11], v235 offset:20480\n\t" \
  "ds_read_b64_tr_b16 v[12:13], v235 offset:24576\n\t" \
  "ds_read_b64_tr_b16 v[14:15], v235 offset:28672\n\t" \
  "v_mfma_f32_16x16x32_bf16 a[72:75], v[44:47], v[220:223], a[72:75]\n\t" \
  "s_waitcnt lgkmcnt(12)\n\t" \
  "v_xor_b32 v236, 0xc0e0, v234\n\t" \
  "v_mfma_f32_16x16x32_bf16 a[88:91], a[192:195], v[208:211], a[88:91]\n\t" \
  "ds_read_b64_tr_b16 v[16:17], v236 offset:0\n\t" \
  "ds_read_b64_tr_b16 v[18:19], v236 offset:4096\n\t" \
  "ds_read_b64_tr_b16 v[20:21], v236 offset:8192\n\t" \
  "v_mfma_f32_16x16x32_bf16 a[88:91], a[196:199], v[212:215], a[88:91]\n\t" \
  "s_waitcnt lgkmcnt(11)\n\t" \
  "buffer_store_dwordx4 v[172:175], v242, %[ws], s76 offen sc1\n\t" \
  "v_accvgpr_read_b32 v160, a8\n\t" \
  "v_mfma_f32_16x16x32_bf16 a[88:91], a[200:203], v[216:219], a[88:91]\n\t" \
  "ds_read_b64_tr_b16 v[22:23], v236 offset:12288\n\t" \
  "v_accvgpr_read_b32 v161, a9\n\t" \
  "v_accvgpr_read_b32 v162, a10\n\t" \
  "v_mfma_f32_16x16x32_bf16 a[88:91], a[204:207], v[220:223], a[88:91]\n\t" \
  "s_waitcnt lgkmcnt(8)\n\t" \
  "ds_read_b64_tr_b16 v[24:25], v236 offset:16384\n\t" \
  "v_accvgpr_read_b32 v163, a11\n\t" \
  "v_accvgpr_read_b32 v164, a24\n\t" \
  "v_mfma_f32_16x16x32_bf16 a[104:107], v[0:3], v[208:211], a[104:107]\n\t" \
  "ds_read_b64_tr_b16 v[26:27], v236 offset:20480\n\t" \
  "v_accvgpr_read_b32 v165, a25\n\t" \
  "v_accvgpr_read_b32 v166, a26\n\t" \
  "v_mfma_f32_16x16x32_bf16 a[104:107], v[4:7], v[212:215], a[104:107]\n\t" \
  "s_waitcnt lgkmcnt(6)\n\t" \
  "ds_read_b64_tr_b16 v[28:29], v236 offset:24576\n\t" \
  "v_accvgpr_read_b32 v167, a27\n\t" \
  "v_cvt_pk_bf16_f32 v168, v160, v161\n\t" \
  "v_mfma_f32_16x16x32_bf16 a[104:107], v[8:11], v[216:219], a[104:107]\n\t" \
  "ds_read_b64_tr_b16 v[30:31], v236 offset:28672\n\t" \
  "ds_read_b128 a[208:211], v242 offset:16384\n\t" \
  "v_cvt_pk_bf16_f32 v169, v162, v163\n\t" \
  "v_mfma_f32_16x16x32_bf16 a[104:107], v[12:15], v[220:223], a[104:107]\n\t" \
  "s_waitcnt lgkmcnt(5)\n\t" \
  "ds_read_b128 a[212:215], v242 offset:17408\n\t" \
  "v_cvt_pk_bf16_f32 v170, v164, v165\n\t" \
  "v_cvt_pk_bf16_f32 v171, v166, v167\n\t" \
  "v_mfma_f32_16x16x32_bf16 a[120:123], v[16:19], v[208:211], a[120:123]\n\t" \
  "s_xor_b32 s76, s62, 0\n\t" \
  "ds_read_b128 a[216:219], v242 offset:18432\n\t" \
  "ds_read_b128 a[220:223], v242 offset:19456\n\t" \
  "v_mfma_f32_16x16x32_bf16 a[120:123], v[20:23], v[212:215], a[120:123]\n\t" \
  "s_waitcnt lgkmcnt(4)\n\t" \
  "buffer_store_dwordx4 v[168:171], v242, %[ws], s76 offen sc1\n\t" \
  "v_accvgpr_read_b32 v160, a40\n\t" \
  "v_mfma_f32_16x16x32_bf16 a[120:123], v[24:27], v[216:219], a[120:123]\n\t" \
  "ds_read_b128 v[32:35], v242 offset:20480\n\t" \
  "v_accvgpr_read_b32 v161, a41\n\t" \
  "v_accvgpr_read_b32 v162, a42\n\t" \
  "v_mfma_f32_16x16x32_bf16 a[120:123], v[28:31], v[220:223], a[120:123]\n\t" \
  "s_waitcnt lgkmcnt(1)\n\t" \
  "ds_read_b128 v[36:39], v242 offset:21504\n\t" \
  "v_accvgpr_read_b32 v163, a43\n\t" \
  "v_accvgpr_read_b32 v164, a56\n\t" \
  STP("v_writelane_b32 %[stv], s94, 34\n\t") \
  STP("v_writelane_b32 %[stv], s92, 35\n\t") \
  STP("s_memrealtime s[94:95]\n\t") \
  STP("s_memtime s[92:93]\n\t") \
  "v_mfma_f32_16x16x32_bf16 v[96:99], v[64:67], a[208:211], v[96:99]\n\t" \
  "ds_read_b128 v[40:43], v242 offset:22528\n\t" \
  "v_accvgpr_read_b32 v165, a57\n\t" \
  "v_accvgpr_read_b32 v166, a58\n\t" \
  "v_mfma_f32_16x16x32_bf16 v[96:99], v[68:71], a[212:215], v[96:99]\n\t" \
  "ds_read_b128 v[44:47], v242 offset:23552\n\t" \
  "v_accvgpr_read_b32 v167, a59\n\t" \
  "v_cvt_pk_bf16_f32 v172, v160, v161\n\t" \
  "v_mfma_f32_16x16x32_bf16 v[96:99], v[72:75], a[216:219], v[96:99]\n\t" \
  "ds_read_b128 a[192:195], v242 offset:24576\n\t" \
  "v_cvt_pk_bf16_f32 v173, v162, v163\n\t" \
  "v_cvt_pk_bf16_f32 v174, v164, v165\n\t" \
  "v_mfma_f32_16x16x32_bf16 v[96:99], v[76:79], a[220:223], v[96:99]\n\t" \
  "ds_read_b128 a[196:199], v242 offset:25600\n\t" \
  "v_cvt_pk_bf16_f32 v175, v166, v167\n\t" \
  "s_xor_b32 s76, s62, 1024\n\t" \
  "v_mfma_f32_16x16x32_bf16 v[112:115], v[92:95], a[220:223], v[112:115]\n\t" \
  "buffer_store_dwordx4 v[172:175], v242, %[ws], s76 offen sc1\n\t" \
  "v_accvgpr_read_b32 v160, a72\n\t" \
  "v_mfma_f32_16x16x32_bf16 v[112:115], v[88:91], a[216:219], v[112:115]\n\t" \
  "ds_read_b128 a[200:203], v242 offset:26624\n\t" \
  "v_accvgpr_read_b32 v161, a73\n\t" \
  "v_accvgpr_read_b32 v162, a74\n\t" \
  "v_mfma_f32_16x16x32_bf16 v[112:115], v[84:87], a[212:215], v[112:115]\n\t" \
  "ds_read_b128 a[204:207], v242 offset:27648\n\t" \
  "v_accvgpr_read_b32 v163, a75\n\t" \
  "v_accvgpr_read_b32 v164, a88\n\t" \
  "v_mfma_f32_16x16x32_bf16 v[112:115], v[80:83], a[208:211], v[112:115]\n\t" \
  "s_waitcnt lgkmcnt(4)\n\t" \
  "v_accvgpr_read_b32 v165, a89\n\t" \
  "v_accvgpr_read_b32 v166, a90\n\t" \
  "v_accvgpr_read_b32 v167, a91\n\t" \
  "v_mfma_f32_16x16x32_bf16 v[100:103], v[64:67], v[32:35], v[100:103]\n\t" \
  "buffer_load_dwordx4 v242, %[ws], s71 offen offset:0 sc1 lds\n\t" \
  "v_cvt_pk_bf16_f32 v168, v160, v161\n\t" \
  "v_mfma_f32_16x16x32_bf16 v[100:103], v[68:71], v[36:39], v[100:103]\n\t" \
  "ds_read_b128 a[208:211], v242 offset:28672\n\t" \
  "v_cvt_pk_bf16_f32 v169, v162, v163\n\t" \
  "v_cvt_pk_bf16_f32 v170, v164, v165\n\t" \
  "v_mfma_f32_16x16x32_bf16 v[100:103], v[72:75], v[40:43], v[100:103]\n\t" \
  "ds_read_b128 a[212:215], v242 offset:29696\n\t" \
  "v_cvt_pk_bf16_f32 v171, v166, v167\n\t" \
  "s_xor_b32 s76, s62, 2048\n\t" \
  "v_mfma_f32_16x16x32_bf16 v[100:103], v[76:79], v[44:47], v[100:103]\n\t" \
  "buffer_store_dwordx4 v[168:171], v242, %[ws], s76 offen sc1\n\t" \
  "v_accvgpr_read_b32 v160, a104\n\t" \
  "v_mfma_f32_16x16x32_bf16 v[116:119], v[92:95], v[44:47], v[116:119]\n\t" \
  "s_mov_b32 s74, 0\n\t" \
  "ds_read_b128 a[216:219], v242 offset:30720\n\t" \
  "v_accvgpr_read_b32 v161, a105\n\t" \
  "v_accvgpr_read_b32 v162, a106\n\t" \
  "v_mfma_f32_16x16x32_bf16 v[116:119], v[88:91], v[40:43], v[116:119]\n\t" \
  "buffer_load_dwordx4 v242, %[ws], s71 offen offset:1024 sc1 lds\n\t" \
  "v_accvgpr_read_b32 v163, a107\n\t" \
  "v_mfma_f32_16x16x32_bf16 v[116:119], v[84:87], v[36:39], v[116:119]\n\t" \
  "ds_read_b128 a[220:223], v242 offset:31744\n\t" \
  "v_accvgpr_read_b32 v164, a120\n\t" \
  "v_accvgpr_read_b32 v165, a121\n\t" \
  "v_mfma_f32_16x16x32_bf16 v[116:119], v[80:83], v[32:35], v[116:119]\n\t" \
  "s_waitcnt lgkmcnt(4)\n\t" \
  "s_barrier\n\t" \
  "v_mfma_f32_16x16x32_bf16 v[104:107], v[64:67], a[192:195], v[104:107]\n\t" \
  "ds_read_b128 v[208:211], v242 offset:32768\n\t" \
  "v_accvgpr_read_b32 v166, a122\n\t" \
  "v_accvgpr_read_b32 v167, a123\n\t" \
  "v_mfma_f32_16x16x32_bf16 v[104:107], v[68:71], a[196:199], v[104:107]\n\t" \
  "ds_read_b128 v[212:215], v242 offset:33792\n\t" \
  "v_cvt_pk_bf16_f32 v172, v160, v161\n\t" \
  "v_cvt_pk_bf16_f32 v173, v162, v163\n\t" \
  "v_mfma_f32_16x16x32_bf16 v[104:107], v[72:75], a[200:203], v[104:107]\n\t" \
  "ds_read_b128 v[216:219], v242 offset:34816\n\t" \
  "v_cvt_pk_bf16_f32 v174, v164, v165\n\t" \
  "v_cvt_pk_bf16_f32 v175, v166, v167\n\t" \
  "v_mfma_f32_16x16x32_bf16 v[104:107], v[76:79], a[204:207], v[104:107]\n\t" \
  "s_xor_b32 s76, s62, 3072\n\t" \
  "ds_read_b128 v[220:223], v242 offset:35840\n\t" \
  "buffer_store_dwordx4 v[172:175], v242, %[ws], s76 offen sc1\n\t" \
  "v_mfma_f32_16x16x32_bf16 v[120:123], v[92:95], a[204:207], v[120:123]\n\t" \
  "v_mfma_f32_16x16x32_bf16 v[120:123], v[88:91], a[200:203], v[120:123]\n\t" \
  "buffer_load_dwordx4 v242, %[ws], s71 offen offset:2048 sc1 lds\n\t" \
  "v_mfma_f32_16x16x32_bf16 v[120:123], v[84:87], a[196:199], v[120:123]\n\t" \
  "v_mfma_f32_16x16x32_bf16 v[120:123], v[80:83], a[192:195], v[120:123]\n\t" \
  "s_waitcnt lgkmcnt(4)\n\t" \
  "buffer_load_dwordx4 v242, %[ws], s71 offen offset:3072 sc1 lds\n\t" \
  "s_add_u32 m0, %[m], 131072\n\t" \
  "v_mfma_f32_16x16x32_bf16 v[108:111], v[64:67], a[208:211], v[108:111]\n\t" \
  "s_add_u32 s71, %[xso], 0\n\t" \
  "ds_read_b128 a[192:195], v242 offset:36864\n\t" \
  "v_mfma_f32_16x16x32_bf16 v[108:111], v[68:71], a[212:215], v[108:111]\n\t" \
  "ds_read_b128 a[196:199], v242 offset:37888\n\t" \
  "v_mfma_f32_16x16x32_bf16 v[108:111], v[72:75], a[216:219], v[108:111]\n\t" \
  "ds_read_b128 a[200:203], v242 offset:38912\n\t" \
  "v_mfma_f32_16x16x32_bf16 v[108:111], v[76:79], a[220:223], v[108:111]\n\t" \
  "buffer_load_dwordx4 v248, %[sx], s71 offen offset:0 lds\n\t" \
  "s_add_u32 s71, %[xso], 7168\n\t" \
  "v_mfma_f32_16x16x32_bf16 v[124:127], v[92:95], a[220:223], v[124:127]\n\t" \
  "ds_read_b128 a[204:207], v242 offset:39936\n\t" \
  "v_mfma_f32_16x16x32_bf16 v[124:127], v[88:91], a[216:219], v[124:127]\n\t" \
  "v_mfma_f32_16x16x32_bf16 v[124:127], v[84:87], a[212:215], v[124:127]\n\t" \
  "v_xor_b32 v237, 0x14000, v234\n\t" \
  "v_mfma_f32_16x16x32_bf16 v[124:127], v[80:83], a[208:211], v[124:127]\n\t" \
  "s_waitcnt lgkmcnt(4)\n\t" \
  "buffer_load_dwordx4 v249, %[sx], s71 offen offset:1024 lds\n\t" \
  "s_add_u32 s71, %[xso], 14336\n\t" \
  "v_mfma_f32_16x16x32_bf16 v[96:99], a[160:163], v[208:211], v[96:99]\n\t" \
  "ds_read_b64_tr_b16 a[96:97], v237 offset:0\n\t" \
  "v_mfma_f32_16x16x32_bf16 v[96:99], a[164:167], v[212:215], v[96:99]\n\t" \
  "ds_read_b64_tr_b16 a[98:99], v237 offset:4096\n\t" \
  "v_mfma_f32_16x16x32_bf16 v[96:99], a[168:171], v[216:219], v[96:99]\n\t" \
  "s_waitcnt vmcnt(10)\n\t" \
  "buffer_store_dword v243, v225, %[ws], 0 offen sc1\n\t" \
  "v_mfma_f32_16x16x32_bf16 v[96:99], a[172:175], v[220:223], v[96:99]\n\t" \
  "ds_read_b64_tr_b16 a[112:113], v237 offset:8192\n\t" \
  "v_mfma_f32_16x16x32_bf16 v[112:115], a[188:191], v[220:223], v[112:115]\n\t" \
  "buffer_load_dwordx4 v250, %[sx], s71 offen offset:2048 lds\n\t" \
  "s_add_u32 s71, %[xso], 21504\n\t" \
  "v_mfma_f32_16x16x32_bf16 v[112:115], a[184:187], v[216:219], v[112:115]\n\t" \
  "ds_read_b64_tr_b16 a[114:115], v237 offset:12288\n\t" \
  "v_mfma_f32_16x16x32_bf16 v[112:115], a[180:183], v[212:215], v[112:115]\n\t" \
  "ds_read_b64_tr_b16 a[100:101], v237 offset:32768\n\t" \
  "v_mfma_f32_16x16x32_bf16 v[112:115], a[176:179], v[208:211], v[112:115]\n\t" \
  "s_waitcnt lgkmcnt(5)\n\t" \
  "ds_read_b64_tr_b16 a[102:103], v237 offset:36864\n\t" \
  "v_mfma_f32_16x16x32_bf16 v[100:103], a[160:163], a[192:195], v[100:103]\n\t" \
  "ds_read_b64_tr_b16 a[116:117], v237 offset:40960\n\t" \
  "v_mfma_f32_16x16x32_bf16 v[100:103], a[164:167], a[196:199], v[100:103]\n\t" \
  "buffer_load_dwordx4 v251, %[sx], s71 offen offset:3072 lds\n\t" \
  "s_add_u32 m0, %[m], 49152\n\t" \
  "v_mfma_f32_16x16x32_bf16 v[100:103], a[168:171], a[200:203], v[100:103]\n\t" \
  "s_xor_b32 s71, %[eso], 622592\n\t" \
  "ds_read_b64_tr_b16 a[118:119], v237 offset:45056\n\t" \
  "ds_read_b128 a[208:211], v242 offset:40960\n\t" \
  "ds_read_b128 a[212:215], v242 offset:41984\n\t" \
  "v_mfma_f32_16x16x32_bf16 v[100:103], a[172:175], a[204:207], v[100:103]\n\t" \
  "ds_read_b128 a[216:219], v242 offset:43008\n\t" \
  "ds_read_b128 a[220:223], v242 offset:44032\n\t" \
  "v_mfma_f32_16x16x32_bf16 v[116:119], a[188:191], a[204:207], v[116:119]\n\t" \
  "ds_read_b128 v[208:211], v242 offset:45056\n\t" \
  "v_mfma_f32_16x16x32_bf16 v[116:119], a[184:187], a[200:203], v[116:119]\n\t" \
  "buffer_load_dwordx4 v242, %[ws], s71 offen offset:0 sc1 lds\n\t" \
  "v_mfma_f32_16x16x32_bf16 v[116:119], a[180:183], a[196:199], v[116:119]\n\t" \
  "ds_read_b128 v[212:215], v242 offset:46080\n\t" \
  "ds_read_b128 v[216:219], v242 offset:47104\n\t" \
  "v_mfma_f32_16x16x32_bf16 v[116:119], a[176:179], a[192:195], v[116:119]\n\t" \
  "s_waitcnt lgkmcnt(3)\n\t" \
  "s_barrier\n\t" \
  "v_mfma_f32_16x16x32_bf16 v[104:107], a[160:163], a[208:211], v[104:107]\n\t" \
  "ds_read_b128 v[220:223], v242 offset:48128\n\t" \
  "v_xor_b32 v235, 0x14020, v234\n\t" \
  "v_mfma_f32_16x16x32_bf16 v[104:107], a[164:167], a[212:215], v[104:107]\n\t" \
  "buffer_load_dwordx4 v242, %[ws], s71 offen offset:1024 sc1 lds\n\t" \
  "ds_read_b64_tr_b16 a[64:65], v235 offset:0\n\t" \
  "v_mfma_f32_16x16x32_bf16 v[104:107], a[168:171], a[216:219], v[104:107]\n\t" \
  "ds_read_b64_tr_b16 a[66:67], v235 offset:4096\n\t" \
  "ds_read_b64_tr_b16 a[68:69], v235 offset:8192\n\t" \
  "ds_read_b64_tr_b16 a[70:71], v235 offset:12288\n\t" \
  "v_mfma_f32_16x16x32_bf16 v[104:107], a[172:175], a[220:223], v[104:107]\n\t" \
  "ds_read_b64_tr_b16 a[80:81], v235 offset:32768\n\t" \
  "ds_read_b64_tr_b16 a[82:83], v235 offset:36864\n\t" \
  "v_mfma_f32_16x16x32_bf16 v[120:123], a[188:191], a[220:223], v[120:123]\n\t" \
  "ds_read_b64_tr_b16 a[84:85], v235 offset:40960\n\t" \
  "ds_read_b64_tr_b16 a[86:87], v235 offset:45056\n\t" \
  "v_mfma_f32_16x16x32_bf16 v[120:123], a[184:187], a[216:219], v[120:123]\n\t" \
  "ds_read_b128 a[192:195], v230 offset:32768\n\t" \
  "ds_read_b128 a[196:199], v230 offset:33792\n\t" \
  "v_and_b32 v235, 16, v242\n\t" \
  "v_mfma_f32_16x16x32_bf16 v[120:123], a[180:183], a[212:215], v[120:123]\n\t" \
  "buffer_load_dwordx4 v242, %[ws], s71 offen offset:2048 sc1 lds\n\t" \
  "v_accvgpr_read_b32 v224, a224\n\t" \
  "v_mfma_f32_16x16x32_bf16 v[120:123], a[176:179], a[208:211], v[120:123]\n\t" \
  "s_waitcnt lgkmcnt(10)\n\t" \
  "ds_read_b128 a[200:203], v230 offset:34816\n\t" \
  "ds_read_b128 a[204:207], v230 offset:35840\n\t" \
  "v_mov_b32 v229, 0x3f80\n\t" \
  "v_mfma_f32_16x16x32_bf16 v[108:111], a[160:163], v[208:211], v[108:111]\n\t" \
  "ds_read_b128 v[0:3], v230 offset:36864\n\t" \
  "v_accvgpr_read_b32 v227, a228\n\t" \
  "v_lshlrev_b32 v229, v235, v229\n\t" \
  "v_mfma_f32_16x16x32_bf16 v[108:111], a[164:167], v[212:215], v[108:111]\n\t" \
  "s_mov_b32 s82, 0x300003\n\t" \
  "ds_read_b128 v[4:7], v230 offset:37888\n\t" \
  "v_perm_b32 v64, v227, v224, s79\n\t" \
  "v_mfma_f32_16x16x32_bf16 v[108:111], a[168:171], v[216:219], v[108:111]\n\t" \
  "buffer_load_dwordx4 v242, %[ws], s71 offen offset:3072 sc1 lds\n\t" \
  "s_add_u32 m0, %[m], 65536\n\t" \
  "v_mfma_f32_16x16x32_bf16 v[108:111], a[172:175], v[220:223], v[108:111]\n\t" \
  "s_xor_b32 s71, %[eso], 884736\n\t" \
  "s_mov_b32 s83, 0x30000300\n\t" \
  "ds_read_b128 v[8:11], v230 offset:38912\n\t" \
  "v_mfma_f32_16x16x32_bf16 v[124:127], a[188:191], v[220:223], v[124:127]\n\t" \
  "s_mov_b32 s84, 0xc0000c\n\t" \
  "ds_read_b128 v[12:15], v230 offset:39936\n\t" \
  "v_perm_b32 v68, v227, v224, s80\n\t" \
  "v_mfma_f32_16x16x32_bf16 v[124:127], a[184:187], v[216:219], v[124:127]\n\t" \
  "s_mov_b32 s85, 0xc0000c00\n\t" \
  "v_accvgpr_read_b32 v224, a225\n\t" \
  "v_cndmask_b32 v238, 0, v229, s[82:83]\n\t" \
  "v_mfma_f32_16x16x32_bf16 v[124:127], a[180:183], v[212:215], v[124:127]\n\t" \
  "v_cndmask_b32 v239, 0, v229, s[84:85]\n\t" \
  "v_accvgpr_read_b32 v227, a229\n\t" \
  "v_mfma_f32_16x16x32_bf16 v[124:127], a[176:179], v[208:211], v[124:127]\n\t" \
  "s_waitcnt lgkmcnt(4)\n\t" \
  "buffer_load_dwordx4 v242, %[ws], s71 offen offset:0 sc1 lds\n\t" \
  "v_mfma_f32_16x16x32_bf16 v[96:99], a[128:131], a[192:195], v[96:99]\n\t" \
  "ds_read_b128 v[208:211], v230 offset:40960\n\t" \
  "v_and_b32 v236, 15, v244\n\t" \
  "v_mfma_f32_16x16x32_bf16 v[96:99], a[132:135], a[196:199], v[96:99]\n\t" \
  "ds_read_b128 v[212:215], v230 offset:41984\n\t" \
  "v_lshrrev_b32 v229, 4, v244\n\t" \
  "v_mfma_f32_16x16x32_bf16 v[96:99], a[136:139], a[200:203], v[96:99]\n\t" \
  "ds_read_b128 v[216:219], v230 offset:43008\n\t" \
  "v_perm_b32 v65, v227, v224, s79\n\t" \
  "v_mfma_f32_16x16x32_bf16 v[96:99], a[140:143], a[204:207], v[96:99]\n\t" \
  "s_waitcnt vmcnt(12)\n\t" \
  "buffer_store_dword v243, v226, %[ws], 0 offen sc1\n\t" \
  "v_mfma_f32_16x16x32_bf16 v[112:115], a[156:159], a[204:207], v[112:115]\n\t" \
  "ds_read_b128 v[220:223], v230 offset:44032\n\t" \
  "v_lshlrev_b32 v235, 2, v236\n\t" \
  "v_mfma_f32_16x16x32_bf16 v[112:115], a[152:155], a[200:203], v[112:115]\n\t" \
  "buffer_load_dwordx4 v242, %[ws], s71 offen offset:1024 sc1 lds\n\t" \
  "v_mfma_f32_16x16x32_bf16 v[112:115], a[148:151], a[196:199], v[112:115]\n\t" \
  "v_perm_b32 v69, v227, v224, s80\n\t" \
  "v_mul_u32_u24 v236, 80, v236\n\t" \
  "v_mfma_f32_16x16x32_bf16 v[112:115], a[144:147], a[192:195], v[112:115]\n\t" \
  "s_waitcnt lgkmcnt(4)\n\t" \
  "v_lshlrev_b32 v237, 4, v229\n\t" \
  "v_accvgpr_read_b32 v224, a226\n\t" \
  "v_mfma_f32_16x16x32_bf16 v[100:103], a[128:131], v[0:3], v[100:103]\n\t" \
  "buffer_load_dwordx4 v242, %[ws], s71 offen offset:2048 sc1 lds\n\t" \
  "v_mfma_f32_16x16x32_bf16 v[100:103], a[132:135], v[4:7], v[100:103]\n\t" \
  "ds_read_b128 a[192:195], v230 offset:45056\n\t" \
  "v_add_u32 v237, s70, v237\n\t" \
  "v_mfma_f32_16x16x32_bf16 v[100:103], a[136:139], v[8:11], v[100:103]\n\t" \
  "ds_read_b128 a[196:199], v230 offset:46080\n\t" \
  "v_add_u32 v236, v237, v236\n\t" \
  "v_mfma_f32_16x16x32_bf16 v[100:103], a[140:143], v[12:15], v[100:103]\n\t" \
  "ds_read_b128 a[200:203], v230 offset:47104\n\t" \
  "v_accvgpr_read_b32 v227, a230\n\t" \
  "v_mfma_f32_16x16x32_bf16 v[116:119], a[156:159], v[12:15], v[116:119]\n\t" \
  "buffer_load_dwordx4 v242, %[ws], s71 offen offset:3072 sc1 lds\n\t" \
  "s_add_u32 m0, %[m], 16384\n\t" \
  "v_mfma_f32_16x16x32_bf16 v[116:119], a[152:155], v[8:11], v[116:119]\n\t" \
  "s_xor_b32 s71, %[eso], 229376\n\t" \
  "ds_read_b128 a[204:207], v230 offset:48128\n\t" \
  "ds_read_b128 a[208:211], v236 offset:960\n\t" \
  "v_mfma_f32_16x16x32_bf16 v[116:119], a[148:151], v[4:7], v[116:119]\n\t" \
  "ds_read_b128 a[212:215], v236 offset:640\n\t" \
  "v_perm_b32 v66, v227, v224, s79\n\t" \
  "v_mfma_f32_16x16x32_bf16 v[116:119], a[144:147], v[0:3], v[116:119]\n\t" \
  "s_waitcnt vmcnt(14) lgkmcnt(6)\n\t" \
  "s_barrier\n\t" \
  "v_mfma_f32_16x16x32_bf16 v[104:107], a[128:131], v[208:211], v[104:107]\n\t" \
  "buffer_load_dwordx2 v[32:33], v252, %[sd], s74 offen offset:0\n\t" \
  "v_mfma_f32_16x16x32_bf16 v[104:107], a[132:135], v[212:215], v[104:107]\n\t" \
  "ds_read_b128 a[160:163], v242 offset:0\n\t" \
  "v_perm_b32 v70, v227, v224, s80\n\t" \
  "v_mfma_f32_16x16x32_bf16 v[104:107], a[136:139], v[216:219], v[104:107]\n\t" \
  "ds_read_b128 a[164:167], v242 offset:1024\n\t" \
  "v_accvgpr_read_b32 v224, a227\n\t" \
  "v_mfma_f32_16x16x32_bf16 v[104:107], a[140:143], v[220:223], v[104:107]\n\t" \
  "ds_read_b128 a[168:171], v242 offset:2048\n\t" \
  "v_accvgpr_read_b32 v227, a231\n\t" \
  "v_mfma_f32_16x16x32_bf16 v[120:123], a[156:159], v[220:223], v[120:123]\n\t" \
  "buffer_load_dwordx2 v[34:35], v252, %[sd], s74 offen offset:2048\n\t" \
  "s_mov_b32 s74, 4096\n\t" \
  "v_mfma_f32_16x16x32_bf16 v[120:123], a[152:155], v[216:219], v[120:123]\n\t" \
  "ds_read_b128 a[172:175], v242 offset:3072\n\t" \
  "v_perm_b32 v67, v227, v224, s79\n\t" \
  "v_mfma_f32_16x16x32_bf16 v[120:123], a[148:151], v[212:215], v[120:123]\n\t" \
  "ds_read_b128 a[216:219], v236 offset:320\n\t" \
  "v_perm_b32 v71, v227, v224, s80\n\t" \
  "v_mfma_f32_16x16x32_bf16 v[120:123], a[144:147], v[208:211], v[120:123]\n\t" \
  "s_waitcnt lgkmcnt(7)\n\t" \
  "v_accvgpr_read_b32 v224, a232\n\t" \
  "v_accvgpr_read_b32 v227, a236\n\t" \
  "v_mfma_f32_16x16x32_bf16 v[108:111], a[128:131], a[192:195], v[108:111]\n\t" \
  "buffer_load_dwordx2 v[36:37], v252, %[sd], s74 offen offset:0\n\t" \
  "v_mfma_f32_16x16x32_bf16 v[108:111], a[132:135], a[196:199], v[108:111]\n\t" \
  "ds_read_b128 v[208:211], v242 offset:4096\n\t" \
  "v_perm_b32 v72, v227, v224, s79\n\t" \
  "v_mfma_f32_16x16x32_bf16 v[108:111], a[136:139], a[200:203], v[108:111]\n\t" \
  "ds_read_b128 v[212:215], v242 offset:5120\n\t" \
  "v_perm_b32 v76, v227, v224, s80\n\t" \
  "v_mfma_f32_16x16x32_bf16 v[108:111], a[140:143], a[204:207], v[108:111]\n\t" \
  "ds_read_b128 v[216:219], v242 offset:6144\n\t" \
  "v_accvgpr_read_b32 v224, a233\n\t" \
  "v_mfma_f32_16x16x32_bf16 v[124:127], a[156:159], a[204:207], v[124:127]\n\t" \
  "buffer_load_dwordx2 v[38:39], v252, %[sd], s74 offen offset:2048\n\t" \
  "s_mov_b32 s74, 8192\n\t" \
  "v_mfma_f32_16x16x32_bf16 v[124:127], a[152:155], a[200:203], v[124:127]\n\t" \
  "ds_read_b128 v[220:223], v242 offset:7168\n\t" \
  "ds_read_b128 a[220:223], v236 offset:0\n\t" \
  "buffer_load_dword v246, v235, %[ws], s68 offen sc1\n\t" \
  "v_mfma_f32_16x16x32_bf16 v[124:127], a[148:151], a[196:199], v[124:127]\n\t" \
  "v_accvgpr_read_b32 v227, a237\n\t" \
  "v_mfma_f32_16x16x32_bf16 v[124:127], a[144:147], a[192:195], v[124:127]\n\t" \
  "s_waitcnt lgkmcnt(6)\n\t" \
  "v_perm_b32 v73, v227, v224, s79\n\t" \
  "v_mfma_f32_16x16x32_bf16 v[96:99], v[128:131], a[160:163], v[96:99]\n\t" \
  "ds_read_b128 a[128:131], v242 offset:8192\n\t" \
  "v_mfma_f32_16x16x32_bf16 v[96:99], v[132:135], a[164:167], v[96:99]\n\t" \
  "buffer_load_dwordx2 v[40:41], v252, %[sd], s74 offen offset:0\n\t" \
  "v_mfma_f32_16x16x32_bf16 v[96:99], v[136:139], a[168:171], v[96:99]\n\t" \
  "ds_read_b128 a[132:135], v242 offset:9216\n\t" \
  "v_perm_b32 v77, v227, v224, s80\n\t" \
  "v_mfma_f32_16x16x32_bf16 v[96:99], v[140:143], a[172:175], v[96:99]\n\t" \
  "ds_read_b128 a[136:139], v242 offset:10240\n\t" \
  "v_mfma_f32_16x16x32_bf16 v[112:115], v[156:159], a[172:175], v[112:115]\n\t" \
  ".Lhs15:\n\ts_cbranch_execz .Lhs15n\n\t" \
  "ds_read_b128 a[140:143], v242 offset:11264\n\t" \
  "v_accvgpr_read_b32 v224, a234\n\t" \
  "v_mfma_f32_16x16x32_bf16 v[112:115], v[152:155], a[168:171], v[112:115]\n\t" \
  "buffer_load_dwordx2 v[42:43], v252, %[sd], s74 offen offset:2048\n\t" \
  "s_mov_b32 s74, 12288\n\t" \
  "v_mfma_f32_16x16x32_bf16 v[112:115], v[148:151], a[164:167], v[112:115]\n\t" \
  "v_mfma_f32_16x16x32_bf16 v[112:115], v[144:147], a[160:163], v[112:115]\n\t" \
  "s_waitcnt lgkmcnt(5)\n\t" \
  "v_accvgpr_read_b32 v227, a238\n\t" \
  "v_mfma_f32_16x16x32_bf16 v[100:103], v[128:131], v[208:211], v[100:103]\n\t" \
  "ds_read_b128 a[144:147], v242 offset:12288\n\t" \
  "v_accvgpr_write_b32 a192, 0\n\t" \
  "v_mfma_f32_16x16x32_bf16 v[100:103], v[132:135], v[212:215], v[100:103]\n\t" \
  "buffer_load_dwordx2 v[44:45], v252, %[sd], s74 offen offset:0\n\t" \
  "v_mfma_f32_16x16x32_bf16 v[100:103], v[136:139], v[216:219], v[100:103]\n\t" \
  "ds_read_b128 a[148:151], v242 offset:13312\n\t" \
  "v_accvgpr_write_b32 a193, 0\n\t" \
  "v_mfma_f32_16x16x32_bf16 v[100:103], v[140:143], v[220:223], v[100:103]\n\t" \
  "ds_read_b128 a[152:155], v242 offset:14336\n\t" \
  "v_accvgpr_write_b32 a194, v238\n\t" \
  "v_mfma_f32_16x16x32_bf16 v[116:119], v[156:159], v[220:223], v[116:119]\n\t" \
  "ds_read_b128 a[156:159], v242 offset:15360\n\t" \
  "v_accvgpr_write_b32 a195, v239\n\t" \
  "v_mfma_f32_16x16x32_bf16 v[116:119], v[152:155], v[216:219], v[116:119]\n\t" \
  "buffer_load_dwordx2 v[46:47], v252, %[sd], s74 offen offset:2048\n\t" \
  "v_mfma_f32_16x16x32_bf16 v[116:119], v[148:151], v[212:215], v[116:119]\n\t" \
  "v_accvgpr_write_b32 a196, v238\n\t" \
  "v_accvgpr_write_b32 a197, v239\n\t" \
  "v_mfma_f32_16x16x32_bf16 v[116:119], v[144:147], v[208:211], v[116:119]\n\t" \
  "s_waitcnt vmcnt(14) lgkmcnt(4)\n\t" \
  "s_barrier\n\t" \
  "v_mfma_f32_16x16x32_bf16 v[104:107], v[128:131], a[128:131], v[104:107]\n\t" \
  "ds_read_b128 v[16:19], v242 offset:49152\n\t" \
  "v_accvgpr_write_b32 a198, 0\n\t" \
  "v_mfma_f32_16x16x32_bf16 v[104:107], v[132:135], a[132:135], v[104:107]\n\t" \
  "ds_read_b128 v[20:23], v242 offset:50176\n\t" \
  "v_accvgpr_write_b32 a199, 0\n\t" \
  "v_mfma_f32_16x16x32_bf16 v[104:107], v[136:139], a[136:139], v[104:107]\n\t" \
  "buffer_load_dwordx4 v242, %[ws], s71 offen offset:0 sc1 lds\n\t" \
  "v_mfma_f32_16x16x32_bf16 v[104:107], v[140:143], a[140:143], v[104:107]\n\t" \
  "ds_read_b128 v[24:27], v242 offset:51200\n\t" \
  "ds_read_b64 v[208:209], v231 offset:0\n\t" \
  "v_mfma_f32_16x16x32_bf16 v[120:123], v[156:159], a[140:143], v[120:123]\n\t" \
  "ds_read_b128 v[28:31], v242 offset:52224\n\t" \
  "v_perm_b32 v74, v227, v224, s79\n\t" \
  "v_mfma_f32_16x16x32_bf16 v[120:123], v[152:155], a[136:139], v[120:123]\n\t" \
  "ds_read_b64 v[210:211], v231 offset:4096\n\t" \
  "v_perm_b32 v78, v227, v224, s80\n\t" \
  "v_mfma_f32_16x16x32_bf16 v[120:123], v[148:151], a[132:135], v[120:123]\n\t" \
  "buffer_load_dwordx4 v242, %[ws], s71 offen offset:1024 sc1 lds\n\t" \
  "v_mfma_f32_16x16x32_bf16 v[120:123], v[144:147], a[128:131], v[120:123]\n\t" \
  "s_waitcnt lgkmcnt(6)\n\t" \
  "ds_read_b64 v[212:213], v231 offset:8192\n\t" \
  "v_accvgpr_read_b32 v224, a235\n\t" \
  "v_mfma_f32_16x16x32_bf16 v[108:111], v[128:131], a[144:147], v[108:111]\n\t" \
  "ds_read_b128 a[8:11], v242 offset:53248\n\t" \
  "ds_read_b64 v[214:215], v231 offset:12288\n\t" \
  "v_mfma_f32_16x16x32_bf16 v[108:111], v[132:135], a[148:151], v[108:111]\n\t" \
  "ds_read_b128 a[24:27], v242 offset:54272\n\t" \
  "ds_read_b64 v[216:217], v233 offset:0\n\t" \
  "v_mfma_f32_16x16x32_bf16 v[108:111], v[136:139], a[152:155], v[108:111]\n\t" \
  "ds_read_b128 a[40:43], v242 offset:55296\n\t" \
  "v_accvgpr_read_b32 v227, a239\n\t" \
  "v_mfma_f32_16x16x32_bf16 v[108:111], v[140:143], a[156:159], v[108:111]\n\t" \
  "buffer_load_dwordx4 v242, %[ws], s71 offen offset:2048 sc1 lds\n\t" \
  "v_mfma_f32_16x16x32_bf16 v[124:127], v[156:159], a[156:159], v[124:127]\n\t" \
  "ds_read_b64 v[218:219], v233 offset:4096\n\t" \
  "ds_read_b64 v[220:221], v233 offset:8192\n\t" \
  "v_mfma_f32_16x16x32_bf16 v[124:127], v[152:155], a[152:155], v[124:127]\n\t" \
  "ds_read_b128 a[56:59], v242 offset:56320\n\t" \
  "v_perm_b32 v75, v227, v224, s79\n\t" \
  "ds_read_b64 v[222:223], v233 offset:12288\n\t" \
  "v_mfma_f32_16x16x32_bf16 v[124:127], v[148:151], a[148:151], v[124:127]\n\t" \
  "v_perm_b32 v79, v227, v224, s80\n\t" \
  "v_accvgpr_read_b32 v224, a240\n\t" \
  "v_mfma_f32_16x16x32_bf16 v[124:127], v[144:147], a[144:147], v[124:127]\n\t" \
  "s_waitcnt lgkmcnt(11)\n\t" \
  "buffer_load_dwordx4 v242, %[ws], s71 offen offset:3072 sc1 lds\n\t" \
  "v_mfma_f32_16x16x32_bf16 v[96:99], a[12:15], v[16:19], v[96:99]\n\t" \
  "ds_read_b128 v[128:131], v242 offset:57344\n\t" \
  "v_accvgpr_read_b32 v227, a244\n\t" \
  "v_mfma_f32_16x16x32_bf16 v[96:99], a[28:31], v[20:23], v[96:99]\n\t" \
  "buffer_load_dword v228, v235, %[ws], s68 offen sc1\n\t" \
  "v_mfma_f32_16x16x32_bf16 v[96:99], a[44:47], v[24:27], v[96:99]\n\t" \
  "ds_read_b128 v[132:135], v242 offset:58368\n\t" \
  "v_perm_b32 v80, v227, v224, s79\n\t" \
  "v_mfma_f32_16x16x32_bf16 v[96:99], a[60:63], v[28:31], v[96:99]\n\t" \
  "ds_read_b128 a[72:75], v242 offset:59392\n\t" \
  "v_perm_b32 v84, v227, v224, s80\n\t" \
  "s_waitcnt vmcnt(9)\n\t" \
  "v_cmp_ne_u32 vcc, %[tag], v246\n\t" \
  "s_and_b32 vcc_lo, vcc_lo, 0xff\n\t" \
  "s_or_b32 s81, s81, vcc_lo\n\t" \
  "v_mfma_f32_16x16x32_bf16 v[112:115], a[124:127], v[28:31], v[112:115]\n\t" \
  "ds_read_b128 a[88:91], v242 offset:60416\n\t" \
  "v_accvgpr_read_b32 v224, a241\n\t" \
  "v_mfma_f32_16x16x32_bf16 v[112:115], a[108:111], v[24:27], v[112:115]\n\t" \
  "s_and_b32 s72, s81, 15\n\t" \
  "s_cselect_b32 s72, -1, 0\n\t" \
  "v_lshl_or_b32 v247, s72, 31, v242\n\t" \
  "s_add_u32 s72, s63, 0\n\t" \
  "v_mfma_f32_16x16x32_bf16 v[112:115], a[92:95], v[20:23], v[112:115]\n\t" \
  "buffer_load_dwordx4 a[128:131], v247, %[ws], s72 offen sc1\n\t" \
  "v_mfma_f32_16x16x32_bf16 v[112:115], a[76:79], v[16:19], v[112:115]\n\t" \
  "s_waitcnt lgkmcnt(5)\n\t" \
  "s_add_u32 s72, s64, 32768\n\t" \
  "v_accvgpr_read_b32 v227, a245\n\t" \
  "v_mfma_f32_16x16x32_bf16 v[100:103], a[12:15], a[8:11], v[100:103]\n\t" \
  "ds_read_b128 a[104:107], v242 offset:61440\n\t" \
  "v_perm_b32 v81, v227, v224, s79\n\t" \
  "v_mfma_f32_16x16x32_bf16 v[100:103], a[28:31], a[24:27], v[100:103]\n\t" \
  "buffer_load_dwordx4 a[140:143], v247, %[ws], s72 offen sc1\n\t" \
  "v_mfma_f32_16x16x32_bf16 v[100:103], a[44:47], a[40:43], v[100:103]\n\t" \
  "s_add_u32 s72, s63, 65536\n\t" \
  "ds_read_b128 a[120:123], v242 offset:62464\n\t" \
  "v_perm_b32 v85, v227, v224, s80\n\t" \
  "v_mfma_f32_16x16x32_bf16 v[100:103], a[60:63], a[56:59], v[100:103]\n\t" \
  "ds_read_b128 a[176:179], v242 offset:63488\n\t" \
  "v_mfma_f32_16x16x32_bf16 v[116:119], a[124:127], a[56:59], v[116:119]\n\t" \
  "ds_read_b128 a[180:183], v242 offset:64512\n\t" \
  "v_accvgpr_read_b32 v224, a242\n\t" \
  "v_mfma_f32_16x16x32_bf16 v[116:119], a[108:111], a[40:43], v[116:119]\n\t" \
  "buffer_load_dwordx4 a[152:155], v247, %[ws], s72 offen sc1\n\t" \
  "s_add_u32 s72, s64, 98304\n\t" \
  "v_mfma_f32_16x16x32_bf16 v[116:119], a[92:95], a[24:27], v[116:119]\n\t" \
  "s_waitcnt lgkmcnt(8)\n\t" \
  "v_lshlrev_b32 v0, 16, v208\n\t" \
  "v_and_b32 v1, 0xffff0000, v208\n\t" \
  "v_mfma_f32_16x16x32_bf16 v[116:119], a[76:79], a[8:11], v[116:119]\n\t" \
  "s_waitcnt lgkmcnt(4)\n\t" \
  "s_barrier\n\t" \
  "v_mfma_f32_16x16x32_bf16 v[104:107], a[12:15], v[128:131], v[104:107]\n\t" \
  "ds_read_b128 a[184:187], v230 offset:0\n\t" \
  "v_lshlrev_b32 v2, 16, v209\n\t" \
  "v_mfma_f32_16x16x32_bf16 v[104:107], a[28:31], v[132:135], v[104:107]\n\t" \
  "ds_read_b128 a[188:191], v230 offset:1024\n\t" \
  "v_and_b32 v3, 0xffff0000, v209\n\t" \
  "v_mfma_f32_16x16x32_bf16 v[104:107], a[44:47], a[72:75], v[104:107]\n\t" \
  "buffer_load_dwordx4 a[164:167], v247, %[ws], s72 offen sc1\n\t" \
  "v_mfma_f32_16x16x32_bf16 v[104:107], a[60:63], a[88:91], v[104:107]\n\t" \
  "s_and_b32 s72, s81, 240\n\t" \
  "s_cselect_b32 s72, -1, 0\n\t" \
  "v_lshl_or_b32 v247, s72, 31, v242\n\t" \
  "s_add_u32 s72, s63, 4096\n\t" \
  "v_mfma_f32_16x16x32_bf16 v[120:123], a[124:127], a[88:91], v[120:123]\n\t" \
  "ds_read_b128 v[136:139], v230 offset:2048\n\t" \
  "v_lshlrev_b32 v4, 16, v210\n\t" \
  "v_mfma_f32_16x16x32_bf16 v[120:123], a[108:111], a[72:75], v[120:123]\n\t" \
  "ds_read_b128 v[140:143], v230 offset:3072\n\t" \
  "v_and_b32 v5, 0xffff0000, v210\n\t" \
  "v_mfma_f32_16x16x32_bf16 v[120:123], a[92:95], v[132:135], v[120:123]\n\t" \
  "buffer_load_dwordx4 a[132:135], v247, %[ws], s72 offen sc1\n\t" \
  "s_add_u32 s72, s64, 36864\n\t" \
  "v_mfma_f32_16x16x32_bf16 v[120:123], a[76:79], v[128:131], v[120:123]\n\t" \
  "s_waitcnt lgkmcnt(4)\n\t" \
  "v_lshlrev_b32 v6, 16, v211\n\t" \
  "v_and_b32 v7, 0xffff0000, v211\n\t" \
  "v_mfma_f32_16x16x32_bf16 v[108:111], a[12:15], a[104:107], v[108:111]\n\t" \
  "ds_read_b128 v[128:131], v230 offset:4096\n\t" \
  "v_accvgpr_read_b32 v227, a246\n\t" \
  "v_mfma_f32_16x16x32_bf16 v[108:111], a[28:31], a[120:123], v[108:111]\n\t" \
  "ds_read_b128 v[132:135], v230 offset:5120\n\t" \
  "v_lshlrev_b32 v8, 16, v212\n\t" \
  "v_mfma_f32_16x16x32_bf16 v[108:111], a[44:47], a[176:179], v[108:111]\n\t" \
  "ds_read_b128 v[144:147], v230 offset:6144\n\t" \
  "v_perm_b32 v82, v227, v224, s79\n\t" \
  "v_mfma_f32_16x16x32_bf16 v[108:111], a[60:63], a[180:183], v[108:111]\n\t" \
  "buffer_load_dwordx4 a[144:147], v247, %[ws], s72 offen sc1\n\t" \
  "s_add_u32 s72, s63, 69632\n\t" \
  "v_mfma_f32_16x16x32_bf16 v[124:127], a[124:127], a[180:183], v[124:127]\n\t" \
  "ds_read_b128 v[148:151], v230 offset:7168\n\t" \
  "v_and_b32 v9, 0xffff0000, v212\n\t" \
  "v_mfma_f32_16x16x32_bf16 v[124:127], a[108:111], a[176:179], v[124:127]\n\t" \
  "v_perm_b32 v86, v227, v224, s80\n\t" \
  "v_lshlrev_b32 v10, 16, v213\n\t" \
  "v_mfma_f32_16x16x32_bf16 v[124:127], a[92:95], a[120:123], v[124:127]\n\t" \
  "v_accvgpr_read_b32 v224, a243\n\t" \
  "v_and_b32 v11, 0xffff0000, v213\n\t" \
  "v_mfma_f32_16x16x32_bf16 v[124:127], a[76:79], a[104:107], v[124:127]\n\t" \
  "s_waitcnt lgkmcnt(4)\n\t" \
  "buffer_load_dwordx4 a[156:159], v247, %[ws], s72 offen sc1\n\t" \
  "s_add_u32 s72, s64, 102400\n\t" \
  "v_mfma_f32_16x16x32_bf16 v[96:99], a[96:99], a[184:187], v[96:99]\n\t" \
  "ds_read_b128 v[152:155], v230 offset:8192\n\t" \
  "v_lshlrev_b32 v12, 16, v214\n\t" \
  "v_mfma_f32_16x16x32_bf16 v[96:99], a[112:115], a[188:191], v[96:99]\n\t" \
  "ds_read_b128 v[156:159], v230 offset:9216\n\t" \
  "v_accvgpr_read_b32 v227, a247\n\t" \
  "v_mfma_f32_16x16x32_bf16 v[96:99], a[100:103], v[136:139], v[96:99]\n\t" \
  "ds_read_b128 a[8:11], v230 offset:10240\n\t" \
  "v_and_b32 v13, 0xffff0000, v214\n\t" \
  "v_mfma_f32_16x16x32_bf16 v[96:99], a[116:119], v[140:143], v[96:99]\n\t" \
  "buffer_load_dwordx4 a[168:171], v247, %[ws], s72 offen sc1\n\t" \
  "v_mfma_f32_16x16x32_bf16 v[112:115], a[84:87], v[140:143], v[112:115]\n\t" \
  "ds_read_b128 a[24:27], v230 offset:11264\n\t" \
  "v_perm_b32 v83, v227, v224, s79\n\t" \
  "v_mfma_f32_16x16x32_bf16 v[112:115], a[80:83], v[136:139], v[112:115]\n\t" \
  "v_lshlrev_b32 v14, 16, v215\n\t" \
  "v_perm_b32 v87, v227, v224, s80\n\t" \
  "v_mfma_f32_16x16x32_bf16 v[112:115], a[68:71], a[188:191], v[112:115]\n\t" \
  "v_and_b32 v15, 0xffff0000, v215\n\t" \
  "v_accvgpr_read_b32 v224, a248\n\t" \
  "v_mfma_f32_16x16x32_bf16 v[112:115], a[64:67], a[184:187], v[112:115]\n\t" \
  "s_waitcnt lgkmcnt(4)\n\t" \
  "v_lshlrev_b32 v16, 16, v216\n\t" \
  "v_and_b32 v17, 0xffff0000, v216\n\t" \
  "v_mfma_f32_16x16x32_bf16 v[100:103], a[96:99], v[128:131], v[100:103]\n\t" \
  "ds_read_b128 a[40:43], v230 offset:12288\n\t" \
  "v_accvgpr_read_b32 v227, a252\n\t" \
  "v_mfma_f32_16x16x32_bf16 v[100:103], a[112:115], v[132:135], v[100:103]\n\t" \
  "ds_read_b128 a[56:59], v230 offset:13312\n\t" \
  "v_lshlrev_b32 v18, 16, v217\n\t" \
  "v_mfma_f32_16x16x32_bf16 v[100:103], a[100:103], v[144:147], v[100:103]\n\t" \
  "ds_read_b128 a[72:75], v230 offset:14336\n\t" \
  "v_perm_b32 v88, v227, v224, s79\n\t" \
  "v_mfma_f32_16x16x32_bf16 v[100:103], a[116:119], v[148:151], v[100:103]\n\t" \
  "ds_read_b128 a[88:91], v230 offset:15360\n\t" \
  "v_and_b32 v19, 0xffff0000, v217\n\t" \
  "s_waitcnt vmcnt(8)\n\t" \
  "v_cmp_ne_u32 vcc, %[tag], v228\n\t" \
  "s_and_b32 vcc_lo, vcc_lo, 0xf00\n\t" \
  "s_or_b32 s81, s81, vcc_lo\n\t" \
  "v_mfma_f32_16x16x32_bf16 v[116:119], a[84:87], v[148:151], v[116:119]\n\t" \
  "s_and_b32 s72, s81, 3840\n\t" \
  "s_cselect_b32 s72, -1, 0\n\t" \
  "v_lshl_or_b32 v247, s72, 31, v242\n\t" \
  "s_add_u32 s72, s63, 16384\n\t" \
  "v_mfma_f32_16x16x32_bf16 v[116:119], a[80:83], v[144:147], v[116:119]\n\t" \
  "buffer_load_dwordx4 a[136:139], v247, %[ws], s72 offen sc1\n\t" \
  "v_mfma_f32_16x16x32_bf16 v[116:119], a[68:71], v[132:135], v[116:119]\n\t" \
  "s_add_u32 s72, s64, 49152\n\t" \
  "v_perm_b32 v92, v227, v224, s80\n\t" \
  "v_lshlrev_b32 v20, 16, v218\n\t" \
  "v_mfma_f32_16x16x32_bf16 v[116:119], a[64:67], v[128:131], v[116:119]\n\t" \
  "s_waitcnt lgkmcnt(4)\n\t" \
  "s_barrier\n\t" \
  "v_mfma_f32_16x16x32_bf16 v[104:107], a[96:99], v[152:155], v[104:107]\n\t" \
  "buffer_load_dwordx4 a[148:151], v247, %[ws], s72 offen sc1\n\t" \
  "v_mfma_f32_16x16x32_bf16 v[104:107], a[112:115], v[156:159], v[104:107]\n\t" \
  "s_add_u32 s72, s63, 81920\n\t" \
  "ds_read_b128 v[128:131], v242 offset:16384\n\t" \
  "v_accvgpr_read_b32 v224, a249\n\t" \
  "v_mfma_f32_16x16x32_bf16 v[104:107], a[100:103], a[8:11], v[104:107]\n\t" \
  "ds_read_b128 v[132:135], v242 offset:17408\n\t" \
  "v_and_b32 v21, 0xffff0000, v218\n\t" \
  "v_mfma_f32_16x16x32_bf16 v[104:107], a[116:119], a[24:27], v[104:107]\n\t" \
  "buffer_load_dwordx4 a[160:163], v247, %[ws], s72 offen sc1\n\t" \
  "v_mfma_f32_16x16x32_bf16 v[120:123], a[84:87], a[24:27], v[120:123]\n\t" \
  "s_add_u32 s72, s64, 114688\n\t" \
  "ds_read_b128 a[12:15], v242 offset:18432\n\t" \
  "v_lshlrev_b32 v22, 16, v219\n\t" \
  "v_mfma_f32_16x16x32_bf16 v[120:123], a[80:83], a[8:11], v[120:123]\n\t" \
  "ds_read_b128 a[28:31], v242 offset:19456\n\t" \
  "v_accvgpr_read_b32 v227, a253\n\t" \
  "v_mfma_f32_16x16x32_bf16 v[120:123], a[68:71], v[156:159], v[120:123]\n\t" \
  "v_and_b32 v23, 0xffff0000, v219\n\t" \
  "v_perm_b32 v89, v227, v224, s79\n\t" \
  "v_mfma_f32_16x16x32_bf16 v[120:123], a[64:67], v[152:155], v[120:123]\n\t" \
  "s_waitcnt lgkmcnt(4)\n\t" \
  "buffer_load_dwordx4 a[172:175], v247, %[ws], s72 offen sc1\n\t" \
  "v_mfma_f32_16x16x32_bf16 v[108:111], a[96:99], a[40:43], v[108:111]\n\t" \
  "ds_read_b128 a[44:47], v242 offset:20480\n\t" \
  "v_lshlrev_b32 v24, 16, v220\n\t" \
  "v_mfma_f32_16x16x32_bf16 v[108:111], a[112:115], a[56:59], v[108:111]\n\t" \
  "ds_read_b128 a[60:63], v242 offset:21504\n\t" \
  "v_perm_b32 v93, v227, v224, s80\n\t" \
  "v_mfma_f32_16x16x32_bf16 v[108:111], a[100:103], a[72:75], v[108:111]\n\t" \
  "ds_read_b128 a[76:79], v242 offset:22528\n\t" \
  "v_and_b32 v25, 0xffff0000, v220\n\t" \
  "v_mfma_f32_16x16x32_bf16 v[108:111], a[116:119], a[88:91], v[108:111]\n\t" \
  "ds_read_b128 a[92:95], v242 offset:23552\n\t" \
  "v_accvgpr_read_b32 v224, a250\n\t" \
  "v_mfma_f32_16x16x32_bf16 v[124:127], a[84:87], a[88:91], v[124:127]\n\t" \
  "v_lshlrev_b32 v26, 16, v221\n\t" \
  "v_and_b32 v27, 0xffff0000, v221\n\t" \
  "v_mfma_f32_16x16x32_bf16 v[124:127], a[80:83], a[72:75], v[124:127]\n\t" \
  "v_accvgpr_read_b32 v227, a254\n\t" \
  "v_lshlrev_b32 v28, 16, v222\n\t" \
  "v_mfma_f32_16x16x32_bf16 v[124:127], a[68:71], a[56:59], v[124:127]\n\t" \
  "v_perm_b32 v90, v227, v224, s79\n\t" \
  "v_and_b32 v29, 0xffff0000, v222\n\t" \
  "v_mfma_f32_16x16x32_bf16 v[124:127], a[64:67], a[40:43], v[124:127]\n\t" \
  "s_waitcnt lgkmcnt(4)\n\t" \
  "v_perm_b32 v94, v227, v224, s80\n\t" \
  "v_lshlrev_b32 v30, 16, v223\n\t" \
  "v_mfma_f32_16x16x32_bf16 v[96:99], a[32:35], v[128:131], v[96:99]\n\t" \
  "ds_read_b128 a[104:107], v242 offset:24576\n\t" \
  "v_accvgpr_read_b32 v224, a251\n\t" \
  "v_mfma_f32_16x16x32_bf16 v[96:99], a[48:51], v[132:135], v[96:99]\n\t" \
  "ds_read_b128 a[108:111], v242 offset:25600\n\t" \
  "v_and_b32 v31, 0xffff0000, v223\n\t" \
  "v_mfma_f32_16x16x32_bf16 v[96:99], a[36:39], a[12:15], v[96:99]\n\t" \
  "buffer_load_dword v246, v235, %[ws], s68 offen sc1\n\t" \
  "v_mfma_f32_16x16x32_bf16 v[96:99], a[52:55], a[28:31], v[96:99]\n\t" \
  "ds_read_b128 a[120:123], v242 offset:26624\n\t" \
  "v_accvgpr_read_b32 v227, a255\n\t" \
  "v_mfma_f32_16x16x32_bf16 v[112:115], a[20:23], a[28:31], v[112:115]\n\t" \
  "ds_read_b128 a[124:127], v242 offset:27648\n\t" \
  "v_perm_b32 v91, v227, v224, s79\n\t" \
  "v_mfma_f32_16x16x32_bf16 v[112:115], a[4:7], a[12:15], v[112:115]\n\t" \
  "v_mfma_f32_16x16x32_bf16 v[112:115], a[16:19], v[132:135], v[112:115]\n\t" \
  "v_perm_b32 v95, v227, v224, s80\n\t" \
  "v_mfma_f32_16x16x32_bf16 v[112:115], a[0:3], v[128:131], v[112:115]\n\t" \
  "s_waitcnt lgkmcnt(4)\n\t" \
  "v_mfma_f32_16x16x32_bf16 v[100:103], a[32:35], a[44:47], v[100:103]\n\t" \
  "ds_read_b128 a[176:179], v242 offset:28672\n\t" \
  "v_mfma_f32_16x16x32_bf16 v[100:103], a[48:51], a[60:63], v[100:103]\n\t" \
  "ds_read_b128 a[180:183], v242 offset:29696\n\t" \
  "v_mfma_f32_16x16x32_bf16 v[100:103], a[36:39], a[76:79], v[100:103]\n\t" \
  "ds_read_b128 a[184:187], v242 offset:30720\n\t" \
  "v_mfma_f32_16x16x32_bf16 v[100:103], a[52:55], a[92:95], v[100:103]\n\t" \
  "ds_read_b128 a[188:191], v242 offset:31744\n\t" \
  "v_mfma_f32_16x16x32_bf16 v[116:119], a[20:23], a[92:95], v[116:119]\n\t" \
  "v_mfma_f32_16x16x32_bf16 v[116:119], a[4:7], a[76:79], v[116:119]\n\t" \
  "v_mfma_f32_16x16x32_bf16 v[116:119], a[16:19], a[60:63], v[116:119]\n\t" \
  "v_mfma_f32_16x16x32_bf16 v[116:119], a[0:3], a[44:47], v[116:119]\n\t" \
  "s_waitcnt lgkmcnt(4)\n\t" \
  "v_mfma_f32_16x16x32_bf16 v[104:107], a[32:35], a[104:107], v[104:107]\n\t" \
  "ds_read_b128 v[128:131], v237 offset:2560\n\t" \
  "v_mfma_f32_16x16x32_bf16 v[104:107], a[48:51], a[108:111], v[104:107]\n\t" \
  "ds_read_b128 v[132:135], v237 offset:2624\n\t" \
  "v_mfma_f32_16x16x32_bf16 v[104:107], a[36:39], a[120:123], v[104:107]\n\t" \
  "v_mfma_f32_16x16x32_bf16 v[104:107], a[52:55], a[124:127], v[104:107]\n\t" \
  "v_mfma_f32_16x16x32_bf16 v[120:123], a[20:23], a[124:127], v[120:123]\n\t" \
  "v_mfma_f32_16x16x32_bf16 v[120:123], a[4:7], a[120:123], v[120:123]\n\t" \
  "v_mfma_f32_16x16x32_bf16 v[120:123], a[16:19], a[108:111], v[120:123]\n\t" \
  "v_mfma_f32_16x16x32_bf16 v[120:123], a[0:3], a[104:107], v[120:123]\n\t" \
  "s_waitcnt lgkmcnt(2)\n\t" \
  "v_mfma_f32_16x16x32_bf16 v[108:111], a[32:35], a[176:179], v[108:111]\n\t" \
  "v_mfma_f32_16x16x32_bf16 v[108:111], a[48:51], a[180:183], v[108:111]\n\t" \
  "v_mfma_f32_16x16x32_bf16 v[108:111], a[36:39], a[184:187], v[108:111]\n\t" \
  "v_mfma_f32_16x16x32_bf16 v[108:111], a[52:55], a[188:191], v[108:111]\n\t" \
  "v_mfma_f32_16x16x32_bf16 v[124:127], a[20:23], a[188:191], v[124:127]\n\t" \
  "v_mfma_f32_16x16x32_bf16 v[124:127], a[4:7], a[184:187], v[124:127]\n\t" \
  "v_mfma_f32_16x16x32_bf16 v[124:127], a[16:19], a[180:183], v[124:127]\n\t" \
  "v_mfma_f32_16x16x32_bf16 v[124:127], a[0:3], a[176:179], v[124:127]\n\t" \
  STP("v_writelane_b32 %[stv], s94, 36\n\t") \
  STP("v_writelane_b32 %[stv], s92, 37\n\t") \
  STP("s_memrealtime s[94:95]\n\t") \
  STP("s_memtime s[92:93]\n\t") \
  "s_cmp_lg_u32 s81, 0\n\t" \
  "s_cbranch_scc1 .Lop_s99\n\t" \
  ".Lop_d2:\n\t" \
  "s_waitcnt vmcnt(9)\n\t" \
  "v_mfma_f32_16x16x32_bf16 v[96:99], a[196:199], a[128:131], v[96:99]\n\t" \
  "v_mfma_f32_16x16x32_bf16 v[100:103], a[196:199], a[140:143], v[100:103]\n\t" \
  "v_mfma_f32_16x16x32_bf16 v[104:107], a[196:199], a[152:155], v[104:107]\n\t" \
  "v_mfma_f32_16x16x32_bf16 v[108:111], a[196:199], a[164:167], v[108:111]\n\t" \
  "s_waitcnt vmcnt(5)\n\t" \
  "v_mfma_f32_16x16x32_bf16 v[112:115], a[194:197], a[130:133], v[112:115]\n\t" \
  "v_mfma_f32_16x16x32_bf16 v[116:119], a[194:197], a[142:145], v[116:119]\n\t" \
  "v_mfma_f32_16x16x32_bf16 v[120:123], a[194:197], a[154:157], v[120:123]\n\t" \
  "v_mfma_f32_16x16x32_bf16 v[124:127], a[194:197], a[166:169], v[124:127]\n\t" \
  "s_waitcnt vmcnt(1)\n\t" \
  "v_mfma_f32_16x16x32_bf16 v[96:99], a[194:197], a[134:137], v[96:99]\n\t" \
  "v_mfma_f32_16x16x32_bf16 v[100:103], a[194:197], a[146:149], v[100:103]\n\t" \
  "v_mfma_f32_16x16x32_bf16 v[104:107], a[194:197], a[158:161], v[104:107]\n\t" \
  "v_mfma_f32_16x16x32_bf16 v[108:111], a[194:197], a[170:173], v[108:111]\n\t" \
  "v_mfma_f32_16x16x32_bf16 v[112:115], a[192:195], a[136:139], v[112:115]\n\t" \
  "v_mfma_f32_16x16x32_bf16 v[116:119], a[192:195], a[148:151], v[116:119]\n\t" \
  "v_mfma_f32_16x16x32_bf16 v[120:123], a[192:195], a[160:163], v[120:123]\n\t" \
  "v_mfma_f32_16x16x32_bf16 v[124:127], a[192:195], a[172:175], v[124:127]\n\t" \
  "s_waitcnt vmcnt(0) lgkmcnt(0)\n\t" \
  STP("v_writelane_b32 %[stv], s94, 38\n\t") \
  STP("v_writelane_b32 %[stv], s92, 39\n\t") \
  "s_nop 7\n\t" \
  ".subsection 1\n\t" \
  ".Lop_s0:\n\t" \
  "s_mov_b32 s78, 0\n\t" \
  ".Lop_r0:\n\t" \
  "s_sleep 2\n\t" \
  "buffer_load_dword v246, v245, %[ws], %[fw2] offen sc1\n\t" \
  "s_waitcnt vmcnt(0)\n\t" \
  "v_cmp_ne_u32 vcc, %[tag], v246\n\t" \
  "s_and_b32 vcc_lo, vcc_lo, 0x6\n\t" \
  "s_cbranch_scc0 .Lop_k0\n\t" \
  "s_add_u32 s78, s78, 1\n\t" \
  "s_cmp_lt_u32 s78, %[cap]\n\t" \
  "s_cbranch_scc1 .Lop_r0\n\t" \
  "buffer_store_dword v243, off, %[ws], 0 offset:256 sc1\n\t" \
  "s_branch .Lop_k0\n\t" \
  ".Lop_s1:\n\t" \
  "s_mov_b32 s78, 0\n\t" \
  ".Lop_r1:\n\t" \
  "s_sleep 2\n\t" \
  "buffer_load_dword v246, v245, %[ws], %[fw2] offen sc1\n\t" \
  "s_waitcnt vmcnt(0)\n\t" \
  "v_cmp_ne_u32 vcc, %[tag], v246\n\t" \
  "s_and_b32 vcc_lo, vcc_lo, 0xe00\n\t" \
  "s_cbranch_scc0 .Lop_k1\n\t" \
  "s_add_u32 s78, s78, 1\n\t" \
  "s_cmp_lt_u32 s78, %[cap]\n\t" \
  "s_cbranch_scc1 .Lop_r1\n\t" \
  "buffer_store_dword v243, off, %[ws], 0 offset:256 sc1\n\t" \
  "s_branch .Lop_k1\n\t" \
  ".Lop_s2:\n\t" \
  "s_mov_b32 s78, 0\n\t" \
  ".Lop_r2:\n\t" \
  "s_sleep 2\n\t" \
  "buffer_load_dword v246, v245, %[ws], %[fw2] offen sc1\n\t" \
  "s_waitcnt vmcnt(0)\n\t" \
  "v_cmp_ne_u32 vcc, %[tag], v246\n\t" \
  "s_and_b32 vcc_lo, vcc_lo, 0xfe00\n\t" \
  "s_cbranch_scc0 .Lop_k2\n\t" \
  "s_add_u32 s78, s78, 1\n\t" \
  "s_cmp_lt_u32 s78, %[cap]\n\t" \
  "s_cbranch_scc1 .Lop_r2\n\t" \
  "buffer_store_dword v243, off, %[ws], 0 offset:256 sc1\n\t" \
  "s_branch .Lop_k2\n\t" \
  ".Lop_s3:\n\t" \
  "s_mov_b32 s78, 0\n\t" \
  ".Lop_r3:\n\t" \
  "s_sleep 2\n\t" \
  "buffer_load_dword v246, v245, %[ws], %[fw2] offen sc1\n\t" \
  "s_waitcnt vmcnt(0)\n\t" \
  "v_cmp_ne_u32 vcc, %[tag], v246\n\t" \
  "s_and_b32 vcc_lo, vcc_lo, 0xfe0000\n\t" \
  "s_cbranch_scc0 .Lop_k3\n\t" \
  "s_add_u32 s78, s78, 1\n\t" \
  "s_cmp_lt_u32 s78, %[cap]\n\t" \
  "s_cbranch_scc1 .Lop_r3\n\t" \
  "buffer_store_dword v243, off, %[ws], 0 offset:256 sc1\n\t" \
  "s_branch .Lop_k3\n\t" \
  ".Lop_s4:\n\t" \
  "s_mov_b32 s78, 0\n\t" \
  ".Lop_r4:\n\t" \
  "s_sleep 4\n\t" \
  "s_add_u32 s75, s69, 0\n\t" \
  "buffer_load_dwordx4 v[208:211], v170, %[ws], s75 offen sc1\n\t" \
  "s_add_u32 s75, s69, 32768\n\t" \
  "buffer_load_dwordx4 v[212:215], v170, %[ws], s75 offen sc1\n\t" \
  "s_add_u32 s75, s69, 65536\n\t" \
  "buffer_load_dwordx4 v[216:219], v170, %[ws], s75 offen sc1\n\t" \
  "s_add_u32 s75, s69, 98304\n\t" \
  "buffer_load_dwordx4 v[220:223], v170, %[ws], s75 offen sc1\n\t" \
  "s_waitcnt vmcnt(0)\n\t" \
  "v_xor_b32 v246, v243, v209\n\t" \
  "v_bitop3_b32 v246, v246, v211, v243 bitop3:0xf6\n\t" \
  "v_bitop3_b32 v246, v246, v213, v243 bitop3:0xf6\n\t" \
  "v_bitop3_b32 v246, v246, v215, v243 bitop3:0xf6\n\t" \
  "v_bitop3_b32 v246, v246, v217, v243 bitop3:0xf6\n\t" \
  "v_bitop3_b32 v246, v246, v219, v243 bitop3:0xf6\n\t" \
  "v_bitop3_b32 v246, v246, v221, v243 bitop3:0xf6\n\t" \
  "v_bitop3_b32 v246, v246, v223, v243 bitop3:0xf6\n\t" \
  "v_cmp_ne_u32 vcc, 0, v246\n\t" \
  "s_cbranch_vccz .Lop_k4\n\t" \
  "s_add_u32 s78, s78, 1\n\t" \
  "s_cmp_lt_u32 s78, %[cap]\n\t" \
  "s_cbranch_scc1 .Lop_r4\n\t" \
  "buffer_store_dword v243, off, %[ws], 0 offset:256 sc1\n\t" \
  "s_branch .Lop_k4\n\t" \
  ".Lop_rd:\n\t" \
  "s_waitcnt vmcnt(0) lgkmcnt(0)\n\t" \
  "s_barrier\n\t" \
  "s_mov_b32 %[pm], 2\n\t" \
  "s_branch .Lop_end\n\t" \
  ".Lop_s99:\n\t" \
  "s_waitcnt vmcnt(0)\n\t" \
  "v_cmp_ne_u32 vcc, %[tag], v246\n\t" \
  "s_and_b32 vcc_lo, vcc_lo, 0xfff\n\t" \
  "s_cbranch_scc0 .Lop_k99\n\t" \
  "s_mov_b32 s78, 0\n\t" \
  ".Lop_r99:\n\t" \
  "buffer_load_dword v246, v235, %[ws], s68 offen sc1\n\t" \
  "s_waitcnt vmcnt(0)\n\t" \
  "v_cmp_ne_u32 vcc, %[tag], v246\n\t" \
  "s_and_b32 vcc_lo, vcc_lo, 0xfff\n\t" \
  "s_cbranch_scc0 .Lop_k99\n\t" \
  "s_add_u32 s78, s78, 1\n\t" \
  "s_cmp_lt_u32 s78, %[cap]\n\t" \
  "s_cbranch_scc1 .Lop_r99\n\t" \
  "buffer_store_dword v243, off, %[ws], 0 offset:256 sc1\n\t" \
  "s_branch .Lop_k99\n\t" \
  ".Lop_k99:\n\t" \
  "s_and_b32 s72, s81, 15\n\t" \
  "s_cbranch_scc0 .Lop_m0\n\t" \
  "s_add_u32 s72, s63, 0\n\t" \
  "buffer_load_dwordx4 a[128:131], v242, %[ws], s72 offen sc1\n\t" \
  "s_add_u32 s72, s64, 32768\n\t" \
  "buffer_load_dwordx4 a[140:143], v242, %[ws], s72 offen sc1\n\t" \
  "s_add_u32 s72, s63, 65536\n\t" \
  "buffer_load_dwordx4 a[152:155], v242, %[ws], s72 offen sc1\n\t" \
  "s_add_u32 s72, s64, 98304\n\t" \
  "buffer_load_dwordx4 a[164:167], v242, %[ws], s72 offen sc1\n\t" \
  ".Lop_m0:\n\t" \
  "s_and_b32 s72, s81, 240\n\t" \
  "s_cbranch_scc0 .Lop_m1\n\t" \
  "s_add_u32 s72, s63, 4096\n\t" \
  "buffer_load_dwordx4 a[132:135], v242, %[ws], s72 offen sc1\n\t" \
  "s_add_u32 s72, s64, 36864\n\t" \
  "buffer_load_dwordx4 a[144:147], v242, %[ws], s72 offen sc1\n\t" \
  "s_add_u32 s72, s63, 69632\n\t" \
  "buffer_load_dwordx4 a[156:159], v242, %[ws], s72 offen sc1\n\t" \
  "s_add_u32 s72, s64, 102400\n\t" \
  "buffer_load_dwordx4 a[168:171], v242, %[ws], s72 offen sc1\n\t" \
  ".Lop_m1:\n\t" \
  "s_and_b32 s72, s81, 3840\n\t" \
  "s_cbranch_scc0 .Lop_m2\n\t" \
  "s_add_u32 s72, s63, 16384\n\t" \
  "buffer_load_dwordx4 a[136:139], v242, %[ws], s72 offen sc1\n\t" \
  "s_add_u32 s72, s64, 49152\n\t" \
  "buffer_load_dwordx4 a[148:151], v242, %[ws], s72 offen sc1\n\t" \
  "s_add_u32 s72, s63, 81920\n\t" \
  "buffer_load_dwordx4 a[160:163], v242, %[ws], s72 offen sc1\n\t" \
  "s_add_u32 s72, s64, 114688\n\t" \
  "buffer_load_dwordx4 a[172:175], v242, %[ws], s72 offen sc1\n\t" \
  ".Lop_m2:\n\t" \
  "s_waitcnt vmcnt(0)\n\t" \
  "s_branch .Lop_d2\n\t" \
  ".Lfz_z:\n\t" \
  ".fill 128, 4, 0xBF800000\n\t" \
  ".subsection 0\n\t" \
  ".Lop_end:\n\t" \
  ""
#define OP_CLOB "v208", "v209", "v210", "v211", "v212", "v214", "v215", "v216", "v217", "v218", "v219", "v220", "v221", "v222", "v223", "v228", "v229", "v230", "v231", "v232", "v233", "v235", "v236", "v238", "v239", "v246", "v248", "v249", "v250", "v251", "s60", "s61", "s62", "s63", "s64", "s65", "s66", "s67", "s68", "s69", "s70", "s71", "s72", "s73", "s74", "s75", "s76", "s77", "s78", "s79", "s80", "s81", "s82", "s83", "s84", "s85", "s92", "s93", "s94", "s95", "vcc", "scc", "memory"
#define ST_ASM "s_nop 1\n\t" "v_mfma_f32_16x16x32_bf16 v[80:83], a[208:211], v[160:163], 0\n\t" "v_mfma_f32_16x16x32_bf16 v[80:83], a[212:215], v[164:167], v[80:83]\n\t" "v_mfma_f32_16x16x32_bf16 v[80:83], a[216:219], v[168:171], v[80:83]\n\t" "v_mfma_f32_16x16x32_bf16 v[80:83], a[220:223], v[172:175], v[80:83]\n\t" "v_dot2_f32_bf16 v64, v160, v160, 0\n\t" "v_dot2_f32_bf16 v65, v164, v164, 0\n\t" "v_dot2_f32_bf16 v66, v168, v168, 0\n\t" "v_dot2_f32_bf16 v67, v172, v172, 0\n\t" "v_dot2_f32_bf16 v64, v161, v161, v64\n\t" "v_dot2_f32_bf16 v65, v165, v165, v65\n\t" "v_dot2_f32_bf16 v66, v169, v169, v66\n\t" "v_dot2_f32_bf16 v67, v173, v173, v67\n\t" "v_dot2_f32_bf16 v64, v162, v162, v64\n\t" "v_dot2_f32_bf16 v65, v166, v166, v65\n\t" "v_dot2_f32_bf16 v66, v170, v170, v66\n\t" "v_dot2_f32_bf16 v67, v174, v174, v67\n\t" "v_dot2_f32_bf16 v64, v163, v163, v64\n\t" "v_dot2_f32_bf16 v65, v167, v167, v65\n\t" "v_dot2_f32_bf16 v66, v171, v171, v66\n\t" "v_dot2_f32_bf16 v67, v175, v175, v67\n\t" "s_nop 2\n\t"
static_assert(W_MODB == 2228224);
constexpr int GSLOT = 49152;
constexpr unsigned KRS = 256;
struct KVP { srd_t srdK, srdV; unsigned vkb[4], vvb[2], vksrc[4], vvsrc[2], sofk; };
struct H4 {
  srd_t srdW;
  unsigned voffX, voffW[4];
  unsigned xb1, wb[2][8];
  unsigned mX, mW, soX, rot;
  unsigned pfd, fo, fl, fvo;
  u32x32 q[4];
};
#define W4(a) "+v"(a[0]), "+v"(a[1]), "+v"(a[2]), "+v"(a[3])
#define W8(a) W4(a), "+v"(a[4]), "+v"(a[5]), "+v"(a[6]), "+v"(a[7])
constexpr unsigned kXT = 65536, kWT = 64 * QD * 2;
DI void w_dma4(unsigned m0, srd_t srd, const unsigned (&vo)[4], unsigned so) {
  asm volatile("s_mov_b32 m0, %0\n\ts_nop 0\n\tbuffer_load_dwordx4 %1, %5, %6 offen lds\n\tbuffer_load_dwordx4 %2, %5, %6 offen offset:1024 lds\n\t"
               "buffer_load_dwordx4 %3, %5, %6 offen offset:2048 lds\n\tbuffer_load_dwordx4 %4, %5, %6 offen offset:3072 lds"
               :: "s"(m0), "v"(vo[0]), "v"(vo[1]), "v"(vo[2]), "v"(vo[3]), "s"(srd), "s"(so) : "memory");
}
DI void w_voffs(unsigned (&vo)[4], int ldw, unsigned wid, unsigned lane, unsigned k0) {
#pragma unroll
  for (int j = 0; j < 4; ++j) {
    const unsigned k = (4 * wid + j) * 4 + (lane >> 4), c16 = lane & 15, sw = (k & 3) | (((k >> 3) & 1) << 2);
    vo[j] = (k ^ (32 * k0)) * ldw * 2 + ((((c16 >> 1) ^ sw) << 1) + (c16 & 1)) * 16 - 1024 * j;
  }
}
DI void h4_init_addr(H4& c, unsigned ldsbase, unsigned wid, unsigned lane, unsigned k0) {
  const unsigned r16 = lane & 15, g = lane >> 4, q = (lane >> 2) & 3, p = lane & 3;
  c.xb1 = ldsbase + GSLOT + wid * 8192 + k0 * 4096 + g * 1024 + r16 * 16;
#pragma unroll
  for (int sl = 0; sl < 2; ++sl)
#pragma unroll
    for (int nb = 0; nb < 8; ++nb) { c.wb[sl][nb] = ldsbase + 2 * sl * GSLOT + 32768 + (8 * g + q) * 256 + ((nb ^ (q | ((g & 1) << 2))) << 5) + p * 8; asm volatile("" : "+v"(c.wb[sl][nb])); }
}
#define FSTP(x)
#define FSTS(x)
#define FQ_STV
#define FQ_STC
#define FQ_ASM \
  "s_mov_b32 s98, 0xffff0000\n\t" \
  ".p2align 3\n\t" \
  "ds_read_b128 v[128:131], v208 offset:0\n\t" \
  "ds_read_b128 v[132:135], v208 offset:16\n\t" \
  "ds_read_b128 v[136:139], v208 offset:256\n\t" \
  "ds_read_b128 v[140:143], v208 offset:272\n\t" \
  "ds_read_b128 v[32:35], v204 offset:49152\n\t" \
  "ds_read_b128 v[36:39], v204 offset:51200\n\t" \
  "ds_read_b128 v[40:43], v204 offset:53248\n\t" \
  "ds_read_b128 v[44:47], v204 offset:55296\n\t" \
  "ds_read_b128 v[144:147], v208 offset:128\n\t" \
  "ds_read_b128 v[148:151], v208 offset:144\n\t" \
  "ds_read_b128 v[152:155], v208 offset:384\n\t" \
  "ds_read_b128 v[156:159], v208 offset:400\n\t" \
  "ds_read_b128 v[48:51], v205 offset:49152\n\t" \
  "ds_read_b128 v[52:55], v205 offset:51200\n\t" \
  "ds_read_b128 v[56:59], v205 offset:53248\n\t" \
  "ds_read_b128 v[60:63], v205 offset:55296\n\t" \
  "v_lshlrev_b32 v176, 16, v0\n\t" \
  "v_and_b32 v177, s98, v0\n\t" \
  "v_lshlrev_b32 v178, 16, v1\n\t" \
  "v_and_b32 v179, s98, v1\n\t" \
  "v_lshlrev_b32 v180, 16, v2\n\t" \
  "v_and_b32 v181, s98, v2\n\t" \
  "v_lshlrev_b32 v182, 16, v3\n\t" \
  "v_and_b32 v183, s98, v3\n\t" \
  "v_pk_fma_f32 v[176:177], v[176:177], v[160:161], v[162:163] op_sel_hi:[1,0,0]\n\t" \
  "v_pk_fma_f32 v[178:179], v[178:179], v[160:161], v[162:163] op_sel_hi:[1,0,0]\n\t" \
  "v_pk_fma_f32 v[180:181], v[180:181], v[160:161], v[162:163] op_sel_hi:[1,0,0]\n\t" \
  "v_pk_fma_f32 v[182:183], v[182:183], v[160:161], v[162:163] op_sel_hi:[1,0,0]\n\t" \
  "s_waitcnt lgkmcnt(12)\n\t" \
  "s_nop 0\n\t" \
  "s_xor_b32 s97, %[pso], 0\n\t" \
  ".p2align 3\n\t" \
  "v_pk_fma_f32 v[176:177], v[176:177], v[128:129], v[136:137]\n\t" \
  "v_pk_fma_f32 v[178:179], v[178:179], v[130:131], v[138:139]\n\t" \
  "v_pk_fma_f32 v[180:181], v[180:181], v[132:133], v[140:141]\n\t" \
  "v_pk_fma_f32 v[182:183], v[182:183], v[134:135], v[142:143]\n\t" \
  "v_cvt_pk_bf16_f32 v0, v176, v177\n\t" \
  "v_cvt_pk_bf16_f32 v1, v178, v179\n\t" \
  "v_cvt_pk_bf16_f32 v2, v180, v181\n\t" \
  "v_cvt_pk_bf16_f32 v3, v182, v183\n\t" \
  "buffer_store_dwordx4 v[0:3], v214, %[ws], s97 offen sc1\n\t" \
  "v_lshlrev_b32 v176, 16, v4\n\t" \
  "v_and_b32 v177, s98, v4\n\t" \
  "v_lshlrev_b32 v178, 16, v5\n\t" \
  "v_and_b32 v179, s98, v5\n\t" \
  "v_lshlrev_b32 v180, 16, v6\n\t" \
  "v_and_b32 v181, s98, v6\n\t" \
  "v_lshlrev_b32 v182, 16, v7\n\t" \
  "v_and_b32 v183, s98, v7\n\t" \
  "v_pk_fma_f32 v[176:177], v[176:177], v[164:165], v[166:167] op_sel_hi:[1,0,0]\n\t" \
  "v_pk_fma_f32 v[178:179], v[178:179], v[164:165], v[166:167] op_sel_hi:[1,0,0]\n\t" \
  "v_pk_fma_f32 v[180:181], v[180:181], v[164:165], v[166:167] op_sel_hi:[1,0,0]\n\t" \
  "v_pk_fma_f32 v[182:183], v[182:183], v[164:165], v[166:167] op_sel_hi:[1,0,0]\n\t" \
  "s_xor_b32 s97, %[pso], 256\n\t" \
  ".p2align 3\n\t" \
  "v_pk_fma_f32 v[176:177], v[176:177], v[128:129], v[136:137]\n\t" \
  "v_pk_fma_f32 v[178:179], v[178:179], v[130:131], v[138:139]\n\t" \
  "v_pk_fma_f32 v[180:181], v[180:181], v[132:133], v[140:141]\n\t" \
  "v_pk_fma_f32 v[182:183], v[182:183], v[134:135], v[142:143]\n\t" \
  "v_cvt_pk_bf16_f32 v4, v176, v177\n\t" \
  "v_cvt_pk_bf16_f32 v5, v178, v179\n\t" \
  "v_cvt_pk_bf16_f32 v6, v180, v181\n\t" \
  "v_cvt_pk_bf16_f32 v7, v182, v183\n\t" \
  "buffer_store_dwordx4 v[4:7], v214, %[ws], s97 offen sc1\n\t" \
  "v_lshlrev_b32 v176, 16, v8\n\t" \
  "v_and_b32 v177, s98, v8\n\t" \
  "v_lshlrev_b32 v178, 16, v9\n\t" \
  "v_and_b32 v179, s98, v9\n\t" \
  "v_lshlrev_b32 v180, 16, v10\n\t" \
  "v_and_b32 v181, s98, v10\n\t" \
  "v_lshlrev_b32 v182, 16, v11\n\t" \
  "v_and_b32 v183, s98, v11\n\t" \
  "v_pk_fma_f32 v[176:177], v[176:177], v[168:169], v[170:171] op_sel_hi:[1,0,0]\n\t" \
  "v_pk_fma_f32 v[178:179], v[178:179], v[168:169], v[170:171] op_sel_hi:[1,0,0]\n\t" \
  "v_pk_fma_f32 v[180:181], v[180:181], v[168:169], v[170:171] op_sel_hi:[1,0,0]\n\t" \
  "v_pk_fma_f32 v[182:183], v[182:183], v[168:169], v[170:171] op_sel_hi:[1,0,0]\n\t" \
  "s_xor_b32 s97, %[pso], 512\n\t" \
  ".p2align 3\n\t" \
  "v_pk_fma_f32 v[176:177], v[176:177], v[128:129], v[136:137]\n\t" \
  "v_pk_fma_f32 v[178:179], v[178:179], v[130:131], v[138:139]\n\t" \
  "v_pk_fma_f32 v[180:181], v[180:181], v[132:133], v[140:141]\n\t" \
  "v_pk_fma_f32 v[182:183], v[182:183], v[134:135], v[142:143]\n\t" \
  "v_cvt_pk_bf16_f32 v8, v176, v177\n\t" \
  "v_cvt_pk_bf16_f32 v9, v178, v179\n\t" \
  "v_cvt_pk_bf16_f32 v10, v180, v181\n\t" \
  "v_cvt_pk_bf16_f32 v11, v182, v183\n\t" \
  "buffer_store_dwordx4 v[8:11], v214, %[ws], s97 offen sc1\n\t" \
  "v_lshlrev_b32 v176, 16, v12\n\t" \
  "v_and_b32 v177, s98, v12\n\t" \
  "v_lshlrev_b32 v178, 16, v13\n\t" \
  "v_and_b32 v179, s98, v13\n\t" \
  "v_lshlrev_b32 v180, 16, v14\n\t" \
  "v_and_b32 v181, s98, v14\n\t" \
  "v_lshlrev_b32 v182, 16, v15\n\t" \
  "v_and_b32 v183, s98, v15\n\t" \
  "v_pk_fma_f32 v[176:177], v[176:177], v[172:173], v[174:175] op_sel_hi:[1,0,0]\n\t" \
  "v_pk_fma_f32 v[178:179], v[178:179], v[172:173], v[174:175] op_sel_hi:[1,0,0]\n\t" \
  "v_pk_fma_f32 v[180:181], v[180:181], v[172:173], v[174:175] op_sel_hi:[1,0,0]\n\t" \
  "v_pk_fma_f32 v[182:183], v[182:183], v[172:173], v[174:175] op_sel_hi:[1,0,0]\n\t" \
  "s_xor_b32 s97, %[pso], 768\n\t" \
  ".p2align 3\n\t" \
  "v_pk_fma_f32 v[176:177], v[176:177], v[128:129], v[136:137]\n\t" \
  "v_pk_fma_f32 v[178:179], v[178:179], v[130:131], v[138:139]\n\t" \
  "v_pk_fma_f32 v[180:181], v[180:181], v[132:133], v[140:141]\n\t" \
  "v_pk_fma_f32 v[182:183], v[182:183], v[134:135], v[142:143]\n\t" \
  "v_cvt_pk_bf16_f32 v12, v176, v177\n\t" \
  "v_cvt_pk_bf16_f32 v13, v178, v179\n\t" \
  "v_cvt_pk_bf16_f32 v14, v180, v181\n\t" \
  "v_cvt_pk_bf16_f32 v15, v182, v183\n\t" \
  "buffer_store_dwordx4 v[12:15], v214, %[ws], s97 offen sc1\n\t" \
  "ds_read_b128 v[128:131], v208 offset:512\n\t" \
  "ds_read_b128 v[132:135], v208 offset:528\n\t" \
  "ds_read_b128 v[136:139], v208 offset:768\n\t" \
  "ds_read_b128 v[140:143], v208 offset:784\n\t" \
  FSTP("s_memrealtime s[94:95]\n\t") \
  FSTP("s_memtime s[92:93]\n\t") \
  "v_lshlrev_b32 v176, 16, v16\n\t" \
  "v_and_b32 v177, s98, v16\n\t" \
  "v_lshlrev_b32 v178, 16, v17\n\t" \
  "v_and_b32 v179, s98, v17\n\t" \
  "v_lshlrev_b32 v180, 16, v18\n\t" \
  "v_and_b32 v181, s98, v18\n\t" \
  "v_lshlrev_b32 v182, 16, v19\n\t" \
  "v_and_b32 v183, s98, v19\n\t" \
  "v_pk_fma_f32 v[176:177], v[176:177], v[160:161], v[162:163] op_sel_hi:[1,0,0]\n\t" \
  "v_pk_fma_f32 v[178:179], v[178:179], v[160:161], v[162:163] op_sel_hi:[1,0,0]\n\t" \
  "v_pk_fma_f32 v[180:181], v[180:181], v[160:161], v[162:163] op_sel_hi:[1,0,0]\n\t" \
  "v_pk_fma_f32 v[182:183], v[182:183], v[160:161], v[162:163] op_sel_hi:[1,0,0]\n\t" \
  "s_waitcnt lgkmcnt(8)\n\t" \
  "s_nop 0\n\t" \
  "v_pk_fma_f32 v[176:177], v[176:177], v[144:145], v[152:153]\n\t" \
  "v_pk_fma_f32 v[178:179], v[178:179], v[146:147], v[154:155]\n\t" \
  "v_pk_fma_f32 v[180:181], v[180:181], v[148:149], v[156:157]\n\t" \
  "v_pk_fma_f32 v[182:183], v[182:183], v[150:151], v[158:159]\n\t" \
  "v_cvt_pk_bf16_f32 v16, v176, v177\n\t" \
  "v_cvt_pk_bf16_f32 v17, v178, v179\n\t" \
  "v_cvt_pk_bf16_f32 v18, v180, v181\n\t" \
  "v_cvt_pk_bf16_f32 v19, v182, v183\n\t" \
  "s_add_u32 m0, %[mx], 98304\n\t" \
  "s_xor_b32 s96, %[c17], 0\n\t" \
  ".p2align 3\n\t" \
  "s_add_u32 s96, s96, %[sl2]\n\t" \
  "buffer_load_dwordx4 v212, %[sx], s96 offen lds\n\t" \
  ".p2align 3\n\t" \
  "v_lshlrev_b32 v176, 16, v20\n\t" \
  "v_and_b32 v177, s98, v20\n\t" \
  "v_lshlrev_b32 v178, 16, v21\n\t" \
  "v_and_b32 v179, s98, v21\n\t" \
  "v_lshlrev_b32 v180, 16, v22\n\t" \
  "v_and_b32 v181, s98, v22\n\t" \
  "v_lshlrev_b32 v182, 16, v23\n\t" \
  "v_and_b32 v183, s98, v23\n\t" \
  "v_pk_fma_f32 v[176:177], v[176:177], v[164:165], v[166:167] op_sel_hi:[1,0,0]\n\t" \
  "v_pk_fma_f32 v[178:179], v[178:179], v[164:165], v[166:167] op_sel_hi:[1,0,0]\n\t" \
  "v_pk_fma_f32 v[180:181], v[180:181], v[164:165], v[166:167] op_sel_hi:[1,0,0]\n\t" \
  "v_pk_fma_f32 v[182:183], v[182:183], v[164:165], v[166:167] op_sel_hi:[1,0,0]\n\t" \
  "v_pk_fma_f32 v[176:177], v[176:177], v[144:145], v[152:153]\n\t" \
  "v_pk_fma_f32 v[178:179], v[178:179], v[146:147], v[154:155]\n\t" \
  "v_pk_fma_f32 v[180:181], v[180:181], v[148:149], v[156:157]\n\t" \
  "v_pk_fma_f32 v[182:183], v[182:183], v[150:151], v[158:159]\n\t" \
  "v_cvt_pk_bf16_f32 v20, v176, v177\n\t" \
  "v_cvt_pk_bf16_f32 v21, v178, v179\n\t" \
  "v_cvt_pk_bf16_f32 v22, v180, v181\n\t" \
  "v_cvt_pk_bf16_f32 v23, v182, v183\n\t" \
  "buffer_load_dwordx4 v213, %[sx], s96 offen offset:1024 lds\n\t" \
  ".p2align 3\n\t" \
  "v_lshlrev_b32 v176, 16, v24\n\t" \
  "v_and_b32 v177, s98, v24\n\t" \
  "v_lshlrev_b32 v178, 16, v25\n\t" \
  "v_and_b32 v179, s98, v25\n\t" \
  "v_lshlrev_b32 v180, 16, v26\n\t" \
  "v_and_b32 v181, s98, v26\n\t" \
  "v_lshlrev_b32 v182, 16, v27\n\t" \
  "v_and_b32 v183, s98, v27\n\t" \
  "v_pk_fma_f32 v[176:177], v[176:177], v[168:169], v[170:171] op_sel_hi:[1,0,0]\n\t" \
  "v_pk_fma_f32 v[178:179], v[178:179], v[168:169], v[170:171] op_sel_hi:[1,0,0]\n\t" \
  "v_pk_fma_f32 v[180:181], v[180:181], v[168:169], v[170:171] op_sel_hi:[1,0,0]\n\t" \
  "v_pk_fma_f32 v[182:183], v[182:183], v[168:169], v[170:171] op_sel_hi:[1,0,0]\n\t" \
  "v_pk_fma_f32 v[176:177], v[176:177], v[144:145], v[152:153]\n\t" \
  "v_pk_fma_f32 v[178:179], v[178:179], v[146:147], v[154:155]\n\t" \
  "v_pk_fma_f32 v[180:181], v[180:181], v[148:149], v[156:157]\n\t" \
  "v_pk_fma_f32 v[182:183], v[182:183], v[150:151], v[158:159]\n\t" \
  "v_cvt_pk_bf16_f32 v24, v176, v177\n\t" \
  "v_cvt_pk_bf16_f32 v25, v178, v179\n\t" \
  "v_cvt_pk_bf16_f32 v26, v180, v181\n\t" \
  "v_cvt_pk_bf16_f32 v27, v182, v183\n\t" \
  "s_add_u32 m0, %[mx], 100352\n\t" \
  "s_xor_b32 s96, %[c17], 131072\n\t" \
  ".p2align 3\n\t" \
  "s_add_u32 s96, s96, %[sl2]\n\t" \
  "buffer_load_dwordx4 v212, %[sx], s96 offen lds\n\t" \
  ".p2align 3\n\t" \
  "v_lshlrev_b32 v176, 16, v28\n\t" \
  "v_and_b32 v177, s98, v28\n\t" \
  "v_lshlrev_b32 v178, 16, v29\n\t" \
  "v_and_b32 v179, s98, v29\n\t" \
  "v_lshlrev_b32 v180, 16, v30\n\t" \
  "v_and_b32 v181, s98, v30\n\t" \
  "v_lshlrev_b32 v182, 16, v31\n\t" \
  "v_and_b32 v183, s98, v31\n\t" \
  "v_pk_fma_f32 v[176:177], v[176:177], v[172:173], v[174:175] op_sel_hi:[1,0,0]\n\t" \
  "v_pk_fma_f32 v[178:179], v[178:179], v[172:173], v[174:175] op_sel_hi:[1,0,0]\n\t" \
  "v_pk_fma_f32 v[180:181], v[180:181], v[172:173], v[174:175] op_sel_hi:[1,0,0]\n\t" \
  "v_pk_fma_f32 v[182:183], v[182:183], v[172:173], v[174:175] op_sel_hi:[1,0,0]\n\t" \
  "v_pk_fma_f32 v[176:177], v[176:177], v[144:145], v[152:153]\n\t" \
  "v_pk_fma_f32 v[178:179], v[178:179], v[146:147], v[154:155]\n\t" \
  "v_pk_fma_f32 v[180:181], v[180:181], v[148:149], v[156:157]\n\t" \
  "v_pk_fma_f32 v[182:183], v[182:183], v[150:151], v[158:159]\n\t" \
  "v_cvt_pk_bf16_f32 v28, v176, v177\n\t" \
  "v_cvt_pk_bf16_f32 v29, v178, v179\n\t" \
  "v_cvt_pk_bf16_f32 v30, v180, v181\n\t" \
  "v_cvt_pk_bf16_f32 v31, v182, v183\n\t" \
  "buffer_load_dwordx4 v213, %[sx], s96 offen offset:1024 lds\n\t" \
  ".p2align 3\n\t" \
  "ds_read_b128 v[144:147], v208 offset:640\n\t" \
  "ds_read_b128 v[148:151], v208 offset:656\n\t" \
  "ds_read_b128 v[152:155], v208 offset:896\n\t" \
  "ds_read_b128 v[156:159], v208 offset:912\n\t" \
  FSTP("v_writelane_b32 %[stv], s94, 28\n\t") \
  FSTP("v_writelane_b32 %[stv], s92, 29\n\t") \
  FSTP("s_memrealtime s[94:95]\n\t") \
  FSTP("s_memtime s[92:93]\n\t") \
  "v_lshlrev_b32 v176, 16, v32\n\t" \
  "v_and_b32 v177, s98, v32\n\t" \
  "v_lshlrev_b32 v178, 16, v33\n\t" \
  "v_and_b32 v179, s98, v33\n\t" \
  "v_lshlrev_b32 v180, 16, v34\n\t" \
  "v_and_b32 v181, s98, v34\n\t" \
  "v_lshlrev_b32 v182, 16, v35\n\t" \
  "v_and_b32 v183, s98, v35\n\t" \
  "v_pk_fma_f32 v[176:177], v[176:177], v[160:161], v[162:163] op_sel_hi:[1,0,0]\n\t" \
  "v_pk_fma_f32 v[178:179], v[178:179], v[160:161], v[162:163] op_sel_hi:[1,0,0]\n\t" \
  "v_pk_fma_f32 v[180:181], v[180:181], v[160:161], v[162:163] op_sel_hi:[1,0,0]\n\t" \
  "v_pk_fma_f32 v[182:183], v[182:183], v[160:161], v[162:163] op_sel_hi:[1,0,0]\n\t" \
  "s_waitcnt lgkmcnt(4)\n\t" \
  "s_nop 0\n\t" \
  "v_pk_fma_f32 v[176:177], v[176:177], v[128:129], v[136:137]\n\t" \
  "v_pk_fma_f32 v[178:179], v[178:179], v[130:131], v[138:139]\n\t" \
  "v_pk_fma_f32 v[180:181], v[180:181], v[132:133], v[140:141]\n\t" \
  "v_pk_fma_f32 v[182:183], v[182:183], v[134:135], v[142:143]\n\t" \
  "v_cvt_pk_bf16_f32 v32, v176, v177\n\t" \
  "v_cvt_pk_bf16_f32 v33, v178, v179\n\t" \
  "v_cvt_pk_bf16_f32 v34, v180, v181\n\t" \
  "v_cvt_pk_bf16_f32 v35, v182, v183\n\t" \
  "s_add_u32 m0, %[mx], 102400\n\t" \
  "s_xor_b32 s96, %[c17], 262144\n\t" \
  ".p2align 3\n\t" \
  "s_add_u32 s96, s96, %[sl2]\n\t" \
  "buffer_load_dwordx4 v212, %[sx], s96 offen lds\n\t" \
  ".p2align 3\n\t" \
  "v_lshlrev_b32 v176, 16, v36\n\t" \
  "v_and_b32 v177, s98, v36\n\t" \
  "v_lshlrev_b32 v178, 16, v37\n\t" \
  "v_and_b32 v179, s98, v37\n\t" \
  "v_lshlrev_b32 v180, 16, v38\n\t" \
  "v_and_b32 v181, s98, v38\n\t" \
  "v_lshlrev_b32 v182, 16, v39\n\t" \
  "v_and_b32 v183, s98, v39\n\t" \
  "v_pk_fma_f32 v[176:177], v[176:177], v[164:165], v[166:167] op_sel_hi:[1,0,0]\n\t" \
  "v_pk_fma_f32 v[178:179], v[178:179], v[164:165], v[166:167] op_sel_hi:[1,0,0]\n\t" \
  "v_pk_fma_f32 v[180:181], v[180:181], v[164:165], v[166:167] op_sel_hi:[1,0,0]\n\t" \
  "v_pk_fma_f32 v[182:183], v[182:183], v[164:165], v[166:167] op_sel_hi:[1,0,0]\n\t" \
  "v_pk_fma_f32 v[176:177], v[176:177], v[128:129], v[136:137]\n\t" \
  "v_pk_fma_f32 v[178:179], v[178:179], v[130:131], v[138:139]\n\t" \
  "v_pk_fma_f32 v[180:181], v[180:181], v[132:133], v[140:141]\n\t" \
  "v_pk_fma_f32 v[182:183], v[182:183], v[134:135], v[142:143]\n\t" \
  "v_cvt_pk_bf16_f32 v36, v176, v177\n\t" \
  "v_cvt_pk_bf16_f32 v37, v178, v179\n\t" \
  "v_cvt_pk_bf16_f32 v38, v180, v181\n\t" \
  "v_cvt_pk_bf16_f32 v39, v182, v183\n\t" \
  "buffer_load_dwordx4 v213, %[sx], s96 offen offset:1024 lds\n\t" \
  ".p2align 3\n\t" \
  "v_lshlrev_b32 v176, 16, v40\n\t" \
  "v_and_b32 v177, s98, v40\n\t" \
  "v_lshlrev_b32 v178, 16, v41\n\t" \
  "v_and_b32 v179, s98, v41\n\t" \
  "v_lshlrev_b32 v180, 16, v42\n\t" \
  "v_and_b32 v181, s98, v42\n\t" \
  "v_lshlrev_b32 v182, 16, v43\n\t" \
  "v_and_b32 v183, s98, v43\n\t" \
  "v_pk_fma_f32 v[176:177], v[176:177], v[168:169], v[170:171] op_sel_hi:[1,0,0]\n\t" \
  "v_pk_fma_f32 v[178:179], v[178:179], v[168:169], v[170:171] op_sel_hi:[1,0,0]\n\t" \
  "v_pk_fma_f32 v[180:181], v[180:181], v[168:169], v[170:171] op_sel_hi:[1,0,0]\n\t" \
  "v_pk_fma_f32 v[182:183], v[182:183], v[168:169], v[170:171] op_sel_hi:[1,0,0]\n\t" \
  "v_pk_fma_f32 v[176:177], v[176:177], v[128:129], v[136:137]\n\t" \
  "v_pk_fma_f32 v[178:179], v[178:179], v[130:131], v[138:139]\n\t" \
  "v_pk_fma_f32 v[180:181], v[180:181], v[132:133], v[140:141]\n\t" \
  "v_pk_fma_f32 v[182:183], v[182:183], v[134:135], v[142:143]\n\t" \
  "v_cvt_pk_bf16_f32 v40, v176, v177\n\t" \
  "v_cvt_pk_bf16_f32 v41, v178, v179\n\t" \
  "v_cvt_pk_bf16_f32 v42, v180, v181\n\t" \
  "v_cvt_pk_bf16_f32 v43, v182, v183\n\t" \
  "s_add_u32 m0, %[mx], 104448\n\t" \
  "s_xor_b32 s96, %[c17], 393216\n\t" \
  ".p2align 3\n\t" \
  "s_add_u32 s96, s96, %[sl2]\n\t" \
  "buffer_load_dwordx4 v212, %[sx], s96 offen lds\n\t" \
  ".p2align 3\n\t" \
  "v_lshlrev_b32 v176, 16, v44\n\t" \
  "v_and_b32 v177, s98, v44\n\t" \
  "v_lshlrev_b32 v178, 16, v45\n\t" \
  "v_and_b32 v179, s98, v45\n\t" \
  "v_lshlrev_b32 v180, 16, v46\n\t" \
  "v_and_b32 v181, s98, v46\n\t" \
  "v_lshlrev_b32 v182, 16, v47\n\t" \
  "v_and_b32 v183, s98, v47\n\t" \
  "v_pk_fma_f32 v[176:177], v[176:177], v[172:173], v[174:175] op_sel_hi:[1,0,0]\n\t" \
  "v_pk_fma_f32 v[178:179], v[178:179], v[172:173], v[174:175] op_sel_hi:[1,0,0]\n\t" \
  "v_pk_fma_f32 v[180:181], v[180:181], v[172:173], v[174:175] op_sel_hi:[1,0,0]\n\t" \
  "v_pk_fma_f32 v[182:183], v[182:183], v[172:173], v[174:175] op_sel_hi:[1,0,0]\n\t" \
  "v_pk_fma_f32 v[176:177], v[176:177], v[128:129], v[136:137]\n\t" \
  "v_pk_fma_f32 v[178:179], v[178:179], v[130:131], v[138:139]\n\t" \
  "v_pk_fma_f32 v[180:181], v[180:181], v[132:133], v[140:141]\n\t" \
  "v_pk_fma_f32 v[182:183], v[182:183], v[134:135], v[142:143]\n\t" \
  "v_cvt_pk_bf16_f32 v44, v176, v177\n\t" \
  "v_cvt_pk_bf16_f32 v45, v178, v179\n\t" \
  "v_cvt_pk_bf16_f32 v46, v180, v181\n\t" \
  "v_cvt_pk_bf16_f32 v47, v182, v183\n\t" \
  "buffer_load_dwordx4 v213, %[sx], s96 offen offset:1024 lds\n\t" \
  ".p2align 3\n\t" \
  "ds_read_b128 v[128:131], v208 offset:1024\n\t" \
  "ds_read_b128 v[132:135], v208 offset:1040\n\t" \
  "ds_read_b128 v[136:139], v208 offset:1280\n\t" \
  "ds_read_b128 v[140:143], v208 offset:1296\n\t" \
  FSTP("v_writelane_b32 %[stv], s94, 30\n\t") \
  FSTP("v_writelane_b32 %[stv], s92, 31\n\t") \
  FSTP("s_memrealtime s[94:95]\n\t") \
  FSTP("s_memtime s[92:93]\n\t") \
  "v_lshlrev_b32 v176, 16, v48\n\t" \
  "v_and_b32 v177, s98, v48\n\t" \
  "v_lshlrev_b32 v178, 16, v49\n\t" \
  "v_and_b32 v179, s98, v49\n\t" \
  "v_lshlrev_b32 v180, 16, v50\n\t" \
  "v_and_b32 v181, s98, v50\n\t" \
  "v_lshlrev_b32 v182, 16, v51\n\t" \
  "v_and_b32 v183, s98, v51\n\t" \
  "v_pk_fma_f32 v[176:177], v[176:177], v[160:161], v[162:163] op_sel_hi:[1,0,0]\n\t" \
  "v_pk_fma_f32 v[178:179], v[178:179], v[160:161], v[162:163] op_sel_hi:[1,0,0]\n\t" \
  "v_pk_fma_f32 v[180:181], v[180:181], v[160:161], v[162:163] op_sel_hi:[1,0,0]\n\t" \
  "v_pk_fma_f32 v[182:183], v[182:183], v[160:161], v[162:163] op_sel_hi:[1,0,0]\n\t" \
  "s_waitcnt lgkmcnt(4)\n\t" \
  "s_nop 0\n\t" \
  "v_pk_fma_f32 v[176:177], v[176:177], v[144:145], v[152:153]\n\t" \
  "v_pk_fma_f32 v[178:179], v[178:179], v[146:147], v[154:155]\n\t" \
  "v_pk_fma_f32 v[180:181], v[180:181], v[148:149], v[156:157]\n\t" \
  "v_pk_fma_f32 v[182:183], v[182:183], v[150:151], v[158:159]\n\t" \
  "v_cvt_pk_bf16_f32 v48, v176, v177\n\t" \
  "v_cvt_pk_bf16_f32 v49, v178, v179\n\t" \
  "v_cvt_pk_bf16_f32 v50, v180, v181\n\t" \
  "v_cvt_pk_bf16_f32 v51, v182, v183\n\t" \
  "s_add_u32 m0, %[mw], 49152\n\t" \
  "s_mov_b32 s96, %[w1]\n\t" \
  ".p2align 3\n\t" \
  "buffer_load_dwordx4 v200, %[sw], s96 offen offset:0 lds\n\t" \
  ".p2align 3\n\t" \
  "ds_read_b64_tr_b16 a[128:129], v184 offset:0\n\t" \
  "ds_read_b64_tr_b16 a[130:131], v184 offset:1024\n\t" \
  "ds_read_b64_tr_b16 a[132:133], v185 offset:0\n\t" \
  "ds_read_b64_tr_b16 a[134:135], v185 offset:1024\n\t" \
  "v_lshlrev_b32 v176, 16, v52\n\t" \
  "v_and_b32 v177, s98, v52\n\t" \
  "v_lshlrev_b32 v178, 16, v53\n\t" \
  "v_and_b32 v179, s98, v53\n\t" \
  "v_lshlrev_b32 v180, 16, v54\n\t" \
  "v_and_b32 v181, s98, v54\n\t" \
  "v_lshlrev_b32 v182, 16, v55\n\t" \
  "v_and_b32 v183, s98, v55\n\t" \
  "v_pk_fma_f32 v[176:177], v[176:177], v[164:165], v[166:167] op_sel_hi:[1,0,0]\n\t" \
  "v_pk_fma_f32 v[178:179], v[178:179], v[164:165], v[166:167] op_sel_hi:[1,0,0]\n\t" \
  "v_pk_fma_f32 v[180:181], v[180:181], v[164:165], v[166:167] op_sel_hi:[1,0,0]\n\t" \
  "v_pk_fma_f32 v[182:183], v[182:183], v[164:165], v[166:167] op_sel_hi:[1,0,0]\n\t" \
  "v_pk_fma_f32 v[176:177], v[176:177], v[144:145], v[152:153]\n\t" \
  "v_pk_fma_f32 v[178:179], v[178:179], v[146:147], v[154:155]\n\t" \
  "v_pk_fma_f32 v[180:181], v[180:181], v[148:149], v[156:157]\n\t" \
  "v_pk_fma_f32 v[182:183], v[182:183], v[150:151], v[158:159]\n\t" \
  "v_cvt_pk_bf16_f32 v52, v176, v177\n\t" \
  "v_cvt_pk_bf16_f32 v53, v178, v179\n\t" \
  "v_cvt_pk_bf16_f32 v54, v180, v181\n\t" \
  "v_cvt_pk_bf16_f32 v55, v182, v183\n\t" \
  "buffer_load_dwordx4 v201, %[sw], s96 offen offset:1024 lds\n\t" \
  ".p2align 3\n\t" \
  "ds_read_b64_tr_b16 a[136:137], v186 offset:0\n\t" \
  "ds_read_b64_tr_b16 a[138:139], v186 offset:1024\n\t" \
  "ds_read_b64_tr_b16 a[140:141], v187 offset:0\n\t" \
  "ds_read_b64_tr_b16 a[142:143], v187 offset:1024\n\t" \
  "v_lshlrev_b32 v176, 16, v56\n\t" \
  "v_and_b32 v177, s98, v56\n\t" \
  "v_lshlrev_b32 v178, 16, v57\n\t" \
  "v_and_b32 v179, s98, v57\n\t" \
  "v_lshlrev_b32 v180, 16, v58\n\t" \
  "v_and_b32 v181, s98, v58\n\t" \
  "v_lshlrev_b32 v182, 16, v59\n\t" \
  "v_and_b32 v183, s98, v59\n\t" \
  "v_pk_fma_f32 v[176:177], v[176:177], v[168:169], v[170:171] op_sel_hi:[1,0,0]\n\t" \
  "v_pk_fma_f32 v[178:179], v[178:179], v[168:169], v[170:171] op_sel_hi:[1,0,0]\n\t" \
  "v_pk_fma_f32 v[180:181], v[180:181], v[168:169], v[170:171] op_sel_hi:[1,0,0]\n\t" \
  "v_pk_fma_f32 v[182:183], v[182:183], v[168:169], v[170:171] op_sel_hi:[1,0,0]\n\t" \
  "v_pk_fma_f32 v[176:177], v[176:177], v[144:145], v[152:153]\n\t" \
  "v_pk_fma_f32 v[178:179], v[178:179], v[146:147], v[154:155]\n\t" \
  "v_pk_fma_f32 v[180:181], v[180:181], v[148:149], v[156:157]\n\t" \
  "v_pk_fma_f32 v[182:183], v[182:183], v[150:151], v[158:159]\n\t" \
  "v_cvt_pk_bf16_f32 v56, v176, v177\n\t" \
  "v_cvt_pk_bf16_f32 v57, v178, v179\n\t" \
  "v_cvt_pk_bf16_f32 v58, v180, v181\n\t" \
  "v_cvt_pk_bf16_f32 v59, v182, v183\n\t" \
  "buffer_load_dwordx4 v202, %[sw], s96 offen offset:2048 lds\n\t" \
  ".p2align 3\n\t" \
  "ds_read_b64_tr_b16 a[144:145], v188 offset:0\n\t" \
  "ds_read_b64_tr_b16 a[146:147], v188 offset:1024\n\t" \
  "ds_read_b64_tr_b16 a[148:149], v189 offset:0\n\t" \
  "ds_read_b64_tr_b16 a[150:151], v189 offset:1024\n\t" \
  "v_lshlrev_b32 v176, 16, v60\n\t" \
  "v_and_b32 v177, s98, v60\n\t" \
  "v_lshlrev_b32 v178, 16, v61\n\t" \
  "v_and_b32 v179, s98, v61\n\t" \
  "v_lshlrev_b32 v180, 16, v62\n\t" \
  "v_and_b32 v181, s98, v62\n\t" \
  "v_lshlrev_b32 v182, 16, v63\n\t" \
  "v_and_b32 v183, s98, v63\n\t" \
  "v_pk_fma_f32 v[176:177], v[176:177], v[172:173], v[174:175] op_sel_hi:[1,0,0]\n\t" \
  "v_pk_fma_f32 v[178:179], v[178:179], v[172:173], v[174:175] op_sel_hi:[1,0,0]\n\t" \
  "v_pk_fma_f32 v[180:181], v[180:181], v[172:173], v[174:175] op_sel_hi:[1,0,0]\n\t" \
  "v_pk_fma_f32 v[182:183], v[182:183], v[172:173], v[174:175] op_sel_hi:[1,0,0]\n\t" \
  "v_pk_fma_f32 v[176:177], v[176:177], v[144:145], v[152:153]\n\t" \
  "v_pk_fma_f32 v[178:179], v[178:179], v[146:147], v[154:155]\n\t" \
  "v_pk_fma_f32 v[180:181], v[180:181], v[148:149], v[156:157]\n\t" \
  "v_pk_fma_f32 v[182:183], v[182:183], v[150:151], v[158:159]\n\t" \
  "v_cvt_pk_bf16_f32 v60, v176, v177\n\t" \
  "v_cvt_pk_bf16_f32 v61, v178, v179\n\t" \
  "v_cvt_pk_bf16_f32 v62, v180, v181\n\t" \
  "v_cvt_pk_bf16_f32 v63, v182, v183\n\t" \
  "buffer_load_dwordx4 v203, %[sw], s96 offen offset:3072 lds\n\t" \
  ".p2align 3\n\t" \
  "ds_read_b64_tr_b16 a[152:153], v190 offset:0\n\t" \
  "ds_read_b64_tr_b16 a[154:155], v190 offset:1024\n\t" \
  "ds_read_b64_tr_b16 a[156:157], v191 offset:0\n\t" \
  "ds_read_b64_tr_b16 a[158:159], v191 offset:1024\n\t" \
  "ds_read_b128 v[144:147], v208 offset:1152\n\t" \
  "ds_read_b128 v[148:151], v208 offset:1168\n\t" \
  "ds_read_b128 v[152:155], v208 offset:1408\n\t" \
  "ds_read_b128 v[156:159], v208 offset:1424\n\t" \
  FSTP("v_writelane_b32 %[stv], s94, 32\n\t") \
  FSTP("v_writelane_b32 %[stv], s92, 33\n\t") \
  FSTP("s_memrealtime s[94:95]\n\t") \
  FSTP("s_memtime s[92:93]\n\t") \
  "s_waitcnt vmcnt(12)\n\t" \
  "s_nop 0\n\t" \
  "s_mov_b64 exec, 1\n\t" \
  "buffer_store_dword v211, off, %[ws], %[fw] sc1\n\t" \
  "s_mov_b64 exec, -1\n\t" \
  FSTP("v_writelane_b32 %[stv], s94, 34\n\t") \
  FSTP("v_writelane_b32 %[stv], s92, 35\n\t") \
  FSTP("s_memrealtime s[94:95]\n\t") \
  FSTP("s_memtime s[92:93]\n\t") \
  "s_waitcnt lgkmcnt(4)\n\t" \
  "v_mfma_f32_16x16x32_bf16 a[0:3], a[128:131], v[0:3], 0\n\t" \
  "s_add_u32 m0, %[mw], 98304\n\t" \
  ".Lhs0:\n\ts_cbranch_execz .Lhs0n\n\t" \
  "ds_read_b64_tr_b16 a[160:161], v184 offset:8192\n\t" \
  "v_mfma_f32_16x16x32_bf16 a[4:7], a[128:131], v[4:7], 0\n\t" \
  "s_mov_b32 s96, %[w2]\n\t" \
  "ds_read_b64_tr_b16 a[162:163], v184 offset:9216\n\t" \
  "v_mfma_f32_16x16x32_bf16 a[8:11], a[128:131], v[8:11], 0\n\t" \
  "buffer_load_dwordx4 v200, %[sw], s96 offen offset:0 lds\n\t" \
  "v_mfma_f32_16x16x32_bf16 a[12:15], a[128:131], v[12:15], 0\n\t" \
  "buffer_load_dwordx4 v201, %[sw], s96 offen offset:1024 lds\n\t" \
  "v_mfma_f32_16x16x32_bf16 a[28:31], a[132:135], v[12:15], 0\n\t" \
  "ds_read_b64_tr_b16 a[164:165], v185 offset:8192\n\t" \
  "v_mfma_f32_16x16x32_bf16 a[24:27], a[132:135], v[8:11], 0\n\t" \
  "ds_read_b64_tr_b16 a[166:167], v185 offset:9216\n\t" \
  "v_mfma_f32_16x16x32_bf16 a[20:23], a[132:135], v[4:7], 0\n\t" \
  "buffer_load_dwordx4 v202, %[sw], s96 offen offset:2048 lds\n\t" \
  "v_mfma_f32_16x16x32_bf16 a[16:19], a[132:135], v[0:3], 0\n\t" \
  "ds_read_b64_tr_b16 a[168:169], v186 offset:8192\n\t" \
  "v_mfma_f32_16x16x32_bf16 a[32:35], a[136:139], v[0:3], 0\n\t" \
  "s_waitcnt vmcnt(8)\n\t" \
  "ds_read_b64_tr_b16 a[170:171], v186 offset:9216\n\t" \
  "ds_read_b128 v[64:67], v206 offset:0\n\t" \
  "v_mfma_f32_16x16x32_bf16 a[36:39], a[136:139], v[4:7], 0\n\t" \
  "buffer_load_dwordx4 v203, %[sw], s96 offen offset:3072 lds\n\t" \
  "v_mfma_f32_16x16x32_bf16 a[40:43], a[136:139], v[8:11], 0\n\t" \
  "s_add_u32 m0, %[mx], 0\n\t" \
  "ds_read_b64_tr_b16 a[172:173], v187 offset:8192\n\t" \
  "ds_read_b128 v[68:71], v206 offset:2048\n\t" \
  "v_mfma_f32_16x16x32_bf16 a[44:47], a[136:139], v[12:15], 0\n\t" \
  "s_xor_b32 s96, %[c17], 0\n\t" \
  "ds_read_b64_tr_b16 a[174:175], v187 offset:9216\n\t" \
  "ds_read_b128 v[72:75], v206 offset:4096\n\t" \
  "v_mfma_f32_16x16x32_bf16 a[60:63], a[140:143], v[12:15], 0\n\t" \
  "s_add_u32 s96, s96, %[sl3]\n\t" \
  "ds_read_b64_tr_b16 a[176:177], v188 offset:8192\n\t" \
  "ds_read_b128 v[76:79], v206 offset:6144\n\t" \
  "s_waitcnt lgkmcnt(0)\n\t" \
  "v_lshlrev_b32 v176, 16, v64\n\t" \
  "v_and_b32 v177, s98, v64\n\t" \
  "v_mfma_f32_16x16x32_bf16 a[56:59], a[140:143], v[8:11], 0\n\t" \
  "buffer_load_dwordx4 v212, %[sx], s96 offen lds\n\t" \
  "v_lshlrev_b32 v178, 16, v65\n\t" \
  "v_and_b32 v179, s98, v65\n\t" \
  "v_lshlrev_b32 v180, 16, v66\n\t" \
  "v_mfma_f32_16x16x32_bf16 a[52:55], a[140:143], v[4:7], 0\n\t" \
  "ds_read_b64_tr_b16 a[178:179], v188 offset:9216\n\t" \
  "v_and_b32 v181, s98, v66\n\t" \
  "v_lshlrev_b32 v182, 16, v67\n\t" \
  "v_and_b32 v183, s98, v67\n\t" \
  "v_fma_f32 v176, v176, v160, v162\n\t" \
  "v_mfma_f32_16x16x32_bf16 a[48:51], a[140:143], v[0:3], 0\n\t" \
  "buffer_load_dwordx4 v213, %[sx], s96 offen offset:1024 lds\n\t" \
  "v_fma_f32 v177, v177, v160, v162\n\t" \
  "v_fma_f32 v178, v178, v160, v162\n\t" \
  "v_fma_f32 v179, v179, v160, v162\n\t" \
  "v_mfma_f32_16x16x32_bf16 a[64:67], a[144:147], v[0:3], 0\n\t" \
  "s_add_u32 m0, %[mx], 2048\n\t" \
  "ds_read_b64_tr_b16 a[180:181], v189 offset:8192\n\t" \
  "v_fma_f32 v180, v180, v160, v162\n\t" \
  "v_fma_f32 v181, v181, v160, v162\n\t" \
  "v_fma_f32 v182, v182, v160, v162\n\t" \
  "v_fma_f32 v183, v183, v160, v162\n\t" \
  "v_mfma_f32_16x16x32_bf16 a[68:71], a[144:147], v[4:7], 0\n\t" \
  "s_xor_b32 s96, %[c17], 131072\n\t" \
  "ds_read_b64_tr_b16 a[182:183], v189 offset:9216\n\t" \
  "v_fma_f32 v176, v176, v128, v136\n\t" \
  "v_fma_f32 v177, v177, v129, v137\n\t" \
  "v_fma_f32 v178, v178, v130, v138\n\t" \
  "v_fma_f32 v179, v179, v131, v139\n\t" \
  "v_mfma_f32_16x16x32_bf16 a[72:75], a[144:147], v[8:11], 0\n\t" \
  "s_add_u32 s96, s96, %[sl3]\n\t" \
  "buffer_load_dwordx4 v212, %[sx], s96 offen lds\n\t" \
  "v_fma_f32 v180, v180, v132, v140\n\t" \
  "v_fma_f32 v181, v181, v133, v141\n\t" \
  "v_mfma_f32_16x16x32_bf16 a[76:79], a[144:147], v[12:15], 0\n\t" \
  "ds_read_b64_tr_b16 a[184:185], v190 offset:8192\n\t" \
  "v_fma_f32 v182, v182, v134, v142\n\t" \
  "v_fma_f32 v183, v183, v135, v143\n\t" \
  "v_cvt_pk_bf16_f32 v64, v176, v177\n\t" \
  "v_cvt_pk_bf16_f32 v65, v178, v179\n\t" \
  "v_mfma_f32_16x16x32_bf16 a[92:95], a[148:151], v[12:15], 0\n\t" \
  "buffer_load_dwordx4 v213, %[sx], s96 offen offset:1024 lds\n\t" \
  "v_cvt_pk_bf16_f32 v66, v180, v181\n\t" \
  "v_cvt_pk_bf16_f32 v67, v182, v183\n\t" \
  "v_lshlrev_b32 v176, 16, v68\n\t" \
  "v_mfma_f32_16x16x32_bf16 a[88:91], a[148:151], v[8:11], 0\n\t" \
  "s_add_u32 m0, %[mx], 4096\n\t" \
  "ds_read_b64_tr_b16 a[186:187], v190 offset:9216\n\t" \
  "v_and_b32 v177, s98, v68\n\t" \
  "v_lshlrev_b32 v178, 16, v69\n\t" \
  "v_and_b32 v179, s98, v69\n\t" \
  "v_lshlrev_b32 v180, 16, v70\n\t" \
  "v_mfma_f32_16x16x32_bf16 a[84:87], a[148:151], v[4:7], 0\n\t" \
  "s_xor_b32 s96, %[c17], 262144\n\t" \
  "s_add_u32 s96, s96, %[sl3]\n\t" \
  "buffer_load_dwordx4 v212, %[sx], s96 offen lds\n\t" \
  "v_and_b32 v181, s98, v70\n\t" \
  "v_mfma_f32_16x16x32_bf16 a[80:83], a[148:151], v[0:3], 0\n\t" \
  "ds_read_b64_tr_b16 a[188:189], v191 offset:8192\n\t" \
  "v_lshlrev_b32 v182, 16, v71\n\t" \
  "v_and_b32 v183, s98, v71\n\t" \
  "v_fma_f32 v176, v176, v164, v166\n\t" \
  "v_fma_f32 v177, v177, v164, v166\n\t" \
  "v_mfma_f32_16x16x32_bf16 a[96:99], a[152:155], v[0:3], 0\n\t" \
  "buffer_load_dwordx4 v213, %[sx], s96 offen offset:1024 lds\n\t" \
  "v_fma_f32 v178, v178, v164, v166\n\t" \
  "v_fma_f32 v179, v179, v164, v166\n\t" \
  "v_fma_f32 v180, v180, v164, v166\n\t" \
  "v_mfma_f32_16x16x32_bf16 a[100:103], a[152:155], v[4:7], 0\n\t" \
  "s_add_u32 m0, %[mx], 6144\n\t" \
  "ds_read_b64_tr_b16 a[190:191], v191 offset:9216\n\t" \
  "v_fma_f32 v181, v181, v164, v166\n\t" \
  "v_fma_f32 v182, v182, v164, v166\n\t" \
  "v_fma_f32 v183, v183, v164, v166\n\t" \
  "v_fma_f32 v176, v176, v128, v136\n\t" \
  "v_mfma_f32_16x16x32_bf16 a[104:107], a[152:155], v[8:11], 0\n\t" \
  "s_xor_b32 s96, %[c17], 393216\n\t" \
  "s_add_u32 s96, s96, %[sl3]\n\t" \
  "buffer_load_dwordx4 v212, %[sx], s96 offen lds\n\t" \
  "v_fma_f32 v177, v177, v129, v137\n\t" \
  "v_mfma_f32_16x16x32_bf16 a[108:111], a[152:155], v[12:15], 0\n\t" \
  "v_fma_f32 v178, v178, v130, v138\n\t" \
  "v_fma_f32 v179, v179, v131, v139\n\t" \
  "v_fma_f32 v180, v180, v132, v140\n\t" \
  "v_fma_f32 v181, v181, v133, v141\n\t" \
  "v_fma_f32 v182, v182, v134, v142\n\t" \
  "v_mfma_f32_16x16x32_bf16 a[124:127], a[156:159], v[12:15], 0\n\t" \
  "buffer_load_dwordx4 v213, %[sx], s96 offen offset:1024 lds\n\t" \
  "v_fma_f32 v183, v183, v135, v143\n\t" \
  "v_cvt_pk_bf16_f32 v68, v176, v177\n\t" \
  "v_cvt_pk_bf16_f32 v69, v178, v179\n\t" \
  "v_mfma_f32_16x16x32_bf16 a[120:123], a[156:159], v[8:11], 0\n\t" \
  "v_cvt_pk_bf16_f32 v70, v180, v181\n\t" \
  "v_cvt_pk_bf16_f32 v71, v182, v183\n\t" \
  "v_lshlrev_b32 v176, 16, v72\n\t" \
  "v_and_b32 v177, s98, v72\n\t" \
  "v_mfma_f32_16x16x32_bf16 a[116:119], a[156:159], v[4:7], 0\n\t" \
  "v_lshlrev_b32 v178, 16, v73\n\t" \
  "v_and_b32 v179, s98, v73\n\t" \
  "v_lshlrev_b32 v180, 16, v74\n\t" \
  "v_and_b32 v181, s98, v74\n\t" \
  "v_lshlrev_b32 v182, 16, v75\n\t" \
  "v_and_b32 v183, s98, v75\n\t" \
  "v_mfma_f32_16x16x32_bf16 a[112:115], a[156:159], v[0:3], 0\n\t" \
  FSTP("v_writelane_b32 %[stv], s94, 36\n\t") \
  FSTP("v_writelane_b32 %[stv], s92, 37\n\t") \
  FSTP("s_memrealtime s[94:95]\n\t") \
  FSTP("s_memtime s[92:93]\n\t") \
  "s_waitcnt vmcnt(13) lgkmcnt(0)\n\t" \
  "s_barrier\n\t" \
  "v_mfma_f32_16x16x32_bf16 a[0:3], a[160:163], v[16:19], a[0:3]\n\t" \
  "s_add_u32 m0, %[mw], 0\n\t" \
  "ds_read_b64_tr_b16 a[128:129], v184 offset:49152\n\t" \
  "v_fma_f32 v176, v176, v168, v170\n\t" \
  "v_fma_f32 v177, v177, v168, v170\n\t" \
  "v_mfma_f32_16x16x32_bf16 a[4:7], a[160:163], v[20:23], a[4:7]\n\t" \
  "s_mov_b32 s96, %[w3]\n\t" \
  "ds_read_b64_tr_b16 a[130:131], v184 offset:50176\n\t" \
  "v_fma_f32 v178, v178, v168, v170\n\t" \
  "v_fma_f32 v179, v179, v168, v170\n\t" \
  "v_mfma_f32_16x16x32_bf16 a[8:11], a[160:163], v[24:27], a[8:11]\n\t" \
  "ds_read_b64_tr_b16 a[132:133], v185 offset:49152\n\t" \
  "v_fma_f32 v180, v180, v168, v170\n\t" \
  "v_fma_f32 v181, v181, v168, v170\n\t" \
  "v_mfma_f32_16x16x32_bf16 a[12:15], a[160:163], v[28:31], a[12:15]\n\t" \
  "ds_read_b64_tr_b16 a[134:135], v185 offset:50176\n\t" \
  "v_fma_f32 v182, v182, v168, v170\n\t" \
  "v_fma_f32 v183, v183, v168, v170\n\t" \
  "v_mfma_f32_16x16x32_bf16 a[28:31], a[164:167], v[28:31], a[28:31]\n\t" \
  "buffer_load_dwordx4 v200, %[sw], s96 offen offset:0 lds\n\t" \
  "v_fma_f32 v176, v176, v128, v136\n\t" \
  "v_mfma_f32_16x16x32_bf16 a[24:27], a[164:167], v[24:27], a[24:27]\n\t" \
  "ds_read_b64_tr_b16 a[136:137], v186 offset:49152\n\t" \
  "v_fma_f32 v177, v177, v129, v137\n\t" \
  "v_fma_f32 v178, v178, v130, v138\n\t" \
  "v_mfma_f32_16x16x32_bf16 a[20:23], a[164:167], v[20:23], a[20:23]\n\t" \
  "ds_read_b64_tr_b16 a[138:139], v186 offset:50176\n\t" \
  "v_fma_f32 v179, v179, v131, v139\n\t" \
  "v_fma_f32 v180, v180, v132, v140\n\t" \
  "v_mfma_f32_16x16x32_bf16 a[16:19], a[164:167], v[16:19], a[16:19]\n\t" \
  "buffer_load_dwordx4 v201, %[sw], s96 offen offset:1024 lds\n\t" \
  "v_fma_f32 v181, v181, v133, v141\n\t" \
  "v_mfma_f32_16x16x32_bf16 a[32:35], a[168:171], v[16:19], a[32:35]\n\t" \
  "ds_read_b64_tr_b16 a[140:141], v187 offset:49152\n\t" \
  "v_fma_f32 v182, v182, v134, v142\n\t" \
  "v_fma_f32 v183, v183, v135, v143\n\t" \
  "v_mfma_f32_16x16x32_bf16 a[36:39], a[168:171], v[20:23], a[36:39]\n\t" \
  "ds_read_b64_tr_b16 a[142:143], v187 offset:50176\n\t" \
  "v_cvt_pk_bf16_f32 v72, v176, v177\n\t" \
  "v_cvt_pk_bf16_f32 v73, v178, v179\n\t" \
  "v_mfma_f32_16x16x32_bf16 a[40:43], a[168:171], v[24:27], a[40:43]\n\t" \
  "ds_read_b64_tr_b16 a[144:145], v188 offset:49152\n\t" \
  "v_cvt_pk_bf16_f32 v74, v180, v181\n\t" \
  "v_cvt_pk_bf16_f32 v75, v182, v183\n\t" \
  "v_mfma_f32_16x16x32_bf16 a[44:47], a[168:171], v[28:31], a[44:47]\n\t" \
  "buffer_load_dwordx4 v202, %[sw], s96 offen offset:2048 lds\n\t" \
  "v_mfma_f32_16x16x32_bf16 a[60:63], a[172:175], v[28:31], a[60:63]\n\t" \
  "ds_read_b64_tr_b16 a[146:147], v188 offset:50176\n\t" \
  "ds_read_b128 v[80:83], v207 offset:0\n\t" \
  "v_mfma_f32_16x16x32_bf16 a[56:59], a[172:175], v[24:27], a[56:59]\n\t" \
  "ds_read_b64_tr_b16 a[148:149], v189 offset:49152\n\t" \
  "ds_read_b128 v[84:87], v207 offset:2048\n\t" \
  "v_mfma_f32_16x16x32_bf16 a[52:55], a[172:175], v[20:23], a[52:55]\n\t" \
  "ds_read_b64_tr_b16 a[150:151], v189 offset:50176\n\t" \
  "ds_read_b128 v[88:91], v207 offset:4096\n\t" \
  "v_mfma_f32_16x16x32_bf16 a[48:51], a[172:175], v[16:19], a[48:51]\n\t" \
  "ds_read_b64_tr_b16 a[152:153], v190 offset:49152\n\t" \
  "ds_read_b128 v[92:95], v207 offset:6144\n\t" \
  "v_lshlrev_b32 v176, 16, v76\n\t" \
  "v_and_b32 v177, s98, v76\n\t" \
  "v_mfma_f32_16x16x32_bf16 a[64:67], a[176:179], v[16:19], a[64:67]\n\t" \
  "buffer_load_dwordx4 v203, %[sw], s96 offen offset:3072 lds\n\t" \
  "v_lshlrev_b32 v178, 16, v77\n\t" \
  "v_and_b32 v179, s98, v77\n\t" \
  "v_mfma_f32_16x16x32_bf16 a[68:71], a[176:179], v[20:23], a[68:71]\n\t" \
  "ds_read_b64_tr_b16 a[154:155], v190 offset:50176\n\t" \
  "v_lshlrev_b32 v180, 16, v78\n\t" \
  "v_and_b32 v181, s98, v78\n\t" \
  "v_lshlrev_b32 v182, 16, v79\n\t" \
  "v_mfma_f32_16x16x32_bf16 a[72:75], a[176:179], v[24:27], a[72:75]\n\t" \
  "ds_read_b64_tr_b16 a[156:157], v191 offset:49152\n\t" \
  "v_and_b32 v183, s98, v79\n\t" \
  "v_fma_f32 v176, v176, v172, v174\n\t" \
  "v_fma_f32 v177, v177, v172, v174\n\t" \
  "v_mfma_f32_16x16x32_bf16 a[76:79], a[176:179], v[28:31], a[76:79]\n\t" \
  "ds_read_b64_tr_b16 a[158:159], v191 offset:50176\n\t" \
  "v_fma_f32 v178, v178, v172, v174\n\t" \
  "v_fma_f32 v179, v179, v172, v174\n\t" \
  "v_mfma_f32_16x16x32_bf16 a[92:95], a[180:183], v[28:31], a[92:95]\n\t" \
  "v_fma_f32 v180, v180, v172, v174\n\t" \
  "v_fma_f32 v181, v181, v172, v174\n\t" \
  "v_fma_f32 v182, v182, v172, v174\n\t" \
  "v_mfma_f32_16x16x32_bf16 a[88:91], a[180:183], v[24:27], a[88:91]\n\t" \
  "v_fma_f32 v183, v183, v172, v174\n\t" \
  "v_fma_f32 v176, v176, v128, v136\n\t" \
  "v_fma_f32 v177, v177, v129, v137\n\t" \
  "v_mfma_f32_16x16x32_bf16 a[84:87], a[180:183], v[20:23], a[84:87]\n\t" \
  "global_load_dword %[pf], v215, %[wq]\n\t" \
  "v_fma_f32 v178, v178, v130, v138\n\t" \
  "v_mfma_f32_16x16x32_bf16 a[80:83], a[180:183], v[16:19], a[80:83]\n\t" \
  "v_fma_f32 v179, v179, v131, v139\n\t" \
  "v_fma_f32 v180, v180, v132, v140\n\t" \
  "v_fma_f32 v181, v181, v133, v141\n\t" \
  "v_mfma_f32_16x16x32_bf16 a[96:99], a[184:187], v[16:19], a[96:99]\n\t" \
  "v_fma_f32 v182, v182, v134, v142\n\t" \
  "v_fma_f32 v183, v183, v135, v143\n\t" \
  "v_cvt_pk_bf16_f32 v76, v176, v177\n\t" \
  "v_mfma_f32_16x16x32_bf16 a[100:103], a[184:187], v[20:23], a[100:103]\n\t" \
  "v_cvt_pk_bf16_f32 v77, v178, v179\n\t" \
  "v_cvt_pk_bf16_f32 v78, v180, v181\n\t" \
  "v_cvt_pk_bf16_f32 v79, v182, v183\n\t" \
  "v_mfma_f32_16x16x32_bf16 a[104:107], a[184:187], v[24:27], a[104:107]\n\t" \
  "ds_read_b128 v[128:131], v208 offset:1536\n\t" \
  "ds_read_b128 v[132:135], v208 offset:1552\n\t" \
  "v_mfma_f32_16x16x32_bf16 a[108:111], a[184:187], v[28:31], a[108:111]\n\t" \
  "ds_read_b128 v[136:139], v208 offset:1792\n\t" \
  "ds_read_b128 v[140:143], v208 offset:1808\n\t" \
  "s_waitcnt lgkmcnt(7)\n\t" \
  "v_lshlrev_b32 v176, 16, v80\n\t" \
  "v_mfma_f32_16x16x32_bf16 a[124:127], a[188:191], v[28:31], a[124:127]\n\t" \
  "v_and_b32 v177, s98, v80\n\t" \
  "v_lshlrev_b32 v178, 16, v81\n\t" \
  "v_and_b32 v179, s98, v81\n\t" \
  "v_lshlrev_b32 v180, 16, v82\n\t" \
  "v_mfma_f32_16x16x32_bf16 a[120:123], a[188:191], v[24:27], a[120:123]\n\t" \
  "v_and_b32 v181, s98, v82\n\t" \
  "v_lshlrev_b32 v182, 16, v83\n\t" \
  "v_and_b32 v183, s98, v83\n\t" \
  "v_mfma_f32_16x16x32_bf16 a[116:119], a[188:191], v[20:23], a[116:119]\n\t" \
  "v_fma_f32 v176, v176, v160, v162\n\t" \
  "v_fma_f32 v177, v177, v160, v162\n\t" \
  "v_fma_f32 v178, v178, v160, v162\n\t" \
  "v_fma_f32 v179, v179, v160, v162\n\t" \
  "v_mfma_f32_16x16x32_bf16 a[112:115], a[188:191], v[16:19], a[112:115]\n\t" \
  FSTP("v_writelane_b32 %[stv], s94, 38\n\t") \
  FSTP("v_writelane_b32 %[stv], s92, 39\n\t") \
  FSTP("s_memrealtime s[94:95]\n\t") \
  FSTP("s_memtime s[92:93]\n\t") \
  "s_waitcnt lgkmcnt(4)\n\t" \
  "v_mfma_f32_16x16x32_bf16 a[0:3], a[128:131], v[32:35], a[0:3]\n\t" \
  "global_load_dword %[pf], v217, %[kv]\n\t" \
  "v_fma_f32 v180, v180, v160, v162\n\t" \
  "v_mfma_f32_16x16x32_bf16 a[4:7], a[128:131], v[36:39], a[4:7]\n\t" \
  "ds_read_b64_tr_b16 a[160:161], v184 offset:57344\n\t" \
  "v_fma_f32 v181, v181, v160, v162\n\t" \
  "v_fma_f32 v182, v182, v160, v162\n\t" \
  "v_mfma_f32_16x16x32_bf16 a[8:11], a[128:131], v[40:43], a[8:11]\n\t" \
  "ds_read_b64_tr_b16 a[162:163], v184 offset:58368\n\t" \
  "v_fma_f32 v183, v183, v160, v162\n\t" \
  "v_fma_f32 v176, v176, v144, v152\n\t" \
  "v_mfma_f32_16x16x32_bf16 a[12:15], a[128:131], v[44:47], a[12:15]\n\t" \
  "ds_read_b64_tr_b16 a[164:165], v185 offset:57344\n\t" \
  "v_fma_f32 v177, v177, v145, v153\n\t" \
  "v_fma_f32 v178, v178, v146, v154\n\t" \
  "v_mfma_f32_16x16x32_bf16 a[28:31], a[132:135], v[44:47], a[28:31]\n\t" \
  "ds_read_b64_tr_b16 a[166:167], v185 offset:58368\n\t" \
  "v_fma_f32 v179, v179, v147, v155\n\t" \
  "v_fma_f32 v180, v180, v148, v156\n\t" \
  "v_mfma_f32_16x16x32_bf16 a[24:27], a[132:135], v[40:43], a[24:27]\n\t" \
  "ds_read_b64_tr_b16 a[168:169], v186 offset:57344\n\t" \
  "v_fma_f32 v181, v181, v149, v157\n\t" \
  "v_fma_f32 v182, v182, v150, v158\n\t" \
  "v_mfma_f32_16x16x32_bf16 a[20:23], a[132:135], v[36:39], a[20:23]\n\t" \
  "ds_read_b64_tr_b16 a[170:171], v186 offset:58368\n\t" \
  "v_fma_f32 v183, v183, v151, v159\n\t" \
  "v_cvt_pk_bf16_f32 v80, v176, v177\n\t" \
  "v_mfma_f32_16x16x32_bf16 a[16:19], a[132:135], v[32:35], a[16:19]\n\t" \
  "ds_read_b64_tr_b16 a[172:173], v187 offset:57344\n\t" \
  "v_cvt_pk_bf16_f32 v81, v178, v179\n\t" \
  "v_cvt_pk_bf16_f32 v82, v180, v181\n\t" \
  "v_mfma_f32_16x16x32_bf16 a[32:35], a[136:139], v[32:35], a[32:35]\n\t" \
  "ds_read_b64_tr_b16 a[174:175], v187 offset:58368\n\t" \
  "v_cvt_pk_bf16_f32 v83, v182, v183\n\t" \
  "v_lshlrev_b32 v176, 16, v84\n\t" \
  "v_mfma_f32_16x16x32_bf16 a[36:39], a[136:139], v[36:39], a[36:39]\n\t" \
  "ds_read_b64_tr_b16 a[176:177], v188 offset:57344\n\t" \
  "v_and_b32 v177, s98, v84\n\t" \
  "v_lshlrev_b32 v178, 16, v85\n\t" \
  "v_mfma_f32_16x16x32_bf16 a[40:43], a[136:139], v[40:43], a[40:43]\n\t" \
  "ds_read_b64_tr_b16 a[178:179], v188 offset:58368\n\t" \
  "v_and_b32 v179, s98, v85\n\t" \
  "v_lshlrev_b32 v180, 16, v86\n\t" \
  "v_mfma_f32_16x16x32_bf16 a[44:47], a[136:139], v[44:47], a[44:47]\n\t" \
  "ds_read_b64_tr_b16 a[180:181], v189 offset:57344\n\t" \
  "v_and_b32 v181, s98, v86\n\t" \
  "v_lshlrev_b32 v182, 16, v87\n\t" \
  "v_mfma_f32_16x16x32_bf16 a[60:63], a[140:143], v[44:47], a[60:63]\n\t" \
  "ds_read_b64_tr_b16 a[182:183], v189 offset:58368\n\t" \
  "v_and_b32 v183, s98, v87\n\t" \
  "v_fma_f32 v176, v176, v164, v166\n\t" \
  "v_mfma_f32_16x16x32_bf16 a[56:59], a[140:143], v[40:43], a[56:59]\n\t" \
  "ds_read_b64_tr_b16 a[184:185], v190 offset:57344\n\t" \
  "v_fma_f32 v177, v177, v164, v166\n\t" \
  "v_fma_f32 v178, v178, v164, v166\n\t" \
  "v_mfma_f32_16x16x32_bf16 a[52:55], a[140:143], v[36:39], a[52:55]\n\t" \
  "buffer_store_short v218, v219, %[ws], 0 offen sc1\n\t" \
  "v_fma_f32 v179, v179, v164, v166\n\t" \
  "v_mfma_f32_16x16x32_bf16 a[48:51], a[140:143], v[32:35], a[48:51]\n\t" \
  "ds_read_b64_tr_b16 a[186:187], v190 offset:58368\n\t" \
  "v_fma_f32 v180, v180, v164, v166\n\t" \
  "v_fma_f32 v181, v181, v164, v166\n\t" \
  "v_mfma_f32_16x16x32_bf16 a[64:67], a[144:147], v[32:35], a[64:67]\n\t" \
  "ds_read_b64_tr_b16 a[188:189], v191 offset:57344\n\t" \
  "v_fma_f32 v182, v182, v164, v166\n\t" \
  "v_fma_f32 v183, v183, v164, v166\n\t" \
  "v_mfma_f32_16x16x32_bf16 a[68:71], a[144:147], v[36:39], a[68:71]\n\t" \
  "ds_read_b64_tr_b16 a[190:191], v191 offset:58368\n\t" \
  "v_fma_f32 v176, v176, v144, v152\n\t" \
  "v_fma_f32 v177, v177, v145, v153\n\t" \
  "v_mfma_f32_16x16x32_bf16 a[72:75], a[144:147], v[40:43], a[72:75]\n\t" \
  "v_fma_f32 v178, v178, v146, v154\n\t" \
  "v_fma_f32 v179, v179, v147, v155\n\t" \
  "v_fma_f32 v180, v180, v148, v156\n\t" \
  "v_mfma_f32_16x16x32_bf16 a[76:79], a[144:147], v[44:47], a[76:79]\n\t" \
  "v_fma_f32 v181, v181, v149, v157\n\t" \
  "v_fma_f32 v182, v182, v150, v158\n\t" \
  "v_fma_f32 v183, v183, v151, v159\n\t" \
  "v_mfma_f32_16x16x32_bf16 a[92:95], a[148:151], v[44:47], a[92:95]\n\t" \
  "v_cvt_pk_bf16_f32 v84, v176, v177\n\t" \
  "v_cvt_pk_bf16_f32 v85, v178, v179\n\t" \
  "v_cvt_pk_bf16_f32 v86, v180, v181\n\t" \
  "v_mfma_f32_16x16x32_bf16 a[88:91], a[148:151], v[40:43], a[88:91]\n\t" \
  "v_cvt_pk_bf16_f32 v87, v182, v183\n\t" \
  "v_lshlrev_b32 v176, 16, v88\n\t" \
  "v_and_b32 v177, s98, v88\n\t" \
  "v_mfma_f32_16x16x32_bf16 a[84:87], a[148:151], v[36:39], a[84:87]\n\t" \
  "v_lshlrev_b32 v178, 16, v89\n\t" \
  "v_and_b32 v179, s98, v89\n\t" \
  "v_lshlrev_b32 v180, 16, v90\n\t" \
  "v_mfma_f32_16x16x32_bf16 a[80:83], a[148:151], v[32:35], a[80:83]\n\t" \
  "v_and_b32 v181, s98, v90\n\t" \
  "v_lshlrev_b32 v182, 16, v91\n\t" \
  "v_and_b32 v183, s98, v91\n\t" \
  "v_mfma_f32_16x16x32_bf16 a[96:99], a[152:155], v[32:35], a[96:99]\n\t" \
  "v_fma_f32 v176, v176, v168, v170\n\t" \
  "v_fma_f32 v177, v177, v168, v170\n\t" \
  "v_fma_f32 v178, v178, v168, v170\n\t" \
  "v_mfma_f32_16x16x32_bf16 a[100:103], a[152:155], v[36:39], a[100:103]\n\t" \
  "v_fma_f32 v179, v179, v168, v170\n\t" \
  "v_fma_f32 v180, v180, v168, v170\n\t" \
  "v_fma_f32 v181, v181, v168, v170\n\t" \
  "v_mfma_f32_16x16x32_bf16 a[104:107], a[152:155], v[40:43], a[104:107]\n\t" \
  "v_fma_f32 v182, v182, v168, v170\n\t" \
  "v_fma_f32 v183, v183, v168, v170\n\t" \
  "v_mfma_f32_16x16x32_bf16 a[108:111], a[152:155], v[44:47], a[108:111]\n\t" \
  "v_fma_f32 v176, v176, v144, v152\n\t" \
  "v_fma_f32 v177, v177, v145, v153\n\t" \
  "v_mfma_f32_16x16x32_bf16 a[124:127], a[156:159], v[44:47], a[124:127]\n\t" \
  "v_fma_f32 v178, v178, v146, v154\n\t" \
  "v_fma_f32 v179, v179, v147, v155\n\t" \
  "v_mfma_f32_16x16x32_bf16 a[120:123], a[156:159], v[40:43], a[120:123]\n\t" \
  "v_fma_f32 v180, v180, v148, v156\n\t" \
  "v_fma_f32 v181, v181, v149, v157\n\t" \
  "v_mfma_f32_16x16x32_bf16 a[116:119], a[156:159], v[36:39], a[116:119]\n\t" \
  "v_fma_f32 v182, v182, v150, v158\n\t" \
  "v_fma_f32 v183, v183, v151, v159\n\t" \
  "v_mfma_f32_16x16x32_bf16 a[112:115], a[156:159], v[32:35], a[112:115]\n\t" \
  FSTP("v_writelane_b32 %[stv], s94, 40\n\t") \
  FSTP("v_writelane_b32 %[stv], s92, 41\n\t") \
  FSTP("s_memrealtime s[94:95]\n\t") \
  FSTP("s_memtime s[92:93]\n\t") \
  "s_waitcnt vmcnt(15) lgkmcnt(0)\n\t" \
  "s_barrier\n\t" \
  "v_mfma_f32_16x16x32_bf16 a[0:3], a[160:163], v[48:51], a[0:3]\n\t" \
  "buffer_load_dword %[fl], v210, %[ws], %[fo] offen sc1\n\t" \
  "v_cvt_pk_bf16_f32 v88, v176, v177\n\t" \
  "v_cvt_pk_bf16_f32 v89, v178, v179\n\t" \
  "v_mfma_f32_16x16x32_bf16 a[4:7], a[160:163], v[52:55], a[4:7]\n\t" \
  "s_add_u32 s96, %[rot19], 2097152\n\t" \
  "ds_read_b64_tr_b16 a[128:129], v192 offset:0\n\t" \
  "v_cvt_pk_bf16_f32 v90, v180, v181\n\t" \
  "v_cvt_pk_bf16_f32 v91, v182, v183\n\t" \
  "s_waitcnt vmcnt(8)\n\t" \
  "v_mfma_f32_16x16x32_bf16 a[8:11], a[160:163], v[56:59], a[8:11]\n\t" \
  "s_and_b32 s96, s96, 0x780000\n\t" \
  "ds_read_b64_tr_b16 a[130:131], v192 offset:1024\n\t" \
  "ds_read_b128 v[96:99], v204 offset:0\n\t" \
  "v_mfma_f32_16x16x32_bf16 a[12:15], a[160:163], v[60:63], a[12:15]\n\t" \
  "s_add_u32 m0, %[mw], 49152\n\t" \
  "ds_read_b64_tr_b16 a[132:133], v193 offset:0\n\t" \
  "ds_read_b128 v[100:103], v204 offset:2048\n\t" \
  "v_mfma_f32_16x16x32_bf16 a[28:31], a[164:167], v[60:63], a[28:31]\n\t" \
  "buffer_load_dwordx4 v200, %[sw], s96 offen offset:0 lds\n\t" \
  "v_mfma_f32_16x16x32_bf16 a[24:27], a[164:167], v[56:59], a[24:27]\n\t" \
  "ds_read_b64_tr_b16 a[134:135], v193 offset:1024\n\t" \
  "ds_read_b128 v[104:107], v204 offset:4096\n\t" \
  "v_mfma_f32_16x16x32_bf16 a[20:23], a[164:167], v[52:55], a[20:23]\n\t" \
  "ds_read_b64_tr_b16 a[136:137], v194 offset:0\n\t" \
  "ds_read_b128 v[108:111], v204 offset:6144\n\t" \
  "v_lshlrev_b32 v176, 16, v92\n\t" \
  "v_and_b32 v177, s98, v92\n\t" \
  "v_mfma_f32_16x16x32_bf16 a[16:19], a[164:167], v[48:51], a[16:19]\n\t" \
  "ds_read_b64_tr_b16 a[138:139], v194 offset:1024\n\t" \
  "v_lshlrev_b32 v178, 16, v93\n\t" \
  "v_and_b32 v179, s98, v93\n\t" \
  "v_lshlrev_b32 v180, 16, v94\n\t" \
  "v_mfma_f32_16x16x32_bf16 a[32:35], a[168:171], v[48:51], a[32:35]\n\t" \
  "ds_read_b64_tr_b16 a[140:141], v195 offset:0\n\t" \
  "v_and_b32 v181, s98, v94\n\t" \
  "v_lshlrev_b32 v182, 16, v95\n\t" \
  "v_and_b32 v183, s98, v95\n\t" \
  "v_mfma_f32_16x16x32_bf16 a[36:39], a[168:171], v[52:55], a[36:39]\n\t" \
  "buffer_load_dwordx4 v201, %[sw], s96 offen offset:1024 lds\n\t" \
  "v_fma_f32 v176, v176, v172, v174\n\t" \
  "v_fma_f32 v177, v177, v172, v174\n\t" \
  "v_mfma_f32_16x16x32_bf16 a[40:43], a[168:171], v[56:59], a[40:43]\n\t" \
  "ds_read_b64_tr_b16 a[142:143], v195 offset:1024\n\t" \
  "v_fma_f32 v178, v178, v172, v174\n\t" \
  "v_fma_f32 v179, v179, v172, v174\n\t" \
  "v_fma_f32 v180, v180, v172, v174\n\t" \
  "v_mfma_f32_16x16x32_bf16 a[44:47], a[168:171], v[60:63], a[44:47]\n\t" \
  "ds_read_b64_tr_b16 a[144:145], v196 offset:0\n\t" \
  "v_fma_f32 v181, v181, v172, v174\n\t" \
  "v_fma_f32 v182, v182, v172, v174\n\t" \
  "v_fma_f32 v183, v183, v172, v174\n\t" \
  "v_mfma_f32_16x16x32_bf16 a[60:63], a[172:175], v[60:63], a[60:63]\n\t" \
  "ds_read_b64_tr_b16 a[146:147], v196 offset:1024\n\t" \
  "v_fma_f32 v176, v176, v144, v152\n\t" \
  "v_fma_f32 v177, v177, v145, v153\n\t" \
  "v_fma_f32 v178, v178, v146, v154\n\t" \
  "v_mfma_f32_16x16x32_bf16 a[56:59], a[172:175], v[56:59], a[56:59]\n\t" \
  "ds_read_b64_tr_b16 a[148:149], v197 offset:0\n\t" \
  "v_fma_f32 v179, v179, v147, v155\n\t" \
  "v_fma_f32 v180, v180, v148, v156\n\t" \
  "v_fma_f32 v181, v181, v149, v157\n\t" \
  "v_mfma_f32_16x16x32_bf16 a[52:55], a[172:175], v[52:55], a[52:55]\n\t" \
  "buffer_load_dwordx4 v202, %[sw], s96 offen offset:2048 lds\n\t" \
  "v_fma_f32 v182, v182, v150, v158\n\t" \
  "v_mfma_f32_16x16x32_bf16 a[48:51], a[172:175], v[48:51], a[48:51]\n\t" \
  "ds_read_b64_tr_b16 a[150:151], v197 offset:1024\n\t" \
  "v_fma_f32 v183, v183, v151, v159\n\t" \
  "v_cvt_pk_bf16_f32 v92, v176, v177\n\t" \
  "v_mfma_f32_16x16x32_bf16 a[64:67], a[176:179], v[48:51], a[64:67]\n\t" \
  "ds_read_b64_tr_b16 a[152:153], v198 offset:0\n\t" \
  "v_cvt_pk_bf16_f32 v93, v178, v179\n\t" \
  "v_cvt_pk_bf16_f32 v94, v180, v181\n\t" \
  "v_mfma_f32_16x16x32_bf16 a[68:71], a[176:179], v[52:55], a[68:71]\n\t" \
  "ds_read_b64_tr_b16 a[154:155], v198 offset:1024\n\t" \
  "v_cvt_pk_bf16_f32 v95, v182, v183\n\t" \
  "v_mfma_f32_16x16x32_bf16 a[72:75], a[176:179], v[56:59], a[72:75]\n\t" \
  "buffer_load_dwordx4 v203, %[sw], s96 offen offset:3072 lds\n\t" \
  "v_mfma_f32_16x16x32_bf16 a[76:79], a[176:179], v[60:63], a[76:79]\n\t" \
  "ds_read_b64_tr_b16 a[156:157], v199 offset:0\n\t" \
  "ds_read_b128 v[144:147], v208 offset:1664\n\t" \
  "v_mfma_f32_16x16x32_bf16 a[92:95], a[180:183], v[60:63], a[92:95]\n\t" \
  "ds_read_b64_tr_b16 a[158:159], v199 offset:1024\n\t" \
  "ds_read_b128 v[148:151], v208 offset:1680\n\t" \
  "v_mfma_f32_16x16x32_bf16 a[88:91], a[180:183], v[56:59], a[88:91]\n\t" \
  "ds_read_b128 v[152:155], v208 offset:1920\n\t" \
  "ds_read_b128 v[156:159], v208 offset:1936\n\t" \
  "s_waitcnt lgkmcnt(15)\n\t" \
  "v_lshlrev_b32 v176, 16, v96\n\t" \
  "v_mfma_f32_16x16x32_bf16 a[84:87], a[180:183], v[52:55], a[84:87]\n\t" \
  "v_and_b32 v177, s98, v96\n\t" \
  "v_lshlrev_b32 v178, 16, v97\n\t" \
  "v_and_b32 v179, s98, v97\n\t" \
  "v_lshlrev_b32 v180, 16, v98\n\t" \
  "v_mfma_f32_16x16x32_bf16 a[80:83], a[180:183], v[48:51], a[80:83]\n\t" \
  "global_load_dword %[pf], v216, %[wq]\n\t" \
  "v_and_b32 v181, s98, v98\n\t" \
  "v_lshlrev_b32 v182, 16, v99\n\t" \
  "v_mfma_f32_16x16x32_bf16 a[96:99], a[184:187], v[48:51], a[96:99]\n\t" \
  "s_lshr_b32 s99, %[c17], 9\n\t" \
  "v_xor_b32 v248, s99, v214\n\t" \
  "v_xor_b32 v249, 256, v248\n\t" \
  "v_xor_b32 v250, 512, v248\n\t" \
  "v_xor_b32 v251, 768, v248\n\t" \
  "v_mfma_f32_16x16x32_bf16 a[100:103], a[184:187], v[52:55], a[100:103]\n\t" \
  "v_and_b32 v183, s98, v99\n\t" \
  "v_fma_f32 v176, v176, v160, v162\n\t" \
  "v_fma_f32 v177, v177, v160, v162\n\t" \
  "v_fma_f32 v178, v178, v160, v162\n\t" \
  "v_mfma_f32_16x16x32_bf16 a[104:107], a[184:187], v[56:59], a[104:107]\n\t" \
  "v_fma_f32 v179, v179, v160, v162\n\t" \
  "v_fma_f32 v180, v180, v160, v162\n\t" \
  "v_fma_f32 v181, v181, v160, v162\n\t" \
  "v_mfma_f32_16x16x32_bf16 a[108:111], a[184:187], v[60:63], a[108:111]\n\t" \
  "v_fma_f32 v182, v182, v160, v162\n\t" \
  "v_fma_f32 v183, v183, v160, v162\n\t" \
  "v_fma_f32 v176, v176, v128, v136\n\t" \
  "v_mfma_f32_16x16x32_bf16 a[124:127], a[188:191], v[60:63], a[124:127]\n\t" \
  "v_fma_f32 v177, v177, v129, v137\n\t" \
  "v_fma_f32 v178, v178, v130, v138\n\t" \
  "v_fma_f32 v179, v179, v131, v139\n\t" \
  "v_mfma_f32_16x16x32_bf16 a[120:123], a[188:191], v[56:59], a[120:123]\n\t" \
  "v_fma_f32 v180, v180, v132, v140\n\t" \
  "v_fma_f32 v181, v181, v133, v141\n\t" \
  "v_fma_f32 v182, v182, v134, v142\n\t" \
  "v_mfma_f32_16x16x32_bf16 a[116:119], a[188:191], v[52:55], a[116:119]\n\t" \
  "v_fma_f32 v183, v183, v135, v143\n\t" \
  "v_cvt_pk_bf16_f32 v96, v176, v177\n\t" \
  "v_cvt_pk_bf16_f32 v97, v178, v179\n\t" \
  "v_cvt_pk_bf16_f32 v98, v180, v181\n\t" \
  "v_mfma_f32_16x16x32_bf16 a[112:115], a[188:191], v[48:51], a[112:115]\n\t" \
  FSTP("v_writelane_b32 %[stv], s94, 42\n\t") \
  FSTP("v_writelane_b32 %[stv], s92, 43\n\t") \
  FSTP("s_memrealtime s[94:95]\n\t") \
  FSTP("s_memtime s[92:93]\n\t") \
  "s_waitcnt lgkmcnt(3)\n\t" \
  "v_mfma_f32_16x16x32_bf16 a[0:3], a[128:131], v[64:67], a[0:3]\n\t" \
  "s_waitcnt vmcnt(5)\n\t" \
  "v_cmp_ne_u32 vcc, v211, %[fl]\n\t" \
  "s_cbranch_vccnz .Lfq_s\n\t" \
  ".Lfq_k:\n\t" \
  "v_mfma_f32_16x16x32_bf16 a[4:7], a[128:131], v[68:71], a[4:7]\n\t" \
  "s_add_u32 s97, %[rot19], 2097152\n\t" \
  "ds_read_b64_tr_b16 a[160:161], v192 offset:8192\n\t" \
  "v_cvt_pk_bf16_f32 v99, v182, v183\n\t" \
  "v_lshlrev_b32 v176, 16, v100\n\t" \
  "v_and_b32 v177, s98, v100\n\t" \
  "v_mfma_f32_16x16x32_bf16 a[8:11], a[128:131], v[72:75], a[8:11]\n\t" \
  "s_bfe_u32 s97, s97, 0x140003\n\t" \
  "ds_read_b64_tr_b16 a[162:163], v192 offset:9216\n\t" \
  "v_lshlrev_b32 v178, 16, v101\n\t" \
  "v_and_b32 v179, s98, v101\n\t" \
  "v_lshlrev_b32 v180, 16, v102\n\t" \
  "v_mfma_f32_16x16x32_bf16 a[12:15], a[128:131], v[76:79], a[12:15]\n\t" \
  "s_add_u32 s97, s97, %[sox]\n\t" \
  "ds_read_b64_tr_b16 a[164:165], v193 offset:8192\n\t" \
  "v_and_b32 v181, s98, v102\n\t" \
  "v_lshlrev_b32 v182, 16, v103\n\t" \
  "v_and_b32 v183, s98, v103\n\t" \
  "v_mfma_f32_16x16x32_bf16 a[28:31], a[132:135], v[76:79], a[28:31]\n\t" \
  "buffer_load_dwordx4 a[192:195], v248, %[ws], s97 offen sc1\n\t" \
  "v_fma_f32 v176, v176, v164, v166\n\t" \
  "v_fma_f32 v177, v177, v164, v166\n\t" \
  "v_mfma_f32_16x16x32_bf16 a[24:27], a[132:135], v[72:75], a[24:27]\n\t" \
  "buffer_load_dwordx4 a[196:199], v249, %[ws], s97 offen sc1\n\t" \
  "v_fma_f32 v178, v178, v164, v166\n\t" \
  "v_fma_f32 v179, v179, v164, v166\n\t" \
  "v_mfma_f32_16x16x32_bf16 a[20:23], a[132:135], v[68:71], a[20:23]\n\t" \
  "ds_read_b64_tr_b16 a[166:167], v193 offset:9216\n\t" \
  "v_fma_f32 v180, v180, v164, v166\n\t" \
  "v_fma_f32 v181, v181, v164, v166\n\t" \
  "v_fma_f32 v182, v182, v164, v166\n\t" \
  "v_mfma_f32_16x16x32_bf16 a[16:19], a[132:135], v[64:67], a[16:19]\n\t" \
  "ds_read_b64_tr_b16 a[168:169], v194 offset:8192\n\t" \
  "v_fma_f32 v183, v183, v164, v166\n\t" \
  "v_fma_f32 v176, v176, v128, v136\n\t" \
  "v_fma_f32 v177, v177, v129, v137\n\t" \
  "v_mfma_f32_16x16x32_bf16 a[32:35], a[136:139], v[64:67], a[32:35]\n\t" \
  "buffer_load_dwordx4 a[200:203], v250, %[ws], s97 offen sc1\n\t" \
  "v_fma_f32 v178, v178, v130, v138\n\t" \
  ".Lhs1:\n\ts_cbranch_execz .Lhs1n\n\t" \
  "v_fma_f32 v179, v179, v131, v139\n\t" \
  "v_mfma_f32_16x16x32_bf16 a[36:39], a[136:139], v[68:71], a[36:39]\n\t" \
  "ds_read_b64_tr_b16 a[170:171], v194 offset:9216\n\t" \
  "v_fma_f32 v180, v180, v132, v140\n\t" \
  "v_fma_f32 v181, v181, v133, v141\n\t" \
  "v_mfma_f32_16x16x32_bf16 a[40:43], a[136:139], v[72:75], a[40:43]\n\t" \
  "ds_read_b64_tr_b16 a[172:173], v195 offset:8192\n\t" \
  "v_fma_f32 v182, v182, v134, v142\n\t" \
  "v_fma_f32 v183, v183, v135, v143\n\t" \
  "v_mfma_f32_16x16x32_bf16 a[44:47], a[136:139], v[76:79], a[44:47]\n\t" \
  "buffer_load_dwordx4 a[204:207], v251, %[ws], s97 offen sc1\n\t" \
  "v_cvt_pk_bf16_f32 v100, v176, v177\n\t" \
  "v_mfma_f32_16x16x32_bf16 a[60:63], a[140:143], v[76:79], a[60:63]\n\t" \
  "s_add_u32 s97, s97, %[dk]\n\t" \
  "ds_read_b64_tr_b16 a[174:175], v195 offset:9216\n\t" \
  "v_cvt_pk_bf16_f32 v101, v178, v179\n\t" \
  "v_cvt_pk_bf16_f32 v102, v180, v181\n\t" \
  "v_mfma_f32_16x16x32_bf16 a[56:59], a[140:143], v[72:75], a[56:59]\n\t" \
  "ds_read_b64_tr_b16 a[176:177], v196 offset:8192\n\t" \
  "v_cvt_pk_bf16_f32 v103, v182, v183\n\t" \
  "v_lshlrev_b32 v176, 16, v104\n\t" \
  "v_mfma_f32_16x16x32_bf16 a[52:55], a[140:143], v[68:71], a[52:55]\n\t" \
  "buffer_load_dwordx4 a[208:211], v248, %[ws], s97 offen sc1\n\t" \
  "v_and_b32 v177, s98, v104\n\t" \
  "v_mfma_f32_16x16x32_bf16 a[48:51], a[140:143], v[64:67], a[48:51]\n\t" \
  "ds_read_b64_tr_b16 a[178:179], v196 offset:9216\n\t" \
  "v_lshlrev_b32 v178, 16, v105\n\t" \
  "v_and_b32 v179, s98, v105\n\t" \
  "v_mfma_f32_16x16x32_bf16 a[64:67], a[144:147], v[64:67], a[64:67]\n\t" \
  "ds_read_b64_tr_b16 a[180:181], v197 offset:8192\n\t" \
  "v_lshlrev_b32 v180, 16, v106\n\t" \
  "v_and_b32 v181, s98, v106\n\t" \
  "v_mfma_f32_16x16x32_bf16 a[68:71], a[144:147], v[68:71], a[68:71]\n\t" \
  "buffer_load_dwordx4 a[212:215], v249, %[ws], s97 offen sc1\n\t" \
  "v_lshlrev_b32 v182, 16, v107\n\t" \
  "v_mfma_f32_16x16x32_bf16 a[72:75], a[144:147], v[72:75], a[72:75]\n\t" \
  "ds_read_b64_tr_b16 a[182:183], v197 offset:9216\n\t" \
  "v_and_b32 v183, s98, v107\n\t" \
  "v_fma_f32 v176, v176, v168, v170\n\t" \
  "v_mfma_f32_16x16x32_bf16 a[76:79], a[144:147], v[76:79], a[76:79]\n\t" \
  "ds_read_b64_tr_b16 a[184:185], v198 offset:8192\n\t" \
  "v_fma_f32 v177, v177, v168, v170\n\t" \
  "v_fma_f32 v178, v178, v168, v170\n\t" \
  "v_mfma_f32_16x16x32_bf16 a[92:95], a[148:151], v[76:79], a[92:95]\n\t" \
  "ds_read_b64_tr_b16 a[186:187], v198 offset:9216\n\t" \
  "v_fma_f32 v179, v179, v168, v170\n\t" \
  "v_fma_f32 v180, v180, v168, v170\n\t" \
  "v_mfma_f32_16x16x32_bf16 a[88:91], a[148:151], v[72:75], a[88:91]\n\t" \
  "buffer_load_dwordx4 a[216:219], v250, %[ws], s97 offen sc1\n\t" \
  "v_fma_f32 v181, v181, v168, v170\n\t" \
  "v_mfma_f32_16x16x32_bf16 a[84:87], a[148:151], v[68:71], a[84:87]\n\t" \
  "ds_read_b64_tr_b16 a[188:189], v199 offset:8192\n\t" \
  "v_fma_f32 v182, v182, v168, v170\n\t" \
  "v_fma_f32 v183, v183, v168, v170\n\t" \
  "v_mfma_f32_16x16x32_bf16 a[80:83], a[148:151], v[64:67], a[80:83]\n\t" \
  "ds_read_b64_tr_b16 a[190:191], v199 offset:9216\n\t" \
  "v_fma_f32 v176, v176, v128, v136\n\t" \
  "v_fma_f32 v177, v177, v129, v137\n\t" \
  "v_mfma_f32_16x16x32_bf16 a[96:99], a[152:155], v[64:67], a[96:99]\n\t" \
  "buffer_load_dwordx4 a[220:223], v251, %[ws], s97 offen sc1\n\t" \
  "v_fma_f32 v178, v178, v130, v138\n\t" \
  "v_mfma_f32_16x16x32_bf16 a[100:103], a[152:155], v[68:71], a[100:103]\n\t" \
  "v_fma_f32 v179, v179, v131, v139\n\t" \
  "v_fma_f32 v180, v180, v132, v140\n\t" \
  "v_fma_f32 v181, v181, v133, v141\n\t" \
  "v_mfma_f32_16x16x32_bf16 a[104:107], a[152:155], v[72:75], a[104:107]\n\t" \
  "v_fma_f32 v182, v182, v134, v142\n\t" \
  "v_fma_f32 v183, v183, v135, v143\n\t" \
  "v_cvt_pk_bf16_f32 v104, v176, v177\n\t" \
  "v_mfma_f32_16x16x32_bf16 a[108:111], a[152:155], v[76:79], a[108:111]\n\t" \
  "v_cvt_pk_bf16_f32 v105, v178, v179\n\t" \
  "v_cvt_pk_bf16_f32 v106, v180, v181\n\t" \
  "v_cvt_pk_bf16_f32 v107, v182, v183\n\t" \
  "v_mfma_f32_16x16x32_bf16 a[124:127], a[156:159], v[76:79], a[124:127]\n\t" \
  "ds_read_b128 v[112:115], v205 offset:0\n\t" \
  "ds_read_b128 v[116:119], v205 offset:2048\n\t" \
  "v_mfma_f32_16x16x32_bf16 a[120:123], a[156:159], v[72:75], a[120:123]\n\t" \
  "ds_read_b128 v[120:123], v205 offset:4096\n\t" \
  "ds_read_b128 v[124:127], v205 offset:6144\n\t" \
  "v_lshlrev_b32 v176, 16, v108\n\t" \
  "v_and_b32 v177, s98, v108\n\t" \
  "v_mfma_f32_16x16x32_bf16 a[116:119], a[156:159], v[68:71], a[116:119]\n\t" \
  "v_lshlrev_b32 v178, 16, v109\n\t" \
  "v_and_b32 v179, s98, v109\n\t" \
  "v_lshlrev_b32 v180, 16, v110\n\t" \
  "v_and_b32 v181, s98, v110\n\t" \
  "v_mfma_f32_16x16x32_bf16 a[112:115], a[156:159], v[64:67], a[112:115]\n\t" \
  FSTP("v_writelane_b32 %[stv], s94, 44\n\t") \
  FSTP("v_writelane_b32 %[stv], s92, 45\n\t") \
  FSTP("s_memrealtime s[94:95]\n\t") \
  FSTP("s_memtime s[92:93]\n\t") \
  "s_waitcnt lgkmcnt(4)\n\t" \
  "s_barrier\n\t" \
  "v_mfma_f32_16x16x32_bf16 a[0:3], a[160:163], v[80:83], a[0:3]\n\t" \
  "s_add_u32 s96, %[rot19], 2621440\n\t" \
  "ds_read_b64_tr_b16 a[128:129], v184 offset:0\n\t" \
  "v_lshlrev_b32 v182, 16, v111\n\t" \
  "v_and_b32 v183, s98, v111\n\t" \
  "v_fma_f32 v176, v176, v172, v174\n\t" \
  "v_mfma_f32_16x16x32_bf16 a[4:7], a[160:163], v[84:87], a[4:7]\n\t" \
  "s_and_b32 s96, s96, 0x780000\n\t" \
  "ds_read_b64_tr_b16 a[130:131], v184 offset:1024\n\t" \
  "v_fma_f32 v177, v177, v172, v174\n\t" \
  "v_fma_f32 v178, v178, v172, v174\n\t" \
  "v_fma_f32 v179, v179, v172, v174\n\t" \
  "v_mfma_f32_16x16x32_bf16 a[8:11], a[160:163], v[88:91], a[8:11]\n\t" \
  "s_add_u32 m0, %[mw], 98304\n\t" \
  "ds_read_b64_tr_b16 a[132:133], v185 offset:0\n\t" \
  "v_fma_f32 v180, v180, v172, v174\n\t" \
  "v_fma_f32 v181, v181, v172, v174\n\t" \
  "v_fma_f32 v182, v182, v172, v174\n\t" \
  "v_mfma_f32_16x16x32_bf16 a[12:15], a[160:163], v[92:95], a[12:15]\n\t" \
  "buffer_load_dwordx4 v200, %[sw], s96 offen offset:0 lds\n\t" \
  "v_fma_f32 v183, v183, v172, v174\n\t" \
  "v_fma_f32 v176, v176, v128, v136\n\t" \
  "v_mfma_f32_16x16x32_bf16 a[28:31], a[164:167], v[92:95], a[28:31]\n\t" \
  "buffer_load_dwordx4 v201, %[sw], s96 offen offset:1024 lds\n\t" \
  "v_fma_f32 v177, v177, v129, v137\n\t" \
  "v_fma_f32 v178, v178, v130, v138\n\t" \
  "v_mfma_f32_16x16x32_bf16 a[24:27], a[164:167], v[88:91], a[24:27]\n\t" \
  "ds_read_b64_tr_b16 a[134:135], v185 offset:1024\n\t" \
  "v_fma_f32 v179, v179, v131, v139\n\t" \
  "v_fma_f32 v180, v180, v132, v140\n\t" \
  "v_mfma_f32_16x16x32_bf16 a[20:23], a[164:167], v[84:87], a[20:23]\n\t" \
  "ds_read_b64_tr_b16 a[136:137], v186 offset:0\n\t" \
  "v_fma_f32 v181, v181, v133, v141\n\t" \
  "v_fma_f32 v182, v182, v134, v142\n\t" \
  "v_mfma_f32_16x16x32_bf16 a[16:19], a[164:167], v[80:83], a[16:19]\n\t" \
  "buffer_load_dwordx4 v202, %[sw], s96 offen offset:2048 lds\n\t" \
  "v_fma_f32 v183, v183, v135, v143\n\t" \
  "v_mfma_f32_16x16x32_bf16 a[32:35], a[168:171], v[80:83], a[32:35]\n\t" \
  "ds_read_b64_tr_b16 a[138:139], v186 offset:1024\n\t" \
  "v_cvt_pk_bf16_f32 v108, v176, v177\n\t" \
  "v_cvt_pk_bf16_f32 v109, v178, v179\n\t" \
  "v_mfma_f32_16x16x32_bf16 a[36:39], a[168:171], v[84:87], a[36:39]\n\t" \
  "ds_read_b64_tr_b16 a[140:141], v187 offset:0\n\t" \
  "v_cvt_pk_bf16_f32 v110, v180, v181\n\t" \
  "v_cvt_pk_bf16_f32 v111, v182, v183\n\t" \
  "s_waitcnt lgkmcnt(7)\n\t" \
  "v_mfma_f32_16x16x32_bf16 a[40:43], a[168:171], v[88:91], a[40:43]\n\t" \
  "buffer_load_dwordx4 v203, %[sw], s96 offen offset:3072 lds\n\t" \
  "v_lshlrev_b32 v176, 16, v112\n\t" \
  "v_mfma_f32_16x16x32_bf16 a[44:47], a[168:171], v[92:95], a[44:47]\n\t" \
  "s_add_u32 s97, %[rot19], 2621440\n\t" \
  "ds_read_b64_tr_b16 a[142:143], v187 offset:1024\n\t" \
  "v_and_b32 v177, s98, v112\n\t" \
  "v_lshlrev_b32 v178, 16, v113\n\t" \
  "v_mfma_f32_16x16x32_bf16 a[60:63], a[172:175], v[92:95], a[60:63]\n\t" \
  "s_bfe_u32 s97, s97, 0x140003\n\t" \
  "ds_read_b64_tr_b16 a[144:145], v188 offset:0\n\t" \
  "v_and_b32 v179, s98, v113\n\t" \
  "v_lshlrev_b32 v180, 16, v114\n\t" \
  "v_mfma_f32_16x16x32_bf16 a[56:59], a[172:175], v[88:91], a[56:59]\n\t" \
  "s_add_u32 s97, s97, %[sox]\n\t" \
  "ds_read_b64_tr_b16 a[146:147], v188 offset:1024\n\t" \
  "v_and_b32 v181, s98, v114\n\t" \
  "v_lshlrev_b32 v182, 16, v115\n\t" \
  "v_mfma_f32_16x16x32_bf16 a[52:55], a[172:175], v[84:87], a[52:55]\n\t" \
  "buffer_load_dwordx4 a[224:227], v248, %[ws], s97 offen sc1\n\t" \
  "v_and_b32 v183, s98, v115\n\t" \
  "v_mfma_f32_16x16x32_bf16 a[48:51], a[172:175], v[80:83], a[48:51]\n\t" \
  "ds_read_b64_tr_b16 a[148:149], v189 offset:0\n\t" \
  "v_fma_f32 v176, v176, v160, v162\n\t" \
  "v_fma_f32 v177, v177, v160, v162\n\t" \
  "v_mfma_f32_16x16x32_bf16 a[64:67], a[176:179], v[80:83], a[64:67]\n\t" \
  "ds_read_b64_tr_b16 a[150:151], v189 offset:1024\n\t" \
  "v_fma_f32 v178, v178, v160, v162\n\t" \
  "v_fma_f32 v179, v179, v160, v162\n\t" \
  "v_mfma_f32_16x16x32_bf16 a[68:71], a[176:179], v[84:87], a[68:71]\n\t" \
  "buffer_load_dwordx4 a[228:231], v249, %[ws], s97 offen sc1\n\t" \
  "v_fma_f32 v180, v180, v160, v162\n\t" \
  "v_mfma_f32_16x16x32_bf16 a[72:75], a[176:179], v[88:91], a[72:75]\n\t" \
  "ds_read_b64_tr_b16 a[152:153], v190 offset:0\n\t" \
  "v_fma_f32 v181, v181, v160, v162\n\t" \
  "v_fma_f32 v182, v182, v160, v162\n\t" \
  "v_mfma_f32_16x16x32_bf16 a[76:79], a[176:179], v[92:95], a[76:79]\n\t" \
  "ds_read_b64_tr_b16 a[154:155], v190 offset:1024\n\t" \
  "v_fma_f32 v183, v183, v160, v162\n\t" \
  "v_fma_f32 v176, v176, v144, v152\n\t" \
  "v_mfma_f32_16x16x32_bf16 a[92:95], a[180:183], v[92:95], a[92:95]\n\t" \
  "ds_read_b64_tr_b16 a[156:157], v191 offset:0\n\t" \
  "v_fma_f32 v177, v177, v145, v153\n\t" \
  "v_fma_f32 v178, v178, v146, v154\n\t" \
  "v_mfma_f32_16x16x32_bf16 a[88:91], a[180:183], v[88:91], a[88:91]\n\t" \
  "buffer_load_dwordx4 a[232:235], v250, %[ws], s97 offen sc1\n\t" \
  "v_fma_f32 v179, v179, v147, v155\n\t" \
  "v_mfma_f32_16x16x32_bf16 a[84:87], a[180:183], v[84:87], a[84:87]\n\t" \
  "ds_read_b64_tr_b16 a[158:159], v191 offset:1024\n\t" \
  "v_fma_f32 v180, v180, v148, v156\n\t" \
  "v_fma_f32 v181, v181, v149, v157\n\t" \
  "v_mfma_f32_16x16x32_bf16 a[80:83], a[180:183], v[80:83], a[80:83]\n\t" \
  "v_fma_f32 v182, v182, v150, v158\n\t" \
  "v_fma_f32 v183, v183, v151, v159\n\t" \
  "v_cvt_pk_bf16_f32 v112, v176, v177\n\t" \
  "v_mfma_f32_16x16x32_bf16 a[96:99], a[184:187], v[80:83], a[96:99]\n\t" \
  "buffer_load_dwordx4 a[236:239], v251, %[ws], s97 offen sc1\n\t" \
  "v_cvt_pk_bf16_f32 v113, v178, v179\n\t" \
  "v_mfma_f32_16x16x32_bf16 a[100:103], a[184:187], v[84:87], a[100:103]\n\t" \
  "v_cvt_pk_bf16_f32 v114, v180, v181\n\t" \
  "v_cvt_pk_bf16_f32 v115, v182, v183\n\t" \
  "v_lshlrev_b32 v176, 16, v116\n\t" \
  "v_mfma_f32_16x16x32_bf16 a[104:107], a[184:187], v[88:91], a[104:107]\n\t" \
  "v_and_b32 v177, s98, v116\n\t" \
  "v_lshlrev_b32 v178, 16, v117\n\t" \
  "v_and_b32 v179, s98, v117\n\t" \
  "v_mfma_f32_16x16x32_bf16 a[108:111], a[184:187], v[92:95], a[108:111]\n\t" \
  "v_lshlrev_b32 v180, 16, v118\n\t" \
  "v_and_b32 v181, s98, v118\n\t" \
  "v_lshlrev_b32 v182, 16, v119\n\t" \
  "v_mfma_f32_16x16x32_bf16 a[124:127], a[188:191], v[92:95], a[124:127]\n\t" \
  "v_and_b32 v183, s98, v119\n\t" \
  "v_fma_f32 v176, v176, v164, v166\n\t" \
  "v_fma_f32 v177, v177, v164, v166\n\t" \
  "v_mfma_f32_16x16x32_bf16 a[120:123], a[188:191], v[88:91], a[120:123]\n\t" \
  "v_fma_f32 v178, v178, v164, v166\n\t" \
  "v_fma_f32 v179, v179, v164, v166\n\t" \
  "v_fma_f32 v180, v180, v164, v166\n\t" \
  "v_mfma_f32_16x16x32_bf16 a[116:119], a[188:191], v[84:87], a[116:119]\n\t" \
  "v_fma_f32 v181, v181, v164, v166\n\t" \
  "v_fma_f32 v182, v182, v164, v166\n\t" \
  "v_fma_f32 v183, v183, v164, v166\n\t" \
  "v_fma_f32 v176, v176, v144, v152\n\t" \
  "v_mfma_f32_16x16x32_bf16 a[112:115], a[188:191], v[80:83], a[112:115]\n\t" \
  FSTP("v_writelane_b32 %[stv], s94, 46\n\t") \
  FSTP("v_writelane_b32 %[stv], s92, 47\n\t") \
  FSTP("s_memrealtime s[94:95]\n\t") \
  FSTP("s_memtime s[92:93]\n\t") \
  "s_waitcnt lgkmcnt(0)\n\t" \
  "v_mfma_f32_16x16x32_bf16 a[0:3], a[128:131], v[96:99], a[0:3]\n\t" \
  "s_add_u32 s96, %[rot19], 3145728\n\t" \
  "ds_read_b64_tr_b16 a[160:161], v184 offset:8192\n\t" \
  "v_fma_f32 v177, v177, v145, v153\n\t" \
  "v_fma_f32 v178, v178, v146, v154\n\t" \
  "v_fma_f32 v179, v179, v147, v155\n\t" \
  "v_mfma_f32_16x16x32_bf16 a[4:7], a[128:131], v[100:103], a[4:7]\n\t" \
  "s_and_b32 s96, s96, 0x780000\n\t" \
  "ds_read_b64_tr_b16 a[162:163], v184 offset:9216\n\t" \
  "v_fma_f32 v180, v180, v148, v156\n\t" \
  "v_fma_f32 v181, v181, v149, v157\n\t" \
  "v_fma_f32 v182, v182, v150, v158\n\t" \
  "v_mfma_f32_16x16x32_bf16 a[8:11], a[128:131], v[104:107], a[8:11]\n\t" \
  "s_add_u32 m0, %[mw], 16384\n\t" \
  "ds_read_b64_tr_b16 a[164:165], v185 offset:8192\n\t" \
  "v_fma_f32 v183, v183, v151, v159\n\t" \
  "v_cvt_pk_bf16_f32 v116, v176, v177\n\t" \
  "v_cvt_pk_bf16_f32 v117, v178, v179\n\t" \
  "v_mfma_f32_16x16x32_bf16 a[12:15], a[128:131], v[108:111], a[12:15]\n\t" \
  "buffer_load_dwordx4 v200, %[sw], s96 offen offset:0 lds\n\t" \
  "v_cvt_pk_bf16_f32 v118, v180, v181\n\t" \
  "v_mfma_f32_16x16x32_bf16 a[28:31], a[132:135], v[108:111], a[28:31]\n\t" \
  "ds_read_b64_tr_b16 a[166:167], v185 offset:9216\n\t" \
  "v_cvt_pk_bf16_f32 v119, v182, v183\n\t" \
  "v_lshlrev_b32 v176, 16, v120\n\t" \
  "v_mfma_f32_16x16x32_bf16 a[24:27], a[132:135], v[104:107], a[24:27]\n\t" \
  "ds_read_b64_tr_b16 a[168:169], v186 offset:8192\n\t" \
  "v_and_b32 v177, s98, v120\n\t" \
  "v_lshlrev_b32 v178, 16, v121\n\t" \
  "v_mfma_f32_16x16x32_bf16 a[20:23], a[132:135], v[100:103], a[20:23]\n\t" \
  "ds_read_b64_tr_b16 a[170:171], v186 offset:9216\n\t" \
  "v_and_b32 v179, s98, v121\n\t" \
  "v_lshlrev_b32 v180, 16, v122\n\t" \
  "v_mfma_f32_16x16x32_bf16 a[16:19], a[132:135], v[96:99], a[16:19]\n\t" \
  "buffer_load_dwordx4 v201, %[sw], s96 offen offset:1024 lds\n\t" \
  "v_and_b32 v181, s98, v122\n\t" \
  "v_mfma_f32_16x16x32_bf16 a[32:35], a[136:139], v[96:99], a[32:35]\n\t" \
  "ds_read_b64_tr_b16 a[172:173], v187 offset:8192\n\t" \
  "v_lshlrev_b32 v182, 16, v123\n\t" \
  "v_and_b32 v183, s98, v123\n\t" \
  "v_mfma_f32_16x16x32_bf16 a[36:39], a[136:139], v[100:103], a[36:39]\n\t" \
  "ds_read_b64_tr_b16 a[174:175], v187 offset:9216\n\t" \
  "v_fma_f32 v176, v176, v168, v170\n\t" \
  "v_fma_f32 v177, v177, v168, v170\n\t" \
  "v_mfma_f32_16x16x32_bf16 a[40:43], a[136:139], v[104:107], a[40:43]\n\t" \
  "ds_read_b64_tr_b16 a[176:177], v188 offset:8192\n\t" \
  "v_fma_f32 v178, v178, v168, v170\n\t" \
  "v_fma_f32 v179, v179, v168, v170\n\t" \
  "v_mfma_f32_16x16x32_bf16 a[44:47], a[136:139], v[108:111], a[44:47]\n\t" \
  "ds_read_b64_tr_b16 a[178:179], v188 offset:9216\n\t" \
  "v_fma_f32 v180, v180, v168, v170\n\t" \
  "v_fma_f32 v181, v181, v168, v170\n\t" \
  "v_mfma_f32_16x16x32_bf16 a[60:63], a[140:143], v[108:111], a[60:63]\n\t" \
  "ds_read_b64_tr_b16 a[180:181], v189 offset:8192\n\t" \
  "v_fma_f32 v182, v182, v168, v170\n\t" \
  "v_fma_f32 v183, v183, v168, v170\n\t" \
  "v_mfma_f32_16x16x32_bf16 a[56:59], a[140:143], v[104:107], a[56:59]\n\t" \
  "ds_read_b64_tr_b16 a[182:183], v189 offset:9216\n\t" \
  "v_fma_f32 v176, v176, v144, v152\n\t" \
  "v_fma_f32 v177, v177, v145, v153\n\t" \
  "v_mfma_f32_16x16x32_bf16 a[52:55], a[140:143], v[100:103], a[52:55]\n\t" \
  "buffer_load_dwordx4 v202, %[sw], s96 offen offset:2048 lds\n\t" \
  "v_fma_f32 v178, v178, v146, v154\n\t" \
  "v_mfma_f32_16x16x32_bf16 a[48:51], a[140:143], v[96:99], a[48:51]\n\t" \
  "ds_read_b64_tr_b16 a[184:185], v190 offset:8192\n\t" \
  "v_fma_f32 v179, v179, v147, v155\n\t" \
  "v_fma_f32 v180, v180, v148, v156\n\t" \
  "v_mfma_f32_16x16x32_bf16 a[64:67], a[144:147], v[96:99], a[64:67]\n\t" \
  "ds_read_b64_tr_b16 a[186:187], v190 offset:9216\n\t" \
  "v_fma_f32 v181, v181, v149, v157\n\t" \
  "v_fma_f32 v182, v182, v150, v158\n\t" \
  "v_mfma_f32_16x16x32_bf16 a[68:71], a[144:147], v[100:103], a[68:71]\n\t" \
  "ds_read_b64_tr_b16 a[188:189], v191 offset:8192\n\t" \
  "v_fma_f32 v183, v183, v151, v159\n\t" \
  "v_cvt_pk_bf16_f32 v120, v176, v177\n\t" \
  "v_mfma_f32_16x16x32_bf16 a[72:75], a[144:147], v[104:107], a[72:75]\n\t" \
  "ds_read_b64_tr_b16 a[190:191], v191 offset:9216\n\t" \
  "v_cvt_pk_bf16_f32 v121, v178, v179\n\t" \
  "v_cvt_pk_bf16_f32 v122, v180, v181\n\t" \
  "v_mfma_f32_16x16x32_bf16 a[76:79], a[144:147], v[108:111], a[76:79]\n\t" \
  "v_cvt_pk_bf16_f32 v123, v182, v183\n\t" \
  "v_lshlrev_b32 v176, 16, v124\n\t" \
  "v_and_b32 v177, s98, v124\n\t" \
  "v_mfma_f32_16x16x32_bf16 a[92:95], a[148:151], v[108:111], a[92:95]\n\t" \
  "v_lshlrev_b32 v178, 16, v125\n\t" \
  "v_and_b32 v179, s98, v125\n\t" \
  "v_lshlrev_b32 v180, 16, v126\n\t" \
  "v_mfma_f32_16x16x32_bf16 a[88:91], a[148:151], v[104:107], a[88:91]\n\t" \
  "buffer_load_dwordx4 v203, %[sw], s96 offen offset:3072 lds\n\t" \
  "v_and_b32 v181, s98, v126\n\t" \
  "v_mfma_f32_16x16x32_bf16 a[84:87], a[148:151], v[100:103], a[84:87]\n\t" \
  "v_lshlrev_b32 v182, 16, v127\n\t" \
  "v_and_b32 v183, s98, v127\n\t" \
  "v_fma_f32 v176, v176, v172, v174\n\t" \
  "v_mfma_f32_16x16x32_bf16 a[80:83], a[148:151], v[96:99], a[80:83]\n\t" \
  "v_fma_f32 v177, v177, v172, v174\n\t" \
  "v_fma_f32 v178, v178, v172, v174\n\t" \
  "v_fma_f32 v179, v179, v172, v174\n\t" \
  "v_mfma_f32_16x16x32_bf16 a[96:99], a[152:155], v[96:99], a[96:99]\n\t" \
  "v_mbcnt_lo_u32_b32 v217, -1, 0\n\t" \
  "v_mbcnt_hi_u32_b32 v217, -1, v217\n\t" \
  "v_cmp_gt_u32 vcc, 16, v217\n\t" \
  "v_mfma_f32_16x16x32_bf16 a[100:103], a[152:155], v[100:103], a[100:103]\n\t" \
  "v_lshlrev_b32 v217, 7, v217\n\t" \
  "v_bfrev_b32 v215, 1\n\t" \
  "v_cndmask_b32 v217, v215, v217, vcc\n\t" \
  "v_mfma_f32_16x16x32_bf16 a[104:107], a[152:155], v[104:107], a[104:107]\n\t" \
  "v_fma_f32 v180, v180, v172, v174\n\t" \
  "v_fma_f32 v181, v181, v172, v174\n\t" \
  "v_fma_f32 v182, v182, v172, v174\n\t" \
  "v_mfma_f32_16x16x32_bf16 a[108:111], a[152:155], v[108:111], a[108:111]\n\t" \
  "v_fma_f32 v183, v183, v172, v174\n\t" \
  "v_fma_f32 v176, v176, v144, v152\n\t" \
  "v_fma_f32 v177, v177, v145, v153\n\t" \
  "v_mfma_f32_16x16x32_bf16 a[124:127], a[156:159], v[108:111], a[124:127]\n\t" \
  "v_fma_f32 v178, v178, v146, v154\n\t" \
  "v_fma_f32 v179, v179, v147, v155\n\t" \
  "v_fma_f32 v180, v180, v148, v156\n\t" \
  "v_mfma_f32_16x16x32_bf16 a[120:123], a[156:159], v[104:107], a[120:123]\n\t" \
  "v_fma_f32 v181, v181, v149, v157\n\t" \
  "v_fma_f32 v182, v182, v150, v158\n\t" \
  "v_fma_f32 v183, v183, v151, v159\n\t" \
  "v_mfma_f32_16x16x32_bf16 a[116:119], a[156:159], v[100:103], a[116:119]\n\t" \
  "v_cvt_pk_bf16_f32 v124, v176, v177\n\t" \
  "v_cvt_pk_bf16_f32 v125, v178, v179\n\t" \
  "v_cvt_pk_bf16_f32 v126, v180, v181\n\t" \
  "v_cvt_pk_bf16_f32 v127, v182, v183\n\t" \
  "v_mfma_f32_16x16x32_bf16 a[112:115], a[156:159], v[96:99], a[112:115]\n\t" \
  FSTP("v_writelane_b32 %[stv], s94, 48\n\t") \
  FSTP("v_writelane_b32 %[stv], s92, 49\n\t") \
  FSTP("s_memrealtime s[94:95]\n\t") \
  FSTP("s_memtime s[92:93]\n\t") \
  "s_waitcnt vmcnt(21) lgkmcnt(0)\n\t" \
  "s_barrier\n\t" \
  "v_mfma_f32_16x16x32_bf16 a[0:3], a[160:163], v[112:115], a[0:3]\n\t" \
  "s_add_u32 s97, s97, %[dk]\n\t" \
  "ds_read_b64_tr_b16 a[128:129], v184 offset:49152\n\t" \
  "v_mfma_f32_16x16x32_bf16 a[4:7], a[160:163], v[116:119], a[4:7]\n\t" \
  "buffer_load_dwordx4 a[240:243], v248, %[ws], s97 offen sc1\n\t" \
  "v_mfma_f32_16x16x32_bf16 a[8:11], a[160:163], v[120:123], a[8:11]\n\t" \
  "ds_read_b64_tr_b16 a[130:131], v184 offset:50176\n\t" \
  "v_mfma_f32_16x16x32_bf16 a[12:15], a[160:163], v[124:127], a[12:15]\n\t" \
  "buffer_load_dwordx4 a[244:247], v249, %[ws], s97 offen sc1\n\t" \
  "v_mfma_f32_16x16x32_bf16 a[28:31], a[164:167], v[124:127], a[28:31]\n\t" \
  "ds_read_b64_tr_b16 a[132:133], v185 offset:49152\n\t" \
  "v_mfma_f32_16x16x32_bf16 a[24:27], a[164:167], v[120:123], a[24:27]\n\t" \
  "ds_read_b64_tr_b16 a[134:135], v185 offset:50176\n\t" \
  "v_mfma_f32_16x16x32_bf16 a[20:23], a[164:167], v[116:119], a[20:23]\n\t" \
  "buffer_load_dwordx4 a[248:251], v250, %[ws], s97 offen sc1\n\t" \
  "v_mfma_f32_16x16x32_bf16 a[16:19], a[164:167], v[112:115], a[16:19]\n\t" \
  "ds_read_b64_tr_b16 a[136:137], v186 offset:49152\n\t" \
  "v_mfma_f32_16x16x32_bf16 a[32:35], a[168:171], v[112:115], a[32:35]\n\t" \
  "ds_read_b64_tr_b16 a[138:139], v186 offset:50176\n\t" \
  "v_mfma_f32_16x16x32_bf16 a[36:39], a[168:171], v[116:119], a[36:39]\n\t" \
  "buffer_load_dwordx4 a[252:255], v251, %[ws], s97 offen sc1\n\t" \
  "v_mfma_f32_16x16x32_bf16 a[40:43], a[168:171], v[120:123], a[40:43]\n\t" \
  "s_add_u32 s96, %[rot19], 3670016\n\t" \
  "ds_read_b64_tr_b16 a[140:141], v187 offset:49152\n\t" \
  "v_mfma_f32_16x16x32_bf16 a[44:47], a[168:171], v[124:127], a[44:47]\n\t" \
  "s_and_b32 s96, s96, 0x780000\n\t" \
  "ds_read_b64_tr_b16 a[142:143], v187 offset:50176\n\t" \
  "v_mfma_f32_16x16x32_bf16 a[60:63], a[172:175], v[124:127], a[60:63]\n\t" \
  "s_add_u32 m0, %[mw], 32768\n\t" \
  "ds_read_b64_tr_b16 a[144:145], v188 offset:49152\n\t" \
  "v_mfma_f32_16x16x32_bf16 a[56:59], a[172:175], v[120:123], a[56:59]\n\t" \
  "buffer_load_dwordx4 v200, %[sw], s96 offen offset:0 lds\n\t" \
  "v_mfma_f32_16x16x32_bf16 a[52:55], a[172:175], v[116:119], a[52:55]\n\t" \
  "ds_read_b64_tr_b16 a[146:147], v188 offset:50176\n\t" \
  "v_mfma_f32_16x16x32_bf16 a[48:51], a[172:175], v[112:115], a[48:51]\n\t" \
  "ds_read_b64_tr_b16 a[148:149], v189 offset:49152\n\t" \
  "v_mfma_f32_16x16x32_bf16 a[64:67], a[176:179], v[112:115], a[64:67]\n\t" \
  "ds_read_b64_tr_b16 a[150:151], v189 offset:50176\n\t" \
  "v_mfma_f32_16x16x32_bf16 a[68:71], a[176:179], v[116:119], a[68:71]\n\t" \
  "buffer_load_dwordx4 v201, %[sw], s96 offen offset:1024 lds\n\t" \
  "v_mfma_f32_16x16x32_bf16 a[72:75], a[176:179], v[120:123], a[72:75]\n\t" \
  "ds_read_b64_tr_b16 a[152:153], v190 offset:49152\n\t" \
  "v_mfma_f32_16x16x32_bf16 a[76:79], a[176:179], v[124:127], a[76:79]\n\t" \
  "ds_read_b64_tr_b16 a[154:155], v190 offset:50176\n\t" \
  "v_mfma_f32_16x16x32_bf16 a[92:95], a[180:183], v[124:127], a[92:95]\n\t" \
  "ds_read_b64_tr_b16 a[156:157], v191 offset:49152\n\t" \
  "v_mfma_f32_16x16x32_bf16 a[88:91], a[180:183], v[120:123], a[88:91]\n\t" \
  "buffer_load_dwordx4 v202, %[sw], s96 offen offset:2048 lds\n\t" \
  "v_mfma_f32_16x16x32_bf16 a[84:87], a[180:183], v[116:119], a[84:87]\n\t" \
  "ds_read_b64_tr_b16 a[158:159], v191 offset:50176\n\t" \
  "v_mfma_f32_16x16x32_bf16 a[80:83], a[180:183], v[112:115], a[80:83]\n\t" \
  "v_mfma_f32_16x16x32_bf16 a[96:99], a[184:187], v[112:115], a[96:99]\n\t" \
  "v_mfma_f32_16x16x32_bf16 a[100:103], a[184:187], v[116:119], a[100:103]\n\t" \
  "buffer_load_dwordx4 v203, %[sw], s96 offen offset:3072 lds\n\t" \
  "v_mfma_f32_16x16x32_bf16 a[104:107], a[184:187], v[120:123], a[104:107]\n\t" \
  "s_add_u32 s99, %[pf1], 0\n\t" \
  "v_mfma_f32_16x16x32_bf16 a[108:111], a[184:187], v[124:127], a[108:111]\n\t" \
  "buffer_load_dword %[pf], v217, %[wo], s99 offen\n\t" \
  "v_mfma_f32_16x16x32_bf16 a[124:127], a[188:191], v[124:127], a[124:127]\n\t" \
  "v_mfma_f32_16x16x32_bf16 a[120:123], a[188:191], v[120:123], a[120:123]\n\t" \
  "v_mfma_f32_16x16x32_bf16 a[116:119], a[188:191], v[116:119], a[116:119]\n\t" \
  "v_mfma_f32_16x16x32_bf16 a[112:115], a[188:191], v[112:115], a[112:115]\n\t" \
  FSTP("v_writelane_b32 %[stv], s94, 50\n\t") \
  FSTP("v_writelane_b32 %[stv], s92, 51\n\t") \
  FSTP("s_memrealtime s[94:95]\n\t") \
  FSTP("s_memtime s[92:93]\n\t") \
  FSTS("s_memrealtime s[94:95]\n\t") \
  FSTS("s_waitcnt lgkmcnt(0)\n\t") \
  FSTS("v_writelane_b32 %[stv], s94, 14\n\t") \
  "s_waitcnt vmcnt(25) lgkmcnt(0)\n\t" \
  "v_mfma_f32_16x16x32_bf16 a[0:3], a[128:131], a[192:195], a[0:3]\n\t" \
  "s_add_u32 s96, %[rot19], 4194304\n\t" \
  "ds_read_b64_tr_b16 a[160:161], v184 offset:57344\n\t" \
  "v_mfma_f32_16x16x32_bf16 a[4:7], a[128:131], a[196:199], a[4:7]\n\t" \
  "s_and_b32 s96, s96, 0x780000\n\t" \
  "v_mfma_f32_16x16x32_bf16 a[8:11], a[128:131], a[200:203], a[8:11]\n\t" \
  "s_add_u32 m0, %[mw], 0\n\t" \
  "ds_read_b64_tr_b16 a[162:163], v184 offset:58368\n\t" \
  "v_mfma_f32_16x16x32_bf16 a[12:15], a[128:131], a[204:207], a[12:15]\n\t" \
  "buffer_load_dwordx4 v200, %[sw], s96 offen offset:0 lds\n\t" \
  "v_mfma_f32_16x16x32_bf16 a[28:31], a[132:135], a[204:207], a[28:31]\n\t" \
  "ds_read_b64_tr_b16 a[164:165], v185 offset:57344\n\t" \
  "v_mfma_f32_16x16x32_bf16 a[24:27], a[132:135], a[200:203], a[24:27]\n\t" \
  "ds_read_b64_tr_b16 a[166:167], v185 offset:58368\n\t" \
  "v_mfma_f32_16x16x32_bf16 a[20:23], a[132:135], a[196:199], a[20:23]\n\t" \
  "ds_read_b64_tr_b16 a[168:169], v186 offset:57344\n\t" \
  "v_mfma_f32_16x16x32_bf16 a[16:19], a[132:135], a[192:195], a[16:19]\n\t" \
  "buffer_load_dwordx4 v201, %[sw], s96 offen offset:1024 lds\n\t" \
  "v_mfma_f32_16x16x32_bf16 a[32:35], a[136:139], a[192:195], a[32:35]\n\t" \
  "ds_read_b64_tr_b16 a[170:171], v186 offset:58368\n\t" \
  "v_accvgpr_read_b32 v0, a0\n\t" \
  "v_accvgpr_read_b32 v1, a1\n\t" \
  "v_mfma_f32_16x16x32_bf16 a[36:39], a[136:139], a[196:199], a[36:39]\n\t" \
  "ds_read_b64_tr_b16 a[172:173], v187 offset:57344\n\t" \
  "v_accvgpr_read_b32 v2, a2\n\t" \
  "v_mfma_f32_16x16x32_bf16 a[40:43], a[136:139], a[200:203], a[40:43]\n\t" \
  "v_accvgpr_read_b32 v3, a3\n\t" \
  "v_accvgpr_read_b32 v32, a4\n\t" \
  "v_mfma_f32_16x16x32_bf16 a[44:47], a[136:139], a[204:207], a[44:47]\n\t" \
  "ds_read_b64_tr_b16 a[174:175], v187 offset:58368\n\t" \
  "v_accvgpr_read_b32 v33, a5\n\t" \
  "v_mfma_f32_16x16x32_bf16 a[60:63], a[140:143], a[204:207], a[60:63]\n\t" \
  "ds_read_b64_tr_b16 a[176:177], v188 offset:57344\n\t" \
  "v_accvgpr_read_b32 v34, a6\n\t" \
  "v_mfma_f32_16x16x32_bf16 a[56:59], a[140:143], a[200:203], a[56:59]\n\t" \
  "v_accvgpr_read_b32 v35, a7\n\t" \
  "v_accvgpr_read_b32 v64, a8\n\t" \
  "v_mfma_f32_16x16x32_bf16 a[52:55], a[140:143], a[196:199], a[52:55]\n\t" \
  "buffer_load_dwordx4 v202, %[sw], s96 offen offset:2048 lds\n\t" \
  "v_mfma_f32_16x16x32_bf16 a[48:51], a[140:143], a[192:195], a[48:51]\n\t" \
  "ds_read_b64_tr_b16 a[178:179], v188 offset:58368\n\t" \
  "v_accvgpr_read_b32 v65, a9\n\t" \
  "v_mfma_f32_16x16x32_bf16 a[64:67], a[144:147], a[192:195], a[64:67]\n\t" \
  "ds_read_b64_tr_b16 a[180:181], v189 offset:57344\n\t" \
  "v_accvgpr_read_b32 v66, a10\n\t" \
  "v_mfma_f32_16x16x32_bf16 a[68:71], a[144:147], a[196:199], a[68:71]\n\t" \
  "ds_read_b64_tr_b16 a[182:183], v189 offset:58368\n\t" \
  "v_accvgpr_read_b32 v67, a11\n\t" \
  "v_mfma_f32_16x16x32_bf16 a[72:75], a[144:147], a[200:203], a[72:75]\n\t" \
  "ds_read_b64_tr_b16 a[184:185], v190 offset:57344\n\t" \
  "v_accvgpr_read_b32 v96, a12\n\t" \
  "v_mfma_f32_16x16x32_bf16 a[76:79], a[144:147], a[204:207], a[76:79]\n\t" \
  "v_accvgpr_read_b32 v97, a13\n\t" \
  "v_accvgpr_read_b32 v98, a14\n\t" \
  "v_mfma_f32_16x16x32_bf16 a[92:95], a[148:151], a[204:207], a[92:95]\n\t" \
  "ds_read_b64_tr_b16 a[186:187], v190 offset:58368\n\t" \
  "v_accvgpr_read_b32 v99, a15\n\t" \
  "v_mfma_f32_16x16x32_bf16 a[88:91], a[148:151], a[200:203], a[88:91]\n\t" \
  "buffer_load_dwordx4 v203, %[sw], s96 offen offset:3072 lds\n\t" \
  "v_mfma_f32_16x16x32_bf16 a[84:87], a[148:151], a[196:199], a[84:87]\n\t" \
  "ds_read_b64_tr_b16 a[188:189], v191 offset:57344\n\t" \
  "v_accvgpr_read_b32 v100, a28\n\t" \
  "v_mfma_f32_16x16x32_bf16 a[80:83], a[148:151], a[192:195], a[80:83]\n\t" \
  "ds_read_b64_tr_b16 a[190:191], v191 offset:58368\n\t" \
  "v_accvgpr_read_b32 v101, a29\n\t" \
  "v_mfma_f32_16x16x32_bf16 a[96:99], a[152:155], a[192:195], a[96:99]\n\t" \
  "v_accvgpr_read_b32 v102, a30\n\t" \
  "v_accvgpr_read_b32 v103, a31\n\t" \
  "v_mfma_f32_16x16x32_bf16 a[100:103], a[152:155], a[196:199], a[100:103]\n\t" \
  "v_accvgpr_read_b32 v68, a24\n\t" \
  "v_accvgpr_read_b32 v69, a25\n\t" \
  "v_mfma_f32_16x16x32_bf16 a[104:107], a[152:155], a[200:203], a[104:107]\n\t" \
  "v_accvgpr_read_b32 v70, a26\n\t" \
  "v_accvgpr_read_b32 v71, a27\n\t" \
  "v_mfma_f32_16x16x32_bf16 a[108:111], a[152:155], a[204:207], a[108:111]\n\t" \
  "v_accvgpr_read_b32 v36, a20\n\t" \
  "v_accvgpr_read_b32 v37, a21\n\t" \
  "v_mfma_f32_16x16x32_bf16 a[124:127], a[156:159], a[204:207], a[124:127]\n\t" \
  "v_accvgpr_read_b32 v38, a22\n\t" \
  "v_accvgpr_read_b32 v39, a23\n\t" \
  "v_mfma_f32_16x16x32_bf16 a[120:123], a[156:159], a[200:203], a[120:123]\n\t" \
  "v_accvgpr_read_b32 v4, a16\n\t" \
  "v_accvgpr_read_b32 v5, a17\n\t" \
  "v_mfma_f32_16x16x32_bf16 a[116:119], a[156:159], a[196:199], a[116:119]\n\t" \
  "v_accvgpr_read_b32 v6, a18\n\t" \
  "v_accvgpr_read_b32 v7, a19\n\t" \
  "v_mfma_f32_16x16x32_bf16 a[112:115], a[156:159], a[192:195], a[112:115]\n\t" \
  "s_waitcnt vmcnt(21) lgkmcnt(0)\n\t" \
  "s_barrier\n\t" \
  "v_mfma_f32_16x16x32_bf16 a[32:35], a[168:171], a[208:211], a[32:35]\n\t" \
  "s_add_u32 s97, %[rot19], 3145728\n\t" \
  "ds_read_b64_tr_b16 a[128:129], v192 offset:0\n\t" \
  "v_mfma_f32_16x16x32_bf16 a[36:39], a[168:171], a[212:215], a[36:39]\n\t" \
  "s_bfe_u32 s97, s97, 0x140003\n\t" \
  "v_mfma_f32_16x16x32_bf16 a[40:43], a[168:171], a[216:219], a[40:43]\n\t" \
  "s_add_u32 s97, s97, %[sox]\n\t" \
  "ds_read_b64_tr_b16 a[130:131], v192 offset:1024\n\t" \
  "v_mfma_f32_16x16x32_bf16 a[44:47], a[168:171], a[220:223], a[44:47]\n\t" \
  "buffer_load_dwordx4 a[192:195], v248, %[ws], s97 offen sc1\n\t" \
  "v_mfma_f32_16x16x32_bf16 a[60:63], a[172:175], a[220:223], a[60:63]\n\t" \
  "buffer_load_dwordx4 a[196:199], v249, %[ws], s97 offen sc1\n\t" \
  "v_mfma_f32_16x16x32_bf16 a[56:59], a[172:175], a[216:219], a[56:59]\n\t" \
  "ds_read_b64_tr_b16 a[132:133], v193 offset:0\n\t" \
  "v_mfma_f32_16x16x32_bf16 a[52:55], a[172:175], a[212:215], a[52:55]\n\t" \
  "ds_read_b64_tr_b16 a[134:135], v193 offset:1024\n\t" \
  "v_mfma_f32_16x16x32_bf16 a[48:51], a[172:175], a[208:211], a[48:51]\n\t" \
  "buffer_load_dwordx4 a[200:203], v250, %[ws], s97 offen sc1\n\t" \
  "v_mfma_f32_16x16x32_bf16 a[64:67], a[176:179], a[208:211], a[64:67]\n\t" \
  "ds_read_b64_tr_b16 a[136:137], v194 offset:0\n\t" \
  "v_accvgpr_read_b32 v8, a32\n\t" \
  "v_accvgpr_read_b32 v9, a33\n\t" \
  "v_mfma_f32_16x16x32_bf16 a[68:71], a[176:179], a[212:215], a[68:71]\n\t" \
  "ds_read_b64_tr_b16 a[138:139], v194 offset:1024\n\t" \
  "v_accvgpr_read_b32 v10, a34\n\t" \
  "v_accvgpr_read_b32 v11, a35\n\t" \
  "v_mfma_f32_16x16x32_bf16 a[72:75], a[176:179], a[216:219], a[72:75]\n\t" \
  "ds_read_b64_tr_b16 a[140:141], v195 offset:0\n\t" \
  "v_accvgpr_read_b32 v40, a36\n\t" \
  "v_accvgpr_read_b32 v41, a37\n\t" \
  "v_mfma_f32_16x16x32_bf16 a[76:79], a[176:179], a[220:223], a[76:79]\n\t" \
  "buffer_load_dwordx4 a[204:207], v251, %[ws], s97 offen sc1\n\t" \
  "v_accvgpr_read_b32 v42, a38\n\t" \
  "v_mfma_f32_16x16x32_bf16 a[92:95], a[180:183], a[220:223], a[92:95]\n\t" \
  "s_add_u32 s96, %[rot19], 4718592\n\t" \
  "ds_read_b64_tr_b16 a[142:143], v195 offset:1024\n\t" \
  "v_accvgpr_read_b32 v43, a39\n\t" \
  "v_accvgpr_read_b32 v72, a40\n\t" \
  "v_mfma_f32_16x16x32_bf16 a[88:91], a[180:183], a[216:219], a[88:91]\n\t" \
  "s_and_b32 s96, s96, 0x780000\n\t" \
  "ds_read_b64_tr_b16 a[144:145], v196 offset:0\n\t" \
  "v_accvgpr_read_b32 v73, a41\n\t" \
  "v_accvgpr_read_b32 v74, a42\n\t" \
  ".Lhs2:\n\ts_cbranch_execz .Lhs2n\n\t" \
  "v_mfma_f32_16x16x32_bf16 a[84:87], a[180:183], a[212:215], a[84:87]\n\t" \
  "s_add_u32 m0, %[mw], 49152\n\t" \
  "ds_read_b64_tr_b16 a[146:147], v196 offset:1024\n\t" \
  "v_accvgpr_read_b32 v75, a43\n\t" \
  "v_accvgpr_read_b32 v104, a44\n\t" \
  "v_mfma_f32_16x16x32_bf16 a[80:83], a[180:183], a[208:211], a[80:83]\n\t" \
  "buffer_load_dwordx4 v200, %[sw], s96 offen offset:0 lds\n\t" \
  "v_accvgpr_read_b32 v105, a45\n\t" \
  "v_mfma_f32_16x16x32_bf16 a[96:99], a[184:187], a[208:211], a[96:99]\n\t" \
  "ds_read_b64_tr_b16 a[148:149], v197 offset:0\n\t" \
  "v_accvgpr_read_b32 v106, a46\n\t" \
  "v_mfma_f32_16x16x32_bf16 a[100:103], a[184:187], a[212:215], a[100:103]\n\t" \
  "ds_read_b64_tr_b16 a[150:151], v197 offset:1024\n\t" \
  "v_accvgpr_read_b32 v107, a47\n\t" \
  "v_mfma_f32_16x16x32_bf16 a[104:107], a[184:187], a[216:219], a[104:107]\n\t" \
  "ds_read_b64_tr_b16 a[152:153], v198 offset:0\n\t" \
  "v_accvgpr_read_b32 v108, a60\n\t" \
  "v_mfma_f32_16x16x32_bf16 a[108:111], a[184:187], a[220:223], a[108:111]\n\t" \
  "buffer_load_dwordx4 v201, %[sw], s96 offen offset:1024 lds\n\t" \
  "v_mfma_f32_16x16x32_bf16 a[124:127], a[188:191], a[220:223], a[124:127]\n\t" \
  "ds_read_b64_tr_b16 a[154:155], v198 offset:1024\n\t" \
  "v_accvgpr_read_b32 v109, a61\n\t" \
  "v_mfma_f32_16x16x32_bf16 a[120:123], a[188:191], a[216:219], a[120:123]\n\t" \
  "ds_read_b64_tr_b16 a[156:157], v199 offset:0\n\t" \
  "v_accvgpr_read_b32 v110, a62\n\t" \
  "v_mfma_f32_16x16x32_bf16 a[116:119], a[188:191], a[212:215], a[116:119]\n\t" \
  "v_accvgpr_read_b32 v111, a63\n\t" \
  "v_accvgpr_read_b32 v76, a56\n\t" \
  "v_mfma_f32_16x16x32_bf16 a[112:115], a[188:191], a[208:211], a[112:115]\n\t" \
  "buffer_load_dwordx4 v202, %[sw], s96 offen offset:2048 lds\n\t" \
  "v_mfma_f32_16x16x32_bf16 v[0:3], a[160:163], a[208:211], v[0:3]\n\t" \
  "ds_read_b64_tr_b16 a[158:159], v199 offset:1024\n\t" \
  "v_accvgpr_read_b32 v77, a57\n\t" \
  "v_mfma_f32_16x16x32_bf16 v[32:35], a[160:163], a[212:215], v[32:35]\n\t" \
  "v_accvgpr_read_b32 v78, a58\n\t" \
  "v_accvgpr_read_b32 v79, a59\n\t" \
  "v_mfma_f32_16x16x32_bf16 v[64:67], a[160:163], a[216:219], v[64:67]\n\t" \
  "v_accvgpr_read_b32 v44, a52\n\t" \
  "v_accvgpr_read_b32 v45, a53\n\t" \
  "v_mfma_f32_16x16x32_bf16 v[96:99], a[160:163], a[220:223], v[96:99]\n\t" \
  "buffer_load_dwordx4 v203, %[sw], s96 offen offset:3072 lds\n\t" \
  "v_mfma_f32_16x16x32_bf16 v[100:103], a[164:167], a[220:223], v[100:103]\n\t" \
  "v_accvgpr_read_b32 v46, a54\n\t" \
  "v_accvgpr_read_b32 v47, a55\n\t" \
  "v_mfma_f32_16x16x32_bf16 v[68:71], a[164:167], a[216:219], v[68:71]\n\t" \
  "v_accvgpr_read_b32 v12, a48\n\t" \
  "v_accvgpr_read_b32 v13, a49\n\t" \
  "v_mfma_f32_16x16x32_bf16 v[36:39], a[164:167], a[212:215], v[36:39]\n\t" \
  "v_accvgpr_read_b32 v14, a50\n\t" \
  "v_accvgpr_read_b32 v15, a51\n\t" \
  "v_mfma_f32_16x16x32_bf16 v[4:7], a[164:167], a[208:211], v[4:7]\n\t" \
  "s_waitcnt vmcnt(25) lgkmcnt(0)\n\t" \
  "v_mfma_f32_16x16x32_bf16 a[64:67], a[144:147], a[224:227], a[64:67]\n\t" \
  "ds_read_b64_tr_b16 a[160:161], v192 offset:8192\n\t" \
  "v_add_u32 v216, 3145728, v216\n\t" \
  "v_mfma_f32_16x16x32_bf16 a[68:71], a[144:147], a[228:231], a[68:71]\n\t" \
  "v_and_b32 v216, 0x7fffff, v216\n\t" \
  "v_mfma_f32_16x16x32_bf16 a[72:75], a[144:147], a[232:235], a[72:75]\n\t" \
  "global_load_dword %[pf], v216, %[wq]\n\t" \
  "v_mfma_f32_16x16x32_bf16 a[76:79], a[144:147], a[236:239], a[76:79]\n\t" \
  "s_add_u32 s97, s97, %[dk]\n\t" \
  "ds_read_b64_tr_b16 a[162:163], v192 offset:9216\n\t" \
  "v_mfma_f32_16x16x32_bf16 a[92:95], a[148:151], a[236:239], a[92:95]\n\t" \
  "ds_read_b64_tr_b16 a[164:165], v193 offset:8192\n\t" \
  "v_mfma_f32_16x16x32_bf16 a[88:91], a[148:151], a[232:235], a[88:91]\n\t" \
  "buffer_load_dwordx4 a[208:211], v248, %[ws], s97 offen sc1\n\t" \
  "v_mfma_f32_16x16x32_bf16 a[84:87], a[148:151], a[228:231], a[84:87]\n\t" \
  "ds_read_b64_tr_b16 a[166:167], v193 offset:9216\n\t" \
  "v_mfma_f32_16x16x32_bf16 a[80:83], a[148:151], a[224:227], a[80:83]\n\t" \
  "ds_read_b64_tr_b16 a[168:169], v194 offset:8192\n\t" \
  "v_mfma_f32_16x16x32_bf16 a[96:99], a[152:155], a[224:227], a[96:99]\n\t" \
  "ds_read_b64_tr_b16 a[170:171], v194 offset:9216\n\t" \
  "v_accvgpr_read_b32 v16, a64\n\t" \
  "v_accvgpr_read_b32 v17, a65\n\t" \
  "v_mfma_f32_16x16x32_bf16 a[100:103], a[152:155], a[228:231], a[100:103]\n\t" \
  "ds_read_b64_tr_b16 a[172:173], v195 offset:8192\n\t" \
  "v_accvgpr_read_b32 v18, a66\n\t" \
  "v_accvgpr_read_b32 v19, a67\n\t" \
  "v_mfma_f32_16x16x32_bf16 a[104:107], a[152:155], a[232:235], a[104:107]\n\t" \
  "v_accvgpr_read_b32 v48, a68\n\t" \
  "v_accvgpr_read_b32 v49, a69\n\t" \
  "v_accvgpr_read_b32 v50, a70\n\t" \
  "v_mfma_f32_16x16x32_bf16 a[108:111], a[152:155], a[236:239], a[108:111]\n\t" \
  "buffer_load_dwordx4 a[212:215], v249, %[ws], s97 offen sc1\n\t" \
  "v_mfma_f32_16x16x32_bf16 a[124:127], a[156:159], a[236:239], a[124:127]\n\t" \
  "ds_read_b64_tr_b16 a[174:175], v195 offset:9216\n\t" \
  "v_accvgpr_read_b32 v51, a71\n\t" \
  "v_mfma_f32_16x16x32_bf16 a[120:123], a[156:159], a[232:235], a[120:123]\n\t" \
  "ds_read_b64_tr_b16 a[176:177], v196 offset:8192\n\t" \
  "v_accvgpr_read_b32 v80, a72\n\t" \
  "v_mfma_f32_16x16x32_bf16 a[116:119], a[156:159], a[228:231], a[116:119]\n\t" \
  "ds_read_b64_tr_b16 a[178:179], v196 offset:9216\n\t" \
  "v_accvgpr_read_b32 v81, a73\n\t" \
  "v_mfma_f32_16x16x32_bf16 a[112:115], a[156:159], a[224:227], a[112:115]\n\t" \
  "ds_read_b64_tr_b16 a[180:181], v197 offset:8192\n\t" \
  "v_accvgpr_read_b32 v82, a74\n\t" \
  "v_mfma_f32_16x16x32_bf16 v[0:3], a[128:131], a[224:227], v[0:3]\n\t" \
  "buffer_load_dwordx4 a[216:219], v250, %[ws], s97 offen sc1\n\t" \
  "v_mfma_f32_16x16x32_bf16 v[32:35], a[128:131], a[228:231], v[32:35]\n\t" \
  "ds_read_b64_tr_b16 a[182:183], v197 offset:9216\n\t" \
  "v_accvgpr_read_b32 v83, a75\n\t" \
  "v_mfma_f32_16x16x32_bf16 v[64:67], a[128:131], a[232:235], v[64:67]\n\t" \
  "ds_read_b64_tr_b16 a[184:185], v198 offset:8192\n\t" \
  "v_accvgpr_read_b32 v112, a76\n\t" \
  "v_mfma_f32_16x16x32_bf16 v[96:99], a[128:131], a[236:239], v[96:99]\n\t" \
  "v_accvgpr_read_b32 v113, a77\n\t" \
  "v_accvgpr_read_b32 v114, a78\n\t" \
  "v_mfma_f32_16x16x32_bf16 v[100:103], a[132:135], a[236:239], v[100:103]\n\t" \
  "ds_read_b64_tr_b16 a[186:187], v198 offset:9216\n\t" \
  "v_accvgpr_read_b32 v115, a79\n\t" \
  "v_mfma_f32_16x16x32_bf16 v[68:71], a[132:135], a[232:235], v[68:71]\n\t" \
  "ds_read_b64_tr_b16 a[188:189], v199 offset:8192\n\t" \
  "v_accvgpr_read_b32 v116, a92\n\t" \
  "v_mfma_f32_16x16x32_bf16 v[36:39], a[132:135], a[228:231], v[36:39]\n\t" \
  "buffer_load_dwordx4 a[220:223], v251, %[ws], s97 offen sc1\n\t" \
  "v_mfma_f32_16x16x32_bf16 v[4:7], a[132:135], a[224:227], v[4:7]\n\t" \
  "ds_read_b64_tr_b16 a[190:191], v199 offset:9216\n\t" \
  "v_accvgpr_read_b32 v117, a93\n\t" \
  "v_mfma_f32_16x16x32_bf16 v[8:11], a[136:139], a[224:227], v[8:11]\n\t" \
  "v_accvgpr_read_b32 v118, a94\n\t" \
  "v_accvgpr_read_b32 v119, a95\n\t" \
  "v_mfma_f32_16x16x32_bf16 v[40:43], a[136:139], a[228:231], v[40:43]\n\t" \
  "v_accvgpr_read_b32 v84, a88\n\t" \
  "v_accvgpr_read_b32 v85, a89\n\t" \
  "v_mfma_f32_16x16x32_bf16 v[72:75], a[136:139], a[232:235], v[72:75]\n\t" \
  "v_accvgpr_read_b32 v86, a90\n\t" \
  "v_accvgpr_read_b32 v87, a91\n\t" \
  "v_mfma_f32_16x16x32_bf16 v[104:107], a[136:139], a[236:239], v[104:107]\n\t" \
  "v_accvgpr_read_b32 v52, a84\n\t" \
  "v_accvgpr_read_b32 v53, a85\n\t" \
  "v_mfma_f32_16x16x32_bf16 v[108:111], a[140:143], a[236:239], v[108:111]\n\t" \
  "v_accvgpr_read_b32 v54, a86\n\t" \
  "v_accvgpr_read_b32 v55, a87\n\t" \
  "v_mfma_f32_16x16x32_bf16 v[76:79], a[140:143], a[232:235], v[76:79]\n\t" \
  "v_accvgpr_read_b32 v20, a80\n\t" \
  "v_accvgpr_read_b32 v21, a81\n\t" \
  "v_mfma_f32_16x16x32_bf16 v[44:47], a[140:143], a[228:231], v[44:47]\n\t" \
  "v_accvgpr_read_b32 v22, a82\n\t" \
  "v_accvgpr_read_b32 v23, a83\n\t" \
  "v_mfma_f32_16x16x32_bf16 v[12:15], a[140:143], a[224:227], v[12:15]\n\t" \
  "s_waitcnt vmcnt(22) lgkmcnt(0)\n\t" \
  "s_barrier\n\t" \
  "v_mfma_f32_16x16x32_bf16 a[96:99], a[184:187], a[240:243], a[96:99]\n\t" \
  "s_add_u32 s97, %[rot19], 3670016\n\t" \
  "ds_read_b64_tr_b16 a[128:129], v184 offset:16384\n\t" \
  "v_mfma_f32_16x16x32_bf16 a[100:103], a[184:187], a[244:247], a[100:103]\n\t" \
  "s_bfe_u32 s97, s97, 0x140003\n\t" \
  "v_mfma_f32_16x16x32_bf16 a[104:107], a[184:187], a[248:251], a[104:107]\n\t" \
  "s_add_u32 s97, s97, %[sox]\n\t" \
  "ds_read_b64_tr_b16 a[130:131], v184 offset:17408\n\t" \
  "v_mfma_f32_16x16x32_bf16 a[108:111], a[184:187], a[252:255], a[108:111]\n\t" \
  "buffer_load_dwordx4 a[224:227], v248, %[ws], s97 offen sc1\n\t" \
  "v_mfma_f32_16x16x32_bf16 a[124:127], a[188:191], a[252:255], a[124:127]\n\t" \
  "buffer_load_dwordx4 a[228:231], v249, %[ws], s97 offen sc1\n\t" \
  "v_mfma_f32_16x16x32_bf16 a[120:123], a[188:191], a[248:251], a[120:123]\n\t" \
  "ds_read_b64_tr_b16 a[132:133], v185 offset:16384\n\t" \
  "v_mfma_f32_16x16x32_bf16 a[116:119], a[188:191], a[244:247], a[116:119]\n\t" \
  "ds_read_b64_tr_b16 a[134:135], v185 offset:17408\n\t" \
  "v_mfma_f32_16x16x32_bf16 a[112:115], a[188:191], a[240:243], a[112:115]\n\t" \
  "buffer_load_dwordx4 a[232:235], v250, %[ws], s97 offen sc1\n\t" \
  "v_mfma_f32_16x16x32_bf16 v[0:3], a[160:163], a[240:243], v[0:3]\n\t" \
  "ds_read_b64_tr_b16 a[136:137], v186 offset:16384\n\t" \
  "v_accvgpr_read_b32 v24, a96\n\t" \
  "v_accvgpr_read_b32 v25, a97\n\t" \
  "v_mfma_f32_16x16x32_bf16 v[32:35], a[160:163], a[244:247], v[32:35]\n\t" \
  "ds_read_b64_tr_b16 a[138:139], v186 offset:17408\n\t" \
  "v_accvgpr_read_b32 v26, a98\n\t" \
  "v_accvgpr_read_b32 v27, a99\n\t" \
  "v_mfma_f32_16x16x32_bf16 v[64:67], a[160:163], a[248:251], v[64:67]\n\t" \
  "ds_read_b64_tr_b16 a[140:141], v187 offset:16384\n\t" \
  "v_accvgpr_read_b32 v56, a100\n\t" \
  "v_accvgpr_read_b32 v57, a101\n\t" \
  "v_mfma_f32_16x16x32_bf16 v[96:99], a[160:163], a[252:255], v[96:99]\n\t" \
  "buffer_load_dwordx4 a[236:239], v251, %[ws], s97 offen sc1\n\t" \
  "v_accvgpr_read_b32 v58, a102\n\t" \
  "v_mfma_f32_16x16x32_bf16 v[100:103], a[164:167], a[252:255], v[100:103]\n\t" \
  "s_add_u32 s96, %[rot19], 5242880\n\t" \
  "ds_read_b64_tr_b16 a[142:143], v187 offset:17408\n\t" \
  "v_accvgpr_read_b32 v59, a103\n\t" \
  "v_accvgpr_read_b32 v88, a104\n\t" \
  "v_mfma_f32_16x16x32_bf16 v[68:71], a[164:167], a[248:251], v[68:71]\n\t" \
  "s_and_b32 s96, s96, 0x780000\n\t" \
  "ds_read_b64_tr_b16 a[144:145], v188 offset:16384\n\t" \
  "v_accvgpr_read_b32 v89, a105\n\t" \
  "v_accvgpr_read_b32 v90, a106\n\t" \
  "v_mfma_f32_16x16x32_bf16 v[36:39], a[164:167], a[244:247], v[36:39]\n\t" \
  "s_add_u32 m0, %[mw], 98304\n\t" \
  "ds_read_b64_tr_b16 a[146:147], v188 offset:17408\n\t" \
  "v_accvgpr_read_b32 v91, a107\n\t" \
  "v_accvgpr_read_b32 v120, a108\n\t" \
  "v_mfma_f32_16x16x32_bf16 v[4:7], a[164:167], a[240:243], v[4:7]\n\t" \
  "buffer_load_dwordx4 v200, %[sw], s96 offen offset:0 lds\n\t" \
  "v_accvgpr_read_b32 v121, a109\n\t" \
  "v_mfma_f32_16x16x32_bf16 v[8:11], a[168:171], a[240:243], v[8:11]\n\t" \
  "ds_read_b64_tr_b16 a[148:149], v189 offset:16384\n\t" \
  "v_accvgpr_read_b32 v122, a110\n\t" \
  "v_mfma_f32_16x16x32_bf16 v[40:43], a[168:171], a[244:247], v[40:43]\n\t" \
  "ds_read_b64_tr_b16 a[150:151], v189 offset:17408\n\t" \
  "v_accvgpr_read_b32 v123, a111\n\t" \
  "v_mfma_f32_16x16x32_bf16 v[72:75], a[168:171], a[248:251], v[72:75]\n\t" \
  "ds_read_b64_tr_b16 a[152:153], v190 offset:16384\n\t" \
  "v_accvgpr_read_b32 v124, a124\n\t" \
  "v_mfma_f32_16x16x32_bf16 v[104:107], a[168:171], a[252:255], v[104:107]\n\t" \
  "buffer_load_dwordx4 v201, %[sw], s96 offen offset:1024 lds\n\t" \
  "v_mfma_f32_16x16x32_bf16 v[108:111], a[172:175], a[252:255], v[108:111]\n\t" \
  "ds_read_b64_tr_b16 a[154:155], v190 offset:17408\n\t" \
  "v_accvgpr_read_b32 v125, a125\n\t" \
  "v_mfma_f32_16x16x32_bf16 v[76:79], a[172:175], a[248:251], v[76:79]\n\t" \
  "ds_read_b64_tr_b16 a[156:157], v191 offset:16384\n\t" \
  "v_accvgpr_read_b32 v126, a126\n\t" \
  "v_mfma_f32_16x16x32_bf16 v[44:47], a[172:175], a[244:247], v[44:47]\n\t" \
  "v_accvgpr_read_b32 v127, a127\n\t" \
  "v_accvgpr_read_b32 v92, a120\n\t" \
  "v_mfma_f32_16x16x32_bf16 v[12:15], a[172:175], a[240:243], v[12:15]\n\t" \
  "buffer_load_dwordx4 v202, %[sw], s96 offen offset:2048 lds\n\t" \
  "v_mfma_f32_16x16x32_bf16 v[16:19], a[176:179], a[240:243], v[16:19]\n\t" \
  "ds_read_b64_tr_b16 a[158:159], v191 offset:17408\n\t" \
  "v_accvgpr_read_b32 v93, a121\n\t" \
  "v_mfma_f32_16x16x32_bf16 v[48:51], a[176:179], a[244:247], v[48:51]\n\t" \
  "v_accvgpr_read_b32 v94, a122\n\t" \
  "v_accvgpr_read_b32 v95, a123\n\t" \
  "v_mfma_f32_16x16x32_bf16 v[80:83], a[176:179], a[248:251], v[80:83]\n\t" \
  "v_accvgpr_read_b32 v60, a116\n\t" \
  "v_accvgpr_read_b32 v61, a117\n\t" \
  "v_mfma_f32_16x16x32_bf16 v[112:115], a[176:179], a[252:255], v[112:115]\n\t" \
  "buffer_load_dwordx4 v203, %[sw], s96 offen offset:3072 lds\n\t" \
  "v_mfma_f32_16x16x32_bf16 v[116:119], a[180:183], a[252:255], v[116:119]\n\t" \
  "v_accvgpr_read_b32 v62, a118\n\t" \
  "v_accvgpr_read_b32 v63, a119\n\t" \
  "v_mfma_f32_16x16x32_bf16 v[84:87], a[180:183], a[248:251], v[84:87]\n\t" \
  "v_accvgpr_read_b32 v28, a112\n\t" \
  "v_accvgpr_read_b32 v29, a113\n\t" \
  "v_mfma_f32_16x16x32_bf16 v[52:55], a[180:183], a[244:247], v[52:55]\n\t" \
  "v_accvgpr_read_b32 v30, a114\n\t" \
  "v_accvgpr_read_b32 v31, a115\n\t" \
  "v_mfma_f32_16x16x32_bf16 v[20:23], a[180:183], a[240:243], v[20:23]\n\t" \
  "s_waitcnt vmcnt(17) lgkmcnt(0)\n\t" \
  "v_mfma_f32_16x16x32_bf16 v[0:3], a[128:131], a[192:195], v[0:3]\n\t" \
  "s_add_u32 s97, s97, %[dk]\n\t" \
  "ds_read_b64_tr_b16 a[160:161], v184 offset:24576\n\t" \
  "v_mfma_f32_16x16x32_bf16 v[32:35], a[128:131], a[196:199], v[32:35]\n\t" \
  "buffer_load_dwordx4 a[240:243], v248, %[ws], s97 offen sc1\n\t" \
  "v_mfma_f32_16x16x32_bf16 v[64:67], a[128:131], a[200:203], v[64:67]\n\t" \
  "ds_read_b64_tr_b16 a[162:163], v184 offset:25600\n\t" \
  "v_mfma_f32_16x16x32_bf16 v[96:99], a[128:131], a[204:207], v[96:99]\n\t" \
  "buffer_load_dwordx4 a[244:247], v249, %[ws], s97 offen sc1\n\t" \
  "v_mfma_f32_16x16x32_bf16 v[100:103], a[132:135], a[204:207], v[100:103]\n\t" \
  "ds_read_b64_tr_b16 a[164:165], v185 offset:24576\n\t" \
  "v_mfma_f32_16x16x32_bf16 v[68:71], a[132:135], a[200:203], v[68:71]\n\t" \
  "ds_read_b64_tr_b16 a[166:167], v185 offset:25600\n\t" \
  "v_mfma_f32_16x16x32_bf16 v[36:39], a[132:135], a[196:199], v[36:39]\n\t" \
  "ds_read_b64_tr_b16 a[168:169], v186 offset:24576\n\t" \
  "v_mfma_f32_16x16x32_bf16 v[4:7], a[132:135], a[192:195], v[4:7]\n\t" \
  "buffer_load_dwordx4 a[248:251], v250, %[ws], s97 offen sc1\n\t" \
  "v_mfma_f32_16x16x32_bf16 v[8:11], a[136:139], a[192:195], v[8:11]\n\t" \
  "ds_read_b64_tr_b16 a[170:171], v186 offset:25600\n\t" \
  "v_mfma_f32_16x16x32_bf16 v[40:43], a[136:139], a[196:199], v[40:43]\n\t" \
  "ds_read_b64_tr_b16 a[172:173], v187 offset:24576\n\t" \
  "v_mfma_f32_16x16x32_bf16 v[72:75], a[136:139], a[200:203], v[72:75]\n\t" \
  "v_mfma_f32_16x16x32_bf16 v[104:107], a[136:139], a[204:207], v[104:107]\n\t" \
  "buffer_load_dwordx4 a[252:255], v251, %[ws], s97 offen sc1\n\t" \
  "v_mfma_f32_16x16x32_bf16 v[108:111], a[140:143], a[204:207], v[108:111]\n\t" \
  "s_add_u32 s97, %[rot19], 4194304\n\t" \
  "ds_read_b64_tr_b16 a[174:175], v187 offset:25600\n\t" \
  "v_mfma_f32_16x16x32_bf16 v[76:79], a[140:143], a[200:203], v[76:79]\n\t" \
  "s_bfe_u32 s97, s97, 0x140003\n\t" \
  "ds_read_b64_tr_b16 a[176:177], v188 offset:24576\n\t" \
  "v_mfma_f32_16x16x32_bf16 v[44:47], a[140:143], a[196:199], v[44:47]\n\t" \
  "s_add_u32 s97, s97, %[sox]\n\t" \
  "ds_read_b64_tr_b16 a[178:179], v188 offset:25600\n\t" \
  "v_mfma_f32_16x16x32_bf16 v[12:15], a[140:143], a[192:195], v[12:15]\n\t" \
  "buffer_load_dwordx4 a[32:35], v248, %[ws], s97 offen sc1\n\t" \
  "v_mfma_f32_16x16x32_bf16 v[16:19], a[144:147], a[192:195], v[16:19]\n\t" \
  "ds_read_b64_tr_b16 a[180:181], v189 offset:24576\n\t" \
  "v_mfma_f32_16x16x32_bf16 v[48:51], a[144:147], a[196:199], v[48:51]\n\t" \
  "ds_read_b64_tr_b16 a[182:183], v189 offset:25600\n\t" \
  "v_mfma_f32_16x16x32_bf16 v[80:83], a[144:147], a[200:203], v[80:83]\n\t" \
  "ds_read_b64_tr_b16 a[184:185], v190 offset:24576\n\t" \
  "v_mfma_f32_16x16x32_bf16 v[112:115], a[144:147], a[204:207], v[112:115]\n\t" \
  "buffer_load_dwordx4 a[36:39], v249, %[ws], s97 offen sc1\n\t" \
  "v_mfma_f32_16x16x32_bf16 v[116:119], a[148:151], a[204:207], v[116:119]\n\t" \
  "ds_read_b64_tr_b16 a[186:187], v190 offset:25600\n\t" \
  "v_mfma_f32_16x16x32_bf16 v[84:87], a[148:151], a[200:203], v[84:87]\n\t" \
  "ds_read_b64_tr_b16 a[188:189], v191 offset:24576\n\t" \
  "v_mfma_f32_16x16x32_bf16 v[52:55], a[148:151], a[196:199], v[52:55]\n\t" \
  "v_mfma_f32_16x16x32_bf16 v[20:23], a[148:151], a[192:195], v[20:23]\n\t" \
  "buffer_load_dwordx4 a[40:43], v250, %[ws], s97 offen sc1\n\t" \
  "v_mfma_f32_16x16x32_bf16 v[24:27], a[152:155], a[192:195], v[24:27]\n\t" \
  "ds_read_b64_tr_b16 a[190:191], v191 offset:25600\n\t" \
  "v_mfma_f32_16x16x32_bf16 v[56:59], a[152:155], a[196:199], v[56:59]\n\t" \
  "v_mfma_f32_16x16x32_bf16 v[88:91], a[152:155], a[200:203], v[88:91]\n\t" \
  "v_mfma_f32_16x16x32_bf16 v[120:123], a[152:155], a[204:207], v[120:123]\n\t" \
  "buffer_load_dwordx4 a[44:47], v251, %[ws], s97 offen sc1\n\t" \
  "v_mfma_f32_16x16x32_bf16 v[124:127], a[156:159], a[204:207], v[124:127]\n\t" \
  "v_mfma_f32_16x16x32_bf16 v[92:95], a[156:159], a[200:203], v[92:95]\n\t" \
  "v_mfma_f32_16x16x32_bf16 v[60:63], a[156:159], a[196:199], v[60:63]\n\t" \
  "v_mfma_f32_16x16x32_bf16 v[28:31], a[156:159], a[192:195], v[28:31]\n\t" \
  "s_waitcnt vmcnt(16) lgkmcnt(0)\n\t" \
  "s_barrier\n\t" \
  "v_mfma_f32_16x16x32_bf16 v[0:3], a[160:163], a[208:211], v[0:3]\n\t" \
  "s_add_u32 s97, s97, %[dk]\n\t" \
  "ds_read_b64_tr_b16 a[128:129], v184 offset:32768\n\t" \
  "ds_read_b64_tr_b16 a[130:131], v184 offset:33792\n\t" \
  "v_mfma_f32_16x16x32_bf16 v[32:35], a[160:163], a[212:215], v[32:35]\n\t" \
  "buffer_load_dwordx4 a[48:51], v248, %[ws], s97 offen sc1\n\t" \
  "v_mfma_f32_16x16x32_bf16 v[64:67], a[160:163], a[216:219], v[64:67]\n\t" \
  "s_bfe_u32 %[hl], %[bid], 0x30003\n\t" \
  "ds_read_b64_tr_b16 a[132:133], v185 offset:32768\n\t" \
  "ds_read_b64_tr_b16 a[134:135], v185 offset:33792\n\t" \
  "v_mfma_f32_16x16x32_bf16 v[96:99], a[160:163], a[220:223], v[96:99]\n\t" \
  "buffer_load_dwordx4 a[52:55], v249, %[ws], s97 offen sc1\n\t" \
  "v_mfma_f32_16x16x32_bf16 v[100:103], a[164:167], a[220:223], v[100:103]\n\t" \
  "s_lshl_b32 %[hl18], %[hl], 18\n\t" \
  "ds_read_b64_tr_b16 a[136:137], v186 offset:32768\n\t" \
  "ds_read_b64_tr_b16 a[138:139], v186 offset:33792\n\t" \
  "v_mfma_f32_16x16x32_bf16 v[68:71], a[164:167], a[216:219], v[68:71]\n\t" \
  "s_lshr_b32 s98, %[bid], 3\n\t" \
  "ds_read_b64_tr_b16 a[140:141], v187 offset:32768\n\t" \
  "ds_read_b64_tr_b16 a[142:143], v187 offset:33792\n\t" \
  "v_mfma_f32_16x16x32_bf16 v[36:39], a[164:167], a[212:215], v[36:39]\n\t" \
  "s_lshl_b32 %[cto], %[wid], 5\n\t" \
  "ds_read_b64_tr_b16 a[144:145], v188 offset:32768\n\t" \
  "ds_read_b64_tr_b16 a[146:147], v188 offset:33792\n\t" \
  "v_mfma_f32_16x16x32_bf16 v[4:7], a[164:167], a[208:211], v[4:7]\n\t" \
  "buffer_load_dwordx4 a[56:59], v250, %[ws], s97 offen sc1\n\t" \
  "v_mfma_f32_16x16x32_bf16 v[8:11], a[168:171], a[208:211], v[8:11]\n\t" \
  "s_add_u32 %[cto], %[cto], s98\n\t" \
  "ds_read_b64_tr_b16 a[148:149], v189 offset:32768\n\t" \
  "ds_read_b64_tr_b16 a[150:151], v189 offset:33792\n\t" \
  "v_mfma_f32_16x16x32_bf16 v[40:43], a[168:171], a[212:215], v[40:43]\n\t" \
  "s_lshl_b32 %[cto], %[cto], 7\n\t" \
  "ds_read_b64_tr_b16 a[152:153], v190 offset:32768\n\t" \
  "ds_read_b64_tr_b16 a[154:155], v190 offset:33792\n\t" \
  "v_mfma_f32_16x16x32_bf16 v[72:75], a[168:171], a[216:219], v[72:75]\n\t" \
  "s_lshl2_add_u32 %[pf2], s98, %[wid]\n\t" \
  "ds_read_b64_tr_b16 a[156:157], v191 offset:32768\n\t" \
  "ds_read_b64_tr_b16 a[158:159], v191 offset:33792\n\t" \
  "v_mfma_f32_16x16x32_bf16 v[104:107], a[168:171], a[220:223], v[104:107]\n\t" \
  "buffer_load_dwordx4 a[60:63], v251, %[ws], s97 offen sc1\n\t" \
  "v_mfma_f32_16x16x32_bf16 v[108:111], a[172:175], a[220:223], v[108:111]\n\t" \
  "s_add_u32 s97, %[rot19], 4718592\n\t" \
  "ds_read_b64_tr_b16 a[64:65], v184 offset:40960\n\t" \
  "ds_read_b64_tr_b16 a[66:67], v184 offset:41984\n\t" \
  "v_mfma_f32_16x16x32_bf16 v[76:79], a[172:175], a[216:219], v[76:79]\n\t" \
  "s_bfe_u32 s97, s97, 0x140003\n\t" \
  "ds_read_b64_tr_b16 a[68:69], v185 offset:40960\n\t" \
  "ds_read_b64_tr_b16 a[70:71], v185 offset:41984\n\t" \
  "v_mfma_f32_16x16x32_bf16 v[44:47], a[172:175], a[212:215], v[44:47]\n\t" \
  "s_add_u32 s97, s97, %[sox]\n\t" \
  "v_mbcnt_lo_u32_b32 v233, -1, 0\n\t" \
  "v_mfma_f32_16x16x32_bf16 v[12:15], a[172:175], a[208:211], v[12:15]\n\t" \
  "buffer_load_dwordx4 a[96:99], v248, %[ws], s97 offen sc1\n\t" \
  "v_mfma_f32_16x16x32_bf16 v[16:19], a[176:179], a[208:211], v[16:19]\n\t" \
  "s_mul_i32 %[pf2], %[pf2], 98304\n\t" \
  "ds_read_b64_tr_b16 a[72:73], v186 offset:40960\n\t" \
  "ds_read_b64_tr_b16 a[74:75], v186 offset:41984\n\t" \
  "v_mfma_f32_16x16x32_bf16 v[48:51], a[176:179], a[212:215], v[48:51]\n\t" \
  "s_lshr_b32 s98, %[bid], 2\n\t" \
  "ds_read_b64_tr_b16 a[76:77], v187 offset:40960\n\t" \
  "ds_read_b64_tr_b16 a[78:79], v187 offset:41984\n\t" \
  "v_mfma_f32_16x16x32_bf16 v[80:83], a[176:179], a[216:219], v[80:83]\n\t" \
  "s_xor_b32 s98, s98, %[wid]\n\t" \
  "ds_read_b64_tr_b16 a[80:81], v188 offset:40960\n\t" \
  "v_mbcnt_hi_u32_b32 v233, -1, v233\n\t" \
  "v_mfma_f32_16x16x32_bf16 v[112:115], a[176:179], a[220:223], v[112:115]\n\t" \
  "buffer_load_dwordx4 a[100:103], v249, %[ws], s97 offen sc1\n\t" \
  "v_mfma_f32_16x16x32_bf16 v[116:119], a[180:183], a[220:223], v[116:119]\n\t" \
  "s_lshl_b32 s98, s98, 13\n\t" \
  "ds_read_b64_tr_b16 a[82:83], v188 offset:41984\n\t" \
  "ds_read_b64_tr_b16 a[84:85], v189 offset:40960\n\t" \
  "v_mfma_f32_16x16x32_bf16 v[84:87], a[180:183], a[216:219], v[84:87]\n\t" \
  "s_and_b32 %[po], s98, 0x2000\n\t" \
  "ds_read_b64_tr_b16 a[86:87], v189 offset:41984\n\t" \
  "v_lshrrev_b32 v129, 4, v233\n\t" \
  "v_mfma_f32_16x16x32_bf16 v[52:55], a[180:183], a[212:215], v[52:55]\n\t" \
  "s_lshr_b32 s98, %[bid], 6\n\t" \
  "ds_read_b64_tr_b16 a[88:89], v190 offset:40960\n\t" \
  "v_and_b32 v130, 15, v233\n\t" \
  "v_mfma_f32_16x16x32_bf16 v[20:23], a[180:183], a[208:211], v[20:23]\n\t" \
  "buffer_load_dwordx4 a[104:107], v250, %[ws], s97 offen sc1\n\t" \
  "v_mfma_f32_16x16x32_bf16 v[24:27], a[184:187], a[208:211], v[24:27]\n\t" \
  "s_lshl_b32 s98, s98, 22\n\t" \
  "ds_read_b64_tr_b16 a[90:91], v190 offset:41984\n\t" \
  "ds_read_b64_tr_b16 a[92:93], v191 offset:40960\n\t" \
  "v_mfma_f32_16x16x32_bf16 v[56:59], a[184:187], a[212:215], v[56:59]\n\t" \
  "s_add_u32 %[po], %[po], s98\n\t" \
  "ds_read_b64_tr_b16 a[94:95], v191 offset:41984\n\t" \
  "v_bfe_u32 v131, v233, 2, 2\n\t" \
  "v_mfma_f32_16x16x32_bf16 v[88:91], a[184:187], a[216:219], v[88:91]\n\t" \
  "s_lshl_b32 %[xcb], %[wid], 15\n\t" \
  "v_and_b32 v132, 3, v233\n\t" \
  "v_mfma_f32_16x16x32_bf16 v[120:123], a[184:187], a[220:223], v[120:123]\n\t" \
  "buffer_load_dwordx4 a[108:111], v251, %[ws], s97 offen sc1\n\t" \
  "v_mfma_f32_16x16x32_bf16 v[124:127], a[188:191], a[220:223], v[124:127]\n\t" \
  "s_add_u32 %[xcb], %[xcb], 0x1800000\n\t" \
  "v_cmp_lt_u32 vcc, 6, v233\n\t" \
  "v_mfma_f32_16x16x32_bf16 v[92:95], a[188:191], a[216:219], v[92:95]\n\t" \
  "s_add_u32 %[xcb], %[xcb], %[po]\n\t" \
  "v_mov_b32 v133, 6272\n\t" \
  "v_mfma_f32_16x16x32_bf16 v[60:63], a[188:191], a[212:215], v[60:63]\n\t" \
  "s_and_b32 s98, %[bid], 0x38\n\t" \
  "v_cndmask_b32 v133, 0, v133, vcc\n\t" \
  "v_mfma_f32_16x16x32_bf16 v[28:31], a[188:191], a[208:211], v[28:31]\n\t" \
  "s_waitcnt vmcnt(12) lgkmcnt(0)\n\t" \
  "s_barrier\n\t" \
  "v_mfma_f32_16x16x32_bf16 v[0:3], a[128:131], a[224:227], v[0:3]\n\t" \
  "s_add_u32 s97, s97, %[dk]\n\t" \
  "ds_read_b64_tr_b16 a[160:161], v184 offset:0\n\t" \
  "v_lshl_add_u32 v133, v233, 7, v133\n\t" \
  "v_mfma_f32_16x16x32_bf16 v[0:3], a[64:67], a[240:243], v[0:3]\n\t" \
  "buffer_load_dwordx4 a[112:115], v248, %[ws], s97 offen sc1\n\t" \
  "v_mfma_f32_16x16x32_bf16 v[32:35], a[64:67], a[244:247], v[32:35]\n\t" \
  "s_lshl_b32 s98, s98, 16\n\t" \
  "ds_read_b64_tr_b16 a[162:163], v184 offset:1024\n\t" \
  "v_cmp_gt_u32 vcc, 14, v233\n\t" \
  "v_mfma_f32_16x16x32_bf16 v[32:35], a[128:131], a[228:231], v[32:35]\n\t" \
  "buffer_load_dwordx4 a[116:119], v249, %[ws], s97 offen sc1\n\t" \
  "v_mfma_f32_16x16x32_bf16 v[64:67], a[128:131], a[232:235], v[64:67]\n\t" \
  "s_add_u32 %[xcb], %[xcb], s98\n\t" \
  "ds_read_b64_tr_b16 a[164:165], v185 offset:0\n\t" \
  "v_bfrev_b32 v247, 1\n\t" \
  "v_mfma_f32_16x16x32_bf16 v[64:67], a[64:67], a[248:251], v[64:67]\n\t" \
  "s_add_u32 %[otb], %[po], 0x800000\n\t" \
  "ds_read_b64_tr_b16 a[166:167], v185 offset:1024\n\t" \
  "v_cndmask_b32 v245, v247, v133, vcc\n\t" \
  "v_mfma_f32_16x16x32_bf16 v[96:99], a[64:67], a[252:255], v[96:99]\n\t" \
  "s_and_b32 s98, %[bid], 0x3\n\t" \
  "ds_read_b64_tr_b16 a[168:169], v186 offset:0\n\t" \
  "v_writelane_b32 v247, %[prb], 0\n\t" \
  "v_mfma_f32_16x16x32_bf16 v[96:99], a[128:131], a[236:239], v[96:99]\n\t" \
  "buffer_load_dwordx4 a[120:123], v250, %[ws], s97 offen sc1\n\t" \
  "v_mfma_f32_16x16x32_bf16 v[4:7], a[132:135], a[224:227], v[4:7]\n\t" \
  "s_lshl_b32 s98, s98, 20\n\t" \
  "ds_read_b64_tr_b16 a[170:171], v186 offset:1024\n\t" \
  "v_lshrrev_b32 v133, 1, v129\n\t" \
  "v_mfma_f32_16x16x32_bf16 v[4:7], a[68:71], a[240:243], v[4:7]\n\t" \
  "s_add_u32 %[otb], %[otb], s98\n\t" \
  "ds_read_b64_tr_b16 a[172:173], v187 offset:0\n\t" \
  "v_and_b32 v134, 1, v129\n\t" \
  "v_mfma_f32_16x16x32_bf16 v[36:39], a[68:71], a[244:247], v[36:39]\n\t" \
  "s_and_b32 s98, %[wid], 2\n\t" \
  "v_lshl_or_b32 v133, v134, 1, v133\n\t" \
  "v_mfma_f32_16x16x32_bf16 v[36:39], a[132:135], a[228:231], v[36:39]\n\t" \
  "buffer_load_dwordx4 a[124:127], v251, %[ws], s97 offen sc1\n\t" \
  "v_mfma_f32_16x16x32_bf16 v[68:71], a[132:135], a[232:235], v[68:71]\n\t" \
  "s_add_u32 s96, %[rot19], 5767168\n\t" \
  "ds_read_b64_tr_b16 a[174:175], v187 offset:1024\n\t" \
  "v_xor_b32 v133, v133, v130\n\t" \
  "v_mfma_f32_16x16x32_bf16 v[68:71], a[68:71], a[248:251], v[68:71]\n\t" \
  "s_and_b32 s96, s96, 0x780000\n\t" \
  "ds_read_b64_tr_b16 a[176:177], v188 offset:0\n\t" \
  "v_lshlrev_b32 v133, 4, v133\n\t" \
  "v_mfma_f32_16x16x32_bf16 v[100:103], a[68:71], a[252:255], v[100:103]\n\t" \
  "s_add_u32 m0, %[mw], 16384\n\t" \
  "ds_read_b64_tr_b16 a[178:179], v188 offset:1024\n\t" \
  "v_lshl_add_u32 v133, v130, 8, v133\n\t" \
  "v_mfma_f32_16x16x32_bf16 v[100:103], a[132:135], a[236:239], v[100:103]\n\t" \
  "buffer_load_dwordx4 v200, %[sw], s96 offen offset:0 lds\n\t" \
  "v_mfma_f32_16x16x32_bf16 v[8:11], a[136:139], a[224:227], v[8:11]\n\t" \
  "s_lshl_b32 s98, s98, 13\n\t" \
  "ds_read_b64_tr_b16 a[180:181], v189 offset:0\n\t" \
  "v_add_u32 v220, 49152, v133\n\t" \
  "v_mfma_f32_16x16x32_bf16 v[8:11], a[72:75], a[240:243], v[8:11]\n\t" \
  "s_add_u32 %[otb], %[otb], s98\n\t" \
  "ds_read_b64_tr_b16 a[182:183], v189 offset:1024\n\t" \
  "v_xor_b32 v221, 64, v220\n\t" \
  "v_mfma_f32_16x16x32_bf16 v[40:43], a[72:75], a[244:247], v[40:43]\n\t" \
  "s_and_b32 s98, %[wid], 1\n\t" \
  "ds_read_b64_tr_b16 a[184:185], v190 offset:0\n\t" \
  "v_xor_b32 v222, 128, v220\n\t" \
  "v_mfma_f32_16x16x32_bf16 v[40:43], a[136:139], a[228:231], v[40:43]\n\t" \
  "buffer_load_dwordx4 v201, %[sw], s96 offen offset:1024 lds\n\t" \
  "v_mfma_f32_16x16x32_bf16 v[72:75], a[136:139], a[232:235], v[72:75]\n\t" \
  "s_lshl_b32 s98, s98, 12\n\t" \
  "ds_read_b64_tr_b16 a[186:187], v190 offset:1024\n\t" \
  "v_xor_b32 v223, 192, v220\n\t" \
  "v_mfma_f32_16x16x32_bf16 v[72:75], a[72:75], a[248:251], v[72:75]\n\t" \
  "s_add_u32 %[otb], %[otb], s98\n\t" \
  "ds_read_b64_tr_b16 a[188:189], v191 offset:0\n\t" \
  "v_xor_b32 v133, v130, v129\n\t" \
  "v_mfma_f32_16x16x32_bf16 v[104:107], a[72:75], a[252:255], v[104:107]\n\t" \
  "s_lshr_b32 %[eso], %[c17], 2\n\t" \
  "v_lshlrev_b32 v133, 4, v133\n\t" \
  "v_mfma_f32_16x16x32_bf16 v[104:107], a[136:139], a[236:239], v[104:107]\n\t" \
  "buffer_load_dwordx4 v202, %[sw], s96 offen offset:2048 lds\n\t" \
  "v_mfma_f32_16x16x32_bf16 v[12:15], a[140:143], a[224:227], v[12:15]\n\t" \
  "s_add_u32 %[eso], %[eso], %[otb]\n\t" \
  "ds_read_b64_tr_b16 a[190:191], v191 offset:1024\n\t" \
  "v_lshl_add_u32 v224, v129, 8, v133\n\t" \
  "v_mfma_f32_16x16x32_bf16 v[12:15], a[76:79], a[240:243], v[12:15]\n\t" \
  "s_and_b32 s98, %[bid], 0x38\n\t" \
  "ds_read_b64_tr_b16 a[0:1], v184 offset:8192\n\t" \
  "v_xor_b32 v225, 64, v224\n\t" \
  "v_mfma_f32_16x16x32_bf16 v[44:47], a[76:79], a[244:247], v[44:47]\n\t" \
  "s_lshl_b32 s98, s98, 14\n\t" \
  "ds_read_b64_tr_b16 a[2:3], v184 offset:9216\n\t" \
  "v_xor_b32 v226, 128, v224\n\t" \
  "v_mfma_f32_16x16x32_bf16 v[44:47], a[140:143], a[228:231], v[44:47]\n\t" \
  "buffer_load_dwordx4 v203, %[sw], s96 offen offset:3072 lds\n\t" \
  "v_mfma_f32_16x16x32_bf16 v[76:79], a[140:143], a[232:235], v[76:79]\n\t" \
  "s_add_u32 s97, %[rot19], 5242880\n\t" \
  "ds_read_b64_tr_b16 a[4:5], v185 offset:8192\n\t" \
  "v_xor_b32 v227, 192, v224\n\t" \
  "v_mfma_f32_16x16x32_bf16 v[76:79], a[76:79], a[248:251], v[76:79]\n\t" \
  "s_bfe_u32 s97, s97, 0x140003\n\t" \
  "ds_read_b64_tr_b16 a[6:7], v185 offset:9216\n\t" \
  "v_lshlrev_b32 v133, 3, v132\n\t" \
  "v_mfma_f32_16x16x32_bf16 v[108:111], a[76:79], a[252:255], v[108:111]\n\t" \
  "s_add_u32 s97, s97, %[sox]\n\t" \
  "ds_read_b64_tr_b16 a[8:9], v186 offset:8192\n\t" \
  "v_lshl_add_u32 v133, v129, 10, v133\n\t" \
  "v_mfma_f32_16x16x32_bf16 v[108:111], a[140:143], a[236:239], v[108:111]\n\t" \
  "buffer_load_dwordx4 a[192:195], v248, %[ws], s97 offen sc1\n\t" \
  "v_mfma_f32_16x16x32_bf16 v[16:19], a[144:147], a[224:227], v[16:19]\n\t" \
  "s_add_u32 %[eso], %[eso], s98\n\t" \
  "ds_read_b64_tr_b16 a[10:11], v186 offset:9216\n\t" \
  "v_lshl_add_u32 v228, v131, 6, v133\n\t" \
  "v_mfma_f32_16x16x32_bf16 v[16:19], a[80:83], a[240:243], v[16:19]\n\t" \
  "s_lshr_b32 s98, %[bid], 1\n\t" \
  "ds_read_b64_tr_b16 a[12:13], v187 offset:8192\n\t" \
  "v_lshl_add_u32 v228, v134, 5, v228\n\t" \
  "v_mfma_f32_16x16x32_bf16 v[48:51], a[80:83], a[244:247], v[48:51]\n\t" \
  "s_xor_b32 s98, s98, %[wid]\n\t" \
  "v_add_u32 v228, 81920, v228\n\t" \
  "v_mfma_f32_16x16x32_bf16 v[48:51], a[144:147], a[228:231], v[48:51]\n\t" \
  "buffer_load_dwordx4 a[196:199], v249, %[ws], s97 offen sc1\n\t" \
  "v_mfma_f32_16x16x32_bf16 v[80:83], a[144:147], a[232:235], v[80:83]\n\t" \
  "s_lshl_b32 s98, s98, 13\n\t" \
  "ds_read_b64_tr_b16 a[14:15], v187 offset:9216\n\t" \
  "v_xor_b32 v229, 32, v228\n\t" \
  "v_mfma_f32_16x16x32_bf16 v[80:83], a[80:83], a[248:251], v[80:83]\n\t" \
  "s_and_b32 %[po], s98, 0x2000\n\t" \
  "ds_read_b64_tr_b16 a[16:17], v188 offset:8192\n\t" \
  "v_lshl_add_u32 v244, v131, 8, v133\n\t" \
  "v_mfma_f32_16x16x32_bf16 v[112:115], a[80:83], a[252:255], v[112:115]\n\t" \
  "s_lshr_b32 s98, %[bid], 5\n\t" \
  "ds_read_b64_tr_b16 a[18:19], v188 offset:9216\n\t" \
  "v_lshl_add_u32 v244, v131, 5, v244\n\t" \
  "v_mfma_f32_16x16x32_bf16 v[112:115], a[144:147], a[236:239], v[112:115]\n\t" \
  "buffer_load_dwordx4 a[200:203], v250, %[ws], s97 offen sc1\n\t" \
  "v_mfma_f32_16x16x32_bf16 v[20:23], a[148:151], a[224:227], v[20:23]\n\t" \
  "s_xor_b32 s98, s98, %[wid]\n\t" \
  "ds_read_b64_tr_b16 a[20:21], v189 offset:8192\n\t" \
  "v_lshl_add_u32 v244, v134, 7, v244\n\t" \
  "v_mfma_f32_16x16x32_bf16 v[20:23], a[84:87], a[240:243], v[20:23]\n\t" \
  "s_lshl_b32 s98, s98, 13\n\t" \
  "ds_read_b64_tr_b16 a[22:23], v189 offset:9216\n\t" \
  ".Lhs3:\n\ts_cbranch_execz .Lhs3n\n\t" \
  "v_lshlrev_b32 v133, 4, v132\n\t" \
  "v_mfma_f32_16x16x32_bf16 v[52:55], a[84:87], a[244:247], v[52:55]\n\t" \
  "s_and_b32 %[px], s98, 0x2000\n\t" \
  "ds_read_b64_tr_b16 a[24:25], v190 offset:8192\n\t" \
  "v_lshl_add_u32 v133, v129, 6, v133\n\t" \
  "v_mfma_f32_16x16x32_bf16 v[52:55], a[148:151], a[228:231], v[52:55]\n\t" \
  "buffer_load_dwordx4 a[204:207], v251, %[ws], s97 offen sc1\n\t" \
  "v_mfma_f32_16x16x32_bf16 v[84:87], a[148:151], a[232:235], v[84:87]\n\t" \
  "s_add_u32 s97, s97, %[dk]\n\t" \
  "ds_read_b64_tr_b16 a[26:27], v190 offset:9216\n\t" \
  "v_lshl_add_u32 v230, v131, 8, v133\n\t" \
  "v_mfma_f32_16x16x32_bf16 v[84:87], a[84:87], a[248:251], v[84:87]\n\t" \
  "s_lshl_b32 %[frb], %[wid], 2\n\t" \
  "ds_read_b64_tr_b16 a[28:29], v191 offset:8192\n\t" \
  "v_xor_b32 v231, 32, v230\n\t" \
  "v_mfma_f32_16x16x32_bf16 v[116:119], a[84:87], a[252:255], v[116:119]\n\t" \
  "s_add_u32 %[frb], %[frb], 0x8000\n\t" \
  "v_lshlrev_b32 v133, 1, v129\n\t" \
  "v_mfma_f32_16x16x32_bf16 v[116:119], a[148:151], a[236:239], v[116:119]\n\t" \
  "buffer_load_dwordx4 a[208:211], v248, %[ws], s97 offen sc1\n\t" \
  "v_mfma_f32_16x16x32_bf16 v[24:27], a[152:155], a[224:227], v[24:27]\n\t" \
  "s_and_b32 s98, %[bid], 0x80\n\t" \
  "ds_read_b64_tr_b16 a[30:31], v191 offset:9216\n\t" \
  "v_xor_b32 v133, v133, v130\n\t" \
  "v_mfma_f32_16x16x32_bf16 v[24:27], a[88:91], a[240:243], v[24:27]\n\t" \
  "s_lshl_b32 s98, s98, 7\n\t" \
  "v_lshlrev_b32 v133, 4, v133\n\t" \
  "v_mfma_f32_16x16x32_bf16 v[56:59], a[88:91], a[244:247], v[56:59]\n\t" \
  "s_add_u32 %[frb], %[frb], s98\n\t" \
  "v_lshl_add_u32 v240, v129, 11, v133\n\t" \
  "v_mfma_f32_16x16x32_bf16 v[56:59], a[152:155], a[228:231], v[56:59]\n\t" \
  "buffer_load_dwordx4 a[212:215], v249, %[ws], s97 offen sc1\n\t" \
  "v_mfma_f32_16x16x32_bf16 v[88:91], a[152:155], a[232:235], v[88:91]\n\t" \
  "s_and_b32 s98, %[bid], 0x4\n\t" \
  "v_xor_b32 v241, 0x80, v240\n\t" \
  "v_mfma_f32_16x16x32_bf16 v[88:91], a[88:91], a[248:251], v[88:91]\n\t" \
  "s_lshl_b32 s98, s98, 11\n\t" \
  "v_lshrrev_b32 v133, 1, v233\n\t" \
  "v_mfma_f32_16x16x32_bf16 v[120:123], a[88:91], a[252:255], v[120:123]\n\t" \
  "s_add_u32 %[frb], %[frb], s98\n\t" \
  "v_mul_u32_u24 v133, 57344, v133\n\t" \
  "v_mfma_f32_16x16x32_bf16 v[120:123], a[152:155], a[236:239], v[120:123]\n\t" \
  "buffer_load_dwordx4 a[216:219], v250, %[ws], s97 offen sc1\n\t" \
  "v_mfma_f32_16x16x32_bf16 v[28:31], a[156:159], a[224:227], v[28:31]\n\t" \
  "s_and_b32 s98, %[bid], 0x78\n\t" \
  "v_and_b32 v134, 1, v233\n\t" \
  "v_mfma_f32_16x16x32_bf16 v[28:31], a[92:95], a[240:243], v[28:31]\n\t" \
  "s_lshl_b32 s98, s98, 6\n\t" \
  "v_lshl_add_u32 v133, v134, 4, v133\n\t" \
  "v_mfma_f32_16x16x32_bf16 v[60:63], a[92:95], a[244:247], v[60:63]\n\t" \
  "s_add_u32 %[frb], %[frb], s98\n\t" \
  "v_or_b32 v232, %[idm], v133\n\t" \
  "v_mfma_f32_16x16x32_bf16 v[60:63], a[156:159], a[228:231], v[60:63]\n\t" \
  "buffer_load_dwordx4 a[220:223], v251, %[ws], s97 offen sc1\n\t" \
  "v_mfma_f32_16x16x32_bf16 v[92:95], a[156:159], a[232:235], v[92:95]\n\t" \
  "s_lshl_b32 %[fw2], %[wid], 7\n\t" \
  "v_mfma_f32_16x16x32_bf16 v[92:95], a[92:95], a[248:251], v[92:95]\n\t" \
  "s_add_u32 %[fw2], %[fw2], 0x20000\n\t" \
  "v_mfma_f32_16x16x32_bf16 v[124:127], a[92:95], a[252:255], v[124:127]\n\t" \
  "s_and_b32 s98, %[bid], 0xc0\n\t" \
  "v_mfma_f32_16x16x32_bf16 v[124:127], a[156:159], a[236:239], v[124:127]\n\t" \
  "s_waitcnt vmcnt(20) lgkmcnt(0)\n\t" \
  "s_barrier\n\t" \
  "v_mfma_f32_16x16x32_bf16 v[0:3], a[160:163], a[32:35], v[0:3]\n\t" \
  "s_add_u32 s96, %[rot19], 6291456\n\t" \
  "ds_read_b64_tr_b16 a[128:129], v184 offset:49152\n\t" \
  "v_mfma_f32_16x16x32_bf16 v[0:3], a[0:3], a[48:51], v[0:3]\n\t" \
  "s_and_b32 s96, s96, 0x780000\n\t" \
  "v_mfma_f32_16x16x32_bf16 v[32:35], a[0:3], a[52:55], v[32:35]\n\t" \
  "s_add_u32 m0, %[mw], 32768\n\t" \
  "ds_read_b64_tr_b16 a[130:131], v184 offset:50176\n\t" \
  "v_mfma_f32_16x16x32_bf16 v[32:35], a[160:163], a[36:39], v[32:35]\n\t" \
  "buffer_load_dwordx4 v200, %[sw], s96 offen offset:0 lds\n\t" \
  "v_mfma_f32_16x16x32_bf16 v[64:67], a[160:163], a[40:43], v[64:67]\n\t" \
  "buffer_load_dwordx4 v201, %[sw], s96 offen offset:1024 lds\n\t" \
  "v_mfma_f32_16x16x32_bf16 v[64:67], a[0:3], a[56:59], v[64:67]\n\t" \
  "s_lshl_b32 s98, s98, 9\n\t" \
  "ds_read_b64_tr_b16 a[132:133], v185 offset:49152\n\t" \
  "v_mfma_f32_16x16x32_bf16 v[96:99], a[0:3], a[60:63], v[96:99]\n\t" \
  "s_add_u32 %[fw2], %[fw2], s98\n\t" \
  "ds_read_b64_tr_b16 a[134:135], v185 offset:50176\n\t" \
  "v_mfma_f32_16x16x32_bf16 v[96:99], a[160:163], a[44:47], v[96:99]\n\t" \
  "s_and_b32 s98, %[bid], 0x2\n\t" \
  "ds_read_b64_tr_b16 a[136:137], v186 offset:49152\n\t" \
  "v_mfma_f32_16x16x32_bf16 v[4:7], a[164:167], a[32:35], v[4:7]\n\t" \
  "s_lshl_b32 s98, s98, 13\n\t" \
  "ds_read_b64_tr_b16 a[138:139], v186 offset:50176\n\t" \
  "v_mfma_f32_16x16x32_bf16 v[4:7], a[4:7], a[48:51], v[4:7]\n\t" \
  "buffer_load_dwordx4 v202, %[sw], s96 offen offset:2048 lds\n\t" \
  "v_mfma_f32_16x16x32_bf16 v[36:39], a[4:7], a[52:55], v[36:39]\n\t" \
  "s_add_u32 %[fw2], %[fw2], s98\n\t" \
  "ds_read_b64_tr_b16 a[140:141], v187 offset:49152\n\t" \
  "v_mfma_f32_16x16x32_bf16 v[36:39], a[164:167], a[36:39], v[36:39]\n\t" \
  "s_and_b32 s98, %[bid], 0x4\n\t" \
  "ds_read_b64_tr_b16 a[142:143], v187 offset:50176\n\t" \
  "v_mfma_f32_16x16x32_bf16 v[68:71], a[164:167], a[40:43], v[68:71]\n\t" \
  "s_lshl_b32 s98, s98, 11\n\t" \
  "ds_read_b64_tr_b16 a[144:145], v188 offset:49152\n\t" \
  "v_mfma_f32_16x16x32_bf16 v[68:71], a[4:7], a[56:59], v[68:71]\n\t" \
  "buffer_load_dwordx4 v203, %[sw], s96 offen offset:3072 lds\n\t" \
  "v_mfma_f32_16x16x32_bf16 v[100:103], a[4:7], a[60:63], v[100:103]\n\t" \
  "s_add_u32 s97, %[rot19], 5767168\n\t" \
  "ds_read_b64_tr_b16 a[146:147], v188 offset:50176\n\t" \
  "v_mfma_f32_16x16x32_bf16 v[100:103], a[164:167], a[44:47], v[100:103]\n\t" \
  "s_bfe_u32 s97, s97, 0x140003\n\t" \
  "ds_read_b64_tr_b16 a[148:149], v189 offset:49152\n\t" \
  "v_mfma_f32_16x16x32_bf16 v[8:11], a[168:171], a[32:35], v[8:11]\n\t" \
  "s_add_u32 s97, s97, %[sox]\n\t" \
  "v_mfma_f32_16x16x32_bf16 v[8:11], a[8:11], a[48:51], v[8:11]\n\t" \
  "s_add_u32 %[fw2], %[fw2], s98\n\t" \
  "ds_read_b64_tr_b16 a[150:151], v189 offset:50176\n\t" \
  "v_mfma_f32_16x16x32_bf16 v[40:43], a[8:11], a[52:55], v[40:43]\n\t" \
  "buffer_load_dwordx4 a[224:227], v248, %[ws], s97 offen sc1\n\t" \
  "v_mfma_f32_16x16x32_bf16 v[40:43], a[168:171], a[36:39], v[40:43]\n\t" \
  "s_and_b32 s98, %[bid], 0x1\n\t" \
  "ds_read_b64_tr_b16 a[152:153], v190 offset:49152\n\t" \
  "v_mfma_f32_16x16x32_bf16 v[72:75], a[168:171], a[40:43], v[72:75]\n\t" \
  "s_lshl_b32 s98, s98, 12\n\t" \
  "ds_read_b64_tr_b16 a[154:155], v190 offset:50176\n\t" \
  "v_mfma_f32_16x16x32_bf16 v[72:75], a[8:11], a[56:59], v[72:75]\n\t" \
  "s_add_u32 %[fw2], %[fw2], s98\n\t" \
  "ds_read_b64_tr_b16 a[156:157], v191 offset:49152\n\t" \
  "v_mfma_f32_16x16x32_bf16 v[104:107], a[8:11], a[60:63], v[104:107]\n\t" \
  "s_lshl2_add_u32 %[fw2], %[hl], %[fw2]\n\t" \
  "v_mfma_f32_16x16x32_bf16 v[104:107], a[168:171], a[44:47], v[104:107]\n\t" \
  "buffer_load_dwordx4 a[228:231], v249, %[ws], s97 offen sc1\n\t" \
  "v_mfma_f32_16x16x32_bf16 v[12:15], a[172:175], a[32:35], v[12:15]\n\t" \
  "s_lshl_b32 %[xso], %[wid], 15\n\t" \
  "ds_read_b64_tr_b16 a[158:159], v191 offset:50176\n\t" \
  "v_mfma_f32_16x16x32_bf16 v[12:15], a[12:15], a[48:51], v[12:15]\n\t" \
  "s_and_b32 s98, %[bid], 0x4\n\t" \
  "ds_read_b64_tr_b16 a[64:65], v184 offset:57344\n\t" \
  "v_mfma_f32_16x16x32_bf16 v[44:47], a[12:15], a[52:55], v[44:47]\n\t" \
  "s_lshl_b32 s98, s98, 19\n\t" \
  "ds_read_b64_tr_b16 a[66:67], v184 offset:58368\n\t" \
  "v_mfma_f32_16x16x32_bf16 v[44:47], a[172:175], a[36:39], v[44:47]\n\t" \
  "buffer_load_dwordx4 a[232:235], v250, %[ws], s97 offen sc1\n\t" \
  "v_mfma_f32_16x16x32_bf16 v[76:79], a[172:175], a[40:43], v[76:79]\n\t" \
  "s_add_u32 %[xso], %[xso], s98\n\t" \
  "ds_read_b64_tr_b16 a[68:69], v185 offset:57344\n\t" \
  "v_mfma_f32_16x16x32_bf16 v[76:79], a[12:15], a[56:59], v[76:79]\n\t" \
  "s_and_b32 s98, %[bid], 0xc0\n\t" \
  "ds_read_b64_tr_b16 a[70:71], v185 offset:58368\n\t" \
  "v_mfma_f32_16x16x32_bf16 v[108:111], a[12:15], a[60:63], v[108:111]\n\t" \
  "s_lshl_b32 s98, s98, 13\n\t" \
  "ds_read_b64_tr_b16 a[72:73], v186 offset:57344\n\t" \
  "v_mfma_f32_16x16x32_bf16 v[108:111], a[172:175], a[44:47], v[108:111]\n\t" \
  "s_add_u32 %[xso], %[xso], s98\n\t" \
  "v_mfma_f32_16x16x32_bf16 v[16:19], a[176:179], a[32:35], v[16:19]\n\t" \
  "buffer_load_dwordx4 a[236:239], v251, %[ws], s97 offen sc1\n\t" \
  "v_mfma_f32_16x16x32_bf16 v[16:19], a[16:19], a[48:51], v[16:19]\n\t" \
  "s_add_u32 s99, %[pf1], 2048\n\t" \
  "ds_read_b64_tr_b16 a[74:75], v186 offset:58368\n\t" \
  "v_mfma_f32_16x16x32_bf16 v[48:51], a[16:19], a[52:55], v[48:51]\n\t" \
  "s_and_b32 s98, %[bid], 0x3\n\t" \
  "ds_read_b64_tr_b16 a[76:77], v187 offset:57344\n\t" \
  "v_mfma_f32_16x16x32_bf16 v[48:51], a[176:179], a[36:39], v[48:51]\n\t" \
  "s_lshl_b32 s98, s98, 17\n\t" \
  "ds_read_b64_tr_b16 a[78:79], v187 offset:58368\n\t" \
  "v_mfma_f32_16x16x32_bf16 v[80:83], a[176:179], a[40:43], v[80:83]\n\t" \
  "buffer_load_dword %[pf], v217, %[wo], s99 offen\n\t" \
  "v_mfma_f32_16x16x32_bf16 v[80:83], a[16:19], a[56:59], v[80:83]\n\t" \
  "s_add_u32 s97, s97, %[dk]\n\t" \
  "ds_read_b64_tr_b16 a[80:81], v188 offset:57344\n\t" \
  "v_mfma_f32_16x16x32_bf16 v[112:115], a[16:19], a[60:63], v[112:115]\n\t" \
  "s_add_u32 %[xso], %[xso], s98\n\t" \
  "ds_read_b64_tr_b16 a[82:83], v188 offset:58368\n\t" \
  "v_mfma_f32_16x16x32_bf16 v[112:115], a[176:179], a[44:47], v[112:115]\n\t" \
  "s_and_b32 s98, %[bid], 0x38\n\t" \
  "ds_read_b64_tr_b16 a[84:85], v189 offset:57344\n\t" \
  "v_mfma_f32_16x16x32_bf16 v[20:23], a[180:183], a[32:35], v[20:23]\n\t" \
  "s_lshl_b32 s98, s98, 5\n\t" \
  "v_mfma_f32_16x16x32_bf16 v[20:23], a[20:23], a[48:51], v[20:23]\n\t" \
  "buffer_load_dwordx4 a[240:243], v248, %[ws], s97 offen sc1\n\t" \
  "v_mfma_f32_16x16x32_bf16 v[52:55], a[20:23], a[52:55], v[52:55]\n\t" \
  "s_add_u32 %[xso], %[xso], s98\n\t" \
  "ds_read_b64_tr_b16 a[86:87], v189 offset:58368\n\t" \
  "v_mfma_f32_16x16x32_bf16 v[52:55], a[180:183], a[36:39], v[52:55]\n\t" \
  "s_lshl_b32 %[ss], %[wid], 7\n\t" \
  "ds_read_b64_tr_b16 a[88:89], v190 offset:57344\n\t" \
  "v_mfma_f32_16x16x32_bf16 v[84:87], a[180:183], a[40:43], v[84:87]\n\t" \
  "s_add_u32 %[ss], %[ss], 0x240000\n\t" \
  "ds_read_b64_tr_b16 a[90:91], v190 offset:58368\n\t" \
  "v_mfma_f32_16x16x32_bf16 v[84:87], a[20:23], a[56:59], v[84:87]\n\t" \
  "s_and_b32 s98, %[bid], 0xff\n\t" \
  "ds_read_b64_tr_b16 a[92:93], v191 offset:57344\n\t" \
  "v_mfma_f32_16x16x32_bf16 v[116:119], a[20:23], a[60:63], v[116:119]\n\t" \
  "buffer_load_dwordx4 a[244:247], v249, %[ws], s97 offen sc1\n\t" \
  "v_mfma_f32_16x16x32_bf16 v[116:119], a[180:183], a[44:47], v[116:119]\n\t" \
  "s_lshl_b32 s98, s98, 9\n\t" \
  "ds_read_b64_tr_b16 a[94:95], v191 offset:58368\n\t" \
  "v_mfma_f32_16x16x32_bf16 v[24:27], a[184:187], a[32:35], v[24:27]\n\t" \
  "s_add_u32 %[ss], %[ss], s98\n\t" \
  "v_mfma_f32_16x16x32_bf16 v[24:27], a[24:27], a[48:51], v[24:27]\n\t" \
  "s_lshl_b32 %[mbp], %[wid], 7\n\t" \
  "v_mfma_f32_16x16x32_bf16 v[56:59], a[24:27], a[52:55], v[56:59]\n\t" \
  "buffer_load_dwordx4 a[248:251], v250, %[ws], s97 offen sc1\n\t" \
  "v_mfma_f32_16x16x32_bf16 v[56:59], a[184:187], a[36:39], v[56:59]\n\t" \
  "s_add_u32 %[mbp], %[mbp], 0x220000\n\t" \
  "v_mfma_f32_16x16x32_bf16 v[88:91], a[184:187], a[40:43], v[88:91]\n\t" \
  "s_and_b32 s98, %[bid], 0xc0\n\t" \
  "v_mfma_f32_16x16x32_bf16 v[88:91], a[24:27], a[56:59], v[88:91]\n\t" \
  "s_lshl_b32 s98, s98, 9\n\t" \
  "v_mfma_f32_16x16x32_bf16 v[120:123], a[24:27], a[60:63], v[120:123]\n\t" \
  "s_add_u32 %[mbp], %[mbp], s98\n\t" \
  "v_mfma_f32_16x16x32_bf16 v[120:123], a[184:187], a[44:47], v[120:123]\n\t" \
  "buffer_load_dwordx4 a[252:255], v251, %[ws], s97 offen sc1\n\t" \
  "v_mfma_f32_16x16x32_bf16 v[28:31], a[188:191], a[32:35], v[28:31]\n\t" \
  "s_and_b32 s98, %[bid], 0x3\n\t" \
  "v_mfma_f32_16x16x32_bf16 v[28:31], a[28:31], a[48:51], v[28:31]\n\t" \
  "s_lshl_b32 s98, s98, 13\n\t" \
  "v_mfma_f32_16x16x32_bf16 v[60:63], a[28:31], a[52:55], v[60:63]\n\t" \
  "s_add_u32 %[mbp], %[mbp], s98\n\t" \
  "v_mfma_f32_16x16x32_bf16 v[60:63], a[188:191], a[36:39], v[60:63]\n\t" \
  "s_and_b32 s98, %[bid], 0x4\n\t" \
  "v_mfma_f32_16x16x32_bf16 v[92:95], a[188:191], a[40:43], v[92:95]\n\t" \
  "s_lshl_b32 s98, s98, 10\n\t" \
  "v_mfma_f32_16x16x32_bf16 v[92:95], a[28:31], a[56:59], v[92:95]\n\t" \
  "s_add_u32 %[mbp], %[mbp], s98\n\t" \
  "v_mfma_f32_16x16x32_bf16 v[124:127], a[28:31], a[60:63], v[124:127]\n\t" \
  "s_and_b32 s98, %[bid], 0x38\n\t" \
  "v_mfma_f32_16x16x32_bf16 v[124:127], a[188:191], a[44:47], v[124:127]\n\t" \
  "s_waitcnt vmcnt(25) lgkmcnt(0)\n\t" \
  "s_barrier\n\t" \
  "v_mfma_f32_16x16x32_bf16 v[0:3], a[128:131], a[96:99], v[0:3]\n\t" \
  "s_add_u32 s96, %[rot19], 6815744\n\t" \
  "ds_read_b64_tr_b16 a[160:161], v192 offset:0\n\t" \
  "v_mfma_f32_16x16x32_bf16 v[0:3], a[64:67], a[112:115], v[0:3]\n\t" \
  "s_and_b32 s96, s96, 0x780000\n\t" \
  "v_mfma_f32_16x16x32_bf16 v[32:35], a[64:67], a[116:119], v[32:35]\n\t" \
  "s_add_u32 m0, %[mw], 49152\n\t" \
  "ds_read_b64_tr_b16 a[162:163], v192 offset:1024\n\t" \
  "v_mfma_f32_16x16x32_bf16 v[32:35], a[128:131], a[100:103], v[32:35]\n\t" \
  "buffer_load_dwordx4 v200, %[sw], s96 offen offset:0 lds\n\t" \
  "v_mfma_f32_16x16x32_bf16 v[64:67], a[128:131], a[104:107], v[64:67]\n\t" \
  "buffer_load_dwordx4 v201, %[sw], s96 offen offset:1024 lds\n\t" \
  "v_mfma_f32_16x16x32_bf16 v[64:67], a[64:67], a[120:123], v[64:67]\n\t" \
  "s_lshl_b32 s98, s98, 6\n\t" \
  "ds_read_b64_tr_b16 a[164:165], v193 offset:0\n\t" \
  "v_mfma_f32_16x16x32_bf16 v[96:99], a[64:67], a[124:127], v[96:99]\n\t" \
  "s_add_u32 %[mbp], %[mbp], s98\n\t" \
  "ds_read_b64_tr_b16 a[166:167], v193 offset:1024\n\t" \
  "v_mfma_f32_16x16x32_bf16 v[96:99], a[128:131], a[108:111], v[96:99]\n\t" \
  "s_lshr_b32 s98, %[bid], 6\n\t" \
  "ds_read_b64_tr_b16 a[168:169], v194 offset:0\n\t" \
  "v_mfma_f32_16x16x32_bf16 v[4:7], a[132:135], a[96:99], v[4:7]\n\t" \
  "s_lshl2_add_u32 %[gvo], s98, %[wid]\n\t" \
  "ds_read_b64_tr_b16 a[170:171], v194 offset:1024\n\t" \
  "v_mfma_f32_16x16x32_bf16 v[4:7], a[68:71], a[112:115], v[4:7]\n\t" \
  "buffer_load_dwordx4 v202, %[sw], s96 offen offset:2048 lds\n\t" \
  "v_mfma_f32_16x16x32_bf16 v[36:39], a[68:71], a[116:119], v[36:39]\n\t" \
  "s_mul_i32 %[gvo], %[gvo], 786432\n\t" \
  "ds_read_b64_tr_b16 a[172:173], v195 offset:0\n\t" \
  "v_mfma_f32_16x16x32_bf16 v[36:39], a[132:135], a[100:103], v[36:39]\n\t" \
  "s_and_b32 s98, %[bid], 0x38\n\t" \
  "ds_read_b64_tr_b16 a[174:175], v195 offset:1024\n\t" \
  "v_mfma_f32_16x16x32_bf16 v[68:71], a[132:135], a[104:107], v[68:71]\n\t" \
  "s_lshl_b32 s98, s98, 4\n\t" \
  "ds_read_b64_tr_b16 a[176:177], v196 offset:0\n\t" \
  "v_mfma_f32_16x16x32_bf16 v[68:71], a[68:71], a[120:123], v[68:71]\n\t" \
  "buffer_load_dwordx4 v203, %[sw], s96 offen offset:3072 lds\n\t" \
  "v_mfma_f32_16x16x32_bf16 v[100:103], a[68:71], a[124:127], v[100:103]\n\t" \
  "s_add_u32 s97, %[rot19], 6291456\n\t" \
  "ds_read_b64_tr_b16 a[178:179], v196 offset:1024\n\t" \
  "v_mfma_f32_16x16x32_bf16 v[100:103], a[132:135], a[108:111], v[100:103]\n\t" \
  "s_bfe_u32 s97, s97, 0x140003\n\t" \
  "ds_read_b64_tr_b16 a[180:181], v197 offset:0\n\t" \
  "v_mfma_f32_16x16x32_bf16 v[8:11], a[136:139], a[96:99], v[8:11]\n\t" \
  "s_add_u32 s97, s97, %[sox]\n\t" \
  "v_mfma_f32_16x16x32_bf16 v[8:11], a[72:75], a[112:115], v[8:11]\n\t" \
  "s_add_u32 %[gvo], %[gvo], s98\n\t" \
  "ds_read_b64_tr_b16 a[182:183], v197 offset:1024\n\t" \
  "v_mfma_f32_16x16x32_bf16 v[40:43], a[72:75], a[116:119], v[40:43]\n\t" \
  "buffer_load_dwordx4 a[32:35], v248, %[ws], s97 offen sc1\n\t" \
  "v_mfma_f32_16x16x32_bf16 v[40:43], a[136:139], a[100:103], v[40:43]\n\t" \
  "s_lshr_b32 s98, %[bid], 2\n\t" \
  "ds_read_b64_tr_b16 a[184:185], v198 offset:0\n\t" \
  "v_mfma_f32_16x16x32_bf16 v[72:75], a[136:139], a[104:107], v[72:75]\n\t" \
  "s_xor_b32 s98, s98, %[bid]\n\t" \
  "ds_read_b64_tr_b16 a[186:187], v198 offset:1024\n\t" \
  "v_mfma_f32_16x16x32_bf16 v[72:75], a[72:75], a[120:123], v[72:75]\n\t" \
  "s_and_b32 s98, s98, 4\n\t" \
  "ds_read_b64_tr_b16 a[188:189], v199 offset:0\n\t" \
  "v_mfma_f32_16x16x32_bf16 v[104:107], a[72:75], a[124:127], v[104:107]\n\t" \
  "s_lshl_b32 %[vo], s98, 4\n\t" \
  "v_mfma_f32_16x16x32_bf16 v[104:107], a[136:139], a[108:111], v[104:107]\n\t" \
  "buffer_load_dwordx4 a[36:39], v249, %[ws], s97 offen sc1\n\t" \
  "v_mfma_f32_16x16x32_bf16 v[12:15], a[140:143], a[96:99], v[12:15]\n\t" \
  "s_and_b32 s98, %[bid], 0x8\n\t" \
  "ds_read_b64_tr_b16 a[190:191], v199 offset:1024\n\t" \
  "v_mfma_f32_16x16x32_bf16 v[12:15], a[76:79], a[112:115], v[12:15]\n\t" \
  "s_lshl_b32 s98, s98, 2\n\t" \
  "ds_read_b64_tr_b16 a[0:1], v192 offset:8192\n\t" \
  "v_mfma_f32_16x16x32_bf16 v[44:47], a[76:79], a[116:119], v[44:47]\n\t" \
  "s_add_u32 %[vo], %[vo], s98\n\t" \
  "ds_read_b64_tr_b16 a[2:3], v192 offset:9216\n\t" \
  "v_mfma_f32_16x16x32_bf16 v[44:47], a[140:143], a[100:103], v[44:47]\n\t" \
  "buffer_load_dwordx4 a[40:43], v250, %[ws], s97 offen sc1\n\t" \
  "v_mfma_f32_16x16x32_bf16 v[76:79], a[140:143], a[104:107], v[76:79]\n\t" \
  "s_and_b32 s98, %[bid], 0x3\n\t" \
  "ds_read_b64_tr_b16 a[4:5], v193 offset:8192\n\t" \
  "v_mfma_f32_16x16x32_bf16 v[76:79], a[76:79], a[120:123], v[76:79]\n\t" \
  "s_lshl_b32 s98, s98, 3\n\t" \
  "ds_read_b64_tr_b16 a[6:7], v193 offset:9216\n\t" \
  "v_mfma_f32_16x16x32_bf16 v[108:111], a[76:79], a[124:127], v[108:111]\n\t" \
  "s_add_u32 %[vo], %[vo], s98\n\t" \
  "ds_read_b64_tr_b16 a[8:9], v194 offset:8192\n\t" \
  "v_mfma_f32_16x16x32_bf16 v[108:111], a[140:143], a[108:111], v[108:111]\n\t" \
  "s_and_b32 s98, %[bid], 0x4\n\t" \
  "v_mfma_f32_16x16x32_bf16 v[16:19], a[144:147], a[96:99], v[16:19]\n\t" \
  "buffer_load_dwordx4 a[44:47], v251, %[ws], s97 offen sc1\n\t" \
  "v_mfma_f32_16x16x32_bf16 v[16:19], a[80:83], a[112:115], v[16:19]\n\t" \
  "s_add_u32 s97, s97, %[dk]\n\t" \
  "ds_read_b64_tr_b16 a[10:11], v194 offset:9216\n\t" \
  "v_mfma_f32_16x16x32_bf16 v[48:51], a[80:83], a[116:119], v[48:51]\n\t" \
  "s_lshl_b32 s98, s98, 0\n\t" \
  "ds_read_b64_tr_b16 a[12:13], v195 offset:8192\n\t" \
  "v_mfma_f32_16x16x32_bf16 v[48:51], a[144:147], a[100:103], v[48:51]\n\t" \
  "s_add_u32 %[vo], %[vo], s98\n\t" \
  "ds_read_b64_tr_b16 a[14:15], v195 offset:9216\n\t" \
  "v_mfma_f32_16x16x32_bf16 v[80:83], a[144:147], a[104:107], v[80:83]\n\t" \
  "buffer_load_dwordx4 a[48:51], v248, %[ws], s97 offen sc1\n\t" \
  "v_mfma_f32_16x16x32_bf16 v[80:83], a[80:83], a[120:123], v[80:83]\n\t" \
  "s_add_u32 %[vo], %[vo], 0x2000\n\t" \
  "ds_read_b64_tr_b16 a[16:17], v196 offset:8192\n\t" \
  "v_mfma_f32_16x16x32_bf16 v[112:115], a[80:83], a[124:127], v[112:115]\n\t" \
  "ds_read_b64_tr_b16 a[18:19], v196 offset:9216\n\t" \
  "v_mfma_f32_16x16x32_bf16 v[112:115], a[144:147], a[108:111], v[112:115]\n\t" \
  "ds_read_b64_tr_b16 a[20:21], v197 offset:8192\n\t" \
  "v_mfma_f32_16x16x32_bf16 v[20:23], a[148:151], a[96:99], v[20:23]\n\t" \
  "s_cmp_lt_i32 %[prb], 0\n\t" \
  "s_cselect_b32 %[vo], %[prb], %[vo]\n\t" \
  "v_mfma_f32_16x16x32_bf16 v[20:23], a[84:87], a[112:115], v[20:23]\n\t" \
  "buffer_load_dwordx4 a[52:55], v249, %[ws], s97 offen sc1\n\t" \
  "v_mfma_f32_16x16x32_bf16 v[52:55], a[84:87], a[116:119], v[52:55]\n\t" \
  "ds_read_b64_tr_b16 a[22:23], v197 offset:9216\n\t" \
  "v_mfma_f32_16x16x32_bf16 v[52:55], a[148:151], a[100:103], v[52:55]\n\t" \
  "ds_read_b64_tr_b16 a[24:25], v198 offset:8192\n\t" \
  "v_mfma_f32_16x16x32_bf16 v[84:87], a[148:151], a[104:107], v[84:87]\n\t" \
  "ds_read_b64_tr_b16 a[26:27], v198 offset:9216\n\t" \
  "v_mfma_f32_16x16x32_bf16 v[84:87], a[84:87], a[120:123], v[84:87]\n\t" \
  "ds_read_b64_tr_b16 a[28:29], v199 offset:8192\n\t" \
  "v_mfma_f32_16x16x32_bf16 v[116:119], a[84:87], a[124:127], v[116:119]\n\t" \
  "buffer_load_dwordx4 a[56:59], v250, %[ws], s97 offen sc1\n\t" \
  "v_mfma_f32_16x16x32_bf16 v[116:119], a[148:151], a[108:111], v[116:119]\n\t" \
  "ds_read_b64_tr_b16 a[30:31], v199 offset:9216\n\t" \
  "v_mfma_f32_16x16x32_bf16 v[24:27], a[152:155], a[96:99], v[24:27]\n\t" \
  "v_mfma_f32_16x16x32_bf16 v[24:27], a[88:91], a[112:115], v[24:27]\n\t" \
  "v_mfma_f32_16x16x32_bf16 v[56:59], a[88:91], a[116:119], v[56:59]\n\t" \
  "buffer_load_dwordx4 a[60:63], v251, %[ws], s97 offen sc1\n\t" \
  "v_mfma_f32_16x16x32_bf16 v[56:59], a[152:155], a[100:103], v[56:59]\n\t" \
  "s_add_u32 s99, %[pf1], 4096\n\t" \
  "v_mfma_f32_16x16x32_bf16 v[88:91], a[152:155], a[104:107], v[88:91]\n\t" \
  "v_mfma_f32_16x16x32_bf16 v[88:91], a[88:91], a[120:123], v[88:91]\n\t" \
  "v_mfma_f32_16x16x32_bf16 v[120:123], a[88:91], a[124:127], v[120:123]\n\t" \
  "v_mfma_f32_16x16x32_bf16 v[120:123], a[152:155], a[108:111], v[120:123]\n\t" \
  "buffer_load_dword %[pf], v217, %[wo], s99 offen\n\t" \
  "v_mfma_f32_16x16x32_bf16 v[28:31], a[156:159], a[96:99], v[28:31]\n\t" \
  "v_mfma_f32_16x16x32_bf16 v[28:31], a[92:95], a[112:115], v[28:31]\n\t" \
  "v_mfma_f32_16x16x32_bf16 v[60:63], a[92:95], a[116:119], v[60:63]\n\t" \
  "v_mfma_f32_16x16x32_bf16 v[60:63], a[156:159], a[100:103], v[60:63]\n\t" \
  "v_mfma_f32_16x16x32_bf16 v[92:95], a[156:159], a[104:107], v[92:95]\n\t" \
  "v_mfma_f32_16x16x32_bf16 v[92:95], a[92:95], a[120:123], v[92:95]\n\t" \
  "v_mfma_f32_16x16x32_bf16 v[124:127], a[92:95], a[124:127], v[124:127]\n\t" \
  "v_mfma_f32_16x16x32_bf16 v[124:127], a[156:159], a[108:111], v[124:127]\n\t" \
  "s_waitcnt vmcnt(26) lgkmcnt(0)\n\t" \
  "s_barrier\n\t" \
  "v_mfma_f32_16x16x32_bf16 v[0:3], a[160:163], a[192:195], v[0:3]\n\t" \
  "s_add_u32 s96, %[rot19], 7340032\n\t" \
  "ds_read_b64_tr_b16 a[128:129], v184 offset:16384\n\t" \
  "v_mfma_f32_16x16x32_bf16 v[0:3], a[0:3], a[208:211], v[0:3]\n\t" \
  "s_and_b32 s96, s96, 0x780000\n\t" \
  "v_mfma_f32_16x16x32_bf16 v[32:35], a[0:3], a[212:215], v[32:35]\n\t" \
  "s_add_u32 m0, %[mw], 0\n\t" \
  "ds_read_b64_tr_b16 a[130:131], v184 offset:17408\n\t" \
  "v_mfma_f32_16x16x32_bf16 v[32:35], a[160:163], a[196:199], v[32:35]\n\t" \
  "buffer_load_dwordx4 v200, %[sw], s96 offen offset:0 lds\n\t" \
  "v_mfma_f32_16x16x32_bf16 v[64:67], a[160:163], a[200:203], v[64:67]\n\t" \
  "ds_read_b64_tr_b16 a[132:133], v185 offset:16384\n\t" \
  "v_mfma_f32_16x16x32_bf16 v[64:67], a[0:3], a[216:219], v[64:67]\n\t" \
  "buffer_load_dwordx4 v201, %[sw], s96 offen offset:1024 lds\n\t" \
  "v_mfma_f32_16x16x32_bf16 v[96:99], a[0:3], a[220:223], v[96:99]\n\t" \
  "ds_read_b64_tr_b16 a[134:135], v185 offset:17408\n\t" \
  "v_mfma_f32_16x16x32_bf16 v[96:99], a[160:163], a[204:207], v[96:99]\n\t" \
  "ds_read_b64_tr_b16 a[136:137], v186 offset:16384\n\t" \
  "v_mfma_f32_16x16x32_bf16 v[4:7], a[164:167], a[192:195], v[4:7]\n\t" \
  "ds_read_b64_tr_b16 a[138:139], v186 offset:17408\n\t" \
  "v_mfma_f32_16x16x32_bf16 v[4:7], a[4:7], a[208:211], v[4:7]\n\t" \
  "ds_read_b64_tr_b16 a[140:141], v187 offset:16384\n\t" \
  "v_mfma_f32_16x16x32_bf16 v[36:39], a[4:7], a[212:215], v[36:39]\n\t" \
  "buffer_load_dwordx4 v202, %[sw], s96 offen offset:2048 lds\n\t" \
  "v_mfma_f32_16x16x32_bf16 v[36:39], a[164:167], a[196:199], v[36:39]\n\t" \
  "ds_read_b64_tr_b16 a[142:143], v187 offset:17408\n\t" \
  "v_mfma_f32_16x16x32_bf16 v[68:71], a[164:167], a[200:203], v[68:71]\n\t" \
  "ds_read_b64_tr_b16 a[144:145], v188 offset:16384\n\t" \
  "v_mfma_f32_16x16x32_bf16 v[68:71], a[4:7], a[216:219], v[68:71]\n\t" \
  "v_mfma_f32_16x16x32_bf16 v[100:103], a[4:7], a[220:223], v[100:103]\n\t" \
  "ds_read_b64_tr_b16 a[146:147], v188 offset:17408\n\t" \
  "v_mfma_f32_16x16x32_bf16 v[100:103], a[164:167], a[204:207], v[100:103]\n\t" \
  "buffer_load_dwordx4 v203, %[sw], s96 offen offset:3072 lds\n\t" \
  "v_mfma_f32_16x16x32_bf16 v[8:11], a[168:171], a[192:195], v[8:11]\n\t" \
  "s_add_u32 s97, %[rot19], 6815744\n\t" \
  "ds_read_b64_tr_b16 a[148:149], v189 offset:16384\n\t" \
  "v_mfma_f32_16x16x32_bf16 v[8:11], a[8:11], a[208:211], v[8:11]\n\t" \
  "s_bfe_u32 s97, s97, 0x140003\n\t" \
  "ds_read_b64_tr_b16 a[150:151], v189 offset:17408\n\t" \
  "v_mfma_f32_16x16x32_bf16 v[40:43], a[8:11], a[212:215], v[40:43]\n\t" \
  "s_add_u32 s97, s97, %[sox]\n\t" \
  "ds_read_b64_tr_b16 a[152:153], v190 offset:16384\n\t" \
  "v_mfma_f32_16x16x32_bf16 v[40:43], a[168:171], a[196:199], v[40:43]\n\t" \
  "v_mfma_f32_16x16x32_bf16 v[72:75], a[168:171], a[200:203], v[72:75]\n\t" \
  "buffer_load_dwordx4 a[96:99], v248, %[ws], s97 offen sc1\n\t" \
  "v_mfma_f32_16x16x32_bf16 v[72:75], a[8:11], a[216:219], v[72:75]\n\t" \
  "ds_read_b64_tr_b16 a[154:155], v190 offset:17408\n\t" \
  "v_mfma_f32_16x16x32_bf16 v[104:107], a[8:11], a[220:223], v[104:107]\n\t" \
  "ds_read_b64_tr_b16 a[156:157], v191 offset:16384\n\t" \
  "v_mfma_f32_16x16x32_bf16 v[104:107], a[168:171], a[204:207], v[104:107]\n\t" \
  "ds_read_b64_tr_b16 a[158:159], v191 offset:17408\n\t" \
  "v_mfma_f32_16x16x32_bf16 v[12:15], a[172:175], a[192:195], v[12:15]\n\t" \
  "ds_read_b64_tr_b16 a[64:65], v184 offset:24576\n\t" \
  "v_mfma_f32_16x16x32_bf16 v[12:15], a[12:15], a[208:211], v[12:15]\n\t" \
  "buffer_load_dwordx4 a[100:103], v249, %[ws], s97 offen sc1\n\t" \
  "v_mfma_f32_16x16x32_bf16 v[44:47], a[12:15], a[212:215], v[44:47]\n\t" \
  "ds_read_b64_tr_b16 a[66:67], v184 offset:25600\n\t" \
  "v_mfma_f32_16x16x32_bf16 v[44:47], a[172:175], a[196:199], v[44:47]\n\t" \
  "ds_read_b64_tr_b16 a[68:69], v185 offset:24576\n\t" \
  "v_mfma_f32_16x16x32_bf16 v[76:79], a[172:175], a[200:203], v[76:79]\n\t" \
  "v_mfma_f32_16x16x32_bf16 v[76:79], a[12:15], a[216:219], v[76:79]\n\t" \
  "ds_read_b64_tr_b16 a[70:71], v185 offset:25600\n\t" \
  "v_mfma_f32_16x16x32_bf16 v[108:111], a[12:15], a[220:223], v[108:111]\n\t" \
  "buffer_load_dwordx4 a[104:107], v250, %[ws], s97 offen sc1\n\t" \
  "v_mfma_f32_16x16x32_bf16 v[108:111], a[172:175], a[204:207], v[108:111]\n\t" \
  "ds_read_b64_tr_b16 a[72:73], v186 offset:24576\n\t" \
  "v_mfma_f32_16x16x32_bf16 v[16:19], a[176:179], a[192:195], v[16:19]\n\t" \
  "ds_read_b64_tr_b16 a[74:75], v186 offset:25600\n\t" \
  "v_mfma_f32_16x16x32_bf16 v[16:19], a[16:19], a[208:211], v[16:19]\n\t" \
  "ds_read_b64_tr_b16 a[76:77], v187 offset:24576\n\t" \
  "v_mfma_f32_16x16x32_bf16 v[48:51], a[16:19], a[212:215], v[48:51]\n\t" \
  "v_mfma_f32_16x16x32_bf16 v[48:51], a[176:179], a[196:199], v[48:51]\n\t" \
  "buffer_load_dwordx4 a[108:111], v251, %[ws], s97 offen sc1\n\t" \
  "v_mfma_f32_16x16x32_bf16 v[80:83], a[176:179], a[200:203], v[80:83]\n\t" \
  "s_add_u32 s97, s97, %[dk]\n\t" \
  "ds_read_b64_tr_b16 a[78:79], v187 offset:25600\n\t" \
  "v_mfma_f32_16x16x32_bf16 v[80:83], a[16:19], a[216:219], v[80:83]\n\t" \
  "ds_read_b64_tr_b16 a[80:81], v188 offset:24576\n\t" \
  "v_mfma_f32_16x16x32_bf16 v[112:115], a[16:19], a[220:223], v[112:115]\n\t" \
  "ds_read_b64_tr_b16 a[82:83], v188 offset:25600\n\t" \
  "v_mfma_f32_16x16x32_bf16 v[112:115], a[176:179], a[204:207], v[112:115]\n\t" \
  "ds_read_b64_tr_b16 a[84:85], v189 offset:24576\n\t" \
  "v_mfma_f32_16x16x32_bf16 v[20:23], a[180:183], a[192:195], v[20:23]\n\t" \
  "buffer_load_dwordx4 a[112:115], v248, %[ws], s97 offen sc1\n\t" \
  "v_mfma_f32_16x16x32_bf16 v[20:23], a[20:23], a[208:211], v[20:23]\n\t" \
  "ds_read_b64_tr_b16 a[86:87], v189 offset:25600\n\t" \
  "v_mfma_f32_16x16x32_bf16 v[52:55], a[20:23], a[212:215], v[52:55]\n\t" \
  "ds_read_b64_tr_b16 a[88:89], v190 offset:24576\n\t" \
  "v_mfma_f32_16x16x32_bf16 v[52:55], a[180:183], a[196:199], v[52:55]\n\t" \
  "v_mfma_f32_16x16x32_bf16 v[84:87], a[180:183], a[200:203], v[84:87]\n\t" \
  "ds_read_b64_tr_b16 a[90:91], v190 offset:25600\n\t" \
  "v_mfma_f32_16x16x32_bf16 v[84:87], a[20:23], a[216:219], v[84:87]\n\t" \
  "buffer_load_dwordx4 a[116:119], v249, %[ws], s97 offen sc1\n\t" \
  "v_mfma_f32_16x16x32_bf16 v[116:119], a[20:23], a[220:223], v[116:119]\n\t" \
  "ds_read_b64_tr_b16 a[92:93], v191 offset:24576\n\t" \
  "v_mfma_f32_16x16x32_bf16 v[116:119], a[180:183], a[204:207], v[116:119]\n\t" \
  "ds_read_b64_tr_b16 a[94:95], v191 offset:25600\n\t" \
  "v_mfma_f32_16x16x32_bf16 v[24:27], a[184:187], a[192:195], v[24:27]\n\t" \
  "v_mfma_f32_16x16x32_bf16 v[24:27], a[24:27], a[208:211], v[24:27]\n\t" \
  "v_mfma_f32_16x16x32_bf16 v[56:59], a[24:27], a[212:215], v[56:59]\n\t" \
  "buffer_load_dwordx4 a[120:123], v250, %[ws], s97 offen sc1\n\t" \
  "v_mfma_f32_16x16x32_bf16 v[56:59], a[184:187], a[196:199], v[56:59]\n\t" \
  "v_mfma_f32_16x16x32_bf16 v[88:91], a[184:187], a[200:203], v[88:91]\n\t" \
  "v_mfma_f32_16x16x32_bf16 v[88:91], a[24:27], a[216:219], v[88:91]\n\t" \
  "v_mfma_f32_16x16x32_bf16 v[120:123], a[24:27], a[220:223], v[120:123]\n\t" \
  "v_mfma_f32_16x16x32_bf16 v[120:123], a[184:187], a[204:207], v[120:123]\n\t" \
  "buffer_load_dwordx4 a[124:127], v251, %[ws], s97 offen sc1\n\t" \
  "v_mfma_f32_16x16x32_bf16 v[28:31], a[188:191], a[192:195], v[28:31]\n\t" \
  "v_mfma_f32_16x16x32_bf16 v[28:31], a[28:31], a[208:211], v[28:31]\n\t" \
  "v_mfma_f32_16x16x32_bf16 v[60:63], a[28:31], a[212:215], v[60:63]\n\t" \
  "v_mfma_f32_16x16x32_bf16 v[60:63], a[188:191], a[196:199], v[60:63]\n\t" \
  "v_mfma_f32_16x16x32_bf16 v[92:95], a[188:191], a[200:203], v[92:95]\n\t" \
  "v_mfma_f32_16x16x32_bf16 v[92:95], a[28:31], a[216:219], v[92:95]\n\t" \
  "v_mfma_f32_16x16x32_bf16 v[124:127], a[28:31], a[220:223], v[124:127]\n\t" \
  "v_mfma_f32_16x16x32_bf16 v[124:127], a[188:191], a[204:207], v[124:127]\n\t" \
  "s_waitcnt vmcnt(25) lgkmcnt(0)\n\t" \
  "s_barrier\n\t" \
  "v_mfma_f32_16x16x32_bf16 v[0:3], a[128:131], a[224:227], v[0:3]\n\t" \
  "s_add_u32 s96, %[rot19], 7864320\n\t" \
  "ds_read_b64_tr_b16 a[160:161], v184 offset:32768\n\t" \
  "v_mfma_f32_16x16x32_bf16 v[0:3], a[64:67], a[240:243], v[0:3]\n\t" \
  "s_and_b32 s96, s96, 0x780000\n\t" \
  "v_mfma_f32_16x16x32_bf16 v[32:35], a[64:67], a[244:247], v[32:35]\n\t" \
  "s_add_u32 m0, %[mw], 98304\n\t" \
  "ds_read_b64_tr_b16 a[162:163], v184 offset:33792\n\t" \
  "v_mfma_f32_16x16x32_bf16 v[32:35], a[128:131], a[228:231], v[32:35]\n\t" \
  "buffer_load_dwordx4 v200, %[sw], s96 offen offset:0 lds\n\t" \
  "v_mfma_f32_16x16x32_bf16 v[64:67], a[128:131], a[232:235], v[64:67]\n\t" \
  "buffer_load_dwordx4 v201, %[sw], s96 offen offset:1024 lds\n\t" \
  "v_mfma_f32_16x16x32_bf16 v[64:67], a[64:67], a[248:251], v[64:67]\n\t" \
  "ds_read_b64_tr_b16 a[164:165], v185 offset:32768\n\t" \
  "v_mfma_f32_16x16x32_bf16 v[96:99], a[64:67], a[252:255], v[96:99]\n\t" \
  "ds_read_b64_tr_b16 a[166:167], v185 offset:33792\n\t" \
  "v_mfma_f32_16x16x32_bf16 v[96:99], a[128:131], a[236:239], v[96:99]\n\t" \
  "ds_read_b64_tr_b16 a[168:169], v186 offset:32768\n\t" \
  "v_mfma_f32_16x16x32_bf16 v[4:7], a[132:135], a[224:227], v[4:7]\n\t" \
  "ds_read_b64_tr_b16 a[170:171], v186 offset:33792\n\t" \
  "v_mfma_f32_16x16x32_bf16 v[4:7], a[68:71], a[240:243], v[4:7]\n\t" \
  "buffer_load_dwordx4 v202, %[sw], s96 offen offset:2048 lds\n\t" \
  "v_mfma_f32_16x16x32_bf16 v[36:39], a[68:71], a[244:247], v[36:39]\n\t" \
  "ds_read_b64_tr_b16 a[172:173], v187 offset:32768\n\t" \
  "v_mfma_f32_16x16x32_bf16 v[36:39], a[132:135], a[228:231], v[36:39]\n\t" \
  "ds_read_b64_tr_b16 a[174:175], v187 offset:33792\n\t" \
  "v_mfma_f32_16x16x32_bf16 v[68:71], a[132:135], a[232:235], v[68:71]\n\t" \
  "ds_read_b64_tr_b16 a[176:177], v188 offset:32768\n\t" \
  "v_mfma_f32_16x16x32_bf16 v[68:71], a[68:71], a[248:251], v[68:71]\n\t" \
  "buffer_load_dwordx4 v203, %[sw], s96 offen offset:3072 lds\n\t" \
  "v_mfma_f32_16x16x32_bf16 v[100:103], a[68:71], a[252:255], v[100:103]\n\t" \
  "s_add_u32 s97, %[rot19], 7340032\n\t" \
  "ds_read_b64_tr_b16 a[178:179], v188 offset:33792\n\t" \
  "v_mfma_f32_16x16x32_bf16 v[100:103], a[132:135], a[236:239], v[100:103]\n\t" \
  "s_bfe_u32 s97, s97, 0x140003\n\t" \
  "ds_read_b64_tr_b16 a[180:181], v189 offset:32768\n\t" \
  "v_mfma_f32_16x16x32_bf16 v[8:11], a[136:139], a[224:227], v[8:11]\n\t" \
  "s_add_u32 s97, s97, %[sox]\n\t" \
  "v_mfma_f32_16x16x32_bf16 v[8:11], a[72:75], a[240:243], v[8:11]\n\t" \
  "ds_read_b64_tr_b16 a[182:183], v189 offset:33792\n\t" \
  "v_mfma_f32_16x16x32_bf16 v[40:43], a[72:75], a[244:247], v[40:43]\n\t" \
  "buffer_load_dwordx4 a[192:195], v248, %[ws], s97 offen sc1\n\t" \
  "v_mfma_f32_16x16x32_bf16 v[40:43], a[136:139], a[228:231], v[40:43]\n\t" \
  "ds_read_b64_tr_b16 a[184:185], v190 offset:32768\n\t" \
  "v_mfma_f32_16x16x32_bf16 v[72:75], a[136:139], a[232:235], v[72:75]\n\t" \
  "ds_read_b64_tr_b16 a[186:187], v190 offset:33792\n\t" \
  "v_mfma_f32_16x16x32_bf16 v[72:75], a[72:75], a[248:251], v[72:75]\n\t" \
  "ds_read_b64_tr_b16 a[188:189], v191 offset:32768\n\t" \
  "v_mfma_f32_16x16x32_bf16 v[104:107], a[72:75], a[252:255], v[104:107]\n\t" \
  "v_mfma_f32_16x16x32_bf16 v[104:107], a[136:139], a[236:239], v[104:107]\n\t" \
  "buffer_load_dwordx4 a[196:199], v249, %[ws], s97 offen sc1\n\t" \
  "v_mfma_f32_16x16x32_bf16 v[12:15], a[140:143], a[224:227], v[12:15]\n\t" \
  "ds_read_b64_tr_b16 a[190:191], v191 offset:33792\n\t" \
  "v_mfma_f32_16x16x32_bf16 v[12:15], a[76:79], a[240:243], v[12:15]\n\t" \
  "ds_read_b64_tr_b16 a[0:1], v184 offset:40960\n\t" \
  "v_mfma_f32_16x16x32_bf16 v[44:47], a[76:79], a[244:247], v[44:47]\n\t" \
  "ds_read_b64_tr_b16 a[2:3], v184 offset:41984\n\t" \
  "v_mfma_f32_16x16x32_bf16 v[44:47], a[140:143], a[228:231], v[44:47]\n\t" \
  "buffer_load_dwordx4 a[200:203], v250, %[ws], s97 offen sc1\n\t" \
  "v_mfma_f32_16x16x32_bf16 v[76:79], a[140:143], a[232:235], v[76:79]\n\t" \
  "ds_read_b64_tr_b16 a[4:5], v185 offset:40960\n\t" \
  "v_mfma_f32_16x16x32_bf16 v[76:79], a[76:79], a[248:251], v[76:79]\n\t" \
  "ds_read_b64_tr_b16 a[6:7], v185 offset:41984\n\t" \
  "v_mfma_f32_16x16x32_bf16 v[108:111], a[76:79], a[252:255], v[108:111]\n\t" \
  "ds_read_b64_tr_b16 a[8:9], v186 offset:40960\n\t" \
  "v_mfma_f32_16x16x32_bf16 v[108:111], a[140:143], a[236:239], v[108:111]\n\t" \
  "v_mfma_f32_16x16x32_bf16 v[16:19], a[144:147], a[224:227], v[16:19]\n\t" \
  "buffer_load_dwordx4 a[204:207], v251, %[ws], s97 offen sc1\n\t" \
  "v_mfma_f32_16x16x32_bf16 v[16:19], a[80:83], a[240:243], v[16:19]\n\t" \
  "s_add_u32 s99, %[pf1], 6144\n\t" \
  "ds_read_b64_tr_b16 a[10:11], v186 offset:41984\n\t" \
  "v_mfma_f32_16x16x32_bf16 v[48:51], a[80:83], a[244:247], v[48:51]\n\t" \
  "ds_read_b64_tr_b16 a[12:13], v187 offset:40960\n\t" \
  "v_mfma_f32_16x16x32_bf16 v[48:51], a[144:147], a[228:231], v[48:51]\n\t" \
  ".Lhs4:\n\ts_cbranch_execz .Lhs4n\n\t" \
  "ds_read_b64_tr_b16 a[14:15], v187 offset:41984\n\t" \
  "v_mfma_f32_16x16x32_bf16 v[80:83], a[144:147], a[232:235], v[80:83]\n\t" \
  "buffer_load_dword %[pf], v217, %[wo], s99 offen\n\t" \
  "v_mfma_f32_16x16x32_bf16 v[80:83], a[80:83], a[248:251], v[80:83]\n\t" \
  "s_add_u32 s97, s97, %[dk]\n\t" \
  "ds_read_b64_tr_b16 a[16:17], v188 offset:40960\n\t" \
  "v_mfma_f32_16x16x32_bf16 v[112:115], a[80:83], a[252:255], v[112:115]\n\t" \
  "ds_read_b64_tr_b16 a[18:19], v188 offset:41984\n\t" \
  "v_mfma_f32_16x16x32_bf16 v[112:115], a[144:147], a[236:239], v[112:115]\n\t" \
  "ds_read_b64_tr_b16 a[20:21], v189 offset:40960\n\t" \
  "v_mfma_f32_16x16x32_bf16 v[20:23], a[148:151], a[224:227], v[20:23]\n\t" \
  "v_mfma_f32_16x16x32_bf16 v[20:23], a[84:87], a[240:243], v[20:23]\n\t" \
  "buffer_load_dwordx4 a[208:211], v248, %[ws], s97 offen sc1\n\t" \
  "v_mfma_f32_16x16x32_bf16 v[52:55], a[84:87], a[244:247], v[52:55]\n\t" \
  "ds_read_b64_tr_b16 a[22:23], v189 offset:41984\n\t" \
  "v_mfma_f32_16x16x32_bf16 v[52:55], a[148:151], a[228:231], v[52:55]\n\t" \
  "ds_read_b64_tr_b16 a[24:25], v190 offset:40960\n\t" \
  "v_mfma_f32_16x16x32_bf16 v[84:87], a[148:151], a[232:235], v[84:87]\n\t" \
  "ds_read_b64_tr_b16 a[26:27], v190 offset:41984\n\t" \
  "v_mfma_f32_16x16x32_bf16 v[84:87], a[84:87], a[248:251], v[84:87]\n\t" \
  "ds_read_b64_tr_b16 a[28:29], v191 offset:40960\n\t" \
  "v_mfma_f32_16x16x32_bf16 v[116:119], a[84:87], a[252:255], v[116:119]\n\t" \
  "buffer_load_dwordx4 a[212:215], v249, %[ws], s97 offen sc1\n\t" \
  "v_mfma_f32_16x16x32_bf16 v[116:119], a[148:151], a[236:239], v[116:119]\n\t" \
  "ds_read_b64_tr_b16 a[30:31], v191 offset:41984\n\t" \
  "v_mfma_f32_16x16x32_bf16 v[24:27], a[152:155], a[224:227], v[24:27]\n\t" \
  "v_mfma_f32_16x16x32_bf16 v[24:27], a[88:91], a[240:243], v[24:27]\n\t" \
  "v_mfma_f32_16x16x32_bf16 v[56:59], a[88:91], a[244:247], v[56:59]\n\t" \
  "buffer_load_dwordx4 a[216:219], v250, %[ws], s97 offen sc1\n\t" \
  "v_mfma_f32_16x16x32_bf16 v[56:59], a[152:155], a[228:231], v[56:59]\n\t" \
  "v_mfma_f32_16x16x32_bf16 v[88:91], a[152:155], a[232:235], v[88:91]\n\t" \
  "v_mfma_f32_16x16x32_bf16 v[88:91], a[88:91], a[248:251], v[88:91]\n\t" \
  "v_mfma_f32_16x16x32_bf16 v[120:123], a[88:91], a[252:255], v[120:123]\n\t" \
  "v_mfma_f32_16x16x32_bf16 v[120:123], a[152:155], a[236:239], v[120:123]\n\t" \
  "buffer_load_dwordx4 a[220:223], v251, %[ws], s97 offen sc1\n\t" \
  "v_mfma_f32_16x16x32_bf16 v[28:31], a[156:159], a[224:227], v[28:31]\n\t" \
  "v_mfma_f32_16x16x32_bf16 v[28:31], a[92:95], a[240:243], v[28:31]\n\t" \
  "v_mfma_f32_16x16x32_bf16 v[60:63], a[92:95], a[244:247], v[60:63]\n\t" \
  "v_mfma_f32_16x16x32_bf16 v[60:63], a[156:159], a[228:231], v[60:63]\n\t" \
  "v_mfma_f32_16x16x32_bf16 v[92:95], a[156:159], a[232:235], v[92:95]\n\t" \
  "v_mfma_f32_16x16x32_bf16 v[92:95], a[92:95], a[248:251], v[92:95]\n\t" \
  "v_mfma_f32_16x16x32_bf16 v[124:127], a[92:95], a[252:255], v[124:127]\n\t" \
  "v_mfma_f32_16x16x32_bf16 v[124:127], a[156:159], a[236:239], v[124:127]\n\t" \
  "s_waitcnt vmcnt(26) lgkmcnt(0)\n\t" \
  "s_barrier\n\t" \
  "v_mfma_f32_16x16x32_bf16 v[0:3], a[160:163], a[32:35], v[0:3]\n\t" \
  "s_add_u32 s97, %[rot19], 7864320\n\t" \
  "ds_read_b64_tr_b16 a[128:129], v184 offset:49152\n\t" \
  "v_mfma_f32_16x16x32_bf16 v[0:3], a[0:3], a[48:51], v[0:3]\n\t" \
  "s_bfe_u32 s97, s97, 0x140003\n\t" \
  "v_mfma_f32_16x16x32_bf16 v[32:35], a[0:3], a[52:55], v[32:35]\n\t" \
  "s_add_u32 s97, s97, %[sox]\n\t" \
  "ds_read_b64_tr_b16 a[130:131], v184 offset:50176\n\t" \
  "v_mfma_f32_16x16x32_bf16 v[32:35], a[160:163], a[36:39], v[32:35]\n\t" \
  "buffer_load_dwordx4 a[224:227], v248, %[ws], s97 offen sc1\n\t" \
  "v_mfma_f32_16x16x32_bf16 v[64:67], a[160:163], a[40:43], v[64:67]\n\t" \
  "ds_read_b64_tr_b16 a[132:133], v185 offset:49152\n\t" \
  "v_mfma_f32_16x16x32_bf16 v[64:67], a[0:3], a[56:59], v[64:67]\n\t" \
  "ds_read_b64_tr_b16 a[134:135], v185 offset:50176\n\t" \
  "v_mfma_f32_16x16x32_bf16 v[96:99], a[0:3], a[60:63], v[96:99]\n\t" \
  "buffer_load_dwordx4 a[228:231], v249, %[ws], s97 offen sc1\n\t" \
  "v_mfma_f32_16x16x32_bf16 v[96:99], a[160:163], a[44:47], v[96:99]\n\t" \
  "ds_read_b64_tr_b16 a[136:137], v186 offset:49152\n\t" \
  "v_mfma_f32_16x16x32_bf16 v[4:7], a[164:167], a[32:35], v[4:7]\n\t" \
  "ds_read_b64_tr_b16 a[138:139], v186 offset:50176\n\t" \
  "v_mfma_f32_16x16x32_bf16 v[4:7], a[4:7], a[48:51], v[4:7]\n\t" \
  "ds_read_b64_tr_b16 a[140:141], v187 offset:49152\n\t" \
  "v_mfma_f32_16x16x32_bf16 v[36:39], a[4:7], a[52:55], v[36:39]\n\t" \
  "v_mfma_f32_16x16x32_bf16 v[36:39], a[164:167], a[36:39], v[36:39]\n\t" \
  "ds_read_b64_tr_b16 a[142:143], v187 offset:50176\n\t" \
  "v_mfma_f32_16x16x32_bf16 v[68:71], a[164:167], a[40:43], v[68:71]\n\t" \
  "buffer_load_dwordx4 a[232:235], v250, %[ws], s97 offen sc1\n\t" \
  "v_mfma_f32_16x16x32_bf16 v[68:71], a[4:7], a[56:59], v[68:71]\n\t" \
  "ds_read_b64_tr_b16 a[144:145], v188 offset:49152\n\t" \
  "v_mfma_f32_16x16x32_bf16 v[100:103], a[4:7], a[60:63], v[100:103]\n\t" \
  "ds_read_b64_tr_b16 a[146:147], v188 offset:50176\n\t" \
  "v_mfma_f32_16x16x32_bf16 v[100:103], a[164:167], a[44:47], v[100:103]\n\t" \
  "ds_read_b64_tr_b16 a[148:149], v189 offset:49152\n\t" \
  "v_mfma_f32_16x16x32_bf16 v[8:11], a[168:171], a[32:35], v[8:11]\n\t" \
  "v_mfma_f32_16x16x32_bf16 v[8:11], a[8:11], a[48:51], v[8:11]\n\t" \
  "ds_read_b64_tr_b16 a[150:151], v189 offset:50176\n\t" \
  "v_mfma_f32_16x16x32_bf16 v[40:43], a[8:11], a[52:55], v[40:43]\n\t" \
  "buffer_load_dwordx4 a[236:239], v251, %[ws], s97 offen sc1\n\t" \
  "v_mfma_f32_16x16x32_bf16 v[40:43], a[168:171], a[36:39], v[40:43]\n\t" \
  "ds_read_b64_tr_b16 a[152:153], v190 offset:49152\n\t" \
  "v_mfma_f32_16x16x32_bf16 v[72:75], a[168:171], a[40:43], v[72:75]\n\t" \
  "s_add_u32 s97, s97, %[dk]\n\t" \
  "ds_read_b64_tr_b16 a[154:155], v190 offset:50176\n\t" \
  "v_mfma_f32_16x16x32_bf16 v[72:75], a[8:11], a[56:59], v[72:75]\n\t" \
  "ds_read_b64_tr_b16 a[156:157], v191 offset:49152\n\t" \
  "v_mfma_f32_16x16x32_bf16 v[104:107], a[8:11], a[60:63], v[104:107]\n\t" \
  "v_mfma_f32_16x16x32_bf16 v[104:107], a[168:171], a[44:47], v[104:107]\n\t" \
  "ds_read_b64_tr_b16 a[158:159], v191 offset:50176\n\t" \
  "v_mfma_f32_16x16x32_bf16 v[12:15], a[172:175], a[32:35], v[12:15]\n\t" \
  "buffer_load_dwordx4 a[240:243], v248, %[ws], s97 offen sc1\n\t" \
  "v_mfma_f32_16x16x32_bf16 v[12:15], a[12:15], a[48:51], v[12:15]\n\t" \
  "ds_read_b64_tr_b16 a[64:65], v184 offset:57344\n\t" \
  "v_mfma_f32_16x16x32_bf16 v[44:47], a[12:15], a[52:55], v[44:47]\n\t" \
  "ds_read_b64_tr_b16 a[66:67], v184 offset:58368\n\t" \
  "v_mfma_f32_16x16x32_bf16 v[44:47], a[172:175], a[36:39], v[44:47]\n\t" \
  "ds_read_b64_tr_b16 a[68:69], v185 offset:57344\n\t" \
  "v_mfma_f32_16x16x32_bf16 v[76:79], a[172:175], a[40:43], v[76:79]\n\t" \
  "v_mfma_f32_16x16x32_bf16 v[76:79], a[12:15], a[56:59], v[76:79]\n\t" \
  "ds_read_b64_tr_b16 a[70:71], v185 offset:58368\n\t" \
  "v_mfma_f32_16x16x32_bf16 v[108:111], a[12:15], a[60:63], v[108:111]\n\t" \
  "buffer_load_dwordx4 a[244:247], v249, %[ws], s97 offen sc1\n\t" \
  "v_mfma_f32_16x16x32_bf16 v[108:111], a[172:175], a[44:47], v[108:111]\n\t" \
  "ds_read_b64_tr_b16 a[72:73], v186 offset:57344\n\t" \
  "v_mfma_f32_16x16x32_bf16 v[16:19], a[176:179], a[32:35], v[16:19]\n\t" \
  "ds_read_b64_tr_b16 a[74:75], v186 offset:58368\n\t" \
  "v_mfma_f32_16x16x32_bf16 v[16:19], a[16:19], a[48:51], v[16:19]\n\t" \
  "ds_read_b64_tr_b16 a[76:77], v187 offset:57344\n\t" \
  "v_mfma_f32_16x16x32_bf16 v[48:51], a[16:19], a[52:55], v[48:51]\n\t" \
  "v_mfma_f32_16x16x32_bf16 v[48:51], a[176:179], a[36:39], v[48:51]\n\t" \
  "ds_read_b64_tr_b16 a[78:79], v187 offset:58368\n\t" \
  "v_mfma_f32_16x16x32_bf16 v[80:83], a[176:179], a[40:43], v[80:83]\n\t" \
  "buffer_load_dwordx4 a[248:251], v250, %[ws], s97 offen sc1\n\t" \
  "v_mfma_f32_16x16x32_bf16 v[80:83], a[16:19], a[56:59], v[80:83]\n\t" \
  "ds_read_b64_tr_b16 a[80:81], v188 offset:57344\n\t" \
  "v_mfma_f32_16x16x32_bf16 v[112:115], a[16:19], a[60:63], v[112:115]\n\t" \
  "ds_read_b64_tr_b16 a[82:83], v188 offset:58368\n\t" \
  "v_mfma_f32_16x16x32_bf16 v[112:115], a[176:179], a[44:47], v[112:115]\n\t" \
  "ds_read_b64_tr_b16 a[84:85], v189 offset:57344\n\t" \
  "v_mfma_f32_16x16x32_bf16 v[20:23], a[180:183], a[32:35], v[20:23]\n\t" \
  "v_mfma_f32_16x16x32_bf16 v[20:23], a[20:23], a[48:51], v[20:23]\n\t" \
  "ds_read_b64_tr_b16 a[86:87], v189 offset:58368\n\t" \
  "v_mfma_f32_16x16x32_bf16 v[52:55], a[20:23], a[52:55], v[52:55]\n\t" \
  "buffer_load_dwordx4 a[252:255], v251, %[ws], s97 offen sc1\n\t" \
  "v_mfma_f32_16x16x32_bf16 v[52:55], a[180:183], a[36:39], v[52:55]\n\t" \
  "ds_read_b64_tr_b16 a[88:89], v190 offset:57344\n\t" \
  "v_mfma_f32_16x16x32_bf16 v[84:87], a[180:183], a[40:43], v[84:87]\n\t" \
  "ds_read_b64_tr_b16 a[90:91], v190 offset:58368\n\t" \
  "v_mfma_f32_16x16x32_bf16 v[84:87], a[20:23], a[56:59], v[84:87]\n\t" \
  "ds_read_b64_tr_b16 a[92:93], v191 offset:57344\n\t" \
  "v_mfma_f32_16x16x32_bf16 v[116:119], a[20:23], a[60:63], v[116:119]\n\t" \
  "v_mfma_f32_16x16x32_bf16 v[116:119], a[180:183], a[44:47], v[116:119]\n\t" \
  "ds_read_b64_tr_b16 a[94:95], v191 offset:58368\n\t" \
  "v_mfma_f32_16x16x32_bf16 v[24:27], a[184:187], a[32:35], v[24:27]\n\t" \
  "buffer_load_dword %[pf], v217, %[ws], %[pto] offen sc1\n\t" \
  "v_mfma_f32_16x16x32_bf16 v[24:27], a[24:27], a[48:51], v[24:27]\n\t" \
  "v_mfma_f32_16x16x32_bf16 v[56:59], a[24:27], a[52:55], v[56:59]\n\t" \
  "s_mul_i32 s99, %[sofk], 5\n\t" \
  "v_mfma_f32_16x16x32_bf16 v[56:59], a[184:187], a[36:39], v[56:59]\n\t" \
  "s_xor_b32 s99, s99, 0\n\t" \
  "v_mfma_f32_16x16x32_bf16 v[88:91], a[184:187], a[40:43], v[88:91]\n\t" \
  "s_lshr_b32 s99, s99, 1\n\t" \
  "v_mfma_f32_16x16x32_bf16 v[88:91], a[24:27], a[56:59], v[88:91]\n\t" \
  "v_mfma_f32_16x16x32_bf16 v[120:123], a[24:27], a[60:63], v[120:123]\n\t" \
  "buffer_load_dword %[pf], v217, %[sD], s99 offen\n\t" \
  "v_mfma_f32_16x16x32_bf16 v[120:123], a[184:187], a[44:47], v[120:123]\n\t" \
  "v_mfma_f32_16x16x32_bf16 v[28:31], a[188:191], a[32:35], v[28:31]\n\t" \
  "v_mfma_f32_16x16x32_bf16 v[28:31], a[28:31], a[48:51], v[28:31]\n\t" \
  "v_mfma_f32_16x16x32_bf16 v[60:63], a[28:31], a[52:55], v[60:63]\n\t" \
  "v_mfma_f32_16x16x32_bf16 v[60:63], a[188:191], a[36:39], v[60:63]\n\t" \
  "v_mfma_f32_16x16x32_bf16 v[92:95], a[188:191], a[40:43], v[92:95]\n\t" \
  "v_mfma_f32_16x16x32_bf16 v[92:95], a[28:31], a[56:59], v[92:95]\n\t" \
  "v_mfma_f32_16x16x32_bf16 v[124:127], a[28:31], a[60:63], v[124:127]\n\t" \
  "v_mfma_f32_16x16x32_bf16 v[124:127], a[188:191], a[44:47], v[124:127]\n\t" \
  "s_waitcnt vmcnt(23) lgkmcnt(0)\n\t" \
  "s_barrier\n\t" \
  "v_mfma_f32_16x16x32_bf16 v[0:3], a[128:131], a[96:99], v[0:3]\n\t" \
  "s_mul_i32 s99, %[sofk], 5\n\t" \
  "ds_read_b64_tr_b16 a[160:161], v184 offset:0\n\t" \
  "v_mfma_f32_16x16x32_bf16 v[0:3], a[64:67], a[112:115], v[0:3]\n\t" \
  "s_xor_b32 s99, s99, 4096\n\t" \
  "v_mfma_f32_16x16x32_bf16 v[32:35], a[64:67], a[116:119], v[32:35]\n\t" \
  "s_lshr_b32 s99, s99, 1\n\t" \
  "ds_read_b64_tr_b16 a[162:163], v184 offset:1024\n\t" \
  "v_mfma_f32_16x16x32_bf16 v[32:35], a[128:131], a[100:103], v[32:35]\n\t" \
  "buffer_load_dword %[pf], v217, %[sD], s99 offen\n\t" \
  "v_mfma_f32_16x16x32_bf16 v[64:67], a[128:131], a[104:107], v[64:67]\n\t" \
  "s_add_u32 m0, %[mw], 16384\n\t" \
  "ds_read_b64_tr_b16 a[164:165], v185 offset:0\n\t" \
  "v_mfma_f32_16x16x32_bf16 v[64:67], a[64:67], a[120:123], v[64:67]\n\t" \
  "s_add_u32 s96, %[sofk], 0\n\t" \
  "ds_read_b64_tr_b16 a[166:167], v185 offset:1024\n\t" \
  "v_mfma_f32_16x16x32_bf16 v[96:99], a[64:67], a[124:127], v[96:99]\n\t" \
  "ds_read_b64_tr_b16 a[168:169], v186 offset:0\n\t" \
  "v_mfma_f32_16x16x32_bf16 v[96:99], a[128:131], a[108:111], v[96:99]\n\t" \
  "v_mfma_f32_16x16x32_bf16 v[4:7], a[132:135], a[96:99], v[4:7]\n\t" \
  "buffer_load_dwordx4 v224, %[sK], s96 offen offset:0 lds\n\t" \
  "v_mfma_f32_16x16x32_bf16 v[4:7], a[68:71], a[112:115], v[4:7]\n\t" \
  "ds_read_b64_tr_b16 a[170:171], v186 offset:1024\n\t" \
  "v_mfma_f32_16x16x32_bf16 v[36:39], a[68:71], a[116:119], v[36:39]\n\t" \
  "ds_read_b64_tr_b16 a[172:173], v187 offset:0\n\t" \
  "v_mfma_f32_16x16x32_bf16 v[36:39], a[132:135], a[100:103], v[36:39]\n\t" \
  "ds_read_b64_tr_b16 a[174:175], v187 offset:1024\n\t" \
  "v_mfma_f32_16x16x32_bf16 v[68:71], a[132:135], a[104:107], v[68:71]\n\t" \
  "ds_read_b64_tr_b16 a[176:177], v188 offset:0\n\t" \
  "v_mfma_f32_16x16x32_bf16 v[68:71], a[68:71], a[120:123], v[68:71]\n\t" \
  "v_mfma_f32_16x16x32_bf16 v[100:103], a[68:71], a[124:127], v[100:103]\n\t" \
  "ds_read_b64_tr_b16 a[178:179], v188 offset:1024\n\t" \
  "v_mfma_f32_16x16x32_bf16 v[100:103], a[132:135], a[108:111], v[100:103]\n\t" \
  "ds_read_b64_tr_b16 a[180:181], v189 offset:0\n\t" \
  "v_mfma_f32_16x16x32_bf16 v[8:11], a[136:139], a[96:99], v[8:11]\n\t" \
  "v_mfma_f32_16x16x32_bf16 v[8:11], a[72:75], a[112:115], v[8:11]\n\t" \
  "buffer_load_dwordx4 v225, %[sK], s96 offen offset:1024 lds\n\t" \
  "v_mfma_f32_16x16x32_bf16 v[40:43], a[72:75], a[116:119], v[40:43]\n\t" \
  "ds_read_b64_tr_b16 a[182:183], v189 offset:1024\n\t" \
  "v_mfma_f32_16x16x32_bf16 v[40:43], a[136:139], a[100:103], v[40:43]\n\t" \
  "ds_read_b64_tr_b16 a[184:185], v190 offset:0\n\t" \
  "v_mfma_f32_16x16x32_bf16 v[72:75], a[136:139], a[104:107], v[72:75]\n\t" \
  "ds_read_b64_tr_b16 a[186:187], v190 offset:1024\n\t" \
  "v_mfma_f32_16x16x32_bf16 v[72:75], a[72:75], a[120:123], v[72:75]\n\t" \
  "ds_read_b64_tr_b16 a[188:189], v191 offset:0\n\t" \
  "v_mfma_f32_16x16x32_bf16 v[104:107], a[72:75], a[124:127], v[104:107]\n\t" \
  "v_mfma_f32_16x16x32_bf16 v[104:107], a[136:139], a[108:111], v[104:107]\n\t" \
  "ds_read_b64_tr_b16 a[190:191], v191 offset:1024\n\t" \
  "v_mfma_f32_16x16x32_bf16 v[12:15], a[140:143], a[96:99], v[12:15]\n\t" \
  "ds_read_b64_tr_b16 a[0:1], v184 offset:8192\n\t" \
  "v_mfma_f32_16x16x32_bf16 v[12:15], a[76:79], a[112:115], v[12:15]\n\t" \
  "buffer_load_dwordx4 v226, %[sK], s96 offen offset:2048 lds\n\t" \
  "v_mfma_f32_16x16x32_bf16 v[44:47], a[76:79], a[116:119], v[44:47]\n\t" \
  "ds_read_b64_tr_b16 a[2:3], v184 offset:9216\n\t" \
  "v_mfma_f32_16x16x32_bf16 v[44:47], a[140:143], a[100:103], v[44:47]\n\t" \
  "ds_read_b64_tr_b16 a[4:5], v185 offset:8192\n\t" \
  "v_mfma_f32_16x16x32_bf16 v[76:79], a[140:143], a[104:107], v[76:79]\n\t" \
  "v_mfma_f32_16x16x32_bf16 v[76:79], a[76:79], a[120:123], v[76:79]\n\t" \
  "ds_read_b64_tr_b16 a[6:7], v185 offset:9216\n\t" \
  "v_mfma_f32_16x16x32_bf16 v[108:111], a[76:79], a[124:127], v[108:111]\n\t" \
  "ds_read_b64_tr_b16 a[8:9], v186 offset:8192\n\t" \
  "v_mfma_f32_16x16x32_bf16 v[108:111], a[140:143], a[108:111], v[108:111]\n\t" \
  "v_mfma_f32_16x16x32_bf16 v[16:19], a[144:147], a[96:99], v[16:19]\n\t" \
  "ds_read_b64_tr_b16 a[10:11], v186 offset:9216\n\t" \
  "v_mfma_f32_16x16x32_bf16 v[16:19], a[80:83], a[112:115], v[16:19]\n\t" \
  "ds_read_b64_tr_b16 a[12:13], v187 offset:8192\n\t" \
  "v_mfma_f32_16x16x32_bf16 v[48:51], a[80:83], a[116:119], v[48:51]\n\t" \
  "buffer_load_dwordx4 v227, %[sK], s96 offen offset:3072 lds\n\t" \
  "v_mfma_f32_16x16x32_bf16 v[48:51], a[144:147], a[100:103], v[48:51]\n\t" \
  "ds_read_b64_tr_b16 a[14:15], v187 offset:9216\n\t" \
  "v_mfma_f32_16x16x32_bf16 v[80:83], a[144:147], a[104:107], v[80:83]\n\t" \
  "ds_read_b64_tr_b16 a[16:17], v188 offset:8192\n\t" \
  "v_mfma_f32_16x16x32_bf16 v[80:83], a[80:83], a[120:123], v[80:83]\n\t" \
  "v_mfma_f32_16x16x32_bf16 v[112:115], a[80:83], a[124:127], v[112:115]\n\t" \
  "s_add_u32 m0, %[mw], 49152\n\t" \
  "ds_read_b64_tr_b16 a[18:19], v188 offset:9216\n\t" \
  "v_mfma_f32_16x16x32_bf16 v[112:115], a[144:147], a[108:111], v[112:115]\n\t" \
  "s_add_u32 s96, %[sofk], 0\n\t" \
  "ds_read_b64_tr_b16 a[20:21], v189 offset:8192\n\t" \
  "v_mfma_f32_16x16x32_bf16 v[20:23], a[148:151], a[96:99], v[20:23]\n\t" \
  "v_mfma_f32_16x16x32_bf16 v[20:23], a[84:87], a[112:115], v[20:23]\n\t" \
  "ds_read_b64_tr_b16 a[22:23], v189 offset:9216\n\t" \
  "v_mfma_f32_16x16x32_bf16 v[52:55], a[84:87], a[116:119], v[52:55]\n\t" \
  "buffer_load_dwordx4 v230, %[sV], s96 offen offset:0 lds\n\t" \
  "v_mfma_f32_16x16x32_bf16 v[52:55], a[148:151], a[100:103], v[52:55]\n\t" \
  "ds_read_b64_tr_b16 a[24:25], v190 offset:8192\n\t" \
  "v_mfma_f32_16x16x32_bf16 v[84:87], a[148:151], a[104:107], v[84:87]\n\t" \
  "ds_read_b64_tr_b16 a[26:27], v190 offset:9216\n\t" \
  "v_mfma_f32_16x16x32_bf16 v[84:87], a[84:87], a[120:123], v[84:87]\n\t" \
  "ds_read_b64_tr_b16 a[28:29], v191 offset:8192\n\t" \
  "v_mfma_f32_16x16x32_bf16 v[116:119], a[84:87], a[124:127], v[116:119]\n\t" \
  "v_mfma_f32_16x16x32_bf16 v[116:119], a[148:151], a[108:111], v[116:119]\n\t" \
  "ds_read_b64_tr_b16 a[30:31], v191 offset:9216\n\t" \
  "v_mfma_f32_16x16x32_bf16 v[24:27], a[152:155], a[96:99], v[24:27]\n\t" \
  "v_mfma_f32_16x16x32_bf16 v[24:27], a[88:91], a[112:115], v[24:27]\n\t" \
  "v_mfma_f32_16x16x32_bf16 v[56:59], a[88:91], a[116:119], v[56:59]\n\t" \
  "v_mfma_f32_16x16x32_bf16 v[56:59], a[152:155], a[100:103], v[56:59]\n\t" \
  "buffer_load_dwordx4 v231, %[sV], s96 offen offset:1024 lds\n\t" \
  "v_mfma_f32_16x16x32_bf16 v[88:91], a[152:155], a[104:107], v[88:91]\n\t" \
  "v_mfma_f32_16x16x32_bf16 v[88:91], a[88:91], a[120:123], v[88:91]\n\t" \
  "v_mfma_f32_16x16x32_bf16 v[120:123], a[88:91], a[124:127], v[120:123]\n\t" \
  "v_mfma_f32_16x16x32_bf16 v[120:123], a[152:155], a[108:111], v[120:123]\n\t" \
  "v_mfma_f32_16x16x32_bf16 v[28:31], a[156:159], a[96:99], v[28:31]\n\t" \
  "v_mfma_f32_16x16x32_bf16 v[28:31], a[92:95], a[112:115], v[28:31]\n\t" \
  "v_mfma_f32_16x16x32_bf16 v[60:63], a[92:95], a[116:119], v[60:63]\n\t" \
  "v_mfma_f32_16x16x32_bf16 v[60:63], a[156:159], a[100:103], v[60:63]\n\t" \
  "v_mfma_f32_16x16x32_bf16 v[92:95], a[156:159], a[104:107], v[92:95]\n\t" \
  "v_mfma_f32_16x16x32_bf16 v[92:95], a[92:95], a[120:123], v[92:95]\n\t" \
  "v_mfma_f32_16x16x32_bf16 v[124:127], a[92:95], a[124:127], v[124:127]\n\t" \
  "v_mfma_f32_16x16x32_bf16 v[124:127], a[156:159], a[108:111], v[124:127]\n\t" \
  "s_waitcnt vmcnt(17) lgkmcnt(0)\n\t" \
  "s_barrier\n\t" \
  "v_mfma_f32_16x16x32_bf16 v[0:3], a[160:163], a[192:195], v[0:3]\n\t" \
  "buffer_load_dwordx4 v230, %[sV], s96 offen offset:2048 lds\n\t" \
  "v_mfma_f32_16x16x32_bf16 v[0:3], a[0:3], a[208:211], v[0:3]\n\t" \
  "ds_read_b64_tr_b16 a[128:129], v192 offset:0\n\t" \
  "v_mfma_f32_16x16x32_bf16 v[32:35], a[0:3], a[212:215], v[32:35]\n\t" \
  "ds_read_b64_tr_b16 a[130:131], v192 offset:1024\n\t" \
  "v_mfma_f32_16x16x32_bf16 v[32:35], a[160:163], a[196:199], v[32:35]\n\t" \
  "ds_read_b64_tr_b16 a[132:133], v193 offset:0\n\t" \
  "v_mfma_f32_16x16x32_bf16 v[64:67], a[160:163], a[200:203], v[64:67]\n\t" \
  "v_mfma_f32_16x16x32_bf16 v[64:67], a[0:3], a[216:219], v[64:67]\n\t" \
  "ds_read_b64_tr_b16 a[134:135], v193 offset:1024\n\t" \
  "v_mfma_f32_16x16x32_bf16 v[96:99], a[0:3], a[220:223], v[96:99]\n\t" \
  "ds_read_b64_tr_b16 a[136:137], v194 offset:0\n\t" \
  "v_mfma_f32_16x16x32_bf16 v[96:99], a[160:163], a[204:207], v[96:99]\n\t" \
  "buffer_load_dwordx4 v231, %[sV], s96 offen offset:3072 lds\n\t" \
  "v_mfma_f32_16x16x32_bf16 v[4:7], a[164:167], a[192:195], v[4:7]\n\t" \
  "ds_read_b64_tr_b16 a[138:139], v194 offset:1024\n\t" \
  "v_mfma_f32_16x16x32_bf16 v[4:7], a[4:7], a[208:211], v[4:7]\n\t" \
  "ds_read_b64_tr_b16 a[140:141], v195 offset:0\n\t" \
  "v_mfma_f32_16x16x32_bf16 v[36:39], a[4:7], a[212:215], v[36:39]\n\t" \
  "v_mfma_f32_16x16x32_bf16 v[36:39], a[164:167], a[196:199], v[36:39]\n\t" \
  "s_add_u32 m0, %[mw], 32768\n\t" \
  "ds_read_b64_tr_b16 a[142:143], v195 offset:1024\n\t" \
  "v_mfma_f32_16x16x32_bf16 v[68:71], a[164:167], a[200:203], v[68:71]\n\t" \
  "s_add_u32 s96, %[sofk], 16384\n\t" \
  "ds_read_b64_tr_b16 a[144:145], v196 offset:0\n\t" \
  "v_mfma_f32_16x16x32_bf16 v[68:71], a[4:7], a[216:219], v[68:71]\n\t" \
  "v_mfma_f32_16x16x32_bf16 v[100:103], a[4:7], a[220:223], v[100:103]\n\t" \
  "ds_read_b64_tr_b16 a[146:147], v196 offset:1024\n\t" \
  "v_mfma_f32_16x16x32_bf16 v[100:103], a[164:167], a[204:207], v[100:103]\n\t" \
  "buffer_load_dwordx4 v224, %[sK], s96 offen offset:0 lds\n\t" \
  "v_mfma_f32_16x16x32_bf16 v[8:11], a[168:171], a[192:195], v[8:11]\n\t" \
  "ds_read_b64_tr_b16 a[148:149], v197 offset:0\n\t" \
  "v_mfma_f32_16x16x32_bf16 v[8:11], a[8:11], a[208:211], v[8:11]\n\t" \
  "ds_read_b64_tr_b16 a[150:151], v197 offset:1024\n\t" \
  "v_mfma_f32_16x16x32_bf16 v[40:43], a[8:11], a[212:215], v[40:43]\n\t" \
  "ds_read_b64_tr_b16 a[152:153], v198 offset:0\n\t" \
  "v_mfma_f32_16x16x32_bf16 v[40:43], a[168:171], a[196:199], v[40:43]\n\t" \
  "v_mfma_f32_16x16x32_bf16 v[72:75], a[168:171], a[200:203], v[72:75]\n\t" \
  "ds_read_b64_tr_b16 a[154:155], v198 offset:1024\n\t" \
  "v_mfma_f32_16x16x32_bf16 v[72:75], a[8:11], a[216:219], v[72:75]\n\t" \
  "ds_read_b64_tr_b16 a[156:157], v199 offset:0\n\t" \
  "v_mfma_f32_16x16x32_bf16 v[104:107], a[8:11], a[220:223], v[104:107]\n\t" \
  "buffer_load_dwordx4 v225, %[sK], s96 offen offset:1024 lds\n\t" \
  "v_mfma_f32_16x16x32_bf16 v[104:107], a[168:171], a[204:207], v[104:107]\n\t" \
  "ds_read_b64_tr_b16 a[158:159], v199 offset:1024\n\t" \
  "v_mfma_f32_16x16x32_bf16 v[12:15], a[172:175], a[192:195], v[12:15]\n\t" \
  "ds_read_b64_tr_b16 a[64:65], v192 offset:8192\n\t" \
  "v_mfma_f32_16x16x32_bf16 v[12:15], a[12:15], a[208:211], v[12:15]\n\t" \
  "v_mfma_f32_16x16x32_bf16 v[44:47], a[12:15], a[212:215], v[44:47]\n\t" \
  "ds_read_b64_tr_b16 a[66:67], v192 offset:9216\n\t" \
  "v_mfma_f32_16x16x32_bf16 v[44:47], a[172:175], a[196:199], v[44:47]\n\t" \
  "ds_read_b64_tr_b16 a[68:69], v193 offset:8192\n\t" \
  "v_mfma_f32_16x16x32_bf16 v[76:79], a[172:175], a[200:203], v[76:79]\n\t" \
  "v_mfma_f32_16x16x32_bf16 v[76:79], a[12:15], a[216:219], v[76:79]\n\t" \
  "ds_read_b64_tr_b16 a[70:71], v193 offset:9216\n\t" \
  "v_mfma_f32_16x16x32_bf16 v[108:111], a[12:15], a[220:223], v[108:111]\n\t" \
  "buffer_load_dwordx4 v226, %[sK], s96 offen offset:2048 lds\n\t" \
  "v_mfma_f32_16x16x32_bf16 v[108:111], a[172:175], a[204:207], v[108:111]\n\t" \
  "ds_read_b64_tr_b16 a[72:73], v194 offset:8192\n\t" \
  "v_mfma_f32_16x16x32_bf16 v[16:19], a[176:179], a[192:195], v[16:19]\n\t" \
  "ds_read_b64_tr_b16 a[74:75], v194 offset:9216\n\t" \
  "v_mfma_f32_16x16x32_bf16 v[16:19], a[16:19], a[208:211], v[16:19]\n\t" \
  "ds_read_b64_tr_b16 a[76:77], v195 offset:8192\n\t" \
  "v_mfma_f32_16x16x32_bf16 v[48:51], a[16:19], a[212:215], v[48:51]\n\t" \
  "v_mfma_f32_16x16x32_bf16 v[48:51], a[176:179], a[196:199], v[48:51]\n\t" \
  "ds_read_b64_tr_b16 a[78:79], v195 offset:9216\n\t" \
  "v_mfma_f32_16x16x32_bf16 v[80:83], a[176:179], a[200:203], v[80:83]\n\t" \
  "ds_read_b64_tr_b16 a[80:81], v196 offset:8192\n\t" \
  "v_mfma_f32_16x16x32_bf16 v[80:83], a[16:19], a[216:219], v[80:83]\n\t" \
  "buffer_load_dwordx4 v227, %[sK], s96 offen offset:3072 lds\n\t" \
  "v_mfma_f32_16x16x32_bf16 v[112:115], a[16:19], a[220:223], v[112:115]\n\t" \
  "ds_read_b64_tr_b16 a[82:83], v196 offset:9216\n\t" \
  "v_mfma_f32_16x16x32_bf16 v[112:115], a[176:179], a[204:207], v[112:115]\n\t" \
  "ds_read_b64_tr_b16 a[84:85], v197 offset:8192\n\t" \
  "v_mfma_f32_16x16x32_bf16 v[20:23], a[180:183], a[192:195], v[20:23]\n\t" \
  "v_mfma_f32_16x16x32_bf16 v[20:23], a[20:23], a[208:211], v[20:23]\n\t" \
  "s_add_u32 s99, %[pf4], 0\n\t" \
  "ds_read_b64_tr_b16 a[86:87], v197 offset:9216\n\t" \
  "v_mfma_f32_16x16x32_bf16 v[52:55], a[20:23], a[212:215], v[52:55]\n\t" \
  "ds_read_b64_tr_b16 a[88:89], v198 offset:8192\n\t" \
  "v_mfma_f32_16x16x32_bf16 v[52:55], a[180:183], a[196:199], v[52:55]\n\t" \
  "v_mfma_f32_16x16x32_bf16 v[84:87], a[180:183], a[200:203], v[84:87]\n\t" \
  "ds_read_b64_tr_b16 a[90:91], v198 offset:9216\n\t" \
  "v_mfma_f32_16x16x32_bf16 v[84:87], a[20:23], a[216:219], v[84:87]\n\t" \
  "buffer_load_dword %[pf], v245, %[sU], s99 offen\n\t" \
  "v_mfma_f32_16x16x32_bf16 v[116:119], a[20:23], a[220:223], v[116:119]\n\t" \
  "ds_read_b64_tr_b16 a[92:93], v199 offset:8192\n\t" \
  "v_mfma_f32_16x16x32_bf16 v[116:119], a[180:183], a[204:207], v[116:119]\n\t" \
  "ds_read_b64_tr_b16 a[94:95], v199 offset:9216\n\t" \
  "v_mfma_f32_16x16x32_bf16 v[24:27], a[184:187], a[192:195], v[24:27]\n\t" \
  "s_add_u32 s99, %[pf4], 14336\n\t" \
  "v_mfma_f32_16x16x32_bf16 v[24:27], a[24:27], a[208:211], v[24:27]\n\t" \
  "v_mfma_f32_16x16x32_bf16 v[56:59], a[24:27], a[212:215], v[56:59]\n\t" \
  "v_mfma_f32_16x16x32_bf16 v[56:59], a[184:187], a[196:199], v[56:59]\n\t" \
  "v_mfma_f32_16x16x32_bf16 v[88:91], a[184:187], a[200:203], v[88:91]\n\t" \
  "buffer_load_dword %[pf], v245, %[sU], s99 offen\n\t" \
  "v_mfma_f32_16x16x32_bf16 v[88:91], a[24:27], a[216:219], v[88:91]\n\t" \
  "v_mfma_f32_16x16x32_bf16 v[120:123], a[24:27], a[220:223], v[120:123]\n\t" \
  "v_mfma_f32_16x16x32_bf16 v[120:123], a[184:187], a[204:207], v[120:123]\n\t" \
  "v_mfma_f32_16x16x32_bf16 v[28:31], a[188:191], a[192:195], v[28:31]\n\t" \
  "v_mfma_f32_16x16x32_bf16 v[28:31], a[28:31], a[208:211], v[28:31]\n\t" \
  "v_mfma_f32_16x16x32_bf16 v[60:63], a[28:31], a[212:215], v[60:63]\n\t" \
  "v_mfma_f32_16x16x32_bf16 v[60:63], a[188:191], a[196:199], v[60:63]\n\t" \
  "v_mfma_f32_16x16x32_bf16 v[92:95], a[188:191], a[200:203], v[92:95]\n\t" \
  "v_mfma_f32_16x16x32_bf16 v[92:95], a[28:31], a[216:219], v[92:95]\n\t" \
  "v_mfma_f32_16x16x32_bf16 v[124:127], a[28:31], a[220:223], v[124:127]\n\t" \
  "v_mfma_f32_16x16x32_bf16 v[124:127], a[188:191], a[204:207], v[124:127]\n\t" \
  "s_waitcnt vmcnt(17)\n\t" \
  "s_waitcnt lgkmcnt(0)\n\t" \
  FSTP("s_waitcnt lgkmcnt(0)\n\t") \
  FSTP("v_writelane_b32 %[stv], s94, 52\n\t") \
  FSTP("v_writelane_b32 %[stv], s92, 53\n\t") \
  ".subsection 1\n\t" \
  ".Lfq_s:\n\t" \
  "s_mov_b32 s97, 0\n\t" \
  ".Lfq_r:\n\t" \
  "buffer_load_dword %[fl], v210, %[ws], %[fo] offen sc1\n\t" \
  "s_waitcnt vmcnt(0)\n\t" \
  "v_cmp_ne_u32 vcc, v211, %[fl]\n\t" \
  "s_cbranch_vccz .Lfq_k\n\t" \
  "s_add_u32 s97, s97, 1\n\t" \
  "s_cmp_lt_u32 s97, " FQ_CAP "\n\t" \
  "s_cbranch_scc1 .Lfq_r\n\t" \
  "buffer_store_dword v211, off, %[ws], 0 offset:256 sc1\n\t" \
  "s_branch .Lfq_k\n\t" \
  ".subsection 0\n\t"
#define FQ_CL "memory", "scc", "vcc", "s96", "s97", "s98", "s99", "v128", "v129", "v130", "v131", "v132", "v133", "v134", "v135", "v136", "v137", "v138", "v139", "v140", "v141", "v142", "v143", "v144", "v145", "v146", "v147", "v148", "v149", "v150", "v151", "v152", "v153", "v154", "v155", "v156", "v157", "v158", "v159", "v176", "v177", "v178", "v179", "v180", "v181", "v182", "v183", "v249", "v250", "v251" FQ_STC
#define FQ_X2E 0
#define FQ_W1L 1
#define FQ_W2E 0
static_assert(W_VOTE == 8192 && W_FLAGR == 32768 && W_FLAGO == 131072 && W_MODB == 2228224 && W_SIL == 2359296 && W_OT == 8388608 && W_XCH == 25165824);
#define FQ_EV 0
#define FQ_SD [sU] "s"(c.sU), [sD] "s"(c.sD),
#define FQ_EB 0
#define FQ_CTO
#define FQ_XD 1
#define STS(x)
#define AT_STV
#define STA(x)
#define AT_ASM \
  "s_cmp_eq_u32 %[pm], 0\n\t" \
  "s_cbranch_scc1 .Lsf_go\n\t" \
  "v_mfma_f32_16x16x32_bf16 v[28:31], a[156:159], a[224:227], v[28:31]\n\t" \
  "v_mov_b32 v216, 0x3f803f80\n\t" \
  "v_lshlrev_b32 v204, 7, v233\n\t" \
  "v_mfma_f32_16x16x32_bf16 v[28:31], a[92:95], a[240:243], v[28:31]\n\t" \
  "v_cmp_gt_u32 vcc, 16, v233\n\t" \
  "v_mov_b32 v217, 0x3f803f80\n\t" \
  "v_mfma_f32_16x16x32_bf16 v[24:27], a[152:155], a[224:227], v[24:27]\n\t" \
  "v_lshrrev_b32 v202, 3, v233\n\t" \
  "v_mov_b32 v218, 0x3f803f80\n\t" \
  "v_mfma_f32_16x16x32_bf16 v[24:27], a[88:91], a[240:243], v[24:27]\n\t" \
  "v_mul_u32_u24 v202, 11264, v202\n\t" \
  "v_mov_b32 v219, 0x3f803f80\n\t" \
  "v_mfma_f32_16x16x32_bf16 v[20:23], a[148:151], a[224:227], v[20:23]\n\t" \
  "v_cvt_pk_bf16_f32 v31, v30, v31\n\t" \
  "v_add_u32 v202, v202, v204\n\t" \
  "v_mfma_f32_16x16x32_bf16 v[20:23], a[84:87], a[240:243], v[20:23]\n\t" \
  "v_cvt_pk_bf16_f32 v30, v28, v29\n\t" \
  "v_lshlrev_b32 v242, 4, v233\n\t" \
  "v_mfma_f32_16x16x32_bf16 v[16:19], a[144:147], a[224:227], v[16:19]\n\t" \
  "s_mov_b32 s99, 0\n\t" \
  "v_bfrev_b32 v195, 1\n\t" \
  "v_cvt_pk_bf16_f32 v28, v24, v25\n\t" \
  "v_mfma_f32_16x16x32_bf16 v[16:19], a[80:83], a[240:243], v[16:19]\n\t" \
  "v_cvt_pk_bf16_f32 v29, v26, v27\n\t" \
  "v_add_u32 v237, 65536, v228\n\t" \
  "v_mfma_f32_16x16x32_bf16 v[12:15], a[140:143], a[224:227], v[12:15]\n\t" \
  "v_cndmask_b32 v204, v195, v204, vcc\n\t" \
  "v_cndmask_b32 v202, v195, v202, vcc\n\t" \
  "v_mfma_f32_16x16x32_bf16 v[12:15], a[76:79], a[240:243], v[12:15]\n\t" \
  "v_cvt_pk_bf16_f32 v27, v22, v23\n\t" \
  "v_add_u32 v238, 65536, v229\n\t" \
  "v_mfma_f32_16x16x32_bf16 v[8:11], a[136:139], a[224:227], v[8:11]\n\t" \
  "s_mov_b32 %[na], 0\n\t" \
  "v_permlane16_swap_b32 v28, v30\n\t" \
  "v_mfma_f32_16x16x32_bf16 v[8:11], a[72:75], a[240:243], v[8:11]\n\t" \
  "v_cvt_pk_bf16_f32 v26, v20, v21\n\t" \
  "v_cvt_pk_bf16_f32 v24, v16, v17\n\t" \
  "v_mfma_f32_16x16x32_bf16 v[4:7], a[132:135], a[224:227], v[4:7]\n\t" \
  "v_permlane16_swap_b32 v29, v31\n\t" \
  "v_mfma_f32_16x16x32_bf16 v[4:7], a[68:71], a[240:243], v[4:7]\n\t" \
  "v_cvt_pk_bf16_f32 v25, v18, v19\n\t" \
  "v_cvt_pk_bf16_f32 v23, v14, v15\n\t" \
  "v_mfma_f32_16x16x32_bf16 v[0:3], a[128:131], a[224:227], v[0:3]\n\t" \
  "v_cvt_pk_bf16_f32 v22, v12, v13\n\t" \
  "v_cvt_pk_bf16_f32 v20, v8, v9\n\t" \
  "v_mfma_f32_16x16x32_bf16 v[0:3], a[64:67], a[240:243], v[0:3]\n\t" \
  "s_waitcnt vmcnt(2)\n\t" \
  "s_barrier\n\t" \
  "v_mfma_f32_16x16x32_bf16 v[60:63], a[156:159], a[228:231], v[60:63]\n\t" \
  "ds_read_b128 a[0:3], v220 offset:0\n\t" \
  "v_cvt_pk_bf16_f32 v21, v10, v11\n\t" \
  "v_mfma_f32_16x16x32_bf16 v[60:63], a[92:95], a[244:247], v[60:63]\n\t" \
  "ds_read_b128 a[4:7], v221 offset:0\n\t" \
  "v_cvt_pk_bf16_f32 v19, v6, v7\n\t" \
  "v_mfma_f32_16x16x32_bf16 v[56:59], a[152:155], a[228:231], v[56:59]\n\t" \
  "s_add_u32 m0, %[m], 0\n\t" \
  "ds_read_b128 a[8:11], v222 offset:0\n\t" \
  "v_cvt_pk_bf16_f32 v18, v4, v5\n\t" \
  "v_mfma_f32_16x16x32_bf16 v[56:59], a[88:91], a[244:247], v[56:59]\n\t" \
  "s_xor_b32 s97, %[wob], 0\n\t" \
  "ds_read_b128 a[12:15], v223 offset:0\n\t" \
  "v_cvt_pk_bf16_f32 v16, v0, v1\n\t" \
  "v_mfma_f32_16x16x32_bf16 v[52:55], a[148:151], a[228:231], v[52:55]\n\t" \
  "ds_read_b128 a[16:19], v220 offset:4096\n\t" \
  "v_cvt_pk_bf16_f32 v17, v2, v3\n\t" \
  "v_mfma_f32_16x16x32_bf16 v[52:55], a[84:87], a[244:247], v[52:55]\n\t" \
  "ds_read_b128 a[20:23], v221 offset:4096\n\t" \
  "v_cvt_pk_bf16_f32 v63, v62, v63\n\t" \
  "v_mfma_f32_16x16x32_bf16 v[48:51], a[144:147], a[228:231], v[48:51]\n\t" \
  "ds_read_b128 a[24:27], v222 offset:4096\n\t" \
  "v_cvt_pk_bf16_f32 v62, v60, v61\n\t" \
  "v_mfma_f32_16x16x32_bf16 v[48:51], a[80:83], a[244:247], v[48:51]\n\t" \
  "ds_read_b128 a[28:31], v223 offset:4096\n\t" \
  "v_cvt_pk_bf16_f32 v60, v56, v57\n\t" \
  "v_mfma_f32_16x16x32_bf16 v[44:47], a[140:143], a[228:231], v[44:47]\n\t" \
  "ds_read_b128 a[32:35], v220 offset:8192\n\t" \
  "v_cvt_pk_bf16_f32 v61, v58, v59\n\t" \
  "v_mfma_f32_16x16x32_bf16 v[44:47], a[76:79], a[244:247], v[44:47]\n\t" \
  "buffer_load_dwordx4 v240, %[wo], s97 offen offset:0 lds\n\t" \
  "v_mfma_f32_16x16x32_bf16 v[40:43], a[136:139], a[228:231], v[40:43]\n\t" \
  "s_add_u32 s97, s97, 7168\n\t" \
  "ds_read_b128 a[36:39], v221 offset:8192\n\t" \
  "v_cvt_pk_bf16_f32 v59, v54, v55\n\t" \
  "v_mfma_f32_16x16x32_bf16 v[40:43], a[72:75], a[244:247], v[40:43]\n\t" \
  "ds_read_b128 a[40:43], v222 offset:8192\n\t" \
  "v_cvt_pk_bf16_f32 v58, v52, v53\n\t" \
  "v_mfma_f32_16x16x32_bf16 v[36:39], a[132:135], a[228:231], v[36:39]\n\t" \
  "ds_read_b128 a[44:47], v223 offset:8192\n\t" \
  "v_cvt_pk_bf16_f32 v56, v48, v49\n\t" \
  "v_mfma_f32_16x16x32_bf16 v[36:39], a[68:71], a[244:247], v[36:39]\n\t" \
  "buffer_load_dwordx4 v241, %[wo], s97 offen offset:1024 lds\n\t" \
  "v_mfma_f32_16x16x32_bf16 v[32:35], a[128:131], a[228:231], v[32:35]\n\t" \
  "s_add_u32 s97, s97, 7168\n\t" \
  "ds_read_b128 a[48:51], v220 offset:12288\n\t" \
  "v_cvt_pk_bf16_f32 v57, v50, v51\n\t" \
  "v_mfma_f32_16x16x32_bf16 v[32:35], a[64:67], a[244:247], v[32:35]\n\t" \
  "ds_read_b128 a[52:55], v221 offset:12288\n\t" \
  "v_cvt_pk_bf16_f32 v55, v46, v47\n\t" \
  "v_mfma_f32_16x16x32_bf16 v[92:95], a[156:159], a[232:235], v[92:95]\n\t" \
  "buffer_load_dwordx4 v240, %[wo], s97 offen offset:2048 lds\n\t" \
  "v_mfma_f32_16x16x32_bf16 v[92:95], a[92:95], a[248:251], v[92:95]\n\t" \
  "s_add_u32 s97, s97, 7168\n\t" \
  "ds_read_b128 a[56:59], v222 offset:12288\n\t" \
  "v_cvt_pk_bf16_f32 v54, v44, v45\n\t" \
  "v_mfma_f32_16x16x32_bf16 v[88:91], a[152:155], a[232:235], v[88:91]\n\t" \
  "ds_read_b128 a[60:63], v223 offset:12288\n\t" \
  "v_cvt_pk_bf16_f32 v52, v40, v41\n\t" \
  "v_mfma_f32_16x16x32_bf16 v[88:91], a[88:91], a[248:251], v[88:91]\n\t" \
  "v_permlane16_swap_b32 v24, v26\n\t" \
  "v_mfma_f32_16x16x32_bf16 v[84:87], a[148:151], a[232:235], v[84:87]\n\t" \
  "buffer_load_dwordx4 v241, %[wo], s97 offen offset:3072 lds\n\t" \
  "v_mfma_f32_16x16x32_bf16 v[84:87], a[84:87], a[248:251], v[84:87]\n\t" \
  "s_add_u32 m0, %[m], 16384\n\t" \
  "v_cvt_pk_bf16_f32 v53, v42, v43\n\t" \
  "v_cvt_pk_bf16_f32 v51, v38, v39\n\t" \
  "v_mfma_f32_16x16x32_bf16 v[80:83], a[144:147], a[232:235], v[80:83]\n\t" \
  "s_xor_b32 s97, %[wob], 131072\n\t" \
  "v_permlane16_swap_b32 v25, v27\n\t" \
  "v_mfma_f32_16x16x32_bf16 v[80:83], a[80:83], a[248:251], v[80:83]\n\t" \
  "v_cvt_pk_bf16_f32 v50, v36, v37\n\t" \
  "v_cvt_pk_bf16_f32 v48, v32, v33\n\t" \
  "v_mfma_f32_16x16x32_bf16 v[76:79], a[140:143], a[232:235], v[76:79]\n\t" \
  "v_permlane16_swap_b32 v20, v22\n\t" \
  "v_mfma_f32_16x16x32_bf16 v[76:79], a[76:79], a[248:251], v[76:79]\n\t" \
  "v_cvt_pk_bf16_f32 v49, v34, v35\n\t" \
  "v_cvt_pk_bf16_f32 v95, v94, v95\n\t" \
  "v_mfma_f32_16x16x32_bf16 v[72:75], a[136:139], a[232:235], v[72:75]\n\t" \
  "v_permlane16_swap_b32 v21, v23\n\t" \
  "v_mfma_f32_16x16x32_bf16 v[72:75], a[72:75], a[248:251], v[72:75]\n\t" \
  "v_cvt_pk_bf16_f32 v94, v92, v93\n\t" \
  "v_cvt_pk_bf16_f32 v92, v88, v89\n\t" \
  "v_mfma_f32_16x16x32_bf16 v[68:71], a[132:135], a[232:235], v[68:71]\n\t" \
  "v_permlane16_swap_b32 v16, v18\n\t" \
  "v_mfma_f32_16x16x32_bf16 v[68:71], a[68:71], a[248:251], v[68:71]\n\t" \
  "v_cvt_pk_bf16_f32 v93, v90, v91\n\t" \
  "v_cvt_pk_bf16_f32 v91, v86, v87\n\t" \
  "v_mfma_f32_16x16x32_bf16 v[64:67], a[128:131], a[232:235], v[64:67]\n\t" \
  "buffer_load_dwordx4 v240, %[wo], s97 offen offset:0 lds\n\t" \
  "v_mfma_f32_16x16x32_bf16 v[64:67], a[64:67], a[248:251], v[64:67]\n\t" \
  "s_add_u32 s97, s97, 7168\n\t" \
  "v_permlane16_swap_b32 v17, v19\n\t" \
  "v_mfma_f32_16x16x32_bf16 v[124:127], a[156:159], a[236:239], v[124:127]\n\t" \
  "v_cvt_pk_bf16_f32 v90, v84, v85\n\t" \
  "v_cvt_pk_bf16_f32 v88, v80, v81\n\t" \
  "v_mfma_f32_16x16x32_bf16 v[124:127], a[92:95], a[252:255], v[124:127]\n\t" \
  "v_permlane16_swap_b32 v60, v62\n\t" \
  "v_mfma_f32_16x16x32_bf16 v[120:123], a[152:155], a[236:239], v[120:123]\n\t" \
  "buffer_load_dwordx4 v241, %[wo], s97 offen offset:1024 lds\n\t" \
  "v_mfma_f32_16x16x32_bf16 v[120:123], a[88:91], a[252:255], v[120:123]\n\t" \
  ".Lhs5:\n\ts_cbranch_execz .Lhs5n\n\t" \
  "s_add_u32 s97, s97, 7168\n\t" \
  "v_cvt_pk_bf16_f32 v89, v82, v83\n\t" \
  "v_cvt_pk_bf16_f32 v87, v78, v79\n\t" \
  "v_mfma_f32_16x16x32_bf16 v[116:119], a[148:151], a[236:239], v[116:119]\n\t" \
  "v_permlane16_swap_b32 v61, v63\n\t" \
  "v_mfma_f32_16x16x32_bf16 v[116:119], a[84:87], a[252:255], v[116:119]\n\t" \
  "buffer_load_dwordx4 v240, %[wo], s97 offen offset:2048 lds\n\t" \
  "v_mfma_f32_16x16x32_bf16 v[112:115], a[144:147], a[236:239], v[112:115]\n\t" \
  "s_add_u32 s97, s97, 7168\n\t" \
  "v_cvt_pk_bf16_f32 v86, v76, v77\n\t" \
  "v_cvt_pk_bf16_f32 v84, v72, v73\n\t" \
  "v_mfma_f32_16x16x32_bf16 v[112:115], a[80:83], a[252:255], v[112:115]\n\t" \
  "v_permlane16_swap_b32 v56, v58\n\t" \
  "v_mfma_f32_16x16x32_bf16 v[108:111], a[140:143], a[236:239], v[108:111]\n\t" \
  "v_cvt_pk_bf16_f32 v85, v74, v75\n\t" \
  "v_cvt_pk_bf16_f32 v83, v70, v71\n\t" \
  "v_mfma_f32_16x16x32_bf16 v[108:111], a[76:79], a[252:255], v[108:111]\n\t" \
  "buffer_load_dwordx4 v241, %[wo], s97 offen offset:3072 lds\n\t" \
  "v_mfma_f32_16x16x32_bf16 v[104:107], a[136:139], a[236:239], v[104:107]\n\t" \
  "v_permlane16_swap_b32 v57, v59\n\t" \
  "v_mfma_f32_16x16x32_bf16 v[104:107], a[72:75], a[252:255], v[104:107]\n\t" \
  "v_cvt_pk_bf16_f32 v82, v68, v69\n\t" \
  "v_cvt_pk_bf16_f32 v80, v64, v65\n\t" \
  "v_mfma_f32_16x16x32_bf16 v[100:103], a[132:135], a[236:239], v[100:103]\n\t" \
  "v_permlane16_swap_b32 v52, v54\n\t" \
  "v_mfma_f32_16x16x32_bf16 v[100:103], a[68:71], a[252:255], v[100:103]\n\t" \
  "buffer_load_dword %[pfd], v247, %[ws], 0 offen sc1\n\t" \
  "v_mfma_f32_16x16x32_bf16 v[96:99], a[128:131], a[236:239], v[96:99]\n\t" \
  "s_add_u32 s98, %[pf4], 28672\n\t" \
  "v_cvt_pk_bf16_f32 v81, v66, v67\n\t" \
  "v_cvt_pk_bf16_f32 v127, v126, v127\n\t" \
  "v_mfma_f32_16x16x32_bf16 v[96:99], a[64:67], a[252:255], v[96:99]\n\t" \
  "s_waitcnt lgkmcnt(0)\n\t" \
  "s_barrier\n\t" \
  STS("s_memrealtime s[94:95]\n\t") \
  STS("s_memtime s[92:93]\n\t") \
  "v_mfma_f32_16x16x32_bf16 v[0:3], a[0:3], v[16:19], 0\n\t" \
  "s_add_u32 m0, %[m], 49152\n\t" \
  "buffer_load_dword %[pfd], v245, %[sU], s98 offen\n\t" \
  "v_mfma_f32_16x16x32_bf16 v[0:3], a[4:7], v[20:23], v[0:3]\n\t" \
  "s_add_u32 s96, %[sofk], 32768\n\t" \
  "ds_read_b128 a[192:195], v220 offset:16384\n\t" \
  "v_cvt_pk_bf16_f32 v126, v124, v125\n\t" \
  "v_mfma_f32_16x16x32_bf16 v[0:3], a[8:11], v[24:27], v[0:3]\n\t" \
  "ds_read_b128 a[196:199], v221 offset:16384\n\t" \
  "v_cvt_pk_bf16_f32 v124, v120, v121\n\t" \
  "v_mfma_f32_16x16x32_bf16 v[0:3], a[12:15], v[28:31], v[0:3]\n\t" \
  "ds_read_b128 a[200:203], v222 offset:16384\n\t" \
  "v_cvt_pk_bf16_f32 v125, v122, v123\n\t" \
  "v_permlane16_swap_b32 v53, v55\n\t" \
  "v_permlane16_swap_b32 v48, v50\n\t" \
  "v_permlane16_swap_b32 v49, v51\n\t" \
  "s_nop 1\n\t" \
  "v_mfma_f32_16x16x32_bf16 v[32:35], a[0:3], v[48:51], 0\n\t" \
  "ds_read_b128 a[204:207], v223 offset:16384\n\t" \
  "v_max3_f32 v194, v0, v1, v2\n\t" \
  "v_mfma_f32_16x16x32_bf16 v[32:35], a[4:7], v[52:55], v[32:35]\n\t" \
  "ds_read_b128 a[208:211], v220 offset:20480\n\t" \
  "v_max_f32 v194, v194, v3\n\t" \
  "v_mfma_f32_16x16x32_bf16 v[32:35], a[8:11], v[56:59], v[32:35]\n\t" \
  "ds_read_b128 a[212:215], v221 offset:20480\n\t" \
  "v_mov_b32 v195, v194\n\t" \
  "v_mfma_f32_16x16x32_bf16 v[32:35], a[12:15], v[60:63], v[32:35]\n\t" \
  "ds_read_b128 a[216:219], v222 offset:20480\n\t" \
  "v_cvt_pk_bf16_f32 v123, v118, v119\n\t" \
  "v_permlane32_swap_b32 v194, v195\n\t" \
  "v_max_f32 v194, v194, v195\n\t" \
  "v_mov_b32 v195, v194\n\t" \
  "s_nop 1\n\t" \
  "v_permlane16_swap_b32 v194, v195\n\t" \
  "v_max_f32 v194, v194, v195\n\t" \
  "v_sub_f32 v176, 0xc2c80000, v194\n\t" \
  "v_sub_f32 v177, 0xc2c80000, v194\n\t" \
  "v_sub_f32 v178, 0xc2c80000, v194\n\t" \
  "v_sub_f32 v179, 0xc2c80000, v194\n\t" \
  "s_nop 1\n\t" \
  "v_mfma_f32_16x16x32_bf16 v[4:7], a[16:19], v[16:19], v[176:179]\n\t" \
  "ds_read_b128 a[220:223], v223 offset:20480\n\t" \
  "v_add_f32 v0, v0, v176\n\t" \
  "v_mfma_f32_16x16x32_bf16 v[4:7], a[20:23], v[20:23], v[4:7]\n\t" \
  "ds_read_b128 a[224:227], v220 offset:24576\n\t" \
  "v_add_f32 v1, v1, v176\n\t" \
  "v_mfma_f32_16x16x32_bf16 v[4:7], a[24:27], v[24:27], v[4:7]\n\t" \
  "ds_read_b128 a[228:231], v221 offset:24576\n\t" \
  "v_add_f32 v2, v2, v176\n\t" \
  "v_mfma_f32_16x16x32_bf16 v[4:7], a[28:31], v[28:31], v[4:7]\n\t" \
  "v_add_f32 v3, v3, v176\n\t" \
  "v_max3_f32 v194, v32, v33, v34\n\t" \
  "v_mfma_f32_16x16x32_bf16 v[8:11], a[32:35], v[16:19], v[176:179]\n\t" \
  STS("v_writelane_b32 %[stv], s94, 18\n\t") \
  STS("v_writelane_b32 %[stv], s92, 19\n\t") \
  "ds_read_b128 a[232:235], v222 offset:24576\n\t" \
  "v_max_f32 v194, v194, v35\n\t" \
  "v_mfma_f32_16x16x32_bf16 v[8:11], a[36:39], v[20:23], v[8:11]\n\t" \
  "v_mov_b32 v195, v194\n\t" \
  "v_cvt_pk_bf16_f32 v122, v116, v117\n\t" \
  "v_mfma_f32_16x16x32_bf16 v[8:11], a[40:43], v[24:27], v[8:11]\n\t" \
  "v_permlane32_swap_b32 v194, v195\n\t" \
  "v_mfma_f32_16x16x32_bf16 v[8:11], a[44:47], v[28:31], v[8:11]\n\t" \
  "ds_read_b128 a[236:239], v223 offset:24576\n\t" \
  "v_max_f32 v194, v194, v195\n\t" \
  "v_mfma_f32_16x16x32_bf16 v[12:15], a[48:51], v[16:19], v[176:179]\n\t" \
  "ds_read_b128 a[240:243], v220 offset:28672\n\t" \
  "v_mov_b32 v195, v194\n\t" \
  "v_mfma_f32_16x16x32_bf16 v[12:15], a[52:55], v[20:23], v[12:15]\n\t" \
  "buffer_load_dwordx4 v224, %[sK], s96 offen offset:0 lds\n\t" \
  "v_mfma_f32_16x16x32_bf16 v[12:15], a[56:59], v[24:27], v[12:15]\n\t" \
  "v_permlane16_swap_b32 v194, v195\n\t" \
  "v_mfma_f32_16x16x32_bf16 v[12:15], a[60:63], v[28:31], v[12:15]\n\t" \
  "ds_read_b128 a[244:247], v221 offset:28672\n\t" \
  "v_max_f32 v194, v194, v195\n\t" \
  "v_permlane16_swap_b32 v92, v94\n\t" \
  "v_permlane16_swap_b32 v93, v95\n\t" \
  "v_permlane16_swap_b32 v88, v90\n\t" \
  "v_permlane16_swap_b32 v89, v91\n\t" \
  "v_permlane16_swap_b32 v84, v86\n\t" \
  "v_permlane16_swap_b32 v85, v87\n\t" \
  "v_permlane16_swap_b32 v80, v82\n\t" \
  "v_permlane16_swap_b32 v81, v83\n\t" \
  "s_nop 1\n\t" \
  "v_mfma_f32_16x16x32_bf16 v[64:67], a[0:3], v[80:83], 0\n\t" \
  "ds_read_b128 a[248:251], v222 offset:28672\n\t" \
  "v_sub_f32 v180, 0xc2c80000, v194\n\t" \
  "v_mfma_f32_16x16x32_bf16 v[64:67], a[4:7], v[84:87], v[64:67]\n\t" \
  "v_sub_f32 v181, 0xc2c80000, v194\n\t" \
  "v_sub_f32 v182, 0xc2c80000, v194\n\t" \
  "v_mfma_f32_16x16x32_bf16 v[64:67], a[8:11], v[88:91], v[64:67]\n\t" \
  "ds_read_b128 a[252:255], v223 offset:28672\n\t" \
  "v_sub_f32 v183, 0xc2c80000, v194\n\t" \
  "v_mfma_f32_16x16x32_bf16 v[64:67], a[12:15], v[92:95], v[64:67]\n\t" \
  "v_add_f32 v32, v32, v180\n\t" \
  "v_add_f32 v33, v33, v180\n\t" \
  "v_mfma_f32_16x16x32_bf16 v[36:39], a[16:19], v[48:51], v[180:183]\n\t" \
  "v_add_f32 v34, v34, v180\n\t" \
  "v_add_f32 v35, v35, v180\n\t" \
  "v_mfma_f32_16x16x32_bf16 v[36:39], a[20:23], v[52:55], v[36:39]\n\t" \
  "buffer_load_dwordx4 v225, %[sK], s96 offen offset:1024 lds\n\t" \
  "v_mfma_f32_16x16x32_bf16 v[36:39], a[24:27], v[56:59], v[36:39]\n\t" \
  "v_max3_f32 v194, v64, v65, v66\n\t" \
  "v_max_f32 v194, v194, v67\n\t" \
  "v_mfma_f32_16x16x32_bf16 v[36:39], a[28:31], v[60:63], v[36:39]\n\t" \
  "v_mov_b32 v195, v194\n\t" \
  "v_cvt_pk_bf16_f32 v120, v112, v113\n\t" \
  "v_mfma_f32_16x16x32_bf16 v[40:43], a[32:35], v[48:51], v[180:183]\n\t" \
  "v_permlane32_swap_b32 v194, v195\n\t" \
  "v_mfma_f32_16x16x32_bf16 v[40:43], a[36:39], v[52:55], v[40:43]\n\t" \
  "v_max_f32 v194, v194, v195\n\t" \
  "v_mov_b32 v195, v194\n\t" \
  "v_mfma_f32_16x16x32_bf16 v[40:43], a[40:43], v[56:59], v[40:43]\n\t" \
  "buffer_load_dwordx4 v226, %[sK], s96 offen offset:2048 lds\n\t" \
  "v_mfma_f32_16x16x32_bf16 v[40:43], a[44:47], v[60:63], v[40:43]\n\t" \
  "v_permlane16_swap_b32 v194, v195\n\t" \
  "v_mfma_f32_16x16x32_bf16 v[44:47], a[48:51], v[48:51], v[180:183]\n\t" \
  "v_max_f32 v194, v194, v195\n\t" \
  "v_sub_f32 v184, 0xc2c80000, v194\n\t" \
  "v_mfma_f32_16x16x32_bf16 v[44:47], a[52:55], v[52:55], v[44:47]\n\t" \
  "v_sub_f32 v185, 0xc2c80000, v194\n\t" \
  "v_sub_f32 v186, 0xc2c80000, v194\n\t" \
  "v_mfma_f32_16x16x32_bf16 v[44:47], a[56:59], v[56:59], v[44:47]\n\t" \
  "v_sub_f32 v187, 0xc2c80000, v194\n\t" \
  "v_add_f32 v64, v64, v184\n\t" \
  "v_mfma_f32_16x16x32_bf16 v[44:47], a[60:63], v[60:63], v[44:47]\n\t" \
  "ds_read_b64_tr_b16 a[128:129], v228 offset:0\n\t" \
  "v_add_f32 v65, v65, v184\n\t" \
  "v_cvt_pk_bf16_f32 v121, v114, v115\n\t" \
  "v_cvt_pk_bf16_f32 v119, v110, v111\n\t" \
  "v_cvt_pk_bf16_f32 v118, v108, v109\n\t" \
  "v_cvt_pk_bf16_f32 v116, v104, v105\n\t" \
  "v_cvt_pk_bf16_f32 v117, v106, v107\n\t" \
  "v_cvt_pk_bf16_f32 v115, v102, v103\n\t" \
  "v_cvt_pk_bf16_f32 v114, v100, v101\n\t" \
  "v_cvt_pk_bf16_f32 v112, v96, v97\n\t" \
  "v_cvt_pk_bf16_f32 v113, v98, v99\n\t" \
  "v_permlane16_swap_b32 v124, v126\n\t" \
  "v_permlane16_swap_b32 v125, v127\n\t" \
  "v_permlane16_swap_b32 v120, v122\n\t" \
  "v_permlane16_swap_b32 v121, v123\n\t" \
  "v_permlane16_swap_b32 v116, v118\n\t" \
  "v_permlane16_swap_b32 v117, v119\n\t" \
  "v_permlane16_swap_b32 v112, v114\n\t" \
  "v_permlane16_swap_b32 v113, v115\n\t" \
  "s_nop 1\n\t" \
  "v_mfma_f32_16x16x32_bf16 v[96:99], a[0:3], v[112:115], 0\n\t" \
  "ds_read_b64_tr_b16 a[130:131], v228 offset:4096\n\t" \
  "v_add_f32 v66, v66, v184\n\t" \
  "v_mfma_f32_16x16x32_bf16 v[96:99], a[4:7], v[116:119], v[96:99]\n\t" \
  "ds_read_b64_tr_b16 a[132:133], v228 offset:8192\n\t" \
  "v_add_f32 v67, v67, v184\n\t" \
  "v_mfma_f32_16x16x32_bf16 v[96:99], a[8:11], v[120:123], v[96:99]\n\t" \
  "buffer_load_dwordx4 v227, %[sK], s96 offen offset:3072 lds\n\t" \
  "v_mfma_f32_16x16x32_bf16 v[96:99], a[12:15], v[124:127], v[96:99]\n\t" \
  "s_add_u32 m0, %[m], 147456\n\t" \
  "v_exp_f32 v0, v0\n\t" \
  "v_mfma_f32_16x16x32_bf16 v[68:71], a[16:19], v[80:83], v[184:187]\n\t" \
  "s_add_u32 s96, %[sofk], 16384\n\t" \
  "v_exp_f32 v1, v1\n\t" \
  "v_mfma_f32_16x16x32_bf16 v[68:71], a[20:23], v[84:87], v[68:71]\n\t" \
  "v_exp_f32 v2, v2\n\t" \
  "v_mfma_f32_16x16x32_bf16 v[68:71], a[24:27], v[88:91], v[68:71]\n\t" \
  "ds_read_b64_tr_b16 a[134:135], v228 offset:12288\n\t" \
  "v_max3_f32 v194, v96, v97, v98\n\t" \
  "v_mfma_f32_16x16x32_bf16 v[68:71], a[28:31], v[92:95], v[68:71]\n\t" \
  "ds_read_b64_tr_b16 a[136:137], v229 offset:0\n\t" \
  "v_max_f32 v194, v194, v99\n\t" \
  "v_mfma_f32_16x16x32_bf16 v[72:75], a[32:35], v[80:83], v[184:187]\n\t" \
  "ds_read_b64_tr_b16 a[138:139], v229 offset:4096\n\t" \
  "v_mov_b32 v195, v194\n\t" \
  "v_mfma_f32_16x16x32_bf16 v[72:75], a[36:39], v[84:87], v[72:75]\n\t" \
  "v_exp_f32 v3, v3\n\t" \
  "v_mfma_f32_16x16x32_bf16 v[72:75], a[40:43], v[88:91], v[72:75]\n\t" \
  "v_permlane32_swap_b32 v194, v195\n\t" \
  "v_mfma_f32_16x16x32_bf16 v[72:75], a[44:47], v[92:95], v[72:75]\n\t" \
  "ds_read_b64_tr_b16 a[140:141], v229 offset:8192\n\t" \
  "v_max_f32 v194, v194, v195\n\t" \
  "v_mfma_f32_16x16x32_bf16 v[76:79], a[48:51], v[80:83], v[184:187]\n\t" \
  "ds_read_b64_tr_b16 a[142:143], v229 offset:12288\n\t" \
  "v_mov_b32 v195, v194\n\t" \
  "v_mfma_f32_16x16x32_bf16 v[76:79], a[52:55], v[84:87], v[76:79]\n\t" \
  "buffer_load_dwordx4 v230, %[sV], s96 offen offset:0 lds\n\t" \
  "v_mfma_f32_16x16x32_bf16 v[76:79], a[56:59], v[88:91], v[76:79]\n\t" \
  "v_permlane16_swap_b32 v194, v195\n\t" \
  "v_mfma_f32_16x16x32_bf16 v[76:79], a[60:63], v[92:95], v[76:79]\n\t" \
  "ds_read_b64_tr_b16 a[144:145], v228 offset:256\n\t" \
  "v_max_f32 v194, v194, v195\n\t" \
  "v_sub_f32 v188, 0xc2c80000, v194\n\t" \
  "v_sub_f32 v189, 0xc2c80000, v194\n\t" \
  "v_sub_f32 v190, 0xc2c80000, v194\n\t" \
  "v_sub_f32 v191, 0xc2c80000, v194\n\t" \
  "s_nop 1\n\t" \
  "v_mfma_f32_16x16x32_bf16 v[100:103], a[16:19], v[112:115], v[188:191]\n\t" \
  "ds_read_b64_tr_b16 a[146:147], v228 offset:4352\n\t" \
  "v_add_f32 v96, v96, v188\n\t" \
  "v_mfma_f32_16x16x32_bf16 v[100:103], a[20:23], v[116:119], v[100:103]\n\t" \
  "ds_read_b64_tr_b16 a[148:149], v228 offset:8448\n\t" \
  "v_add_f32 v97, v97, v188\n\t" \
  "v_mfma_f32_16x16x32_bf16 v[100:103], a[24:27], v[120:123], v[100:103]\n\t" \
  "v_add_f32 v98, v98, v188\n\t" \
  "v_add_f32 v99, v99, v188\n\t" \
  "v_mfma_f32_16x16x32_bf16 v[100:103], a[28:31], v[124:127], v[100:103]\n\t" \
  "buffer_load_dwordx4 v231, %[sV], s96 offen offset:1024 lds\n\t" \
  "v_mfma_f32_16x16x32_bf16 v[104:107], a[32:35], v[112:115], v[188:191]\n\t" \
  "ds_read_b64_tr_b16 a[150:151], v228 offset:12544\n\t" \
  "v_cvt_pk_bf16_f32 v128, v0, v1\n\t" \
  "v_mfma_f32_16x16x32_bf16 v[104:107], a[36:39], v[116:119], v[104:107]\n\t" \
  "v_exp_f32 v4, v4\n\t" \
  "v_mfma_f32_16x16x32_bf16 v[104:107], a[40:43], v[120:123], v[104:107]\n\t" \
  "v_exp_f32 v5, v5\n\t" \
  "v_mfma_f32_16x16x32_bf16 v[104:107], a[44:47], v[124:127], v[104:107]\n\t" \
  "v_exp_f32 v6, v6\n\t" \
  "v_mfma_f32_16x16x32_bf16 v[108:111], a[48:51], v[112:115], v[188:191]\n\t" \
  "buffer_load_dwordx4 v230, %[sV], s96 offen offset:2048 lds\n\t" \
  "v_mfma_f32_16x16x32_bf16 v[108:111], a[52:55], v[116:119], v[108:111]\n\t" \
  "v_exp_f32 v7, v7\n\t" \
  "v_mfma_f32_16x16x32_bf16 v[108:111], a[56:59], v[120:123], v[108:111]\n\t" \
  "ds_read_b64_tr_b16 a[152:153], v229 offset:256\n\t" \
  "v_cvt_pk_bf16_f32 v129, v2, v3\n\t" \
  "v_mfma_f32_16x16x32_bf16 v[108:111], a[60:63], v[124:127], v[108:111]\n\t" \
  "s_waitcnt lgkmcnt(13)\n\t" \
  "v_exp_f32 v8, v8\n\t" \
  STA("s_memrealtime s[94:95]\n\t") \
  STA("s_memtime s[92:93]\n\t") \
  "v_mfma_f32_16x16x32_bf16 v[0:3], a[192:195], v[16:19], v[176:179]\n\t" \
  "v_exp_f32 v9, v9\n\t" \
  "v_mfma_f32_16x16x32_bf16 v[0:3], a[196:199], v[20:23], v[0:3]\n\t" \
  "ds_read_b64_tr_b16 a[154:155], v229 offset:4352\n\t" \
  "v_cvt_pk_bf16_f32 v130, v4, v5\n\t" \
  "v_mfma_f32_16x16x32_bf16 v[0:3], a[200:203], v[24:27], v[0:3]\n\t" \
  "buffer_load_dwordx4 v231, %[sV], s96 offen offset:3072 lds\n\t" \
  "v_mfma_f32_16x16x32_bf16 v[0:3], a[204:207], v[28:31], v[0:3]\n\t" \
  "ds_read_b64_tr_b16 a[156:157], v229 offset:8448\n\t" \
  "v_cvt_pk_bf16_f32 v131, v6, v7\n\t" \
  "v_mfma_f32_16x16x32_bf16 v[4:7], a[220:223], v[28:31], v[176:179]\n\t" \
  "v_exp_f32 v10, v10\n\t" \
  "v_mfma_f32_16x16x32_bf16 v[4:7], a[216:219], v[24:27], v[4:7]\n\t" \
  "s_add_u32 s98, %[pf4], 43008\n\t" \
  "v_exp_f32 v11, v11\n\t" \
  "v_mfma_f32_16x16x32_bf16 v[4:7], a[212:215], v[20:23], v[4:7]\n\t" \
  "v_exp_f32 v12, v12\n\t" \
  "v_mfma_f32_16x16x32_bf16 v[4:7], a[208:211], v[16:19], v[4:7]\n\t" \
  "v_exp_f32 v13, v13\n\t" \
  "v_cvt_pk_bf16_f32 v132, v8, v9\n\t" \
  "v_cvt_pk_bf16_f32 v133, v10, v11\n\t" \
  "v_mfma_f32_16x16x32_bf16 v[8:11], a[224:227], v[16:19], v[176:179]\n\t" \
  "v_exp_f32 v14, v14\n\t" \
  "v_mfma_f32_16x16x32_bf16 v[8:11], a[228:231], v[20:23], v[8:11]\n\t" \
  "v_exp_f32 v15, v15\n\t" \
  "v_mfma_f32_16x16x32_bf16 v[8:11], a[232:235], v[24:27], v[8:11]\n\t" \
  "buffer_load_dword %[pfd], v245, %[sU], s98 offen\n\t" \
  "v_mfma_f32_16x16x32_bf16 v[8:11], a[236:239], v[28:31], v[8:11]\n\t" \
  "v_exp_f32 v32, v32\n\t" \
  "v_cvt_pk_bf16_f32 v134, v12, v13\n\t" \
  "v_cvt_pk_bf16_f32 v135, v14, v15\n\t" \
  "v_mfma_f32_16x16x32_bf16 v[12:15], a[252:255], v[28:31], v[176:179]\n\t" \
  STA("v_writelane_b32 %[stv], s94, 26\n\t") \
  STA("v_writelane_b32 %[stv], s92, 27\n\t") \
  "s_add_u32 s98, %[pf4], 0\n\t" \
  "v_exp_f32 v33, v33\n\t" \
  "v_mfma_f32_16x16x32_bf16 v[12:15], a[248:251], v[24:27], v[12:15]\n\t" \
  "v_exp_f32 v34, v34\n\t" \
  "v_mfma_f32_16x16x32_bf16 v[12:15], a[244:247], v[20:23], v[12:15]\n\t" \
  "v_exp_f32 v35, v35\n\t" \
  "v_mfma_f32_16x16x32_bf16 v[12:15], a[240:243], v[16:19], v[12:15]\n\t" \
  "buffer_load_dword %[pfd], v245, %[sG], s98 offen\n\t" \
  "v_cvt_pk_bf16_f32 v136, v32, v33\n\t" \
  "v_cvt_pk_bf16_f32 v137, v34, v35\n\t" \
  "v_mfma_f32_16x16x32_bf16 v[32:35], a[192:195], v[48:51], v[180:183]\n\t" \
  "ds_read_b64_tr_b16 a[158:159], v229 offset:12544\n\t" \
  "ds_read_b64_tr_b16 a[160:161], v228 offset:512\n\t" \
  "v_exp_f32 v36, v36\n\t" \
  "v_mfma_f32_16x16x32_bf16 v[32:35], a[196:199], v[52:55], v[32:35]\n\t" \
  "v_exp_f32 v37, v37\n\t" \
  "v_mfma_f32_16x16x32_bf16 v[32:35], a[200:203], v[56:59], v[32:35]\n\t" \
  "v_exp_f32 v38, v38\n\t" \
  "v_mfma_f32_16x16x32_bf16 v[32:35], a[204:207], v[60:63], v[32:35]\n\t" \
  "s_add_u32 s98, %[pf4], 14336\n\t" \
  "v_exp_f32 v39, v39\n\t" \
  "v_cvt_pk_bf16_f32 v138, v36, v37\n\t" \
  "v_cvt_pk_bf16_f32 v139, v38, v39\n\t" \
  "v_lshl_add_u32 v207, v233, 14, %[cto]\n\t" \
  "v_min_u32 v207, .Lfz_z-.Lat_t0-4, v207\n\t" \
  "v_mfma_f32_16x16x32_bf16 v[36:39], a[220:223], v[60:63], v[180:183]\n\t" \
  "v_exp_f32 v40, v40\n\t" \
  "v_mfma_f32_16x16x32_bf16 v[36:39], a[216:219], v[56:59], v[36:39]\n\t" \
  "buffer_load_dword %[pfd], v245, %[sG], s98 offen\n\t" \
  "v_exp_f32 v41, v41\n\t" \
  "s_getpc_b64 s[96:97]\n\t" \
  ".Lat_t0:\n\t" \
  "global_load_dword %[pfd], v207, s[96:97]\n\t" \
  "v_mfma_f32_16x16x32_bf16 v[36:39], a[212:215], v[52:55], v[36:39]\n\t" \
  "ds_read_b64_tr_b16 a[162:163], v228 offset:4608\n\t" \
  "v_cvt_pk_bf16_f32 v140, v40, v41\n\t" \
  "v_exp_f32 v42, v42\n\t" \
  "v_mfma_f32_16x16x32_bf16 v[36:39], a[208:211], v[48:51], v[36:39]\n\t" \
  "ds_read_b64_tr_b16 a[164:165], v228 offset:8704\n\t" \
  "ds_read_b64_tr_b16 a[166:167], v228 offset:12800\n\t" \
  "v_exp_f32 v43, v43\n\t" \
  "s_nop 0\n\t" \
  "v_cvt_pk_bf16_f32 v141, v42, v43\n\t" \
  "v_mfma_f32_16x16x32_bf16 v[40:43], a[224:227], v[48:51], v[180:183]\n\t" \
  "v_exp_f32 v44, v44\n\t" \
  "v_mfma_f32_16x16x32_bf16 v[40:43], a[228:231], v[52:55], v[40:43]\n\t" \
  "v_exp_f32 v45, v45\n\t" \
  "v_mfma_f32_16x16x32_bf16 v[40:43], a[232:235], v[56:59], v[40:43]\n\t" \
  "v_exp_f32 v46, v46\n\t" \
  "v_mfma_f32_16x16x32_bf16 v[40:43], a[236:239], v[60:63], v[40:43]\n\t" \
  "v_exp_f32 v47, v47\n\t" \
  "v_cvt_pk_bf16_f32 v142, v44, v45\n\t" \
  "v_cvt_pk_bf16_f32 v143, v46, v47\n\t" \
  "v_mfma_f32_16x16x32_bf16 v[44:47], a[252:255], v[60:63], v[180:183]\n\t" \
  "v_exp_f32 v64, v64\n\t" \
  "v_mfma_f32_16x16x32_bf16 v[44:47], a[248:251], v[56:59], v[44:47]\n\t" \
  "ds_read_b64_tr_b16 a[168:169], v229 offset:512\n\t" \
  "ds_read_b64_tr_b16 a[170:171], v229 offset:4608\n\t" \
  "v_exp_f32 v65, v65\n\t" \
  "v_mfma_f32_16x16x32_bf16 v[44:47], a[244:247], v[52:55], v[44:47]\n\t" \
  "v_exp_f32 v66, v66\n\t" \
  "v_mfma_f32_16x16x32_bf16 v[44:47], a[240:243], v[48:51], v[44:47]\n\t" \
  "v_exp_f32 v67, v67\n\t" \
  "v_cvt_pk_bf16_f32 v144, v64, v65\n\t" \
  "v_cvt_pk_bf16_f32 v145, v66, v67\n\t" \
  "v_mfma_f32_16x16x32_bf16 v[64:67], a[192:195], v[80:83], v[184:187]\n\t" \
  "v_exp_f32 v68, v68\n\t" \
  "v_mfma_f32_16x16x32_bf16 v[64:67], a[196:199], v[84:87], v[64:67]\n\t" \
  "v_exp_f32 v69, v69\n\t" \
  "v_mfma_f32_16x16x32_bf16 v[64:67], a[200:203], v[88:91], v[64:67]\n\t" \
  "ds_read_b64_tr_b16 a[172:173], v229 offset:8704\n\t" \
  "v_cvt_pk_bf16_f32 v146, v68, v69\n\t" \
  "v_exp_f32 v70, v70\n\t" \
  "v_mfma_f32_16x16x32_bf16 v[64:67], a[204:207], v[92:95], v[64:67]\n\t" \
  "v_exp_f32 v71, v71\n\t" \
  "s_nop 0\n\t" \
  "v_cvt_pk_bf16_f32 v147, v70, v71\n\t" \
  "v_mfma_f32_16x16x32_bf16 v[68:71], a[220:223], v[92:95], v[184:187]\n\t" \
  "v_exp_f32 v72, v72\n\t" \
  "v_mfma_f32_16x16x32_bf16 v[68:71], a[216:219], v[88:91], v[68:71]\n\t" \
  "ds_read_b64_tr_b16 a[174:175], v229 offset:12800\n\t" \
  "ds_read_b64_tr_b16 a[176:177], v228 offset:768\n\t" \
  "v_exp_f32 v73, v73\n\t" \
  "v_mfma_f32_16x16x32_bf16 v[68:71], a[212:215], v[84:87], v[68:71]\n\t" \
  "v_exp_f32 v74, v74\n\t" \
  "v_mfma_f32_16x16x32_bf16 v[68:71], a[208:211], v[80:83], v[68:71]\n\t" \
  "v_exp_f32 v75, v75\n\t" \
  "v_cvt_pk_bf16_f32 v148, v72, v73\n\t" \
  "v_cvt_pk_bf16_f32 v149, v74, v75\n\t" \
  "v_mfma_f32_16x16x32_bf16 v[72:75], a[224:227], v[80:83], v[184:187]\n\t" \
  "v_exp_f32 v76, v76\n\t" \
  "v_mfma_f32_16x16x32_bf16 v[72:75], a[228:231], v[84:87], v[72:75]\n\t" \
  "v_exp_f32 v77, v77\n\t" \
  "v_mfma_f32_16x16x32_bf16 v[72:75], a[232:235], v[88:91], v[72:75]\n\t" \
  "ds_read_b64_tr_b16 a[178:179], v228 offset:4864\n\t" \
  "v_cvt_pk_bf16_f32 v150, v76, v77\n\t" \
  "v_exp_f32 v78, v78\n\t" \
  "v_mfma_f32_16x16x32_bf16 v[72:75], a[236:239], v[92:95], v[72:75]\n\t" \
  "v_exp_f32 v79, v79\n\t" \
  "s_nop 0\n\t" \
  "v_cvt_pk_bf16_f32 v151, v78, v79\n\t" \
  "v_mfma_f32_16x16x32_bf16 v[76:79], a[252:255], v[92:95], v[184:187]\n\t" \
  "v_exp_f32 v96, v96\n\t" \
  "v_mfma_f32_16x16x32_bf16 v[76:79], a[248:251], v[88:91], v[76:79]\n\t" \
  "ds_read_b64_tr_b16 a[180:181], v228 offset:8960\n\t" \
  "ds_read_b64_tr_b16 a[182:183], v228 offset:13056\n\t" \
  "v_exp_f32 v97, v97\n\t" \
  "v_mfma_f32_16x16x32_bf16 v[76:79], a[244:247], v[84:87], v[76:79]\n\t" \
  "v_exp_f32 v98, v98\n\t" \
  "v_mfma_f32_16x16x32_bf16 v[76:79], a[240:243], v[80:83], v[76:79]\n\t" \
  "v_exp_f32 v99, v99\n\t" \
  "v_cvt_pk_bf16_f32 v152, v96, v97\n\t" \
  "v_cvt_pk_bf16_f32 v153, v98, v99\n\t" \
  "v_mfma_f32_16x16x32_bf16 v[96:99], a[192:195], v[112:115], v[188:191]\n\t" \
  "v_exp_f32 v100, v100\n\t" \
  "v_mfma_f32_16x16x32_bf16 v[96:99], a[196:199], v[116:119], v[96:99]\n\t" \
  "v_exp_f32 v101, v101\n\t" \
  "v_mfma_f32_16x16x32_bf16 v[96:99], a[200:203], v[120:123], v[96:99]\n\t" \
  "ds_read_b64_tr_b16 a[184:185], v229 offset:768\n\t" \
  "v_cvt_pk_bf16_f32 v154, v100, v101\n\t" \
  "v_exp_f32 v102, v102\n\t" \
  "v_mfma_f32_16x16x32_bf16 v[96:99], a[204:207], v[124:127], v[96:99]\n\t" \
  "v_exp_f32 v103, v103\n\t" \
  "s_nop 0\n\t" \
  "v_cvt_pk_bf16_f32 v155, v102, v103\n\t" \
  "v_mfma_f32_16x16x32_bf16 v[100:103], a[220:223], v[124:127], v[188:191]\n\t" \
  "v_exp_f32 v104, v104\n\t" \
  "v_mfma_f32_16x16x32_bf16 v[100:103], a[216:219], v[120:123], v[100:103]\n\t" \
  "ds_read_b64_tr_b16 a[186:187], v229 offset:4864\n\t" \
  "ds_read_b64_tr_b16 a[188:189], v229 offset:8960\n\t" \
  "v_exp_f32 v105, v105\n\t" \
  "v_mfma_f32_16x16x32_bf16 v[100:103], a[212:215], v[116:119], v[100:103]\n\t" \
  "v_exp_f32 v106, v106\n\t" \
  "v_mfma_f32_16x16x32_bf16 v[100:103], a[208:211], v[112:115], v[100:103]\n\t" \
  "v_exp_f32 v107, v107\n\t" \
  "v_cvt_pk_bf16_f32 v156, v104, v105\n\t" \
  "v_cvt_pk_bf16_f32 v157, v106, v107\n\t" \
  "v_mfma_f32_16x16x32_bf16 v[104:107], a[224:227], v[112:115], v[188:191]\n\t" \
  "v_exp_f32 v108, v108\n\t" \
  "v_mfma_f32_16x16x32_bf16 v[104:107], a[228:231], v[116:119], v[104:107]\n\t" \
  "v_exp_f32 v109, v109\n\t" \
  "v_mfma_f32_16x16x32_bf16 v[104:107], a[232:235], v[120:123], v[104:107]\n\t" \
  "ds_read_b64_tr_b16 a[190:191], v229 offset:13056\n\t" \
  "v_cvt_pk_bf16_f32 v158, v108, v109\n\t" \
  "v_exp_f32 v110, v110\n\t" \
  "v_mfma_f32_16x16x32_bf16 v[104:107], a[236:239], v[124:127], v[104:107]\n\t" \
  "v_exp_f32 v111, v111\n\t" \
  "s_nop 0\n\t" \
  "v_cvt_pk_bf16_f32 v159, v110, v111\n\t" \
  "v_mfma_f32_16x16x32_bf16 v[108:111], a[252:255], v[124:127], v[188:191]\n\t" \
  "v_exp_f32 v0, v0\n\t" \
  "v_mfma_f32_16x16x32_bf16 v[108:111], a[248:251], v[120:123], v[108:111]\n\t" \
  "v_exp_f32 v1, v1\n\t" \
  "v_mfma_f32_16x16x32_bf16 v[108:111], a[244:247], v[116:119], v[108:111]\n\t" \
  "v_exp_f32 v2, v2\n\t" \
  "v_mfma_f32_16x16x32_bf16 v[108:111], a[240:243], v[112:115], v[108:111]\n\t" \
  "s_waitcnt vmcnt(4) lgkmcnt(0)\n\t" \
  "s_barrier\n\t" \
  STA("s_memrealtime s[94:95]\n\t") \
  STA("s_memtime s[92:93]\n\t") \
  "v_mfma_f32_16x16x32_bf16 a[0:3], a[128:131], v[128:131], 0\n\t" \
  "v_exp_f32 v3, v3\n\t" \
  "v_mfma_f32_16x16x32_bf16 a[0:3], a[132:135], v[132:135], a[0:3]\n\t" \
  "v_exp_f32 v4, v4\n\t" \
  "v_mfma_f32_16x16x32_bf16 a[16:19], a[136:139], v[128:131], 0\n\t" \
  "ds_read_b128 a[192:195], v220 offset:0\n\t" \
  "ds_read_b128 a[196:199], v221 offset:0\n\t" \
  "v_mfma_f32_16x16x32_bf16 a[16:19], a[140:143], v[132:135], a[16:19]\n\t" \
  "v_exp_f32 v5, v5\n\t" \
  "v_mfma_f32_16x16x32_bf16 a[32:35], a[144:147], v[128:131], 0\n\t" \
  "v_exp_f32 v6, v6\n\t" \
  "v_mfma_f32_16x16x32_bf16 a[32:35], a[148:151], v[132:135], a[32:35]\n\t" \
  "v_exp_f32 v7, v7\n\t" \
  "v_mfma_f32_16x16x32_bf16 a[48:51], a[152:155], v[128:131], 0\n\t" \
  "v_exp_f32 v8, v8\n\t" \
  "v_mfma_f32_16x16x32_bf16 a[48:51], a[156:159], v[132:135], a[48:51]\n\t" \
  "v_exp_f32 v9, v9\n\t" \
  "v_mfma_f32_16x16x32_bf16 a[64:67], a[160:163], v[128:131], 0\n\t" \
  "v_exp_f32 v10, v10\n\t" \
  "v_mfma_f32_16x16x32_bf16 a[64:67], a[164:167], v[132:135], a[64:67]\n\t" \
  "v_exp_f32 v11, v11\n\t" \
  "v_mfma_f32_16x16x32_bf16 a[80:83], a[168:171], v[128:131], 0\n\t" \
  "ds_read_b128 a[200:203], v222 offset:0\n\t" \
  "ds_read_b128 a[204:207], v223 offset:0\n\t" \
  "v_mfma_f32_16x16x32_bf16 a[80:83], a[172:175], v[132:135], a[80:83]\n\t" \
  "v_exp_f32 v12, v12\n\t" \
  "v_mfma_f32_16x16x32_bf16 a[96:99], a[176:179], v[128:131], 0\n\t" \
  STA("v_writelane_b32 %[stv], s94, 28\n\t") \
  STA("v_writelane_b32 %[stv], s92, 29\n\t") \
  "v_exp_f32 v13, v13\n\t" \
  "v_mfma_f32_16x16x32_bf16 a[96:99], a[180:183], v[132:135], a[96:99]\n\t" \
  "v_exp_f32 v14, v14\n\t" \
  "v_mfma_f32_16x16x32_bf16 a[112:115], a[184:187], v[128:131], 0\n\t" \
  "v_exp_f32 v15, v15\n\t" \
  "v_mfma_f32_16x16x32_bf16 a[112:115], a[188:191], v[132:135], a[112:115]\n\t" \
  "v_exp_f32 v32, v32\n\t" \
  "v_mfma_f32_16x16x32_bf16 v[160:163], v[216:219], v[128:131], 0\n\t" \
  "v_exp_f32 v33, v33\n\t" \
  "v_mfma_f32_16x16x32_bf16 v[160:163], v[216:219], v[132:135], v[160:163]\n\t" \
  "ds_read_b128 a[208:211], v220 offset:4096\n\t" \
  "v_cvt_pk_bf16_f32 v128, v0, v1\n\t" \
  "v_mfma_f32_16x16x32_bf16 a[4:7], a[128:131], v[136:139], 0\n\t" \
  "v_exp_f32 v34, v34\n\t" \
  "v_mfma_f32_16x16x32_bf16 a[4:7], a[132:135], v[140:143], a[4:7]\n\t" \
  "ds_read_b128 a[212:215], v221 offset:4096\n\t" \
  "v_cvt_pk_bf16_f32 v129, v2, v3\n\t" \
  "v_mfma_f32_16x16x32_bf16 a[20:23], a[136:139], v[136:139], 0\n\t" \
  "s_add_u32 m0, %[m], 65536\n\t" \
  "v_exp_f32 v35, v35\n\t" \
  "v_mfma_f32_16x16x32_bf16 a[20:23], a[140:143], v[140:143], a[20:23]\n\t" \
  "s_add_u32 s96, %[sofk], 49152\n\t" \
  "v_exp_f32 v36, v36\n\t" \
  "v_mfma_f32_16x16x32_bf16 a[36:39], a[144:147], v[136:139], 0\n\t" \
  "v_exp_f32 v37, v37\n\t" \
  "v_mfma_f32_16x16x32_bf16 a[36:39], a[148:151], v[140:143], a[36:39]\n\t" \
  "ds_read_b128 a[216:219], v222 offset:4096\n\t" \
  "v_cvt_pk_bf16_f32 v130, v4, v5\n\t" \
  "v_mfma_f32_16x16x32_bf16 a[52:55], a[152:155], v[136:139], 0\n\t" \
  "v_exp_f32 v38, v38\n\t" \
  "v_mfma_f32_16x16x32_bf16 a[52:55], a[156:159], v[140:143], a[52:55]\n\t" \
  "v_exp_f32 v39, v39\n\t" \
  "v_mfma_f32_16x16x32_bf16 a[68:71], a[160:163], v[136:139], 0\n\t" \
  "v_exp_f32 v40, v40\n\t" \
  "v_mfma_f32_16x16x32_bf16 a[68:71], a[164:167], v[140:143], a[68:71]\n\t" \
  "buffer_load_dwordx4 v224, %[sK], s96 offen offset:0 lds\n\t" \
  "v_mfma_f32_16x16x32_bf16 a[84:87], a[168:171], v[136:139], 0\n\t" \
  "ds_read_b128 a[220:223], v223 offset:4096\n\t" \
  "v_cvt_pk_bf16_f32 v131, v6, v7\n\t" \
  "v_mfma_f32_16x16x32_bf16 a[84:87], a[172:175], v[140:143], a[84:87]\n\t" \
  "v_exp_f32 v41, v41\n\t" \
  "v_mfma_f32_16x16x32_bf16 a[100:103], a[176:179], v[136:139], 0\n\t" \
  "ds_read_b128 a[224:227], v220 offset:8192\n\t" \
  "v_cvt_pk_bf16_f32 v132, v8, v9\n\t" \
  "v_mfma_f32_16x16x32_bf16 a[100:103], a[180:183], v[140:143], a[100:103]\n\t" \
  "buffer_load_dwordx4 v225, %[sK], s96 offen offset:1024 lds\n\t" \
  "v_mfma_f32_16x16x32_bf16 a[116:119], a[184:187], v[136:139], 0\n\t" \
  "v_exp_f32 v42, v42\n\t" \
  "v_mfma_f32_16x16x32_bf16 a[116:119], a[188:191], v[140:143], a[116:119]\n\t" \
  "v_exp_f32 v43, v43\n\t" \
  "v_mfma_f32_16x16x32_bf16 v[164:167], v[216:219], v[136:139], 0\n\t" \
  "buffer_load_dwordx4 v226, %[sK], s96 offen offset:2048 lds\n\t" \
  "v_mfma_f32_16x16x32_bf16 v[164:167], v[216:219], v[140:143], v[164:167]\n\t" \
  "ds_read_b128 a[228:231], v221 offset:8192\n\t" \
  "v_cvt_pk_bf16_f32 v133, v10, v11\n\t" \
  "v_mfma_f32_16x16x32_bf16 a[8:11], a[128:131], v[144:147], 0\n\t" \
  "v_exp_f32 v44, v44\n\t" \
  "v_mfma_f32_16x16x32_bf16 a[8:11], a[132:135], v[148:151], a[8:11]\n\t" \
  "v_exp_f32 v45, v45\n\t" \
  "v_mfma_f32_16x16x32_bf16 a[24:27], a[136:139], v[144:147], 0\n\t" \
  "buffer_load_dwordx4 v227, %[sK], s96 offen offset:3072 lds\n\t" \
  "v_mfma_f32_16x16x32_bf16 a[24:27], a[140:143], v[148:151], a[24:27]\n\t" \
  "ds_read_b128 a[232:235], v222 offset:8192\n\t" \
  "v_cvt_pk_bf16_f32 v134, v12, v13\n\t" \
  "v_mfma_f32_16x16x32_bf16 a[40:43], a[144:147], v[144:147], 0\n\t" \
  "v_exp_f32 v46, v46\n\t" \
  "v_mfma_f32_16x16x32_bf16 a[40:43], a[148:151], v[148:151], a[40:43]\n\t" \
  "s_add_u32 m0, %[m], 114688\n\t" \
  "s_mov_b32 s96, 0\n\t" \
  "ds_read_b128 a[236:239], v223 offset:8192\n\t" \
  "v_mfma_f32_16x16x32_bf16 a[56:59], a[152:155], v[144:147], 0\n\t" \
  "buffer_load_dwordx4 v232, %[sU], s96 offen offset:0 lds\n\t" \
  "v_mfma_f32_16x16x32_bf16 a[56:59], a[156:159], v[148:151], a[56:59]\n\t" \
  "s_add_u32 m0, %[m], 81920\n\t" \
  "v_exp_f32 v47, v47\n\t" \
  "v_mfma_f32_16x16x32_bf16 a[72:75], a[160:163], v[144:147], 0\n\t" \
  "s_add_u32 s96, %[sofk], 32768\n\t" \
  "v_exp_f32 v64, v64\n\t" \
  "v_mfma_f32_16x16x32_bf16 a[72:75], a[164:167], v[148:151], a[72:75]\n\t" \
  "ds_read_b128 a[240:243], v220 offset:12288\n\t" \
  "v_cvt_pk_bf16_f32 v135, v14, v15\n\t" \
  "v_mfma_f32_16x16x32_bf16 a[88:91], a[168:171], v[144:147], 0\n\t" \
  "v_exp_f32 v65, v65\n\t" \
  "v_mfma_f32_16x16x32_bf16 a[88:91], a[172:175], v[148:151], a[88:91]\n\t" \
  "v_exp_f32 v66, v66\n\t" \
  "v_mfma_f32_16x16x32_bf16 a[104:107], a[176:179], v[144:147], 0\n\t" \
  "v_exp_f32 v67, v67\n\t" \
  "v_mfma_f32_16x16x32_bf16 a[104:107], a[180:183], v[148:151], a[104:107]\n\t" \
  "ds_read_b128 a[244:247], v221 offset:12288\n\t" \
  "v_cvt_pk_bf16_f32 v136, v32, v33\n\t" \
  "v_mfma_f32_16x16x32_bf16 a[120:123], a[184:187], v[144:147], 0\n\t" \
  "v_exp_f32 v68, v68\n\t" \
  "v_mfma_f32_16x16x32_bf16 a[120:123], a[188:191], v[148:151], a[120:123]\n\t" \
  "buffer_load_dwordx4 v230, %[sV], s96 offen offset:0 lds\n\t" \
  "v_mfma_f32_16x16x32_bf16 v[168:171], v[216:219], v[144:147], 0\n\t" \
  "v_exp_f32 v69, v69\n\t" \
  "v_mfma_f32_16x16x32_bf16 v[168:171], v[216:219], v[148:151], v[168:171]\n\t" \
  "ds_read_b128 a[248:251], v222 offset:12288\n\t" \
  "v_cvt_pk_bf16_f32 v137, v34, v35\n\t" \
  "v_mfma_f32_16x16x32_bf16 a[12:15], a[128:131], v[152:155], 0\n\t" \
  "v_exp_f32 v70, v70\n\t" \
  "v_mfma_f32_16x16x32_bf16 a[12:15], a[132:135], v[156:159], a[12:15]\n\t" \
  "buffer_load_dwordx4 v231, %[sV], s96 offen offset:1024 lds\n\t" \
  "v_mfma_f32_16x16x32_bf16 a[28:31], a[136:139], v[152:155], 0\n\t" \
  "v_exp_f32 v71, v71\n\t" \
  "v_mfma_f32_16x16x32_bf16 a[28:31], a[140:143], v[156:159], a[28:31]\n\t" \
  "ds_read_b128 a[252:255], v223 offset:12288\n\t" \
  "v_cvt_pk_bf16_f32 v138, v36, v37\n\t" \
  "v_mfma_f32_16x16x32_bf16 a[44:47], a[144:147], v[152:155], 0\n\t" \
  "buffer_load_dwordx4 v230, %[sV], s96 offen offset:2048 lds\n\t" \
  "v_mfma_f32_16x16x32_bf16 a[44:47], a[148:151], v[156:159], a[44:47]\n\t" \
  "v_exp_f32 v72, v72\n\t" \
  "v_mfma_f32_16x16x32_bf16 a[60:63], a[152:155], v[152:155], 0\n\t" \
  "v_exp_f32 v73, v73\n\t" \
  "v_mfma_f32_16x16x32_bf16 a[60:63], a[156:159], v[156:159], a[60:63]\n\t" \
  "v_exp_f32 v74, v74\n\t" \
  "v_mfma_f32_16x16x32_bf16 a[76:79], a[160:163], v[152:155], 0\n\t" \
  "buffer_load_dwordx4 v231, %[sV], s96 offen offset:3072 lds\n\t" \
  "v_mfma_f32_16x16x32_bf16 a[76:79], a[164:167], v[156:159], a[76:79]\n\t" \
  "v_exp_f32 v75, v75\n\t" \
  "v_mfma_f32_16x16x32_bf16 a[92:95], a[168:171], v[152:155], 0\n\t" \
  "v_exp_f32 v76, v76\n\t" \
  "v_mfma_f32_16x16x32_bf16 a[92:95], a[172:175], v[156:159], a[92:95]\n\t" \
  "v_cvt_pk_bf16_f32 v139, v38, v39\n\t" \
  "v_cvt_pk_bf16_f32 v140, v40, v41\n\t" \
  "v_mfma_f32_16x16x32_bf16 a[108:111], a[176:179], v[152:155], 0\n\t" \
  "s_add_u32 m0, %[m], 114688\n\t" \
  "s_mov_b32 s96, 6144\n\t" \
  "buffer_load_dwordx4 v232, %[sU], s96 offen offset:1024 lds\n\t" \
  "v_mfma_f32_16x16x32_bf16 a[108:111], a[180:183], v[156:159], a[108:111]\n\t" \
  "v_exp_f32 v77, v77\n\t" \
  "v_mfma_f32_16x16x32_bf16 a[124:127], a[184:187], v[152:155], 0\n\t" \
  "v_exp_f32 v78, v78\n\t" \
  "v_mfma_f32_16x16x32_bf16 a[124:127], a[188:191], v[156:159], a[124:127]\n\t" \
  "v_exp_f32 v79, v79\n\t" \
  "v_mfma_f32_16x16x32_bf16 v[172:175], v[216:219], v[152:155], 0\n\t" \
  "v_exp_f32 v96, v96\n\t" \
  "v_mfma_f32_16x16x32_bf16 v[172:175], v[216:219], v[156:159], v[172:175]\n\t" \
  "s_waitcnt lgkmcnt(0)\n\t" \
  "v_exp_f32 v97, v97\n\t" \
  STA("s_memrealtime s[94:95]\n\t") \
  STA("s_memtime s[92:93]\n\t") \
  "v_mfma_f32_16x16x32_bf16 v[0:3], a[192:195], v[16:19], v[176:179]\n\t" \
  ".Lhs6:\n\ts_cbranch_execz .Lhs6n\n\t" \
  "ds_read_b64_tr_b16 a[128:129], v237 offset:0\n\t" \
  "v_cvt_pk_bf16_f32 v141, v42, v43\n\t" \
  "v_mfma_f32_16x16x32_bf16 v[0:3], a[196:199], v[20:23], v[0:3]\n\t" \
  "v_exp_f32 v98, v98\n\t" \
  "v_mfma_f32_16x16x32_bf16 v[0:3], a[200:203], v[24:27], v[0:3]\n\t" \
  "ds_read_b64_tr_b16 a[130:131], v237 offset:4096\n\t" \
  "v_cvt_pk_bf16_f32 v142, v44, v45\n\t" \
  "v_mfma_f32_16x16x32_bf16 v[0:3], a[204:207], v[28:31], v[0:3]\n\t" \
  "v_exp_f32 v99, v99\n\t" \
  "v_mfma_f32_16x16x32_bf16 v[4:7], a[220:223], v[28:31], v[176:179]\n\t" \
  "ds_read_b64_tr_b16 a[132:133], v237 offset:8192\n\t" \
  "v_cvt_pk_bf16_f32 v143, v46, v47\n\t" \
  "v_mfma_f32_16x16x32_bf16 v[4:7], a[216:219], v[24:27], v[4:7]\n\t" \
  "v_exp_f32 v100, v100\n\t" \
  "v_mfma_f32_16x16x32_bf16 v[4:7], a[212:215], v[20:23], v[4:7]\n\t" \
  "ds_read_b64_tr_b16 a[134:135], v237 offset:12288\n\t" \
  "v_cvt_pk_bf16_f32 v144, v64, v65\n\t" \
  "v_mfma_f32_16x16x32_bf16 v[4:7], a[208:211], v[16:19], v[4:7]\n\t" \
  "ds_read_b64_tr_b16 a[136:137], v238 offset:0\n\t" \
  "v_cvt_pk_bf16_f32 v145, v66, v67\n\t" \
  "v_mfma_f32_16x16x32_bf16 v[8:11], a[224:227], v[16:19], v[176:179]\n\t" \
  "s_add_u32 m0, %[m], 114688\n\t" \
  "s_mov_b32 s96, 12288\n\t" \
  "ds_read_b64_tr_b16 a[138:139], v238 offset:4096\n\t" \
  "v_mfma_f32_16x16x32_bf16 v[8:11], a[228:231], v[20:23], v[8:11]\n\t" \
  "buffer_load_dwordx4 v232, %[sU], s96 offen offset:2048 lds\n\t" \
  "v_mfma_f32_16x16x32_bf16 v[8:11], a[232:235], v[24:27], v[8:11]\n\t" \
  "s_mov_b32 s96, 18432\n\t" \
  "v_exp_f32 v101, v101\n\t" \
  "v_mfma_f32_16x16x32_bf16 v[8:11], a[236:239], v[28:31], v[8:11]\n\t" \
  "ds_read_b64_tr_b16 a[140:141], v238 offset:8192\n\t" \
  "v_cvt_pk_bf16_f32 v146, v68, v69\n\t" \
  "v_mfma_f32_16x16x32_bf16 v[12:15], a[252:255], v[28:31], v[176:179]\n\t" \
  STA("v_writelane_b32 %[stv], s94, 30\n\t") \
  STA("v_writelane_b32 %[stv], s92, 31\n\t") \
  "v_exp_f32 v102, v102\n\t" \
  "v_mfma_f32_16x16x32_bf16 v[12:15], a[248:251], v[24:27], v[12:15]\n\t" \
  "ds_read_b64_tr_b16 a[142:143], v238 offset:12288\n\t" \
  "v_cvt_pk_bf16_f32 v147, v70, v71\n\t" \
  "v_mfma_f32_16x16x32_bf16 v[12:15], a[244:247], v[20:23], v[12:15]\n\t" \
  "ds_read_b64_tr_b16 a[144:145], v237 offset:256\n\t" \
  "v_cvt_pk_bf16_f32 v148, v72, v73\n\t" \
  "v_mfma_f32_16x16x32_bf16 v[12:15], a[240:243], v[16:19], v[12:15]\n\t" \
  "v_exp_f32 v103, v103\n\t" \
  "v_mfma_f32_16x16x32_bf16 v[32:35], a[192:195], v[48:51], v[180:183]\n\t" \
  "ds_read_b64_tr_b16 a[146:147], v237 offset:4352\n\t" \
  "v_cvt_pk_bf16_f32 v149, v74, v75\n\t" \
  "v_mfma_f32_16x16x32_bf16 v[32:35], a[196:199], v[52:55], v[32:35]\n\t" \
  "v_exp_f32 v104, v104\n\t" \
  "v_mfma_f32_16x16x32_bf16 v[32:35], a[200:203], v[56:59], v[32:35]\n\t" \
  "ds_read_b64_tr_b16 a[148:149], v237 offset:8448\n\t" \
  "v_cvt_pk_bf16_f32 v150, v76, v77\n\t" \
  "v_mfma_f32_16x16x32_bf16 v[32:35], a[204:207], v[60:63], v[32:35]\n\t" \
  "v_exp_f32 v105, v105\n\t" \
  "v_mfma_f32_16x16x32_bf16 v[36:39], a[220:223], v[60:63], v[180:183]\n\t" \
  "ds_read_b64_tr_b16 a[150:151], v237 offset:12544\n\t" \
  "v_cvt_pk_bf16_f32 v151, v78, v79\n\t" \
  "v_mfma_f32_16x16x32_bf16 v[36:39], a[216:219], v[56:59], v[36:39]\n\t" \
  "buffer_load_dwordx4 v232, %[sU], s96 offen offset:3072 lds\n\t" \
  "v_mfma_f32_16x16x32_bf16 v[36:39], a[212:215], v[52:55], v[36:39]\n\t" \
  "ds_read_b64_tr_b16 a[152:153], v238 offset:256\n\t" \
  "v_cvt_pk_bf16_f32 v152, v96, v97\n\t" \
  "v_mfma_f32_16x16x32_bf16 v[36:39], a[208:211], v[48:51], v[36:39]\n\t" \
  "ds_read_b64_tr_b16 a[154:155], v238 offset:4352\n\t" \
  "v_cvt_pk_bf16_f32 v153, v98, v99\n\t" \
  "v_mfma_f32_16x16x32_bf16 v[40:43], a[224:227], v[48:51], v[180:183]\n\t" \
  "v_exp_f32 v106, v106\n\t" \
  "v_mfma_f32_16x16x32_bf16 v[40:43], a[228:231], v[52:55], v[40:43]\n\t" \
  "ds_read_b64_tr_b16 a[156:157], v238 offset:8448\n\t" \
  "v_cvt_pk_bf16_f32 v154, v100, v101\n\t" \
  "v_mfma_f32_16x16x32_bf16 v[40:43], a[232:235], v[56:59], v[40:43]\n\t" \
  "v_exp_f32 v107, v107\n\t" \
  "v_mfma_f32_16x16x32_bf16 v[40:43], a[236:239], v[60:63], v[40:43]\n\t" \
  "ds_read_b64_tr_b16 a[158:159], v238 offset:12544\n\t" \
  "v_cvt_pk_bf16_f32 v155, v102, v103\n\t" \
  "v_mfma_f32_16x16x32_bf16 v[44:47], a[252:255], v[60:63], v[180:183]\n\t" \
  "ds_read_b64_tr_b16 a[160:161], v237 offset:512\n\t" \
  "v_cvt_pk_bf16_f32 v156, v104, v105\n\t" \
  "v_mfma_f32_16x16x32_bf16 v[44:47], a[248:251], v[56:59], v[44:47]\n\t" \
  "v_exp_f32 v108, v108\n\t" \
  "v_mfma_f32_16x16x32_bf16 v[44:47], a[244:247], v[52:55], v[44:47]\n\t" \
  "ds_read_b64_tr_b16 a[162:163], v237 offset:4608\n\t" \
  "v_cvt_pk_bf16_f32 v157, v106, v107\n\t" \
  "v_mfma_f32_16x16x32_bf16 v[44:47], a[240:243], v[48:51], v[44:47]\n\t" \
  "v_exp_f32 v109, v109\n\t" \
  "v_mfma_f32_16x16x32_bf16 v[64:67], a[192:195], v[80:83], v[184:187]\n\t" \
  "ds_read_b64_tr_b16 a[164:165], v237 offset:8704\n\t" \
  "v_cvt_pk_bf16_f32 v158, v108, v109\n\t" \
  "v_mfma_f32_16x16x32_bf16 v[64:67], a[196:199], v[84:87], v[64:67]\n\t" \
  "v_exp_f32 v110, v110\n\t" \
  "v_mfma_f32_16x16x32_bf16 v[64:67], a[200:203], v[88:91], v[64:67]\n\t" \
  "ds_read_b64_tr_b16 a[166:167], v237 offset:12800\n\t" \
  "ds_read_b64_tr_b16 a[168:169], v238 offset:512\n\t" \
  "v_mfma_f32_16x16x32_bf16 v[64:67], a[204:207], v[92:95], v[64:67]\n\t" \
  "s_add_u32 m0, %[m], 131072\n\t" \
  "s_mov_b32 s96, 28672\n\t" \
  "buffer_load_dwordx4 v232, %[sU], s96 offen offset:0 lds\n\t" \
  "v_mfma_f32_16x16x32_bf16 v[68:71], a[220:223], v[92:95], v[184:187]\n\t" \
  "s_mov_b32 s96, 34816\n\t" \
  "v_exp_f32 v111, v111\n\t" \
  "v_mfma_f32_16x16x32_bf16 v[68:71], a[216:219], v[88:91], v[68:71]\n\t" \
  "ds_read_b64_tr_b16 a[170:171], v238 offset:4608\n\t" \
  "v_cvt_pk_bf16_f32 v159, v110, v111\n\t" \
  "v_mfma_f32_16x16x32_bf16 v[68:71], a[212:215], v[84:87], v[68:71]\n\t" \
  "v_exp_f32 v0, v0\n\t" \
  "v_mfma_f32_16x16x32_bf16 v[68:71], a[208:211], v[80:83], v[68:71]\n\t" \
  "v_exp_f32 v1, v1\n\t" \
  "v_mfma_f32_16x16x32_bf16 v[72:75], a[224:227], v[80:83], v[184:187]\n\t" \
  "v_exp_f32 v2, v2\n\t" \
  "v_mfma_f32_16x16x32_bf16 v[72:75], a[228:231], v[84:87], v[72:75]\n\t" \
  "ds_read_b64_tr_b16 a[172:173], v238 offset:8704\n\t" \
  "ds_read_b64_tr_b16 a[174:175], v238 offset:12800\n\t" \
  "v_mfma_f32_16x16x32_bf16 v[72:75], a[232:235], v[88:91], v[72:75]\n\t" \
  "v_exp_f32 v3, v3\n\t" \
  "v_mfma_f32_16x16x32_bf16 v[72:75], a[236:239], v[92:95], v[72:75]\n\t" \
  "v_exp_f32 v4, v4\n\t" \
  "v_mfma_f32_16x16x32_bf16 v[76:79], a[252:255], v[92:95], v[184:187]\n\t" \
  "ds_read_b64_tr_b16 a[176:177], v237 offset:768\n\t" \
  "ds_read_b64_tr_b16 a[178:179], v237 offset:4864\n\t" \
  "v_mfma_f32_16x16x32_bf16 v[76:79], a[248:251], v[88:91], v[76:79]\n\t" \
  "v_exp_f32 v5, v5\n\t" \
  "v_mfma_f32_16x16x32_bf16 v[76:79], a[244:247], v[84:87], v[76:79]\n\t" \
  "v_exp_f32 v6, v6\n\t" \
  "v_mfma_f32_16x16x32_bf16 v[76:79], a[240:243], v[80:83], v[76:79]\n\t" \
  "buffer_load_dwordx4 v232, %[sU], s96 offen offset:1024 lds\n\t" \
  "v_mfma_f32_16x16x32_bf16 v[96:99], a[192:195], v[112:115], v[188:191]\n\t" \
  "ds_read_b64_tr_b16 a[180:181], v237 offset:8960\n\t" \
  "ds_read_b64_tr_b16 a[182:183], v237 offset:13056\n\t" \
  "v_mfma_f32_16x16x32_bf16 v[96:99], a[196:199], v[116:119], v[96:99]\n\t" \
  "v_exp_f32 v7, v7\n\t" \
  "v_mfma_f32_16x16x32_bf16 v[96:99], a[200:203], v[120:123], v[96:99]\n\t" \
  "v_exp_f32 v8, v8\n\t" \
  "v_mfma_f32_16x16x32_bf16 v[96:99], a[204:207], v[124:127], v[96:99]\n\t" \
  "ds_read_b64_tr_b16 a[184:185], v238 offset:768\n\t" \
  "ds_read_b64_tr_b16 a[186:187], v238 offset:4864\n\t" \
  "v_mfma_f32_16x16x32_bf16 v[100:103], a[220:223], v[124:127], v[188:191]\n\t" \
  "v_exp_f32 v9, v9\n\t" \
  "v_mfma_f32_16x16x32_bf16 v[100:103], a[216:219], v[120:123], v[100:103]\n\t" \
  "v_exp_f32 v10, v10\n\t" \
  "v_mfma_f32_16x16x32_bf16 v[100:103], a[212:215], v[116:119], v[100:103]\n\t" \
  "v_exp_f32 v11, v11\n\t" \
  "v_mfma_f32_16x16x32_bf16 v[100:103], a[208:211], v[112:115], v[100:103]\n\t" \
  "ds_read_b64_tr_b16 a[188:189], v238 offset:8960\n\t" \
  "ds_read_b64_tr_b16 a[190:191], v238 offset:13056\n\t" \
  "v_mfma_f32_16x16x32_bf16 v[104:107], a[224:227], v[112:115], v[188:191]\n\t" \
  "v_exp_f32 v12, v12\n\t" \
  "v_mfma_f32_16x16x32_bf16 v[104:107], a[228:231], v[116:119], v[104:107]\n\t" \
  "v_exp_f32 v13, v13\n\t" \
  "v_mfma_f32_16x16x32_bf16 v[104:107], a[232:235], v[120:123], v[104:107]\n\t" \
  "v_exp_f32 v14, v14\n\t" \
  "v_mfma_f32_16x16x32_bf16 v[104:107], a[236:239], v[124:127], v[104:107]\n\t" \
  "v_exp_f32 v15, v15\n\t" \
  "v_mfma_f32_16x16x32_bf16 v[108:111], a[252:255], v[124:127], v[188:191]\n\t" \
  "v_exp_f32 v32, v32\n\t" \
  "v_mfma_f32_16x16x32_bf16 v[108:111], a[248:251], v[120:123], v[108:111]\n\t" \
  "v_exp_f32 v33, v33\n\t" \
  "v_mfma_f32_16x16x32_bf16 v[108:111], a[244:247], v[116:119], v[108:111]\n\t" \
  "v_exp_f32 v34, v34\n\t" \
  "v_mfma_f32_16x16x32_bf16 v[108:111], a[240:243], v[112:115], v[108:111]\n\t" \
  "s_waitcnt vmcnt(5) lgkmcnt(0)\n\t" \
  "s_barrier\n\t" \
  STA("s_memrealtime s[94:95]\n\t") \
  STA("s_memtime s[92:93]\n\t") \
  "v_mfma_f32_16x16x32_bf16 a[0:3], a[128:131], v[128:131], a[0:3]\n\t" \
  "v_exp_f32 v35, v35\n\t" \
  "v_mfma_f32_16x16x32_bf16 a[0:3], a[132:135], v[132:135], a[0:3]\n\t" \
  "v_exp_f32 v36, v36\n\t" \
  "v_mfma_f32_16x16x32_bf16 a[16:19], a[136:139], v[128:131], a[16:19]\n\t" \
  "ds_read_b128 a[192:195], v220 offset:16384\n\t" \
  "ds_read_b128 a[196:199], v221 offset:16384\n\t" \
  "v_mfma_f32_16x16x32_bf16 a[16:19], a[140:143], v[132:135], a[16:19]\n\t" \
  "v_exp_f32 v37, v37\n\t" \
  "v_mfma_f32_16x16x32_bf16 a[32:35], a[144:147], v[128:131], a[32:35]\n\t" \
  "v_exp_f32 v38, v38\n\t" \
  "v_mfma_f32_16x16x32_bf16 a[32:35], a[148:151], v[132:135], a[32:35]\n\t" \
  "v_exp_f32 v39, v39\n\t" \
  "v_mfma_f32_16x16x32_bf16 a[48:51], a[152:155], v[128:131], a[48:51]\n\t" \
  "v_exp_f32 v40, v40\n\t" \
  "v_mfma_f32_16x16x32_bf16 a[48:51], a[156:159], v[132:135], a[48:51]\n\t" \
  "v_exp_f32 v41, v41\n\t" \
  "v_mfma_f32_16x16x32_bf16 a[64:67], a[160:163], v[128:131], a[64:67]\n\t" \
  "v_exp_f32 v42, v42\n\t" \
  "v_mfma_f32_16x16x32_bf16 a[64:67], a[164:167], v[132:135], a[64:67]\n\t" \
  "v_exp_f32 v43, v43\n\t" \
  "v_mfma_f32_16x16x32_bf16 a[80:83], a[168:171], v[128:131], a[80:83]\n\t" \
  "ds_read_b128 a[200:203], v222 offset:16384\n\t" \
  "ds_read_b128 a[204:207], v223 offset:16384\n\t" \
  "v_mfma_f32_16x16x32_bf16 a[80:83], a[172:175], v[132:135], a[80:83]\n\t" \
  "v_exp_f32 v44, v44\n\t" \
  "v_mfma_f32_16x16x32_bf16 a[96:99], a[176:179], v[128:131], a[96:99]\n\t" \
  STA("v_writelane_b32 %[stv], s94, 32\n\t") \
  STA("v_writelane_b32 %[stv], s92, 33\n\t") \
  "buffer_store_dword v233, v247, %[ws], 0 offen sc1\n\t" \
  "v_mfma_f32_16x16x32_bf16 a[96:99], a[180:183], v[132:135], a[96:99]\n\t" \
  "v_exp_f32 v45, v45\n\t" \
  "v_mfma_f32_16x16x32_bf16 a[112:115], a[184:187], v[128:131], a[112:115]\n\t" \
  "v_exp_f32 v46, v46\n\t" \
  "v_mfma_f32_16x16x32_bf16 a[112:115], a[188:191], v[132:135], a[112:115]\n\t" \
  "v_exp_f32 v47, v47\n\t" \
  "v_mfma_f32_16x16x32_bf16 v[160:163], v[216:219], v[128:131], v[160:163]\n\t" \
  "v_exp_f32 v64, v64\n\t" \
  "v_mfma_f32_16x16x32_bf16 v[160:163], v[216:219], v[132:135], v[160:163]\n\t" \
  "ds_read_b128 a[208:211], v220 offset:20480\n\t" \
  "v_cvt_pk_bf16_f32 v128, v0, v1\n\t" \
  "v_mfma_f32_16x16x32_bf16 a[4:7], a[128:131], v[136:139], a[4:7]\n\t" \
  "v_exp_f32 v65, v65\n\t" \
  "v_mfma_f32_16x16x32_bf16 a[4:7], a[132:135], v[140:143], a[4:7]\n\t" \
  "ds_read_b128 a[212:215], v221 offset:20480\n\t" \
  "v_cvt_pk_bf16_f32 v129, v2, v3\n\t" \
  "v_mfma_f32_16x16x32_bf16 a[20:23], a[136:139], v[136:139], a[20:23]\n\t" \
  "s_add_u32 m0, %[m], 49152\n\t" \
  "v_exp_f32 v66, v66\n\t" \
  "v_mfma_f32_16x16x32_bf16 a[20:23], a[140:143], v[140:143], a[20:23]\n\t" \
  "s_add_u32 s96, %[sofk], 65536\n\t" \
  "v_exp_f32 v67, v67\n\t" \
  "v_mfma_f32_16x16x32_bf16 a[36:39], a[144:147], v[136:139], a[36:39]\n\t" \
  "v_exp_f32 v68, v68\n\t" \
  "v_mfma_f32_16x16x32_bf16 a[36:39], a[148:151], v[140:143], a[36:39]\n\t" \
  "ds_read_b128 a[216:219], v222 offset:20480\n\t" \
  "v_cvt_pk_bf16_f32 v130, v4, v5\n\t" \
  "v_mfma_f32_16x16x32_bf16 a[52:55], a[152:155], v[136:139], a[52:55]\n\t" \
  "v_exp_f32 v69, v69\n\t" \
  "v_mfma_f32_16x16x32_bf16 a[52:55], a[156:159], v[140:143], a[52:55]\n\t" \
  "v_exp_f32 v70, v70\n\t" \
  "v_mfma_f32_16x16x32_bf16 a[68:71], a[160:163], v[136:139], a[68:71]\n\t" \
  "v_exp_f32 v71, v71\n\t" \
  "v_mfma_f32_16x16x32_bf16 a[68:71], a[164:167], v[140:143], a[68:71]\n\t" \
  "buffer_load_dwordx4 v224, %[sK], s96 offen offset:0 lds\n\t" \
  "v_mfma_f32_16x16x32_bf16 a[84:87], a[168:171], v[136:139], a[84:87]\n\t" \
  "ds_read_b128 a[220:223], v223 offset:20480\n\t" \
  "v_cvt_pk_bf16_f32 v131, v6, v7\n\t" \
  "v_mfma_f32_16x16x32_bf16 a[84:87], a[172:175], v[140:143], a[84:87]\n\t" \
  "v_exp_f32 v72, v72\n\t" \
  "v_mfma_f32_16x16x32_bf16 a[100:103], a[176:179], v[136:139], a[100:103]\n\t" \
  "ds_read_b128 a[224:227], v220 offset:24576\n\t" \
  "v_cvt_pk_bf16_f32 v132, v8, v9\n\t" \
  "v_mfma_f32_16x16x32_bf16 a[100:103], a[180:183], v[140:143], a[100:103]\n\t" \
  "buffer_load_dwordx4 v225, %[sK], s96 offen offset:1024 lds\n\t" \
  "v_mfma_f32_16x16x32_bf16 a[116:119], a[184:187], v[136:139], a[116:119]\n\t" \
  "v_exp_f32 v73, v73\n\t" \
  "v_mfma_f32_16x16x32_bf16 a[116:119], a[188:191], v[140:143], a[116:119]\n\t" \
  "v_exp_f32 v74, v74\n\t" \
  "v_mfma_f32_16x16x32_bf16 v[164:167], v[216:219], v[136:139], v[164:167]\n\t" \
  "buffer_load_dwordx4 v226, %[sK], s96 offen offset:2048 lds\n\t" \
  "v_mfma_f32_16x16x32_bf16 v[164:167], v[216:219], v[140:143], v[164:167]\n\t" \
  "ds_read_b128 a[228:231], v221 offset:24576\n\t" \
  "v_cvt_pk_bf16_f32 v133, v10, v11\n\t" \
  "v_mfma_f32_16x16x32_bf16 a[8:11], a[128:131], v[144:147], a[8:11]\n\t" \
  "v_exp_f32 v75, v75\n\t" \
  "v_mfma_f32_16x16x32_bf16 a[8:11], a[132:135], v[148:151], a[8:11]\n\t" \
  "v_exp_f32 v76, v76\n\t" \
  "v_mfma_f32_16x16x32_bf16 a[24:27], a[136:139], v[144:147], a[24:27]\n\t" \
  "buffer_load_dwordx4 v227, %[sK], s96 offen offset:3072 lds\n\t" \
  "v_mfma_f32_16x16x32_bf16 a[24:27], a[140:143], v[148:151], a[24:27]\n\t" \
  "ds_read_b128 a[232:235], v222 offset:24576\n\t" \
  "v_cvt_pk_bf16_f32 v134, v12, v13\n\t" \
  "v_mfma_f32_16x16x32_bf16 a[40:43], a[144:147], v[144:147], a[40:43]\n\t" \
  "v_exp_f32 v77, v77\n\t" \
  "v_mfma_f32_16x16x32_bf16 a[40:43], a[148:151], v[148:151], a[40:43]\n\t" \
  "s_add_u32 m0, %[m], 131072\n\t" \
  "s_mov_b32 s96, 40960\n\t" \
  "ds_read_b128 a[236:239], v223 offset:24576\n\t" \
  "v_mfma_f32_16x16x32_bf16 a[56:59], a[152:155], v[144:147], a[56:59]\n\t" \
  "buffer_load_dwordx4 v232, %[sU], s96 offen offset:2048 lds\n\t" \
  "v_mfma_f32_16x16x32_bf16 a[56:59], a[156:159], v[148:151], a[56:59]\n\t" \
  "s_add_u32 m0, %[m], 147456\n\t" \
  "v_exp_f32 v78, v78\n\t" \
  "v_mfma_f32_16x16x32_bf16 a[72:75], a[160:163], v[144:147], a[72:75]\n\t" \
  "s_add_u32 s96, %[sofk], 49152\n\t" \
  "v_exp_f32 v79, v79\n\t" \
  "v_mfma_f32_16x16x32_bf16 a[72:75], a[164:167], v[148:151], a[72:75]\n\t" \
  "ds_read_b128 a[240:243], v220 offset:28672\n\t" \
  "v_cvt_pk_bf16_f32 v135, v14, v15\n\t" \
  "v_mfma_f32_16x16x32_bf16 a[88:91], a[168:171], v[144:147], a[88:91]\n\t" \
  "v_exp_f32 v96, v96\n\t" \
  "v_mfma_f32_16x16x32_bf16 a[88:91], a[172:175], v[148:151], a[88:91]\n\t" \
  "v_exp_f32 v97, v97\n\t" \
  "v_mfma_f32_16x16x32_bf16 a[104:107], a[176:179], v[144:147], a[104:107]\n\t" \
  "v_exp_f32 v98, v98\n\t" \
  "v_mfma_f32_16x16x32_bf16 a[104:107], a[180:183], v[148:151], a[104:107]\n\t" \
  "ds_read_b128 a[244:247], v221 offset:28672\n\t" \
  "v_cvt_pk_bf16_f32 v136, v32, v33\n\t" \
  "v_mfma_f32_16x16x32_bf16 a[120:123], a[184:187], v[144:147], a[120:123]\n\t" \
  "v_exp_f32 v99, v99\n\t" \
  "v_mfma_f32_16x16x32_bf16 a[120:123], a[188:191], v[148:151], a[120:123]\n\t" \
  "buffer_load_dwordx4 v230, %[sV], s96 offen offset:0 lds\n\t" \
  "v_mfma_f32_16x16x32_bf16 v[168:171], v[216:219], v[144:147], v[168:171]\n\t" \
  "v_exp_f32 v100, v100\n\t" \
  "v_mfma_f32_16x16x32_bf16 v[168:171], v[216:219], v[148:151], v[168:171]\n\t" \
  "ds_read_b128 a[248:251], v222 offset:28672\n\t" \
  "v_cvt_pk_bf16_f32 v137, v34, v35\n\t" \
  "v_mfma_f32_16x16x32_bf16 a[12:15], a[128:131], v[152:155], a[12:15]\n\t" \
  "v_exp_f32 v101, v101\n\t" \
  "v_mfma_f32_16x16x32_bf16 a[12:15], a[132:135], v[156:159], a[12:15]\n\t" \
  "buffer_load_dwordx4 v231, %[sV], s96 offen offset:1024 lds\n\t" \
  "v_mfma_f32_16x16x32_bf16 a[28:31], a[136:139], v[152:155], a[28:31]\n\t" \
  "v_exp_f32 v102, v102\n\t" \
  "v_mfma_f32_16x16x32_bf16 a[28:31], a[140:143], v[156:159], a[28:31]\n\t" \
  "ds_read_b128 a[252:255], v223 offset:28672\n\t" \
  "v_cvt_pk_bf16_f32 v138, v36, v37\n\t" \
  "v_mfma_f32_16x16x32_bf16 a[44:47], a[144:147], v[152:155], a[44:47]\n\t" \
  "buffer_load_dwordx4 v230, %[sV], s96 offen offset:2048 lds\n\t" \
  "v_mfma_f32_16x16x32_bf16 a[44:47], a[148:151], v[156:159], a[44:47]\n\t" \
  "v_exp_f32 v103, v103\n\t" \
  "v_mfma_f32_16x16x32_bf16 a[60:63], a[152:155], v[152:155], a[60:63]\n\t" \
  "v_exp_f32 v104, v104\n\t" \
  "v_mfma_f32_16x16x32_bf16 a[60:63], a[156:159], v[156:159], a[60:63]\n\t" \
  "v_exp_f32 v105, v105\n\t" \
  "v_mfma_f32_16x16x32_bf16 a[76:79], a[160:163], v[152:155], a[76:79]\n\t" \
  "buffer_load_dwordx4 v231, %[sV], s96 offen offset:3072 lds\n\t" \
  "v_mfma_f32_16x16x32_bf16 a[76:79], a[164:167], v[156:159], a[76:79]\n\t" \
  "v_exp_f32 v106, v106\n\t" \
  "v_mfma_f32_16x16x32_bf16 a[92:95], a[168:171], v[152:155], a[92:95]\n\t" \
  "v_exp_f32 v107, v107\n\t" \
  "v_mfma_f32_16x16x32_bf16 a[92:95], a[172:175], v[156:159], a[92:95]\n\t" \
  "v_cvt_pk_bf16_f32 v139, v38, v39\n\t" \
  "v_cvt_pk_bf16_f32 v140, v40, v41\n\t" \
  "v_mfma_f32_16x16x32_bf16 a[108:111], a[176:179], v[152:155], a[108:111]\n\t" \
  "s_add_u32 m0, %[m], 131072\n\t" \
  "s_mov_b32 s96, 47104\n\t" \
  "buffer_load_dwordx4 v232, %[sU], s96 offen offset:3072 lds\n\t" \
  "v_mfma_f32_16x16x32_bf16 a[108:111], a[180:183], v[156:159], a[108:111]\n\t" \
  "v_exp_f32 v108, v108\n\t" \
  "v_mfma_f32_16x16x32_bf16 a[124:127], a[184:187], v[152:155], a[124:127]\n\t" \
  "v_exp_f32 v109, v109\n\t" \
  "v_mfma_f32_16x16x32_bf16 a[124:127], a[188:191], v[156:159], a[124:127]\n\t" \
  "v_exp_f32 v110, v110\n\t" \
  "v_mfma_f32_16x16x32_bf16 v[172:175], v[216:219], v[152:155], v[172:175]\n\t" \
  "v_cvt_pk_bf16_f32 v141, v42, v43\n\t" \
  "v_cvt_pk_bf16_f32 v142, v44, v45\n\t" \
  "v_mfma_f32_16x16x32_bf16 v[172:175], v[216:219], v[156:159], v[172:175]\n\t" \
  "s_waitcnt lgkmcnt(0)\n\t" \
  "s_mul_i32 s98, %[sofk], 5\n\t" \
  "s_xor_b32 s98, s98, 8192\n\t" \
  "s_lshr_b32 s98, s98, 1\n\t" \
  "v_cvt_pk_bf16_f32 v143, v46, v47\n\t" \
  STA("s_memrealtime s[94:95]\n\t") \
  STA("s_memtime s[92:93]\n\t") \
  "v_mfma_f32_16x16x32_bf16 v[0:3], a[192:195], v[16:19], v[176:179]\n\t" \
  "buffer_load_dword %[pfd], v204, %[sD], s98 offen\n\t" \
  "v_mfma_f32_16x16x32_bf16 v[0:3], a[196:199], v[20:23], v[0:3]\n\t" \
  "ds_read_b64_tr_b16 a[128:129], v228 offset:0\n\t" \
  "v_cvt_pk_bf16_f32 v144, v64, v65\n\t" \
  "v_mfma_f32_16x16x32_bf16 v[0:3], a[200:203], v[24:27], v[0:3]\n\t" \
  "ds_read_b64_tr_b16 a[130:131], v228 offset:4096\n\t" \
  "v_cvt_pk_bf16_f32 v145, v66, v67\n\t" \
  "v_mfma_f32_16x16x32_bf16 v[0:3], a[204:207], v[28:31], v[0:3]\n\t" \
  "v_exp_f32 v111, v111\n\t" \
  "v_mfma_f32_16x16x32_bf16 v[4:7], a[220:223], v[28:31], v[176:179]\n\t" \
  "ds_read_b64_tr_b16 a[132:133], v228 offset:8192\n\t" \
  "v_cvt_pk_bf16_f32 v146, v68, v69\n\t" \
  "v_mfma_f32_16x16x32_bf16 v[4:7], a[216:219], v[24:27], v[4:7]\n\t" \
  "ds_read_b64_tr_b16 a[134:135], v228 offset:12288\n\t" \
  "v_cvt_pk_bf16_f32 v147, v70, v71\n\t" \
  "v_mfma_f32_16x16x32_bf16 v[4:7], a[212:215], v[20:23], v[4:7]\n\t" \
  "v_exp_f32 v0, v0\n\t" \
  "v_mfma_f32_16x16x32_bf16 v[4:7], a[208:211], v[16:19], v[4:7]\n\t" \
  "ds_read_b64_tr_b16 a[136:137], v229 offset:0\n\t" \
  "v_cvt_pk_bf16_f32 v148, v72, v73\n\t" \
  "v_mfma_f32_16x16x32_bf16 v[8:11], a[224:227], v[16:19], v[176:179]\n\t" \
  "v_exp_f32 v1, v1\n\t" \
  "v_mfma_f32_16x16x32_bf16 v[8:11], a[228:231], v[20:23], v[8:11]\n\t" \
  "ds_read_b64_tr_b16 a[138:139], v229 offset:4096\n\t" \
  "v_cvt_pk_bf16_f32 v149, v74, v75\n\t" \
  "v_mfma_f32_16x16x32_bf16 v[8:11], a[232:235], v[24:27], v[8:11]\n\t" \
  "v_exp_f32 v2, v2\n\t" \
  "v_mfma_f32_16x16x32_bf16 v[8:11], a[236:239], v[28:31], v[8:11]\n\t" \
  "ds_read_b64_tr_b16 a[140:141], v229 offset:8192\n\t" \
  "v_cvt_pk_bf16_f32 v150, v76, v77\n\t" \
  "v_mfma_f32_16x16x32_bf16 v[12:15], a[252:255], v[28:31], v[176:179]\n\t" \
  STA("v_writelane_b32 %[stv], s94, 34\n\t") \
  STA("v_writelane_b32 %[stv], s92, 35\n\t") \
  "s_mul_i32 s98, %[sofk], 5\n\t" \
  "s_xor_b32 s98, s98, 12288\n\t" \
  "s_lshr_b32 s98, s98, 1\n\t" \
  "v_cvt_pk_bf16_f32 v151, v78, v79\n\t" \
  "v_mfma_f32_16x16x32_bf16 v[12:15], a[248:251], v[24:27], v[12:15]\n\t" \
  "buffer_load_dword %[pfd], v204, %[sD], s98 offen\n\t" \
  "v_mfma_f32_16x16x32_bf16 v[12:15], a[244:247], v[20:23], v[12:15]\n\t" \
  "ds_read_b64_tr_b16 a[142:143], v229 offset:12288\n\t" \
  "v_cvt_pk_bf16_f32 v152, v96, v97\n\t" \
  "v_mfma_f32_16x16x32_bf16 v[12:15], a[240:243], v[16:19], v[12:15]\n\t" \
  "ds_read_b64_tr_b16 a[144:145], v228 offset:256\n\t" \
  "v_cvt_pk_bf16_f32 v153, v98, v99\n\t" \
  "v_mfma_f32_16x16x32_bf16 v[32:35], a[192:195], v[48:51], v[180:183]\n\t" \
  "ds_read_b64_tr_b16 a[146:147], v228 offset:4352\n\t" \
  "v_cvt_pk_bf16_f32 v154, v100, v101\n\t" \
  "v_mfma_f32_16x16x32_bf16 v[32:35], a[196:199], v[52:55], v[32:35]\n\t" \
  "v_exp_f32 v3, v3\n\t" \
  "v_mfma_f32_16x16x32_bf16 v[32:35], a[200:203], v[56:59], v[32:35]\n\t" \
  "ds_read_b64_tr_b16 a[148:149], v228 offset:8448\n\t" \
  "v_cvt_pk_bf16_f32 v155, v102, v103\n\t" \
  "v_mfma_f32_16x16x32_bf16 v[32:35], a[204:207], v[60:63], v[32:35]\n\t" \
  "v_exp_f32 v4, v4\n\t" \
  "v_mfma_f32_16x16x32_bf16 v[36:39], a[220:223], v[60:63], v[180:183]\n\t" \
  "ds_read_b64_tr_b16 a[150:151], v228 offset:12544\n\t" \
  "v_cvt_pk_bf16_f32 v156, v104, v105\n\t" \
  "v_mfma_f32_16x16x32_bf16 v[36:39], a[216:219], v[56:59], v[36:39]\n\t" \
  "ds_read_b64_tr_b16 a[152:153], v229 offset:256\n\t" \
  "v_cvt_pk_bf16_f32 v157, v106, v107\n\t" \
  "v_mfma_f32_16x16x32_bf16 v[36:39], a[212:215], v[52:55], v[36:39]\n\t" \
  "v_exp_f32 v5, v5\n\t" \
  "v_mfma_f32_16x16x32_bf16 v[36:39], a[208:211], v[48:51], v[36:39]\n\t" \
  "ds_read_b64_tr_b16 a[154:155], v229 offset:4352\n\t" \
  "v_cvt_pk_bf16_f32 v158, v108, v109\n\t" \
  "v_mfma_f32_16x16x32_bf16 v[40:43], a[224:227], v[48:51], v[180:183]\n\t" \
  "v_exp_f32 v6, v6\n\t" \
  "v_mfma_f32_16x16x32_bf16 v[40:43], a[228:231], v[52:55], v[40:43]\n\t" \
  "ds_read_b64_tr_b16 a[156:157], v229 offset:8448\n\t" \
  "v_cvt_pk_bf16_f32 v159, v110, v111\n\t" \
  "v_mfma_f32_16x16x32_bf16 v[40:43], a[232:235], v[56:59], v[40:43]\n\t" \
  "v_exp_f32 v7, v7\n\t" \
  "v_mfma_f32_16x16x32_bf16 v[40:43], a[236:239], v[60:63], v[40:43]\n\t" \
  "v_exp_f32 v8, v8\n\t" \
  "v_mfma_f32_16x16x32_bf16 v[44:47], a[252:255], v[60:63], v[180:183]\n\t" \
  "v_exp_f32 v9, v9\n\t" \
  "v_mfma_f32_16x16x32_bf16 v[44:47], a[248:251], v[56:59], v[44:47]\n\t" \
  "ds_read_b64_tr_b16 a[158:159], v229 offset:12544\n\t" \
  "ds_read_b64_tr_b16 a[160:161], v228 offset:512\n\t" \
  "v_mfma_f32_16x16x32_bf16 v[44:47], a[244:247], v[52:55], v[44:47]\n\t" \
  "v_exp_f32 v10, v10\n\t" \
  "v_mfma_f32_16x16x32_bf16 v[44:47], a[240:243], v[48:51], v[44:47]\n\t" \
  "v_exp_f32 v11, v11\n\t" \
  "v_mfma_f32_16x16x32_bf16 v[64:67], a[192:195], v[80:83], v[184:187]\n\t" \
  "buffer_load_dword %[pfd], v247, %[ws], 0 offen sc1\n\t" \
  "v_mfma_f32_16x16x32_bf16 v[64:67], a[196:199], v[84:87], v[64:67]\n\t" \
  "ds_read_b64_tr_b16 a[162:163], v228 offset:4608\n\t" \
  "ds_read_b64_tr_b16 a[164:165], v228 offset:8704\n\t" \
  "v_mfma_f32_16x16x32_bf16 v[64:67], a[200:203], v[88:91], v[64:67]\n\t" \
  "v_exp_f32 v12, v12\n\t" \
  "v_mfma_f32_16x16x32_bf16 v[64:67], a[204:207], v[92:95], v[64:67]\n\t" \
  "v_exp_f32 v13, v13\n\t" \
  "v_mfma_f32_16x16x32_bf16 v[68:71], a[220:223], v[92:95], v[184:187]\n\t" \
  "ds_read_b64_tr_b16 a[166:167], v228 offset:12800\n\t" \
  "ds_read_b64_tr_b16 a[168:169], v229 offset:512\n\t" \
  "v_mfma_f32_16x16x32_bf16 v[68:71], a[216:219], v[88:91], v[68:71]\n\t" \
  "v_exp_f32 v14, v14\n\t" \
  "v_mfma_f32_16x16x32_bf16 v[68:71], a[212:215], v[84:87], v[68:71]\n\t" \
  "v_exp_f32 v15, v15\n\t" \
  "v_mfma_f32_16x16x32_bf16 v[68:71], a[208:211], v[80:83], v[68:71]\n\t" \
  "ds_read_b64_tr_b16 a[170:171], v229 offset:4608\n\t" \
  "ds_read_b64_tr_b16 a[172:173], v229 offset:8704\n\t" \
  "v_mfma_f32_16x16x32_bf16 v[72:75], a[224:227], v[80:83], v[184:187]\n\t" \
  "v_exp_f32 v32, v32\n\t" \
  "v_mfma_f32_16x16x32_bf16 v[72:75], a[228:231], v[84:87], v[72:75]\n\t" \
  "v_exp_f32 v33, v33\n\t" \
  "v_mfma_f32_16x16x32_bf16 v[72:75], a[232:235], v[88:91], v[72:75]\n\t" \
  "v_exp_f32 v34, v34\n\t" \
  "v_mfma_f32_16x16x32_bf16 v[72:75], a[236:239], v[92:95], v[72:75]\n\t" \
  "ds_read_b64_tr_b16 a[174:175], v229 offset:12800\n\t" \
  "ds_read_b64_tr_b16 a[176:177], v228 offset:768\n\t" \
  "v_mfma_f32_16x16x32_bf16 v[76:79], a[252:255], v[92:95], v[184:187]\n\t" \
  "v_exp_f32 v35, v35\n\t" \
  "v_mfma_f32_16x16x32_bf16 v[76:79], a[248:251], v[88:91], v[76:79]\n\t" \
  "v_exp_f32 v36, v36\n\t" \
  "v_mfma_f32_16x16x32_bf16 v[76:79], a[244:247], v[84:87], v[76:79]\n\t" \
  "ds_read_b64_tr_b16 a[178:179], v228 offset:4864\n\t" \
  "ds_read_b64_tr_b16 a[180:181], v228 offset:8960\n\t" \
  "v_mfma_f32_16x16x32_bf16 v[76:79], a[240:243], v[80:83], v[76:79]\n\t" \
  "v_exp_f32 v37, v37\n\t" \
  "v_mfma_f32_16x16x32_bf16 v[96:99], a[192:195], v[112:115], v[188:191]\n\t" \
  "v_exp_f32 v38, v38\n\t" \
  "v_mfma_f32_16x16x32_bf16 v[96:99], a[196:199], v[116:119], v[96:99]\n\t" \
  "v_exp_f32 v39, v39\n\t" \
  "v_mfma_f32_16x16x32_bf16 v[96:99], a[200:203], v[120:123], v[96:99]\n\t" \
  "ds_read_b64_tr_b16 a[182:183], v228 offset:13056\n\t" \
  "ds_read_b64_tr_b16 a[184:185], v229 offset:768\n\t" \
  "v_mfma_f32_16x16x32_bf16 v[96:99], a[204:207], v[124:127], v[96:99]\n\t" \
  "v_exp_f32 v40, v40\n\t" \
  "v_mfma_f32_16x16x32_bf16 v[100:103], a[220:223], v[124:127], v[188:191]\n\t" \
  "v_exp_f32 v41, v41\n\t" \
  "v_mfma_f32_16x16x32_bf16 v[100:103], a[216:219], v[120:123], v[100:103]\n\t" \
  "ds_read_b64_tr_b16 a[186:187], v229 offset:4864\n\t" \
  "ds_read_b64_tr_b16 a[188:189], v229 offset:8960\n\t" \
  "v_mfma_f32_16x16x32_bf16 v[100:103], a[212:215], v[116:119], v[100:103]\n\t" \
  "v_exp_f32 v42, v42\n\t" \
  "v_mfma_f32_16x16x32_bf16 v[100:103], a[208:211], v[112:115], v[100:103]\n\t" \
  "v_exp_f32 v43, v43\n\t" \
  "v_mfma_f32_16x16x32_bf16 v[104:107], a[224:227], v[112:115], v[188:191]\n\t" \
  "v_exp_f32 v44, v44\n\t" \
  "v_mfma_f32_16x16x32_bf16 v[104:107], a[228:231], v[116:119], v[104:107]\n\t" \
  "ds_read_b64_tr_b16 a[190:191], v229 offset:13056\n\t" \
  "v_mfma_f32_16x16x32_bf16 v[104:107], a[232:235], v[120:123], v[104:107]\n\t" \
  "s_getreg_b32 s98, hwreg(HW_REG_IB_STS, 0, 4)\n\t" \
  "s_cmp_lg_u32 s98, 0\n\t" \
  "s_addc_u32 %[na], %[na], 0\n\t" \
  "v_mfma_f32_16x16x32_bf16 v[104:107], a[236:239], v[124:127], v[104:107]\n\t" \
  "v_exp_f32 v45, v45\n\t" \
  "v_mfma_f32_16x16x32_bf16 v[108:111], a[252:255], v[124:127], v[188:191]\n\t" \
  "v_exp_f32 v46, v46\n\t" \
  "v_mfma_f32_16x16x32_bf16 v[108:111], a[248:251], v[120:123], v[108:111]\n\t" \
  "s_getreg_b32 s98, hwreg(HW_REG_IB_STS, 0, 4)\n\t" \
  "s_cmp_lg_u32 s98, 0\n\t" \
  "s_addc_u32 %[na], %[na], 0\n\t" \
  "v_mfma_f32_16x16x32_bf16 v[108:111], a[244:247], v[116:119], v[108:111]\n\t" \
  "v_exp_f32 v47, v47\n\t" \
  "v_mfma_f32_16x16x32_bf16 v[108:111], a[240:243], v[112:115], v[108:111]\n\t" \
  "s_waitcnt vmcnt(4) lgkmcnt(0)\n\t" \
  "s_barrier\n\t" \
  STA("s_memrealtime s[94:95]\n\t") \
  STA("s_memtime s[92:93]\n\t") \
  "v_mfma_f32_16x16x32_bf16 a[0:3], a[128:131], v[128:131], a[0:3]\n\t" \
  "v_exp_f32 v64, v64\n\t" \
  "v_mfma_f32_16x16x32_bf16 a[0:3], a[132:135], v[132:135], a[0:3]\n\t" \
  "v_exp_f32 v65, v65\n\t" \
  "v_mfma_f32_16x16x32_bf16 a[16:19], a[136:139], v[128:131], a[16:19]\n\t" \
  "ds_read_b128 a[192:195], v220 offset:0\n\t" \
  "ds_read_b128 a[196:199], v221 offset:0\n\t" \
  "v_mfma_f32_16x16x32_bf16 a[16:19], a[140:143], v[132:135], a[16:19]\n\t" \
  "v_exp_f32 v66, v66\n\t" \
  "v_mfma_f32_16x16x32_bf16 a[32:35], a[144:147], v[128:131], a[32:35]\n\t" \
  "v_exp_f32 v67, v67\n\t" \
  "v_mfma_f32_16x16x32_bf16 a[32:35], a[148:151], v[132:135], a[32:35]\n\t" \
  "v_exp_f32 v68, v68\n\t" \
  "v_mfma_f32_16x16x32_bf16 a[48:51], a[152:155], v[128:131], a[48:51]\n\t" \
  STS("v_writelane_b32 %[stv], %[na], 58\n\t") \
  "s_cmp_ge_u32 %[na], 1\n\t" \
  "s_cselect_b32 s98, 0x4000, 0\n\t" \
  "s_or_b32 s98, s98, %[tag]\n\t" \
  "s_cmp_ge_u32 %[na], 2\n\t" \
  "s_cselect_b32 %[na], 2.0, 0\n\t" \
  "s_or_b32 s98, s98, %[na]\n\t" \
  "v_mov_b32 v209, s98\n\t" \
  "v_mfma_f32_16x16x32_bf16 a[48:51], a[156:159], v[132:135], a[48:51]\n\t" \
  "v_exp_f32 v69, v69\n\t" \
  "v_mfma_f32_16x16x32_bf16 a[64:67], a[160:163], v[128:131], a[64:67]\n\t" \
  "ds_read_b128 a[200:203], v222 offset:0\n\t" \
  "ds_read_b128 a[204:207], v223 offset:0\n\t" \
  "v_mfma_f32_16x16x32_bf16 a[64:67], a[164:167], v[132:135], a[64:67]\n\t" \
  "v_exp_f32 v70, v70\n\t" \
  "v_mfma_f32_16x16x32_bf16 a[80:83], a[168:171], v[128:131], a[80:83]\n\t" \
  "ds_read_b128 a[208:211], v220 offset:4096\n\t" \
  "ds_read_b128 a[212:215], v221 offset:4096\n\t" \
  "v_mfma_f32_16x16x32_bf16 a[80:83], a[172:175], v[132:135], a[80:83]\n\t" \
  "v_bfrev_b32 v205, 1\n\t" \
  "v_writelane_b32 v205, %[vo], 0\n\t" \
  "v_mfma_f32_16x16x32_bf16 a[96:99], a[176:179], v[128:131], a[96:99]\n\t" \
  STA("v_writelane_b32 %[stv], s94, 36\n\t") \
  STA("v_writelane_b32 %[stv], s92, 37\n\t") \
  "buffer_store_dword v209, v205, %[ws], 0 offen sc1\n\t" \
  "v_mfma_f32_16x16x32_bf16 a[96:99], a[180:183], v[132:135], a[96:99]\n\t" \
  "v_exp_f32 v71, v71\n\t" \
  "v_mfma_f32_16x16x32_bf16 a[112:115], a[184:187], v[128:131], a[112:115]\n\t" \
  "v_exp_f32 v72, v72\n\t" \
  "v_mfma_f32_16x16x32_bf16 a[112:115], a[188:191], v[132:135], a[112:115]\n\t" \
  "v_exp_f32 v73, v73\n\t" \
  "v_mfma_f32_16x16x32_bf16 v[160:163], v[216:219], v[128:131], v[160:163]\n\t" \
  "v_exp_f32 v74, v74\n\t" \
  "v_mfma_f32_16x16x32_bf16 v[160:163], v[216:219], v[132:135], v[160:163]\n\t" \
  "v_exp_f32 v75, v75\n\t" \
  "v_mfma_f32_16x16x32_bf16 a[4:7], a[128:131], v[136:139], a[4:7]\n\t" \
  "v_exp_f32 v76, v76\n\t" \
  "v_mfma_f32_16x16x32_bf16 a[4:7], a[132:135], v[140:143], a[4:7]\n\t" \
  "v_exp_f32 v77, v77\n\t" \
  "v_mfma_f32_16x16x32_bf16 a[20:23], a[136:139], v[136:139], a[20:23]\n\t" \
  "s_add_u32 m0, %[m], 65536\n\t" \
  "v_exp_f32 v78, v78\n\t" \
  "v_mfma_f32_16x16x32_bf16 a[20:23], a[140:143], v[140:143], a[20:23]\n\t" \
  "s_add_u32 s96, %[sofk], 81920\n\t" \
  "v_exp_f32 v79, v79\n\t" \
  "v_mfma_f32_16x16x32_bf16 a[36:39], a[144:147], v[136:139], a[36:39]\n\t" \
  "v_exp_f32 v96, v96\n\t" \
  "v_mfma_f32_16x16x32_bf16 a[36:39], a[148:151], v[140:143], a[36:39]\n\t" \
  "ds_read_b128 a[216:219], v222 offset:4096\n\t" \
  "v_cvt_pk_bf16_f32 v128, v0, v1\n\t" \
  "v_mfma_f32_16x16x32_bf16 a[52:55], a[152:155], v[136:139], a[52:55]\n\t" \
  "v_exp_f32 v97, v97\n\t" \
  "v_mfma_f32_16x16x32_bf16 a[52:55], a[156:159], v[140:143], a[52:55]\n\t" \
  "v_exp_f32 v98, v98\n\t" \
  "v_mfma_f32_16x16x32_bf16 a[68:71], a[160:163], v[136:139], a[68:71]\n\t" \
  "v_exp_f32 v99, v99\n\t" \
  "v_mfma_f32_16x16x32_bf16 a[68:71], a[164:167], v[140:143], a[68:71]\n\t" \
  "buffer_load_dwordx4 v224, %[sK], s96 offen offset:0 lds\n\t" \
  "v_mfma_f32_16x16x32_bf16 a[84:87], a[168:171], v[136:139], a[84:87]\n\t" \
  "ds_read_b128 a[220:223], v223 offset:4096\n\t" \
  "v_cvt_pk_bf16_f32 v129, v2, v3\n\t" \
  "v_mfma_f32_16x16x32_bf16 a[84:87], a[172:175], v[140:143], a[84:87]\n\t" \
  "v_exp_f32 v100, v100\n\t" \
  "v_mfma_f32_16x16x32_bf16 a[100:103], a[176:179], v[136:139], a[100:103]\n\t" \
  "ds_read_b128 a[224:227], v220 offset:8192\n\t" \
  "v_cvt_pk_bf16_f32 v130, v4, v5\n\t" \
  "v_mfma_f32_16x16x32_bf16 a[100:103], a[180:183], v[140:143], a[100:103]\n\t" \
  "buffer_load_dwordx4 v225, %[sK], s96 offen offset:1024 lds\n\t" \
  "v_mfma_f32_16x16x32_bf16 a[116:119], a[184:187], v[136:139], a[116:119]\n\t" \
  "v_exp_f32 v101, v101\n\t" \
  "v_mfma_f32_16x16x32_bf16 a[116:119], a[188:191], v[140:143], a[116:119]\n\t" \
  "v_exp_f32 v102, v102\n\t" \
  "v_mfma_f32_16x16x32_bf16 v[164:167], v[216:219], v[136:139], v[164:167]\n\t" \
  "buffer_load_dwordx4 v226, %[sK], s96 offen offset:2048 lds\n\t" \
  "v_mfma_f32_16x16x32_bf16 v[164:167], v[216:219], v[140:143], v[164:167]\n\t" \
  "ds_read_b128 a[228:231], v221 offset:8192\n\t" \
  "v_cvt_pk_bf16_f32 v131, v6, v7\n\t" \
  "v_mfma_f32_16x16x32_bf16 a[8:11], a[128:131], v[144:147], a[8:11]\n\t" \
  "v_exp_f32 v103, v103\n\t" \
  "v_mfma_f32_16x16x32_bf16 a[8:11], a[132:135], v[148:151], a[8:11]\n\t" \
  "v_exp_f32 v104, v104\n\t" \
  "v_mfma_f32_16x16x32_bf16 a[24:27], a[136:139], v[144:147], a[24:27]\n\t" \
  "buffer_load_dwordx4 v227, %[sK], s96 offen offset:3072 lds\n\t" \
  "v_mfma_f32_16x16x32_bf16 a[24:27], a[140:143], v[148:151], a[24:27]\n\t" \
  "ds_read_b128 a[232:235], v222 offset:8192\n\t" \
  "v_cvt_pk_bf16_f32 v132, v8, v9\n\t" \
  "v_mfma_f32_16x16x32_bf16 a[40:43], a[144:147], v[144:147], a[40:43]\n\t" \
  "v_exp_f32 v105, v105\n\t" \
  "v_mfma_f32_16x16x32_bf16 a[40:43], a[148:151], v[148:151], a[40:43]\n\t" \
  "s_add_u32 m0, %[m], 32768\n\t" \
  "s_mov_b32 s96, 0\n\t" \
  ".Lhs7:\n\ts_cbranch_execz .Lhs7n\n\t" \
  "ds_read_b128 a[236:239], v223 offset:8192\n\t" \
  "v_mfma_f32_16x16x32_bf16 a[56:59], a[152:155], v[144:147], a[56:59]\n\t" \
  "buffer_load_dwordx4 v232, %[sG], s96 offen offset:0 lds\n\t" \
  "v_mfma_f32_16x16x32_bf16 a[56:59], a[156:159], v[148:151], a[56:59]\n\t" \
  "s_add_u32 m0, %[m], 81920\n\t" \
  "v_exp_f32 v106, v106\n\t" \
  "v_mfma_f32_16x16x32_bf16 a[72:75], a[160:163], v[144:147], a[72:75]\n\t" \
  "s_add_u32 s96, %[sofk], 65536\n\t" \
  "v_exp_f32 v107, v107\n\t" \
  "v_mfma_f32_16x16x32_bf16 a[72:75], a[164:167], v[148:151], a[72:75]\n\t" \
  "ds_read_b128 a[240:243], v220 offset:12288\n\t" \
  "v_cvt_pk_bf16_f32 v133, v10, v11\n\t" \
  "v_mfma_f32_16x16x32_bf16 a[88:91], a[168:171], v[144:147], a[88:91]\n\t" \
  "v_exp_f32 v108, v108\n\t" \
  "v_mfma_f32_16x16x32_bf16 a[88:91], a[172:175], v[148:151], a[88:91]\n\t" \
  "v_exp_f32 v109, v109\n\t" \
  "v_mfma_f32_16x16x32_bf16 a[104:107], a[176:179], v[144:147], a[104:107]\n\t" \
  "v_exp_f32 v110, v110\n\t" \
  "v_mfma_f32_16x16x32_bf16 a[104:107], a[180:183], v[148:151], a[104:107]\n\t" \
  "ds_read_b128 a[244:247], v221 offset:12288\n\t" \
  "v_cvt_pk_bf16_f32 v134, v12, v13\n\t" \
  "v_mfma_f32_16x16x32_bf16 a[120:123], a[184:187], v[144:147], a[120:123]\n\t" \
  "v_exp_f32 v111, v111\n\t" \
  "v_mfma_f32_16x16x32_bf16 a[120:123], a[188:191], v[148:151], a[120:123]\n\t" \
  "buffer_load_dwordx4 v230, %[sV], s96 offen offset:0 lds\n\t" \
  "v_mfma_f32_16x16x32_bf16 v[168:171], v[216:219], v[144:147], v[168:171]\n\t" \
  "ds_read_b128 a[248:251], v222 offset:12288\n\t" \
  "v_cvt_pk_bf16_f32 v135, v14, v15\n\t" \
  "v_mfma_f32_16x16x32_bf16 v[168:171], v[216:219], v[148:151], v[168:171]\n\t" \
  "ds_read_b128 a[252:255], v223 offset:12288\n\t" \
  "v_cvt_pk_bf16_f32 v136, v32, v33\n\t" \
  "v_mfma_f32_16x16x32_bf16 a[12:15], a[128:131], v[152:155], a[12:15]\n\t" \
  "v_cvt_pk_bf16_f32 v137, v34, v35\n\t" \
  "v_cvt_pk_bf16_f32 v138, v36, v37\n\t" \
  "v_mfma_f32_16x16x32_bf16 a[12:15], a[132:135], v[156:159], a[12:15]\n\t" \
  "buffer_load_dwordx4 v231, %[sV], s96 offen offset:1024 lds\n\t" \
  "v_mfma_f32_16x16x32_bf16 a[28:31], a[136:139], v[152:155], a[28:31]\n\t" \
  "v_cvt_pk_bf16_f32 v139, v38, v39\n\t" \
  "v_cvt_pk_bf16_f32 v140, v40, v41\n\t" \
  "v_mfma_f32_16x16x32_bf16 a[28:31], a[140:143], v[156:159], a[28:31]\n\t" \
  "v_cvt_pk_bf16_f32 v141, v42, v43\n\t" \
  "v_cvt_pk_bf16_f32 v142, v44, v45\n\t" \
  "v_mfma_f32_16x16x32_bf16 a[44:47], a[144:147], v[152:155], a[44:47]\n\t" \
  "buffer_load_dwordx4 v230, %[sV], s96 offen offset:2048 lds\n\t" \
  "v_mfma_f32_16x16x32_bf16 a[44:47], a[148:151], v[156:159], a[44:47]\n\t" \
  "v_cvt_pk_bf16_f32 v143, v46, v47\n\t" \
  "v_cvt_pk_bf16_f32 v144, v64, v65\n\t" \
  "v_mfma_f32_16x16x32_bf16 a[60:63], a[152:155], v[152:155], a[60:63]\n\t" \
  "v_cvt_pk_bf16_f32 v145, v66, v67\n\t" \
  "v_cvt_pk_bf16_f32 v146, v68, v69\n\t" \
  "v_mfma_f32_16x16x32_bf16 a[60:63], a[156:159], v[156:159], a[60:63]\n\t" \
  "v_cvt_pk_bf16_f32 v147, v70, v71\n\t" \
  "v_cvt_pk_bf16_f32 v148, v72, v73\n\t" \
  "v_mfma_f32_16x16x32_bf16 a[76:79], a[160:163], v[152:155], a[76:79]\n\t" \
  "buffer_load_dwordx4 v231, %[sV], s96 offen offset:3072 lds\n\t" \
  "v_mfma_f32_16x16x32_bf16 a[76:79], a[164:167], v[156:159], a[76:79]\n\t" \
  "v_cvt_pk_bf16_f32 v149, v74, v75\n\t" \
  "v_cvt_pk_bf16_f32 v150, v76, v77\n\t" \
  "v_mfma_f32_16x16x32_bf16 a[92:95], a[168:171], v[152:155], a[92:95]\n\t" \
  "s_add_u32 m0, %[m], 32768\n\t" \
  "s_mov_b32 s96, 6144\n\t" \
  "v_cvt_pk_bf16_f32 v151, v78, v79\n\t" \
  "v_mfma_f32_16x16x32_bf16 a[92:95], a[172:175], v[156:159], a[92:95]\n\t" \
  "v_mfma_f32_16x16x32_bf16 a[108:111], a[176:179], v[152:155], a[108:111]\n\t" \
  "buffer_load_dwordx4 v232, %[sG], s96 offen offset:1024 lds\n\t" \
  "v_mfma_f32_16x16x32_bf16 a[108:111], a[180:183], v[156:159], a[108:111]\n\t" \
  "v_mfma_f32_16x16x32_bf16 a[124:127], a[184:187], v[152:155], a[124:127]\n\t" \
  "v_mfma_f32_16x16x32_bf16 a[124:127], a[188:191], v[156:159], a[124:127]\n\t" \
  "s_add_u32 s98, %[pf4], 28672\n\t" \
  "v_mfma_f32_16x16x32_bf16 v[172:175], v[216:219], v[152:155], v[172:175]\n\t" \
  "buffer_load_dword %[pfd], v245, %[sG], s98 offen\n\t" \
  "v_mfma_f32_16x16x32_bf16 v[172:175], v[216:219], v[156:159], v[172:175]\n\t" \
  "s_waitcnt lgkmcnt(0)\n\t" \
  "v_cvt_pk_bf16_f32 v152, v96, v97\n\t" \
  "v_cvt_pk_bf16_f32 v153, v98, v99\n\t" \
  STA("s_memrealtime s[94:95]\n\t") \
  STA("s_memtime s[92:93]\n\t") \
  "v_mfma_f32_16x16x32_bf16 v[0:3], a[192:195], v[16:19], v[176:179]\n\t" \
  "ds_read_b64_tr_b16 a[128:129], v237 offset:0\n\t" \
  "v_cvt_pk_bf16_f32 v154, v100, v101\n\t" \
  "v_mfma_f32_16x16x32_bf16 v[0:3], a[196:199], v[20:23], v[0:3]\n\t" \
  "ds_read_b64_tr_b16 a[130:131], v237 offset:4096\n\t" \
  "v_cvt_pk_bf16_f32 v155, v102, v103\n\t" \
  "v_mfma_f32_16x16x32_bf16 v[0:3], a[200:203], v[24:27], v[0:3]\n\t" \
  "ds_read_b64_tr_b16 a[132:133], v237 offset:8192\n\t" \
  "v_cvt_pk_bf16_f32 v156, v104, v105\n\t" \
  "v_mfma_f32_16x16x32_bf16 v[0:3], a[204:207], v[28:31], v[0:3]\n\t" \
  "ds_read_b64_tr_b16 a[134:135], v237 offset:12288\n\t" \
  "v_cvt_pk_bf16_f32 v157, v106, v107\n\t" \
  "v_mfma_f32_16x16x32_bf16 v[4:7], a[220:223], v[28:31], v[176:179]\n\t" \
  "ds_read_b64_tr_b16 a[136:137], v238 offset:0\n\t" \
  "v_cvt_pk_bf16_f32 v158, v108, v109\n\t" \
  "v_mfma_f32_16x16x32_bf16 v[4:7], a[216:219], v[24:27], v[4:7]\n\t" \
  "ds_read_b64_tr_b16 a[138:139], v238 offset:4096\n\t" \
  "v_cvt_pk_bf16_f32 v159, v110, v111\n\t" \
  "v_mfma_f32_16x16x32_bf16 v[4:7], a[212:215], v[20:23], v[4:7]\n\t" \
  "v_exp_f32 v0, v0\n\t" \
  "v_mfma_f32_16x16x32_bf16 v[4:7], a[208:211], v[16:19], v[4:7]\n\t" \
  "v_exp_f32 v1, v1\n\t" \
  "v_mfma_f32_16x16x32_bf16 v[8:11], a[224:227], v[16:19], v[176:179]\n\t" \
  "v_exp_f32 v2, v2\n\t" \
  "v_mfma_f32_16x16x32_bf16 v[8:11], a[228:231], v[20:23], v[8:11]\n\t" \
  "v_exp_f32 v3, v3\n\t" \
  "v_mfma_f32_16x16x32_bf16 v[8:11], a[232:235], v[24:27], v[8:11]\n\t" \
  "v_mfma_f32_16x16x32_bf16 v[8:11], a[236:239], v[28:31], v[8:11]\n\t" \
  "s_add_u32 s98, %[pf2], 0\n\t" \
  "v_exp_f32 v4, v4\n\t" \
  "v_mfma_f32_16x16x32_bf16 v[12:15], a[252:255], v[28:31], v[176:179]\n\t" \
  STA("v_writelane_b32 %[stv], s94, 38\n\t") \
  STA("v_writelane_b32 %[stv], s92, 39\n\t") \
  "buffer_load_dword %[pfd], v202, %[sM], s98 offen\n\t" \
  "v_mfma_f32_16x16x32_bf16 v[12:15], a[248:251], v[24:27], v[12:15]\n\t" \
  "ds_read_b64_tr_b16 a[140:141], v238 offset:8192\n\t" \
  "ds_read_b64_tr_b16 a[142:143], v238 offset:12288\n\t" \
  "v_mfma_f32_16x16x32_bf16 v[12:15], a[244:247], v[20:23], v[12:15]\n\t" \
  "v_exp_f32 v5, v5\n\t" \
  "v_mfma_f32_16x16x32_bf16 v[12:15], a[240:243], v[16:19], v[12:15]\n\t" \
  "v_exp_f32 v6, v6\n\t" \
  "v_mfma_f32_16x16x32_bf16 v[32:35], a[192:195], v[48:51], v[180:183]\n\t" \
  "ds_read_b64_tr_b16 a[144:145], v237 offset:256\n\t" \
  "ds_read_b64_tr_b16 a[146:147], v237 offset:4352\n\t" \
  "v_mfma_f32_16x16x32_bf16 v[32:35], a[196:199], v[52:55], v[32:35]\n\t" \
  "v_exp_f32 v7, v7\n\t" \
  "v_mfma_f32_16x16x32_bf16 v[32:35], a[200:203], v[56:59], v[32:35]\n\t" \
  "v_exp_f32 v8, v8\n\t" \
  "v_mfma_f32_16x16x32_bf16 v[32:35], a[204:207], v[60:63], v[32:35]\n\t" \
  "v_exp_f32 v9, v9\n\t" \
  "v_mfma_f32_16x16x32_bf16 v[36:39], a[220:223], v[60:63], v[180:183]\n\t" \
  "ds_read_b64_tr_b16 a[148:149], v237 offset:8448\n\t" \
  "ds_read_b64_tr_b16 a[150:151], v237 offset:12544\n\t" \
  "v_mfma_f32_16x16x32_bf16 v[36:39], a[216:219], v[56:59], v[36:39]\n\t" \
  "v_exp_f32 v10, v10\n\t" \
  "v_mfma_f32_16x16x32_bf16 v[36:39], a[212:215], v[52:55], v[36:39]\n\t" \
  "v_exp_f32 v11, v11\n\t" \
  "v_mfma_f32_16x16x32_bf16 v[36:39], a[208:211], v[48:51], v[36:39]\n\t" \
  "ds_read_b64_tr_b16 a[152:153], v238 offset:256\n\t" \
  "ds_read_b64_tr_b16 a[154:155], v238 offset:4352\n\t" \
  "v_mfma_f32_16x16x32_bf16 v[40:43], a[224:227], v[48:51], v[180:183]\n\t" \
  "v_exp_f32 v12, v12\n\t" \
  "v_mfma_f32_16x16x32_bf16 v[40:43], a[228:231], v[52:55], v[40:43]\n\t" \
  "v_exp_f32 v13, v13\n\t" \
  "v_mfma_f32_16x16x32_bf16 v[40:43], a[232:235], v[56:59], v[40:43]\n\t" \
  "v_exp_f32 v14, v14\n\t" \
  "v_mfma_f32_16x16x32_bf16 v[40:43], a[236:239], v[60:63], v[40:43]\n\t" \
  "ds_read_b64_tr_b16 a[156:157], v238 offset:8448\n\t" \
  "ds_read_b64_tr_b16 a[158:159], v238 offset:12544\n\t" \
  "v_mfma_f32_16x16x32_bf16 v[44:47], a[252:255], v[60:63], v[180:183]\n\t" \
  "v_exp_f32 v15, v15\n\t" \
  "v_mfma_f32_16x16x32_bf16 v[44:47], a[248:251], v[56:59], v[44:47]\n\t" \
  "v_exp_f32 v32, v32\n\t" \
  "v_mfma_f32_16x16x32_bf16 v[44:47], a[244:247], v[52:55], v[44:47]\n\t" \
  "ds_read_b64_tr_b16 a[160:161], v237 offset:512\n\t" \
  "ds_read_b64_tr_b16 a[162:163], v237 offset:4608\n\t" \
  "v_mfma_f32_16x16x32_bf16 v[44:47], a[240:243], v[48:51], v[44:47]\n\t" \
  "v_exp_f32 v33, v33\n\t" \
  "v_mfma_f32_16x16x32_bf16 v[64:67], a[192:195], v[80:83], v[184:187]\n\t" \
  "v_exp_f32 v34, v34\n\t" \
  "v_mfma_f32_16x16x32_bf16 v[64:67], a[196:199], v[84:87], v[64:67]\n\t" \
  "v_exp_f32 v35, v35\n\t" \
  "v_mfma_f32_16x16x32_bf16 v[64:67], a[200:203], v[88:91], v[64:67]\n\t" \
  "ds_read_b64_tr_b16 a[164:165], v237 offset:8704\n\t" \
  "ds_read_b64_tr_b16 a[166:167], v237 offset:12800\n\t" \
  "v_mfma_f32_16x16x32_bf16 v[64:67], a[204:207], v[92:95], v[64:67]\n\t" \
  "v_exp_f32 v36, v36\n\t" \
  "v_mfma_f32_16x16x32_bf16 v[68:71], a[220:223], v[92:95], v[184:187]\n\t" \
  "v_exp_f32 v37, v37\n\t" \
  "v_mfma_f32_16x16x32_bf16 v[68:71], a[216:219], v[88:91], v[68:71]\n\t" \
  "ds_read_b64_tr_b16 a[168:169], v238 offset:512\n\t" \
  "ds_read_b64_tr_b16 a[170:171], v238 offset:4608\n\t" \
  "v_mfma_f32_16x16x32_bf16 v[68:71], a[212:215], v[84:87], v[68:71]\n\t" \
  "v_exp_f32 v38, v38\n\t" \
  "v_mfma_f32_16x16x32_bf16 v[68:71], a[208:211], v[80:83], v[68:71]\n\t" \
  "v_exp_f32 v39, v39\n\t" \
  "v_mfma_f32_16x16x32_bf16 v[72:75], a[224:227], v[80:83], v[184:187]\n\t" \
  "v_exp_f32 v40, v40\n\t" \
  "v_mfma_f32_16x16x32_bf16 v[72:75], a[228:231], v[84:87], v[72:75]\n\t" \
  "ds_read_b64_tr_b16 a[172:173], v238 offset:8704\n\t" \
  "ds_read_b64_tr_b16 a[174:175], v238 offset:12800\n\t" \
  "v_mfma_f32_16x16x32_bf16 v[72:75], a[232:235], v[88:91], v[72:75]\n\t" \
  "v_exp_f32 v41, v41\n\t" \
  "v_mfma_f32_16x16x32_bf16 v[72:75], a[236:239], v[92:95], v[72:75]\n\t" \
  "v_exp_f32 v42, v42\n\t" \
  "v_mfma_f32_16x16x32_bf16 v[76:79], a[252:255], v[92:95], v[184:187]\n\t" \
  "ds_read_b64_tr_b16 a[176:177], v237 offset:768\n\t" \
  "ds_read_b64_tr_b16 a[178:179], v237 offset:4864\n\t" \
  "v_mfma_f32_16x16x32_bf16 v[76:79], a[248:251], v[88:91], v[76:79]\n\t" \
  "v_exp_f32 v43, v43\n\t" \
  "v_mfma_f32_16x16x32_bf16 v[76:79], a[244:247], v[84:87], v[76:79]\n\t" \
  "v_exp_f32 v44, v44\n\t" \
  "v_mfma_f32_16x16x32_bf16 v[76:79], a[240:243], v[80:83], v[76:79]\n\t" \
  "v_exp_f32 v45, v45\n\t" \
  "v_mfma_f32_16x16x32_bf16 v[96:99], a[192:195], v[112:115], v[188:191]\n\t" \
  "ds_read_b64_tr_b16 a[180:181], v237 offset:8960\n\t" \
  "ds_read_b64_tr_b16 a[182:183], v237 offset:13056\n\t" \
  "v_mfma_f32_16x16x32_bf16 v[96:99], a[196:199], v[116:119], v[96:99]\n\t" \
  "v_exp_f32 v46, v46\n\t" \
  "v_mfma_f32_16x16x32_bf16 v[96:99], a[200:203], v[120:123], v[96:99]\n\t" \
  "v_exp_f32 v47, v47\n\t" \
  "v_mfma_f32_16x16x32_bf16 v[96:99], a[204:207], v[124:127], v[96:99]\n\t" \
  "ds_read_b64_tr_b16 a[184:185], v238 offset:768\n\t" \
  "ds_read_b64_tr_b16 a[186:187], v238 offset:4864\n\t" \
  "v_mfma_f32_16x16x32_bf16 v[100:103], a[220:223], v[124:127], v[188:191]\n\t" \
  "v_exp_f32 v64, v64\n\t" \
  "v_mfma_f32_16x16x32_bf16 v[100:103], a[216:219], v[120:123], v[100:103]\n\t" \
  "v_exp_f32 v65, v65\n\t" \
  "v_mfma_f32_16x16x32_bf16 v[100:103], a[212:215], v[116:119], v[100:103]\n\t" \
  "v_exp_f32 v66, v66\n\t" \
  "v_mfma_f32_16x16x32_bf16 v[100:103], a[208:211], v[112:115], v[100:103]\n\t" \
  "ds_read_b64_tr_b16 a[188:189], v238 offset:8960\n\t" \
  "ds_read_b64_tr_b16 a[190:191], v238 offset:13056\n\t" \
  "v_mfma_f32_16x16x32_bf16 v[104:107], a[224:227], v[112:115], v[188:191]\n\t" \
  "v_exp_f32 v67, v67\n\t" \
  "v_mfma_f32_16x16x32_bf16 v[104:107], a[228:231], v[116:119], v[104:107]\n\t" \
  "v_exp_f32 v68, v68\n\t" \
  "v_mfma_f32_16x16x32_bf16 v[104:107], a[232:235], v[120:123], v[104:107]\n\t" \
  "v_exp_f32 v69, v69\n\t" \
  "v_mfma_f32_16x16x32_bf16 v[104:107], a[236:239], v[124:127], v[104:107]\n\t" \
  "v_exp_f32 v70, v70\n\t" \
  "v_mfma_f32_16x16x32_bf16 v[108:111], a[252:255], v[124:127], v[188:191]\n\t" \
  "v_exp_f32 v71, v71\n\t" \
  "v_mfma_f32_16x16x32_bf16 v[108:111], a[248:251], v[120:123], v[108:111]\n\t" \
  "v_exp_f32 v72, v72\n\t" \
  "v_mfma_f32_16x16x32_bf16 v[108:111], a[244:247], v[116:119], v[108:111]\n\t" \
  "v_exp_f32 v73, v73\n\t" \
  "v_mfma_f32_16x16x32_bf16 v[108:111], a[240:243], v[112:115], v[108:111]\n\t" \
  "s_waitcnt vmcnt(3) lgkmcnt(0)\n\t" \
  "s_barrier\n\t" \
  STA("s_memrealtime s[94:95]\n\t") \
  STA("s_memtime s[92:93]\n\t") \
  "v_mfma_f32_16x16x32_bf16 a[0:3], a[128:131], v[128:131], a[0:3]\n\t" \
  "v_exp_f32 v74, v74\n\t" \
  "v_mfma_f32_16x16x32_bf16 a[0:3], a[132:135], v[132:135], a[0:3]\n\t" \
  "v_exp_f32 v75, v75\n\t" \
  "v_mfma_f32_16x16x32_bf16 a[16:19], a[136:139], v[128:131], a[16:19]\n\t" \
  "ds_read_b128 a[192:195], v220 offset:16384\n\t" \
  "ds_read_b128 a[196:199], v221 offset:16384\n\t" \
  "v_mfma_f32_16x16x32_bf16 a[16:19], a[140:143], v[132:135], a[16:19]\n\t" \
  "v_exp_f32 v76, v76\n\t" \
  "v_mfma_f32_16x16x32_bf16 a[32:35], a[144:147], v[128:131], a[32:35]\n\t" \
  "s_lshr_b32 s98, %[sofk], 6\n\t" \
  "v_xor_b32 v234, s98, v244\n\t" \
  "v_add_u32 v213, %[m], v242\n\t" \
  "v_mfma_f32_16x16x32_bf16 a[32:35], a[148:151], v[132:135], a[32:35]\n\t" \
  "v_exp_f32 v77, v77\n\t" \
  "v_mfma_f32_16x16x32_bf16 a[48:51], a[152:155], v[128:131], a[48:51]\n\t" \
  "v_exp_f32 v78, v78\n\t" \
  "v_mfma_f32_16x16x32_bf16 a[48:51], a[156:159], v[132:135], a[48:51]\n\t" \
  "v_exp_f32 v79, v79\n\t" \
  "v_mfma_f32_16x16x32_bf16 a[64:67], a[160:163], v[128:131], a[64:67]\n\t" \
  "ds_read_b128 a[200:203], v222 offset:16384\n\t" \
  "v_add_u32 v215, 98304, v213\n\t" \
  "v_mfma_f32_16x16x32_bf16 a[64:67], a[164:167], v[132:135], a[64:67]\n\t" \
  "v_exp_f32 v96, v96\n\t" \
  "v_mfma_f32_16x16x32_bf16 a[80:83], a[168:171], v[128:131], a[80:83]\n\t" \
  "v_exp_f32 v97, v97\n\t" \
  "v_mfma_f32_16x16x32_bf16 a[80:83], a[172:175], v[132:135], a[80:83]\n\t" \
  "v_exp_f32 v98, v98\n\t" \
  "v_mfma_f32_16x16x32_bf16 a[96:99], a[176:179], v[128:131], a[96:99]\n\t" \
  STA("v_writelane_b32 %[stv], s94, 40\n\t") \
  STA("v_writelane_b32 %[stv], s92, 41\n\t") \
  "ds_read_b128 a[204:207], v223 offset:16384\n\t" \
  "v_mov_b32 v243, %[tag]\n\t" \
  "v_mfma_f32_16x16x32_bf16 a[96:99], a[180:183], v[132:135], a[96:99]\n\t" \
  "v_exp_f32 v99, v99\n\t" \
  "v_mfma_f32_16x16x32_bf16 a[112:115], a[184:187], v[128:131], a[112:115]\n\t" \
  "v_exp_f32 v100, v100\n\t" \
  "v_mfma_f32_16x16x32_bf16 a[112:115], a[188:191], v[132:135], a[112:115]\n\t" \
  "ds_read_b128 a[208:211], v220 offset:20480\n\t" \
  "v_lshrrev_b32 v254, 3, v233\n\t" \
  "v_mfma_f32_16x16x32_bf16 v[160:163], v[216:219], v[128:131], v[160:163]\n\t" \
  "ds_read_b128 a[212:215], v221 offset:20480\n\t" \
  "v_min_u32 v254, 1, v254\n\t" \
  "v_mfma_f32_16x16x32_bf16 v[160:163], v[216:219], v[132:135], v[160:163]\n\t" \
  "ds_read_b128 a[216:219], v222 offset:20480\n\t" \
  "v_lshlrev_b32 v254, 31, v254\n\t" \
  "v_mfma_f32_16x16x32_bf16 a[4:7], a[128:131], v[136:139], a[4:7]\n\t" \
  "ds_read_b128 a[220:223], v223 offset:20480\n\t" \
  "v_lshl_or_b32 v254, v233, 9, v254\n\t" \
  "v_mfma_f32_16x16x32_bf16 a[4:7], a[132:135], v[140:143], a[4:7]\n\t" \
  "s_lshr_b32 m0, %[hl18], 18\n\t" \
  "s_brev_b32 s98, 1\n\t" \
  "v_writelane_b32 v254, s98, m0\n\t" \
  "v_mfma_f32_16x16x32_bf16 a[20:23], a[136:139], v[136:139], a[20:23]\n\t" \
  "s_add_u32 m0, %[m], 49152\n\t" \
  "v_exp_f32 v101, v101\n\t" \
  "v_mfma_f32_16x16x32_bf16 a[20:23], a[140:143], v[140:143], a[20:23]\n\t" \
  "s_add_u32 s96, %[sofk], 98304\n\t" \
  "v_exp_f32 v102, v102\n\t" \
  "v_mfma_f32_16x16x32_bf16 a[36:39], a[144:147], v[136:139], a[36:39]\n\t" \
  "v_exp_f32 v103, v103\n\t" \
  "v_mfma_f32_16x16x32_bf16 a[36:39], a[148:151], v[140:143], a[36:39]\n\t" \
  "v_exp_f32 v104, v104\n\t" \
  "v_mfma_f32_16x16x32_bf16 a[52:55], a[152:155], v[136:139], a[52:55]\n\t" \
  "v_exp_f32 v105, v105\n\t" \
  "v_mfma_f32_16x16x32_bf16 a[52:55], a[156:159], v[140:143], a[52:55]\n\t" \
  "v_exp_f32 v106, v106\n\t" \
  "v_mfma_f32_16x16x32_bf16 a[68:71], a[160:163], v[136:139], a[68:71]\n\t" \
  "v_exp_f32 v107, v107\n\t" \
  "v_mfma_f32_16x16x32_bf16 a[68:71], a[164:167], v[140:143], a[68:71]\n\t" \
  "buffer_load_dwordx4 v224, %[sK], s96 offen offset:0 lds\n\t" \
  "v_mfma_f32_16x16x32_bf16 a[84:87], a[168:171], v[136:139], a[84:87]\n\t" \
  "ds_read_b128 a[224:227], v220 offset:24576\n\t" \
  "v_cvt_pk_bf16_f32 v128, v0, v1\n\t" \
  "v_mfma_f32_16x16x32_bf16 a[84:87], a[172:175], v[140:143], a[84:87]\n\t" \
  "v_exp_f32 v108, v108\n\t" \
  "v_mfma_f32_16x16x32_bf16 a[100:103], a[176:179], v[136:139], a[100:103]\n\t" \
  "v_exp_f32 v109, v109\n\t" \
  "v_mfma_f32_16x16x32_bf16 a[100:103], a[180:183], v[140:143], a[100:103]\n\t" \
  "buffer_load_dwordx4 v225, %[sK], s96 offen offset:1024 lds\n\t" \
  "v_mfma_f32_16x16x32_bf16 a[116:119], a[184:187], v[136:139], a[116:119]\n\t" \
  "v_exp_f32 v110, v110\n\t" \
  "v_mfma_f32_16x16x32_bf16 a[116:119], a[188:191], v[140:143], a[116:119]\n\t" \
  "v_exp_f32 v111, v111\n\t" \
  "v_mfma_f32_16x16x32_bf16 v[164:167], v[216:219], v[136:139], v[164:167]\n\t" \
  "buffer_load_dwordx4 v226, %[sK], s96 offen offset:2048 lds\n\t" \
  "v_mfma_f32_16x16x32_bf16 v[164:167], v[216:219], v[140:143], v[164:167]\n\t" \
  "ds_read_b128 a[228:231], v221 offset:24576\n\t" \
  "v_cvt_pk_bf16_f32 v129, v2, v3\n\t" \
  "v_mfma_f32_16x16x32_bf16 a[8:11], a[128:131], v[144:147], a[8:11]\n\t" \
  "ds_read_b128 a[232:235], v222 offset:24576\n\t" \
  "v_cvt_pk_bf16_f32 v130, v4, v5\n\t" \
  "v_mfma_f32_16x16x32_bf16 a[8:11], a[132:135], v[148:151], a[8:11]\n\t" \
  "ds_read_b128 a[236:239], v223 offset:24576\n\t" \
  "v_cvt_pk_bf16_f32 v131, v6, v7\n\t" \
  "v_mfma_f32_16x16x32_bf16 a[24:27], a[136:139], v[144:147], a[24:27]\n\t" \
  "buffer_load_dwordx4 v227, %[sK], s96 offen offset:3072 lds\n\t" \
  "v_mfma_f32_16x16x32_bf16 a[24:27], a[140:143], v[148:151], a[24:27]\n\t" \
  "ds_read_b128 a[240:243], v220 offset:28672\n\t" \
  "v_cvt_pk_bf16_f32 v132, v8, v9\n\t" \
  "v_mfma_f32_16x16x32_bf16 a[40:43], a[144:147], v[144:147], a[40:43]\n\t" \
  "ds_read_b128 a[244:247], v221 offset:28672\n\t" \
  "v_cvt_pk_bf16_f32 v133, v10, v11\n\t" \
  "v_mfma_f32_16x16x32_bf16 a[40:43], a[148:151], v[148:151], a[40:43]\n\t" \
  "s_add_u32 m0, %[m], 32768\n\t" \
  "s_mov_b32 s96, 12288\n\t" \
  "v_cvt_pk_bf16_f32 v134, v12, v13\n\t" \
  "v_mfma_f32_16x16x32_bf16 a[56:59], a[152:155], v[144:147], a[56:59]\n\t" \
  "buffer_load_dwordx4 v232, %[sG], s96 offen offset:2048 lds\n\t" \
  "v_mfma_f32_16x16x32_bf16 a[56:59], a[156:159], v[148:151], a[56:59]\n\t" \
  "s_add_u32 m0, %[m], 147456\n\t" \
  "ds_read_b128 a[248:251], v222 offset:28672\n\t" \
  "v_cvt_pk_bf16_f32 v135, v14, v15\n\t" \
  "v_mfma_f32_16x16x32_bf16 a[72:75], a[160:163], v[144:147], a[72:75]\n\t" \
  "s_add_u32 s96, %[sofk], 81920\n\t" \
  "v_cvt_pk_bf16_f32 v136, v32, v33\n\t" \
  "v_cvt_pk_bf16_f32 v137, v34, v35\n\t" \
  "v_mfma_f32_16x16x32_bf16 a[72:75], a[164:167], v[148:151], a[72:75]\n\t" \
  "v_cvt_pk_bf16_f32 v138, v36, v37\n\t" \
  "v_cvt_pk_bf16_f32 v139, v38, v39\n\t" \
  "v_mfma_f32_16x16x32_bf16 a[88:91], a[168:171], v[144:147], a[88:91]\n\t" \
  "v_cvt_pk_bf16_f32 v140, v40, v41\n\t" \
  "v_cvt_pk_bf16_f32 v141, v42, v43\n\t" \
  "v_mfma_f32_16x16x32_bf16 a[88:91], a[172:175], v[148:151], a[88:91]\n\t" \
  "ds_read_b128 a[252:255], v223 offset:28672\n\t" \
  "v_cvt_pk_bf16_f32 v142, v44, v45\n\t" \
  "v_mfma_f32_16x16x32_bf16 a[104:107], a[176:179], v[144:147], a[104:107]\n\t" \
  "v_cvt_pk_bf16_f32 v143, v46, v47\n\t" \
  "v_mfma_f32_16x16x32_bf16 a[104:107], a[180:183], v[148:151], a[104:107]\n\t" \
  "v_mfma_f32_16x16x32_bf16 a[120:123], a[184:187], v[144:147], a[120:123]\n\t" \
  "v_mfma_f32_16x16x32_bf16 a[120:123], a[188:191], v[148:151], a[120:123]\n\t" \
  "buffer_load_dwordx4 v230, %[sV], s96 offen offset:0 lds\n\t" \
  "v_mfma_f32_16x16x32_bf16 v[168:171], v[216:219], v[144:147], v[168:171]\n\t" \
  "v_mfma_f32_16x16x32_bf16 v[168:171], v[216:219], v[148:151], v[168:171]\n\t" \
  "v_cvt_pk_bf16_f32 v144, v64, v65\n\t" \
  "v_cvt_pk_bf16_f32 v145, v66, v67\n\t" \
  "v_mfma_f32_16x16x32_bf16 a[12:15], a[128:131], v[152:155], a[12:15]\n\t" \
  "v_cvt_pk_bf16_f32 v146, v68, v69\n\t" \
  "v_cvt_pk_bf16_f32 v147, v70, v71\n\t" \
  "v_mfma_f32_16x16x32_bf16 a[12:15], a[132:135], v[156:159], a[12:15]\n\t" \
  "buffer_load_dwordx4 v231, %[sV], s96 offen offset:1024 lds\n\t" \
  "v_mfma_f32_16x16x32_bf16 a[28:31], a[136:139], v[152:155], a[28:31]\n\t" \
  "v_cvt_pk_bf16_f32 v148, v72, v73\n\t" \
  "v_cvt_pk_bf16_f32 v149, v74, v75\n\t" \
  "v_mfma_f32_16x16x32_bf16 a[28:31], a[140:143], v[156:159], a[28:31]\n\t" \
  "v_cvt_pk_bf16_f32 v150, v76, v77\n\t" \
  "v_cvt_pk_bf16_f32 v151, v78, v79\n\t" \
  "v_mfma_f32_16x16x32_bf16 a[44:47], a[144:147], v[152:155], a[44:47]\n\t" \
  "buffer_load_dwordx4 v230, %[sV], s96 offen offset:2048 lds\n\t" \
  "v_mfma_f32_16x16x32_bf16 a[44:47], a[148:151], v[156:159], a[44:47]\n\t" \
  "v_mfma_f32_16x16x32_bf16 a[60:63], a[152:155], v[152:155], a[60:63]\n\t" \
  "v_mfma_f32_16x16x32_bf16 a[60:63], a[156:159], v[156:159], a[60:63]\n\t" \
  "v_mfma_f32_16x16x32_bf16 a[76:79], a[160:163], v[152:155], a[76:79]\n\t" \
  "buffer_load_dwordx4 v231, %[sV], s96 offen offset:3072 lds\n\t" \
  "v_mfma_f32_16x16x32_bf16 a[76:79], a[164:167], v[156:159], a[76:79]\n\t" \
  "s_add_u32 m0, %[m], 32768\n\t" \
  "s_mov_b32 s96, 18432\n\t" \
  "v_mfma_f32_16x16x32_bf16 a[92:95], a[168:171], v[152:155], a[92:95]\n\t" \
  "v_mfma_f32_16x16x32_bf16 a[92:95], a[172:175], v[156:159], a[92:95]\n\t" \
  "v_mfma_f32_16x16x32_bf16 a[108:111], a[176:179], v[152:155], a[108:111]\n\t" \
  "buffer_load_dwordx4 v232, %[sG], s96 offen offset:3072 lds\n\t" \
  "v_mfma_f32_16x16x32_bf16 a[108:111], a[180:183], v[156:159], a[108:111]\n\t" \
  "v_mfma_f32_16x16x32_bf16 a[124:127], a[184:187], v[152:155], a[124:127]\n\t" \
  "v_mfma_f32_16x16x32_bf16 a[124:127], a[188:191], v[156:159], a[124:127]\n\t" \
  "s_add_u32 s98, %[pf4], 43008\n\t" \
  "v_mfma_f32_16x16x32_bf16 v[172:175], v[216:219], v[152:155], v[172:175]\n\t" \
  "buffer_load_dword %[pfd], v245, %[sG], s98 offen\n\t" \
  "v_mfma_f32_16x16x32_bf16 v[172:175], v[216:219], v[156:159], v[172:175]\n\t" \
  "s_waitcnt lgkmcnt(0)\n\t" \
  "v_cvt_pk_bf16_f32 v152, v96, v97\n\t" \
  "v_cvt_pk_bf16_f32 v153, v98, v99\n\t" \
  STA("s_memrealtime s[94:95]\n\t") \
  STA("s_memtime s[92:93]\n\t") \
  "v_mfma_f32_16x16x32_bf16 v[0:3], a[192:195], v[16:19], v[176:179]\n\t" \
  "ds_read_b64_tr_b16 a[128:129], v228 offset:0\n\t" \
  "v_cvt_pk_bf16_f32 v154, v100, v101\n\t" \
  "v_mfma_f32_16x16x32_bf16 v[0:3], a[196:199], v[20:23], v[0:3]\n\t" \
  "ds_read_b64_tr_b16 a[130:131], v228 offset:4096\n\t" \
  "v_cvt_pk_bf16_f32 v155, v102, v103\n\t" \
  "v_mfma_f32_16x16x32_bf16 v[0:3], a[200:203], v[24:27], v[0:3]\n\t" \
  "ds_read_b64_tr_b16 a[132:133], v228 offset:8192\n\t" \
  "v_lshrrev_b32 v252, 3, v233\n\t" \
  "v_mfma_f32_16x16x32_bf16 v[0:3], a[204:207], v[28:31], v[0:3]\n\t" \
  "ds_read_b64_tr_b16 a[134:135], v228 offset:12288\n\t" \
  "v_cvt_pk_bf16_f32 v156, v104, v105\n\t" \
  "v_mfma_f32_16x16x32_bf16 v[4:7], a[220:223], v[28:31], v[176:179]\n\t" \
  "ds_read_b64_tr_b16 a[136:137], v229 offset:0\n\t" \
  "v_cvt_pk_bf16_f32 v157, v106, v107\n\t" \
  "v_mfma_f32_16x16x32_bf16 v[4:7], a[216:219], v[24:27], v[4:7]\n\t" \
  "ds_read_b64_tr_b16 a[138:139], v229 offset:4096\n\t" \
  "v_cvt_pk_bf16_f32 v158, v108, v109\n\t" \
  "v_mfma_f32_16x16x32_bf16 v[4:7], a[212:215], v[20:23], v[4:7]\n\t" \
  "v_exp_f32 v0, v0\n\t" \
  "v_mfma_f32_16x16x32_bf16 v[4:7], a[208:211], v[16:19], v[4:7]\n\t" \
  "v_exp_f32 v1, v1\n\t" \
  "v_mfma_f32_16x16x32_bf16 v[8:11], a[224:227], v[16:19], v[176:179]\n\t" \
  "v_exp_f32 v2, v2\n\t" \
  "v_mfma_f32_16x16x32_bf16 v[8:11], a[228:231], v[20:23], v[8:11]\n\t" \
  "v_exp_f32 v3, v3\n\t" \
  "v_mfma_f32_16x16x32_bf16 v[8:11], a[232:235], v[24:27], v[8:11]\n\t" \
  "ds_read_b64_tr_b16 a[140:141], v229 offset:8192\n\t" \
  "v_mul_u32_u24 v247, 98176, v252\n\t" \
  "v_mfma_f32_16x16x32_bf16 v[8:11], a[236:239], v[28:31], v[8:11]\n\t" \
  "s_add_u32 s98, %[pf2], 24576\n\t" \
  "v_exp_f32 v4, v4\n\t" \
  "v_mfma_f32_16x16x32_bf16 v[12:15], a[252:255], v[28:31], v[176:179]\n\t" \
  STA("v_writelane_b32 %[stv], s94, 42\n\t") \
  STA("v_writelane_b32 %[stv], s92, 43\n\t") \
  "buffer_load_dword %[pfd], v202, %[sM], s98 offen\n\t" \
  "v_mfma_f32_16x16x32_bf16 v[12:15], a[248:251], v[24:27], v[12:15]\n\t" \
  "ds_read_b64_tr_b16 a[142:143], v229 offset:12288\n\t" \
  "v_cvt_pk_bf16_f32 v159, v110, v111\n\t" \
  "v_mfma_f32_16x16x32_bf16 v[12:15], a[244:247], v[20:23], v[12:15]\n\t" \
  "ds_read_b64_tr_b16 a[144:145], v228 offset:256\n\t" \
  "v_lshl_add_u32 v247, v233, 4, v247\n\t" \
  "v_mfma_f32_16x16x32_bf16 v[12:15], a[240:243], v[16:19], v[12:15]\n\t" \
  "v_exp_f32 v5, v5\n\t" \
  "v_mfma_f32_16x16x32_bf16 v[32:35], a[192:195], v[48:51], v[180:183]\n\t" \
  "ds_read_b64_tr_b16 a[146:147], v228 offset:4352\n\t" \
  "v_lshlrev_b32 v252, 4, v252\n\t" \
  "v_mfma_f32_16x16x32_bf16 v[32:35], a[196:199], v[52:55], v[32:35]\n\t" \
  "v_exp_f32 v6, v6\n\t" \
  "v_mfma_f32_16x16x32_bf16 v[32:35], a[200:203], v[56:59], v[32:35]\n\t" \
  "v_exp_f32 v7, v7\n\t" \
  "v_mfma_f32_16x16x32_bf16 v[32:35], a[204:207], v[60:63], v[32:35]\n\t" \
  "v_exp_f32 v8, v8\n\t" \
  "v_mfma_f32_16x16x32_bf16 v[36:39], a[220:223], v[60:63], v[180:183]\n\t" \
  "ds_read_b64_tr_b16 a[148:149], v228 offset:8448\n\t" \
  "ds_read_b64_tr_b16 a[150:151], v228 offset:12544\n\t" \
  "v_mfma_f32_16x16x32_bf16 v[36:39], a[216:219], v[56:59], v[36:39]\n\t" \
  "v_exp_f32 v9, v9\n\t" \
  "v_mfma_f32_16x16x32_bf16 v[36:39], a[212:215], v[52:55], v[36:39]\n\t" \
  "v_exp_f32 v10, v10\n\t" \
  "v_mfma_f32_16x16x32_bf16 v[36:39], a[208:211], v[48:51], v[36:39]\n\t" \
  "ds_read_b64_tr_b16 a[152:153], v229 offset:256\n\t" \
  "ds_read_b64_tr_b16 a[154:155], v229 offset:4352\n\t" \
  "v_mfma_f32_16x16x32_bf16 v[40:43], a[224:227], v[48:51], v[180:183]\n\t" \
  "v_exp_f32 v11, v11\n\t" \
  "v_mfma_f32_16x16x32_bf16 v[40:43], a[228:231], v[52:55], v[40:43]\n\t" \
  "v_exp_f32 v12, v12\n\t" \
  "v_mfma_f32_16x16x32_bf16 v[40:43], a[232:235], v[56:59], v[40:43]\n\t" \
  "v_exp_f32 v13, v13\n\t" \
  "v_mfma_f32_16x16x32_bf16 v[40:43], a[236:239], v[60:63], v[40:43]\n\t" \
  "ds_read_b64_tr_b16 a[156:157], v229 offset:8448\n\t" \
  "ds_read_b64_tr_b16 a[158:159], v229 offset:12544\n\t" \
  "v_mfma_f32_16x16x32_bf16 v[44:47], a[252:255], v[60:63], v[180:183]\n\t" \
  "v_exp_f32 v14, v14\n\t" \
  "v_mfma_f32_16x16x32_bf16 v[44:47], a[248:251], v[56:59], v[44:47]\n\t" \
  "v_exp_f32 v15, v15\n\t" \
  "v_mfma_f32_16x16x32_bf16 v[44:47], a[244:247], v[52:55], v[44:47]\n\t" \
  "ds_read_b64_tr_b16 a[160:161], v228 offset:512\n\t" \
  "ds_read_b64_tr_b16 a[162:163], v228 offset:4608\n\t" \
  "v_mfma_f32_16x16x32_bf16 v[44:47], a[240:243], v[48:51], v[44:47]\n\t" \
  "v_exp_f32 v32, v32\n\t" \
  "v_mfma_f32_16x16x32_bf16 v[64:67], a[192:195], v[80:83], v[184:187]\n\t" \
  "v_exp_f32 v33, v33\n\t" \
  "v_mfma_f32_16x16x32_bf16 v[64:67], a[196:199], v[84:87], v[64:67]\n\t" \
  "v_exp_f32 v34, v34\n\t" \
  "v_mfma_f32_16x16x32_bf16 v[64:67], a[200:203], v[88:91], v[64:67]\n\t" \
  "ds_read_b64_tr_b16 a[164:165], v228 offset:8704\n\t" \
  "ds_read_b64_tr_b16 a[166:167], v228 offset:12800\n\t" \
  "v_mfma_f32_16x16x32_bf16 v[64:67], a[204:207], v[92:95], v[64:67]\n\t" \
  "v_exp_f32 v35, v35\n\t" \
  "v_mfma_f32_16x16x32_bf16 v[68:71], a[220:223], v[92:95], v[184:187]\n\t" \
  "v_exp_f32 v36, v36\n\t" \
  "v_mfma_f32_16x16x32_bf16 v[68:71], a[216:219], v[88:91], v[68:71]\n\t" \
  "ds_read_b64_tr_b16 a[168:169], v229 offset:512\n\t" \
  "ds_read_b64_tr_b16 a[170:171], v229 offset:4608\n\t" \
  "v_mfma_f32_16x16x32_bf16 v[68:71], a[212:215], v[84:87], v[68:71]\n\t" \
  "v_exp_f32 v37, v37\n\t" \
  "v_mfma_f32_16x16x32_bf16 v[68:71], a[208:211], v[80:83], v[68:71]\n\t" \
  "v_exp_f32 v38, v38\n\t" \
  "v_mfma_f32_16x16x32_bf16 v[72:75], a[224:227], v[80:83], v[184:187]\n\t" \
  "v_exp_f32 v39, v39\n\t" \
  "v_mfma_f32_16x16x32_bf16 v[72:75], a[228:231], v[84:87], v[72:75]\n\t" \
  "ds_read_b64_tr_b16 a[172:173], v229 offset:8704\n\t" \
  "ds_read_b64_tr_b16 a[174:175], v229 offset:12800\n\t" \
  "v_mfma_f32_16x16x32_bf16 v[72:75], a[232:235], v[88:91], v[72:75]\n\t" \
  "v_exp_f32 v40, v40\n\t" \
  "v_mfma_f32_16x16x32_bf16 v[72:75], a[236:239], v[92:95], v[72:75]\n\t" \
  "v_exp_f32 v41, v41\n\t" \
  "v_mfma_f32_16x16x32_bf16 v[76:79], a[252:255], v[92:95], v[184:187]\n\t" \
  "ds_read_b64_tr_b16 a[176:177], v228 offset:768\n\t" \
  "ds_read_b64_tr_b16 a[178:179], v228 offset:4864\n\t" \
  "v_mfma_f32_16x16x32_bf16 v[76:79], a[248:251], v[88:91], v[76:79]\n\t" \
  "v_exp_f32 v42, v42\n\t" \
  "v_mfma_f32_16x16x32_bf16 v[76:79], a[244:247], v[84:87], v[76:79]\n\t" \
  "v_exp_f32 v43, v43\n\t" \
  "v_mfma_f32_16x16x32_bf16 v[76:79], a[240:243], v[80:83], v[76:79]\n\t" \
  "v_exp_f32 v44, v44\n\t" \
  "v_mfma_f32_16x16x32_bf16 v[96:99], a[192:195], v[112:115], v[188:191]\n\t" \
  "ds_read_b64_tr_b16 a[180:181], v228 offset:8960\n\t" \
  "ds_read_b64_tr_b16 a[182:183], v228 offset:13056\n\t" \
  "v_mfma_f32_16x16x32_bf16 v[96:99], a[196:199], v[116:119], v[96:99]\n\t" \
  "v_exp_f32 v45, v45\n\t" \
  "v_mfma_f32_16x16x32_bf16 v[96:99], a[200:203], v[120:123], v[96:99]\n\t" \
  "v_exp_f32 v46, v46\n\t" \
  "v_mfma_f32_16x16x32_bf16 v[96:99], a[204:207], v[124:127], v[96:99]\n\t" \
  "ds_read_b64_tr_b16 a[184:185], v229 offset:768\n\t" \
  "ds_read_b64_tr_b16 a[186:187], v229 offset:4864\n\t" \
  "v_mfma_f32_16x16x32_bf16 v[100:103], a[220:223], v[124:127], v[188:191]\n\t" \
  "v_exp_f32 v47, v47\n\t" \
  "v_mfma_f32_16x16x32_bf16 v[100:103], a[216:219], v[120:123], v[100:103]\n\t" \
  "v_exp_f32 v64, v64\n\t" \
  "v_mfma_f32_16x16x32_bf16 v[100:103], a[212:215], v[116:119], v[100:103]\n\t" \
  "v_exp_f32 v65, v65\n\t" \
  "v_mfma_f32_16x16x32_bf16 v[100:103], a[208:211], v[112:115], v[100:103]\n\t" \
  "ds_read_b64_tr_b16 a[188:189], v229 offset:8960\n\t" \
  "ds_read_b64_tr_b16 a[190:191], v229 offset:13056\n\t" \
  "v_mfma_f32_16x16x32_bf16 v[104:107], a[224:227], v[112:115], v[188:191]\n\t" \
  "v_exp_f32 v66, v66\n\t" \
  "v_mfma_f32_16x16x32_bf16 v[104:107], a[228:231], v[116:119], v[104:107]\n\t" \
  "v_exp_f32 v67, v67\n\t" \
  "v_mfma_f32_16x16x32_bf16 v[104:107], a[232:235], v[120:123], v[104:107]\n\t" \
  "v_exp_f32 v68, v68\n\t" \
  "v_mfma_f32_16x16x32_bf16 v[104:107], a[236:239], v[124:127], v[104:107]\n\t" \
  "v_exp_f32 v69, v69\n\t" \
  "v_mfma_f32_16x16x32_bf16 v[108:111], a[252:255], v[124:127], v[188:191]\n\t" \
  "v_exp_f32 v70, v70\n\t" \
  "v_mfma_f32_16x16x32_bf16 v[108:111], a[248:251], v[120:123], v[108:111]\n\t" \
  "v_exp_f32 v71, v71\n\t" \
  "v_mfma_f32_16x16x32_bf16 v[108:111], a[244:247], v[116:119], v[108:111]\n\t" \
  "v_exp_f32 v72, v72\n\t" \
  "v_mfma_f32_16x16x32_bf16 v[108:111], a[240:243], v[112:115], v[108:111]\n\t" \
  "s_waitcnt vmcnt(3) lgkmcnt(0)\n\t" \
  "s_barrier\n\t" \
  STA("s_memrealtime s[94:95]\n\t") \
  STA("s_memtime s[92:93]\n\t") \
  "v_mfma_f32_16x16x32_bf16 a[0:3], a[128:131], v[128:131], a[0:3]\n\t" \
  "v_exp_f32 v73, v73\n\t" \
  "v_mfma_f32_16x16x32_bf16 a[0:3], a[132:135], v[132:135], a[0:3]\n\t" \
  "v_exp_f32 v74, v74\n\t" \
  "v_mfma_f32_16x16x32_bf16 a[16:19], a[136:139], v[128:131], a[16:19]\n\t" \
  "ds_read_b128 a[192:195], v220 offset:0\n\t" \
  "ds_read_b128 a[196:199], v221 offset:0\n\t" \
  "v_mfma_f32_16x16x32_bf16 a[16:19], a[140:143], v[132:135], a[16:19]\n\t" \
  "v_exp_f32 v75, v75\n\t" \
  "v_mfma_f32_16x16x32_bf16 a[32:35], a[144:147], v[128:131], a[32:35]\n\t" \
  "v_exp_f32 v76, v76\n\t" \
  "v_mfma_f32_16x16x32_bf16 a[32:35], a[148:151], v[132:135], a[32:35]\n\t" \
  "v_exp_f32 v77, v77\n\t" \
  "v_mfma_f32_16x16x32_bf16 a[48:51], a[152:155], v[128:131], a[48:51]\n\t" \
  "v_exp_f32 v78, v78\n\t" \
  "v_mfma_f32_16x16x32_bf16 a[48:51], a[156:159], v[132:135], a[48:51]\n\t" \
  "v_exp_f32 v79, v79\n\t" \
  "v_mfma_f32_16x16x32_bf16 a[64:67], a[160:163], v[128:131], a[64:67]\n\t" \
  "v_exp_f32 v96, v96\n\t" \
  "v_mfma_f32_16x16x32_bf16 a[64:67], a[164:167], v[132:135], a[64:67]\n\t" \
  "v_exp_f32 v97, v97\n\t" \
  "v_mfma_f32_16x16x32_bf16 a[80:83], a[168:171], v[128:131], a[80:83]\n\t" \
  "ds_read_b128 a[200:203], v222 offset:0\n\t" \
  "ds_read_b128 a[204:207], v223 offset:0\n\t" \
  "v_mfma_f32_16x16x32_bf16 a[80:83], a[172:175], v[132:135], a[80:83]\n\t" \
  "v_exp_f32 v98, v98\n\t" \
  "v_mfma_f32_16x16x32_bf16 a[96:99], a[176:179], v[128:131], a[96:99]\n\t" \
  STA("v_writelane_b32 %[stv], s94, 44\n\t") \
  STA("v_writelane_b32 %[stv], s92, 45\n\t") \
  "v_exp_f32 v99, v99\n\t" \
  "v_mfma_f32_16x16x32_bf16 a[96:99], a[180:183], v[132:135], a[96:99]\n\t" \
  "v_exp_f32 v100, v100\n\t" \
  "v_mfma_f32_16x16x32_bf16 a[112:115], a[184:187], v[128:131], a[112:115]\n\t" \
  "v_exp_f32 v101, v101\n\t" \
  "v_mfma_f32_16x16x32_bf16 a[112:115], a[188:191], v[132:135], a[112:115]\n\t" \
  "v_exp_f32 v102, v102\n\t" \
  "v_mfma_f32_16x16x32_bf16 v[160:163], v[216:219], v[128:131], v[160:163]\n\t" \
  "v_exp_f32 v103, v103\n\t" \
  "v_mfma_f32_16x16x32_bf16 v[160:163], v[216:219], v[132:135], v[160:163]\n\t" \
  "ds_read_b128 a[208:211], v220 offset:4096\n\t" \
  "v_cvt_pk_bf16_f32 v128, v0, v1\n\t" \
  "v_mfma_f32_16x16x32_bf16 a[4:7], a[128:131], v[136:139], a[4:7]\n\t" \
  "v_exp_f32 v104, v104\n\t" \
  ".Lhs8:\n\ts_cbranch_execz .Lhs8n\n\t" \
  "v_mfma_f32_16x16x32_bf16 a[4:7], a[132:135], v[140:143], a[4:7]\n\t" \
  "ds_read_b128 a[212:215], v221 offset:4096\n\t" \
  "v_cvt_pk_bf16_f32 v129, v2, v3\n\t" \
  "v_mfma_f32_16x16x32_bf16 a[20:23], a[136:139], v[136:139], a[20:23]\n\t" \
  "s_add_u32 m0, %[m], 65536\n\t" \
  "v_exp_f32 v105, v105\n\t" \
  "v_mfma_f32_16x16x32_bf16 a[20:23], a[140:143], v[140:143], a[20:23]\n\t" \
  "s_add_u32 s96, %[sofk], 114688\n\t" \
  "v_exp_f32 v106, v106\n\t" \
  "v_mfma_f32_16x16x32_bf16 a[36:39], a[144:147], v[136:139], a[36:39]\n\t" \
  "v_exp_f32 v107, v107\n\t" \
  "v_mfma_f32_16x16x32_bf16 a[36:39], a[148:151], v[140:143], a[36:39]\n\t" \
  "ds_read_b128 a[216:219], v222 offset:4096\n\t" \
  "v_cvt_pk_bf16_f32 v130, v4, v5\n\t" \
  "v_mfma_f32_16x16x32_bf16 a[52:55], a[152:155], v[136:139], a[52:55]\n\t" \
  "v_exp_f32 v108, v108\n\t" \
  "v_mfma_f32_16x16x32_bf16 a[52:55], a[156:159], v[140:143], a[52:55]\n\t" \
  "v_exp_f32 v109, v109\n\t" \
  "v_mfma_f32_16x16x32_bf16 a[68:71], a[160:163], v[136:139], a[68:71]\n\t" \
  "v_exp_f32 v110, v110\n\t" \
  "v_mfma_f32_16x16x32_bf16 a[68:71], a[164:167], v[140:143], a[68:71]\n\t" \
  "buffer_load_dwordx4 v224, %[sK], s96 offen offset:0 lds\n\t" \
  "v_mfma_f32_16x16x32_bf16 a[84:87], a[168:171], v[136:139], a[84:87]\n\t" \
  "ds_read_b128 a[220:223], v223 offset:4096\n\t" \
  "v_cvt_pk_bf16_f32 v131, v6, v7\n\t" \
  "v_mfma_f32_16x16x32_bf16 a[84:87], a[172:175], v[140:143], a[84:87]\n\t" \
  "v_exp_f32 v111, v111\n\t" \
  "v_mfma_f32_16x16x32_bf16 a[100:103], a[176:179], v[136:139], a[100:103]\n\t" \
  "ds_read_b128 a[224:227], v220 offset:8192\n\t" \
  "v_cvt_pk_bf16_f32 v132, v8, v9\n\t" \
  "v_mfma_f32_16x16x32_bf16 a[100:103], a[180:183], v[140:143], a[100:103]\n\t" \
  "buffer_load_dwordx4 v225, %[sK], s96 offen offset:1024 lds\n\t" \
  "v_mfma_f32_16x16x32_bf16 a[116:119], a[184:187], v[136:139], a[116:119]\n\t" \
  "ds_read_b128 a[228:231], v221 offset:8192\n\t" \
  "v_cvt_pk_bf16_f32 v133, v10, v11\n\t" \
  "v_mfma_f32_16x16x32_bf16 a[116:119], a[188:191], v[140:143], a[116:119]\n\t" \
  "ds_read_b128 a[232:235], v222 offset:8192\n\t" \
  "v_cvt_pk_bf16_f32 v134, v12, v13\n\t" \
  "v_mfma_f32_16x16x32_bf16 v[164:167], v[216:219], v[136:139], v[164:167]\n\t" \
  "buffer_load_dwordx4 v226, %[sK], s96 offen offset:2048 lds\n\t" \
  "v_mfma_f32_16x16x32_bf16 v[164:167], v[216:219], v[140:143], v[164:167]\n\t" \
  "ds_read_b128 a[236:239], v223 offset:8192\n\t" \
  "v_cvt_pk_bf16_f32 v135, v14, v15\n\t" \
  "v_mfma_f32_16x16x32_bf16 a[8:11], a[128:131], v[144:147], a[8:11]\n\t" \
  "ds_read_b128 a[240:243], v220 offset:12288\n\t" \
  "v_cvt_pk_bf16_f32 v136, v32, v33\n\t" \
  "v_mfma_f32_16x16x32_bf16 a[8:11], a[132:135], v[148:151], a[8:11]\n\t" \
  "v_cvt_pk_bf16_f32 v137, v34, v35\n\t" \
  "v_cvt_pk_bf16_f32 v138, v36, v37\n\t" \
  "v_mfma_f32_16x16x32_bf16 a[24:27], a[136:139], v[144:147], a[24:27]\n\t" \
  "buffer_load_dwordx4 v227, %[sK], s96 offen offset:3072 lds\n\t" \
  "v_mfma_f32_16x16x32_bf16 a[24:27], a[140:143], v[148:151], a[24:27]\n\t" \
  "ds_read_b128 a[244:247], v221 offset:12288\n\t" \
  "v_cvt_pk_bf16_f32 v139, v38, v39\n\t" \
  "v_mfma_f32_16x16x32_bf16 a[40:43], a[144:147], v[144:147], a[40:43]\n\t" \
  "s_add_u32 m0, %[m], 98304\n\t" \
  "s_mov_b32 s96, 28672\n\t" \
  "v_cvt_pk_bf16_f32 v140, v40, v41\n\t" \
  "v_mfma_f32_16x16x32_bf16 a[40:43], a[148:151], v[148:151], a[40:43]\n\t" \
  "v_cvt_pk_bf16_f32 v141, v42, v43\n\t" \
  "v_cvt_pk_bf16_f32 v142, v44, v45\n\t" \
  "v_mfma_f32_16x16x32_bf16 a[56:59], a[152:155], v[144:147], a[56:59]\n\t" \
  "buffer_load_dwordx4 v232, %[sG], s96 offen offset:0 lds\n\t" \
  "v_mfma_f32_16x16x32_bf16 a[56:59], a[156:159], v[148:151], a[56:59]\n\t" \
  "s_add_u32 m0, %[m], 81920\n\t" \
  "ds_read_b128 a[248:251], v222 offset:12288\n\t" \
  "v_cvt_pk_bf16_f32 v143, v46, v47\n\t" \
  "v_mfma_f32_16x16x32_bf16 a[72:75], a[160:163], v[144:147], a[72:75]\n\t" \
  "s_add_u32 s96, %[sofk], 98304\n\t" \
  "v_mfma_f32_16x16x32_bf16 a[72:75], a[164:167], v[148:151], a[72:75]\n\t" \
  "v_mfma_f32_16x16x32_bf16 a[88:91], a[168:171], v[144:147], a[88:91]\n\t" \
  "v_mfma_f32_16x16x32_bf16 a[88:91], a[172:175], v[148:151], a[88:91]\n\t" \
  "v_mfma_f32_16x16x32_bf16 a[104:107], a[176:179], v[144:147], a[104:107]\n\t" \
  "v_mfma_f32_16x16x32_bf16 a[104:107], a[180:183], v[148:151], a[104:107]\n\t" \
  "v_mfma_f32_16x16x32_bf16 a[120:123], a[184:187], v[144:147], a[120:123]\n\t" \
  "v_mfma_f32_16x16x32_bf16 a[120:123], a[188:191], v[148:151], a[120:123]\n\t" \
  "buffer_load_dwordx4 v230, %[sV], s96 offen offset:0 lds\n\t" \
  "v_mfma_f32_16x16x32_bf16 v[168:171], v[216:219], v[144:147], v[168:171]\n\t" \
  "v_mfma_f32_16x16x32_bf16 v[168:171], v[216:219], v[148:151], v[168:171]\n\t" \
  "ds_read_b128 a[252:255], v223 offset:12288\n\t" \
  "v_cvt_pk_bf16_f32 v144, v64, v65\n\t" \
  "v_mfma_f32_16x16x32_bf16 a[12:15], a[128:131], v[152:155], a[12:15]\n\t" \
  "v_cvt_pk_bf16_f32 v145, v66, v67\n\t" \
  "v_cvt_pk_bf16_f32 v146, v68, v69\n\t" \
  "v_mfma_f32_16x16x32_bf16 a[12:15], a[132:135], v[156:159], a[12:15]\n\t" \
  "buffer_load_dwordx4 v231, %[sV], s96 offen offset:1024 lds\n\t" \
  "v_mfma_f32_16x16x32_bf16 a[28:31], a[136:139], v[152:155], a[28:31]\n\t" \
  "v_cvt_pk_bf16_f32 v147, v70, v71\n\t" \
  "v_cvt_pk_bf16_f32 v148, v72, v73\n\t" \
  "v_mfma_f32_16x16x32_bf16 a[28:31], a[140:143], v[156:159], a[28:31]\n\t" \
  "v_cvt_pk_bf16_f32 v149, v74, v75\n\t" \
  "v_cvt_pk_bf16_f32 v150, v76, v77\n\t" \
  "v_mfma_f32_16x16x32_bf16 a[44:47], a[144:147], v[152:155], a[44:47]\n\t" \
  "buffer_load_dwordx4 v230, %[sV], s96 offen offset:2048 lds\n\t" \
  "v_mfma_f32_16x16x32_bf16 a[44:47], a[148:151], v[156:159], a[44:47]\n\t" \
  "v_cvt_pk_bf16_f32 v151, v78, v79\n\t" \
  "v_mfma_f32_16x16x32_bf16 a[60:63], a[152:155], v[152:155], a[60:63]\n\t" \
  "v_mfma_f32_16x16x32_bf16 a[60:63], a[156:159], v[156:159], a[60:63]\n\t" \
  "v_mfma_f32_16x16x32_bf16 a[76:79], a[160:163], v[152:155], a[76:79]\n\t" \
  "buffer_load_dwordx4 v231, %[sV], s96 offen offset:3072 lds\n\t" \
  "v_mfma_f32_16x16x32_bf16 a[76:79], a[164:167], v[156:159], a[76:79]\n\t" \
  "v_lshrrev_b32 v205, 1, v233\n\t" \
  "v_lshlrev_b32 v205, 2, v205\n\t" \
  "v_mfma_f32_16x16x32_bf16 a[92:95], a[168:171], v[152:155], a[92:95]\n\t" \
  "v_add_u32 v205, 8192, v205\n\t" \
  "v_mfma_f32_16x16x32_bf16 a[92:95], a[172:175], v[156:159], a[92:95]\n\t" \
  "buffer_load_dword v209, v205, %[ws], 0 offen sc1\n\t" \
  "v_mfma_f32_16x16x32_bf16 a[108:111], a[176:179], v[152:155], a[108:111]\n\t" \
  "s_add_u32 m0, %[m], 98304\n\t" \
  "s_mov_b32 s96, 34816\n\t" \
  "v_mfma_f32_16x16x32_bf16 a[108:111], a[180:183], v[156:159], a[108:111]\n\t" \
  "buffer_load_dwordx4 v232, %[sG], s96 offen offset:1024 lds\n\t" \
  "v_mfma_f32_16x16x32_bf16 a[124:127], a[184:187], v[152:155], a[124:127]\n\t" \
  "v_mfma_f32_16x16x32_bf16 a[124:127], a[188:191], v[156:159], a[124:127]\n\t" \
  "s_add_u32 s98, %[pf2], 49152\n\t" \
  "v_mfma_f32_16x16x32_bf16 v[172:175], v[216:219], v[152:155], v[172:175]\n\t" \
  "buffer_load_dword %[pfd], v202, %[sM], s98 offen\n\t" \
  "v_mfma_f32_16x16x32_bf16 v[172:175], v[216:219], v[156:159], v[172:175]\n\t" \
  "s_waitcnt lgkmcnt(0)\n\t" \
  "v_cvt_pk_bf16_f32 v152, v96, v97\n\t" \
  "v_cvt_pk_bf16_f32 v153, v98, v99\n\t" \
  STA("s_memrealtime s[94:95]\n\t") \
  STA("s_memtime s[92:93]\n\t") \
  "v_mfma_f32_16x16x32_bf16 v[0:3], a[192:195], v[16:19], v[176:179]\n\t" \
  "ds_read_b64_tr_b16 a[128:129], v237 offset:0\n\t" \
  "v_cvt_pk_bf16_f32 v154, v100, v101\n\t" \
  "v_mfma_f32_16x16x32_bf16 v[0:3], a[196:199], v[20:23], v[0:3]\n\t" \
  "ds_read_b64_tr_b16 a[130:131], v237 offset:4096\n\t" \
  "v_cvt_pk_bf16_f32 v155, v102, v103\n\t" \
  "v_mfma_f32_16x16x32_bf16 v[0:3], a[200:203], v[24:27], v[0:3]\n\t" \
  "ds_read_b64_tr_b16 a[132:133], v237 offset:8192\n\t" \
  "v_cvt_pk_bf16_f32 v156, v104, v105\n\t" \
  "v_mfma_f32_16x16x32_bf16 v[0:3], a[204:207], v[28:31], v[0:3]\n\t" \
  "ds_read_b64_tr_b16 a[134:135], v237 offset:12288\n\t" \
  "v_cvt_pk_bf16_f32 v157, v106, v107\n\t" \
  "v_mfma_f32_16x16x32_bf16 v[4:7], a[220:223], v[28:31], v[176:179]\n\t" \
  "ds_read_b64_tr_b16 a[136:137], v238 offset:0\n\t" \
  "v_cvt_pk_bf16_f32 v158, v108, v109\n\t" \
  "v_mfma_f32_16x16x32_bf16 v[4:7], a[216:219], v[24:27], v[4:7]\n\t" \
  "ds_read_b64_tr_b16 a[138:139], v238 offset:4096\n\t" \
  "v_cvt_pk_bf16_f32 v159, v110, v111\n\t" \
  "v_mfma_f32_16x16x32_bf16 v[4:7], a[212:215], v[20:23], v[4:7]\n\t" \
  "v_exp_f32 v0, v0\n\t" \
  "v_mfma_f32_16x16x32_bf16 v[4:7], a[208:211], v[16:19], v[4:7]\n\t" \
  "v_exp_f32 v1, v1\n\t" \
  "v_mfma_f32_16x16x32_bf16 v[8:11], a[224:227], v[16:19], v[176:179]\n\t" \
  "v_exp_f32 v2, v2\n\t" \
  "v_mfma_f32_16x16x32_bf16 v[8:11], a[228:231], v[20:23], v[8:11]\n\t" \
  "v_exp_f32 v3, v3\n\t" \
  "v_mfma_f32_16x16x32_bf16 v[8:11], a[232:235], v[24:27], v[8:11]\n\t" \
  "ds_read_b64_tr_b16 a[140:141], v238 offset:8192\n\t" \
  "ds_read_b64_tr_b16 a[142:143], v238 offset:12288\n\t" \
  "v_mfma_f32_16x16x32_bf16 v[8:11], a[236:239], v[28:31], v[8:11]\n\t" \
  "s_add_u32 s98, %[pf2], 73728\n\t" \
  "v_exp_f32 v4, v4\n\t" \
  "v_mfma_f32_16x16x32_bf16 v[12:15], a[252:255], v[28:31], v[176:179]\n\t" \
  STA("v_writelane_b32 %[stv], s94, 46\n\t") \
  STA("v_writelane_b32 %[stv], s92, 47\n\t") \
  "buffer_load_dword %[pfd], v202, %[sM], s98 offen\n\t" \
  "v_mfma_f32_16x16x32_bf16 v[12:15], a[248:251], v[24:27], v[12:15]\n\t" \
  "ds_read_b64_tr_b16 a[144:145], v237 offset:256\n\t" \
  "ds_read_b64_tr_b16 a[146:147], v237 offset:4352\n\t" \
  "v_lshl_add_u32 v207, v233, 14, %[cto]\n\t" \
  "v_min_u32 v207, .Lfz_z-.Lat_t1-4, v207\n\t" \
  "v_mfma_f32_16x16x32_bf16 v[12:15], a[244:247], v[20:23], v[12:15]\n\t" \
  "s_getpc_b64 s[96:97]\n\t" \
  ".Lat_t1:\n\t" \
  "global_load_dword %[pfd], v207, s[96:97]\n\t" \
  "v_mfma_f32_16x16x32_bf16 v[12:15], a[240:243], v[16:19], v[12:15]\n\t" \
  "v_exp_f32 v5, v5\n\t" \
  "v_mfma_f32_16x16x32_bf16 v[32:35], a[192:195], v[48:51], v[180:183]\n\t" \
  "v_exp_f32 v6, v6\n\t" \
  "v_mfma_f32_16x16x32_bf16 v[32:35], a[196:199], v[52:55], v[32:35]\n\t" \
  "v_exp_f32 v7, v7\n\t" \
  "v_mfma_f32_16x16x32_bf16 v[32:35], a[200:203], v[56:59], v[32:35]\n\t" \
  "v_exp_f32 v8, v8\n\t" \
  "v_mfma_f32_16x16x32_bf16 v[32:35], a[204:207], v[60:63], v[32:35]\n\t" \
  "v_exp_f32 v9, v9\n\t" \
  "v_mfma_f32_16x16x32_bf16 v[36:39], a[220:223], v[60:63], v[180:183]\n\t" \
  "ds_read_b64_tr_b16 a[148:149], v237 offset:8448\n\t" \
  "ds_read_b64_tr_b16 a[150:151], v237 offset:12544\n\t" \
  "v_mfma_f32_16x16x32_bf16 v[36:39], a[216:219], v[56:59], v[36:39]\n\t" \
  "v_exp_f32 v10, v10\n\t" \
  "v_mfma_f32_16x16x32_bf16 v[36:39], a[212:215], v[52:55], v[36:39]\n\t" \
  "v_exp_f32 v11, v11\n\t" \
  "v_mfma_f32_16x16x32_bf16 v[36:39], a[208:211], v[48:51], v[36:39]\n\t" \
  "ds_read_b64_tr_b16 a[152:153], v238 offset:256\n\t" \
  "ds_read_b64_tr_b16 a[154:155], v238 offset:4352\n\t" \
  "v_mfma_f32_16x16x32_bf16 v[40:43], a[224:227], v[48:51], v[180:183]\n\t" \
  "v_exp_f32 v12, v12\n\t" \
  "v_mfma_f32_16x16x32_bf16 v[40:43], a[228:231], v[52:55], v[40:43]\n\t" \
  "v_exp_f32 v13, v13\n\t" \
  "v_mfma_f32_16x16x32_bf16 v[40:43], a[232:235], v[56:59], v[40:43]\n\t" \
  "v_exp_f32 v14, v14\n\t" \
  "v_mfma_f32_16x16x32_bf16 v[40:43], a[236:239], v[60:63], v[40:43]\n\t" \
  "ds_read_b64_tr_b16 a[156:157], v238 offset:8448\n\t" \
  "ds_read_b64_tr_b16 a[158:159], v238 offset:12544\n\t" \
  "v_mfma_f32_16x16x32_bf16 v[44:47], a[252:255], v[60:63], v[180:183]\n\t" \
  "v_exp_f32 v15, v15\n\t" \
  "v_mfma_f32_16x16x32_bf16 v[44:47], a[248:251], v[56:59], v[44:47]\n\t" \
  "v_exp_f32 v32, v32\n\t" \
  "v_mfma_f32_16x16x32_bf16 v[44:47], a[244:247], v[52:55], v[44:47]\n\t" \
  "ds_read_b64_tr_b16 a[160:161], v237 offset:512\n\t" \
  "ds_read_b64_tr_b16 a[162:163], v237 offset:4608\n\t" \
  "v_mfma_f32_16x16x32_bf16 v[44:47], a[240:243], v[48:51], v[44:47]\n\t" \
  "v_exp_f32 v33, v33\n\t" \
  "v_mfma_f32_16x16x32_bf16 v[64:67], a[192:195], v[80:83], v[184:187]\n\t" \
  "v_exp_f32 v34, v34\n\t" \
  "v_mfma_f32_16x16x32_bf16 v[64:67], a[196:199], v[84:87], v[64:67]\n\t" \
  "v_exp_f32 v35, v35\n\t" \
  "v_mfma_f32_16x16x32_bf16 v[64:67], a[200:203], v[88:91], v[64:67]\n\t" \
  "ds_read_b64_tr_b16 a[164:165], v237 offset:8704\n\t" \
  "ds_read_b64_tr_b16 a[166:167], v237 offset:12800\n\t" \
  "v_mfma_f32_16x16x32_bf16 v[64:67], a[204:207], v[92:95], v[64:67]\n\t" \
  "v_exp_f32 v36, v36\n\t" \
  "v_mfma_f32_16x16x32_bf16 v[68:71], a[220:223], v[92:95], v[184:187]\n\t" \
  "v_exp_f32 v37, v37\n\t" \
  "v_mfma_f32_16x16x32_bf16 v[68:71], a[216:219], v[88:91], v[68:71]\n\t" \
  "ds_read_b64_tr_b16 a[168:169], v238 offset:512\n\t" \
  "ds_read_b64_tr_b16 a[170:171], v238 offset:4608\n\t" \
  "v_mfma_f32_16x16x32_bf16 v[68:71], a[212:215], v[84:87], v[68:71]\n\t" \
  "v_exp_f32 v38, v38\n\t" \
  "v_mfma_f32_16x16x32_bf16 v[68:71], a[208:211], v[80:83], v[68:71]\n\t" \
  "v_exp_f32 v39, v39\n\t" \
  "v_mfma_f32_16x16x32_bf16 v[72:75], a[224:227], v[80:83], v[184:187]\n\t" \
  "v_exp_f32 v40, v40\n\t" \
  "v_mfma_f32_16x16x32_bf16 v[72:75], a[228:231], v[84:87], v[72:75]\n\t" \
  "ds_read_b64_tr_b16 a[172:173], v238 offset:8704\n\t" \
  "ds_read_b64_tr_b16 a[174:175], v238 offset:12800\n\t" \
  "v_mfma_f32_16x16x32_bf16 v[72:75], a[232:235], v[88:91], v[72:75]\n\t" \
  "ds_read_b64_tr_b16 a[176:177], v237 offset:768\n\t" \
  "ds_read_b64_tr_b16 a[178:179], v237 offset:4864\n\t" \
  "v_mfma_f32_16x16x32_bf16 v[72:75], a[236:239], v[92:95], v[72:75]\n\t" \
  "s_lshr_b32 s98, %[hl18], 18\n\t" \
  "v_xor_b32 v245, s98, v233\n\t" \
  "v_and_b32 v245, 31, v245\n\t" \
  "v_mfma_f32_16x16x32_bf16 v[76:79], a[252:255], v[92:95], v[184:187]\n\t" \
  "v_exp_f32 v41, v41\n\t" \
  "v_mfma_f32_16x16x32_bf16 v[76:79], a[248:251], v[88:91], v[76:79]\n\t" \
  "v_exp_f32 v42, v42\n\t" \
  "v_mfma_f32_16x16x32_bf16 v[76:79], a[244:247], v[84:87], v[76:79]\n\t" \
  "v_exp_f32 v43, v43\n\t" \
  "v_mfma_f32_16x16x32_bf16 v[76:79], a[240:243], v[80:83], v[76:79]\n\t" \
  "v_exp_f32 v44, v44\n\t" \
  "v_mfma_f32_16x16x32_bf16 v[96:99], a[192:195], v[112:115], v[188:191]\n\t" \
  "ds_read_b64_tr_b16 a[180:181], v237 offset:8960\n\t" \
  "ds_read_b64_tr_b16 a[182:183], v237 offset:13056\n\t" \
  "v_mfma_f32_16x16x32_bf16 v[96:99], a[196:199], v[116:119], v[96:99]\n\t" \
  "v_exp_f32 v45, v45\n\t" \
  "v_mfma_f32_16x16x32_bf16 v[96:99], a[200:203], v[120:123], v[96:99]\n\t" \
  "v_exp_f32 v46, v46\n\t" \
  "v_mfma_f32_16x16x32_bf16 v[96:99], a[204:207], v[124:127], v[96:99]\n\t" \
  "ds_read_b64_tr_b16 a[184:185], v238 offset:768\n\t" \
  "ds_read_b64_tr_b16 a[186:187], v238 offset:4864\n\t" \
  "v_mfma_f32_16x16x32_bf16 v[100:103], a[220:223], v[124:127], v[188:191]\n\t" \
  "v_exp_f32 v47, v47\n\t" \
  "v_mfma_f32_16x16x32_bf16 v[100:103], a[216:219], v[120:123], v[100:103]\n\t" \
  "ds_read_b64_tr_b16 a[188:189], v238 offset:8960\n\t" \
  "ds_read_b64_tr_b16 a[190:191], v238 offset:13056\n\t" \
  "v_mfma_f32_16x16x32_bf16 v[100:103], a[212:215], v[116:119], v[100:103]\n\t" \
  "s_lshr_b32 s98, %[hl18], 18\n\t" \
  "s_mul_i32 s98, s98, 127\n\t" \
  "v_add_lshl_u32 v245, v245, s98, 2\n\t" \
  "v_mfma_f32_16x16x32_bf16 v[100:103], a[208:211], v[112:115], v[100:103]\n\t" \
  "v_exp_f32 v64, v64\n\t" \
  "v_mfma_f32_16x16x32_bf16 v[104:107], a[224:227], v[112:115], v[188:191]\n\t" \
  "v_exp_f32 v65, v65\n\t" \
  "v_mfma_f32_16x16x32_bf16 v[104:107], a[228:231], v[116:119], v[104:107]\n\t" \
  "v_exp_f32 v66, v66\n\t" \
  "v_mfma_f32_16x16x32_bf16 v[104:107], a[232:235], v[120:123], v[104:107]\n\t" \
  "v_exp_f32 v67, v67\n\t" \
  "v_mfma_f32_16x16x32_bf16 v[104:107], a[236:239], v[124:127], v[104:107]\n\t" \
  "v_exp_f32 v68, v68\n\t" \
  "v_mfma_f32_16x16x32_bf16 v[108:111], a[252:255], v[124:127], v[188:191]\n\t" \
  "v_exp_f32 v69, v69\n\t" \
  "v_mfma_f32_16x16x32_bf16 v[108:111], a[248:251], v[120:123], v[108:111]\n\t" \
  "v_exp_f32 v70, v70\n\t" \
  "v_mfma_f32_16x16x32_bf16 v[108:111], a[244:247], v[116:119], v[108:111]\n\t" \
  "v_exp_f32 v71, v71\n\t" \
  "v_mfma_f32_16x16x32_bf16 v[108:111], a[240:243], v[112:115], v[108:111]\n\t" \
  "s_waitcnt vmcnt(5) lgkmcnt(0)\n\t" \
  "s_barrier\n\t" \
  STA("s_memrealtime s[94:95]\n\t") \
  STA("s_memtime s[92:93]\n\t") \
  "v_mfma_f32_16x16x32_bf16 a[0:3], a[128:131], v[128:131], a[0:3]\n\t" \
  "v_exp_f32 v72, v72\n\t" \
  "v_mfma_f32_16x16x32_bf16 a[0:3], a[132:135], v[132:135], a[0:3]\n\t" \
  "v_exp_f32 v73, v73\n\t" \
  "v_mfma_f32_16x16x32_bf16 a[16:19], a[136:139], v[128:131], a[16:19]\n\t" \
  "ds_read_b128 a[192:195], v220 offset:16384\n\t" \
  "ds_read_b128 a[196:199], v221 offset:16384\n\t" \
  "v_mfma_f32_16x16x32_bf16 a[16:19], a[140:143], v[132:135], a[16:19]\n\t" \
  "s_waitcnt vmcnt(4)\n\t" \
  "v_and_b32 v206, 0xbfffbfff, v209\n\t" \
  "v_cmp_ne_u32_e64 s[96:97], %[tag], v206\n\t" \
  "s_cmp_lg_u64 s[96:97], 0\n\t" \
  "s_cbranch_scc1 .Lat_v\n\t" \
  ".Lat_w:\n\t" \
  "v_mfma_f32_16x16x32_bf16 a[32:35], a[144:147], v[128:131], a[32:35]\n\t" \
  "v_exp_f32 v74, v74\n\t" \
  "v_mfma_f32_16x16x32_bf16 a[32:35], a[148:151], v[132:135], a[32:35]\n\t" \
  "v_exp_f32 v75, v75\n\t" \
  "v_mfma_f32_16x16x32_bf16 a[48:51], a[152:155], v[128:131], a[48:51]\n\t" \
  "ds_read_b128 a[200:203], v222 offset:16384\n\t" \
  "ds_read_b128 a[204:207], v223 offset:16384\n\t" \
  "v_mfma_f32_16x16x32_bf16 a[48:51], a[156:159], v[132:135], a[48:51]\n\t" \
  "ds_read_b128 a[208:211], v220 offset:20480\n\t" \
  "v_and_b32 v206, 1, v233\n\t" \
  "v_lshlrev_b32 v206, 4, v206\n\t" \
  "v_mfma_f32_16x16x32_bf16 a[64:67], a[160:163], v[128:131], a[64:67]\n\t" \
  "v_lshrrev_b32 v206, v206, v209\n\t" \
  "v_and_b32 v206, 0x4000, v206\n\t" \
  "v_mfma_f32_16x16x32_bf16 a[64:67], a[164:167], v[132:135], a[64:67]\n\t" \
  "v_cmp_ne_u32_e64 s[96:97], 0, v206\n\t" \
  STS("s_nop 4\n\t") \
  STS("v_writelane_b32 %[stv], s96, 60\n\t") \
  STS("v_writelane_b32 %[stv], s97, 61\n\t") \
  "s_bcnt1_i32_b32 s98, s96\n\t" \
  "s_bcnt1_i32_b32 %[na], s97\n\t" \
  "s_cmp_gt_u32 s98, %[na]\n\t" \
  "s_cselect_b32 %[nb], 0x2000, 0\n\t" \
  "s_cselect_b32 s98, s96, s97\n\t" \
  "s_cselect_b32 s96, s97, s96\n\t" \
  "v_mfma_f32_16x16x32_bf16 a[80:83], a[168:171], v[128:131], a[80:83]\n\t" \
  "s_lshr_b32 s97, s98, 16\n\t" \
  "s_bcnt1_i32_b32 s97, s97\n\t" \
  "s_lshr_b32 s96, s96, 16\n\t" \
  "s_bcnt1_i32_b32 s96, s96\n\t" \
  "s_add_u32 s96, s96, 7\n\t" \
  "s_cmp_ge_u32 s97, s96\n\t" \
  "s_cselect_b32 %[na], -1, 0\n\t" \
  "s_xor_b32 s98, %[po], %[nb]\n\t" \
  "s_and_b32 s98, s98, %[na]\n\t" \
  "s_xor_b32 %[eso], %[eso], s98\n\t" \
  STS("v_writelane_b32 %[stv], %[na], 57\n\t") \
  STS("v_writelane_b32 %[stv], %[nb], 59\n\t") \
  "v_mfma_f32_16x16x32_bf16 a[80:83], a[172:175], v[132:135], a[80:83]\n\t" \
  "v_exp_f32 v76, v76\n\t" \
  "v_mfma_f32_16x16x32_bf16 a[96:99], a[176:179], v[128:131], a[96:99]\n\t" \
  STA("v_writelane_b32 %[stv], s94, 48\n\t") \
  STA("v_writelane_b32 %[stv], s92, 49\n\t") \
  "v_exp_f32 v77, v77\n\t" \
  "v_mfma_f32_16x16x32_bf16 a[96:99], a[180:183], v[132:135], a[96:99]\n\t" \
  "s_xor_b32 s98, %[px], %[nb]\n\t" \
  "s_and_b32 s98, s98, %[na]\n\t" \
  "s_xor_b32 %[xcb], %[xcb], s98\n\t" \
  "s_xor_b32 %[frb], %[frb], %[nb]\n\t" \
  "s_xor_b32 %[fw2], %[fw2], %[nb]\n\t" \
  "v_mfma_f32_16x16x32_bf16 a[112:115], a[184:187], v[128:131], a[112:115]\n\t" \
  "v_exp_f32 v78, v78\n\t" \
  "v_mfma_f32_16x16x32_bf16 a[112:115], a[188:191], v[132:135], a[112:115]\n\t" \
  "v_exp_f32 v79, v79\n\t" \
  "v_mfma_f32_16x16x32_bf16 v[160:163], v[216:219], v[128:131], v[160:163]\n\t" \
  "v_exp_f32 v96, v96\n\t" \
  "v_mfma_f32_16x16x32_bf16 v[160:163], v[216:219], v[132:135], v[160:163]\n\t" \
  "v_exp_f32 v97, v97\n\t" \
  "v_mfma_f32_16x16x32_bf16 a[4:7], a[128:131], v[136:139], a[4:7]\n\t" \
  "v_exp_f32 v98, v98\n\t" \
  "v_mfma_f32_16x16x32_bf16 a[4:7], a[132:135], v[140:143], a[4:7]\n\t" \
  "ds_read_b128 a[212:215], v221 offset:20480\n\t" \
  "v_cvt_pk_bf16_f32 v128, v0, v1\n\t" \
  "v_mfma_f32_16x16x32_bf16 a[20:23], a[136:139], v[136:139], a[20:23]\n\t" \
  "s_add_u32 m0, %[m], 147456\n\t" \
  "v_exp_f32 v99, v99\n\t" \
  "v_mfma_f32_16x16x32_bf16 a[20:23], a[140:143], v[140:143], a[20:23]\n\t" \
  "s_add_u32 s96, %[sofk], 114688\n\t" \
  "v_exp_f32 v100, v100\n\t" \
  "v_mfma_f32_16x16x32_bf16 a[36:39], a[144:147], v[136:139], a[36:39]\n\t" \
  "v_exp_f32 v101, v101\n\t" \
  "v_mfma_f32_16x16x32_bf16 a[36:39], a[148:151], v[140:143], a[36:39]\n\t" \
  "ds_read_b128 a[216:219], v222 offset:20480\n\t" \
  "v_cvt_pk_bf16_f32 v129, v2, v3\n\t" \
  "v_mfma_f32_16x16x32_bf16 a[52:55], a[152:155], v[136:139], a[52:55]\n\t" \
  "v_exp_f32 v102, v102\n\t" \
  "v_mfma_f32_16x16x32_bf16 a[52:55], a[156:159], v[140:143], a[52:55]\n\t" \
  "v_exp_f32 v103, v103\n\t" \
  "v_mfma_f32_16x16x32_bf16 a[68:71], a[160:163], v[136:139], a[68:71]\n\t" \
  "v_exp_f32 v104, v104\n\t" \
  "v_mfma_f32_16x16x32_bf16 a[68:71], a[164:167], v[140:143], a[68:71]\n\t" \
  "buffer_load_dwordx4 v230, %[sV], s96 offen offset:0 lds\n\t" \
  "v_mfma_f32_16x16x32_bf16 a[84:87], a[168:171], v[136:139], a[84:87]\n\t" \
  "ds_read_b128 a[220:223], v223 offset:20480\n\t" \
  "v_cvt_pk_bf16_f32 v130, v4, v5\n\t" \
  "v_mfma_f32_16x16x32_bf16 a[84:87], a[172:175], v[140:143], a[84:87]\n\t" \
  "v_exp_f32 v105, v105\n\t" \
  "v_mfma_f32_16x16x32_bf16 a[100:103], a[176:179], v[136:139], a[100:103]\n\t" \
  "ds_read_b128 a[224:227], v220 offset:24576\n\t" \
  "v_cvt_pk_bf16_f32 v131, v6, v7\n\t" \
  "v_mfma_f32_16x16x32_bf16 a[100:103], a[180:183], v[140:143], a[100:103]\n\t" \
  "buffer_load_dwordx4 v231, %[sV], s96 offen offset:1024 lds\n\t" \
  "v_mfma_f32_16x16x32_bf16 a[116:119], a[184:187], v[136:139], a[116:119]\n\t" \
  "v_exp_f32 v106, v106\n\t" \
  "v_mfma_f32_16x16x32_bf16 a[116:119], a[188:191], v[140:143], a[116:119]\n\t" \
  "v_exp_f32 v107, v107\n\t" \
  "v_mfma_f32_16x16x32_bf16 v[164:167], v[216:219], v[136:139], v[164:167]\n\t" \
  "buffer_load_dwordx4 v230, %[sV], s96 offen offset:2048 lds\n\t" \
  "v_mfma_f32_16x16x32_bf16 v[164:167], v[216:219], v[140:143], v[164:167]\n\t" \
  "ds_read_b128 a[228:231], v221 offset:24576\n\t" \
  "v_cvt_pk_bf16_f32 v132, v8, v9\n\t" \
  "v_mfma_f32_16x16x32_bf16 a[8:11], a[128:131], v[144:147], a[8:11]\n\t" \
  "v_exp_f32 v108, v108\n\t" \
  "v_mfma_f32_16x16x32_bf16 a[8:11], a[132:135], v[148:151], a[8:11]\n\t" \
  "v_exp_f32 v109, v109\n\t" \
  "v_mfma_f32_16x16x32_bf16 a[24:27], a[136:139], v[144:147], a[24:27]\n\t" \
  "buffer_load_dwordx4 v231, %[sV], s96 offen offset:3072 lds\n\t" \
  "v_mfma_f32_16x16x32_bf16 a[24:27], a[140:143], v[148:151], a[24:27]\n\t" \
  "s_add_u32 m0, %[m], 49152\n\t" \
  "ds_read_b128 a[232:235], v222 offset:24576\n\t" \
  "v_cvt_pk_bf16_f32 v133, v10, v11\n\t" \
  "v_mfma_f32_16x16x32_bf16 a[40:43], a[144:147], v[144:147], a[40:43]\n\t" \
  "s_xor_b32 s97, %[wob], 262144\n\t" \
  "v_exp_f32 v110, v110\n\t" \
  "v_mfma_f32_16x16x32_bf16 a[40:43], a[148:151], v[148:151], a[40:43]\n\t" \
  "v_exp_f32 v111, v111\n\t" \
  "v_mfma_f32_16x16x32_bf16 a[56:59], a[152:155], v[144:147], a[56:59]\n\t" \
  "ds_read_b128 a[236:239], v223 offset:24576\n\t" \
  "v_cvt_pk_bf16_f32 v134, v12, v13\n\t" \
  "v_mfma_f32_16x16x32_bf16 a[56:59], a[156:159], v[148:151], a[56:59]\n\t" \
  "ds_read_b128 a[240:243], v220 offset:28672\n\t" \
  "v_cvt_pk_bf16_f32 v135, v14, v15\n\t" \
  "v_mfma_f32_16x16x32_bf16 a[72:75], a[160:163], v[144:147], a[72:75]\n\t" \
  "ds_read_b128 a[244:247], v221 offset:28672\n\t" \
  "v_cvt_pk_bf16_f32 v136, v32, v33\n\t" \
  "v_mfma_f32_16x16x32_bf16 a[72:75], a[164:167], v[148:151], a[72:75]\n\t" \
  "ds_read_b128 a[248:251], v222 offset:28672\n\t" \
  "v_cvt_pk_bf16_f32 v137, v34, v35\n\t" \
  "v_mfma_f32_16x16x32_bf16 a[88:91], a[168:171], v[144:147], a[88:91]\n\t" \
  "v_cvt_pk_bf16_f32 v138, v36, v37\n\t" \
  "v_cvt_pk_bf16_f32 v139, v38, v39\n\t" \
  "v_mfma_f32_16x16x32_bf16 a[88:91], a[172:175], v[148:151], a[88:91]\n\t" \
  "ds_read_b128 a[252:255], v223 offset:28672\n\t" \
  "v_cvt_pk_bf16_f32 v140, v40, v41\n\t" \
  "v_mfma_f32_16x16x32_bf16 a[104:107], a[176:179], v[144:147], a[104:107]\n\t" \
  "buffer_load_dwordx4 v240, %[wo], s97 offen offset:0 lds\n\t" \
  "v_mfma_f32_16x16x32_bf16 a[104:107], a[180:183], v[148:151], a[104:107]\n\t" \
  "s_add_u32 s97, s97, 7168\n\t" \
  "v_cvt_pk_bf16_f32 v141, v42, v43\n\t" \
  "v_cvt_pk_bf16_f32 v142, v44, v45\n\t" \
  "v_mfma_f32_16x16x32_bf16 a[120:123], a[184:187], v[144:147], a[120:123]\n\t" \
  "v_cvt_pk_bf16_f32 v143, v46, v47\n\t" \
  "v_mfma_f32_16x16x32_bf16 a[120:123], a[188:191], v[148:151], a[120:123]\n\t" \
  "buffer_load_dwordx4 v241, %[wo], s97 offen offset:1024 lds\n\t" \
  "v_mfma_f32_16x16x32_bf16 v[168:171], v[216:219], v[144:147], v[168:171]\n\t" \
  "s_add_u32 s97, s97, 7168\n\t" \
  "v_mfma_f32_16x16x32_bf16 v[168:171], v[216:219], v[148:151], v[168:171]\n\t" \
  "v_cvt_pk_bf16_f32 v144, v64, v65\n\t" \
  "v_cvt_pk_bf16_f32 v145, v66, v67\n\t" \
  "v_mfma_f32_16x16x32_bf16 a[12:15], a[128:131], v[152:155], a[12:15]\n\t" \
  "v_cvt_pk_bf16_f32 v146, v68, v69\n\t" \
  "v_cvt_pk_bf16_f32 v147, v70, v71\n\t" \
  "v_mfma_f32_16x16x32_bf16 a[12:15], a[132:135], v[156:159], a[12:15]\n\t" \
  "buffer_load_dwordx4 v240, %[wo], s97 offen offset:2048 lds\n\t" \
  "v_mfma_f32_16x16x32_bf16 a[28:31], a[136:139], v[152:155], a[28:31]\n\t" \
  "s_add_u32 s97, s97, 7168\n\t" \
  "v_cvt_pk_bf16_f32 v148, v72, v73\n\t" \
  "v_cvt_pk_bf16_f32 v149, v74, v75\n\t" \
  "v_mfma_f32_16x16x32_bf16 a[28:31], a[140:143], v[156:159], a[28:31]\n\t" \
  "v_cvt_pk_bf16_f32 v150, v76, v77\n\t" \
  "v_cvt_pk_bf16_f32 v151, v78, v79\n\t" \
  "v_mfma_f32_16x16x32_bf16 a[44:47], a[144:147], v[152:155], a[44:47]\n\t" \
  "buffer_load_dwordx4 v241, %[wo], s97 offen offset:3072 lds\n\t" \
  "v_mfma_f32_16x16x32_bf16 a[44:47], a[148:151], v[156:159], a[44:47]\n\t" \
  "v_mfma_f32_16x16x32_bf16 a[60:63], a[152:155], v[152:155], a[60:63]\n\t" \
  "s_add_u32 m0, %[m], 98304\n\t" \
  "s_mov_b32 s96, 40960\n\t" \
  "v_mfma_f32_16x16x32_bf16 a[60:63], a[156:159], v[156:159], a[60:63]\n\t" \
  "v_mfma_f32_16x16x32_bf16 a[76:79], a[160:163], v[152:155], a[76:79]\n\t" \
  "v_mfma_f32_16x16x32_bf16 a[76:79], a[164:167], v[156:159], a[76:79]\n\t" \
  "buffer_load_dwordx4 v232, %[sG], s96 offen offset:2048 lds\n\t" \
  "v_mfma_f32_16x16x32_bf16 a[92:95], a[168:171], v[152:155], a[92:95]\n\t" \
  "s_mov_b32 s96, 47104\n\t" \
  "v_mfma_f32_16x16x32_bf16 a[92:95], a[172:175], v[156:159], a[92:95]\n\t" \
  "v_mfma_f32_16x16x32_bf16 a[108:111], a[176:179], v[152:155], a[108:111]\n\t" \
  "v_mfma_f32_16x16x32_bf16 a[108:111], a[180:183], v[156:159], a[108:111]\n\t" \
  "buffer_load_dwordx4 v232, %[sG], s96 offen offset:3072 lds\n\t" \
  "v_mfma_f32_16x16x32_bf16 a[124:127], a[184:187], v[152:155], a[124:127]\n\t" \
  "v_mfma_f32_16x16x32_bf16 a[124:127], a[188:191], v[156:159], a[124:127]\n\t" \
  "v_mfma_f32_16x16x32_bf16 v[172:175], v[216:219], v[152:155], v[172:175]\n\t" \
  "v_mfma_f32_16x16x32_bf16 v[172:175], v[216:219], v[156:159], v[172:175]\n\t" \
  "s_waitcnt lgkmcnt(0)\n\t" \
  "s_barrier\n\t" \
  STA("s_memrealtime s[94:95]\n\t") \
  STA("s_memtime s[92:93]\n\t") \
  "v_mfma_f32_16x16x32_bf16 v[0:3], a[192:195], v[16:19], v[176:179]\n\t" \
  "ds_read_b64_tr_b16 a[128:129], v228 offset:0\n\t" \
  "v_cvt_pk_bf16_f32 v152, v96, v97\n\t" \
  "v_mfma_f32_16x16x32_bf16 v[0:3], a[196:199], v[20:23], v[0:3]\n\t" \
  "ds_read_b64_tr_b16 a[130:131], v228 offset:4096\n\t" \
  "v_cvt_pk_bf16_f32 v153, v98, v99\n\t" \
  "v_mfma_f32_16x16x32_bf16 v[0:3], a[200:203], v[24:27], v[0:3]\n\t" \
  "ds_read_b64_tr_b16 a[132:133], v228 offset:8192\n\t" \
  "v_cvt_pk_bf16_f32 v154, v100, v101\n\t" \
  "v_mfma_f32_16x16x32_bf16 v[0:3], a[204:207], v[28:31], v[0:3]\n\t" \
  "ds_read_b64_tr_b16 a[134:135], v228 offset:12288\n\t" \
  "v_cvt_pk_bf16_f32 v155, v102, v103\n\t" \
  "v_mfma_f32_16x16x32_bf16 v[4:7], a[220:223], v[28:31], v[176:179]\n\t" \
  "s_add_u32 m0, %[m], 65536\n\t" \
  "ds_read_b64_tr_b16 a[136:137], v229 offset:0\n\t" \
  "v_cvt_pk_bf16_f32 v156, v104, v105\n\t" \
  "v_mfma_f32_16x16x32_bf16 v[4:7], a[216:219], v[24:27], v[4:7]\n\t" \
  "s_xor_b32 s97, %[wob], 393216\n\t" \
  "v_exp_f32 v0, v0\n\t" \
  "v_mfma_f32_16x16x32_bf16 v[4:7], a[212:215], v[20:23], v[4:7]\n\t" \
  "v_exp_f32 v1, v1\n\t" \
  "v_mfma_f32_16x16x32_bf16 v[4:7], a[208:211], v[16:19], v[4:7]\n\t" \
  "v_exp_f32 v2, v2\n\t" \
  "v_mfma_f32_16x16x32_bf16 v[8:11], a[224:227], v[16:19], v[176:179]\n\t" \
  "v_exp_f32 v3, v3\n\t" \
  "v_mfma_f32_16x16x32_bf16 v[8:11], a[228:231], v[20:23], v[8:11]\n\t" \
  "ds_read_b64_tr_b16 a[138:139], v229 offset:4096\n\t" \
  "v_cvt_pk_bf16_f32 v157, v106, v107\n\t" \
  "v_mfma_f32_16x16x32_bf16 v[8:11], a[232:235], v[24:27], v[8:11]\n\t" \
  "ds_read_b64_tr_b16 a[140:141], v229 offset:8192\n\t" \
  "v_cvt_pk_bf16_f32 v158, v108, v109\n\t" \
  "v_mfma_f32_16x16x32_bf16 v[8:11], a[236:239], v[28:31], v[8:11]\n\t" \
  "v_exp_f32 v4, v4\n\t" \
  "v_mfma_f32_16x16x32_bf16 v[12:15], a[252:255], v[28:31], v[176:179]\n\t" \
  STA("v_writelane_b32 %[stv], s94, 50\n\t") \
  STA("v_writelane_b32 %[stv], s92, 51\n\t") \
  "buffer_load_dwordx4 v240, %[wo], s97 offen offset:0 lds\n\t" \
  "v_mfma_f32_16x16x32_bf16 v[12:15], a[248:251], v[24:27], v[12:15]\n\t" \
  "s_add_u32 s97, s97, 7168\n\t" \
  "ds_read_b64_tr_b16 a[142:143], v229 offset:12288\n\t" \
  "v_cvt_pk_bf16_f32 v159, v110, v111\n\t" \
  "v_mfma_f32_16x16x32_bf16 v[12:15], a[244:247], v[20:23], v[12:15]\n\t" \
  "v_exp_f32 v5, v5\n\t" \
  "v_mfma_f32_16x16x32_bf16 v[12:15], a[240:243], v[16:19], v[12:15]\n\t" \
  "v_exp_f32 v6, v6\n\t" \
  "v_mfma_f32_16x16x32_bf16 v[32:35], a[192:195], v[48:51], v[180:183]\n\t" \
  "buffer_load_dwordx4 v241, %[wo], s97 offen offset:1024 lds\n\t" \
  "v_mfma_f32_16x16x32_bf16 v[32:35], a[196:199], v[52:55], v[32:35]\n\t" \
  "s_add_u32 s97, s97, 7168\n\t" \
  "ds_read_b64_tr_b16 a[144:145], v228 offset:256\n\t" \
  "ds_read_b64_tr_b16 a[146:147], v228 offset:4352\n\t" \
  "v_mfma_f32_16x16x32_bf16 v[32:35], a[200:203], v[56:59], v[32:35]\n\t" \
  "v_exp_f32 v7, v7\n\t" \
  "v_mfma_f32_16x16x32_bf16 v[32:35], a[204:207], v[60:63], v[32:35]\n\t" \
  "v_exp_f32 v8, v8\n\t" \
  "v_mfma_f32_16x16x32_bf16 v[36:39], a[220:223], v[60:63], v[180:183]\n\t" \
  "buffer_load_dwordx4 v240, %[wo], s97 offen offset:2048 lds\n\t" \
  "v_mfma_f32_16x16x32_bf16 v[36:39], a[216:219], v[56:59], v[36:39]\n\t" \
  "s_add_u32 s97, s97, 7168\n\t" \
  "ds_read_b64_tr_b16 a[148:149], v228 offset:8448\n\t" \
  "ds_read_b64_tr_b16 a[150:151], v228 offset:12544\n\t" \
  "v_mfma_f32_16x16x32_bf16 v[36:39], a[212:215], v[52:55], v[36:39]\n\t" \
  "v_exp_f32 v9, v9\n\t" \
  "v_mfma_f32_16x16x32_bf16 v[36:39], a[208:211], v[48:51], v[36:39]\n\t" \
  "ds_read_b64_tr_b16 a[152:153], v229 offset:256\n\t" \
  "ds_read_b64_tr_b16 a[154:155], v229 offset:4352\n\t" \
  "v_mfma_f32_16x16x32_bf16 v[40:43], a[224:227], v[48:51], v[180:183]\n\t" \
  "buffer_load_dwordx4 v241, %[wo], s97 offen offset:3072 lds\n\t" \
  "v_mfma_f32_16x16x32_bf16 v[40:43], a[228:231], v[52:55], v[40:43]\n\t" \
  "v_exp_f32 v10, v10\n\t" \
  "v_mfma_f32_16x16x32_bf16 v[40:43], a[232:235], v[56:59], v[40:43]\n\t" \
  "v_exp_f32 v11, v11\n\t" \
  "v_mfma_f32_16x16x32_bf16 v[40:43], a[236:239], v[60:63], v[40:43]\n\t" \
  "ds_read_b64_tr_b16 a[156:157], v229 offset:8448\n\t" \
  "ds_read_b64_tr_b16 a[158:159], v229 offset:12544\n\t" \
  "v_mfma_f32_16x16x32_bf16 v[44:47], a[252:255], v[60:63], v[180:183]\n\t" \
  "v_exp_f32 v12, v12\n\t" \
  "v_mfma_f32_16x16x32_bf16 v[44:47], a[248:251], v[56:59], v[44:47]\n\t" \
  "v_exp_f32 v13, v13\n\t" \
  "v_mfma_f32_16x16x32_bf16 v[44:47], a[244:247], v[52:55], v[44:47]\n\t" \
  "ds_read_b64_tr_b16 a[160:161], v228 offset:512\n\t" \
  "ds_read_b64_tr_b16 a[162:163], v228 offset:4608\n\t" \
  "v_mfma_f32_16x16x32_bf16 v[44:47], a[240:243], v[48:51], v[44:47]\n\t" \
  "v_exp_f32 v14, v14\n\t" \
  "v_mfma_f32_16x16x32_bf16 v[64:67], a[192:195], v[80:83], v[184:187]\n\t" \
  "v_exp_f32 v15, v15\n\t" \
  "v_mfma_f32_16x16x32_bf16 v[64:67], a[196:199], v[84:87], v[64:67]\n\t" \
  "v_exp_f32 v32, v32\n\t" \
  "v_mfma_f32_16x16x32_bf16 v[64:67], a[200:203], v[88:91], v[64:67]\n\t" \
  "ds_read_b64_tr_b16 a[164:165], v228 offset:8704\n\t" \
  "ds_read_b64_tr_b16 a[166:167], v228 offset:12800\n\t" \
  "v_mfma_f32_16x16x32_bf16 v[64:67], a[204:207], v[92:95], v[64:67]\n\t" \
  "v_exp_f32 v33, v33\n\t" \
  "v_mfma_f32_16x16x32_bf16 v[68:71], a[220:223], v[92:95], v[184:187]\n\t" \
  "v_exp_f32 v34, v34\n\t" \
  "v_mfma_f32_16x16x32_bf16 v[68:71], a[216:219], v[88:91], v[68:71]\n\t" \
  "ds_read_b64_tr_b16 a[168:169], v229 offset:512\n\t" \
  "ds_read_b64_tr_b16 a[170:171], v229 offset:4608\n\t" \
  "v_mfma_f32_16x16x32_bf16 v[68:71], a[212:215], v[84:87], v[68:71]\n\t" \
  "v_exp_f32 v35, v35\n\t" \
  "v_mfma_f32_16x16x32_bf16 v[68:71], a[208:211], v[80:83], v[68:71]\n\t" \
  "v_exp_f32 v36, v36\n\t" \
  "v_mfma_f32_16x16x32_bf16 v[72:75], a[224:227], v[80:83], v[184:187]\n\t" \
  "v_exp_f32 v37, v37\n\t" \
  "v_mfma_f32_16x16x32_bf16 v[72:75], a[228:231], v[84:87], v[72:75]\n\t" \
  "ds_read_b64_tr_b16 a[172:173], v229 offset:8704\n\t" \
  "ds_read_b64_tr_b16 a[174:175], v229 offset:12800\n\t" \
  "v_mfma_f32_16x16x32_bf16 v[72:75], a[232:235], v[88:91], v[72:75]\n\t" \
  "v_exp_f32 v38, v38\n\t" \
  "v_mfma_f32_16x16x32_bf16 v[72:75], a[236:239], v[92:95], v[72:75]\n\t" \
  "v_exp_f32 v39, v39\n\t" \
  "v_mfma_f32_16x16x32_bf16 v[76:79], a[252:255], v[92:95], v[184:187]\n\t" \
  "ds_read_b64_tr_b16 a[176:177], v228 offset:768\n\t" \
  "ds_read_b64_tr_b16 a[178:179], v228 offset:4864\n\t" \
  ".Lhs9:\n\ts_cbranch_execz .Lhs9n\n\t" \
  "v_mfma_f32_16x16x32_bf16 v[76:79], a[248:251], v[88:91], v[76:79]\n\t" \
  "v_exp_f32 v40, v40\n\t" \
  "v_mfma_f32_16x16x32_bf16 v[76:79], a[244:247], v[84:87], v[76:79]\n\t" \
  "v_exp_f32 v41, v41\n\t" \
  "v_mfma_f32_16x16x32_bf16 v[76:79], a[240:243], v[80:83], v[76:79]\n\t" \
  "v_exp_f32 v42, v42\n\t" \
  "v_mfma_f32_16x16x32_bf16 v[96:99], a[192:195], v[112:115], v[188:191]\n\t" \
  "ds_read_b64_tr_b16 a[180:181], v228 offset:8960\n\t" \
  "ds_read_b64_tr_b16 a[182:183], v228 offset:13056\n\t" \
  "v_mfma_f32_16x16x32_bf16 v[96:99], a[196:199], v[116:119], v[96:99]\n\t" \
  "v_exp_f32 v43, v43\n\t" \
  "v_mfma_f32_16x16x32_bf16 v[96:99], a[200:203], v[120:123], v[96:99]\n\t" \
  "v_exp_f32 v44, v44\n\t" \
  "v_mfma_f32_16x16x32_bf16 v[96:99], a[204:207], v[124:127], v[96:99]\n\t" \
  "ds_read_b64_tr_b16 a[184:185], v229 offset:768\n\t" \
  "ds_read_b64_tr_b16 a[186:187], v229 offset:4864\n\t" \
  "v_mfma_f32_16x16x32_bf16 v[100:103], a[220:223], v[124:127], v[188:191]\n\t" \
  "v_exp_f32 v45, v45\n\t" \
  "v_mfma_f32_16x16x32_bf16 v[100:103], a[216:219], v[120:123], v[100:103]\n\t" \
  "v_exp_f32 v46, v46\n\t" \
  "v_mfma_f32_16x16x32_bf16 v[100:103], a[212:215], v[116:119], v[100:103]\n\t" \
  "v_exp_f32 v47, v47\n\t" \
  "v_mfma_f32_16x16x32_bf16 v[100:103], a[208:211], v[112:115], v[100:103]\n\t" \
  "ds_read_b64_tr_b16 a[188:189], v229 offset:8960\n\t" \
  "ds_read_b64_tr_b16 a[190:191], v229 offset:13056\n\t" \
  "v_mfma_f32_16x16x32_bf16 v[104:107], a[224:227], v[112:115], v[188:191]\n\t" \
  "v_exp_f32 v64, v64\n\t" \
  "v_mfma_f32_16x16x32_bf16 v[104:107], a[228:231], v[116:119], v[104:107]\n\t" \
  "v_exp_f32 v65, v65\n\t" \
  "v_mfma_f32_16x16x32_bf16 v[104:107], a[232:235], v[120:123], v[104:107]\n\t" \
  "v_exp_f32 v66, v66\n\t" \
  "v_mfma_f32_16x16x32_bf16 v[104:107], a[236:239], v[124:127], v[104:107]\n\t" \
  "v_exp_f32 v67, v67\n\t" \
  "v_mfma_f32_16x16x32_bf16 v[108:111], a[252:255], v[124:127], v[188:191]\n\t" \
  "v_exp_f32 v68, v68\n\t" \
  "v_mfma_f32_16x16x32_bf16 v[108:111], a[248:251], v[120:123], v[108:111]\n\t" \
  "v_exp_f32 v69, v69\n\t" \
  "v_mfma_f32_16x16x32_bf16 v[108:111], a[244:247], v[116:119], v[108:111]\n\t" \
  "v_exp_f32 v70, v70\n\t" \
  "v_mfma_f32_16x16x32_bf16 v[108:111], a[240:243], v[112:115], v[108:111]\n\t" \
  "s_waitcnt vmcnt(6) lgkmcnt(0)\n\t" \
  "s_barrier\n\t" \
  STA("s_memrealtime s[94:95]\n\t") \
  STA("s_memtime s[92:93]\n\t") \
  "v_mfma_f32_16x16x32_bf16 a[0:3], a[128:131], v[128:131], a[0:3]\n\t" \
  "s_add_u32 m0, %[m], 81920\n\t" \
  "v_exp_f32 v71, v71\n\t" \
  "v_mfma_f32_16x16x32_bf16 a[0:3], a[132:135], v[132:135], a[0:3]\n\t" \
  "s_xor_b32 s97, %[wob], 524288\n\t" \
  "v_exp_f32 v72, v72\n\t" \
  "v_mfma_f32_16x16x32_bf16 a[16:19], a[136:139], v[128:131], a[16:19]\n\t" \
  "ds_read_b64_tr_b16 a[192:193], v237 offset:0\n\t" \
  "ds_read_b64_tr_b16 a[194:195], v237 offset:4096\n\t" \
  "v_mfma_f32_16x16x32_bf16 a[16:19], a[140:143], v[132:135], a[16:19]\n\t" \
  "v_exp_f32 v73, v73\n\t" \
  "v_mfma_f32_16x16x32_bf16 a[32:35], a[144:147], v[128:131], a[32:35]\n\t" \
  "ds_read_b128 v[176:179], v215 offset:16384\n\t" \
  "ds_read_b64_tr_b16 a[196:197], v237 offset:8192\n\t" \
  "v_mfma_f32_16x16x32_bf16 a[32:35], a[148:151], v[132:135], a[32:35]\n\t" \
  "v_exp_f32 v74, v74\n\t" \
  "v_mfma_f32_16x16x32_bf16 a[48:51], a[152:155], v[128:131], a[48:51]\n\t" \
  "buffer_load_dwordx4 v240, %[wo], s97 offen offset:0 lds\n\t" \
  "v_mfma_f32_16x16x32_bf16 a[48:51], a[156:159], v[132:135], a[48:51]\n\t" \
  "s_add_u32 s97, s97, 7168\n\t" \
  "ds_read_b128 v[180:183], v215 offset:17408\n\t" \
  "ds_read_b64_tr_b16 a[198:199], v237 offset:12288\n\t" \
  "v_mfma_f32_16x16x32_bf16 a[64:67], a[160:163], v[128:131], a[64:67]\n\t" \
  "buffer_load_dwordx4 v241, %[wo], s97 offen offset:1024 lds\n\t" \
  "v_mfma_f32_16x16x32_bf16 a[64:67], a[164:167], v[132:135], a[64:67]\n\t" \
  "s_add_u32 s97, s97, 7168\n\t" \
  "ds_read_b64_tr_b16 a[200:201], v238 offset:0\n\t" \
  "ds_read_b128 v[184:187], v215 offset:18432\n\t" \
  "v_mfma_f32_16x16x32_bf16 a[80:83], a[168:171], v[128:131], a[80:83]\n\t" \
  "ds_read_b64_tr_b16 a[202:203], v238 offset:4096\n\t" \
  "ds_read_b128 v[188:191], v215 offset:19456\n\t" \
  "v_mfma_f32_16x16x32_bf16 a[80:83], a[172:175], v[132:135], a[80:83]\n\t" \
  "ds_read_b64_tr_b16 a[204:205], v238 offset:8192\n\t" \
  "ds_read_b128 v[192:195], v215 offset:32768\n\t" \
  "v_mfma_f32_16x16x32_bf16 a[96:99], a[176:179], v[128:131], a[96:99]\n\t" \
  STA("v_writelane_b32 %[stv], s94, 52\n\t") \
  STA("v_writelane_b32 %[stv], s92, 53\n\t") \
  "buffer_load_dwordx4 v240, %[wo], s97 offen offset:2048 lds\n\t" \
  "v_mfma_f32_16x16x32_bf16 a[96:99], a[180:183], v[132:135], a[96:99]\n\t" \
  "s_add_u32 s97, s97, 7168\n\t" \
  "ds_read_b64_tr_b16 a[206:207], v238 offset:12288\n\t" \
  "ds_read_b128 v[196:199], v215 offset:33792\n\t" \
  "v_mfma_f32_16x16x32_bf16 a[112:115], a[184:187], v[128:131], a[112:115]\n\t" \
  "ds_read_b64_tr_b16 a[208:209], v237 offset:256\n\t" \
  "ds_read_b128 v[200:203], v215 offset:34816\n\t" \
  "v_mfma_f32_16x16x32_bf16 a[112:115], a[188:191], v[132:135], a[112:115]\n\t" \
  "ds_read_b64_tr_b16 a[210:211], v237 offset:4352\n\t" \
  "ds_read_b64_tr_b16 a[212:213], v237 offset:8448\n\t" \
  "v_mfma_f32_16x16x32_bf16 v[160:163], v[216:219], v[128:131], v[160:163]\n\t" \
  "buffer_load_dwordx4 v241, %[wo], s97 offen offset:3072 lds\n\t" \
  "v_mfma_f32_16x16x32_bf16 v[160:163], v[216:219], v[132:135], v[160:163]\n\t" \
  "ds_read_b128 v[204:207], v215 offset:35840\n\t" \
  "v_cvt_pk_bf16_f32 v128, v0, v1\n\t" \
  "v_mfma_f32_16x16x32_bf16 a[4:7], a[128:131], v[136:139], a[4:7]\n\t" \
  "ds_read_b64_tr_b16 a[214:215], v237 offset:12544\n\t" \
  "v_cvt_pk_bf16_f32 v129, v2, v3\n\t" \
  "v_mfma_f32_16x16x32_bf16 a[4:7], a[132:135], v[140:143], a[4:7]\n\t" \
  "ds_read_b64_tr_b16 a[216:217], v238 offset:256\n\t" \
  "v_cvt_pk_bf16_f32 v130, v4, v5\n\t" \
  "v_mfma_f32_16x16x32_bf16 a[20:23], a[136:139], v[136:139], a[20:23]\n\t" \
  "s_add_u32 m0, %[m], 114688\n\t" \
  "ds_read_b64_tr_b16 a[218:219], v238 offset:4352\n\t" \
  "v_cvt_pk_bf16_f32 v131, v6, v7\n\t" \
  "v_mfma_f32_16x16x32_bf16 a[20:23], a[140:143], v[140:143], a[20:23]\n\t" \
  "s_xor_b32 s97, %[wob], 786432\n\t" \
  "ds_read_b64_tr_b16 a[220:221], v238 offset:8448\n\t" \
  "v_cvt_pk_bf16_f32 v132, v8, v9\n\t" \
  "v_mfma_f32_16x16x32_bf16 a[36:39], a[144:147], v[136:139], a[36:39]\n\t" \
  "s_waitcnt lgkmcnt(13)\n\t" \
  "ds_read_b64_tr_b16 a[222:223], v238 offset:12544\n\t" \
  "v_cvt_pk_bf16_f32 v133, v10, v11\n\t" \
  "v_mfma_f32_16x16x32_bf16 a[36:39], a[148:151], v[140:143], a[36:39]\n\t" \
  "ds_read_b64_tr_b16 a[224:225], v237 offset:512\n\t" \
  "v_cvt_pk_bf16_f32 v134, v12, v13\n\t" \
  "v_mfma_f32_16x16x32_bf16 a[52:55], a[152:155], v[136:139], a[52:55]\n\t" \
  "ds_read_b64_tr_b16 a[226:227], v237 offset:4608\n\t" \
  "v_cvt_pk_bf16_f32 v135, v14, v15\n\t" \
  "v_mfma_f32_16x16x32_bf16 a[52:55], a[156:159], v[140:143], a[52:55]\n\t" \
  "ds_read_b64_tr_b16 a[228:229], v237 offset:8704\n\t" \
  "ds_read_b64_tr_b16 a[230:231], v237 offset:12800\n\t" \
  "v_mfma_f32_16x16x32_bf16 a[68:71], a[160:163], v[136:139], a[68:71]\n\t" \
  "v_exp_f32 v75, v75\n\t" \
  "v_mfma_f32_16x16x32_bf16 a[68:71], a[164:167], v[140:143], a[68:71]\n\t" \
  "ds_read_b64_tr_b16 a[232:233], v238 offset:512\n\t" \
  "ds_read_b64_tr_b16 a[234:235], v238 offset:4608\n\t" \
  "v_mfma_f32_16x16x32_bf16 a[84:87], a[168:171], v[136:139], a[84:87]\n\t" \
  "buffer_load_dwordx4 v240, %[wo], s97 offen offset:0 lds\n\t" \
  "v_mfma_f32_16x16x32_bf16 a[84:87], a[172:175], v[140:143], a[84:87]\n\t" \
  "s_add_u32 s97, s97, 7168\n\t" \
  "v_exp_f32 v76, v76\n\t" \
  "v_mfma_f32_16x16x32_bf16 a[100:103], a[176:179], v[136:139], a[100:103]\n\t" \
  "ds_read_b64_tr_b16 a[236:237], v238 offset:8704\n\t" \
  "ds_read_b64_tr_b16 a[238:239], v238 offset:12800\n\t" \
  "v_mfma_f32_16x16x32_bf16 a[100:103], a[180:183], v[140:143], a[100:103]\n\t" \
  "buffer_load_dwordx4 v241, %[wo], s97 offen offset:1024 lds\n\t" \
  "v_mfma_f32_16x16x32_bf16 a[116:119], a[184:187], v[136:139], a[116:119]\n\t" \
  "s_add_u32 s97, s97, 7168\n\t" \
  "ds_read_b64_tr_b16 a[240:241], v237 offset:768\n\t" \
  "ds_read_b64_tr_b16 a[242:243], v237 offset:4864\n\t" \
  "v_mfma_f32_16x16x32_bf16 a[116:119], a[188:191], v[140:143], a[116:119]\n\t" \
  "v_exp_f32 v77, v77\n\t" \
  "v_mfma_f32_16x16x32_bf16 v[164:167], v[216:219], v[136:139], v[164:167]\n\t" \
  "buffer_load_dwordx4 v240, %[wo], s97 offen offset:2048 lds\n\t" \
  "v_mfma_f32_16x16x32_bf16 v[164:167], v[216:219], v[140:143], v[164:167]\n\t" \
  "s_add_u32 s97, s97, 7168\n\t" \
  "ds_read_b64_tr_b16 a[244:245], v237 offset:8960\n\t" \
  "v_cvt_pk_bf16_f32 v136, v32, v33\n\t" \
  "v_mfma_f32_16x16x32_bf16 a[0:3], a[192:195], v[128:131], a[0:3]\n\t" \
  "ds_read_b64_tr_b16 a[246:247], v237 offset:13056\n\t" \
  "v_cvt_pk_bf16_f32 v137, v34, v35\n\t" \
  "v_mfma_f32_16x16x32_bf16 a[0:3], a[196:199], v[132:135], a[0:3]\n\t" \
  "ds_read_b64_tr_b16 a[248:249], v238 offset:768\n\t" \
  "v_cvt_pk_bf16_f32 v138, v36, v37\n\t" \
  "v_mfma_f32_16x16x32_bf16 v[160:163], v[216:219], v[128:131], v[160:163]\n\t" \
  "buffer_load_dwordx4 v241, %[wo], s97 offen offset:3072 lds\n\t" \
  "v_mfma_f32_16x16x32_bf16 v[160:163], v[216:219], v[132:135], v[160:163]\n\t" \
  "s_add_u32 m0, %[m], 131072\n\t" \
  "ds_read_b64_tr_b16 a[250:251], v238 offset:4864\n\t" \
  "v_cvt_pk_bf16_f32 v139, v38, v39\n\t" \
  "v_mfma_f32_16x16x32_bf16 a[16:19], a[200:203], v[128:131], a[16:19]\n\t" \
  "s_waitcnt lgkmcnt(15)\n\t" \
  "ds_read_b64_tr_b16 a[252:253], v238 offset:8960\n\t" \
  "v_cvt_pk_bf16_f32 v140, v40, v41\n\t" \
  "v_mfma_f32_16x16x32_bf16 a[16:19], a[204:207], v[132:135], a[16:19]\n\t" \
  "s_xor_b32 s97, %[wob], 917504\n\t" \
  "ds_read_b64_tr_b16 a[254:255], v238 offset:13056\n\t" \
  "v_cvt_pk_bf16_f32 v141, v42, v43\n\t" \
  "v_xor_b32 v235, 64, v234\n\t" \
  "v_mfma_f32_16x16x32_bf16 a[32:35], a[208:211], v[128:131], a[32:35]\n\t" \
  "v_exp_f32 v78, v78\n\t" \
  "v_cvt_pk_bf16_f32 v142, v44, v45\n\t" \
  "v_mfma_f32_16x16x32_bf16 a[32:35], a[212:215], v[132:135], a[32:35]\n\t" \
  "ds_read_b64_tr_b16 v[48:49], v235 offset:0\n\t" \
  "v_cvt_pk_bf16_f32 v143, v46, v47\n\t" \
  "v_mfma_f32_16x16x32_bf16 a[48:51], a[216:219], v[128:131], a[48:51]\n\t" \
  "s_waitcnt lgkmcnt(15)\n\t" \
  "v_exp_f32 v79, v79\n\t" \
  "v_mfma_f32_16x16x32_bf16 a[48:51], a[220:223], v[132:135], a[48:51]\n\t" \
  "ds_read_b64_tr_b16 v[50:51], v235 offset:4096\n\t" \
  "ds_read_b64_tr_b16 v[52:53], v235 offset:8192\n\t" \
  "v_mfma_f32_16x16x32_bf16 a[64:67], a[224:227], v[128:131], a[64:67]\n\t" \
  "s_waitcnt lgkmcnt(15)\n\t" \
  "buffer_load_dwordx4 v240, %[wo], s97 offen offset:0 lds\n\t" \
  "v_mfma_f32_16x16x32_bf16 a[64:67], a[228:231], v[132:135], a[64:67]\n\t" \
  "s_waitcnt lgkmcnt(13)\n\t" \
  "v_rcp_f32 v208, v160\n\t" \
  "v_mfma_f32_16x16x32_bf16 a[80:83], a[232:235], v[128:131], a[80:83]\n\t" \
  "s_waitcnt lgkmcnt(11)\n\t" \
  "v_cmp_lt_f32 vcc, 0x5f800000, v160\n\t" \
  "s_cbranch_vccnz .Lat_x0\n\t" \
  ".Lat_y0:\n\t" \
  "v_accvgpr_read_b32 v16, a0\n\t" \
  "v_mfma_f32_16x16x32_bf16 a[80:83], a[236:239], v[132:135], a[80:83]\n\t" \
  "s_waitcnt lgkmcnt(9)\n\t" \
  "ds_read_b64_tr_b16 v[54:55], v235 offset:12288\n\t" \
  "v_accvgpr_read_b32 v17, a1\n\t" \
  "v_accvgpr_read_b32 v18, a2\n\t" \
  "v_mfma_f32_16x16x32_bf16 a[96:99], a[240:243], v[128:131], a[96:99]\n\t" \
  "s_waitcnt lgkmcnt(8)\n\t" \
  "v_accvgpr_read_b32 v19, a3\n\t" \
  "v_accvgpr_read_b32 v20, a16\n\t" \
  "v_accvgpr_read_b32 v21, a17\n\t" \
  "v_accvgpr_read_b32 v22, a18\n\t" \
  "v_accvgpr_read_b32 v23, a19\n\t" \
  "v_pk_mul_f32 v[16:17], v[16:17], v[208:209] op_sel_hi:[1,0]\n\t" \
  "v_pk_mul_f32 v[18:19], v[18:19], v[208:209] op_sel_hi:[1,0]\n\t" \
  "v_pk_mul_f32 v[20:21], v[20:21], v[208:209] op_sel_hi:[1,0]\n\t" \
  "v_pk_mul_f32 v[22:23], v[22:23], v[208:209] op_sel_hi:[1,0]\n\t" \
  "v_cvt_pk_bf16_f32 v0, v16, v17\n\t" \
  "v_mfma_f32_16x16x32_bf16 a[96:99], a[244:247], v[132:135], a[96:99]\n\t" \
  "s_waitcnt lgkmcnt(6)\n\t" \
  "ds_read_b64_tr_b16 v[56:57], v235 offset:16384\n\t" \
  "v_cvt_pk_bf16_f32 v1, v18, v19\n\t" \
  "v_cvt_pk_bf16_f32 v2, v20, v21\n\t" \
  "v_cvt_pk_bf16_f32 v3, v22, v23\n\t" \
  "v_mfma_f32_16x16x32_bf16 a[112:115], a[248:251], v[128:131], a[112:115]\n\t" \
  "s_waitcnt lgkmcnt(5)\n\t" \
  "ds_read_b64_tr_b16 v[58:59], v235 offset:20480\n\t" \
  "v_max3_f32 v209, |v0|, |v1|, |v2|\n\t" \
  "v_mfma_f32_16x16x32_bf16 a[112:115], a[252:255], v[132:135], a[112:115]\n\t" \
  "s_waitcnt vmcnt(9)\n\t" \
  "s_barrier\n\t" \
  "v_mfma_f32_16x16x32_bf16 a[8:11], a[128:131], v[144:147], a[8:11]\n\t" \
  "s_add_u32 s97, s97, 7168\n\t" \
  "buffer_load_dwordx4 v241, %[wo], s97 offen offset:1024 lds\n\t" \
  "v_mfma_f32_16x16x32_bf16 a[8:11], a[132:135], v[148:151], a[8:11]\n\t" \
  "s_add_u32 s97, s97, 7168\n\t" \
  "v_cmp_lt_f32 vcc, 0x33800000, v209\n\t" \
  "s_cbranch_vccz .Lat_x4\n\t" \
  ".Lat_y4:\n\t" \
  "v_accvgpr_read_b32 v24, a32\n\t" \
  "v_mfma_f32_16x16x32_bf16 a[24:27], a[136:139], v[144:147], a[24:27]\n\t" \
  "buffer_load_dwordx4 v240, %[wo], s97 offen offset:2048 lds\n\t" \
  "v_accvgpr_read_b32 v25, a33\n\t" \
  "v_accvgpr_read_b32 v26, a34\n\t" \
  "v_mfma_f32_16x16x32_bf16 a[24:27], a[140:143], v[148:151], a[24:27]\n\t" \
  "s_add_u32 s97, s97, 7168\n\t" \
  "v_accvgpr_read_b32 v27, a35\n\t" \
  "v_accvgpr_read_b32 v28, a48\n\t" \
  "v_accvgpr_read_b32 v29, a49\n\t" \
  "v_accvgpr_read_b32 v30, a50\n\t" \
  "v_accvgpr_read_b32 v31, a51\n\t" \
  "v_pk_mul_f32 v[24:25], v[24:25], v[208:209] op_sel_hi:[1,0]\n\t" \
  "v_pk_mul_f32 v[26:27], v[26:27], v[208:209] op_sel_hi:[1,0]\n\t" \
  "v_pk_mul_f32 v[28:29], v[28:29], v[208:209] op_sel_hi:[1,0]\n\t" \
  "v_pk_mul_f32 v[30:31], v[30:31], v[208:209] op_sel_hi:[1,0]\n\t" \
  "v_cvt_pk_bf16_f32 v4, v24, v25\n\t" \
  "v_mfma_f32_16x16x32_bf16 a[40:43], a[144:147], v[144:147], a[40:43]\n\t" \
  "s_xor_b32 s98, %[eso], 0\n\t" \
  "ds_read_b64_tr_b16 v[60:61], v235 offset:24576\n\t" \
  "v_cvt_pk_bf16_f32 v5, v26, v27\n\t" \
  "v_cvt_pk_bf16_f32 v6, v28, v29\n\t" \
  "v_cvt_pk_bf16_f32 v7, v30, v31\n\t" \
  "v_mfma_f32_16x16x32_bf16 a[40:43], a[148:151], v[148:151], a[40:43]\n\t" \
  "buffer_load_dwordx4 v241, %[wo], s97 offen offset:3072 lds\n\t" \
  "v_accvgpr_read_b32 v16, a64\n\t" \
  "v_accvgpr_read_b32 v17, a65\n\t" \
  "v_accvgpr_read_b32 v18, a66\n\t" \
  "v_mfma_f32_16x16x32_bf16 a[56:59], a[152:155], v[144:147], a[56:59]\n\t" \
  "v_exp_f32 v96, v96\n\t" \
  "v_mfma_f32_16x16x32_bf16 a[56:59], a[156:159], v[148:151], a[56:59]\n\t" \
  "buffer_store_dwordx4 v[0:3], v242, %[ws], s98 offen offset:0 sc1\n\t" \
  "v_mfma_f32_16x16x32_bf16 a[72:75], a[160:163], v[144:147], a[72:75]\n\t" \
  "v_accvgpr_read_b32 v19, a67\n\t" \
  "v_accvgpr_read_b32 v20, a80\n\t" \
  "v_accvgpr_read_b32 v21, a81\n\t" \
  "v_accvgpr_read_b32 v22, a82\n\t" \
  "v_accvgpr_read_b32 v23, a83\n\t" \
  "v_pk_mul_f32 v[16:17], v[16:17], v[208:209] op_sel_hi:[1,0]\n\t" \
  "v_pk_mul_f32 v[18:19], v[18:19], v[208:209] op_sel_hi:[1,0]\n\t" \
  "v_pk_mul_f32 v[20:21], v[20:21], v[208:209] op_sel_hi:[1,0]\n\t" \
  "v_pk_mul_f32 v[22:23], v[22:23], v[208:209] op_sel_hi:[1,0]\n\t" \
  "v_cvt_pk_bf16_f32 v8, v16, v17\n\t" \
  "v_mfma_f32_16x16x32_bf16 a[72:75], a[164:167], v[148:151], a[72:75]\n\t" \
  "s_xor_b32 s98, %[eso], 0\n\t" \
  "ds_read_b64_tr_b16 v[62:63], v235 offset:28672\n\t" \
  "v_xor_b32 v236, 96, v234\n\t" \
  "v_mfma_f32_16x16x32_bf16 a[88:91], a[168:171], v[144:147], a[88:91]\n\t" \
  "buffer_store_dwordx4 v[4:7], v242, %[ws], s98 offen offset:1024 sc1\n\t" \
  "v_mfma_f32_16x16x32_bf16 a[88:91], a[172:175], v[148:151], a[88:91]\n\t" \
  "ds_read_b64_tr_b16 v[80:81], v236 offset:0\n\t" \
  "v_cvt_pk_bf16_f32 v9, v18, v19\n\t" \
  "v_cvt_pk_bf16_f32 v10, v20, v21\n\t" \
  "v_cvt_pk_bf16_f32 v11, v22, v23\n\t" \
  "v_mfma_f32_16x16x32_bf16 a[104:107], a[176:179], v[144:147], a[104:107]\n\t" \
  "ds_read_b64_tr_b16 v[82:83], v236 offset:4096\n\t" \
  "v_accvgpr_read_b32 v24, a96\n\t" \
  "v_mfma_f32_16x16x32_bf16 a[104:107], a[180:183], v[148:151], a[104:107]\n\t" \
  "ds_read_b64_tr_b16 v[84:85], v236 offset:8192\n\t" \
  "v_accvgpr_read_b32 v25, a97\n\t" \
  "v_accvgpr_read_b32 v26, a98\n\t" \
  "v_mfma_f32_16x16x32_bf16 a[120:123], a[184:187], v[144:147], a[120:123]\n\t" \
  "v_exp_f32 v97, v97\n\t" \
  "v_mfma_f32_16x16x32_bf16 a[120:123], a[188:191], v[148:151], a[120:123]\n\t" \
  "v_accvgpr_read_b32 v27, a99\n\t" \
  "v_accvgpr_read_b32 v28, a112\n\t" \
  "v_accvgpr_read_b32 v29, a113\n\t" \
  "v_accvgpr_read_b32 v30, a114\n\t" \
  "v_accvgpr_read_b32 v31, a115\n\t" \
  "v_pk_mul_f32 v[24:25], v[24:25], v[208:209] op_sel_hi:[1,0]\n\t" \
  "v_pk_mul_f32 v[26:27], v[26:27], v[208:209] op_sel_hi:[1,0]\n\t" \
  "v_pk_mul_f32 v[28:29], v[28:29], v[208:209] op_sel_hi:[1,0]\n\t" \
  "v_pk_mul_f32 v[30:31], v[30:31], v[208:209] op_sel_hi:[1,0]\n\t" \
  "v_cvt_pk_bf16_f32 v12, v24, v25\n\t" \
  "v_mfma_f32_16x16x32_bf16 v[168:171], v[216:219], v[144:147], v[168:171]\n\t" \
  "v_exp_f32 v98, v98\n\t" \
  "v_mfma_f32_16x16x32_bf16 v[168:171], v[216:219], v[148:151], v[168:171]\n\t" \
  "s_xor_b32 s98, %[eso], 0\n\t" \
  "v_exp_f32 v99, v99\n\t" \
  STS("s_memrealtime s[94:95]\n\t") \
  STS("s_memtime s[92:93]\n\t") \
  "v_mfma_f32_16x16x32_bf16 a[4:7], a[192:195], v[136:139], a[4:7]\n\t" \
  "buffer_store_dwordx4 v[8:11], v242, %[ws], s98 offen offset:2048 sc1\n\t" \
  "v_cvt_pk_bf16_f32 v144, v64, v65\n\t" \
  "v_mfma_f32_16x16x32_bf16 a[4:7], a[196:199], v[140:143], a[4:7]\n\t" \
  "ds_read_b64_tr_b16 v[86:87], v236 offset:12288\n\t" \
  "v_cvt_pk_bf16_f32 v13, v26, v27\n\t" \
  "v_cvt_pk_bf16_f32 v14, v28, v29\n\t" \
  "v_cvt_pk_bf16_f32 v15, v30, v31\n\t" \
  "v_cvt_pk_bf16_f32 v145, v66, v67\n\t" \
  "v_mfma_f32_16x16x32_bf16 v[164:167], v[216:219], v[136:139], v[164:167]\n\t" \
  "v_exp_f32 v100, v100\n\t" \
  "v_cvt_pk_bf16_f32 v146, v68, v69\n\t" \
  "v_mfma_f32_16x16x32_bf16 v[164:167], v[216:219], v[140:143], v[164:167]\n\t" \
  "ds_read_b64_tr_b16 v[88:89], v236 offset:16384\n\t" \
  "v_cvt_pk_bf16_f32 v147, v70, v71\n\t" \
  "v_mfma_f32_16x16x32_bf16 a[20:23], a[200:203], v[136:139], a[20:23]\n\t" \
  "v_exp_f32 v101, v101\n\t" \
  "v_cvt_pk_bf16_f32 v148, v72, v73\n\t" \
  "v_mfma_f32_16x16x32_bf16 a[20:23], a[204:207], v[140:143], a[20:23]\n\t" \
  "v_exp_f32 v102, v102\n\t" \
  "v_cvt_pk_bf16_f32 v149, v74, v75\n\t" \
  "v_mfma_f32_16x16x32_bf16 a[36:39], a[208:211], v[136:139], a[36:39]\n\t" \
  "ds_read_b64_tr_b16 v[90:91], v236 offset:20480\n\t" \
  "v_cvt_pk_bf16_f32 v150, v76, v77\n\t" \
  "v_mfma_f32_16x16x32_bf16 a[36:39], a[212:215], v[140:143], a[36:39]\n\t" \
  "s_xor_b32 s98, %[eso], 0\n\t" \
  "v_exp_f32 v103, v103\n\t" \
  "v_cvt_pk_bf16_f32 v151, v78, v79\n\t" \
  "v_mfma_f32_16x16x32_bf16 a[52:55], a[216:219], v[136:139], a[52:55]\n\t" \
  "buffer_store_dwordx4 v[12:15], v242, %[ws], s98 offen offset:3072 sc1\n\t" \
  "v_mfma_f32_16x16x32_bf16 a[52:55], a[220:223], v[140:143], a[52:55]\n\t" \
  "ds_read_b64_tr_b16 v[92:93], v236 offset:24576\n\t" \
  "ds_read_b64_tr_b16 v[94:95], v236 offset:28672\n\t" \
  "v_mfma_f32_16x16x32_bf16 a[68:71], a[224:227], v[136:139], a[68:71]\n\t" \
  "v_rcp_f32 v210, v164\n\t" \
  "v_mfma_f32_16x16x32_bf16 a[68:71], a[228:231], v[140:143], a[68:71]\n\t" \
  "v_cmp_lt_f32 vcc, 0x5f800000, v164\n\t" \
  "s_cbranch_vccnz .Lat_x1\n\t" \
  ".Lat_y1:\n\t" \
  "v_accvgpr_read_b32 v16, a4\n\t" \
  "v_mfma_f32_16x16x32_bf16 a[84:87], a[232:235], v[136:139], a[84:87]\n\t" \
  STS("v_writelane_b32 %[stv], s94, 20\n\t") \
  STS("v_writelane_b32 %[stv], s92, 21\n\t") \
  "v_accvgpr_read_b32 v17, a5\n\t" \
  "v_accvgpr_read_b32 v18, a6\n\t" \
  "v_mfma_f32_16x16x32_bf16 a[84:87], a[236:239], v[140:143], a[84:87]\n\t" \
  "v_accvgpr_read_b32 v19, a7\n\t" \
  "v_accvgpr_read_b32 v20, a20\n\t" \
  "v_accvgpr_read_b32 v21, a21\n\t" \
  "v_accvgpr_read_b32 v22, a22\n\t" \
  "v_accvgpr_read_b32 v23, a23\n\t" \
  "v_pk_mul_f32 v[16:17], v[16:17], v[210:211] op_sel_hi:[1,0]\n\t" \
  "v_pk_mul_f32 v[18:19], v[18:19], v[210:211] op_sel_hi:[1,0]\n\t" \
  "v_pk_mul_f32 v[20:21], v[20:21], v[210:211] op_sel_hi:[1,0]\n\t" \
  "v_pk_mul_f32 v[22:23], v[22:23], v[210:211] op_sel_hi:[1,0]\n\t" \
  "v_cvt_pk_bf16_f32 v32, v16, v17\n\t" \
  "v_mfma_f32_16x16x32_bf16 a[100:103], a[240:243], v[136:139], a[100:103]\n\t" \
  "v_xor_b32 v235, 128, v234\n\t" \
  "v_cvt_pk_bf16_f32 v33, v18, v19\n\t" \
  "v_cvt_pk_bf16_f32 v34, v20, v21\n\t" \
  "v_cvt_pk_bf16_f32 v35, v22, v23\n\t" \
  "v_mfma_f32_16x16x32_bf16 a[100:103], a[244:247], v[140:143], a[100:103]\n\t" \
  "s_xor_b32 s98, %[eso], 32768\n\t" \
  "ds_read_b64_tr_b16 v[112:113], v235 offset:0\n\t" \
  "v_accvgpr_read_b32 v24, a36\n\t" \
  "v_mfma_f32_16x16x32_bf16 a[116:119], a[248:251], v[136:139], a[116:119]\n\t" \
  "buffer_store_dwordx4 v[32:35], v242, %[ws], s98 offen offset:0 sc1\n\t" \
  "v_accvgpr_read_b32 v25, a37\n\t" \
  "v_accvgpr_read_b32 v26, a38\n\t" \
  "v_mfma_f32_16x16x32_bf16 a[116:119], a[252:255], v[140:143], a[116:119]\n\t" \
  "v_exp_f32 v104, v104\n\t" \
  "v_mfma_f32_16x16x32_bf16 a[12:15], a[128:131], v[152:155], a[12:15]\n\t" \
  "v_accvgpr_read_b32 v27, a39\n\t" \
  "v_accvgpr_read_b32 v28, a52\n\t" \
  "v_accvgpr_read_b32 v29, a53\n\t" \
  "v_accvgpr_read_b32 v30, a54\n\t" \
  "v_accvgpr_read_b32 v31, a55\n\t" \
  "v_pk_mul_f32 v[24:25], v[24:25], v[210:211] op_sel_hi:[1,0]\n\t" \
  "v_pk_mul_f32 v[26:27], v[26:27], v[210:211] op_sel_hi:[1,0]\n\t" \
  "v_pk_mul_f32 v[28:29], v[28:29], v[210:211] op_sel_hi:[1,0]\n\t" \
  "v_pk_mul_f32 v[30:31], v[30:31], v[210:211] op_sel_hi:[1,0]\n\t" \
  "v_cvt_pk_bf16_f32 v36, v24, v25\n\t" \
  "v_mfma_f32_16x16x32_bf16 a[12:15], a[132:135], v[156:159], a[12:15]\n\t" \
  "ds_read_b64_tr_b16 v[114:115], v235 offset:4096\n\t" \
  "v_cvt_pk_bf16_f32 v37, v26, v27\n\t" \
  "v_mfma_f32_16x16x32_bf16 a[28:31], a[136:139], v[152:155], a[28:31]\n\t" \
  "v_exp_f32 v105, v105\n\t" \
  "v_mfma_f32_16x16x32_bf16 a[28:31], a[140:143], v[156:159], a[28:31]\n\t" \
  "ds_read_b64_tr_b16 v[116:117], v235 offset:8192\n\t" \
  "v_cvt_pk_bf16_f32 v38, v28, v29\n\t" \
  "v_mfma_f32_16x16x32_bf16 a[44:47], a[144:147], v[152:155], a[44:47]\n\t" \
  "ds_read_b64_tr_b16 v[118:119], v235 offset:12288\n\t" \
  "v_cvt_pk_bf16_f32 v39, v30, v31\n\t" \
  "v_mfma_f32_16x16x32_bf16 a[44:47], a[148:151], v[156:159], a[44:47]\n\t" \
  "s_xor_b32 s98, %[eso], 32768\n\t" \
  "ds_read_b64_tr_b16 v[120:121], v235 offset:16384\n\t" \
  "v_accvgpr_read_b32 v16, a68\n\t" \
  "v_accvgpr_read_b32 v17, a69\n\t" \
  "v_accvgpr_read_b32 v18, a70\n\t" \
  "v_mfma_f32_16x16x32_bf16 a[60:63], a[152:155], v[152:155], a[60:63]\n\t" \
  "buffer_store_dwordx4 v[36:39], v242, %[ws], s98 offen offset:1024 sc1\n\t" \
  "v_mfma_f32_16x16x32_bf16 a[60:63], a[156:159], v[156:159], a[60:63]\n\t" \
  "v_accvgpr_read_b32 v19, a71\n\t" \
  "v_accvgpr_read_b32 v20, a84\n\t" \
  "v_accvgpr_read_b32 v21, a85\n\t" \
  "v_accvgpr_read_b32 v22, a86\n\t" \
  "v_accvgpr_read_b32 v23, a87\n\t" \
  "v_pk_mul_f32 v[16:17], v[16:17], v[210:211] op_sel_hi:[1,0]\n\t" \
  "v_pk_mul_f32 v[18:19], v[18:19], v[210:211] op_sel_hi:[1,0]\n\t" \
  "v_pk_mul_f32 v[20:21], v[20:21], v[210:211] op_sel_hi:[1,0]\n\t" \
  "v_pk_mul_f32 v[22:23], v[22:23], v[210:211] op_sel_hi:[1,0]\n\t" \
  "v_cvt_pk_bf16_f32 v40, v16, v17\n\t" \
  "v_mfma_f32_16x16x32_bf16 a[76:79], a[160:163], v[152:155], a[76:79]\n\t" \
  "v_exp_f32 v106, v106\n\t" \
  "v_mfma_f32_16x16x32_bf16 a[76:79], a[164:167], v[156:159], a[76:79]\n\t" \
  "v_exp_f32 v107, v107\n\t" \
  "v_mfma_f32_16x16x32_bf16 a[92:95], a[168:171], v[152:155], a[92:95]\n\t" \
  "ds_read_b64_tr_b16 v[122:123], v235 offset:20480\n\t" \
  "v_cvt_pk_bf16_f32 v41, v18, v19\n\t" \
  "v_mfma_f32_16x16x32_bf16 a[92:95], a[172:175], v[156:159], a[92:95]\n\t" \
  "ds_read_b64_tr_b16 v[124:125], v235 offset:24576\n\t" \
  "v_cvt_pk_bf16_f32 v42, v20, v21\n\t" \
  "v_cvt_pk_bf16_f32 v43, v22, v23\n\t" \
  "v_mfma_f32_16x16x32_bf16 a[108:111], a[176:179], v[152:155], a[108:111]\n\t" \
  "ds_read_b64_tr_b16 v[126:127], v235 offset:28672\n\t" \
  "v_accvgpr_read_b32 v24, a100\n\t" \
  "v_mfma_f32_16x16x32_bf16 a[108:111], a[180:183], v[156:159], a[108:111]\n\t" \
  "s_xor_b32 s98, %[eso], 32768\n\t" \
  "v_accvgpr_read_b32 v25, a101\n\t" \
  "v_accvgpr_read_b32 v26, a102\n\t" \
  "v_mfma_f32_16x16x32_bf16 a[124:127], a[184:187], v[152:155], a[124:127]\n\t" \
  "buffer_store_dwordx4 v[40:43], v242, %[ws], s98 offen offset:2048 sc1\n\t" \
  "v_mfma_f32_16x16x32_bf16 a[124:127], a[188:191], v[156:159], a[124:127]\n\t" \
  "v_accvgpr_read_b32 v27, a103\n\t" \
  "v_accvgpr_read_b32 v28, a116\n\t" \
  "v_accvgpr_read_b32 v29, a117\n\t" \
  "v_accvgpr_read_b32 v30, a118\n\t" \
  "v_accvgpr_read_b32 v31, a119\n\t" \
  "v_pk_mul_f32 v[24:25], v[24:25], v[210:211] op_sel_hi:[1,0]\n\t" \
  "v_pk_mul_f32 v[26:27], v[26:27], v[210:211] op_sel_hi:[1,0]\n\t" \
  "v_pk_mul_f32 v[28:29], v[28:29], v[210:211] op_sel_hi:[1,0]\n\t" \
  "v_pk_mul_f32 v[30:31], v[30:31], v[210:211] op_sel_hi:[1,0]\n\t" \
  "v_cvt_pk_bf16_f32 v44, v24, v25\n\t" \
  "v_mfma_f32_16x16x32_bf16 v[172:175], v[216:219], v[152:155], v[172:175]\n\t" \
  "v_exp_f32 v108, v108\n\t" \
  "v_mfma_f32_16x16x32_bf16 v[172:175], v[216:219], v[156:159], v[172:175]\n\t" \
  "ds_read_b64_tr_b16 a[128:129], v234 offset:0\n\t" \
  "v_cvt_pk_bf16_f32 v152, v96, v97\n\t" \
  "v_mfma_f32_16x16x32_bf16 a[8:11], a[192:195], v[144:147], a[8:11]\n\t" \
  "ds_read_b64_tr_b16 a[130:131], v234 offset:4096\n\t" \
  "v_cvt_pk_bf16_f32 v153, v98, v99\n\t" \
  "v_mfma_f32_16x16x32_bf16 a[8:11], a[196:199], v[148:151], a[8:11]\n\t" \
  "ds_read_b64_tr_b16 a[132:133], v234 offset:8192\n\t" \
  "v_cvt_pk_bf16_f32 v45, v26, v27\n\t" \
  "v_cvt_pk_bf16_f32 v46, v28, v29\n\t" \
  "v_cvt_pk_bf16_f32 v47, v30, v31\n\t" \
  "v_cvt_pk_bf16_f32 v154, v100, v101\n\t" \
  "v_mfma_f32_16x16x32_bf16 v[168:171], v[216:219], v[144:147], v[168:171]\n\t" \
  "v_exp_f32 v109, v109\n\t" \
  "v_cvt_pk_bf16_f32 v155, v102, v103\n\t" \
  "v_mfma_f32_16x16x32_bf16 v[168:171], v[216:219], v[148:151], v[168:171]\n\t" \
  "s_xor_b32 s98, %[eso], 32768\n\t" \
  "ds_read_b64_tr_b16 a[134:135], v234 offset:12288\n\t" \
  "v_cvt_pk_bf16_f32 v156, v104, v105\n\t" \
  "v_mfma_f32_16x16x32_bf16 a[24:27], a[200:203], v[144:147], a[24:27]\n\t" \
  "buffer_store_dwordx4 v[44:47], v242, %[ws], s98 offen offset:3072 sc1\n\t" \
  "v_cvt_pk_bf16_f32 v157, v106, v107\n\t" \
  "v_mfma_f32_16x16x32_bf16 a[24:27], a[204:207], v[148:151], a[24:27]\n\t" \
  "ds_read_b64_tr_b16 a[136:137], v234 offset:16384\n\t" \
  "v_cvt_pk_bf16_f32 v158, v108, v109\n\t" \
  "v_mfma_f32_16x16x32_bf16 a[40:43], a[208:211], v[144:147], a[40:43]\n\t" \
  "v_exp_f32 v110, v110\n\t" \
  "v_mfma_f32_16x16x32_bf16 a[40:43], a[212:215], v[148:151], a[40:43]\n\t" \
  "ds_read_b64_tr_b16 a[138:139], v234 offset:20480\n\t" \
  "ds_read_b64_tr_b16 a[140:141], v234 offset:24576\n\t" \
  "v_mfma_f32_16x16x32_bf16 a[56:59], a[216:219], v[144:147], a[56:59]\n\t" \
  "v_exp_f32 v111, v111\n\t" \
  "v_mfma_f32_16x16x32_bf16 a[56:59], a[220:223], v[148:151], a[56:59]\n\t" \
  "ds_read_b64_tr_b16 a[142:143], v234 offset:28672\n\t" \
  "v_xor_b32 v236, 32, v234\n\t" \
  "v_mfma_f32_16x16x32_bf16 a[72:75], a[224:227], v[144:147], a[72:75]\n\t" \
  "ds_read_b64_tr_b16 a[144:145], v236 offset:0\n\t" \
  "v_cvt_pk_bf16_f32 v159, v110, v111\n\t" \
  "v_mfma_f32_16x16x32_bf16 a[72:75], a[228:231], v[148:151], a[72:75]\n\t" \
  "ds_read_b64_tr_b16 a[146:147], v236 offset:4096\n\t" \
  "ds_read_b64_tr_b16 a[148:149], v236 offset:8192\n\t" \
  "v_mfma_f32_16x16x32_bf16 a[88:91], a[232:235], v[144:147], a[88:91]\n\t" \
  "v_rcp_f32 v212, v168\n\t" \
  "v_mfma_f32_16x16x32_bf16 a[88:91], a[236:239], v[148:151], a[88:91]\n\t" \
  "ds_read_b64_tr_b16 a[150:151], v236 offset:12288\n\t" \
  "ds_read_b64_tr_b16 a[152:153], v236 offset:16384\n\t" \
  "v_mfma_f32_16x16x32_bf16 a[104:107], a[240:243], v[144:147], a[104:107]\n\t" \
  "v_cmp_lt_f32 vcc, 0x5f800000, v168\n\t" \
  "s_cbranch_vccnz .Lat_x2\n\t" \
  ".Lat_y2:\n\t" \
  "v_accvgpr_read_b32 v16, a8\n\t" \
  "v_mfma_f32_16x16x32_bf16 a[104:107], a[244:247], v[148:151], a[104:107]\n\t" \
  "ds_read_b64_tr_b16 a[154:155], v236 offset:20480\n\t" \
  "v_accvgpr_read_b32 v17, a9\n\t" \
  "v_accvgpr_read_b32 v18, a10\n\t" \
  "v_mfma_f32_16x16x32_bf16 a[120:123], a[248:251], v[144:147], a[120:123]\n\t" \
  "v_accvgpr_read_b32 v19, a11\n\t" \
  "v_accvgpr_read_b32 v20, a24\n\t" \
  "v_accvgpr_read_b32 v21, a25\n\t" \
  "v_accvgpr_read_b32 v22, a26\n\t" \
  "v_accvgpr_read_b32 v23, a27\n\t" \
  "v_pk_mul_f32 v[16:17], v[16:17], v[212:213] op_sel_hi:[1,0]\n\t" \
  "v_pk_mul_f32 v[18:19], v[18:19], v[212:213] op_sel_hi:[1,0]\n\t" \
  "v_pk_mul_f32 v[20:21], v[20:21], v[212:213] op_sel_hi:[1,0]\n\t" \
  "v_pk_mul_f32 v[22:23], v[22:23], v[212:213] op_sel_hi:[1,0]\n\t" \
  "v_cvt_pk_bf16_f32 v64, v16, v17\n\t" \
  "v_mfma_f32_16x16x32_bf16 a[120:123], a[252:255], v[148:151], a[120:123]\n\t" \
  "ds_read_b64_tr_b16 a[156:157], v236 offset:24576\n\t" \
  "v_cvt_pk_bf16_f32 v65, v18, v19\n\t" \
  "v_cvt_pk_bf16_f32 v66, v20, v21\n\t" \
  "v_cvt_pk_bf16_f32 v67, v22, v23\n\t" \
  "v_accvgpr_read_b32 v24, a40\n\t" \
  "v_mfma_f32_16x16x32_bf16 a[12:15], a[192:195], v[152:155], a[12:15]\n\t" \
  "s_xor_b32 s98, %[eso], 65536\n\t" \
  "ds_read_b64_tr_b16 a[158:159], v236 offset:28672\n\t" \
  "v_accvgpr_read_b32 v25, a41\n\t" \
  "v_accvgpr_read_b32 v26, a42\n\t" \
  "v_mfma_f32_16x16x32_bf16 a[12:15], a[196:199], v[156:159], a[12:15]\n\t" \
  "buffer_store_dwordx4 v[64:67], v242, %[ws], s98 offen offset:0 sc1\n\t" \
  "v_mfma_f32_16x16x32_bf16 v[172:175], v[216:219], v[152:155], v[172:175]\n\t" \
  "v_accvgpr_read_b32 v27, a43\n\t" \
  "v_accvgpr_read_b32 v28, a56\n\t" \
  "v_accvgpr_read_b32 v29, a57\n\t" \
  "v_accvgpr_read_b32 v30, a58\n\t" \
  "v_accvgpr_read_b32 v31, a59\n\t" \
  "v_pk_mul_f32 v[24:25], v[24:25], v[212:213] op_sel_hi:[1,0]\n\t" \
  "v_pk_mul_f32 v[26:27], v[26:27], v[212:213] op_sel_hi:[1,0]\n\t" \
  "v_pk_mul_f32 v[28:29], v[28:29], v[212:213] op_sel_hi:[1,0]\n\t" \
  "v_pk_mul_f32 v[30:31], v[30:31], v[212:213] op_sel_hi:[1,0]\n\t" \
  "v_cvt_pk_bf16_f32 v68, v24, v25\n\t" \
  "v_mfma_f32_16x16x32_bf16 v[172:175], v[216:219], v[156:159], v[172:175]\n\t" \
  "v_xor_b32 v235, 192, v234\n\t" \
  "v_cvt_pk_bf16_f32 v69, v26, v27\n\t" \
  "v_mfma_f32_16x16x32_bf16 a[28:31], a[200:203], v[152:155], a[28:31]\n\t" \
  "ds_read_b64_tr_b16 a[160:161], v235 offset:0\n\t" \
  "v_cvt_pk_bf16_f32 v70, v28, v29\n\t" \
  "v_cvt_pk_bf16_f32 v71, v30, v31\n\t" \
  ".Lhs10:\n\ts_cbranch_execz .Lhs10n\n\t" \
  "v_mfma_f32_16x16x32_bf16 a[28:31], a[204:207], v[156:159], a[28:31]\n\t" \
  "s_xor_b32 s98, %[eso], 65536\n\t" \
  "ds_read_b64_tr_b16 a[162:163], v235 offset:4096\n\t" \
  "v_accvgpr_read_b32 v16, a72\n\t" \
  "v_accvgpr_read_b32 v17, a73\n\t" \
  "v_accvgpr_read_b32 v18, a74\n\t" \
  "v_mfma_f32_16x16x32_bf16 a[44:47], a[208:211], v[152:155], a[44:47]\n\t" \
  "buffer_store_dwordx4 v[68:71], v242, %[ws], s98 offen offset:1024 sc1\n\t" \
  "v_mfma_f32_16x16x32_bf16 a[44:47], a[212:215], v[156:159], a[44:47]\n\t" \
  "v_accvgpr_read_b32 v19, a75\n\t" \
  "v_accvgpr_read_b32 v20, a88\n\t" \
  "v_accvgpr_read_b32 v21, a89\n\t" \
  "v_accvgpr_read_b32 v22, a90\n\t" \
  "v_accvgpr_read_b32 v23, a91\n\t" \
  "v_pk_mul_f32 v[16:17], v[16:17], v[212:213] op_sel_hi:[1,0]\n\t" \
  "v_pk_mul_f32 v[18:19], v[18:19], v[212:213] op_sel_hi:[1,0]\n\t" \
  "v_pk_mul_f32 v[20:21], v[20:21], v[212:213] op_sel_hi:[1,0]\n\t" \
  "v_pk_mul_f32 v[22:23], v[22:23], v[212:213] op_sel_hi:[1,0]\n\t" \
  "v_cvt_pk_bf16_f32 v72, v16, v17\n\t" \
  "v_mfma_f32_16x16x32_bf16 a[60:63], a[216:219], v[152:155], a[60:63]\n\t" \
  "ds_read_b64_tr_b16 a[164:165], v235 offset:8192\n\t" \
  "v_cvt_pk_bf16_f32 v73, v18, v19\n\t" \
  "v_mfma_f32_16x16x32_bf16 a[60:63], a[220:223], v[156:159], a[60:63]\n\t" \
  "ds_read_b64_tr_b16 a[166:167], v235 offset:12288\n\t" \
  "v_cvt_pk_bf16_f32 v74, v20, v21\n\t" \
  "v_mfma_f32_16x16x32_bf16 a[76:79], a[224:227], v[152:155], a[76:79]\n\t" \
  "ds_read_b64_tr_b16 a[168:169], v235 offset:16384\n\t" \
  "v_cvt_pk_bf16_f32 v75, v22, v23\n\t" \
  "v_mfma_f32_16x16x32_bf16 a[76:79], a[228:231], v[156:159], a[76:79]\n\t" \
  "s_xor_b32 s98, %[eso], 65536\n\t" \
  "ds_read_b64_tr_b16 a[170:171], v235 offset:20480\n\t" \
  "v_accvgpr_read_b32 v24, a104\n\t" \
  "v_mfma_f32_16x16x32_bf16 a[92:95], a[232:235], v[152:155], a[92:95]\n\t" \
  "buffer_store_dwordx4 v[72:75], v242, %[ws], s98 offen offset:2048 sc1\n\t" \
  "v_mfma_f32_16x16x32_bf16 a[92:95], a[236:239], v[156:159], a[92:95]\n\t" \
  "ds_read_b64_tr_b16 a[172:173], v235 offset:24576\n\t" \
  "v_accvgpr_read_b32 v25, a105\n\t" \
  "v_accvgpr_read_b32 v26, a106\n\t" \
  "v_mfma_f32_16x16x32_bf16 a[108:111], a[240:243], v[152:155], a[108:111]\n\t" \
  "ds_read_b64_tr_b16 a[174:175], v235 offset:28672\n\t" \
  "v_xor_b32 v236, 224, v234\n\t" \
  "v_mfma_f32_16x16x32_bf16 a[108:111], a[244:247], v[156:159], a[108:111]\n\t" \
  "v_accvgpr_read_b32 v27, a107\n\t" \
  "v_accvgpr_read_b32 v28, a120\n\t" \
  "v_accvgpr_read_b32 v29, a121\n\t" \
  "v_accvgpr_read_b32 v30, a122\n\t" \
  "v_accvgpr_read_b32 v31, a123\n\t" \
  "v_pk_mul_f32 v[24:25], v[24:25], v[212:213] op_sel_hi:[1,0]\n\t" \
  "v_pk_mul_f32 v[26:27], v[26:27], v[212:213] op_sel_hi:[1,0]\n\t" \
  "v_pk_mul_f32 v[28:29], v[28:29], v[212:213] op_sel_hi:[1,0]\n\t" \
  "v_pk_mul_f32 v[30:31], v[30:31], v[212:213] op_sel_hi:[1,0]\n\t" \
  "v_cvt_pk_bf16_f32 v76, v24, v25\n\t" \
  "v_mfma_f32_16x16x32_bf16 a[124:127], a[248:251], v[152:155], a[124:127]\n\t" \
  "ds_read_b64_tr_b16 a[176:177], v236 offset:0\n\t" \
  "v_cvt_pk_bf16_f32 v77, v26, v27\n\t" \
  "v_mfma_f32_16x16x32_bf16 a[124:127], a[252:255], v[156:159], a[124:127]\n\t" \
  "s_waitcnt lgkmcnt(15)\n\t" \
  "ds_read_b64_tr_b16 a[178:179], v236 offset:4096\n\t" \
  "v_cvt_pk_bf16_f32 v78, v28, v29\n\t" \
  "v_mfma_f32_16x16x32_bf16 a[32:35], v[48:51], v[0:3], 0\n\t" \
  "ds_read_b64_tr_b16 a[180:181], v236 offset:8192\n\t" \
  "v_cvt_pk_bf16_f32 v79, v30, v31\n\t" \
  "v_mfma_f32_16x16x32_bf16 a[32:35], v[52:55], v[4:7], a[32:35]\n\t" \
  "s_xor_b32 s98, %[eso], 65536\n\t" \
  "ds_read_b64_tr_b16 a[182:183], v236 offset:12288\n\t" \
  "ds_read_b64_tr_b16 a[184:185], v236 offset:16384\n\t" \
  "v_mfma_f32_16x16x32_bf16 a[32:35], v[56:59], v[8:11], a[32:35]\n\t" \
  "buffer_store_dwordx4 v[76:79], v242, %[ws], s98 offen offset:3072 sc1\n\t" \
  "v_mfma_f32_16x16x32_bf16 a[32:35], v[60:63], v[12:15], a[32:35]\n\t" \
  "ds_read_b64_tr_b16 a[186:187], v236 offset:20480\n\t" \
  "ds_read_b64_tr_b16 a[188:189], v236 offset:24576\n\t" \
  "v_mfma_f32_16x16x32_bf16 a[48:51], v[92:95], v[12:15], 0\n\t" \
  "ds_read_b64_tr_b16 a[190:191], v236 offset:28672\n\t" \
  "v_xor_b32 v236, 160, v234\n\t" \
  "v_mfma_f32_16x16x32_bf16 a[48:51], v[88:91], v[8:11], a[48:51]\n\t" \
  "ds_read_b64_tr_b16 v[96:97], v236 offset:0\n\t" \
  "ds_read_b64_tr_b16 v[98:99], v236 offset:4096\n\t" \
  "v_mfma_f32_16x16x32_bf16 a[48:51], v[84:87], v[4:7], a[48:51]\n\t" \
  "v_rcp_f32 v214, v172\n\t" \
  "v_mfma_f32_16x16x32_bf16 a[48:51], v[80:83], v[0:3], a[48:51]\n\t" \
  "v_cmp_lt_f32 vcc, 0x5f800000, v172\n\t" \
  "s_cbranch_vccnz .Lat_x3\n\t" \
  ".Lat_y3:\n\t" \
  "v_mfma_f32_16x16x32_bf16 a[64:67], v[112:115], v[0:3], 0\n\t" \
  "ds_read_b64_tr_b16 v[100:101], v236 offset:8192\n\t" \
  "v_accvgpr_read_b32 v16, a12\n\t" \
  "v_mfma_f32_16x16x32_bf16 a[64:67], v[116:119], v[4:7], a[64:67]\n\t" \
  "ds_read_b64_tr_b16 v[102:103], v236 offset:12288\n\t" \
  "v_accvgpr_read_b32 v17, a13\n\t" \
  "v_mfma_f32_16x16x32_bf16 a[64:67], v[120:123], v[8:11], a[64:67]\n\t" \
  "ds_read_b64_tr_b16 v[104:105], v236 offset:16384\n\t" \
  "v_accvgpr_read_b32 v18, a14\n\t" \
  "v_mfma_f32_16x16x32_bf16 a[64:67], v[124:127], v[12:15], a[64:67]\n\t" \
  "ds_read_b64_tr_b16 v[106:107], v236 offset:20480\n\t" \
  "v_accvgpr_read_b32 v19, a15\n\t" \
  "v_mfma_f32_16x16x32_bf16 a[0:3], a[140:143], v[12:15], 0\n\t" \
  "ds_read_b64_tr_b16 v[108:109], v236 offset:24576\n\t" \
  "v_accvgpr_read_b32 v20, a28\n\t" \
  "v_mfma_f32_16x16x32_bf16 a[0:3], a[136:139], v[8:11], a[0:3]\n\t" \
  "ds_read_b64_tr_b16 v[110:111], v236 offset:28672\n\t" \
  "v_accvgpr_read_b32 v21, a29\n\t" \
  "v_mfma_f32_16x16x32_bf16 a[0:3], a[132:135], v[4:7], a[0:3]\n\t" \
  "ds_read_b128 a[224:227], v213 offset:32768\n\t" \
  "v_accvgpr_read_b32 v22, a30\n\t" \
  "v_mfma_f32_16x16x32_bf16 a[0:3], a[128:131], v[0:3], a[0:3]\n\t" \
  "ds_read_b128 a[228:231], v213 offset:33792\n\t" \
  "v_accvgpr_read_b32 v23, a31\n\t" \
  "v_mfma_f32_16x16x32_bf16 a[16:19], a[144:147], v[0:3], 0\n\t" \
  "s_waitcnt lgkmcnt(15)\n\t" \
  "ds_read_b128 a[232:235], v213 offset:34816\n\t" \
  "v_mul_f32 v16, v16, v214\n\t" \
  "v_mfma_f32_16x16x32_bf16 a[16:19], a[148:151], v[4:7], a[16:19]\n\t" \
  "ds_read_b128 a[236:239], v213 offset:35840\n\t" \
  "v_mul_f32 v17, v17, v214\n\t" \
  "v_mfma_f32_16x16x32_bf16 a[16:19], a[152:155], v[8:11], a[16:19]\n\t" \
  "ds_read_b128 a[240:243], v215 offset:0\n\t" \
  "v_mul_f32 v18, v18, v214\n\t" \
  "v_mfma_f32_16x16x32_bf16 a[16:19], a[156:159], v[12:15], a[16:19]\n\t" \
  "ds_read_b128 a[244:247], v215 offset:1024\n\t" \
  "v_mul_f32 v19, v19, v214\n\t" \
  "v_mfma_f32_16x16x32_bf16 a[96:99], a[172:175], v[12:15], 0\n\t" \
  "buffer_load_dwordx4 v[224:227], v252, %[ws], %[ss] offen sc1\n\t" \
  "v_mfma_f32_16x16x32_bf16 a[96:99], a[168:171], v[8:11], a[96:99]\n\t" \
  "ds_read_b128 a[248:251], v215 offset:2048\n\t" \
  "v_mul_f32 v20, v20, v214\n\t" \
  "v_mfma_f32_16x16x32_bf16 a[96:99], a[164:167], v[4:7], a[96:99]\n\t" \
  "ds_read_b128 a[252:255], v215 offset:3072\n\t" \
  "v_mul_f32 v21, v21, v214\n\t" \
  "v_mfma_f32_16x16x32_bf16 a[96:99], a[160:163], v[0:3], a[96:99]\n\t" \
  "v_mul_f32 v22, v22, v214\n\t" \
  "v_mul_f32 v23, v23, v214\n\t" \
  "v_mfma_f32_16x16x32_bf16 a[112:115], a[176:179], v[0:3], 0\n\t" \
  "s_waitcnt lgkmcnt(15)\n\t" \
  "buffer_load_dwordx4 v[128:131], v247, %[sM], %[gvo] offen\n\t" \
  "v_mfma_f32_16x16x32_bf16 a[112:115], a[180:183], v[4:7], a[112:115]\n\t" \
  "s_add_u32 s98, %[gvo], 12288\n\t" \
  "v_cvt_pk_bf16_f32 v160, v16, v17\n\t" \
  "v_cvt_pk_bf16_f32 v161, v18, v19\n\t" \
  "v_mfma_f32_16x16x32_bf16 a[112:115], a[184:187], v[8:11], a[112:115]\n\t" \
  "v_cvt_pk_bf16_f32 v162, v20, v21\n\t" \
  "v_cvt_pk_bf16_f32 v163, v22, v23\n\t" \
  "v_mfma_f32_16x16x32_bf16 a[112:115], a[188:191], v[12:15], a[112:115]\n\t" \
  "s_waitcnt lgkmcnt(8)\n\t" \
  "s_waitcnt lgkmcnt(4)\n\t" \
  "ds_write_b128 v213, v[160:163] offset:32768\n\t" \
  "v_mfma_f32_16x16x32_bf16 a[80:83], v[108:111], v[12:15], 0\n\t" \
  "buffer_load_dwordx4 v[132:135], v247, %[sM], s98 offen\n\t" \
  "v_mfma_f32_16x16x32_bf16 a[80:83], v[104:107], v[8:11], a[80:83]\n\t" \
  "s_add_u32 s98, %[gvo], 24576\n\t" \
  "v_accvgpr_read_b32 v24, a44\n\t" \
  "v_accvgpr_read_b32 v25, a45\n\t" \
  "v_mfma_f32_16x16x32_bf16 a[80:83], v[100:103], v[4:7], a[80:83]\n\t" \
  "v_accvgpr_read_b32 v26, a46\n\t" \
  "v_accvgpr_read_b32 v27, a47\n\t" \
  "v_mfma_f32_16x16x32_bf16 a[80:83], v[96:99], v[0:3], a[80:83]\n\t" \
  "v_accvgpr_read_b32 v28, a60\n\t" \
  "v_accvgpr_read_b32 v29, a61\n\t" \
  "v_mfma_f32_16x16x32_bf16 a[36:39], v[48:51], v[32:35], 0\n\t" \
  "buffer_load_dwordx4 v[136:139], v247, %[sM], s98 offen\n\t" \
  "v_mfma_f32_16x16x32_bf16 a[36:39], v[52:55], v[36:39], a[36:39]\n\t" \
  "s_add_u32 s98, %[gvo], 36864\n\t" \
  "v_accvgpr_read_b32 v30, a62\n\t" \
  "v_accvgpr_read_b32 v31, a63\n\t" \
  "v_mfma_f32_16x16x32_bf16 a[36:39], v[56:59], v[40:43], a[36:39]\n\t" \
  "v_mul_f32 v24, v24, v214\n\t" \
  "v_mul_f32 v25, v25, v214\n\t" \
  "v_mfma_f32_16x16x32_bf16 a[36:39], v[60:63], v[44:47], a[36:39]\n\t" \
  "v_mul_f32 v26, v26, v214\n\t" \
  "v_mul_f32 v27, v27, v214\n\t" \
  "s_waitcnt vmcnt(8)\n\t" \
  "buffer_store_dword v243, v254, %[ws], %[fw2] offen offset:0 sc1\n\t" \
  "v_mfma_f32_16x16x32_bf16 a[52:55], v[92:95], v[44:47], 0\n\t" \
  "buffer_load_dwordx4 v[140:143], v247, %[sM], s98 offen\n\t" \
  "v_mfma_f32_16x16x32_bf16 a[52:55], v[88:91], v[40:43], a[52:55]\n\t" \
  "s_add_u32 s98, %[gvo], 49152\n\t" \
  "v_mul_f32 v28, v28, v214\n\t" \
  "v_mul_f32 v29, v29, v214\n\t" \
  "v_mfma_f32_16x16x32_bf16 a[52:55], v[84:87], v[36:39], a[52:55]\n\t" \
  "v_mul_f32 v30, v30, v214\n\t" \
  "v_mul_f32 v31, v31, v214\n\t" \
  "v_mfma_f32_16x16x32_bf16 a[52:55], v[80:83], v[32:35], a[52:55]\n\t" \
  "v_cvt_pk_bf16_f32 v164, v24, v25\n\t" \
  "v_cvt_pk_bf16_f32 v165, v26, v27\n\t" \
  "v_mfma_f32_16x16x32_bf16 a[68:71], v[112:115], v[32:35], 0\n\t" \
  "buffer_load_dwordx4 v[144:147], v247, %[sM], s98 offen\n\t" \
  "v_mfma_f32_16x16x32_bf16 a[68:71], v[116:119], v[36:39], a[68:71]\n\t" \
  "s_add_u32 s98, %[gvo], 61440\n\t" \
  "v_cvt_pk_bf16_f32 v166, v28, v29\n\t" \
  "v_cvt_pk_bf16_f32 v167, v30, v31\n\t" \
  "v_mfma_f32_16x16x32_bf16 a[68:71], v[120:123], v[40:43], a[68:71]\n\t" \
  "ds_write_b128 v213, v[164:167] offset:33792\n\t" \
  "v_mfma_f32_16x16x32_bf16 a[68:71], v[124:127], v[44:47], a[68:71]\n\t" \
  "v_accvgpr_read_b32 v16, a76\n\t" \
  "v_accvgpr_read_b32 v17, a77\n\t" \
  "v_mfma_f32_16x16x32_bf16 a[4:7], a[140:143], v[44:47], 0\n\t" \
  "buffer_load_dwordx4 v[148:151], v247, %[sM], s98 offen\n\t" \
  "v_mfma_f32_16x16x32_bf16 a[4:7], a[136:139], v[40:43], a[4:7]\n\t" \
  "s_add_u32 s98, %[gvo], 73728\n\t" \
  "v_accvgpr_read_b32 v18, a78\n\t" \
  "v_accvgpr_read_b32 v19, a79\n\t" \
  "v_mfma_f32_16x16x32_bf16 a[4:7], a[132:135], v[36:39], a[4:7]\n\t" \
  "v_accvgpr_read_b32 v20, a92\n\t" \
  "v_accvgpr_read_b32 v21, a93\n\t" \
  "v_mfma_f32_16x16x32_bf16 a[4:7], a[128:131], v[32:35], a[4:7]\n\t" \
  "v_accvgpr_read_b32 v22, a94\n\t" \
  "v_accvgpr_read_b32 v23, a95\n\t" \
  "v_mfma_f32_16x16x32_bf16 a[20:23], a[144:147], v[32:35], 0\n\t" \
  "buffer_load_dwordx4 v[152:155], v247, %[sM], s98 offen\n\t" \
  "v_mfma_f32_16x16x32_bf16 a[20:23], a[148:151], v[36:39], a[20:23]\n\t" \
  "s_add_u32 s98, %[gvo], 86016\n\t" \
  "v_mul_f32 v16, v16, v214\n\t" \
  "v_mul_f32 v17, v17, v214\n\t" \
  "v_mfma_f32_16x16x32_bf16 a[20:23], a[152:155], v[40:43], a[20:23]\n\t" \
  "v_mul_f32 v18, v18, v214\n\t" \
  "v_mul_f32 v19, v19, v214\n\t" \
  "v_mfma_f32_16x16x32_bf16 a[20:23], a[156:159], v[44:47], a[20:23]\n\t" \
  "v_mul_f32 v20, v20, v214\n\t" \
  "v_mul_f32 v21, v21, v214\n\t" \
  "v_mfma_f32_16x16x32_bf16 a[100:103], a[172:175], v[44:47], 0\n\t" \
  "buffer_load_dwordx4 v[156:159], v247, %[sM], s98 offen\n\t" \
  "v_mfma_f32_16x16x32_bf16 a[100:103], a[168:171], v[40:43], a[100:103]\n\t" \
  "v_mul_f32 v22, v22, v214\n\t" \
  "v_mul_f32 v23, v23, v214\n\t" \
  "v_mfma_f32_16x16x32_bf16 a[100:103], a[164:167], v[36:39], a[100:103]\n\t" \
  "v_cvt_pk_bf16_f32 v168, v16, v17\n\t" \
  "v_cvt_pk_bf16_f32 v169, v18, v19\n\t" \
  "v_mfma_f32_16x16x32_bf16 a[100:103], a[160:163], v[32:35], a[100:103]\n\t" \
  "v_cvt_pk_bf16_f32 v170, v20, v21\n\t" \
  "v_cvt_pk_bf16_f32 v171, v22, v23\n\t" \
  "v_mfma_f32_16x16x32_bf16 a[116:119], a[176:179], v[32:35], 0\n\t" \
  "ds_write_b128 v213, v[168:171] offset:34816\n\t" \
  "v_mfma_f32_16x16x32_bf16 a[116:119], a[180:183], v[36:39], a[116:119]\n\t" \
  "v_accvgpr_read_b32 v24, a108\n\t" \
  "v_accvgpr_read_b32 v25, a109\n\t" \
  "v_mfma_f32_16x16x32_bf16 a[116:119], a[184:187], v[40:43], a[116:119]\n\t" \
  "v_accvgpr_read_b32 v26, a110\n\t" \
  "v_accvgpr_read_b32 v27, a111\n\t" \
  "v_mfma_f32_16x16x32_bf16 a[116:119], a[188:191], v[44:47], a[116:119]\n\t" \
  "v_accvgpr_read_b32 v28, a124\n\t" \
  "v_accvgpr_read_b32 v29, a125\n\t" \
  "v_mfma_f32_16x16x32_bf16 a[84:87], v[108:111], v[44:47], 0\n\t" \
  "s_add_u32 m0, %[m], 147456\n\t" \
  "v_accvgpr_read_b32 v30, a126\n\t" \
  "v_accvgpr_read_b32 v31, a127\n\t" \
  "v_mfma_f32_16x16x32_bf16 a[84:87], v[104:107], v[40:43], a[84:87]\n\t" \
  "s_xor_b32 s97, %[wob], 655360\n\t" \
  "v_mul_f32 v24, v24, v214\n\t" \
  "v_mul_f32 v25, v25, v214\n\t" \
  "v_mfma_f32_16x16x32_bf16 a[84:87], v[100:103], v[36:39], a[84:87]\n\t" \
  "v_mul_f32 v26, v26, v214\n\t" \
  "v_mul_f32 v27, v27, v214\n\t" \
  "v_mfma_f32_16x16x32_bf16 a[84:87], v[96:99], v[32:35], a[84:87]\n\t" \
  "v_mul_f32 v28, v28, v214\n\t" \
  "v_mul_f32 v29, v29, v214\n\t" \
  "v_mfma_f32_16x16x32_bf16 a[40:43], v[48:51], v[64:67], 0\n\t" \
  "v_mul_f32 v30, v30, v214\n\t" \
  "v_mul_f32 v31, v31, v214\n\t" \
  "v_mfma_f32_16x16x32_bf16 a[40:43], v[52:55], v[68:71], a[40:43]\n\t" \
  "v_cvt_pk_bf16_f32 v172, v24, v25\n\t" \
  "v_cvt_pk_bf16_f32 v173, v26, v27\n\t" \
  "v_mfma_f32_16x16x32_bf16 a[40:43], v[56:59], v[72:75], a[40:43]\n\t" \
  "buffer_load_dwordx4 v240, %[wo], s97 offen offset:0 lds\n\t" \
  "v_mfma_f32_16x16x32_bf16 a[40:43], v[60:63], v[76:79], a[40:43]\n\t" \
  "s_add_u32 s97, s97, 7168\n\t" \
  "v_cvt_pk_bf16_f32 v174, v28, v29\n\t" \
  "v_cvt_pk_bf16_f32 v175, v30, v31\n\t" \
  "v_mfma_f32_16x16x32_bf16 a[56:59], v[92:95], v[76:79], 0\n\t" \
  "ds_write_b128 v213, v[172:175] offset:35840\n\t" \
  "v_mfma_f32_16x16x32_bf16 a[56:59], v[88:91], v[72:75], a[56:59]\n\t" \
  "buffer_load_dwordx4 v241, %[wo], s97 offen offset:1024 lds\n\t" \
  "v_mfma_f32_16x16x32_bf16 a[56:59], v[84:87], v[68:71], a[56:59]\n\t" \
  "s_add_u32 s97, s97, 7168\n\t" \
  "v_mfma_f32_16x16x32_bf16 a[56:59], v[80:83], v[64:67], a[56:59]\n\t" \
  "v_mfma_f32_16x16x32_bf16 a[72:75], v[112:115], v[64:67], 0\n\t" \
  "buffer_load_dwordx4 v240, %[wo], s97 offen offset:2048 lds\n\t" \
  "v_mfma_f32_16x16x32_bf16 a[72:75], v[116:119], v[68:71], a[72:75]\n\t" \
  "s_add_u32 s97, s97, 7168\n\t" \
  "v_mfma_f32_16x16x32_bf16 a[72:75], v[120:123], v[72:75], a[72:75]\n\t" \
  "v_mfma_f32_16x16x32_bf16 a[72:75], v[124:127], v[76:79], a[72:75]\n\t" \
  "buffer_load_dwordx4 v241, %[wo], s97 offen offset:3072 lds\n\t" \
  STS("s_memrealtime s[94:95]\n\t") \
  STS("s_memtime s[92:93]\n\t") \
  STS("s_waitcnt lgkmcnt(0)\n\t") \
  STS("v_writelane_b32 %[stv], s94, 22\n\t") \
  STS("v_writelane_b32 %[stv], s92, 23\n\t") \
  ".subsection 1\n\t" \
  "s_endpgm\n\t" \
  ".Lat_v:\n\t" \
  "s_mov_b32 s98, 0\n\t" \
  ".Lat_v1:\n\t" \
  "s_sleep 4\n\t" \
  "buffer_load_dword v209, v205, %[ws], 0 offen sc1\n\t" \
  "s_waitcnt vmcnt(0)\n\t" \
  "v_and_b32 v206, 0xbfffbfff, v209\n\t" \
  "v_cmp_ne_u32_e64 s[96:97], %[tag], v206\n\t" \
  "s_cmp_lg_u64 s[96:97], 0\n\t" \
  "s_cbranch_scc0 .Lat_w\n\t" \
  "s_add_u32 s98, s98, 1\n\t" \
  "s_cmp_lt_u32 s98, %[cap]\n\t" \
  "s_cbranch_scc1 .Lat_v1\n\t" \
  "v_mov_b32 v206, %[tag]\n\t" \
  "buffer_store_dword v206, off, %[ws], 0 offset:256 sc1\n\t" \
  "s_branch .Lat_w\n\t" \
  ".Lat_x0:\n\t" \
  "v_lshlrev_b32 v209, 12, v233\n\t" \
  "v_bfrev_b32 v254, 1\n\t" \
  "s_mov_b64 exec, 15\n\t" \
  "ds_write_b32 v209, v243 offset:49152\n\t" \
  "s_mov_b64 exec, -1\n\t" \
  "s_waitcnt lgkmcnt(0)\n\t" \
  "s_branch .Lat_y0\n\t" \
  ".Lat_x4:\n\t" \
  "v_lshlrev_b32 v209, 12, v233\n\t" \
  "v_bfrev_b32 v254, 1\n\t" \
  "s_mov_b64 exec, 15\n\t" \
  "ds_write_b32 v209, v243 offset:49152\n\t" \
  "s_mov_b64 exec, -1\n\t" \
  "s_waitcnt lgkmcnt(0)\n\t" \
  "s_branch .Lat_y4\n\t" \
  ".Lat_x1:\n\t" \
  "v_lshlrev_b32 v209, 12, v233\n\t" \
  "v_bfrev_b32 v254, 1\n\t" \
  "s_mov_b64 exec, 15\n\t" \
  "ds_write_b32 v209, v243 offset:49152\n\t" \
  "s_mov_b64 exec, -1\n\t" \
  "s_waitcnt lgkmcnt(0)\n\t" \
  "s_branch .Lat_y1\n\t" \
  ".Lat_x2:\n\t" \
  "v_lshlrev_b32 v209, 12, v233\n\t" \
  "v_bfrev_b32 v254, 1\n\t" \
  "s_mov_b64 exec, 15\n\t" \
  "ds_write_b32 v209, v243 offset:49152\n\t" \
  "s_mov_b64 exec, -1\n\t" \
  "s_waitcnt lgkmcnt(0)\n\t" \
  "s_branch .Lat_y2\n\t" \
  ".Lat_x3:\n\t" \
  "v_lshlrev_b32 v209, 12, v233\n\t" \
  "v_bfrev_b32 v254, 1\n\t" \
  "s_mov_b64 exec, 15\n\t" \
  "ds_write_b32 v209, v243 offset:49152\n\t" \
  "s_mov_b64 exec, -1\n\t" \
  "s_waitcnt lgkmcnt(0)\n\t" \
  "s_branch .Lat_y3\n\t" \
  ".subsection 2\n\t" \
  ".Lsf_go:\n\t" \
  "v_mfma_f32_16x16x32_bf16 v[28:31], a[156:159], a[224:227], v[28:31]\n\t" \
  "v_mov_b32 v216, 0x3f803f80\n\t" \
  "v_lshlrev_b32 v204, 7, v233\n\t" \
  "v_mfma_f32_16x16x32_bf16 v[28:31], a[92:95], a[240:243], v[28:31]\n\t" \
  "v_cmp_gt_u32 vcc, 16, v233\n\t" \
  "v_lshrrev_b32 v202, 3, v233\n\t" \
  "v_mfma_f32_16x16x32_bf16 v[24:27], a[152:155], a[224:227], v[24:27]\n\t" \
  "v_mul_u32_u24 v202, 11264, v202\n\t" \
  "v_add_u32 v202, v202, v204\n\t" \
  "v_mfma_f32_16x16x32_bf16 v[24:27], a[88:91], a[240:243], v[24:27]\n\t" \
  "v_bfrev_b32 v195, 1\n\t" \
  "v_mov_b32 v217, 0x3f803f80\n\t" \
  "v_mfma_f32_16x16x32_bf16 v[20:23], a[148:151], a[224:227], v[20:23]\n\t" \
  "v_cvt_pk_bf16_f32 v31, v30, v31\n\t" \
  "v_cvt_pk_bf16_f32 v30, v28, v29\n\t" \
  "v_mfma_f32_16x16x32_bf16 v[20:23], a[84:87], a[240:243], v[20:23]\n\t" \
  "v_cndmask_b32 v204, v195, v204, vcc\n\t" \
  "v_cndmask_b32 v202, v195, v202, vcc\n\t" \
  "v_mfma_f32_16x16x32_bf16 v[16:19], a[144:147], a[224:227], v[16:19]\n\t" \
  "v_cvt_pk_bf16_f32 v28, v24, v25\n\t" \
  "v_cvt_pk_bf16_f32 v29, v26, v27\n\t" \
  "v_mfma_f32_16x16x32_bf16 v[16:19], a[80:83], a[240:243], v[16:19]\n\t" \
  "v_permlane16_swap_b32 v28, v30\n\t" \
  "v_permlane16_swap_b32 v29, v31\n\t" \
  "v_mfma_f32_16x16x32_bf16 v[12:15], a[140:143], a[224:227], v[12:15]\n\t" \
  "v_cvt_pk_bf16_f32 v27, v22, v23\n\t" \
  "v_cvt_pk_bf16_f32 v26, v20, v21\n\t" \
  "v_mfma_f32_16x16x32_bf16 v[12:15], a[76:79], a[240:243], v[12:15]\n\t" \
  "v_mov_b32 v218, 0x3f803f80\n\t" \
  "v_mov_b32 v219, 0x3f803f80\n\t" \
  "v_mfma_f32_16x16x32_bf16 v[8:11], a[136:139], a[224:227], v[8:11]\n\t" \
  "v_cvt_pk_bf16_f32 v24, v16, v17\n\t" \
  "v_cvt_pk_bf16_f32 v25, v18, v19\n\t" \
  "v_mfma_f32_16x16x32_bf16 v[8:11], a[72:75], a[240:243], v[8:11]\n\t" \
  "v_permlane16_swap_b32 v24, v26\n\t" \
  "v_permlane16_swap_b32 v25, v27\n\t" \
  "v_mfma_f32_16x16x32_bf16 v[4:7], a[132:135], a[224:227], v[4:7]\n\t" \
  "v_cvt_pk_bf16_f32 v23, v14, v15\n\t" \
  "v_cvt_pk_bf16_f32 v22, v12, v13\n\t" \
  "v_mfma_f32_16x16x32_bf16 v[4:7], a[68:71], a[240:243], v[4:7]\n\t" \
  "v_mov_b32 v196, 1.0\n\t" \
  "v_mov_b32 v197, 1.0\n\t" \
  "v_mfma_f32_16x16x32_bf16 v[0:3], a[128:131], a[224:227], v[0:3]\n\t" \
  "v_cvt_pk_bf16_f32 v20, v8, v9\n\t" \
  "v_cvt_pk_bf16_f32 v21, v10, v11\n\t" \
  "v_mfma_f32_16x16x32_bf16 v[0:3], a[64:67], a[240:243], v[0:3]\n\t" \
  "v_permlane16_swap_b32 v20, v22\n\t" \
  "v_permlane16_swap_b32 v21, v23\n\t" \
  "s_waitcnt vmcnt(2)\n\t" \
  "s_barrier\n\t" \
  "v_mfma_f32_16x16x32_bf16 v[60:63], a[156:159], a[228:231], v[60:63]\n\t" \
  "v_cvt_pk_bf16_f32 v19, v6, v7\n\t" \
  "v_cvt_pk_bf16_f32 v18, v4, v5\n\t" \
  "v_mfma_f32_16x16x32_bf16 v[60:63], a[92:95], a[244:247], v[60:63]\n\t" \
  "v_cvt_pk_bf16_f32 v16, v0, v1\n\t" \
  "v_cvt_pk_bf16_f32 v17, v2, v3\n\t" \
  "v_mfma_f32_16x16x32_bf16 v[56:59], a[152:155], a[228:231], v[56:59]\n\t" \
  "v_permlane16_swap_b32 v16, v18\n\t" \
  "v_permlane16_swap_b32 v17, v19\n\t" \
  "v_mfma_f32_16x16x32_bf16 v[56:59], a[88:91], a[244:247], v[56:59]\n\t" \
  "v_mov_b32 v198, 1.0\n\t" \
  "v_mov_b32 v199, 1.0\n\t" \
  "v_mfma_f32_16x16x32_bf16 v[52:55], a[148:151], a[228:231], v[52:55]\n\t" \
  "v_cvt_pk_bf16_f32 v63, v62, v63\n\t" \
  "v_cvt_pk_bf16_f32 v62, v60, v61\n\t" \
  "v_mfma_f32_16x16x32_bf16 v[52:55], a[84:87], a[244:247], v[52:55]\n\t" \
  "v_lshlrev_b32 v242, 4, v233\n\t" \
  "s_mov_b32 s99, 0\n\t" \
  "v_mfma_f32_16x16x32_bf16 v[48:51], a[144:147], a[228:231], v[48:51]\n\t" \
  "v_cvt_pk_bf16_f32 v60, v56, v57\n\t" \
  "v_cvt_pk_bf16_f32 v61, v58, v59\n\t" \
  "v_mfma_f32_16x16x32_bf16 v[48:51], a[80:83], a[244:247], v[48:51]\n\t" \
  "v_permlane16_swap_b32 v60, v62\n\t" \
  "v_permlane16_swap_b32 v61, v63\n\t" \
  "v_mfma_f32_16x16x32_bf16 v[44:47], a[140:143], a[228:231], v[44:47]\n\t" \
  "v_cvt_pk_bf16_f32 v59, v54, v55\n\t" \
  "v_cvt_pk_bf16_f32 v58, v52, v53\n\t" \
  "v_mfma_f32_16x16x32_bf16 v[44:47], a[76:79], a[244:247], v[44:47]\n\t" \
  "v_add_u32 v237, 65536, v228\n\t" \
  "v_add_u32 v238, 65536, v229\n\t" \
  "v_mfma_f32_16x16x32_bf16 v[40:43], a[136:139], a[228:231], v[40:43]\n\t" \
  "v_cvt_pk_bf16_f32 v56, v48, v49\n\t" \
  "v_cvt_pk_bf16_f32 v57, v50, v51\n\t" \
  "v_mfma_f32_16x16x32_bf16 v[40:43], a[72:75], a[244:247], v[40:43]\n\t" \
  "v_permlane16_swap_b32 v56, v58\n\t" \
  "v_permlane16_swap_b32 v57, v59\n\t" \
  "v_mfma_f32_16x16x32_bf16 v[36:39], a[132:135], a[228:231], v[36:39]\n\t" \
  "v_cvt_pk_bf16_f32 v55, v46, v47\n\t" \
  "v_cvt_pk_bf16_f32 v54, v44, v45\n\t" \
  "v_mfma_f32_16x16x32_bf16 v[36:39], a[68:71], a[244:247], v[36:39]\n\t" \
  "ds_read_b128 a[0:3], v220 offset:0\n\t" \
  "ds_read_b128 a[4:7], v221 offset:0\n\t" \
  "v_mfma_f32_16x16x32_bf16 v[32:35], a[128:131], a[228:231], v[32:35]\n\t" \
  "v_cvt_pk_bf16_f32 v52, v40, v41\n\t" \
  "v_cvt_pk_bf16_f32 v53, v42, v43\n\t" \
  "v_mfma_f32_16x16x32_bf16 v[32:35], a[64:67], a[244:247], v[32:35]\n\t" \
  "v_permlane16_swap_b32 v52, v54\n\t" \
  "v_permlane16_swap_b32 v53, v55\n\t" \
  "v_mfma_f32_16x16x32_bf16 v[92:95], a[156:159], a[232:235], v[92:95]\n\t" \
  "v_cvt_pk_bf16_f32 v51, v38, v39\n\t" \
  "v_cvt_pk_bf16_f32 v50, v36, v37\n\t" \
  "v_mfma_f32_16x16x32_bf16 v[92:95], a[92:95], a[248:251], v[92:95]\n\t" \
  "s_add_u32 m0, %[m], 0\n\t" \
  "ds_read_b128 a[8:11], v222 offset:0\n\t" \
  "v_mfma_f32_16x16x32_bf16 v[88:91], a[152:155], a[232:235], v[88:91]\n\t" \
  "v_cvt_pk_bf16_f32 v48, v32, v33\n\t" \
  "v_cvt_pk_bf16_f32 v49, v34, v35\n\t" \
  "v_mfma_f32_16x16x32_bf16 v[88:91], a[88:91], a[248:251], v[88:91]\n\t" \
  "v_permlane16_swap_b32 v48, v50\n\t" \
  "v_permlane16_swap_b32 v49, v51\n\t" \
  "v_mfma_f32_16x16x32_bf16 v[84:87], a[148:151], a[232:235], v[84:87]\n\t" \
  "v_cvt_pk_bf16_f32 v95, v94, v95\n\t" \
  "v_cvt_pk_bf16_f32 v94, v92, v93\n\t" \
  "v_mfma_f32_16x16x32_bf16 v[84:87], a[84:87], a[248:251], v[84:87]\n\t" \
  "ds_read_b128 a[12:15], v223 offset:0\n\t" \
  "s_xor_b32 s97, %[wob], 0\n\t" \
  "v_mfma_f32_16x16x32_bf16 v[80:83], a[144:147], a[232:235], v[80:83]\n\t" \
  "v_cvt_pk_bf16_f32 v92, v88, v89\n\t" \
  "v_cvt_pk_bf16_f32 v93, v90, v91\n\t" \
  "v_mfma_f32_16x16x32_bf16 v[80:83], a[80:83], a[248:251], v[80:83]\n\t" \
  "v_permlane16_swap_b32 v92, v94\n\t" \
  "v_permlane16_swap_b32 v93, v95\n\t" \
  "v_mfma_f32_16x16x32_bf16 v[76:79], a[140:143], a[232:235], v[76:79]\n\t" \
  "v_cvt_pk_bf16_f32 v91, v86, v87\n\t" \
  "v_cvt_pk_bf16_f32 v90, v84, v85\n\t" \
  "v_mfma_f32_16x16x32_bf16 v[76:79], a[76:79], a[248:251], v[76:79]\n\t" \
  "ds_read_b128 a[16:19], v220 offset:4096\n\t" \
  "ds_read_b128 a[20:23], v221 offset:4096\n\t" \
  "v_mfma_f32_16x16x32_bf16 v[72:75], a[136:139], a[232:235], v[72:75]\n\t" \
  "v_cvt_pk_bf16_f32 v88, v80, v81\n\t" \
  "v_cvt_pk_bf16_f32 v89, v82, v83\n\t" \
  "v_mfma_f32_16x16x32_bf16 v[72:75], a[72:75], a[248:251], v[72:75]\n\t" \
  "v_permlane16_swap_b32 v88, v90\n\t" \
  "v_permlane16_swap_b32 v89, v91\n\t" \
  "v_mfma_f32_16x16x32_bf16 v[68:71], a[132:135], a[232:235], v[68:71]\n\t" \
  "v_cvt_pk_bf16_f32 v87, v78, v79\n\t" \
  "v_cvt_pk_bf16_f32 v86, v76, v77\n\t" \
  "v_mfma_f32_16x16x32_bf16 v[68:71], a[68:71], a[248:251], v[68:71]\n\t" \
  "ds_read_b128 a[24:27], v222 offset:4096\n\t" \
  "buffer_load_dwordx4 v240, %[wo], s97 offen offset:0 lds\n\t" \
  "v_mfma_f32_16x16x32_bf16 v[64:67], a[128:131], a[232:235], v[64:67]\n\t" \
  "v_cvt_pk_bf16_f32 v84, v72, v73\n\t" \
  "v_cvt_pk_bf16_f32 v85, v74, v75\n\t" \
  "v_mfma_f32_16x16x32_bf16 v[64:67], a[64:67], a[248:251], v[64:67]\n\t" \
  "v_permlane16_swap_b32 v84, v86\n\t" \
  "v_permlane16_swap_b32 v85, v87\n\t" \
  "v_mfma_f32_16x16x32_bf16 v[124:127], a[156:159], a[236:239], v[124:127]\n\t" \
  "v_cvt_pk_bf16_f32 v83, v70, v71\n\t" \
  "v_cvt_pk_bf16_f32 v82, v68, v69\n\t" \
  "v_mfma_f32_16x16x32_bf16 v[124:127], a[92:95], a[252:255], v[124:127]\n\t" \
  "ds_read_b128 a[28:31], v223 offset:4096\n\t" \
  "ds_read_b128 a[32:35], v220 offset:8192\n\t" \
  "v_mfma_f32_16x16x32_bf16 v[120:123], a[152:155], a[236:239], v[120:123]\n\t" \
  "v_cvt_pk_bf16_f32 v80, v64, v65\n\t" \
  "v_cvt_pk_bf16_f32 v81, v66, v67\n\t" \
  "v_mfma_f32_16x16x32_bf16 v[120:123], a[88:91], a[252:255], v[120:123]\n\t" \
  "v_permlane16_swap_b32 v80, v82\n\t" \
  "v_permlane16_swap_b32 v81, v83\n\t" \
  "v_mfma_f32_16x16x32_bf16 v[116:119], a[148:151], a[236:239], v[116:119]\n\t" \
  "v_cvt_pk_bf16_f32 v127, v126, v127\n\t" \
  "v_cvt_pk_bf16_f32 v126, v124, v125\n\t" \
  "v_mfma_f32_16x16x32_bf16 v[116:119], a[84:87], a[252:255], v[116:119]\n\t" \
  "s_add_u32 s97, s97, 7168\n\t" \
  "buffer_load_dwordx4 v241, %[wo], s97 offen offset:1024 lds\n\t" \
  "v_mfma_f32_16x16x32_bf16 v[112:115], a[144:147], a[236:239], v[112:115]\n\t" \
  "v_cvt_pk_bf16_f32 v124, v120, v121\n\t" \
  "v_cvt_pk_bf16_f32 v125, v122, v123\n\t" \
  "v_mfma_f32_16x16x32_bf16 v[112:115], a[80:83], a[252:255], v[112:115]\n\t" \
  "v_permlane16_swap_b32 v124, v126\n\t" \
  "v_permlane16_swap_b32 v125, v127\n\t" \
  "ds_read_b128 a[36:39], v221 offset:8192\n\t" \
  "ds_read_b128 a[40:43], v222 offset:8192\n\t" \
  "ds_read_b128 a[44:47], v223 offset:8192\n\t" \
  "ds_read_b128 a[48:51], v220 offset:12288\n\t" \
  "ds_read_b128 a[52:55], v221 offset:12288\n\t" \
  "ds_read_b128 a[56:59], v222 offset:12288\n\t" \
  "ds_read_b128 a[60:63], v223 offset:12288\n\t" \
  "v_mfma_f32_16x16x32_bf16 v[108:111], a[140:143], a[236:239], v[108:111]\n\t" \
  "v_cvt_pk_bf16_f32 v123, v118, v119\n\t" \
  "v_cvt_pk_bf16_f32 v122, v116, v117\n\t" \
  "v_mfma_f32_16x16x32_bf16 v[108:111], a[76:79], a[252:255], v[108:111]\n\t" \
  "v_cvt_pk_bf16_f32 v120, v112, v113\n\t" \
  "v_cvt_pk_bf16_f32 v121, v114, v115\n\t" \
  "v_mfma_f32_16x16x32_bf16 v[104:107], a[136:139], a[236:239], v[104:107]\n\t" \
  "v_permlane16_swap_b32 v120, v122\n\t" \
  "v_permlane16_swap_b32 v121, v123\n\t" \
  "v_mfma_f32_16x16x32_bf16 v[104:107], a[72:75], a[252:255], v[104:107]\n\t" \
  "s_add_u32 s97, s97, 7168\n\t" \
  "buffer_load_dwordx4 v240, %[wo], s97 offen offset:2048 lds\n\t" \
  "v_mfma_f32_16x16x32_bf16 v[100:103], a[132:135], a[236:239], v[100:103]\n\t" \
  "v_cvt_pk_bf16_f32 v119, v110, v111\n\t" \
  "v_cvt_pk_bf16_f32 v118, v108, v109\n\t" \
  "v_mfma_f32_16x16x32_bf16 v[100:103], a[68:71], a[252:255], v[100:103]\n\t" \
  "s_add_u32 s97, s97, 7168\n\t" \
  "buffer_load_dwordx4 v241, %[wo], s97 offen offset:3072 lds\n\t" \
  "v_mfma_f32_16x16x32_bf16 v[96:99], a[128:131], a[236:239], v[96:99]\n\t" \
  "v_cvt_pk_bf16_f32 v116, v104, v105\n\t" \
  "v_cvt_pk_bf16_f32 v117, v106, v107\n\t" \
  "v_mfma_f32_16x16x32_bf16 v[96:99], a[64:67], a[252:255], v[96:99]\n\t" \
  "v_permlane16_swap_b32 v116, v118\n\t" \
  "v_permlane16_swap_b32 v117, v119\n\t" \
  "s_add_u32 m0, %[m], 16384\n\t" \
  "s_xor_b32 s97, %[wob], 131072\n\t" \
  "buffer_load_dwordx4 v240, %[wo], s97 offen offset:0 lds\n\t" \
  "s_add_u32 s97, s97, 7168\n\t" \
  "buffer_load_dwordx4 v241, %[wo], s97 offen offset:1024 lds\n\t" \
  "s_add_u32 s97, s97, 7168\n\t" \
  "buffer_load_dwordx4 v240, %[wo], s97 offen offset:2048 lds\n\t" \
  "s_add_u32 s97, s97, 7168\n\t" \
  "buffer_load_dwordx4 v241, %[wo], s97 offen offset:3072 lds\n\t" \
  "s_waitcnt lgkmcnt(0)\n\t" \
  "s_barrier\n\t" \
  "v_mfma_f32_16x16x32_bf16 v[0:3], a[0:3], v[16:19], 0\n\t" \
  "v_cvt_pk_bf16_f32 v115, v102, v103\n\t" \
  "v_cvt_pk_bf16_f32 v114, v100, v101\n\t" \
  "v_mfma_f32_16x16x32_bf16 v[0:3], a[4:7], v[20:23], v[0:3]\n\t" \
  "v_cvt_pk_bf16_f32 v112, v96, v97\n\t" \
  "v_cvt_pk_bf16_f32 v113, v98, v99\n\t" \
  "v_mfma_f32_16x16x32_bf16 v[0:3], a[8:11], v[24:27], v[0:3]\n\t" \
  "v_permlane16_swap_b32 v112, v114\n\t" \
  "v_permlane16_swap_b32 v113, v115\n\t" \
  "v_mfma_f32_16x16x32_bf16 v[0:3], a[12:15], v[28:31], v[0:3]\n\t" \
  "s_add_u32 s98, %[pf4], 28672\n\t" \
  "buffer_load_dword %[pfd], v245, %[sU], s98 offen\n\t" \
  "v_mfma_f32_16x16x32_bf16 v[32:35], a[0:3], v[48:51], 0\n\t" \
  "ds_read_b128 a[192:195], v220 offset:16384\n\t" \
  "s_add_u32 m0, %[m], 49152\n\t" \
  "v_mfma_f32_16x16x32_bf16 v[32:35], a[4:7], v[52:55], v[32:35]\n\t" \
  "ds_read_b128 a[196:199], v221 offset:16384\n\t" \
  "ds_read_b128 a[200:203], v222 offset:16384\n\t" \
  "v_mfma_f32_16x16x32_bf16 v[32:35], a[8:11], v[56:59], v[32:35]\n\t" \
  "v_max3_f32 v194, v0, v1, v2\n\t" \
  "v_max_f32 v194, v194, v3\n\t" \
  "v_mfma_f32_16x16x32_bf16 v[32:35], a[12:15], v[60:63], v[32:35]\n\t" \
  "v_mov_b32 v195, v194\n\t" \
  "s_add_u32 s96, %[sofk], 32768\n\t" \
  "s_nop 0\n\t" \
  "v_permlane32_swap_b32 v194, v195\n\t" \
  "v_max_f32 v194, v194, v195\n\t" \
  "v_mov_b32 v195, v194\n\t" \
  "s_nop 1\n\t" \
  "v_permlane16_swap_b32 v194, v195\n\t" \
  "v_max_f32 v194, v194, v195\n\t" \
  "v_sub_f32 v176, 0xc2000000, v194\n\t" \
  "v_sub_f32 v177, 0xc2000000, v194\n\t" \
  "v_sub_f32 v178, 0xc2000000, v194\n\t" \
  "v_sub_f32 v179, 0xc2000000, v194\n\t" \
  "s_nop 1\n\t" \
  "v_mfma_f32_16x16x32_bf16 v[4:7], a[16:19], v[16:19], v[176:179]\n\t" \
  "v_add_f32 v0, v0, v176\n\t" \
  "v_add_f32 v1, v1, v176\n\t" \
  "v_mfma_f32_16x16x32_bf16 v[4:7], a[20:23], v[20:23], v[4:7]\n\t" \
  "v_add_f32 v2, v2, v176\n\t" \
  "v_add_f32 v3, v3, v176\n\t" \
  "v_mfma_f32_16x16x32_bf16 v[4:7], a[24:27], v[24:27], v[4:7]\n\t" \
  "ds_read_b128 a[204:207], v223 offset:16384\n\t" \
  "v_max3_f32 v194, v32, v33, v34\n\t" \
  "v_mfma_f32_16x16x32_bf16 v[4:7], a[28:31], v[28:31], v[4:7]\n\t" \
  "v_max_f32 v194, v194, v35\n\t" \
  "v_mov_b32 v195, v194\n\t" \
  "v_mfma_f32_16x16x32_bf16 v[8:11], a[32:35], v[16:19], v[176:179]\n\t" \
  "ds_read_b128 a[208:211], v220 offset:20480\n\t" \
  "v_permlane32_swap_b32 v194, v195\n\t" \
  "v_mfma_f32_16x16x32_bf16 v[8:11], a[36:39], v[20:23], v[8:11]\n\t" \
  "v_max_f32 v194, v194, v195\n\t" \
  "v_mov_b32 v195, v194\n\t" \
  "v_mfma_f32_16x16x32_bf16 v[8:11], a[40:43], v[24:27], v[8:11]\n\t" \
  "ds_read_b128 a[212:215], v221 offset:20480\n\t" \
  "v_permlane16_swap_b32 v194, v195\n\t" \
  "v_mfma_f32_16x16x32_bf16 v[8:11], a[44:47], v[28:31], v[8:11]\n\t" \
  "v_max_f32 v194, v194, v195\n\t" \
  "v_sub_f32 v180, 0xc2000000, v194\n\t" \
  "v_mfma_f32_16x16x32_bf16 v[12:15], a[48:51], v[16:19], v[176:179]\n\t" \
  "v_sub_f32 v181, 0xc2000000, v194\n\t" \
  "v_sub_f32 v182, 0xc2000000, v194\n\t" \
  "v_mfma_f32_16x16x32_bf16 v[12:15], a[52:55], v[20:23], v[12:15]\n\t" \
  "v_sub_f32 v183, 0xc2000000, v194\n\t" \
  "v_add_f32 v32, v32, v180\n\t" \
  "v_mfma_f32_16x16x32_bf16 v[12:15], a[56:59], v[24:27], v[12:15]\n\t" \
  "v_add_f32 v33, v33, v180\n\t" \
  "v_add_f32 v34, v34, v180\n\t" \
  "v_mfma_f32_16x16x32_bf16 v[12:15], a[60:63], v[28:31], v[12:15]\n\t" \
  "v_add_f32 v35, v35, v180\n\t" \
  "v_max3_f32 v192, v4, v5, v6\n\t" \
  "v_mfma_f32_16x16x32_bf16 v[64:67], a[0:3], v[80:83], 0\n\t" \
  "buffer_load_dwordx4 v224, %[sK], s96 offen offset:0 lds\n\t" \
  "ds_read_b128 a[216:219], v222 offset:20480\n\t" \
  "v_mfma_f32_16x16x32_bf16 v[64:67], a[4:7], v[84:87], v[64:67]\n\t" \
  "ds_read_b128 a[220:223], v223 offset:20480\n\t" \
  "v_max3_f32 v203, v7, v8, v9\n\t" \
  "v_mfma_f32_16x16x32_bf16 v[64:67], a[8:11], v[88:91], v[64:67]\n\t" \
  "v_max3_f32 v192, v192, v10, v11\n\t" \
  "ds_read_b128 a[224:227], v220 offset:24576\n\t" \
  "v_mfma_f32_16x16x32_bf16 v[64:67], a[12:15], v[92:95], v[64:67]\n\t" \
  "buffer_load_dwordx4 v225, %[sK], s96 offen offset:1024 lds\n\t" \
  "ds_read_b128 a[228:231], v221 offset:24576\n\t" \
  "v_mfma_f32_16x16x32_bf16 v[36:39], a[16:19], v[48:51], v[180:183]\n\t" \
  "v_max3_f32 v203, v203, v12, v13\n\t" \
  "v_max3_f32 v192, v192, v14, v15\n\t" \
  "v_mfma_f32_16x16x32_bf16 v[36:39], a[20:23], v[52:55], v[36:39]\n\t" \
  "v_max_f32 v192, v192, v203\n\t" \
  "ds_read_b128 a[232:235], v222 offset:24576\n\t" \
  "v_mfma_f32_16x16x32_bf16 v[36:39], a[24:27], v[56:59], v[36:39]\n\t" \
  "v_cmp_lt_f32 vcc, 0x42800000, v192\n\t" \
  "v_max3_f32 v194, v64, v65, v66\n\t" \
  "v_mfma_f32_16x16x32_bf16 v[36:39], a[28:31], v[60:63], v[36:39]\n\t" \
  "s_cbranch_vccnz .Lsf_c00\n\t" \
  ".Lsf_r00:\n\t" \
  "v_max_f32 v194, v194, v67\n\t" \
  "v_mfma_f32_16x16x32_bf16 v[40:43], a[32:35], v[48:51], v[180:183]\n\t" \
  "v_mov_b32 v195, v194\n\t" \
  "ds_read_b128 a[236:239], v223 offset:24576\n\t" \
  "v_mfma_f32_16x16x32_bf16 v[40:43], a[36:39], v[52:55], v[40:43]\n\t" \
  "v_permlane32_swap_b32 v194, v195\n\t" \
  "v_max_f32 v194, v194, v195\n\t" \
  "v_mfma_f32_16x16x32_bf16 v[40:43], a[40:43], v[56:59], v[40:43]\n\t" \
  "v_mov_b32 v195, v194\n\t" \
  "buffer_load_dwordx4 v226, %[sK], s96 offen offset:2048 lds\n\t" \
  "v_mfma_f32_16x16x32_bf16 v[40:43], a[44:47], v[60:63], v[40:43]\n\t" \
  "v_permlane16_swap_b32 v194, v195\n\t" \
  "v_max_f32 v194, v194, v195\n\t" \
  "v_mfma_f32_16x16x32_bf16 v[44:47], a[48:51], v[48:51], v[180:183]\n\t" \
  "v_sub_f32 v184, 0xc2000000, v194\n\t" \
  "v_sub_f32 v185, 0xc2000000, v194\n\t" \
  "v_mfma_f32_16x16x32_bf16 v[44:47], a[52:55], v[52:55], v[44:47]\n\t" \
  "v_sub_f32 v186, 0xc2000000, v194\n\t" \
  "v_sub_f32 v187, 0xc2000000, v194\n\t" \
  "v_mfma_f32_16x16x32_bf16 v[44:47], a[56:59], v[56:59], v[44:47]\n\t" \
  "v_add_f32 v64, v64, v184\n\t" \
  "v_add_f32 v65, v65, v184\n\t" \
  "v_mfma_f32_16x16x32_bf16 v[44:47], a[60:63], v[60:63], v[44:47]\n\t" \
  "v_add_f32 v66, v66, v184\n\t" \
  "v_add_f32 v67, v67, v184\n\t" \
  "v_mfma_f32_16x16x32_bf16 v[96:99], a[0:3], v[112:115], 0\n\t" \
  "ds_read_b128 a[240:243], v220 offset:28672\n\t" \
  "ds_read_b128 a[244:247], v221 offset:28672\n\t" \
  "v_mfma_f32_16x16x32_bf16 v[96:99], a[4:7], v[116:119], v[96:99]\n\t" \
  "v_max3_f32 v192, v36, v37, v38\n\t" \
  "ds_read_b128 a[248:251], v222 offset:28672\n\t" \
  "v_mfma_f32_16x16x32_bf16 v[96:99], a[8:11], v[120:123], v[96:99]\n\t" \
  "buffer_load_dwordx4 v227, %[sK], s96 offen offset:3072 lds\n\t" \
  "ds_read_b128 a[252:255], v223 offset:28672\n\t" \
  "v_mfma_f32_16x16x32_bf16 v[96:99], a[12:15], v[124:127], v[96:99]\n\t" \
  "v_max3_f32 v203, v39, v40, v41\n\t" \
  "v_max3_f32 v192, v192, v42, v43\n\t" \
  "v_mfma_f32_16x16x32_bf16 v[68:71], a[16:19], v[80:83], v[184:187]\n\t" \
  "ds_read_b64_tr_b16 a[128:129], v228 offset:0\n\t" \
  "v_max3_f32 v203, v203, v44, v45\n\t" \
  "v_mfma_f32_16x16x32_bf16 v[68:71], a[20:23], v[84:87], v[68:71]\n\t" \
  "v_max3_f32 v192, v192, v46, v47\n\t" \
  "v_max_f32 v192, v192, v203\n\t" \
  "v_mfma_f32_16x16x32_bf16 v[68:71], a[24:27], v[88:91], v[68:71]\n\t" \
  "v_cmp_lt_f32 vcc, 0x42800000, v192\n\t" \
  "s_add_u32 m0, %[m], 147456\n\t" \
  "v_mfma_f32_16x16x32_bf16 v[68:71], a[28:31], v[92:95], v[68:71]\n\t" \
  "s_cbranch_vccnz .Lsf_c01\n\t" \
  ".Lsf_r01:\n\t" \
  "ds_read_b64_tr_b16 a[130:131], v228 offset:4096\n\t" \
  "v_mfma_f32_16x16x32_bf16 v[72:75], a[32:35], v[80:83], v[184:187]\n\t" \
  "v_max3_f32 v194, v96, v97, v98\n\t" \
  "v_max_f32 v194, v194, v99\n\t" \
  "v_mfma_f32_16x16x32_bf16 v[72:75], a[36:39], v[84:87], v[72:75]\n\t" \
  "v_mov_b32 v195, v194\n\t" \
  "ds_read_b64_tr_b16 a[132:133], v228 offset:8192\n\t" \
  "v_mfma_f32_16x16x32_bf16 v[72:75], a[40:43], v[88:91], v[72:75]\n\t" \
  "v_permlane32_swap_b32 v194, v195\n\t" \
  "v_max_f32 v194, v194, v195\n\t" \
  "v_mfma_f32_16x16x32_bf16 v[72:75], a[44:47], v[92:95], v[72:75]\n\t" \
  "v_mov_b32 v195, v194\n\t" \
  "s_add_u32 s96, %[sofk], 16384\n\t" \
  "v_mfma_f32_16x16x32_bf16 v[76:79], a[48:51], v[80:83], v[184:187]\n\t" \
  "v_permlane16_swap_b32 v194, v195\n\t" \
  "v_max_f32 v194, v194, v195\n\t" \
  "v_mfma_f32_16x16x32_bf16 v[76:79], a[52:55], v[84:87], v[76:79]\n\t" \
  "v_sub_f32 v188, 0xc2000000, v194\n\t" \
  "v_sub_f32 v189, 0xc2000000, v194\n\t" \
  "v_mfma_f32_16x16x32_bf16 v[76:79], a[56:59], v[88:91], v[76:79]\n\t" \
  "v_sub_f32 v190, 0xc2000000, v194\n\t" \
  "v_sub_f32 v191, 0xc2000000, v194\n\t" \
  "v_mfma_f32_16x16x32_bf16 v[76:79], a[60:63], v[92:95], v[76:79]\n\t" \
  "v_add_f32 v96, v96, v188\n\t" \
  "v_add_f32 v97, v97, v188\n\t" \
  "v_mfma_f32_16x16x32_bf16 v[100:103], a[16:19], v[112:115], v[188:191]\n\t" \
  "v_add_f32 v98, v98, v188\n\t" \
  "v_add_f32 v99, v99, v188\n\t" \
  "v_mfma_f32_16x16x32_bf16 v[100:103], a[20:23], v[116:119], v[100:103]\n\t" \
  "ds_read_b64_tr_b16 a[134:135], v228 offset:12288\n\t" \
  "v_max3_f32 v192, v68, v69, v70\n\t" \
  "v_mfma_f32_16x16x32_bf16 v[100:103], a[24:27], v[120:123], v[100:103]\n\t" \
  "ds_read_b64_tr_b16 a[136:137], v229 offset:0\n\t" \
  "v_max3_f32 v203, v71, v72, v73\n\t" \
  "v_mfma_f32_16x16x32_bf16 v[100:103], a[28:31], v[124:127], v[100:103]\n\t" \
  "v_max3_f32 v192, v192, v74, v75\n\t" \
  "buffer_load_dwordx4 v230, %[sV], s96 offen offset:0 lds\n\t" \
  "v_mfma_f32_16x16x32_bf16 v[104:107], a[32:35], v[112:115], v[188:191]\n\t" \
  "ds_read_b64_tr_b16 a[138:139], v229 offset:4096\n\t" \
  "ds_read_b64_tr_b16 a[140:141], v229 offset:8192\n\t" \
  "v_mfma_f32_16x16x32_bf16 v[104:107], a[36:39], v[116:119], v[104:107]\n\t" \
  "v_max3_f32 v203, v203, v76, v77\n\t" \
  "v_max3_f32 v192, v192, v78, v79\n\t" \
  "v_mfma_f32_16x16x32_bf16 v[104:107], a[40:43], v[120:123], v[104:107]\n\t" \
  "v_max_f32 v192, v192, v203\n\t" \
  "ds_read_b64_tr_b16 a[142:143], v229 offset:12288\n\t" \
  "v_mfma_f32_16x16x32_bf16 v[104:107], a[44:47], v[124:127], v[104:107]\n\t" \
  "v_cmp_lt_f32 vcc, 0x42800000, v192\n\t" \
  "buffer_load_dwordx4 v231, %[sV], s96 offen offset:1024 lds\n\t" \
  "v_mfma_f32_16x16x32_bf16 v[108:111], a[48:51], v[112:115], v[188:191]\n\t" \
  "s_cbranch_vccnz .Lsf_c02\n\t" \
  ".Lsf_r02:\n\t" \
  "v_max3_f32 v192, v100, v101, v102\n\t" \
  "v_mfma_f32_16x16x32_bf16 v[108:111], a[52:55], v[116:119], v[108:111]\n\t" \
  "ds_read_b64_tr_b16 a[144:145], v228 offset:256\n\t" \
  "ds_read_b64_tr_b16 a[146:147], v228 offset:4352\n\t" \
  "v_mfma_f32_16x16x32_bf16 v[108:111], a[56:59], v[120:123], v[108:111]\n\t" \
  "v_max3_f32 v203, v103, v104, v105\n\t" \
  "v_max3_f32 v192, v192, v106, v107\n\t" \
  "v_mfma_f32_16x16x32_bf16 v[108:111], a[60:63], v[124:127], v[108:111]\n\t" \
  "buffer_load_dwordx4 v230, %[sV], s96 offen offset:2048 lds\n\t" \
  "ds_read_b64_tr_b16 a[148:149], v228 offset:8448\n\t" \
  "v_exp_f32 v0, v0\n\t" \
  "v_exp_f32 v1, v1\n\t" \
  "v_exp_f32 v2, v2\n\t" \
  "v_exp_f32 v3, v3\n\t" \
  "v_cvt_pk_bf16_f32 v128, v0, v1\n\t" \
  "v_cvt_pk_bf16_f32 v129, v2, v3\n\t" \
  "s_waitcnt lgkmcnt(11)\n\t" \
  "v_mfma_f32_16x16x32_bf16 v[0:3], a[192:195], v[16:19], v[176:179]\n\t" \
  "v_exp_f32 v4, v4\n\t" \
  "v_mfma_f32_16x16x32_bf16 v[0:3], a[196:199], v[20:23], v[0:3]\n\t" \
  "v_max3_f32 v203, v203, v108, v109\n\t" \
  "v_max3_f32 v192, v192, v110, v111\n\t" \
  "v_mfma_f32_16x16x32_bf16 v[0:3], a[200:203], v[24:27], v[0:3]\n\t" \
  "v_max_f32 v192, v192, v203\n\t" \
  "ds_read_b64_tr_b16 a[150:151], v228 offset:12544\n\t" \
  "v_mfma_f32_16x16x32_bf16 v[0:3], a[204:207], v[28:31], v[0:3]\n\t" \
  "v_cmp_lt_f32 vcc, 0x42800000, v192\n\t" \
  "buffer_load_dwordx4 v231, %[sV], s96 offen offset:3072 lds\n\t" \
  "v_exp_f32 v5, v5\n\t" \
  "v_exp_f32 v6, v6\n\t" \
  "v_exp_f32 v7, v7\n\t" \
  "v_cvt_pk_bf16_f32 v130, v4, v5\n\t" \
  "v_cvt_pk_bf16_f32 v131, v6, v7\n\t" \
  "v_mfma_f32_16x16x32_bf16 v[4:7], a[220:223], v[28:31], v[176:179]\n\t" \
  "s_cbranch_vccnz .Lsf_c03\n\t" \
  ".Lsf_r03:\n\t" \
  "ds_read_b64_tr_b16 a[152:153], v229 offset:256\n\t" \
  "v_mfma_f32_16x16x32_bf16 v[4:7], a[216:219], v[24:27], v[4:7]\n\t" \
  "s_cmp_lg_u32 s99, 0\n\t" \
  "s_cbranch_scc1 .Lsf_p0\n\t" \
  ".Lsf_q0:\n\t" \
  "v_mfma_f32_16x16x32_bf16 v[4:7], a[212:215], v[20:23], v[4:7]\n\t" \
  "v_max3_f32 v192, v0, v1, v2\n\t" \
  "ds_read_b64_tr_b16 a[154:155], v229 offset:4352\n\t" \
  "v_mfma_f32_16x16x32_bf16 v[4:7], a[208:211], v[16:19], v[4:7]\n\t" \
  "v_exp_f32 v8, v8\n\t" \
  "v_exp_f32 v9, v9\n\t" \
  "v_exp_f32 v10, v10\n\t" \
  "v_exp_f32 v11, v11\n\t" \
  "v_cvt_pk_bf16_f32 v132, v8, v9\n\t" \
  "v_cvt_pk_bf16_f32 v133, v10, v11\n\t" \
  "v_mfma_f32_16x16x32_bf16 v[8:11], a[224:227], v[16:19], v[176:179]\n\t" \
  "s_add_u32 s98, %[pf4], 43008\n\t" \
  "buffer_load_dword %[pfd], v245, %[sU], s98 offen\n\t" \
  "v_mfma_f32_16x16x32_bf16 v[8:11], a[228:231], v[20:23], v[8:11]\n\t" \
  "v_max3_f32 v203, v3, v4, v5\n\t" \
  "v_max3_f32 v192, v192, v6, v7\n\t" \
  "v_mfma_f32_16x16x32_bf16 v[8:11], a[232:235], v[24:27], v[8:11]\n\t" \
  "v_exp_f32 v12, v12\n\t" \
  "v_mfma_f32_16x16x32_bf16 v[8:11], a[236:239], v[28:31], v[8:11]\n\t" \
  "ds_read_b64_tr_b16 a[156:157], v229 offset:8448\n\t" \
  "ds_read_b64_tr_b16 a[158:159], v229 offset:12544\n\t" \
  "v_exp_f32 v13, v13\n\t" \
  "v_exp_f32 v14, v14\n\t" \
  "v_exp_f32 v15, v15\n\t" \
  "v_cvt_pk_bf16_f32 v134, v12, v13\n\t" \
  "v_cvt_pk_bf16_f32 v135, v14, v15\n\t" \
  "v_mfma_f32_16x16x32_bf16 v[12:15], a[252:255], v[28:31], v[176:179]\n\t" \
  "v_exp_f32 v32, v32\n\t" \
  "v_mfma_f32_16x16x32_bf16 v[12:15], a[248:251], v[24:27], v[12:15]\n\t" \
  "v_max3_f32 v203, v203, v8, v9\n\t" \
  "v_max3_f32 v192, v192, v10, v11\n\t" \
  "v_mfma_f32_16x16x32_bf16 v[12:15], a[244:247], v[20:23], v[12:15]\n\t" \
  "s_add_u32 s98, %[pf4], 0\n\t" \
  "buffer_load_dword %[pfd], v245, %[sG], s98 offen\n\t" \
  "v_mfma_f32_16x16x32_bf16 v[12:15], a[240:243], v[16:19], v[12:15]\n\t" \
  "v_exp_f32 v33, v33\n\t" \
  "v_exp_f32 v34, v34\n\t" \
  "v_exp_f32 v35, v35\n\t" \
  "v_cvt_pk_bf16_f32 v136, v32, v33\n\t" \
  "v_cvt_pk_bf16_f32 v137, v34, v35\n\t" \
  "v_mfma_f32_16x16x32_bf16 v[32:35], a[192:195], v[48:51], v[180:183]\n\t" \
  "ds_read_b64_tr_b16 a[160:161], v228 offset:512\n\t" \
  "ds_read_b64_tr_b16 a[162:163], v228 offset:4608\n\t" \
  "v_mfma_f32_16x16x32_bf16 v[32:35], a[196:199], v[52:55], v[32:35]\n\t" \
  "v_exp_f32 v36, v36\n\t" \
  "v_mfma_f32_16x16x32_bf16 v[32:35], a[200:203], v[56:59], v[32:35]\n\t" \
  "v_max3_f32 v203, v203, v12, v13\n\t" \
  "v_max3_f32 v192, v192, v14, v15\n\t" \
  "v_mfma_f32_16x16x32_bf16 v[32:35], a[204:207], v[60:63], v[32:35]\n\t" \
  "v_max_f32 v192, v192, v203\n\t" \
  "ds_read_b64_tr_b16 a[164:165], v228 offset:8704\n\t" \
  "v_exp_f32 v37, v37\n\t" \
  "v_exp_f32 v38, v38\n\t" \
  "v_exp_f32 v39, v39\n\t" \
  "v_cvt_pk_bf16_f32 v138, v36, v37\n\t" \
  "v_cvt_pk_bf16_f32 v139, v38, v39\n\t" \
  "v_mfma_f32_16x16x32_bf16 v[36:39], a[220:223], v[60:63], v[180:183]\n\t" \
  "v_cmp_lt_f32 vcc, 0x42800000, v192\n\t" \
  "ds_read_b64_tr_b16 a[166:167], v228 offset:12800\n\t" \
  "v_mfma_f32_16x16x32_bf16 v[36:39], a[216:219], v[56:59], v[36:39]\n\t" \
  "s_cbranch_vccnz .Lsf_c10\n\t" \
  ".Lsf_r10:\n\t" \
  "v_max3_f32 v192, v32, v33, v34\n\t" \
  "v_mfma_f32_16x16x32_bf16 v[36:39], a[212:215], v[52:55], v[36:39]\n\t" \
  "s_add_u32 s98, %[pf4], 14336\n\t" \
  "buffer_load_dword %[pfd], v245, %[sG], s98 offen\n\t" \
  "v_mfma_f32_16x16x32_bf16 v[36:39], a[208:211], v[48:51], v[36:39]\n\t" \
  "v_exp_f32 v40, v40\n\t" \
  "v_exp_f32 v41, v41\n\t" \
  "v_exp_f32 v42, v42\n\t" \
  "v_exp_f32 v43, v43\n\t" \
  "v_cvt_pk_bf16_f32 v140, v40, v41\n\t" \
  "v_cvt_pk_bf16_f32 v141, v42, v43\n\t" \
  "v_mfma_f32_16x16x32_bf16 v[40:43], a[224:227], v[48:51], v[180:183]\n\t" \
  "v_exp_f32 v44, v44\n\t" \
  "v_mfma_f32_16x16x32_bf16 v[40:43], a[228:231], v[52:55], v[40:43]\n\t" \
  "v_max3_f32 v203, v35, v36, v37\n\t" \
  "v_max3_f32 v192, v192, v38, v39\n\t" \
  "v_mfma_f32_16x16x32_bf16 v[40:43], a[232:235], v[56:59], v[40:43]\n\t" \
  "v_exp_f32 v45, v45\n\t" \
  "v_mfma_f32_16x16x32_bf16 v[40:43], a[236:239], v[60:63], v[40:43]\n\t" \
  "v_cvt_pk_bf16_f32 v142, v44, v45\n\t" \
  "ds_read_b64_tr_b16 a[168:169], v229 offset:512\n\t" \
  "v_exp_f32 v46, v46\n\t" \
  "v_exp_f32 v47, v47\n\t" \
  "s_nop 0\n\t" \
  "v_cvt_pk_bf16_f32 v143, v46, v47\n\t" \
  "v_mfma_f32_16x16x32_bf16 v[44:47], a[252:255], v[60:63], v[180:183]\n\t" \
  "v_exp_f32 v64, v64\n\t" \
  "v_mfma_f32_16x16x32_bf16 v[44:47], a[248:251], v[56:59], v[44:47]\n\t" \
  "ds_read_b64_tr_b16 a[170:171], v229 offset:4608\n\t" \
  "v_max3_f32 v203, v203, v40, v41\n\t" \
  "v_mfma_f32_16x16x32_bf16 v[44:47], a[244:247], v[52:55], v[44:47]\n\t" \
  "v_max3_f32 v192, v192, v42, v43\n\t" \
  "ds_read_b64_tr_b16 a[172:173], v229 offset:8704\n\t" \
  "v_mfma_f32_16x16x32_bf16 v[44:47], a[240:243], v[48:51], v[44:47]\n\t" \
  "v_exp_f32 v65, v65\n\t" \
  "v_exp_f32 v66, v66\n\t" \
  "v_exp_f32 v67, v67\n\t" \
  "v_cvt_pk_bf16_f32 v144, v64, v65\n\t" \
  "v_cvt_pk_bf16_f32 v145, v66, v67\n\t" \
  "v_mfma_f32_16x16x32_bf16 v[64:67], a[192:195], v[80:83], v[184:187]\n\t" \
  "v_exp_f32 v68, v68\n\t" \
  "v_mfma_f32_16x16x32_bf16 v[64:67], a[196:199], v[84:87], v[64:67]\n\t" \
  "v_max3_f32 v203, v203, v44, v45\n\t" \
  "v_max3_f32 v192, v192, v46, v47\n\t" \
  "v_mfma_f32_16x16x32_bf16 v[64:67], a[200:203], v[88:91], v[64:67]\n\t" \
  "v_max_f32 v192, v192, v203\n\t" \
  "ds_read_b64_tr_b16 a[174:175], v229 offset:12800\n\t" \
  "v_mfma_f32_16x16x32_bf16 v[64:67], a[204:207], v[92:95], v[64:67]\n\t" \
  "v_cmp_lt_f32 vcc, 0x42800000, v192\n\t" \
  "ds_read_b64_tr_b16 a[176:177], v228 offset:768\n\t" \
  "v_exp_f32 v69, v69\n\t" \
  "v_exp_f32 v70, v70\n\t" \
  "v_exp_f32 v71, v71\n\t" \
  "v_cvt_pk_bf16_f32 v146, v68, v69\n\t" \
  "v_cvt_pk_bf16_f32 v147, v70, v71\n\t" \
  "v_mfma_f32_16x16x32_bf16 v[68:71], a[220:223], v[92:95], v[184:187]\n\t" \
  "s_cbranch_vccnz .Lsf_c11\n\t" \
  ".Lsf_r11:\n\t" \
  "v_max3_f32 v192, v64, v65, v66\n\t" \
  "v_mfma_f32_16x16x32_bf16 v[68:71], a[216:219], v[88:91], v[68:71]\n\t" \
  "v_exp_f32 v72, v72\n\t" \
  "v_mfma_f32_16x16x32_bf16 v[68:71], a[212:215], v[84:87], v[68:71]\n\t" \
  "v_exp_f32 v73, v73\n\t" \
  "v_mfma_f32_16x16x32_bf16 v[68:71], a[208:211], v[80:83], v[68:71]\n\t" \
  "v_cvt_pk_bf16_f32 v148, v72, v73\n\t" \
  "ds_read_b64_tr_b16 a[178:179], v228 offset:4864\n\t" \
  "v_exp_f32 v74, v74\n\t" \
  "v_exp_f32 v75, v75\n\t" \
  "s_nop 0\n\t" \
  "v_cvt_pk_bf16_f32 v149, v74, v75\n\t" \
  "v_mfma_f32_16x16x32_bf16 v[72:75], a[224:227], v[80:83], v[184:187]\n\t" \
  "v_exp_f32 v76, v76\n\t" \
  "v_mfma_f32_16x16x32_bf16 v[72:75], a[228:231], v[84:87], v[72:75]\n\t" \
  "v_max3_f32 v203, v67, v68, v69\n\t" \
  "v_max3_f32 v192, v192, v70, v71\n\t" \
  "v_mfma_f32_16x16x32_bf16 v[72:75], a[232:235], v[88:91], v[72:75]\n\t" \
  "v_exp_f32 v77, v77\n\t" \
  "v_mfma_f32_16x16x32_bf16 v[72:75], a[236:239], v[92:95], v[72:75]\n\t" \
  "v_cvt_pk_bf16_f32 v150, v76, v77\n\t" \
  "ds_read_b64_tr_b16 a[180:181], v228 offset:8960\n\t" \
  "v_exp_f32 v78, v78\n\t" \
  "v_exp_f32 v79, v79\n\t" \
  "s_nop 0\n\t" \
  "v_cvt_pk_bf16_f32 v151, v78, v79\n\t" \
  "v_mfma_f32_16x16x32_bf16 v[76:79], a[252:255], v[92:95], v[184:187]\n\t" \
  "v_exp_f32 v96, v96\n\t" \
  "v_mfma_f32_16x16x32_bf16 v[76:79], a[248:251], v[88:91], v[76:79]\n\t" \
  "ds_read_b64_tr_b16 a[182:183], v228 offset:13056\n\t" \
  "v_max3_f32 v203, v203, v72, v73\n\t" \
  "v_mfma_f32_16x16x32_bf16 v[76:79], a[244:247], v[84:87], v[76:79]\n\t" \
  "v_max3_f32 v192, v192, v74, v75\n\t" \
  "ds_read_b64_tr_b16 a[184:185], v229 offset:768\n\t" \
  "v_mfma_f32_16x16x32_bf16 v[76:79], a[240:243], v[80:83], v[76:79]\n\t" \
  "v_exp_f32 v97, v97\n\t" \
  "v_exp_f32 v98, v98\n\t" \
  "v_exp_f32 v99, v99\n\t" \
  "v_cvt_pk_bf16_f32 v152, v96, v97\n\t" \
  "v_cvt_pk_bf16_f32 v153, v98, v99\n\t" \
  "v_mfma_f32_16x16x32_bf16 v[96:99], a[192:195], v[112:115], v[188:191]\n\t" \
  "v_exp_f32 v100, v100\n\t" \
  "v_mfma_f32_16x16x32_bf16 v[96:99], a[196:199], v[116:119], v[96:99]\n\t" \
  "v_max3_f32 v203, v203, v76, v77\n\t" \
  "v_max3_f32 v192, v192, v78, v79\n\t" \
  "v_mfma_f32_16x16x32_bf16 v[96:99], a[200:203], v[120:123], v[96:99]\n\t" \
  "v_max_f32 v192, v192, v203\n\t" \
  "ds_read_b64_tr_b16 a[186:187], v229 offset:4864\n\t" \
  "v_mfma_f32_16x16x32_bf16 v[96:99], a[204:207], v[124:127], v[96:99]\n\t" \
  "v_cmp_lt_f32 vcc, 0x42800000, v192\n\t" \
  "ds_read_b64_tr_b16 a[188:189], v229 offset:8960\n\t" \
  "v_exp_f32 v101, v101\n\t" \
  "v_exp_f32 v102, v102\n\t" \
  "v_exp_f32 v103, v103\n\t" \
  "v_cvt_pk_bf16_f32 v154, v100, v101\n\t" \
  "v_cvt_pk_bf16_f32 v155, v102, v103\n\t" \
  "v_mfma_f32_16x16x32_bf16 v[100:103], a[220:223], v[124:127], v[188:191]\n\t" \
  "s_cbranch_vccnz .Lsf_c12\n\t" \
  ".Lsf_r12:\n\t" \
  "v_max3_f32 v192, v96, v97, v98\n\t" \
  "v_mfma_f32_16x16x32_bf16 v[100:103], a[216:219], v[120:123], v[100:103]\n\t" \
  "v_exp_f32 v104, v104\n\t" \
  "v_mfma_f32_16x16x32_bf16 v[100:103], a[212:215], v[116:119], v[100:103]\n\t" \
  "v_exp_f32 v105, v105\n\t" \
  "v_mfma_f32_16x16x32_bf16 v[100:103], a[208:211], v[112:115], v[100:103]\n\t" \
  "v_cvt_pk_bf16_f32 v156, v104, v105\n\t" \
  "ds_read_b64_tr_b16 a[190:191], v229 offset:13056\n\t" \
  "v_exp_f32 v106, v106\n\t" \
  "v_exp_f32 v107, v107\n\t" \
  "s_nop 0\n\t" \
  "v_cvt_pk_bf16_f32 v157, v106, v107\n\t" \
  "v_mfma_f32_16x16x32_bf16 v[104:107], a[224:227], v[112:115], v[188:191]\n\t" \
  "v_exp_f32 v108, v108\n\t" \
  "v_mfma_f32_16x16x32_bf16 v[104:107], a[228:231], v[116:119], v[104:107]\n\t" \
  "v_exp_f32 v109, v109\n\t" \
  "v_mfma_f32_16x16x32_bf16 v[104:107], a[232:235], v[120:123], v[104:107]\n\t" \
  "v_cvt_pk_bf16_f32 v158, v108, v109\n\t" \
  "v_max3_f32 v203, v99, v100, v101\n\t" \
  "v_mfma_f32_16x16x32_bf16 v[104:107], a[236:239], v[124:127], v[104:107]\n\t" \
  "v_max3_f32 v192, v192, v102, v103\n\t" \
  "v_exp_f32 v110, v110\n\t" \
  "v_exp_f32 v111, v111\n\t" \
  "s_nop 0\n\t" \
  "v_cvt_pk_bf16_f32 v159, v110, v111\n\t" \
  "v_mfma_f32_16x16x32_bf16 v[108:111], a[252:255], v[124:127], v[188:191]\n\t" \
  "v_exp_f32 v0, v0\n\t" \
  "v_mfma_f32_16x16x32_bf16 v[108:111], a[248:251], v[120:123], v[108:111]\n\t" \
  "v_exp_f32 v1, v1\n\t" \
  "v_mfma_f32_16x16x32_bf16 v[108:111], a[244:247], v[116:119], v[108:111]\n\t" \
  "v_max3_f32 v203, v203, v104, v105\n\t" \
  "v_max3_f32 v192, v192, v106, v107\n\t" \
  "v_mfma_f32_16x16x32_bf16 v[108:111], a[240:243], v[112:115], v[108:111]\n\t" \
  "v_exp_f32 v2, v2\n\t" \
  "s_waitcnt vmcnt(3) lgkmcnt(0)\n\t" \
  "s_barrier\n\t" \
  "v_mfma_f32_16x16x32_bf16 a[0:3], a[128:131], v[128:131], 0\n\t" \
  "v_exp_f32 v3, v3\n\t" \
  "v_mfma_f32_16x16x32_bf16 a[0:3], a[132:135], v[132:135], a[0:3]\n\t" \
  "v_exp_f32 v4, v4\n\t" \
  "v_mfma_f32_16x16x32_bf16 a[16:19], a[136:139], v[128:131], 0\n\t" \
  "ds_read_b128 a[192:195], v220 offset:0\n\t" \
  "v_max3_f32 v203, v203, v108, v109\n\t" \
  "v_mfma_f32_16x16x32_bf16 a[16:19], a[140:143], v[132:135], a[16:19]\n\t" \
  "v_max3_f32 v192, v192, v110, v111\n\t" \
  "v_max_f32 v192, v192, v203\n\t" \
  "v_mfma_f32_16x16x32_bf16 a[32:35], a[144:147], v[128:131], 0\n\t" \
  "v_cmp_lt_f32 vcc, 0x42800000, v192\n\t" \
  "ds_read_b128 a[196:199], v221 offset:0\n\t" \
  "v_mfma_f32_16x16x32_bf16 a[32:35], a[148:151], v[132:135], a[32:35]\n\t" \
  "s_cbranch_vccnz .Lsf_c13\n\t" \
  ".Lsf_r13:\n\t" \
  "ds_read_b128 a[200:203], v222 offset:0\n\t" \
  "v_mfma_f32_16x16x32_bf16 a[48:51], a[152:155], v[128:131], 0\n\t" \
  "v_exp_f32 v5, v5\n\t" \
  "v_mfma_f32_16x16x32_bf16 a[48:51], a[156:159], v[132:135], a[48:51]\n\t" \
  "v_exp_f32 v6, v6\n\t" \
  "v_mfma_f32_16x16x32_bf16 a[64:67], a[160:163], v[128:131], 0\n\t" \
  "v_exp_f32 v7, v7\n\t" \
  "v_mfma_f32_16x16x32_bf16 a[64:67], a[164:167], v[132:135], a[64:67]\n\t" \
  "v_exp_f32 v8, v8\n\t" \
  "v_mfma_f32_16x16x32_bf16 a[80:83], a[168:171], v[128:131], 0\n\t" \
  "v_exp_f32 v9, v9\n\t" \
  "v_mfma_f32_16x16x32_bf16 a[80:83], a[172:175], v[132:135], a[80:83]\n\t" \
  "v_exp_f32 v10, v10\n\t" \
  "v_mfma_f32_16x16x32_bf16 a[96:99], a[176:179], v[128:131], 0\n\t" \
  "v_exp_f32 v11, v11\n\t" \
  "v_mfma_f32_16x16x32_bf16 a[96:99], a[180:183], v[132:135], a[96:99]\n\t" \
  "v_exp_f32 v12, v12\n\t" \
  "v_mfma_f32_16x16x32_bf16 a[112:115], a[184:187], v[128:131], 0\n\t" \
  "ds_read_b128 a[204:207], v223 offset:0\n\t" \
  "ds_read_b128 a[208:211], v220 offset:4096\n\t" \
  "v_mfma_f32_16x16x32_bf16 a[112:115], a[188:191], v[132:135], a[112:115]\n\t" \
  "v_exp_f32 v13, v13\n\t" \
  "v_mfma_f32_16x16x32_bf16 v[160:163], v[216:219], v[128:131], 0\n\t" \
  "v_exp_f32 v14, v14\n\t" \
  "v_mfma_f32_16x16x32_bf16 v[160:163], v[216:219], v[132:135], v[160:163]\n\t" \
  "v_cvt_pk_bf16_f32 v128, v0, v1\n\t" \
  "v_cvt_pk_bf16_f32 v129, v2, v3\n\t" \
  "v_mfma_f32_16x16x32_bf16 a[4:7], a[128:131], v[136:139], 0\n\t" \
  "v_cvt_pk_bf16_f32 v130, v4, v5\n\t" \
  "v_cvt_pk_bf16_f32 v131, v6, v7\n\t" \
  "v_mfma_f32_16x16x32_bf16 a[4:7], a[132:135], v[140:143], a[4:7]\n\t" \
  "v_cvt_pk_bf16_f32 v132, v8, v9\n\t" \
  "v_cvt_pk_bf16_f32 v133, v10, v11\n\t" \
  "v_mfma_f32_16x16x32_bf16 a[20:23], a[136:139], v[136:139], 0\n\t" \
  "v_cvt_pk_bf16_f32 v134, v12, v13\n\t" \
  "ds_read_b128 a[212:215], v221 offset:4096\n\t" \
  "v_mfma_f32_16x16x32_bf16 a[20:23], a[140:143], v[140:143], a[20:23]\n\t" \
  "v_exp_f32 v15, v15\n\t" \
  "v_mfma_f32_16x16x32_bf16 a[36:39], a[144:147], v[136:139], 0\n\t" \
  "v_cvt_pk_bf16_f32 v135, v14, v15\n\t" \
  "s_add_u32 m0, %[m], 65536\n\t" \
  "v_mfma_f32_16x16x32_bf16 a[36:39], a[148:151], v[140:143], a[36:39]\n\t" \
  "v_exp_f32 v32, v32\n\t" \
  "v_mfma_f32_16x16x32_bf16 a[52:55], a[152:155], v[136:139], 0\n\t" \
  "ds_read_b128 a[216:219], v222 offset:4096\n\t" \
  "s_add_u32 s96, %[sofk], 49152\n\t" \
  "v_mfma_f32_16x16x32_bf16 a[52:55], a[156:159], v[140:143], a[52:55]\n\t" \
  "v_exp_f32 v33, v33\n\t" \
  "v_mfma_f32_16x16x32_bf16 a[68:71], a[160:163], v[136:139], 0\n\t" \
  "v_exp_f32 v34, v34\n\t" \
  "v_mfma_f32_16x16x32_bf16 a[68:71], a[164:167], v[140:143], a[68:71]\n\t" \
  "ds_read_b128 a[220:223], v223 offset:4096\n\t" \
  "buffer_load_dwordx4 v224, %[sK], s96 offen offset:0 lds\n\t" \
  "v_mfma_f32_16x16x32_bf16 a[84:87], a[168:171], v[136:139], 0\n\t" \
  "v_exp_f32 v35, v35\n\t" \
  "v_mfma_f32_16x16x32_bf16 a[84:87], a[172:175], v[140:143], a[84:87]\n\t" \
  "v_exp_f32 v36, v36\n\t" \
  "v_mfma_f32_16x16x32_bf16 a[100:103], a[176:179], v[136:139], 0\n\t" \
  "v_exp_f32 v37, v37\n\t" \
  "v_mfma_f32_16x16x32_bf16 a[100:103], a[180:183], v[140:143], a[100:103]\n\t" \
  "ds_read_b128 a[224:227], v220 offset:8192\n\t" \
  "buffer_load_dwordx4 v225, %[sK], s96 offen offset:1024 lds\n\t" \
  "v_mfma_f32_16x16x32_bf16 a[116:119], a[184:187], v[136:139], 0\n\t" \
  "v_exp_f32 v38, v38\n\t" \
  "v_mfma_f32_16x16x32_bf16 a[116:119], a[188:191], v[140:143], a[116:119]\n\t" \
  "v_exp_f32 v39, v39\n\t" \
  "v_mfma_f32_16x16x32_bf16 v[164:167], v[216:219], v[136:139], 0\n\t" \
  "buffer_load_dwordx4 v226, %[sK], s96 offen offset:2048 lds\n\t" \
  "ds_read_b128 a[228:231], v221 offset:8192\n\t" \
  "v_mfma_f32_16x16x32_bf16 v[164:167], v[216:219], v[140:143], v[164:167]\n\t" \
  "v_cvt_pk_bf16_f32 v136, v32, v33\n\t" \
  "v_cvt_pk_bf16_f32 v137, v34, v35\n\t" \
  "v_mfma_f32_16x16x32_bf16 a[8:11], a[128:131], v[144:147], 0\n\t" \
  "v_cvt_pk_bf16_f32 v138, v36, v37\n\t" \
  "v_cvt_pk_bf16_f32 v139, v38, v39\n\t" \
  "v_mfma_f32_16x16x32_bf16 a[8:11], a[132:135], v[148:151], a[8:11]\n\t" \
  "v_exp_f32 v40, v40\n\t" \
  "v_mfma_f32_16x16x32_bf16 a[24:27], a[136:139], v[144:147], 0\n\t" \
  "buffer_load_dwordx4 v227, %[sK], s96 offen offset:3072 lds\n\t" \
  "ds_read_b128 a[232:235], v222 offset:8192\n\t" \
  "v_mfma_f32_16x16x32_bf16 a[24:27], a[140:143], v[148:151], a[24:27]\n\t" \
  "v_exp_f32 v41, v41\n\t" \
  "v_mfma_f32_16x16x32_bf16 a[40:43], a[144:147], v[144:147], 0\n\t" \
  "v_cvt_pk_bf16_f32 v140, v40, v41\n\t" \
  "ds_read_b128 a[236:239], v223 offset:8192\n\t" \
  "v_mfma_f32_16x16x32_bf16 a[40:43], a[148:151], v[148:151], a[40:43]\n\t" \
  "v_exp_f32 v42, v42\n\t" \
  "v_mfma_f32_16x16x32_bf16 a[56:59], a[152:155], v[144:147], 0\n\t" \
  "v_exp_f32 v43, v43\n\t" \
  "v_mfma_f32_16x16x32_bf16 a[56:59], a[156:159], v[148:151], a[56:59]\n\t" \
  "v_cvt_pk_bf16_f32 v141, v42, v43\n\t" \
  "ds_read_b128 a[240:243], v220 offset:12288\n\t" \
  "v_mfma_f32_16x16x32_bf16 a[72:75], a[160:163], v[144:147], 0\n\t" \
  "s_add_u32 m0, %[m], 114688\n\t" \
  "s_mov_b32 s96, 0\n\t" \
  "buffer_load_dwordx4 v232, %[sU], s96 offen offset:0 lds\n\t" \
  "v_mfma_f32_16x16x32_bf16 a[72:75], a[164:167], v[148:151], a[72:75]\n\t" \
  "v_exp_f32 v44, v44\n\t" \
  "v_mfma_f32_16x16x32_bf16 a[88:91], a[168:171], v[144:147], 0\n\t" \
  "s_add_u32 m0, %[m], 81920\n\t" \
  "s_add_u32 s96, %[sofk], 32768\n\t" \
  "v_mfma_f32_16x16x32_bf16 a[88:91], a[172:175], v[148:151], a[88:91]\n\t" \
  "v_exp_f32 v45, v45\n\t" \
  "v_mfma_f32_16x16x32_bf16 a[104:107], a[176:179], v[144:147], 0\n\t" \
  "v_cvt_pk_bf16_f32 v142, v44, v45\n\t" \
  "ds_read_b128 a[244:247], v221 offset:12288\n\t" \
  "v_mfma_f32_16x16x32_bf16 a[104:107], a[180:183], v[148:151], a[104:107]\n\t" \
  "v_exp_f32 v46, v46\n\t" \
  "v_mfma_f32_16x16x32_bf16 a[120:123], a[184:187], v[144:147], 0\n\t" \
  "v_exp_f32 v47, v47\n\t" \
  "v_mfma_f32_16x16x32_bf16 a[120:123], a[188:191], v[148:151], a[120:123]\n\t" \
  "v_cvt_pk_bf16_f32 v143, v46, v47\n\t" \
  "buffer_load_dwordx4 v230, %[sV], s96 offen offset:0 lds\n\t" \
  "v_mfma_f32_16x16x32_bf16 v[168:171], v[216:219], v[144:147], 0\n\t" \
  "v_exp_f32 v64, v64\n\t" \
  "v_mfma_f32_16x16x32_bf16 v[168:171], v[216:219], v[148:151], v[168:171]\n\t" \
  "v_exp_f32 v65, v65\n\t" \
  "v_mfma_f32_16x16x32_bf16 a[12:15], a[128:131], v[152:155], 0\n\t" \
  "v_cvt_pk_bf16_f32 v144, v64, v65\n\t" \
  "ds_read_b128 a[248:251], v222 offset:12288\n\t" \
  "v_mfma_f32_16x16x32_bf16 a[12:15], a[132:135], v[156:159], a[12:15]\n\t" \
  "v_exp_f32 v66, v66\n\t" \
  "v_mfma_f32_16x16x32_bf16 a[28:31], a[136:139], v[152:155], 0\n\t" \
  "v_exp_f32 v67, v67\n\t" \
  "v_mfma_f32_16x16x32_bf16 a[28:31], a[140:143], v[156:159], a[28:31]\n\t" \
  "v_cvt_pk_bf16_f32 v145, v66, v67\n\t" \
  "buffer_load_dwordx4 v231, %[sV], s96 offen offset:1024 lds\n\t" \
  "v_mfma_f32_16x16x32_bf16 a[44:47], a[144:147], v[152:155], 0\n\t" \
  "ds_read_b128 a[252:255], v223 offset:12288\n\t" \
  "buffer_load_dwordx4 v230, %[sV], s96 offen offset:2048 lds\n\t" \
  "v_mfma_f32_16x16x32_bf16 a[44:47], a[148:151], v[156:159], a[44:47]\n\t" \
  "v_exp_f32 v68, v68\n\t" \
  "v_mfma_f32_16x16x32_bf16 a[60:63], a[152:155], v[152:155], 0\n\t" \
  "v_exp_f32 v69, v69\n\t" \
  "v_mfma_f32_16x16x32_bf16 a[60:63], a[156:159], v[156:159], a[60:63]\n\t" \
  "v_cvt_pk_bf16_f32 v146, v68, v69\n\t" \
  "buffer_load_dwordx4 v231, %[sV], s96 offen offset:3072 lds\n\t" \
  "v_mfma_f32_16x16x32_bf16 a[76:79], a[160:163], v[152:155], 0\n\t" \
  "v_exp_f32 v70, v70\n\t" \
  "v_mfma_f32_16x16x32_bf16 a[76:79], a[164:167], v[156:159], a[76:79]\n\t" \
  "v_exp_f32 v71, v71\n\t" \
  "v_mfma_f32_16x16x32_bf16 a[92:95], a[168:171], v[152:155], 0\n\t" \
  "v_cvt_pk_bf16_f32 v147, v70, v71\n\t" \
  "v_mfma_f32_16x16x32_bf16 a[92:95], a[172:175], v[156:159], a[92:95]\n\t" \
  "v_exp_f32 v72, v72\n\t" \
  "v_mfma_f32_16x16x32_bf16 a[108:111], a[176:179], v[152:155], 0\n\t" \
  "v_exp_f32 v73, v73\n\t" \
  "v_mfma_f32_16x16x32_bf16 a[108:111], a[180:183], v[156:159], a[108:111]\n\t" \
  "v_cvt_pk_bf16_f32 v148, v72, v73\n\t" \
  "v_mfma_f32_16x16x32_bf16 a[124:127], a[184:187], v[152:155], 0\n\t" \
  "s_add_u32 m0, %[m], 114688\n\t" \
  "s_mov_b32 s96, 6144\n\t" \
  "buffer_load_dwordx4 v232, %[sU], s96 offen offset:1024 lds\n\t" \
  "v_mfma_f32_16x16x32_bf16 a[124:127], a[188:191], v[156:159], a[124:127]\n\t" \
  "v_exp_f32 v74, v74\n\t" \
  "v_mfma_f32_16x16x32_bf16 v[172:175], v[216:219], v[152:155], 0\n\t" \
  "v_exp_f32 v75, v75\n\t" \
  "v_mfma_f32_16x16x32_bf16 v[172:175], v[216:219], v[156:159], v[172:175]\n\t" \
  "v_cvt_pk_bf16_f32 v149, v74, v75\n\t" \
  "s_cmp_lg_u32 s99, 0\n\t" \
  "s_cbranch_scc1 .Lsf_p1\n\t" \
  ".Lsf_q1:\n\t" \
  "s_waitcnt lgkmcnt(0)\n\t" \
  "v_mfma_f32_16x16x32_bf16 v[0:3], a[192:195], v[16:19], v[176:179]\n\t" \
  "v_exp_f32 v76, v76\n\t" \
  "v_mfma_f32_16x16x32_bf16 v[0:3], a[196:199], v[20:23], v[0:3]\n\t" \
  "v_exp_f32 v77, v77\n\t" \
  "v_mfma_f32_16x16x32_bf16 v[0:3], a[200:203], v[24:27], v[0:3]\n\t" \
  "v_cvt_pk_bf16_f32 v150, v76, v77\n\t" \
  "ds_read_b64_tr_b16 a[128:129], v237 offset:0\n\t" \
  "v_mfma_f32_16x16x32_bf16 v[0:3], a[204:207], v[28:31], v[0:3]\n\t" \
  "v_exp_f32 v78, v78\n\t" \
  "v_mfma_f32_16x16x32_bf16 v[4:7], a[220:223], v[28:31], v[176:179]\n\t" \
  "ds_read_b64_tr_b16 a[130:131], v237 offset:4096\n\t" \
  "ds_read_b64_tr_b16 a[132:133], v237 offset:8192\n\t" \
  "v_mfma_f32_16x16x32_bf16 v[4:7], a[216:219], v[24:27], v[4:7]\n\t" \
  "v_exp_f32 v79, v79\n\t" \
  "v_mfma_f32_16x16x32_bf16 v[4:7], a[212:215], v[20:23], v[4:7]\n\t" \
  "v_cvt_pk_bf16_f32 v151, v78, v79\n\t" \
  "v_max3_f32 v192, v0, v1, v2\n\t" \
  "v_mfma_f32_16x16x32_bf16 v[4:7], a[208:211], v[16:19], v[4:7]\n\t" \
  "v_exp_f32 v96, v96\n\t" \
  "v_mfma_f32_16x16x32_bf16 v[8:11], a[224:227], v[16:19], v[176:179]\n\t" \
  "ds_read_b64_tr_b16 a[134:135], v237 offset:12288\n\t" \
  "ds_read_b64_tr_b16 a[136:137], v238 offset:0\n\t" \
  "v_mfma_f32_16x16x32_bf16 v[8:11], a[228:231], v[20:23], v[8:11]\n\t" \
  "s_add_u32 m0, %[m], 114688\n\t" \
  "s_mov_b32 s96, 12288\n\t" \
  "buffer_load_dwordx4 v232, %[sU], s96 offen offset:2048 lds\n\t" \
  "v_mfma_f32_16x16x32_bf16 v[8:11], a[232:235], v[24:27], v[8:11]\n\t" \
  "v_max3_f32 v203, v3, v4, v5\n\t" \
  "v_max3_f32 v192, v192, v6, v7\n\t" \
  "v_mfma_f32_16x16x32_bf16 v[8:11], a[236:239], v[28:31], v[8:11]\n\t" \
  "ds_read_b64_tr_b16 a[138:139], v238 offset:4096\n\t" \
  "ds_read_b64_tr_b16 a[140:141], v238 offset:8192\n\t" \
  "v_mfma_f32_16x16x32_bf16 v[12:15], a[252:255], v[28:31], v[176:179]\n\t" \
  "v_exp_f32 v97, v97\n\t" \
  "v_mfma_f32_16x16x32_bf16 v[12:15], a[248:251], v[24:27], v[12:15]\n\t" \
  "v_cvt_pk_bf16_f32 v152, v96, v97\n\t" \
  "ds_read_b64_tr_b16 a[142:143], v238 offset:12288\n\t" \
  "v_mfma_f32_16x16x32_bf16 v[12:15], a[244:247], v[20:23], v[12:15]\n\t" \
  "v_max3_f32 v203, v203, v8, v9\n\t" \
  "v_max3_f32 v192, v192, v10, v11\n\t" \
  "v_mfma_f32_16x16x32_bf16 v[12:15], a[240:243], v[16:19], v[12:15]\n\t" \
  "v_exp_f32 v98, v98\n\t" \
  "v_mfma_f32_16x16x32_bf16 v[32:35], a[192:195], v[48:51], v[180:183]\n\t" \
  "ds_read_b64_tr_b16 a[144:145], v237 offset:256\n\t" \
  "ds_read_b64_tr_b16 a[146:147], v237 offset:4352\n\t" \
  "v_mfma_f32_16x16x32_bf16 v[32:35], a[196:199], v[52:55], v[32:35]\n\t" \
  "v_exp_f32 v99, v99\n\t" \
  "v_mfma_f32_16x16x32_bf16 v[32:35], a[200:203], v[56:59], v[32:35]\n\t" \
  "v_cvt_pk_bf16_f32 v153, v98, v99\n\t" \
  "v_max3_f32 v203, v203, v12, v13\n\t" \
  "v_mfma_f32_16x16x32_bf16 v[32:35], a[204:207], v[60:63], v[32:35]\n\t" \
  "v_max3_f32 v192, v192, v14, v15\n\t" \
  "v_max_f32 v192, v192, v203\n\t" \
  "v_mfma_f32_16x16x32_bf16 v[36:39], a[220:223], v[60:63], v[180:183]\n\t" \
  "v_cmp_lt_f32 vcc, 0x42800000, v192\n\t" \
  "ds_read_b64_tr_b16 a[148:149], v237 offset:8448\n\t" \
  "v_mfma_f32_16x16x32_bf16 v[36:39], a[216:219], v[56:59], v[36:39]\n\t" \
  "s_cbranch_vccnz .Lsf_c20\n\t" \
  ".Lsf_r20:\n\t" \
  "ds_read_b64_tr_b16 a[150:151], v237 offset:12544\n\t" \
  "v_mfma_f32_16x16x32_bf16 v[36:39], a[212:215], v[52:55], v[36:39]\n\t" \
  "v_max3_f32 v192, v32, v33, v34\n\t" \
  "ds_read_b64_tr_b16 a[152:153], v238 offset:256\n\t" \
  "v_mfma_f32_16x16x32_bf16 v[36:39], a[208:211], v[48:51], v[36:39]\n\t" \
  "s_mov_b32 s96, 18432\n\t" \
  "buffer_load_dwordx4 v232, %[sU], s96 offen offset:3072 lds\n\t" \
  "v_mfma_f32_16x16x32_bf16 v[40:43], a[224:227], v[48:51], v[180:183]\n\t" \
  "v_exp_f32 v100, v100\n\t" \
  "v_mfma_f32_16x16x32_bf16 v[40:43], a[228:231], v[52:55], v[40:43]\n\t" \
  "ds_read_b64_tr_b16 a[154:155], v238 offset:4352\n\t" \
  "ds_read_b64_tr_b16 a[156:157], v238 offset:8448\n\t" \
  "v_mfma_f32_16x16x32_bf16 v[40:43], a[232:235], v[56:59], v[40:43]\n\t" \
  "v_max3_f32 v203, v35, v36, v37\n\t" \
  "v_max3_f32 v192, v192, v38, v39\n\t" \
  "v_mfma_f32_16x16x32_bf16 v[40:43], a[236:239], v[60:63], v[40:43]\n\t" \
  "v_exp_f32 v101, v101\n\t" \
  "v_mfma_f32_16x16x32_bf16 v[44:47], a[252:255], v[60:63], v[180:183]\n\t" \
  "v_cvt_pk_bf16_f32 v154, v100, v101\n\t" \
  "ds_read_b64_tr_b16 a[158:159], v238 offset:12544\n\t" \
  "v_mfma_f32_16x16x32_bf16 v[44:47], a[248:251], v[56:59], v[44:47]\n\t" \
  "v_exp_f32 v102, v102\n\t" \
  "v_mfma_f32_16x16x32_bf16 v[44:47], a[244:247], v[52:55], v[44:47]\n\t" \
  "ds_read_b64_tr_b16 a[160:161], v237 offset:512\n\t" \
  "v_max3_f32 v203, v203, v40, v41\n\t" \
  "v_mfma_f32_16x16x32_bf16 v[44:47], a[240:243], v[48:51], v[44:47]\n\t" \
  "v_max3_f32 v192, v192, v42, v43\n\t" \
  "ds_read_b64_tr_b16 a[162:163], v237 offset:4608\n\t" \
  "v_mfma_f32_16x16x32_bf16 v[64:67], a[192:195], v[80:83], v[184:187]\n\t" \
  "v_exp_f32 v103, v103\n\t" \
  "v_mfma_f32_16x16x32_bf16 v[64:67], a[196:199], v[84:87], v[64:67]\n\t" \
  "v_cvt_pk_bf16_f32 v155, v102, v103\n\t" \
  "ds_read_b64_tr_b16 a[164:165], v237 offset:8704\n\t" \
  "v_mfma_f32_16x16x32_bf16 v[64:67], a[200:203], v[88:91], v[64:67]\n\t" \
  "v_max3_f32 v203, v203, v44, v45\n\t" \
  "v_max3_f32 v192, v192, v46, v47\n\t" \
  "v_mfma_f32_16x16x32_bf16 v[64:67], a[204:207], v[92:95], v[64:67]\n\t" \
  "v_max_f32 v192, v192, v203\n\t" \
  "ds_read_b64_tr_b16 a[166:167], v237 offset:12800\n\t" \
  "v_mfma_f32_16x16x32_bf16 v[68:71], a[220:223], v[92:95], v[184:187]\n\t" \
  "v_cmp_lt_f32 vcc, 0x42800000, v192\n\t" \
  "ds_read_b64_tr_b16 a[168:169], v238 offset:512\n\t" \
  "v_mfma_f32_16x16x32_bf16 v[68:71], a[216:219], v[88:91], v[68:71]\n\t" \
  "s_cbranch_vccnz .Lsf_c21\n\t" \
  ".Lsf_r21:\n\t" \
  "ds_read_b64_tr_b16 a[170:171], v238 offset:4608\n\t" \
  "v_mfma_f32_16x16x32_bf16 v[68:71], a[212:215], v[84:87], v[68:71]\n\t" \
  "s_add_u32 m0, %[m], 131072\n\t" \
  "s_mov_b32 s96, 28672\n\t" \
  "buffer_load_dwordx4 v232, %[sU], s96 offen offset:0 lds\n\t" \
  "v_mfma_f32_16x16x32_bf16 v[68:71], a[208:211], v[80:83], v[68:71]\n\t" \
  "v_max3_f32 v192, v64, v65, v66\n\t" \
  "ds_read_b64_tr_b16 a[172:173], v238 offset:8704\n\t" \
  "v_mfma_f32_16x16x32_bf16 v[72:75], a[224:227], v[80:83], v[184:187]\n\t" \
  "v_exp_f32 v104, v104\n\t" \
  "v_mfma_f32_16x16x32_bf16 v[72:75], a[228:231], v[84:87], v[72:75]\n\t" \
  "v_exp_f32 v105, v105\n\t" \
  "v_mfma_f32_16x16x32_bf16 v[72:75], a[232:235], v[88:91], v[72:75]\n\t" \
  "v_cvt_pk_bf16_f32 v156, v104, v105\n\t" \
  "v_max3_f32 v203, v67, v68, v69\n\t" \
  "v_mfma_f32_16x16x32_bf16 v[72:75], a[236:239], v[92:95], v[72:75]\n\t" \
  "v_max3_f32 v192, v192, v70, v71\n\t" \
  "ds_read_b64_tr_b16 a[174:175], v238 offset:12800\n\t" \
  "v_mfma_f32_16x16x32_bf16 v[76:79], a[252:255], v[92:95], v[184:187]\n\t" \
  "ds_read_b64_tr_b16 a[176:177], v237 offset:768\n\t" \
  "ds_read_b64_tr_b16 a[178:179], v237 offset:4864\n\t" \
  "v_mfma_f32_16x16x32_bf16 v[76:79], a[248:251], v[88:91], v[76:79]\n\t" \
  "v_exp_f32 v106, v106\n\t" \
  "v_mfma_f32_16x16x32_bf16 v[76:79], a[244:247], v[84:87], v[76:79]\n\t" \
  "v_max3_f32 v203, v203, v72, v73\n\t" \
  "v_max3_f32 v192, v192, v74, v75\n\t" \
  "v_mfma_f32_16x16x32_bf16 v[76:79], a[240:243], v[80:83], v[76:79]\n\t" \
  "ds_read_b64_tr_b16 a[180:181], v237 offset:8960\n\t" \
  "ds_read_b64_tr_b16 a[182:183], v237 offset:13056\n\t" \
  "v_mfma_f32_16x16x32_bf16 v[96:99], a[192:195], v[112:115], v[188:191]\n\t" \
  "v_exp_f32 v107, v107\n\t" \
  "v_mfma_f32_16x16x32_bf16 v[96:99], a[196:199], v[116:119], v[96:99]\n\t" \
  "v_cvt_pk_bf16_f32 v157, v106, v107\n\t" \
  "ds_read_b64_tr_b16 a[184:185], v238 offset:768\n\t" \
  "v_mfma_f32_16x16x32_bf16 v[96:99], a[200:203], v[120:123], v[96:99]\n\t" \
  "s_mov_b32 s96, 34816\n\t" \
  "buffer_load_dwordx4 v232, %[sU], s96 offen offset:1024 lds\n\t" \
  "v_mfma_f32_16x16x32_bf16 v[96:99], a[204:207], v[124:127], v[96:99]\n\t" \
  "v_max3_f32 v203, v203, v76, v77\n\t" \
  "v_max3_f32 v192, v192, v78, v79\n\t" \
  "v_mfma_f32_16x16x32_bf16 v[100:103], a[220:223], v[124:127], v[188:191]\n\t" \
  "v_max_f32 v192, v192, v203\n\t" \
  "ds_read_b64_tr_b16 a[186:187], v238 offset:4864\n\t" \
  "v_mfma_f32_16x16x32_bf16 v[100:103], a[216:219], v[120:123], v[100:103]\n\t" \
  "v_cmp_lt_f32 vcc, 0x42800000, v192\n\t" \
  "ds_read_b64_tr_b16 a[188:189], v238 offset:8960\n\t" \
  "v_mfma_f32_16x16x32_bf16 v[100:103], a[212:215], v[116:119], v[100:103]\n\t" \
  "s_cbranch_vccnz .Lsf_c22\n\t" \
  ".Lsf_r22:\n\t" \
  "v_max3_f32 v192, v96, v97, v98\n\t" \
  "v_mfma_f32_16x16x32_bf16 v[100:103], a[208:211], v[112:115], v[100:103]\n\t" \
  "v_exp_f32 v108, v108\n\t" \
  "v_mfma_f32_16x16x32_bf16 v[104:107], a[224:227], v[112:115], v[188:191]\n\t" \
  "v_exp_f32 v109, v109\n\t" \
  "v_mfma_f32_16x16x32_bf16 v[104:107], a[228:231], v[116:119], v[104:107]\n\t" \
  "v_cvt_pk_bf16_f32 v158, v108, v109\n\t" \
  "ds_read_b64_tr_b16 a[190:191], v238 offset:13056\n\t" \
  "v_mfma_f32_16x16x32_bf16 v[104:107], a[232:235], v[120:123], v[104:107]\n\t" \
  "v_exp_f32 v110, v110\n\t" \
  "v_mfma_f32_16x16x32_bf16 v[104:107], a[236:239], v[124:127], v[104:107]\n\t" \
  "v_max3_f32 v203, v99, v100, v101\n\t" \
  "v_max3_f32 v192, v192, v102, v103\n\t" \
  "v_exp_f32 v111, v111\n\t" \
  "s_nop 0\n\t" \
  "v_cvt_pk_bf16_f32 v159, v110, v111\n\t" \
  "v_mfma_f32_16x16x32_bf16 v[108:111], a[252:255], v[124:127], v[188:191]\n\t" \
  "v_exp_f32 v0, v0\n\t" \
  "v_mfma_f32_16x16x32_bf16 v[108:111], a[248:251], v[120:123], v[108:111]\n\t" \
  "v_exp_f32 v1, v1\n\t" \
  "v_mfma_f32_16x16x32_bf16 v[108:111], a[244:247], v[116:119], v[108:111]\n\t" \
  "v_max3_f32 v203, v203, v104, v105\n\t" \
  "v_max3_f32 v192, v192, v106, v107\n\t" \
  "v_mfma_f32_16x16x32_bf16 v[108:111], a[240:243], v[112:115], v[108:111]\n\t" \
  "v_exp_f32 v2, v2\n\t" \
  "s_waitcnt vmcnt(5) lgkmcnt(0)\n\t" \
  "s_barrier\n\t" \
  "v_mfma_f32_16x16x32_bf16 a[0:3], a[128:131], v[128:131], a[0:3]\n\t" \
  "v_exp_f32 v3, v3\n\t" \
  "v_mfma_f32_16x16x32_bf16 a[0:3], a[132:135], v[132:135], a[0:3]\n\t" \
  "v_exp_f32 v4, v4\n\t" \
  "v_mfma_f32_16x16x32_bf16 a[16:19], a[136:139], v[128:131], a[16:19]\n\t" \
  "ds_read_b128 a[192:195], v220 offset:16384\n\t" \
  "v_max3_f32 v203, v203, v108, v109\n\t" \
  "v_mfma_f32_16x16x32_bf16 a[16:19], a[140:143], v[132:135], a[16:19]\n\t" \
  "v_max3_f32 v192, v192, v110, v111\n\t" \
  "v_max_f32 v192, v192, v203\n\t" \
  "v_mfma_f32_16x16x32_bf16 a[32:35], a[144:147], v[128:131], a[32:35]\n\t" \
  "v_cmp_lt_f32 vcc, 0x42800000, v192\n\t" \
  "ds_read_b128 a[196:199], v221 offset:16384\n\t" \
  "v_mfma_f32_16x16x32_bf16 a[32:35], a[148:151], v[132:135], a[32:35]\n\t" \
  "s_cbranch_vccnz .Lsf_c23\n\t" \
  ".Lsf_r23:\n\t" \
  "ds_read_b128 a[200:203], v222 offset:16384\n\t" \
  "v_mfma_f32_16x16x32_bf16 a[48:51], a[152:155], v[128:131], a[48:51]\n\t" \
  "v_exp_f32 v5, v5\n\t" \
  "v_mfma_f32_16x16x32_bf16 a[48:51], a[156:159], v[132:135], a[48:51]\n\t" \
  "v_exp_f32 v6, v6\n\t" \
  "v_mfma_f32_16x16x32_bf16 a[64:67], a[160:163], v[128:131], a[64:67]\n\t" \
  "v_exp_f32 v7, v7\n\t" \
  "v_mfma_f32_16x16x32_bf16 a[64:67], a[164:167], v[132:135], a[64:67]\n\t" \
  "v_exp_f32 v8, v8\n\t" \
  "v_mfma_f32_16x16x32_bf16 a[80:83], a[168:171], v[128:131], a[80:83]\n\t" \
  "v_exp_f32 v9, v9\n\t" \
  "v_mfma_f32_16x16x32_bf16 a[80:83], a[172:175], v[132:135], a[80:83]\n\t" \
  "v_exp_f32 v10, v10\n\t" \
  "v_mfma_f32_16x16x32_bf16 a[96:99], a[176:179], v[128:131], a[96:99]\n\t" \
  "v_exp_f32 v11, v11\n\t" \
  "v_mfma_f32_16x16x32_bf16 a[96:99], a[180:183], v[132:135], a[96:99]\n\t" \
  "v_exp_f32 v12, v12\n\t" \
  "v_mfma_f32_16x16x32_bf16 a[112:115], a[184:187], v[128:131], a[112:115]\n\t" \
  "ds_read_b128 a[204:207], v223 offset:16384\n\t" \
  "ds_read_b128 a[208:211], v220 offset:20480\n\t" \
  "v_mfma_f32_16x16x32_bf16 a[112:115], a[188:191], v[132:135], a[112:115]\n\t" \
  "v_exp_f32 v13, v13\n\t" \
  "v_mfma_f32_16x16x32_bf16 v[160:163], v[216:219], v[128:131], v[160:163]\n\t" \
  "v_exp_f32 v14, v14\n\t" \
  "v_mfma_f32_16x16x32_bf16 v[160:163], v[216:219], v[132:135], v[160:163]\n\t" \
  "v_cvt_pk_bf16_f32 v128, v0, v1\n\t" \
  "v_cvt_pk_bf16_f32 v129, v2, v3\n\t" \
  "v_mfma_f32_16x16x32_bf16 a[4:7], a[128:131], v[136:139], a[4:7]\n\t" \
  "v_cvt_pk_bf16_f32 v130, v4, v5\n\t" \
  "v_cvt_pk_bf16_f32 v131, v6, v7\n\t" \
  "v_mfma_f32_16x16x32_bf16 a[4:7], a[132:135], v[140:143], a[4:7]\n\t" \
  "v_cvt_pk_bf16_f32 v132, v8, v9\n\t" \
  "v_cvt_pk_bf16_f32 v133, v10, v11\n\t" \
  "v_mfma_f32_16x16x32_bf16 a[20:23], a[136:139], v[136:139], a[20:23]\n\t" \
  "v_cvt_pk_bf16_f32 v134, v12, v13\n\t" \
  "ds_read_b128 a[212:215], v221 offset:20480\n\t" \
  "v_mfma_f32_16x16x32_bf16 a[20:23], a[140:143], v[140:143], a[20:23]\n\t" \
  "v_exp_f32 v15, v15\n\t" \
  "v_mfma_f32_16x16x32_bf16 a[36:39], a[144:147], v[136:139], a[36:39]\n\t" \
  "v_cvt_pk_bf16_f32 v135, v14, v15\n\t" \
  "s_add_u32 m0, %[m], 49152\n\t" \
  "v_mfma_f32_16x16x32_bf16 a[36:39], a[148:151], v[140:143], a[36:39]\n\t" \
  "v_exp_f32 v32, v32\n\t" \
  "v_mfma_f32_16x16x32_bf16 a[52:55], a[152:155], v[136:139], a[52:55]\n\t" \
  "ds_read_b128 a[216:219], v222 offset:20480\n\t" \
  "s_add_u32 s96, %[sofk], 65536\n\t" \
  "v_mfma_f32_16x16x32_bf16 a[52:55], a[156:159], v[140:143], a[52:55]\n\t" \
  "v_exp_f32 v33, v33\n\t" \
  "v_mfma_f32_16x16x32_bf16 a[68:71], a[160:163], v[136:139], a[68:71]\n\t" \
  "v_exp_f32 v34, v34\n\t" \
  "v_mfma_f32_16x16x32_bf16 a[68:71], a[164:167], v[140:143], a[68:71]\n\t" \
  "ds_read_b128 a[220:223], v223 offset:20480\n\t" \
  "buffer_load_dwordx4 v224, %[sK], s96 offen offset:0 lds\n\t" \
  "v_mfma_f32_16x16x32_bf16 a[84:87], a[168:171], v[136:139], a[84:87]\n\t" \
  "v_exp_f32 v35, v35\n\t" \
  "v_mfma_f32_16x16x32_bf16 a[84:87], a[172:175], v[140:143], a[84:87]\n\t" \
  "v_exp_f32 v36, v36\n\t" \
  "v_mfma_f32_16x16x32_bf16 a[100:103], a[176:179], v[136:139], a[100:103]\n\t" \
  "v_exp_f32 v37, v37\n\t" \
  "v_mfma_f32_16x16x32_bf16 a[100:103], a[180:183], v[140:143], a[100:103]\n\t" \
  "ds_read_b128 a[224:227], v220 offset:24576\n\t" \
  "buffer_load_dwordx4 v225, %[sK], s96 offen offset:1024 lds\n\t" \
  "v_mfma_f32_16x16x32_bf16 a[116:119], a[184:187], v[136:139], a[116:119]\n\t" \
  "v_exp_f32 v38, v38\n\t" \
  "v_mfma_f32_16x16x32_bf16 a[116:119], a[188:191], v[140:143], a[116:119]\n\t" \
  "v_exp_f32 v39, v39\n\t" \
  "v_mfma_f32_16x16x32_bf16 v[164:167], v[216:219], v[136:139], v[164:167]\n\t" \
  "buffer_load_dwordx4 v226, %[sK], s96 offen offset:2048 lds\n\t" \
  "ds_read_b128 a[228:231], v221 offset:24576\n\t" \
  "v_mfma_f32_16x16x32_bf16 v[164:167], v[216:219], v[140:143], v[164:167]\n\t" \
  "v_cvt_pk_bf16_f32 v136, v32, v33\n\t" \
  "v_cvt_pk_bf16_f32 v137, v34, v35\n\t" \
  "v_mfma_f32_16x16x32_bf16 a[8:11], a[128:131], v[144:147], a[8:11]\n\t" \
  "v_cvt_pk_bf16_f32 v138, v36, v37\n\t" \
  "v_cvt_pk_bf16_f32 v139, v38, v39\n\t" \
  "v_mfma_f32_16x16x32_bf16 a[8:11], a[132:135], v[148:151], a[8:11]\n\t" \
  "v_exp_f32 v40, v40\n\t" \
  "v_mfma_f32_16x16x32_bf16 a[24:27], a[136:139], v[144:147], a[24:27]\n\t" \
  "buffer_load_dwordx4 v227, %[sK], s96 offen offset:3072 lds\n\t" \
  "ds_read_b128 a[232:235], v222 offset:24576\n\t" \
  "v_mfma_f32_16x16x32_bf16 a[24:27], a[140:143], v[148:151], a[24:27]\n\t" \
  "v_exp_f32 v41, v41\n\t" \
  "v_mfma_f32_16x16x32_bf16 a[40:43], a[144:147], v[144:147], a[40:43]\n\t" \
  "v_cvt_pk_bf16_f32 v140, v40, v41\n\t" \
  "ds_read_b128 a[236:239], v223 offset:24576\n\t" \
  "v_mfma_f32_16x16x32_bf16 a[40:43], a[148:151], v[148:151], a[40:43]\n\t" \
  "v_exp_f32 v42, v42\n\t" \
  "v_mfma_f32_16x16x32_bf16 a[56:59], a[152:155], v[144:147], a[56:59]\n\t" \
  "v_exp_f32 v43, v43\n\t" \
  "v_mfma_f32_16x16x32_bf16 a[56:59], a[156:159], v[148:151], a[56:59]\n\t" \
  "v_cvt_pk_bf16_f32 v141, v42, v43\n\t" \
  "ds_read_b128 a[240:243], v220 offset:28672\n\t" \
  "v_mfma_f32_16x16x32_bf16 a[72:75], a[160:163], v[144:147], a[72:75]\n\t" \
  "s_add_u32 m0, %[m], 131072\n\t" \
  "s_mov_b32 s96, 40960\n\t" \
  "buffer_load_dwordx4 v232, %[sU], s96 offen offset:2048 lds\n\t" \
  "v_mfma_f32_16x16x32_bf16 a[72:75], a[164:167], v[148:151], a[72:75]\n\t" \
  "v_exp_f32 v44, v44\n\t" \
  "v_mfma_f32_16x16x32_bf16 a[88:91], a[168:171], v[144:147], a[88:91]\n\t" \
  "s_add_u32 m0, %[m], 147456\n\t" \
  "s_add_u32 s96, %[sofk], 49152\n\t" \
  "v_mfma_f32_16x16x32_bf16 a[88:91], a[172:175], v[148:151], a[88:91]\n\t" \
  "v_exp_f32 v45, v45\n\t" \
  "v_mfma_f32_16x16x32_bf16 a[104:107], a[176:179], v[144:147], a[104:107]\n\t" \
  "v_cvt_pk_bf16_f32 v142, v44, v45\n\t" \
  "ds_read_b128 a[244:247], v221 offset:28672\n\t" \
  "v_mfma_f32_16x16x32_bf16 a[104:107], a[180:183], v[148:151], a[104:107]\n\t" \
  "v_exp_f32 v46, v46\n\t" \
  "v_mfma_f32_16x16x32_bf16 a[120:123], a[184:187], v[144:147], a[120:123]\n\t" \
  "v_exp_f32 v47, v47\n\t" \
  "v_mfma_f32_16x16x32_bf16 a[120:123], a[188:191], v[148:151], a[120:123]\n\t" \
  "v_cvt_pk_bf16_f32 v143, v46, v47\n\t" \
  "buffer_load_dwordx4 v230, %[sV], s96 offen offset:0 lds\n\t" \
  "v_mfma_f32_16x16x32_bf16 v[168:171], v[216:219], v[144:147], v[168:171]\n\t" \
  "v_exp_f32 v64, v64\n\t" \
  "v_mfma_f32_16x16x32_bf16 v[168:171], v[216:219], v[148:151], v[168:171]\n\t" \
  "v_exp_f32 v65, v65\n\t" \
  "v_mfma_f32_16x16x32_bf16 a[12:15], a[128:131], v[152:155], a[12:15]\n\t" \
  "v_cvt_pk_bf16_f32 v144, v64, v65\n\t" \
  "ds_read_b128 a[248:251], v222 offset:28672\n\t" \
  "v_mfma_f32_16x16x32_bf16 a[12:15], a[132:135], v[156:159], a[12:15]\n\t" \
  "v_exp_f32 v66, v66\n\t" \
  "v_mfma_f32_16x16x32_bf16 a[28:31], a[136:139], v[152:155], a[28:31]\n\t" \
  "v_exp_f32 v67, v67\n\t" \
  "v_mfma_f32_16x16x32_bf16 a[28:31], a[140:143], v[156:159], a[28:31]\n\t" \
  "v_cvt_pk_bf16_f32 v145, v66, v67\n\t" \
  "buffer_load_dwordx4 v231, %[sV], s96 offen offset:1024 lds\n\t" \
  "v_mfma_f32_16x16x32_bf16 a[44:47], a[144:147], v[152:155], a[44:47]\n\t" \
  "ds_read_b128 a[252:255], v223 offset:28672\n\t" \
  "buffer_load_dwordx4 v230, %[sV], s96 offen offset:2048 lds\n\t" \
  "v_mfma_f32_16x16x32_bf16 a[44:47], a[148:151], v[156:159], a[44:47]\n\t" \
  "v_exp_f32 v68, v68\n\t" \
  "v_mfma_f32_16x16x32_bf16 a[60:63], a[152:155], v[152:155], a[60:63]\n\t" \
  "v_exp_f32 v69, v69\n\t" \
  "v_mfma_f32_16x16x32_bf16 a[60:63], a[156:159], v[156:159], a[60:63]\n\t" \
  "v_cvt_pk_bf16_f32 v146, v68, v69\n\t" \
  "buffer_load_dwordx4 v231, %[sV], s96 offen offset:3072 lds\n\t" \
  "v_mfma_f32_16x16x32_bf16 a[76:79], a[160:163], v[152:155], a[76:79]\n\t" \
  "v_exp_f32 v70, v70\n\t" \
  "v_mfma_f32_16x16x32_bf16 a[76:79], a[164:167], v[156:159], a[76:79]\n\t" \
  "v_exp_f32 v71, v71\n\t" \
  "v_mfma_f32_16x16x32_bf16 a[92:95], a[168:171], v[152:155], a[92:95]\n\t" \
  "v_cvt_pk_bf16_f32 v147, v70, v71\n\t" \
  "v_mfma_f32_16x16x32_bf16 a[92:95], a[172:175], v[156:159], a[92:95]\n\t" \
  "v_exp_f32 v72, v72\n\t" \
  "v_mfma_f32_16x16x32_bf16 a[108:111], a[176:179], v[152:155], a[108:111]\n\t" \
  "v_exp_f32 v73, v73\n\t" \
  "v_mfma_f32_16x16x32_bf16 a[108:111], a[180:183], v[156:159], a[108:111]\n\t" \
  "v_cvt_pk_bf16_f32 v148, v72, v73\n\t" \
  "v_mfma_f32_16x16x32_bf16 a[124:127], a[184:187], v[152:155], a[124:127]\n\t" \
  "s_add_u32 m0, %[m], 131072\n\t" \
  "s_mov_b32 s96, 47104\n\t" \
  "buffer_load_dwordx4 v232, %[sU], s96 offen offset:3072 lds\n\t" \
  "v_mfma_f32_16x16x32_bf16 a[124:127], a[188:191], v[156:159], a[124:127]\n\t" \
  "v_exp_f32 v74, v74\n\t" \
  "v_mfma_f32_16x16x32_bf16 v[172:175], v[216:219], v[152:155], v[172:175]\n\t" \
  "v_exp_f32 v75, v75\n\t" \
  "v_mfma_f32_16x16x32_bf16 v[172:175], v[216:219], v[156:159], v[172:175]\n\t" \
  "v_cvt_pk_bf16_f32 v149, v74, v75\n\t" \
  "s_cmp_lg_u32 s99, 0\n\t" \
  "s_cbranch_scc1 .Lsf_p2\n\t" \
  ".Lsf_q2:\n\t" \
  "s_waitcnt lgkmcnt(0)\n\t" \
  "v_mfma_f32_16x16x32_bf16 v[0:3], a[192:195], v[16:19], v[176:179]\n\t" \
  "s_mul_i32 s98, %[sofk], 5\n\t" \
  "s_xor_b32 s98, s98, 8192\n\t" \
  "s_lshr_b32 s98, s98, 1\n\t" \
  "buffer_load_dword %[pfd], v204, %[sD], s98 offen\n\t" \
  "v_mfma_f32_16x16x32_bf16 v[0:3], a[196:199], v[20:23], v[0:3]\n\t" \
  "v_exp_f32 v76, v76\n\t" \
  "v_mfma_f32_16x16x32_bf16 v[0:3], a[200:203], v[24:27], v[0:3]\n\t" \
  "ds_read_b64_tr_b16 a[128:129], v228 offset:0\n\t" \
  "ds_read_b64_tr_b16 a[130:131], v228 offset:4096\n\t" \
  "v_mfma_f32_16x16x32_bf16 v[0:3], a[204:207], v[28:31], v[0:3]\n\t" \
  "v_exp_f32 v77, v77\n\t" \
  "v_mfma_f32_16x16x32_bf16 v[4:7], a[220:223], v[28:31], v[176:179]\n\t" \
  "v_cvt_pk_bf16_f32 v150, v76, v77\n\t" \
  "ds_read_b64_tr_b16 a[132:133], v228 offset:8192\n\t" \
  "v_mfma_f32_16x16x32_bf16 v[4:7], a[216:219], v[24:27], v[4:7]\n\t" \
  "v_exp_f32 v78, v78\n\t" \
  "v_mfma_f32_16x16x32_bf16 v[4:7], a[212:215], v[20:23], v[4:7]\n\t" \
  "v_exp_f32 v79, v79\n\t" \
  "v_mfma_f32_16x16x32_bf16 v[4:7], a[208:211], v[16:19], v[4:7]\n\t" \
  "v_cvt_pk_bf16_f32 v151, v78, v79\n\t" \
  "v_max3_f32 v192, v0, v1, v2\n\t" \
  "v_mfma_f32_16x16x32_bf16 v[8:11], a[224:227], v[16:19], v[176:179]\n\t" \
  "ds_read_b64_tr_b16 a[134:135], v228 offset:12288\n\t" \
  "ds_read_b64_tr_b16 a[136:137], v229 offset:0\n\t" \
  "v_mfma_f32_16x16x32_bf16 v[8:11], a[228:231], v[20:23], v[8:11]\n\t" \
  "v_exp_f32 v96, v96\n\t" \
  "v_mfma_f32_16x16x32_bf16 v[8:11], a[232:235], v[24:27], v[8:11]\n\t" \
  "v_max3_f32 v203, v3, v4, v5\n\t" \
  "v_max3_f32 v192, v192, v6, v7\n\t" \
  "v_mfma_f32_16x16x32_bf16 v[8:11], a[236:239], v[28:31], v[8:11]\n\t" \
  "ds_read_b64_tr_b16 a[138:139], v229 offset:4096\n\t" \
  "ds_read_b64_tr_b16 a[140:141], v229 offset:8192\n\t" \
  "v_mfma_f32_16x16x32_bf16 v[12:15], a[252:255], v[28:31], v[176:179]\n\t" \
  "v_exp_f32 v97, v97\n\t" \
  "v_mfma_f32_16x16x32_bf16 v[12:15], a[248:251], v[24:27], v[12:15]\n\t" \
  "v_cvt_pk_bf16_f32 v152, v96, v97\n\t" \
  "ds_read_b64_tr_b16 a[142:143], v229 offset:12288\n\t" \
  "v_mfma_f32_16x16x32_bf16 v[12:15], a[244:247], v[20:23], v[12:15]\n\t" \
  "v_max3_f32 v203, v203, v8, v9\n\t" \
  "v_max3_f32 v192, v192, v10, v11\n\t" \
  "v_mfma_f32_16x16x32_bf16 v[12:15], a[240:243], v[16:19], v[12:15]\n\t" \
  "s_mul_i32 s98, %[sofk], 5\n\t" \
  "s_xor_b32 s98, s98, 12288\n\t" \
  "s_lshr_b32 s98, s98, 1\n\t" \
  "buffer_load_dword %[pfd], v204, %[sD], s98 offen\n\t" \
  "v_mfma_f32_16x16x32_bf16 v[32:35], a[192:195], v[48:51], v[180:183]\n\t" \
  "ds_read_b64_tr_b16 a[144:145], v228 offset:256\n\t" \
  "ds_read_b64_tr_b16 a[146:147], v228 offset:4352\n\t" \
  "v_mfma_f32_16x16x32_bf16 v[32:35], a[196:199], v[52:55], v[32:35]\n\t" \
  "v_exp_f32 v98, v98\n\t" \
  "v_mfma_f32_16x16x32_bf16 v[32:35], a[200:203], v[56:59], v[32:35]\n\t" \
  "v_max3_f32 v203, v203, v12, v13\n\t" \
  "v_max3_f32 v192, v192, v14, v15\n\t" \
  "v_mfma_f32_16x16x32_bf16 v[32:35], a[204:207], v[60:63], v[32:35]\n\t" \
  "v_max_f32 v192, v192, v203\n\t" \
  "ds_read_b64_tr_b16 a[148:149], v228 offset:8448\n\t" \
  "v_mfma_f32_16x16x32_bf16 v[36:39], a[220:223], v[60:63], v[180:183]\n\t" \
  "v_cmp_lt_f32 vcc, 0x42800000, v192\n\t" \
  "ds_read_b64_tr_b16 a[150:151], v228 offset:12544\n\t" \
  "v_mfma_f32_16x16x32_bf16 v[36:39], a[216:219], v[56:59], v[36:39]\n\t" \
  "s_cbranch_vccnz .Lsf_c30\n\t" \
  ".Lsf_r30:\n\t" \
  "ds_read_b64_tr_b16 a[152:153], v229 offset:256\n\t" \
  "v_mfma_f32_16x16x32_bf16 v[36:39], a[212:215], v[52:55], v[36:39]\n\t" \
  "v_max3_f32 v192, v32, v33, v34\n\t" \
  "ds_read_b64_tr_b16 a[154:155], v229 offset:4352\n\t" \
  "v_mfma_f32_16x16x32_bf16 v[36:39], a[208:211], v[48:51], v[36:39]\n\t" \
  "v_exp_f32 v99, v99\n\t" \
  "v_mfma_f32_16x16x32_bf16 v[40:43], a[224:227], v[48:51], v[180:183]\n\t" \
  "v_cvt_pk_bf16_f32 v153, v98, v99\n\t" \
  "ds_read_b64_tr_b16 a[156:157], v229 offset:8448\n\t" \
  "v_mfma_f32_16x16x32_bf16 v[40:43], a[228:231], v[52:55], v[40:43]\n\t" \
  "v_exp_f32 v100, v100\n\t" \
  "v_mfma_f32_16x16x32_bf16 v[40:43], a[232:235], v[56:59], v[40:43]\n\t" \
  "v_exp_f32 v101, v101\n\t" \
  "v_mfma_f32_16x16x32_bf16 v[40:43], a[236:239], v[60:63], v[40:43]\n\t" \
  "v_cvt_pk_bf16_f32 v154, v100, v101\n\t" \
  "v_max3_f32 v203, v35, v36, v37\n\t" \
  "v_mfma_f32_16x16x32_bf16 v[44:47], a[252:255], v[60:63], v[180:183]\n\t" \
  "v_max3_f32 v192, v192, v38, v39\n\t" \
  "ds_read_b64_tr_b16 a[158:159], v229 offset:12544\n\t" \
  "v_mfma_f32_16x16x32_bf16 v[44:47], a[248:251], v[56:59], v[44:47]\n\t" \
  "v_exp_f32 v102, v102\n\t" \
  "v_mfma_f32_16x16x32_bf16 v[44:47], a[244:247], v[52:55], v[44:47]\n\t" \
  "ds_read_b64_tr_b16 a[160:161], v228 offset:512\n\t" \
  "v_max3_f32 v203, v203, v40, v41\n\t" \
  "v_mfma_f32_16x16x32_bf16 v[44:47], a[240:243], v[48:51], v[44:47]\n\t" \
  "v_max3_f32 v192, v192, v42, v43\n\t" \
  "ds_read_b64_tr_b16 a[162:163], v228 offset:4608\n\t" \
  "v_mfma_f32_16x16x32_bf16 v[64:67], a[192:195], v[80:83], v[184:187]\n\t" \
  "v_exp_f32 v103, v103\n\t" \
  "v_mfma_f32_16x16x32_bf16 v[64:67], a[196:199], v[84:87], v[64:67]\n\t" \
  "v_cvt_pk_bf16_f32 v155, v102, v103\n\t" \
  "ds_read_b64_tr_b16 a[164:165], v228 offset:8704\n\t" \
  "v_mfma_f32_16x16x32_bf16 v[64:67], a[200:203], v[88:91], v[64:67]\n\t" \
  "v_max3_f32 v203, v203, v44, v45\n\t" \
  "v_max3_f32 v192, v192, v46, v47\n\t" \
  "v_mfma_f32_16x16x32_bf16 v[64:67], a[204:207], v[92:95], v[64:67]\n\t" \
  "v_max_f32 v192, v192, v203\n\t" \
  "ds_read_b64_tr_b16 a[166:167], v228 offset:12800\n\t" \
  "v_mfma_f32_16x16x32_bf16 v[68:71], a[220:223], v[92:95], v[184:187]\n\t" \
  "v_cmp_lt_f32 vcc, 0x42800000, v192\n\t" \
  "ds_read_b64_tr_b16 a[168:169], v229 offset:512\n\t" \
  "v_mfma_f32_16x16x32_bf16 v[68:71], a[216:219], v[88:91], v[68:71]\n\t" \
  "s_cbranch_vccnz .Lsf_c31\n\t" \
  ".Lsf_r31:\n\t" \
  "ds_read_b64_tr_b16 a[170:171], v229 offset:4608\n\t" \
  "v_mfma_f32_16x16x32_bf16 v[68:71], a[212:215], v[84:87], v[68:71]\n\t" \
  "v_max3_f32 v192, v64, v65, v66\n\t" \
  "ds_read_b64_tr_b16 a[172:173], v229 offset:8704\n\t" \
  "v_mfma_f32_16x16x32_bf16 v[68:71], a[208:211], v[80:83], v[68:71]\n\t" \
  "v_exp_f32 v104, v104\n\t" \
  "v_mfma_f32_16x16x32_bf16 v[72:75], a[224:227], v[80:83], v[184:187]\n\t" \
  "v_exp_f32 v105, v105\n\t" \
  "v_mfma_f32_16x16x32_bf16 v[72:75], a[228:231], v[84:87], v[72:75]\n\t" \
  "v_cvt_pk_bf16_f32 v156, v104, v105\n\t" \
  "ds_read_b64_tr_b16 a[174:175], v229 offset:12800\n\t" \
  "v_mfma_f32_16x16x32_bf16 v[72:75], a[232:235], v[88:91], v[72:75]\n\t" \
  "v_exp_f32 v106, v106\n\t" \
  "v_mfma_f32_16x16x32_bf16 v[72:75], a[236:239], v[92:95], v[72:75]\n\t" \
  "v_max3_f32 v203, v67, v68, v69\n\t" \
  "v_max3_f32 v192, v192, v70, v71\n\t" \
  "v_mfma_f32_16x16x32_bf16 v[76:79], a[252:255], v[92:95], v[184:187]\n\t" \
  "ds_read_b64_tr_b16 a[176:177], v228 offset:768\n\t" \
  "ds_read_b64_tr_b16 a[178:179], v228 offset:4864\n\t" \
  "v_mfma_f32_16x16x32_bf16 v[76:79], a[248:251], v[88:91], v[76:79]\n\t" \
  "v_exp_f32 v107, v107\n\t" \
  "v_mfma_f32_16x16x32_bf16 v[76:79], a[244:247], v[84:87], v[76:79]\n\t" \
  "v_cvt_pk_bf16_f32 v157, v106, v107\n\t" \
  "v_max3_f32 v203, v203, v72, v73\n\t" \
  "v_mfma_f32_16x16x32_bf16 v[76:79], a[240:243], v[80:83], v[76:79]\n\t" \
  "v_max3_f32 v192, v192, v74, v75\n\t" \
  "ds_read_b64_tr_b16 a[180:181], v228 offset:8960\n\t" \
  "v_mfma_f32_16x16x32_bf16 v[96:99], a[192:195], v[112:115], v[188:191]\n\t" \
  "v_exp_f32 v108, v108\n\t" \
  "v_mfma_f32_16x16x32_bf16 v[96:99], a[196:199], v[116:119], v[96:99]\n\t" \
  "v_exp_f32 v109, v109\n\t" \
  "v_mfma_f32_16x16x32_bf16 v[96:99], a[200:203], v[120:123], v[96:99]\n\t" \
  "v_cvt_pk_bf16_f32 v158, v108, v109\n\t" \
  "ds_read_b64_tr_b16 a[182:183], v228 offset:13056\n\t" \
  "v_mfma_f32_16x16x32_bf16 v[96:99], a[204:207], v[124:127], v[96:99]\n\t" \
  "v_max3_f32 v203, v203, v76, v77\n\t" \
  "v_max3_f32 v192, v192, v78, v79\n\t" \
  "v_mfma_f32_16x16x32_bf16 v[100:103], a[220:223], v[124:127], v[188:191]\n\t" \
  "v_max_f32 v192, v192, v203\n\t" \
  "ds_read_b64_tr_b16 a[184:185], v229 offset:768\n\t" \
  "v_mfma_f32_16x16x32_bf16 v[100:103], a[216:219], v[120:123], v[100:103]\n\t" \
  "v_cmp_lt_f32 vcc, 0x42800000, v192\n\t" \
  "ds_read_b64_tr_b16 a[186:187], v229 offset:4864\n\t" \
  "v_mfma_f32_16x16x32_bf16 v[100:103], a[212:215], v[116:119], v[100:103]\n\t" \
  "s_cbranch_vccnz .Lsf_c32\n\t" \
  ".Lsf_r32:\n\t" \
  "v_max3_f32 v192, v96, v97, v98\n\t" \
  "v_mfma_f32_16x16x32_bf16 v[100:103], a[208:211], v[112:115], v[100:103]\n\t" \
  "ds_read_b64_tr_b16 a[188:189], v229 offset:8960\n\t" \
  "ds_read_b64_tr_b16 a[190:191], v229 offset:13056\n\t" \
  "v_mfma_f32_16x16x32_bf16 v[104:107], a[224:227], v[112:115], v[188:191]\n\t" \
  "v_exp_f32 v110, v110\n\t" \
  "v_mfma_f32_16x16x32_bf16 v[104:107], a[228:231], v[116:119], v[104:107]\n\t" \
  "v_exp_f32 v111, v111\n\t" \
  "v_mfma_f32_16x16x32_bf16 v[104:107], a[232:235], v[120:123], v[104:107]\n\t" \
  "v_cvt_pk_bf16_f32 v159, v110, v111\n\t" \
  "v_max3_f32 v203, v99, v100, v101\n\t" \
  "v_mfma_f32_16x16x32_bf16 v[104:107], a[236:239], v[124:127], v[104:107]\n\t" \
  "v_max3_f32 v192, v192, v102, v103\n\t" \
  "v_mfma_f32_16x16x32_bf16 v[108:111], a[252:255], v[124:127], v[188:191]\n\t" \
  "v_exp_f32 v0, v0\n\t" \
  "v_mfma_f32_16x16x32_bf16 v[108:111], a[248:251], v[120:123], v[108:111]\n\t" \
  "v_exp_f32 v1, v1\n\t" \
  "v_mfma_f32_16x16x32_bf16 v[108:111], a[244:247], v[116:119], v[108:111]\n\t" \
  "v_exp_f32 v2, v2\n\t" \
  "v_mfma_f32_16x16x32_bf16 v[108:111], a[240:243], v[112:115], v[108:111]\n\t" \
  "v_max3_f32 v203, v203, v104, v105\n\t" \
  "v_max3_f32 v192, v192, v106, v107\n\t" \
  "s_waitcnt vmcnt(3) lgkmcnt(0)\n\t" \
  "s_barrier\n\t" \
  "v_mfma_f32_16x16x32_bf16 a[0:3], a[128:131], v[128:131], a[0:3]\n\t" \
  "v_exp_f32 v3, v3\n\t" \
  "v_mfma_f32_16x16x32_bf16 a[0:3], a[132:135], v[132:135], a[0:3]\n\t" \
  "v_exp_f32 v4, v4\n\t" \
  "v_mfma_f32_16x16x32_bf16 a[16:19], a[136:139], v[128:131], a[16:19]\n\t" \
  "ds_read_b128 a[192:195], v220 offset:0\n\t" \
  "v_max3_f32 v203, v203, v108, v109\n\t" \
  "v_mfma_f32_16x16x32_bf16 a[16:19], a[140:143], v[132:135], a[16:19]\n\t" \
  "v_max3_f32 v192, v192, v110, v111\n\t" \
  "v_max_f32 v192, v192, v203\n\t" \
  "v_mfma_f32_16x16x32_bf16 a[32:35], a[144:147], v[128:131], a[32:35]\n\t" \
  "v_cmp_lt_f32 vcc, 0x42800000, v192\n\t" \
  "ds_read_b128 a[196:199], v221 offset:0\n\t" \
  "v_mfma_f32_16x16x32_bf16 a[32:35], a[148:151], v[132:135], a[32:35]\n\t" \
  "s_cbranch_vccnz .Lsf_c33\n\t" \
  ".Lsf_r33:\n\t" \
  "ds_read_b128 a[200:203], v222 offset:0\n\t" \
  "v_mfma_f32_16x16x32_bf16 a[48:51], a[152:155], v[128:131], a[48:51]\n\t" \
  "v_exp_f32 v5, v5\n\t" \
  "v_mfma_f32_16x16x32_bf16 a[48:51], a[156:159], v[132:135], a[48:51]\n\t" \
  "v_exp_f32 v6, v6\n\t" \
  "v_mfma_f32_16x16x32_bf16 a[64:67], a[160:163], v[128:131], a[64:67]\n\t" \
  "v_exp_f32 v7, v7\n\t" \
  "v_mfma_f32_16x16x32_bf16 a[64:67], a[164:167], v[132:135], a[64:67]\n\t" \
  "v_exp_f32 v8, v8\n\t" \
  "v_mfma_f32_16x16x32_bf16 a[80:83], a[168:171], v[128:131], a[80:83]\n\t" \
  "v_exp_f32 v9, v9\n\t" \
  "v_mfma_f32_16x16x32_bf16 a[80:83], a[172:175], v[132:135], a[80:83]\n\t" \
  "v_exp_f32 v10, v10\n\t" \
  "v_mfma_f32_16x16x32_bf16 a[96:99], a[176:179], v[128:131], a[96:99]\n\t" \
  "v_exp_f32 v11, v11\n\t" \
  "v_mfma_f32_16x16x32_bf16 a[96:99], a[180:183], v[132:135], a[96:99]\n\t" \
  "v_exp_f32 v12, v12\n\t" \
  "v_mfma_f32_16x16x32_bf16 a[112:115], a[184:187], v[128:131], a[112:115]\n\t" \
  "ds_read_b128 a[204:207], v223 offset:0\n\t" \
  "ds_read_b128 a[208:211], v220 offset:4096\n\t" \
  "v_mfma_f32_16x16x32_bf16 a[112:115], a[188:191], v[132:135], a[112:115]\n\t" \
  "v_exp_f32 v13, v13\n\t" \
  "v_mfma_f32_16x16x32_bf16 v[160:163], v[216:219], v[128:131], v[160:163]\n\t" \
  "v_exp_f32 v14, v14\n\t" \
  "v_mfma_f32_16x16x32_bf16 v[160:163], v[216:219], v[132:135], v[160:163]\n\t" \
  "v_cvt_pk_bf16_f32 v128, v0, v1\n\t" \
  "v_cvt_pk_bf16_f32 v129, v2, v3\n\t" \
  "v_mfma_f32_16x16x32_bf16 a[4:7], a[128:131], v[136:139], a[4:7]\n\t" \
  "v_cvt_pk_bf16_f32 v130, v4, v5\n\t" \
  "v_cvt_pk_bf16_f32 v131, v6, v7\n\t" \
  "v_mfma_f32_16x16x32_bf16 a[4:7], a[132:135], v[140:143], a[4:7]\n\t" \
  "v_cvt_pk_bf16_f32 v132, v8, v9\n\t" \
  "v_cvt_pk_bf16_f32 v133, v10, v11\n\t" \
  "v_mfma_f32_16x16x32_bf16 a[20:23], a[136:139], v[136:139], a[20:23]\n\t" \
  "v_cvt_pk_bf16_f32 v134, v12, v13\n\t" \
  "ds_read_b128 a[212:215], v221 offset:4096\n\t" \
  "v_mfma_f32_16x16x32_bf16 a[20:23], a[140:143], v[140:143], a[20:23]\n\t" \
  "v_exp_f32 v15, v15\n\t" \
  "v_mfma_f32_16x16x32_bf16 a[36:39], a[144:147], v[136:139], a[36:39]\n\t" \
  "v_cvt_pk_bf16_f32 v135, v14, v15\n\t" \
  "s_add_u32 m0, %[m], 65536\n\t" \
  "v_mfma_f32_16x16x32_bf16 a[36:39], a[148:151], v[140:143], a[36:39]\n\t" \
  "v_exp_f32 v32, v32\n\t" \
  "v_mfma_f32_16x16x32_bf16 a[52:55], a[152:155], v[136:139], a[52:55]\n\t" \
  "ds_read_b128 a[216:219], v222 offset:4096\n\t" \
  "s_add_u32 s96, %[sofk], 81920\n\t" \
  "v_mfma_f32_16x16x32_bf16 a[52:55], a[156:159], v[140:143], a[52:55]\n\t" \
  "v_exp_f32 v33, v33\n\t" \
  "v_mfma_f32_16x16x32_bf16 a[68:71], a[160:163], v[136:139], a[68:71]\n\t" \
  "v_exp_f32 v34, v34\n\t" \
  "v_mfma_f32_16x16x32_bf16 a[68:71], a[164:167], v[140:143], a[68:71]\n\t" \
  "ds_read_b128 a[220:223], v223 offset:4096\n\t" \
  "buffer_load_dwordx4 v224, %[sK], s96 offen offset:0 lds\n\t" \
  "v_mfma_f32_16x16x32_bf16 a[84:87], a[168:171], v[136:139], a[84:87]\n\t" \
  "v_exp_f32 v35, v35\n\t" \
  "v_mfma_f32_16x16x32_bf16 a[84:87], a[172:175], v[140:143], a[84:87]\n\t" \
  "v_exp_f32 v36, v36\n\t" \
  "v_mfma_f32_16x16x32_bf16 a[100:103], a[176:179], v[136:139], a[100:103]\n\t" \
  "v_exp_f32 v37, v37\n\t" \
  "v_mfma_f32_16x16x32_bf16 a[100:103], a[180:183], v[140:143], a[100:103]\n\t" \
  "ds_read_b128 a[224:227], v220 offset:8192\n\t" \
  "buffer_load_dwordx4 v225, %[sK], s96 offen offset:1024 lds\n\t" \
  "v_mfma_f32_16x16x32_bf16 a[116:119], a[184:187], v[136:139], a[116:119]\n\t" \
  "v_exp_f32 v38, v38\n\t" \
  "v_mfma_f32_16x16x32_bf16 a[116:119], a[188:191], v[140:143], a[116:119]\n\t" \
  "v_exp_f32 v39, v39\n\t" \
  "v_mfma_f32_16x16x32_bf16 v[164:167], v[216:219], v[136:139], v[164:167]\n\t" \
  "buffer_load_dwordx4 v226, %[sK], s96 offen offset:2048 lds\n\t" \
  "ds_read_b128 a[228:231], v221 offset:8192\n\t" \
  "v_mfma_f32_16x16x32_bf16 v[164:167], v[216:219], v[140:143], v[164:167]\n\t" \
  "v_cvt_pk_bf16_f32 v136, v32, v33\n\t" \
  "v_cvt_pk_bf16_f32 v137, v34, v35\n\t" \
  "v_mfma_f32_16x16x32_bf16 a[8:11], a[128:131], v[144:147], a[8:11]\n\t" \
  "v_cvt_pk_bf16_f32 v138, v36, v37\n\t" \
  "v_cvt_pk_bf16_f32 v139, v38, v39\n\t" \
  "v_mfma_f32_16x16x32_bf16 a[8:11], a[132:135], v[148:151], a[8:11]\n\t" \
  "v_exp_f32 v40, v40\n\t" \
  "v_mfma_f32_16x16x32_bf16 a[24:27], a[136:139], v[144:147], a[24:27]\n\t" \
  "buffer_load_dwordx4 v227, %[sK], s96 offen offset:3072 lds\n\t" \
  "ds_read_b128 a[232:235], v222 offset:8192\n\t" \
  "v_mfma_f32_16x16x32_bf16 a[24:27], a[140:143], v[148:151], a[24:27]\n\t" \
  "v_exp_f32 v41, v41\n\t" \
  "v_mfma_f32_16x16x32_bf16 a[40:43], a[144:147], v[144:147], a[40:43]\n\t" \
  "v_cvt_pk_bf16_f32 v140, v40, v41\n\t" \
  "ds_read_b128 a[236:239], v223 offset:8192\n\t" \
  "v_mfma_f32_16x16x32_bf16 a[40:43], a[148:151], v[148:151], a[40:43]\n\t" \
  "v_exp_f32 v42, v42\n\t" \
  "v_mfma_f32_16x16x32_bf16 a[56:59], a[152:155], v[144:147], a[56:59]\n\t" \
  "v_exp_f32 v43, v43\n\t" \
  "v_mfma_f32_16x16x32_bf16 a[56:59], a[156:159], v[148:151], a[56:59]\n\t" \
  "v_cvt_pk_bf16_f32 v141, v42, v43\n\t" \
  "ds_read_b128 a[240:243], v220 offset:12288\n\t" \
  "v_mfma_f32_16x16x32_bf16 a[72:75], a[160:163], v[144:147], a[72:75]\n\t" \
  "s_add_u32 m0, %[m], 32768\n\t" \
  "s_mov_b32 s96, 0\n\t" \
  "buffer_load_dwordx4 v232, %[sG], s96 offen offset:0 lds\n\t" \
  "v_mfma_f32_16x16x32_bf16 a[72:75], a[164:167], v[148:151], a[72:75]\n\t" \
  "v_exp_f32 v44, v44\n\t" \
  "v_mfma_f32_16x16x32_bf16 a[88:91], a[168:171], v[144:147], a[88:91]\n\t" \
  "s_add_u32 m0, %[m], 81920\n\t" \
  "s_add_u32 s96, %[sofk], 65536\n\t" \
  "v_mfma_f32_16x16x32_bf16 a[88:91], a[172:175], v[148:151], a[88:91]\n\t" \
  "v_exp_f32 v45, v45\n\t" \
  "v_mfma_f32_16x16x32_bf16 a[104:107], a[176:179], v[144:147], a[104:107]\n\t" \
  "v_cvt_pk_bf16_f32 v142, v44, v45\n\t" \
  "ds_read_b128 a[244:247], v221 offset:12288\n\t" \
  "v_mfma_f32_16x16x32_bf16 a[104:107], a[180:183], v[148:151], a[104:107]\n\t" \
  "v_exp_f32 v46, v46\n\t" \
  "v_mfma_f32_16x16x32_bf16 a[120:123], a[184:187], v[144:147], a[120:123]\n\t" \
  "v_exp_f32 v47, v47\n\t" \
  "v_mfma_f32_16x16x32_bf16 a[120:123], a[188:191], v[148:151], a[120:123]\n\t" \
  "v_cvt_pk_bf16_f32 v143, v46, v47\n\t" \
  "buffer_load_dwordx4 v230, %[sV], s96 offen offset:0 lds\n\t" \
  "v_mfma_f32_16x16x32_bf16 v[168:171], v[216:219], v[144:147], v[168:171]\n\t" \
  "v_exp_f32 v64, v64\n\t" \
  "v_mfma_f32_16x16x32_bf16 v[168:171], v[216:219], v[148:151], v[168:171]\n\t" \
  "v_exp_f32 v65, v65\n\t" \
  "v_mfma_f32_16x16x32_bf16 a[12:15], a[128:131], v[152:155], a[12:15]\n\t" \
  "v_cvt_pk_bf16_f32 v144, v64, v65\n\t" \
  "ds_read_b128 a[248:251], v222 offset:12288\n\t" \
  "v_mfma_f32_16x16x32_bf16 a[12:15], a[132:135], v[156:159], a[12:15]\n\t" \
  "v_exp_f32 v66, v66\n\t" \
  "v_mfma_f32_16x16x32_bf16 a[28:31], a[136:139], v[152:155], a[28:31]\n\t" \
  "v_exp_f32 v67, v67\n\t" \
  "v_mfma_f32_16x16x32_bf16 a[28:31], a[140:143], v[156:159], a[28:31]\n\t" \
  "v_cvt_pk_bf16_f32 v145, v66, v67\n\t" \
  "buffer_load_dwordx4 v231, %[sV], s96 offen offset:1024 lds\n\t" \
  "v_mfma_f32_16x16x32_bf16 a[44:47], a[144:147], v[152:155], a[44:47]\n\t" \
  "ds_read_b128 a[252:255], v223 offset:12288\n\t" \
  "buffer_load_dwordx4 v230, %[sV], s96 offen offset:2048 lds\n\t" \
  "v_mfma_f32_16x16x32_bf16 a[44:47], a[148:151], v[156:159], a[44:47]\n\t" \
  "v_exp_f32 v68, v68\n\t" \
  "v_mfma_f32_16x16x32_bf16 a[60:63], a[152:155], v[152:155], a[60:63]\n\t" \
  "v_exp_f32 v69, v69\n\t" \
  "v_mfma_f32_16x16x32_bf16 a[60:63], a[156:159], v[156:159], a[60:63]\n\t" \
  "v_cvt_pk_bf16_f32 v146, v68, v69\n\t" \
  "buffer_load_dwordx4 v231, %[sV], s96 offen offset:3072 lds\n\t" \
  "v_mfma_f32_16x16x32_bf16 a[76:79], a[160:163], v[152:155], a[76:79]\n\t" \
  "v_exp_f32 v70, v70\n\t" \
  "v_mfma_f32_16x16x32_bf16 a[76:79], a[164:167], v[156:159], a[76:79]\n\t" \
  "v_exp_f32 v71, v71\n\t" \
  "v_mfma_f32_16x16x32_bf16 a[92:95], a[168:171], v[152:155], a[92:95]\n\t" \
  "v_cvt_pk_bf16_f32 v147, v70, v71\n\t" \
  "v_mfma_f32_16x16x32_bf16 a[92:95], a[172:175], v[156:159], a[92:95]\n\t" \
  "v_exp_f32 v72, v72\n\t" \
  "v_mfma_f32_16x16x32_bf16 a[108:111], a[176:179], v[152:155], a[108:111]\n\t" \
  "v_exp_f32 v73, v73\n\t" \
  "v_mfma_f32_16x16x32_bf16 a[108:111], a[180:183], v[156:159], a[108:111]\n\t" \
  "v_cvt_pk_bf16_f32 v148, v72, v73\n\t" \
  "v_mfma_f32_16x16x32_bf16 a[124:127], a[184:187], v[152:155], a[124:127]\n\t" \
  "s_add_u32 m0, %[m], 32768\n\t" \
  "s_mov_b32 s96, 6144\n\t" \
  "buffer_load_dwordx4 v232, %[sG], s96 offen offset:1024 lds\n\t" \
  "v_mfma_f32_16x16x32_bf16 a[124:127], a[188:191], v[156:159], a[124:127]\n\t" \
  "v_exp_f32 v74, v74\n\t" \
  "v_mfma_f32_16x16x32_bf16 v[172:175], v[216:219], v[152:155], v[172:175]\n\t" \
  "v_exp_f32 v75, v75\n\t" \
  "v_mfma_f32_16x16x32_bf16 v[172:175], v[216:219], v[156:159], v[172:175]\n\t" \
  "v_cvt_pk_bf16_f32 v149, v74, v75\n\t" \
  "s_cmp_lg_u32 s99, 0\n\t" \
  "s_cbranch_scc1 .Lsf_p3\n\t" \
  ".Lsf_q3:\n\t" \
  "s_waitcnt lgkmcnt(0)\n\t" \
  "v_mfma_f32_16x16x32_bf16 v[0:3], a[192:195], v[16:19], v[176:179]\n\t" \
  "s_add_u32 s98, %[pf4], 28672\n\t" \
  "buffer_load_dword %[pfd], v245, %[sG], s98 offen\n\t" \
  "v_mfma_f32_16x16x32_bf16 v[0:3], a[196:199], v[20:23], v[0:3]\n\t" \
  "v_exp_f32 v76, v76\n\t" \
  "v_mfma_f32_16x16x32_bf16 v[0:3], a[200:203], v[24:27], v[0:3]\n\t" \
  "ds_read_b64_tr_b16 a[128:129], v237 offset:0\n\t" \
  "ds_read_b64_tr_b16 a[130:131], v237 offset:4096\n\t" \
  "v_mfma_f32_16x16x32_bf16 v[0:3], a[204:207], v[28:31], v[0:3]\n\t" \
  "v_exp_f32 v77, v77\n\t" \
  "v_mfma_f32_16x16x32_bf16 v[4:7], a[220:223], v[28:31], v[176:179]\n\t" \
  "v_cvt_pk_bf16_f32 v150, v76, v77\n\t" \
  "ds_read_b64_tr_b16 a[132:133], v237 offset:8192\n\t" \
  "v_mfma_f32_16x16x32_bf16 v[4:7], a[216:219], v[24:27], v[4:7]\n\t" \
  "v_exp_f32 v78, v78\n\t" \
  "v_mfma_f32_16x16x32_bf16 v[4:7], a[212:215], v[20:23], v[4:7]\n\t" \
  "v_exp_f32 v79, v79\n\t" \
  "v_mfma_f32_16x16x32_bf16 v[4:7], a[208:211], v[16:19], v[4:7]\n\t" \
  "v_cvt_pk_bf16_f32 v151, v78, v79\n\t" \
  "v_max3_f32 v192, v0, v1, v2\n\t" \
  "v_mfma_f32_16x16x32_bf16 v[8:11], a[224:227], v[16:19], v[176:179]\n\t" \
  "ds_read_b64_tr_b16 a[134:135], v237 offset:12288\n\t" \
  "ds_read_b64_tr_b16 a[136:137], v238 offset:0\n\t" \
  "v_mfma_f32_16x16x32_bf16 v[8:11], a[228:231], v[20:23], v[8:11]\n\t" \
  "v_exp_f32 v96, v96\n\t" \
  "v_mfma_f32_16x16x32_bf16 v[8:11], a[232:235], v[24:27], v[8:11]\n\t" \
  "v_max3_f32 v203, v3, v4, v5\n\t" \
  "v_max3_f32 v192, v192, v6, v7\n\t" \
  "v_mfma_f32_16x16x32_bf16 v[8:11], a[236:239], v[28:31], v[8:11]\n\t" \
  "ds_read_b64_tr_b16 a[138:139], v238 offset:4096\n\t" \
  "ds_read_b64_tr_b16 a[140:141], v238 offset:8192\n\t" \
  "v_mfma_f32_16x16x32_bf16 v[12:15], a[252:255], v[28:31], v[176:179]\n\t" \
  "v_exp_f32 v97, v97\n\t" \
  "v_mfma_f32_16x16x32_bf16 v[12:15], a[248:251], v[24:27], v[12:15]\n\t" \
  "v_cvt_pk_bf16_f32 v152, v96, v97\n\t" \
  "ds_read_b64_tr_b16 a[142:143], v238 offset:12288\n\t" \
  "v_mfma_f32_16x16x32_bf16 v[12:15], a[244:247], v[20:23], v[12:15]\n\t" \
  "v_max3_f32 v203, v203, v8, v9\n\t" \
  "v_max3_f32 v192, v192, v10, v11\n\t" \
  "v_mfma_f32_16x16x32_bf16 v[12:15], a[240:243], v[16:19], v[12:15]\n\t" \
  "s_add_u32 s98, %[pf2], 0\n\t" \
  "buffer_load_dword %[pfd], v202, %[sM], s98 offen\n\t" \
  "v_mfma_f32_16x16x32_bf16 v[32:35], a[192:195], v[48:51], v[180:183]\n\t" \
  "ds_read_b64_tr_b16 a[144:145], v237 offset:256\n\t" \
  "ds_read_b64_tr_b16 a[146:147], v237 offset:4352\n\t" \
  "v_mfma_f32_16x16x32_bf16 v[32:35], a[196:199], v[52:55], v[32:35]\n\t" \
  "v_exp_f32 v98, v98\n\t" \
  "v_mfma_f32_16x16x32_bf16 v[32:35], a[200:203], v[56:59], v[32:35]\n\t" \
  "v_max3_f32 v203, v203, v12, v13\n\t" \
  "v_max3_f32 v192, v192, v14, v15\n\t" \
  "v_mfma_f32_16x16x32_bf16 v[32:35], a[204:207], v[60:63], v[32:35]\n\t" \
  "v_max_f32 v192, v192, v203\n\t" \
  "ds_read_b64_tr_b16 a[148:149], v237 offset:8448\n\t" \
  "v_mfma_f32_16x16x32_bf16 v[36:39], a[220:223], v[60:63], v[180:183]\n\t" \
  "v_cmp_lt_f32 vcc, 0x42800000, v192\n\t" \
  "ds_read_b64_tr_b16 a[150:151], v237 offset:12544\n\t" \
  "v_mfma_f32_16x16x32_bf16 v[36:39], a[216:219], v[56:59], v[36:39]\n\t" \
  "s_cbranch_vccnz .Lsf_c40\n\t" \
  ".Lsf_r40:\n\t" \
  "ds_read_b64_tr_b16 a[152:153], v238 offset:256\n\t" \
  "v_mfma_f32_16x16x32_bf16 v[36:39], a[212:215], v[52:55], v[36:39]\n\t" \
  "v_max3_f32 v192, v32, v33, v34\n\t" \
  "ds_read_b64_tr_b16 a[154:155], v238 offset:4352\n\t" \
  "v_mfma_f32_16x16x32_bf16 v[36:39], a[208:211], v[48:51], v[36:39]\n\t" \
  "v_exp_f32 v99, v99\n\t" \
  "v_mfma_f32_16x16x32_bf16 v[40:43], a[224:227], v[48:51], v[180:183]\n\t" \
  "v_cvt_pk_bf16_f32 v153, v98, v99\n\t" \
  "ds_read_b64_tr_b16 a[156:157], v238 offset:8448\n\t" \
  "v_mfma_f32_16x16x32_bf16 v[40:43], a[228:231], v[52:55], v[40:43]\n\t" \
  "v_exp_f32 v100, v100\n\t" \
  "v_mfma_f32_16x16x32_bf16 v[40:43], a[232:235], v[56:59], v[40:43]\n\t" \
  "v_exp_f32 v101, v101\n\t" \
  "v_mfma_f32_16x16x32_bf16 v[40:43], a[236:239], v[60:63], v[40:43]\n\t" \
  "v_cvt_pk_bf16_f32 v154, v100, v101\n\t" \
  "v_max3_f32 v203, v35, v36, v37\n\t" \
  "v_mfma_f32_16x16x32_bf16 v[44:47], a[252:255], v[60:63], v[180:183]\n\t" \
  "v_max3_f32 v192, v192, v38, v39\n\t" \
  "ds_read_b64_tr_b16 a[158:159], v238 offset:12544\n\t" \
  "v_mfma_f32_16x16x32_bf16 v[44:47], a[248:251], v[56:59], v[44:47]\n\t" \
  "v_exp_f32 v102, v102\n\t" \
  "v_mfma_f32_16x16x32_bf16 v[44:47], a[244:247], v[52:55], v[44:47]\n\t" \
  "ds_read_b64_tr_b16 a[160:161], v237 offset:512\n\t" \
  "v_max3_f32 v203, v203, v40, v41\n\t" \
  "v_mfma_f32_16x16x32_bf16 v[44:47], a[240:243], v[48:51], v[44:47]\n\t" \
  "v_max3_f32 v192, v192, v42, v43\n\t" \
  "ds_read_b64_tr_b16 a[162:163], v237 offset:4608\n\t" \
  "v_mfma_f32_16x16x32_bf16 v[64:67], a[192:195], v[80:83], v[184:187]\n\t" \
  "v_exp_f32 v103, v103\n\t" \
  "v_mfma_f32_16x16x32_bf16 v[64:67], a[196:199], v[84:87], v[64:67]\n\t" \
  "v_cvt_pk_bf16_f32 v155, v102, v103\n\t" \
  "ds_read_b64_tr_b16 a[164:165], v237 offset:8704\n\t" \
  "v_mfma_f32_16x16x32_bf16 v[64:67], a[200:203], v[88:91], v[64:67]\n\t" \
  "v_max3_f32 v203, v203, v44, v45\n\t" \
  "v_max3_f32 v192, v192, v46, v47\n\t" \
  "v_mfma_f32_16x16x32_bf16 v[64:67], a[204:207], v[92:95], v[64:67]\n\t" \
  "v_max_f32 v192, v192, v203\n\t" \
  "ds_read_b64_tr_b16 a[166:167], v237 offset:12800\n\t" \
  "v_mfma_f32_16x16x32_bf16 v[68:71], a[220:223], v[92:95], v[184:187]\n\t" \
  "v_cmp_lt_f32 vcc, 0x42800000, v192\n\t" \
  "ds_read_b64_tr_b16 a[168:169], v238 offset:512\n\t" \
  "v_mfma_f32_16x16x32_bf16 v[68:71], a[216:219], v[88:91], v[68:71]\n\t" \
  "s_cbranch_vccnz .Lsf_c41\n\t" \
  ".Lsf_r41:\n\t" \
  "ds_read_b64_tr_b16 a[170:171], v238 offset:4608\n\t" \
  "v_mfma_f32_16x16x32_bf16 v[68:71], a[212:215], v[84:87], v[68:71]\n\t" \
  "v_max3_f32 v192, v64, v65, v66\n\t" \
  "ds_read_b64_tr_b16 a[172:173], v238 offset:8704\n\t" \
  "v_mfma_f32_16x16x32_bf16 v[68:71], a[208:211], v[80:83], v[68:71]\n\t" \
  "v_exp_f32 v104, v104\n\t" \
  "v_mfma_f32_16x16x32_bf16 v[72:75], a[224:227], v[80:83], v[184:187]\n\t" \
  "v_exp_f32 v105, v105\n\t" \
  "v_mfma_f32_16x16x32_bf16 v[72:75], a[228:231], v[84:87], v[72:75]\n\t" \
  "v_cvt_pk_bf16_f32 v156, v104, v105\n\t" \
  "ds_read_b64_tr_b16 a[174:175], v238 offset:12800\n\t" \
  "v_mfma_f32_16x16x32_bf16 v[72:75], a[232:235], v[88:91], v[72:75]\n\t" \
  "v_exp_f32 v106, v106\n\t" \
  "v_mfma_f32_16x16x32_bf16 v[72:75], a[236:239], v[92:95], v[72:75]\n\t" \
  "v_max3_f32 v203, v67, v68, v69\n\t" \
  "v_max3_f32 v192, v192, v70, v71\n\t" \
  "v_mfma_f32_16x16x32_bf16 v[76:79], a[252:255], v[92:95], v[184:187]\n\t" \
  "ds_read_b64_tr_b16 a[176:177], v237 offset:768\n\t" \
  "ds_read_b64_tr_b16 a[178:179], v237 offset:4864\n\t" \
  "v_mfma_f32_16x16x32_bf16 v[76:79], a[248:251], v[88:91], v[76:79]\n\t" \
  "v_exp_f32 v107, v107\n\t" \
  "v_mfma_f32_16x16x32_bf16 v[76:79], a[244:247], v[84:87], v[76:79]\n\t" \
  "v_cvt_pk_bf16_f32 v157, v106, v107\n\t" \
  "v_max3_f32 v203, v203, v72, v73\n\t" \
  "v_mfma_f32_16x16x32_bf16 v[76:79], a[240:243], v[80:83], v[76:79]\n\t" \
  "v_max3_f32 v192, v192, v74, v75\n\t" \
  "ds_read_b64_tr_b16 a[180:181], v237 offset:8960\n\t" \
  "v_mfma_f32_16x16x32_bf16 v[96:99], a[192:195], v[112:115], v[188:191]\n\t" \
  "v_exp_f32 v108, v108\n\t" \
  "v_mfma_f32_16x16x32_bf16 v[96:99], a[196:199], v[116:119], v[96:99]\n\t" \
  "v_exp_f32 v109, v109\n\t" \
  "v_mfma_f32_16x16x32_bf16 v[96:99], a[200:203], v[120:123], v[96:99]\n\t" \
  "v_cvt_pk_bf16_f32 v158, v108, v109\n\t" \
  "ds_read_b64_tr_b16 a[182:183], v237 offset:13056\n\t" \
  "v_mfma_f32_16x16x32_bf16 v[96:99], a[204:207], v[124:127], v[96:99]\n\t" \
  "v_max3_f32 v203, v203, v76, v77\n\t" \
  "v_max3_f32 v192, v192, v78, v79\n\t" \
  "v_mfma_f32_16x16x32_bf16 v[100:103], a[220:223], v[124:127], v[188:191]\n\t" \
  "v_max_f32 v192, v192, v203\n\t" \
  "ds_read_b64_tr_b16 a[184:185], v238 offset:768\n\t" \
  "v_mfma_f32_16x16x32_bf16 v[100:103], a[216:219], v[120:123], v[100:103]\n\t" \
  "v_cmp_lt_f32 vcc, 0x42800000, v192\n\t" \
  "ds_read_b64_tr_b16 a[186:187], v238 offset:4864\n\t" \
  "v_mfma_f32_16x16x32_bf16 v[100:103], a[212:215], v[116:119], v[100:103]\n\t" \
  "s_cbranch_vccnz .Lsf_c42\n\t" \
  ".Lsf_r42:\n\t" \
  "v_max3_f32 v192, v96, v97, v98\n\t" \
  "v_mfma_f32_16x16x32_bf16 v[100:103], a[208:211], v[112:115], v[100:103]\n\t" \
  "ds_read_b64_tr_b16 a[188:189], v238 offset:8960\n\t" \
  "ds_read_b64_tr_b16 a[190:191], v238 offset:13056\n\t" \
  "v_mfma_f32_16x16x32_bf16 v[104:107], a[224:227], v[112:115], v[188:191]\n\t" \
  "v_exp_f32 v110, v110\n\t" \
  "v_mfma_f32_16x16x32_bf16 v[104:107], a[228:231], v[116:119], v[104:107]\n\t" \
  "v_exp_f32 v111, v111\n\t" \
  "v_mfma_f32_16x16x32_bf16 v[104:107], a[232:235], v[120:123], v[104:107]\n\t" \
  "v_cvt_pk_bf16_f32 v159, v110, v111\n\t" \
  "v_max3_f32 v203, v99, v100, v101\n\t" \
  "v_mfma_f32_16x16x32_bf16 v[104:107], a[236:239], v[124:127], v[104:107]\n\t" \
  "v_max3_f32 v192, v192, v102, v103\n\t" \
  "v_mfma_f32_16x16x32_bf16 v[108:111], a[252:255], v[124:127], v[188:191]\n\t" \
  "v_exp_f32 v0, v0\n\t" \
  "v_mfma_f32_16x16x32_bf16 v[108:111], a[248:251], v[120:123], v[108:111]\n\t" \
  "v_exp_f32 v1, v1\n\t" \
  "v_mfma_f32_16x16x32_bf16 v[108:111], a[244:247], v[116:119], v[108:111]\n\t" \
  "v_exp_f32 v2, v2\n\t" \
  "v_mfma_f32_16x16x32_bf16 v[108:111], a[240:243], v[112:115], v[108:111]\n\t" \
  "v_max3_f32 v203, v203, v104, v105\n\t" \
  "v_max3_f32 v192, v192, v106, v107\n\t" \
  "s_waitcnt vmcnt(3) lgkmcnt(0)\n\t" \
  "s_barrier\n\t" \
  "v_mfma_f32_16x16x32_bf16 a[0:3], a[128:131], v[128:131], a[0:3]\n\t" \
  "v_exp_f32 v3, v3\n\t" \
  "v_mfma_f32_16x16x32_bf16 a[0:3], a[132:135], v[132:135], a[0:3]\n\t" \
  "v_exp_f32 v4, v4\n\t" \
  "v_mfma_f32_16x16x32_bf16 a[16:19], a[136:139], v[128:131], a[16:19]\n\t" \
  "ds_read_b128 a[192:195], v220 offset:16384\n\t" \
  "v_max3_f32 v203, v203, v108, v109\n\t" \
  "v_mfma_f32_16x16x32_bf16 a[16:19], a[140:143], v[132:135], a[16:19]\n\t" \
  "v_max3_f32 v192, v192, v110, v111\n\t" \
  "v_max_f32 v192, v192, v203\n\t" \
  "v_mfma_f32_16x16x32_bf16 a[32:35], a[144:147], v[128:131], a[32:35]\n\t" \
  "v_cmp_lt_f32 vcc, 0x42800000, v192\n\t" \
  "ds_read_b128 a[196:199], v221 offset:16384\n\t" \
  "v_mfma_f32_16x16x32_bf16 a[32:35], a[148:151], v[132:135], a[32:35]\n\t" \
  "s_cbranch_vccnz .Lsf_c43\n\t" \
  ".Lsf_r43:\n\t" \
  "ds_read_b128 a[200:203], v222 offset:16384\n\t" \
  "v_mfma_f32_16x16x32_bf16 a[48:51], a[152:155], v[128:131], a[48:51]\n\t" \
  "s_lshr_b32 s98, %[sofk], 6\n\t" \
  "v_xor_b32 v234, s98, v244\n\t" \
  "v_mfma_f32_16x16x32_bf16 a[48:51], a[156:159], v[132:135], a[48:51]\n\t" \
  "v_add_u32 v213, %[m], v242\n\t" \
  "v_add_u32 v215, 98304, v213\n\t" \
  "v_mfma_f32_16x16x32_bf16 a[64:67], a[160:163], v[128:131], a[64:67]\n\t" \
  "v_exp_f32 v5, v5\n\t" \
  "v_mfma_f32_16x16x32_bf16 a[64:67], a[164:167], v[132:135], a[64:67]\n\t" \
  "v_exp_f32 v6, v6\n\t" \
  "v_mfma_f32_16x16x32_bf16 a[80:83], a[168:171], v[128:131], a[80:83]\n\t" \
  "v_mov_b32 v243, %[tag]\n\t" \
  "v_lshrrev_b32 v254, 3, v233\n\t" \
  "v_mfma_f32_16x16x32_bf16 a[80:83], a[172:175], v[132:135], a[80:83]\n\t" \
  "v_exp_f32 v7, v7\n\t" \
  "v_mfma_f32_16x16x32_bf16 a[96:99], a[176:179], v[128:131], a[96:99]\n\t" \
  "v_min_u32 v254, 1, v254\n\t" \
  "ds_read_b128 a[204:207], v223 offset:16384\n\t" \
  "v_mfma_f32_16x16x32_bf16 a[96:99], a[180:183], v[132:135], a[96:99]\n\t" \
  "v_exp_f32 v8, v8\n\t" \
  "v_mfma_f32_16x16x32_bf16 a[112:115], a[184:187], v[128:131], a[112:115]\n\t" \
  "v_exp_f32 v9, v9\n\t" \
  "v_mfma_f32_16x16x32_bf16 a[112:115], a[188:191], v[132:135], a[112:115]\n\t" \
  "v_lshlrev_b32 v254, 31, v254\n\t" \
  "v_lshl_or_b32 v254, v233, 9, v254\n\t" \
  "v_mfma_f32_16x16x32_bf16 v[160:163], v[216:219], v[128:131], v[160:163]\n\t" \
  "v_exp_f32 v10, v10\n\t" \
  "v_mfma_f32_16x16x32_bf16 v[160:163], v[216:219], v[132:135], v[160:163]\n\t" \
  "v_cvt_pk_bf16_f32 v128, v0, v1\n\t" \
  "v_cvt_pk_bf16_f32 v129, v2, v3\n\t" \
  "v_mfma_f32_16x16x32_bf16 a[4:7], a[128:131], v[136:139], a[4:7]\n\t" \
  "v_cvt_pk_bf16_f32 v130, v4, v5\n\t" \
  "v_cvt_pk_bf16_f32 v131, v6, v7\n\t" \
  "v_mfma_f32_16x16x32_bf16 a[4:7], a[132:135], v[140:143], a[4:7]\n\t" \
  "v_cvt_pk_bf16_f32 v132, v8, v9\n\t" \
  "ds_read_b128 a[208:211], v220 offset:20480\n\t" \
  "s_lshr_b32 m0, %[hl18], 18\n\t" \
  "s_brev_b32 s98, 1\n\t" \
  "v_writelane_b32 v254, s98, m0\n\t" \
  "v_mfma_f32_16x16x32_bf16 a[20:23], a[136:139], v[136:139], a[20:23]\n\t" \
  "ds_read_b128 a[212:215], v221 offset:20480\n\t" \
  "s_add_u32 m0, %[m], 49152\n\t" \
  "v_mfma_f32_16x16x32_bf16 a[20:23], a[140:143], v[140:143], a[20:23]\n\t" \
  "v_exp_f32 v11, v11\n\t" \
  "v_mfma_f32_16x16x32_bf16 a[36:39], a[144:147], v[136:139], a[36:39]\n\t" \
  "v_cvt_pk_bf16_f32 v133, v10, v11\n\t" \
  "ds_read_b128 a[216:219], v222 offset:20480\n\t" \
  "v_mfma_f32_16x16x32_bf16 a[36:39], a[148:151], v[140:143], a[36:39]\n\t" \
  "v_exp_f32 v12, v12\n\t" \
  "v_mfma_f32_16x16x32_bf16 a[52:55], a[152:155], v[136:139], a[52:55]\n\t" \
  "v_exp_f32 v13, v13\n\t" \
  "v_mfma_f32_16x16x32_bf16 a[52:55], a[156:159], v[140:143], a[52:55]\n\t" \
  "v_cvt_pk_bf16_f32 v134, v12, v13\n\t" \
  "s_add_u32 s96, %[sofk], 98304\n\t" \
  "v_mfma_f32_16x16x32_bf16 a[68:71], a[160:163], v[136:139], a[68:71]\n\t" \
  "v_exp_f32 v14, v14\n\t" \
  "v_mfma_f32_16x16x32_bf16 a[68:71], a[164:167], v[140:143], a[68:71]\n\t" \
  "ds_read_b128 a[220:223], v223 offset:20480\n\t" \
  "buffer_load_dwordx4 v224, %[sK], s96 offen offset:0 lds\n\t" \
  "v_mfma_f32_16x16x32_bf16 a[84:87], a[168:171], v[136:139], a[84:87]\n\t" \
  "v_exp_f32 v15, v15\n\t" \
  "v_mfma_f32_16x16x32_bf16 a[84:87], a[172:175], v[140:143], a[84:87]\n\t" \
  "v_cvt_pk_bf16_f32 v135, v14, v15\n\t" \
  "ds_read_b128 a[224:227], v220 offset:24576\n\t" \
  "v_mfma_f32_16x16x32_bf16 a[100:103], a[176:179], v[136:139], a[100:103]\n\t" \
  "v_exp_f32 v32, v32\n\t" \
  "v_mfma_f32_16x16x32_bf16 a[100:103], a[180:183], v[140:143], a[100:103]\n\t" \
  "v_exp_f32 v33, v33\n\t" \
  "v_mfma_f32_16x16x32_bf16 a[116:119], a[184:187], v[136:139], a[116:119]\n\t" \
  "v_exp_f32 v34, v34\n\t" \
  "v_mfma_f32_16x16x32_bf16 a[116:119], a[188:191], v[140:143], a[116:119]\n\t" \
  "buffer_load_dwordx4 v225, %[sK], s96 offen offset:1024 lds\n\t" \
  "buffer_load_dwordx4 v226, %[sK], s96 offen offset:2048 lds\n\t" \
  "v_mfma_f32_16x16x32_bf16 v[164:167], v[216:219], v[136:139], v[164:167]\n\t" \
  "v_exp_f32 v35, v35\n\t" \
  "v_mfma_f32_16x16x32_bf16 v[164:167], v[216:219], v[140:143], v[164:167]\n\t" \
  "v_cvt_pk_bf16_f32 v136, v32, v33\n\t" \
  "v_cvt_pk_bf16_f32 v137, v34, v35\n\t" \
  "v_mfma_f32_16x16x32_bf16 a[8:11], a[128:131], v[144:147], a[8:11]\n\t" \
  "ds_read_b128 a[228:231], v221 offset:24576\n\t" \
  "buffer_load_dwordx4 v227, %[sK], s96 offen offset:3072 lds\n\t" \
  "v_mfma_f32_16x16x32_bf16 a[8:11], a[132:135], v[148:151], a[8:11]\n\t" \
  "v_exp_f32 v36, v36\n\t" \
  "v_mfma_f32_16x16x32_bf16 a[24:27], a[136:139], v[144:147], a[24:27]\n\t" \
  "v_exp_f32 v37, v37\n\t" \
  "v_mfma_f32_16x16x32_bf16 a[24:27], a[140:143], v[148:151], a[24:27]\n\t" \
  "v_cvt_pk_bf16_f32 v138, v36, v37\n\t" \
  "ds_read_b128 a[232:235], v222 offset:24576\n\t" \
  "v_mfma_f32_16x16x32_bf16 a[40:43], a[144:147], v[144:147], a[40:43]\n\t" \
  "v_exp_f32 v38, v38\n\t" \
  "v_mfma_f32_16x16x32_bf16 a[40:43], a[148:151], v[148:151], a[40:43]\n\t" \
  "v_exp_f32 v39, v39\n\t" \
  "v_mfma_f32_16x16x32_bf16 a[56:59], a[152:155], v[144:147], a[56:59]\n\t" \
  "v_cvt_pk_bf16_f32 v139, v38, v39\n\t" \
  "ds_read_b128 a[236:239], v223 offset:24576\n\t" \
  "v_mfma_f32_16x16x32_bf16 a[56:59], a[156:159], v[148:151], a[56:59]\n\t" \
  "s_add_u32 m0, %[m], 32768\n\t" \
  "s_mov_b32 s96, 12288\n\t" \
  "buffer_load_dwordx4 v232, %[sG], s96 offen offset:2048 lds\n\t" \
  "v_mfma_f32_16x16x32_bf16 a[72:75], a[160:163], v[144:147], a[72:75]\n\t" \
  "v_exp_f32 v40, v40\n\t" \
  "v_mfma_f32_16x16x32_bf16 a[72:75], a[164:167], v[148:151], a[72:75]\n\t" \
  "s_add_u32 m0, %[m], 147456\n\t" \
  "ds_read_b128 a[240:243], v220 offset:28672\n\t" \
  "v_mfma_f32_16x16x32_bf16 a[88:91], a[168:171], v[144:147], a[88:91]\n\t" \
  "v_exp_f32 v41, v41\n\t" \
  "v_mfma_f32_16x16x32_bf16 a[88:91], a[172:175], v[148:151], a[88:91]\n\t" \
  "v_cvt_pk_bf16_f32 v140, v40, v41\n\t" \
  "s_add_u32 s96, %[sofk], 81920\n\t" \
  "v_mfma_f32_16x16x32_bf16 a[104:107], a[176:179], v[144:147], a[104:107]\n\t" \
  "v_exp_f32 v42, v42\n\t" \
  "v_mfma_f32_16x16x32_bf16 a[104:107], a[180:183], v[148:151], a[104:107]\n\t" \
  "v_exp_f32 v43, v43\n\t" \
  "v_mfma_f32_16x16x32_bf16 a[120:123], a[184:187], v[144:147], a[120:123]\n\t" \
  "v_cvt_pk_bf16_f32 v141, v42, v43\n\t" \
  "ds_read_b128 a[244:247], v221 offset:28672\n\t" \
  "v_mfma_f32_16x16x32_bf16 a[120:123], a[188:191], v[148:151], a[120:123]\n\t" \
  "v_exp_f32 v44, v44\n\t" \
  "v_mfma_f32_16x16x32_bf16 v[168:171], v[216:219], v[144:147], v[168:171]\n\t" \
  "v_exp_f32 v45, v45\n\t" \
  "v_mfma_f32_16x16x32_bf16 v[168:171], v[216:219], v[148:151], v[168:171]\n\t" \
  "v_cvt_pk_bf16_f32 v142, v44, v45\n\t" \
  "buffer_load_dwordx4 v230, %[sV], s96 offen offset:0 lds\n\t" \
  "v_mfma_f32_16x16x32_bf16 a[12:15], a[128:131], v[152:155], a[12:15]\n\t" \
  "v_exp_f32 v46, v46\n\t" \
  "v_mfma_f32_16x16x32_bf16 a[12:15], a[132:135], v[156:159], a[12:15]\n\t" \
  "ds_read_b128 a[248:251], v222 offset:28672\n\t" \
  "buffer_load_dwordx4 v231, %[sV], s96 offen offset:1024 lds\n\t" \
  "v_mfma_f32_16x16x32_bf16 a[28:31], a[136:139], v[152:155], a[28:31]\n\t" \
  "v_exp_f32 v47, v47\n\t" \
  "v_mfma_f32_16x16x32_bf16 a[28:31], a[140:143], v[156:159], a[28:31]\n\t" \
  "v_cvt_pk_bf16_f32 v143, v46, v47\n\t" \
  "ds_read_b128 a[252:255], v223 offset:28672\n\t" \
  "v_mfma_f32_16x16x32_bf16 a[44:47], a[144:147], v[152:155], a[44:47]\n\t" \
  "v_exp_f32 v64, v64\n\t" \
  "v_mfma_f32_16x16x32_bf16 a[44:47], a[148:151], v[156:159], a[44:47]\n\t" \
  "v_exp_f32 v65, v65\n\t" \
  "v_mfma_f32_16x16x32_bf16 a[60:63], a[152:155], v[152:155], a[60:63]\n\t" \
  "v_cvt_pk_bf16_f32 v144, v64, v65\n\t" \
  "buffer_load_dwordx4 v230, %[sV], s96 offen offset:2048 lds\n\t" \
  "v_mfma_f32_16x16x32_bf16 a[60:63], a[156:159], v[156:159], a[60:63]\n\t" \
  "v_exp_f32 v66, v66\n\t" \
  "v_mfma_f32_16x16x32_bf16 a[76:79], a[160:163], v[152:155], a[76:79]\n\t" \
  "v_exp_f32 v67, v67\n\t" \
  "v_mfma_f32_16x16x32_bf16 a[76:79], a[164:167], v[156:159], a[76:79]\n\t" \
  "v_cvt_pk_bf16_f32 v145, v66, v67\n\t" \
  "buffer_load_dwordx4 v231, %[sV], s96 offen offset:3072 lds\n\t" \
  "v_mfma_f32_16x16x32_bf16 a[92:95], a[168:171], v[152:155], a[92:95]\n\t" \
  "v_exp_f32 v68, v68\n\t" \
  "v_mfma_f32_16x16x32_bf16 a[92:95], a[172:175], v[156:159], a[92:95]\n\t" \
  "v_exp_f32 v69, v69\n\t" \
  "v_mfma_f32_16x16x32_bf16 a[108:111], a[176:179], v[152:155], a[108:111]\n\t" \
  "v_cvt_pk_bf16_f32 v146, v68, v69\n\t" \
  "v_mfma_f32_16x16x32_bf16 a[108:111], a[180:183], v[156:159], a[108:111]\n\t" \
  "s_add_u32 m0, %[m], 32768\n\t" \
  "s_mov_b32 s96, 18432\n\t" \
  "buffer_load_dwordx4 v232, %[sG], s96 offen offset:3072 lds\n\t" \
  "v_mfma_f32_16x16x32_bf16 a[124:127], a[184:187], v[152:155], a[124:127]\n\t" \
  "v_exp_f32 v70, v70\n\t" \
  "v_mfma_f32_16x16x32_bf16 a[124:127], a[188:191], v[156:159], a[124:127]\n\t" \
  "v_exp_f32 v71, v71\n\t" \
  "v_mfma_f32_16x16x32_bf16 v[172:175], v[216:219], v[152:155], v[172:175]\n\t" \
  "v_cvt_pk_bf16_f32 v147, v70, v71\n\t" \
  "v_mfma_f32_16x16x32_bf16 v[172:175], v[216:219], v[156:159], v[172:175]\n\t" \
  "v_exp_f32 v72, v72\n\t" \
  "s_cmp_lg_u32 s99, 0\n\t" \
  "s_cbranch_scc1 .Lsf_p4\n\t" \
  ".Lsf_q4:\n\t" \
  "s_waitcnt lgkmcnt(0)\n\t" \
  "v_mfma_f32_16x16x32_bf16 v[0:3], a[192:195], v[16:19], v[176:179]\n\t" \
  "s_add_u32 s98, %[pf4], 43008\n\t" \
  "buffer_load_dword %[pfd], v245, %[sG], s98 offen\n\t" \
  "v_mfma_f32_16x16x32_bf16 v[0:3], a[196:199], v[20:23], v[0:3]\n\t" \
  "ds_read_b64_tr_b16 a[128:129], v228 offset:0\n\t" \
  "v_lshrrev_b32 v252, 3, v233\n\t" \
  "v_mfma_f32_16x16x32_bf16 v[0:3], a[200:203], v[24:27], v[0:3]\n\t" \
  "v_exp_f32 v73, v73\n\t" \
  "v_mfma_f32_16x16x32_bf16 v[0:3], a[204:207], v[28:31], v[0:3]\n\t" \
  "v_cvt_pk_bf16_f32 v148, v72, v73\n\t" \
  "ds_read_b64_tr_b16 a[130:131], v228 offset:4096\n\t" \
  "v_mfma_f32_16x16x32_bf16 v[4:7], a[220:223], v[28:31], v[176:179]\n\t" \
  "ds_read_b64_tr_b16 a[132:133], v228 offset:8192\n\t" \
  "v_mul_u32_u24 v247, 98176, v252\n\t" \
  "v_mfma_f32_16x16x32_bf16 v[4:7], a[216:219], v[24:27], v[4:7]\n\t" \
  "v_exp_f32 v74, v74\n\t" \
  "v_mfma_f32_16x16x32_bf16 v[4:7], a[212:215], v[20:23], v[4:7]\n\t" \
  "v_max3_f32 v192, v0, v1, v2\n\t" \
  "ds_read_b64_tr_b16 a[134:135], v228 offset:12288\n\t" \
  "v_mfma_f32_16x16x32_bf16 v[4:7], a[208:211], v[16:19], v[4:7]\n\t" \
  "v_exp_f32 v75, v75\n\t" \
  "v_mfma_f32_16x16x32_bf16 v[8:11], a[224:227], v[16:19], v[176:179]\n\t" \
  "v_cvt_pk_bf16_f32 v149, v74, v75\n\t" \
  "ds_read_b64_tr_b16 a[136:137], v229 offset:0\n\t" \
  "v_mfma_f32_16x16x32_bf16 v[8:11], a[228:231], v[20:23], v[8:11]\n\t" \
  "v_lshl_add_u32 v247, v233, 4, v247\n\t" \
  "ds_read_b64_tr_b16 a[138:139], v229 offset:4096\n\t" \
  "v_mfma_f32_16x16x32_bf16 v[8:11], a[232:235], v[24:27], v[8:11]\n\t" \
  "v_max3_f32 v203, v3, v4, v5\n\t" \
  "v_max3_f32 v192, v192, v6, v7\n\t" \
  "v_mfma_f32_16x16x32_bf16 v[8:11], a[236:239], v[28:31], v[8:11]\n\t" \
  "v_exp_f32 v76, v76\n\t" \
  "v_mfma_f32_16x16x32_bf16 v[12:15], a[252:255], v[28:31], v[176:179]\n\t" \
  "ds_read_b64_tr_b16 a[140:141], v229 offset:8192\n\t" \
  "v_lshlrev_b32 v252, 4, v252\n\t" \
  "v_mfma_f32_16x16x32_bf16 v[12:15], a[248:251], v[24:27], v[12:15]\n\t" \
  "s_add_u32 s98, %[pf2], 24576\n\t" \
  "buffer_load_dword %[pfd], v202, %[sM], s98 offen\n\t" \
  "v_mfma_f32_16x16x32_bf16 v[12:15], a[244:247], v[20:23], v[12:15]\n\t" \
  "v_max3_f32 v203, v203, v8, v9\n\t" \
  "v_max3_f32 v192, v192, v10, v11\n\t" \
  "v_mfma_f32_16x16x32_bf16 v[12:15], a[240:243], v[16:19], v[12:15]\n\t" \
  "ds_read_b64_tr_b16 a[142:143], v229 offset:12288\n\t" \
  "ds_read_b64_tr_b16 a[144:145], v228 offset:256\n\t" \
  "v_mfma_f32_16x16x32_bf16 v[32:35], a[192:195], v[48:51], v[180:183]\n\t" \
  "v_exp_f32 v77, v77\n\t" \
  "v_mfma_f32_16x16x32_bf16 v[32:35], a[196:199], v[52:55], v[32:35]\n\t" \
  "v_cvt_pk_bf16_f32 v150, v76, v77\n\t" \
  "ds_read_b64_tr_b16 a[146:147], v228 offset:4352\n\t" \
  "v_mfma_f32_16x16x32_bf16 v[32:35], a[200:203], v[56:59], v[32:35]\n\t" \
  "v_max3_f32 v203, v203, v12, v13\n\t" \
  "v_max3_f32 v192, v192, v14, v15\n\t" \
  "v_mfma_f32_16x16x32_bf16 v[32:35], a[204:207], v[60:63], v[32:35]\n\t" \
  "v_max_f32 v192, v192, v203\n\t" \
  "ds_read_b64_tr_b16 a[148:149], v228 offset:8448\n\t" \
  "v_mfma_f32_16x16x32_bf16 v[36:39], a[220:223], v[60:63], v[180:183]\n\t" \
  "v_cmp_lt_f32 vcc, 0x42800000, v192\n\t" \
  "ds_read_b64_tr_b16 a[150:151], v228 offset:12544\n\t" \
  "v_mfma_f32_16x16x32_bf16 v[36:39], a[216:219], v[56:59], v[36:39]\n\t" \
  "s_cbranch_vccnz .Lsf_c50\n\t" \
  ".Lsf_r50:\n\t" \
  "ds_read_b64_tr_b16 a[152:153], v229 offset:256\n\t" \
  "v_mfma_f32_16x16x32_bf16 v[36:39], a[212:215], v[52:55], v[36:39]\n\t" \
  "v_max3_f32 v192, v32, v33, v34\n\t" \
  "ds_read_b64_tr_b16 a[154:155], v229 offset:4352\n\t" \
  "v_mfma_f32_16x16x32_bf16 v[36:39], a[208:211], v[48:51], v[36:39]\n\t" \
  "v_exp_f32 v78, v78\n\t" \
  "v_mfma_f32_16x16x32_bf16 v[40:43], a[224:227], v[48:51], v[180:183]\n\t" \
  "v_exp_f32 v79, v79\n\t" \
  "v_mfma_f32_16x16x32_bf16 v[40:43], a[228:231], v[52:55], v[40:43]\n\t" \
  "v_cvt_pk_bf16_f32 v151, v78, v79\n\t" \
  "ds_read_b64_tr_b16 a[156:157], v229 offset:8448\n\t" \
  "v_mfma_f32_16x16x32_bf16 v[40:43], a[232:235], v[56:59], v[40:43]\n\t" \
  "v_exp_f32 v96, v96\n\t" \
  "v_mfma_f32_16x16x32_bf16 v[40:43], a[236:239], v[60:63], v[40:43]\n\t" \
  "v_max3_f32 v203, v35, v36, v37\n\t" \
  "v_max3_f32 v192, v192, v38, v39\n\t" \
  "v_mfma_f32_16x16x32_bf16 v[44:47], a[252:255], v[60:63], v[180:183]\n\t" \
  "v_exp_f32 v97, v97\n\t" \
  "v_mfma_f32_16x16x32_bf16 v[44:47], a[248:251], v[56:59], v[44:47]\n\t" \
  "v_cvt_pk_bf16_f32 v152, v96, v97\n\t" \
  "ds_read_b64_tr_b16 a[158:159], v229 offset:12544\n\t" \
  "v_mfma_f32_16x16x32_bf16 v[44:47], a[244:247], v[52:55], v[44:47]\n\t" \
  "ds_read_b64_tr_b16 a[160:161], v228 offset:512\n\t" \
  "v_max3_f32 v203, v203, v40, v41\n\t" \
  "v_mfma_f32_16x16x32_bf16 v[44:47], a[240:243], v[48:51], v[44:47]\n\t" \
  "v_max3_f32 v192, v192, v42, v43\n\t" \
  "ds_read_b64_tr_b16 a[162:163], v228 offset:4608\n\t" \
  "v_mfma_f32_16x16x32_bf16 v[64:67], a[192:195], v[80:83], v[184:187]\n\t" \
  "v_exp_f32 v98, v98\n\t" \
  "v_mfma_f32_16x16x32_bf16 v[64:67], a[196:199], v[84:87], v[64:67]\n\t" \
  "v_exp_f32 v99, v99\n\t" \
  "v_mfma_f32_16x16x32_bf16 v[64:67], a[200:203], v[88:91], v[64:67]\n\t" \
  "v_cvt_pk_bf16_f32 v153, v98, v99\n\t" \
  "ds_read_b64_tr_b16 a[164:165], v228 offset:8704\n\t" \
  "v_mfma_f32_16x16x32_bf16 v[64:67], a[204:207], v[92:95], v[64:67]\n\t" \
  "v_max3_f32 v203, v203, v44, v45\n\t" \
  "v_max3_f32 v192, v192, v46, v47\n\t" \
  "v_mfma_f32_16x16x32_bf16 v[68:71], a[220:223], v[92:95], v[184:187]\n\t" \
  "v_max_f32 v192, v192, v203\n\t" \
  "ds_read_b64_tr_b16 a[166:167], v228 offset:12800\n\t" \
  "v_mfma_f32_16x16x32_bf16 v[68:71], a[216:219], v[88:91], v[68:71]\n\t" \
  "v_cmp_lt_f32 vcc, 0x42800000, v192\n\t" \
  "ds_read_b64_tr_b16 a[168:169], v229 offset:512\n\t" \
  "v_mfma_f32_16x16x32_bf16 v[68:71], a[212:215], v[84:87], v[68:71]\n\t" \
  "s_cbranch_vccnz .Lsf_c51\n\t" \
  ".Lsf_r51:\n\t" \
  "v_max3_f32 v192, v64, v65, v66\n\t" \
  "v_mfma_f32_16x16x32_bf16 v[68:71], a[208:211], v[80:83], v[68:71]\n\t" \
  "ds_read_b64_tr_b16 a[170:171], v229 offset:4608\n\t" \
  "ds_read_b64_tr_b16 a[172:173], v229 offset:8704\n\t" \
  "v_mfma_f32_16x16x32_bf16 v[72:75], a[224:227], v[80:83], v[184:187]\n\t" \
  "v_exp_f32 v100, v100\n\t" \
  "v_mfma_f32_16x16x32_bf16 v[72:75], a[228:231], v[84:87], v[72:75]\n\t" \
  "v_exp_f32 v101, v101\n\t" \
  "v_mfma_f32_16x16x32_bf16 v[72:75], a[232:235], v[88:91], v[72:75]\n\t" \
  "v_cvt_pk_bf16_f32 v154, v100, v101\n\t" \
  "v_max3_f32 v203, v67, v68, v69\n\t" \
  "v_mfma_f32_16x16x32_bf16 v[72:75], a[236:239], v[92:95], v[72:75]\n\t" \
  "v_max3_f32 v192, v192, v70, v71\n\t" \
  "ds_read_b64_tr_b16 a[174:175], v229 offset:12800\n\t" \
  "v_mfma_f32_16x16x32_bf16 v[76:79], a[252:255], v[92:95], v[184:187]\n\t" \
  "ds_read_b64_tr_b16 a[176:177], v228 offset:768\n\t" \
  "ds_read_b64_tr_b16 a[178:179], v228 offset:4864\n\t" \
  "v_mfma_f32_16x16x32_bf16 v[76:79], a[248:251], v[88:91], v[76:79]\n\t" \
  "v_exp_f32 v102, v102\n\t" \
  "v_mfma_f32_16x16x32_bf16 v[76:79], a[244:247], v[84:87], v[76:79]\n\t" \
  "v_max3_f32 v203, v203, v72, v73\n\t" \
  "v_max3_f32 v192, v192, v74, v75\n\t" \
  "v_mfma_f32_16x16x32_bf16 v[76:79], a[240:243], v[80:83], v[76:79]\n\t" \
  "v_exp_f32 v103, v103\n\t" \
  "v_mfma_f32_16x16x32_bf16 v[96:99], a[192:195], v[112:115], v[188:191]\n\t" \
  "v_cvt_pk_bf16_f32 v155, v102, v103\n\t" \
  "ds_read_b64_tr_b16 a[180:181], v228 offset:8960\n\t" \
  "v_mfma_f32_16x16x32_bf16 v[96:99], a[196:199], v[116:119], v[96:99]\n\t" \
  "v_exp_f32 v104, v104\n\t" \
  "v_mfma_f32_16x16x32_bf16 v[96:99], a[200:203], v[120:123], v[96:99]\n\t" \
  "ds_read_b64_tr_b16 a[182:183], v228 offset:13056\n\t" \
  "v_max3_f32 v203, v203, v76, v77\n\t" \
  "v_mfma_f32_16x16x32_bf16 v[96:99], a[204:207], v[124:127], v[96:99]\n\t" \
  "v_max3_f32 v192, v192, v78, v79\n\t" \
  "v_max_f32 v192, v192, v203\n\t" \
  "v_mfma_f32_16x16x32_bf16 v[100:103], a[220:223], v[124:127], v[188:191]\n\t" \
  "v_cmp_lt_f32 vcc, 0x42800000, v192\n\t" \
  "ds_read_b64_tr_b16 a[184:185], v229 offset:768\n\t" \
  "v_mfma_f32_16x16x32_bf16 v[100:103], a[216:219], v[120:123], v[100:103]\n\t" \
  "s_cbranch_vccnz .Lsf_c52\n\t" \
  ".Lsf_r52:\n\t" \
  "ds_read_b64_tr_b16 a[186:187], v229 offset:4864\n\t" \
  "v_mfma_f32_16x16x32_bf16 v[100:103], a[212:215], v[116:119], v[100:103]\n\t" \
  "v_max3_f32 v192, v96, v97, v98\n\t" \
  "ds_read_b64_tr_b16 a[188:189], v229 offset:8960\n\t" \
  "v_mfma_f32_16x16x32_bf16 v[100:103], a[208:211], v[112:115], v[100:103]\n\t" \
  "v_exp_f32 v105, v105\n\t" \
  "v_exp_f32 v106, v106\n\t" \
  "v_exp_f32 v107, v107\n\t" \
  "v_cvt_pk_bf16_f32 v156, v104, v105\n\t" \
  "v_cvt_pk_bf16_f32 v157, v106, v107\n\t" \
  "v_mfma_f32_16x16x32_bf16 v[104:107], a[224:227], v[112:115], v[188:191]\n\t" \
  "v_exp_f32 v108, v108\n\t" \
  "v_mfma_f32_16x16x32_bf16 v[104:107], a[228:231], v[116:119], v[104:107]\n\t" \
  "ds_read_b64_tr_b16 a[190:191], v229 offset:13056\n\t" \
  "v_max3_f32 v203, v99, v100, v101\n\t" \
  "v_mfma_f32_16x16x32_bf16 v[104:107], a[232:235], v[120:123], v[104:107]\n\t" \
  "v_max3_f32 v192, v192, v102, v103\n\t" \
  "v_mfma_f32_16x16x32_bf16 v[104:107], a[236:239], v[124:127], v[104:107]\n\t" \
  "v_exp_f32 v109, v109\n\t" \
  "v_exp_f32 v110, v110\n\t" \
  "v_exp_f32 v111, v111\n\t" \
  "v_cvt_pk_bf16_f32 v158, v108, v109\n\t" \
  "v_cvt_pk_bf16_f32 v159, v110, v111\n\t" \
  "v_mfma_f32_16x16x32_bf16 v[108:111], a[252:255], v[124:127], v[188:191]\n\t" \
  "v_exp_f32 v0, v0\n\t" \
  "v_mfma_f32_16x16x32_bf16 v[108:111], a[248:251], v[120:123], v[108:111]\n\t" \
  "v_exp_f32 v1, v1\n\t" \
  "v_mfma_f32_16x16x32_bf16 v[108:111], a[244:247], v[116:119], v[108:111]\n\t" \
  "v_max3_f32 v203, v203, v104, v105\n\t" \
  "v_max3_f32 v192, v192, v106, v107\n\t" \
  "v_mfma_f32_16x16x32_bf16 v[108:111], a[240:243], v[112:115], v[108:111]\n\t" \
  "v_exp_f32 v2, v2\n\t" \
  "s_waitcnt vmcnt(3) lgkmcnt(0)\n\t" \
  "s_barrier\n\t" \
  "v_mfma_f32_16x16x32_bf16 a[0:3], a[128:131], v[128:131], a[0:3]\n\t" \
  "v_exp_f32 v3, v3\n\t" \
  "v_mfma_f32_16x16x32_bf16 a[0:3], a[132:135], v[132:135], a[0:3]\n\t" \
  "v_exp_f32 v4, v4\n\t" \
  "v_mfma_f32_16x16x32_bf16 a[16:19], a[136:139], v[128:131], a[16:19]\n\t" \
  "ds_read_b128 a[192:195], v220 offset:0\n\t" \
  "v_max3_f32 v203, v203, v108, v109\n\t" \
  "v_mfma_f32_16x16x32_bf16 a[16:19], a[140:143], v[132:135], a[16:19]\n\t" \
  "v_max3_f32 v192, v192, v110, v111\n\t" \
  "v_max_f32 v192, v192, v203\n\t" \
  "v_mfma_f32_16x16x32_bf16 a[32:35], a[144:147], v[128:131], a[32:35]\n\t" \
  "v_cmp_lt_f32 vcc, 0x42800000, v192\n\t" \
  "ds_read_b128 a[196:199], v221 offset:0\n\t" \
  "v_mfma_f32_16x16x32_bf16 a[32:35], a[148:151], v[132:135], a[32:35]\n\t" \
  "s_cbranch_vccnz .Lsf_c53\n\t" \
  ".Lsf_r53:\n\t" \
  "ds_read_b128 a[200:203], v222 offset:0\n\t" \
  "v_mfma_f32_16x16x32_bf16 a[48:51], a[152:155], v[128:131], a[48:51]\n\t" \
  "v_exp_f32 v5, v5\n\t" \
  "v_mfma_f32_16x16x32_bf16 a[48:51], a[156:159], v[132:135], a[48:51]\n\t" \
  "v_exp_f32 v6, v6\n\t" \
  "v_mfma_f32_16x16x32_bf16 a[64:67], a[160:163], v[128:131], a[64:67]\n\t" \
  "v_exp_f32 v7, v7\n\t" \
  "v_mfma_f32_16x16x32_bf16 a[64:67], a[164:167], v[132:135], a[64:67]\n\t" \
  "v_exp_f32 v8, v8\n\t" \
  "v_mfma_f32_16x16x32_bf16 a[80:83], a[168:171], v[128:131], a[80:83]\n\t" \
  "v_exp_f32 v9, v9\n\t" \
  "v_mfma_f32_16x16x32_bf16 a[80:83], a[172:175], v[132:135], a[80:83]\n\t" \
  "v_exp_f32 v10, v10\n\t" \
  "v_mfma_f32_16x16x32_bf16 a[96:99], a[176:179], v[128:131], a[96:99]\n\t" \
  "v_exp_f32 v11, v11\n\t" \
  "v_mfma_f32_16x16x32_bf16 a[96:99], a[180:183], v[132:135], a[96:99]\n\t" \
  "v_exp_f32 v12, v12\n\t" \
  "v_mfma_f32_16x16x32_bf16 a[112:115], a[184:187], v[128:131], a[112:115]\n\t" \
  "ds_read_b128 a[204:207], v223 offset:0\n\t" \
  "ds_read_b128 a[208:211], v220 offset:4096\n\t" \
  "v_mfma_f32_16x16x32_bf16 a[112:115], a[188:191], v[132:135], a[112:115]\n\t" \
  "v_exp_f32 v13, v13\n\t" \
  "v_mfma_f32_16x16x32_bf16 v[160:163], v[216:219], v[128:131], v[160:163]\n\t" \
  "v_exp_f32 v14, v14\n\t" \
  "v_mfma_f32_16x16x32_bf16 v[160:163], v[216:219], v[132:135], v[160:163]\n\t" \
  "v_cvt_pk_bf16_f32 v128, v0, v1\n\t" \
  "v_cvt_pk_bf16_f32 v129, v2, v3\n\t" \
  "v_mfma_f32_16x16x32_bf16 a[4:7], a[128:131], v[136:139], a[4:7]\n\t" \
  "v_cvt_pk_bf16_f32 v130, v4, v5\n\t" \
  "v_cvt_pk_bf16_f32 v131, v6, v7\n\t" \
  "v_mfma_f32_16x16x32_bf16 a[4:7], a[132:135], v[140:143], a[4:7]\n\t" \
  "v_cvt_pk_bf16_f32 v132, v8, v9\n\t" \
  "v_cvt_pk_bf16_f32 v133, v10, v11\n\t" \
  "v_mfma_f32_16x16x32_bf16 a[20:23], a[136:139], v[136:139], a[20:23]\n\t" \
  "v_cvt_pk_bf16_f32 v134, v12, v13\n\t" \
  "ds_read_b128 a[212:215], v221 offset:4096\n\t" \
  "v_mfma_f32_16x16x32_bf16 a[20:23], a[140:143], v[140:143], a[20:23]\n\t" \
  "v_exp_f32 v15, v15\n\t" \
  "v_mfma_f32_16x16x32_bf16 a[36:39], a[144:147], v[136:139], a[36:39]\n\t" \
  "v_cvt_pk_bf16_f32 v135, v14, v15\n\t" \
  "s_add_u32 m0, %[m], 65536\n\t" \
  "v_mfma_f32_16x16x32_bf16 a[36:39], a[148:151], v[140:143], a[36:39]\n\t" \
  "v_exp_f32 v32, v32\n\t" \
  "v_mfma_f32_16x16x32_bf16 a[52:55], a[152:155], v[136:139], a[52:55]\n\t" \
  "ds_read_b128 a[216:219], v222 offset:4096\n\t" \
  "s_add_u32 s96, %[sofk], 114688\n\t" \
  "v_mfma_f32_16x16x32_bf16 a[52:55], a[156:159], v[140:143], a[52:55]\n\t" \
  "v_exp_f32 v33, v33\n\t" \
  "v_mfma_f32_16x16x32_bf16 a[68:71], a[160:163], v[136:139], a[68:71]\n\t" \
  "v_exp_f32 v34, v34\n\t" \
  "v_mfma_f32_16x16x32_bf16 a[68:71], a[164:167], v[140:143], a[68:71]\n\t" \
  "ds_read_b128 a[220:223], v223 offset:4096\n\t" \
  "buffer_load_dwordx4 v224, %[sK], s96 offen offset:0 lds\n\t" \
  "v_mfma_f32_16x16x32_bf16 a[84:87], a[168:171], v[136:139], a[84:87]\n\t" \
  "v_exp_f32 v35, v35\n\t" \
  "v_mfma_f32_16x16x32_bf16 a[84:87], a[172:175], v[140:143], a[84:87]\n\t" \
  "v_exp_f32 v36, v36\n\t" \
  "v_mfma_f32_16x16x32_bf16 a[100:103], a[176:179], v[136:139], a[100:103]\n\t" \
  "v_exp_f32 v37, v37\n\t" \
  "v_mfma_f32_16x16x32_bf16 a[100:103], a[180:183], v[140:143], a[100:103]\n\t" \
  "ds_read_b128 a[224:227], v220 offset:8192\n\t" \
  "buffer_load_dwordx4 v225, %[sK], s96 offen offset:1024 lds\n\t" \
  "v_mfma_f32_16x16x32_bf16 a[116:119], a[184:187], v[136:139], a[116:119]\n\t" \
  "v_exp_f32 v38, v38\n\t" \
  "v_mfma_f32_16x16x32_bf16 a[116:119], a[188:191], v[140:143], a[116:119]\n\t" \
  "v_exp_f32 v39, v39\n\t" \
  "v_mfma_f32_16x16x32_bf16 v[164:167], v[216:219], v[136:139], v[164:167]\n\t" \
  "buffer_load_dwordx4 v226, %[sK], s96 offen offset:2048 lds\n\t" \
  "ds_read_b128 a[228:231], v221 offset:8192\n\t" \
  "v_mfma_f32_16x16x32_bf16 v[164:167], v[216:219], v[140:143], v[164:167]\n\t" \
  "v_cvt_pk_bf16_f32 v136, v32, v33\n\t" \
  "v_cvt_pk_bf16_f32 v137, v34, v35\n\t" \
  "v_mfma_f32_16x16x32_bf16 a[8:11], a[128:131], v[144:147], a[8:11]\n\t" \
  "v_cvt_pk_bf16_f32 v138, v36, v37\n\t" \
  "v_cvt_pk_bf16_f32 v139, v38, v39\n\t" \
  "v_mfma_f32_16x16x32_bf16 a[8:11], a[132:135], v[148:151], a[8:11]\n\t" \
  "v_exp_f32 v40, v40\n\t" \
  "v_mfma_f32_16x16x32_bf16 a[24:27], a[136:139], v[144:147], a[24:27]\n\t" \
  "buffer_load_dwordx4 v227, %[sK], s96 offen offset:3072 lds\n\t" \
  "ds_read_b128 a[232:235], v222 offset:8192\n\t" \
  "v_mfma_f32_16x16x32_bf16 a[24:27], a[140:143], v[148:151], a[24:27]\n\t" \
  "v_exp_f32 v41, v41\n\t" \
  "v_mfma_f32_16x16x32_bf16 a[40:43], a[144:147], v[144:147], a[40:43]\n\t" \
  "v_cvt_pk_bf16_f32 v140, v40, v41\n\t" \
  "ds_read_b128 a[236:239], v223 offset:8192\n\t" \
  "v_mfma_f32_16x16x32_bf16 a[40:43], a[148:151], v[148:151], a[40:43]\n\t" \
  "v_exp_f32 v42, v42\n\t" \
  "v_mfma_f32_16x16x32_bf16 a[56:59], a[152:155], v[144:147], a[56:59]\n\t" \
  "v_exp_f32 v43, v43\n\t" \
  "v_mfma_f32_16x16x32_bf16 a[56:59], a[156:159], v[148:151], a[56:59]\n\t" \
  "v_cvt_pk_bf16_f32 v141, v42, v43\n\t" \
  "ds_read_b128 a[240:243], v220 offset:12288\n\t" \
  "v_mfma_f32_16x16x32_bf16 a[72:75], a[160:163], v[144:147], a[72:75]\n\t" \
  "s_add_u32 m0, %[m], 98304\n\t" \
  "s_mov_b32 s96, 28672\n\t" \
  "buffer_load_dwordx4 v232, %[sG], s96 offen offset:0 lds\n\t" \
  "v_mfma_f32_16x16x32_bf16 a[72:75], a[164:167], v[148:151], a[72:75]\n\t" \
  "v_exp_f32 v44, v44\n\t" \
  "v_mfma_f32_16x16x32_bf16 a[88:91], a[168:171], v[144:147], a[88:91]\n\t" \
  "s_add_u32 m0, %[m], 81920\n\t" \
  "s_add_u32 s96, %[sofk], 98304\n\t" \
  "v_mfma_f32_16x16x32_bf16 a[88:91], a[172:175], v[148:151], a[88:91]\n\t" \
  "v_exp_f32 v45, v45\n\t" \
  "v_mfma_f32_16x16x32_bf16 a[104:107], a[176:179], v[144:147], a[104:107]\n\t" \
  "v_cvt_pk_bf16_f32 v142, v44, v45\n\t" \
  "ds_read_b128 a[244:247], v221 offset:12288\n\t" \
  "v_mfma_f32_16x16x32_bf16 a[104:107], a[180:183], v[148:151], a[104:107]\n\t" \
  "v_exp_f32 v46, v46\n\t" \
  "v_mfma_f32_16x16x32_bf16 a[120:123], a[184:187], v[144:147], a[120:123]\n\t" \
  "v_exp_f32 v47, v47\n\t" \
  "v_mfma_f32_16x16x32_bf16 a[120:123], a[188:191], v[148:151], a[120:123]\n\t" \
  "v_cvt_pk_bf16_f32 v143, v46, v47\n\t" \
  "buffer_load_dwordx4 v230, %[sV], s96 offen offset:0 lds\n\t" \
  "v_mfma_f32_16x16x32_bf16 v[168:171], v[216:219], v[144:147], v[168:171]\n\t" \
  "v_exp_f32 v64, v64\n\t" \
  "v_mfma_f32_16x16x32_bf16 v[168:171], v[216:219], v[148:151], v[168:171]\n\t" \
  "v_exp_f32 v65, v65\n\t" \
  "v_mfma_f32_16x16x32_bf16 a[12:15], a[128:131], v[152:155], a[12:15]\n\t" \
  "v_cvt_pk_bf16_f32 v144, v64, v65\n\t" \
  "ds_read_b128 a[248:251], v222 offset:12288\n\t" \
  "v_mfma_f32_16x16x32_bf16 a[12:15], a[132:135], v[156:159], a[12:15]\n\t" \
  "v_exp_f32 v66, v66\n\t" \
  "v_mfma_f32_16x16x32_bf16 a[28:31], a[136:139], v[152:155], a[28:31]\n\t" \
  "v_exp_f32 v67, v67\n\t" \
  "v_mfma_f32_16x16x32_bf16 a[28:31], a[140:143], v[156:159], a[28:31]\n\t" \
  "v_cvt_pk_bf16_f32 v145, v66, v67\n\t" \
  "buffer_load_dwordx4 v231, %[sV], s96 offen offset:1024 lds\n\t" \
  "v_mfma_f32_16x16x32_bf16 a[44:47], a[144:147], v[152:155], a[44:47]\n\t" \
  "ds_read_b128 a[252:255], v223 offset:12288\n\t" \
  "buffer_load_dwordx4 v230, %[sV], s96 offen offset:2048 lds\n\t" \
  "v_mfma_f32_16x16x32_bf16 a[44:47], a[148:151], v[156:159], a[44:47]\n\t" \
  "v_exp_f32 v68, v68\n\t" \
  "v_mfma_f32_16x16x32_bf16 a[60:63], a[152:155], v[152:155], a[60:63]\n\t" \
  "v_exp_f32 v69, v69\n\t" \
  "v_mfma_f32_16x16x32_bf16 a[60:63], a[156:159], v[156:159], a[60:63]\n\t" \
  "v_cvt_pk_bf16_f32 v146, v68, v69\n\t" \
  "buffer_load_dwordx4 v231, %[sV], s96 offen offset:3072 lds\n\t" \
  "v_mfma_f32_16x16x32_bf16 a[76:79], a[160:163], v[152:155], a[76:79]\n\t" \
  "v_exp_f32 v70, v70\n\t" \
  "v_mfma_f32_16x16x32_bf16 a[76:79], a[164:167], v[156:159], a[76:79]\n\t" \
  "v_exp_f32 v71, v71\n\t" \
  "v_mfma_f32_16x16x32_bf16 a[92:95], a[168:171], v[152:155], a[92:95]\n\t" \
  "v_cvt_pk_bf16_f32 v147, v70, v71\n\t" \
  "v_mfma_f32_16x16x32_bf16 a[92:95], a[172:175], v[156:159], a[92:95]\n\t" \
  "v_lshrrev_b32 v205, 1, v233\n\t" \
  "v_lshlrev_b32 v205, 2, v205\n\t" \
  "v_mfma_f32_16x16x32_bf16 a[108:111], a[176:179], v[152:155], a[108:111]\n\t" \
  "v_add_u32 v205, 8192, v205\n\t" \
  "buffer_load_dword v209, v205, %[ws], 0 offen sc1\n\t" \
  "v_mfma_f32_16x16x32_bf16 a[108:111], a[180:183], v[156:159], a[108:111]\n\t" \
  "s_add_u32 m0, %[m], 98304\n\t" \
  "s_mov_b32 s96, 34816\n\t" \
  "buffer_load_dwordx4 v232, %[sG], s96 offen offset:1024 lds\n\t" \
  "v_mfma_f32_16x16x32_bf16 a[124:127], a[184:187], v[152:155], a[124:127]\n\t" \
  "v_exp_f32 v72, v72\n\t" \
  "v_mfma_f32_16x16x32_bf16 a[124:127], a[188:191], v[156:159], a[124:127]\n\t" \
  "v_exp_f32 v73, v73\n\t" \
  "v_mfma_f32_16x16x32_bf16 v[172:175], v[216:219], v[152:155], v[172:175]\n\t" \
  "v_cvt_pk_bf16_f32 v148, v72, v73\n\t" \
  "v_mfma_f32_16x16x32_bf16 v[172:175], v[216:219], v[156:159], v[172:175]\n\t" \
  "v_exp_f32 v74, v74\n\t" \
  "s_cmp_lg_u32 s99, 0\n\t" \
  "s_cbranch_scc1 .Lsf_p5\n\t" \
  ".Lsf_q5:\n\t" \
  "s_waitcnt lgkmcnt(0)\n\t" \
  "v_mfma_f32_16x16x32_bf16 v[0:3], a[192:195], v[16:19], v[176:179]\n\t" \
  "s_add_u32 s98, %[pf2], 49152\n\t" \
  "buffer_load_dword %[pfd], v202, %[sM], s98 offen\n\t" \
  "v_mfma_f32_16x16x32_bf16 v[0:3], a[196:199], v[20:23], v[0:3]\n\t" \
  "v_exp_f32 v75, v75\n\t" \
  "v_mfma_f32_16x16x32_bf16 v[0:3], a[200:203], v[24:27], v[0:3]\n\t" \
  "v_cvt_pk_bf16_f32 v149, v74, v75\n\t" \
  "ds_read_b64_tr_b16 a[128:129], v237 offset:0\n\t" \
  "v_mfma_f32_16x16x32_bf16 v[0:3], a[204:207], v[28:31], v[0:3]\n\t" \
  "v_exp_f32 v76, v76\n\t" \
  "v_mfma_f32_16x16x32_bf16 v[4:7], a[220:223], v[28:31], v[176:179]\n\t" \
  "ds_read_b64_tr_b16 a[130:131], v237 offset:4096\n\t" \
  "ds_read_b64_tr_b16 a[132:133], v237 offset:8192\n\t" \
  "v_mfma_f32_16x16x32_bf16 v[4:7], a[216:219], v[24:27], v[4:7]\n\t" \
  "v_exp_f32 v77, v77\n\t" \
  "v_mfma_f32_16x16x32_bf16 v[4:7], a[212:215], v[20:23], v[4:7]\n\t" \
  "v_cvt_pk_bf16_f32 v150, v76, v77\n\t" \
  "v_max3_f32 v192, v0, v1, v2\n\t" \
  "v_mfma_f32_16x16x32_bf16 v[4:7], a[208:211], v[16:19], v[4:7]\n\t" \
  "v_exp_f32 v78, v78\n\t" \
  "v_mfma_f32_16x16x32_bf16 v[8:11], a[224:227], v[16:19], v[176:179]\n\t" \
  "ds_read_b64_tr_b16 a[134:135], v237 offset:12288\n\t" \
  "ds_read_b64_tr_b16 a[136:137], v238 offset:0\n\t" \
  "v_mfma_f32_16x16x32_bf16 v[8:11], a[228:231], v[20:23], v[8:11]\n\t" \
  "v_exp_f32 v79, v79\n\t" \
  "v_mfma_f32_16x16x32_bf16 v[8:11], a[232:235], v[24:27], v[8:11]\n\t" \
  "v_cvt_pk_bf16_f32 v151, v78, v79\n\t" \
  "v_max3_f32 v203, v3, v4, v5\n\t" \
  "v_mfma_f32_16x16x32_bf16 v[8:11], a[236:239], v[28:31], v[8:11]\n\t" \
  "v_max3_f32 v192, v192, v6, v7\n\t" \
  "ds_read_b64_tr_b16 a[138:139], v238 offset:4096\n\t" \
  "v_mfma_f32_16x16x32_bf16 v[12:15], a[252:255], v[28:31], v[176:179]\n\t" \
  "ds_read_b64_tr_b16 a[140:141], v238 offset:8192\n\t" \
  "ds_read_b64_tr_b16 a[142:143], v238 offset:12288\n\t" \
  "v_mfma_f32_16x16x32_bf16 v[12:15], a[248:251], v[24:27], v[12:15]\n\t" \
  "v_exp_f32 v96, v96\n\t" \
  "v_mfma_f32_16x16x32_bf16 v[12:15], a[244:247], v[20:23], v[12:15]\n\t" \
  "v_max3_f32 v203, v203, v8, v9\n\t" \
  "v_max3_f32 v192, v192, v10, v11\n\t" \
  "v_mfma_f32_16x16x32_bf16 v[12:15], a[240:243], v[16:19], v[12:15]\n\t" \
  "s_add_u32 s98, %[pf2], 73728\n\t" \
  "buffer_load_dword %[pfd], v202, %[sM], s98 offen\n\t" \
  "v_mfma_f32_16x16x32_bf16 v[32:35], a[192:195], v[48:51], v[180:183]\n\t" \
  "ds_read_b64_tr_b16 a[144:145], v237 offset:256\n\t" \
  "ds_read_b64_tr_b16 a[146:147], v237 offset:4352\n\t" \
  "v_mfma_f32_16x16x32_bf16 v[32:35], a[196:199], v[52:55], v[32:35]\n\t" \
  "v_exp_f32 v97, v97\n\t" \
  "v_mfma_f32_16x16x32_bf16 v[32:35], a[200:203], v[56:59], v[32:35]\n\t" \
  "v_cvt_pk_bf16_f32 v152, v96, v97\n\t" \
  "v_max3_f32 v203, v203, v12, v13\n\t" \
  "v_mfma_f32_16x16x32_bf16 v[32:35], a[204:207], v[60:63], v[32:35]\n\t" \
  "v_max3_f32 v192, v192, v14, v15\n\t" \
  "v_max_f32 v192, v192, v203\n\t" \
  "v_mfma_f32_16x16x32_bf16 v[36:39], a[220:223], v[60:63], v[180:183]\n\t" \
  "v_cmp_lt_f32 vcc, 0x42800000, v192\n\t" \
  "ds_read_b64_tr_b16 a[148:149], v237 offset:8448\n\t" \
  "v_mfma_f32_16x16x32_bf16 v[36:39], a[216:219], v[56:59], v[36:39]\n\t" \
  "s_cbranch_vccnz .Lsf_c60\n\t" \
  ".Lsf_r60:\n\t" \
  "ds_read_b64_tr_b16 a[150:151], v237 offset:12544\n\t" \
  "v_mfma_f32_16x16x32_bf16 v[36:39], a[212:215], v[52:55], v[36:39]\n\t" \
  "v_max3_f32 v192, v32, v33, v34\n\t" \
  "ds_read_b64_tr_b16 a[152:153], v238 offset:256\n\t" \
  "v_mfma_f32_16x16x32_bf16 v[36:39], a[208:211], v[48:51], v[36:39]\n\t" \
  "v_exp_f32 v98, v98\n\t" \
  "v_mfma_f32_16x16x32_bf16 v[40:43], a[224:227], v[48:51], v[180:183]\n\t" \
  "v_exp_f32 v99, v99\n\t" \
  "v_mfma_f32_16x16x32_bf16 v[40:43], a[228:231], v[52:55], v[40:43]\n\t" \
  "v_cvt_pk_bf16_f32 v153, v98, v99\n\t" \
  "ds_read_b64_tr_b16 a[154:155], v238 offset:4352\n\t" \
  "v_mfma_f32_16x16x32_bf16 v[40:43], a[232:235], v[56:59], v[40:43]\n\t" \
  "v_exp_f32 v100, v100\n\t" \
  "v_mfma_f32_16x16x32_bf16 v[40:43], a[236:239], v[60:63], v[40:43]\n\t" \
  "v_max3_f32 v203, v35, v36, v37\n\t" \
  "v_max3_f32 v192, v192, v38, v39\n\t" \
  "v_mfma_f32_16x16x32_bf16 v[44:47], a[252:255], v[60:63], v[180:183]\n\t" \
  "ds_read_b64_tr_b16 a[156:157], v238 offset:8448\n\t" \
  "ds_read_b64_tr_b16 a[158:159], v238 offset:12544\n\t" \
  "v_mfma_f32_16x16x32_bf16 v[44:47], a[248:251], v[56:59], v[44:47]\n\t" \
  "v_exp_f32 v101, v101\n\t" \
  "v_mfma_f32_16x16x32_bf16 v[44:47], a[244:247], v[52:55], v[44:47]\n\t" \
  "v_cvt_pk_bf16_f32 v154, v100, v101\n\t" \
  "ds_read_b64_tr_b16 a[160:161], v237 offset:512\n\t" \
  "v_mfma_f32_16x16x32_bf16 v[44:47], a[240:243], v[48:51], v[44:47]\n\t" \
  "v_max3_f32 v203, v203, v40, v41\n\t" \
  "v_max3_f32 v192, v192, v42, v43\n\t" \
  "v_mfma_f32_16x16x32_bf16 v[64:67], a[192:195], v[80:83], v[184:187]\n\t" \
  "ds_read_b64_tr_b16 a[162:163], v237 offset:4608\n\t" \
  "ds_read_b64_tr_b16 a[164:165], v237 offset:8704\n\t" \
  "v_mfma_f32_16x16x32_bf16 v[64:67], a[196:199], v[84:87], v[64:67]\n\t" \
  "v_exp_f32 v102, v102\n\t" \
  "v_mfma_f32_16x16x32_bf16 v[64:67], a[200:203], v[88:91], v[64:67]\n\t" \
  "v_max3_f32 v203, v203, v44, v45\n\t" \
  "v_max3_f32 v192, v192, v46, v47\n\t" \
  "v_mfma_f32_16x16x32_bf16 v[64:67], a[204:207], v[92:95], v[64:67]\n\t" \
  "v_max_f32 v192, v192, v203\n\t" \
  "ds_read_b64_tr_b16 a[166:167], v237 offset:12800\n\t" \
  "v_mfma_f32_16x16x32_bf16 v[68:71], a[220:223], v[92:95], v[184:187]\n\t" \
  "v_cmp_lt_f32 vcc, 0x42800000, v192\n\t" \
  "ds_read_b64_tr_b16 a[168:169], v238 offset:512\n\t" \
  "v_mfma_f32_16x16x32_bf16 v[68:71], a[216:219], v[88:91], v[68:71]\n\t" \
  "s_cbranch_vccnz .Lsf_c61\n\t" \
  ".Lsf_r61:\n\t" \
  "ds_read_b64_tr_b16 a[170:171], v238 offset:4608\n\t" \
  "v_mfma_f32_16x16x32_bf16 v[68:71], a[212:215], v[84:87], v[68:71]\n\t" \
  "v_max3_f32 v192, v64, v65, v66\n\t" \
  "ds_read_b64_tr_b16 a[172:173], v238 offset:8704\n\t" \
  "v_mfma_f32_16x16x32_bf16 v[68:71], a[208:211], v[80:83], v[68:71]\n\t" \
  "v_exp_f32 v103, v103\n\t" \
  "v_mfma_f32_16x16x32_bf16 v[72:75], a[224:227], v[80:83], v[184:187]\n\t" \
  "v_cvt_pk_bf16_f32 v155, v102, v103\n\t" \
  "ds_read_b64_tr_b16 a[174:175], v238 offset:12800\n\t" \
  "v_mfma_f32_16x16x32_bf16 v[72:75], a[228:231], v[84:87], v[72:75]\n\t" \
  "v_exp_f32 v104, v104\n\t" \
  "v_mfma_f32_16x16x32_bf16 v[72:75], a[232:235], v[88:91], v[72:75]\n\t" \
  "s_lshr_b32 s98, %[hl18], 18\n\t" \
  "v_xor_b32 v245, s98, v233\n\t" \
  "v_mfma_f32_16x16x32_bf16 v[72:75], a[236:239], v[92:95], v[72:75]\n\t" \
  "v_max3_f32 v203, v67, v68, v69\n\t" \
  "v_max3_f32 v192, v192, v70, v71\n\t" \
  "v_mfma_f32_16x16x32_bf16 v[76:79], a[252:255], v[92:95], v[184:187]\n\t" \
  "ds_read_b64_tr_b16 a[176:177], v237 offset:768\n\t" \
  "ds_read_b64_tr_b16 a[178:179], v237 offset:4864\n\t" \
  "v_mfma_f32_16x16x32_bf16 v[76:79], a[248:251], v[88:91], v[76:79]\n\t" \
  "v_exp_f32 v105, v105\n\t" \
  "v_mfma_f32_16x16x32_bf16 v[76:79], a[244:247], v[84:87], v[76:79]\n\t" \
  "v_cvt_pk_bf16_f32 v156, v104, v105\n\t" \
  "v_max3_f32 v203, v203, v72, v73\n\t" \
  "v_mfma_f32_16x16x32_bf16 v[76:79], a[240:243], v[80:83], v[76:79]\n\t" \
  "v_max3_f32 v192, v192, v74, v75\n\t" \
  "v_and_b32 v245, 31, v245\n\t" \
  "v_mfma_f32_16x16x32_bf16 v[96:99], a[192:195], v[112:115], v[188:191]\n\t" \
  "ds_read_b64_tr_b16 a[180:181], v237 offset:8960\n\t" \
  "ds_read_b64_tr_b16 a[182:183], v237 offset:13056\n\t" \
  "v_mfma_f32_16x16x32_bf16 v[96:99], a[196:199], v[116:119], v[96:99]\n\t" \
  "v_exp_f32 v106, v106\n\t" \
  "v_mfma_f32_16x16x32_bf16 v[96:99], a[200:203], v[120:123], v[96:99]\n\t" \
  "v_max3_f32 v203, v203, v76, v77\n\t" \
  "v_max3_f32 v192, v192, v78, v79\n\t" \
  "v_mfma_f32_16x16x32_bf16 v[96:99], a[204:207], v[124:127], v[96:99]\n\t" \
  "v_max_f32 v192, v192, v203\n\t" \
  "ds_read_b64_tr_b16 a[184:185], v238 offset:768\n\t" \
  "v_mfma_f32_16x16x32_bf16 v[100:103], a[220:223], v[124:127], v[188:191]\n\t" \
  "v_cmp_lt_f32 vcc, 0x42800000, v192\n\t" \
  "ds_read_b64_tr_b16 a[186:187], v238 offset:4864\n\t" \
  "v_mfma_f32_16x16x32_bf16 v[100:103], a[216:219], v[120:123], v[100:103]\n\t" \
  "s_cbranch_vccnz .Lsf_c62\n\t" \
  ".Lsf_r62:\n\t" \
  "ds_read_b64_tr_b16 a[188:189], v238 offset:8960\n\t" \
  "v_mfma_f32_16x16x32_bf16 v[100:103], a[212:215], v[116:119], v[100:103]\n\t" \
  "s_lshr_b32 s98, %[hl18], 18\n\t" \
  "s_mul_i32 s98, s98, 127\n\t" \
  "v_add_lshl_u32 v245, v245, s98, 2\n\t" \
  "v_mfma_f32_16x16x32_bf16 v[100:103], a[208:211], v[112:115], v[100:103]\n\t" \
  "v_max3_f32 v192, v96, v97, v98\n\t" \
  "ds_read_b64_tr_b16 a[190:191], v238 offset:13056\n\t" \
  "v_exp_f32 v107, v107\n\t" \
  "s_nop 0\n\t" \
  "v_cvt_pk_bf16_f32 v157, v106, v107\n\t" \
  "v_mfma_f32_16x16x32_bf16 v[104:107], a[224:227], v[112:115], v[188:191]\n\t" \
  "v_exp_f32 v108, v108\n\t" \
  "v_mfma_f32_16x16x32_bf16 v[104:107], a[228:231], v[116:119], v[104:107]\n\t" \
  "v_exp_f32 v109, v109\n\t" \
  "v_mfma_f32_16x16x32_bf16 v[104:107], a[232:235], v[120:123], v[104:107]\n\t" \
  "v_cvt_pk_bf16_f32 v158, v108, v109\n\t" \
  "v_max3_f32 v203, v99, v100, v101\n\t" \
  "v_mfma_f32_16x16x32_bf16 v[104:107], a[236:239], v[124:127], v[104:107]\n\t" \
  "v_max3_f32 v192, v192, v102, v103\n\t" \
  "v_exp_f32 v110, v110\n\t" \
  "v_exp_f32 v111, v111\n\t" \
  "s_nop 0\n\t" \
  "v_cvt_pk_bf16_f32 v159, v110, v111\n\t" \
  "v_mfma_f32_16x16x32_bf16 v[108:111], a[252:255], v[124:127], v[188:191]\n\t" \
  "v_exp_f32 v0, v0\n\t" \
  "v_mfma_f32_16x16x32_bf16 v[108:111], a[248:251], v[120:123], v[108:111]\n\t" \
  "v_exp_f32 v1, v1\n\t" \
  "v_mfma_f32_16x16x32_bf16 v[108:111], a[244:247], v[116:119], v[108:111]\n\t" \
  "v_max3_f32 v203, v203, v104, v105\n\t" \
  "v_max3_f32 v192, v192, v106, v107\n\t" \
  "v_mfma_f32_16x16x32_bf16 v[108:111], a[240:243], v[112:115], v[108:111]\n\t" \
  "v_exp_f32 v2, v2\n\t" \
  "s_waitcnt vmcnt(4) lgkmcnt(0)\n\t" \
  "s_barrier\n\t" \
  "v_mfma_f32_16x16x32_bf16 a[0:3], a[128:131], v[128:131], a[0:3]\n\t" \
  "v_exp_f32 v3, v3\n\t" \
  "v_mfma_f32_16x16x32_bf16 a[0:3], a[132:135], v[132:135], a[0:3]\n\t" \
  "ds_read_b128 a[192:195], v220 offset:16384\n\t" \
  "ds_read_b128 a[196:199], v221 offset:16384\n\t" \
  "v_mfma_f32_16x16x32_bf16 a[16:19], a[136:139], v[128:131], a[16:19]\n\t" \
  "v_max3_f32 v203, v203, v108, v109\n\t" \
  "v_max3_f32 v192, v192, v110, v111\n\t" \
  "v_mfma_f32_16x16x32_bf16 a[16:19], a[140:143], v[132:135], a[16:19]\n\t" \
  "v_max_f32 v192, v192, v203\n\t" \
  "ds_read_b128 a[200:203], v222 offset:16384\n\t" \
  "v_mfma_f32_16x16x32_bf16 a[32:35], a[144:147], v[128:131], a[32:35]\n\t" \
  "v_cmp_lt_f32 vcc, 0x42800000, v192\n\t" \
  "ds_read_b128 a[204:207], v223 offset:16384\n\t" \
  "v_mfma_f32_16x16x32_bf16 a[32:35], a[148:151], v[132:135], a[32:35]\n\t" \
  "s_cbranch_vccnz .Lsf_c63\n\t" \
  ".Lsf_r63:\n\t" \
  "ds_read_b128 a[208:211], v220 offset:20480\n\t" \
  "v_mfma_f32_16x16x32_bf16 a[48:51], a[152:155], v[128:131], a[48:51]\n\t" \
  "s_waitcnt vmcnt(3)\n\t" \
  "v_and_b32 v206, 0xbfffbfff, v209\n\t" \
  "v_cmp_ne_u32_e64 s[96:97], %[tag], v206\n\t" \
  "s_cmp_lg_u64 s[96:97], 0\n\t" \
  "s_cbranch_scc1 .Lsf_v\n\t" \
  ".Lsf_w:\n\t" \
  "v_mfma_f32_16x16x32_bf16 a[48:51], a[156:159], v[132:135], a[48:51]\n\t" \
  "v_and_b32 v206, 1, v233\n\t" \
  "v_lshlrev_b32 v206, 4, v206\n\t" \
  "v_mfma_f32_16x16x32_bf16 a[64:67], a[160:163], v[128:131], a[64:67]\n\t" \
  "v_lshrrev_b32 v206, v206, v209\n\t" \
  "v_and_b32 v206, 0x4000, v206\n\t" \
  "v_mfma_f32_16x16x32_bf16 a[64:67], a[164:167], v[132:135], a[64:67]\n\t" \
  "v_cmp_ne_u32_e64 s[96:97], 0, v206\n\t" \
  "s_bcnt1_i32_b32 s98, s96\n\t" \
  "s_bcnt1_i32_b32 %[na], s97\n\t" \
  "s_cmp_gt_u32 s98, %[na]\n\t" \
  "s_cselect_b32 %[nb], 0x2000, 0\n\t" \
  "s_cselect_b32 s98, s96, s97\n\t" \
  "s_cselect_b32 s96, s97, s96\n\t" \
  "v_mfma_f32_16x16x32_bf16 a[80:83], a[168:171], v[128:131], a[80:83]\n\t" \
  "s_lshr_b32 s97, s98, 16\n\t" \
  "s_bcnt1_i32_b32 s97, s97\n\t" \
  "v_mfma_f32_16x16x32_bf16 a[80:83], a[172:175], v[132:135], a[80:83]\n\t" \
  "s_lshr_b32 s96, s96, 16\n\t" \
  "s_bcnt1_i32_b32 s96, s96\n\t" \
  "v_mfma_f32_16x16x32_bf16 a[96:99], a[176:179], v[128:131], a[96:99]\n\t" \
  "s_add_u32 s96, s96, 7\n\t" \
  "s_cmp_ge_u32 s97, s96\n\t" \
  "s_cselect_b32 %[na], -1, 0\n\t" \
  "v_mfma_f32_16x16x32_bf16 a[96:99], a[180:183], v[132:135], a[96:99]\n\t" \
  "s_xor_b32 s98, %[po], %[nb]\n\t" \
  "s_and_b32 s98, s98, %[na]\n\t" \
  "s_xor_b32 %[eso], %[eso], s98\n\t" \
  "v_mfma_f32_16x16x32_bf16 a[112:115], a[184:187], v[128:131], a[112:115]\n\t" \
  "s_xor_b32 s98, %[px], %[nb]\n\t" \
  "s_and_b32 s98, s98, %[na]\n\t" \
  "s_xor_b32 %[xcb], %[xcb], s98\n\t" \
  "v_mfma_f32_16x16x32_bf16 a[112:115], a[188:191], v[132:135], a[112:115]\n\t" \
  "s_xor_b32 %[frb], %[frb], %[nb]\n\t" \
  "s_xor_b32 %[fw2], %[fw2], %[nb]\n\t" \
  "v_mfma_f32_16x16x32_bf16 v[160:163], v[216:219], v[128:131], v[160:163]\n\t" \
  "v_exp_f32 v4, v4\n\t" \
  "v_mfma_f32_16x16x32_bf16 v[160:163], v[216:219], v[132:135], v[160:163]\n\t" \
  "v_cvt_pk_bf16_f32 v128, v0, v1\n\t" \
  "v_cvt_pk_bf16_f32 v129, v2, v3\n\t" \
  "v_mfma_f32_16x16x32_bf16 a[4:7], a[128:131], v[136:139], a[4:7]\n\t" \
  "v_exp_f32 v5, v5\n\t" \
  "v_mfma_f32_16x16x32_bf16 a[4:7], a[132:135], v[140:143], a[4:7]\n\t" \
  "v_cvt_pk_bf16_f32 v130, v4, v5\n\t" \
  "ds_read_b128 a[212:215], v221 offset:20480\n\t" \
  "v_mfma_f32_16x16x32_bf16 a[20:23], a[136:139], v[136:139], a[20:23]\n\t" \
  "v_exp_f32 v6, v6\n\t" \
  "v_mfma_f32_16x16x32_bf16 a[20:23], a[140:143], v[140:143], a[20:23]\n\t" \
  "v_exp_f32 v7, v7\n\t" \
  "v_mfma_f32_16x16x32_bf16 a[36:39], a[144:147], v[136:139], a[36:39]\n\t" \
  "v_cvt_pk_bf16_f32 v131, v6, v7\n\t" \
  "s_add_u32 m0, %[m], 147456\n\t" \
  "v_mfma_f32_16x16x32_bf16 a[36:39], a[148:151], v[140:143], a[36:39]\n\t" \
  "v_exp_f32 v8, v8\n\t" \
  "v_mfma_f32_16x16x32_bf16 a[52:55], a[152:155], v[136:139], a[52:55]\n\t" \
  "ds_read_b128 a[216:219], v222 offset:20480\n\t" \
  "s_add_u32 s96, %[sofk], 114688\n\t" \
  "v_mfma_f32_16x16x32_bf16 a[52:55], a[156:159], v[140:143], a[52:55]\n\t" \
  "v_exp_f32 v9, v9\n\t" \
  "v_mfma_f32_16x16x32_bf16 a[68:71], a[160:163], v[136:139], a[68:71]\n\t" \
  "v_cvt_pk_bf16_f32 v132, v8, v9\n\t" \
  "ds_read_b128 a[220:223], v223 offset:20480\n\t" \
  "v_mfma_f32_16x16x32_bf16 a[68:71], a[164:167], v[140:143], a[68:71]\n\t" \
  "v_exp_f32 v10, v10\n\t" \
  "v_mfma_f32_16x16x32_bf16 a[84:87], a[168:171], v[136:139], a[84:87]\n\t" \
  "v_exp_f32 v11, v11\n\t" \
  "v_mfma_f32_16x16x32_bf16 a[84:87], a[172:175], v[140:143], a[84:87]\n\t" \
  "v_cvt_pk_bf16_f32 v133, v10, v11\n\t" \
  "buffer_load_dwordx4 v230, %[sV], s96 offen offset:0 lds\n\t" \
  "v_mfma_f32_16x16x32_bf16 a[100:103], a[176:179], v[136:139], a[100:103]\n\t" \
  "v_exp_f32 v12, v12\n\t" \
  "v_mfma_f32_16x16x32_bf16 a[100:103], a[180:183], v[140:143], a[100:103]\n\t" \
  "ds_read_b128 a[224:227], v220 offset:24576\n\t" \
  "buffer_load_dwordx4 v231, %[sV], s96 offen offset:1024 lds\n\t" \
  "v_mfma_f32_16x16x32_bf16 a[116:119], a[184:187], v[136:139], a[116:119]\n\t" \
  "v_exp_f32 v13, v13\n\t" \
  "v_mfma_f32_16x16x32_bf16 a[116:119], a[188:191], v[140:143], a[116:119]\n\t" \
  "v_cvt_pk_bf16_f32 v134, v12, v13\n\t" \
  "buffer_load_dwordx4 v230, %[sV], s96 offen offset:2048 lds\n\t" \
  "v_mfma_f32_16x16x32_bf16 v[164:167], v[216:219], v[136:139], v[164:167]\n\t" \
  "v_exp_f32 v14, v14\n\t" \
  "v_mfma_f32_16x16x32_bf16 v[164:167], v[216:219], v[140:143], v[164:167]\n\t" \
  "v_exp_f32 v15, v15\n\t" \
  "v_mfma_f32_16x16x32_bf16 a[8:11], a[128:131], v[144:147], a[8:11]\n\t" \
  "v_cvt_pk_bf16_f32 v135, v14, v15\n\t" \
  "ds_read_b128 a[228:231], v221 offset:24576\n\t" \
  "v_mfma_f32_16x16x32_bf16 a[8:11], a[132:135], v[148:151], a[8:11]\n\t" \
  "v_exp_f32 v32, v32\n\t" \
  "v_mfma_f32_16x16x32_bf16 a[24:27], a[136:139], v[144:147], a[24:27]\n\t" \
  "buffer_load_dwordx4 v231, %[sV], s96 offen offset:3072 lds\n\t" \
  "ds_read_b128 a[232:235], v222 offset:24576\n\t" \
  "v_mfma_f32_16x16x32_bf16 a[24:27], a[140:143], v[148:151], a[24:27]\n\t" \
  "v_exp_f32 v33, v33\n\t" \
  "v_mfma_f32_16x16x32_bf16 a[40:43], a[144:147], v[144:147], a[40:43]\n\t" \
  "v_cvt_pk_bf16_f32 v136, v32, v33\n\t" \
  "s_add_u32 m0, %[m], 49152\n\t" \
  "v_mfma_f32_16x16x32_bf16 a[40:43], a[148:151], v[148:151], a[40:43]\n\t" \
  "v_exp_f32 v34, v34\n\t" \
  "v_mfma_f32_16x16x32_bf16 a[56:59], a[152:155], v[144:147], a[56:59]\n\t" \
  "v_exp_f32 v35, v35\n\t" \
  "v_mfma_f32_16x16x32_bf16 a[56:59], a[156:159], v[148:151], a[56:59]\n\t" \
  "v_cvt_pk_bf16_f32 v137, v34, v35\n\t" \
  "ds_read_b128 a[236:239], v223 offset:24576\n\t" \
  "v_mfma_f32_16x16x32_bf16 a[72:75], a[160:163], v[144:147], a[72:75]\n\t" \
  "v_exp_f32 v36, v36\n\t" \
  "v_mfma_f32_16x16x32_bf16 a[72:75], a[164:167], v[148:151], a[72:75]\n\t" \
  "s_xor_b32 s97, %[wob], 262144\n\t" \
  "ds_read_b128 a[240:243], v220 offset:28672\n\t" \
  "v_mfma_f32_16x16x32_bf16 a[88:91], a[168:171], v[144:147], a[88:91]\n\t" \
  "v_exp_f32 v37, v37\n\t" \
  "v_mfma_f32_16x16x32_bf16 a[88:91], a[172:175], v[148:151], a[88:91]\n\t" \
  "v_cvt_pk_bf16_f32 v138, v36, v37\n\t" \
  "buffer_load_dwordx4 v240, %[wo], s97 offen offset:0 lds\n\t" \
  "v_mfma_f32_16x16x32_bf16 a[104:107], a[176:179], v[144:147], a[104:107]\n\t" \
  "v_exp_f32 v38, v38\n\t" \
  "v_mfma_f32_16x16x32_bf16 a[104:107], a[180:183], v[148:151], a[104:107]\n\t" \
  "v_exp_f32 v39, v39\n\t" \
  "v_mfma_f32_16x16x32_bf16 a[120:123], a[184:187], v[144:147], a[120:123]\n\t" \
  "v_cvt_pk_bf16_f32 v139, v38, v39\n\t" \
  "ds_read_b128 a[244:247], v221 offset:28672\n\t" \
  "v_mfma_f32_16x16x32_bf16 a[120:123], a[188:191], v[148:151], a[120:123]\n\t" \
  "v_exp_f32 v40, v40\n\t" \
  "v_mfma_f32_16x16x32_bf16 v[168:171], v[216:219], v[144:147], v[168:171]\n\t" \
  "v_exp_f32 v41, v41\n\t" \
  "v_mfma_f32_16x16x32_bf16 v[168:171], v[216:219], v[148:151], v[168:171]\n\t" \
  "v_cvt_pk_bf16_f32 v140, v40, v41\n\t" \
  "ds_read_b128 a[248:251], v222 offset:28672\n\t" \
  "v_mfma_f32_16x16x32_bf16 a[12:15], a[128:131], v[152:155], a[12:15]\n\t" \
  "s_add_u32 s97, s97, 7168\n\t" \
  "buffer_load_dwordx4 v241, %[wo], s97 offen offset:1024 lds\n\t" \
  "v_mfma_f32_16x16x32_bf16 a[12:15], a[132:135], v[156:159], a[12:15]\n\t" \
  "v_exp_f32 v42, v42\n\t" \
  "v_mfma_f32_16x16x32_bf16 a[28:31], a[136:139], v[152:155], a[28:31]\n\t" \
  "v_exp_f32 v43, v43\n\t" \
  "v_mfma_f32_16x16x32_bf16 a[28:31], a[140:143], v[156:159], a[28:31]\n\t" \
  "v_cvt_pk_bf16_f32 v141, v42, v43\n\t" \
  "ds_read_b128 a[252:255], v223 offset:28672\n\t" \
  "v_mfma_f32_16x16x32_bf16 a[44:47], a[144:147], v[152:155], a[44:47]\n\t" \
  "s_add_u32 s97, s97, 7168\n\t" \
  "buffer_load_dwordx4 v240, %[wo], s97 offen offset:2048 lds\n\t" \
  "v_mfma_f32_16x16x32_bf16 a[44:47], a[148:151], v[156:159], a[44:47]\n\t" \
  "v_exp_f32 v44, v44\n\t" \
  "v_mfma_f32_16x16x32_bf16 a[60:63], a[152:155], v[152:155], a[60:63]\n\t" \
  "s_add_u32 s97, s97, 7168\n\t" \
  "buffer_load_dwordx4 v241, %[wo], s97 offen offset:3072 lds\n\t" \
  "v_mfma_f32_16x16x32_bf16 a[60:63], a[156:159], v[156:159], a[60:63]\n\t" \
  "v_exp_f32 v45, v45\n\t" \
  "v_mfma_f32_16x16x32_bf16 a[76:79], a[160:163], v[152:155], a[76:79]\n\t" \
  "v_cvt_pk_bf16_f32 v142, v44, v45\n\t" \
  "v_mfma_f32_16x16x32_bf16 a[76:79], a[164:167], v[156:159], a[76:79]\n\t" \
  "v_exp_f32 v46, v46\n\t" \
  "v_mfma_f32_16x16x32_bf16 a[92:95], a[168:171], v[152:155], a[92:95]\n\t" \
  "s_add_u32 m0, %[m], 98304\n\t" \
  "s_mov_b32 s96, 40960\n\t" \
  "buffer_load_dwordx4 v232, %[sG], s96 offen offset:2048 lds\n\t" \
  "v_mfma_f32_16x16x32_bf16 a[92:95], a[172:175], v[156:159], a[92:95]\n\t" \
  "v_exp_f32 v47, v47\n\t" \
  "v_mfma_f32_16x16x32_bf16 a[108:111], a[176:179], v[152:155], a[108:111]\n\t" \
  "v_cvt_pk_bf16_f32 v143, v46, v47\n\t" \
  "v_mfma_f32_16x16x32_bf16 a[108:111], a[180:183], v[156:159], a[108:111]\n\t" \
  "s_mov_b32 s96, 47104\n\t" \
  "buffer_load_dwordx4 v232, %[sG], s96 offen offset:3072 lds\n\t" \
  "v_mfma_f32_16x16x32_bf16 a[124:127], a[184:187], v[152:155], a[124:127]\n\t" \
  "v_exp_f32 v64, v64\n\t" \
  "v_mfma_f32_16x16x32_bf16 a[124:127], a[188:191], v[156:159], a[124:127]\n\t" \
  "v_exp_f32 v65, v65\n\t" \
  "v_mfma_f32_16x16x32_bf16 v[172:175], v[216:219], v[152:155], v[172:175]\n\t" \
  "v_cvt_pk_bf16_f32 v144, v64, v65\n\t" \
  "v_mfma_f32_16x16x32_bf16 v[172:175], v[216:219], v[156:159], v[172:175]\n\t" \
  "v_exp_f32 v66, v66\n\t" \
  "s_cmp_lg_u32 s99, 0\n\t" \
  "s_cbranch_scc1 .Lsf_p6\n\t" \
  ".Lsf_q6:\n\t" \
  "s_waitcnt lgkmcnt(0)\n\t" \
  "s_barrier\n\t" \
  "v_mfma_f32_16x16x32_bf16 v[0:3], a[192:195], v[16:19], v[176:179]\n\t" \
  "v_exp_f32 v67, v67\n\t" \
  "v_mfma_f32_16x16x32_bf16 v[0:3], a[196:199], v[20:23], v[0:3]\n\t" \
  "v_cvt_pk_bf16_f32 v145, v66, v67\n\t" \
  "ds_read_b64_tr_b16 a[128:129], v228 offset:0\n\t" \
  "v_mfma_f32_16x16x32_bf16 v[0:3], a[200:203], v[24:27], v[0:3]\n\t" \
  "v_exp_f32 v68, v68\n\t" \
  "v_mfma_f32_16x16x32_bf16 v[0:3], a[204:207], v[28:31], v[0:3]\n\t" \
  "v_exp_f32 v69, v69\n\t" \
  "v_mfma_f32_16x16x32_bf16 v[4:7], a[220:223], v[28:31], v[176:179]\n\t" \
  "v_cvt_pk_bf16_f32 v146, v68, v69\n\t" \
  "ds_read_b64_tr_b16 a[130:131], v228 offset:4096\n\t" \
  "v_mfma_f32_16x16x32_bf16 v[4:7], a[216:219], v[24:27], v[4:7]\n\t" \
  "ds_read_b64_tr_b16 a[132:133], v228 offset:8192\n\t" \
  "s_add_u32 m0, %[m], 65536\n\t" \
  "v_mfma_f32_16x16x32_bf16 v[4:7], a[212:215], v[20:23], v[4:7]\n\t" \
  "v_max3_f32 v192, v0, v1, v2\n\t" \
  "ds_read_b64_tr_b16 a[134:135], v228 offset:12288\n\t" \
  "v_mfma_f32_16x16x32_bf16 v[4:7], a[208:211], v[16:19], v[4:7]\n\t" \
  "v_exp_f32 v70, v70\n\t" \
  "v_mfma_f32_16x16x32_bf16 v[8:11], a[224:227], v[16:19], v[176:179]\n\t" \
  "ds_read_b64_tr_b16 a[136:137], v229 offset:0\n\t" \
  "s_xor_b32 s97, %[wob], 393216\n\t" \
  "v_mfma_f32_16x16x32_bf16 v[8:11], a[228:231], v[20:23], v[8:11]\n\t" \
  "v_exp_f32 v71, v71\n\t" \
  "v_mfma_f32_16x16x32_bf16 v[8:11], a[232:235], v[24:27], v[8:11]\n\t" \
  "v_cvt_pk_bf16_f32 v147, v70, v71\n\t" \
  "v_max3_f32 v203, v3, v4, v5\n\t" \
  "v_mfma_f32_16x16x32_bf16 v[8:11], a[236:239], v[28:31], v[8:11]\n\t" \
  "v_max3_f32 v192, v192, v6, v7\n\t" \
  "ds_read_b64_tr_b16 a[138:139], v229 offset:4096\n\t" \
  "v_mfma_f32_16x16x32_bf16 v[12:15], a[252:255], v[28:31], v[176:179]\n\t" \
  "ds_read_b64_tr_b16 a[140:141], v229 offset:8192\n\t" \
  "buffer_load_dwordx4 v240, %[wo], s97 offen offset:0 lds\n\t" \
  "v_mfma_f32_16x16x32_bf16 v[12:15], a[248:251], v[24:27], v[12:15]\n\t" \
  "v_exp_f32 v72, v72\n\t" \
  "v_mfma_f32_16x16x32_bf16 v[12:15], a[244:247], v[20:23], v[12:15]\n\t" \
  "v_max3_f32 v203, v203, v8, v9\n\t" \
  "v_max3_f32 v192, v192, v10, v11\n\t" \
  "v_mfma_f32_16x16x32_bf16 v[12:15], a[240:243], v[16:19], v[12:15]\n\t" \
  "ds_read_b64_tr_b16 a[142:143], v229 offset:12288\n\t" \
  "ds_read_b64_tr_b16 a[144:145], v228 offset:256\n\t" \
  "v_mfma_f32_16x16x32_bf16 v[32:35], a[192:195], v[48:51], v[180:183]\n\t" \
  "ds_read_b64_tr_b16 a[146:147], v228 offset:4352\n\t" \
  "ds_read_b64_tr_b16 a[148:149], v228 offset:8448\n\t" \
  "v_mfma_f32_16x16x32_bf16 v[32:35], a[196:199], v[52:55], v[32:35]\n\t" \
  "v_exp_f32 v73, v73\n\t" \
  "v_mfma_f32_16x16x32_bf16 v[32:35], a[200:203], v[56:59], v[32:35]\n\t" \
  "v_cvt_pk_bf16_f32 v148, v72, v73\n\t" \
  "v_max3_f32 v203, v203, v12, v13\n\t" \
  "v_mfma_f32_16x16x32_bf16 v[32:35], a[204:207], v[60:63], v[32:35]\n\t" \
  "v_max3_f32 v192, v192, v14, v15\n\t" \
  "v_max_f32 v192, v192, v203\n\t" \
  "v_mfma_f32_16x16x32_bf16 v[36:39], a[220:223], v[60:63], v[180:183]\n\t" \
  "v_cmp_lt_f32 vcc, 0x42800000, v192\n\t" \
  "ds_read_b64_tr_b16 a[150:151], v228 offset:12544\n\t" \
  "v_mfma_f32_16x16x32_bf16 v[36:39], a[216:219], v[56:59], v[36:39]\n\t" \
  "s_cbranch_vccnz .Lsf_c70\n\t" \
  ".Lsf_r70:\n\t" \
  "ds_read_b64_tr_b16 a[152:153], v229 offset:256\n\t" \
  "v_mfma_f32_16x16x32_bf16 v[36:39], a[212:215], v[52:55], v[36:39]\n\t" \
  "s_add_u32 s97, s97, 7168\n\t" \
  "buffer_load_dwordx4 v241, %[wo], s97 offen offset:1024 lds\n\t" \
  "v_mfma_f32_16x16x32_bf16 v[36:39], a[208:211], v[48:51], v[36:39]\n\t" \
  "v_max3_f32 v192, v32, v33, v34\n\t" \
  "ds_read_b64_tr_b16 a[154:155], v229 offset:4352\n\t" \
  "v_mfma_f32_16x16x32_bf16 v[40:43], a[224:227], v[48:51], v[180:183]\n\t" \
  "s_add_u32 s97, s97, 7168\n\t" \
  "buffer_load_dwordx4 v240, %[wo], s97 offen offset:2048 lds\n\t" \
  "v_mfma_f32_16x16x32_bf16 v[40:43], a[228:231], v[52:55], v[40:43]\n\t" \
  "s_add_u32 s97, s97, 7168\n\t" \
  "buffer_load_dwordx4 v241, %[wo], s97 offen offset:3072 lds\n\t" \
  "v_mfma_f32_16x16x32_bf16 v[40:43], a[232:235], v[56:59], v[40:43]\n\t" \
  "v_max3_f32 v203, v35, v36, v37\n\t" \
  "v_max3_f32 v192, v192, v38, v39\n\t" \
  "v_mfma_f32_16x16x32_bf16 v[40:43], a[236:239], v[60:63], v[40:43]\n\t" \
  "ds_read_b64_tr_b16 a[156:157], v229 offset:8448\n\t" \
  "ds_read_b64_tr_b16 a[158:159], v229 offset:12544\n\t" \
  "v_mfma_f32_16x16x32_bf16 v[44:47], a[252:255], v[60:63], v[180:183]\n\t" \
  "v_exp_f32 v74, v74\n\t" \
  "v_mfma_f32_16x16x32_bf16 v[44:47], a[248:251], v[56:59], v[44:47]\n\t" \
  "v_exp_f32 v75, v75\n\t" \
  "v_mfma_f32_16x16x32_bf16 v[44:47], a[244:247], v[52:55], v[44:47]\n\t" \
  "v_cvt_pk_bf16_f32 v149, v74, v75\n\t" \
  "ds_read_b64_tr_b16 a[160:161], v228 offset:512\n\t" \
  "v_mfma_f32_16x16x32_bf16 v[44:47], a[240:243], v[48:51], v[44:47]\n\t" \
  "v_max3_f32 v203, v203, v40, v41\n\t" \
  "v_max3_f32 v192, v192, v42, v43\n\t" \
  "v_mfma_f32_16x16x32_bf16 v[64:67], a[192:195], v[80:83], v[184:187]\n\t" \
  "ds_read_b64_tr_b16 a[162:163], v228 offset:4608\n\t" \
  "ds_read_b64_tr_b16 a[164:165], v228 offset:8704\n\t" \
  "v_mfma_f32_16x16x32_bf16 v[64:67], a[196:199], v[84:87], v[64:67]\n\t" \
  "v_exp_f32 v76, v76\n\t" \
  "v_mfma_f32_16x16x32_bf16 v[64:67], a[200:203], v[88:91], v[64:67]\n\t" \
  "v_max3_f32 v203, v203, v44, v45\n\t" \
  "v_max3_f32 v192, v192, v46, v47\n\t" \
  "v_mfma_f32_16x16x32_bf16 v[64:67], a[204:207], v[92:95], v[64:67]\n\t" \
  "v_max_f32 v192, v192, v203\n\t" \
  "ds_read_b64_tr_b16 a[166:167], v228 offset:12800\n\t" \
  "v_mfma_f32_16x16x32_bf16 v[68:71], a[220:223], v[92:95], v[184:187]\n\t" \
  "v_cmp_lt_f32 vcc, 0x42800000, v192\n\t" \
  "ds_read_b64_tr_b16 a[168:169], v229 offset:512\n\t" \
  "v_mfma_f32_16x16x32_bf16 v[68:71], a[216:219], v[88:91], v[68:71]\n\t" \
  "s_cbranch_vccnz .Lsf_c71\n\t" \
  ".Lsf_r71:\n\t" \
  "ds_read_b64_tr_b16 a[170:171], v229 offset:4608\n\t" \
  "v_mfma_f32_16x16x32_bf16 v[68:71], a[212:215], v[84:87], v[68:71]\n\t" \
  "v_max3_f32 v192, v64, v65, v66\n\t" \
  "ds_read_b64_tr_b16 a[172:173], v229 offset:8704\n\t" \
  "v_mfma_f32_16x16x32_bf16 v[68:71], a[208:211], v[80:83], v[68:71]\n\t" \
  "v_exp_f32 v77, v77\n\t" \
  "v_mfma_f32_16x16x32_bf16 v[72:75], a[224:227], v[80:83], v[184:187]\n\t" \
  "v_cvt_pk_bf16_f32 v150, v76, v77\n\t" \
  "ds_read_b64_tr_b16 a[174:175], v229 offset:12800\n\t" \
  "v_mfma_f32_16x16x32_bf16 v[72:75], a[228:231], v[84:87], v[72:75]\n\t" \
  "v_exp_f32 v78, v78\n\t" \
  "v_mfma_f32_16x16x32_bf16 v[72:75], a[232:235], v[88:91], v[72:75]\n\t" \
  "v_exp_f32 v79, v79\n\t" \
  "v_mfma_f32_16x16x32_bf16 v[72:75], a[236:239], v[92:95], v[72:75]\n\t" \
  "v_cvt_pk_bf16_f32 v151, v78, v79\n\t" \
  "v_max3_f32 v203, v67, v68, v69\n\t" \
  "v_mfma_f32_16x16x32_bf16 v[76:79], a[252:255], v[92:95], v[184:187]\n\t" \
  "v_max3_f32 v192, v192, v70, v71\n\t" \
  "ds_read_b64_tr_b16 a[176:177], v228 offset:768\n\t" \
  "v_mfma_f32_16x16x32_bf16 v[76:79], a[248:251], v[88:91], v[76:79]\n\t" \
  "v_exp_f32 v96, v96\n\t" \
  "v_mfma_f32_16x16x32_bf16 v[76:79], a[244:247], v[84:87], v[76:79]\n\t" \
  "ds_read_b64_tr_b16 a[178:179], v228 offset:4864\n\t" \
  "v_max3_f32 v203, v203, v72, v73\n\t" \
  "v_mfma_f32_16x16x32_bf16 v[76:79], a[240:243], v[80:83], v[76:79]\n\t" \
  "v_max3_f32 v192, v192, v74, v75\n\t" \
  "ds_read_b64_tr_b16 a[180:181], v228 offset:8960\n\t" \
  "v_exp_f32 v97, v97\n\t" \
  "v_exp_f32 v98, v98\n\t" \
  "v_exp_f32 v99, v99\n\t" \
  "v_cvt_pk_bf16_f32 v152, v96, v97\n\t" \
  "v_cvt_pk_bf16_f32 v153, v98, v99\n\t" \
  "v_mfma_f32_16x16x32_bf16 v[96:99], a[192:195], v[112:115], v[188:191]\n\t" \
  "ds_read_b64_tr_b16 a[182:183], v228 offset:13056\n\t" \
  "v_max3_f32 v203, v203, v76, v77\n\t" \
  "v_mfma_f32_16x16x32_bf16 v[96:99], a[196:199], v[116:119], v[96:99]\n\t" \
  "v_max3_f32 v192, v192, v78, v79\n\t" \
  "v_max_f32 v192, v192, v203\n\t" \
  "v_mfma_f32_16x16x32_bf16 v[96:99], a[200:203], v[120:123], v[96:99]\n\t" \
  "v_cmp_lt_f32 vcc, 0x42800000, v192\n\t" \
  "ds_read_b64_tr_b16 a[184:185], v229 offset:768\n\t" \
  "v_mfma_f32_16x16x32_bf16 v[96:99], a[204:207], v[124:127], v[96:99]\n\t" \
  "s_cbranch_vccnz .Lsf_c72\n\t" \
  ".Lsf_r72:\n\t" \
  "ds_read_b64_tr_b16 a[186:187], v229 offset:4864\n\t" \
  "v_exp_f32 v100, v100\n\t" \
  "v_exp_f32 v101, v101\n\t" \
  "v_exp_f32 v102, v102\n\t" \
  "v_exp_f32 v103, v103\n\t" \
  "v_cvt_pk_bf16_f32 v154, v100, v101\n\t" \
  "v_cvt_pk_bf16_f32 v155, v102, v103\n\t" \
  "v_mfma_f32_16x16x32_bf16 v[100:103], a[220:223], v[124:127], v[188:191]\n\t" \
  "v_exp_f32 v104, v104\n\t" \
  "v_mfma_f32_16x16x32_bf16 v[100:103], a[216:219], v[120:123], v[100:103]\n\t" \
  "v_max3_f32 v192, v96, v97, v98\n\t" \
  "ds_read_b64_tr_b16 a[188:189], v229 offset:8960\n\t" \
  "v_mfma_f32_16x16x32_bf16 v[100:103], a[212:215], v[116:119], v[100:103]\n\t" \
  "v_exp_f32 v105, v105\n\t" \
  "v_mfma_f32_16x16x32_bf16 v[100:103], a[208:211], v[112:115], v[100:103]\n\t" \
  "v_cvt_pk_bf16_f32 v156, v104, v105\n\t" \
  "ds_read_b64_tr_b16 a[190:191], v229 offset:13056\n\t" \
  "v_exp_f32 v106, v106\n\t" \
  "v_exp_f32 v107, v107\n\t" \
  "s_nop 0\n\t" \
  "v_cvt_pk_bf16_f32 v157, v106, v107\n\t" \
  "v_mfma_f32_16x16x32_bf16 v[104:107], a[224:227], v[112:115], v[188:191]\n\t" \
  "v_exp_f32 v108, v108\n\t" \
  "v_mfma_f32_16x16x32_bf16 v[104:107], a[228:231], v[116:119], v[104:107]\n\t" \
  "v_exp_f32 v109, v109\n\t" \
  "v_mfma_f32_16x16x32_bf16 v[104:107], a[232:235], v[120:123], v[104:107]\n\t" \
  "v_cvt_pk_bf16_f32 v158, v108, v109\n\t" \
  "v_max3_f32 v203, v99, v100, v101\n\t" \
  "v_mfma_f32_16x16x32_bf16 v[104:107], a[236:239], v[124:127], v[104:107]\n\t" \
  "v_max3_f32 v192, v192, v102, v103\n\t" \
  "v_exp_f32 v110, v110\n\t" \
  "v_exp_f32 v111, v111\n\t" \
  "s_nop 0\n\t" \
  "v_cvt_pk_bf16_f32 v159, v110, v111\n\t" \
  "v_mfma_f32_16x16x32_bf16 v[108:111], a[252:255], v[124:127], v[188:191]\n\t" \
  "v_exp_f32 v0, v0\n\t" \
  "v_mfma_f32_16x16x32_bf16 v[108:111], a[248:251], v[120:123], v[108:111]\n\t" \
  "v_exp_f32 v1, v1\n\t" \
  "v_mfma_f32_16x16x32_bf16 v[108:111], a[244:247], v[116:119], v[108:111]\n\t" \
  "v_max3_f32 v203, v203, v104, v105\n\t" \
  "v_max3_f32 v192, v192, v106, v107\n\t" \
  "v_mfma_f32_16x16x32_bf16 v[108:111], a[240:243], v[112:115], v[108:111]\n\t" \
  "v_exp_f32 v2, v2\n\t" \
  "s_waitcnt vmcnt(6) lgkmcnt(0)\n\t" \
  "s_barrier\n\t" \
  "v_mfma_f32_16x16x32_bf16 a[0:3], a[128:131], v[128:131], a[0:3]\n\t" \
  "ds_read_b64_tr_b16 a[192:193], v237 offset:0\n\t" \
  "s_add_u32 m0, %[m], 81920\n\t" \
  "v_mfma_f32_16x16x32_bf16 a[0:3], a[132:135], v[132:135], a[0:3]\n\t" \
  "v_exp_f32 v3, v3\n\t" \
  "v_mfma_f32_16x16x32_bf16 a[16:19], a[136:139], v[128:131], a[16:19]\n\t" \
  "v_max3_f32 v203, v203, v108, v109\n\t" \
  "v_max3_f32 v192, v192, v110, v111\n\t" \
  "v_mfma_f32_16x16x32_bf16 a[16:19], a[140:143], v[132:135], a[16:19]\n\t" \
  "v_max_f32 v192, v192, v203\n\t" \
  "ds_read_b64_tr_b16 a[194:195], v237 offset:4096\n\t" \
  "v_mfma_f32_16x16x32_bf16 a[32:35], a[144:147], v[128:131], a[32:35]\n\t" \
  "v_cmp_lt_f32 vcc, 0x42800000, v192\n\t" \
  "ds_read_b64_tr_b16 a[196:197], v237 offset:8192\n\t" \
  "v_mfma_f32_16x16x32_bf16 a[32:35], a[148:151], v[132:135], a[32:35]\n\t" \
  "s_cbranch_vccnz .Lsf_c73\n\t" \
  ".Lsf_r73:\n\t" \
  "s_xor_b32 s97, %[wob], 524288\n\t" \
  "v_mfma_f32_16x16x32_bf16 a[48:51], a[152:155], v[128:131], a[48:51]\n\t" \
  "ds_read_b64_tr_b16 a[198:199], v237 offset:12288\n\t" \
  "ds_read_b64_tr_b16 a[200:201], v238 offset:0\n\t" \
  "v_mfma_f32_16x16x32_bf16 a[48:51], a[156:159], v[132:135], a[48:51]\n\t" \
  "buffer_load_dwordx4 v240, %[wo], s97 offen offset:0 lds\n\t" \
  "ds_read_b64_tr_b16 a[202:203], v238 offset:4096\n\t" \
  "v_mfma_f32_16x16x32_bf16 a[64:67], a[160:163], v[128:131], a[64:67]\n\t" \
  "ds_read_b64_tr_b16 a[204:205], v238 offset:8192\n\t" \
  "ds_read_b64_tr_b16 a[206:207], v238 offset:12288\n\t" \
  "v_mfma_f32_16x16x32_bf16 a[64:67], a[164:167], v[132:135], a[64:67]\n\t" \
  "v_exp_f32 v4, v4\n\t" \
  "v_mfma_f32_16x16x32_bf16 a[80:83], a[168:171], v[128:131], a[80:83]\n\t" \
  "s_add_u32 s97, s97, 7168\n\t" \
  "buffer_load_dwordx4 v241, %[wo], s97 offen offset:1024 lds\n\t" \
  "v_mfma_f32_16x16x32_bf16 a[80:83], a[172:175], v[132:135], a[80:83]\n\t" \
  "ds_read_b64_tr_b16 a[208:209], v237 offset:256\n\t" \
  "ds_read_b64_tr_b16 a[210:211], v237 offset:4352\n\t" \
  "v_mfma_f32_16x16x32_bf16 a[96:99], a[176:179], v[128:131], a[96:99]\n\t" \
  "v_exp_f32 v5, v5\n\t" \
  "v_mfma_f32_16x16x32_bf16 a[96:99], a[180:183], v[132:135], a[96:99]\n\t" \
  "s_add_u32 s97, s97, 7168\n\t" \
  "buffer_load_dwordx4 v240, %[wo], s97 offen offset:2048 lds\n\t" \
  "v_mfma_f32_16x16x32_bf16 a[112:115], a[184:187], v[128:131], a[112:115]\n\t" \
  "ds_read_b64_tr_b16 a[212:213], v237 offset:8448\n\t" \
  "ds_read_b64_tr_b16 a[214:215], v237 offset:12544\n\t" \
  "v_mfma_f32_16x16x32_bf16 a[112:115], a[188:191], v[132:135], a[112:115]\n\t" \
  "s_add_u32 s97, s97, 7168\n\t" \
  "buffer_load_dwordx4 v241, %[wo], s97 offen offset:3072 lds\n\t" \
  "v_mfma_f32_16x16x32_bf16 v[160:163], v[216:219], v[128:131], v[160:163]\n\t" \
  "v_exp_f32 v6, v6\n\t" \
  "v_mfma_f32_16x16x32_bf16 v[160:163], v[216:219], v[132:135], v[160:163]\n\t" \
  "v_cvt_pk_bf16_f32 v128, v0, v1\n\t" \
  "v_cvt_pk_bf16_f32 v129, v2, v3\n\t" \
  "v_mfma_f32_16x16x32_bf16 a[4:7], a[128:131], v[136:139], a[4:7]\n\t" \
  "v_cvt_pk_bf16_f32 v130, v4, v5\n\t" \
  "ds_read_b64_tr_b16 a[216:217], v238 offset:256\n\t" \
  "v_mfma_f32_16x16x32_bf16 a[4:7], a[132:135], v[140:143], a[4:7]\n\t" \
  "ds_read_b64_tr_b16 a[218:219], v238 offset:4352\n\t" \
  "ds_read_b64_tr_b16 a[220:221], v238 offset:8448\n\t" \
  "v_mfma_f32_16x16x32_bf16 a[20:23], a[136:139], v[136:139], a[20:23]\n\t" \
  "ds_read_b64_tr_b16 a[222:223], v238 offset:12544\n\t" \
  "ds_read_b64_tr_b16 a[224:225], v237 offset:512\n\t" \
  "v_mfma_f32_16x16x32_bf16 a[20:23], a[140:143], v[140:143], a[20:23]\n\t" \
  "v_exp_f32 v7, v7\n\t" \
  "v_mfma_f32_16x16x32_bf16 a[36:39], a[144:147], v[136:139], a[36:39]\n\t" \
  "v_cvt_pk_bf16_f32 v131, v6, v7\n\t" \
  "s_add_u32 m0, %[m], 114688\n\t" \
  "v_mfma_f32_16x16x32_bf16 a[36:39], a[148:151], v[140:143], a[36:39]\n\t" \
  "ds_read_b64_tr_b16 a[226:227], v237 offset:4608\n\t" \
  "ds_read_b64_tr_b16 a[228:229], v237 offset:8704\n\t" \
  "v_mfma_f32_16x16x32_bf16 a[52:55], a[152:155], v[136:139], a[52:55]\n\t" \
  "s_xor_b32 s97, %[wob], 786432\n\t" \
  "ds_read_b64_tr_b16 a[230:231], v237 offset:12800\n\t" \
  "v_mfma_f32_16x16x32_bf16 a[52:55], a[156:159], v[140:143], a[52:55]\n\t" \
  "v_exp_f32 v8, v8\n\t" \
  "v_mfma_f32_16x16x32_bf16 a[68:71], a[160:163], v[136:139], a[68:71]\n\t" \
  "v_exp_f32 v9, v9\n\t" \
  "v_mfma_f32_16x16x32_bf16 a[68:71], a[164:167], v[140:143], a[68:71]\n\t" \
  "v_cvt_pk_bf16_f32 v132, v8, v9\n\t" \
  "ds_read_b64_tr_b16 a[232:233], v238 offset:512\n\t" \
  "v_mfma_f32_16x16x32_bf16 a[84:87], a[168:171], v[136:139], a[84:87]\n\t" \
  "v_exp_f32 v10, v10\n\t" \
  "v_mfma_f32_16x16x32_bf16 a[84:87], a[172:175], v[140:143], a[84:87]\n\t" \
  "ds_read_b64_tr_b16 a[234:235], v238 offset:4608\n\t" \
  "ds_read_b64_tr_b16 a[236:237], v238 offset:8704\n\t" \
  "v_mfma_f32_16x16x32_bf16 a[100:103], a[176:179], v[136:139], a[100:103]\n\t" \
  "v_exp_f32 v11, v11\n\t" \
  "v_mfma_f32_16x16x32_bf16 a[100:103], a[180:183], v[140:143], a[100:103]\n\t" \
  "v_cvt_pk_bf16_f32 v133, v10, v11\n\t" \
  "ds_read_b64_tr_b16 a[238:239], v238 offset:12800\n\t" \
  "v_mfma_f32_16x16x32_bf16 a[116:119], a[184:187], v[136:139], a[116:119]\n\t" \
  "ds_read_b64_tr_b16 a[240:241], v237 offset:768\n\t" \
  "ds_read_b64_tr_b16 a[242:243], v237 offset:4864\n\t" \
  "v_mfma_f32_16x16x32_bf16 a[116:119], a[188:191], v[140:143], a[116:119]\n\t" \
  "v_exp_f32 v12, v12\n\t" \
  "v_mfma_f32_16x16x32_bf16 v[164:167], v[216:219], v[136:139], v[164:167]\n\t" \
  "v_exp_f32 v13, v13\n\t" \
  "v_mfma_f32_16x16x32_bf16 v[164:167], v[216:219], v[140:143], v[164:167]\n\t" \
  "v_cvt_pk_bf16_f32 v134, v12, v13\n\t" \
  "ds_read_b64_tr_b16 a[244:245], v237 offset:8960\n\t" \
  "v_mfma_f32_16x16x32_bf16 a[8:11], a[128:131], v[144:147], a[8:11]\n\t" \
  "ds_read_b64_tr_b16 a[246:247], v237 offset:13056\n\t" \
  "ds_read_b64_tr_b16 a[248:249], v238 offset:768\n\t" \
  "v_mfma_f32_16x16x32_bf16 a[8:11], a[132:135], v[148:151], a[8:11]\n\t" \
  "v_exp_f32 v14, v14\n\t" \
  "v_mfma_f32_16x16x32_bf16 a[24:27], a[136:139], v[144:147], a[24:27]\n\t" \
  "v_exp_f32 v15, v15\n\t" \
  "v_mfma_f32_16x16x32_bf16 a[24:27], a[140:143], v[148:151], a[24:27]\n\t" \
  "v_cvt_pk_bf16_f32 v135, v14, v15\n\t" \
  "ds_read_b64_tr_b16 a[250:251], v238 offset:4864\n\t" \
  "v_mfma_f32_16x16x32_bf16 a[40:43], a[144:147], v[144:147], a[40:43]\n\t" \
  "ds_read_b64_tr_b16 a[252:253], v238 offset:8960\n\t" \
  "ds_read_b64_tr_b16 a[254:255], v238 offset:13056\n\t" \
  "v_mfma_f32_16x16x32_bf16 a[40:43], a[148:151], v[148:151], a[40:43]\n\t" \
  "v_exp_f32 v32, v32\n\t" \
  "v_mfma_f32_16x16x32_bf16 a[56:59], a[152:155], v[144:147], a[56:59]\n\t" \
  "v_xor_b32 v235, 64, v234\n\t" \
  "ds_read_b64_tr_b16 v[48:49], v235 offset:0\n\t" \
  "v_mfma_f32_16x16x32_bf16 a[56:59], a[156:159], v[148:151], a[56:59]\n\t" \
  "v_exp_f32 v33, v33\n\t" \
  "v_mfma_f32_16x16x32_bf16 a[72:75], a[160:163], v[144:147], a[72:75]\n\t" \
  "v_cvt_pk_bf16_f32 v136, v32, v33\n\t" \
  "ds_read_b64_tr_b16 v[50:51], v235 offset:4096\n\t" \
  "v_mfma_f32_16x16x32_bf16 a[72:75], a[164:167], v[148:151], a[72:75]\n\t" \
  "v_exp_f32 v34, v34\n\t" \
  "v_mfma_f32_16x16x32_bf16 a[88:91], a[168:171], v[144:147], a[88:91]\n\t" \
  "v_exp_f32 v35, v35\n\t" \
  "v_mfma_f32_16x16x32_bf16 a[88:91], a[172:175], v[148:151], a[88:91]\n\t" \
  "v_cvt_pk_bf16_f32 v137, v34, v35\n\t" \
  "ds_read_b64_tr_b16 v[52:53], v235 offset:8192\n\t" \
  "v_mfma_f32_16x16x32_bf16 a[104:107], a[176:179], v[144:147], a[104:107]\n\t" \
  "v_exp_f32 v36, v36\n\t" \
  "v_mfma_f32_16x16x32_bf16 a[104:107], a[180:183], v[148:151], a[104:107]\n\t" \
  "v_exp_f32 v37, v37\n\t" \
  "v_mfma_f32_16x16x32_bf16 a[120:123], a[184:187], v[144:147], a[120:123]\n\t" \
  "v_cvt_pk_bf16_f32 v138, v36, v37\n\t" \
  "ds_read_b64_tr_b16 v[54:55], v235 offset:12288\n\t" \
  "v_mfma_f32_16x16x32_bf16 a[120:123], a[188:191], v[148:151], a[120:123]\n\t" \
  "v_exp_f32 v38, v38\n\t" \
  "v_mfma_f32_16x16x32_bf16 v[168:171], v[216:219], v[144:147], v[168:171]\n\t" \
  "v_exp_f32 v39, v39\n\t" \
  "v_mfma_f32_16x16x32_bf16 v[168:171], v[216:219], v[148:151], v[168:171]\n\t" \
  "v_cvt_pk_bf16_f32 v139, v38, v39\n\t" \
  "ds_read_b64_tr_b16 v[56:57], v235 offset:16384\n\t" \
  "v_mfma_f32_16x16x32_bf16 a[12:15], a[128:131], v[152:155], a[12:15]\n\t" \
  "v_exp_f32 v40, v40\n\t" \
  "v_mfma_f32_16x16x32_bf16 a[12:15], a[132:135], v[156:159], a[12:15]\n\t" \
  "v_exp_f32 v41, v41\n\t" \
  "v_mfma_f32_16x16x32_bf16 a[28:31], a[136:139], v[152:155], a[28:31]\n\t" \
  "v_cvt_pk_bf16_f32 v140, v40, v41\n\t" \
  "ds_read_b64_tr_b16 v[58:59], v235 offset:20480\n\t" \
  "v_mfma_f32_16x16x32_bf16 a[28:31], a[140:143], v[156:159], a[28:31]\n\t" \
  "v_exp_f32 v42, v42\n\t" \
  "v_mfma_f32_16x16x32_bf16 a[44:47], a[144:147], v[152:155], a[44:47]\n\t" \
  "ds_read_b64_tr_b16 v[60:61], v235 offset:24576\n\t" \
  "ds_read_b64_tr_b16 v[62:63], v235 offset:28672\n\t" \
  "v_mfma_f32_16x16x32_bf16 a[44:47], a[148:151], v[156:159], a[44:47]\n\t" \
  "v_exp_f32 v43, v43\n\t" \
  "v_mfma_f32_16x16x32_bf16 a[60:63], a[152:155], v[152:155], a[60:63]\n\t" \
  "v_cvt_pk_bf16_f32 v141, v42, v43\n\t" \
  "v_xor_b32 v236, 96, v234\n\t" \
  "v_mfma_f32_16x16x32_bf16 a[60:63], a[156:159], v[156:159], a[60:63]\n\t" \
  "v_exp_f32 v44, v44\n\t" \
  "v_mfma_f32_16x16x32_bf16 a[76:79], a[160:163], v[152:155], a[76:79]\n\t" \
  "v_exp_f32 v45, v45\n\t" \
  "v_mfma_f32_16x16x32_bf16 a[76:79], a[164:167], v[156:159], a[76:79]\n\t" \
  "v_cvt_pk_bf16_f32 v142, v44, v45\n\t" \
  "ds_read_b64_tr_b16 v[80:81], v236 offset:0\n\t" \
  "v_mfma_f32_16x16x32_bf16 a[92:95], a[168:171], v[152:155], a[92:95]\n\t" \
  "v_exp_f32 v46, v46\n\t" \
  "v_mfma_f32_16x16x32_bf16 a[92:95], a[172:175], v[156:159], a[92:95]\n\t" \
  "v_exp_f32 v47, v47\n\t" \
  "v_mfma_f32_16x16x32_bf16 a[108:111], a[176:179], v[152:155], a[108:111]\n\t" \
  "v_cvt_pk_bf16_f32 v143, v46, v47\n\t" \
  "ds_read_b64_tr_b16 v[82:83], v236 offset:4096\n\t" \
  "v_mfma_f32_16x16x32_bf16 a[108:111], a[180:183], v[156:159], a[108:111]\n\t" \
  "v_exp_f32 v64, v64\n\t" \
  "v_mfma_f32_16x16x32_bf16 a[124:127], a[184:187], v[152:155], a[124:127]\n\t" \
  "v_exp_f32 v65, v65\n\t" \
  "v_mfma_f32_16x16x32_bf16 a[124:127], a[188:191], v[156:159], a[124:127]\n\t" \
  "v_cvt_pk_bf16_f32 v144, v64, v65\n\t" \
  "ds_read_b64_tr_b16 v[84:85], v236 offset:8192\n\t" \
  "v_mfma_f32_16x16x32_bf16 v[172:175], v[216:219], v[152:155], v[172:175]\n\t" \
  "v_exp_f32 v66, v66\n\t" \
  "v_mfma_f32_16x16x32_bf16 v[172:175], v[216:219], v[156:159], v[172:175]\n\t" \
  "ds_read_b64_tr_b16 v[86:87], v236 offset:12288\n\t" \
  "ds_read_b64_tr_b16 v[88:89], v236 offset:16384\n\t" \
  "s_cmp_lg_u32 s99, 0\n\t" \
  "s_cbranch_scc1 .Lsf_p7\n\t" \
  ".Lsf_q7:\n\t" \
  "s_waitcnt lgkmcnt(15)\n\t" \
  "v_mfma_f32_16x16x32_bf16 a[0:3], a[192:195], v[128:131], a[0:3]\n\t" \
  "v_exp_f32 v67, v67\n\t" \
  "v_mfma_f32_16x16x32_bf16 a[0:3], a[196:199], v[132:135], a[0:3]\n\t" \
  "v_cvt_pk_bf16_f32 v145, v66, v67\n\t" \
  "ds_read_b128 v[176:179], v215 offset:16384\n\t" \
  "v_mfma_f32_16x16x32_bf16 v[160:163], v[216:219], v[128:131], v[160:163]\n\t" \
  "v_exp_f32 v68, v68\n\t" \
  "v_mfma_f32_16x16x32_bf16 v[160:163], v[216:219], v[132:135], v[160:163]\n\t" \
  "ds_read_b64_tr_b16 v[90:91], v236 offset:20480\n\t" \
  "ds_read_b128 v[180:183], v215 offset:17408\n\t" \
  "v_mfma_f32_16x16x32_bf16 a[16:19], a[200:203], v[128:131], a[16:19]\n\t" \
  "ds_read_b64_tr_b16 v[92:93], v236 offset:24576\n\t" \
  "ds_read_b128 v[184:187], v215 offset:18432\n\t" \
  "v_mfma_f32_16x16x32_bf16 a[16:19], a[204:207], v[132:135], a[16:19]\n\t" \
  "v_exp_f32 v69, v69\n\t" \
  "v_mfma_f32_16x16x32_bf16 a[32:35], a[208:211], v[128:131], a[32:35]\n\t" \
  "v_cvt_pk_bf16_f32 v146, v68, v69\n\t" \
  "ds_read_b128 v[188:191], v215 offset:19456\n\t" \
  "v_mfma_f32_16x16x32_bf16 a[32:35], a[212:215], v[132:135], a[32:35]\n\t" \
  "s_waitcnt lgkmcnt(0)\n\t" \
  "buffer_load_dwordx4 v240, %[wo], s97 offen offset:0 lds\n\t" \
  "v_mfma_f32_16x16x32_bf16 a[48:51], a[216:219], v[128:131], a[48:51]\n\t" \
  "s_add_u32 s97, s97, 7168\n\t" \
  "buffer_load_dwordx4 v241, %[wo], s97 offen offset:1024 lds\n\t" \
  "v_mfma_f32_16x16x32_bf16 a[48:51], a[220:223], v[132:135], a[48:51]\n\t" \
  "s_add_u32 s97, s97, 7168\n\t" \
  "buffer_load_dwordx4 v240, %[wo], s97 offen offset:2048 lds\n\t" \
  "v_mfma_f32_16x16x32_bf16 a[64:67], a[224:227], v[128:131], a[64:67]\n\t" \
  "s_add_u32 s97, s97, 7168\n\t" \
  "buffer_load_dwordx4 v241, %[wo], s97 offen offset:3072 lds\n\t" \
  "v_mfma_f32_16x16x32_bf16 a[64:67], a[228:231], v[132:135], a[64:67]\n\t" \
  "s_add_u32 m0, %[m], 131072\n\t" \
  "s_xor_b32 s97, %[wob], 917504\n\t" \
  "v_mfma_f32_16x16x32_bf16 a[80:83], a[232:235], v[128:131], a[80:83]\n\t" \
  "ds_read_b64_tr_b16 v[94:95], v236 offset:28672\n\t" \
  "ds_read_b128 v[192:195], v215 offset:32768\n\t" \
  "v_mfma_f32_16x16x32_bf16 a[80:83], a[236:239], v[132:135], a[80:83]\n\t" \
  "v_rcp_f32 v208, v160\n\t" \
  "v_mfma_f32_16x16x32_bf16 a[96:99], a[240:243], v[128:131], a[96:99]\n\t" \
  "v_accvgpr_read_b32 v16, a0\n\t" \
  "v_accvgpr_read_b32 v17, a1\n\t" \
  "v_mfma_f32_16x16x32_bf16 a[96:99], a[244:247], v[132:135], a[96:99]\n\t" \
  "v_accvgpr_read_b32 v18, a2\n\t" \
  "v_accvgpr_read_b32 v19, a3\n\t" \
  "v_mfma_f32_16x16x32_bf16 a[112:115], a[248:251], v[128:131], a[112:115]\n\t" \
  "v_accvgpr_read_b32 v20, a16\n\t" \
  "v_accvgpr_read_b32 v21, a17\n\t" \
  "v_mfma_f32_16x16x32_bf16 a[112:115], a[252:255], v[132:135], a[112:115]\n\t" \
  "v_accvgpr_read_b32 v22, a18\n\t" \
  "v_accvgpr_read_b32 v23, a19\n\t" \
  "s_waitcnt vmcnt(8)\n\t" \
  "s_barrier\n\t" \
  "v_mfma_f32_16x16x32_bf16 a[4:7], a[192:195], v[136:139], a[4:7]\n\t" \
  "v_xor_b32 v235, 128, v234\n\t" \
  "ds_read_b128 v[196:199], v215 offset:33792\n\t" \
  "v_mfma_f32_16x16x32_bf16 a[4:7], a[196:199], v[140:143], a[4:7]\n\t" \
  "v_mul_f32 v16, v16, v208\n\t" \
  "v_mul_f32 v17, v17, v208\n\t" \
  "v_mfma_f32_16x16x32_bf16 v[164:167], v[216:219], v[136:139], v[164:167]\n\t" \
  "v_mul_f32 v18, v18, v208\n\t" \
  "v_mul_f32 v19, v19, v208\n\t" \
  "v_mfma_f32_16x16x32_bf16 v[164:167], v[216:219], v[140:143], v[164:167]\n\t" \
  "v_mul_f32 v20, v20, v208\n\t" \
  "v_mul_f32 v21, v21, v208\n\t" \
  "v_mfma_f32_16x16x32_bf16 a[20:23], a[200:203], v[136:139], a[20:23]\n\t" \
  "v_mul_f32 v22, v22, v208\n\t" \
  "v_mul_f32 v23, v23, v208\n\t" \
  "v_mfma_f32_16x16x32_bf16 a[20:23], a[204:207], v[140:143], a[20:23]\n\t" \
  "ds_read_b128 v[200:203], v215 offset:34816\n\t" \
  "ds_read_b64_tr_b16 v[112:113], v235 offset:0\n\t" \
  "v_mfma_f32_16x16x32_bf16 a[36:39], a[208:211], v[136:139], a[36:39]\n\t" \
  "ds_read_b128 v[204:207], v215 offset:35840\n\t" \
  "s_waitcnt lgkmcnt(0)\n\t" \
  "v_mfma_f32_16x16x32_bf16 a[36:39], a[212:215], v[140:143], a[36:39]\n\t" \
  "buffer_load_dwordx4 v240, %[wo], s97 offen offset:0 lds\n\t" \
  "ds_read_b64_tr_b16 v[114:115], v235 offset:4096\n\t" \
  "v_mfma_f32_16x16x32_bf16 a[52:55], a[216:219], v[136:139], a[52:55]\n\t" \
  "s_add_u32 s97, s97, 7168\n\t" \
  "buffer_load_dwordx4 v241, %[wo], s97 offen offset:1024 lds\n\t" \
  "v_mfma_f32_16x16x32_bf16 a[52:55], a[220:223], v[140:143], a[52:55]\n\t" \
  "s_add_u32 s97, s97, 7168\n\t" \
  "buffer_load_dwordx4 v240, %[wo], s97 offen offset:2048 lds\n\t" \
  "v_mfma_f32_16x16x32_bf16 a[68:71], a[224:227], v[136:139], a[68:71]\n\t" \
  "s_add_u32 s97, s97, 7168\n\t" \
  "buffer_load_dwordx4 v241, %[wo], s97 offen offset:3072 lds\n\t" \
  "v_mfma_f32_16x16x32_bf16 a[68:71], a[228:231], v[140:143], a[68:71]\n\t" \
  "v_cvt_pk_bf16_f32 v0, v16, v17\n\t" \
  "v_cvt_pk_bf16_f32 v1, v18, v19\n\t" \
  "v_mfma_f32_16x16x32_bf16 a[84:87], a[232:235], v[136:139], a[84:87]\n\t" \
  "v_cvt_pk_bf16_f32 v2, v20, v21\n\t" \
  "v_cvt_pk_bf16_f32 v3, v22, v23\n\t" \
  "v_mfma_f32_16x16x32_bf16 a[84:87], a[236:239], v[140:143], a[84:87]\n\t" \
  "s_xor_b32 s98, %[eso], 0\n\t" \
  "buffer_store_dwordx4 v[0:3], v242, %[ws], s98 offen offset:0 sc1\n\t" \
  "v_mfma_f32_16x16x32_bf16 a[100:103], a[240:243], v[136:139], a[100:103]\n\t" \
  "v_accvgpr_read_b32 v24, a32\n\t" \
  "v_accvgpr_read_b32 v25, a33\n\t" \
  "v_mfma_f32_16x16x32_bf16 a[100:103], a[244:247], v[140:143], a[100:103]\n\t" \
  "v_accvgpr_read_b32 v26, a34\n\t" \
  "v_accvgpr_read_b32 v27, a35\n\t" \
  "v_mfma_f32_16x16x32_bf16 a[116:119], a[248:251], v[136:139], a[116:119]\n\t" \
  "v_accvgpr_read_b32 v28, a48\n\t" \
  "v_accvgpr_read_b32 v29, a49\n\t" \
  "v_mfma_f32_16x16x32_bf16 a[116:119], a[252:255], v[140:143], a[116:119]\n\t" \
  "v_accvgpr_read_b32 v30, a50\n\t" \
  "v_accvgpr_read_b32 v31, a51\n\t" \
  "v_exp_f32 v70, v70\n\t" \
  "v_exp_f32 v71, v71\n\t" \
  "s_nop 0\n\t" \
  "v_cvt_pk_bf16_f32 v147, v70, v71\n\t" \
  "s_nop 1\n\t" \
  "v_mfma_f32_16x16x32_bf16 a[8:11], a[192:195], v[144:147], a[8:11]\n\t" \
  "ds_read_b64_tr_b16 v[116:117], v235 offset:8192\n\t" \
  "ds_read_b64_tr_b16 v[118:119], v235 offset:12288\n\t" \
  "v_exp_f32 v72, v72\n\t" \
  "v_exp_f32 v73, v73\n\t" \
  "v_exp_f32 v74, v74\n\t" \
  "v_exp_f32 v75, v75\n\t" \
  "v_exp_f32 v76, v76\n\t" \
  "v_exp_f32 v77, v77\n\t" \
  "v_exp_f32 v78, v78\n\t" \
  "v_exp_f32 v79, v79\n\t" \
  "v_cvt_pk_bf16_f32 v148, v72, v73\n\t" \
  "v_cvt_pk_bf16_f32 v149, v74, v75\n\t" \
  "v_cvt_pk_bf16_f32 v150, v76, v77\n\t" \
  "v_cvt_pk_bf16_f32 v151, v78, v79\n\t" \
  "s_nop 1\n\t" \
  "v_mfma_f32_16x16x32_bf16 a[8:11], a[196:199], v[148:151], a[8:11]\n\t" \
  "v_mul_f32 v24, v24, v208\n\t" \
  "v_mul_f32 v25, v25, v208\n\t" \
  "v_mfma_f32_16x16x32_bf16 v[168:171], v[216:219], v[144:147], v[168:171]\n\t" \
  "v_mul_f32 v26, v26, v208\n\t" \
  "v_mul_f32 v27, v27, v208\n\t" \
  "v_mfma_f32_16x16x32_bf16 v[168:171], v[216:219], v[148:151], v[168:171]\n\t" \
  "v_mul_f32 v28, v28, v208\n\t" \
  "v_mul_f32 v29, v29, v208\n\t" \
  "v_mfma_f32_16x16x32_bf16 a[24:27], a[200:203], v[144:147], a[24:27]\n\t" \
  "v_mul_f32 v30, v30, v208\n\t" \
  "v_mul_f32 v31, v31, v208\n\t" \
  "v_mfma_f32_16x16x32_bf16 a[24:27], a[204:207], v[148:151], a[24:27]\n\t" \
  "ds_read_b64_tr_b16 v[120:121], v235 offset:16384\n\t" \
  "v_cvt_pk_bf16_f32 v4, v24, v25\n\t" \
  "v_mfma_f32_16x16x32_bf16 a[40:43], a[208:211], v[144:147], a[40:43]\n\t" \
  "v_cvt_pk_bf16_f32 v5, v26, v27\n\t" \
  "v_cvt_pk_bf16_f32 v6, v28, v29\n\t" \
  "v_mfma_f32_16x16x32_bf16 a[40:43], a[212:215], v[148:151], a[40:43]\n\t" \
  "v_cvt_pk_bf16_f32 v7, v30, v31\n\t" \
  "ds_read_b64_tr_b16 v[122:123], v235 offset:20480\n\t" \
  "v_mfma_f32_16x16x32_bf16 a[56:59], a[216:219], v[144:147], a[56:59]\n\t" \
  "s_xor_b32 s98, %[eso], 0\n\t" \
  "buffer_store_dwordx4 v[4:7], v242, %[ws], s98 offen offset:1024 sc1\n\t" \
  "v_mfma_f32_16x16x32_bf16 a[56:59], a[220:223], v[148:151], a[56:59]\n\t" \
  "v_accvgpr_read_b32 v16, a64\n\t" \
  "v_accvgpr_read_b32 v17, a65\n\t" \
  "v_mfma_f32_16x16x32_bf16 a[72:75], a[224:227], v[144:147], a[72:75]\n\t" \
  "v_accvgpr_read_b32 v18, a66\n\t" \
  "v_accvgpr_read_b32 v19, a67\n\t" \
  "v_mfma_f32_16x16x32_bf16 a[72:75], a[228:231], v[148:151], a[72:75]\n\t" \
  "v_accvgpr_read_b32 v20, a80\n\t" \
  "v_accvgpr_read_b32 v21, a81\n\t" \
  "v_mfma_f32_16x16x32_bf16 a[88:91], a[232:235], v[144:147], a[88:91]\n\t" \
  "v_accvgpr_read_b32 v22, a82\n\t" \
  "v_accvgpr_read_b32 v23, a83\n\t" \
  "v_mfma_f32_16x16x32_bf16 a[88:91], a[236:239], v[148:151], a[88:91]\n\t" \
  "ds_read_b64_tr_b16 v[124:125], v235 offset:24576\n\t" \
  "v_mul_f32 v16, v16, v208\n\t" \
  "v_mfma_f32_16x16x32_bf16 a[104:107], a[240:243], v[144:147], a[104:107]\n\t" \
  "v_mul_f32 v17, v17, v208\n\t" \
  "v_mul_f32 v18, v18, v208\n\t" \
  "v_mfma_f32_16x16x32_bf16 a[104:107], a[244:247], v[148:151], a[104:107]\n\t" \
  "v_mul_f32 v19, v19, v208\n\t" \
  "v_mul_f32 v20, v20, v208\n\t" \
  "v_mfma_f32_16x16x32_bf16 a[120:123], a[248:251], v[144:147], a[120:123]\n\t" \
  "v_mul_f32 v21, v21, v208\n\t" \
  "v_mul_f32 v22, v22, v208\n\t" \
  "v_mfma_f32_16x16x32_bf16 a[120:123], a[252:255], v[148:151], a[120:123]\n\t" \
  "v_mul_f32 v23, v23, v208\n\t" \
  "ds_read_b64_tr_b16 v[126:127], v235 offset:28672\n\t" \
  "v_exp_f32 v96, v96\n\t" \
  "v_exp_f32 v97, v97\n\t" \
  "v_exp_f32 v98, v98\n\t" \
  "v_exp_f32 v99, v99\n\t" \
  "v_exp_f32 v100, v100\n\t" \
  "v_exp_f32 v101, v101\n\t" \
  "v_exp_f32 v102, v102\n\t" \
  "v_exp_f32 v103, v103\n\t" \
  "v_cvt_pk_bf16_f32 v152, v96, v97\n\t" \
  "v_cvt_pk_bf16_f32 v153, v98, v99\n\t" \
  "v_cvt_pk_bf16_f32 v154, v100, v101\n\t" \
  "v_cvt_pk_bf16_f32 v155, v102, v103\n\t" \
  "s_nop 1\n\t" \
  "v_mfma_f32_16x16x32_bf16 a[12:15], a[192:195], v[152:155], a[12:15]\n\t" \
  "v_cvt_pk_bf16_f32 v8, v16, v17\n\t" \
  "v_cvt_pk_bf16_f32 v9, v18, v19\n\t" \
  "v_exp_f32 v104, v104\n\t" \
  "v_exp_f32 v105, v105\n\t" \
  "v_exp_f32 v106, v106\n\t" \
  "v_exp_f32 v107, v107\n\t" \
  "v_exp_f32 v108, v108\n\t" \
  "v_exp_f32 v109, v109\n\t" \
  "v_exp_f32 v110, v110\n\t" \
  "v_exp_f32 v111, v111\n\t" \
  "v_cvt_pk_bf16_f32 v156, v104, v105\n\t" \
  "v_cvt_pk_bf16_f32 v157, v106, v107\n\t" \
  "v_cvt_pk_bf16_f32 v158, v108, v109\n\t" \
  "v_cvt_pk_bf16_f32 v159, v110, v111\n\t" \
  "s_nop 1\n\t" \
  "v_mfma_f32_16x16x32_bf16 a[12:15], a[196:199], v[156:159], a[12:15]\n\t" \
  "v_cvt_pk_bf16_f32 v10, v20, v21\n\t" \
  "v_cvt_pk_bf16_f32 v11, v22, v23\n\t" \
  "v_mfma_f32_16x16x32_bf16 v[172:175], v[216:219], v[152:155], v[172:175]\n\t" \
  "s_xor_b32 s98, %[eso], 0\n\t" \
  "buffer_store_dwordx4 v[8:11], v242, %[ws], s98 offen offset:2048 sc1\n\t" \
  "v_mfma_f32_16x16x32_bf16 v[172:175], v[216:219], v[156:159], v[172:175]\n\t" \
  "ds_read_b64_tr_b16 a[128:129], v234 offset:0\n\t" \
  "ds_read_b64_tr_b16 a[130:131], v234 offset:4096\n\t" \
  "v_mfma_f32_16x16x32_bf16 a[28:31], a[200:203], v[152:155], a[28:31]\n\t" \
  "v_accvgpr_read_b32 v24, a96\n\t" \
  "v_accvgpr_read_b32 v25, a97\n\t" \
  "v_mfma_f32_16x16x32_bf16 a[28:31], a[204:207], v[156:159], a[28:31]\n\t" \
  "v_accvgpr_read_b32 v26, a98\n\t" \
  "v_accvgpr_read_b32 v27, a99\n\t" \
  "v_mfma_f32_16x16x32_bf16 a[44:47], a[208:211], v[152:155], a[44:47]\n\t" \
  "v_accvgpr_read_b32 v28, a112\n\t" \
  "v_accvgpr_read_b32 v29, a113\n\t" \
  "v_mfma_f32_16x16x32_bf16 a[44:47], a[212:215], v[156:159], a[44:47]\n\t" \
  "v_accvgpr_read_b32 v30, a114\n\t" \
  "v_accvgpr_read_b32 v31, a115\n\t" \
  "v_mfma_f32_16x16x32_bf16 a[60:63], a[216:219], v[152:155], a[60:63]\n\t" \
  "ds_read_b64_tr_b16 a[132:133], v234 offset:8192\n\t" \
  "ds_read_b64_tr_b16 a[134:135], v234 offset:12288\n\t" \
  "v_mfma_f32_16x16x32_bf16 a[60:63], a[220:223], v[156:159], a[60:63]\n\t" \
  "v_mul_f32 v24, v24, v208\n\t" \
  "v_mul_f32 v25, v25, v208\n\t" \
  "v_mfma_f32_16x16x32_bf16 a[76:79], a[224:227], v[152:155], a[76:79]\n\t" \
  "v_mul_f32 v26, v26, v208\n\t" \
  "v_mul_f32 v27, v27, v208\n\t" \
  "v_mfma_f32_16x16x32_bf16 a[76:79], a[228:231], v[156:159], a[76:79]\n\t" \
  "v_mul_f32 v28, v28, v208\n\t" \
  "v_mul_f32 v29, v29, v208\n\t" \
  "v_mfma_f32_16x16x32_bf16 a[92:95], a[232:235], v[152:155], a[92:95]\n\t" \
  "v_mul_f32 v30, v30, v208\n\t" \
  "v_mul_f32 v31, v31, v208\n\t" \
  "v_mfma_f32_16x16x32_bf16 a[92:95], a[236:239], v[156:159], a[92:95]\n\t" \
  "ds_read_b64_tr_b16 a[136:137], v234 offset:16384\n\t" \
  "ds_read_b64_tr_b16 a[138:139], v234 offset:20480\n\t" \
  "v_mfma_f32_16x16x32_bf16 a[108:111], a[240:243], v[152:155], a[108:111]\n\t" \
  "v_cvt_pk_bf16_f32 v12, v24, v25\n\t" \
  "v_cvt_pk_bf16_f32 v13, v26, v27\n\t" \
  "v_mfma_f32_16x16x32_bf16 a[108:111], a[244:247], v[156:159], a[108:111]\n\t" \
  "v_cvt_pk_bf16_f32 v14, v28, v29\n\t" \
  "v_cvt_pk_bf16_f32 v15, v30, v31\n\t" \
  "v_mfma_f32_16x16x32_bf16 a[124:127], a[248:251], v[152:155], a[124:127]\n\t" \
  "s_xor_b32 s98, %[eso], 0\n\t" \
  "buffer_store_dwordx4 v[12:15], v242, %[ws], s98 offen offset:3072 sc1\n\t" \
  "v_mfma_f32_16x16x32_bf16 a[124:127], a[252:255], v[156:159], a[124:127]\n\t" \
  "ds_read_b64_tr_b16 a[140:141], v234 offset:24576\n\t" \
  "ds_read_b64_tr_b16 a[142:143], v234 offset:28672\n\t" \
  "v_mfma_f32_16x16x32_bf16 a[32:35], v[48:51], v[0:3], 0\n\t" \
  "v_rcp_f32 v210, v164\n\t" \
  "v_mfma_f32_16x16x32_bf16 a[32:35], v[52:55], v[4:7], a[32:35]\n\t" \
  "v_accvgpr_read_b32 v16, a4\n\t" \
  "v_accvgpr_read_b32 v17, a5\n\t" \
  "v_mfma_f32_16x16x32_bf16 a[32:35], v[56:59], v[8:11], a[32:35]\n\t" \
  "v_accvgpr_read_b32 v18, a6\n\t" \
  "v_accvgpr_read_b32 v19, a7\n\t" \
  "v_mfma_f32_16x16x32_bf16 a[32:35], v[60:63], v[12:15], a[32:35]\n\t" \
  "v_accvgpr_read_b32 v20, a20\n\t" \
  "v_accvgpr_read_b32 v21, a21\n\t" \
  "v_mfma_f32_16x16x32_bf16 a[48:51], v[92:95], v[12:15], 0\n\t" \
  "v_accvgpr_read_b32 v22, a22\n\t" \
  "v_accvgpr_read_b32 v23, a23\n\t" \
  "v_mfma_f32_16x16x32_bf16 a[48:51], v[88:91], v[8:11], a[48:51]\n\t" \
  "v_xor_b32 v236, 32, v234\n\t" \
  "ds_read_b64_tr_b16 a[144:145], v236 offset:0\n\t" \
  "v_mfma_f32_16x16x32_bf16 a[48:51], v[84:87], v[4:7], a[48:51]\n\t" \
  "v_mul_f32 v16, v16, v210\n\t" \
  "v_mul_f32 v17, v17, v210\n\t" \
  "v_mfma_f32_16x16x32_bf16 a[48:51], v[80:83], v[0:3], a[48:51]\n\t" \
  "v_mul_f32 v18, v18, v210\n\t" \
  "v_mul_f32 v19, v19, v210\n\t" \
  "s_waitcnt lgkmcnt(15)\n\t" \
  "v_mfma_f32_16x16x32_bf16 a[64:67], v[112:115], v[0:3], 0\n\t" \
  "v_mul_f32 v20, v20, v210\n\t" \
  "v_mul_f32 v21, v21, v210\n\t" \
  "s_waitcnt lgkmcnt(13)\n\t" \
  "v_mfma_f32_16x16x32_bf16 a[64:67], v[116:119], v[4:7], a[64:67]\n\t" \
  "v_mul_f32 v22, v22, v210\n\t" \
  "v_mul_f32 v23, v23, v210\n\t" \
  "s_waitcnt lgkmcnt(11)\n\t" \
  "v_mfma_f32_16x16x32_bf16 a[64:67], v[120:123], v[8:11], a[64:67]\n\t" \
  "ds_read_b64_tr_b16 a[146:147], v236 offset:4096\n\t" \
  "ds_read_b64_tr_b16 a[148:149], v236 offset:8192\n\t" \
  "s_waitcnt lgkmcnt(11)\n\t" \
  "v_mfma_f32_16x16x32_bf16 a[64:67], v[124:127], v[12:15], a[64:67]\n\t" \
  "v_cvt_pk_bf16_f32 v32, v16, v17\n\t" \
  "v_cvt_pk_bf16_f32 v33, v18, v19\n\t" \
  "ds_read_b64_tr_b16 a[150:151], v236 offset:12288\n\t" \
  "ds_read_b64_tr_b16 a[152:153], v236 offset:16384\n\t" \
  "ds_read_b64_tr_b16 a[154:155], v236 offset:20480\n\t" \
  "ds_read_b64_tr_b16 a[156:157], v236 offset:24576\n\t" \
  "ds_read_b64_tr_b16 a[158:159], v236 offset:28672\n\t" \
  "s_waitcnt lgkmcnt(8)\n\t" \
  "v_mfma_f32_16x16x32_bf16 a[0:3], a[140:143], v[12:15], 0\n\t" \
  "v_cvt_pk_bf16_f32 v34, v20, v21\n\t" \
  "v_cvt_pk_bf16_f32 v35, v22, v23\n\t" \
  "v_mfma_f32_16x16x32_bf16 a[0:3], a[136:139], v[8:11], a[0:3]\n\t" \
  "s_xor_b32 s98, %[eso], 32768\n\t" \
  "buffer_store_dwordx4 v[32:35], v242, %[ws], s98 offen offset:0 sc1\n\t" \
  "v_mfma_f32_16x16x32_bf16 a[0:3], a[132:135], v[4:7], a[0:3]\n\t" \
  "v_accvgpr_read_b32 v24, a36\n\t" \
  "v_accvgpr_read_b32 v25, a37\n\t" \
  "v_mfma_f32_16x16x32_bf16 a[0:3], a[128:131], v[0:3], a[0:3]\n\t" \
  "v_accvgpr_read_b32 v26, a38\n\t" \
  "v_accvgpr_read_b32 v27, a39\n\t" \
  "v_xor_b32 v235, 192, v234\n\t" \
  "ds_read_b64_tr_b16 a[160:161], v235 offset:0\n\t" \
  "ds_read_b64_tr_b16 a[162:163], v235 offset:4096\n\t" \
  "ds_read_b64_tr_b16 a[164:165], v235 offset:8192\n\t" \
  "ds_read_b64_tr_b16 a[166:167], v235 offset:12288\n\t" \
  "ds_read_b64_tr_b16 a[168:169], v235 offset:16384\n\t" \
  "ds_read_b64_tr_b16 a[170:171], v235 offset:20480\n\t" \
  "ds_read_b64_tr_b16 a[172:173], v235 offset:24576\n\t" \
  "ds_read_b64_tr_b16 a[174:175], v235 offset:28672\n\t" \
  "s_waitcnt lgkmcnt(14)\n\t" \
  "v_mfma_f32_16x16x32_bf16 a[16:19], a[144:147], v[0:3], 0\n\t" \
  "v_accvgpr_read_b32 v28, a52\n\t" \
  "v_accvgpr_read_b32 v29, a53\n\t" \
  "s_waitcnt lgkmcnt(12)\n\t" \
  "v_mfma_f32_16x16x32_bf16 a[16:19], a[148:151], v[4:7], a[16:19]\n\t" \
  "v_accvgpr_read_b32 v30, a54\n\t" \
  "v_accvgpr_read_b32 v31, a55\n\t" \
  "s_waitcnt lgkmcnt(10)\n\t" \
  "v_mfma_f32_16x16x32_bf16 a[16:19], a[152:155], v[8:11], a[16:19]\n\t" \
  "v_mul_f32 v24, v24, v210\n\t" \
  "v_mul_f32 v25, v25, v210\n\t" \
  "s_waitcnt lgkmcnt(8)\n\t" \
  "v_mfma_f32_16x16x32_bf16 a[16:19], a[156:159], v[12:15], a[16:19]\n\t" \
  "v_mul_f32 v26, v26, v210\n\t" \
  "v_mul_f32 v27, v27, v210\n\t" \
  "v_xor_b32 v236, 224, v234\n\t" \
  "ds_read_b64_tr_b16 a[176:177], v236 offset:0\n\t" \
  "ds_read_b64_tr_b16 a[178:179], v236 offset:4096\n\t" \
  "ds_read_b64_tr_b16 a[180:181], v236 offset:8192\n\t" \
  "ds_read_b64_tr_b16 a[182:183], v236 offset:12288\n\t" \
  "ds_read_b64_tr_b16 a[184:185], v236 offset:16384\n\t" \
  "ds_read_b64_tr_b16 a[186:187], v236 offset:20480\n\t" \
  "ds_read_b64_tr_b16 a[188:189], v236 offset:24576\n\t" \
  "ds_read_b64_tr_b16 a[190:191], v236 offset:28672\n\t" \
  "s_waitcnt lgkmcnt(8)\n\t" \
  "v_mfma_f32_16x16x32_bf16 a[96:99], a[172:175], v[12:15], 0\n\t" \
  "v_mul_f32 v28, v28, v210\n\t" \
  "v_mul_f32 v29, v29, v210\n\t" \
  "v_mfma_f32_16x16x32_bf16 a[96:99], a[168:171], v[8:11], a[96:99]\n\t" \
  "v_mul_f32 v30, v30, v210\n\t" \
  "v_mul_f32 v31, v31, v210\n\t" \
  "v_mfma_f32_16x16x32_bf16 a[96:99], a[164:167], v[4:7], a[96:99]\n\t" \
  "v_cvt_pk_bf16_f32 v36, v24, v25\n\t" \
  "v_cvt_pk_bf16_f32 v37, v26, v27\n\t" \
  "v_mfma_f32_16x16x32_bf16 a[96:99], a[160:163], v[0:3], a[96:99]\n\t" \
  "v_cvt_pk_bf16_f32 v38, v28, v29\n\t" \
  "v_cvt_pk_bf16_f32 v39, v30, v31\n\t" \
  "v_xor_b32 v236, 160, v234\n\t" \
  "ds_read_b64_tr_b16 v[96:97], v236 offset:0\n\t" \
  "ds_read_b64_tr_b16 v[98:99], v236 offset:4096\n\t" \
  "ds_read_b64_tr_b16 v[100:101], v236 offset:8192\n\t" \
  "ds_read_b64_tr_b16 v[102:103], v236 offset:12288\n\t" \
  "ds_read_b64_tr_b16 v[104:105], v236 offset:16384\n\t" \
  "ds_read_b64_tr_b16 v[106:107], v236 offset:20480\n\t" \
  "ds_read_b64_tr_b16 v[108:109], v236 offset:24576\n\t" \
  "ds_read_b64_tr_b16 v[110:111], v236 offset:28672\n\t" \
  "s_waitcnt lgkmcnt(14)\n\t" \
  "v_mfma_f32_16x16x32_bf16 a[112:115], a[176:179], v[0:3], 0\n\t" \
  "s_xor_b32 s98, %[eso], 32768\n\t" \
  "buffer_store_dwordx4 v[36:39], v242, %[ws], s98 offen offset:1024 sc1\n\t" \
  "s_waitcnt lgkmcnt(12)\n\t" \
  "v_mfma_f32_16x16x32_bf16 a[112:115], a[180:183], v[4:7], a[112:115]\n\t" \
  "v_accvgpr_read_b32 v16, a68\n\t" \
  "v_accvgpr_read_b32 v17, a69\n\t" \
  "s_waitcnt lgkmcnt(10)\n\t" \
  "v_mfma_f32_16x16x32_bf16 a[112:115], a[184:187], v[8:11], a[112:115]\n\t" \
  "v_accvgpr_read_b32 v18, a70\n\t" \
  "v_accvgpr_read_b32 v19, a71\n\t" \
  "s_waitcnt lgkmcnt(8)\n\t" \
  "v_mfma_f32_16x16x32_bf16 a[112:115], a[188:191], v[12:15], a[112:115]\n\t" \
  "v_accvgpr_read_b32 v20, a84\n\t" \
  "v_accvgpr_read_b32 v21, a85\n\t" \
  "s_waitcnt lgkmcnt(0)\n\t" \
  "v_mfma_f32_16x16x32_bf16 a[80:83], v[108:111], v[12:15], 0\n\t" \
  "v_accvgpr_read_b32 v22, a86\n\t" \
  "v_accvgpr_read_b32 v23, a87\n\t" \
  "v_mfma_f32_16x16x32_bf16 a[80:83], v[104:107], v[8:11], a[80:83]\n\t" \
  "v_mul_f32 v16, v16, v210\n\t" \
  "v_mul_f32 v17, v17, v210\n\t" \
  "v_mfma_f32_16x16x32_bf16 a[80:83], v[100:103], v[4:7], a[80:83]\n\t" \
  "v_mul_f32 v18, v18, v210\n\t" \
  "v_mul_f32 v19, v19, v210\n\t" \
  "v_mfma_f32_16x16x32_bf16 a[80:83], v[96:99], v[0:3], a[80:83]\n\t" \
  "v_mul_f32 v20, v20, v210\n\t" \
  "v_mul_f32 v21, v21, v210\n\t" \
  "v_mul_f32 v22, v22, v210\n\t" \
  "v_mul_f32 v23, v23, v210\n\t" \
  "v_cvt_pk_bf16_f32 v40, v16, v17\n\t" \
  "v_cvt_pk_bf16_f32 v41, v18, v19\n\t" \
  "v_cvt_pk_bf16_f32 v42, v20, v21\n\t" \
  "v_cvt_pk_bf16_f32 v43, v22, v23\n\t" \
  "v_accvgpr_read_b32 v24, a100\n\t" \
  "v_accvgpr_read_b32 v25, a101\n\t" \
  "v_accvgpr_read_b32 v26, a102\n\t" \
  "v_accvgpr_read_b32 v27, a103\n\t" \
  "v_accvgpr_read_b32 v28, a116\n\t" \
  "v_accvgpr_read_b32 v29, a117\n\t" \
  "v_accvgpr_read_b32 v30, a118\n\t" \
  "v_accvgpr_read_b32 v31, a119\n\t" \
  "v_mul_f32 v24, v24, v210\n\t" \
  "v_mul_f32 v25, v25, v210\n\t" \
  "v_mul_f32 v26, v26, v210\n\t" \
  "v_mul_f32 v27, v27, v210\n\t" \
  "v_mul_f32 v28, v28, v210\n\t" \
  "v_mul_f32 v29, v29, v210\n\t" \
  "v_mul_f32 v30, v30, v210\n\t" \
  "v_mul_f32 v31, v31, v210\n\t" \
  "v_cvt_pk_bf16_f32 v44, v24, v25\n\t" \
  "v_cvt_pk_bf16_f32 v45, v26, v27\n\t" \
  "v_cvt_pk_bf16_f32 v46, v28, v29\n\t" \
  "v_cvt_pk_bf16_f32 v47, v30, v31\n\t" \
  "v_mfma_f32_16x16x32_bf16 a[36:39], v[48:51], v[32:35], 0\n\t" \
  "s_xor_b32 s98, %[eso], 32768\n\t" \
  "buffer_store_dwordx4 v[40:43], v242, %[ws], s98 offen offset:2048 sc1\n\t" \
  "v_mfma_f32_16x16x32_bf16 a[36:39], v[52:55], v[36:39], a[36:39]\n\t" \
  "s_xor_b32 s98, %[eso], 32768\n\t" \
  "buffer_store_dwordx4 v[44:47], v242, %[ws], s98 offen offset:3072 sc1\n\t" \
  "v_mfma_f32_16x16x32_bf16 a[36:39], v[56:59], v[40:43], a[36:39]\n\t" \
  "v_rcp_f32 v212, v168\n\t" \
  "v_mfma_f32_16x16x32_bf16 a[36:39], v[60:63], v[44:47], a[36:39]\n\t" \
  "v_accvgpr_read_b32 v16, a8\n\t" \
  "v_accvgpr_read_b32 v17, a9\n\t" \
  "s_waitcnt vmcnt(0)\n\t" \
  "buffer_store_dword v243, v254, %[ws], %[fw2] offen offset:0 sc1\n\t" \
  "v_mfma_f32_16x16x32_bf16 a[52:55], v[92:95], v[44:47], 0\n\t" \
  "v_accvgpr_read_b32 v18, a10\n\t" \
  "v_accvgpr_read_b32 v19, a11\n\t" \
  "v_mfma_f32_16x16x32_bf16 a[52:55], v[88:91], v[40:43], a[52:55]\n\t" \
  "v_accvgpr_read_b32 v20, a24\n\t" \
  "v_accvgpr_read_b32 v21, a25\n\t" \
  "v_mfma_f32_16x16x32_bf16 a[52:55], v[84:87], v[36:39], a[52:55]\n\t" \
  "v_accvgpr_read_b32 v22, a26\n\t" \
  "v_accvgpr_read_b32 v23, a27\n\t" \
  "v_mfma_f32_16x16x32_bf16 a[52:55], v[80:83], v[32:35], a[52:55]\n\t" \
  "v_mul_f32 v16, v16, v212\n\t" \
  "v_mul_f32 v17, v17, v212\n\t" \
  "ds_read_b128 a[224:227], v213 offset:32768\n\t" \
  "ds_read_b128 a[228:231], v213 offset:33792\n\t" \
  "ds_read_b128 a[232:235], v213 offset:34816\n\t" \
  "ds_read_b128 a[236:239], v213 offset:35840\n\t" \
  "ds_read_b128 a[240:243], v215 offset:0\n\t" \
  "ds_read_b128 a[244:247], v215 offset:1024\n\t" \
  "ds_read_b128 a[248:251], v215 offset:2048\n\t" \
  "ds_read_b128 a[252:255], v215 offset:3072\n\t" \
  "v_mfma_f32_16x16x32_bf16 a[68:71], v[112:115], v[32:35], 0\n\t" \
  "v_mul_f32 v18, v18, v212\n\t" \
  "v_mul_f32 v19, v19, v212\n\t" \
  "v_mfma_f32_16x16x32_bf16 a[68:71], v[116:119], v[36:39], a[68:71]\n\t" \
  "v_mul_f32 v20, v20, v212\n\t" \
  "v_mul_f32 v21, v21, v212\n\t" \
  "v_mfma_f32_16x16x32_bf16 a[68:71], v[120:123], v[40:43], a[68:71]\n\t" \
  "v_mul_f32 v22, v22, v212\n\t" \
  "v_mul_f32 v23, v23, v212\n\t" \
  "v_mfma_f32_16x16x32_bf16 a[68:71], v[124:127], v[44:47], a[68:71]\n\t" \
  "v_cvt_pk_bf16_f32 v64, v16, v17\n\t" \
  "v_cvt_pk_bf16_f32 v65, v18, v19\n\t" \
  "v_mfma_f32_16x16x32_bf16 a[4:7], a[140:143], v[44:47], 0\n\t" \
  "v_cvt_pk_bf16_f32 v66, v20, v21\n\t" \
  "v_cvt_pk_bf16_f32 v67, v22, v23\n\t" \
  "v_mfma_f32_16x16x32_bf16 a[4:7], a[136:139], v[40:43], a[4:7]\n\t" \
  "s_xor_b32 s98, %[eso], 65536\n\t" \
  "buffer_store_dwordx4 v[64:67], v242, %[ws], s98 offen offset:0 sc1\n\t" \
  "v_mfma_f32_16x16x32_bf16 a[4:7], a[132:135], v[36:39], a[4:7]\n\t" \
  "v_accvgpr_read_b32 v24, a40\n\t" \
  "v_accvgpr_read_b32 v25, a41\n\t" \
  "v_mfma_f32_16x16x32_bf16 a[4:7], a[128:131], v[32:35], a[4:7]\n\t" \
  "v_accvgpr_read_b32 v26, a42\n\t" \
  "v_accvgpr_read_b32 v27, a43\n\t" \
  "v_mfma_f32_16x16x32_bf16 a[20:23], a[144:147], v[32:35], 0\n\t" \
  "v_accvgpr_read_b32 v28, a56\n\t" \
  "v_accvgpr_read_b32 v29, a57\n\t" \
  "v_mfma_f32_16x16x32_bf16 a[20:23], a[148:151], v[36:39], a[20:23]\n\t" \
  "v_accvgpr_read_b32 v30, a58\n\t" \
  "v_accvgpr_read_b32 v31, a59\n\t" \
  "v_mfma_f32_16x16x32_bf16 a[20:23], a[152:155], v[40:43], a[20:23]\n\t" \
  "v_mul_f32 v24, v24, v212\n\t" \
  "v_mul_f32 v25, v25, v212\n\t" \
  "v_mfma_f32_16x16x32_bf16 a[20:23], a[156:159], v[44:47], a[20:23]\n\t" \
  "v_mul_f32 v26, v26, v212\n\t" \
  "v_mul_f32 v27, v27, v212\n\t" \
  "v_mfma_f32_16x16x32_bf16 a[100:103], a[172:175], v[44:47], 0\n\t" \
  "v_mul_f32 v28, v28, v212\n\t" \
  "v_mul_f32 v29, v29, v212\n\t" \
  "v_mfma_f32_16x16x32_bf16 a[100:103], a[168:171], v[40:43], a[100:103]\n\t" \
  "v_mul_f32 v30, v30, v212\n\t" \
  "v_mul_f32 v31, v31, v212\n\t" \
  "v_mfma_f32_16x16x32_bf16 a[100:103], a[164:167], v[36:39], a[100:103]\n\t" \
  "v_cvt_pk_bf16_f32 v68, v24, v25\n\t" \
  "v_cvt_pk_bf16_f32 v69, v26, v27\n\t" \
  "v_mfma_f32_16x16x32_bf16 a[100:103], a[160:163], v[32:35], a[100:103]\n\t" \
  "v_cvt_pk_bf16_f32 v70, v28, v29\n\t" \
  "v_cvt_pk_bf16_f32 v71, v30, v31\n\t" \
  "v_mfma_f32_16x16x32_bf16 a[116:119], a[176:179], v[32:35], 0\n\t" \
  "s_xor_b32 s98, %[eso], 65536\n\t" \
  "buffer_store_dwordx4 v[68:71], v242, %[ws], s98 offen offset:1024 sc1\n\t" \
  "v_mfma_f32_16x16x32_bf16 a[116:119], a[180:183], v[36:39], a[116:119]\n\t" \
  "v_accvgpr_read_b32 v16, a72\n\t" \
  "v_accvgpr_read_b32 v17, a73\n\t" \
  "v_mfma_f32_16x16x32_bf16 a[116:119], a[184:187], v[40:43], a[116:119]\n\t" \
  "v_accvgpr_read_b32 v18, a74\n\t" \
  "v_accvgpr_read_b32 v19, a75\n\t" \
  "v_mfma_f32_16x16x32_bf16 a[116:119], a[188:191], v[44:47], a[116:119]\n\t" \
  "v_accvgpr_read_b32 v20, a88\n\t" \
  "v_accvgpr_read_b32 v21, a89\n\t" \
  "v_mfma_f32_16x16x32_bf16 a[84:87], v[108:111], v[44:47], 0\n\t" \
  "v_accvgpr_read_b32 v22, a90\n\t" \
  "v_accvgpr_read_b32 v23, a91\n\t" \
  "v_mfma_f32_16x16x32_bf16 a[84:87], v[104:107], v[40:43], a[84:87]\n\t" \
  "v_mul_f32 v16, v16, v212\n\t" \
  "v_mul_f32 v17, v17, v212\n\t" \
  "v_mfma_f32_16x16x32_bf16 a[84:87], v[100:103], v[36:39], a[84:87]\n\t" \
  "v_mul_f32 v18, v18, v212\n\t" \
  "v_mul_f32 v19, v19, v212\n\t" \
  "v_mfma_f32_16x16x32_bf16 a[84:87], v[96:99], v[32:35], a[84:87]\n\t" \
  "v_mul_f32 v20, v20, v212\n\t" \
  "v_mul_f32 v21, v21, v212\n\t" \
  "v_mul_f32 v22, v22, v212\n\t" \
  "v_mul_f32 v23, v23, v212\n\t" \
  "v_cvt_pk_bf16_f32 v72, v16, v17\n\t" \
  "v_cvt_pk_bf16_f32 v73, v18, v19\n\t" \
  "v_cvt_pk_bf16_f32 v74, v20, v21\n\t" \
  "v_cvt_pk_bf16_f32 v75, v22, v23\n\t" \
  "v_accvgpr_read_b32 v24, a104\n\t" \
  "v_accvgpr_read_b32 v25, a105\n\t" \
  "v_accvgpr_read_b32 v26, a106\n\t" \
  "v_accvgpr_read_b32 v27, a107\n\t" \
  "v_accvgpr_read_b32 v28, a120\n\t" \
  "v_accvgpr_read_b32 v29, a121\n\t" \
  "v_accvgpr_read_b32 v30, a122\n\t" \
  "v_accvgpr_read_b32 v31, a123\n\t" \
  "v_mul_f32 v24, v24, v212\n\t" \
  "v_mul_f32 v25, v25, v212\n\t" \
  "v_mul_f32 v26, v26, v212\n\t" \
  "v_mul_f32 v27, v27, v212\n\t" \
  "v_mul_f32 v28, v28, v212\n\t" \
  "v_mul_f32 v29, v29, v212\n\t" \
  "v_mul_f32 v30, v30, v212\n\t" \
  "v_mul_f32 v31, v31, v212\n\t" \
  "v_cvt_pk_bf16_f32 v76, v24, v25\n\t" \
  "v_cvt_pk_bf16_f32 v77, v26, v27\n\t" \
  "v_cvt_pk_bf16_f32 v78, v28, v29\n\t" \
  "v_cvt_pk_bf16_f32 v79, v30, v31\n\t" \
  "v_mfma_f32_16x16x32_bf16 a[40:43], v[48:51], v[64:67], 0\n\t" \
  "s_xor_b32 s98, %[eso], 65536\n\t" \
  "buffer_store_dwordx4 v[72:75], v242, %[ws], s98 offen offset:2048 sc1\n\t" \
  "v_mfma_f32_16x16x32_bf16 a[40:43], v[52:55], v[68:71], a[40:43]\n\t" \
  "s_xor_b32 s98, %[eso], 65536\n\t" \
  "buffer_store_dwordx4 v[76:79], v242, %[ws], s98 offen offset:3072 sc1\n\t" \
  "v_mfma_f32_16x16x32_bf16 a[40:43], v[56:59], v[72:75], a[40:43]\n\t" \
  "buffer_load_dwordx4 v[224:227], v252, %[ws], %[ss] offen sc1\n\t" \
  "buffer_load_dwordx4 v[128:131], v247, %[sM], %[gvo] offen\n\t" \
  "v_mfma_f32_16x16x32_bf16 a[40:43], v[60:63], v[76:79], a[40:43]\n\t" \
  "v_rcp_f32 v214, v172\n\t" \
  "v_mfma_f32_16x16x32_bf16 a[56:59], v[92:95], v[76:79], 0\n\t" \
  "v_accvgpr_read_b32 v16, a12\n\t" \
  "v_accvgpr_read_b32 v17, a13\n\t" \
  "v_mfma_f32_16x16x32_bf16 a[56:59], v[88:91], v[72:75], a[56:59]\n\t" \
  "v_accvgpr_read_b32 v18, a14\n\t" \
  "v_accvgpr_read_b32 v19, a15\n\t" \
  "v_mfma_f32_16x16x32_bf16 a[56:59], v[84:87], v[68:71], a[56:59]\n\t" \
  "v_accvgpr_read_b32 v20, a28\n\t" \
  "v_accvgpr_read_b32 v21, a29\n\t" \
  "v_mfma_f32_16x16x32_bf16 a[56:59], v[80:83], v[64:67], a[56:59]\n\t" \
  "v_accvgpr_read_b32 v22, a30\n\t" \
  "v_accvgpr_read_b32 v23, a31\n\t" \
  "v_mfma_f32_16x16x32_bf16 a[72:75], v[112:115], v[64:67], 0\n\t" \
  "v_mul_f32 v16, v16, v214\n\t" \
  "v_mul_f32 v17, v17, v214\n\t" \
  "v_mul_f32 v18, v18, v214\n\t" \
  "v_mul_f32 v19, v19, v214\n\t" \
  "v_mul_f32 v20, v20, v214\n\t" \
  "v_mul_f32 v21, v21, v214\n\t" \
  "v_mul_f32 v22, v22, v214\n\t" \
  "v_mul_f32 v23, v23, v214\n\t" \
  "v_cvt_pk_bf16_f32 v160, v16, v17\n\t" \
  "v_cvt_pk_bf16_f32 v161, v18, v19\n\t" \
  "v_cvt_pk_bf16_f32 v162, v20, v21\n\t" \
  "v_cvt_pk_bf16_f32 v163, v22, v23\n\t" \
  "s_waitcnt lgkmcnt(4)\n\t" \
  "ds_write_b128 v213, v[160:163] offset:32768\n\t" \
  "v_accvgpr_read_b32 v24, a44\n\t" \
  "v_accvgpr_read_b32 v25, a45\n\t" \
  "v_accvgpr_read_b32 v26, a46\n\t" \
  "v_accvgpr_read_b32 v27, a47\n\t" \
  "v_accvgpr_read_b32 v28, a60\n\t" \
  "v_accvgpr_read_b32 v29, a61\n\t" \
  "v_accvgpr_read_b32 v30, a62\n\t" \
  "v_accvgpr_read_b32 v31, a63\n\t" \
  "v_mul_f32 v24, v24, v214\n\t" \
  "v_mul_f32 v25, v25, v214\n\t" \
  "v_mul_f32 v26, v26, v214\n\t" \
  "v_mul_f32 v27, v27, v214\n\t" \
  "v_mul_f32 v28, v28, v214\n\t" \
  "v_mul_f32 v29, v29, v214\n\t" \
  "v_mul_f32 v30, v30, v214\n\t" \
  "v_mul_f32 v31, v31, v214\n\t" \
  "v_cvt_pk_bf16_f32 v164, v24, v25\n\t" \
  "v_cvt_pk_bf16_f32 v165, v26, v27\n\t" \
  "v_cvt_pk_bf16_f32 v166, v28, v29\n\t" \
  "v_cvt_pk_bf16_f32 v167, v30, v31\n\t" \
  "ds_write_b128 v213, v[164:167] offset:33792\n\t" \
  "v_accvgpr_read_b32 v16, a76\n\t" \
  "v_accvgpr_read_b32 v17, a77\n\t" \
  "v_accvgpr_read_b32 v18, a78\n\t" \
  "v_accvgpr_read_b32 v19, a79\n\t" \
  "v_accvgpr_read_b32 v20, a92\n\t" \
  "v_accvgpr_read_b32 v21, a93\n\t" \
  "v_accvgpr_read_b32 v22, a94\n\t" \
  "v_accvgpr_read_b32 v23, a95\n\t" \
  "v_mul_f32 v16, v16, v214\n\t" \
  "v_mul_f32 v17, v17, v214\n\t" \
  "v_mul_f32 v18, v18, v214\n\t" \
  "v_mul_f32 v19, v19, v214\n\t" \
  "v_mul_f32 v20, v20, v214\n\t" \
  "v_mul_f32 v21, v21, v214\n\t" \
  "v_mul_f32 v22, v22, v214\n\t" \
  "v_mul_f32 v23, v23, v214\n\t" \
  "v_cvt_pk_bf16_f32 v168, v16, v17\n\t" \
  "v_cvt_pk_bf16_f32 v169, v18, v19\n\t" \
  "v_cvt_pk_bf16_f32 v170, v20, v21\n\t" \
  "v_cvt_pk_bf16_f32 v171, v22, v23\n\t" \
  "ds_write_b128 v213, v[168:171] offset:34816\n\t" \
  "v_accvgpr_read_b32 v24, a108\n\t" \
  "v_accvgpr_read_b32 v25, a109\n\t" \
  "v_accvgpr_read_b32 v26, a110\n\t" \
  "v_accvgpr_read_b32 v27, a111\n\t" \
  "v_accvgpr_read_b32 v28, a124\n\t" \
  "v_accvgpr_read_b32 v29, a125\n\t" \
  "v_accvgpr_read_b32 v30, a126\n\t" \
  "v_accvgpr_read_b32 v31, a127\n\t" \
  "v_mul_f32 v24, v24, v214\n\t" \
  "v_mul_f32 v25, v25, v214\n\t" \
  "v_mul_f32 v26, v26, v214\n\t" \
  "v_mul_f32 v27, v27, v214\n\t" \
  "v_mul_f32 v28, v28, v214\n\t" \
  "v_mul_f32 v29, v29, v214\n\t" \
  "v_mul_f32 v30, v30, v214\n\t" \
  "v_mul_f32 v31, v31, v214\n\t" \
  "v_cvt_pk_bf16_f32 v172, v24, v25\n\t" \
  "v_cvt_pk_bf16_f32 v173, v26, v27\n\t" \
  "v_cvt_pk_bf16_f32 v174, v28, v29\n\t" \
  "v_cvt_pk_bf16_f32 v175, v30, v31\n\t" \
  "ds_write_b128 v213, v[172:175] offset:35840\n\t" \
  "v_mfma_f32_16x16x32_bf16 a[72:75], v[116:119], v[68:71], a[72:75]\n\t" \
  "s_add_u32 s98, %[gvo], 12288\n\t" \
  "buffer_load_dwordx4 v[132:135], v247, %[sM], s98 offen\n\t" \
  "v_mfma_f32_16x16x32_bf16 a[72:75], v[120:123], v[72:75], a[72:75]\n\t" \
  "s_add_u32 s98, %[gvo], 24576\n\t" \
  "buffer_load_dwordx4 v[136:139], v247, %[sM], s98 offen\n\t" \
  "v_mfma_f32_16x16x32_bf16 a[72:75], v[124:127], v[76:79], a[72:75]\n\t" \
  "s_add_u32 s98, %[gvo], 36864\n\t" \
  "buffer_load_dwordx4 v[140:143], v247, %[sM], s98 offen\n\t" \
  "s_add_u32 m0, %[m], 147456\n\t" \
  "s_xor_b32 s97, %[wob], 655360\n\t" \
  "buffer_load_dwordx4 v240, %[wo], s97 offen offset:0 lds\n\t" \
  "s_add_u32 s97, s97, 7168\n\t" \
  "buffer_load_dwordx4 v241, %[wo], s97 offen offset:1024 lds\n\t" \
  "s_add_u32 s97, s97, 7168\n\t" \
  "buffer_load_dwordx4 v240, %[wo], s97 offen offset:2048 lds\n\t" \
  "s_add_u32 s97, s97, 7168\n\t" \
  "buffer_load_dwordx4 v241, %[wo], s97 offen offset:3072 lds\n\t" \
  "s_add_u32 s98, %[gvo], 49152\n\t" \
  "buffer_load_dwordx4 v[144:147], v247, %[sM], s98 offen\n\t" \
  "s_add_u32 s98, %[gvo], 61440\n\t" \
  "buffer_load_dwordx4 v[148:151], v247, %[sM], s98 offen\n\t" \
  "s_add_u32 s98, %[gvo], 73728\n\t" \
  "buffer_load_dwordx4 v[152:155], v247, %[sM], s98 offen\n\t" \
  "s_add_u32 s98, %[gvo], 86016\n\t" \
  "buffer_load_dwordx4 v[156:159], v247, %[sM], s98 offen\n\t" \
  "s_waitcnt vmcnt(0)\n\t" \
  "s_branch .Lat_end\n\t" \
  ".Lsf_c00:\n\t" \
  "s_mov_b32 vcc_lo, 0\n\t" \
  "s_branch .Lsf_s0\n\t" \
  ".Lsf_c10:\n\t" \
  "s_mov_b32 vcc_lo, 1\n\t" \
  "s_branch .Lsf_s0\n\t" \
  ".Lsf_c20:\n\t" \
  "s_mov_b32 vcc_lo, 2\n\t" \
  "s_branch .Lsf_s0\n\t" \
  ".Lsf_c30:\n\t" \
  "s_mov_b32 vcc_lo, 3\n\t" \
  "s_branch .Lsf_s0\n\t" \
  ".Lsf_c40:\n\t" \
  "s_mov_b32 vcc_lo, 4\n\t" \
  "s_branch .Lsf_s0\n\t" \
  ".Lsf_c50:\n\t" \
  "s_mov_b32 vcc_lo, 5\n\t" \
  "s_branch .Lsf_s0\n\t" \
  ".Lsf_c60:\n\t" \
  "s_mov_b32 vcc_lo, 6\n\t" \
  "s_branch .Lsf_s0\n\t" \
  ".Lsf_c70:\n\t" \
  "s_mov_b32 vcc_lo, 7\n\t" \
  "s_branch .Lsf_s0\n\t" \
  ".Lsf_s0:\n\t" \
  "s_nop 7\n\t" \
  "s_nop 1\n\t" \
  "v_mov_b32 v193, v192\n\t" \
  "s_nop 1\n\t" \
  "v_permlane32_swap_b32 v192, v193\n\t" \
  "s_nop 1\n\t" \
  "v_max_f32 v192, v192, v193\n\t" \
  "s_nop 1\n\t" \
  "v_mov_b32 v193, v192\n\t" \
  "s_nop 1\n\t" \
  "v_permlane16_swap_b32 v192, v193\n\t" \
  "s_nop 1\n\t" \
  "v_max_f32 v192, v192, v193\n\t" \
  "v_max_f32 v192, 0, v192\n\t" \
  "v_sub_f32 v0, v0, v192\n\t" \
  "v_sub_f32 v1, v1, v192\n\t" \
  "v_sub_f32 v2, v2, v192\n\t" \
  "v_sub_f32 v3, v3, v192\n\t" \
  "v_sub_f32 v4, v4, v192\n\t" \
  "v_sub_f32 v5, v5, v192\n\t" \
  "v_sub_f32 v6, v6, v192\n\t" \
  "v_sub_f32 v7, v7, v192\n\t" \
  "v_sub_f32 v8, v8, v192\n\t" \
  "v_sub_f32 v9, v9, v192\n\t" \
  "v_sub_f32 v10, v10, v192\n\t" \
  "v_sub_f32 v11, v11, v192\n\t" \
  "v_sub_f32 v12, v12, v192\n\t" \
  "v_sub_f32 v13, v13, v192\n\t" \
  "v_sub_f32 v14, v14, v192\n\t" \
  "v_sub_f32 v15, v15, v192\n\t" \
  "v_sub_f32 v176, v176, v192\n\t" \
  "v_sub_f32 v177, v177, v192\n\t" \
  "v_sub_f32 v178, v178, v192\n\t" \
  "v_sub_f32 v179, v179, v192\n\t" \
  "v_exp_f32 v196, -v192\n\t" \
  "s_mov_b32 s99, 1\n\t" \
  "s_nop 1\n\t" \
  "s_cselect_b32 vcc_hi, 1, 0\n\t" \
  "s_cmp_eq_u32 vcc_lo, 0\n\t" \
  "s_cbranch_scc1 .Lsf_b00\n\t" \
  "s_cmp_eq_u32 vcc_lo, 1\n\t" \
  "s_cbranch_scc1 .Lsf_b10\n\t" \
  "s_cmp_eq_u32 vcc_lo, 2\n\t" \
  "s_cbranch_scc1 .Lsf_b20\n\t" \
  "s_cmp_eq_u32 vcc_lo, 3\n\t" \
  "s_cbranch_scc1 .Lsf_b30\n\t" \
  "s_cmp_eq_u32 vcc_lo, 4\n\t" \
  "s_cbranch_scc1 .Lsf_b40\n\t" \
  "s_cmp_eq_u32 vcc_lo, 5\n\t" \
  "s_cbranch_scc1 .Lsf_b50\n\t" \
  "s_cmp_eq_u32 vcc_lo, 6\n\t" \
  "s_cbranch_scc1 .Lsf_b60\n\t" \
  "s_cmp_eq_u32 vcc_lo, 7\n\t" \
  "s_cbranch_scc1 .Lsf_b70\n\t" \
  ".Lsf_b00:\n\t" \
  "s_cmp_lg_u32 vcc_hi, 0\n\t" \
  "s_branch .Lsf_r00\n\t" \
  ".Lsf_b10:\n\t" \
  "s_cmp_lg_u32 vcc_hi, 0\n\t" \
  "s_branch .Lsf_r10\n\t" \
  ".Lsf_b20:\n\t" \
  "s_cmp_lg_u32 vcc_hi, 0\n\t" \
  "s_branch .Lsf_r20\n\t" \
  ".Lsf_b30:\n\t" \
  "s_cmp_lg_u32 vcc_hi, 0\n\t" \
  "s_branch .Lsf_r30\n\t" \
  ".Lsf_b40:\n\t" \
  "s_cmp_lg_u32 vcc_hi, 0\n\t" \
  "s_branch .Lsf_r40\n\t" \
  ".Lsf_b50:\n\t" \
  "s_cmp_lg_u32 vcc_hi, 0\n\t" \
  "s_branch .Lsf_r50\n\t" \
  ".Lsf_b60:\n\t" \
  "s_cmp_lg_u32 vcc_hi, 0\n\t" \
  "s_branch .Lsf_r60\n\t" \
  ".Lsf_b70:\n\t" \
  "s_cmp_lg_u32 vcc_hi, 0\n\t" \
  "s_branch .Lsf_r70\n\t" \
  ".Lsf_c01:\n\t" \
  "s_mov_b32 vcc_lo, 0\n\t" \
  "s_branch .Lsf_s1\n\t" \
  ".Lsf_c11:\n\t" \
  "s_mov_b32 vcc_lo, 1\n\t" \
  "s_branch .Lsf_s1\n\t" \
  ".Lsf_c21:\n\t" \
  "s_mov_b32 vcc_lo, 2\n\t" \
  "s_branch .Lsf_s1\n\t" \
  ".Lsf_c31:\n\t" \
  "s_mov_b32 vcc_lo, 3\n\t" \
  "s_branch .Lsf_s1\n\t" \
  ".Lsf_c41:\n\t" \
  "s_mov_b32 vcc_lo, 4\n\t" \
  "s_branch .Lsf_s1\n\t" \
  ".Lsf_c51:\n\t" \
  "s_mov_b32 vcc_lo, 5\n\t" \
  "s_branch .Lsf_s1\n\t" \
  ".Lsf_c61:\n\t" \
  "s_mov_b32 vcc_lo, 6\n\t" \
  "s_branch .Lsf_s1\n\t" \
  ".Lsf_c71:\n\t" \
  "s_mov_b32 vcc_lo, 7\n\t" \
  "s_branch .Lsf_s1\n\t" \
  ".Lsf_s1:\n\t" \
  "s_nop 7\n\t" \
  "s_nop 1\n\t" \
  "v_mov_b32 v193, v192\n\t" \
  "s_nop 1\n\t" \
  "v_permlane32_swap_b32 v192, v193\n\t" \
  "s_nop 1\n\t" \
  "v_max_f32 v192, v192, v193\n\t" \
  "s_nop 1\n\t" \
  "v_mov_b32 v193, v192\n\t" \
  "s_nop 1\n\t" \
  "v_permlane16_swap_b32 v192, v193\n\t" \
  "s_nop 1\n\t" \
  "v_max_f32 v192, v192, v193\n\t" \
  "v_max_f32 v192, 0, v192\n\t" \
  "v_sub_f32 v32, v32, v192\n\t" \
  "v_sub_f32 v33, v33, v192\n\t" \
  "v_sub_f32 v34, v34, v192\n\t" \
  "v_sub_f32 v35, v35, v192\n\t" \
  "v_sub_f32 v36, v36, v192\n\t" \
  "v_sub_f32 v37, v37, v192\n\t" \
  "v_sub_f32 v38, v38, v192\n\t" \
  "v_sub_f32 v39, v39, v192\n\t" \
  "v_sub_f32 v40, v40, v192\n\t" \
  "v_sub_f32 v41, v41, v192\n\t" \
  "v_sub_f32 v42, v42, v192\n\t" \
  "v_sub_f32 v43, v43, v192\n\t" \
  "v_sub_f32 v44, v44, v192\n\t" \
  "v_sub_f32 v45, v45, v192\n\t" \
  "v_sub_f32 v46, v46, v192\n\t" \
  "v_sub_f32 v47, v47, v192\n\t" \
  "v_sub_f32 v180, v180, v192\n\t" \
  "v_sub_f32 v181, v181, v192\n\t" \
  "v_sub_f32 v182, v182, v192\n\t" \
  "v_sub_f32 v183, v183, v192\n\t" \
  "v_exp_f32 v197, -v192\n\t" \
  "s_mov_b32 s99, 1\n\t" \
  "s_nop 1\n\t" \
  "s_cselect_b32 vcc_hi, 1, 0\n\t" \
  "s_cmp_eq_u32 vcc_lo, 0\n\t" \
  "s_cbranch_scc1 .Lsf_b01\n\t" \
  "s_cmp_eq_u32 vcc_lo, 1\n\t" \
  "s_cbranch_scc1 .Lsf_b11\n\t" \
  "s_cmp_eq_u32 vcc_lo, 2\n\t" \
  "s_cbranch_scc1 .Lsf_b21\n\t" \
  "s_cmp_eq_u32 vcc_lo, 3\n\t" \
  "s_cbranch_scc1 .Lsf_b31\n\t" \
  "s_cmp_eq_u32 vcc_lo, 4\n\t" \
  "s_cbranch_scc1 .Lsf_b41\n\t" \
  "s_cmp_eq_u32 vcc_lo, 5\n\t" \
  "s_cbranch_scc1 .Lsf_b51\n\t" \
  "s_cmp_eq_u32 vcc_lo, 6\n\t" \
  "s_cbranch_scc1 .Lsf_b61\n\t" \
  "s_cmp_eq_u32 vcc_lo, 7\n\t" \
  "s_cbranch_scc1 .Lsf_b71\n\t" \
  ".Lsf_b01:\n\t" \
  "s_cmp_lg_u32 vcc_hi, 0\n\t" \
  "s_branch .Lsf_r01\n\t" \
  ".Lsf_b11:\n\t" \
  "s_cmp_lg_u32 vcc_hi, 0\n\t" \
  "s_branch .Lsf_r11\n\t" \
  ".Lsf_b21:\n\t" \
  "s_cmp_lg_u32 vcc_hi, 0\n\t" \
  "s_branch .Lsf_r21\n\t" \
  ".Lsf_b31:\n\t" \
  "s_cmp_lg_u32 vcc_hi, 0\n\t" \
  "s_branch .Lsf_r31\n\t" \
  ".Lsf_b41:\n\t" \
  "s_cmp_lg_u32 vcc_hi, 0\n\t" \
  "s_branch .Lsf_r41\n\t" \
  ".Lsf_b51:\n\t" \
  "s_cmp_lg_u32 vcc_hi, 0\n\t" \
  "s_branch .Lsf_r51\n\t" \
  ".Lsf_b61:\n\t" \
  "s_cmp_lg_u32 vcc_hi, 0\n\t" \
  "s_branch .Lsf_r61\n\t" \
  ".Lsf_b71:\n\t" \
  "s_cmp_lg_u32 vcc_hi, 0\n\t" \
  "s_branch .Lsf_r71\n\t" \
  ".Lsf_c02:\n\t" \
  "s_mov_b32 vcc_lo, 0\n\t" \
  "s_branch .Lsf_s2\n\t" \
  ".Lsf_c12:\n\t" \
  "s_mov_b32 vcc_lo, 1\n\t" \
  "s_branch .Lsf_s2\n\t" \
  ".Lsf_c22:\n\t" \
  "s_mov_b32 vcc_lo, 2\n\t" \
  "s_branch .Lsf_s2\n\t" \
  ".Lsf_c32:\n\t" \
  "s_mov_b32 vcc_lo, 3\n\t" \
  "s_branch .Lsf_s2\n\t" \
  ".Lsf_c42:\n\t" \
  "s_mov_b32 vcc_lo, 4\n\t" \
  "s_branch .Lsf_s2\n\t" \
  ".Lsf_c52:\n\t" \
  "s_mov_b32 vcc_lo, 5\n\t" \
  "s_branch .Lsf_s2\n\t" \
  ".Lsf_c62:\n\t" \
  "s_mov_b32 vcc_lo, 6\n\t" \
  "s_branch .Lsf_s2\n\t" \
  ".Lsf_c72:\n\t" \
  "s_mov_b32 vcc_lo, 7\n\t" \
  "s_branch .Lsf_s2\n\t" \
  ".Lsf_s2:\n\t" \
  "s_nop 7\n\t" \
  "s_nop 1\n\t" \
  "v_mov_b32 v193, v192\n\t" \
  "s_nop 1\n\t" \
  "v_permlane32_swap_b32 v192, v193\n\t" \
  "s_nop 1\n\t" \
  "v_max_f32 v192, v192, v193\n\t" \
  "s_nop 1\n\t" \
  "v_mov_b32 v193, v192\n\t" \
  "s_nop 1\n\t" \
  "v_permlane16_swap_b32 v192, v193\n\t" \
  "s_nop 1\n\t" \
  "v_max_f32 v192, v192, v193\n\t" \
  "v_max_f32 v192, 0, v192\n\t" \
  "v_sub_f32 v64, v64, v192\n\t" \
  "v_sub_f32 v65, v65, v192\n\t" \
  "v_sub_f32 v66, v66, v192\n\t" \
  "v_sub_f32 v67, v67, v192\n\t" \
  "v_sub_f32 v68, v68, v192\n\t" \
  "v_sub_f32 v69, v69, v192\n\t" \
  "v_sub_f32 v70, v70, v192\n\t" \
  "v_sub_f32 v71, v71, v192\n\t" \
  "v_sub_f32 v72, v72, v192\n\t" \
  "v_sub_f32 v73, v73, v192\n\t" \
  "v_sub_f32 v74, v74, v192\n\t" \
  "v_sub_f32 v75, v75, v192\n\t" \
  "v_sub_f32 v76, v76, v192\n\t" \
  "v_sub_f32 v77, v77, v192\n\t" \
  "v_sub_f32 v78, v78, v192\n\t" \
  "v_sub_f32 v79, v79, v192\n\t" \
  "v_sub_f32 v184, v184, v192\n\t" \
  "v_sub_f32 v185, v185, v192\n\t" \
  "v_sub_f32 v186, v186, v192\n\t" \
  "v_sub_f32 v187, v187, v192\n\t" \
  "v_exp_f32 v198, -v192\n\t" \
  "s_mov_b32 s99, 1\n\t" \
  "s_nop 1\n\t" \
  "s_cselect_b32 vcc_hi, 1, 0\n\t" \
  "s_cmp_eq_u32 vcc_lo, 0\n\t" \
  "s_cbranch_scc1 .Lsf_b02\n\t" \
  "s_cmp_eq_u32 vcc_lo, 1\n\t" \
  "s_cbranch_scc1 .Lsf_b12\n\t" \
  "s_cmp_eq_u32 vcc_lo, 2\n\t" \
  "s_cbranch_scc1 .Lsf_b22\n\t" \
  "s_cmp_eq_u32 vcc_lo, 3\n\t" \
  "s_cbranch_scc1 .Lsf_b32\n\t" \
  "s_cmp_eq_u32 vcc_lo, 4\n\t" \
  "s_cbranch_scc1 .Lsf_b42\n\t" \
  "s_cmp_eq_u32 vcc_lo, 5\n\t" \
  "s_cbranch_scc1 .Lsf_b52\n\t" \
  "s_cmp_eq_u32 vcc_lo, 6\n\t" \
  "s_cbranch_scc1 .Lsf_b62\n\t" \
  "s_cmp_eq_u32 vcc_lo, 7\n\t" \
  "s_cbranch_scc1 .Lsf_b72\n\t" \
  ".Lsf_b02:\n\t" \
  "s_cmp_lg_u32 vcc_hi, 0\n\t" \
  "s_branch .Lsf_r02\n\t" \
  ".Lsf_b12:\n\t" \
  "s_cmp_lg_u32 vcc_hi, 0\n\t" \
  "s_branch .Lsf_r12\n\t" \
  ".Lsf_b22:\n\t" \
  "s_cmp_lg_u32 vcc_hi, 0\n\t" \
  "s_branch .Lsf_r22\n\t" \
  ".Lsf_b32:\n\t" \
  "s_cmp_lg_u32 vcc_hi, 0\n\t" \
  "s_branch .Lsf_r32\n\t" \
  ".Lsf_b42:\n\t" \
  "s_cmp_lg_u32 vcc_hi, 0\n\t" \
  "s_branch .Lsf_r42\n\t" \
  ".Lsf_b52:\n\t" \
  "s_cmp_lg_u32 vcc_hi, 0\n\t" \
  "s_branch .Lsf_r52\n\t" \
  ".Lsf_b62:\n\t" \
  "s_cmp_lg_u32 vcc_hi, 0\n\t" \
  "s_branch .Lsf_r62\n\t" \
  ".Lsf_b72:\n\t" \
  "s_cmp_lg_u32 vcc_hi, 0\n\t" \
  "s_branch .Lsf_r72\n\t" \
  ".Lsf_c03:\n\t" \
  "s_mov_b32 vcc_lo, 0\n\t" \
  "s_branch .Lsf_s3\n\t" \
  ".Lsf_c13:\n\t" \
  "s_mov_b32 vcc_lo, 1\n\t" \
  "s_branch .Lsf_s3\n\t" \
  ".Lsf_c23:\n\t" \
  "s_mov_b32 vcc_lo, 2\n\t" \
  "s_branch .Lsf_s3\n\t" \
  ".Lsf_c33:\n\t" \
  "s_mov_b32 vcc_lo, 3\n\t" \
  "s_branch .Lsf_s3\n\t" \
  ".Lsf_c43:\n\t" \
  "s_mov_b32 vcc_lo, 4\n\t" \
  "s_branch .Lsf_s3\n\t" \
  ".Lsf_c53:\n\t" \
  "s_mov_b32 vcc_lo, 5\n\t" \
  "s_branch .Lsf_s3\n\t" \
  ".Lsf_c63:\n\t" \
  "s_mov_b32 vcc_lo, 6\n\t" \
  "s_branch .Lsf_s3\n\t" \
  ".Lsf_c73:\n\t" \
  "s_mov_b32 vcc_lo, 7\n\t" \
  "s_branch .Lsf_s3\n\t" \
  ".Lsf_s3:\n\t" \
  "s_nop 7\n\t" \
  "s_nop 1\n\t" \
  "v_mov_b32 v193, v192\n\t" \
  "s_nop 1\n\t" \
  "v_permlane32_swap_b32 v192, v193\n\t" \
  "s_nop 1\n\t" \
  "v_max_f32 v192, v192, v193\n\t" \
  "s_nop 1\n\t" \
  "v_mov_b32 v193, v192\n\t" \
  "s_nop 1\n\t" \
  "v_permlane16_swap_b32 v192, v193\n\t" \
  "s_nop 1\n\t" \
  "v_max_f32 v192, v192, v193\n\t" \
  "v_max_f32 v192, 0, v192\n\t" \
  "v_sub_f32 v96, v96, v192\n\t" \
  "v_sub_f32 v97, v97, v192\n\t" \
  "v_sub_f32 v98, v98, v192\n\t" \
  "v_sub_f32 v99, v99, v192\n\t" \
  "v_sub_f32 v100, v100, v192\n\t" \
  "v_sub_f32 v101, v101, v192\n\t" \
  "v_sub_f32 v102, v102, v192\n\t" \
  "v_sub_f32 v103, v103, v192\n\t" \
  "v_sub_f32 v104, v104, v192\n\t" \
  "v_sub_f32 v105, v105, v192\n\t" \
  "v_sub_f32 v106, v106, v192\n\t" \
  "v_sub_f32 v107, v107, v192\n\t" \
  "v_sub_f32 v108, v108, v192\n\t" \
  "v_sub_f32 v109, v109, v192\n\t" \
  "v_sub_f32 v110, v110, v192\n\t" \
  "v_sub_f32 v111, v111, v192\n\t" \
  "v_sub_f32 v188, v188, v192\n\t" \
  "v_sub_f32 v189, v189, v192\n\t" \
  "v_sub_f32 v190, v190, v192\n\t" \
  "v_sub_f32 v191, v191, v192\n\t" \
  "v_exp_f32 v199, -v192\n\t" \
  "s_mov_b32 s99, 1\n\t" \
  "s_nop 1\n\t" \
  "s_cselect_b32 vcc_hi, 1, 0\n\t" \
  "s_cmp_eq_u32 vcc_lo, 0\n\t" \
  "s_cbranch_scc1 .Lsf_b03\n\t" \
  "s_cmp_eq_u32 vcc_lo, 1\n\t" \
  "s_cbranch_scc1 .Lsf_b13\n\t" \
  "s_cmp_eq_u32 vcc_lo, 2\n\t" \
  "s_cbranch_scc1 .Lsf_b23\n\t" \
  "s_cmp_eq_u32 vcc_lo, 3\n\t" \
  "s_cbranch_scc1 .Lsf_b33\n\t" \
  "s_cmp_eq_u32 vcc_lo, 4\n\t" \
  "s_cbranch_scc1 .Lsf_b43\n\t" \
  "s_cmp_eq_u32 vcc_lo, 5\n\t" \
  "s_cbranch_scc1 .Lsf_b53\n\t" \
  "s_cmp_eq_u32 vcc_lo, 6\n\t" \
  "s_cbranch_scc1 .Lsf_b63\n\t" \
  "s_cmp_eq_u32 vcc_lo, 7\n\t" \
  "s_cbranch_scc1 .Lsf_b73\n\t" \
  ".Lsf_b03:\n\t" \
  "s_cmp_lg_u32 vcc_hi, 0\n\t" \
  "s_branch .Lsf_r03\n\t" \
  ".Lsf_b13:\n\t" \
  "s_cmp_lg_u32 vcc_hi, 0\n\t" \
  "s_branch .Lsf_r13\n\t" \
  ".Lsf_b23:\n\t" \
  "s_cmp_lg_u32 vcc_hi, 0\n\t" \
  "s_branch .Lsf_r23\n\t" \
  ".Lsf_b33:\n\t" \
  "s_cmp_lg_u32 vcc_hi, 0\n\t" \
  "s_branch .Lsf_r33\n\t" \
  ".Lsf_b43:\n\t" \
  "s_cmp_lg_u32 vcc_hi, 0\n\t" \
  "s_branch .Lsf_r43\n\t" \
  ".Lsf_b53:\n\t" \
  "s_cmp_lg_u32 vcc_hi, 0\n\t" \
  "s_branch .Lsf_r53\n\t" \
  ".Lsf_b63:\n\t" \
  "s_cmp_lg_u32 vcc_hi, 0\n\t" \
  "s_branch .Lsf_r63\n\t" \
  ".Lsf_b73:\n\t" \
  "s_cmp_lg_u32 vcc_hi, 0\n\t" \
  "s_branch .Lsf_r73\n\t" \
  ".Lsf_p0:\n\t" \
  "s_mov_b32 vcc_lo, 0\n\t" \
  "s_branch .Lsf_rs\n\t" \
  ".Lsf_p1:\n\t" \
  "s_mov_b32 vcc_lo, 1\n\t" \
  "s_branch .Lsf_rs\n\t" \
  ".Lsf_p2:\n\t" \
  "s_mov_b32 vcc_lo, 2\n\t" \
  "s_branch .Lsf_rs\n\t" \
  ".Lsf_p3:\n\t" \
  "s_mov_b32 vcc_lo, 3\n\t" \
  "s_branch .Lsf_rs\n\t" \
  ".Lsf_p4:\n\t" \
  "s_mov_b32 vcc_lo, 4\n\t" \
  "s_branch .Lsf_rs\n\t" \
  ".Lsf_p5:\n\t" \
  "s_mov_b32 vcc_lo, 5\n\t" \
  "s_branch .Lsf_rs\n\t" \
  ".Lsf_p6:\n\t" \
  "s_mov_b32 vcc_lo, 6\n\t" \
  "s_branch .Lsf_rs\n\t" \
  ".Lsf_p7:\n\t" \
  "s_mov_b32 vcc_lo, 7\n\t" \
  "s_branch .Lsf_rs\n\t" \
  ".Lsf_rs:\n\t" \
  "s_nop 7\n\t" \
  "s_nop 7\n\t" \
  "v_accvgpr_read_b32 v193, a0\n\t" \
  "v_mul_f32 v193, v196, v193\n\t" \
  "v_accvgpr_write_b32 a0, v193\n\t" \
  "v_accvgpr_read_b32 v193, a1\n\t" \
  "v_mul_f32 v193, v196, v193\n\t" \
  "v_accvgpr_write_b32 a1, v193\n\t" \
  "v_accvgpr_read_b32 v193, a2\n\t" \
  "v_mul_f32 v193, v196, v193\n\t" \
  "v_accvgpr_write_b32 a2, v193\n\t" \
  "v_accvgpr_read_b32 v193, a3\n\t" \
  "v_mul_f32 v193, v196, v193\n\t" \
  "v_accvgpr_write_b32 a3, v193\n\t" \
  "v_accvgpr_read_b32 v193, a4\n\t" \
  "v_mul_f32 v193, v197, v193\n\t" \
  "v_accvgpr_write_b32 a4, v193\n\t" \
  "v_accvgpr_read_b32 v193, a5\n\t" \
  "v_mul_f32 v193, v197, v193\n\t" \
  "v_accvgpr_write_b32 a5, v193\n\t" \
  "v_accvgpr_read_b32 v193, a6\n\t" \
  "v_mul_f32 v193, v197, v193\n\t" \
  "v_accvgpr_write_b32 a6, v193\n\t" \
  "v_accvgpr_read_b32 v193, a7\n\t" \
  "v_mul_f32 v193, v197, v193\n\t" \
  "v_accvgpr_write_b32 a7, v193\n\t" \
  "v_accvgpr_read_b32 v193, a8\n\t" \
  "v_mul_f32 v193, v198, v193\n\t" \
  "v_accvgpr_write_b32 a8, v193\n\t" \
  "v_accvgpr_read_b32 v193, a9\n\t" \
  "v_mul_f32 v193, v198, v193\n\t" \
  "v_accvgpr_write_b32 a9, v193\n\t" \
  "v_accvgpr_read_b32 v193, a10\n\t" \
  "v_mul_f32 v193, v198, v193\n\t" \
  "v_accvgpr_write_b32 a10, v193\n\t" \
  "v_accvgpr_read_b32 v193, a11\n\t" \
  "v_mul_f32 v193, v198, v193\n\t" \
  "v_accvgpr_write_b32 a11, v193\n\t" \
  "v_accvgpr_read_b32 v193, a12\n\t" \
  "v_mul_f32 v193, v199, v193\n\t" \
  "v_accvgpr_write_b32 a12, v193\n\t" \
  "v_accvgpr_read_b32 v193, a13\n\t" \
  "v_mul_f32 v193, v199, v193\n\t" \
  "v_accvgpr_write_b32 a13, v193\n\t" \
  "v_accvgpr_read_b32 v193, a14\n\t" \
  "v_mul_f32 v193, v199, v193\n\t" \
  "v_accvgpr_write_b32 a14, v193\n\t" \
  "v_accvgpr_read_b32 v193, a15\n\t" \
  "v_mul_f32 v193, v199, v193\n\t" \
  "v_accvgpr_write_b32 a15, v193\n\t" \
  "v_accvgpr_read_b32 v193, a16\n\t" \
  "v_mul_f32 v193, v196, v193\n\t" \
  "v_accvgpr_write_b32 a16, v193\n\t" \
  "v_accvgpr_read_b32 v193, a17\n\t" \
  "v_mul_f32 v193, v196, v193\n\t" \
  "v_accvgpr_write_b32 a17, v193\n\t" \
  "v_accvgpr_read_b32 v193, a18\n\t" \
  "v_mul_f32 v193, v196, v193\n\t" \
  "v_accvgpr_write_b32 a18, v193\n\t" \
  "v_accvgpr_read_b32 v193, a19\n\t" \
  "v_mul_f32 v193, v196, v193\n\t" \
  "v_accvgpr_write_b32 a19, v193\n\t" \
  "v_accvgpr_read_b32 v193, a20\n\t" \
  "v_mul_f32 v193, v197, v193\n\t" \
  "v_accvgpr_write_b32 a20, v193\n\t" \
  "v_accvgpr_read_b32 v193, a21\n\t" \
  "v_mul_f32 v193, v197, v193\n\t" \
  "v_accvgpr_write_b32 a21, v193\n\t" \
  "v_accvgpr_read_b32 v193, a22\n\t" \
  "v_mul_f32 v193, v197, v193\n\t" \
  "v_accvgpr_write_b32 a22, v193\n\t" \
  "v_accvgpr_read_b32 v193, a23\n\t" \
  "v_mul_f32 v193, v197, v193\n\t" \
  "v_accvgpr_write_b32 a23, v193\n\t" \
  "v_accvgpr_read_b32 v193, a24\n\t" \
  "v_mul_f32 v193, v198, v193\n\t" \
  "v_accvgpr_write_b32 a24, v193\n\t" \
  "v_accvgpr_read_b32 v193, a25\n\t" \
  "v_mul_f32 v193, v198, v193\n\t" \
  "v_accvgpr_write_b32 a25, v193\n\t" \
  "v_accvgpr_read_b32 v193, a26\n\t" \
  "v_mul_f32 v193, v198, v193\n\t" \
  "v_accvgpr_write_b32 a26, v193\n\t" \
  "v_accvgpr_read_b32 v193, a27\n\t" \
  "v_mul_f32 v193, v198, v193\n\t" \
  "v_accvgpr_write_b32 a27, v193\n\t" \
  "v_accvgpr_read_b32 v193, a28\n\t" \
  "v_mul_f32 v193, v199, v193\n\t" \
  "v_accvgpr_write_b32 a28, v193\n\t" \
  "v_accvgpr_read_b32 v193, a29\n\t" \
  "v_mul_f32 v193, v199, v193\n\t" \
  "v_accvgpr_write_b32 a29, v193\n\t" \
  "v_accvgpr_read_b32 v193, a30\n\t" \
  "v_mul_f32 v193, v199, v193\n\t" \
  "v_accvgpr_write_b32 a30, v193\n\t" \
  "v_accvgpr_read_b32 v193, a31\n\t" \
  "v_mul_f32 v193, v199, v193\n\t" \
  "v_accvgpr_write_b32 a31, v193\n\t" \
  "v_accvgpr_read_b32 v193, a32\n\t" \
  "v_mul_f32 v193, v196, v193\n\t" \
  "v_accvgpr_write_b32 a32, v193\n\t" \
  "v_accvgpr_read_b32 v193, a33\n\t" \
  "v_mul_f32 v193, v196, v193\n\t" \
  "v_accvgpr_write_b32 a33, v193\n\t" \
  "v_accvgpr_read_b32 v193, a34\n\t" \
  "v_mul_f32 v193, v196, v193\n\t" \
  "v_accvgpr_write_b32 a34, v193\n\t" \
  "v_accvgpr_read_b32 v193, a35\n\t" \
  "v_mul_f32 v193, v196, v193\n\t" \
  "v_accvgpr_write_b32 a35, v193\n\t" \
  "v_accvgpr_read_b32 v193, a36\n\t" \
  "v_mul_f32 v193, v197, v193\n\t" \
  "v_accvgpr_write_b32 a36, v193\n\t" \
  "v_accvgpr_read_b32 v193, a37\n\t" \
  "v_mul_f32 v193, v197, v193\n\t" \
  "v_accvgpr_write_b32 a37, v193\n\t" \
  "v_accvgpr_read_b32 v193, a38\n\t" \
  "v_mul_f32 v193, v197, v193\n\t" \
  "v_accvgpr_write_b32 a38, v193\n\t" \
  "v_accvgpr_read_b32 v193, a39\n\t" \
  "v_mul_f32 v193, v197, v193\n\t" \
  "v_accvgpr_write_b32 a39, v193\n\t" \
  "v_accvgpr_read_b32 v193, a40\n\t" \
  "v_mul_f32 v193, v198, v193\n\t" \
  "v_accvgpr_write_b32 a40, v193\n\t" \
  "v_accvgpr_read_b32 v193, a41\n\t" \
  "v_mul_f32 v193, v198, v193\n\t" \
  "v_accvgpr_write_b32 a41, v193\n\t" \
  "v_accvgpr_read_b32 v193, a42\n\t" \
  "v_mul_f32 v193, v198, v193\n\t" \
  "v_accvgpr_write_b32 a42, v193\n\t" \
  "v_accvgpr_read_b32 v193, a43\n\t" \
  "v_mul_f32 v193, v198, v193\n\t" \
  "v_accvgpr_write_b32 a43, v193\n\t" \
  "v_accvgpr_read_b32 v193, a44\n\t" \
  "v_mul_f32 v193, v199, v193\n\t" \
  "v_accvgpr_write_b32 a44, v193\n\t" \
  "v_accvgpr_read_b32 v193, a45\n\t" \
  "v_mul_f32 v193, v199, v193\n\t" \
  "v_accvgpr_write_b32 a45, v193\n\t" \
  "v_accvgpr_read_b32 v193, a46\n\t" \
  "v_mul_f32 v193, v199, v193\n\t" \
  "v_accvgpr_write_b32 a46, v193\n\t" \
  "v_accvgpr_read_b32 v193, a47\n\t" \
  "v_mul_f32 v193, v199, v193\n\t" \
  "v_accvgpr_write_b32 a47, v193\n\t" \
  "v_accvgpr_read_b32 v193, a48\n\t" \
  "v_mul_f32 v193, v196, v193\n\t" \
  "v_accvgpr_write_b32 a48, v193\n\t" \
  "v_accvgpr_read_b32 v193, a49\n\t" \
  "v_mul_f32 v193, v196, v193\n\t" \
  "v_accvgpr_write_b32 a49, v193\n\t" \
  "v_accvgpr_read_b32 v193, a50\n\t" \
  "v_mul_f32 v193, v196, v193\n\t" \
  "v_accvgpr_write_b32 a50, v193\n\t" \
  "v_accvgpr_read_b32 v193, a51\n\t" \
  "v_mul_f32 v193, v196, v193\n\t" \
  "v_accvgpr_write_b32 a51, v193\n\t" \
  "v_accvgpr_read_b32 v193, a52\n\t" \
  "v_mul_f32 v193, v197, v193\n\t" \
  "v_accvgpr_write_b32 a52, v193\n\t" \
  "v_accvgpr_read_b32 v193, a53\n\t" \
  "v_mul_f32 v193, v197, v193\n\t" \
  "v_accvgpr_write_b32 a53, v193\n\t" \
  "v_accvgpr_read_b32 v193, a54\n\t" \
  "v_mul_f32 v193, v197, v193\n\t" \
  "v_accvgpr_write_b32 a54, v193\n\t" \
  "v_accvgpr_read_b32 v193, a55\n\t" \
  "v_mul_f32 v193, v197, v193\n\t" \
  "v_accvgpr_write_b32 a55, v193\n\t" \
  "v_accvgpr_read_b32 v193, a56\n\t" \
  "v_mul_f32 v193, v198, v193\n\t" \
  "v_accvgpr_write_b32 a56, v193\n\t" \
  "v_accvgpr_read_b32 v193, a57\n\t" \
  "v_mul_f32 v193, v198, v193\n\t" \
  "v_accvgpr_write_b32 a57, v193\n\t" \
  "v_accvgpr_read_b32 v193, a58\n\t" \
  "v_mul_f32 v193, v198, v193\n\t" \
  "v_accvgpr_write_b32 a58, v193\n\t" \
  "v_accvgpr_read_b32 v193, a59\n\t" \
  "v_mul_f32 v193, v198, v193\n\t" \
  "v_accvgpr_write_b32 a59, v193\n\t" \
  "v_accvgpr_read_b32 v193, a60\n\t" \
  "v_mul_f32 v193, v199, v193\n\t" \
  "v_accvgpr_write_b32 a60, v193\n\t" \
  "v_accvgpr_read_b32 v193, a61\n\t" \
  "v_mul_f32 v193, v199, v193\n\t" \
  "v_accvgpr_write_b32 a61, v193\n\t" \
  "v_accvgpr_read_b32 v193, a62\n\t" \
  "v_mul_f32 v193, v199, v193\n\t" \
  "v_accvgpr_write_b32 a62, v193\n\t" \
  "v_accvgpr_read_b32 v193, a63\n\t" \
  "v_mul_f32 v193, v199, v193\n\t" \
  "v_accvgpr_write_b32 a63, v193\n\t" \
  "v_accvgpr_read_b32 v193, a64\n\t" \
  "v_mul_f32 v193, v196, v193\n\t" \
  "v_accvgpr_write_b32 a64, v193\n\t" \
  "v_accvgpr_read_b32 v193, a65\n\t" \
  "v_mul_f32 v193, v196, v193\n\t" \
  "v_accvgpr_write_b32 a65, v193\n\t" \
  "v_accvgpr_read_b32 v193, a66\n\t" \
  "v_mul_f32 v193, v196, v193\n\t" \
  "v_accvgpr_write_b32 a66, v193\n\t" \
  "v_accvgpr_read_b32 v193, a67\n\t" \
  "v_mul_f32 v193, v196, v193\n\t" \
  "v_accvgpr_write_b32 a67, v193\n\t" \
  "v_accvgpr_read_b32 v193, a68\n\t" \
  "v_mul_f32 v193, v197, v193\n\t" \
  "v_accvgpr_write_b32 a68, v193\n\t" \
  "v_accvgpr_read_b32 v193, a69\n\t" \
  "v_mul_f32 v193, v197, v193\n\t" \
  "v_accvgpr_write_b32 a69, v193\n\t" \
  "v_accvgpr_read_b32 v193, a70\n\t" \
  "v_mul_f32 v193, v197, v193\n\t" \
  "v_accvgpr_write_b32 a70, v193\n\t" \
  "v_accvgpr_read_b32 v193, a71\n\t" \
  "v_mul_f32 v193, v197, v193\n\t" \
  "v_accvgpr_write_b32 a71, v193\n\t" \
  "v_accvgpr_read_b32 v193, a72\n\t" \
  "v_mul_f32 v193, v198, v193\n\t" \
  "v_accvgpr_write_b32 a72, v193\n\t" \
  "v_accvgpr_read_b32 v193, a73\n\t" \
  "v_mul_f32 v193, v198, v193\n\t" \
  "v_accvgpr_write_b32 a73, v193\n\t" \
  "v_accvgpr_read_b32 v193, a74\n\t" \
  "v_mul_f32 v193, v198, v193\n\t" \
  "v_accvgpr_write_b32 a74, v193\n\t" \
  "v_accvgpr_read_b32 v193, a75\n\t" \
  "v_mul_f32 v193, v198, v193\n\t" \
  "v_accvgpr_write_b32 a75, v193\n\t" \
  "v_accvgpr_read_b32 v193, a76\n\t" \
  "v_mul_f32 v193, v199, v193\n\t" \
  "v_accvgpr_write_b32 a76, v193\n\t" \
  "v_accvgpr_read_b32 v193, a77\n\t" \
  "v_mul_f32 v193, v199, v193\n\t" \
  "v_accvgpr_write_b32 a77, v193\n\t" \
  "v_accvgpr_read_b32 v193, a78\n\t" \
  "v_mul_f32 v193, v199, v193\n\t" \
  "v_accvgpr_write_b32 a78, v193\n\t" \
  "v_accvgpr_read_b32 v193, a79\n\t" \
  "v_mul_f32 v193, v199, v193\n\t" \
  "v_accvgpr_write_b32 a79, v193\n\t" \
  "v_accvgpr_read_b32 v193, a80\n\t" \
  "v_mul_f32 v193, v196, v193\n\t" \
  "v_accvgpr_write_b32 a80, v193\n\t" \
  "v_accvgpr_read_b32 v193, a81\n\t" \
  "v_mul_f32 v193, v196, v193\n\t" \
  "v_accvgpr_write_b32 a81, v193\n\t" \
  "v_accvgpr_read_b32 v193, a82\n\t" \
  "v_mul_f32 v193, v196, v193\n\t" \
  "v_accvgpr_write_b32 a82, v193\n\t" \
  "v_accvgpr_read_b32 v193, a83\n\t" \
  "v_mul_f32 v193, v196, v193\n\t" \
  "v_accvgpr_write_b32 a83, v193\n\t" \
  "v_accvgpr_read_b32 v193, a84\n\t" \
  "v_mul_f32 v193, v197, v193\n\t" \
  "v_accvgpr_write_b32 a84, v193\n\t" \
  "v_accvgpr_read_b32 v193, a85\n\t" \
  "v_mul_f32 v193, v197, v193\n\t" \
  "v_accvgpr_write_b32 a85, v193\n\t" \
  "v_accvgpr_read_b32 v193, a86\n\t" \
  "v_mul_f32 v193, v197, v193\n\t" \
  "v_accvgpr_write_b32 a86, v193\n\t" \
  "v_accvgpr_read_b32 v193, a87\n\t" \
  "v_mul_f32 v193, v197, v193\n\t" \
  "v_accvgpr_write_b32 a87, v193\n\t" \
  "v_accvgpr_read_b32 v193, a88\n\t" \
  "v_mul_f32 v193, v198, v193\n\t" \
  "v_accvgpr_write_b32 a88, v193\n\t" \
  "v_accvgpr_read_b32 v193, a89\n\t" \
  "v_mul_f32 v193, v198, v193\n\t" \
  "v_accvgpr_write_b32 a89, v193\n\t" \
  "v_accvgpr_read_b32 v193, a90\n\t" \
  "v_mul_f32 v193, v198, v193\n\t" \
  "v_accvgpr_write_b32 a90, v193\n\t" \
  "v_accvgpr_read_b32 v193, a91\n\t" \
  "v_mul_f32 v193, v198, v193\n\t" \
  "v_accvgpr_write_b32 a91, v193\n\t" \
  "v_accvgpr_read_b32 v193, a92\n\t" \
  "v_mul_f32 v193, v199, v193\n\t" \
  "v_accvgpr_write_b32 a92, v193\n\t" \
  "v_accvgpr_read_b32 v193, a93\n\t" \
  "v_mul_f32 v193, v199, v193\n\t" \
  "v_accvgpr_write_b32 a93, v193\n\t" \
  "v_accvgpr_read_b32 v193, a94\n\t" \
  "v_mul_f32 v193, v199, v193\n\t" \
  "v_accvgpr_write_b32 a94, v193\n\t" \
  "v_accvgpr_read_b32 v193, a95\n\t" \
  "v_mul_f32 v193, v199, v193\n\t" \
  "v_accvgpr_write_b32 a95, v193\n\t" \
  "v_accvgpr_read_b32 v193, a96\n\t" \
  "v_mul_f32 v193, v196, v193\n\t" \
  "v_accvgpr_write_b32 a96, v193\n\t" \
  "v_accvgpr_read_b32 v193, a97\n\t" \
  "v_mul_f32 v193, v196, v193\n\t" \
  "v_accvgpr_write_b32 a97, v193\n\t" \
  "v_accvgpr_read_b32 v193, a98\n\t" \
  "v_mul_f32 v193, v196, v193\n\t" \
  "v_accvgpr_write_b32 a98, v193\n\t" \
  "v_accvgpr_read_b32 v193, a99\n\t" \
  "v_mul_f32 v193, v196, v193\n\t" \
  "v_accvgpr_write_b32 a99, v193\n\t" \
  "v_accvgpr_read_b32 v193, a100\n\t" \
  "v_mul_f32 v193, v197, v193\n\t" \
  "v_accvgpr_write_b32 a100, v193\n\t" \
  "v_accvgpr_read_b32 v193, a101\n\t" \
  "v_mul_f32 v193, v197, v193\n\t" \
  "v_accvgpr_write_b32 a101, v193\n\t" \
  "v_accvgpr_read_b32 v193, a102\n\t" \
  "v_mul_f32 v193, v197, v193\n\t" \
  "v_accvgpr_write_b32 a102, v193\n\t" \
  "v_accvgpr_read_b32 v193, a103\n\t" \
  "v_mul_f32 v193, v197, v193\n\t" \
  "v_accvgpr_write_b32 a103, v193\n\t" \
  "v_accvgpr_read_b32 v193, a104\n\t" \
  "v_mul_f32 v193, v198, v193\n\t" \
  "v_accvgpr_write_b32 a104, v193\n\t" \
  "v_accvgpr_read_b32 v193, a105\n\t" \
  "v_mul_f32 v193, v198, v193\n\t" \
  "v_accvgpr_write_b32 a105, v193\n\t" \
  "v_accvgpr_read_b32 v193, a106\n\t" \
  "v_mul_f32 v193, v198, v193\n\t" \
  "v_accvgpr_write_b32 a106, v193\n\t" \
  "v_accvgpr_read_b32 v193, a107\n\t" \
  "v_mul_f32 v193, v198, v193\n\t" \
  "v_accvgpr_write_b32 a107, v193\n\t" \
  "v_accvgpr_read_b32 v193, a108\n\t" \
  "v_mul_f32 v193, v199, v193\n\t" \
  "v_accvgpr_write_b32 a108, v193\n\t" \
  "v_accvgpr_read_b32 v193, a109\n\t" \
  "v_mul_f32 v193, v199, v193\n\t" \
  "v_accvgpr_write_b32 a109, v193\n\t" \
  "v_accvgpr_read_b32 v193, a110\n\t" \
  "v_mul_f32 v193, v199, v193\n\t" \
  "v_accvgpr_write_b32 a110, v193\n\t" \
  "v_accvgpr_read_b32 v193, a111\n\t" \
  "v_mul_f32 v193, v199, v193\n\t" \
  "v_accvgpr_write_b32 a111, v193\n\t" \
  "v_accvgpr_read_b32 v193, a112\n\t" \
  "v_mul_f32 v193, v196, v193\n\t" \
  "v_accvgpr_write_b32 a112, v193\n\t" \
  "v_accvgpr_read_b32 v193, a113\n\t" \
  "v_mul_f32 v193, v196, v193\n\t" \
  "v_accvgpr_write_b32 a113, v193\n\t" \
  "v_accvgpr_read_b32 v193, a114\n\t" \
  "v_mul_f32 v193, v196, v193\n\t" \
  "v_accvgpr_write_b32 a114, v193\n\t" \
  "v_accvgpr_read_b32 v193, a115\n\t" \
  "v_mul_f32 v193, v196, v193\n\t" \
  "v_accvgpr_write_b32 a115, v193\n\t" \
  "v_accvgpr_read_b32 v193, a116\n\t" \
  "v_mul_f32 v193, v197, v193\n\t" \
  "v_accvgpr_write_b32 a116, v193\n\t" \
  "v_accvgpr_read_b32 v193, a117\n\t" \
  "v_mul_f32 v193, v197, v193\n\t" \
  "v_accvgpr_write_b32 a117, v193\n\t" \
  "v_accvgpr_read_b32 v193, a118\n\t" \
  "v_mul_f32 v193, v197, v193\n\t" \
  "v_accvgpr_write_b32 a118, v193\n\t" \
  "v_accvgpr_read_b32 v193, a119\n\t" \
  "v_mul_f32 v193, v197, v193\n\t" \
  "v_accvgpr_write_b32 a119, v193\n\t" \
  "v_accvgpr_read_b32 v193, a120\n\t" \
  "v_mul_f32 v193, v198, v193\n\t" \
  "v_accvgpr_write_b32 a120, v193\n\t" \
  "v_accvgpr_read_b32 v193, a121\n\t" \
  "v_mul_f32 v193, v198, v193\n\t" \
  "v_accvgpr_write_b32 a121, v193\n\t" \
  "v_accvgpr_read_b32 v193, a122\n\t" \
  "v_mul_f32 v193, v198, v193\n\t" \
  "v_accvgpr_write_b32 a122, v193\n\t" \
  "v_accvgpr_read_b32 v193, a123\n\t" \
  "v_mul_f32 v193, v198, v193\n\t" \
  "v_accvgpr_write_b32 a123, v193\n\t" \
  "v_accvgpr_read_b32 v193, a124\n\t" \
  "v_mul_f32 v193, v199, v193\n\t" \
  "v_accvgpr_write_b32 a124, v193\n\t" \
  "v_accvgpr_read_b32 v193, a125\n\t" \
  "v_mul_f32 v193, v199, v193\n\t" \
  "v_accvgpr_write_b32 a125, v193\n\t" \
  "v_accvgpr_read_b32 v193, a126\n\t" \
  "v_mul_f32 v193, v199, v193\n\t" \
  "v_accvgpr_write_b32 a126, v193\n\t" \
  "v_accvgpr_read_b32 v193, a127\n\t" \
  "v_mul_f32 v193, v199, v193\n\t" \
  "v_accvgpr_write_b32 a127, v193\n\t" \
  "v_mul_f32 v160, v196, v160\n\t" \
  "v_mul_f32 v161, v196, v161\n\t" \
  "v_mul_f32 v162, v196, v162\n\t" \
  "v_mul_f32 v163, v196, v163\n\t" \
  "v_mov_b32 v196, 1.0\n\t" \
  "v_mul_f32 v164, v197, v164\n\t" \
  "v_mul_f32 v165, v197, v165\n\t" \
  "v_mul_f32 v166, v197, v166\n\t" \
  "v_mul_f32 v167, v197, v167\n\t" \
  "v_mov_b32 v197, 1.0\n\t" \
  "v_mul_f32 v168, v198, v168\n\t" \
  "v_mul_f32 v169, v198, v169\n\t" \
  "v_mul_f32 v170, v198, v170\n\t" \
  "v_mul_f32 v171, v198, v171\n\t" \
  "v_mov_b32 v198, 1.0\n\t" \
  "v_mul_f32 v172, v199, v172\n\t" \
  "v_mul_f32 v173, v199, v173\n\t" \
  "v_mul_f32 v174, v199, v174\n\t" \
  "v_mul_f32 v175, v199, v175\n\t" \
  "v_mov_b32 v199, 1.0\n\t" \
  "s_mov_b32 s99, 0\n\t" \
  "s_nop 1\n\t" \
  "s_cmp_eq_u32 vcc_lo, 0\n\t" \
  "s_cbranch_scc1 .Lsf_q0\n\t" \
  "s_cmp_eq_u32 vcc_lo, 1\n\t" \
  "s_cbranch_scc1 .Lsf_q1\n\t" \
  "s_cmp_eq_u32 vcc_lo, 2\n\t" \
  "s_cbranch_scc1 .Lsf_q2\n\t" \
  "s_cmp_eq_u32 vcc_lo, 3\n\t" \
  "s_cbranch_scc1 .Lsf_q3\n\t" \
  "s_cmp_eq_u32 vcc_lo, 4\n\t" \
  "s_cbranch_scc1 .Lsf_q4\n\t" \
  "s_cmp_eq_u32 vcc_lo, 5\n\t" \
  "s_cbranch_scc1 .Lsf_q5\n\t" \
  "s_cmp_eq_u32 vcc_lo, 6\n\t" \
  "s_cbranch_scc1 .Lsf_q6\n\t" \
  "s_cmp_eq_u32 vcc_lo, 7\n\t" \
  "s_cbranch_scc1 .Lsf_q7\n\t" \
  "s_endpgm\n\t" \
  ".Lsf_v:\n\t" \
  "s_mov_b32 s98, 0\n\t" \
  ".Lsf_v1:\n\t" \
  "s_sleep 4\n\t" \
  "buffer_load_dword v209, v205, %[ws], 0 offen sc1\n\t" \
  "s_waitcnt vmcnt(0)\n\t" \
  "v_and_b32 v206, 0xbfffbfff, v209\n\t" \
  "v_cmp_ne_u32_e64 s[96:97], %[tag], v206\n\t" \
  "s_cmp_lg_u64 s[96:97], 0\n\t" \
  "s_cbranch_scc0 .Lsf_w\n\t" \
  "s_add_u32 s98, s98, 1\n\t" \
  "s_cmp_lt_u32 s98, %[cap]\n\t" \
  "s_cbranch_scc1 .Lsf_v1\n\t" \
  "v_mov_b32 v206, %[tag]\n\t" \
  "buffer_store_dword v206, off, %[ws], 0 offset:256 sc1\n\t" \
  "s_branch .Lsf_w\n\t" \
  ".subsection 0\n\t" \
  ".Lat_end:\n\t" \
  ""
#define AT_CLOB "v208", "v209", "v210", "v211", "v212", "v214", "v215", "v216", "v217", "v218", "v219", "v235", "v236", "v237", "v238", "s92", "s93", "s94", "s95", "s96", "s97", "s98", "s99", "vcc", "scc", "memory"
#define AT_PF1
#define AT_NU 1
#define AT_BN 0
#define AT_XF "+{v[0:15]}"
#define AT_WF "+{v[16:47]}"
#define TL_ASM \
  "v_pk_fma_f32 v[0:1], v[128:129], v[96:97], v[0:1]\n\t" \
  "v_pk_fma_f32 v[2:3], v[130:131], v[98:99], v[2:3]\n\t" \
  "v_pk_fma_f32 v[16:17], v[132:133], v[112:113], v[16:17]\n\t" \
  "v_pk_fma_f32 v[18:19], v[134:135], v[114:115], v[18:19]\n\t" \
  "v_pk_fma_f32 v[4:5], v[128:129], v[100:101], v[4:5]\n\t" \
  "v_pk_fma_f32 v[6:7], v[130:131], v[102:103], v[6:7]\n\t" \
  "v_pk_fma_f32 v[20:21], v[132:133], v[116:117], v[20:21]\n\t" \
  "v_pk_fma_f32 v[22:23], v[134:135], v[118:119], v[22:23]\n\t" \
  "v_cvt_pk_bf16_f32 v160, v0, v1\n\t" \
  "v_cvt_pk_bf16_f32 v161, v2, v3\n\t" \
  "v_cvt_pk_bf16_f32 v162, v16, v17\n\t" \
  "v_cvt_pk_bf16_f32 v163, v18, v19\n\t" \
  "v_pk_fma_f32 v[8:9], v[128:129], v[104:105], v[8:9]\n\t" \
  "v_pk_fma_f32 v[10:11], v[130:131], v[106:107], v[10:11]\n\t" \
  "v_pk_fma_f32 v[24:25], v[132:133], v[120:121], v[24:25]\n\t" \
  "v_pk_fma_f32 v[26:27], v[134:135], v[122:123], v[26:27]\n\t" \
  "v_cvt_pk_bf16_f32 v164, v4, v5\n\t" \
  "v_cvt_pk_bf16_f32 v165, v6, v7\n\t" \
  "v_cvt_pk_bf16_f32 v166, v20, v21\n\t" \
  "v_cvt_pk_bf16_f32 v167, v22, v23\n\t" \
  "v_pk_fma_f32 v[12:13], v[128:129], v[108:109], v[12:13]\n\t" \
  "v_pk_fma_f32 v[14:15], v[130:131], v[110:111], v[14:15]\n\t" \
  "v_pk_fma_f32 v[28:29], v[132:133], v[124:125], v[28:29]\n\t" \
  "v_pk_fma_f32 v[30:31], v[134:135], v[126:127], v[30:31]\n\t" \
  "v_cvt_pk_bf16_f32 v168, v8, v9\n\t" \
  "v_cvt_pk_bf16_f32 v169, v10, v11\n\t" \
  "v_cvt_pk_bf16_f32 v170, v24, v25\n\t" \
  "v_cvt_pk_bf16_f32 v171, v26, v27\n\t" \
  "v_cvt_pk_bf16_f32 v172, v12, v13\n\t" \
  "v_cvt_pk_bf16_f32 v173, v14, v15\n\t" \
  "v_cvt_pk_bf16_f32 v174, v28, v29\n\t" \
  "v_cvt_pk_bf16_f32 v175, v30, v31\n\t" \
  "v_dot2_f32_bf16 v152, v160, v160, 0\n\t" \
  "v_dot2_f32_bf16 v153, v164, v164, 0\n\t" \
  "v_dot2_f32_bf16 v154, v168, v168, 0\n\t" \
  "v_dot2_f32_bf16 v155, v172, v172, 0\n\t" \
  "v_dot2_f32_bf16 v152, v161, v161, v152\n\t" \
  "v_dot2_f32_bf16 v153, v165, v165, v153\n\t" \
  "v_dot2_f32_bf16 v154, v169, v169, v154\n\t" \
  "v_dot2_f32_bf16 v155, v173, v173, v155\n\t" \
  "v_dot2_f32_bf16 v152, v162, v162, v152\n\t" \
  "v_dot2_f32_bf16 v153, v166, v166, v153\n\t" \
  "v_dot2_f32_bf16 v154, v170, v170, v154\n\t" \
  "v_dot2_f32_bf16 v155, v174, v174, v155\n\t" \
  "v_dot2_f32_bf16 v152, v163, v163, v152\n\t" \
  "v_dot2_f32_bf16 v153, v167, v167, v153\n\t" \
  "v_dot2_f32_bf16 v154, v171, v171, v154\n\t" \
  "v_dot2_f32_bf16 v155, v175, v175, v155\n\t" \
  "v_mfma_f32_16x16x32_bf16 v[156:159], a[208:211], v[160:163], 0\n\t" \
  "s_lshl_b32 s64, %[wid], 10\n\t" \
  "v_add_u32 v234, s64, v242\n\t" \
  "v_mov_b32 v250, s87\n\t" \
  "v_mfma_f32_16x16x32_bf16 v[156:159], a[212:215], v[164:167], v[156:159]\n\t" \
  "s_lshl_b32 s61, %[bid], 11\n\t" \
  "v_permlane32_swap_b32 v152, v154\n\t" \
  "v_mfma_f32_16x16x32_bf16 v[156:159], a[216:219], v[168:171], v[156:159]\n\t" \
  "s_and_b32 s61, s61, 0x2000\n\t" \
  "v_permlane32_swap_b32 v153, v155\n\t" \
  "v_mfma_f32_16x16x32_bf16 v[156:159], a[220:223], v[172:175], v[156:159]\n\t" \
  "s_xor_b32 s61, s61, %[nb]\n\t" \
  "v_add_f32 v152, v152, v154\n\t" \
  "v_add_f32 v153, v153, v155\n\t" \
  "v_writelane_b32 v250, s88, 1\n\t" \
  "s_and_b32 s80, %[bid], 3\n\t" \
  "v_permlane16_swap_b32 v152, v153\n\t" \
  "s_lshr_b32 s60, %[bid], 6\n\t" \
  "s_lshl2_add_u32 s60, s60, s80\n\t" \
  "v_add_f32 v158, v157, v158\n\t" \
  "v_add_f32 v157, v152, v153\n\t" \
  "v_mov_b32 v159, v250\n\t" \
  "ds_write_b128 v234, v[156:159]\n\t" \
  "s_lshl_b32 s62, s60, 15\n\t" \
  "s_add_u32 s62, s62, s61\n\t" \
  "s_add_u32 s62, s62, 524288\n\t" \
  "s_and_b32 s63, %[bid], 0x20\n\t" \
  "s_lshl_b32 s63, s63, 9\n\t" \
  "s_add_u32 s77, s62, s63\n\t" \
  "s_and_b32 s63, %[bid], 0x18\n\t" \
  "s_lshl_b32 s63, s63, 8\n\t" \
  "s_add_u32 s77, s77, s63\n\t" \
  "s_add_u32 s77, s77, s64\n\t" \
  "v_lshl_add_u32 v248, %[wid], 3, v242\n\t" \
  "v_mov_b32 v217, v243\n\t" \
  "v_mov_b32 v219, v243\n\t" \
  "s_cmp_gt_u32 %[wid], 1\n\t" \
  "s_waitcnt lgkmcnt(0)\n\t" \
  "s_barrier\n\t" \
  "s_cbranch_scc1 .Ltl_np\n\t" \
  "ds_read2st64_b64 v[208:211], v248 offset1:2\n\t" \
  "ds_read2st64_b64 v[212:215], v248 offset0:4 offset1:6\n\t" \
  "s_waitcnt lgkmcnt(1)\n\t" \
  "v_pk_add_f32 v[208:209], v[208:209], v[210:211]\n\t" \
  "s_waitcnt lgkmcnt(0)\n\t" \
  "v_pk_add_f32 v[212:213], v[212:213], v[214:215]\n\t" \
  "s_nop 0\n\t" \
  "v_add_f32 v216, v208, v212\n\t" \
  "v_add_f32 v218, v209, v213\n\t" \
  "buffer_store_dwordx4 v[216:219], v242, %[ws], s77 offen sc1\n\t" \
  ".Ltl_np:\n\t" \
  STP("s_memrealtime s[90:91]\n\t") \
  STP("s_memtime s[82:83]\n\t") \
  STP("s_waitcnt lgkmcnt(0)\n\t") \
  STP("s_or_b32 s90, s90, 1\n\t") \
  STP("v_writelane_b32 %[stv], s90, 40\n\t") \
  STP("v_writelane_b32 %[stv], s82, 41\n\t") \
  "s_and_b32 s63, %[wid], 2\n\t" \
  "s_lshl_b32 s63, s63, 13\n\t" \
  "s_add_u32 s62, s62, s63\n\t" \
  "s_and_b32 s63, %[wid], 1\n\t" \
  "s_lshl_b32 s63, s63, 12\n\t" \
  "s_add_u32 s62, s62, s63\n\t" \
  "ds_read_b128 v[128:131], v237 offset:2688\n\t" \
  "ds_read_b128 v[132:135], v237 offset:2752\n\t" \
  "ds_read_b128 v[136:139], v237 offset:2816\n\t" \
  "ds_read_b128 v[140:143], v237 offset:2880\n\t" \
  "ds_read_b128 v[144:147], v237 offset:2944\n\t" \
  "ds_read_b128 v[148:151], v237 offset:3008\n\t" \
  "s_mov_b32 s75, 0x05040100\n\t" \
  "s_mov_b32 s76, 0x07060302\n\t" \
  "s_mov_b32 s72, 0\n\t" \
  "s_mov_b32 s81, 0xffff0000\n\t" \
  "s_bfe_u32 s79, %[bid], 0x30003\n\t" \
  "s_and_b32 s63, %[bid], 0x4\n\t" \
  "s_lshl_b32 s63, s63, 2\n\t" \
  "s_add_u32 s60, s60, s63\n\t" \
  "v_lshlrev_b32 v0, 16, v160\n\t" \
  "v_and_b32 v1, 0xffff0000, v160\n\t" \
  "v_lshlrev_b32 v2, 16, v161\n\t" \
  "v_and_b32 v3, 0xffff0000, v161\n\t" \
  "v_lshlrev_b32 v16, 16, v162\n\t" \
  "v_and_b32 v17, 0xffff0000, v162\n\t" \
  "v_lshlrev_b32 v18, 16, v163\n\t" \
  "v_and_b32 v19, 0xffff0000, v163\n\t" \
  "v_lshlrev_b32 v4, 16, v164\n\t" \
  "v_and_b32 v5, 0xffff0000, v164\n\t" \
  "v_lshlrev_b32 v6, 16, v165\n\t" \
  "v_and_b32 v7, 0xffff0000, v165\n\t" \
  "v_lshlrev_b32 v20, 16, v166\n\t" \
  "v_and_b32 v21, 0xffff0000, v166\n\t" \
  "v_lshlrev_b32 v22, 16, v167\n\t" \
  "v_and_b32 v23, 0xffff0000, v167\n\t" \
  "v_lshlrev_b32 v8, 16, v168\n\t" \
  "v_and_b32 v9, 0xffff0000, v168\n\t" \
  "v_lshlrev_b32 v10, 16, v169\n\t" \
  "v_and_b32 v11, 0xffff0000, v169\n\t" \
  "v_lshlrev_b32 v24, 16, v170\n\t" \
  "v_and_b32 v25, 0xffff0000, v170\n\t" \
  "v_lshlrev_b32 v26, 16, v171\n\t" \
  "v_and_b32 v27, 0xffff0000, v171\n\t" \
  "v_lshlrev_b32 v12, 16, v172\n\t" \
  "v_and_b32 v13, 0xffff0000, v172\n\t" \
  "buffer_load_dwordx4 v[208:211], v242, %[ws], s62 offen offset:0 sc1\n\t" \
  "buffer_load_dwordx4 v[212:215], v242, %[ws], s62 offen offset:1024 sc1\n\t" \
  "buffer_load_dwordx4 v[216:219], v242, %[ws], s62 offen offset:2048 sc1\n\t" \
  "buffer_load_dwordx4 v[220:223], v242, %[ws], s62 offen offset:3072 sc1\n\t" \
  "s_branch .Lht0\n\t" \
  ".p2align 12\n\t" \
  ".Lht0:\n\t" \
  "s_cbranch_execz .Lht0n\n\t" \
  "v_lshlrev_b32 v14, 16, v173\n\t" \
  "v_and_b32 v15, 0xffff0000, v173\n\t" \
  "v_lshlrev_b32 v28, 16, v174\n\t" \
  "v_and_b32 v29, 0xffff0000, v174\n\t" \
  "v_lshlrev_b32 v30, 16, v175\n\t" \
  "v_and_b32 v31, 0xffff0000, v175\n\t" \
  "s_lshl_b32 s63, s60, 6\n\t" \
  "s_sub_u32 s63, 0x7ff, s63\n\t" \
  "v_sub_u32 v248, s63, v244\n\t" \
  "v_and_b32 v246, 31, v244\n\t" \
  "s_and_b32 s63, %[bid], 0x18\n\t" \
  "s_lshl_b32 s63, s63, 7\n\t" \
  "s_add_u32 s65, s61, s63\n\t" \
  "s_and_b32 s63, %[bid], 0x3\n\t" \
  "s_lshl_b32 s63, s63, 8\n\t" \
  "s_add_u32 s65, s65, s63\n\t" \
  "s_add_u32 s65, s65, 65536\n\t" \
  "v_lshl_add_u32 v235, v246, 3, s65\n\t" \
  "s_lshl_b32 s68, 1, s60\n\t" \
  "s_lshl2_add_u32 s65, s79, %[wid]\n\t" \
  "s_lshl_b32 s63, s65, 13\n\t" \
  "s_and_b32 s63, s63, 0x2000\n\t" \
  "s_xor_b32 s63, s63, %[nb]\n\t" \
  "s_lshr_b32 s65, s65, 1\n\t" \
  "s_lshl_b32 s65, s65, 8\n\t" \
  "s_add_u32 s65, s65, s63\n\t" \
  "s_lshl3_add_u32 s65, s60, s65\n\t" \
  "s_add_u32 s65, s65, 65536\n\t" \
  "v_bfrev_b32 v249, 1\n\t" \
  "s_nop 0\n\t" \
  "v_writelane_b32 v249, 0, 63\n\t" \
  "v_mov_b32 v159, v243\n\t" \
  "s_add_u32 s63, s61, 262144\n\t" \
  "v_and_b32 v236, -32, v234\n\t" \
  "v_add_u32 v236, s63, v236\n\t" \
  "v_mov_b32 v113, v243\n\t" \
  "v_mov_b32 v115, v243\n\t" \
  "v_mov_b32 v117, v243\n\t" \
  "v_mov_b32 v119, v243\n\t" \
  "v_mov_b32 v121, v243\n\t" \
  "v_mov_b32 v123, v243\n\t" \
  "v_mov_b32 v125, v243\n\t" \
  "v_mov_b32 v127, v243\n\t" \
  "v_and_b32 v239, 48, v244\n\t" \
  "v_lshl_add_u32 v239, %[wid], 7, v239\n\t" \
  "v_and_b32 v157, 15, v244\n\t" \
  "v_bfrev_b32 v156, 1\n\t" \
  "s_lshl_b32 s66, s60, 13\n\t" \
  "s_and_b32 s66, s66, 0x3c000\n\t" \
  "s_and_b32 s63, %[bid], 0x1\n\t" \
  "s_lshl_b32 s63, s63, 12\n\t" \
  "s_add_u32 s66, s66, s63\n\t" \
  "s_and_b32 s63, %[bid], 0x38\n\t" \
  "s_lshl_b32 s63, s63, 6\n\t" \
  "s_add_u32 s66, s66, s63\n\t" \
  "s_add_u32 s66, s66, 262144\n\t" \
  "s_xor_b32 s77, s66, 0x2000\n\t" \
  "s_lshl_b32 s67, s60, 12\n\t" \
  "s_and_b32 s67, s67, 0x1c000\n\t" \
  "s_and_b32 s63, %[bid], 0x20\n\t" \
  "s_lshl_b32 s63, s63, 8\n\t" \
  "s_add_u32 s67, s67, s63\n\t" \
  "s_xor_b32 s67, s67, %[nb]\n\t" \
  "s_and_b32 s63, %[bid], 0x3\n\t" \
  "s_lshl_b32 s63, s63, 11\n\t" \
  "s_add_u32 s67, s67, s63\n\t" \
  "s_and_b32 s63, %[bid], 0x18\n\t" \
  "s_lshl_b32 s63, s63, 6\n\t" \
  "s_add_u32 s67, s67, s63\n\t" \
  "s_add_u32 s67, s67, 1966080\n\t" \
  "v_perm_b32 v240, v34, v32, s75\n\t" \
  "v_perm_b32 v34, v34, v32, s76\n\t" \
  "v_perm_b32 v32, v35, v33, s75\n\t" \
  "v_perm_b32 v35, v35, v33, s76\n\t" \
  "v_perm_b32 v33, v38, v36, s75\n\t" \
  "v_perm_b32 v38, v38, v36, s76\n\t" \
  "v_perm_b32 v36, v39, v37, s75\n\t" \
  "v_perm_b32 v39, v39, v37, s76\n\t" \
  "v_perm_b32 v37, v42, v40, s75\n\t" \
  "v_perm_b32 v42, v42, v40, s76\n\t" \
  "v_perm_b32 v40, v43, v41, s75\n\t" \
  "v_perm_b32 v43, v43, v41, s76\n\t" \
  "v_perm_b32 v41, v46, v44, s75\n\t" \
  "v_perm_b32 v46, v46, v44, s76\n\t" \
  "v_perm_b32 v44, v47, v45, s75\n\t" \
  "v_perm_b32 v47, v47, v45, s76\n\t" \
  "v_perm_b32 v45, v50, v48, s75\n\t" \
  "v_perm_b32 v50, v50, v48, s76\n\t" \
  "v_perm_b32 v48, v51, v49, s75\n\t" \
  "v_perm_b32 v51, v51, v49, s76\n\t" \
  "v_perm_b32 v49, v54, v52, s75\n\t" \
  "v_perm_b32 v54, v54, v52, s76\n\t" \
  "v_perm_b32 v52, v55, v53, s75\n\t" \
  "v_perm_b32 v55, v55, v53, s76\n\t" \
  "v_perm_b32 v53, v58, v56, s75\n\t" \
  "v_perm_b32 v58, v58, v56, s76\n\t" \
  "v_perm_b32 v56, v59, v57, s75\n\t" \
  "v_perm_b32 v59, v59, v57, s76\n\t" \
  "v_perm_b32 v57, v62, v60, s75\n\t" \
  "v_perm_b32 v62, v62, v60, s76\n\t" \
  "v_perm_b32 v60, v63, v61, s75\n\t" \
  "v_perm_b32 v63, v63, v61, s76\n\t" \
  "s_waitcnt vmcnt(0)\n\t" \
  ".Ltl_c0:\n\t" \
  "v_xor_b32 v152, %[tag], v209\n\t" \
  "v_bitop3_b32 v152, v152, v211, %[tag] bitop3:0xf6\n\t" \
  "v_bitop3_b32 v152, v152, v213, %[tag] bitop3:0xf6\n\t" \
  "v_bitop3_b32 v152, v152, v215, %[tag] bitop3:0xf6\n\t" \
  "v_bitop3_b32 v152, v152, v217, %[tag] bitop3:0xf6\n\t" \
  "v_bitop3_b32 v152, v152, v219, %[tag] bitop3:0xf6\n\t" \
  "v_bitop3_b32 v152, v152, v221, %[tag] bitop3:0xf6\n\t" \
  "v_bitop3_b32 v152, v152, v223, %[tag] bitop3:0xf6\n\t" \
  "v_cmp_ne_u32 vcc, 0, v152\n\t" \
  "v_add_f32 v152, v208, v216\n\t" \
  "v_add_f32 v153, v210, v218\n\t" \
  "v_add_f32 v154, v212, v220\n\t" \
  "v_add_f32 v155, v214, v222\n\t" \
  "s_cbranch_vccnz .Ltl_s0\n\t" \
  ".Ltl_k0:\n\t" \
  "ds_write_b128 v234, v[152:155] offset:4096\n\t" \
  "buffer_load_dwordx2 v[216:217], v235, %[ws], 0 offen sc1\n\t" \
  "s_waitcnt lgkmcnt(0)\n\t" \
  "s_barrier\n\t" \
  STP("s_memrealtime s[90:91]\n\t") \
  STP("s_memtime s[82:83]\n\t") \
  STP("s_waitcnt lgkmcnt(0)\n\t") \
  STP("s_or_b32 s90, s90, 1\n\t") \
  STP("v_writelane_b32 %[stv], s90, 42\n\t") \
  STP("v_writelane_b32 %[stv], s82, 43\n\t") \
  "ds_read_b128 v[96:99], v242 offset:4096\n\t" \
  "ds_read_b128 v[100:103], v242 offset:5120\n\t" \
  "ds_read_b128 v[104:107], v242 offset:6144\n\t" \
  "ds_read_b128 v[108:111], v242 offset:7168\n\t" \
  "s_waitcnt lgkmcnt(2)\n\t" \
  "v_pk_add_f32 v[96:97], v[96:97], v[100:101]\n\t" \
  "v_pk_add_f32 v[98:99], v[98:99], v[102:103]\n\t" \
  "s_waitcnt lgkmcnt(0)\n\t" \
  "v_pk_add_f32 v[104:105], v[104:105], v[108:109]\n\t" \
  "v_pk_add_f32 v[106:107], v[106:107], v[110:111]\n\t" \
  "v_pk_add_f32 v[96:97], v[96:97], v[104:105]\n\t" \
  "v_pk_add_f32 v[98:99], v[98:99], v[106:107]\n\t" \
  "v_mul_f32 v96, 0x3a800000, v96\n\t" \
  "v_mul_f32 v97, 0x3a800000, v97\n\t" \
  "v_readlane_b32 s87, v99, 0\n\t" \
  "v_fma_f32 v97, -v96, v96, v97\n\t" \
  "v_readlane_b32 s88, v99, 1\n\t" \
  "v_max_f32 v97, 0, v97\n\t" \
  "v_add_f32 v97, 0x3727c5ac, v97\n\t" \
  "v_rsq_f32 v208, v97\n\t" \
  "v_fma_f32 v98, -v96, s87, v98\n\t" \
  "v_fma_f32 v98, v208, v98, s88\n\t" \
  "v_cvt_pk_bf16_f32 v98, v98, v98\n\t" \
  "v_ashrrev_i32 v99, 31, v98\n\t" \
  "v_or_b32 v99, 0x80000000, v99\n\t" \
  "v_xor_b32 v98, v98, v99\n\t" \
  "v_and_or_b32 v158, v98, s81, v248\n\t" \
  "v_mul_f32 v209, v208, -v96\n\t" \
  "v_mov_b32 v212, v208\n\t" \
  "v_max_u32_dpp v158, v158, v158 quad_perm:[1,0,3,2] row_mask:0xf bank_mask:0xf\n\t" \
  "v_mov_b32 v213, v209\n\t" \
  "v_permlane32_swap_b32 v208, v212\n\t" \
  "v_max_u32_dpp v158, v158, v158 quad_perm:[2,3,0,1] row_mask:0xf bank_mask:0xf\n\t" \
  "v_permlane32_swap_b32 v209, v213\n\t" \
  "v_mov_b32 v210, v208\n\t" \
  "v_max_u32_dpp v158, v158, v158 row_half_mirror row_mask:0xf bank_mask:0xf\n\t" \
  "v_mov_b32 v211, v209\n\t" \
  "v_mov_b32 v214, v212\n\t" \
  "v_max_u32_dpp v158, v158, v158 row_mirror row_mask:0xf bank_mask:0xf\n\t" \
  "v_mov_b32 v215, v213\n\t" \
  "v_permlane16_swap_b32 v208, v210\n\t" \
  "v_max_u32_dpp v158, v158, v158 row_bcast:15 row_mask:0xa bank_mask:0xf\n\t" \
  "v_permlane16_swap_b32 v209, v211\n\t" \
  "v_permlane16_swap_b32 v212, v214\n\t" \
  "v_max_u32_dpp v158, v158, v158 row_bcast:31 row_mask:0xc bank_mask:0xf\n\t" \
  "buffer_store_dwordx2 v[158:159], v249, %[ws], s65 offen sc1\n\t" \
  "v_readlane_b32 s69, v158, 63\n\t" \
  STP("s_memrealtime s[90:91]\n\t") \
  STP("s_memtime s[82:83]\n\t") \
  STP("s_waitcnt lgkmcnt(0)\n\t") \
  STP("s_or_b32 s90, s90, 1\n\t") \
  STP("v_writelane_b32 %[stv], s90, 44\n\t") \
  STP("v_writelane_b32 %[stv], s82, 45\n\t") \
  "v_permlane16_swap_b32 v213, v215\n\t" \
  "s_andn2_b32 s73, 63, s69\n\t" \
  "s_waitcnt vmcnt(1)\n\t" \
  "v_cmp_eq_u32 vcc, %[tag], v217\n\t" \
  "s_mov_b32 s74, 0x100\n\t" \
  "s_nop 0\n\t" \
  "v_cndmask_b32 v152, 0, v216, vcc\n\t" \
  "s_bcnt1_i32_b32 s78, vcc_lo\n\t" \
  "v_cmp_lt_u32 vcc, s69, v152\n\t" \
  "s_cbranch_vccz .Ltl_cd\n\t" \
  ".Ltl_ce:\n\t" \
  "v_max_u32_dpp v152, v152, v152 quad_perm:[1,0,3,2] row_mask:0xf bank_mask:0xf\n\t" \
  "s_lshl_b32 s63, s60, 17\n\t" \
  "s_lshl_b32 s64, s79, 8\n\t" \
  "v_max_u32_dpp v152, v152, v152 quad_perm:[2,3,0,1] row_mask:0xf bank_mask:0xf\n\t" \
  "s_add_u32 s63, s63, s64\n\t" \
  "s_lshl_b32 s64, %[wid], 6\n\t" \
  "v_max_u32_dpp v152, v152, v152 row_half_mirror row_mask:0xf bank_mask:0xf\n\t" \
  "s_add_u32 s63, s63, s64\n\t" \
  "v_lshl_add_u32 v238, v157, 11, s63\n\t" \
  "v_max_u32_dpp v152, v152, v152 row_mirror row_mask:0xf bank_mask:0xf\n\t" \
  "s_nop 1\n\t" \
  "v_max_u32_dpp v152, v152, v152 row_bcast:15 row_mask:0xa bank_mask:0xf\n\t" \
  "s_nop 0\n\t" \
  "v_readlane_b32 s70, v152, 31\n\t" \
  "s_max_u32 s70, s70, s69\n\t" \
  "s_andn2_b32 s63, 0x780, s70\n\t" \
  "s_lshl_b32 s63, s63, 7\n\t" \
  "s_andn2_b32 s71, 64, s70\n\t" \
  "s_lshl_b32 s71, s71, 6\n\t" \
  "s_or_b32 s71, s71, s63\n\t" \
  "buffer_load_dwordx2 v[216:217], v235, %[ws], 0 offen sc1\n\t" \
  "buffer_load_dwordx4 v[218:221], v236, %[ws], s71 offen sc1\n\t" \
  "buffer_load_dwordx4 v[222:225], v236, %[ws], s71 offen offset:16 sc1\n\t" \
  "v_bfe_u32 v153, v244, 4, 1\n\t" \
  "v_lshl_add_u32 v238, v153, 5, v238\n\t" \
  "v_lshrrev_b32 v154, 5, v244\n\t" \
  "v_lshl_add_u32 v238, v154, 4, v238\n\t" \
  "s_mov_b64 s[96:97], %[out]\n\t" \
  "s_and_b32 s97, s97, 0xffff\n\t" \
  "s_mov_b32 s98, 0x400000\n\t" \
  "s_mov_b32 s99, 0x20000\n\t" \
  "s_lshr_b32 s64, %[bid], 3\n\t" \
  "s_and_b32 s63, s64, 27\n\t" \
  "s_cmp_lt_u32 s64, 24\n\t" \
  "s_cselect_b32 s64, s64, s63\n\t" \
  "s_mul_i32 s63, s80, 56\n\t" \
  "s_add_u32 s64, s64, s63\n\t" \
  "s_and_b32 s63, %[bid], 4\n\t" \
  "s_mul_i32 s63, s63, 7\n\t" \
  "s_add_u32 s64, s64, s63\n\t" \
  "s_lshl_b32 s63, %[wid], 12\n\t" \
  "s_and_b32 s63, s63, 0x2000\n\t" \
  "s_xor_b32 s85, s63, %[nb]\n\t" \
  "s_lshr_b32 s63, s64, 2\n\t" \
  "s_lshl_b32 s63, s63, 14\n\t" \
  "s_add_u32 s85, s85, s63\n\t" \
  "s_and_b32 s63, s64, 3\n\t" \
  "s_lshl_b32 s63, s63, 11\n\t" \
  "s_add_u32 s85, s85, s63\n\t" \
  "s_add_u32 s85, s85, 1048576\n\t" \
  "v_and_b32 v247, 0x7f0, v234\n\t" \
  "v_lshlrev_b32 v245, 6, v154\n\t" \
  "v_lshl_add_u32 v245, v153, 4, v245\n\t" \
  "v_and_b32 v153, 1, v244\n\t" \
  "v_lshl_add_u32 v245, v153, 5, v245\n\t" \
  "v_lshl_add_u32 v245, %[wid], 7, v245\n\t" \
  "v_and_b32 v153, 14, v244\n\t" \
  "v_lshl_add_u32 v245, v153, 8, v245\n\t" \
  "v_lshlrev_b32 v246, 2, v246\n\t" \
  "v_add_u32 v248, 0x80, v246\n\t" \
  "s_mov_b32 s72, 0\n\t" \
  "s_lshr_b32 s62, %[bid], 5\n\t" \
  "s_bfe_u32 s63, %[bid], 0x10002\n\t" \
  "s_sub_u32 s62, s62, s63\n\t" \
  "s_cmp_ge_u32 s78, 0\n\t" \
  "s_cselect_b32 s84, 1, 0\n\t" \
  "s_cmp_eq_u32 s62, 6\n\t" \
  "s_cselect_b32 s84, 0, s84\n\t" \
  "s_cmp_lt_u32 s78, 33\n\t" \
  "s_cselect_b32 s78, 0, 0x80000000\n\t" \
  "s_cmp_lg_u32 s84, 0\n\t" \
  "s_cbranch_scc1 .Lht1\n\t" \
  ".Ltl_h2:\n\t" \
  "v_pk_fma_f32 v[112:113], v[0:1], v[208:209], v[208:209] op_sel:[0,0,1] op_sel_hi:[1,0,1]\n\t" \
  "v_pk_fma_f32 v[114:115], v[2:3], v[208:209], v[208:209] op_sel:[0,0,1] op_sel_hi:[1,0,1]\n\t" \
  "v_pk_fma_f32 v[116:117], v[16:17], v[208:209], v[208:209] op_sel:[0,0,1] op_sel_hi:[1,0,1]\n\t" \
  "v_pk_fma_f32 v[118:119], v[18:19], v[208:209], v[208:209] op_sel:[0,0,1] op_sel_hi:[1,0,1]\n\t" \
  "v_pk_fma_f32 v[112:113], v[112:113], v[136:137], v[128:129]\n\t" \
  "v_pk_fma_f32 v[114:115], v[114:115], v[138:139], v[130:131]\n\t" \
  "v_pk_fma_f32 v[116:117], v[116:117], v[140:141], v[132:133]\n\t" \
  "v_pk_fma_f32 v[118:119], v[118:119], v[142:143], v[134:135]\n\t" \
  "v_cvt_pk_bf16_f32 v96, v112, v113\n\t" \
  "v_cvt_pk_bf16_f32 v97, v114, v115\n\t" \
  "v_cvt_pk_bf16_f32 v98, v116, v117\n\t" \
  "v_cvt_pk_bf16_f32 v99, v118, v119\n\t" \
  "v_pk_fma_f32 v[120:121], v[4:5], v[210:211], v[210:211] op_sel:[0,0,1] op_sel_hi:[1,0,1]\n\t" \
  "v_pk_fma_f32 v[122:123], v[6:7], v[210:211], v[210:211] op_sel:[0,0,1] op_sel_hi:[1,0,1]\n\t" \
  "v_pk_fma_f32 v[124:125], v[20:21], v[210:211], v[210:211] op_sel:[0,0,1] op_sel_hi:[1,0,1]\n\t" \
  "v_pk_fma_f32 v[126:127], v[22:23], v[210:211], v[210:211] op_sel:[0,0,1] op_sel_hi:[1,0,1]\n\t" \
  "v_pk_fma_f32 v[120:121], v[120:121], v[136:137], v[128:129]\n\t" \
  "v_pk_fma_f32 v[122:123], v[122:123], v[138:139], v[130:131]\n\t" \
  "v_pk_fma_f32 v[124:125], v[124:125], v[140:141], v[132:133]\n\t" \
  "v_pk_fma_f32 v[126:127], v[126:127], v[142:143], v[134:135]\n\t" \
  "v_cvt_pk_bf16_f32 v100, v120, v121\n\t" \
  "v_cvt_pk_bf16_f32 v101, v122, v123\n\t" \
  "v_cvt_pk_bf16_f32 v102, v124, v125\n\t" \
  "v_cvt_pk_bf16_f32 v103, v126, v127\n\t" \
  "v_pk_fma_f32 v[112:113], v[8:9], v[212:213], v[212:213] op_sel:[0,0,1] op_sel_hi:[1,0,1]\n\t" \
  "v_pk_fma_f32 v[114:115], v[10:11], v[212:213], v[212:213] op_sel:[0,0,1] op_sel_hi:[1,0,1]\n\t" \
  "v_pk_fma_f32 v[116:117], v[24:25], v[212:213], v[212:213] op_sel:[0,0,1] op_sel_hi:[1,0,1]\n\t" \
  "v_pk_fma_f32 v[118:119], v[26:27], v[212:213], v[212:213] op_sel:[0,0,1] op_sel_hi:[1,0,1]\n\t" \
  "v_pk_fma_f32 v[112:113], v[112:113], v[136:137], v[128:129]\n\t" \
  "v_pk_fma_f32 v[114:115], v[114:115], v[138:139], v[130:131]\n\t" \
  "v_pk_fma_f32 v[116:117], v[116:117], v[140:141], v[132:133]\n\t" \
  "v_pk_fma_f32 v[118:119], v[118:119], v[142:143], v[134:135]\n\t" \
  "v_cvt_pk_bf16_f32 v104, v112, v113\n\t" \
  "v_cvt_pk_bf16_f32 v105, v114, v115\n\t" \
  "v_cvt_pk_bf16_f32 v106, v116, v117\n\t" \
  "v_cvt_pk_bf16_f32 v107, v118, v119\n\t" \
  "v_pk_fma_f32 v[120:121], v[12:13], v[214:215], v[214:215] op_sel:[0,0,1] op_sel_hi:[1,0,1]\n\t" \
  "v_pk_fma_f32 v[122:123], v[14:15], v[214:215], v[214:215] op_sel:[0,0,1] op_sel_hi:[1,0,1]\n\t" \
  "v_pk_fma_f32 v[124:125], v[28:29], v[214:215], v[214:215] op_sel:[0,0,1] op_sel_hi:[1,0,1]\n\t" \
  "v_pk_fma_f32 v[126:127], v[30:31], v[214:215], v[214:215] op_sel:[0,0,1] op_sel_hi:[1,0,1]\n\t" \
  "v_pk_fma_f32 v[120:121], v[120:121], v[136:137], v[128:129]\n\t" \
  "v_pk_fma_f32 v[122:123], v[122:123], v[138:139], v[130:131]\n\t" \
  "v_pk_fma_f32 v[124:125], v[124:125], v[140:141], v[132:133]\n\t" \
  "v_pk_fma_f32 v[126:127], v[126:127], v[142:143], v[134:135]\n\t" \
  "v_cvt_pk_bf16_f32 v108, v120, v121\n\t" \
  "v_cvt_pk_bf16_f32 v109, v122, v123\n\t" \
  "v_cvt_pk_bf16_f32 v110, v124, v125\n\t" \
  "v_cvt_pk_bf16_f32 v111, v126, v127\n\t" \
  "v_sub_u32 v152, s74, v157\n\t" \
  "v_bfrev_b32 v155, 1\n\t" \
  "v_cmp_eq_u32 vcc, 0, v152\n\t" \
  "v_lshlrev_b32 v160, 16, v96\n\t" \
  "v_and_b32 v161, 0xffff0000, v96\n\t" \
  "v_lshlrev_b32 v162, 16, v97\n\t" \
  "v_and_b32 v163, 0xffff0000, v97\n\t" \
  "v_lshlrev_b32 v164, 16, v98\n\t" \
  "v_and_b32 v165, 0xffff0000, v98\n\t" \
  "v_lshlrev_b32 v166, 16, v99\n\t" \
  "v_and_b32 v167, 0xffff0000, v99\n\t" \
  "v_pk_fma_f32 v[160:161], v[144:145], v[160:161], v[0:1]\n\t" \
  "v_pk_fma_f32 v[162:163], v[146:147], v[162:163], v[2:3]\n\t" \
  "v_pk_fma_f32 v[164:165], v[148:149], v[164:165], v[16:17]\n\t" \
  "v_pk_fma_f32 v[166:167], v[150:151], v[166:167], v[18:19]\n\t" \
  "v_cndmask_b32 v228, v238, v155, vcc\n\t" \
  "v_cvt_pk_bf16_f32 v112, v160, v161\n\t" \
  "v_cvt_pk_bf16_f32 v114, v164, v165\n\t" \
  "v_cvt_pk_bf16_f32 v113, v162, v163\n\t" \
  "v_cvt_pk_bf16_f32 v115, v166, v167\n\t" \
  "v_or_b32 v232, s78, v228\n\t" \
  "s_mov_b32 s64, 0\n\t" \
  "v_permlane16_swap_b32 v112, v114\n\t" \
  "v_permlane16_swap_b32 v113, v115\n\t" \
  "buffer_store_dwordx4 v[112:115], v232, s[96:99], s64 offen sc0 sc1\n\t" \
  "v_cmp_eq_u32 vcc, 16, v152\n\t" \
  "v_lshlrev_b32 v160, 16, v100\n\t" \
  "v_and_b32 v161, 0xffff0000, v100\n\t" \
  "v_lshlrev_b32 v162, 16, v101\n\t" \
  "v_and_b32 v163, 0xffff0000, v101\n\t" \
  "v_lshlrev_b32 v164, 16, v102\n\t" \
  "v_and_b32 v165, 0xffff0000, v102\n\t" \
  "v_lshlrev_b32 v166, 16, v103\n\t" \
  "v_and_b32 v167, 0xffff0000, v103\n\t" \
  "v_pk_fma_f32 v[160:161], v[144:145], v[160:161], v[4:5]\n\t" \
  "v_pk_fma_f32 v[162:163], v[146:147], v[162:163], v[6:7]\n\t" \
  "v_pk_fma_f32 v[164:165], v[148:149], v[164:165], v[20:21]\n\t" \
  "v_pk_fma_f32 v[166:167], v[150:151], v[166:167], v[22:23]\n\t" \
  "v_cndmask_b32 v229, v238, v155, vcc\n\t" \
  "v_cvt_pk_bf16_f32 v116, v160, v161\n\t" \
  "v_cvt_pk_bf16_f32 v118, v164, v165\n\t" \
  "v_cvt_pk_bf16_f32 v117, v162, v163\n\t" \
  "v_cvt_pk_bf16_f32 v119, v166, v167\n\t" \
  "v_or_b32 v232, s78, v229\n\t" \
  "s_mov_b32 s64, 32768\n\t" \
  "v_permlane16_swap_b32 v116, v118\n\t" \
  "v_permlane16_swap_b32 v117, v119\n\t" \
  "buffer_store_dwordx4 v[116:119], v232, s[96:99], s64 offen sc0 sc1\n\t" \
  "v_cmp_eq_u32 vcc, 32, v152\n\t" \
  "v_lshlrev_b32 v160, 16, v104\n\t" \
  "v_and_b32 v161, 0xffff0000, v104\n\t" \
  "v_lshlrev_b32 v162, 16, v105\n\t" \
  "v_and_b32 v163, 0xffff0000, v105\n\t" \
  "v_lshlrev_b32 v164, 16, v106\n\t" \
  "v_and_b32 v165, 0xffff0000, v106\n\t" \
  "v_lshlrev_b32 v166, 16, v107\n\t" \
  "v_and_b32 v167, 0xffff0000, v107\n\t" \
  "v_pk_fma_f32 v[160:161], v[144:145], v[160:161], v[8:9]\n\t" \
  "v_pk_fma_f32 v[162:163], v[146:147], v[162:163], v[10:11]\n\t" \
  "v_pk_fma_f32 v[164:165], v[148:149], v[164:165], v[24:25]\n\t" \
  "v_pk_fma_f32 v[166:167], v[150:151], v[166:167], v[26:27]\n\t" \
  "v_cndmask_b32 v230, v238, v155, vcc\n\t" \
  "v_cvt_pk_bf16_f32 v120, v160, v161\n\t" \
  "v_cvt_pk_bf16_f32 v122, v164, v165\n\t" \
  "v_cvt_pk_bf16_f32 v121, v162, v163\n\t" \
  "v_cvt_pk_bf16_f32 v123, v166, v167\n\t" \
  "v_or_b32 v232, s78, v230\n\t" \
  "s_mov_b32 s64, 65536\n\t" \
  "v_permlane16_swap_b32 v120, v122\n\t" \
  "v_permlane16_swap_b32 v121, v123\n\t" \
  "buffer_store_dwordx4 v[120:123], v232, s[96:99], s64 offen sc0 sc1\n\t" \
  "v_cmp_eq_u32 vcc, 48, v152\n\t" \
  "v_lshlrev_b32 v160, 16, v108\n\t" \
  "v_and_b32 v161, 0xffff0000, v108\n\t" \
  "v_lshlrev_b32 v162, 16, v109\n\t" \
  "v_and_b32 v163, 0xffff0000, v109\n\t" \
  "v_lshlrev_b32 v164, 16, v110\n\t" \
  "v_and_b32 v165, 0xffff0000, v110\n\t" \
  "v_lshlrev_b32 v166, 16, v111\n\t" \
  "v_and_b32 v167, 0xffff0000, v111\n\t" \
  "v_pk_fma_f32 v[160:161], v[144:145], v[160:161], v[12:13]\n\t" \
  "v_pk_fma_f32 v[162:163], v[146:147], v[162:163], v[14:15]\n\t" \
  "v_pk_fma_f32 v[164:165], v[148:149], v[164:165], v[28:29]\n\t" \
  "v_pk_fma_f32 v[166:167], v[150:151], v[166:167], v[30:31]\n\t" \
  "v_cndmask_b32 v231, v238, v155, vcc\n\t" \
  "v_cvt_pk_bf16_f32 v124, v160, v161\n\t" \
  "v_cvt_pk_bf16_f32 v126, v164, v165\n\t" \
  "v_cvt_pk_bf16_f32 v125, v162, v163\n\t" \
  "v_cvt_pk_bf16_f32 v127, v166, v167\n\t" \
  "v_or_b32 v232, s78, v231\n\t" \
  "s_mov_b32 s64, 98304\n\t" \
  "v_permlane16_swap_b32 v124, v126\n\t" \
  "v_permlane16_swap_b32 v125, v127\n\t" \
  "buffer_store_dwordx4 v[124:127], v232, s[96:99], s64 offen sc0 sc1\n\t" \
  STP("s_memrealtime s[90:91]\n\t") \
  STP("s_memtime s[82:83]\n\t") \
  STP("s_waitcnt lgkmcnt(0)\n\t") \
  STP("s_or_b32 s90, s90, 1\n\t") \
  STP("v_writelane_b32 %[stv], s90, 46\n\t") \
  STP("v_writelane_b32 %[stv], s82, 47\n\t") \
  "s_cmp_lg_u32 s84, 0\n\t" \
  "s_cbranch_scc1 .Ltl_oe\n\t" \
  "s_waitcnt vmcnt(4)\n\t" \
  "s_branch .Ltl_c1\n\t" \
  ".Ltl_s0:\n\t" \
  "s_sleep 1\n\t" \
  "buffer_load_dwordx4 v[208:211], v242, %[ws], s62 offen offset:0 sc1\n\t" \
  "buffer_load_dwordx4 v[212:215], v242, %[ws], s62 offen offset:1024 sc1\n\t" \
  "buffer_load_dwordx4 v[216:219], v242, %[ws], s62 offen offset:2048 sc1\n\t" \
  "buffer_load_dwordx4 v[220:223], v242, %[ws], s62 offen offset:3072 sc1\n\t" \
  "s_add_u32 s72, s72, 1\n\t" \
  "s_cmp_lt_u32 s72, %[cap]\n\t" \
  "s_waitcnt vmcnt(0)\n\t" \
  "s_cbranch_scc1 .Ltl_c0\n\t" \
  "buffer_store_dword v243, off, %[ws], 0 offset:256 sc1\n\t" \
  "s_branch .Ltl_k0\n\t" \
  ".Ltl_cd:\n\t" \
  "s_mov_b32 s74, s73\n\t" \
  "s_and_b32 s63, s73, 15\n\t" \
  "v_cmp_eq_u32 vcc, s63, v157\n\t" \
  "s_cmp_lt_u32 s73, 32\n\t" \
  "s_cbranch_scc1 .Ltl_d0\n\t" \
  "s_cmp_lt_u32 s73, 48\n\t" \
  "s_cbranch_scc1 .Ltl_e2\n\t" \
  ".Ltl_e3:\n\t" \
  "v_pk_fma_f32 v[96:97], v[12:13], v[214:215], v[214:215] op_sel:[0,0,1] op_sel_hi:[1,0,1]\n\t" \
  "v_pk_fma_f32 v[98:99], v[14:15], v[214:215], v[214:215] op_sel:[0,0,1] op_sel_hi:[1,0,1]\n\t" \
  "v_pk_fma_f32 v[100:101], v[28:29], v[214:215], v[214:215] op_sel:[0,0,1] op_sel_hi:[1,0,1]\n\t" \
  "v_pk_fma_f32 v[102:103], v[30:31], v[214:215], v[214:215] op_sel:[0,0,1] op_sel_hi:[1,0,1]\n\t" \
  "v_mov_b32 v120, v172\n\t" \
  "v_mov_b32 v122, v173\n\t" \
  "v_mov_b32 v124, v174\n\t" \
  "v_mov_b32 v126, v175\n\t" \
  "s_branch .Ltl_cs\n\t" \
  ".Ltl_e2:\n\t" \
  "v_pk_fma_f32 v[96:97], v[8:9], v[212:213], v[212:213] op_sel:[0,0,1] op_sel_hi:[1,0,1]\n\t" \
  "v_pk_fma_f32 v[98:99], v[10:11], v[212:213], v[212:213] op_sel:[0,0,1] op_sel_hi:[1,0,1]\n\t" \
  "v_pk_fma_f32 v[100:101], v[24:25], v[212:213], v[212:213] op_sel:[0,0,1] op_sel_hi:[1,0,1]\n\t" \
  "v_pk_fma_f32 v[102:103], v[26:27], v[212:213], v[212:213] op_sel:[0,0,1] op_sel_hi:[1,0,1]\n\t" \
  "v_mov_b32 v120, v168\n\t" \
  "v_mov_b32 v122, v169\n\t" \
  "v_mov_b32 v124, v170\n\t" \
  "v_mov_b32 v126, v171\n\t" \
  "s_branch .Ltl_cs\n\t" \
  ".Ltl_d0:\n\t" \
  "s_cmp_lt_u32 s73, 16\n\t" \
  "s_cbranch_scc1 .Ltl_e0\n\t" \
  ".Ltl_e1:\n\t" \
  "v_pk_fma_f32 v[96:97], v[4:5], v[210:211], v[210:211] op_sel:[0,0,1] op_sel_hi:[1,0,1]\n\t" \
  "v_pk_fma_f32 v[98:99], v[6:7], v[210:211], v[210:211] op_sel:[0,0,1] op_sel_hi:[1,0,1]\n\t" \
  "v_pk_fma_f32 v[100:101], v[20:21], v[210:211], v[210:211] op_sel:[0,0,1] op_sel_hi:[1,0,1]\n\t" \
  "v_pk_fma_f32 v[102:103], v[22:23], v[210:211], v[210:211] op_sel:[0,0,1] op_sel_hi:[1,0,1]\n\t" \
  "v_mov_b32 v120, v164\n\t" \
  "v_mov_b32 v122, v165\n\t" \
  "v_mov_b32 v124, v166\n\t" \
  "v_mov_b32 v126, v167\n\t" \
  "s_branch .Ltl_cs\n\t" \
  ".Ltl_e0:\n\t" \
  "v_pk_fma_f32 v[96:97], v[0:1], v[208:209], v[208:209] op_sel:[0,0,1] op_sel_hi:[1,0,1]\n\t" \
  "v_pk_fma_f32 v[98:99], v[2:3], v[208:209], v[208:209] op_sel:[0,0,1] op_sel_hi:[1,0,1]\n\t" \
  "v_pk_fma_f32 v[100:101], v[16:17], v[208:209], v[208:209] op_sel:[0,0,1] op_sel_hi:[1,0,1]\n\t" \
  "v_pk_fma_f32 v[102:103], v[18:19], v[208:209], v[208:209] op_sel:[0,0,1] op_sel_hi:[1,0,1]\n\t" \
  "v_mov_b32 v120, v160\n\t" \
  "v_mov_b32 v122, v161\n\t" \
  "v_mov_b32 v124, v162\n\t" \
  "v_mov_b32 v126, v163\n\t" \
  ".Ltl_cs:\n\t" \
  "v_pk_fma_f32 v[96:97], v[96:97], v[136:137], v[128:129]\n\t" \
  "v_pk_fma_f32 v[98:99], v[98:99], v[138:139], v[130:131]\n\t" \
  "v_pk_fma_f32 v[100:101], v[100:101], v[140:141], v[132:133]\n\t" \
  "v_pk_fma_f32 v[102:103], v[102:103], v[142:143], v[134:135]\n\t" \
  "v_cndmask_b32 v239, v156, v239, vcc\n\t" \
  "v_cvt_pk_bf16_f32 v112, v96, v97\n\t" \
  "v_cvt_pk_bf16_f32 v114, v98, v99\n\t" \
  "v_cvt_pk_bf16_f32 v116, v100, v101\n\t" \
  "v_cvt_pk_bf16_f32 v118, v102, v103\n\t" \
  "buffer_store_dwordx4 v[112:115], v239, %[ws], s66 offen offset:0 sc1\n\t" \
  "buffer_store_dwordx4 v[116:119], v239, %[ws], s66 offen offset:64 sc1\n\t" \
  "buffer_store_dwordx4 v[112:115], v239, %[ws], s77 offen offset:0 sc1\n\t" \
  "buffer_store_dwordx4 v[116:119], v239, %[ws], s77 offen offset:64 sc1\n\t" \
  "buffer_store_dwordx4 v[120:123], v239, %[ws], s67 offen offset:0 sc1\n\t" \
  "buffer_store_dwordx4 v[124:127], v239, %[ws], s67 offen offset:64 sc1\n\t" \
  "s_branch .Ltl_ce\n\t" \
  ".p2align 12\n\t" \
  ".Lht1:\n\t" \
  "s_cbranch_execz .Lht1n\n\t" \
  "s_waitcnt vmcnt(0)\n\t" \
  ".Ltl_c1:\n\t" \
  "v_cmp_eq_u32 vcc, %[tag], v217\n\t" \
  "v_xor_b32 v153, %[tag], v219\n\t" \
  "v_bitop3_b32 v153, v153, v221, %[tag] bitop3:0xf6\n\t" \
  "v_cndmask_b32 v152, 0, v216, vcc\n\t" \
  "v_bitop3_b32 v153, v153, v223, %[tag] bitop3:0xf6\n\t" \
  "v_bitop3_b32 v153, v153, v225, %[tag] bitop3:0xf6\n\t" \
  "v_max_u32_dpp v152, v152, v152 quad_perm:[1,0,3,2] row_mask:0xf bank_mask:0xf\n\t" \
  "s_or_b32 s77, vcc_lo, s68\n\t" \
  "v_cmp_ne_u32 vcc, 0, v153\n\t" \
  "v_max_u32_dpp v152, v152, v152 quad_perm:[2,3,0,1] row_mask:0xf bank_mask:0xf\n\t" \
  "s_nop 1\n\t" \
  "v_max_u32_dpp v152, v152, v152 row_half_mirror row_mask:0xf bank_mask:0xf\n\t" \
  "s_nop 1\n\t" \
  "v_max_u32_dpp v152, v152, v152 row_mirror row_mask:0xf bank_mask:0xf\n\t" \
  "s_nop 1\n\t" \
  "v_max_u32_dpp v152, v152, v152 row_bcast:15 row_mask:0xa bank_mask:0xf\n\t" \
  "s_nop 0\n\t" \
  "v_readlane_b32 s70, v152, 31\n\t" \
  "s_max_u32 s70, s70, s69\n\t" \
  "s_andn2_b32 s63, 0x780, s70\n\t" \
  "s_lshl_b32 s63, s63, 7\n\t" \
  "s_andn2_b32 s64, 64, s70\n\t" \
  "s_lshl_b32 s64, s64, 6\n\t" \
  "s_or_b32 s64, s64, s63\n\t" \
  "s_xor_b32 s63, s64, s71\n\t" \
  "s_mov_b32 s71, s64\n\t" \
  "s_orn2_b32 s63, s63, s77\n\t" \
  "s_or_b32 vcc_lo, vcc_lo, vcc_hi\n\t" \
  "s_or_b32 s63, s63, vcc_lo\n\t" \
  "s_cbranch_scc1 .Ltl_s1\n\t" \
  ".Ltl_k1:\n\t" \
  STP("s_memrealtime s[90:91]\n\t") \
  STP("s_memtime s[82:83]\n\t") \
  STP("s_waitcnt lgkmcnt(0)\n\t") \
  STP("s_or_b32 s90, s90, 1\n\t") \
  STP("v_writelane_b32 %[stv], s90, 48\n\t") \
  STP("v_writelane_b32 %[stv], s82, 49\n\t") \
  "s_cmp_eq_u32 s62, 6\n\t" \
  "s_cbranch_scc1 .Ltl_nf\n\t" \
  "v_dot2_f32_bf16 v96, v64, v218, 0\n\t" \
  "v_dot2_f32_bf16 v104, v241, v218, 0\n\t" \
  "v_dot2_f32_bf16 v97, v68, v218, 0\n\t" \
  "v_dot2_f32_bf16 v105, v180, v218, 0\n\t" \
  "v_dot2_f32_bf16 v98, v65, v218, 0\n\t" \
  "v_dot2_f32_bf16 v106, v176, v218, 0\n\t" \
  "v_dot2_f32_bf16 v99, v69, v218, 0\n\t" \
  "v_dot2_f32_bf16 v107, v181, v218, 0\n\t" \
  "v_dot2_f32_bf16 v100, v66, v218, 0\n\t" \
  "v_dot2_f32_bf16 v108, v177, v218, 0\n\t" \
  "v_dot2_f32_bf16 v101, v70, v218, 0\n\t" \
  "v_dot2_f32_bf16 v109, v182, v218, 0\n\t" \
  "v_dot2_f32_bf16 v102, v67, v218, 0\n\t" \
  "v_dot2_f32_bf16 v110, v178, v218, 0\n\t" \
  "v_dot2_f32_bf16 v103, v71, v218, 0\n\t" \
  "v_dot2_f32_bf16 v111, v183, v218, 0\n\t" \
  "v_dot2_f32_bf16 v96, v72, v220, v96\n\t" \
  "v_dot2_f32_bf16 v104, v179, v220, v104\n\t" \
  "v_dot2_f32_bf16 v97, v76, v220, v97\n\t" \
  "v_dot2_f32_bf16 v105, v188, v220, v105\n\t" \
  "v_dot2_f32_bf16 v98, v73, v220, v98\n\t" \
  "v_dot2_f32_bf16 v106, v184, v220, v106\n\t" \
  "v_dot2_f32_bf16 v99, v77, v220, v99\n\t" \
  "v_dot2_f32_bf16 v107, v189, v220, v107\n\t" \
  "v_dot2_f32_bf16 v100, v74, v220, v100\n\t" \
  "v_dot2_f32_bf16 v108, v185, v220, v108\n\t" \
  "v_dot2_f32_bf16 v101, v78, v220, v101\n\t" \
  "v_dot2_f32_bf16 v109, v190, v220, v109\n\t" \
  "v_dot2_f32_bf16 v102, v75, v220, v102\n\t" \
  "v_dot2_f32_bf16 v110, v186, v220, v110\n\t" \
  "v_dot2_f32_bf16 v103, v79, v220, v103\n\t" \
  "v_dot2_f32_bf16 v111, v191, v220, v111\n\t" \
  "v_dot2_f32_bf16 v96, v80, v222, v96\n\t" \
  "v_dot2_f32_bf16 v104, v187, v222, v104\n\t" \
  "v_dot2_f32_bf16 v97, v84, v222, v97\n\t" \
  "v_dot2_f32_bf16 v105, v196, v222, v105\n\t" \
  "v_dot2_f32_bf16 v98, v81, v222, v98\n\t" \
  "v_dot2_f32_bf16 v106, v192, v222, v106\n\t" \
  "v_dot2_f32_bf16 v99, v85, v222, v99\n\t" \
  "v_dot2_f32_bf16 v107, v197, v222, v107\n\t" \
  "v_dot2_f32_bf16 v100, v82, v222, v100\n\t" \
  "v_dot2_f32_bf16 v108, v193, v222, v108\n\t" \
  "v_dot2_f32_bf16 v101, v86, v222, v101\n\t" \
  "v_dot2_f32_bf16 v109, v198, v222, v109\n\t" \
  "v_dot2_f32_bf16 v102, v83, v222, v102\n\t" \
  "v_dot2_f32_bf16 v110, v194, v222, v110\n\t" \
  "v_dot2_f32_bf16 v103, v87, v222, v103\n\t" \
  "v_dot2_f32_bf16 v111, v199, v222, v111\n\t" \
  "v_dot2_f32_bf16 v96, v88, v224, v96\n\t" \
  "v_dot2_f32_bf16 v104, v195, v224, v104\n\t" \
  "v_dot2_f32_bf16 v97, v92, v224, v97\n\t" \
  "v_dot2_f32_bf16 v105, v204, v224, v105\n\t" \
  "v_dot2_f32_bf16 v98, v89, v224, v98\n\t" \
  "v_dot2_f32_bf16 v106, v200, v224, v106\n\t" \
  "v_dot2_f32_bf16 v99, v93, v224, v99\n\t" \
  "v_dot2_f32_bf16 v107, v205, v224, v107\n\t" \
  "v_dot2_f32_bf16 v100, v90, v224, v100\n\t" \
  "v_dot2_f32_bf16 v108, v201, v224, v108\n\t" \
  "v_dot2_f32_bf16 v101, v94, v224, v101\n\t" \
  "v_dot2_f32_bf16 v109, v206, v224, v109\n\t" \
  "v_dot2_f32_bf16 v102, v91, v224, v102\n\t" \
  "v_dot2_f32_bf16 v110, v202, v224, v110\n\t" \
  "v_dot2_f32_bf16 v103, v95, v224, v103\n\t" \
  "v_dot2_f32_bf16 v111, v207, v224, v111\n\t" \
  "v_permlane32_swap_b32 v96, v104\n\t" \
  "v_permlane32_swap_b32 v97, v105\n\t" \
  "v_permlane32_swap_b32 v98, v106\n\t" \
  "v_permlane32_swap_b32 v99, v107\n\t" \
  "v_permlane32_swap_b32 v100, v108\n\t" \
  "v_permlane32_swap_b32 v101, v109\n\t" \
  "v_permlane32_swap_b32 v102, v110\n\t" \
  "v_permlane32_swap_b32 v103, v111\n\t" \
  "v_pk_add_f32 v[96:97], v[96:97], v[104:105]\n\t" \
  "v_pk_add_f32 v[100:101], v[100:101], v[108:109]\n\t" \
  "v_pk_add_f32 v[98:99], v[98:99], v[106:107]\n\t" \
  "v_pk_add_f32 v[102:103], v[102:103], v[110:111]\n\t" \
  "v_permlane16_swap_b32 v96, v100\n\t" \
  "v_permlane16_swap_b32 v97, v101\n\t" \
  "v_permlane16_swap_b32 v98, v102\n\t" \
  "v_permlane16_swap_b32 v99, v103\n\t" \
  "v_add_f32 v96, v96, v100\n\t" \
  "v_add_f32 v97, v97, v101\n\t" \
  "v_add_f32 v98, v98, v102\n\t" \
  "v_add_f32 v99, v99, v103\n\t" \
  "v_add_f32_dpp v96, v96, v96 row_ror:8 row_mask:0xf bank_mask:0xf\n\t" \
  "v_add_f32_dpp v97, v97, v97 row_ror:8 row_mask:0xf bank_mask:0xf\n\t" \
  "v_add_f32_dpp v98, v98, v98 row_ror:8 row_mask:0xf bank_mask:0xf\n\t" \
  "v_add_f32_dpp v99, v99, v99 row_ror:8 row_mask:0xf bank_mask:0xf\n\t" \
  "v_add_f32_dpp v96, v96, v96 row_ror:4 row_mask:0xf bank_mask:0xf\n\t" \
  "v_add_f32_dpp v97, v97, v97 row_ror:4 row_mask:0xf bank_mask:0xf\n\t" \
  "v_add_f32_dpp v98, v98, v98 row_ror:4 row_mask:0xf bank_mask:0xf\n\t" \
  "v_add_f32_dpp v99, v99, v99 row_ror:4 row_mask:0xf bank_mask:0xf\n\t" \
  "v_add_f32_dpp v96, v96, v96 row_ror:2 row_mask:0xf bank_mask:0xf\n\t" \
  "v_add_f32_dpp v97, v97, v97 row_ror:2 row_mask:0xf bank_mask:0xf\n\t" \
  "v_add_f32_dpp v98, v98, v98 row_ror:2 row_mask:0xf bank_mask:0xf\n\t" \
  "v_add_f32_dpp v99, v99, v99 row_ror:2 row_mask:0xf bank_mask:0xf\n\t" \
  "ds_write_b128 v245, v[96:99] offset:8192\n\t" \
  "v_mov_b32 v169, v243\n\t" \
  "v_mov_b32 v171, v243\n\t" \
  "s_waitcnt lgkmcnt(0)\n\t" \
  "s_barrier\n\t" \
  "ds_read2st64_b32 v[96:97], v246 offset0:32 offset1:33\n\t" \
  "ds_read2st64_b32 v[98:99], v248 offset0:32 offset1:33\n\t" \
  "s_waitcnt lgkmcnt(0)\n\t" \
  "v_pk_add_f32 v[96:97], v[96:97], v[98:99]\n\t" \
  "s_nop 0\n\t" \
  "v_add_f32 v96, v96, v97\n\t" \
  "v_cvt_pk_bf16_f32 v96, v96, v96\n\t" \
  "v_lshlrev_b32 v97, 16, v96\n\t" \
  "v_lshlrev_b32 v96, 16, v96\n\t" \
  "s_nop 1\n\t" \
  "v_permlane16_swap_b32 v96, v97\n\t" \
  "v_mul_f32 v98, 0xbfb8aa3b, v96\n\t" \
  "v_exp_f32 v98, v98\n\t" \
  "s_nop 0\n\t" \
  "v_add_f32 v98, 1.0, v98\n\t" \
  "v_rcp_f32 v98, v98\n\t" \
  "s_nop 0\n\t" \
  "v_mul_f32 v96, v98, v96\n\t" \
  "v_cvt_pk_bf16_f32 v96, v96, v96\n\t" \
  "v_lshlrev_b32 v96, 16, v96\n\t" \
  "v_mul_f32 v96, v97, v96\n\t" \
  "s_nop 1\n\t" \
  "v_mov_b32_dpp v97, v96 quad_perm:[1,0,3,2] row_mask:0xf bank_mask:0xf\n\t" \
  "v_cvt_pk_bf16_f32 v96, v96, v97\n\t" \
  "s_nop 0\n\t" \
  "v_readlane_b32 s75, v96, 0\n\t" \
  "v_readlane_b32 s76, v96, 2\n\t" \
  "v_readlane_b32 s63, v96, 4\n\t" \
  "v_readlane_b32 s64, v96, 6\n\t" \
  "v_readlane_b32 s65, v96, 8\n\t" \
  "v_readlane_b32 s77, v96, 10\n\t" \
  "v_readlane_b32 s87, v96, 12\n\t" \
  "v_readlane_b32 s88, v96, 14\n\t" \
  "v_dot2_f32_bf16 v100, v240, s75, 0\n\t" \
  "v_dot2_f32_bf16 v101, v34, s75, 0\n\t" \
  "v_dot2_f32_bf16 v102, v32, s75, 0\n\t" \
  "v_dot2_f32_bf16 v103, v35, s75, 0\n\t" \
  "v_dot2_f32_bf16 v100, v33, s76, v100\n\t" \
  "v_dot2_f32_bf16 v101, v38, s76, v101\n\t" \
  "v_dot2_f32_bf16 v102, v36, s76, v102\n\t" \
  "v_dot2_f32_bf16 v103, v39, s76, v103\n\t" \
  "v_dot2_f32_bf16 v100, v37, s63, v100\n\t" \
  "v_dot2_f32_bf16 v101, v42, s63, v101\n\t" \
  "v_dot2_f32_bf16 v102, v40, s63, v102\n\t" \
  "v_dot2_f32_bf16 v103, v43, s63, v103\n\t" \
  "v_dot2_f32_bf16 v100, v41, s64, v100\n\t" \
  "v_dot2_f32_bf16 v101, v46, s64, v101\n\t" \
  "v_dot2_f32_bf16 v102, v44, s64, v102\n\t" \
  "v_dot2_f32_bf16 v103, v47, s64, v103\n\t" \
  "v_dot2_f32_bf16 v100, v45, s65, v100\n\t" \
  "v_dot2_f32_bf16 v101, v50, s65, v101\n\t" \
  "v_dot2_f32_bf16 v102, v48, s65, v102\n\t" \
  "v_dot2_f32_bf16 v103, v51, s65, v103\n\t" \
  "v_dot2_f32_bf16 v100, v49, s77, v100\n\t" \
  "v_dot2_f32_bf16 v101, v54, s77, v101\n\t" \
  "v_dot2_f32_bf16 v102, v52, s77, v102\n\t" \
  "v_dot2_f32_bf16 v103, v55, s77, v103\n\t" \
  "v_dot2_f32_bf16 v100, v53, s87, v100\n\t" \
  "v_dot2_f32_bf16 v101, v58, s87, v101\n\t" \
  "v_dot2_f32_bf16 v102, v56, s87, v102\n\t" \
  "v_dot2_f32_bf16 v103, v59, s87, v103\n\t" \
  "v_dot2_f32_bf16 v100, v57, s88, v100\n\t" \
  "v_dot2_f32_bf16 v101, v62, s88, v101\n\t" \
  "v_dot2_f32_bf16 v102, v60, s88, v102\n\t" \
  "v_dot2_f32_bf16 v103, v63, s88, v103\n\t" \
  "s_nop 0\n\t" \
  "v_cvt_pk_bf16_f32 v168, v100, v101\n\t" \
  "s_nop 0\n\t" \
  "v_cvt_pk_bf16_f32 v170, v102, v103\n\t" \
  "buffer_store_dwordx4 v[168:171], v247, %[ws], s85 offen sc1\n\t" \
  ".Ltl_nf:\n\t" \
  STP("s_memrealtime s[90:91]\n\t") \
  STP("s_memtime s[82:83]\n\t") \
  STP("s_waitcnt lgkmcnt(0)\n\t") \
  STP("s_or_b32 s90, s90, 1\n\t") \
  STP("v_writelane_b32 %[stv], s90, 50\n\t") \
  STP("v_writelane_b32 %[stv], s82, 51\n\t") \
  "s_cmp_lg_u32 s84, 0\n\t" \
  "s_cbranch_scc1 .Ltl_df\n\t" \
  "s_cmp_lg_u32 s78, 0\n\t" \
  "s_cbranch_scc1 .Ltl_o1\n\t" \
  "s_cmp_lt_u32 s74, 64\n\t" \
  "s_cbranch_scc1 .Ltl_o1\n\t" \
  ".Ltl_oe:\n\t" \
  "s_or_b32 s63, %[bid], %[wid]\n\t" \
  "s_cbranch_scc0 .Ltl_ep\n\t" \
  ".Ltl_ee:\n\t" \
  "s_cmp_eq_u32 s62, 6\n\t" \
  "s_cbranch_scc0 .Ltl_end\n\t" \
  "s_lshl_b32 s77, s79, 9\n\t" \
  "s_lshl_b32 s63, s80, 7\n\t" \
  "s_add_u32 s77, s77, s63\n\t" \
  "v_lshl_add_u32 v37, v157, 3, s77\n\t" \
  "s_add_u32 s63, s71, s61\n\t" \
  "s_add_u32 s63, s63, 262144\n\t" \
  "v_add_u32 v33, s63, v37\n\t" \
  "s_andn2_b32 s63, 0x700, s70\n\t" \
  "s_lshl_b32 s63, s63, 6\n\t" \
  "s_andn2_b32 s64, 0xc0, s70\n\t" \
  "s_lshl_b32 s64, s64, 5\n\t" \
  "s_add_u32 s63, s63, s64\n\t" \
  "s_add_u32 s63, s63, s61\n\t" \
  "s_add_u32 s63, s63, 1966080\n\t" \
  "v_and_b32 v34, 0x7ff, v37\n\t" \
  "v_add_u32 v34, s63, v34\n\t" \
  "s_and_b32 s77, s77, 0x7ff\n\t" \
  "s_add_u32 s77, s77, s61\n\t" \
  "s_lshl_b32 s63, %[wid], 15\n\t" \
  "s_add_u32 s77, s77, s63\n\t" \
  "v_and_b32 v36, 0x70, v242\n\t" \
  "v_lshl_add_u32 v32, v154, 14, v36\n\t" \
  "v_and_b32 v35, 24, v244\n\t" \
  "v_lshl_add_u32 v32, v35, 8, v32\n\t" \
  "s_add_u32 s60, s77, 1048576\n\t" \
  "s_add_u32 s65, s77, 1179648\n\t" \
  "s_add_u32 s66, s77, 1310720\n\t" \
  "s_add_u32 s67, s77, 1441792\n\t" \
  "s_add_u32 s68, s77, 1572864\n\t" \
  "s_add_u32 s73, s77, 1703936\n\t" \
  "s_add_u32 s74, s77, 1835008\n\t" \
  "s_lshl_b32 s63, s80, 12\n\t" \
  "s_add_u32 s63, s63, 147456\n\t" \
  "v_lshl_add_u32 v40, v157, 3, s63\n\t" \
  "ds_read_b64 v[40:41], v40 offset:2944\n\t" \
  "s_ashr_i32 s63, s70, 31\n\t" \
  "s_orn2_b32 s63, 0x80000000, s63\n\t" \
  "s_xor_b32 s63, s63, s70\n\t" \
  "s_and_b32 s63, s63, 0xffff0000\n\t" \
  "v_mov_b32 v38, 0xbfb8aa3b\n\t" \
  "v_mul_f32 v38, s63, v38\n\t" \
  "v_exp_f32 v38, v38\n\t" \
  "v_lshrrev_b32 v37, 1, v37\n\t" \
  "v_add_f32 v38, 1.0, v38\n\t" \
  "v_rcp_f32 v38, v38\n\t" \
  "s_andn2_b32 s64, 0x7ff, s70\n\t" \
  "s_lshl_b32 s64, s64, 11\n\t" \
  "v_cvt_pk_bf16_f32 v38, v38, v38\n\t" \
  "v_lshlrev_b32 v38, 16, v38\n\t" \
  "v_lshrrev_b32 v153, 4, v244\n\t" \
  "v_lshl_add_u32 v36, v153, 2, v36\n\t" \
  "v_lshl_add_u32 v36, %[wid], 7, v36\n\t" \
  "v_and_b32 v35, 8, v244\n\t" \
  "v_lshl_add_u32 v36, v35, 7, v36\n\t" \
  "v_lshlrev_b32 v35, 3, v157\n\t" \
  "v_add_u32 v35, 0x3000, v35\n\t" \
  "v_min_u32 v153, 1, v153\n\t" \
  "v_lshl_or_b32 v37, v153, 31, v37\n\t" \
  "s_mov_b32 s75, 0x3f80\n\t" \
  "s_mov_b32 s76, 0x3f800000\n\t" \
  "s_mov_b32 s72, 0\n\t" \
  "buffer_load_dwordx2 v[42:43], v33, %[ws], 0 offen sc1\n\t" \
  "buffer_load_dwordx2 v[44:45], v34, %[ws], 0 offen sc1\n\t" \
  "buffer_load_dwordx4 v[0:3], v32, %[ws], s60 offen sc1\n\t" \
  "buffer_load_dwordx4 v[4:7], v32, %[ws], s65 offen sc1\n\t" \
  "buffer_load_dwordx4 v[8:11], v32, %[ws], s66 offen sc1\n\t" \
  "buffer_load_dwordx4 v[12:15], v32, %[ws], s67 offen sc1\n\t" \
  "buffer_load_dwordx4 v[16:19], v32, %[ws], s68 offen sc1\n\t" \
  "buffer_load_dwordx4 v[20:23], v32, %[ws], s73 offen sc1\n\t" \
  "buffer_load_dwordx4 v[24:27], v32, %[ws], s74 offen sc1\n\t" \
  "s_waitcnt vmcnt(0)\n\t" \
  ".Ltl_c2:\n\t" \
  "v_xor_b32 v152, %[tag], v43\n\t" \
  "v_bitop3_b32 v152, v152, v45, %[tag] bitop3:0xf6\n\t" \
  "v_bitop3_b32 v152, v152, v1, %[tag] bitop3:0xf6\n\t" \
  "v_bitop3_b32 v152, v152, v3, %[tag] bitop3:0xf6\n\t" \
  "v_bitop3_b32 v152, v152, v5, %[tag] bitop3:0xf6\n\t" \
  "v_bitop3_b32 v152, v152, v7, %[tag] bitop3:0xf6\n\t" \
  "v_bitop3_b32 v152, v152, v9, %[tag] bitop3:0xf6\n\t" \
  "v_bitop3_b32 v152, v152, v11, %[tag] bitop3:0xf6\n\t" \
  "v_bitop3_b32 v152, v152, v13, %[tag] bitop3:0xf6\n\t" \
  "v_bitop3_b32 v152, v152, v15, %[tag] bitop3:0xf6\n\t" \
  "v_bitop3_b32 v152, v152, v17, %[tag] bitop3:0xf6\n\t" \
  "v_bitop3_b32 v152, v152, v19, %[tag] bitop3:0xf6\n\t" \
  "v_bitop3_b32 v152, v152, v21, %[tag] bitop3:0xf6\n\t" \
  "v_bitop3_b32 v152, v152, v23, %[tag] bitop3:0xf6\n\t" \
  "v_bitop3_b32 v152, v152, v25, %[tag] bitop3:0xf6\n\t" \
  "v_bitop3_b32 v152, v152, v27, %[tag] bitop3:0xf6\n\t" \
  "v_cmp_ne_u32 vcc, 0, v152\n\t" \
  "s_cbranch_vccnz .Ltl_s2\n\t" \
  ".Ltl_k2:\n\t" \
  "v_dot2_f32_bf16 v48, v0, s75, 0\n\t" \
  "v_dot2_f32_bf16 v49, v0, s76, 0\n\t" \
  "v_dot2_f32_bf16 v50, v2, s75, 0\n\t" \
  "v_dot2_f32_bf16 v51, v2, s76, 0\n\t" \
  "v_dot2_f32_bf16 v48, v4, s75, v48\n\t" \
  "v_dot2_f32_bf16 v49, v4, s76, v49\n\t" \
  "v_dot2_f32_bf16 v50, v6, s75, v50\n\t" \
  "v_dot2_f32_bf16 v51, v6, s76, v51\n\t" \
  "v_dot2_f32_bf16 v48, v8, s75, v48\n\t" \
  "v_dot2_f32_bf16 v49, v8, s76, v49\n\t" \
  "v_dot2_f32_bf16 v50, v10, s75, v50\n\t" \
  "v_dot2_f32_bf16 v51, v10, s76, v51\n\t" \
  "v_dot2_f32_bf16 v48, v12, s75, v48\n\t" \
  "v_dot2_f32_bf16 v49, v12, s76, v49\n\t" \
  "v_dot2_f32_bf16 v50, v14, s75, v50\n\t" \
  "v_dot2_f32_bf16 v51, v14, s76, v51\n\t" \
  "v_dot2_f32_bf16 v48, v16, s75, v48\n\t" \
  "v_dot2_f32_bf16 v49, v16, s76, v49\n\t" \
  "v_dot2_f32_bf16 v50, v18, s75, v50\n\t" \
  "v_dot2_f32_bf16 v51, v18, s76, v51\n\t" \
  "v_dot2_f32_bf16 v48, v20, s75, v48\n\t" \
  "v_dot2_f32_bf16 v49, v20, s76, v49\n\t" \
  "v_dot2_f32_bf16 v50, v22, s75, v50\n\t" \
  "v_dot2_f32_bf16 v51, v22, s76, v51\n\t" \
  "v_dot2_f32_bf16 v48, v24, s75, v48\n\t" \
  "v_dot2_f32_bf16 v49, v24, s76, v49\n\t" \
  "v_dot2_f32_bf16 v50, v26, s75, v50\n\t" \
  "v_dot2_f32_bf16 v51, v26, s76, v51\n\t" \
  "s_nop 1\n\t" \
  "v_permlane32_swap_b32 v48, v50\n\t" \
  "v_permlane32_swap_b32 v49, v51\n\t" \
  "v_add_f32 v48, v48, v50\n\t" \
  "v_add_f32 v49, v49, v51\n\t" \
  "v_and_b32 v43, 0xffff0000, v42\n\t" \
  "v_lshlrev_b32 v42, 16, v42\n\t" \
  "v_permlane16_swap_b32 v48, v49\n\t" \
  "v_add_f32 v48, v48, v49\n\t" \
  "v_and_b32 v45, 0xffff0000, v44\n\t" \
  "v_lshlrev_b32 v44, 16, v44\n\t" \
  "v_add_f32_dpp v48, v48, v48 row_ror:8 row_mask:0xf bank_mask:0xf\n\t" \
  "ds_write_b32 v36, v48 offset:12288\n\t" \
  "s_cmp_lg_u32 %[wid], 0\n\t" \
  "s_waitcnt lgkmcnt(0)\n\t" \
  "s_barrier\n\t" \
  "s_cbranch_scc1 .Ltl_end\n\t" \
  "ds_read2_b64 v[0:3], v35 offset1:16\n\t" \
  "ds_read2_b64 v[4:7], v35 offset0:32 offset1:48\n\t" \
  "s_waitcnt lgkmcnt(1)\n\t" \
  "v_pk_add_f32 v[0:1], v[0:1], v[2:3]\n\t" \
  "s_waitcnt lgkmcnt(0)\n\t" \
  "v_pk_add_f32 v[4:5], v[4:5], v[6:7]\n\t" \
  "s_nop 0\n\t" \
  "v_pk_add_f32 v[0:1], v[0:1], v[4:5]\n\t" \
  "s_nop 0\n\t" \
  "v_cvt_pk_bf16_f32 v0, v0, v1\n\t" \
  "v_and_b32 v1, 0xffff0000, v0\n\t" \
  "v_lshlrev_b32 v0, 16, v0\n\t" \
  "v_mul_f32 v0, v38, v0\n\t" \
  "v_mul_f32 v1, v38, v1\n\t" \
  "v_cvt_pk_bf16_f32 v0, v0, v1\n\t" \
  "v_and_b32 v1, 0xffff0000, v0\n\t" \
  "v_lshlrev_b32 v0, 16, v0\n\t" \
  "v_add_f32 v0, v0, v42\n\t" \
  "v_add_f32 v1, v1, v43\n\t" \
  "v_cvt_pk_bf16_f32 v0, v0, v1\n\t" \
  "v_and_b32 v1, 0xffff0000, v0\n\t" \
  "v_lshlrev_b32 v0, 16, v0\n\t" \
  "v_mul_f32 v0, v0, v40\n\t" \
  "v_mul_f32 v1, v1, v41\n\t" \
  "v_cvt_pk_bf16_f32 v0, v0, v1\n\t" \
  "v_and_b32 v1, 0xffff0000, v0\n\t" \
  "v_lshlrev_b32 v0, 16, v0\n\t" \
  "v_add_f32 v0, v0, v44\n\t" \
  "v_add_f32 v1, v1, v45\n\t" \
  "v_cvt_pk_bf16_f32 v0, v0, v1\n\t" \
  "buffer_store_dword v0, v37, s[96:99], s64 offen sc0 sc1\n\t" \
  "s_branch .Ltl_end\n\t" \
  ".Ltl_s1:\n\t" \
  "s_sleep 1\n\t" \
  "buffer_load_dwordx2 v[216:217], v235, %[ws], 0 offen sc1\n\t" \
  "buffer_load_dwordx4 v[218:221], v236, %[ws], s71 offen sc1\n\t" \
  "buffer_load_dwordx4 v[222:225], v236, %[ws], s71 offen offset:16 sc1\n\t" \
  "s_add_u32 s72, s72, 1\n\t" \
  "s_cmp_lt_u32 s72, %[cap]\n\t" \
  "s_waitcnt vmcnt(0)\n\t" \
  "s_cbranch_scc1 .Ltl_c1\n\t" \
  "buffer_store_dword v243, off, %[ws], 0 offset:256 sc1\n\t" \
  "s_branch .Ltl_k1\n\t" \
  ".Ltl_o1:\n\t" \
  "s_cmp_eq_u32 s70, s69\n\t" \
  "s_cselect_b32 s63, 0x80000000, 0\n\t" \
  "s_xor_b32 s77, s78, 0x80000000\n\t" \
  "v_or_b32 v152, s63, v238\n\t" \
  "v_or_b32 v153, s77, v238\n\t" \
  "v_cmp_gt_i32 vcc, 0, v228\n\t" \
  "s_mov_b32 s64, 0\n\t" \
  "s_nop 0\n\t" \
  "v_cndmask_b32 v228, v153, v152, vcc\n\t" \
  "buffer_store_dwordx4 v[112:115], v228, s[96:99], s64 offen sc0 sc1\n\t" \
  "v_cmp_gt_i32 vcc, 0, v229\n\t" \
  "s_mov_b32 s64, 32768\n\t" \
  "s_nop 0\n\t" \
  "v_cndmask_b32 v229, v153, v152, vcc\n\t" \
  "buffer_store_dwordx4 v[116:119], v229, s[96:99], s64 offen sc0 sc1\n\t" \
  "v_cmp_gt_i32 vcc, 0, v230\n\t" \
  "s_mov_b32 s64, 65536\n\t" \
  "s_nop 0\n\t" \
  "v_cndmask_b32 v230, v153, v152, vcc\n\t" \
  "buffer_store_dwordx4 v[120:123], v230, s[96:99], s64 offen sc0 sc1\n\t" \
  "v_cmp_gt_i32 vcc, 0, v231\n\t" \
  "s_mov_b32 s64, 98304\n\t" \
  "s_nop 0\n\t" \
  "v_cndmask_b32 v231, v153, v152, vcc\n\t" \
  "buffer_store_dwordx4 v[124:127], v231, s[96:99], s64 offen sc0 sc1\n\t" \
  "s_branch .Ltl_oe\n\t" \
  ".Ltl_df:\n\t" \
  "s_mov_b32 s78, 0\n\t" \
  "s_cmp_eq_u32 s70, s69\n\t" \
  "s_cselect_b32 s74, s74, 0x100\n\t" \
  "s_branch .Ltl_h2\n\t" \
  ".Ltl_ep:\n\t" \
  "s_mov_b32 s63, 2490368\n\t" \
  "buffer_store_dword v243, v249, %[ws], s63 offen sc1\n\t" \
  "s_branch .Ltl_ee\n\t" \
  ".Ltl_s2:\n\t" \
  "s_sleep 1\n\t" \
  "buffer_load_dwordx2 v[42:43], v33, %[ws], 0 offen sc1\n\t" \
  "buffer_load_dwordx2 v[44:45], v34, %[ws], 0 offen sc1\n\t" \
  "buffer_load_dwordx4 v[0:3], v32, %[ws], s60 offen sc1\n\t" \
  "buffer_load_dwordx4 v[4:7], v32, %[ws], s65 offen sc1\n\t" \
  "buffer_load_dwordx4 v[8:11], v32, %[ws], s66 offen sc1\n\t" \
  "buffer_load_dwordx4 v[12:15], v32, %[ws], s67 offen sc1\n\t" \
  "buffer_load_dwordx4 v[16:19], v32, %[ws], s68 offen sc1\n\t" \
  "buffer_load_dwordx4 v[20:23], v32, %[ws], s73 offen sc1\n\t" \
  "buffer_load_dwordx4 v[24:27], v32, %[ws], s74 offen sc1\n\t" \
  "s_add_u32 s72, s72, 1\n\t" \
  "s_cmp_lt_u32 s72, %[cap]\n\t" \
  "s_waitcnt vmcnt(0)\n\t" \
  "s_cbranch_scc1 .Ltl_c2\n\t" \
  "buffer_store_dword v243, off, %[ws], 0 offset:256 sc1\n\t" \
  "s_branch .Ltl_k2\n\t" \
  ".Ltl_end:\n\t" \
  ""
#define TL_CLOB "v208", "v209", "v210", "v211", "v212", "v213", "v214", "v215", "v216", "v217", "v218", "v219", "v220", "v221", "v222", "v223", "v224", "v225", "v226", "v227", "v228", "v229", "v230", "v231", "v232", "v233", "v234", "v235", "v236", "v238", "v239", "v245", "v246", "v247", "v248", "v249", "v250", "s60", "s61", "s62", "s63", "s64", "s65", "s66", "s67", "s68", "s69", "s70", "s71", "s72", "s73", "s74", "s75", "s76", "s77", "s78", "s79", "s80", "s81", "s82", "s83", "s84", "s85", "s90", "s91", "s96", "s97", "s98", "s99", "vcc", "scc", "memory"
static_assert(W_EPOCH == 2490368 && W_GKEY == 65536 && W_CANDH == 262144 && W_STAT == 524288 && W_YG == 1048576 && W_CANDX == 1966080);
struct WoP { srd_t srd; unsigned wv[2], m, hl, base, wa; };
struct W4C {
  KVP kv;
  unsigned pf1, pf2, pf4, cto, po, px, vo, eso, hl, hl18, frb, xcb, xso, ss, mbp, gvo, l16, gvm, vs, c3;
  WoP wo; u16* out;
  srd_t sG, sU, sD, sX, sR, sM;
  unsigned gv, wa0, fw2, tagv, fbv, tlb, pre, otb, na, gra, fl; u32x32 q[4], up; f32x32 wm; u32x16 o3;
};
struct Ptrs { const u16 *wo, *w_r, *wg, *wu, *wd; u16* out; };
DI void attn_setup(const Ctx& k, W4C& c, const u16* kc, const u16* vc, const u16* pm, srd_t srdx, const Ptrs& a) {
  const unsigned wid = k.wid, fb = k.xcd * 28 + ffn_j(k);
  c.kv.sofk = KRS * 16u * wid;
  const size_t kvoff = ((size_t)k.kvh * SKV + (SKV - WIN)) * DH;
  c.kv.srdK = mk_srd(kc + kvoff, WIN * DH * 2); c.kv.srdV = mk_srd(vc + kvoff, WIN * DH * 2);
  const unsigned go = wid * 256 * FF + fb * 16, nG = (DM * FF - go) * 2;
  c.sG = mk_srd(a.wg + go, nG); c.sU = mk_srd(a.wu + go, nG);
  c.sM = mk_srd(pm + 2048 + k.xcd * 512, DM * 6 * DM * 2 - 4096 - k.xcd * 1024);
  c.wo.srd = mk_srd(a.wo, QD * DM * 2); c.sX = srdx;
  c.sD = mk_srd(a.wd + (size_t)fb * 16 * DM, ffn_idle(k) ? 0u : 32768u); c.sR = mk_srd(a.w_r, DM * 2);
  c.out = a.out;
  asm volatile("" : "+s"(c.out), "+s"(c.kv.srdK), "+s"(c.kv.srdV), "+s"(c.sG), "+s"(c.sU), "+s"(c.sM), "+s"(c.wo.srd), "+s"(c.sD), "+s"(c.sR));
  c.wo.m = wid * 4096; c.wo.base = (k.kvh * 1024 + k.hl * 128 + wid * 16) * (DM * 2) + k.hl * 256;
  c.pf1 = (k.kvh * 1024 + k.j * 32 + wid * 8) * (DM * 2) + (k.xcd & 1) * 8192;
  c.pf4 = k.j * (8 * FF * 2) - ffn_j(k) * 32;
}
DI void attention(Ctx& k, H4& g, W4C& c STAMP_ARG) {
  unsigned nb;
  asm volatile(AT_ASM
               : "+{v[0:31]}"(g.q[0]), "+{v[32:63]}"(g.q[1]), "+{v[64:95]}"(g.q[2]), "+{v[96:127]}"(g.q[3]), [pfd] "+{v246}"(g.pfd), "=&{v[128:159]}"(c.wm), "=&{v[160:175]}"(c.o3), "+{v224}"(c.kv.vksrc[0]), "+{v225}"(c.kv.vksrc[1]), "+{v226}"(c.kv.vksrc[2]), "+{v227}"(c.kv.vksrc[3]), "=&{v[176:207]}"(c.up),
                 "=&{v234}"(c.wo.wa), "=&{v213}"(c.gra), "+{v245}"(g.fvo), "=&{v243}"(c.tagv), "=&{v254}"(c.fbv),
                 "=&{v242}"(c.l16), "+{v247}"(c.tlb), "=&{v252}"(c.vs), [na] "=&s"(c.na), [nb] "=&s"(nb), [eso] "+s"(c.eso), [xcb] "+s"(c.xcb), [frb] "+s"(c.frb), [fw2] "+s"(c.fw2) AT_STV
               : "{v220}"(c.kv.vkb[0]), "{v221}"(c.kv.vkb[1]), "{v222}"(c.kv.vkb[2]), "{v223}"(c.kv.vkb[3]),
                 "{v228}"(c.kv.vvb[0]), "{v229}"(c.kv.vvb[1]), "{v230}"(c.kv.vvsrc[0]), "{v231}"(c.kv.vvsrc[1]), "{v232}"(c.gv), "{v233}"(c.gvm), "{v240}"(c.wo.wv[0]), "{v241}"(c.wo.wv[1]), "{v244}"(c.wa0), "{v248}"(c.c3),
                 [ws] "s"(k.ws), [sK] "s"(c.kv.srdK), [sV] "s"(c.kv.srdV), [sG] "s"(c.sG), [sU] "s"(c.sU), [wo] "s"(c.wo.srd), [sM] "s"(c.sM), [sD] "s"(c.sD), [sR] "s"(c.sR),
                 [m] "s"(c.wo.m), [sofk] "s"(c.wo.m), [hl18] "s"(c.hl18), [wob] "s"(c.wo.base), [pf2] "s"(c.pf2), AT_PF1 [pf4] "s"(c.pf4),
                 [tag] "s"(k.tag), [cap] "i"(kSpinCap), [cto] "s"(c.cto), [po] "s"(c.po), [px] "s"(c.px), [vo] "s"(c.vo), [pm] "s"(k.pm), [ss] "s"(c.ss), [gvo] "s"(c.gvo)
               : AT_CLOB, ACLOB_LO, ACLOB_HI);
  k.nb = nb; c.fl = g.fvo;
#pragma unroll
  for (int i = 0; i < 4; ++i) c.q[i] = g.q[i];
  GAP();
}
constexpr unsigned SCR = 9 * UNIT;
#define STAMPF(I) do { } while (0)
#define FQ_STR2(x) #x
#define FQ_STR(x) FQ_STR2(x)
#define FQ_CAP FQ_STR(SPINCAP)
DI bool tags2(const u32x4& a, unsigned tag) { return (a[1] == tag) & (a[3] == tag); }
DI void bcast4(float v, float (&o)[4]) {
  auto a = __builtin_amdgcn_permlane32_swap(__float_as_uint(v), __float_as_uint(v), false, false);
  auto b = __builtin_amdgcn_permlane16_swap(a[0], a[0], false, false);
  auto c = __builtin_amdgcn_permlane16_swap(a[1], a[1], false, false);
  o[0] = __uint_as_float(b[0]); o[1] = __uint_as_float(b[1]); o[2] = __uint_as_float(c[0]); o[3] = __uint_as_float(c[1]);
}
DI void x_raw2(unsigned m0, srd_t srd, unsigned v0, unsigned v1, unsigned so) {
  asm volatile("s_mov_b32 m0, %0\n\ts_nop 0\n\tbuffer_load_dwordx4 %1, %3, %4 offen lds\n\tbuffer_load_dwordx4 %2, %3, %4 offen offset:1024 lds"
               :: "s"(m0), "v"(v0), "v"(v1), "s"(srd), "s"(so) : "memory");
}
DI void w_touch2(H4& g, const u16* wq, unsigned base, unsigned t) {
  touch(g.pfd, wq, base + ((g.rot + t) & 15u) * kWT);
}
constexpr unsigned TB0 = 6, TBN = 6;
#define SE 0
#define F1 0
#define FT 0
#define MK 0
#define PGA 0
#define WT 0
#define LT 0
#define EL 0
#define PT0 0
#define XH 0
#define S3 0
#define X1L 0
#define PBK 1
#define IL 1
#define PPOS 2
DI void front(Ctx& k, H4& g, W4C& c STAMP_ARG) {
  argp_t ap = args();
  const u16 *px = ap->x, *kc = ap->kc, *vc = ap->vc, *wqp = ap->wq, *pc_ = ap->c, *pm = ap->w_mod; char* pw = ap->ws;
  GAP();
  const unsigned lane = lane_id(), wid = k.wid, tid = wid * 64 + lane;
  g.pfd = 0;
  {
    unsigned long long pc; unsigned len;
    asm volatile("s_getpc_b64 %0\n.Lfz_a:\n\ts_mov_b32 %1, .Lfz_z-.Lfz_a-4" : "=s"(pc), "=s"(len));
    touch(g.pfd, (const void*)pc, min(((lane * 4 + wid) * 32 + k.j) * 128, len));
  }
  GAP();
  k.ws = mk_srd((char*)(((uintptr_t)pw + 0x1fffff) & ~(uintptr_t)0x1fffff), W_END);
  const srd_t srdx = mk_srd(px, S * DM * 2), srdm = mk_srd(pm, DM * 6 * DM * 2), srdc = mk_srd(pc_, DM * 2);
  asm volatile("" : "+s"(kc), "+s"(vc));
  const unsigned head = k.kvh * 8 + k.hl, row64 = k.rblk * 256 + k.kvh * 64, rot = k.kvh * 4, u0 = k.hl >> 1, kt = rot + ((u0 + wid) & 3), k0 = k.hl & 1, cq = k.kvh ^ 3;
  g.srdW = mk_srd(wqp + head * DH, (DM * QD - head * DH) * 2); g.rot = rot;
  u32x32 xa;
  const unsigned st1 = W_ST1 + (k.rblk << 12);
  u32x4 xs[4], wm[4];
  const unsigned cbk = (k.xcd & 1) * 32 + k.j, kp = k.kvh;
  const unsigned s3 = S3 && (k.j & 14) == 6, od = s3 & k.j, rg = tid >> (2 + s3), cm = 3 + 4 * s3;
  u32x2 cv = ald8<0>(srdc, rg * 8, kp * 512 + od * 256);
  { const unsigned vo = rg * (4 * 6 * DM * 2) + (tid & cm) * 16, so = kp * (256 * 6 * DM * 2) + cbk * 64 + od * (128 * 6 * DM * 2 - 64);
#pragma unroll
    for (int i = 0; i < 4; ++i) wm[i] = ald16<0>(srdm, vo, so + i * (6 * DM * 2)); }
  unsigned E = ald4<0, true>(k.ws, EL == 0 || lane == 0 ? W_EPOCH : lane == 1 ? W_HT + ((k.rblk & 3) << 20) : lane == 2 ? st1 + (k.kvh * 8 + k.hl) * 128 : W_FLAGH + (k.rblk * 4 + wid) * 128, 0u);
  { const unsigned so = (row64 + k.hl * 8 + 2 * wid) * (DM * 2);
    xs[0] = ald16<0>(srdx, lane * 16, so); xs[1] = ald16<1024>(srdx, lane * 16, so); xs[2] = ald16<2048>(srdx, lane * 16, so); xs[3] = ald16<3072>(srdx, lane * 16, so); }
  unsigned vx = (lane >> 3) * (DM * 2) + (((lane & 7) ^ (lane >> 4)) << 4), vx1 = (vx ^ 64) + 15360, sl[4];
  g.mX = k.ldsb + wid * 8192;
#pragma unroll
  for (int u = 0; u < 4; ++u) sl[u] = (k.rblk * 256 + wid * 16) * (DM * 2) + (rot + ((u0 + u) & 3)) * 128;
#define XRAW(u, rb) x_raw2(g.mX + (u) * GSLOT + (rb) * 2048, srdx, vx, vx1, sl[u] + (((rb) ^ cq) << 17))
  const unsigned hx = XH ? 2 * k0 : 0;
  if (!(LT & 2)) { XRAW(0, hx); XRAW(0, hx + 1); }
  const unsigned tw2 = (k.j * 2 + (wid >> 1)) * (QD * 2) + k.kvh * 2048 + (lane & 7) * 256 + (wid & 1) * 128;
  if (!(LT & 1)) w_touch2(g, wqp, tw2, (lane >> 3) & 3);
  constexpr int NL = (LT & 1) + 2 * (LT & 2);
  GAP();
  g.mW = k.ldsb + 32768 + wid * 4096;
#define X1() do { XRAW(1, 0); XRAW(1, 1); XRAW(1, 2); XRAW(1, 3); } while (0)
#define X01() do { XRAW(0, 2 - hx); XRAW(0, 3 - hx); if (!X1L) X1(); } while (0)
#define X2() do { XRAW(2, 0); XRAW(2, 1); XRAW(2, 2); XRAW(2, 3); } while (0)
  const unsigned tb = k.ldsb + SCR + 1024;
  u32x16 wbs; u32x8 vw, ma; u32x4 mb; const u16* kvt; unsigned pso, fw, w2, w3, gso, gpo;
  auto setup_w = [&]() {
    w_voffs(g.voffW, QD, wid, lane, k0);
    gso = st1 + (wid * 16 + (lane & 15)) * 16; gpo = W_MODA + ((lane >> 5) * 1024 + kt * 64 + (lane & 31) * 2) * 8;
    asm volatile("" : W4(g.voffW), "+v"(gso), "+v"(gpo));
    GAP();
  };
  auto setup_b = [&]() {
    g.voffX = wid * 8192 + ((lane >> 4) ^ cq) * 256 + (lane & 15) * 16; g.soX = W_HT + ((k.rblk & 3) << 20) + (((k.xcd ^ wid) & 1) << 13) + wid * 8192; g.fo = W_FLAGH + (k.rblk * 4 + wid) * 128;
    h4_init_addr(g, k.ldsb, wid, lane, k0);
    const unsigned xr = g.mX + (lane & 15) * 128 + (((4 * k0 + (lane >> 4)) ^ ((lane >> 1) & 7)) << 4);
    vw = u32x8{g.voffW[0], g.voffW[1], g.voffW[2], g.voffW[3], xr, xr ^ 64, xr + 2 * GSLOT, (xr ^ 64) + 2 * GSLOT};
#pragma unroll
    for (int i = 0; i < 8; ++i) { wbs[i] = g.wb[0][i]; wbs[8 + i] = g.wb[1][i]; }
    ma = u32x8{tb + (lane >> 4) * 32, g.voffX, (lane & 31) * 4, 0u, vx, vx1, g.xb1,
               WT == 1 ? tw2 + ((rot + 4 + (k.xcd & 1) + 2 * min(lane >> 3, 5u)) & 15u) * kWT : WT == 2 ? tw2 + ((rot + 4 + (k.xcd & 1) + 2 * min(lane >> 3, 2u)) & 15u) * kWT
                       : tw2 - (lane & 7) * 256 + (1 + 4 * (lane & 1)) * 256 + ((rot + 4 + min(lane >> 1, 11u)) & 15u) * kWT};
    mb = u32x4{WT == 2 ? tw2 + ((rot + 10 + (k.xcd & 1) + 2 * min(lane >> 3, 2u)) & 15u) * kWT : (tw2 & ~(((lane & 3) == 1) ? 256u : 0u)) + ((rot + TB0 + min(lane >> 3, TBN - 1u)) & 15u) * kWT, ((k.j & 15) * 64 + wid * 16 + (lane & 15)) * 128, 0u, W_SIL + k.bid * 512 + tid * 2};
    kvt = ((k.j & 16) ? vc : kc) + ((size_t)k.kvh * SKV + (SKV - WIN)) * DH;
    pso = g.soX + (rot + u0) * kXT + cq * 256 - GSLOT - k.ldsb; fw = g.fo + head * 4; w2 = (rot + ((u0 + 2) & 3)) * kWT; w3 = (rot + ((u0 + 3) & 3)) * kWT;
    asm volatile("" : "+{v[184:199]}"(wbs), "+{v[200:207]}"(vw), "+{v[208:215]}"(ma), "+{v[216:219]}"(mb), "+s"(kvt), "+s"(pso), "+s"(fw), "+s"(w2), "+s"(w3));
    GAP();
  };
  if constexpr (SE & 2) { setup_w(); setup_b(); }
  unsigned tag;
  HOP_GO(2, wid);
  unsigned la = k.ldsb + SCR + wid * 256 + (4 * (lane & 7) + 2 * (lane >> 5) + ((lane >> 4) & 1)) * 8 + (lane & 8) * 512, lr = k.ldsb + SCR + lane * 8;
  unsigned pa_ = (W_MODA + od * (W_MODC - W_MODA - 256) + (kp * 2048 + cbk * 32 + lane * 2) * 8) | (lane < (16u << s3) ? 0u : 0x80000000u);
  unsigned ps_ = (st1 + (k.kvh * 64 + k.hl * 8 + 2 * wid + (lane >> 5)) * 16) | ((lane & 31) == 0 ? 0u : 0x80000000u);
  asm volatile("" : "+v"(la), "+v"(lr), "+v"(pa_), "+v"(ps_));
  GAP();
  asm volatile("s_waitcnt vmcnt(%1)" : "+v"(cv) : "i"(14 + PT0 - NL) : "memory");
  unsigned sc[2];
#pragma unroll
  for (int i = 0; i < 2; ++i) { const float a = lo16f(cv[i]), b = hi16f(cv[i]); sc[i] = cvtpk(a * __builtin_amdgcn_rcpf(1.f + __expf(-a)), b * __builtin_amdgcn_rcpf(1.f + __expf(-b))); }
  asm volatile("" : "+v"(sc[0]), "+v"(sc[1]));
  GAP();
  STAMPF(27);
  asm volatile("s_waitcnt vmcnt(%4)" : W4(wm) : "i"(10 + PT0 - NL) : "memory");
  STAMP(1);
  if (LT & 2) { XRAW(0, 0); XRAW(0, 1); }
  if (LT & 1) w_touch2(g, wqp, tw2, (lane >> 3) & 3);
  GAP();
  {
    float a[8];
#pragma unroll
    for (int d = 0; d < 4; ++d) {
      a[2 * d] = dot2(plo(wm[2][d], wm[3][d]), sc[1], dot2z(plo(wm[0][d], wm[1][d]), sc[0]));
      a[2 * d + 1] = dot2(phi(wm[2][d], wm[3][d]), sc[1], dot2z(phi(wm[0][d], wm[1][d]), sc[0]));
    }
    float c0 = fold16(fold32(a[0], a[4]), fold32(a[2], a[6])), c1 = fold16(fold32(a[1], a[5]), fold32(a[3], a[7]));
    c0 += dpp_f<0x128>(c0); c1 += dpp_f<0x128>(c1);
    if (S3) { const float kf = s3 ? 0.f : 1.f; c0 = fmaf(dpp_f<0x124>(c0), kf, c0); c1 = fmaf(dpp_f<0x124>(c1), kf, c1); }
    else    { c0 += dpp_f<0x124>(c0); c1 += dpp_f<0x124>(c1); }
    lds_st(la, f32x2{c0, c1});
    asm volatile("s_waitcnt vmcnt(%1)" : "+v"(E) : "i"(9 + PT0) : "memory");
    tag = ((rfl(E) | TAGM) + TAGC) & ~TAGM;
    k.tag = tag;
    unsigned tv = tag;
    asm volatile("s_waitcnt lgkmcnt(0)\n\ts_barrier" : "+v"(tv) :: "memory");
    if (wid == 0) {
      const f32x2 s = (lds_ld<f32x2>(lr) + lds_ld<f32x2>(lr + 256)) + (lds_ld<f32x2>(lr + 512) + lds_ld<f32x2>(lr + 768));
      st16(k.ws, pa_, u32x4{__float_as_uint(s[0]), tv, __float_as_uint(s[1]), tv});
    }
  }
  GAP();
  asm volatile("s_waitcnt vmcnt(%5)\n\ts_cmp_eq_u32 %4, 0\n\ts_cbranch_scc1 .Lxs%=\n\ts_waitcnt vmcnt(%6)\n.Lxs%=:" : W4(xs) : "s"(wid), "i"(6 + PT0), "i"(5 + PT0) : "memory", "scc");
  STAMP(2);
  {
    float sm[2], sq[2];
#pragma unroll
    for (int r = 0; r < 2; ++r) { sm[r] = dot2z(xs[2 * r][0], 0x3f803f80u); sq[r] = dot2z(xs[2 * r][0], xs[2 * r][0]); }
#pragma unroll
    for (int e = 1; e < 8; ++e)
#pragma unroll
      for (int r = 0; r < 2; ++r) { const unsigned w = xs[2 * r + (e >> 2)][e & 3]; sm[r] = dot2(w, 0x3f803f80u, sm[r]); sq[r] = dot2(w, w, sq[r]); }
    const float z = sum_row16(fold16(fold32(sm[0], sm[1]), fold32(sq[0], sq[1]))) * (1.f / DM);
    auto zz = __builtin_amdgcn_permlane16_swap(__float_as_uint(z), __float_as_uint(z), false, false);
    const float mean = __uint_as_float(zz[0]);
    const float rstd = __builtin_amdgcn_rsqf(fmaxf(fmaf(-mean, mean, __uint_as_float(zz[1])), 0.f) + 1e-5f);
    st16(k.ws, ps_, u32x4{__float_as_uint(rstd), tag, __float_as_uint(-mean * rstd), tag});
  }
  GAP();
  STAMP(3);
  Ptrs pa;
  { argp_t aq = args(); pa = Ptrs{aq->wo, aq->w_r, aq->wg, aq->wu, aq->wd, aq->out}; }
  if (IL) { XRAW(0, 2 - hx); XRAW(0, 3 - hx); if (IL & 2) XRAW(1, 0); GAP(); } else X01();
  unsigned cb;
  asm volatile("buffer_load_ushort %0, %1, %2, %3 offen" : "=v"(cb) : "v"(tid * 2), "s"(srdc), "s"((k.j >> 3) * 512) : "memory");
  if constexpr (!(SE & 2)) setup_w();
  if (IL) { if (!(IL & 2)) XRAW(1, 0); XRAW(1, 1); if (IL & 2) XRAW(1, 2); } else w_dma4(g.mW, g.srdW, g.voffW, (rot + u0) * kWT);
#define W1() w_dma4(g.mW + GSLOT, g.srdW, g.voffW, (rot + ((u0 + 1) & 3)) * kWT)
#define W2() w_dma4(g.mW + 2 * GSLOT, g.srdW, g.voffW, (rot + ((u0 + 2) & 3)) * kWT)
  if constexpr (FQ_W1L == 0) W1();
  if constexpr (FQ_X2E == 2) X2();
  if constexpr (FQ_W2E == 2) W2();
  u32x4 st[4], p[4], p2[4];
  const bool k3 = S3 && (kt & 7) == 3;
  const unsigned g2 = gpo | (k3 ? 0u : 0x80000000u);
#define POLL() do { \
    asm volatile("buffer_load_dwordx4 %0, %4, %5, %6 offen sc1\n\tbuffer_load_dwordx4 %1, %4, %5, %7 offen sc1\n\tbuffer_load_dwordx4 %2, %4, %5, %8 offen sc1\n\tbuffer_load_dwordx4 %3, %4, %5, %9 offen sc1" \
                 : "=&{v[160:163]}"(st[0]), "=&{v[164:167]}"(st[1]), "=&{v[168:171]}"(st[2]), "=&{v[172:175]}"(st[3]) : "v"(gso), "s"(k.ws), "s"(cq << 10), "s"((cq ^ 1) << 10), "s"((cq ^ 2) << 10), "s"((cq ^ 3) << 10) : "memory"); \
    asm volatile("buffer_load_dwordx4 %0, %4, %5, 0 offen sc1\n\tbuffer_load_dwordx4 %1, %4, %5, %6 offen sc1\n\tbuffer_load_dwordx4 %2, %4, %5, %7 offen sc1\n\tbuffer_load_dwordx4 %3, %4, %5, %8 offen sc1" \
                 : "=&{v[128:131]}"(p[0]), "=&{v[132:135]}"(p[1]), "=&{v[136:139]}"(p[2]), "=&{v[140:143]}"(p[3]) : "v"(gpo), "s"(k.ws), "s"(16384u), "s"(32768u), "s"(49152u) : "memory"); \
    if (S3) asm volatile("buffer_load_dwordx4 %0, %4, %5, %6 offen sc1\n\tbuffer_load_dwordx4 %1, %4, %5, %7 offen sc1\n\tbuffer_load_dwordx4 %2, %4, %5, %8 offen sc1\n\tbuffer_load_dwordx4 %3, %4, %5, %9 offen sc1" \
                 : "=&{v[144:147]}"(p2[0]), "=&{v[148:151]}"(p2[1]), "=&{v[152:155]}"(p2[2]), "=&{v[156:159]}"(p2[3]) : "v"(g2), "s"(k.ws), "s"(W_MODC - W_MODA), "s"(W_MODC - W_MODA + 16384u), "s"(W_MODC - W_MODA + 32768u), "s"(W_MODC - W_MODA + 49152u) : "memory"); \
    GAP(); } while (0)
#define POLL_WAIT(N) asm volatile("s_waitcnt vmcnt(%12)" : "+{v[160:163]}"(st[0]), "+{v[164:167]}"(st[1]), "+{v[168:171]}"(st[2]), "+{v[172:175]}"(st[3]), "+{v[128:131]}"(p[0]), "+{v[132:135]}"(p[1]), "+{v[136:139]}"(p[2]), \
                                  "+{v[140:143]}"(p[3]), "+{v[144:147]}"(p2[0]), "+{v[148:151]}"(p2[1]), "+{v[152:155]}"(p2[2]), "+{v[156:159]}"(p2[3]) : "i"(N) : "memory")
  GAP();
  if (PBK) { HOP_FRONT(); }
  if constexpr (PPOS == 0) POLL();
  if constexpr (!(SE & 2)) setup_b();
  if constexpr (PPOS == 2 && !IL) POLL();
  if (X1L) X1();
  if (IL) { if (!(IL & 2)) XRAW(1, 2); XRAW(1, 3); if (IL & 2) w_dma4(g.mW, g.srdW, g.voffW, (rot + u0) * kWT); GAP(); }
  attn_setup(k, c, kc, vc, pm, srdx, pa);
  GAP();
  if (IL && !(IL & 2)) { w_dma4(g.mW, g.srdW, g.voffW, (rot + u0) * kWT); GAP(); }
  if (IL & 4) POLL();
  ma[3] = tag;
  static_assert(W_XCH == W_OT + (16u << 20));
  const unsigned vp = k.xcd * 2 + ((k.j >> 1) & 1);
  unsigned o_rot = rot << 19, o_sox = g.soX - GSLOT - k.ldsb, o_dk = 4096u - k0 * 8192u, o_c17 = cq << 17, o_w1 = (rot + ((u0 + 1) & 3)) * kWT, o_idm = ffn_idle(k) ? 0x80000000u : 0u;
  unsigned o_prb = ((k.j & 1) ? W_OT + (vp << 21) : W_PROBE + ((vp & 1) << 13) + vp * 128) | (ffn_idle(k) && wid == 0 ? 0u : 0x80000000u), o_pto = (blockIdx.x * 4 + wid) * 2048;
  unsigned ta = tb + wid * 512 + (((lane ^ 32) * 8) ^ (k0 * 128)); float one = lane >= 32 ? 1.f : 0.f;
  asm volatile("" : "+{v[208:215]}"(ma), "+s"(o_rot), "+s"(o_sox), "+s"(o_dk), "+s"(o_c17), "+s"(o_w1), "+s"(o_idm), "+s"(o_prb), "+s"(o_pto), "+s"(sl[2]), "+s"(sl[3]), "+s"(c.pf1), "+s"(c.pf4), "+s"(c.wo.base),
                    "+s"(k.pm), "+v"(ta), "+v"(one));
  GAP();
  if (IL && !(IL & 4)) POLL();
  if constexpr (FQ_W1L == 2) W1();
  if constexpr (FQ_X2E == 1) X2();
  if constexpr (FQ_W2E == 1) W2();
  GAP();
  STAMPF(5);
  HOP_GO(0, wid);
  if (!PBK) { HOP_FRONT(); }
  STAMPF(6);
  {
    POLL_WAIT((X1L ? 8 : 0) + (FQ_X2E == 1 ? 8 : 0) + (FQ_W1L == 2 ? 4 : 0) + (FQ_W2E == 1 ? 4 : 0));
    asm volatile("ds_read_b128 v[0:3], %3\n\tds_read_b128 v[4:7], %3 offset:2048\n\tds_read_b128 v[8:11], %3 offset:4096\n\tds_read_b128 v[12:15], %3 offset:6144\n\t"
                 "ds_read_b128 v[16:19], %4\n\tds_read_b128 v[20:23], %4 offset:2048\n\tds_read_b128 v[24:27], %4 offset:4096\n\tds_read_b128 v[28:31], %4 offset:6144"
                 : "=&{v[0:31]}"(xa), "+v"(g.pfd), "+v"(cb) : "v"(vw[4]), "v"(vw[5]) : "memory");
    STAMPF(7);
    static_assert(W_MODC - W_MODA == 0x60000);
#define FR_C2
#define FR_L3
    unsigned bad, sp = 0;
    asm volatile(".Lfr_c:\n\t" "v_xor_b32 %[b], %[t], v161\n\t" "v_bitop3_b32 %[b], %[b], v163, %[t] bitop3:0xf6\n\t" "v_bitop3_b32 %[b], %[b], v165, %[t] bitop3:0xf6\n\t" "v_bitop3_b32 %[b], %[b], v167, %[t] bitop3:0xf6\n\t" "v_bitop3_b32 %[b], %[b], v169, %[t] bitop3:0xf6\n\t" "v_bitop3_b32 %[b], %[b], v171, %[t] bitop3:0xf6\n\t" "v_bitop3_b32 %[b], %[b], v173, %[t] bitop3:0xf6\n\t" "v_bitop3_b32 %[b], %[b], v175, %[t] bitop3:0xf6\n\t" "v_bitop3_b32 %[b], %[b], v129, %[t] bitop3:0xf6\n\t" "v_bitop3_b32 %[b], %[b], v131, %[t] bitop3:0xf6\n\t" "v_bitop3_b32 %[b], %[b], v133, %[t] bitop3:0xf6\n\t" "v_bitop3_b32 %[b], %[b], v135, %[t] bitop3:0xf6\n\t" "v_bitop3_b32 %[b], %[b], v137, %[t] bitop3:0xf6\n\t" "v_bitop3_b32 %[b], %[b], v139, %[t] bitop3:0xf6\n\t" "v_bitop3_b32 %[b], %[b], v141, %[t] bitop3:0xf6\n\t" "v_bitop3_b32 %[b], %[b], v143, %[t] bitop3:0xf6\n\t" FR_C2 "v_cmp_ne_u32 vcc, 0, %[b]\n\ts_cbranch_vccnz .Lfr_s\n.Lfr_k:\n\t.subsection 1\n.Lfr_s:\n\ts_sleep 1\n\t" "buffer_load_dwordx4 v[160:163], %[gs], %[ws], %[q0] offen sc1\n\t" "buffer_load_dwordx4 v[164:167], %[gs], %[ws], %[q1] offen sc1\n\t" "buffer_load_dwordx4 v[168:171], %[gs], %[ws], %[q2] offen sc1\n\t" "buffer_load_dwordx4 v[172:175], %[gs], %[ws], %[q3] offen sc1\n\t" "s_mov_b32 vcc_lo, 0\n\t" "buffer_load_dwordx4 v[128:131], %[gp], %[ws], vcc_lo offen sc1\n\t" "s_mov_b32 vcc_lo, 16384\n\t" "buffer_load_dwordx4 v[132:135], %[gp], %[ws], vcc_lo offen sc1\n\t" "s_mov_b32 vcc_lo, 32768\n\t" "buffer_load_dwordx4 v[136:139], %[gp], %[ws], vcc_lo offen sc1\n\t" "s_mov_b32 vcc_lo, 49152\n\t" "buffer_load_dwordx4 v[140:143], %[gp], %[ws], vcc_lo offen sc1\n\t" FR_L3
                 "s_add_u32 %[sp], %[sp], 1\n\ts_cmp_lt_u32 %[sp], %[cap]\n\ts_waitcnt vmcnt(0)\n\ts_cbranch_scc1 .Lfr_c\n\tbuffer_store_dword v211, off, %[ws], 0 offset:256 sc1\n\ts_branch .Lfr_k\n\t.subsection 0"
                 : "+{v[160:163]}"(st[0]), "+{v[164:167]}"(st[1]), "+{v[168:171]}"(st[2]), "+{v[172:175]}"(st[3]), "+{v[128:131]}"(p[0]), "+{v[132:135]}"(p[1]), "+{v[136:139]}"(p[2]), "+{v[140:143]}"(p[3]),
                   "+{v[144:147]}"(p2[0]), "+{v[148:151]}"(p2[1]), "+{v[152:155]}"(p2[2]), "+{v[156:159]}"(p2[3]), [b] "=&v"(bad), [sp] "+s"(sp)
                 : [t] "s"(tag), [t2] "s"(k3 ? tag : 0u), [gs] "v"(gso), [gp] "v"(gpo), [g2] "v"(g2), [ws] "s"(k.ws), [q0] "s"(cq << 10), [q1] "s"((cq ^ 1) << 10), [q2] "s"((cq ^ 2) << 10), [q3] "s"((cq ^ 3) << 10),
                   [cap] "i"(kSpinCap), "{v[208:215]}"(ma) : "vcc", "scc", "memory");
    STAMP(4);
    f32x2 m[4];
#pragma unroll
    for (int i = 0; i < 4; ++i) { m[i] = f32x2{__uint_as_float(p[i][0]), __uint_as_float(p[i][2])}; if (S3) m[i] += f32x2{__uint_as_float(p2[i][0]), __uint_as_float(p2[i][2])}; }
    f32x2 s = (m[0] + m[1]) + (m[2] + m[3]);
    s = up2(cvtpk(s[0], s[1])) + one;
    lds_st(ta, up2(cvtpk(s[0], s[1])) * QSCALE);
    GAP();
    mb[2] = __float_as_uint(silu_bf(lo16f(cb))) >> 16;
    asm volatile("" : "+{v[216:219]}"(mb));
    STAMPF(12);
    BARRIER_LGKM();
    STAMPF(13);
  }
  g.q[0] = xa;
  asm volatile(FQ_ASM : "+{v[0:31]}"(g.q[0]), "=&{v[32:63]}"(g.q[1]), "=&{v[64:95]}"(g.q[2]), "=&{v[96:127]}"(g.q[3]), [fl] "=&v"(g.fl), [pf] "+{v246}"(g.pfd), "+{v[200:207]}"(vw), "+{v[208:215]}"(ma), "+{v[216:219]}"(mb),
                 "=&{v220}"(c.kv.vkb[0]), "=&{v221}"(c.kv.vkb[1]), "=&{v222}"(c.kv.vkb[2]), "=&{v223}"(c.kv.vkb[3]), "=&{v224}"(c.kv.vksrc[0]), "=&{v225}"(c.kv.vksrc[1]), "=&{v226}"(c.kv.vksrc[2]), "=&{v227}"(c.kv.vksrc[3]),
                 "=&{v228}"(c.kv.vvb[0]), "=&{v229}"(c.kv.vvb[1]), "=&{v230}"(c.kv.vvsrc[0]), "=&{v231}"(c.kv.vvsrc[1]), "=&{v232}"(c.gv), "=&{v240}"(c.wo.wv[0]), "=&{v241}"(c.wo.wv[1]), "=&{v244}"(c.wa0),
                 "=&{v245}"(g.fvo), "=&{v247}"(c.tlb), "=&{v233}"(c.gvm), "=&{v248}"(c.c3),
                 [hl] "=&s"(c.hl), [hl18] "=&s"(c.hl18), [cto] "=&s"(c.cto), [pf2] "=&s"(c.pf2), [po] "=&s"(c.po), [px] "=&s"(c.px), [vo] "=&s"(c.vo), [eso] "=&s"(c.eso), [otb] "=&s"(c.otb),
                 [xcb] "=&s"(c.xcb), [frb] "=&s"(c.frb), [fw2] "=&s"(c.fw2), [xso] "=&s"(c.xso), [ss] "=&s"(c.ss), [mbp] "=&s"(c.mbp), [gvo] "=&s"(c.gvo) FQ_STV
               : "{v[160:163]}"(st[0]), "{v[164:167]}"(st[1]), "{v[168:171]}"(st[2]), "{v[172:175]}"(st[3]), "{v[184:199]}"(wbs),
                 [ws] "s"(k.ws), [sw] "s"(g.srdW), [sx] "s"(srdx), [sK] "s"(c.kv.srdK), [sV] "s"(c.kv.srdV), FQ_SD [mw] "s"(g.mW), [mx] "s"(g.mX),
                 [rot19] "s"(o_rot), [w2] "s"(w2), [w3] "s"(w3), [sox] "s"(o_sox), [dk] "s"(o_dk), [fo] "s"(g.fo), [fw] "s"(fw), [pso] "s"(pso), [c17] "s"(o_c17), [sl2] "s"(sl[2]), [w1] "s"(o_w1),
                 [sl3] "s"(sl[3]), [wq] "s"(wqp), [kv] "s"(kvt), [sofk] "s"(c.wo.m), [pf4] "s"(c.pf4), [pf1] "s"(c.pf1), [wo] "s"(c.wo.srd),
                 [bid] "s"(k.bid), [wid] "s"(wid), [idm] "s"(o_idm), [prb] "s"(o_prb), [pto] "s"(o_pto)
               : FQ_CL, ACLOB_LO, ACLOB_HI);
  GAP();
}
DI void oproj(Ctx& k, W4C& c STAMP_ARG) {
  const unsigned wid = k.wid, tag = k.tag; unsigned gad, ln;
  u32x32 UP = c.up, DN = c.q[1]; f32x32 A3 = __builtin_bit_cast(f32x32, c.q[3]), XT = __builtin_bit_cast(f32x32, c.q[0]); float cww, bbw;
  asm volatile(OP_ASM
               : "+{v[176:207]}"(UP), "+{v[160:175]}"(c.o3), "+{v[32:63]}"(DN), "+{v[96:127]}"(A3), "+{v[0:31]}"(XT), "+{v[64:95]}"(c.q[2]), "+{v[128:159]}"(c.wm), "+{v224}"(c.kv.vksrc[0]), "+{v225}"(c.kv.vksrc[1]), "+{v226}"(c.kv.vksrc[2]), "+{v227}"(c.kv.vksrc[3]), "=&{s87}"(cww), "=&{s88}"(bbw),
                 "=&{v237}"(gad), "=&{v244}"(ln), "+{v213}"(c.gra), "+{v240}"(c.wo.wv[0]), "+{v241}"(c.wo.wv[1]), "+{v247}"(c.tlb), "+{v252}"(c.vs), [pm] "+s"(k.pm)
               : "{v234}"(c.wo.wa), "{v242}"(c.l16), "{v243}"(c.tagv), "{v245}"(c.fl), "{v254}"(c.fbv),
                 [ws] "s"(k.ws), [wo] "s"(c.wo.srd), [sd] "s"(c.sD), [sx] "s"(c.sX), [sr] "s"(c.sR),
                 [tag] "s"(tag), [wid] "s"(wid), [hl] "s"(c.hl), [eso] "s"(c.eso), [na] "s"(c.na), [frb] "s"(c.frb), [xcb] "s"(c.xcb), [xso] "s"(c.xso),
                 [m] "s"(c.wo.m), [wob] "s"(c.wo.base), [ss] "s"(c.ss), [mbp] "s"(c.mbp), [cap] "i"(kSpinCap), [fw2] "s"(c.fw2)
               : OP_CLOB, ACLOB_LO, ACLOB_HI);
  if (__builtin_expect(k.pm == 2, 0)) {
    asm volatile("s_getpc_b64 vcc\n.Lrs_a:\n\ts_sub_u32 vcc_lo, vcc_lo, .Lrs_a-.Lfz_a\n\ts_subb_u32 vcc_hi, vcc_hi, 0\n\ts_and_b32 vcc_lo, vcc_lo, 0xfffff000\n\ts_setpc_b64 vcc"
                 :: "{s[0:1]}"(__builtin_amdgcn_kernarg_segment_ptr()), "{s2}"(blockIdx.x), "{v0}"((wid * 64 + lane_id()) | 0x10000u) : "vcc", "scc", "memory");
    __builtin_unreachable();
  }
  asm volatile(TL_ASM
               : "+{v[176:207]}"(UP), "+{v[32:63]}"(DN), "+{v[96:127]}"(A3), "+{v[0:31]}"(XT), "+{v[64:95]}"(c.q[2]), "+{v[128:159]}"(c.wm), "+{v[160:175]}"(c.o3), "+{s87}"(cww), "+{s88}"(bbw), "+{v240}"(c.wo.wv[0])
               : "{v237}"(gad), "{v241}"(c.wo.wv[1]), "{v242}"(c.l16), "{v243}"(c.tagv), "{v244}"(ln),
                 [ws] "s"(k.ws), [tag] "s"(tag), [wid] "s"(wid), [bid] "s"(k.bid), [nb] "s"(k.nb), [out] "s"(c.out), [cap] "i"(kSpinCap)
               : TL_CLOB);
  STAMPO(26);
}
#define PERM 0
__global__ __launch_bounds__(256, 1) __attribute__((aligned(4096))) void k_fused(Args a) {
  Ctx k;
  const unsigned bid = PERM ? ((blockIdx.x << 3) | (blockIdx.x >> 5)) & 255u : blockIdx.x;
  unsigned t0;
  asm volatile("v_readfirstlane_b32 %0, %1" : "=s"(t0) : "v"(threadIdx.x));
  const unsigned wid = (t0 >> 6) & 3;
  k.pm = 1 - (t0 >> 16);
  k.wid = wid; k.bid = bid; k.xcd = ((bid & 3) << 1) | ((bid >> 2) & 1); k.j = bid >> 3; k.kvh = k.xcd >> 1; k.hl = k.j & 7; k.rblk = (k.xcd & 1) * 4 + (k.j >> 3);
  k.cs = (k.xcd & 1) << 13; k.nb = 0;
  STAMP_DECL;
  STAMP(0);
#define SPINTO(i) do { } while (0)
  W4C c;
  SPINTO(0);
  {
    H4 g;
    front(k, g, c STAMP_PASS);
    STAMP(8);
    SPINTO(1);
    attention(k, g, c STAMP_PASS);
  }
  SPINTO(2);
  oproj(k, c STAMP_PASS);
  SPINTO(3);
  STAMP_FLUSH();
  asm volatile(HOP_DEFS);
}

extern "C" void kernel_launch(void* const* d_in, const int* in_sizes, int n_in, void* d_out, int out_size, void* d_ws, size_t ws_size,
                              hipStream_t stream) {
  Args a;
  a.x = (const u16*)d_in[0]; a.kc = (const u16*)d_in[1]; a.vc = (const u16*)d_in[2]; a.wq = (const u16*)d_in[3]; a.wo = (const u16*)d_in[4];
  a.w_r = (const u16*)d_in[6]; a.wg = (const u16*)d_in[7]; a.wu = (const u16*)d_in[8]; a.wd = (const u16*)d_in[9]; a.c = (const u16*)d_in[10];
  a.w_mod = (const u16*)d_in[11]; a.out = (u16*)d_out; a.ws = (char*)d_ws;
  (void)hipFuncSetAttribute((const void*)k_fused, hipFuncAttributeMaxDynamicSharedMemorySize, 163840);
  k_fused<<<256, 256, 163840, stream>>>(a);
}
```

```cpp
#include <hip/hip_runtime.h>
#include <hip/hip_bf16.h>
#include <stdint.h>
#include <utility>

typedef unsigned short u16;
using bf16x8 = __attribute__((ext_vector_type(8))) short;
using s16x4  = __attribute__((ext_vector_type(4))) short;
using f32x16 = __attribute__((ext_vector_type(16))) float;
using u32x16 = __attribute__((ext_vector_type(16))) unsigned;
using f32x4  = __attribute__((ext_vector_type(4))) float;
using f32x2  = __attribute__((ext_vector_type(2))) float;
using u32x4  = __attribute__((ext_vector_type(4))) unsigned;
using u32x2  = __attribute__((ext_vector_type(2))) unsigned;
using u32x8 = __attribute__((ext_vector_type(8))) unsigned;
using f32x8 = __attribute__((ext_vector_type(8))) float;
using f32x32 = __attribute__((ext_vector_type(32))) float;
using u32x32 = __attribute__((ext_vector_type(32))) unsigned;
typedef __bf16 bf16x2_t __attribute__((ext_vector_type(2)));
#define DI __device__ __forceinline__
#define NTMASK 0
#define NT_SEL(bit) ((NTMASK >> (bit)) & 1)
#if NT_SEL(0)
#define NTX " nt"
#else
#define NTX ""
#endif
#if NT_SEL(1)
#define NTM " nt"
#else
#define NTM ""
#endif
#if NT_SEL(2)
#define NTQ " nt"
#else
#define NTQ ""
#endif
#if NT_SEL(3)
#define NTO " nt"
#else
#define NTO ""
#endif
#if NT_SEL(4)
#define NTG " nt"
#else
#define NTG ""
#endif
#if NT_SEL(5)
#define NTD " nt"
#else
#define NTD ""
#endif
#if NT_SEL(6)
#define NTT " nt"
#else
#define NTT ""
#endif
#if NT_SEL(7)
#define NTF " nt"
#else
#define NTF ""
#endif

constexpr int S = 2048, DM = 1024, HQ = 32, HKV = 4, DH = 128, SKV = 2048, WIN = 512, FF = 3584;
constexpr int QD = HQ * DH;
constexpr float QSCALE = 0.12751743082f;

DI float bf2f(u16 v) { return __uint_as_float(((unsigned)v) << 16); }
DI u16 f2bf(float x) { unsigned u = __float_as_uint(x); u += 0x7fffu + ((u >> 16) & 1u); return (u16)(u >> 16); }
DI unsigned cvtpk(float lo, float hi) { f32x2 v = {lo, hi}; bf16x2_t b = __builtin_convertvector(v, bf16x2_t); return __builtin_bit_cast(unsigned, b); }
DI float lo16f(unsigned w) { return __uint_as_float(w << 16); }
DI float hi16f(unsigned w) { return __uint_as_float(w & 0xffff0000u); }
DI float rbf(float x) { return lo16f(cvtpk(x, x)); }
DI f32x2 up2(unsigned w) { return f32x2{lo16f(w), hi16f(w)}; }
DI float dot2(unsigned a, unsigned b, float c) { return __builtin_amdgcn_fdot2_f32_bf16(__builtin_bit_cast(bf16x2_t, a), __builtin_bit_cast(bf16x2_t, b), c, false); }
DI float dot2z(unsigned a, unsigned b) { return __builtin_amdgcn_fdot2_f32_bf16(__builtin_bit_cast(bf16x2_t, a), __builtin_bit_cast(bf16x2_t, b), 0.f, false); }
DI unsigned plo(unsigned a, unsigned b) { return __builtin_amdgcn_perm(b, a, 0x05040100u); }
DI unsigned phi(unsigned a, unsigned b) { return __builtin_amdgcn_perm(b, a, 0x07060302u); }
DI unsigned rfl(unsigned x) { return __builtin_amdgcn_readfirstlane(x); }
template <int CTRL> DI float dpp_f(float v) { return __int_as_float(__builtin_amdgcn_update_dpp(0, __float_as_int(v), CTRL, 0xf, 0xf, true)); }
template <int CTRL> DI unsigned dpp_u(unsigned v) { return (unsigned)__builtin_amdgcn_update_dpp(0, (int)v, CTRL, 0xf, 0xf, true); }
DI float fold16(float a, float b) { auto r = __builtin_amdgcn_permlane16_swap(__float_as_uint(a), __float_as_uint(b), false, false); return __uint_as_float(r[0]) + __uint_as_float(r[1]); }
DI float fold32(float a, float b) { auto r = __builtin_amdgcn_permlane32_swap(__float_as_uint(a), __float_as_uint(b), false, false); return __uint_as_float(r[0]) + __uint_as_float(r[1]); }
DI float sum16(float v) { return fold16(v, v); }
DI float sum32(float v) { return fold32(v, v); }
DI float sum_row16(float v) { v += dpp_f<0xB1>(v); v += dpp_f<0x4E>(v); v += dpp_f<0x141>(v); v += dpp_f<0x140>(v); return v; }
DI float wave_sum(float v) { return sum32(sum16(sum_row16(v))); }
DI unsigned wave_maxu(unsigned v) {
  v = max(v, dpp_u<0xB1>(v)); v = max(v, dpp_u<0x4E>(v)); v = max(v, dpp_u<0x141>(v)); v = max(v, dpp_u<0x140>(v));
  { auto r = __builtin_amdgcn_permlane16_swap(v, v, false, false); v = max(r[0], r[1]); }
  { auto r = __builtin_amdgcn_permlane32_swap(v, v, false, false); v = max(r[0], r[1]); }
  return v;
}
#define GAP() __builtin_amdgcn_sched_barrier(0)
template <int N> using iseq = std::make_integer_sequence<int, N>;
template <int... Is> using ilist = std::integer_sequence<int, Is...>;
typedef __amdgpu_buffer_rsrc_t srd_t;
#define SRD3 0x00020000
DI srd_t mk_srd(const void* p, unsigned bytes) { return __builtin_amdgcn_make_buffer_rsrc((void*)p, (short)0, (int)bytes, SRD3); }
DI unsigned lane_id() { unsigned l; asm volatile("v_mbcnt_lo_u32_b32 %0, -1, 0\n\tv_mbcnt_hi_u32_b32 %0, -1, %0" : "=v"(l)); return l; }
struct Args { const u16 *x, *kc, *vc, *wq, *wo, *w_r, *wg, *wu, *wd, *c, *w_mod; u16* out; char* ws; };
typedef const __attribute__((address_space(4))) Args* argp_t;
DI argp_t args() { argp_t p = (argp_t)__builtin_amdgcn_kernarg_segment_ptr(); asm volatile("" : "+s"(p)); return p; }
#define BARRIER_LGKM() do { asm volatile("s_waitcnt lgkmcnt(0)\n\ts_barrier" ::: "memory"); } while (0)

constexpr unsigned W_EPOCH = 0x260000, W_TMO = 256;
constexpr unsigned W_FLAGH = 0x261000;
constexpr unsigned W_VOTE = 0x2000, W_PROBE = 0x4000;
constexpr unsigned W_GKEY = 0x10000;
constexpr unsigned W_FLAGR = 0x8000;
constexpr unsigned W_FLAGO = 0x20000;
constexpr unsigned W_CANDH = 0x40000;
constexpr unsigned W_STAT = 0x80000;
constexpr unsigned W_YG = 0x100000;
constexpr unsigned W_CANDX = 0x1E0000;
constexpr unsigned W_ST1 = 0x200000;
constexpr unsigned W_MODA = 0x210000;
constexpr unsigned W_MODC = 0x270000;
constexpr unsigned W_MODB = 0x220000;
constexpr unsigned W_SIL = 0x240000;
constexpr unsigned W_HT = 4u << 20;
constexpr unsigned W_OT = 8u << 20;
constexpr unsigned W_XCH = 24u << 20;
constexpr unsigned W_END = 40u << 20;
constexpr unsigned TAGM = 0x40004000u, TAGC = 0x9E3739B9u;
#define SPINCAP 262144
constexpr unsigned kSpinCap = SPINCAP;
#define SL0 1
#define SL1 1
#define SL2 1
#define SL3 1
template <int SL = 1> DI bool spin_fail(unsigned& spins, srd_t ws, unsigned tag) {
  __builtin_amdgcn_s_sleep(SL);
  if (++spins > kSpinCap) { __builtin_amdgcn_raw_buffer_store_b32(tag, ws, W_TMO, 0, 16); return true; }
  return (spins & 255u) == 0u && rfl(__builtin_amdgcn_raw_buffer_load_b32(ws, W_TMO, 0, 16)) == tag;
}
DI u32x4 ld16(srd_t ws, unsigned off) { return __builtin_amdgcn_raw_buffer_load_b128(ws, off, 0, 16); }
DI u32x2 ld8(srd_t ws, unsigned off) { return __builtin_amdgcn_raw_buffer_load_b64(ws, off, 0, 16); }
DI void st16(srd_t ws, unsigned off, u32x4 v) { __builtin_amdgcn_raw_buffer_store_b128(v, ws, off, 0, 16); }
DI void st8(srd_t ws, unsigned off, u32x2 v) { __builtin_amdgcn_raw_buffer_store_b64(v, ws, off, 0, 16); }
DI void st4(srd_t ws, unsigned off, unsigned v) { __builtin_amdgcn_raw_buffer_store_b32(v, ws, off, 0, 16); }
DI void wait_flags(srd_t ws, unsigned off, unsigned tag) {
  const unsigned o = off + (lane_id() & 31u) * 4u;
  for (unsigned spins = 0;;) {
    const unsigned f = __builtin_amdgcn_raw_buffer_load_b32(ws, o, 0, 16);
    if (__all(f == tag)) break;
    if (spin_fail(spins, ws, tag)) break;
  }
  asm volatile("" ::: "memory");
}
template <int OFF, bool DEV = false> DI u32x4 ald16(srd_t s, unsigned vo, unsigned so) {
  u32x4 r;
  if constexpr (DEV) asm volatile("buffer_load_dwordx4 %0, %1, %2, %3 offen offset:%4 sc1" : "=v"(r) : "v"(vo), "s"(s), "s"(so), "i"(OFF) : "memory");
  else               asm volatile("buffer_load_dwordx4 %0, %1, %2, %3 offen offset:%4" NTF : "=v"(r) : "v"(vo), "s"(s), "s"(so), "i"(OFF) : "memory");
  return r;
}
template <int OFF> DI u32x2 ald8(srd_t s, unsigned vo, unsigned so) {
  u32x2 r; asm volatile("buffer_load_dwordx2 %0, %1, %2, %3 offen offset:%4" : "=v"(r) : "v"(vo), "s"(s), "s"(so), "i"(OFF) : "memory"); return r;
}
DI u32x2 ald8d(srd_t s, unsigned vo) { u32x2 r; asm volatile("buffer_load_dwordx2 %0, %1, %2, 0 offen sc1" : "=v"(r) : "v"(vo), "s"(s) : "memory"); return r; }
template <int OFF, bool DEV = false> DI unsigned ald4(srd_t s, unsigned vo, unsigned so) {
  unsigned r;
  if constexpr (DEV) asm volatile("buffer_load_dword %0, %1, %2, %3 offen offset:%4 sc1" : "=v"(r) : "v"(vo), "s"(s), "s"(so), "i"(OFF) : "memory");
  else               asm volatile("buffer_load_dword %0, %1, %2, %3 offen offset:%4" : "=v"(r) : "v"(vo), "s"(s), "s"(so), "i"(OFF) : "memory");
  return r;
}
DI void touch(unsigned& d, const void* base, unsigned vo) { asm volatile("global_load_dword %0, %1, %2" : "+v"(d) : "v"(vo), "s"(base) : "memory"); }
DI void touchq(unsigned& d, const void* base, unsigned vo) { asm volatile("global_load_dword %0, %1, %2" NTQ : "+v"(d) : "v"(vo), "s"(base) : "memory"); }
template <class T> DI T lds_ld(unsigned a) { return *reinterpret_cast<const __attribute__((address_space(3))) T*>(a); }
template <class T> DI void lds_st(unsigned a, T v) { *reinterpret_cast<__attribute__((address_space(3))) T*>(a) = v; }
DI float silu_bf(float v) { return rbf(v * __builtin_amdgcn_rcpf(1.f + __expf(-v))); }
struct Ctx { static constexpr unsigned ldsb = 0; unsigned pm, bid, wid, xcd, j, kvh, hl, rblk, tag, cs, nb; srd_t ws; };
#define TAIL2 1
DI unsigned ffn_j(const Ctx& k) { return TAIL2 ? (k.j < 24 ? k.j : 24 + (k.j & 3)) : min(k.j, 27u); }
DI bool ffn_idle(const Ctx& k) { return TAIL2 ? (k.j >> 2) == 6 + (k.xcd & 1) : k.j >= 28; }
constexpr unsigned UNIT = 16384;
#define STAMPA(I) do { } while (0)
#define STAMPO(I) do { } while (0)
#define STAMP_DECL
#define STAMP(I) do { } while (0)
#define STAMP_FLUSH() do { } while (0)
#define STAMP_ARG
#define STAMP_PASS
#define ACLOB_LO "a0","a1","a2","a3","a4","a5","a6","a7","a8","a9","a10","a11","a12","a13","a14","a15","a16","a17","a18","a19","a20","a21","a22","a23","a24","a25","a26","a27","a28","a29","a30","a31","a32","a33","a34","a35","a36","a37","a38","a39","a40","a41","a42","a43","a44","a45","a46","a47","a48","a49","a50","a51","a52","a53","a54","a55","a56","a57","a58","a59","a60","a61","a62","a63","a64","a65","a66","a67","a68","a69","a70","a71","a72","a73","a74","a75","a76","a77","a78","a79","a80","a81","a82","a83","a84","a85","a86","a87","a88","a89","a90","a91","a92","a93","a94","a95","a96","a97","a98","a99","a100","a101","a102","a103","a104","a105","a106","a107","a108","a109","a110","a111","a112","a113","a114","a115","a116","a117","a118","a119","a120","a121","a122","a123","a124","a125","a126","a127"
#define ACLOB_HI "a128","a129","a130","a131","a132","a133","a134","a135","a136","a137","a138","a139","a140","a141","a142","a143","a144","a145","a146","a147","a148","a149","a150","a151","a152","a153","a154","a155","a156","a157","a158","a159","a160","a161","a162","a163","a164","a165","a166","a167","a168","a169","a170","a171","a172","a173","a174","a175","a176","a177","a178","a179","a180","a181","a182","a183","a184","a185","a186","a187","a188","a189","a190","a191","a192","a193","a194","a195","a196","a197","a198","a199","a200","a201","a202","a203","a204","a205","a206","a207","a208","a209","a210","a211","a212","a213","a214","a215","a216","a217","a218","a219","a220","a221","a222","a223","a224","a225","a226","a227","a228","a229","a230","a231","a232","a233","a234","a235","a236","a237","a238","a239","a240","a241","a242","a243","a244","a245","a246","a247","a248","a249","a250","a251","a252","a253","a254","a255"
#define HOP_DEFS ".set .Lhc00, .Lhs0\n\t.set .Lhs0n, .Lhs4\n\t.set .Lhs4n, .Lhs8\n\t.set .Lhs8n, .Lhr0\n\t.set .Lhc01, .Lhs1\n\t.set .Lhs1n, .Lhs5\n\t.set .Lhs5n, .Lhs9\n\t.set .Lhs9n, .Lhr0\n\t.set .Lhc02, .Lhs2\n\t.set .Lhs2n, .Lhs6\n\t.set .Lhs6n, .Lhs10\n\t.set .Lhs10n, .Lhr0\n\t.set .Lhc03, .Lhs3\n\t.set .Lhs3n, .Lhs7\n\t.set .Lhs7n, .Lhr0\n\t.set .Lhc10, .Lhs11\n\t.set .Lhs11n, .Lhs15\n\t.set .Lhs15n, .Lhr1\n\t.set .Lhc11, .Lhs12\n\t.set .Lhs12n, .Lht0\n\t.set .Lht0n, .Lhr1\n\t.set .Lhc12, .Lhs13\n\t.set .Lhs13n, .Lht1\n\t.set .Lht1n, .Lhr1\n\t.set .Lhc13, .Lhs14\n\t.set .Lhs14n, .Lhr1\n\t.set .Lhc20, .Lhf\n\t.set .Lhfn, .Lhr2\n\t.set .Lhc21, .Lhr2\n\t.set .Lhc22, .Lhr2\n\t.set .Lhc23, .Lhr2"
#define HOP_PAGE(n) asm volatile("s_branch .Lht" #n "\n\t.p2align 12\n.Lht" #n ":\n\ts_cbranch_execz .Lht" #n "n")
#define PAGE_ALIGN() asm volatile("s_branch .Lpa%=\n\t.p2align 12\n.Lpa%=:" ::: "memory")
#define HOP_FRONT() asm volatile("s_branch .Lhf\n\t.p2align 12\n.Lhf:\n\ts_cbranch_execz .Lhfn")
#define HOP_GO(g, w) asm volatile("s_mov_b64 exec, 0\n\ts_cmp_eq_u32 %0, 0\n\ts_cbranch_scc1 .Lhc" #g "0\n\ts_cmp_eq_u32 %0, 1\n\ts_cbranch_scc1 .Lhc" #g "1\n\ts_cmp_eq_u32 %0, 2\n\ts_cbranch_scc1 .Lhc" #g "2\n\ts_branch .Lhc" #g "3\n.Lhr" #g ":\n\ts_mov_b64 exec, -1" :: "s"(w) : "scc", "memory")
#define STP(x)
#define OP_ASM \
  "s_waitcnt lgkmcnt(0)\n\t" \
  "s_barrier\n\t" \
  STP("s_memrealtime s[94:95]\n\t") \
  STP("s_memtime s[92:93]\n\t") \
  "ds_read_b32 v228, v213 offset:49152\n\t" \
  "v_mfma_f32_16x16x32_bf16 a[8:11], a[140:143], v[76:79], 0\n\t" \
  "v_add_u32 v238, 14336, v240\n\t" \
  "v_add_u32 v239, 21504, v241\n\t" \
  "v_mfma_f32_16x16x32_bf16 a[8:11], a[136:139], v[72:75], a[8:11]\n\t" \
  "ds_read_b128 a[12:15], v242 offset:32768\n\t" \
  "v_add_u32 v241, 7168, v241\n\t" \
  "v_mfma_f32_16x16x32_bf16 a[8:11], a[132:135], v[68:71], a[8:11]\n\t" \
  "s_mov_b32 s79, 0x05040100\n\t" \
  "ds_read_b128 a[28:31], v242 offset:33792\n\t" \
  "s_mov_b32 s80, 0x07060302\n\t" \
  "v_mfma_f32_16x16x32_bf16 a[8:11], a[128:131], v[64:67], a[8:11]\n\t" \
  "ds_read_b128 a[44:47], v242 offset:34816\n\t" \
  "v_mfma_f32_16x16x32_bf16 a[24:27], a[144:147], v[64:67], 0\n\t" \
  "ds_read_b128 a[60:63], v242 offset:35840\n\t" \
  "v_mfma_f32_16x16x32_bf16 a[24:27], a[148:151], v[68:71], a[24:27]\n\t" \
  "v_mfma_f32_16x16x32_bf16 a[24:27], a[152:155], v[72:75], a[24:27]\n\t" \
  "s_waitcnt vmcnt(14)\n\t" \
  "buffer_store_dword v243, v254, %[ws], %[fw2] offen offset:32 sc1\n\t" \
  "v_mfma_f32_16x16x32_bf16 a[24:27], a[156:159], v[76:79], a[24:27]\n\t" \
  "ds_read_b128 a[76:79], v242 offset:36864\n\t" \
  "v_mfma_f32_16x16x32_bf16 a[104:107], a[172:175], v[76:79], 0\n\t" \
  "ds_read_b128 a[92:95], v242 offset:37888\n\t" \
  "v_mfma_f32_16x16x32_bf16 a[104:107], a[168:171], v[72:75], a[104:107]\n\t" \
  "ds_read_b128 a[108:111], v242 offset:38912\n\t" \
  "s_waitcnt lgkmcnt(7)\n\t" \
  "v_cmp_eq_u32 vcc, %[tag], v228\n\t" \
  "s_and_b32 vcc_lo, vcc_lo, %[pm]\n\t" \
  "s_cbranch_scc1 .Lop_rd\n\t" \
  "v_mfma_f32_16x16x32_bf16 a[104:107], a[164:167], v[68:71], a[104:107]\n\t" \
  "ds_read_b128 a[124:127], v242 offset:39936\n\t" \
  "v_mfma_f32_16x16x32_bf16 a[104:107], a[160:163], v[64:67], a[104:107]\n\t" \
  "ds_read_b128 a[208:211], v242 offset:40960\n\t" \
  "v_mfma_f32_16x16x32_bf16 a[120:123], a[176:179], v[64:67], 0\n\t" \
  "buffer_load_dword v246, v245, %[ws], %[fw2] offen sc1\n\t" \
  "v_mfma_f32_16x16x32_bf16 a[120:123], a[180:183], v[68:71], a[120:123]\n\t" \
  "ds_read_b128 a[212:215], v242 offset:41984\n\t" \
  "v_mfma_f32_16x16x32_bf16 a[120:123], a[184:187], v[72:75], a[120:123]\n\t" \
  "ds_read_b128 a[216:219], v242 offset:43008\n\t" \
  "v_mfma_f32_16x16x32_bf16 a[120:123], a[188:191], v[76:79], a[120:123]\n\t" \
  "s_xor_b32 s73, %[eso], 131072\n\t" \
  "ds_read_b128 a[220:223], v242 offset:44032\n\t" \
  "v_mfma_f32_16x16x32_bf16 a[88:91], v[108:111], v[76:79], 0\n\t" \
  "s_add_u32 m0, %[m], 0\n\t" \
  "ds_read_b128 a[192:195], v242 offset:45056\n\t" \
  "v_mfma_f32_16x16x32_bf16 a[88:91], v[104:107], v[72:75], a[88:91]\n\t" \
  "ds_read_b128 a[196:199], v242 offset:46080\n\t" \
  "v_mfma_f32_16x16x32_bf16 a[88:91], v[100:103], v[68:71], a[88:91]\n\t" \
  "ds_read_b128 a[200:203], v242 offset:47104\n\t" \
  "ds_read_b128 a[204:207], v242 offset:48128\n\t" \
  "v_mfma_f32_16x16x32_bf16 a[88:91], v[96:99], v[64:67], a[88:91]\n\t" \
  "s_waitcnt lgkmcnt(12)\n\t" \
  "s_mov_b64 exec, 0\n\t" \
  "s_cmp_eq_u32 %[wid], 0\n\t" \
  "s_cbranch_scc1 .Lhc10\n\t" \
  "s_cmp_eq_u32 %[wid], 1\n\t" \
  "s_cbranch_scc1 .Lhc11\n\t" \
  "s_cmp_eq_u32 %[wid], 2\n\t" \
  "s_cbranch_scc1 .Lhc12\n\t" \
  "s_branch .Lhc13\n\t" \
  ".Lhr1:\n\t" \
  "s_mov_b64 exec, -1\n\t" \
  "s_waitcnt vmcnt(6)\n\t" \
  "s_waitcnt vmcnt(0)\n\t" \
  "v_cmp_ne_u32 vcc, %[tag], v246\n\t" \
  "s_and_b32 vcc_lo, vcc_lo, 0x6\n\t" \
  "s_cbranch_scc1 .Lop_s0\n\t" \
  ".Lop_k0:\n\t" \
  "buffer_load_dwordx4 v[16:19], v242, %[ws], s73 offen offset:0 sc1\n\t" \
  "v_perm_b32 v248, v132, v128, s79\n\t" \
  "v_perm_b32 v128, v132, v128, s80\n\t" \
  "v_dot2_f32_bf16 v132, v248, v224, 0\n\t" \
  "v_dot2_f32_bf16 v128, v128, v224, 0\n\t" \
  "v_perm_b32 v249, v133, v129, s79\n\t" \
  "v_perm_b32 v129, v133, v129, s80\n\t" \
  "v_dot2_f32_bf16 v133, v249, v224, 0\n\t" \
  "v_dot2_f32_bf16 v129, v129, v224, 0\n\t" \
  "buffer_load_dwordx4 v[20:23], v242, %[ws], s73 offen offset:1024 sc1\n\t" \
  "v_perm_b32 v248, v134, v130, s79\n\t" \
  "v_perm_b32 v130, v134, v130, s80\n\t" \
  "v_dot2_f32_bf16 v134, v248, v224, 0\n\t" \
  "v_dot2_f32_bf16 v130, v130, v224, 0\n\t" \
  "v_perm_b32 v249, v135, v131, s79\n\t" \
  "v_perm_b32 v131, v135, v131, s80\n\t" \
  "v_dot2_f32_bf16 v135, v249, v224, 0\n\t" \
  "v_dot2_f32_bf16 v131, v131, v224, 0\n\t" \
  "buffer_load_dwordx4 v[24:27], v242, %[ws], s73 offen offset:2048 sc1\n\t" \
  "v_perm_b32 v248, v140, v136, s79\n\t" \
  "v_perm_b32 v136, v140, v136, s80\n\t" \
  "v_dot2_f32_bf16 v132, v248, v225, v132\n\t" \
  "v_dot2_f32_bf16 v128, v136, v225, v128\n\t" \
  "v_perm_b32 v249, v141, v137, s79\n\t" \
  "v_perm_b32 v137, v141, v137, s80\n\t" \
  "v_dot2_f32_bf16 v133, v249, v225, v133\n\t" \
  "v_dot2_f32_bf16 v129, v137, v225, v129\n\t" \
  "buffer_load_dwordx4 v[28:31], v242, %[ws], s73 offen offset:3072 sc1\n\t" \
  "v_perm_b32 v248, v142, v138, s79\n\t" \
  "v_perm_b32 v138, v142, v138, s80\n\t" \
  "v_dot2_f32_bf16 v134, v248, v225, v134\n\t" \
  "v_dot2_f32_bf16 v130, v138, v225, v130\n\t" \
  "v_perm_b32 v249, v143, v139, s79\n\t" \
  "v_perm_b32 v139, v143, v139, s80\n\t" \
  "v_dot2_f32_bf16 v135, v249, v225, v135\n\t" \
  "v_dot2_f32_bf16 v131, v139, v225, v131\n\t" \
  "s_xor_b32 s73, %[eso], 163840\n\t" \
  "buffer_load_dwordx4 v[0:3], v242, %[ws], s73 offen offset:0 sc1\n\t" \
  "v_perm_b32 v248, v148, v144, s79\n\t" \
  "v_perm_b32 v144, v148, v144, s80\n\t" \
  "v_dot2_f32_bf16 v132, v248, v226, v132\n\t" \
  "v_dot2_f32_bf16 v128, v144, v226, v128\n\t" \
  "v_perm_b32 v249, v149, v145, s79\n\t" \
  "v_perm_b32 v145, v149, v145, s80\n\t" \
  "v_dot2_f32_bf16 v133, v249, v226, v133\n\t" \
  "v_dot2_f32_bf16 v129, v145, v226, v129\n\t" \
  "buffer_load_dwordx4 v[4:7], v242, %[ws], s73 offen offset:1024 sc1\n\t" \
  "v_perm_b32 v248, v150, v146, s79\n\t" \
  "v_perm_b32 v146, v150, v146, s80\n\t" \
  "v_dot2_f32_bf16 v134, v248, v226, v134\n\t" \
  "v_dot2_f32_bf16 v130, v146, v226, v130\n\t" \
  "v_perm_b32 v249, v151, v147, s79\n\t" \
  "v_perm_b32 v147, v151, v147, s80\n\t" \
  "v_dot2_f32_bf16 v135, v249, v226, v135\n\t" \
  "v_dot2_f32_bf16 v131, v147, v226, v131\n\t" \
  "buffer_load_dwordx4 v[8:11], v242, %[ws], s73 offen offset:2048 sc1\n\t" \
  "v_perm_b32 v248, v156, v152, s79\n\t" \
  "v_perm_b32 v152, v156, v152, s80\n\t" \
  "v_dot2_f32_bf16 v132, v248, v227, v132\n\t" \
  "v_dot2_f32_bf16 v128, v152, v227, v128\n\t" \
  "v_perm_b32 v249, v157, v153, s79\n\t" \
  "v_perm_b32 v153, v157, v153, s80\n\t" \
  "v_dot2_f32_bf16 v133, v249, v227, v133\n\t" \
  "v_dot2_f32_bf16 v129, v153, v227, v129\n\t" \
  "buffer_load_dwordx4 v[12:15], v242, %[ws], s73 offen offset:3072 sc1\n\t" \
  "v_perm_b32 v248, v158, v154, s79\n\t" \
  "v_perm_b32 v154, v158, v154, s80\n\t" \
  "v_dot2_f32_bf16 v134, v248, v227, v134\n\t" \
  "v_dot2_f32_bf16 v130, v154, v227, v130\n\t" \
  "v_perm_b32 v249, v159, v155, s79\n\t" \
  "v_perm_b32 v155, v159, v155, s80\n\t" \
  "v_dot2_f32_bf16 v135, v249, v227, v135\n\t" \
  "v_dot2_f32_bf16 v131, v155, v227, v131\n\t" \
  "v_mfma_f32_16x16x32_bf16 v[96:99], a[128:131], a[12:15], 0\n\t" \
  "s_xor_b32 s73, %[eso], 262144\n\t" \
  "s_xor_b32 s71, %[wob], 1048576\n\t" \
  "v_mfma_f32_16x16x32_bf16 v[96:99], a[132:135], a[28:31], v[96:99]\n\t" \
  "v_mfma_f32_16x16x32_bf16 v[96:99], a[136:139], a[44:47], v[96:99]\n\t" \
  "v_mfma_f32_16x16x32_bf16 v[96:99], a[140:143], a[60:63], v[96:99]\n\t" \
  "v_mfma_f32_16x16x32_bf16 v[112:115], a[156:159], a[60:63], 0\n\t" \
  "buffer_load_dwordx4 v[48:51], v242, %[ws], s73 offen offset:0 sc1\n\t" \
  "v_mfma_f32_16x16x32_bf16 v[112:115], a[152:155], a[44:47], v[112:115]\n\t" \
  "v_mfma_f32_16x16x32_bf16 v[112:115], a[148:151], a[28:31], v[112:115]\n\t" \
  "v_mfma_f32_16x16x32_bf16 v[112:115], a[144:147], a[12:15], v[112:115]\n\t" \
  "s_waitcnt lgkmcnt(8)\n\t" \
  "v_mfma_f32_16x16x32_bf16 v[100:103], a[128:131], a[76:79], 0\n\t" \
  "buffer_load_dwordx4 v[52:55], v242, %[ws], s73 offen offset:1024 sc1\n\t" \
  "v_mfma_f32_16x16x32_bf16 v[100:103], a[132:135], a[92:95], v[100:103]\n\t" \
  "v_mfma_f32_16x16x32_bf16 v[100:103], a[136:139], a[108:111], v[100:103]\n\t" \
  "v_mfma_f32_16x16x32_bf16 v[100:103], a[140:143], a[124:127], v[100:103]\n\t" \
  "v_mfma_f32_16x16x32_bf16 v[116:119], a[156:159], a[124:127], 0\n\t" \
  "v_mfma_f32_16x16x32_bf16 v[116:119], a[152:155], a[108:111], v[116:119]\n\t" \
  "buffer_load_dwordx4 v[56:59], v242, %[ws], s73 offen offset:2048 sc1\n\t" \
  "v_mfma_f32_16x16x32_bf16 v[116:119], a[148:151], a[92:95], v[116:119]\n\t" \
  "v_mfma_f32_16x16x32_bf16 v[116:119], a[144:147], a[76:79], v[116:119]\n\t" \
  "s_waitcnt lgkmcnt(4)\n\t" \
  "v_mfma_f32_16x16x32_bf16 v[104:107], a[128:131], a[208:211], 0\n\t" \
  "v_xor_b32 v236, 0xc000, v234\n\t" \
  "v_mfma_f32_16x16x32_bf16 v[104:107], a[132:135], a[212:215], v[104:107]\n\t" \
  "buffer_load_dwordx4 v[60:63], v242, %[ws], s73 offen offset:3072 sc1\n\t" \
  "s_xor_b32 s73, %[eso], 294912\n\t" \
  "v_mfma_f32_16x16x32_bf16 v[104:107], a[136:139], a[216:219], v[104:107]\n\t" \
  "ds_read_b64_tr_b16 a[160:161], v236 offset:0\n\t" \
  "v_mfma_f32_16x16x32_bf16 v[104:107], a[140:143], a[220:223], v[104:107]\n\t" \
  "ds_read_b64_tr_b16 a[162:163], v236 offset:4096\n\t" \
  "v_mfma_f32_16x16x32_bf16 v[120:123], a[156:159], a[220:223], 0\n\t" \
  "ds_read_b64_tr_b16 a[164:165], v236 offset:8192\n\t" \
  "v_mfma_f32_16x16x32_bf16 v[120:123], a[152:155], a[216:219], v[120:123]\n\t" \
  "ds_read_b64_tr_b16 a[166:167], v236 offset:12288\n\t" \
  "v_mfma_f32_16x16x32_bf16 v[120:123], a[148:151], a[212:215], v[120:123]\n\t" \
  "buffer_load_dwordx4 v[80:83], v242, %[ws], s73 offen offset:0 sc1\n\t" \
  "v_mfma_f32_16x16x32_bf16 v[120:123], a[144:147], a[208:211], v[120:123]\n\t" \
  "s_waitcnt lgkmcnt(4)\n\t" \
  "ds_read_b64_tr_b16 a[168:169], v236 offset:16384\n\t" \
  "v_mfma_f32_16x16x32_bf16 v[108:111], a[128:131], a[192:195], 0\n\t" \
  "ds_read_b64_tr_b16 a[170:171], v236 offset:20480\n\t" \
  "v_mfma_f32_16x16x32_bf16 v[108:111], a[132:135], a[196:199], v[108:111]\n\t" \
  "ds_read_b64_tr_b16 a[172:173], v236 offset:24576\n\t" \
  "v_mfma_f32_16x16x32_bf16 v[108:111], a[136:139], a[200:203], v[108:111]\n\t" \
  "buffer_load_dwordx4 v[84:87], v242, %[ws], s73 offen offset:1024 sc1\n\t" \
  "v_mfma_f32_16x16x32_bf16 v[108:111], a[140:143], a[204:207], v[108:111]\n\t" \
  "ds_read_b64_tr_b16 a[174:175], v236 offset:28672\n\t" \
  "v_xor_b32 v237, 0xc020, v234\n\t" \
  "v_mfma_f32_16x16x32_bf16 v[124:127], a[156:159], a[204:207], 0\n\t" \
  "ds_read_b64_tr_b16 a[176:177], v237 offset:0\n\t" \
  "v_mfma_f32_16x16x32_bf16 v[124:127], a[152:155], a[200:203], v[124:127]\n\t" \
  "ds_read_b64_tr_b16 a[178:179], v237 offset:4096\n\t" \
  "v_mfma_f32_16x16x32_bf16 v[124:127], a[148:151], a[196:199], v[124:127]\n\t" \
  "buffer_load_dwordx4 v[88:91], v242, %[ws], s73 offen offset:2048 sc1\n\t" \
  "v_mfma_f32_16x16x32_bf16 v[124:127], a[144:147], a[192:195], v[124:127]\n\t" \
  "s_waitcnt vmcnt(11) lgkmcnt(6)\n\t" \
  "ds_read_b64_tr_b16 a[180:181], v237 offset:8192\n\t" \
  "ds_read_b64_tr_b16 a[182:183], v237 offset:12288\n\t" \
  "buffer_load_dword v246, v245, %[ws], %[fw2] offen sc1\n\t" \
  STP("v_writelane_b32 %[stv], s94, 24\n\t") \
  STP("v_writelane_b32 %[stv], s92, 25\n\t") \
  STP("s_memrealtime s[94:95]\n\t") \
  STP("s_memtime s[92:93]\n\t") \
  "v_mfma_f32_16x16x32_bf16 a[0:3], a[160:163], v[16:19], a[0:3]\n\t" \
  "ds_read_b64_tr_b16 a[184:185], v237 offset:16384\n\t" \
  "v_mfma_f32_16x16x32_bf16 a[0:3], a[164:167], v[20:23], a[0:3]\n\t" \
  "s_waitcnt lgkmcnt(5)\n\t" \
  "ds_read_b64_tr_b16 a[186:187], v237 offset:20480\n\t" \
  "v_mfma_f32_16x16x32_bf16 a[0:3], a[168:171], v[24:27], a[0:3]\n\t" \
  "ds_read_b64_tr_b16 a[188:189], v237 offset:24576\n\t" \
  "v_mfma_f32_16x16x32_bf16 a[0:3], a[172:175], v[28:31], a[0:3]\n\t" \
  "s_waitcnt vmcnt(8)\n\t" \
  "ds_read_b64_tr_b16 a[190:191], v237 offset:28672\n\t" \
  "v_xor_b32 v235, 0xc040, v234\n\t" \
  "v_mfma_f32_16x16x32_bf16 a[4:7], a[172:175], v[12:15], a[4:7]\n\t" \
  "buffer_load_dwordx4 v[92:95], v242, %[ws], s73 offen offset:3072 sc1\n\t" \
  "v_mfma_f32_16x16x32_bf16 a[4:7], a[168:171], v[8:11], a[4:7]\n\t" \
  "ds_read_b64_tr_b16 a[128:129], v235 offset:0\n\t" \
  "ds_read_b64_tr_b16 a[130:131], v235 offset:4096\n\t" \
  "v_mfma_f32_16x16x32_bf16 a[4:7], a[164:167], v[4:7], a[4:7]\n\t" \
  "ds_read_b64_tr_b16 a[132:133], v235 offset:8192\n\t" \
  "v_mfma_f32_16x16x32_bf16 a[4:7], a[160:163], v[0:3], a[4:7]\n\t" \
  "s_waitcnt lgkmcnt(7)\n\t" \
  "ds_read_b64_tr_b16 a[134:135], v235 offset:12288\n\t" \
  "v_mfma_f32_16x16x32_bf16 a[16:19], a[176:179], v[16:19], a[16:19]\n\t" \
  "buffer_load_dwordx4 v240, %[wo], s71 offen offset:0 lds\n\t" \
  "v_mfma_f32_16x16x32_bf16 a[16:19], a[180:183], v[20:23], a[16:19]\n\t" \
  "s_waitcnt lgkmcnt(4)\n\t" \
  "ds_read_b64_tr_b16 a[136:137], v235 offset:16384\n\t" \
  "ds_read_b64_tr_b16 a[138:139], v235 offset:20480\n\t" \
  "v_mfma_f32_16x16x32_bf16 a[16:19], a[184:187], v[24:27], a[16:19]\n\t" \
  "ds_read_b64_tr_b16 a[140:141], v235 offset:24576\n\t" \
  "v_mfma_f32_16x16x32_bf16 a[16:19], a[188:191], v[28:31], a[16:19]\n\t" \
  "ds_read_b64_tr_b16 a[142:143], v235 offset:28672\n\t" \
  "v_xor_b32 v236, 0xc060, v234\n\t" \
  "v_mfma_f32_16x16x32_bf16 a[20:23], a[188:191], v[12:15], a[20:23]\n\t" \
  "ds_read_b64_tr_b16 a[160:161], v236 offset:0\n\t" \
  "v_mfma_f32_16x16x32_bf16 a[20:23], a[184:187], v[8:11], a[20:23]\n\t" \
  "buffer_load_dwordx4 v241, %[wo], s71 offen offset:1024 lds\n\t" \
  "v_mfma_f32_16x16x32_bf16 a[20:23], a[180:183], v[4:7], a[20:23]\n\t" \
  "ds_read_b64_tr_b16 a[162:163], v236 offset:4096\n\t" \
  "ds_read_b64_tr_b16 a[164:165], v236 offset:8192\n\t" \
  "v_mfma_f32_16x16x32_bf16 a[20:23], a[176:179], v[0:3], a[20:23]\n\t" \
  "s_waitcnt lgkmcnt(7)\n\t" \
  "ds_read_b64_tr_b16 a[166:167], v236 offset:12288\n\t" \
  "v_mfma_f32_16x16x32_bf16 a[32:35], a[128:131], v[16:19], a[32:35]\n\t" \
  "s_xor_b32 s73, %[eso], 327680\n\t" \
  "ds_read_b64_tr_b16 a[168:169], v236 offset:16384\n\t" \
  "v_mfma_f32_16x16x32_bf16 a[32:35], a[132:135], v[20:23], a[32:35]\n\t" \
  "s_waitcnt lgkmcnt(5)\n\t" \
  "buffer_load_dwordx4 v238, %[wo], s71 offen offset:2048 lds\n\t" \
  "v_mfma_f32_16x16x32_bf16 a[32:35], a[136:139], v[24:27], a[32:35]\n\t" \
  "ds_read_b64_tr_b16 a[170:171], v236 offset:20480\n\t" \
  "ds_read_b64_tr_b16 a[172:173], v236 offset:24576\n\t" \
  "v_mfma_f32_16x16x32_bf16 a[32:35], a[140:143], v[28:31], a[32:35]\n\t" \
  "ds_read_b64_tr_b16 a[174:175], v236 offset:28672\n\t" \
  "v_xor_b32 v237, 0xc080, v234\n\t" \
  "v_mfma_f32_16x16x32_bf16 a[36:39], a[140:143], v[12:15], a[36:39]\n\t" \
  "ds_read_b64_tr_b16 a[176:177], v237 offset:0\n\t" \
  "v_mfma_f32_16x16x32_bf16 a[36:39], a[136:139], v[8:11], a[36:39]\n\t" \
  "ds_read_b64_tr_b16 a[178:179], v237 offset:4096\n\t" \
  "v_mfma_f32_16x16x32_bf16 a[36:39], a[132:135], v[4:7], a[36:39]\n\t" \
  "buffer_load_dwordx4 v239, %[wo], s71 offen offset:3072 lds\n\t" \
  "s_add_u32 m0, %[m], 16384\n\t" \
  "v_mfma_f32_16x16x32_bf16 a[36:39], a[128:131], v[0:3], a[36:39]\n\t" \
  "s_waitcnt lgkmcnt(6)\n\t" \
  "s_xor_b32 s71, %[wob], 1179648\n\t" \
  "ds_read_b64_tr_b16 a[180:181], v237 offset:8192\n\t" \
  "v_mfma_f32_16x16x32_bf16 a[48:51], a[160:163], v[16:19], a[48:51]\n\t" \
  "ds_read_b64_tr_b16 a[182:183], v237 offset:12288\n\t" \
  ".Lhs11:\n\ts_cbranch_execz .Lhs11n\n\t" \
  "ds_read_b64_tr_b16 a[184:185], v237 offset:16384\n\t" \
  "v_mfma_f32_16x16x32_bf16 a[48:51], a[164:167], v[20:23], a[48:51]\n\t" \
  "s_waitcnt lgkmcnt(5)\n\t" \
  "ds_read_b64_tr_b16 a[186:187], v237 offset:20480\n\t" \
  "s_waitcnt vmcnt(5)\n\t" \
  "v_cmp_ne_u32 vcc, %[tag], v246\n\t" \
  "s_and_b32 vcc_lo, vcc_lo, 0xe00\n\t" \
  "s_cbranch_scc1 .Lop_s1\n\t" \
  ".Lop_k1:\n\t" \
  "v_mfma_f32_16x16x32_bf16 a[48:51], a[168:171], v[24:27], a[48:51]\n\t" \
  "buffer_load_dwordx4 v[64:67], v242, %[ws], s73 offen offset:0 sc1\n\t" \
  "v_mfma_f32_16x16x32_bf16 a[48:51], a[172:175], v[28:31], a[48:51]\n\t" \
  "ds_read_b64_tr_b16 a[188:189], v237 offset:24576\n\t" \
  "ds_read_b64_tr_b16 a[190:191], v237 offset:28672\n\t" \
  "v_mfma_f32_16x16x32_bf16 a[52:55], a[172:175], v[12:15], a[52:55]\n\t" \
  "v_xor_b32 v235, 0xc0a0, v234\n\t" \
  "v_mfma_f32_16x16x32_bf16 a[52:55], a[168:171], v[8:11], a[52:55]\n\t" \
  "ds_read_b64_tr_b16 a[144:145], v235 offset:0\n\t" \
  "ds_read_b64_tr_b16 a[146:147], v235 offset:4096\n\t" \
  "v_mfma_f32_16x16x32_bf16 a[52:55], a[164:167], v[4:7], a[52:55]\n\t" \
  "buffer_load_dwordx4 v[68:71], v242, %[ws], s73 offen offset:1024 sc1\n\t" \
  "v_mfma_f32_16x16x32_bf16 a[52:55], a[160:163], v[0:3], a[52:55]\n\t" \
  "s_waitcnt lgkmcnt(6)\n\t" \
  "ds_read_b64_tr_b16 a[148:149], v235 offset:8192\n\t" \
  "ds_read_b64_tr_b16 a[150:151], v235 offset:12288\n\t" \
  "v_mfma_f32_16x16x32_bf16 a[64:67], a[176:179], v[16:19], a[64:67]\n\t" \
  "ds_read_b64_tr_b16 a[152:153], v235 offset:16384\n\t" \
  "v_mfma_f32_16x16x32_bf16 a[64:67], a[180:183], v[20:23], a[64:67]\n\t" \
  "s_waitcnt lgkmcnt(5)\n\t" \
  "ds_read_b64_tr_b16 a[154:155], v235 offset:20480\n\t" \
  "v_mfma_f32_16x16x32_bf16 a[64:67], a[184:187], v[24:27], a[64:67]\n\t" \
  "ds_read_b64_tr_b16 a[156:157], v235 offset:24576\n\t" \
  "v_mfma_f32_16x16x32_bf16 a[64:67], a[188:191], v[28:31], a[64:67]\n\t" \
  "buffer_load_dwordx4 v[72:75], v242, %[ws], s73 offen offset:2048 sc1\n\t" \
  "v_mfma_f32_16x16x32_bf16 a[68:71], a[188:191], v[12:15], a[68:71]\n\t" \
  "ds_read_b64_tr_b16 a[158:159], v235 offset:28672\n\t" \
  "v_xor_b32 v236, 0xc0c0, v234\n\t" \
  "v_mfma_f32_16x16x32_bf16 a[68:71], a[184:187], v[8:11], a[68:71]\n\t" \
  "ds_read_b64_tr_b16 a[128:129], v236 offset:0\n\t" \
  "ds_read_b64_tr_b16 a[130:131], v236 offset:4096\n\t" \
  "v_mfma_f32_16x16x32_bf16 a[68:71], a[180:183], v[4:7], a[68:71]\n\t" \
  "ds_read_b64_tr_b16 a[132:133], v236 offset:8192\n\t" \
  "v_mfma_f32_16x16x32_bf16 a[68:71], a[176:179], v[0:3], a[68:71]\n\t" \
  "s_waitcnt lgkmcnt(7)\n\t" \
  "s_barrier\n\t" \
  "v_mfma_f32_16x16x32_bf16 a[80:83], a[144:147], v[16:19], a[80:83]\n\t" \
  "buffer_load_dwordx4 v[76:79], v242, %[ws], s73 offen offset:3072 sc1\n\t" \
  "v_mfma_f32_16x16x32_bf16 a[80:83], a[148:151], v[20:23], a[80:83]\n\t" \
  "s_waitcnt lgkmcnt(3)\n\t" \
  "ds_read_b64_tr_b16 a[134:135], v236 offset:12288\n\t" \
  "ds_read_b64_tr_b16 a[136:137], v236 offset:16384\n\t" \
  "v_mfma_f32_16x16x32_bf16 a[80:83], a[152:155], v[24:27], a[80:83]\n\t" \
  "ds_read_b64_tr_b16 a[138:139], v236 offset:20480\n\t" \
  "ds_read_b64_tr_b16 a[140:141], v236 offset:24576\n\t" \
  "v_mfma_f32_16x16x32_bf16 a[80:83], a[156:159], v[28:31], a[80:83]\n\t" \
  "ds_read_b64_tr_b16 a[142:143], v236 offset:28672\n\t" \
  "v_xor_b32 v237, 0xc0e0, v234\n\t" \
  "v_mfma_f32_16x16x32_bf16 a[84:87], a[156:159], v[12:15], a[84:87]\n\t" \
  "buffer_load_dwordx4 v240, %[wo], s71 offen offset:0 lds\n\t" \
  "v_mfma_f32_16x16x32_bf16 a[84:87], a[152:155], v[8:11], a[84:87]\n\t" \
  "ds_read_b64_tr_b16 a[12:13], v237 offset:0\n\t" \
  "ds_read_b64_tr_b16 a[14:15], v237 offset:4096\n\t" \
  "v_mfma_f32_16x16x32_bf16 a[84:87], a[148:151], v[4:7], a[84:87]\n\t" \
  "ds_read_b64_tr_b16 a[28:29], v237 offset:8192\n\t" \
  "v_mfma_f32_16x16x32_bf16 a[84:87], a[144:147], v[0:3], a[84:87]\n\t" \
  "s_waitcnt lgkmcnt(7)\n\t" \
  "ds_read_b64_tr_b16 a[30:31], v237 offset:12288\n\t" \
  "v_mfma_f32_16x16x32_bf16 a[96:99], a[128:131], v[16:19], a[96:99]\n\t" \
  "buffer_load_dwordx4 v241, %[wo], s71 offen offset:1024 lds\n\t" \
  "v_mfma_f32_16x16x32_bf16 a[96:99], a[132:135], v[20:23], a[96:99]\n\t" \
  "s_waitcnt lgkmcnt(4)\n\t" \
  "ds_read_b64_tr_b16 a[44:45], v237 offset:16384\n\t" \
  "ds_read_b64_tr_b16 a[46:47], v237 offset:20480\n\t" \
  "v_mfma_f32_16x16x32_bf16 a[96:99], a[136:139], v[24:27], a[96:99]\n\t" \
  "ds_read_b64_tr_b16 a[60:61], v237 offset:24576\n\t" \
  "v_lshrrev_b32 v244, 4, v242\n\t" \
  "v_mfma_f32_16x16x32_bf16 a[96:99], a[140:143], v[28:31], a[96:99]\n\t" \
  "s_lshl_b32 s77, %[hl], 2\n\t" \
  "ds_read_b64_tr_b16 a[62:63], v237 offset:28672\n\t" \
  "v_xor_b32 v235, 0x14000, v234\n\t" \
  "v_mfma_f32_16x16x32_bf16 a[100:103], a[140:143], v[12:15], a[100:103]\n\t" \
  "buffer_load_dwordx4 v238, %[wo], s71 offen offset:2048 lds\n\t" \
  "v_mfma_f32_16x16x32_bf16 a[100:103], a[136:139], v[8:11], a[100:103]\n\t" \
  "s_add_u32 s77, s77, %[wid]\n\t" \
  "ds_read_b64_tr_b16 a[160:161], v235 offset:0\n\t" \
  "ds_read_b64_tr_b16 a[162:163], v235 offset:4096\n\t" \
  "v_mfma_f32_16x16x32_bf16 a[100:103], a[132:135], v[4:7], a[100:103]\n\t" \
  "s_lshl_b32 s77, s77, 6\n\t" \
  "ds_read_b64_tr_b16 a[164:165], v235 offset:8192\n\t" \
  "v_add_u32 v230, 0x10000, v242\n\t" \
  "v_mfma_f32_16x16x32_bf16 a[100:103], a[128:131], v[0:3], a[100:103]\n\t" \
  "s_waitcnt lgkmcnt(7)\n\t" \
  "ds_read_b64_tr_b16 a[166:167], v235 offset:12288\n\t" \
  "v_xor_b32 v236, 0x24000, v234\n\t" \
  "v_mfma_f32_16x16x32_bf16 a[112:115], a[12:15], v[16:19], a[112:115]\n\t" \
  "ds_read_b64_tr_b16 a[168:169], v236 offset:0\n\t" \
  "v_add_u32 v232, 0x20000, v242\n\t" \
  "v_mfma_f32_16x16x32_bf16 a[112:115], a[28:31], v[20:23], a[112:115]\n\t" \
  "s_waitcnt lgkmcnt(5)\n\t" \
  "buffer_load_dwordx4 v239, %[wo], s71 offen offset:3072 lds\n\t" \
  "s_add_u32 m0, %[m], 98304\n\t" \
  "v_mfma_f32_16x16x32_bf16 a[112:115], a[44:47], v[24:27], a[112:115]\n\t" \
  "s_xor_b32 s71, %[wob], 1310720\n\t" \
  "ds_read_b64_tr_b16 a[170:171], v236 offset:4096\n\t" \
  "ds_read_b64_tr_b16 a[172:173], v236 offset:8192\n\t" \
  "v_mfma_f32_16x16x32_bf16 a[112:115], a[60:63], v[28:31], a[112:115]\n\t" \
  "s_xor_b32 s73, %[eso], 393216\n\t" \
  "ds_read_b64_tr_b16 a[174:175], v236 offset:12288\n\t" \
  "s_add_u32 s70, %[m], 147456\n\t" \
  "v_mfma_f32_16x16x32_bf16 a[116:119], a[60:63], v[12:15], a[116:119]\n\t" \
  "s_lshl_b32 s69, s77, 2\n\t" \
  "v_xor_b32 v237, 0x14020, v234\n\t" \
  "s_add_u32 s69, s69, 2228224\n\t" \
  "v_mfma_f32_16x16x32_bf16 a[116:119], a[44:47], v[8:11], a[116:119]\n\t" \
  "buffer_load_dwordx4 v240, %[wo], s71 offen offset:0 lds\n\t" \
  "v_mfma_f32_16x16x32_bf16 a[116:119], a[28:31], v[4:7], a[116:119]\n\t" \
  "s_bfe_u32 s68, %[eso], 0x2000f\n\t" \
  "ds_read_b64_tr_b16 a[176:177], v237 offset:0\n\t" \
  "v_mfma_f32_16x16x32_bf16 a[116:119], a[12:15], v[0:3], a[116:119]\n\t" \
  "s_waitcnt lgkmcnt(5)\n\t" \
  "ds_read_b64_tr_b16 a[178:179], v237 offset:4096\n\t" \
  "s_lshl_b32 s60, s68, 17\n\t" \
  STP("v_writelane_b32 %[stv], s94, 26\n\t") \
  STP("v_writelane_b32 %[stv], s92, 27\n\t") \
  STP("s_memrealtime s[94:95]\n\t") \
  STP("s_memtime s[92:93]\n\t") \
  "v_mfma_f32_16x16x32_bf16 a[0:3], a[160:163], v[48:51], a[0:3]\n\t" \
  "ds_read_b64_tr_b16 a[180:181], v237 offset:8192\n\t" \
  "v_mfma_f32_16x16x32_bf16 a[0:3], a[164:167], v[52:55], a[0:3]\n\t" \
  "s_waitcnt lgkmcnt(3)\n\t" \
  "ds_read_b64_tr_b16 a[182:183], v237 offset:12288\n\t" \
  "v_xor_b32 v235, 0x24020, v234\n\t" \
  "v_mfma_f32_16x16x32_bf16 a[0:3], a[168:171], v[56:59], a[0:3]\n\t" \
  "buffer_load_dwordx4 v[16:19], v242, %[ws], s73 offen offset:0 sc1\n\t" \
  "v_mfma_f32_16x16x32_bf16 a[0:3], a[172:175], v[60:63], a[0:3]\n\t" \
  "s_waitcnt vmcnt(14)\n\t" \
  "ds_read_b64_tr_b16 a[184:185], v235 offset:0\n\t" \
  "s_add_u32 s60, s60, %[xcb]\n\t" \
  "v_mfma_f32_16x16x32_bf16 a[4:7], a[172:175], v[92:95], a[4:7]\n\t" \
  "s_lshl_b32 s63, %[wid], 10\n\t" \
  "ds_read_b64_tr_b16 a[186:187], v235 offset:4096\n\t" \
  "v_and_b32 v250, 7, v244\n\t" \
  "v_mfma_f32_16x16x32_bf16 a[4:7], a[168:171], v[88:91], a[4:7]\n\t" \
  "s_add_u32 s60, s60, s63\n\t" \
  "ds_read_b64_tr_b16 a[188:189], v235 offset:8192\n\t" \
  "v_lshlrev_b32 v250, 5, v250\n\t" \
  "v_mfma_f32_16x16x32_bf16 a[4:7], a[164:167], v[84:87], a[4:7]\n\t" \
  "buffer_load_dwordx4 v[20:23], v242, %[ws], s73 offen offset:1024 sc1\n\t" \
  "v_mfma_f32_16x16x32_bf16 a[4:7], a[160:163], v[80:83], a[4:7]\n\t" \
  "s_waitcnt vmcnt(7)\n\t" \
  "ds_read_b64_tr_b16 a[190:191], v235 offset:12288\n\t" \
  "v_lshrrev_b32 v251, 4, v244\n\t" \
  "v_mfma_f32_16x16x32_bf16 a[8:11], a[160:163], v[64:67], a[8:11]\n\t" \
  "s_xor_b32 s61, s60, 135168\n\t" \
  "v_xor_b32 v236, 0x14040, v234\n\t" \
  "v_lshl_add_u32 v250, v251, 3, v250\n\t" \
  "v_mfma_f32_16x16x32_bf16 a[8:11], a[164:167], v[68:71], a[8:11]\n\t" \
  "ds_read_b64_tr_b16 a[144:145], v236 offset:0\n\t" \
  "v_mfma_f32_16x16x32_bf16 a[8:11], a[168:171], v[72:75], a[8:11]\n\t" \
  "buffer_load_dwordx4 v[24:27], v242, %[ws], s73 offen offset:2048 sc1\n\t" \
  "v_mfma_f32_16x16x32_bf16 a[8:11], a[172:175], v[76:79], a[8:11]\n\t" \
  "s_waitcnt lgkmcnt(5)\n\t" \
  "ds_read_b64_tr_b16 a[146:147], v236 offset:4096\n\t" \
  "s_xor_b32 s62, s60, 278528\n\t" \
  "v_mfma_f32_16x16x32_bf16 a[16:19], a[176:179], v[48:51], a[16:19]\n\t" \
  "s_lshl_b32 s65, s68, 7\n\t" \
  "ds_read_b64_tr_b16 a[148:149], v236 offset:8192\n\t" \
  "v_mfma_f32_16x16x32_bf16 a[16:19], a[180:183], v[52:55], a[16:19]\n\t" \
  "s_waitcnt lgkmcnt(3)\n\t" \
  "ds_read_b64_tr_b16 a[150:151], v236 offset:12288\n\t" \
  "v_xor_b32 v237, 0x24040, v234\n\t" \
  "v_mfma_f32_16x16x32_bf16 a[16:19], a[184:187], v[56:59], a[16:19]\n\t" \
  "s_add_u32 s65, s65, %[frb]\n\t" \
  "ds_read_b64_tr_b16 a[152:153], v237 offset:0\n\t" \
  "v_mfma_f32_16x16x32_bf16 a[16:19], a[188:191], v[60:63], a[16:19]\n\t" \
  "buffer_load_dwordx4 v[28:31], v242, %[ws], s73 offen offset:3072 sc1\n\t" \
  "s_xor_b32 s73, %[eso], 425984\n\t" \
  "v_mfma_f32_16x16x32_bf16 a[20:23], a[188:191], v[92:95], a[20:23]\n\t" \
  "s_xor_b32 s66, s65, 144\n\t" \
  "ds_read_b64_tr_b16 a[154:155], v237 offset:4096\n\t" \
  "v_mfma_f32_16x16x32_bf16 a[20:23], a[184:187], v[88:91], a[20:23]\n\t" \
  "ds_read_b64_tr_b16 a[156:157], v237 offset:8192\n\t" \
  "v_mfma_f32_16x16x32_bf16 a[20:23], a[180:183], v[84:87], a[20:23]\n\t" \
  "ds_read_b64_tr_b16 a[158:159], v237 offset:12288\n\t" \
  "v_mfma_f32_16x16x32_bf16 a[20:23], a[176:179], v[80:83], a[20:23]\n\t" \
  "buffer_load_dwordx4 v[0:3], v242, %[ws], s73 offen offset:0 sc1\n\t" \
  "v_mfma_f32_16x16x32_bf16 a[24:27], a[176:179], v[64:67], a[24:27]\n\t" \
  "s_xor_b32 s67, s65, 288\n\t" \
  "v_xor_b32 v235, 0x14060, v234\n\t" \
  "v_mfma_f32_16x16x32_bf16 a[24:27], a[180:183], v[68:71], a[24:27]\n\t" \
  "s_xor_b32 s68, s65, 384\n\t" \
  "ds_read_b64_tr_b16 a[128:129], v235 offset:0\n\t" \
  "v_mfma_f32_16x16x32_bf16 a[24:27], a[184:187], v[72:75], a[24:27]\n\t" \
  "s_lshr_b32 s75, %[m], 4\n\t" \
  "s_bitset1_b32 s75, 17\n\t" \
  "v_add_u32 v250, s75, v250\n\t" \
  "v_mfma_f32_16x16x32_bf16 a[24:27], a[188:191], v[76:79], a[24:27]\n\t" \
  "s_waitcnt lgkmcnt(5)\n\t" \
  "ds_read_b64_tr_b16 a[130:131], v235 offset:4096\n\t" \
  "s_andn2_b32 s68, s68, 12\n\t" \
  "v_mfma_f32_16x16x32_bf16 a[32:35], a[144:147], v[48:51], a[32:35]\n\t" \
  "buffer_load_dwordx4 v[4:7], v242, %[ws], s73 offen offset:1024 sc1\n\t" \
  "v_mfma_f32_16x16x32_bf16 a[32:35], a[148:151], v[52:55], a[32:35]\n\t" \
  "s_waitcnt lgkmcnt(2)\n\t" \
  "ds_read_b64_tr_b16 a[132:133], v235 offset:8192\n\t" \
  "s_andn2_b32 s64, 0x2000, %[na]\n\t" \
  "v_mfma_f32_16x16x32_bf16 a[32:35], a[152:155], v[56:59], a[32:35]\n\t" \
  "v_permlane32_swap_b32 v132, v134\n\t" \
  "v_mfma_f32_16x16x32_bf16 a[32:35], a[156:159], v[60:63], a[32:35]\n\t" \
  "ds_read_b64_tr_b16 a[134:135], v235 offset:12288\n\t" \
  "v_xor_b32 v236, 0x24060, v234\n\t" \
  "v_mfma_f32_16x16x32_bf16 a[36:39], a[156:159], v[92:95], a[36:39]\n\t" \
  "buffer_load_dwordx4 v[8:11], v242, %[ws], s73 offen offset:2048 sc1\n\t" \
  "v_mfma_f32_16x16x32_bf16 a[36:39], a[152:155], v[88:91], a[36:39]\n\t" \
  "s_xor_b32 s63, s60, 393216\n\t" \
  "ds_read_b64_tr_b16 a[136:137], v236 offset:0\n\t" \
  "ds_read_b64_tr_b16 a[138:139], v236 offset:4096\n\t" \
  "v_mfma_f32_16x16x32_bf16 a[36:39], a[148:151], v[84:87], a[36:39]\n\t" \
  "s_andn2_b32 s63, s63, 98304\n\t" \
  "v_permlane32_swap_b32 v128, v130\n\t" \
  "v_mfma_f32_16x16x32_bf16 a[36:39], a[144:147], v[80:83], a[36:39]\n\t" \
  "ds_read_b64_tr_b16 a[140:141], v236 offset:8192\n\t" \
  "ds_read_b64_tr_b16 a[142:143], v236 offset:12288\n\t" \
  "v_mfma_f32_16x16x32_bf16 a[40:43], a[144:147], v[64:67], a[40:43]\n\t" \
  "buffer_load_dwordx4 v[12:15], v242, %[ws], s73 offen offset:3072 sc1\n\t" \
  "s_xor_b32 s73, %[eso], 458752\n\t" \
  "v_mfma_f32_16x16x32_bf16 a[40:43], a[148:151], v[68:71], a[40:43]\n\t" \
  "v_permlane32_swap_b32 v133, v135\n\t" \
  "v_mfma_f32_16x16x32_bf16 a[40:43], a[152:155], v[72:75], a[40:43]\n\t" \
  "v_permlane32_swap_b32 v129, v131\n\t" \
  "v_mfma_f32_16x16x32_bf16 a[40:43], a[156:159], v[76:79], a[40:43]\n\t" \
  "s_waitcnt lgkmcnt(4)\n\t" \
  "v_xor_b32 v237, 0x14080, v234\n\t" \
  "v_add_f32 v132, v132, v134\n\t" \
  "v_mfma_f32_16x16x32_bf16 a[48:51], a[128:131], v[48:51], a[48:51]\n\t" \
  "ds_read_b64_tr_b16 a[76:77], v237 offset:0\n\t" \
  "v_add_f32 v128, v128, v130\n\t" \
  "v_mfma_f32_16x16x32_bf16 a[48:51], a[132:135], v[52:55], a[48:51]\n\t" \
  "s_waitcnt lgkmcnt(1)\n\t" \
  "buffer_load_dwordx4 v[32:35], v242, %[ws], s73 offen offset:0 sc1\n\t" \
  "v_mfma_f32_16x16x32_bf16 a[48:51], a[136:139], v[56:59], a[48:51]\n\t" \
  "ds_read_b64_tr_b16 a[78:79], v237 offset:4096\n\t" \
  "ds_read_b64_tr_b16 a[92:93], v237 offset:8192\n\t" \
  "v_mfma_f32_16x16x32_bf16 a[48:51], a[140:143], v[60:63], a[48:51]\n\t" \
  "s_barrier\n\t" \
  "v_mfma_f32_16x16x32_bf16 a[52:55], a[140:143], v[92:95], a[52:55]\n\t" \
  "ds_read_b64_tr_b16 a[94:95], v237 offset:12288\n\t" \
  "v_xor_b32 v235, 0x24080, v234\n\t" \
  "v_mfma_f32_16x16x32_bf16 a[52:55], a[136:139], v[88:91], a[52:55]\n\t" \
  "buffer_load_dwordx4 v[36:39], v242, %[ws], s73 offen offset:1024 sc1\n\t" \
  "buffer_load_dword v246, v245, %[ws], %[fw2] offen sc1\n\t" \
  "v_mfma_f32_16x16x32_bf16 a[52:55], a[132:135], v[84:87], a[52:55]\n\t" \
  "ds_read_b64_tr_b16 a[108:109], v235 offset:0\n\t" \
  "ds_read_b64_tr_b16 a[110:111], v235 offset:4096\n\t" \
  "v_add_f32 v133, v133, v135\n\t" \
  "v_mfma_f32_16x16x32_bf16 a[52:55], a[128:131], v[80:83], a[52:55]\n\t" \
  "ds_read_b64_tr_b16 a[124:125], v235 offset:8192\n\t" \
  "ds_read_b64_tr_b16 a[126:127], v235 offset:12288\n\t" \
  "v_mfma_f32_16x16x32_bf16 a[56:59], a[128:131], v[64:67], a[56:59]\n\t" \
  "s_bitcmp1_b32 %[wid], 0\n\t" \
  "s_cselect_b32 s72, s64, 0\n\t" \
  "s_xor_b32 s63, s63, s72\n\t" \
  "v_mfma_f32_16x16x32_bf16 a[56:59], a[132:135], v[68:71], a[56:59]\n\t" \
  "s_xor_b32 s64, s64, s63\n\t" \
  "v_add_f32 v129, v129, v131\n\t" \
  "v_xor_b32 v236, 0x140a0, v234\n\t" \
  "v_mfma_f32_16x16x32_bf16 a[56:59], a[136:139], v[72:75], a[56:59]\n\t" \
  "ds_read_b64_tr_b16 a[144:145], v236 offset:0\n\t" \
  "v_mfma_f32_16x16x32_bf16 a[56:59], a[140:143], v[76:79], a[56:59]\n\t" \
  "s_waitcnt lgkmcnt(5)\n\t" \
  "buffer_load_dwordx4 v[40:43], v242, %[ws], s73 offen offset:2048 sc1\n\t" \
  "v_mfma_f32_16x16x32_bf16 a[64:67], a[76:79], v[48:51], a[64:67]\n\t" \
  "ds_read_b64_tr_b16 a[146:147], v236 offset:4096\n\t" \
  "v_permlane16_swap_b32 v132, v133\n\t" \
  "v_mfma_f32_16x16x32_bf16 a[64:67], a[92:95], v[52:55], a[64:67]\n\t" \
  "s_waitcnt lgkmcnt(2)\n\t" \
  "ds_read_b64_tr_b16 a[148:149], v236 offset:8192\n\t" \
  "v_mfma_f32_16x16x32_bf16 a[64:67], a[108:111], v[56:59], a[64:67]\n\t" \
  "ds_read_b64_tr_b16 a[150:151], v236 offset:12288\n\t" \
  "v_xor_b32 v237, 0x240a0, v234\n\t" \
  "v_mfma_f32_16x16x32_bf16 a[64:67], a[124:127], v[60:63], a[64:67]\n\t" \
  "buffer_load_dwordx4 v[44:47], v242, %[ws], s73 offen offset:3072 sc1\n\t" \
  "v_mfma_f32_16x16x32_bf16 a[68:71], a[124:127], v[92:95], a[68:71]\n\t" \
  "ds_read_b64_tr_b16 a[152:153], v237 offset:0\n\t" \
  "v_permlane16_swap_b32 v128, v129\n\t" \
  "v_mfma_f32_16x16x32_bf16 a[68:71], a[108:111], v[88:91], a[68:71]\n\t" \
  "ds_read_b64_tr_b16 a[154:155], v237 offset:4096\n\t" \
  "v_add_f32 v132, v132, v133\n\t" \
  "v_mfma_f32_16x16x32_bf16 a[68:71], a[92:95], v[84:87], a[68:71]\n\t" \
  "ds_read_b64_tr_b16 a[156:157], v237 offset:8192\n\t" \
  "v_add_f32 v128, v128, v129\n\t" \
  "v_mfma_f32_16x16x32_bf16 a[68:71], a[76:79], v[80:83], a[68:71]\n\t" \
  "ds_read_b64_tr_b16 a[158:159], v237 offset:12288\n\t" \
  "v_add_f32_dpp v248, v132, v132 row_ror:8 row_mask:0xf bank_mask:0xf\n\t" \
  "v_mfma_f32_16x16x32_bf16 a[72:75], a[76:79], v[64:67], a[72:75]\n\t" \
  "buffer_load_dwordx4 v241, %[wo], s71 offen offset:1024 lds\n\t" \
  "v_mfma_f32_16x16x32_bf16 a[72:75], a[92:95], v[68:71], a[72:75]\n\t" \
  "v_xor_b32 v235, 0x140c0, v234\n\t" \
  "v_add_f32_dpp v249, v128, v128 row_ror:8 row_mask:0xf bank_mask:0xf\n\t" \
  "v_mfma_f32_16x16x32_bf16 a[72:75], a[108:111], v[72:75], a[72:75]\n\t" \
  "ds_read_b64_tr_b16 a[12:13], v235 offset:0\n\t" \
  "v_mfma_f32_16x16x32_bf16 a[72:75], a[124:127], v[76:79], a[72:75]\n\t" \
  "s_waitcnt lgkmcnt(5)\n\t" \
  "ds_read_b64_tr_b16 a[14:15], v235 offset:4096\n\t" \
  "v_mfma_f32_16x16x32_bf16 a[80:83], a[144:147], v[48:51], a[80:83]\n\t" \
  "buffer_load_dwordx4 v238, %[wo], s71 offen offset:2048 lds\n\t" \
  "v_mfma_f32_16x16x32_bf16 a[80:83], a[148:151], v[52:55], a[80:83]\n\t" \
  "s_waitcnt lgkmcnt(2)\n\t" \
  "ds_read_b64_tr_b16 a[28:29], v235 offset:8192\n\t" \
  "v_mfma_f32_16x16x32_bf16 a[80:83], a[152:155], v[56:59], a[80:83]\n\t" \
  "s_xor_b32 s74, %[eso], 98304\n\t" \
  "ds_read_b64_tr_b16 a[30:31], v235 offset:12288\n\t" \
  "v_xor_b32 v236, 0x240c0, v234\n\t" \
  "v_mfma_f32_16x16x32_bf16 a[80:83], a[156:159], v[60:63], a[80:83]\n\t" \
  "ds_read_b64_tr_b16 a[44:45], v236 offset:0\n\t" \
  "v_mfma_f32_16x16x32_bf16 a[84:87], a[156:159], v[92:95], a[84:87]\n\t" \
  "ds_read_b64_tr_b16 a[46:47], v236 offset:4096\n\t" \
  "v_mfma_f32_16x16x32_bf16 a[84:87], a[152:155], v[88:91], a[84:87]\n\t" \
  "buffer_load_dwordx4 v239, %[wo], s71 offen offset:3072 lds\n\t" \
  "s_add_u32 m0, %[m], 32768\n\t" \
  "v_mfma_f32_16x16x32_bf16 a[84:87], a[148:151], v[84:87], a[84:87]\n\t" \
  "s_xor_b32 s71, %[wob], 1441792\n\t" \
  "ds_read_b64_tr_b16 a[60:61], v236 offset:8192\n\t" \
  "v_mfma_f32_16x16x32_bf16 a[84:87], a[144:147], v[80:83], a[84:87]\n\t" \
  "buffer_store_dwordx4 v[160:163], v242, %[ws], s74 offen offset:0 sc1\n\t" \
  "v_mfma_f32_16x16x32_bf16 a[88:91], a[144:147], v[64:67], a[88:91]\n\t" \
  "s_xor_b32 s74, %[eso], 98304\n\t" \
  "ds_read_b64_tr_b16 a[62:63], v236 offset:12288\n\t" \
  "v_xor_b32 v237, 0x140e0, v234\n\t" \
  "v_mfma_f32_16x16x32_bf16 a[88:91], a[148:151], v[68:71], a[88:91]\n\t" \
  "ds_read_b64_tr_b16 a[76:77], v237 offset:0\n\t" \
  "v_mfma_f32_16x16x32_bf16 a[88:91], a[152:155], v[72:75], a[88:91]\n\t" \
  "ds_read_b64_tr_b16 a[78:79], v237 offset:4096\n\t" \
  "v_mfma_f32_16x16x32_bf16 a[88:91], a[156:159], v[76:79], a[88:91]\n\t" \
  "s_waitcnt lgkmcnt(6)\n\t" \
  "buffer_load_dwordx4 v240, %[wo], s71 offen offset:0 lds\n\t" \
  "v_mfma_f32_16x16x32_bf16 a[96:99], a[12:15], v[48:51], a[96:99]\n\t" \
  "ds_read_b64_tr_b16 a[92:93], v237 offset:8192\n\t" \
  "v_mfma_f32_16x16x32_bf16 a[96:99], a[28:31], v[52:55], a[96:99]\n\t" \
  "s_waitcnt lgkmcnt(3)\n\t" \
  "buffer_store_dwordx4 v[164:167], v242, %[ws], s74 offen offset:1024 sc1\n\t" \
  "v_mfma_f32_16x16x32_bf16 a[96:99], a[44:47], v[56:59], a[96:99]\n\t" \
  "s_xor_b32 s74, %[eso], 98304\n\t" \
  "ds_read_b64_tr_b16 a[94:95], v237 offset:12288\n\t" \
  "v_xor_b32 v235, 0x240e0, v234\n\t" \
  "v_mfma_f32_16x16x32_bf16 a[96:99], a[60:63], v[60:63], a[96:99]\n\t" \
  "ds_read_b64_tr_b16 a[108:109], v235 offset:0\n\t" \
  "v_mfma_f32_16x16x32_bf16 a[100:103], a[60:63], v[92:95], a[100:103]\n\t" \
  "ds_read_b64_tr_b16 a[110:111], v235 offset:4096\n\t" \
  "v_mfma_f32_16x16x32_bf16 a[100:103], a[44:47], v[88:91], a[100:103]\n\t" \
  "buffer_load_dwordx4 v241, %[wo], s71 offen offset:1024 lds\n\t" \
  "v_mfma_f32_16x16x32_bf16 a[100:103], a[28:31], v[84:87], a[100:103]\n\t" \
  "ds_read_b64_tr_b16 a[124:125], v235 offset:8192\n\t" \
  "v_mfma_f32_16x16x32_bf16 a[100:103], a[12:15], v[80:83], a[100:103]\n\t" \
  "buffer_store_dwordx4 v[168:171], v242, %[ws], s74 offen offset:2048 sc1\n\t" \
  "s_waitcnt vmcnt(10)\n\t" \
  "v_cmp_ne_u32 vcc, %[tag], v246\n\t" \
  "s_and_b32 vcc_lo, vcc_lo, 0xfe00\n\t" \
  "s_cbranch_scc1 .Lop_s2\n\t" \
  ".Lop_k2:\n\t" \
  "v_mfma_f32_16x16x32_bf16 a[104:107], a[12:15], v[64:67], a[104:107]\n\t" \
  "s_xor_b32 s74, %[eso], 98304\n\t" \
  "ds_read_b64_tr_b16 a[126:127], v235 offset:12288\n\t" \
  "v_xor_b32 v236, 0x1c000, v234\n\t" \
  "v_mfma_f32_16x16x32_bf16 a[104:107], a[28:31], v[68:71], a[104:107]\n\t" \
  "ds_read_b64_tr_b16 a[128:129], v236 offset:0\n\t" \
  "v_mfma_f32_16x16x32_bf16 a[104:107], a[44:47], v[72:75], a[104:107]\n\t" \
  "ds_read_b64_tr_b16 a[130:131], v236 offset:4096\n\t" \
  "v_mfma_f32_16x16x32_bf16 a[104:107], a[60:63], v[76:79], a[104:107]\n\t" \
  "s_waitcnt lgkmcnt(6)\n\t" \
  "ds_read_b64_tr_b16 a[132:133], v236 offset:8192\n\t" \
  "v_mfma_f32_16x16x32_bf16 a[112:115], a[76:79], v[48:51], a[112:115]\n\t" \
  "buffer_store_dwordx4 v[172:175], v242, %[ws], s74 offen offset:3072 sc1\n\t" \
  "v_mfma_f32_16x16x32_bf16 a[112:115], a[92:95], v[52:55], a[112:115]\n\t" \
  "s_waitcnt lgkmcnt(3)\n\t" \
  "ds_read_b64_tr_b16 a[134:135], v236 offset:12288\n\t" \
  "v_mfma_f32_16x16x32_bf16 a[112:115], a[108:111], v[56:59], a[112:115]\n\t" \
  "buffer_load_dwordx4 v238, %[wo], s71 offen offset:2048 lds\n\t" \
  "v_mfma_f32_16x16x32_bf16 a[112:115], a[124:127], v[60:63], a[112:115]\n\t" \
  "ds_read_b64_tr_b16 a[136:137], v236 offset:16384\n\t" \
  "v_mfma_f32_16x16x32_bf16 a[116:119], a[124:127], v[92:95], a[116:119]\n\t" \
  "ds_read_b64_tr_b16 a[138:139], v236 offset:20480\n\t" \
  "v_mfma_f32_16x16x32_bf16 a[116:119], a[108:111], v[88:91], a[116:119]\n\t" \
  "ds_read_b64_tr_b16 a[140:141], v236 offset:24576\n\t" \
  "v_mfma_f32_16x16x32_bf16 a[116:119], a[92:95], v[84:87], a[116:119]\n\t" \
  "buffer_load_dwordx4 v239, %[wo], s71 offen offset:3072 lds\n\t" \
  "v_mfma_f32_16x16x32_bf16 a[116:119], a[76:79], v[80:83], a[116:119]\n\t" \
  "s_xor_b32 s73, %[eso], 524288\n\t" \
  "ds_read_b64_tr_b16 a[142:143], v236 offset:28672\n\t" \
  "v_mfma_f32_16x16x32_bf16 a[120:123], a[76:79], v[64:67], a[120:123]\n\t" \
  "v_xor_b32 v237, 0x1c020, v234\n\t" \
  "v_mfma_f32_16x16x32_bf16 a[120:123], a[92:95], v[68:71], a[120:123]\n\t" \
  "ds_read_b64_tr_b16 a[144:145], v237 offset:0\n\t" \
  "v_mfma_f32_16x16x32_bf16 a[120:123], a[108:111], v[72:75], a[120:123]\n\t" \
  "ds_read_b64_tr_b16 a[146:147], v237 offset:4096\n\t" \
  "v_mfma_f32_16x16x32_bf16 a[120:123], a[124:127], v[76:79], a[120:123]\n\t" \
  "s_waitcnt lgkmcnt(6)\n\t" \
  "buffer_load_dwordx4 v[48:51], v242, %[ws], s73 offen offset:0 sc1\n\t" \
  STP("v_writelane_b32 %[stv], s94, 28\n\t") \
  STP("v_writelane_b32 %[stv], s92, 29\n\t") \
  STP("s_memrealtime s[94:95]\n\t") \
  STP("s_memtime s[92:93]\n\t") \
  "v_mfma_f32_16x16x32_bf16 a[0:3], a[128:131], v[16:19], a[0:3]\n\t" \
  "ds_read_b64_tr_b16 a[148:149], v237 offset:8192\n\t" \
  "v_mfma_f32_16x16x32_bf16 a[0:3], a[132:135], v[20:23], a[0:3]\n\t" \
  "s_waitcnt lgkmcnt(3)\n\t" \
  "ds_read_b64_tr_b16 a[150:151], v237 offset:12288\n\t" \
  "v_mfma_f32_16x16x32_bf16 a[0:3], a[136:139], v[24:27], a[0:3]\n\t" \
  "ds_read_b64_tr_b16 a[152:153], v237 offset:16384\n\t" \
  "v_mfma_f32_16x16x32_bf16 a[0:3], a[140:143], v[28:31], a[0:3]\n\t" \
  "buffer_load_dwordx4 v[52:55], v242, %[ws], s73 offen offset:1024 sc1\n\t" \
  "v_mfma_f32_16x16x32_bf16 a[4:7], a[140:143], v[12:15], a[4:7]\n\t" \
  "ds_read_b64_tr_b16 a[154:155], v237 offset:20480\n\t" \
  "v_mfma_f32_16x16x32_bf16 a[4:7], a[136:139], v[8:11], a[4:7]\n\t" \
  "ds_read_b64_tr_b16 a[156:157], v237 offset:24576\n\t" \
  "v_mfma_f32_16x16x32_bf16 a[4:7], a[132:135], v[4:7], a[4:7]\n\t" \
  "ds_read_b64_tr_b16 a[158:159], v237 offset:28672\n\t" \
  "v_mfma_f32_16x16x32_bf16 a[4:7], a[128:131], v[0:3], a[4:7]\n\t" \
  "s_waitcnt vmcnt(13)\n\t" \
  "buffer_load_dwordx4 v[56:59], v242, %[ws], s73 offen offset:2048 sc1\n\t" \
  "v_mfma_f32_16x16x32_bf16 a[8:11], a[128:131], v[32:35], a[8:11]\n\t" \
  "v_xor_b32 v235, 0x1c040, v234\n\t" \
  "v_mfma_f32_16x16x32_bf16 a[8:11], a[132:135], v[36:39], a[8:11]\n\t" \
  "ds_read_b64_tr_b16 a[12:13], v235 offset:0\n\t" \
  "v_mfma_f32_16x16x32_bf16 a[8:11], a[136:139], v[40:43], a[8:11]\n\t" \
  "ds_read_b64_tr_b16 a[14:15], v235 offset:4096\n\t" \
  "v_mfma_f32_16x16x32_bf16 a[8:11], a[140:143], v[44:47], a[8:11]\n\t" \
  "s_waitcnt lgkmcnt(6)\n\t" \
  "ds_read_b64_tr_b16 a[28:29], v235 offset:8192\n\t" \
  "v_mfma_f32_16x16x32_bf16 a[16:19], a[144:147], v[16:19], a[16:19]\n\t" \
  "buffer_load_dwordx4 v[60:63], v242, %[ws], s73 offen offset:3072 sc1\n\t" \
  "s_xor_b32 s73, %[eso], 557056\n\t" \
  "v_mfma_f32_16x16x32_bf16 a[16:19], a[148:151], v[20:23], a[16:19]\n\t" \
  "s_waitcnt lgkmcnt(3)\n\t" \
  "ds_read_b64_tr_b16 a[30:31], v235 offset:12288\n\t" \
  "v_mfma_f32_16x16x32_bf16 a[16:19], a[152:155], v[24:27], a[16:19]\n\t" \
  "ds_read_b64_tr_b16 a[44:45], v235 offset:16384\n\t" \
  "v_mfma_f32_16x16x32_bf16 a[16:19], a[156:159], v[28:31], a[16:19]\n\t" \
  "ds_read_b64_tr_b16 a[46:47], v235 offset:20480\n\t" \
  "v_mfma_f32_16x16x32_bf16 a[20:23], a[156:159], v[12:15], a[20:23]\n\t" \
  "buffer_load_dwordx4 v[80:83], v242, %[ws], s73 offen offset:0 sc1\n\t" \
  "v_mfma_f32_16x16x32_bf16 a[20:23], a[152:155], v[8:11], a[20:23]\n\t" \
  "ds_read_b64_tr_b16 a[60:61], v235 offset:24576\n\t" \
  "v_mfma_f32_16x16x32_bf16 a[20:23], a[148:151], v[4:7], a[20:23]\n\t" \
  "ds_read_b64_tr_b16 a[62:63], v235 offset:28672\n\t" \
  "v_mfma_f32_16x16x32_bf16 a[20:23], a[144:147], v[0:3], a[20:23]\n\t" \
  "v_mfma_f32_16x16x32_bf16 a[24:27], a[144:147], v[32:35], a[24:27]\n\t" \
  "v_xor_b32 v236, 0x1c060, v234\n\t" \
  "v_mfma_f32_16x16x32_bf16 a[24:27], a[148:151], v[36:39], a[24:27]\n\t" \
  "buffer_load_dwordx4 v[84:87], v242, %[ws], s73 offen offset:1024 sc1\n\t" \
  "v_mfma_f32_16x16x32_bf16 a[24:27], a[152:155], v[40:43], a[24:27]\n\t" \
  "ds_read_b64_tr_b16 a[76:77], v236 offset:0\n\t" \
  "v_mfma_f32_16x16x32_bf16 a[24:27], a[156:159], v[44:47], a[24:27]\n\t" \
  "s_waitcnt lgkmcnt(5)\n\t" \
  "ds_read_b64_tr_b16 a[78:79], v236 offset:4096\n\t" \
  "v_mfma_f32_16x16x32_bf16 a[32:35], a[12:15], v[16:19], a[32:35]\n\t" \
  "ds_read_b64_tr_b16 a[92:93], v236 offset:8192\n\t" \
  "v_mfma_f32_16x16x32_bf16 a[32:35], a[28:31], v[20:23], a[32:35]\n\t" \
  "s_waitcnt lgkmcnt(3)\n\t" \
  "buffer_load_dwordx4 v[88:91], v242, %[ws], s73 offen offset:2048 sc1\n\t" \
  "v_mfma_f32_16x16x32_bf16 a[32:35], a[44:47], v[24:27], a[32:35]\n\t" \
  "ds_read_b64_tr_b16 a[94:95], v236 offset:12288\n\t" \
  "v_mfma_f32_16x16x32_bf16 a[32:35], a[60:63], v[28:31], a[32:35]\n\t" \
  "ds_read_b64_tr_b16 a[108:109], v236 offset:16384\n\t" \
  "v_mfma_f32_16x16x32_bf16 a[36:39], a[60:63], v[12:15], a[36:39]\n\t" \
  "ds_read_b64_tr_b16 a[110:111], v236 offset:20480\n\t" \
  "v_mfma_f32_16x16x32_bf16 a[36:39], a[44:47], v[8:11], a[36:39]\n\t" \
  "buffer_load_dwordx4 v[92:95], v242, %[ws], s73 offen offset:3072 sc1\n\t" \
  "s_xor_b32 s73, %[eso], 589824\n\t" \
  "v_mfma_f32_16x16x32_bf16 a[36:39], a[28:31], v[4:7], a[36:39]\n\t" \
  "ds_read_b64_tr_b16 a[124:125], v236 offset:24576\n\t" \
  "v_mfma_f32_16x16x32_bf16 a[36:39], a[12:15], v[0:3], a[36:39]\n\t" \
  "ds_read_b64_tr_b16 a[126:127], v236 offset:28672\n\t" \
  "v_mfma_f32_16x16x32_bf16 a[40:43], a[12:15], v[32:35], a[40:43]\n\t" \
  "v_xor_b32 v237, 0x1c080, v234\n\t" \
  "v_mfma_f32_16x16x32_bf16 a[40:43], a[28:31], v[36:39], a[40:43]\n\t" \
  "ds_read_b64_tr_b16 v[128:129], v237 offset:0\n\t" \
  "v_mfma_f32_16x16x32_bf16 a[40:43], a[44:47], v[40:43], a[40:43]\n\t" \
  "buffer_load_dwordx4 v[64:67], v242, %[ws], s73 offen offset:0 sc1\n\t" \
  "v_mfma_f32_16x16x32_bf16 a[40:43], a[60:63], v[44:47], a[40:43]\n\t" \
  "s_waitcnt lgkmcnt(5)\n\t" \
  "ds_read_b64_tr_b16 v[130:131], v237 offset:4096\n\t" \
  "v_mfma_f32_16x16x32_bf16 a[48:51], a[76:79], v[16:19], a[48:51]\n\t" \
  "s_add_u32 m0, %[m], 81920\n\t" \
  "ds_read_b64_tr_b16 v[132:133], v237 offset:8192\n\t" \
  "v_mfma_f32_16x16x32_bf16 a[48:51], a[92:95], v[20:23], a[48:51]\n\t" \
  "s_waitcnt lgkmcnt(3)\n\t" \
  "ds_read_b64_tr_b16 v[134:135], v237 offset:12288\n\t" \
  "v_mfma_f32_16x16x32_bf16 a[48:51], a[108:111], v[24:27], a[48:51]\n\t" \
  "buffer_load_dwordx4 v[68:71], v242, %[ws], s73 offen offset:1024 sc1\n\t" \
  "v_mfma_f32_16x16x32_bf16 a[48:51], a[124:127], v[28:31], a[48:51]\n\t" \
  "s_barrier\n\t" \
  "v_mfma_f32_16x16x32_bf16 a[52:55], a[124:127], v[12:15], a[52:55]\n\t" \
  "s_xor_b32 s71, %[wob], 1572864\n\t" \
  "ds_write_b64 v250, v[248:249] offset:19456\n\t" \
  "v_mfma_f32_16x16x32_bf16 a[52:55], a[108:111], v[8:11], a[52:55]\n\t" \
  "ds_read_b64_tr_b16 v[136:137], v237 offset:16384\n\t" \
  "ds_read_b64_tr_b16 v[138:139], v237 offset:20480\n\t" \
  "v_mfma_f32_16x16x32_bf16 a[52:55], a[92:95], v[4:7], a[52:55]\n\t" \
  "ds_read_b64_tr_b16 v[140:141], v237 offset:24576\n\t" \
  "v_mfma_f32_16x16x32_bf16 a[52:55], a[76:79], v[0:3], a[52:55]\n\t" \
  "buffer_load_dwordx4 v[72:75], v242, %[ws], s73 offen offset:2048 sc1\n\t" \
  "v_mfma_f32_16x16x32_bf16 a[56:59], a[76:79], v[32:35], a[56:59]\n\t" \
  "ds_read_b64_tr_b16 v[142:143], v237 offset:28672\n\t" \
  "v_xor_b32 v235, 0x1c0a0, v234\n\t" \
  "v_mfma_f32_16x16x32_bf16 a[56:59], a[92:95], v[36:39], a[56:59]\n\t" \
  "ds_read_b64_tr_b16 a[12:13], v235 offset:0\n\t" \
  "v_mfma_f32_16x16x32_bf16 a[56:59], a[108:111], v[40:43], a[56:59]\n\t" \
  "ds_read_b64_tr_b16 a[14:15], v235 offset:4096\n\t" \
  "v_mfma_f32_16x16x32_bf16 a[56:59], a[124:127], v[44:47], a[56:59]\n\t" \
  "s_waitcnt lgkmcnt(7)\n\t" \
  "buffer_load_dwordx4 v[76:79], v242, %[ws], s73 offen offset:3072 sc1\n\t" \
  "v_mfma_f32_16x16x32_bf16 a[64:67], v[128:131], v[16:19], a[64:67]\n\t" \
  "ds_read_b64_tr_b16 a[28:29], v235 offset:8192\n\t" \
  "v_mfma_f32_16x16x32_bf16 a[64:67], v[132:135], v[20:23], a[64:67]\n\t" \
  "s_waitcnt lgkmcnt(3)\n\t" \
  "ds_read_b64_tr_b16 a[30:31], v235 offset:12288\n\t" \
  "v_mfma_f32_16x16x32_bf16 a[64:67], v[136:139], v[24:27], a[64:67]\n\t" \
  "ds_read_b64_tr_b16 a[44:45], v235 offset:16384\n\t" \
  "v_mfma_f32_16x16x32_bf16 a[64:67], v[140:143], v[28:31], a[64:67]\n\t" \
  "buffer_load_dwordx4 v240, %[wo], s71 offen offset:0 lds\n\t" \
  "v_mfma_f32_16x16x32_bf16 a[68:71], v[140:143], v[12:15], a[68:71]\n\t" \
  "ds_read_b64_tr_b16 a[46:47], v235 offset:20480\n\t" \
  "v_mfma_f32_16x16x32_bf16 a[68:71], v[136:139], v[8:11], a[68:71]\n\t" \
  "ds_read_b64_tr_b16 a[60:61], v235 offset:24576\n\t" \
  "v_mfma_f32_16x16x32_bf16 a[68:71], v[132:135], v[4:7], a[68:71]\n\t" \
  "ds_read_b64_tr_b16 a[62:63], v235 offset:28672\n\t" \
  "v_mfma_f32_16x16x32_bf16 a[68:71], v[128:131], v[0:3], a[68:71]\n\t" \
  "v_mfma_f32_16x16x32_bf16 a[72:75], v[128:131], v[32:35], a[72:75]\n\t" \
  "buffer_load_dwordx4 v241, %[wo], s71 offen offset:1024 lds\n\t" \
  "v_mfma_f32_16x16x32_bf16 a[72:75], v[132:135], v[36:39], a[72:75]\n\t" \
  "v_xor_b32 v236, 0x1c0c0, v234\n\t" \
  "v_mfma_f32_16x16x32_bf16 a[72:75], v[136:139], v[40:43], a[72:75]\n\t" \
  "ds_read_b64_tr_b16 v[144:145], v236 offset:0\n\t" \
  "v_mfma_f32_16x16x32_bf16 a[72:75], v[140:143], v[44:47], a[72:75]\n\t" \
  "s_waitcnt lgkmcnt(5)\n\t" \
  "ds_read_b64_tr_b16 v[146:147], v236 offset:4096\n\t" \
  "v_mfma_f32_16x16x32_bf16 a[80:83], a[12:15], v[16:19], a[80:83]\n\t" \
  "buffer_load_dwordx4 v238, %[wo], s71 offen offset:2048 lds\n\t" \
  "v_mfma_f32_16x16x32_bf16 a[80:83], a[28:31], v[20:23], a[80:83]\n\t" \
  "s_waitcnt lgkmcnt(2)\n\t" \
  "ds_read_b64_tr_b16 v[148:149], v236 offset:8192\n\t" \
  "v_mfma_f32_16x16x32_bf16 a[80:83], a[44:47], v[24:27], a[80:83]\n\t" \
  "ds_read_b64_tr_b16 v[150:151], v236 offset:12288\n\t" \
  "v_mfma_f32_16x16x32_bf16 a[80:83], a[60:63], v[28:31], a[80:83]\n\t" \
  "s_barrier\n\t" \
  "v_mfma_f32_16x16x32_bf16 a[84:87], a[60:63], v[12:15], a[84:87]\n\t" \
  "s_waitcnt vmcnt(17)\n\t" \
  "buffer_store_dword v243, v254, %[ws], %[fw2] offen offset:64 sc1\n\t" \
  "v_mfma_f32_16x16x32_bf16 a[84:87], a[44:47], v[8:11], a[84:87]\n\t" \
  "ds_read_b64_tr_b16 v[152:153], v236 offset:16384\n\t" \
  "ds_read_b64_tr_b16 v[154:155], v236 offset:20480\n\t" \
  "v_mfma_f32_16x16x32_bf16 a[84:87], a[28:31], v[4:7], a[84:87]\n\t" \
  "buffer_load_dwordx4 v239, %[wo], s71 offen offset:3072 lds\n\t" \
  "v_mfma_f32_16x16x32_bf16 a[84:87], a[12:15], v[0:3], a[84:87]\n\t" \
  "ds_read_b64_tr_b16 v[156:157], v236 offset:24576\n\t" \
  "ds_read_b64_tr_b16 v[158:159], v236 offset:28672\n\t" \
  "v_mfma_f32_16x16x32_bf16 a[88:91], a[12:15], v[32:35], a[88:91]\n\t" \
  "v_and_b32 v250, 3, v244\n\t" \
  "v_xor_b32 v237, 0x1c0e0, v234\n\t" \
  "v_mfma_f32_16x16x32_bf16 a[88:91], a[28:31], v[36:39], a[88:91]\n\t" \
  "ds_read_b64_tr_b16 v[128:129], v237 offset:0\n\t" \
  "v_bfe_u32 v251, v244, 4, 1\n\t" \
  "v_mfma_f32_16x16x32_bf16 a[88:91], a[44:47], v[40:43], a[88:91]\n\t" \
  ".Lhs12:\n\ts_cbranch_execz .Lhs12n\n\t" \
  "ds_read_b64_tr_b16 v[130:131], v237 offset:4096\n\t" \
  "v_lshl_add_u32 v250, v251, 2, v250\n\t" \
  "v_mfma_f32_16x16x32_bf16 a[88:91], a[60:63], v[44:47], a[88:91]\n\t" \
  "s_waitcnt lgkmcnt(6)\n\t" \
  "ds_read_b64_tr_b16 v[132:133], v237 offset:8192\n\t" \
  "v_bfe_u32 v251, v244, 2, 2\n\t" \
  "v_mfma_f32_16x16x32_bf16 a[96:99], v[144:147], v[16:19], a[96:99]\n\t" \
  "ds_read_b64_tr_b16 v[134:135], v237 offset:12288\n\t" \
  "v_lshlrev_b32 v251, 8, v251\n\t" \
  "v_mfma_f32_16x16x32_bf16 a[96:99], v[148:151], v[20:23], a[96:99]\n\t" \
  "s_waitcnt lgkmcnt(4)\n\t" \
  "ds_read_b64_tr_b16 v[136:137], v237 offset:16384\n\t" \
  "v_lshl_add_u32 v251, v250, 3, v251\n\t" \
  "v_mfma_f32_16x16x32_bf16 a[96:99], v[152:155], v[24:27], a[96:99]\n\t" \
  "ds_read_b64_tr_b16 v[138:139], v237 offset:20480\n\t" \
  "v_mfma_f32_16x16x32_bf16 a[96:99], v[156:159], v[28:31], a[96:99]\n\t" \
  "ds_read_b64_tr_b16 v[140:141], v237 offset:24576\n\t" \
  "v_mfma_f32_16x16x32_bf16 a[100:103], v[156:159], v[12:15], a[100:103]\n\t" \
  "ds_read_b64_tr_b16 v[142:143], v237 offset:28672\n\t" \
  "v_mfma_f32_16x16x32_bf16 a[100:103], v[152:155], v[8:11], a[100:103]\n\t" \
  "s_lshr_b32 s75, %[m], 6\n\t" \
  "s_bitset1_b32 s75, 17\n\t" \
  "v_add_u32 v251, s75, v251\n\t" \
  "v_mfma_f32_16x16x32_bf16 a[100:103], v[148:151], v[4:7], a[100:103]\n\t" \
  "ds_read_b64 v[224:225], v251 offset:19456\n\t" \
  "v_lshlrev_b32 v228, 4, v250\n\t" \
  "v_mfma_f32_16x16x32_bf16 a[100:103], v[144:147], v[0:3], a[100:103]\n\t" \
  "v_mov_b32 v249, %[tag]\n\t" \
  "v_mov_b32 v251, %[tag]\n\t" \
  "v_mfma_f32_16x16x32_bf16 a[104:107], v[144:147], v[32:35], a[104:107]\n\t" \
  "s_mov_b32 s82, 0x000f000f\n\t" \
  "v_xor_b32 v235, 0x0, v234\n\t" \
  "s_mov_b32 s83, 0\n\t" \
  "v_mfma_f32_16x16x32_bf16 a[104:107], v[148:151], v[36:39], a[104:107]\n\t" \
  "ds_read_b64_tr_b16 a[12:13], v235 offset:0\n\t" \
  "v_mfma_f32_16x16x32_bf16 a[104:107], v[152:155], v[40:43], a[104:107]\n\t" \
  "ds_read_b64_tr_b16 a[14:15], v235 offset:4096\n\t" \
  "v_mfma_f32_16x16x32_bf16 a[104:107], v[156:159], v[44:47], a[104:107]\n\t" \
  "s_waitcnt lgkmcnt(7)\n\t" \
  "ds_read_b64_tr_b16 a[28:29], v235 offset:8192\n\t" \
  "v_mfma_f32_16x16x32_bf16 a[112:115], v[128:131], v[16:19], a[112:115]\n\t" \
  "ds_read_b64_tr_b16 a[30:31], v235 offset:12288\n\t" \
  "v_mfma_f32_16x16x32_bf16 a[112:115], v[132:135], v[20:23], a[112:115]\n\t" \
  "s_waitcnt lgkmcnt(5)\n\t" \
  "ds_read_b64_tr_b16 a[44:45], v235 offset:16384\n\t" \
  "v_mfma_f32_16x16x32_bf16 a[112:115], v[136:139], v[24:27], a[112:115]\n\t" \
  "s_waitcnt lgkmcnt(5)\n\t" \
  "v_add_f32_dpp v226, v224, v224 row_ror:4 row_mask:0xf bank_mask:0xf\n\t" \
  "v_add_f32_dpp v227, v225, v225 row_ror:4 row_mask:0xf bank_mask:0xf\n\t" \
  "v_mfma_f32_16x16x32_bf16 a[112:115], v[140:143], v[28:31], a[112:115]\n\t" \
  "ds_read_b64_tr_b16 a[46:47], v235 offset:20480\n\t" \
  "v_add_f32_dpp v248, v226, v226 row_ror:8 row_mask:0xf bank_mask:0xf\n\t" \
  "v_mfma_f32_16x16x32_bf16 a[116:119], v[140:143], v[12:15], a[116:119]\n\t" \
  "ds_read_b64_tr_b16 a[60:61], v235 offset:24576\n\t" \
  "v_add_f32_dpp v250, v227, v227 row_ror:8 row_mask:0xf bank_mask:0xf\n\t" \
  "v_mfma_f32_16x16x32_bf16 a[116:119], v[136:139], v[8:11], a[116:119]\n\t" \
  "s_mov_b64 exec, s[82:83]\n\t" \
  "buffer_store_dwordx4 v[248:251], v228, %[ws], %[mbp] offen sc1\n\t" \
  "s_mov_b64 exec, -1\n\t" \
  "v_mfma_f32_16x16x32_bf16 a[116:119], v[132:135], v[4:7], a[116:119]\n\t" \
  "ds_read_b64_tr_b16 a[62:63], v235 offset:28672\n\t" \
  "v_mfma_f32_16x16x32_bf16 a[116:119], v[128:131], v[0:3], a[116:119]\n\t" \
  "s_xor_b32 s73, %[eso], 655360\n\t" \
  "v_mfma_f32_16x16x32_bf16 a[120:123], v[128:131], v[32:35], a[120:123]\n\t" \
  "v_xor_b32 v236, 0x20, v234\n\t" \
  "v_mfma_f32_16x16x32_bf16 a[120:123], v[132:135], v[36:39], a[120:123]\n\t" \
  "ds_read_b64_tr_b16 a[76:77], v236 offset:0\n\t" \
  "v_mfma_f32_16x16x32_bf16 a[120:123], v[136:139], v[40:43], a[120:123]\n\t" \
  "ds_read_b64_tr_b16 a[78:79], v236 offset:4096\n\t" \
  "v_mov_b32 v160, 0\n\t" \
  "v_mfma_f32_16x16x32_bf16 a[120:123], v[140:143], v[44:47], a[120:123]\n\t" \
  "s_waitcnt vmcnt(14) lgkmcnt(6)\n\t" \
  "buffer_load_dwordx4 v[16:19], v242, %[ws], s73 offen offset:0 sc1\n\t" \
  "v_mfma_f32_16x16x32_bf16 a[0:3], a[12:15], v[48:51], a[0:3]\n\t" \
  "ds_read_b64_tr_b16 a[92:93], v236 offset:8192\n\t" \
  "v_mov_b32 v161, 0\n\t" \
  "v_mfma_f32_16x16x32_bf16 a[0:3], a[28:31], v[52:55], a[0:3]\n\t" \
  "s_waitcnt lgkmcnt(3)\n\t" \
  "buffer_load_dwordx4 v[20:23], v242, %[ws], s73 offen offset:1024 sc1\n\t" \
  "v_mfma_f32_16x16x32_bf16 a[0:3], a[44:47], v[56:59], a[0:3]\n\t" \
  "ds_read_b64_tr_b16 a[94:95], v236 offset:12288\n\t" \
  "v_mov_b32 v162, 0\n\t" \
  "v_mfma_f32_16x16x32_bf16 a[0:3], a[60:63], v[60:63], a[0:3]\n\t" \
  "s_waitcnt vmcnt(12)\n\t" \
  "ds_read_b64_tr_b16 a[108:109], v236 offset:16384\n\t" \
  "v_mov_b32 v163, 0\n\t" \
  "v_mfma_f32_16x16x32_bf16 a[4:7], a[60:63], v[92:95], a[4:7]\n\t" \
  "ds_read_b64_tr_b16 a[110:111], v236 offset:20480\n\t" \
  "v_mov_b32 v164, 0x3f803f80\n\t" \
  "v_mfma_f32_16x16x32_bf16 a[4:7], a[44:47], v[88:91], a[4:7]\n\t" \
  "buffer_load_dwordx4 v[24:27], v242, %[ws], s73 offen offset:2048 sc1\n\t" \
  "v_mfma_f32_16x16x32_bf16 a[4:7], a[28:31], v[84:87], a[4:7]\n\t" \
  "ds_read_b64_tr_b16 a[124:125], v236 offset:24576\n\t" \
  "v_add_u32 v165, s70, v242\n\t" \
  "v_mfma_f32_16x16x32_bf16 a[4:7], a[12:15], v[80:83], a[4:7]\n\t" \
  "s_waitcnt vmcnt(9)\n\t" \
  "ds_read_b64_tr_b16 a[126:127], v236 offset:28672\n\t" \
  "v_mfma_f32_16x16x32_bf16 a[8:11], a[12:15], v[64:67], a[8:11]\n\t" \
  "v_xor_b32 v237, 0x40, v234\n\t" \
  "v_mfma_f32_16x16x32_bf16 a[8:11], a[28:31], v[68:71], a[8:11]\n\t" \
  "buffer_load_dwordx4 v[28:31], v242, %[ws], s73 offen offset:3072 sc1\n\t" \
  "s_xor_b32 s73, %[eso], 688128\n\t" \
  "v_mfma_f32_16x16x32_bf16 a[8:11], a[44:47], v[72:75], a[8:11]\n\t" \
  "ds_read_b64_tr_b16 v[144:145], v237 offset:0\n\t" \
  "v_mfma_f32_16x16x32_bf16 a[8:11], a[60:63], v[76:79], a[8:11]\n\t" \
  "s_waitcnt lgkmcnt(5)\n\t" \
  "ds_read_b64_tr_b16 v[146:147], v237 offset:4096\n\t" \
  "v_mfma_f32_16x16x32_bf16 a[16:19], a[76:79], v[48:51], a[16:19]\n\t" \
  "ds_read_b64_tr_b16 v[148:149], v237 offset:8192\n\t" \
  "v_mfma_f32_16x16x32_bf16 a[16:19], a[92:95], v[52:55], a[16:19]\n\t" \
  "s_waitcnt lgkmcnt(3)\n\t" \
  "ds_read_b64_tr_b16 v[150:151], v237 offset:12288\n\t" \
  "v_mfma_f32_16x16x32_bf16 a[16:19], a[108:111], v[56:59], a[16:19]\n\t" \
  "buffer_load_dwordx4 v[0:3], v242, %[ws], s73 offen offset:0 sc1\n\t" \
  "v_mfma_f32_16x16x32_bf16 a[16:19], a[124:127], v[60:63], a[16:19]\n\t" \
  "ds_read_b64_tr_b16 v[152:153], v237 offset:16384\n\t" \
  "ds_write_b128 v165, v[160:163]\n\t" \
  "v_mfma_f32_16x16x32_bf16 a[20:23], a[124:127], v[92:95], a[20:23]\n\t" \
  "ds_read_b64_tr_b16 v[154:155], v237 offset:20480\n\t" \
  "v_and_b32 v171, 15, v244\n\t" \
  "v_mfma_f32_16x16x32_bf16 a[20:23], a[108:111], v[88:91], a[20:23]\n\t" \
  "ds_read_b64_tr_b16 v[156:157], v237 offset:24576\n\t" \
  "v_lshrrev_b32 v172, 4, v244\n\t" \
  "v_mfma_f32_16x16x32_bf16 a[20:23], a[92:95], v[84:87], a[20:23]\n\t" \
  "buffer_load_dwordx4 v[4:7], v242, %[ws], s73 offen offset:1024 sc1\n\t" \
  "v_mfma_f32_16x16x32_bf16 a[20:23], a[76:79], v[80:83], a[20:23]\n\t" \
  "ds_read_b64_tr_b16 v[158:159], v237 offset:28672\n\t" \
  "v_lshlrev_b32 v170, 13, v172\n\t" \
  "v_mfma_f32_16x16x32_bf16 a[24:27], a[76:79], v[64:67], a[24:27]\n\t" \
  "v_xor_b32 v235, 0x60, v234\n\t" \
  "v_lshl_add_u32 v170, v171, 4, v170\n\t" \
  "v_mfma_f32_16x16x32_bf16 a[24:27], a[92:95], v[68:71], a[24:27]\n\t" \
  "ds_read_b64_tr_b16 v[128:129], v235 offset:0\n\t" \
  "v_lshlrev_b32 v174, 7, v172\n\t" \
  "v_mfma_f32_16x16x32_bf16 a[24:27], a[108:111], v[72:75], a[24:27]\n\t" \
  "buffer_load_dwordx4 v[8:11], v242, %[ws], s73 offen offset:2048 sc1\n\t" \
  "v_mfma_f32_16x16x32_bf16 a[24:27], a[124:127], v[76:79], a[24:27]\n\t" \
  "s_waitcnt lgkmcnt(6)\n\t" \
  "ds_read_b64_tr_b16 v[130:131], v235 offset:4096\n\t" \
  "v_lshl_add_u32 v174, v171, 3, v174\n\t" \
  "v_mfma_f32_16x16x32_bf16 a[32:35], v[144:147], v[48:51], a[32:35]\n\t" \
  "ds_read_b64_tr_b16 v[132:133], v235 offset:8192\n\t" \
  "v_add_u32 v174, s70, v174\n\t" \
  "v_mfma_f32_16x16x32_bf16 a[32:35], v[148:151], v[52:55], a[32:35]\n\t" \
  "s_waitcnt lgkmcnt(3)\n\t" \
  "ds_read_b64_tr_b16 v[134:135], v235 offset:12288\n\t" \
  "v_mfma_f32_16x16x32_bf16 a[32:35], v[152:155], v[56:59], a[32:35]\n\t" \
  "ds_read_b64_tr_b16 v[136:137], v235 offset:16384\n\t" \
  "v_mfma_f32_16x16x32_bf16 a[32:35], v[156:159], v[60:63], a[32:35]\n\t" \
  "buffer_load_dwordx4 v[12:15], v242, %[ws], s73 offen offset:3072 sc1\n\t" \
  "s_xor_b32 s73, %[eso], 720896\n\t" \
  "v_mfma_f32_16x16x32_bf16 a[36:39], v[156:159], v[92:95], a[36:39]\n\t" \
  "ds_read_b64_tr_b16 v[138:139], v235 offset:20480\n\t" \
  "v_mfma_f32_16x16x32_bf16 a[36:39], v[152:155], v[88:91], a[36:39]\n\t" \
  "ds_read_b64_tr_b16 v[140:141], v235 offset:24576\n\t" \
  "v_mfma_f32_16x16x32_bf16 a[36:39], v[148:151], v[84:87], a[36:39]\n\t" \
  "ds_read_b64_tr_b16 v[142:143], v235 offset:28672\n\t" \
  "ds_write_b128 v165, v[160:163] offset:1024\n\t" \
  "v_mfma_f32_16x16x32_bf16 a[36:39], v[144:147], v[80:83], a[36:39]\n\t" \
  "buffer_load_dwordx4 v[32:35], v242, %[ws], s73 offen offset:0 sc1\n\t" \
  "v_mfma_f32_16x16x32_bf16 a[40:43], v[144:147], v[64:67], a[40:43]\n\t" \
  "v_lshlrev_b32 v171, 2, v171\n\t" \
  "v_xor_b32 v236, 0x80, v234\n\t" \
  "v_mfma_f32_16x16x32_bf16 a[40:43], v[148:151], v[68:71], a[40:43]\n\t" \
  "v_cmp_eq_u32 vcc, 2, v172\n\t" \
  "s_nop 1\n\t" \
  "v_cndmask_b32 v172, 0, 1.0, vcc\n\t" \
  "v_mfma_f32_16x16x32_bf16 a[40:43], v[152:155], v[72:75], a[40:43]\n\t" \
  "ds_read_b64_tr_b16 a[208:209], v236 offset:0\n\t" \
  "v_mfma_f32_16x16x32_bf16 a[40:43], v[156:159], v[76:79], a[40:43]\n\t" \
  "s_waitcnt lgkmcnt(6)\n\t" \
  "ds_read_b64_tr_b16 a[210:211], v236 offset:4096\n\t" \
  "v_mfma_f32_16x16x32_bf16 a[48:51], v[128:131], v[48:51], a[48:51]\n\t" \
  "s_add_u32 m0, %[m], 114688\n\t" \
  "buffer_load_dwordx4 v[36:39], v242, %[ws], s73 offen offset:1024 sc1\n\t" \
  "v_mfma_f32_16x16x32_bf16 a[48:51], v[132:135], v[52:55], a[48:51]\n\t" \
  "s_waitcnt lgkmcnt(3)\n\t" \
  "ds_read_b64_tr_b16 a[212:213], v236 offset:8192\n\t" \
  "ds_write_b128 v165, v[160:163] offset:1536\n\t" \
  "v_mfma_f32_16x16x32_bf16 a[48:51], v[136:139], v[56:59], a[48:51]\n\t" \
  "ds_read_b64_tr_b16 a[214:215], v236 offset:12288\n\t" \
  "s_waitcnt lgkmcnt(1)\n\t" \
  "v_bfe_u32 v160, v244, 3, 1\n\t" \
  "v_mfma_f32_16x16x32_bf16 a[48:51], v[140:143], v[60:63], a[48:51]\n\t" \
  "s_barrier\n\t" \
  "v_mfma_f32_16x16x32_bf16 a[52:55], v[140:143], v[92:95], a[52:55]\n\t" \
  "buffer_load_dwordx4 v[40:43], v242, %[ws], s73 offen offset:2048 sc1\n\t" \
  "v_bfe_u32 v173, v244, 1, 2\n\t" \
  "v_mfma_f32_16x16x32_bf16 a[52:55], v[136:139], v[88:91], a[52:55]\n\t" \
  "s_xor_b32 s71, %[wob], 1703936\n\t" \
  "ds_read_b64_tr_b16 a[216:217], v236 offset:16384\n\t" \
  "ds_read_b64_tr_b16 a[218:219], v236 offset:20480\n\t" \
  "v_mfma_f32_16x16x32_bf16 a[52:55], v[132:135], v[84:87], a[52:55]\n\t" \
  "ds_read_b64_tr_b16 a[220:221], v236 offset:24576\n\t" \
  "v_lshlrev_b32 v173, 4, v173\n\t" \
  "v_mfma_f32_16x16x32_bf16 a[52:55], v[128:131], v[80:83], a[52:55]\n\t" \
  "ds_read_b64_tr_b16 a[222:223], v236 offset:28672\n\t" \
  "v_lshl_add_u32 v173, v160, 3, v173\n\t" \
  "v_mfma_f32_16x16x32_bf16 a[56:59], v[128:131], v[64:67], a[56:59]\n\t" \
  "buffer_load_dwordx4 v[44:47], v242, %[ws], s73 offen offset:3072 sc1\n\t" \
  "v_mfma_f32_16x16x32_bf16 a[56:59], v[132:135], v[68:71], a[56:59]\n\t" \
  "v_xor_b32 v237, 0xa0, v234\n\t" \
  "v_and_b32 v160, 1, v244\n\t" \
  "v_mfma_f32_16x16x32_bf16 a[56:59], v[136:139], v[72:75], a[56:59]\n\t" \
  "ds_read_b64_tr_b16 v[144:145], v237 offset:0\n\t" \
  "v_lshl_add_u32 v173, v160, 2, v173\n\t" \
  "v_mfma_f32_16x16x32_bf16 a[56:59], v[140:143], v[76:79], a[56:59]\n\t" \
  "s_waitcnt lgkmcnt(5)\n\t" \
  "ds_read_b64_tr_b16 v[146:147], v237 offset:4096\n\t" \
  "v_add_u32 v173, s70, v173\n\t" \
  "v_mfma_f32_16x16x32_bf16 a[64:67], a[208:211], v[48:51], a[64:67]\n\t" \
  "ds_read_b64_tr_b16 v[148:149], v237 offset:8192\n\t" \
  "v_mfma_f32_16x16x32_bf16 a[64:67], a[212:215], v[52:55], a[64:67]\n\t" \
  "s_waitcnt lgkmcnt(3)\n\t" \
  "buffer_load_dwordx4 v240, %[wo], s71 offen offset:0 lds\n\t" \
  "v_mfma_f32_16x16x32_bf16 a[64:67], a[216:219], v[56:59], a[64:67]\n\t" \
  "ds_read_b64_tr_b16 v[150:151], v237 offset:12288\n\t" \
  "v_mfma_f32_16x16x32_bf16 a[64:67], a[220:223], v[60:63], a[64:67]\n\t" \
  "ds_read_b64_tr_b16 v[152:153], v237 offset:16384\n\t" \
  "v_mfma_f32_16x16x32_bf16 a[68:71], a[220:223], v[92:95], a[68:71]\n\t" \
  "ds_read_b64_tr_b16 v[154:155], v237 offset:20480\n\t" \
  "v_mfma_f32_16x16x32_bf16 a[68:71], a[216:219], v[88:91], a[68:71]\n\t" \
  "buffer_load_dwordx4 v241, %[wo], s71 offen offset:1024 lds\n\t" \
  "v_mfma_f32_16x16x32_bf16 a[68:71], a[212:215], v[84:87], a[68:71]\n\t" \
  "s_mov_b64 exec, 0xffff\n\t" \
  "ds_write_b32 v173, v164 offset:960\n\t" \
  "s_mov_b64 exec, -1\n\t" \
  "v_mfma_f32_16x16x32_bf16 a[68:71], a[208:211], v[80:83], a[68:71]\n\t" \
  "ds_read_b64_tr_b16 v[156:157], v237 offset:24576\n\t" \
  "ds_read_b64_tr_b16 v[158:159], v237 offset:28672\n\t" \
  "v_lshrrev_b32 v229, 4, v244\n\t" \
  "v_mfma_f32_16x16x32_bf16 a[72:75], a[208:211], v[64:67], a[72:75]\n\t" \
  "v_and_b32 v231, 15, v244\n\t" \
  "v_xor_b32 v248, v231, v229\n\t" \
  "v_mfma_f32_16x16x32_bf16 a[72:75], a[212:215], v[68:71], a[72:75]\n\t" \
  "v_lshlrev_b32 v248, 4, v248\n\t" \
  "v_xor_b32 v235, 0xc0, v234\n\t" \
  "v_mfma_f32_16x16x32_bf16 a[72:75], a[216:219], v[72:75], a[72:75]\n\t" \
  "buffer_load_dwordx4 v238, %[wo], s71 offen offset:2048 lds\n\t" \
  "v_lshl_add_u32 v248, v229, 11, v248\n\t" \
  "v_mfma_f32_16x16x32_bf16 a[72:75], a[220:223], v[76:79], a[72:75]\n\t" \
  "s_waitcnt lgkmcnt(5)\n\t" \
  "ds_read_b64_tr_b16 a[192:193], v235 offset:0\n\t" \
  "v_xor_b32 v249, 64, v248\n\t" \
  "v_mfma_f32_16x16x32_bf16 a[80:83], v[144:147], v[48:51], a[80:83]\n\t" \
  "ds_read_b64_tr_b16 a[194:195], v235 offset:4096\n\t" \
  "v_xor_b32 v250, 0x80, v248\n\t" \
  "v_mfma_f32_16x16x32_bf16 a[80:83], v[148:151], v[52:55], a[80:83]\n\t" \
  "s_waitcnt lgkmcnt(2)\n\t" \
  "ds_read_b64_tr_b16 a[196:197], v235 offset:8192\n\t" \
  "v_xor_b32 v251, 0xc0, v248\n\t" \
  "v_mfma_f32_16x16x32_bf16 a[80:83], v[152:155], v[56:59], a[80:83]\n\t" \
  "buffer_load_dwordx4 v239, %[wo], s71 offen offset:3072 lds\n\t" \
  "s_add_u32 m0, %[m], 131072\n\t" \
  "v_mfma_f32_16x16x32_bf16 a[80:83], v[156:159], v[60:63], a[80:83]\n\t" \
  "s_xor_b32 s71, %[wob], 1835008\n\t" \
  "ds_read_b64_tr_b16 a[198:199], v235 offset:12288\n\t" \
  "v_lshrrev_b32 v227, 5, v244\n\t" \
  "s_lshl_b32 s75, %[wid], 2\n\t" \
  "v_mfma_f32_16x16x32_bf16 a[84:87], v[156:159], v[92:95], a[84:87]\n\t" \
  "ds_read_b64_tr_b16 a[200:201], v235 offset:16384\n\t" \
  "v_add_u32 v227, s75, v227\n\t" \
  "v_mfma_f32_16x16x32_bf16 a[84:87], v[152:155], v[88:91], a[84:87]\n\t" \
  "ds_read_b64_tr_b16 a[202:203], v235 offset:20480\n\t" \
  "v_xor_b32 v227, v227, v231\n\t" \
  "v_mfma_f32_16x16x32_bf16 a[84:87], v[148:151], v[84:87], a[84:87]\n\t" \
  "buffer_load_dwordx4 v240, %[wo], s71 offen offset:0 lds\n\t" \
  "v_mfma_f32_16x16x32_bf16 a[84:87], v[144:147], v[80:83], a[84:87]\n\t" \
  "ds_read_b64_tr_b16 a[204:205], v235 offset:24576\n\t" \
  "ds_read_b64_tr_b16 a[206:207], v235 offset:28672\n\t" \
  "v_mfma_f32_16x16x32_bf16 a[88:91], v[144:147], v[64:67], a[88:91]\n\t" \
  "v_xor_b32 v236, 0xe0, v234\n\t" \
  "v_lshlrev_b32 v227, 4, v227\n\t" \
  "v_mfma_f32_16x16x32_bf16 a[88:91], v[148:151], v[68:71], a[88:91]\n\t" \
  "ds_read_b64_tr_b16 a[208:209], v236 offset:0\n\t" \
  "v_lshl_add_u32 v227, v231, 8, v227\n\t" \
  "v_mfma_f32_16x16x32_bf16 a[88:91], v[152:155], v[72:75], a[88:91]\n\t" \
  "ds_read_b64_tr_b16 a[210:211], v236 offset:4096\n\t" \
  "v_and_b32 v229, 1, v229\n\t" \
  "v_mfma_f32_16x16x32_bf16 a[88:91], v[156:159], v[76:79], a[88:91]\n\t" \
  "s_waitcnt lgkmcnt(6)\n\t" \
  "buffer_load_dwordx4 v241, %[wo], s71 offen offset:1024 lds\n\t" \
  "v_mfma_f32_16x16x32_bf16 a[96:99], a[192:195], v[48:51], a[96:99]\n\t" \
  "ds_read_b64_tr_b16 a[212:213], v236 offset:8192\n\t" \
  "v_perm_b32 v254, v180, v176, s79\n\t" \
  "v_mfma_f32_16x16x32_bf16 a[96:99], a[196:199], v[52:55], a[96:99]\n\t" \
  "s_waitcnt lgkmcnt(3)\n\t" \
  "ds_read_b64_tr_b16 a[214:215], v236 offset:12288\n\t" \
  "v_perm_b32 v180, v180, v176, s80\n\t" \
  "v_mfma_f32_16x16x32_bf16 a[96:99], a[200:203], v[56:59], a[96:99]\n\t" \
  "ds_read_b64_tr_b16 a[216:217], v236 offset:16384\n\t" \
  "v_lshl_add_u32 v227, v229, 3, v227\n\t" \
  "v_mfma_f32_16x16x32_bf16 a[96:99], a[204:207], v[60:63], a[96:99]\n\t" \
  "buffer_load_dwordx4 v238, %[wo], s71 offen offset:2048 lds\n\t" \
  "v_mfma_f32_16x16x32_bf16 a[100:103], a[204:207], v[92:95], a[100:103]\n\t" \
  "ds_read_b64_tr_b16 a[218:219], v236 offset:20480\n\t" \
  "v_add_u32 v231, 131072, v227\n\t" \
  "v_mfma_f32_16x16x32_bf16 a[100:103], a[200:203], v[88:91], a[100:103]\n\t" \
  "ds_read_b64_tr_b16 a[220:221], v236 offset:24576\n\t" \
  "v_perm_b32 v176, v181, v177, s79\n\t" \
  "v_mfma_f32_16x16x32_bf16 a[100:103], a[196:199], v[84:87], a[100:103]\n\t" \
  "ds_read_b64_tr_b16 a[222:223], v236 offset:28672\n\t" \
  "v_xor_b32 v233, 32, v231\n\t" \
  "v_mfma_f32_16x16x32_bf16 a[100:103], a[192:195], v[80:83], a[100:103]\n\t" \
  "s_lshl_b32 s75, %[wid], 9\n\t" \
  "v_perm_b32 v181, v181, v177, s80\n\t" \
  "v_lshl_add_u32 v252, v244, 3, s75\n\t" \
  "v_mfma_f32_16x16x32_bf16 a[104:107], a[192:195], v[64:67], a[104:107]\n\t" \
  "buffer_load_dwordx4 v239, %[wo], s71 offen offset:3072 lds\n\t" \
  "v_mfma_f32_16x16x32_bf16 a[104:107], a[196:199], v[68:71], a[104:107]\n\t" \
  "v_xor_b32 v237, 0x18000, v234\n\t" \
  "v_perm_b32 v177, v182, v178, s79\n\t" \
  "v_mfma_f32_16x16x32_bf16 a[104:107], a[200:203], v[72:75], a[104:107]\n\t" \
  "ds_read_b64_tr_b16 v[128:129], v237 offset:0\n\t" \
  "v_mfma_f32_16x16x32_bf16 a[104:107], a[204:207], v[76:79], a[104:107]\n\t" \
  "s_waitcnt lgkmcnt(5)\n\t" \
  "ds_read_b64_tr_b16 v[130:131], v237 offset:4096\n\t" \
  "v_perm_b32 v182, v182, v178, s80\n\t" \
  "v_mfma_f32_16x16x32_bf16 a[112:115], a[208:211], v[48:51], a[112:115]\n\t" \
  "ds_read_b64_tr_b16 v[132:133], v237 offset:8192\n\t" \
  "v_mfma_f32_16x16x32_bf16 a[112:115], a[212:215], v[52:55], a[112:115]\n\t" \
  "s_waitcnt lgkmcnt(3)\n\t" \
  "ds_read_b64_tr_b16 v[134:135], v237 offset:12288\n\t" \
  "v_xor_b32 v235, 0x8000, v234\n\t" \
  "v_mfma_f32_16x16x32_bf16 a[112:115], a[216:219], v[56:59], a[112:115]\n\t" \
  "ds_read_b64_tr_b16 v[136:137], v235 offset:0\n\t" \
  "v_mfma_f32_16x16x32_bf16 a[112:115], a[220:223], v[60:63], a[112:115]\n\t" \
  "ds_read_b64_tr_b16 v[138:139], v235 offset:4096\n\t" \
  "v_perm_b32 v178, v183, v179, s79\n\t" \
  "v_mfma_f32_16x16x32_bf16 a[116:119], a[220:223], v[92:95], a[116:119]\n\t" \
  "ds_read_b64_tr_b16 v[140:141], v235 offset:8192\n\t" \
  "v_mfma_f32_16x16x32_bf16 a[116:119], a[216:219], v[88:91], a[116:119]\n\t" \
  "ds_read_b64_tr_b16 v[142:143], v235 offset:12288\n\t" \
  "v_mfma_f32_16x16x32_bf16 a[116:119], a[212:215], v[84:87], a[116:119]\n\t" \
  "v_mfma_f32_16x16x32_bf16 a[116:119], a[208:211], v[80:83], a[116:119]\n\t" \
  "s_xor_b32 s73, %[eso], 786432\n\t" \
  "v_perm_b32 v183, v183, v179, s80\n\t" \
  "v_mfma_f32_16x16x32_bf16 a[120:123], a[208:211], v[64:67], a[120:123]\n\t" \
  "v_xor_b32 v236, 0x18020, v234\n\t" \
  "v_mfma_f32_16x16x32_bf16 a[120:123], a[212:215], v[68:71], a[120:123]\n\t" \
  "ds_read_b64_tr_b16 v[144:145], v236 offset:0\n\t" \
  "v_mfma_f32_16x16x32_bf16 a[120:123], a[216:219], v[72:75], a[120:123]\n\t" \
  "ds_read_b64_tr_b16 v[146:147], v236 offset:4096\n\t" \
  "v_perm_b32 v179, v188, v184, s79\n\t" \
  "v_mfma_f32_16x16x32_bf16 a[120:123], a[220:223], v[76:79], a[120:123]\n\t" \
  "s_waitcnt vmcnt(16) lgkmcnt(6)\n\t" \
  "buffer_load_dwordx4 v[48:51], v242, %[ws], s73 offen offset:0 sc1\n\t" \
  "v_mfma_f32_16x16x32_bf16 a[0:3], v[128:131], v[16:19], a[0:3]\n\t" \
  "ds_read_b64_tr_b16 v[148:149], v236 offset:8192\n\t" \
  "v_mfma_f32_16x16x32_bf16 a[0:3], v[132:135], v[20:23], a[0:3]\n\t" \
  "s_waitcnt lgkmcnt(3)\n\t" \
  "buffer_load_dwordx4 v[52:55], v242, %[ws], s73 offen offset:1024 sc1\n\t" \
  "v_mfma_f32_16x16x32_bf16 a[0:3], v[136:139], v[24:27], a[0:3]\n\t" \
  "ds_read_b64_tr_b16 v[150:151], v236 offset:12288\n\t" \
  "v_xor_b32 v237, 0x8020, v234\n\t" \
  "v_mfma_f32_16x16x32_bf16 a[0:3], v[140:143], v[28:31], a[0:3]\n\t" \
  "s_waitcnt vmcnt(14)\n\t" \
  "ds_read_b64_tr_b16 v[152:153], v237 offset:0\n\t" \
  "v_perm_b32 v188, v188, v184, s80\n\t" \
  "v_mfma_f32_16x16x32_bf16 a[4:7], v[140:143], v[12:15], a[4:7]\n\t" \
  "ds_read_b64_tr_b16 v[154:155], v237 offset:4096\n\t" \
  "v_mfma_f32_16x16x32_bf16 a[4:7], v[136:139], v[8:11], a[4:7]\n\t" \
  "buffer_load_dwordx4 v[56:59], v242, %[ws], s73 offen offset:2048 sc1\n\t" \
  "v_mfma_f32_16x16x32_bf16 a[4:7], v[132:135], v[4:7], a[4:7]\n\t" \
  "ds_read_b64_tr_b16 v[156:157], v237 offset:8192\n\t" \
  "v_perm_b32 v184, v189, v185, s79\n\t" \
  "v_mfma_f32_16x16x32_bf16 a[4:7], v[128:131], v[0:3], a[4:7]\n\t" \
  "s_waitcnt vmcnt(11)\n\t" \
  "ds_read_b64_tr_b16 v[158:159], v237 offset:12288\n\t" \
  "v_mfma_f32_16x16x32_bf16 a[8:11], v[128:131], v[32:35], a[8:11]\n\t" \
  "v_xor_b32 v235, 0x18040, v234\n\t" \
  "v_mfma_f32_16x16x32_bf16 a[8:11], v[132:135], v[36:39], a[8:11]\n\t" \
  "buffer_load_dwordx4 v[60:63], v242, %[ws], s73 offen offset:3072 sc1\n\t" \
  "s_xor_b32 s73, %[eso], 819200\n\t" \
  "v_mfma_f32_16x16x32_bf16 a[8:11], v[136:139], v[40:43], a[8:11]\n\t" \
  "ds_read_b64_tr_b16 a[208:209], v235 offset:0\n\t" \
  "v_perm_b32 v189, v189, v185, s80\n\t" \
  "v_mfma_f32_16x16x32_bf16 a[8:11], v[140:143], v[44:47], a[8:11]\n\t" \
  "s_waitcnt lgkmcnt(5)\n\t" \
  "ds_read_b64_tr_b16 a[210:211], v235 offset:4096\n\t" \
  "buffer_load_dword v246, v245, %[ws], %[fw2] offen sc1\n\t" \
  "v_mfma_f32_16x16x32_bf16 a[16:19], v[144:147], v[16:19], a[16:19]\n\t" \
  "ds_read_b64_tr_b16 a[212:213], v235 offset:8192\n\t" \
  "v_mfma_f32_16x16x32_bf16 a[16:19], v[148:151], v[20:23], a[16:19]\n\t" \
  "s_waitcnt lgkmcnt(3)\n\t" \
  "ds_read_b64_tr_b16 a[214:215], v235 offset:12288\n\t" \
  "v_xor_b32 v236, 0x8040, v234\n\t" \
  "v_mfma_f32_16x16x32_bf16 a[16:19], v[152:155], v[24:27], a[16:19]\n\t" \
  "ds_read_b64_tr_b16 a[216:217], v236 offset:0\n\t" \
  "v_perm_b32 v185, v190, v186, s79\n\t" \
  "v_mfma_f32_16x16x32_bf16 a[16:19], v[156:159], v[28:31], a[16:19]\n\t" \
  "buffer_load_dwordx4 v[80:83], v242, %[ws], s73 offen offset:0 sc1\n\t" \
  "v_mfma_f32_16x16x32_bf16 a[20:23], v[156:159], v[12:15], a[20:23]\n\t" \
  "ds_read_b64_tr_b16 a[218:219], v236 offset:4096\n\t" \
  "v_mfma_f32_16x16x32_bf16 a[20:23], v[152:155], v[8:11], a[20:23]\n\t" \
  "ds_read_b64_tr_b16 a[220:221], v236 offset:8192\n\t" \
  "v_perm_b32 v190, v190, v186, s80\n\t" \
  "v_mfma_f32_16x16x32_bf16 a[20:23], v[148:151], v[4:7], a[20:23]\n\t" \
  "ds_read_b64_tr_b16 a[222:223], v236 offset:12288\n\t" \
  "v_mfma_f32_16x16x32_bf16 a[20:23], v[144:147], v[0:3], a[20:23]\n\t" \
  "buffer_load_dwordx4 v[84:87], v242, %[ws], s73 offen offset:1024 sc1\n\t" \
  "v_mfma_f32_16x16x32_bf16 a[24:27], v[144:147], v[32:35], a[24:27]\n\t" \
  "v_xor_b32 v237, 0x18060, v234\n\t" \
  "v_mfma_f32_16x16x32_bf16 a[24:27], v[148:151], v[36:39], a[24:27]\n\t" \
  "ds_read_b64_tr_b16 a[192:193], v237 offset:0\n\t" \
  "v_perm_b32 v186, v191, v187, s79\n\t" \
  "v_mfma_f32_16x16x32_bf16 a[24:27], v[152:155], v[40:43], a[24:27]\n\t" \
  "ds_read_b64_tr_b16 a[194:195], v237 offset:4096\n\t" \
  "v_mfma_f32_16x16x32_bf16 a[24:27], v[156:159], v[44:47], a[24:27]\n\t" \
  "s_waitcnt lgkmcnt(6)\n\t" \
  "ds_read_b64_tr_b16 a[196:197], v237 offset:8192\n\t" \
  "v_mfma_f32_16x16x32_bf16 a[32:35], a[208:211], v[16:19], a[32:35]\n\t" \
  "buffer_load_dwordx4 v[88:91], v242, %[ws], s73 offen offset:2048 sc1\n\t" \
  "v_mfma_f32_16x16x32_bf16 a[32:35], a[212:215], v[20:23], a[32:35]\n\t" \
  "s_waitcnt lgkmcnt(3)\n\t" \
  "ds_read_b64_tr_b16 a[198:199], v237 offset:12288\n\t" \
  "v_xor_b32 v235, 0x8060, v234\n\t" \
  "v_mfma_f32_16x16x32_bf16 a[32:35], a[216:219], v[24:27], a[32:35]\n\t" \
  "ds_read_b64_tr_b16 a[200:201], v235 offset:0\n\t" \
  "v_perm_b32 v191, v191, v187, s80\n\t" \
  "v_mfma_f32_16x16x32_bf16 a[32:35], a[220:223], v[28:31], a[32:35]\n\t" \
  "ds_read_b64_tr_b16 a[202:203], v235 offset:4096\n\t" \
  "v_mfma_f32_16x16x32_bf16 a[36:39], a[220:223], v[12:15], a[36:39]\n\t" \
  "buffer_load_dwordx4 v[92:95], v242, %[ws], s73 offen offset:3072 sc1\n\t" \
  "s_xor_b32 s73, %[eso], 851968\n\t" \
  "v_mfma_f32_16x16x32_bf16 a[36:39], a[216:219], v[8:11], a[36:39]\n\t" \
  "ds_read_b64_tr_b16 a[204:205], v235 offset:8192\n\t" \
  "v_perm_b32 v187, v196, v192, s79\n\t" \
  "v_mfma_f32_16x16x32_bf16 a[36:39], a[212:215], v[4:7], a[36:39]\n\t" \
  "ds_read_b64_tr_b16 a[206:207], v235 offset:12288\n\t" \
  "v_mfma_f32_16x16x32_bf16 a[36:39], a[208:211], v[0:3], a[36:39]\n\t" \
  "v_mfma_f32_16x16x32_bf16 a[40:43], a[208:211], v[32:35], a[40:43]\n\t" \
  "buffer_load_dwordx4 v[64:67], v242, %[ws], s73 offen offset:0 sc1\n\t" \
  "v_mfma_f32_16x16x32_bf16 a[40:43], a[212:215], v[36:39], a[40:43]\n\t" \
  "v_perm_b32 v196, v196, v192, s80\n\t" \
  "v_mfma_f32_16x16x32_bf16 a[40:43], a[216:219], v[40:43], a[40:43]\n\t" \
  "v_mfma_f32_16x16x32_bf16 a[40:43], a[220:223], v[44:47], a[40:43]\n\t" \
  "s_waitcnt lgkmcnt(4)\n\t" \
  "v_xor_b32 v236, 0x18080, v234\n\t" \
  "v_mfma_f32_16x16x32_bf16 a[48:51], a[192:195], v[16:19], a[48:51]\n\t" \
  "s_add_u32 m0, %[m], 0\n\t" \
  "ds_read_b64_tr_b16 a[208:209], v236 offset:0\n\t" \
  "v_perm_b32 v192, v197, v193, s79\n\t" \
  "v_mfma_f32_16x16x32_bf16 a[48:51], a[196:199], v[20:23], a[48:51]\n\t" \
  "s_waitcnt lgkmcnt(1)\n\t" \
  "buffer_load_dwordx4 v[68:71], v242, %[ws], s73 offen offset:1024 sc1\n\t" \
  "v_mfma_f32_16x16x32_bf16 a[48:51], a[200:203], v[24:27], a[48:51]\n\t" \
  "ds_read_b64_tr_b16 a[210:211], v236 offset:4096\n\t" \
  "v_mfma_f32_16x16x32_bf16 a[48:51], a[204:207], v[28:31], a[48:51]\n\t" \
  "s_waitcnt vmcnt(15)\n\t" \
  "s_barrier\n\t" \
  "v_mfma_f32_16x16x32_bf16 a[52:55], a[204:207], v[12:15], a[52:55]\n\t" \
  "s_xor_b32 s71, %[wob], 1966080\n\t" \
  "ds_read_b64_tr_b16 a[212:213], v236 offset:8192\n\t" \
  "v_perm_b32 v197, v197, v193, s80\n\t" \
  "v_mfma_f32_16x16x32_bf16 a[52:55], a[200:203], v[8:11], a[52:55]\n\t" \
  "buffer_load_dwordx4 v[72:75], v242, %[ws], s73 offen offset:2048 sc1\n\t" \
  "v_mfma_f32_16x16x32_bf16 a[52:55], a[196:199], v[4:7], a[52:55]\n\t" \
  "ds_read_b64_tr_b16 a[214:215], v236 offset:12288\n\t" \
  "v_xor_b32 v237, 0x8080, v234\n\t" \
  "v_mfma_f32_16x16x32_bf16 a[52:55], a[192:195], v[0:3], a[52:55]\n\t" \
  "ds_read_b64_tr_b16 a[216:217], v237 offset:0\n\t" \
  "v_perm_b32 v193, v198, v194, s79\n\t" \
  "v_mfma_f32_16x16x32_bf16 a[56:59], a[192:195], v[32:35], a[56:59]\n\t" \
  "ds_read_b64_tr_b16 a[218:219], v237 offset:4096\n\t" \
  "v_mfma_f32_16x16x32_bf16 a[56:59], a[196:199], v[36:39], a[56:59]\n\t" \
  "ds_read_b64_tr_b16 a[220:221], v237 offset:8192\n\t" \
  "v_mfma_f32_16x16x32_bf16 a[56:59], a[200:203], v[40:43], a[56:59]\n\t" \
  "buffer_load_dwordx4 v[76:79], v242, %[ws], s73 offen offset:3072 sc1\n\t" \
  "ds_read_b64_tr_b16 a[222:223], v237 offset:12288\n\t" \
  "v_mfma_f32_16x16x32_bf16 a[56:59], a[204:207], v[44:47], a[56:59]\n\t" \
  "s_waitcnt lgkmcnt(4)\n\t" \
  "v_xor_b32 v235, 0x180a0, v234\n\t" \
  "v_perm_b32 v198, v198, v194, s80\n\t" \
  "v_mfma_f32_16x16x32_bf16 a[64:67], a[208:211], v[16:19], a[64:67]\n\t" \
  "s_add_u32 s75, s69, 0\n\t" \
  "ds_read_b64_tr_b16 a[192:193], v235 offset:0\n\t" \
  "v_mfma_f32_16x16x32_bf16 a[64:67], a[212:215], v[20:23], a[64:67]\n\t" \
  "s_waitcnt lgkmcnt(1)\n\t" \
  "s_waitcnt vmcnt(8)\n\t" \
  "v_cmp_ne_u32 vcc, %[tag], v246\n\t" \
  "s_and_b32 vcc_lo, vcc_lo, 0xfe0000\n\t" \
  "s_cbranch_scc1 .Lop_s3\n\t" \
  ".Lop_k3:\n\t" \
  "v_mfma_f32_16x16x32_bf16 a[64:67], a[216:219], v[24:27], a[64:67]\n\t" \
  "buffer_load_dwordx4 v240, %[wo], s71 offen offset:0 lds\n\t" \
  "v_mfma_f32_16x16x32_bf16 a[64:67], a[220:223], v[28:31], a[64:67]\n\t" \
  "ds_read_b64_tr_b16 a[194:195], v235 offset:4096\n\t" \
  "v_perm_b32 v194, v199, v195, s79\n\t" \
  "v_mfma_f32_16x16x32_bf16 a[68:71], a[220:223], v[12:15], a[68:71]\n\t" \
  "ds_read_b64_tr_b16 a[196:197], v235 offset:8192\n\t" \
  "v_mfma_f32_16x16x32_bf16 a[68:71], a[216:219], v[8:11], a[68:71]\n\t" \
  "ds_read_b64_tr_b16 a[198:199], v235 offset:12288\n\t" \
  "v_xor_b32 v236, 0x80a0, v234\n\t" \
  "v_mfma_f32_16x16x32_bf16 a[68:71], a[212:215], v[4:7], a[68:71]\n\t" \
  "buffer_load_dwordx4 v[208:211], v170, %[ws], s75 offen sc1\n\t" \
  "v_mfma_f32_16x16x32_bf16 a[68:71], a[208:211], v[0:3], a[68:71]\n\t" \
  "s_add_u32 s75, s69, 32768\n\t" \
  "ds_read_b64_tr_b16 a[200:201], v236 offset:0\n\t" \
  "v_perm_b32 v199, v199, v195, s80\n\t" \
  "v_mfma_f32_16x16x32_bf16 a[72:75], a[208:211], v[32:35], a[72:75]\n\t" \
  "ds_read_b64_tr_b16 a[202:203], v236 offset:4096\n\t" \
  "v_mfma_f32_16x16x32_bf16 a[72:75], a[212:215], v[36:39], a[72:75]\n\t" \
  "ds_read_b64_tr_b16 a[204:205], v236 offset:8192\n\t" \
  "v_mfma_f32_16x16x32_bf16 a[72:75], a[216:219], v[40:43], a[72:75]\n\t" \
  "ds_read_b64_tr_b16 a[206:207], v236 offset:12288\n\t" \
  "v_perm_b32 v195, v204, v200, s79\n\t" \
  "v_mfma_f32_16x16x32_bf16 a[72:75], a[220:223], v[44:47], a[72:75]\n\t" \
  "s_waitcnt lgkmcnt(4)\n\t" \
  "buffer_load_dwordx4 v[212:215], v170, %[ws], s75 offen sc1\n\t" \
  "v_mfma_f32_16x16x32_bf16 a[80:83], a[192:195], v[16:19], a[80:83]\n\t" \
  "s_add_u32 s75, s69, 65536\n\t" \
  "v_xor_b32 v237, 0x180c0, v234\n\t" \
  "v_mfma_f32_16x16x32_bf16 a[80:83], a[196:199], v[20:23], a[80:83]\n\t" \
  "s_waitcnt lgkmcnt(0)\n\t" \
  "ds_read_b64_tr_b16 a[208:209], v237 offset:0\n\t" \
  "v_mfma_f32_16x16x32_bf16 a[80:83], a[200:203], v[24:27], a[80:83]\n\t" \
  "ds_read_b64_tr_b16 a[210:211], v237 offset:4096\n\t" \
  "v_perm_b32 v204, v204, v200, s80\n\t" \
  "v_mfma_f32_16x16x32_bf16 a[80:83], a[204:207], v[28:31], a[80:83]\n\t" \
  "buffer_load_dwordx4 v[216:219], v170, %[ws], s75 offen sc1\n\t" \
  "v_mfma_f32_16x16x32_bf16 a[84:87], a[204:207], v[12:15], a[84:87]\n\t" \
  "s_add_u32 s75, s69, 98304\n\t" \
  "ds_read_b64_tr_b16 a[212:213], v237 offset:8192\n\t" \
  "v_mfma_f32_16x16x32_bf16 a[84:87], a[200:203], v[8:11], a[84:87]\n\t" \
  "ds_read_b64_tr_b16 a[214:215], v237 offset:12288\n\t" \
  "v_xor_b32 v235, 0x80c0, v234\n\t" \
  "v_mfma_f32_16x16x32_bf16 a[84:87], a[196:199], v[4:7], a[84:87]\n\t" \
  "ds_read_b64_tr_b16 a[216:217], v235 offset:0\n\t" \
  "v_perm_b32 v200, v205, v201, s79\n\t" \
  "v_mfma_f32_16x16x32_bf16 a[84:87], a[192:195], v[0:3], a[84:87]\n\t" \
  "ds_read_b64_tr_b16 a[218:219], v235 offset:4096\n\t" \
  "v_mfma_f32_16x16x32_bf16 a[88:91], a[192:195], v[32:35], a[88:91]\n\t" \
  "buffer_load_dwordx4 v[220:223], v170, %[ws], s75 offen sc1\n\t" \
  "v_mfma_f32_16x16x32_bf16 a[88:91], a[196:199], v[36:39], a[88:91]\n\t" \
  "ds_read_b64_tr_b16 a[220:221], v235 offset:8192\n\t" \
  "v_mfma_f32_16x16x32_bf16 a[88:91], a[200:203], v[40:43], a[88:91]\n\t" \
  "ds_read_b64_tr_b16 a[222:223], v235 offset:12288\n\t" \
  "v_perm_b32 v205, v205, v201, s80\n\t" \
  "v_mfma_f32_16x16x32_bf16 a[88:91], a[204:207], v[44:47], a[88:91]\n\t" \
  "s_waitcnt lgkmcnt(4)\n\t" \
  "v_xor_b32 v236, 0x180e0, v234\n\t" \
  "v_mfma_f32_16x16x32_bf16 a[96:99], a[208:211], v[16:19], a[96:99]\n\t" \
  "buffer_load_dwordx4 v241, %[wo], s71 offen offset:1024 lds\n\t" \
  "v_mfma_f32_16x16x32_bf16 a[96:99], a[212:215], v[20:23], a[96:99]\n\t" \
  "s_waitcnt lgkmcnt(0)\n\t" \
  "ds_read_b64_tr_b16 a[192:193], v236 offset:0\n\t" \
  "v_perm_b32 v201, v206, v202, s79\n\t" \
  "v_mfma_f32_16x16x32_bf16 a[96:99], a[216:219], v[24:27], a[96:99]\n\t" \
  "ds_read_b64_tr_b16 a[194:195], v236 offset:4096\n\t" \
  "v_mfma_f32_16x16x32_bf16 a[96:99], a[220:223], v[28:31], a[96:99]\n\t" \
  "ds_read_b64_tr_b16 a[196:197], v236 offset:8192\n\t" \
  "v_mfma_f32_16x16x32_bf16 a[100:103], a[220:223], v[12:15], a[100:103]\n\t" \
  "buffer_load_dwordx4 v238, %[wo], s71 offen offset:2048 lds\n\t" \
  "v_mfma_f32_16x16x32_bf16 a[100:103], a[216:219], v[8:11], a[100:103]\n\t" \
  "ds_read_b64_tr_b16 a[198:199], v236 offset:12288\n\t" \
  "v_xor_b32 v237, 0x80e0, v234\n\t" \
  "v_mfma_f32_16x16x32_bf16 a[100:103], a[212:215], v[4:7], a[100:103]\n\t" \
  ".Lhs13:\n\ts_cbranch_execz .Lhs13n\n\t" \
  "ds_read_b64_tr_b16 a[200:201], v237 offset:0\n\t" \
  "v_perm_b32 v206, v206, v202, s80\n\t" \
  "v_mfma_f32_16x16x32_bf16 a[100:103], a[208:211], v[0:3], a[100:103]\n\t" \
  "ds_read_b64_tr_b16 a[202:203], v237 offset:4096\n\t" \
  "v_mfma_f32_16x16x32_bf16 a[104:107], a[208:211], v[32:35], a[104:107]\n\t" \
  "ds_read_b64_tr_b16 a[204:205], v237 offset:8192\n\t" \
  "v_mfma_f32_16x16x32_bf16 a[104:107], a[212:215], v[36:39], a[104:107]\n\t" \
  "buffer_load_dwordx4 v239, %[wo], s71 offen offset:3072 lds\n\t" \
  "s_add_u32 m0, %[m], 16384\n\t" \
  "buffer_load_dword v224, v171, %[sr], s77 offen\n\t" \
  "v_mfma_f32_16x16x32_bf16 a[104:107], a[216:219], v[40:43], a[104:107]\n\t" \
  "s_xor_b32 s71, %[eso], 1015808\n\t" \
  "ds_read_b64_tr_b16 a[206:207], v237 offset:12288\n\t" \
  "v_perm_b32 v202, v207, v203, s79\n\t" \
  "v_mfma_f32_16x16x32_bf16 a[104:107], a[220:223], v[44:47], a[104:107]\n\t" \
  "s_waitcnt lgkmcnt(4)\n\t" \
  "v_xor_b32 v235, 0x14000, v234\n\t" \
  "v_mfma_f32_16x16x32_bf16 a[112:115], a[192:195], v[16:19], a[112:115]\n\t" \
  "ds_read_b64_tr_b16 a[208:209], v235 offset:0\n\t" \
  "v_mfma_f32_16x16x32_bf16 a[112:115], a[196:199], v[20:23], a[112:115]\n\t" \
  "s_waitcnt lgkmcnt(1)\n\t" \
  "ds_read_b64_tr_b16 a[210:211], v235 offset:4096\n\t" \
  "v_perm_b32 v207, v207, v203, s80\n\t" \
  "v_mfma_f32_16x16x32_bf16 a[112:115], a[200:203], v[24:27], a[112:115]\n\t" \
  "ds_read_b64_tr_b16 a[212:213], v235 offset:8192\n\t" \
  "v_mfma_f32_16x16x32_bf16 a[112:115], a[204:207], v[28:31], a[112:115]\n\t" \
  "buffer_load_dwordx4 v242, %[ws], s71 offen offset:0 sc1 lds\n\t" \
  "v_mfma_f32_16x16x32_bf16 a[116:119], a[204:207], v[12:15], a[116:119]\n\t" \
  "ds_read_b64_tr_b16 a[214:215], v235 offset:12288\n\t" \
  "v_mfma_f32_16x16x32_bf16 a[116:119], a[200:203], v[8:11], a[116:119]\n\t" \
  "ds_read_b64_tr_b16 a[216:217], v235 offset:32768\n\t" \
  "v_mfma_f32_16x16x32_bf16 a[116:119], a[196:199], v[4:7], a[116:119]\n\t" \
  "ds_read_b64_tr_b16 a[218:219], v235 offset:36864\n\t" \
  "v_mfma_f32_16x16x32_bf16 a[116:119], a[192:195], v[0:3], a[116:119]\n\t" \
  "buffer_load_dwordx4 v242, %[ws], s71 offen offset:1024 sc1 lds\n\t" \
  "v_mfma_f32_16x16x32_bf16 a[120:123], a[192:195], v[32:35], a[120:123]\n\t" \
  "s_xor_b32 s73, %[eso], 917504\n\t" \
  "ds_read_b64_tr_b16 a[220:221], v235 offset:40960\n\t" \
  "v_mfma_f32_16x16x32_bf16 a[120:123], a[196:199], v[36:39], a[120:123]\n\t" \
  "ds_read_b64_tr_b16 a[222:223], v235 offset:45056\n\t" \
  "v_mfma_f32_16x16x32_bf16 a[120:123], a[200:203], v[40:43], a[120:123]\n\t" \
  "v_mfma_f32_16x16x32_bf16 a[120:123], a[204:207], v[44:47], a[120:123]\n\t" \
  "s_waitcnt lgkmcnt(4)\n\t" \
  "buffer_load_dwordx4 v[16:19], v242, %[ws], s73 offen offset:0 sc1\n\t" \
  STP("v_writelane_b32 %[stv], s94, 30\n\t") \
  STP("v_writelane_b32 %[stv], s92, 31\n\t") \
  STP("s_memrealtime s[94:95]\n\t") \
  STP("s_memtime s[92:93]\n\t") \
  "v_mfma_f32_16x16x32_bf16 a[0:3], a[208:211], v[48:51], a[0:3]\n\t" \
  "v_xor_b32 v236, 0x14020, v234\n\t" \
  "v_mfma_f32_16x16x32_bf16 a[0:3], a[212:215], v[52:55], a[0:3]\n\t" \
  "s_waitcnt lgkmcnt(0)\n\t" \
  "ds_read_b64_tr_b16 a[192:193], v236 offset:0\n\t" \
  "v_mfma_f32_16x16x32_bf16 a[0:3], a[216:219], v[56:59], a[0:3]\n\t" \
  "ds_read_b64_tr_b16 a[194:195], v236 offset:4096\n\t" \
  "v_mfma_f32_16x16x32_bf16 a[0:3], a[220:223], v[60:63], a[0:3]\n\t" \
  "s_waitcnt vmcnt(16)\n\t" \
  "ds_read_b64_tr_b16 a[196:197], v236 offset:8192\n\t" \
  "v_mfma_f32_16x16x32_bf16 a[4:7], a[220:223], v[92:95], a[4:7]\n\t" \
  "buffer_load_dwordx4 v[20:23], v242, %[ws], s73 offen offset:1024 sc1\n\t" \
  "v_mfma_f32_16x16x32_bf16 a[4:7], a[216:219], v[88:91], a[4:7]\n\t" \
  "ds_read_b64_tr_b16 a[198:199], v236 offset:12288\n\t" \
  "v_mfma_f32_16x16x32_bf16 a[4:7], a[212:215], v[84:87], a[4:7]\n\t" \
  "ds_read_b64_tr_b16 a[200:201], v236 offset:32768\n\t" \
  "v_mfma_f32_16x16x32_bf16 a[4:7], a[208:211], v[80:83], a[4:7]\n\t" \
  "s_waitcnt vmcnt(13)\n\t" \
  "ds_read_b64_tr_b16 a[202:203], v236 offset:36864\n\t" \
  "v_mfma_f32_16x16x32_bf16 a[8:11], a[208:211], v[64:67], a[8:11]\n\t" \
  "buffer_load_dwordx4 v[24:27], v242, %[ws], s73 offen offset:2048 sc1\n\t" \
  "v_mfma_f32_16x16x32_bf16 a[8:11], a[212:215], v[68:71], a[8:11]\n\t" \
  "ds_read_b64_tr_b16 a[204:205], v236 offset:40960\n\t" \
  "v_mfma_f32_16x16x32_bf16 a[8:11], a[216:219], v[72:75], a[8:11]\n\t" \
  "ds_read_b64_tr_b16 a[206:207], v236 offset:45056\n\t" \
  "v_mfma_f32_16x16x32_bf16 a[8:11], a[220:223], v[76:79], a[8:11]\n\t" \
  "s_waitcnt lgkmcnt(4)\n\t" \
  "v_xor_b32 v237, 0x14040, v234\n\t" \
  "v_mfma_f32_16x16x32_bf16 a[16:19], a[192:195], v[48:51], a[16:19]\n\t" \
  "ds_read_b64_tr_b16 a[208:209], v237 offset:0\n\t" \
  "v_mfma_f32_16x16x32_bf16 a[16:19], a[196:199], v[52:55], a[16:19]\n\t" \
  "s_waitcnt lgkmcnt(1)\n\t" \
  "buffer_load_dwordx4 v[28:31], v242, %[ws], s73 offen offset:3072 sc1\n\t" \
  "s_xor_b32 s73, %[eso], 950272\n\t" \
  "v_mfma_f32_16x16x32_bf16 a[16:19], a[200:203], v[56:59], a[16:19]\n\t" \
  "ds_read_b64_tr_b16 a[210:211], v237 offset:4096\n\t" \
  "v_mfma_f32_16x16x32_bf16 a[16:19], a[204:207], v[60:63], a[16:19]\n\t" \
  "ds_read_b64_tr_b16 a[212:213], v237 offset:8192\n\t" \
  "v_mfma_f32_16x16x32_bf16 a[20:23], a[204:207], v[92:95], a[20:23]\n\t" \
  "ds_read_b64_tr_b16 a[214:215], v237 offset:12288\n\t" \
  "v_bfrev_b32 v225, 1\n\t" \
  "v_mfma_f32_16x16x32_bf16 a[20:23], a[200:203], v[88:91], a[20:23]\n\t" \
  "buffer_load_dwordx4 v[0:3], v242, %[ws], s73 offen offset:0 sc1\n\t" \
  "v_mfma_f32_16x16x32_bf16 a[20:23], a[196:199], v[84:87], a[20:23]\n\t" \
  "ds_read_b64_tr_b16 a[216:217], v237 offset:32768\n\t" \
  "v_bfrev_b32 v226, 1\n\t" \
  "v_mfma_f32_16x16x32_bf16 a[20:23], a[192:195], v[80:83], a[20:23]\n\t" \
  "ds_read_b64_tr_b16 a[218:219], v237 offset:36864\n\t" \
  "v_writelane_b32 v225, s65, 0\n\t" \
  "v_mfma_f32_16x16x32_bf16 a[24:27], a[192:195], v[64:67], a[24:27]\n\t" \
  "ds_read_b64_tr_b16 a[220:221], v237 offset:40960\n\t" \
  "v_writelane_b32 v225, s66, 1\n\t" \
  "v_mfma_f32_16x16x32_bf16 a[24:27], a[196:199], v[68:71], a[24:27]\n\t" \
  "buffer_load_dwordx4 v[4:7], v242, %[ws], s73 offen offset:1024 sc1\n\t" \
  "v_mfma_f32_16x16x32_bf16 a[24:27], a[200:203], v[72:75], a[24:27]\n\t" \
  "ds_read_b64_tr_b16 a[222:223], v237 offset:45056\n\t" \
  "v_writelane_b32 v226, s67, 2\n\t" \
  "v_mfma_f32_16x16x32_bf16 a[24:27], a[204:207], v[76:79], a[24:27]\n\t" \
  "s_waitcnt lgkmcnt(4)\n\t" \
  "s_waitcnt vmcnt(8)\n\t" \
  "v_xor_b32 v246, v243, v209\n\t" \
  "v_bitop3_b32 v246, v246, v211, v243 bitop3:0xf6\n\t" \
  "v_mfma_f32_16x16x32_bf16 a[32:35], a[208:211], v[48:51], a[32:35]\n\t" \
  "v_bitop3_b32 v246, v246, v213, v243 bitop3:0xf6\n\t" \
  "v_xor_b32 v235, 0x14060, v234\n\t" \
  "v_mfma_f32_16x16x32_bf16 a[32:35], a[212:215], v[52:55], a[32:35]\n\t" \
  "s_waitcnt lgkmcnt(0)\n\t" \
  "ds_read_b64_tr_b16 a[192:193], v235 offset:0\n\t" \
  "v_bitop3_b32 v246, v246, v215, v243 bitop3:0xf6\n\t" \
  "v_mfma_f32_16x16x32_bf16 a[32:35], a[216:219], v[56:59], a[32:35]\n\t" \
  "buffer_load_dwordx4 v[8:11], v242, %[ws], s73 offen offset:2048 sc1\n\t" \
  "v_mfma_f32_16x16x32_bf16 a[32:35], a[220:223], v[60:63], a[32:35]\n\t" \
  "s_mov_b32 s81, 0\n\t" \
  "ds_read_b64_tr_b16 a[194:195], v235 offset:4096\n\t" \
  "v_bitop3_b32 v246, v246, v217, v243 bitop3:0xf6\n\t" \
  "v_mfma_f32_16x16x32_bf16 a[36:39], a[220:223], v[92:95], a[36:39]\n\t" \
  "ds_read_b64_tr_b16 a[196:197], v235 offset:8192\n\t" \
  "v_bitop3_b32 v246, v246, v219, v243 bitop3:0xf6\n\t" \
  "v_mfma_f32_16x16x32_bf16 a[36:39], a[216:219], v[88:91], a[36:39]\n\t" \
  "ds_read_b64_tr_b16 a[198:199], v235 offset:12288\n\t" \
  "v_bitop3_b32 v246, v246, v221, v243 bitop3:0xf6\n\t" \
  "v_mfma_f32_16x16x32_bf16 a[36:39], a[212:215], v[84:87], a[36:39]\n\t" \
  "buffer_load_dwordx4 v[12:15], v242, %[ws], s73 offen offset:3072 sc1\n\t" \
  "s_xor_b32 s73, %[eso], 983040\n\t" \
  "v_mfma_f32_16x16x32_bf16 a[36:39], a[208:211], v[80:83], a[36:39]\n\t" \
  "ds_read_b64_tr_b16 a[200:201], v235 offset:32768\n\t" \
  "v_bitop3_b32 v246, v246, v223, v243 bitop3:0xf6\n\t" \
  "v_mfma_f32_16x16x32_bf16 a[40:43], a[208:211], v[64:67], a[40:43]\n\t" \
  "ds_read_b64_tr_b16 a[202:203], v235 offset:36864\n\t" \
  "v_mfma_f32_16x16x32_bf16 a[40:43], a[212:215], v[68:71], a[40:43]\n\t" \
  "ds_read_b64_tr_b16 a[204:205], v235 offset:40960\n\t" \
  "v_mfma_f32_16x16x32_bf16 a[40:43], a[216:219], v[72:75], a[40:43]\n\t" \
  "ds_read_b64_tr_b16 a[206:207], v235 offset:45056\n\t" \
  "v_mfma_f32_16x16x32_bf16 a[40:43], a[220:223], v[76:79], a[40:43]\n\t" \
  "s_waitcnt lgkmcnt(4)\n\t" \
  "buffer_load_dwordx4 v[32:35], v242, %[ws], s73 offen offset:0 sc1\n\t" \
  "v_mfma_f32_16x16x32_bf16 a[48:51], a[192:195], v[48:51], a[48:51]\n\t" \
  "v_cmp_ne_u32 vcc, 0, v246\n\t" \
  "s_cbranch_vccnz .Lop_s4\n\t" \
  ".Lop_k4:\n\t" \
  "v_mfma_f32_16x16x32_bf16 a[48:51], a[196:199], v[52:55], a[48:51]\n\t" \
  "s_waitcnt lgkmcnt(0)\n\t" \
  "v_add_f32 v208, v208, v212\n\t" \
  "v_add_f32 v216, v216, v220\n\t" \
  "v_add_f32 v210, v210, v214\n\t" \
  "v_mfma_f32_16x16x32_bf16 a[48:51], a[200:203], v[56:59], a[48:51]\n\t" \
  "v_xor_b32 v236, 0x14080, v234\n\t" \
  "v_add_f32 v218, v218, v222\n\t" \
  "v_add_f32 v208, v208, v216\n\t" \
  "v_mfma_f32_16x16x32_bf16 a[48:51], a[204:207], v[60:63], a[48:51]\n\t" \
  "s_barrier\n\t" \
  "v_mfma_f32_16x16x32_bf16 a[52:55], a[204:207], v[92:95], a[52:55]\n\t" \
  "buffer_load_dwordx4 v[36:39], v242, %[ws], s73 offen offset:1024 sc1\n\t" \
  "v_add_f32 v210, v210, v218\n\t" \
  "v_mfma_f32_16x16x32_bf16 a[52:55], a[200:203], v[88:91], a[52:55]\n\t" \
  "ds_read_b64_tr_b16 a[208:209], v236 offset:0\n\t" \
  "ds_read_b64_tr_b16 a[210:211], v236 offset:4096\n\t" \
  "v_lshlrev_b32 v163, 16, v224\n\t" \
  "v_mfma_f32_16x16x32_bf16 a[52:55], a[196:199], v[84:87], a[52:55]\n\t" \
  "ds_read_b64_tr_b16 a[212:213], v236 offset:8192\n\t" \
  "ds_read_b64_tr_b16 a[214:215], v236 offset:12288\n\t" \
  "v_and_b32 v164, 0xffff0000, v224\n\t" \
  "v_mfma_f32_16x16x32_bf16 a[52:55], a[192:195], v[80:83], a[52:55]\n\t" \
  "buffer_load_dwordx4 v[40:43], v242, %[ws], s73 offen offset:2048 sc1\n\t" \
  "v_cvt_pk_bf16_f32 v162, v208, v210\n\t" \
  "v_mfma_f32_16x16x32_bf16 a[56:59], a[192:195], v[64:67], a[56:59]\n\t" \
  "ds_read_b64_tr_b16 a[216:217], v236 offset:32768\n\t" \
  "ds_read_b64_tr_b16 a[218:219], v236 offset:36864\n\t" \
  "v_lshlrev_b32 v160, 16, v162\n\t" \
  "v_mfma_f32_16x16x32_bf16 a[56:59], a[196:199], v[68:71], a[56:59]\n\t" \
  "ds_read_b64_tr_b16 a[220:221], v236 offset:40960\n\t" \
  "v_and_b32 v161, 0xffff0000, v162\n\t" \
  "v_mfma_f32_16x16x32_bf16 a[56:59], a[200:203], v[72:75], a[56:59]\n\t" \
  "ds_read_b64_tr_b16 a[222:223], v236 offset:45056\n\t" \
  "v_add_f32 v160, v160, v172\n\t" \
  "v_mfma_f32_16x16x32_bf16 a[56:59], a[204:207], v[76:79], a[56:59]\n\t" \
  "s_waitcnt lgkmcnt(4)\n\t" \
  "v_add_f32 v161, v161, v172\n\t" \
  "v_xor_b32 v237, 0x140a0, v234\n\t" \
  "v_mfma_f32_16x16x32_bf16 a[64:67], a[208:211], v[48:51], a[64:67]\n\t" \
  "buffer_load_dwordx4 v[44:47], v242, %[ws], s73 offen offset:3072 sc1\n\t" \
  "v_mfma_f32_16x16x32_bf16 a[64:67], a[212:215], v[52:55], a[64:67]\n\t" \
  "s_waitcnt lgkmcnt(0)\n\t" \
  "ds_read_b64_tr_b16 a[192:193], v237 offset:0\n\t" \
  "v_cvt_pk_bf16_f32 v162, v160, v161\n\t" \
  "v_mfma_f32_16x16x32_bf16 a[64:67], a[216:219], v[56:59], a[64:67]\n\t" \
  "ds_read_b64_tr_b16 a[194:195], v237 offset:4096\n\t" \
  "v_lshlrev_b32 v160, 16, v162\n\t" \
  "v_mfma_f32_16x16x32_bf16 a[64:67], a[220:223], v[60:63], a[64:67]\n\t" \
  "ds_read_b64_tr_b16 a[196:197], v237 offset:8192\n\t" \
  "v_and_b32 v161, 0xffff0000, v162\n\t" \
  "v_mfma_f32_16x16x32_bf16 a[68:71], a[220:223], v[92:95], a[68:71]\n\t" \
  "buffer_load_dwordx4 v242, %[ws], s71 offen offset:2048 sc1 lds\n\t" \
  "v_mfma_f32_16x16x32_bf16 a[68:71], a[216:219], v[88:91], a[68:71]\n\t" \
  "ds_read_b64_tr_b16 a[198:199], v237 offset:12288\n\t" \
  "ds_write_b64 v174, v[160:161] offset:2560\n\t" \
  "v_mfma_f32_16x16x32_bf16 a[68:71], a[212:215], v[84:87], a[68:71]\n\t" \
  "ds_read_b64_tr_b16 a[200:201], v237 offset:32768\n\t" \
  "v_mul_f32 v166, v160, v163\n\t" \
  "v_mfma_f32_16x16x32_bf16 a[68:71], a[208:211], v[80:83], a[68:71]\n\t" \
  "ds_read_b64_tr_b16 a[202:203], v237 offset:36864\n\t" \
  "v_mul_f32 v167, v161, v164\n\t" \
  "v_mfma_f32_16x16x32_bf16 a[72:75], a[208:211], v[64:67], a[72:75]\n\t" \
  "ds_read_b64_tr_b16 a[204:205], v237 offset:40960\n\t" \
  "v_cvt_pk_bf16_f32 v168, v166, v167\n\t" \
  "v_mfma_f32_16x16x32_bf16 a[72:75], a[212:215], v[68:71], a[72:75]\n\t" \
  "buffer_load_dwordx4 v242, %[ws], s71 offen offset:3072 sc1 lds\n\t" \
  "s_add_u32 m0, %[m], 32768\n\t" \
  "v_mfma_f32_16x16x32_bf16 a[72:75], a[216:219], v[72:75], a[72:75]\n\t" \
  "s_xor_b32 s71, %[eso], 360448\n\t" \
  "ds_read_b64_tr_b16 a[206:207], v237 offset:45056\n\t" \
  "v_add_f32 v162, v166, v167\n\t" \
  "v_mfma_f32_16x16x32_bf16 a[72:75], a[220:223], v[76:79], a[72:75]\n\t" \
  "s_waitcnt lgkmcnt(5)\n\t" \
  "v_lshlrev_b32 v163, 16, v168\n\t" \
  "v_xor_b32 v235, 0x140c0, v234\n\t" \
  "v_mfma_f32_16x16x32_bf16 a[80:83], a[192:195], v[48:51], a[80:83]\n\t" \
  "ds_read_b64_tr_b16 a[208:209], v235 offset:0\n\t" \
  "v_and_b32 v164, 0xffff0000, v168\n\t" \
  "v_mfma_f32_16x16x32_bf16 a[80:83], a[196:199], v[52:55], a[80:83]\n\t" \
  "s_waitcnt lgkmcnt(1)\n\t" \
  "buffer_load_dwordx4 v242, %[ws], s71 offen offset:0 sc1 lds\n\t" \
  "v_mfma_f32_16x16x32_bf16 a[80:83], a[200:203], v[56:59], a[80:83]\n\t" \
  "ds_read_b64_tr_b16 a[210:211], v235 offset:4096\n\t" \
  "v_add_f32_dpp v162, v162, v162 row_ror:8 row_mask:0xf bank_mask:0xf\n\t" \
  "v_mfma_f32_16x16x32_bf16 a[80:83], a[204:207], v[60:63], a[80:83]\n\t" \
  "ds_read_b64_tr_b16 a[212:213], v235 offset:8192\n\t" \
  "v_sub_f32 v166, v166, v163\n\t" \
  "v_mfma_f32_16x16x32_bf16 a[84:87], a[204:207], v[92:95], a[84:87]\n\t" \
  "ds_read_b64_tr_b16 a[214:215], v235 offset:12288\n\t" \
  "v_sub_f32 v167, v167, v164\n\t" \
  "v_mfma_f32_16x16x32_bf16 a[84:87], a[200:203], v[88:91], a[84:87]\n\t" \
  "buffer_load_dwordx4 v242, %[ws], s71 offen offset:1024 sc1 lds\n\t" \
  "v_mfma_f32_16x16x32_bf16 a[84:87], a[196:199], v[84:87], a[84:87]\n\t" \
  "ds_read_b64_tr_b16 a[216:217], v235 offset:32768\n\t" \
  "v_add_f32_dpp v162, v162, v162 row_ror:4 row_mask:0xf bank_mask:0xf\n\t" \
  "v_mfma_f32_16x16x32_bf16 a[84:87], a[192:195], v[80:83], a[84:87]\n\t" \
  "ds_read_b64_tr_b16 a[218:219], v235 offset:36864\n\t" \
  "v_cvt_pk_bf16_f32 v169, v166, v167\n\t" \
  "v_mfma_f32_16x16x32_bf16 a[88:91], a[192:195], v[64:67], a[88:91]\n\t" \
  "s_mov_b32 s82, 0\n\t" \
  "ds_read_b64_tr_b16 a[220:221], v235 offset:40960\n\t" \
  "v_add_f32_dpp v162, v162, v162 row_ror:2 row_mask:0xf bank_mask:0xf\n\t" \
  "v_mfma_f32_16x16x32_bf16 a[88:91], a[196:199], v[68:71], a[88:91]\n\t" \
  "s_mov_b32 s83, 0xffff\n\t" \
  "ds_read_b64_tr_b16 a[222:223], v235 offset:45056\n\t" \
  "v_add_f32_dpp v162, v162, v162 row_ror:1 row_mask:0xf bank_mask:0xf\n\t" \
  "v_mfma_f32_16x16x32_bf16 a[88:91], a[200:203], v[72:75], a[88:91]\n\t" \
  "buffer_load_dwordx4 v242, %[ws], s71 offen offset:2048 sc1 lds\n\t" \
  "v_mfma_f32_16x16x32_bf16 a[88:91], a[204:207], v[76:79], a[88:91]\n\t" \
  "s_waitcnt lgkmcnt(4)\n\t" \
  "v_xor_b32 v236, 0x140e0, v234\n\t" \
  "v_mfma_f32_16x16x32_bf16 a[96:99], a[208:211], v[48:51], a[96:99]\n\t" \
  "ds_read_b64_tr_b16 a[192:193], v236 offset:0\n\t" \
  "v_mfma_f32_16x16x32_bf16 a[96:99], a[212:215], v[52:55], a[96:99]\n\t" \
  "s_waitcnt lgkmcnt(1)\n\t" \
  "s_mov_b64 exec, s[82:83]\n\t" \
  "ds_write_b32 v173, v168 offset:1040\n\t" \
  "ds_write_b32 v173, v169 offset:1120\n\t" \
  "s_mov_b64 exec, -1\n\t" \
  "v_mfma_f32_16x16x32_bf16 a[96:99], a[216:219], v[56:59], a[96:99]\n\t" \
  "buffer_load_dwordx4 v242, %[ws], s71 offen offset:3072 sc1 lds\n\t" \
  "s_add_u32 m0, %[m], 98304\n\t" \
  "v_mfma_f32_16x16x32_bf16 a[96:99], a[220:223], v[60:63], a[96:99]\n\t" \
  "s_xor_b32 s71, %[eso], 491520\n\t" \
  "ds_read_b64_tr_b16 a[194:195], v236 offset:4096\n\t" \
  "v_readlane_b32 s87, v162, 32\n\t" \
  "v_mfma_f32_16x16x32_bf16 a[100:103], a[220:223], v[92:95], a[100:103]\n\t" \
  "ds_read_b64_tr_b16 a[196:197], v236 offset:8192\n\t" \
  "v_readlane_b32 s88, v162, 16\n\t" \
  "v_mfma_f32_16x16x32_bf16 a[100:103], a[216:219], v[88:91], a[100:103]\n\t" \
  "ds_read_b64_tr_b16 a[198:199], v236 offset:12288\n\t" \
  "v_mfma_f32_16x16x32_bf16 a[100:103], a[212:215], v[84:87], a[100:103]\n\t" \
  "s_xor_b32 s73, %[eso], 196608\n\t" \
  "ds_read_b64_tr_b16 a[200:201], v236 offset:32768\n\t" \
  "v_mfma_f32_16x16x32_bf16 a[100:103], a[208:211], v[80:83], a[100:103]\n\t" \
  "buffer_load_dwordx4 v242, %[ws], s71 offen offset:0 sc1 lds\n\t" \
  "v_mfma_f32_16x16x32_bf16 a[104:107], a[208:211], v[64:67], a[104:107]\n\t" \
  "ds_read_b64_tr_b16 a[202:203], v236 offset:36864\n\t" \
  "v_mfma_f32_16x16x32_bf16 a[104:107], a[212:215], v[68:71], a[104:107]\n\t" \
  "ds_read_b64_tr_b16 a[204:205], v236 offset:40960\n\t" \
  "v_mfma_f32_16x16x32_bf16 a[104:107], a[216:219], v[72:75], a[104:107]\n\t" \
  "ds_read_b64_tr_b16 a[206:207], v236 offset:45056\n\t" \
  "v_mfma_f32_16x16x32_bf16 a[104:107], a[220:223], v[76:79], a[104:107]\n\t" \
  "s_waitcnt lgkmcnt(4)\n\t" \
  "buffer_load_dwordx4 v[208:211], v242, %[ws], s73 offen offset:0 sc1\n\t" \
  "v_mfma_f32_16x16x32_bf16 a[112:115], a[192:195], v[48:51], a[112:115]\n\t" \
  "v_xor_b32 v237, 0x20040, v234\n\t" \
  "v_mfma_f32_16x16x32_bf16 a[112:115], a[196:199], v[52:55], a[112:115]\n\t" \
  "s_waitcnt lgkmcnt(0)\n\t" \
  "ds_read_b64_tr_b16 a[208:209], v237 offset:0\n\t" \
  "v_mfma_f32_16x16x32_bf16 a[112:115], a[200:203], v[56:59], a[112:115]\n\t" \
  "ds_read_b64_tr_b16 a[210:211], v237 offset:4096\n\t" \
  "v_mfma_f32_16x16x32_bf16 a[112:115], a[204:207], v[60:63], a[112:115]\n\t" \
  "buffer_load_dwordx4 v[212:215], v242, %[ws], s73 offen offset:1024 sc1\n\t" \
  "v_mfma_f32_16x16x32_bf16 a[116:119], a[204:207], v[92:95], a[116:119]\n\t" \
  "ds_read_b64_tr_b16 a[212:213], v237 offset:8192\n\t" \
  "v_mfma_f32_16x16x32_bf16 a[116:119], a[200:203], v[88:91], a[116:119]\n\t" \
  "ds_read_b64_tr_b16 a[214:215], v237 offset:12288\n\t" \
  "v_xor_b32 v235, 0x40, v234\n\t" \
  "v_mfma_f32_16x16x32_bf16 a[116:119], a[196:199], v[84:87], a[116:119]\n\t" \
  "ds_read_b64_tr_b16 a[216:217], v235 offset:0\n\t" \
  "v_mfma_f32_16x16x32_bf16 a[116:119], a[192:195], v[80:83], a[116:119]\n\t" \
  "ds_read_b64_tr_b16 a[218:219], v235 offset:4096\n\t" \
  "v_mfma_f32_16x16x32_bf16 a[120:123], a[192:195], v[64:67], a[120:123]\n\t" \
  "buffer_load_dwordx4 v[216:219], v242, %[ws], s73 offen offset:2048 sc1\n\t" \
  "v_mfma_f32_16x16x32_bf16 a[120:123], a[196:199], v[68:71], a[120:123]\n\t" \
  "s_mov_b32 s74, 16384\n\t" \
  "ds_read_b64_tr_b16 a[220:221], v235 offset:8192\n\t" \
  "v_mfma_f32_16x16x32_bf16 a[120:123], a[200:203], v[72:75], a[120:123]\n\t" \
  "ds_read_b64_tr_b16 a[222:223], v235 offset:12288\n\t" \
  "v_mfma_f32_16x16x32_bf16 a[120:123], a[204:207], v[76:79], a[120:123]\n\t" \
  "s_waitcnt vmcnt(18) lgkmcnt(4)\n\t" \
  "v_xor_b32 v236, 0x20060, v234\n\t" \
  STP("v_writelane_b32 %[stv], s94, 32\n\t") \
  STP("v_writelane_b32 %[stv], s92, 33\n\t") \
  STP("s_memrealtime s[94:95]\n\t") \
  STP("s_memtime s[92:93]\n\t") \
  "v_mfma_f32_16x16x32_bf16 a[32:35], a[208:211], v[16:19], a[32:35]\n\t" \
  "buffer_load_dwordx4 v[220:223], v242, %[ws], s73 offen offset:3072 sc1\n\t" \
  "v_mfma_f32_16x16x32_bf16 a[32:35], a[212:215], v[20:23], a[32:35]\n\t" \
  "s_waitcnt lgkmcnt(0)\n\t" \
  "ds_read_b64_tr_b16 a[192:193], v236 offset:0\n\t" \
  "v_mfma_f32_16x16x32_bf16 a[32:35], a[216:219], v[24:27], a[32:35]\n\t" \
  "ds_read_b64_tr_b16 a[194:195], v236 offset:4096\n\t" \
  "v_mfma_f32_16x16x32_bf16 a[32:35], a[220:223], v[28:31], a[32:35]\n\t" \
  "s_waitcnt vmcnt(15)\n\t" \
  "ds_read_b64_tr_b16 a[196:197], v236 offset:8192\n\t" \
  "v_mfma_f32_16x16x32_bf16 a[36:39], a[220:223], v[12:15], a[36:39]\n\t" \
  "ds_read_b64_tr_b16 a[198:199], v236 offset:12288\n\t" \
  "v_xor_b32 v237, 0x60, v234\n\t" \
  "v_mfma_f32_16x16x32_bf16 a[36:39], a[216:219], v[8:11], a[36:39]\n\t" \
  "buffer_load_dwordx4 v242, %[ws], s71 offen offset:1024 sc1 lds\n\t" \
  "v_mfma_f32_16x16x32_bf16 a[36:39], a[212:215], v[4:7], a[36:39]\n\t" \
  "ds_read_b64_tr_b16 a[200:201], v237 offset:0\n\t" \
  "v_mfma_f32_16x16x32_bf16 a[36:39], a[208:211], v[0:3], a[36:39]\n\t" \
  "s_waitcnt vmcnt(12)\n\t" \
  "ds_read_b64_tr_b16 a[202:203], v237 offset:4096\n\t" \
  "v_mfma_f32_16x16x32_bf16 a[40:43], a[208:211], v[32:35], a[40:43]\n\t" \
  "ds_read_b64_tr_b16 a[204:205], v237 offset:8192\n\t" \
  "v_mfma_f32_16x16x32_bf16 a[40:43], a[212:215], v[36:39], a[40:43]\n\t" \
  "buffer_load_dwordx4 v242, %[ws], s71 offen offset:2048 sc1 lds\n\t" \
  "v_mfma_f32_16x16x32_bf16 a[40:43], a[216:219], v[40:43], a[40:43]\n\t" \
  "ds_read_b64_tr_b16 a[206:207], v237 offset:12288\n\t" \
  "v_xor_b32 v235, 0x20000, v234\n\t" \
  "v_mfma_f32_16x16x32_bf16 a[40:43], a[220:223], v[44:47], a[40:43]\n\t" \
  "s_waitcnt lgkmcnt(4)\n\t" \
  "ds_read_b64_tr_b16 v[64:65], v235 offset:0\n\t" \
  "v_mfma_f32_16x16x32_bf16 a[48:51], a[192:195], v[16:19], a[48:51]\n\t" \
  "ds_read_b64_tr_b16 v[66:67], v235 offset:4096\n\t" \
  "v_mfma_f32_16x16x32_bf16 a[48:51], a[196:199], v[20:23], a[48:51]\n\t" \
  "s_waitcnt lgkmcnt(2)\n\t" \
  "buffer_load_dwordx4 v242, %[ws], s71 offen offset:3072 sc1 lds\n\t" \
  "v_mfma_f32_16x16x32_bf16 a[48:51], a[200:203], v[24:27], a[48:51]\n\t" \
  "ds_read_b64_tr_b16 v[68:69], v235 offset:8192\n\t" \
  "v_mfma_f32_16x16x32_bf16 a[48:51], a[204:207], v[28:31], a[48:51]\n\t" \
  "ds_read_b64_tr_b16 v[70:71], v235 offset:12288\n\t" \
  "v_xor_b32 v236, 0x0, v234\n\t" \
  "v_mfma_f32_16x16x32_bf16 a[52:55], a[204:207], v[12:15], a[52:55]\n\t" \
  "ds_read_b64_tr_b16 v[72:73], v236 offset:0\n\t" \
  "v_accvgpr_read_b32 v160, a32\n\t" \
  "v_mfma_f32_16x16x32_bf16 a[52:55], a[200:203], v[8:11], a[52:55]\n\t" \
  "buffer_load_dwordx2 v[48:49], v252, %[sd], s74 offen offset:0\n\t" \
  "v_mfma_f32_16x16x32_bf16 a[52:55], a[196:199], v[4:7], a[52:55]\n\t" \
  "ds_read_b64_tr_b16 v[74:75], v236 offset:4096\n\t" \
  "ds_read_b64_tr_b16 v[76:77], v236 offset:8192\n\t" \
  "v_mfma_f32_16x16x32_bf16 a[52:55], a[192:195], v[0:3], a[52:55]\n\t" \
  "ds_read_b64_tr_b16 v[78:79], v236 offset:12288\n\t" \
  "v_accvgpr_read_b32 v161, a33\n\t" \
  "v_mfma_f32_16x16x32_bf16 a[56:59], a[192:195], v[32:35], a[56:59]\n\t" \
  "v_xor_b32 v237, 0x20020, v234\n\t" \
  "v_accvgpr_read_b32 v162, a34\n\t" \
  "v_mfma_f32_16x16x32_bf16 a[56:59], a[196:199], v[36:39], a[56:59]\n\t" \
  "buffer_load_dwordx2 v[50:51], v252, %[sd], s74 offen offset:2048\n\t" \
  "s_mov_b32 s74, 20480\n\t" \
  "v_mfma_f32_16x16x32_bf16 a[56:59], a[200:203], v[40:43], a[56:59]\n\t" \
  "ds_read_b64_tr_b16 v[80:81], v237 offset:0\n\t" \
  "v_accvgpr_read_b32 v163, a35\n\t" \
  "v_mfma_f32_16x16x32_bf16 a[56:59], a[204:207], v[44:47], a[56:59]\n\t" \
  "s_waitcnt lgkmcnt(5)\n\t" \
  "ds_read_b64_tr_b16 v[82:83], v237 offset:4096\n\t" \
  "v_accvgpr_read_b32 v164, a48\n\t" \
  "v_mfma_f32_16x16x32_bf16 a[0:3], v[64:67], v[16:19], a[0:3]\n\t" \
  "ds_read_b64_tr_b16 v[84:85], v237 offset:8192\n\t" \
  "v_accvgpr_read_b32 v165, a49\n\t" \
  "v_mfma_f32_16x16x32_bf16 a[0:3], v[68:71], v[20:23], a[0:3]\n\t" \
  "s_waitcnt lgkmcnt(3)\n\t" \
  "buffer_load_dwordx2 v[52:53], v252, %[sd], s74 offen offset:0\n\t" \
  "v_accvgpr_read_b32 v166, a50\n\t" \
  "v_mfma_f32_16x16x32_bf16 a[0:3], v[72:75], v[24:27], a[0:3]\n\t" \
  "ds_read_b64_tr_b16 v[86:87], v237 offset:12288\n\t" \
  "v_accvgpr_read_b32 v167, a51\n\t" \
  "v_cvt_pk_bf16_f32 v168, v160, v161\n\t" \
  "v_mfma_f32_16x16x32_bf16 a[0:3], v[76:79], v[28:31], a[0:3]\n\t" \
  "v_xor_b32 v235, 0x20, v234\n\t" \
  "v_cvt_pk_bf16_f32 v169, v162, v163\n\t" \
  "v_cvt_pk_bf16_f32 v170, v164, v165\n\t" \
  "v_mfma_f32_16x16x32_bf16 a[4:7], v[76:79], v[12:15], a[4:7]\n\t" \
  "ds_read_b64_tr_b16 v[88:89], v235 offset:0\n\t" \
  "v_cvt_pk_bf16_f32 v171, v166, v167\n\t" \
  "s_xor_b32 s76, s60, 1024\n\t" \
  "v_mfma_f32_16x16x32_bf16 a[4:7], v[72:75], v[8:11], a[4:7]\n\t" \
  "buffer_store_dwordx4 v[168:171], v242, %[ws], s76 offen sc1\n\t" \
  "v_accvgpr_read_b32 v160, a36\n\t" \
  "v_mfma_f32_16x16x32_bf16 a[4:7], v[68:71], v[4:7], a[4:7]\n\t" \
  "ds_read_b64_tr_b16 v[90:91], v235 offset:4096\n\t" \
  "ds_read_b64_tr_b16 v[92:93], v235 offset:8192\n\t" \
  "v_accvgpr_read_b32 v161, a37\n\t" \
  "v_mfma_f32_16x16x32_bf16 a[4:7], v[64:67], v[0:3], a[4:7]\n\t" \
  "buffer_load_dwordx2 v[54:55], v252, %[sd], s74 offen offset:2048\n\t" \
  "s_mov_b32 s74, 24576\n\t" \
  "v_mfma_f32_16x16x32_bf16 a[8:11], v[64:67], v[32:35], a[8:11]\n\t" \
  "ds_read_b64_tr_b16 v[94:95], v235 offset:12288\n\t" \
  "v_accvgpr_read_b32 v162, a38\n\t" \
  "v_accvgpr_read_b32 v163, a39\n\t" \
  "v_mfma_f32_16x16x32_bf16 a[8:11], v[68:71], v[36:39], a[8:11]\n\t" \
  "v_accvgpr_read_b32 v164, a52\n\t" \
  "v_accvgpr_read_b32 v165, a53\n\t" \
  "v_accvgpr_read_b32 v166, a54\n\t" \
  "v_mfma_f32_16x16x32_bf16 a[8:11], v[72:75], v[40:43], a[8:11]\n\t" \
  "v_accvgpr_read_b32 v167, a55\n\t" \
  "v_cvt_pk_bf16_f32 v174, v160, v161\n\t" \
  "v_cvt_pk_bf16_f32 v175, v162, v163\n\t" \
  "v_mfma_f32_16x16x32_bf16 a[8:11], v[76:79], v[44:47], a[8:11]\n\t" \
  "s_waitcnt lgkmcnt(4)\n\t" \
  "buffer_load_dwordx2 v[56:57], v252, %[sd], s74 offen offset:0\n\t" \
  "v_cvt_pk_bf16_f32 v172, v164, v165\n\t" \
  "v_mfma_f32_16x16x32_bf16 a[16:19], v[80:83], v[16:19], a[16:19]\n\t" \
  "v_xor_b32 v236, 0x20080, v234\n\t" \
  "v_cvt_pk_bf16_f32 v173, v166, v167\n\t" \
  "s_xor_b32 s76, s61, 1024\n\t" \
  "v_mfma_f32_16x16x32_bf16 a[16:19], v[84:87], v[20:23], a[16:19]\n\t" \
  "s_waitcnt lgkmcnt(0)\n\t" \
  "buffer_store_dwordx4 v[172:175], v242, %[ws], s76 offen sc1\n\t" \
  "v_mfma_f32_16x16x32_bf16 a[16:19], v[88:91], v[24:27], a[16:19]\n\t" \
  "ds_read_b64_tr_b16 a[208:209], v236 offset:0\n\t" \
  "ds_read_b64_tr_b16 a[210:211], v236 offset:4096\n\t" \
  "v_mfma_f32_16x16x32_bf16 a[16:19], v[92:95], v[28:31], a[16:19]\n\t" \
  "s_barrier\n\t" \
  "v_mfma_f32_16x16x32_bf16 a[20:23], v[92:95], v[12:15], a[20:23]\n\t" \
  "ds_read_b64_tr_b16 a[212:213], v236 offset:8192\n\t" \
  "ds_read_b64_tr_b16 a[214:215], v236 offset:12288\n\t" \
  "v_mfma_f32_16x16x32_bf16 a[20:23], v[88:91], v[8:11], a[20:23]\n\t" \
  "buffer_load_dwordx2 v[58:59], v252, %[sd], s74 offen offset:2048\n\t" \
  "s_mov_b32 s74, 28672\n\t" \
  "v_mfma_f32_16x16x32_bf16 a[20:23], v[84:87], v[4:7], a[20:23]\n\t" \
  "v_xor_b32 v237, 0x80, v234\n\t" \
  "v_accvgpr_read_b32 v160, a0\n\t" \
  "v_mfma_f32_16x16x32_bf16 a[20:23], v[80:83], v[0:3], a[20:23]\n\t" \
  "ds_read_b64_tr_b16 a[216:217], v237 offset:0\n\t" \
  "ds_read_b64_tr_b16 a[218:219], v237 offset:4096\n\t" \
  "v_mfma_f32_16x16x32_bf16 a[24:27], v[80:83], v[32:35], a[24:27]\n\t" \
  "ds_read_b64_tr_b16 a[220:221], v237 offset:8192\n\t" \
  "ds_read_b64_tr_b16 a[222:223], v237 offset:12288\n\t" \
  "v_mfma_f32_16x16x32_bf16 a[24:27], v[84:87], v[36:39], a[24:27]\n\t" \
  "buffer_load_dwordx2 v[60:61], v252, %[sd], s74 offen offset:0\n\t" \
  "v_mfma_f32_16x16x32_bf16 a[24:27], v[88:91], v[40:43], a[24:27]\n\t" \
  "v_xor_b32 v235, 0x200a0, v234\n\t" \
  "v_accvgpr_read_b32 v161, a1\n\t" \
  "v_mfma_f32_16x16x32_bf16 a[24:27], v[92:95], v[44:47], a[24:27]\n\t" \
  "s_waitcnt lgkmcnt(4)\n\t" \
  "ds_read_b64_tr_b16 a[192:193], v235 offset:0\n\t" \
  "v_accvgpr_read_b32 v162, a2\n\t" \
  "v_accvgpr_read_b32 v163, a3\n\t" \
  "v_mfma_f32_16x16x32_bf16 a[64:67], a[208:211], v[16:19], a[64:67]\n\t" \
  "ds_read_b64_tr_b16 a[194:195], v235 offset:4096\n\t" \
  "v_accvgpr_read_b32 v164, a16\n\t" \
  "v_accvgpr_read_b32 v165, a17\n\t" \
  "v_mfma_f32_16x16x32_bf16 a[64:67], a[212:215], v[20:23], a[64:67]\n\t" \
  "s_waitcnt lgkmcnt(2)\n\t" \
  "buffer_load_dwordx2 v[62:63], v252, %[sd], s74 offen offset:2048\n\t" \
  "v_accvgpr_read_b32 v166, a18\n\t" \
  "v_mfma_f32_16x16x32_bf16 a[64:67], a[216:219], v[24:27], a[64:67]\n\t" \
  "ds_read_b64_tr_b16 a[196:197], v235 offset:8192\n\t" \
  "v_accvgpr_read_b32 v167, a19\n\t" \
  "v_cvt_pk_bf16_f32 v168, v160, v161\n\t" \
  "v_mfma_f32_16x16x32_bf16 a[64:67], a[220:223], v[28:31], a[64:67]\n\t" \
  "ds_read_b64_tr_b16 a[198:199], v235 offset:12288\n\t" \
  "v_xor_b32 v236, 0xa0, v234\n\t" \
  "v_cvt_pk_bf16_f32 v169, v162, v163\n\t" \
  "v_mfma_f32_16x16x32_bf16 a[68:71], a[220:223], v[12:15], a[68:71]\n\t" \
  "ds_read_b64_tr_b16 a[200:201], v236 offset:0\n\t" \
  "v_cvt_pk_bf16_f32 v170, v164, v165\n\t" \
  "v_cvt_pk_bf16_f32 v171, v166, v167\n\t" \
  "v_mfma_f32_16x16x32_bf16 a[68:71], a[216:219], v[8:11], a[68:71]\n\t" \
  "s_xor_b32 s76, s60, 0\n\t" \
  "ds_read_b64_tr_b16 a[202:203], v236 offset:4096\n\t" \
  "buffer_store_dwordx4 v[168:171], v242, %[ws], s76 offen sc1\n\t" \
  "v_mfma_f32_16x16x32_bf16 a[68:71], a[212:215], v[4:7], a[68:71]\n\t" \
  "ds_read_b64_tr_b16 a[204:205], v236 offset:8192\n\t" \
  "v_accvgpr_read_b32 v160, a4\n\t" \
  "v_accvgpr_read_b32 v161, a5\n\t" \
  "v_mfma_f32_16x16x32_bf16 a[68:71], a[208:211], v[0:3], a[68:71]\n\t" \
  "ds_read_b64_tr_b16 a[206:207], v236 offset:12288\n\t" \
  "v_accvgpr_read_b32 v162, a6\n\t" \
  "v_accvgpr_read_b32 v163, a7\n\t" \
  "v_mfma_f32_16x16x32_bf16 a[72:75], a[208:211], v[32:35], a[72:75]\n\t" \
  "v_accvgpr_read_b32 v164, a20\n\t" \
  "v_accvgpr_read_b32 v165, a21\n\t" \
  "v_accvgpr_read_b32 v166, a22\n\t" \
  "v_mfma_f32_16x16x32_bf16 a[72:75], a[212:215], v[36:39], a[72:75]\n\t" \
  "v_accvgpr_read_b32 v167, a23\n\t" \
  "v_cvt_pk_bf16_f32 v174, v160, v161\n\t" \
  "v_cvt_pk_bf16_f32 v175, v162, v163\n\t" \
  "v_mfma_f32_16x16x32_bf16 a[72:75], a[216:219], v[40:43], a[72:75]\n\t" \
  "v_cvt_pk_bf16_f32 v172, v164, v165\n\t" \
  "v_cvt_pk_bf16_f32 v173, v166, v167\n\t" \
  "s_xor_b32 s76, s61, 0\n\t" \
  "v_mfma_f32_16x16x32_bf16 a[72:75], a[220:223], v[44:47], a[72:75]\n\t" \
  "s_waitcnt lgkmcnt(4)\n\t" \
  "buffer_store_dwordx4 v[172:175], v242, %[ws], s76 offen sc1\n\t" \
  "v_mfma_f32_16x16x32_bf16 a[80:83], a[192:195], v[16:19], a[80:83]\n\t" \
  "v_xor_b32 v237, 0x200c0, v234\n\t" \
  "v_mfma_f32_16x16x32_bf16 a[80:83], a[196:199], v[20:23], a[80:83]\n\t" \
  "s_waitcnt lgkmcnt(0)\n\t" \
  "ds_read_b64_tr_b16 a[208:209], v237 offset:0\n\t" \
  "v_mfma_f32_16x16x32_bf16 a[80:83], a[200:203], v[24:27], a[80:83]\n\t" \
  "ds_read_b64_tr_b16 a[210:211], v237 offset:4096\n\t" \
  "v_mfma_f32_16x16x32_bf16 a[80:83], a[204:207], v[28:31], a[80:83]\n\t" \
  "ds_read_b64_tr_b16 a[212:213], v237 offset:8192\n\t" \
  "v_mfma_f32_16x16x32_bf16 a[84:87], a[204:207], v[12:15], a[84:87]\n\t" \
  "ds_read_b64_tr_b16 a[214:215], v237 offset:12288\n\t" \
  "v_xor_b32 v235, 0xc0, v234\n\t" \
  "v_mfma_f32_16x16x32_bf16 a[84:87], a[200:203], v[8:11], a[84:87]\n\t" \
  "ds_read_b64_tr_b16 a[216:217], v235 offset:0\n\t" \
  "v_accvgpr_read_b32 v160, a64\n\t" \
  "v_mfma_f32_16x16x32_bf16 a[84:87], a[196:199], v[4:7], a[84:87]\n\t" \
  "ds_read_b64_tr_b16 a[218:219], v235 offset:4096\n\t" \
  "v_accvgpr_read_b32 v161, a65\n\t" \
  "v_mfma_f32_16x16x32_bf16 a[84:87], a[192:195], v[0:3], a[84:87]\n\t" \
  "ds_read_b64_tr_b16 a[220:221], v235 offset:8192\n\t" \
  "v_accvgpr_read_b32 v162, a66\n\t" \
  "v_mfma_f32_16x16x32_bf16 a[88:91], a[192:195], v[32:35], a[88:91]\n\t" \
  "ds_read_b64_tr_b16 a[222:223], v235 offset:12288\n\t" \
  "v_accvgpr_read_b32 v163, a67\n\t" \
  "v_mfma_f32_16x16x32_bf16 a[88:91], a[196:199], v[36:39], a[88:91]\n\t" \
  "v_accvgpr_read_b32 v164, a80\n\t" \
  "v_accvgpr_read_b32 v165, a81\n\t" \
  "v_mfma_f32_16x16x32_bf16 a[88:91], a[200:203], v[40:43], a[88:91]\n\t" \
  "v_accvgpr_read_b32 v166, a82\n\t" \
  "v_accvgpr_read_b32 v167, a83\n\t" \
  "v_mfma_f32_16x16x32_bf16 a[88:91], a[204:207], v[44:47], a[88:91]\n\t" \
  "s_waitcnt lgkmcnt(4)\n\t" \
  "v_xor_b32 v236, 0x200e0, v234\n\t" \
  "v_cvt_pk_bf16_f32 v168, v160, v161\n\t" \
  "v_mfma_f32_16x16x32_bf16 a[96:99], a[208:211], v[16:19], a[96:99]\n\t" \
  "ds_read_b64_tr_b16 a[192:193], v236 offset:0\n\t" \
  ".Lhs14:\n\ts_cbranch_execz .Lhs14n\n\t" \
  "v_cvt_pk_bf16_f32 v169, v162, v163\n\t" \
  "v_mfma_f32_16x16x32_bf16 a[96:99], a[212:215], v[20:23], a[96:99]\n\t" \
  "s_waitcnt lgkmcnt(1)\n\t" \
  "ds_read_b64_tr_b16 a[194:195], v236 offset:4096\n\t" \
  "v_cvt_pk_bf16_f32 v170, v164, v165\n\t" \
  "v_mfma_f32_16x16x32_bf16 a[96:99], a[216:219], v[24:27], a[96:99]\n\t" \
  "ds_read_b64_tr_b16 a[196:197], v236 offset:8192\n\t" \
  "v_cvt_pk_bf16_f32 v171, v166, v167\n\t" \
  "s_xor_b32 s76, s60, 2048\n\t" \
  "v_mfma_f32_16x16x32_bf16 a[96:99], a[220:223], v[28:31], a[96:99]\n\t" \
  "buffer_store_dwordx4 v[168:171], v242, %[ws], s76 offen sc1\n\t" \
  "v_mfma_f32_16x16x32_bf16 a[100:103], a[220:223], v[12:15], a[100:103]\n\t" \
  "ds_read_b64_tr_b16 a[198:199], v236 offset:12288\n\t" \
  "v_accvgpr_read_b32 v160, a68\n\t" \
  "v_mfma_f32_16x16x32_bf16 a[100:103], a[216:219], v[8:11], a[100:103]\n\t" \
  "v_xor_b32 v237, 0xe0, v234\n\t" \
  "v_accvgpr_read_b32 v161, a69\n\t" \
  "v_mfma_f32_16x16x32_bf16 a[100:103], a[212:215], v[4:7], a[100:103]\n\t" \
  "ds_read_b64_tr_b16 a[200:201], v237 offset:0\n\t" \
  "v_accvgpr_read_b32 v162, a70\n\t" \
  "v_accvgpr_read_b32 v163, a71\n\t" \
  "v_mfma_f32_16x16x32_bf16 a[100:103], a[208:211], v[0:3], a[100:103]\n\t" \
  "ds_read_b64_tr_b16 a[202:203], v237 offset:4096\n\t" \
  "v_accvgpr_read_b32 v164, a84\n\t" \
  "v_accvgpr_read_b32 v165, a85\n\t" \
  "v_mfma_f32_16x16x32_bf16 a[104:107], a[208:211], v[32:35], a[104:107]\n\t" \
  "ds_read_b64_tr_b16 a[204:205], v237 offset:8192\n\t" \
  "v_accvgpr_read_b32 v166, a86\n\t" \
  "v_accvgpr_read_b32 v167, a87\n\t" \
  "v_mfma_f32_16x16x32_bf16 a[104:107], a[212:215], v[36:39], a[104:107]\n\t" \
  "ds_read_b64_tr_b16 a[206:207], v237 offset:12288\n\t" \
  "v_xor_b32 v235, 0xc000, v234\n\t" \
  "v_cvt_pk_bf16_f32 v174, v160, v161\n\t" \
  "v_mfma_f32_16x16x32_bf16 a[104:107], a[216:219], v[40:43], a[104:107]\n\t" \
  "ds_read_b64_tr_b16 a[32:33], v235 offset:0\n\t" \
  "ds_read_b64_tr_b16 a[34:35], v235 offset:4096\n\t" \
  "ds_read_b64_tr_b16 a[48:49], v235 offset:8192\n\t" \
  "v_mfma_f32_16x16x32_bf16 a[104:107], a[220:223], v[44:47], a[104:107]\n\t" \
  "s_waitcnt lgkmcnt(7)\n\t" \
  "ds_read_b64_tr_b16 a[50:51], v235 offset:12288\n\t" \
  "ds_read_b64_tr_b16 a[36:37], v235 offset:16384\n\t" \
  "ds_read_b64_tr_b16 a[38:39], v235 offset:20480\n\t" \
  "v_mfma_f32_16x16x32_bf16 a[112:115], a[192:195], v[16:19], a[112:115]\n\t" \
  "ds_read_b64_tr_b16 a[52:53], v235 offset:24576\n\t" \
  "ds_read_b64_tr_b16 a[54:55], v235 offset:28672\n\t" \
  "v_xor_b32 v236, 0xc020, v234\n\t" \
  "v_mfma_f32_16x16x32_bf16 a[112:115], a[196:199], v[20:23], a[112:115]\n\t" \
  "s_waitcnt lgkmcnt(8)\n\t" \
  "ds_read_b64_tr_b16 a[0:1], v236 offset:0\n\t" \
  "ds_read_b64_tr_b16 a[2:3], v236 offset:4096\n\t" \
  "ds_read_b64_tr_b16 a[16:17], v236 offset:8192\n\t" \
  "v_mfma_f32_16x16x32_bf16 a[112:115], a[200:203], v[24:27], a[112:115]\n\t" \
  "ds_read_b64_tr_b16 a[18:19], v236 offset:12288\n\t" \
  "ds_read_b64_tr_b16 a[4:5], v236 offset:16384\n\t" \
  "ds_read_b64_tr_b16 a[6:7], v236 offset:20480\n\t" \
  "v_mfma_f32_16x16x32_bf16 a[112:115], a[204:207], v[28:31], a[112:115]\n\t" \
  "ds_read_b64_tr_b16 a[20:21], v236 offset:24576\n\t" \
  "ds_read_b64_tr_b16 a[22:23], v236 offset:28672\n\t" \
  "v_xor_b32 v237, 0xc040, v234\n\t" \
  "v_mfma_f32_16x16x32_bf16 a[116:119], a[204:207], v[12:15], a[116:119]\n\t" \
  "ds_read_b64_tr_b16 a[64:65], v237 offset:0\n\t" \
  "ds_read_b64_tr_b16 a[66:67], v237 offset:4096\n\t" \
  "v_cvt_pk_bf16_f32 v175, v162, v163\n\t" \
  "v_mfma_f32_16x16x32_bf16 a[116:119], a[200:203], v[8:11], a[116:119]\n\t" \
  "ds_read_b64_tr_b16 a[80:81], v237 offset:8192\n\t" \
  "v_cvt_pk_bf16_f32 v172, v164, v165\n\t" \
  "v_cvt_pk_bf16_f32 v173, v166, v167\n\t" \
  "v_mfma_f32_16x16x32_bf16 a[116:119], a[196:199], v[4:7], a[116:119]\n\t" \
  "s_xor_b32 s76, s61, 2048\n\t" \
  "ds_read_b64_tr_b16 a[82:83], v237 offset:12288\n\t" \
  "ds_read_b64_tr_b16 a[68:69], v237 offset:16384\n\t" \
  "ds_read_b64_tr_b16 a[70:71], v237 offset:20480\n\t" \
  "v_mfma_f32_16x16x32_bf16 a[116:119], a[192:195], v[0:3], a[116:119]\n\t" \
  "buffer_store_dwordx4 v[172:175], v242, %[ws], s76 offen sc1\n\t" \
  "ds_read_b64_tr_b16 a[84:85], v237 offset:24576\n\t" \
  "v_mfma_f32_16x16x32_bf16 a[120:123], a[192:195], v[32:35], a[120:123]\n\t" \
  "ds_read_b64_tr_b16 a[86:87], v237 offset:28672\n\t" \
  "v_xor_b32 v235, 0xc060, v234\n\t" \
  "v_mfma_f32_16x16x32_bf16 a[120:123], a[196:199], v[36:39], a[120:123]\n\t" \
  "ds_read_b64_tr_b16 a[208:209], v235 offset:0\n\t" \
  "ds_read_b64_tr_b16 a[210:211], v235 offset:4096\n\t" \
  "ds_read_b64_tr_b16 a[212:213], v235 offset:8192\n\t" \
  "v_mfma_f32_16x16x32_bf16 a[120:123], a[200:203], v[40:43], a[120:123]\n\t" \
  "ds_read_b64_tr_b16 a[214:215], v235 offset:12288\n\t" \
  "ds_read_b64_tr_b16 a[216:217], v235 offset:16384\n\t" \
  "ds_read_b64_tr_b16 a[218:219], v235 offset:20480\n\t" \
  "v_mfma_f32_16x16x32_bf16 a[120:123], a[204:207], v[44:47], a[120:123]\n\t" \
  "s_waitcnt vmcnt(17) lgkmcnt(15)\n\t" \
  "ds_read_b64_tr_b16 a[220:221], v235 offset:24576\n\t" \
  "ds_read_b64_tr_b16 a[222:223], v235 offset:28672\n\t" \
  "v_accvgpr_read_b32 v160, a96\n\t" \
  "v_mfma_f32_16x16x32_bf16 a[8:11], a[32:35], v[208:211], a[8:11]\n\t" \
  "v_accvgpr_read_b32 v161, a97\n\t" \
  "v_accvgpr_read_b32 v162, a98\n\t" \
  "v_accvgpr_read_b32 v163, a99\n\t" \
  "v_mfma_f32_16x16x32_bf16 a[8:11], a[48:51], v[212:215], a[8:11]\n\t" \
  "v_accvgpr_read_b32 v164, a112\n\t" \
  "v_accvgpr_read_b32 v165, a113\n\t" \
  "v_accvgpr_read_b32 v166, a114\n\t" \
  "v_mfma_f32_16x16x32_bf16 a[8:11], a[36:39], v[216:219], a[8:11]\n\t" \
  "v_xor_b32 v236, 0xc080, v234\n\t" \
  "v_accvgpr_read_b32 v167, a115\n\t" \
  "v_cvt_pk_bf16_f32 v168, v160, v161\n\t" \
  "v_mfma_f32_16x16x32_bf16 a[8:11], a[52:55], v[220:223], a[8:11]\n\t" \
  "ds_read_b64_tr_b16 v[32:33], v236 offset:0\n\t" \
  "v_cvt_pk_bf16_f32 v169, v162, v163\n\t" \
  "v_cvt_pk_bf16_f32 v170, v164, v165\n\t" \
  "v_mfma_f32_16x16x32_bf16 a[24:27], a[0:3], v[208:211], a[24:27]\n\t" \
  "ds_read_b64_tr_b16 v[34:35], v236 offset:4096\n\t" \
  "v_cvt_pk_bf16_f32 v171, v166, v167\n\t" \
  "s_xor_b32 s76, s60, 3072\n\t" \
  "v_mfma_f32_16x16x32_bf16 a[24:27], a[16:19], v[212:215], a[24:27]\n\t" \
  "s_waitcnt lgkmcnt(15)\n\t" \
  "buffer_store_dwordx4 v[168:171], v242, %[ws], s76 offen sc1\n\t" \
  "v_accvgpr_read_b32 v160, a100\n\t" \
  "v_mfma_f32_16x16x32_bf16 a[24:27], a[4:7], v[216:219], a[24:27]\n\t" \
  "ds_read_b64_tr_b16 v[36:37], v236 offset:8192\n\t" \
  "ds_read_b64_tr_b16 v[38:39], v236 offset:12288\n\t" \
  "v_accvgpr_read_b32 v161, a101\n\t" \
  "v_mfma_f32_16x16x32_bf16 a[24:27], a[20:23], v[220:223], a[24:27]\n\t" \
  "s_waitcnt lgkmcnt(15)\n\t" \
  "ds_read_b64_tr_b16 v[40:41], v236 offset:16384\n\t" \
  "v_accvgpr_read_b32 v162, a102\n\t" \
  "v_accvgpr_read_b32 v163, a103\n\t" \
  "v_mfma_f32_16x16x32_bf16 a[40:43], a[64:67], v[208:211], a[40:43]\n\t" \
  "ds_read_b64_tr_b16 v[42:43], v236 offset:20480\n\t" \
  "v_accvgpr_read_b32 v164, a116\n\t" \
  "v_accvgpr_read_b32 v165, a117\n\t" \
  "v_mfma_f32_16x16x32_bf16 a[40:43], a[80:83], v[212:215], a[40:43]\n\t" \
  "s_waitcnt lgkmcnt(14)\n\t" \
  "ds_read_b64_tr_b16 v[44:45], v236 offset:24576\n\t" \
  "v_accvgpr_read_b32 v166, a118\n\t" \
  "v_accvgpr_read_b32 v167, a119\n\t" \
  "v_mfma_f32_16x16x32_bf16 a[40:43], a[68:71], v[216:219], a[40:43]\n\t" \
  "ds_read_b64_tr_b16 v[46:47], v236 offset:28672\n\t" \
  "v_xor_b32 v237, 0xc0a0, v234\n\t" \
  "v_cvt_pk_bf16_f32 v174, v160, v161\n\t" \
  "v_mfma_f32_16x16x32_bf16 a[40:43], a[84:87], v[220:223], a[40:43]\n\t" \
  "s_waitcnt lgkmcnt(12)\n\t" \
  "ds_read_b64_tr_b16 a[192:193], v237 offset:0\n\t" \
  "ds_read_b64_tr_b16 a[194:195], v237 offset:4096\n\t" \
  "ds_read_b64_tr_b16 a[196:197], v237 offset:8192\n\t" \
  "v_mfma_f32_16x16x32_bf16 a[56:59], a[208:211], v[208:211], a[56:59]\n\t" \
  "s_add_u32 m0, %[m], 0\n\t" \
  "ds_read_b64_tr_b16 a[198:199], v237 offset:12288\n\t" \
  "ds_read_b64_tr_b16 a[200:201], v237 offset:16384\n\t" \
  "ds_read_b64_tr_b16 a[202:203], v237 offset:20480\n\t" \
  "v_mfma_f32_16x16x32_bf16 a[56:59], a[212:215], v[212:215], a[56:59]\n\t" \
  "s_waitcnt lgkmcnt(14)\n\t" \
  "ds_read_b64_tr_b16 a[204:205], v237 offset:24576\n\t" \
  "ds_read_b64_tr_b16 a[206:207], v237 offset:28672\n\t" \
  "v_xor_b32 v235, 0xc0c0, v234\n\t" \
  "v_mfma_f32_16x16x32_bf16 a[56:59], a[216:219], v[216:219], a[56:59]\n\t" \
  "ds_read_b64_tr_b16 v[0:1], v235 offset:0\n\t" \
  "ds_read_b64_tr_b16 v[2:3], v235 offset:4096\n\t" \
  "ds_read_b64_tr_b16 v[4:5], v235 offset:8192\n\t" \
  "v_mfma_f32_16x16x32_bf16 a[56:59], a[220:223], v[220:223], a[56:59]\n\t" \
  "s_waitcnt lgkmcnt(15)\n\t" \
  "s_barrier\n\t" \
  "v_mfma_f32_16x16x32_bf16 a[72:75], v[32:35], v[208:211], a[72:75]\n\t" \
  "s_xor_b32 s71, %[eso], 753664\n\t" \
  "ds_read_b64_tr_b16 v[6:7], v235 offset:12288\n\t" \
  "v_cvt_pk_bf16_f32 v175, v162, v163\n\t" \
  "v_cvt_pk_bf16_f32 v172, v164, v165\n\t" \
  "v_mfma_f32_16x16x32_bf16 a[72:75], v[36:39], v[212:215], a[72:75]\n\t" \
  "s_waitcnt lgkmcnt(12)\n\t" \
  "ds_read_b64_tr_b16 v[8:9], v235 offset:16384\n\t" \
  "v_cvt_pk_bf16_f32 v173, v166, v167\n\t" \
  "s_xor_b32 s76, s61, 3072\n\t" \
  "v_mfma_f32_16x16x32_bf16 a[72:75], v[40:43], v[216:219], a[72:75]\n\t" \
  "ds_read_b64_tr_b16 v[10:11], v235 offset:20480\n\t" \
  "ds_read_b64_tr_b16 v[12:13], v235 offset:24576\n\t" \
  "ds_read_b64_tr_b16 v[14:15], v235 offset:28672\n\t" \
  "v_mfma_f32_16x16x32_bf16 a[72:75], v[44:47], v[220:223], a[72:75]\n\t" \
  "s_waitcnt lgkmcnt(12)\n\t" \
  "v_xor_b32 v236, 0xc0e0, v234\n\t" \
  "v_mfma_f32_16x16x32_bf16 a[88:91], a[192:195], v[208:211], a[88:91]\n\t" \
  "ds_read_b64_tr_b16 v[16:17], v236 offset:0\n\t" \
  "ds_read_b64_tr_b16 v[18:19], v236 offset:4096\n\t" \
  "ds_read_b64_tr_b16 v[20:21], v236 offset:8192\n\t" \
  "v_mfma_f32_16x16x32_bf16 a[88:91], a[196:199], v[212:215], a[88:91]\n\t" \
  "s_waitcnt lgkmcnt(11)\n\t" \
  "buffer_store_dwordx4 v[172:175], v242, %[ws], s76 offen sc1\n\t" \
  "v_accvgpr_read_b32 v160, a8\n\t" \
  "v_mfma_f32_16x16x32_bf16 a[88:91], a[200:203], v[216:219], a[88:91]\n\t" \
  "ds_read_b64_tr_b16 v[22:23], v236 offset:12288\n\t" \
  "v_accvgpr_read_b32 v161, a9\n\t" \
  "v_accvgpr_read_b32 v162, a10\n\t" \
  "v_mfma_f32_16x16x32_bf16 a[88:91], a[204:207], v[220:223], a[88:91]\n\t" \
  "s_waitcnt lgkmcnt(8)\n\t" \
  "ds_read_b64_tr_b16 v[24:25], v236 offset:16384\n\t" \
  "v_accvgpr_read_b32 v163, a11\n\t" \
  "v_accvgpr_read_b32 v164, a24\n\t" \
  "v_mfma_f32_16x16x32_bf16 a[104:107], v[0:3], v[208:211], a[104:107]\n\t" \
  "ds_read_b64_tr_b16 v[26:27], v236 offset:20480\n\t" \
  "v_accvgpr_read_b32 v165, a25\n\t" \
  "v_accvgpr_read_b32 v166, a26\n\t" \
  "v_mfma_f32_16x16x32_bf16 a[104:107], v[4:7], v[212:215], a[104:107]\n\t" \
  "s_waitcnt lgkmcnt(6)\n\t" \
  "ds_read_b64_tr_b16 v[28:29], v236 offset:24576\n\t" \
  "v_accvgpr_read_b32 v167, a27\n\t" \
  "v_cvt_pk_bf16_f32 v168, v160, v161\n\t" \
  "v_mfma_f32_16x16x32_bf16 a[104:107], v[8:11], v[216:219], a[104:107]\n\t" \
  "ds_read_b64_tr_b16 v[30:31], v236 offset:28672\n\t" \
  "ds_read_b128 a[208:211], v242 offset:16384\n\t" \
  "v_cvt_pk_bf16_f32 v169, v162, v163\n\t" \
  "v_mfma_f32_16x16x32_bf16 a[104:107], v[12:15], v[220:223], a[104:107]\n\t" \
  "s_waitcnt lgkmcnt(5)\n\t" \
  "ds_read_b128 a[212:215], v242 offset:17408\n\t" \
  "v_cvt_pk_bf16_f32 v170, v164, v165\n\t" \
  "v_cvt_pk_bf16_f32 v171, v166, v167\n\t" \
  "v_mfma_f32_16x16x32_bf16 a[120:123], v[16:19], v[208:211], a[120:123]\n\t" \
  "s_xor_b32 s76, s62, 0\n\t" \
  "ds_read_b128 a[216:219], v242 offset:18432\n\t" \
  "ds_read_b128 a[220:223], v242 offset:19456\n\t" \
  "v_mfma_f32_16x16x32_bf16 a[120:123], v[20:23], v[212:215], a[120:123]\n\t" \
  "s_waitcnt lgkmcnt(4)\n\t" \
  "buffer_store_dwordx4 v[168:171], v242, %[ws], s76 offen sc1\n\t" \
  "v_accvgpr_read_b32 v160, a40\n\t" \
  "v_mfma_f32_16x16x32_bf16 a[120:123], v[24:27], v[216:219], a[120:123]\n\t" \
  "ds_read_b128 v[32:35], v242 offset:20480\n\t" \
  "v_accvgpr_read_b32 v161, a41\n\t" \
  "v_accvgpr_read_b32 v162, a42\n\t" \
  "v_mfma_f32_16x16x32_bf16 a[120:123], v[28:31], v[220:223], a[120:123]\n\t" \
  "s_waitcnt lgkmcnt(1)\n\t" \
  "ds_read_b128 v[36:39], v242 offset:21504\n\t" \
  "v_accvgpr_read_b32 v163, a43\n\t" \
  "v_accvgpr_read_b32 v164, a56\n\t" \
  STP("v_writelane_b32 %[stv], s94, 34\n\t") \
  STP("v_writelane_b32 %[stv], s92, 35\n\t") \
  STP("s_memrealtime s[94:95]\n\t") \
  STP("s_memtime s[92:93]\n\t") \
  "v_mfma_f32_16x16x32_bf16 v[96:99], v[64:67], a[208:211], v[96:99]\n\t" \
  "ds_read_b128 v[40:43], v242 offset:22528\n\t" \
  "v_accvgpr_read_b32 v165, a57\n\t" \
  "v_accvgpr_read_b32 v166, a58\n\t" \
  "v_mfma_f32_16x16x32_bf16 v[96:99], v[68:71], a[212:215], v[96:99]\n\t" \
  "ds_read_b128 v[44:47], v242 offset:23552\n\t" \
  "v_accvgpr_read_b32 v167, a59\n\t" \
  "v_cvt_pk_bf16_f32 v172, v160, v161\n\t" \
  "v_mfma_f32_16x16x32_bf16 v[96:99], v[72:75], a[216:219], v[96:99]\n\t" \
  "ds_read_b128 a[192:195], v242 offset:24576\n\t" \
  "v_cvt_pk_bf16_f32 v173, v162, v163\n\t" \
  "v_cvt_pk_bf16_f32 v174, v164, v165\n\t" \
  "v_mfma_f32_16x16x32_bf16 v[96:99], v[76:79], a[220:223], v[96:99]\n\t" \
  "ds_read_b128 a[196:199], v242 offset:25600\n\t" \
  "v_cvt_pk_bf16_f32 v175, v166, v167\n\t" \
  "s_xor_b32 s76, s62, 1024\n\t" \
  "v_mfma_f32_16x16x32_bf16 v[112:115], v[92:95], a[220:223], v[112:115]\n\t" \
  "buffer_store_dwordx4 v[172:175], v242, %[ws], s76 offen sc1\n\t" \
  "v_accvgpr_read_b32 v160, a72\n\t" \
  "v_mfma_f32_16x16x32_bf16 v[112:115], v[88:91], a[216:219], v[112:115]\n\t" \
  "ds_read_b128 a[200:203], v242 offset:26624\n\t" \
  "v_accvgpr_read_b32 v161, a73\n\t" \
  "v_accvgpr_read_b32 v162, a74\n\t" \
  "v_mfma_f32_16x16x32_bf16 v[112:115], v[84:87], a[212:215], v[112:115]\n\t" \
  "ds_read_b128 a[204:207], v242 offset:27648\n\t" \
  "v_accvgpr_read_b32 v163, a75\n\t" \
  "v_accvgpr_read_b32 v164, a88\n\t" \
  "v_mfma_f32_16x16x32_bf16 v[112:115], v[80:83], a[208:211], v[112:115]\n\t" \
  "s_waitcnt lgkmcnt(4)\n\t" \
  "v_accvgpr_read_b32 v165, a89\n\t" \
  "v_accvgpr_read_b32 v166, a90\n\t" \
  "v_accvgpr_read_b32 v167, a91\n\t" \
  "v_mfma_f32_16x16x32_bf16 v[100:103], v[64:67], v[32:35], v[100:103]\n\t" \
  "buffer_load_dwordx4 v242, %[ws], s71 offen offset:0 sc1 lds\n\t" \
  "v_cvt_pk_bf16_f32 v168, v160, v161\n\t" \
  "v_mfma_f32_16x16x32_bf16 v[100:103], v[68:71], v[36:39], v[100:103]\n\t" \
  "ds_read_b128 a[208:211], v242 offset:28672\n\t" \
  "v_cvt_pk_bf16_f32 v169, v162, v163\n\t" \
  "v_cvt_pk_bf16_f32 v170, v164, v165\n\t" \
  "v_mfma_f32_16x16x32_bf16 v[100:103], v[72:75], v[40:43], v[100:103]\n\t" \
  "ds_read_b128 a[212:215], v242 offset:29696\n\t" \
  "v_cvt_pk_bf16_f32 v171, v166, v167\n\t" \
  "s_xor_b32 s76, s62, 2048\n\t" \
  "v_mfma_f32_16x16x32_bf16 v[100:103], v[76:79], v[44:47], v[100:103]\n\t" \
  "buffer_store_dwordx4 v[168:171], v242, %[ws], s76 offen sc1\n\t" \
  "v_accvgpr_read_b32 v160, a104\n\t" \
  "v_mfma_f32_16x16x32_bf16 v[116:119], v[92:95], v[44:47], v[116:119]\n\t" \
  "s_mov_b32 s74, 0\n\t" \
  "ds_read_b128 a[216:219], v242 offset:30720\n\t" \
  "v_accvgpr_read_b32 v161, a105\n\t" \
  "v_accvgpr_read_b32 v162, a106\n\t" \
  "v_mfma_f32_16x16x32_bf16 v[116:119], v[88:91], v[40:43], v[116:119]\n\t" \
  "buffer_load_dwordx4 v242, %[ws], s71 offen offset:1024 sc1 lds\n\t" \
  "v_accvgpr_read_b32 v163, a107\n\t" \
  "v_mfma_f32_16x16x32_bf16 v[116:119], v[84:87], v[36:39], v[116:119]\n\t" \
  "ds_read_b128 a[220:223], v242 offset:31744\n\t" \
  "v_accvgpr_read_b32 v164, a120\n\t" \
  "v_accvgpr_read_b32 v165, a121\n\t" \
  "v_mfma_f32_16x16x32_bf16 v[116:119], v[80:83], v[32:35], v[116:119]\n\t" \
  "s_waitcnt lgkmcnt(4)\n\t" \
  "s_barrier\n\t" \
  "v_mfma_f32_16x16x32_bf16 v[104:107], v[64:67], a[192:195], v[104:107]\n\t" \
  "ds_read_b128 v[208:211], v242 offset:32768\n\t" \
  "v_accvgpr_read_b32 v166, a122\n\t" \
  "v_accvgpr_read_b32 v167, a123\n\t" \
  "v_mfma_f32_16x16x32_bf16 v[104:107], v[68:71], a[196:199], v[104:107]\n\t" \
  "ds_read_b128 v[212:215], v242 offset:33792\n\t" \
  "v_cvt_pk_bf16_f32 v172, v160, v161\n\t" \
  "v_cvt_pk_bf16_f32 v173, v162, v163\n\t" \
  "v_mfma_f32_16x16x32_bf16 v[104:107], v[72:75], a[200:203], v[104:107]\n\t" \
  "ds_read_b128 v[216:219], v242 offset:34816\n\t" \
  "v_cvt_pk_bf16_f32 v174, v164, v165\n\t" \
  "v_cvt_pk_bf16_f32 v175, v166, v167\n\t" \
  "v_mfma_f32_16x16x32_bf16 v[104:107], v[76:79], a[204:207], v[104:107]\n\t" \
  "s_xor_b32 s76, s62, 3072\n\t" \
  "ds_read_b128 v[220:223], v242 offset:35840\n\t" \
  "buffer_store_dwordx4 v[172:175], v242, %[ws], s76 offen sc1\n\t" \
  "v_mfma_f32_16x16x32_bf16 v[120:123], v[92:95], a[204:207], v[120:123]\n\t" \
  "v_mfma_f32_16x16x32_bf16 v[120:123], v[88:91], a[200:203], v[120:123]\n\t" \
  "buffer_load_dwordx4 v242, %[ws], s71 offen offset:2048 sc1 lds\n\t" \
  "v_mfma_f32_16x16x32_bf16 v[120:123], v[84:87], a[196:199], v[120:123]\n\t" \
  "v_mfma_f32_16x16x32_bf16 v[120:123], v[80:83], a[192:195], v[120:123]\n\t" \
  "s_waitcnt lgkmcnt(4)\n\t" \
  "buffer_load_dwordx4 v242, %[ws], s71 offen offset:3072 sc1 lds\n\t" \
  "s_add_u32 m0, %[m], 131072\n\t" \
  "v_mfma_f32_16x16x32_bf16 v[108:111], v[64:67], a[208:211], v[108:111]\n\t" \
  "s_add_u32 s71, %[xso], 0\n\t" \
  "ds_read_b128 a[192:195], v242 offset:36864\n\t" \
  "v_mfma_f32_16x16x32_bf16 v[108:111], v[68:71], a[212:215], v[108:111]\n\t" \
  "ds_read_b128 a[196:199], v242 offset:37888\n\t" \
  "v_mfma_f32_16x16x32_bf16 v[108:111], v[72:75], a[216:219], v[108:111]\n\t" \
  "ds_read_b128 a[200:203], v242 offset:38912\n\t" \
  "v_mfma_f32_16x16x32_bf16 v[108:111], v[76:79], a[220:223], v[108:111]\n\t" \
  "buffer_load_dwordx4 v248, %[sx], s71 offen offset:0 lds\n\t" \
  "s_add_u32 s71, %[xso], 7168\n\t" \
  "v_mfma_f32_16x16x32_bf16 v[124:127], v[92:95], a[220:223], v[124:127]\n\t" \
  "ds_read_b128 a[204:207], v242 offset:39936\n\t" \
  "v_mfma_f32_16x16x32_bf16 v[124:127], v[88:91], a[216:219], v[124:127]\n\t" \
  "v_mfma_f32_16x16x32_bf16 v[124:127], v[84:87], a[212:215], v[124:127]\n\t" \
  "v_xor_b32 v237, 0x14000, v234\n\t" \
  "v_mfma_f32_16x16x32_bf16 v[124:127], v[80:83], a[208:211], v[124:127]\n\t" \
  "s_waitcnt lgkmcnt(4)\n\t" \
  "buffer_load_dwordx4 v249, %[sx], s71 offen offset:1024 lds\n\t" \
  "s_add_u32 s71, %[xso], 14336\n\t" \
  "v_mfma_f32_16x16x32_bf16 v[96:99], a[160:163], v[208:211], v[96:99]\n\t" \
  "ds_read_b64_tr_b16 a[96:97], v237 offset:0\n\t" \
  "v_mfma_f32_16x16x32_bf16 v[96:99], a[164:167], v[212:215], v[96:99]\n\t" \
  "ds_read_b64_tr_b16 a[98:99], v237 offset:4096\n\t" \
  "v_mfma_f32_16x16x32_bf16 v[96:99], a[168:171], v[216:219], v[96:99]\n\t" \
  "s_waitcnt vmcnt(10)\n\t" \
  "buffer_store_dword v243, v225, %[ws], 0 offen sc1\n\t" \
  "v_mfma_f32_16x16x32_bf16 v[96:99], a[172:175], v[220:223], v[96:99]\n\t" \
  "ds_read_b64_tr_b16 a[112:113], v237 offset:8192\n\t" \
  "v_mfma_f32_16x16x32_bf16 v[112:115], a[188:191], v[220:223], v[112:115]\n\t" \
  "buffer_load_dwordx4 v250, %[sx], s71 offen offset:2048 lds\n\t" \
  "s_add_u32 s71, %[xso], 21504\n\t" \
  "v_mfma_f32_16x16x32_bf16 v[112:115], a[184:187], v[216:219], v[112:115]\n\t" \
  "ds_read_b64_tr_b16 a[114:115], v237 offset:12288\n\t" \
  "v_mfma_f32_16x16x32_bf16 v[112:115], a[180:183], v[212:215], v[112:115]\n\t" \
  "ds_read_b64_tr_b16 a[100:101], v237 offset:32768\n\t" \
  "v_mfma_f32_16x16x32_bf16 v[112:115], a[176:179], v[208:211], v[112:115]\n\t" \
  "s_waitcnt lgkmcnt(5)\n\t" \
  "ds_read_b64_tr_b16 a[102:103], v237 offset:36864\n\t" \
  "v_mfma_f32_16x16x32_bf16 v[100:103], a[160:163], a[192:195], v[100:103]\n\t" \
  "ds_read_b64_tr_b16 a[116:117], v237 offset:40960\n\t" \
  "v_mfma_f32_16x16x32_bf16 v[100:103], a[164:167], a[196:199], v[100:103]\n\t" \
  "buffer_load_dwordx4 v251, %[sx], s71 offen offset:3072 lds\n\t" \
  "s_add_u32 m0, %[m], 49152\n\t" \
  "v_mfma_f32_16x16x32_bf16 v[100:103], a[168:171], a[200:203], v[100:103]\n\t" \
  "s_xor_b32 s71, %[eso], 622592\n\t" \
  "ds_read_b64_tr_b16 a[118:119], v237 offset:45056\n\t" \
  "ds_read_b128 a[208:211], v242 offset:40960\n\t" \
  "ds_read_b128 a[212:215], v242 offset:41984\n\t" \
  "v_mfma_f32_16x16x32_bf16 v[100:103], a[172:175], a[204:207], v[100:103]\n\t" \
  "ds_read_b128 a[216:219], v242 offset:43008\n\t" \
  "ds_read_b128 a[220:223], v242 offset:44032\n\t" \
  "v_mfma_f32_16x16x32_bf16 v[116:119], a[188:191], a[204:207], v[116:119]\n\t" \
  "ds_read_b128 v[208:211], v242 offset:45056\n\t" \
  "v_mfma_f32_16x16x32_bf16 v[116:119], a[184:187], a[200:203], v[116:119]\n\t" \
  "buffer_load_dwordx4 v242, %[ws], s71 offen offset:0 sc1 lds\n\t" \
  "v_mfma_f32_16x16x32_bf16 v[116:119], a[180:183], a[196:199], v[116:119]\n\t" \
  "ds_read_b128 v[212:215], v242 offset:46080\n\t" \
  "ds_read_b128 v[216:219], v242 offset:47104\n\t" \
  "v_mfma_f32_16x16x32_bf16 v[116:119], a[176:179], a[192:195], v[116:119]\n\t" \
  "s_waitcnt lgkmcnt(3)\n\t" \
  "s_barrier\n\t" \
  "v_mfma_f32_16x16x32_bf16 v[104:107], a[160:163], a[208:211], v[104:107]\n\t" \
  "ds_read_b128 v[220:223], v242 offset:48128\n\t" \
  "v_xor_b32 v235, 0x14020, v234\n\t" \
  "v_mfma_f32_16x16x32_bf16 v[104:107], a[164:167], a[212:215], v[104:107]\n\t" \
  "buffer_load_dwordx4 v242, %[ws], s71 offen offset:1024 sc1 lds\n\t" \
  "ds_read_b64_tr_b16 a[64:65], v235 offset:0\n\t" \
  "v_mfma_f32_16x16x32_bf16 v[104:107], a[168:171], a[216:219], v[104:107]\n\t" \
  "ds_read_b64_tr_b16 a[66:67], v235 offset:4096\n\t" \
  "ds_read_b64_tr_b16 a[68:69], v235 offset:8192\n\t" \
  "ds_read_b64_tr_b16 a[70:71], v235 offset:12288\n\t" \
  "v_mfma_f32_16x16x32_bf16 v[104:107], a[172:175], a[220:223], v[104:107]\n\t" \
  "ds_read_b64_tr_b16 a[80:81], v235 offset:32768\n\t" \
  "ds_read_b64_tr_b16 a[82:83], v235 offset:36864\n\t" \
  "v_mfma_f32_16x16x32_bf16 v[120:123], a[188:191], a[220:223], v[120:123]\n\t" \
  "ds_read_b64_tr_b16 a[84:85], v235 offset:40960\n\t" \
  "ds_read_b64_tr_b16 a[86:87], v235 offset:45056\n\t" \
  "v_mfma_f32_16x16x32_bf16 v[120:123], a[184:187], a[216:219], v[120:123]\n\t" \
  "ds_read_b128 a[192:195], v230 offset:32768\n\t" \
  "ds_read_b128 a[196:199], v230 offset:33792\n\t" \
  "v_and_b32 v235, 16, v242\n\t" \
  "v_mfma_f32_16x16x32_bf16 v[120:123], a[180:183], a[212:215], v[120:123]\n\t" \
  "buffer_load_dwordx4 v242, %[ws], s71 offen offset:2048 sc1 lds\n\t" \
  "v_accvgpr_read_b32 v224, a224\n\t" \
  "v_mfma_f32_16x16x32_bf16 v[120:123], a[176:179], a[208:211], v[120:123]\n\t" \
  "s_waitcnt lgkmcnt(10)\n\t" \
  "ds_read_b128 a[200:203], v230 offset:34816\n\t" \
  "ds_read_b128 a[204:207], v230 offset:35840\n\t" \
  "v_mov_b32 v229, 0x3f80\n\t" \
  "v_mfma_f32_16x16x32_bf16 v[108:111], a[160:163], v[208:211], v[108:111]\n\t" \
  "ds_read_b128 v[0:3], v230 offset:36864\n\t" \
  "v_accvgpr_read_b32 v227, a228\n\t" \
  "v_lshlrev_b32 v229, v235, v229\n\t" \
  "v_mfma_f32_16x16x32_bf16 v[108:111], a[164:167], v[212:215], v[108:111]\n\t" \
  "s_mov_b32 s82, 0x300003\n\t" \
  "ds_read_b128 v[4:7], v230 offset:37888\n\t" \
  "v_perm_b32 v64, v227, v224, s79\n\t" \
  "v_mfma_f32_16x16x32_bf16 v[108:111], a[168:171], v[216:219], v[108:111]\n\t" \
  "buffer_load_dwordx4 v242, %[ws], s71 offen offset:3072 sc1 lds\n\t" \
  "s_add_u32 m0, %[m], 65536\n\t" \
  "v_mfma_f32_16x16x32_bf16 v[108:111], a[172:175], v[220:223], v[108:111]\n\t" \
  "s_xor_b32 s71, %[eso], 884736\n\t" \
  "s_mov_b32 s83, 0x30000300\n\t" \
  "ds_read_b128 v[8:11], v230 offset:38912\n\t" \
  "v_mfma_f32_16x16x32_bf16 v[124:127], a[188:191], v[220:223], v[124:127]\n\t" \
  "s_mov_b32 s84, 0xc0000c\n\t" \
  "ds_read_b128 v[12:15], v230 offset:39936\n\t" \
  "v_perm_b32 v68, v227, v224, s80\n\t" \
  "v_mfma_f32_16x16x32_bf16 v[124:127], a[184:187], v[216:219], v[124:127]\n\t" \
  "s_mov_b32 s85, 0xc0000c00\n\t" \
  "v_accvgpr_read_b32 v224, a225\n\t" \
  "v_cndmask_b32 v238, 0, v229, s[82:83]\n\t" \
  "v_mfma_f32_16x16x32_bf16 v[124:127], a[180:183], v[212:215], v[124:127]\n\t" \
  "v_cndmask_b32 v239, 0, v229, s[84:85]\n\t" \
  "v_accvgpr_read_b32 v227, a229\n\t" \
  "v_mfma_f32_16x16x32_bf16 v[124:127], a[176:179], v[208:211], v[124:127]\n\t" \
  "s_waitcnt lgkmcnt(4)\n\t" \
  "buffer_load_dwordx4 v242, %[ws], s71 offen offset:0 sc1 lds\n\t" \
  "v_mfma_f32_16x16x32_bf16 v[96:99], a[128:131], a[192:195], v[96:99]\n\t" \
  "ds_read_b128 v[208:211], v230 offset:40960\n\t" \
  "v_and_b32 v236, 15, v244\n\t" \
  "v_mfma_f32_16x16x32_bf16 v[96:99], a[132:135], a[196:199], v[96:99]\n\t" \
  "ds_read_b128 v[212:215], v230 offset:41984\n\t" \
  "v_lshrrev_b32 v229, 4, v244\n\t" \
  "v_mfma_f32_16x16x32_bf16 v[96:99], a[136:139], a[200:203], v[96:99]\n\t" \
  "ds_read_b128 v[216:219], v230 offset:43008\n\t" \
  "v_perm_b32 v65, v227, v224, s79\n\t" \
  "v_mfma_f32_16x16x32_bf16 v[96:99], a[140:143], a[204:207], v[96:99]\n\t" \
  "s_waitcnt vmcnt(12)\n\t" \
  "buffer_store_dword v243, v226, %[ws], 0 offen sc1\n\t" \
  "v_mfma_f32_16x16x32_bf16 v[112:115], a[156:159], a[204:207], v[112:115]\n\t" \
  "ds_read_b128 v[220:223], v230 offset:44032\n\t" \
  "v_lshlrev_b32 v235, 2, v236\n\t" \
  "v_mfma_f32_16x16x32_bf16 v[112:115], a[152:155], a[200:203], v[112:115]\n\t" \
  "buffer_load_dwordx4 v242, %[ws], s71 offen offset:1024 sc1 lds\n\t" \
  "v_mfma_f32_16x16x32_bf16 v[112:115], a[148:151], a[196:199], v[112:115]\n\t" \
  "v_perm_b32 v69, v227, v224, s80\n\t" \
  "v_mul_u32_u24 v236, 80, v236\n\t" \
  "v_mfma_f32_16x16x32_bf16 v[112:115], a[144:147], a[192:195], v[112:115]\n\t" \
  "s_waitcnt lgkmcnt(4)\n\t" \
  "v_lshlrev_b32 v237, 4, v229\n\t" \
  "v_accvgpr_read_b32 v224, a226\n\t" \
  "v_mfma_f32_16x16x32_bf16 v[100:103], a[128:131], v[0:3], v[100:103]\n\t" \
  "buffer_load_dwordx4 v242, %[ws], s71 offen offset:2048 sc1 lds\n\t" \
  "v_mfma_f32_16x16x32_bf16 v[100:103], a[132:135], v[4:7], v[100:103]\n\t" \
  "ds_read_b128 a[192:195], v230 offset:45056\n\t" \
  "v_add_u32 v237, s70, v237\n\t" \
  "v_mfma_f32_16x16x32_bf16 v[100:103], a[136:139], v[8:11], v[100:103]\n\t" \
  "ds_read_b128 a[196:199], v230 offset:46080\n\t" \
  "v_add_u32 v236, v237, v236\n\t" \
  "v_mfma_f32_16x16x32_bf16 v[100:103], a[140:143], v[12:15], v[100:103]\n\t" \
  "ds_read_b128 a[200:203], v230 offset:47104\n\t" \
  "v_accvgpr_read_b32 v227, a230\n\t" \
  "v_mfma_f32_16x16x32_bf16 v[116:119], a[156:159], v[12:15], v[116:119]\n\t" \
  "buffer_load_dwordx4 v242, %[ws], s71 offen offset:3072 sc1 lds\n\t" \
  "s_add_u32 m0, %[m], 16384\n\t" \
  "v_mfma_f32_16x16x32_bf16 v[116:119], a[152:155], v[8:11], v[116:119]\n\t" \
  "s_xor_b32 s71, %[eso], 229376\n\t" \
  "ds_read_b128 a[204:207], v230 offset:48128\n\t" \
  "ds_read_b128 a[208:211], v236 offset:960\n\t" \
  "v_mfma_f32_16x16x32_bf16 v[116:119], a[148:151], v[4:7], v[116:119]\n\t" \
  "ds_read_b128 a[212:215], v236 offset:640\n\t" \
  "v_perm_b32 v66, v227, v224, s79\n\t" \
  "v_mfma_f32_16x16x32_bf16 v[116:119], a[144:147], v[0:3], v[116:119]\n\t" \
  "s_waitcnt vmcnt(14) lgkmcnt(6)\n\t" \
  "s_barrier\n\t" \
  "v_mfma_f32_16x16x32_bf16 v[104:107], a[128:131], v[208:211], v[104:107]\n\t" \
  "buffer_load_dword v246, v235, %[ws], s68 offen sc1\n\t" \
  "v_mfma_f32_16x16x32_bf16 v[104:107], a[132:135], v[212:215], v[104:107]\n\t" \
  "ds_read_b128 a[160:163], v242 offset:0\n\t" \
  "v_perm_b32 v70, v227, v224, s80\n\t" \
  "v_mfma_f32_16x16x32_bf16 v[104:107], a[136:139], v[216:219], v[104:107]\n\t" \
  "buffer_load_dwordx2 v[32:33], v252, %[sd], s74 offen offset:0\n\t" \
  "v_mfma_f32_16x16x32_bf16 v[104:107], a[140:143], v[220:223], v[104:107]\n\t" \
  "ds_read_b128 a[164:167], v242 offset:1024\n\t" \
  "v_accvgpr_read_b32 v224, a227\n\t" \
  "v_mfma_f32_16x16x32_bf16 v[120:123], a[156:159], v[220:223], v[120:123]\n\t" \
  "ds_read_b128 a[168:171], v242 offset:2048\n\t" \
  "v_accvgpr_read_b32 v227, a231\n\t" \
  "v_mfma_f32_16x16x32_bf16 v[120:123], a[152:155], v[216:219], v[120:123]\n\t" \
  "ds_read_b128 a[172:175], v242 offset:3072\n\t" \
  "v_perm_b32 v67, v227, v224, s79\n\t" \
  "v_mfma_f32_16x16x32_bf16 v[120:123], a[148:151], v[212:215], v[120:123]\n\t" \
  "buffer_load_dwordx2 v[34:35], v252, %[sd], s74 offen offset:2048\n\t" \
  "s_mov_b32 s74, 4096\n\t" \
  "v_mfma_f32_16x16x32_bf16 v[120:123], a[144:147], v[208:211], v[120:123]\n\t" \
  "s_waitcnt lgkmcnt(6)\n\t" \
  "ds_read_b128 a[216:219], v236 offset:320\n\t" \
  "v_perm_b32 v71, v227, v224, s80\n\t" \
  "v_mfma_f32_16x16x32_bf16 v[108:111], a[128:131], a[192:195], v[108:111]\n\t" \
  "ds_read_b128 v[208:211], v242 offset:4096\n\t" \
  "v_accvgpr_read_b32 v224, a232\n\t" \
  "v_mfma_f32_16x16x32_bf16 v[108:111], a[132:135], a[196:199], v[108:111]\n\t" \
  "ds_read_b128 v[212:215], v242 offset:5120\n\t" \
  "v_accvgpr_read_b32 v227, a236\n\t" \
  "v_mfma_f32_16x16x32_bf16 v[108:111], a[136:139], a[200:203], v[108:111]\n\t" \
  "buffer_load_dwordx2 v[36:37], v252, %[sd], s74 offen offset:0\n\t" \
  "v_mfma_f32_16x16x32_bf16 v[108:111], a[140:143], a[204:207], v[108:111]\n\t" \
  "ds_read_b128 v[216:219], v242 offset:6144\n\t" \
  "v_perm_b32 v72, v227, v224, s79\n\t" \
  "v_mfma_f32_16x16x32_bf16 v[124:127], a[156:159], a[204:207], v[124:127]\n\t" \
  "ds_read_b128 v[220:223], v242 offset:7168\n\t" \
  "v_perm_b32 v76, v227, v224, s80\n\t" \
  "v_mfma_f32_16x16x32_bf16 v[124:127], a[152:155], a[200:203], v[124:127]\n\t" \
  "buffer_load_dwordx2 v[38:39], v252, %[sd], s74 offen offset:2048\n\t" \
  "s_mov_b32 s74, 8192\n\t" \
  "v_mfma_f32_16x16x32_bf16 v[124:127], a[148:151], a[196:199], v[124:127]\n\t" \
  "ds_read_b128 a[220:223], v236 offset:0\n\t" \
  "v_accvgpr_read_b32 v224, a233\n\t" \
  "v_mfma_f32_16x16x32_bf16 v[124:127], a[144:147], a[192:195], v[124:127]\n\t" \
  "s_waitcnt lgkmcnt(6)\n\t" \
  "v_accvgpr_read_b32 v227, a237\n\t" \
  "v_perm_b32 v73, v227, v224, s79\n\t" \
  "v_mfma_f32_16x16x32_bf16 v[96:99], v[128:131], a[160:163], v[96:99]\n\t" \
  "ds_read_b128 a[128:131], v242 offset:8192\n\t" \
  "v_mfma_f32_16x16x32_bf16 v[96:99], v[132:135], a[164:167], v[96:99]\n\t" \
  "buffer_load_dwordx2 v[40:41], v252, %[sd], s74 offen offset:0\n\t" \
  "v_mfma_f32_16x16x32_bf16 v[96:99], v[136:139], a[168:171], v[96:99]\n\t" \
  "ds_read_b128 a[132:135], v242 offset:9216\n\t" \
  "v_perm_b32 v77, v227, v224, s80\n\t" \
  "v_mfma_f32_16x16x32_bf16 v[96:99], v[140:143], a[172:175], v[96:99]\n\t" \
  "ds_read_b128 a[136:139], v242 offset:10240\n\t" \
  "v_mfma_f32_16x16x32_bf16 v[112:115], v[156:159], a[172:175], v[112:115]\n\t" \
  ".Lhs15:\n\ts_cbranch_execz .Lhs15n\n\t" \
  "ds_read_b128 a[140:143], v242 offset:11264\n\t" \
  "v_accvgpr_read_b32 v224, a234\n\t" \
  "v_mfma_f32_16x16x32_bf16 v[112:115], v[152:155], a[168:171], v[112:115]\n\t" \
  "buffer_load_dwordx2 v[42:43], v252, %[sd], s74 offen offset:2048\n\t" \
  "s_mov_b32 s74, 12288\n\t" \
  "v_mfma_f32_16x16x32_bf16 v[112:115], v[148:151], a[164:167], v[112:115]\n\t" \
  "v_mfma_f32_16x16x32_bf16 v[112:115], v[144:147], a[160:163], v[112:115]\n\t" \
  "s_waitcnt lgkmcnt(5)\n\t" \
  "v_accvgpr_read_b32 v227, a238\n\t" \
  "v_mfma_f32_16x16x32_bf16 v[100:103], v[128:131], v[208:211], v[100:103]\n\t" \
  "ds_read_b128 a[144:147], v242 offset:12288\n\t" \
  "v_accvgpr_write_b32 a192, 0\n\t" \
  "v_mfma_f32_16x16x32_bf16 v[100:103], v[132:135], v[212:215], v[100:103]\n\t" \
  "buffer_load_dwordx2 v[44:45], v252, %[sd], s74 offen offset:0\n\t" \
  "v_mfma_f32_16x16x32_bf16 v[100:103], v[136:139], v[216:219], v[100:103]\n\t" \
  "ds_read_b128 a[148:151], v242 offset:13312\n\t" \
  "v_accvgpr_write_b32 a193, 0\n\t" \
  "v_mfma_f32_16x16x32_bf16 v[100:103], v[140:143], v[220:223], v[100:103]\n\t" \
  "ds_read_b128 a[152:155], v242 offset:14336\n\t" \
  "v_accvgpr_write_b32 a194, v238\n\t" \
  "v_mfma_f32_16x16x32_bf16 v[116:119], v[156:159], v[220:223], v[116:119]\n\t" \
  "ds_read_b128 a[156:159], v242 offset:15360\n\t" \
  "v_accvgpr_write_b32 a195, v239\n\t" \
  "v_mfma_f32_16x16x32_bf16 v[116:119], v[152:155], v[216:219], v[116:119]\n\t" \
  "buffer_load_dwordx2 v[46:47], v252, %[sd], s74 offen offset:2048\n\t" \
  "v_mfma_f32_16x16x32_bf16 v[116:119], v[148:151], v[212:215], v[116:119]\n\t" \
  "v_accvgpr_write_b32 a196, v238\n\t" \
  "v_accvgpr_write_b32 a197, v239\n\t" \
  "v_mfma_f32_16x16x32_bf16 v[116:119], v[144:147], v[208:211], v[116:119]\n\t" \
  "s_waitcnt vmcnt(14) lgkmcnt(4)\n\t" \
  "s_barrier\n\t" \
  "v_mfma_f32_16x16x32_bf16 v[104:107], v[128:131], a[128:131], v[104:107]\n\t" \
  "ds_read_b128 v[16:19], v242 offset:49152\n\t" \
  "v_accvgpr_write_b32 a198, 0\n\t" \
  "v_mfma_f32_16x16x32_bf16 v[104:107], v[132:135], a[132:135], v[104:107]\n\t" \
  "ds_read_b128 v[20:23], v242 offset:50176\n\t" \
  "v_accvgpr_write_b32 a199, 0\n\t" \
  "v_mfma_f32_16x16x32_bf16 v[104:107], v[136:139], a[136:139], v[104:107]\n\t" \
  "buffer_load_dwordx4 v242, %[ws], s71 offen offset:0 sc1 lds\n\t" \
  "v_mfma_f32_16x16x32_bf16 v[104:107], v[140:143], a[140:143], v[104:107]\n\t" \
  "ds_read_b128 v[24:27], v242 offset:51200\n\t" \
  "ds_read_b64 v[208:209], v231 offset:0\n\t" \
  "v_mfma_f32_16x16x32_bf16 v[120:123], v[156:159], a[140:143], v[120:123]\n\t" \
  "ds_read_b128 v[28:31], v242 offset:52224\n\t" \
  "v_perm_b32 v74, v227, v224, s79\n\t" \
  "v_mfma_f32_16x16x32_bf16 v[120:123], v[152:155], a[136:139], v[120:123]\n\t" \
  "ds_read_b64 v[210:211], v231 offset:4096\n\t" \
  "v_perm_b32 v78, v227, v224, s80\n\t" \
  "v_mfma_f32_16x16x32_bf16 v[120:123], v[148:151], a[132:135], v[120:123]\n\t" \
  "buffer_load_dwordx4 v242, %[ws], s71 offen offset:1024 sc1 lds\n\t" \
  "v_mfma_f32_16x16x32_bf16 v[120:123], v[144:147], a[128:131], v[120:123]\n\t" \
  "s_waitcnt lgkmcnt(6)\n\t" \
  "ds_read_b64 v[212:213], v231 offset:8192\n\t" \
  "v_accvgpr_read_b32 v224, a235\n\t" \
  "v_mfma_f32_16x16x32_bf16 v[108:111], v[128:131], a[144:147], v[108:111]\n\t" \
  "ds_read_b128 a[8:11], v242 offset:53248\n\t" \
  "ds_read_b64 v[214:215], v231 offset:12288\n\t" \
  "v_mfma_f32_16x16x32_bf16 v[108:111], v[132:135], a[148:151], v[108:111]\n\t" \
  "ds_read_b128 a[24:27], v242 offset:54272\n\t" \
  "ds_read_b64 v[216:217], v233 offset:0\n\t" \
  "v_mfma_f32_16x16x32_bf16 v[108:111], v[136:139], a[152:155], v[108:111]\n\t" \
  "ds_read_b128 a[40:43], v242 offset:55296\n\t" \
  "v_accvgpr_read_b32 v227, a239\n\t" \
  "v_mfma_f32_16x16x32_bf16 v[108:111], v[140:143], a[156:159], v[108:111]\n\t" \
  "buffer_load_dwordx4 v242, %[ws], s71 offen offset:2048 sc1 lds\n\t" \
  "v_mfma_f32_16x16x32_bf16 v[124:127], v[156:159], a[156:159], v[124:127]\n\t" \
  "ds_read_b64 v[218:219], v233 offset:4096\n\t" \
  "ds_read_b64 v[220:221], v233 offset:8192\n\t" \
  "v_mfma_f32_16x16x32_bf16 v[124:127], v[152:155], a[152:155], v[124:127]\n\t" \
  "ds_read_b128 a[56:59], v242 offset:56320\n\t" \
  "v_perm_b32 v75, v227, v224, s79\n\t" \
  "ds_read_b64 v[222:223], v233 offset:12288\n\t" \
  "buffer_load_dword v228, v235, %[ws], s68 offen sc1\n\t" \
  "v_mfma_f32_16x16x32_bf16 v[124:127], v[148:151], a[148:151], v[124:127]\n\t" \
  "v_perm_b32 v79, v227, v224, s80\n\t" \
  "v_accvgpr_read_b32 v224, a240\n\t" \
  "v_mfma_f32_16x16x32_bf16 v[124:127], v[144:147], a[144:147], v[124:127]\n\t" \
  "s_waitcnt lgkmcnt(11)\n\t" \
  "v_accvgpr_read_b32 v227, a244\n\t" \
  "v_perm_b32 v80, v227, v224, s79\n\t" \
  "v_mfma_f32_16x16x32_bf16 v[96:99], a[12:15], v[16:19], v[96:99]\n\t" \
  "buffer_load_dwordx4 v242, %[ws], s71 offen offset:3072 sc1 lds\n\t" \
  "v_mfma_f32_16x16x32_bf16 v[96:99], a[28:31], v[20:23], v[96:99]\n\t" \
  "ds_read_b128 v[128:131], v242 offset:57344\n\t" \
  "v_perm_b32 v84, v227, v224, s80\n\t" \
  "v_mfma_f32_16x16x32_bf16 v[96:99], a[44:47], v[24:27], v[96:99]\n\t" \
  "ds_read_b128 v[132:135], v242 offset:58368\n\t" \
  "v_accvgpr_read_b32 v224, a241\n\t" \
  "v_mfma_f32_16x16x32_bf16 v[96:99], a[60:63], v[28:31], v[96:99]\n\t" \
  "ds_read_b128 a[72:75], v242 offset:59392\n\t" \
  "s_waitcnt vmcnt(13)\n\t" \
  "v_cmp_ne_u32 vcc, %[tag], v246\n\t" \
  "s_and_b32 vcc_lo, vcc_lo, 0xff\n\t" \
  "s_or_b32 s81, s81, vcc_lo\n\t" \
  "v_mfma_f32_16x16x32_bf16 v[112:115], a[124:127], v[28:31], v[112:115]\n\t" \
  "ds_read_b128 a[88:91], v242 offset:60416\n\t" \
  "v_mfma_f32_16x16x32_bf16 v[112:115], a[108:111], v[24:27], v[112:115]\n\t" \
  "s_and_b32 s72, s81, 15\n\t" \
  "s_cselect_b32 s72, -1, 0\n\t" \
  "v_lshl_or_b32 v247, s72, 31, v242\n\t" \
  "s_add_u32 s72, s63, 0\n\t" \
  "v_mfma_f32_16x16x32_bf16 v[112:115], a[92:95], v[20:23], v[112:115]\n\t" \
  "buffer_load_dwordx4 a[128:131], v247, %[ws], s72 offen sc1\n\t" \
  "v_mfma_f32_16x16x32_bf16 v[112:115], a[76:79], v[16:19], v[112:115]\n\t" \
  "s_waitcnt lgkmcnt(5)\n\t" \
  "s_add_u32 s72, s64, 32768\n\t" \
  "v_accvgpr_read_b32 v227, a245\n\t" \
  "v_mfma_f32_16x16x32_bf16 v[100:103], a[12:15], a[8:11], v[100:103]\n\t" \
  "ds_read_b128 a[104:107], v242 offset:61440\n\t" \
  "v_perm_b32 v81, v227, v224, s79\n\t" \
  "v_mfma_f32_16x16x32_bf16 v[100:103], a[28:31], a[24:27], v[100:103]\n\t" \
  "buffer_load_dwordx4 a[140:143], v247, %[ws], s72 offen sc1\n\t" \
  "v_mfma_f32_16x16x32_bf16 v[100:103], a[44:47], a[40:43], v[100:103]\n\t" \
  "s_add_u32 s72, s63, 65536\n\t" \
  "ds_read_b128 a[120:123], v242 offset:62464\n\t" \
  "v_perm_b32 v85, v227, v224, s80\n\t" \
  "v_mfma_f32_16x16x32_bf16 v[100:103], a[60:63], a[56:59], v[100:103]\n\t" \
  "ds_read_b128 a[176:179], v242 offset:63488\n\t" \
  "v_mfma_f32_16x16x32_bf16 v[116:119], a[124:127], a[56:59], v[116:119]\n\t" \
  "ds_read_b128 a[180:183], v242 offset:64512\n\t" \
  "v_accvgpr_read_b32 v224, a242\n\t" \
  "v_mfma_f32_16x16x32_bf16 v[116:119], a[108:111], a[40:43], v[116:119]\n\t" \
  "buffer_load_dwordx4 a[152:155], v247, %[ws], s72 offen sc1\n\t" \
  "s_add_u32 s72, s64, 98304\n\t" \
  "v_mfma_f32_16x16x32_bf16 v[116:119], a[92:95], a[24:27], v[116:119]\n\t" \
  "s_waitcnt lgkmcnt(8)\n\t" \
  "v_lshlrev_b32 v0, 16, v208\n\t" \
  "v_and_b32 v1, 0xffff0000, v208\n\t" \
  "v_mfma_f32_16x16x32_bf16 v[116:119], a[76:79], a[8:11], v[116:119]\n\t" \
  "s_waitcnt lgkmcnt(4)\n\t" \
  "s_barrier\n\t" \
  "v_mfma_f32_16x16x32_bf16 v[104:107], a[12:15], v[128:131], v[104:107]\n\t" \
  "ds_read_b128 a[184:187], v230 offset:0\n\t" \
  "v_lshlrev_b32 v2, 16, v209\n\t" \
  "v_mfma_f32_16x16x32_bf16 v[104:107], a[28:31], v[132:135], v[104:107]\n\t" \
  "ds_read_b128 a[188:191], v230 offset:1024\n\t" \
  "v_and_b32 v3, 0xffff0000, v209\n\t" \
  "v_mfma_f32_16x16x32_bf16 v[104:107], a[44:47], a[72:75], v[104:107]\n\t" \
  "buffer_load_dwordx4 a[164:167], v247, %[ws], s72 offen sc1\n\t" \
  "v_mfma_f32_16x16x32_bf16 v[104:107], a[60:63], a[88:91], v[104:107]\n\t" \
  "s_and_b32 s72, s81, 240\n\t" \
  "s_cselect_b32 s72, -1, 0\n\t" \
  "v_lshl_or_b32 v247, s72, 31, v242\n\t" \
  "s_add_u32 s72, s63, 4096\n\t" \
  "v_mfma_f32_16x16x32_bf16 v[120:123], a[124:127], a[88:91], v[120:123]\n\t" \
  "ds_read_b128 v[136:139], v230 offset:2048\n\t" \
  "v_lshlrev_b32 v4, 16, v210\n\t" \
  "v_mfma_f32_16x16x32_bf16 v[120:123], a[108:111], a[72:75], v[120:123]\n\t" \
  "ds_read_b128 v[140:143], v230 offset:3072\n\t" \
  "v_and_b32 v5, 0xffff0000, v210\n\t" \
  "v_mfma_f32_16x16x32_bf16 v[120:123], a[92:95], v[132:135], v[120:123]\n\t" \
  "buffer_load_dwordx4 a[132:135], v247, %[ws], s72 offen sc1\n\t" \
  "s_add_u32 s72, s64, 36864\n\t" \
  "v_mfma_f32_16x16x32_bf16 v[120:123], a[76:79], v[128:131], v[120:123]\n\t" \
  "s_waitcnt lgkmcnt(4)\n\t" \
  "v_lshlrev_b32 v6, 16, v211\n\t" \
  "v_and_b32 v7, 0xffff0000, v211\n\t" \
  "v_mfma_f32_16x16x32_bf16 v[108:111], a[12:15], a[104:107], v[108:111]\n\t" \
  "ds_read_b128 v[128:131], v230 offset:4096\n\t" \
  "v_accvgpr_read_b32 v227, a246\n\t" \
  "v_mfma_f32_16x16x32_bf16 v[108:111], a[28:31], a[120:123], v[108:111]\n\t" \
  "ds_read_b128 v[132:135], v230 offset:5120\n\t" \
  "v_lshlrev_b32 v8, 16, v212\n\t" \
  "v_mfma_f32_16x16x32_bf16 v[108:111], a[44:47], a[176:179], v[108:111]\n\t" \
  "ds_read_b128 v[144:147], v230 offset:6144\n\t" \
  "v_perm_b32 v82, v227, v224, s79\n\t" \
  "v_mfma_f32_16x16x32_bf16 v[108:111], a[60:63], a[180:183], v[108:111]\n\t" \
  "buffer_load_dwordx4 a[144:147], v247, %[ws], s72 offen sc1\n\t" \
  "s_add_u32 s72, s63, 69632\n\t" \
  "v_mfma_f32_16x16x32_bf16 v[124:127], a[124:127], a[180:183], v[124:127]\n\t" \
  "ds_read_b128 v[148:151], v230 offset:7168\n\t" \
  "v_and_b32 v9, 0xffff0000, v212\n\t" \
  "v_mfma_f32_16x16x32_bf16 v[124:127], a[108:111], a[176:179], v[124:127]\n\t" \
  "v_perm_b32 v86, v227, v224, s80\n\t" \
  "v_lshlrev_b32 v10, 16, v213\n\t" \
  "v_mfma_f32_16x16x32_bf16 v[124:127], a[92:95], a[120:123], v[124:127]\n\t" \
  "v_accvgpr_read_b32 v224, a243\n\t" \
  "v_and_b32 v11, 0xffff0000, v213\n\t" \
  "v_mfma_f32_16x16x32_bf16 v[124:127], a[76:79], a[104:107], v[124:127]\n\t" \
  "s_waitcnt lgkmcnt(4)\n\t" \
  "buffer_load_dwordx4 a[156:159], v247, %[ws], s72 offen sc1\n\t" \
  "s_add_u32 s72, s64, 102400\n\t" \
  "v_mfma_f32_16x16x32_bf16 v[96:99], a[96:99], a[184:187], v[96:99]\n\t" \
  "ds_read_b128 v[152:155], v230 offset:8192\n\t" \
  "v_lshlrev_b32 v12, 16, v214\n\t" \
  "v_mfma_f32_16x16x32_bf16 v[96:99], a[112:115], a[188:191], v[96:99]\n\t" \
  "ds_read_b128 v[156:159], v230 offset:9216\n\t" \
  "v_accvgpr_read_b32 v227, a247\n\t" \
  "v_mfma_f32_16x16x32_bf16 v[96:99], a[100:103], v[136:139], v[96:99]\n\t" \
  "ds_read_b128 a[8:11], v230 offset:10240\n\t" \
  "v_and_b32 v13, 0xffff0000, v214\n\t" \
  "v_mfma_f32_16x16x32_bf16 v[96:99], a[116:119], v[140:143], v[96:99]\n\t" \
  "buffer_load_dwordx4 a[168:171], v247, %[ws], s72 offen sc1\n\t" \
  "v_mfma_f32_16x16x32_bf16 v[112:115], a[84:87], v[140:143], v[112:115]\n\t" \
  "ds_read_b128 a[24:27], v230 offset:11264\n\t" \
  "v_perm_b32 v83, v227, v224, s79\n\t" \
  "v_mfma_f32_16x16x32_bf16 v[112:115], a[80:83], v[136:139], v[112:115]\n\t" \
  "v_lshlrev_b32 v14, 16, v215\n\t" \
  "v_perm_b32 v87, v227, v224, s80\n\t" \
  "v_mfma_f32_16x16x32_bf16 v[112:115], a[68:71], a[188:191], v[112:115]\n\t" \
  "v_and_b32 v15, 0xffff0000, v215\n\t" \
  "v_accvgpr_read_b32 v224, a248\n\t" \
  "v_mfma_f32_16x16x32_bf16 v[112:115], a[64:67], a[184:187], v[112:115]\n\t" \
  "s_waitcnt lgkmcnt(4)\n\t" \
  "v_lshlrev_b32 v16, 16, v216\n\t" \
  "v_and_b32 v17, 0xffff0000, v216\n\t" \
  "v_mfma_f32_16x16x32_bf16 v[100:103], a[96:99], v[128:131], v[100:103]\n\t" \
  "ds_read_b128 a[40:43], v230 offset:12288\n\t" \
  "v_accvgpr_read_b32 v227, a252\n\t" \
  "v_mfma_f32_16x16x32_bf16 v[100:103], a[112:115], v[132:135], v[100:103]\n\t" \
  "ds_read_b128 a[56:59], v230 offset:13312\n\t" \
  "v_lshlrev_b32 v18, 16, v217\n\t" \
  "v_mfma_f32_16x16x32_bf16 v[100:103], a[100:103], v[144:147], v[100:103]\n\t" \
  "ds_read_b128 a[72:75], v230 offset:14336\n\t" \
  "v_perm_b32 v88, v227, v224, s79\n\t" \
  "v_mfma_f32_16x16x32_bf16 v[100:103], a[116:119], v[148:151], v[100:103]\n\t" \
  "ds_read_b128 a[88:91], v230 offset:15360\n\t" \
  "v_and_b32 v19, 0xffff0000, v217\n\t" \
  "s_waitcnt vmcnt(9)\n\t" \
  "v_cmp_ne_u32 vcc, %[tag], v228\n\t" \
  "s_and_b32 vcc_lo, vcc_lo, 0xf00\n\t" \
  "s_or_b32 s81, s81, vcc_lo\n\t" \
  "v_mfma_f32_16x16x32_bf16 v[116:119], a[84:87], v[148:151], v[116:119]\n\t" \
  "s_and_b32 s72, s81, 3840\n\t" \
  "s_cselect_b32 s72, -1, 0\n\t" \
  "v_lshl_or_b32 v247, s72, 31, v242\n\t" \
  "s_add_u32 s72, s63, 16384\n\t" \
  "v_mfma_f32_16x16x32_bf16 v[116:119], a[80:83], v[144:147], v[116:119]\n\t" \
  "buffer_load_dwordx4 a[136:139], v247, %[ws], s72 offen sc1\n\t" \
  "v_mfma_f32_16x16x32_bf16 v[116:119], a[68:71], v[132:135], v[116:119]\n\t" \
  "s_add_u32 s72, s64, 49152\n\t" \
  "v_perm_b32 v92, v227, v224, s80\n\t" \
  "v_lshlrev_b32 v20, 16, v218\n\t" \
  "v_mfma_f32_16x16x32_bf16 v[116:119], a[64:67], v[128:131], v[116:119]\n\t" \
  "s_waitcnt vmcnt(9) lgkmcnt(4)\n\t" \
  "s_barrier\n\t" \
  "v_mfma_f32_16x16x32_bf16 v[104:107], a[96:99], v[152:155], v[104:107]\n\t" \
  "buffer_load_dwordx4 a[148:151], v247, %[ws], s72 offen sc1\n\t" \
  "v_mfma_f32_16x16x32_bf16 v[104:107], a[112:115], v[156:159], v[104:107]\n\t" \
  "s_add_u32 s72, s63, 81920\n\t" \
  "ds_read_b128 v[128:131], v242 offset:16384\n\t" \
  "v_accvgpr_read_b32 v224, a249\n\t" \
  "v_mfma_f32_16x16x32_bf16 v[104:107], a[100:103], a[8:11], v[104:107]\n\t" \
  "ds_read_b128 v[132:135], v242 offset:17408\n\t" \
  "v_and_b32 v21, 0xffff0000, v218\n\t" \
  "v_mfma_f32_16x16x32_bf16 v[104:107], a[116:119], a[24:27], v[104:107]\n\t" \
  "buffer_load_dwordx4 a[160:163], v247, %[ws], s72 offen sc1\n\t" \
  "v_mfma_f32_16x16x32_bf16 v[120:123], a[84:87], a[24:27], v[120:123]\n\t" \
  "s_add_u32 s72, s64, 114688\n\t" \
  "ds_read_b128 a[12:15], v242 offset:18432\n\t" \
  "v_lshlrev_b32 v22, 16, v219\n\t" \
  "v_mfma_f32_16x16x32_bf16 v[120:123], a[80:83], a[8:11], v[120:123]\n\t" \
  "ds_read_b128 a[28:31], v242 offset:19456\n\t" \
  "v_accvgpr_read_b32 v227, a253\n\t" \
  "v_mfma_f32_16x16x32_bf16 v[120:123], a[68:71], v[156:159], v[120:123]\n\t" \
  "v_and_b32 v23, 0xffff0000, v219\n\t" \
  "v_perm_b32 v89, v227, v224, s79\n\t" \
  "v_mfma_f32_16x16x32_bf16 v[120:123], a[64:67], v[152:155], v[120:123]\n\t" \
  "s_waitcnt lgkmcnt(4)\n\t" \
  "buffer_load_dwordx4 a[172:175], v247, %[ws], s72 offen sc1\n\t" \
  "v_mfma_f32_16x16x32_bf16 v[108:111], a[96:99], a[40:43], v[108:111]\n\t" \
  "ds_read_b128 a[44:47], v242 offset:20480\n\t" \
  "v_lshlrev_b32 v24, 16, v220\n\t" \
  "v_mfma_f32_16x16x32_bf16 v[108:111], a[112:115], a[56:59], v[108:111]\n\t" \
  "ds_read_b128 a[60:63], v242 offset:21504\n\t" \
  "v_perm_b32 v93, v227, v224, s80\n\t" \
  "v_mfma_f32_16x16x32_bf16 v[108:111], a[100:103], a[72:75], v[108:111]\n\t" \
  "ds_read_b128 a[76:79], v242 offset:22528\n\t" \
  "v_and_b32 v25, 0xffff0000, v220\n\t" \
  "v_mfma_f32_16x16x32_bf16 v[108:111], a[116:119], a[88:91], v[108:111]\n\t" \
  "ds_read_b128 a[92:95], v242 offset:23552\n\t" \
  "v_accvgpr_read_b32 v224, a250\n\t" \
  "v_mfma_f32_16x16x32_bf16 v[124:127], a[84:87], a[88:91], v[124:127]\n\t" \
  "v_lshlrev_b32 v26, 16, v221\n\t" \
  "v_and_b32 v27, 0xffff0000, v221\n\t" \
  "v_mfma_f32_16x16x32_bf16 v[124:127], a[80:83], a[72:75], v[124:127]\n\t" \
  "v_accvgpr_read_b32 v227, a254\n\t" \
  "v_lshlrev_b32 v28, 16, v222\n\t" \
  "v_mfma_f32_16x16x32_bf16 v[124:127], a[68:71], a[56:59], v[124:127]\n\t" \
  "v_perm_b32 v90, v227, v224, s79\n\t" \
  "v_and_b32 v29, 0xffff0000, v222\n\t" \
  "v_mfma_f32_16x16x32_bf16 v[124:127], a[64:67], a[40:43], v[124:127]\n\t" \
  "s_waitcnt lgkmcnt(4)\n\t" \
  "v_perm_b32 v94, v227, v224, s80\n\t" \
  "v_lshlrev_b32 v30, 16, v223\n\t" \
  "v_mfma_f32_16x16x32_bf16 v[96:99], a[32:35], v[128:131], v[96:99]\n\t" \
  "ds_read_b128 a[104:107], v242 offset:24576\n\t" \
  "v_accvgpr_read_b32 v224, a251\n\t" \
  "v_mfma_f32_16x16x32_bf16 v[96:99], a[48:51], v[132:135], v[96:99]\n\t" \
  "ds_read_b128 a[108:111], v242 offset:25600\n\t" \
  "v_and_b32 v31, 0xffff0000, v223\n\t" \
  "v_mfma_f32_16x16x32_bf16 v[96:99], a[36:39], a[12:15], v[96:99]\n\t" \
  "buffer_load_dword v246, v235, %[ws], s68 offen sc1\n\t" \
  "v_mfma_f32_16x16x32_bf16 v[96:99], a[52:55], a[28:31], v[96:99]\n\t" \
  "ds_read_b128 a[120:123], v242 offset:26624\n\t" \
  "v_accvgpr_read_b32 v227, a255\n\t" \
  "v_mfma_f32_16x16x32_bf16 v[112:115], a[20:23], a[28:31], v[112:115]\n\t" \
  "ds_read_b128 a[124:127], v242 offset:27648\n\t" \
  "v_perm_b32 v91, v227, v224, s79\n\t" \
  "v_mfma_f32_16x16x32_bf16 v[112:115], a[4:7], a[12:15], v[112:115]\n\t" \
  "v_mfma_f32_16x16x32_bf16 v[112:115], a[16:19], v[132:135], v[112:115]\n\t" \
  "v_perm_b32 v95, v227, v224, s80\n\t" \
  "v_mfma_f32_16x16x32_bf16 v[112:115], a[0:3], v[128:131], v[112:115]\n\t" \
  "s_waitcnt lgkmcnt(4)\n\t" \
  "v_mfma_f32_16x16x32_bf16 v[100:103], a[32:35], a[44:47], v[100:103]\n\t" \
  "ds_read_b128 a[176:179], v242 offset:28672\n\t" \
  "v_mfma_f32_16x16x32_bf16 v[100:103], a[48:51], a[60:63], v[100:103]\n\t" \
  "ds_read_b128 a[180:183], v242 offset:29696\n\t" \
  "v_mfma_f32_16x16x32_bf16 v[100:103], a[36:39], a[76:79], v[100:103]\n\t" \
  "ds_read_b128 a[184:187], v242 offset:30720\n\t" \
  "v_mfma_f32_16x16x32_bf16 v[100:103], a[52:55], a[92:95], v[100:103]\n\t" \
  "ds_read_b128 a[188:191], v242 offset:31744\n\t" \
  "v_mfma_f32_16x16x32_bf16 v[116:119], a[20:23], a[92:95], v[116:119]\n\t" \
  "v_mfma_f32_16x16x32_bf16 v[116:119], a[4:7], a[76:79], v[116:119]\n\t" \
  "v_mfma_f32_16x16x32_bf16 v[116:119], a[16:19], a[60:63], v[116:119]\n\t" \
  "v_mfma_f32_16x16x32_bf16 v[116:119], a[0:3], a[44:47], v[116:119]\n\t" \
  "s_waitcnt lgkmcnt(4)\n\t" \
  "v_mfma_f32_16x16x32_bf16 v[104:107], a[32:35], a[104:107], v[104:107]\n\t" \
  "ds_read_b128 v[128:131], v237 offset:2560\n\t" \
  "v_mfma_f32_16x16x32_bf16 v[104:107], a[48:51], a[108:111], v[104:107]\n\t" \
  "ds_read_b128 v[132:135], v237 offset:2624\n\t" \
  "v_mfma_f32_16x16x32_bf16 v[104:107], a[36:39], a[120:123], v[104:107]\n\t" \
  "v_mfma_f32_16x16x32_bf16 v[104:107], a[52:55], a[124:127], v[104:107]\n\t" \
  "v_mfma_f32_16x16x32_bf16 v[120:123], a[20:23], a[124:127], v[120:123]\n\t" \
  "v_mfma_f32_16x16x32_bf16 v[120:123], a[4:7], a[120:123], v[120:123]\n\t" \
  "v_mfma_f32_16x16x32_bf16 v[120:123], a[16:19], a[108:111], v[120:123]\n\t" \
  "v_mfma_f32_16x16x32_bf16 v[120:123], a[0:3], a[104:107], v[120:123]\n\t" \
  "s_waitcnt lgkmcnt(2)\n\t" \
  "v_mfma_f32_16x16x32_bf16 v[108:111], a[32:35], a[176:179], v[108:111]\n\t" \
  "v_mfma_f32_16x16x32_bf16 v[108:111], a[48:51], a[180:183], v[108:111]\n\t" \
  "v_mfma_f32_16x16x32_bf16 v[108:111], a[36:39], a[184:187], v[108:111]\n\t" \
  "v_mfma_f32_16x16x32_bf16 v[108:111], a[52:55], a[188:191], v[108:111]\n\t" \
  "v_mfma_f32_16x16x32_bf16 v[124:127], a[20:23], a[188:191], v[124:127]\n\t" \
  "v_mfma_f32_16x16x32_bf16 v[124:127], a[4:7], a[184:187], v[124:127]\n\t" \
  "v_mfma_f32_16x16x32_bf16 v[124:127], a[16:19], a[180:183], v[124:127]\n\t" \
  "v_mfma_f32_16x16x32_bf16 v[124:127], a[0:3], a[176:179], v[124:127]\n\t" \
  STP("v_writelane_b32 %[stv], s94, 36\n\t") \
  STP("v_writelane_b32 %[stv], s92, 37\n\t") \
  STP("s_memrealtime s[94:95]\n\t") \
  STP("s_memtime s[92:93]\n\t") \
  "s_cmp_lg_u32 s81, 0\n\t" \
  "s_cbranch_scc1 .Lop_s99\n\t" \
  ".Lop_d2:\n\t" \
  "s_waitcnt vmcnt(9)\n\t" \
  "v_mfma_f32_16x16x32_bf16 v[96:99], a[196:199], a[128:131], v[96:99]\n\t" \
  "v_mfma_f32_16x16x32_bf16 v[100:103], a[196:199], a[140:143], v[100:103]\n\t" \
  "v_mfma_f32_16x16x32_bf16 v[104:107], a[196:199], a[152:155], v[104:107]\n\t" \
  "v_mfma_f32_16x16x32_bf16 v[108:111], a[196:199], a[164:167], v[108:111]\n\t" \
  "s_waitcnt vmcnt(5)\n\t" \
  "v_mfma_f32_16x16x32_bf16 v[112:115], a[194:197], a[130:133], v[112:115]\n\t" \
  "v_mfma_f32_16x16x32_bf16 v[116:119], a[194:197], a[142:145], v[116:119]\n\t" \
  "v_mfma_f32_16x16x32_bf16 v[120:123], a[194:197], a[154:157], v[120:123]\n\t" \
  "v_mfma_f32_16x16x32_bf16 v[124:127], a[194:197], a[166:169], v[124:127]\n\t" \
  "s_waitcnt vmcnt(1)\n\t" \
  "v_mfma_f32_16x16x32_bf16 v[96:99], a[194:197], a[134:137], v[96:99]\n\t" \
  "v_mfma_f32_16x16x32_bf16 v[100:103], a[194:197], a[146:149], v[100:103]\n\t" \
  "v_mfma_f32_16x16x32_bf16 v[104:107], a[194:197], a[158:161], v[104:107]\n\t" \
  "v_mfma_f32_16x16x32_bf16 v[108:111], a[194:197], a[170:173], v[108:111]\n\t" \
  "v_mfma_f32_16x16x32_bf16 v[112:115], a[192:195], a[136:139], v[112:115]\n\t" \
  "v_mfma_f32_16x16x32_bf16 v[116:119], a[192:195], a[148:151], v[116:119]\n\t" \
  "v_mfma_f32_16x16x32_bf16 v[120:123], a[192:195], a[160:163], v[120:123]\n\t" \
  "v_mfma_f32_16x16x32_bf16 v[124:127], a[192:195], a[172:175], v[124:127]\n\t" \
  "s_waitcnt vmcnt(0) lgkmcnt(0)\n\t" \
  STP("v_writelane_b32 %[stv], s94, 38\n\t") \
  STP("v_writelane_b32 %[stv], s92, 39\n\t") \
  "s_nop 7\n\t" \
  ".subsection 1\n\t" \
  ".Lop_s0:\n\t" \
  "s_mov_b32 s78, 0\n\t" \
  ".Lop_r0:\n\t" \
  "s_sleep 2\n\t" \
  "buffer_load_dword v246, v245, %[ws], %[fw2] offen sc1\n\t" \
  "s_waitcnt vmcnt(0)\n\t" \
  "v_cmp_ne_u32 vcc, %[tag], v246\n\t" \
  "s_and_b32 vcc_lo, vcc_lo, 0x6\n\t" \
  "s_cbranch_scc0 .Lop_k0\n\t" \
  "s_add_u32 s78, s78, 1\n\t" \
  "s_cmp_lt_u32 s78, %[cap]\n\t" \
  "s_cbranch_scc1 .Lop_r0\n\t" \
  "buffer_store_dword v243, off, %[ws], 0 offset:256 sc1\n\t" \
  "s_branch .Lop_k0\n\t" \
  ".Lop_s1:\n\t" \
  "s_mov_b32 s78, 0\n\t" \
  ".Lop_r1:\n\t" \
  "s_sleep 2\n\t" \
  "buffer_load_dword v246, v245, %[ws], %[fw2] offen sc1\n\t" \
  "s_waitcnt vmcnt(0)\n\t" \
  "v_cmp_ne_u32 vcc, %[tag], v246\n\t" \
  "s_and_b32 vcc_lo, vcc_lo, 0xe00\n\t" \
  "s_cbranch_scc0 .Lop_k1\n\t" \
  "s_add_u32 s78, s78, 1\n\t" \
  "s_cmp_lt_u32 s78, %[cap]\n\t" \
  "s_cbranch_scc1 .Lop_r1\n\t" \
  "buffer_store_dword v243, off, %[ws], 0 offset:256 sc1\n\t" \
  "s_branch .Lop_k1\n\t" \
  ".Lop_s2:\n\t" \
  "s_mov_b32 s78, 0\n\t" \
  ".Lop_r2:\n\t" \
  "s_sleep 2\n\t" \
  "buffer_load_dword v246, v245, %[ws], %[fw2] offen sc1\n\t" \
  "s_waitcnt vmcnt(0)\n\t" \
  "v_cmp_ne_u32 vcc, %[tag], v246\n\t" \
  "s_and_b32 vcc_lo, vcc_lo, 0xfe00\n\t" \
  "s_cbranch_scc0 .Lop_k2\n\t" \
  "s_add_u32 s78, s78, 1\n\t" \
  "s_cmp_lt_u32 s78, %[cap]\n\t" \
  "s_cbranch_scc1 .Lop_r2\n\t" \
  "buffer_store_dword v243, off, %[ws], 0 offset:256 sc1\n\t" \
  "s_branch .Lop_k2\n\t" \
  ".Lop_s3:\n\t" \
  "s_mov_b32 s78, 0\n\t" \
  ".Lop_r3:\n\t" \
  "s_sleep 2\n\t" \
  "buffer_load_dword v246, v245, %[ws], %[fw2] offen sc1\n\t" \
  "s_waitcnt vmcnt(0)\n\t" \
  "v_cmp_ne_u32 vcc, %[tag], v246\n\t" \
  "s_and_b32 vcc_lo, vcc_lo, 0xfe0000\n\t" \
  "s_cbranch_scc0 .Lop_k3\n\t" \
  "s_add_u32 s78, s78, 1\n\t" \
  "s_cmp_lt_u32 s78, %[cap]\n\t" \
  "s_cbranch_scc1 .Lop_r3\n\t" \
  "buffer_store_dword v243, off, %[ws], 0 offset:256 sc1\n\t" \
  "s_branch .Lop_k3\n\t" \
  ".Lop_s4:\n\t" \
  "s_mov_b32 s78, 0\n\t" \
  ".Lop_r4:\n\t" \
  "s_sleep 4\n\t" \
  "s_add_u32 s75, s69, 0\n\t" \
  "buffer_load_dwordx4 v[208:211], v170, %[ws], s75 offen sc1\n\t" \
  "s_add_u32 s75, s69, 32768\n\t" \
  "buffer_load_dwordx4 v[212:215], v170, %[ws], s75 offen sc1\n\t" \
  "s_add_u32 s75, s69, 65536\n\t" \
  "buffer_load_dwordx4 v[216:219], v170, %[ws], s75 offen sc1\n\t" \
  "s_add_u32 s75, s69, 98304\n\t" \
  "buffer_load_dwordx4 v[220:223], v170, %[ws], s75 offen sc1\n\t" \
  "s_waitcnt vmcnt(0)\n\t" \
  "v_xor_b32 v246, v243, v209\n\t" \
  "v_bitop3_b32 v246, v246, v211, v243 bitop3:0xf6\n\t" \
  "v_bitop3_b32 v246, v246, v213, v243 bitop3:0xf6\n\t" \
  "v_bitop3_b32 v246, v246, v215, v243 bitop3:0xf6\n\t" \
  "v_bitop3_b32 v246, v246, v217, v243 bitop3:0xf6\n\t" \
  "v_bitop3_b32 v246, v246, v219, v243 bitop3:0xf6\n\t" \
  "v_bitop3_b32 v246, v246, v221, v243 bitop3:0xf6\n\t" \
  "v_bitop3_b32 v246, v246, v223, v243 bitop3:0xf6\n\t" \
  "v_cmp_ne_u32 vcc, 0, v246\n\t" \
  "s_cbranch_vccz .Lop_k4\n\t" \
  "s_add_u32 s78, s78, 1\n\t" \
  "s_cmp_lt_u32 s78, %[cap]\n\t" \
  "s_cbranch_scc1 .Lop_r4\n\t" \
  "buffer_store_dword v243, off, %[ws], 0 offset:256 sc1\n\t" \
  "s_branch .Lop_k4\n\t" \
  ".Lop_rd:\n\t" \
  "s_waitcnt vmcnt(0) lgkmcnt(0)\n\t" \
  "s_barrier\n\t" \
  "s_mov_b32 %[pm], 2\n\t" \
  "s_branch .Lop_end\n\t" \
  ".Lop_s99:\n\t" \
  "s_waitcnt vmcnt(0)\n\t" \
  "v_cmp_ne_u32 vcc, %[tag], v246\n\t" \
  "s_and_b32 vcc_lo, vcc_lo, 0xfff\n\t" \
  "s_cbranch_scc0 .Lop_k99\n\t" \
  "s_mov_b32 s78, 0\n\t" \
  ".Lop_r99:\n\t" \
  "buffer_load_dword v246, v235, %[ws], s68 offen sc1\n\t" \
  "s_waitcnt vmcnt(0)\n\t" \
  "v_cmp_ne_u32 vcc, %[tag], v246\n\t" \
  "s_and_b32 vcc_lo, vcc_lo, 0xfff\n\t" \
  "s_cbranch_scc0 .Lop_k99\n\t" \
  "s_add_u32 s78, s78, 1\n\t" \
  "s_cmp_lt_u32 s78, %[cap]\n\t" \
  "s_cbranch_scc1 .Lop_r99\n\t" \
  "buffer_store_dword v243, off, %[ws], 0 offset:256 sc1\n\t" \
  "s_branch .Lop_k99\n\t" \
  ".Lop_k99:\n\t" \
  "s_and_b32 s72, s81, 15\n\t" \
  "s_cbranch_scc0 .Lop_m0\n\t" \
  "s_add_u32 s72, s63, 0\n\t" \
  "buffer_load_dwordx4 a[128:131], v242, %[ws], s72 offen sc1\n\t" \
  "s_add_u32 s72, s64, 32768\n\t" \
  "buffer_load_dwordx4 a[140:143], v242, %[ws], s72 offen sc1\n\t" \
  "s_add_u32 s72, s63, 65536\n\t" \
  "buffer_load_dwordx4 a[152:155], v242, %[ws], s72 offen sc1\n\t" \
  "s_add_u32 s72, s64, 98304\n\t" \
  "buffer_load_dwordx4 a[164:167], v242, %[ws], s72 offen sc1\n\t" \
  ".Lop_m0:\n\t" \
  "s_and_b32 s72, s81, 240\n\t" \
  "s_cbranch_scc0 .Lop_m1\n\t" \
  "s_add_u32 s72, s63, 4096\n\t" \
  "buffer_load_dwordx4 a[132:135], v242, %[ws], s72 offen sc1\n\t" \
  "s_add_u32 s72, s64, 36864\n\t" \
  "buffer_load_dwordx4 a[144:147], v242, %[ws], s72 offen sc1\n\t" \
  "s_add_u32 s72, s63, 69632\n\t" \
  "buffer_load_dwordx4 a[156:159], v242, %[ws], s72 offen sc1\n\t" \
  "s_add_u32 s72, s64, 102400\n\t" \
  "buffer_load_dwordx4 a[168:171], v242, %[ws], s72 offen sc1\n\t" \
  ".Lop_m1:\n\t" \
  "s_and_b32 s72, s81, 3840\n\t" \
  "s_cbranch_scc0 .Lop_m2\n\t" \
  "s_add_u32 s72, s63, 16384\n\t" \
  "buffer_load_dwordx4 a[136:139], v242, %[ws], s72 offen sc1\n\t" \
  "s_add_u32 s72, s64, 49152\n\t" \
  "buffer_load_dwordx4 a[148:151], v242, %[ws], s72 offen sc1\n\t" \
  "s_add_u32 s72, s63, 81920\n\t" \
  "buffer_load_dwordx4 a[160:163], v242, %[ws], s72 offen sc1\n\t" \
  "s_add_u32 s72, s64, 114688\n\t" \
  "buffer_load_dwordx4 a[172:175], v242, %[ws], s72 offen sc1\n\t" \
  ".Lop_m2:\n\t" \
  "s_waitcnt vmcnt(0)\n\t" \
  "s_branch .Lop_d2\n\t" \
  ".Lfz_z:\n\t" \
  ".fill 128, 4, 0xBF800000\n\t" \
  ".subsection 0\n\t" \
  ".Lop_end:\n\t" \
  ""
#define OP_CLOB "v208", "v209", "v210", "v211", "v212", "v214", "v215", "v216", "v217", "v218", "v219", "v220", "v221", "v222", "v223", "v228", "v229", "v230", "v231", "v232", "v233", "v235", "v236", "v238", "v239", "v246", "v248", "v249", "v250", "v251", "s60", "s61", "s62", "s63", "s64", "s65", "s66", "s67", "s68", "s69", "s70", "s71", "s72", "s73", "s74", "s75", "s76", "s77", "s78", "s79", "s80", "s81", "s82", "s83", "s84", "s85", "s92", "s93", "s94", "s95", "vcc", "scc", "memory"
#define ST_ASM "s_nop 1\n\t" "v_mfma_f32_16x16x32_bf16 v[80:83], a[208:211], v[160:163], 0\n\t" "v_mfma_f32_16x16x32_bf16 v[80:83], a[212:215], v[164:167], v[80:83]\n\t" "v_mfma_f32_16x16x32_bf16 v[80:83], a[216:219], v[168:171], v[80:83]\n\t" "v_mfma_f32_16x16x32_bf16 v[80:83], a[220:223], v[172:175], v[80:83]\n\t" "v_dot2_f32_bf16 v64, v160, v160, 0\n\t" "v_dot2_f32_bf16 v65, v164, v164, 0\n\t" "v_dot2_f32_bf16 v66, v168, v168, 0\n\t" "v_dot2_f32_bf16 v67, v172, v172, 0\n\t" "v_dot2_f32_bf16 v64, v161, v161, v64\n\t" "v_dot2_f32_bf16 v65, v165, v165, v65\n\t" "v_dot2_f32_bf16 v66, v169, v169, v66\n\t" "v_dot2_f32_bf16 v67, v173, v173, v67\n\t" "v_dot2_f32_bf16 v64, v162, v162, v64\n\t" "v_dot2_f32_bf16 v65, v166, v166, v65\n\t" "v_dot2_f32_bf16 v66, v170, v170, v66\n\t" "v_dot2_f32_bf16 v67, v174, v174, v67\n\t" "v_dot2_f32_bf16 v64, v163, v163, v64\n\t" "v_dot2_f32_bf16 v65, v167, v167, v65\n\t" "v_dot2_f32_bf16 v66, v171, v171, v66\n\t" "v_dot2_f32_bf16 v67, v175, v175, v67\n\t" "s_nop 2\n\t"
static_assert(W_MODB == 2228224);
constexpr int GSLOT = 49152;
constexpr unsigned KRS = 256;
struct KVP { srd_t srdK, srdV; unsigned vkb[4], vvb[2], vksrc[4], vvsrc[2], sofk; };
struct H4 {
  srd_t srdW;
  unsigned voffX, voffW[4];
  unsigned xb1, wb[2][8];
  unsigned mX, mW, soX, rot;
  unsigned pfd, fo, fl, fvo;
  u32x32 q[4];
};
#define W4(a) "+v"(a[0]), "+v"(a[1]), "+v"(a[2]), "+v"(a[3])
#define W8(a) W4(a), "+v"(a[4]), "+v"(a[5]), "+v"(a[6]), "+v"(a[7])
constexpr unsigned kXT = 65536, kWT = 64 * QD * 2;
DI void w_dma4(unsigned m0, srd_t srd, const unsigned (&vo)[4], unsigned so) {
  asm volatile("s_mov_b32 m0, %0\n\ts_nop 0\n\tbuffer_load_dwordx4 %1, %5, %6 offen lds\n\tbuffer_load_dwordx4 %2, %5, %6 offen offset:1024 lds\n\t"
               "buffer_load_dwordx4 %3, %5, %6 offen offset:2048 lds\n\tbuffer_load_dwordx4 %4, %5, %6 offen offset:3072 lds"
               :: "s"(m0), "v"(vo[0]), "v"(vo[1]), "v"(vo[2]), "v"(vo[3]), "s"(srd), "s"(so) : "memory");
}
DI void w_voffs(unsigned (&vo)[4], int ldw, unsigned wid, unsigned lane, unsigned k0) {
#pragma unroll
  for (int j = 0; j < 4; ++j) {
    const unsigned k = (4 * wid + j) * 4 + (lane >> 4), c16 = lane & 15, sw = (k & 3) | (((k >> 3) & 1) << 2);
    vo[j] = (k ^ (32 * k0)) * ldw * 2 + ((((c16 >> 1) ^ sw) << 1) + (c16 & 1)) * 16 - 1024 * j;
  }
}
DI void h4_init_addr(H4& c, unsigned ldsbase, unsigned wid, unsigned lane, unsigned k0) {
  const unsigned r16 = lane & 15, g = lane >> 4, q = (lane >> 2) & 3, p = lane & 3;
  c.xb1 = ldsbase + GSLOT + wid * 8192 + k0 * 4096 + g * 1024 + r16 * 16;
#pragma unroll
  for (int sl = 0; sl < 2; ++sl)
#pragma unroll
    for (int nb = 0; nb < 8; ++nb) { c.wb[sl][nb] = ldsbase + 2 * sl * GSLOT + 32768 + (8 * g + q) * 256 + ((nb ^ (q | ((g & 1) << 2))) << 5) + p * 8; asm volatile("" : "+v"(c.wb[sl][nb])); }
}
#define FSTP(x)
#define FSTS(x)
#define FQ_STV
#define FQ_STC
#define FQ_ASM \
  "s_mov_b32 s98, 0xffff0000\n\t" \
  ".p2align 3\n\t" \
  "ds_read_b128 v[128:131], v208 offset:0\n\t" \
  "ds_read_b128 v[132:135], v208 offset:16\n\t" \
  "ds_read_b128 v[136:139], v208 offset:256\n\t" \
  "ds_read_b128 v[140:143], v208 offset:272\n\t" \
  "ds_read_b128 v[32:35], v204 offset:49152\n\t" \
  "ds_read_b128 v[36:39], v204 offset:51200\n\t" \
  "ds_read_b128 v[40:43], v204 offset:53248\n\t" \
  "ds_read_b128 v[44:47], v204 offset:55296\n\t" \
  "ds_read_b128 v[144:147], v208 offset:128\n\t" \
  "ds_read_b128 v[148:151], v208 offset:144\n\t" \
  "ds_read_b128 v[152:155], v208 offset:384\n\t" \
  "ds_read_b128 v[156:159], v208 offset:400\n\t" \
  "ds_read_b128 v[48:51], v205 offset:49152\n\t" \
  "ds_read_b128 v[52:55], v205 offset:51200\n\t" \
  "ds_read_b128 v[56:59], v205 offset:53248\n\t" \
  "ds_read_b128 v[60:63], v205 offset:55296\n\t" \
  "v_lshlrev_b32 v176, 16, v0\n\t" \
  "v_and_b32 v177, s98, v0\n\t" \
  "v_lshlrev_b32 v178, 16, v1\n\t" \
  "v_and_b32 v179, s98, v1\n\t" \
  "v_lshlrev_b32 v180, 16, v2\n\t" \
  "v_and_b32 v181, s98, v2\n\t" \
  "v_lshlrev_b32 v182, 16, v3\n\t" \
  "v_and_b32 v183, s98, v3\n\t" \
  "v_pk_fma_f32 v[176:177], v[176:177], v[160:161], v[162:163] op_sel_hi:[1,0,0]\n\t" \
  "v_pk_fma_f32 v[178:179], v[178:179], v[160:161], v[162:163] op_sel_hi:[1,0,0]\n\t" \
  "v_pk_fma_f32 v[180:181], v[180:181], v[160:161], v[162:163] op_sel_hi:[1,0,0]\n\t" \
  "v_pk_fma_f32 v[182:183], v[182:183], v[160:161], v[162:163] op_sel_hi:[1,0,0]\n\t" \
  "s_waitcnt lgkmcnt(12)\n\t" \
  "s_nop 0\n\t" \
  "s_xor_b32 s97, %[pso], 0\n\t" \
  ".p2align 3\n\t" \
  "v_pk_fma_f32 v[176:177], v[176:177], v[128:129], v[136:137]\n\t" \
  "v_pk_fma_f32 v[178:179], v[178:179], v[130:131], v[138:139]\n\t" \
  "v_pk_fma_f32 v[180:181], v[180:181], v[132:133], v[140:141]\n\t" \
  "v_pk_fma_f32 v[182:183], v[182:183], v[134:135], v[142:143]\n\t" \
  "v_cvt_pk_bf16_f32 v0, v176, v177\n\t" \
  "v_cvt_pk_bf16_f32 v1, v178, v179\n\t" \
  "v_cvt_pk_bf16_f32 v2, v180, v181\n\t" \
  "v_cvt_pk_bf16_f32 v3, v182, v183\n\t" \
  "buffer_store_dwordx4 v[0:3], v214, %[ws], s97 offen sc1\n\t" \
  "v_lshlrev_b32 v176, 16, v4\n\t" \
  "v_and_b32 v177, s98, v4\n\t" \
  "v_lshlrev_b32 v178, 16, v5\n\t" \
  "v_and_b32 v179, s98, v5\n\t" \
  "v_lshlrev_b32 v180, 16, v6\n\t" \
  "v_and_b32 v181, s98, v6\n\t" \
  "v_lshlrev_b32 v182, 16, v7\n\t" \
  "v_and_b32 v183, s98, v7\n\t" \
  "v_pk_fma_f32 v[176:177], v[176:177], v[164:165], v[166:167] op_sel_hi:[1,0,0]\n\t" \
  "v_pk_fma_f32 v[178:179], v[178:179], v[164:165], v[166:167] op_sel_hi:[1,0,0]\n\t" \
  "v_pk_fma_f32 v[180:181], v[180:181], v[164:165], v[166:167] op_sel_hi:[1,0,0]\n\t" \
  "v_pk_fma_f32 v[182:183], v[182:183], v[164:165], v[166:167] op_sel_hi:[1,0,0]\n\t" \
  "s_xor_b32 s97, %[pso], 256\n\t" \
  ".p2align 3\n\t" \
  "v_pk_fma_f32 v[176:177], v[176:177], v[128:129], v[136:137]\n\t" \
  "v_pk_fma_f32 v[178:179], v[178:179], v[130:131], v[138:139]\n\t" \
  "v_pk_fma_f32 v[180:181], v[180:181], v[132:133], v[140:141]\n\t" \
  "v_pk_fma_f32 v[182:183], v[182:183], v[134:135], v[142:143]\n\t" \
  "v_cvt_pk_bf16_f32 v4, v176, v177\n\t" \
  "v_cvt_pk_bf16_f32 v5, v178, v179\n\t" \
  "v_cvt_pk_bf16_f32 v6, v180, v181\n\t" \
  "v_cvt_pk_bf16_f32 v7, v182, v183\n\t" \
  "buffer_store_dwordx4 v[4:7], v214, %[ws], s97 offen sc1\n\t" \
  "v_lshlrev_b32 v176, 16, v8\n\t" \
  "v_and_b32 v177, s98, v8\n\t" \
  "v_lshlrev_b32 v178, 16, v9\n\t" \
  "v_and_b32 v179, s98, v9\n\t" \
  "v_lshlrev_b32 v180, 16, v10\n\t" \
  "v_and_b32 v181, s98, v10\n\t" \
  "v_lshlrev_b32 v182, 16, v11\n\t" \
  "v_and_b32 v183, s98, v11\n\t" \
  "v_pk_fma_f32 v[176:177], v[176:177], v[168:169], v[170:171] op_sel_hi:[1,0,0]\n\t" \
  "v_pk_fma_f32 v[178:179], v[178:179], v[168:169], v[170:171] op_sel_hi:[1,0,0]\n\t" \
  "v_pk_fma_f32 v[180:181], v[180:181], v[168:169], v[170:171] op_sel_hi:[1,0,0]\n\t" \
  "v_pk_fma_f32 v[182:183], v[182:183], v[168:169], v[170:171] op_sel_hi:[1,0,0]\n\t" \
  "s_xor_b32 s97, %[pso], 512\n\t" \
  ".p2align 3\n\t" \
  "v_pk_fma_f32 v[176:177], v[176:177], v[128:129], v[136:137]\n\t" \
  "v_pk_fma_f32 v[178:179], v[178:179], v[130:131], v[138:139]\n\t" \
  "v_pk_fma_f32 v[180:181], v[180:181], v[132:133], v[140:141]\n\t" \
  "v_pk_fma_f32 v[182:183], v[182:183], v[134:135], v[142:143]\n\t" \
  "v_cvt_pk_bf16_f32 v8, v176, v177\n\t" \
  "v_cvt_pk_bf16_f32 v9, v178, v179\n\t" \
  "v_cvt_pk_bf16_f32 v10, v180, v181\n\t" \
  "v_cvt_pk_bf16_f32 v11, v182, v183\n\t" \
  "buffer_store_dwordx4 v[8:11], v214, %[ws], s97 offen sc1\n\t" \
  "v_lshlrev_b32 v176, 16, v12\n\t" \
  "v_and_b32 v177, s98, v12\n\t" \
  "v_lshlrev_b32 v178, 16, v13\n\t" \
  "v_and_b32 v179, s98, v13\n\t" \
  "v_lshlrev_b32 v180, 16, v14\n\t" \
  "v_and_b32 v181, s98, v14\n\t" \
  "v_lshlrev_b32 v182, 16, v15\n\t" \
  "v_and_b32 v183, s98, v15\n\t" \
  "v_pk_fma_f32 v[176:177], v[176:177], v[172:173], v[174:175] op_sel_hi:[1,0,0]\n\t" \
  "v_pk_fma_f32 v[178:179], v[178:179], v[172:173], v[174:175] op_sel_hi:[1,0,0]\n\t" \
  "v_pk_fma_f32 v[180:181], v[180:181], v[172:173], v[174:175] op_sel_hi:[1,0,0]\n\t" \
  "v_pk_fma_f32 v[182:183], v[182:183], v[172:173], v[174:175] op_sel_hi:[1,0,0]\n\t" \
  "s_xor_b32 s97, %[pso], 768\n\t" \
  ".p2align 3\n\t" \
  "v_pk_fma_f32 v[176:177], v[176:177], v[128:129], v[136:137]\n\t" \
  "v_pk_fma_f32 v[178:179], v[178:179], v[130:131], v[138:139]\n\t" \
  "v_pk_fma_f32 v[180:181], v[180:181], v[132:133], v[140:141]\n\t" \
  "v_pk_fma_f32 v[182:183], v[182:183], v[134:135], v[142:143]\n\t" \
  "v_cvt_pk_bf16_f32 v12, v176, v177\n\t" \
  "v_cvt_pk_bf16_f32 v13, v178, v179\n\t" \
  "v_cvt_pk_bf16_f32 v14, v180, v181\n\t" \
  "v_cvt_pk_bf16_f32 v15, v182, v183\n\t" \
  "buffer_store_dwordx4 v[12:15], v214, %[ws], s97 offen sc1\n\t" \
  "ds_read_b128 v[128:131], v208 offset:512\n\t" \
  "ds_read_b128 v[132:135], v208 offset:528\n\t" \
  "ds_read_b128 v[136:139], v208 offset:768\n\t" \
  "ds_read_b128 v[140:143], v208 offset:784\n\t" \
  FSTP("s_memrealtime s[94:95]\n\t") \
  FSTP("s_memtime s[92:93]\n\t") \
  "v_lshlrev_b32 v176, 16, v16\n\t" \
  "v_and_b32 v177, s98, v16\n\t" \
  "v_lshlrev_b32 v178, 16, v17\n\t" \
  "v_and_b32 v179, s98, v17\n\t" \
  "v_lshlrev_b32 v180, 16, v18\n\t" \
  "v_and_b32 v181, s98, v18\n\t" \
  "v_lshlrev_b32 v182, 16, v19\n\t" \
  "v_and_b32 v183, s98, v19\n\t" \
  "v_pk_fma_f32 v[176:177], v[176:177], v[160:161], v[162:163] op_sel_hi:[1,0,0]\n\t" \
  "v_pk_fma_f32 v[178:179], v[178:179], v[160:161], v[162:163] op_sel_hi:[1,0,0]\n\t" \
  "v_pk_fma_f32 v[180:181], v[180:181], v[160:161], v[162:163] op_sel_hi:[1,0,0]\n\t" \
  "v_pk_fma_f32 v[182:183], v[182:183], v[160:161], v[162:163] op_sel_hi:[1,0,0]\n\t" \
  "s_waitcnt lgkmcnt(8)\n\t" \
  "s_nop 0\n\t" \
  "v_pk_fma_f32 v[176:177], v[176:177], v[144:145], v[152:153]\n\t" \
  "v_pk_fma_f32 v[178:179], v[178:179], v[146:147], v[154:155]\n\t" \
  "v_pk_fma_f32 v[180:181], v[180:181], v[148:149], v[156:157]\n\t" \
  "v_pk_fma_f32 v[182:183], v[182:183], v[150:151], v[158:159]\n\t" \
  "v_cvt_pk_bf16_f32 v16, v176, v177\n\t" \
  "v_cvt_pk_bf16_f32 v17, v178, v179\n\t" \
  "v_cvt_pk_bf16_f32 v18, v180, v181\n\t" \
  "v_cvt_pk_bf16_f32 v19, v182, v183\n\t" \
  "s_add_u32 m0, %[mx], 98304\n\t" \
  "s_xor_b32 s96, %[c17], 0\n\t" \
  ".p2align 3\n\t" \
  "s_add_u32 s96, s96, %[sl2]\n\t" \
  "buffer_load_dwordx4 v212, %[sx], s96 offen lds\n\t" \
  ".p2align 3\n\t" \
  "v_lshlrev_b32 v176, 16, v20\n\t" \
  "v_and_b32 v177, s98, v20\n\t" \
  "v_lshlrev_b32 v178, 16, v21\n\t" \
  "v_and_b32 v179, s98, v21\n\t" \
  "v_lshlrev_b32 v180, 16, v22\n\t" \
  "v_and_b32 v181, s98, v22\n\t" \
  "v_lshlrev_b32 v182, 16, v23\n\t" \
  "v_and_b32 v183, s98, v23\n\t" \
  "v_pk_fma_f32 v[176:177], v[176:177], v[164:165], v[166:167] op_sel_hi:[1,0,0]\n\t" \
  "v_pk_fma_f32 v[178:179], v[178:179], v[164:165], v[166:167] op_sel_hi:[1,0,0]\n\t" \
  "v_pk_fma_f32 v[180:181], v[180:181], v[164:165], v[166:167] op_sel_hi:[1,0,0]\n\t" \
  "v_pk_fma_f32 v[182:183], v[182:183], v[164:165], v[166:167] op_sel_hi:[1,0,0]\n\t" \
  "v_pk_fma_f32 v[176:177], v[176:177], v[144:145], v[152:153]\n\t" \
  "v_pk_fma_f32 v[178:179], v[178:179], v[146:147], v[154:155]\n\t" \
  "v_pk_fma_f32 v[180:181], v[180:181], v[148:149], v[156:157]\n\t" \
  "v_pk_fma_f32 v[182:183], v[182:183], v[150:151], v[158:159]\n\t" \
  "v_cvt_pk_bf16_f32 v20, v176, v177\n\t" \
  "v_cvt_pk_bf16_f32 v21, v178, v179\n\t" \
  "v_cvt_pk_bf16_f32 v22, v180, v181\n\t" \
  "v_cvt_pk_bf16_f32 v23, v182, v183\n\t" \
  "buffer_load_dwordx4 v213, %[sx], s96 offen offset:1024 lds\n\t" \
  ".p2align 3\n\t" \
  "v_lshlrev_b32 v176, 16, v24\n\t" \
  "v_and_b32 v177, s98, v24\n\t" \
  "v_lshlrev_b32 v178, 16, v25\n\t" \
  "v_and_b32 v179, s98, v25\n\t" \
  "v_lshlrev_b32 v180, 16, v26\n\t" \
  "v_and_b32 v181, s98, v26\n\t" \
  "v_lshlrev_b32 v182, 16, v27\n\t" \
  "v_and_b32 v183, s98, v27\n\t" \
  "v_pk_fma_f32 v[176:177], v[176:177], v[168:169], v[170:171] op_sel_hi:[1,0,0]\n\t" \
  "v_pk_fma_f32 v[178:179], v[178:179], v[168:169], v[170:171] op_sel_hi:[1,0,0]\n\t" \
  "v_pk_fma_f32 v[180:181], v[180:181], v[168:169], v[170:171] op_sel_hi:[1,0,0]\n\t" \
  "v_pk_fma_f32 v[182:183], v[182:183], v[168:169], v[170:171] op_sel_hi:[1,0,0]\n\t" \
  "v_pk_fma_f32 v[176:177], v[176:177], v[144:145], v[152:153]\n\t" \
  "v_pk_fma_f32 v[178:179], v[178:179], v[146:147], v[154:155]\n\t" \
  "v_pk_fma_f32 v[180:181], v[180:181], v[148:149], v[156:157]\n\t" \
  "v_pk_fma_f32 v[182:183], v[182:183], v[150:151], v[158:159]\n\t" \
  "v_cvt_pk_bf16_f32 v24, v176, v177\n\t" \
  "v_cvt_pk_bf16_f32 v25, v178, v179\n\t" \
  "v_cvt_pk_bf16_f32 v26, v180, v181\n\t" \
  "v_cvt_pk_bf16_f32 v27, v182, v183\n\t" \
  "s_add_u32 m0, %[mx], 100352\n\t" \
  "s_xor_b32 s96, %[c17], 131072\n\t" \
  ".p2align 3\n\t" \
  "s_add_u32 s96, s96, %[sl2]\n\t" \
  "buffer_load_dwordx4 v212, %[sx], s96 offen lds\n\t" \
  ".p2align 3\n\t" \
  "v_lshlrev_b32 v176, 16, v28\n\t" \
  "v_and_b32 v177, s98, v28\n\t" \
  "v_lshlrev_b32 v178, 16, v29\n\t" \
  "v_and_b32 v179, s98, v29\n\t" \
  "v_lshlrev_b32 v180, 16, v30\n\t" \
  "v_and_b32 v181, s98, v30\n\t" \
  "v_lshlrev_b32 v182, 16, v31\n\t" \
  "v_and_b32 v183, s98, v31\n\t" \
  "v_pk_fma_f32 v[176:177], v[176:177], v[172:173], v[174:175] op_sel_hi:[1,0,0]\n\t" \
  "v_pk_fma_f32 v[178:179], v[178:179], v[172:173], v[174:175] op_sel_hi:[1,0,0]\n\t" \
  "v_pk_fma_f32 v[180:181], v[180:181], v[172:173], v[174:175] op_sel_hi:[1,0,0]\n\t" \
  "v_pk_fma_f32 v[182:183], v[182:183], v[172:173], v[174:175] op_sel_hi:[1,0,0]\n\t" \
  "v_pk_fma_f32 v[176:177], v[176:177], v[144:145], v[152:153]\n\t" \
  "v_pk_fma_f32 v[178:179], v[178:179], v[146:147], v[154:155]\n\t" \
  "v_pk_fma_f32 v[180:181], v[180:181], v[148:149], v[156:157]\n\t" \
  "v_pk_fma_f32 v[182:183], v[182:183], v[150:151], v[158:159]\n\t" \
  "v_cvt_pk_bf16_f32 v28, v176, v177\n\t" \
  "v_cvt_pk_bf16_f32 v29, v178, v179\n\t" \
  "v_cvt_pk_bf16_f32 v30, v180, v181\n\t" \
  "v_cvt_pk_bf16_f32 v31, v182, v183\n\t" \
  "buffer_load_dwordx4 v213, %[sx], s96 offen offset:1024 lds\n\t" \
  ".p2align 3\n\t" \
  "ds_read_b128 v[144:147], v208 offset:640\n\t" \
  "ds_read_b128 v[148:151], v208 offset:656\n\t" \
  "ds_read_b128 v[152:155], v208 offset:896\n\t" \
  "ds_read_b128 v[156:159], v208 offset:912\n\t" \
  FSTP("v_writelane_b32 %[stv], s94, 28\n\t") \
  FSTP("v_writelane_b32 %[stv], s92, 29\n\t") \
  FSTP("s_memrealtime s[94:95]\n\t") \
  FSTP("s_memtime s[92:93]\n\t") \
  "v_lshlrev_b32 v176, 16, v32\n\t" \
  "v_and_b32 v177, s98, v32\n\t" \
  "v_lshlrev_b32 v178, 16, v33\n\t" \
  "v_and_b32 v179, s98, v33\n\t" \
  "v_lshlrev_b32 v180, 16, v34\n\t" \
  "v_and_b32 v181, s98, v34\n\t" \
  "v_lshlrev_b32 v182, 16, v35\n\t" \
  "v_and_b32 v183, s98, v35\n\t" \
  "v_pk_fma_f32 v[176:177], v[176:177], v[160:161], v[162:163] op_sel_hi:[1,0,0]\n\t" \
  "v_pk_fma_f32 v[178:179], v[178:179], v[160:161], v[162:163] op_sel_hi:[1,0,0]\n\t" \
  "v_pk_fma_f32 v[180:181], v[180:181], v[160:161], v[162:163] op_sel_hi:[1,0,0]\n\t" \
  "v_pk_fma_f32 v[182:183], v[182:183], v[160:161], v[162:163] op_sel_hi:[1,0,0]\n\t" \
  "s_waitcnt lgkmcnt(4)\n\t" \
  "s_nop 0\n\t" \
  "v_pk_fma_f32 v[176:177], v[176:177], v[128:129], v[136:137]\n\t" \
  "v_pk_fma_f32 v[178:179], v[178:179], v[130:131], v[138:139]\n\t" \
  "v_pk_fma_f32 v[180:181], v[180:181], v[132:133], v[140:141]\n\t" \
  "v_pk_fma_f32 v[182:183], v[182:183], v[134:135], v[142:143]\n\t" \
  "v_cvt_pk_bf16_f32 v32, v176, v177\n\t" \
  "v_cvt_pk_bf16_f32 v33, v178, v179\n\t" \
  "v_cvt_pk_bf16_f32 v34, v180, v181\n\t" \
  "v_cvt_pk_bf16_f32 v35, v182, v183\n\t" \
  "s_add_u32 m0, %[mx], 102400\n\t" \
  "s_xor_b32 s96, %[c17], 262144\n\t" \
  ".p2align 3\n\t" \
  "s_add_u32 s96, s96, %[sl2]\n\t" \
  "buffer_load_dwordx4 v212, %[sx], s96 offen lds\n\t" \
  ".p2align 3\n\t" \
  "v_lshlrev_b32 v176, 16, v36\n\t" \
  "v_and_b32 v177, s98, v36\n\t" \
  "v_lshlrev_b32 v178, 16, v37\n\t" \
  "v_and_b32 v179, s98, v37\n\t" \
  "v_lshlrev_b32 v180, 16, v38\n\t" \
  "v_and_b32 v181, s98, v38\n\t" \
  "v_lshlrev_b32 v182, 16, v39\n\t" \
  "v_and_b32 v183, s98, v39\n\t" \
  "v_pk_fma_f32 v[176:177], v[176:177], v[164:165], v[166:167] op_sel_hi:[1,0,0]\n\t" \
  "v_pk_fma_f32 v[178:179], v[178:179], v[164:165], v[166:167] op_sel_hi:[1,0,0]\n\t" \
  "v_pk_fma_f32 v[180:181], v[180:181], v[164:165], v[166:167] op_sel_hi:[1,0,0]\n\t" \
  "v_pk_fma_f32 v[182:183], v[182:183], v[164:165], v[166:167] op_sel_hi:[1,0,0]\n\t" \
  "v_pk_fma_f32 v[176:177], v[176:177], v[128:129], v[136:137]\n\t" \
  "v_pk_fma_f32 v[178:179], v[178:179], v[130:131], v[138:139]\n\t" \
  "v_pk_fma_f32 v[180:181], v[180:181], v[132:133], v[140:141]\n\t" \
  "v_pk_fma_f32 v[182:183], v[182:183], v[134:135], v[142:143]\n\t" \
  "v_cvt_pk_bf16_f32 v36, v176, v177\n\t" \
  "v_cvt_pk_bf16_f32 v37, v178, v179\n\t" \
  "v_cvt_pk_bf16_f32 v38, v180, v181\n\t" \
  "v_cvt_pk_bf16_f32 v39, v182, v183\n\t" \
  "buffer_load_dwordx4 v213, %[sx], s96 offen offset:1024 lds\n\t" \
  ".p2align 3\n\t" \
  "v_lshlrev_b32 v176, 16, v40\n\t" \
  "v_and_b32 v177, s98, v40\n\t" \
  "v_lshlrev_b32 v178, 16, v41\n\t" \
  "v_and_b32 v179, s98, v41\n\t" \
  "v_lshlrev_b32 v180, 16, v42\n\t" \
  "v_and_b32 v181, s98, v42\n\t" \
  "v_lshlrev_b32 v182, 16, v43\n\t" \
  "v_and_b32 v183, s98, v43\n\t" \
  "v_pk_fma_f32 v[176:177], v[176:177], v[168:169], v[170:171] op_sel_hi:[1,0,0]\n\t" \
  "v_pk_fma_f32 v[178:179], v[178:179], v[168:169], v[170:171] op_sel_hi:[1,0,0]\n\t" \
  "v_pk_fma_f32 v[180:181], v[180:181], v[168:169], v[170:171] op_sel_hi:[1,0,0]\n\t" \
  "v_pk_fma_f32 v[182:183], v[182:183], v[168:169], v[170:171] op_sel_hi:[1,0,0]\n\t" \
  "v_pk_fma_f32 v[176:177], v[176:177], v[128:129], v[136:137]\n\t" \
  "v_pk_fma_f32 v[178:179], v[178:179], v[130:131], v[138:139]\n\t" \
  "v_pk_fma_f32 v[180:181], v[180:181], v[132:133], v[140:141]\n\t" \
  "v_pk_fma_f32 v[182:183], v[182:183], v[134:135], v[142:143]\n\t" \
  "v_cvt_pk_bf16_f32 v40, v176, v177\n\t" \
  "v_cvt_pk_bf16_f32 v41, v178, v179\n\t" \
  "v_cvt_pk_bf16_f32 v42, v180, v181\n\t" \
  "v_cvt_pk_bf16_f32 v43, v182, v183\n\t" \
  "s_add_u32 m0, %[mx], 104448\n\t" \
  "s_xor_b32 s96, %[c17], 393216\n\t" \
  ".p2align 3\n\t" \
  "s_add_u32 s96, s96, %[sl2]\n\t" \
  "buffer_load_dwordx4 v212, %[sx], s96 offen lds\n\t" \
  ".p2align 3\n\t" \
  "v_lshlrev_b32 v176, 16, v44\n\t" \
  "v_and_b32 v177, s98, v44\n\t" \
  "v_lshlrev_b32 v178, 16, v45\n\t" \
  "v_and_b32 v179, s98, v45\n\t" \
  "v_lshlrev_b32 v180, 16, v46\n\t" \
  "v_and_b32 v181, s98, v46\n\t" \
  "v_lshlrev_b32 v182, 16, v47\n\t" \
  "v_and_b32 v183, s98, v47\n\t" \
  "v_pk_fma_f32 v[176:177], v[176:177], v[172:173], v[174:175] op_sel_hi:[1,0,0]\n\t" \
  "v_pk_fma_f32 v[178:179], v[178:179], v[172:173], v[174:175] op_sel_hi:[1,0,0]\n\t" \
  "v_pk_fma_f32 v[180:181], v[180:181], v[172:173], v[174:175] op_sel_hi:[1,0,0]\n\t" \
  "v_pk_fma_f32 v[182:183], v[182:183], v[172:173], v[174:175] op_sel_hi:[1,0,0]\n\t" \
  "v_pk_fma_f32 v[176:177], v[176:177], v[128:129], v[136:137]\n\t" \
  "v_pk_fma_f32 v[178:179], v[178:179], v[130:131], v[138:139]\n\t" \
  "v_pk_fma_f32 v[180:181], v[180:181], v[132:133], v[140:141]\n\t" \
  "v_pk_fma_f32 v[182:183], v[182:183], v[134:135], v[142:143]\n\t" \
  "v_cvt_pk_bf16_f32 v44, v176, v177\n\t" \
  "v_cvt_pk_bf16_f32 v45, v178, v179\n\t" \
  "v_cvt_pk_bf16_f32 v46, v180, v181\n\t" \
  "v_cvt_pk_bf16_f32 v47, v182, v183\n\t" \
  "buffer_load_dwordx4 v213, %[sx], s96 offen offset:1024 lds\n\t" \
  ".p2align 3\n\t" \
  "ds_read_b128 v[128:131], v208 offset:1024\n\t" \
  "ds_read_b128 v[132:135], v208 offset:1040\n\t" \
  "ds_read_b128 v[136:139], v208 offset:1280\n\t" \
  "ds_read_b128 v[140:143], v208 offset:1296\n\t" \
  FSTP("v_writelane_b32 %[stv], s94, 30\n\t") \
  FSTP("v_writelane_b32 %[stv], s92, 31\n\t") \
  FSTP("s_memrealtime s[94:95]\n\t") \
  FSTP("s_memtime s[92:93]\n\t") \
  "v_lshlrev_b32 v176, 16, v48\n\t" \
  "v_and_b32 v177, s98, v48\n\t" \
  "v_lshlrev_b32 v178, 16, v49\n\t" \
  "v_and_b32 v179, s98, v49\n\t" \
  "v_lshlrev_b32 v180, 16, v50\n\t" \
  "v_and_b32 v181, s98, v50\n\t" \
  "v_lshlrev_b32 v182, 16, v51\n\t" \
  "v_and_b32 v183, s98, v51\n\t" \
  "v_pk_fma_f32 v[176:177], v[176:177], v[160:161], v[162:163] op_sel_hi:[1,0,0]\n\t" \
  "v_pk_fma_f32 v[178:179], v[178:179], v[160:161], v[162:163] op_sel_hi:[1,0,0]\n\t" \
  "v_pk_fma_f32 v[180:181], v[180:181], v[160:161], v[162:163] op_sel_hi:[1,0,0]\n\t" \
  "v_pk_fma_f32 v[182:183], v[182:183], v[160:161], v[162:163] op_sel_hi:[1,0,0]\n\t" \
  "s_waitcnt lgkmcnt(4)\n\t" \
  "s_nop 0\n\t" \
  "v_pk_fma_f32 v[176:177], v[176:177], v[144:145], v[152:153]\n\t" \
  "v_pk_fma_f32 v[178:179], v[178:179], v[146:147], v[154:155]\n\t" \
  "v_pk_fma_f32 v[180:181], v[180:181], v[148:149], v[156:157]\n\t" \
  "v_pk_fma_f32 v[182:183], v[182:183], v[150:151], v[158:159]\n\t" \
  "v_cvt_pk_bf16_f32 v48, v176, v177\n\t" \
  "v_cvt_pk_bf16_f32 v49, v178, v179\n\t" \
  "v_cvt_pk_bf16_f32 v50, v180, v181\n\t" \
  "v_cvt_pk_bf16_f32 v51, v182, v183\n\t" \
  "s_add_u32 m0, %[mw], 49152\n\t" \
  "s_mov_b32 s96, %[w1]\n\t" \
  ".p2align 3\n\t" \
  "buffer_load_dwordx4 v200, %[sw], s96 offen offset:0 lds\n\t" \
  ".p2align 3\n\t" \
  "ds_read_b64_tr_b16 a[128:129], v184 offset:0\n\t" \
  "ds_read_b64_tr_b16 a[130:131], v184 offset:1024\n\t" \
  "ds_read_b64_tr_b16 a[132:133], v185 offset:0\n\t" \
  "ds_read_b64_tr_b16 a[134:135], v185 offset:1024\n\t" \
  "v_lshlrev_b32 v176, 16, v52\n\t" \
  "v_and_b32 v177, s98, v52\n\t" \
  "v_lshlrev_b32 v178, 16, v53\n\t" \
  "v_and_b32 v179, s98, v53\n\t" \
  "v_lshlrev_b32 v180, 16, v54\n\t" \
  "v_and_b32 v181, s98, v54\n\t" \
  "v_lshlrev_b32 v182, 16, v55\n\t" \
  "v_and_b32 v183, s98, v55\n\t" \
  "v_pk_fma_f32 v[176:177], v[176:177], v[164:165], v[166:167] op_sel_hi:[1,0,0]\n\t" \
  "v_pk_fma_f32 v[178:179], v[178:179], v[164:165], v[166:167] op_sel_hi:[1,0,0]\n\t" \
  "v_pk_fma_f32 v[180:181], v[180:181], v[164:165], v[166:167] op_sel_hi:[1,0,0]\n\t" \
  "v_pk_fma_f32 v[182:183], v[182:183], v[164:165], v[166:167] op_sel_hi:[1,0,0]\n\t" \
  "v_pk_fma_f32 v[176:177], v[176:177], v[144:145], v[152:153]\n\t" \
  "v_pk_fma_f32 v[178:179], v[178:179], v[146:147], v[154:155]\n\t" \
  "v_pk_fma_f32 v[180:181], v[180:181], v[148:149], v[156:157]\n\t" \
  "v_pk_fma_f32 v[182:183], v[182:183], v[150:151], v[158:159]\n\t" \
  "v_cvt_pk_bf16_f32 v52, v176, v177\n\t" \
  "v_cvt_pk_bf16_f32 v53, v178, v179\n\t" \
  "v_cvt_pk_bf16_f32 v54, v180, v181\n\t" \
  "v_cvt_pk_bf16_f32 v55, v182, v183\n\t" \
  "buffer_load_dwordx4 v201, %[sw], s96 offen offset:1024 lds\n\t" \
  ".p2align 3\n\t" \
  "ds_read_b64_tr_b16 a[136:137], v186 offset:0\n\t" \
  "ds_read_b64_tr_b16 a[138:139], v186 offset:1024\n\t" \
  "ds_read_b64_tr_b16 a[140:141], v187 offset:0\n\t" \
  "ds_read_b64_tr_b16 a[142:143], v187 offset:1024\n\t" \
  "v_lshlrev_b32 v176, 16, v56\n\t" \
  "v_and_b32 v177, s98, v56\n\t" \
  "v_lshlrev_b32 v178, 16, v57\n\t" \
  "v_and_b32 v179, s98, v57\n\t" \
  "v_lshlrev_b32 v180, 16, v58\n\t" \
  "v_and_b32 v181, s98, v58\n\t" \
  "v_lshlrev_b32 v182, 16, v59\n\t" \
  "v_and_b32 v183, s98, v59\n\t" \
  "v_pk_fma_f32 v[176:177], v[176:177], v[168:169], v[170:171] op_sel_hi:[1,0,0]\n\t" \
  "v_pk_fma_f32 v[178:179], v[178:179], v[168:169], v[170:171] op_sel_hi:[1,0,0]\n\t" \
  "v_pk_fma_f32 v[180:181], v[180:181], v[168:169], v[170:171] op_sel_hi:[1,0,0]\n\t" \
  "v_pk_fma_f32 v[182:183], v[182:183], v[168:169], v[170:171] op_sel_hi:[1,0,0]\n\t" \
  "v_pk_fma_f32 v[176:177], v[176:177], v[144:145], v[152:153]\n\t" \
  "v_pk_fma_f32 v[178:179], v[178:179], v[146:147], v[154:155]\n\t" \
  "v_pk_fma_f32 v[180:181], v[180:181], v[148:149], v[156:157]\n\t" \
  "v_pk_fma_f32 v[182:183], v[182:183], v[150:151], v[158:159]\n\t" \
  "v_cvt_pk_bf16_f32 v56, v176, v177\n\t" \
  "v_cvt_pk_bf16_f32 v57, v178, v179\n\t" \
  "v_cvt_pk_bf16_f32 v58, v180, v181\n\t" \
  "v_cvt_pk_bf16_f32 v59, v182, v183\n\t" \
  "buffer_load_dwordx4 v202, %[sw], s96 offen offset:2048 lds\n\t" \
  ".p2align 3\n\t" \
  "ds_read_b64_tr_b16 a[144:145], v188 offset:0\n\t" \
  "ds_read_b64_tr_b16 a[146:147], v188 offset:1024\n\t" \
  "ds_read_b64_tr_b16 a[148:149], v189 offset:0\n\t" \
  "ds_read_b64_tr_b16 a[150:151], v189 offset:1024\n\t" \
  "v_lshlrev_b32 v176, 16, v60\n\t" \
  "v_and_b32 v177, s98, v60\n\t" \
  "v_lshlrev_b32 v178, 16, v61\n\t" \
  "v_and_b32 v179, s98, v61\n\t" \
  "v_lshlrev_b32 v180, 16, v62\n\t" \
  "v_and_b32 v181, s98, v62\n\t" \
  "v_lshlrev_b32 v182, 16, v63\n\t" \
  "v_and_b32 v183, s98, v63\n\t" \
  "v_pk_fma_f32 v[176:177], v[176:177], v[172:173], v[174:175] op_sel_hi:[1,0,0]\n\t" \
  "v_pk_fma_f32 v[178:179], v[178:179], v[172:173], v[174:175] op_sel_hi:[1,0,0]\n\t" \
  "v_pk_fma_f32 v[180:181], v[180:181], v[172:173], v[174:175] op_sel_hi:[1,0,0]\n\t" \
  "v_pk_fma_f32 v[182:183], v[182:183], v[172:173], v[174:175] op_sel_hi:[1,0,0]\n\t" \
  "v_pk_fma_f32 v[176:177], v[176:177], v[144:145], v[152:153]\n\t" \
  "v_pk_fma_f32 v[178:179], v[178:179], v[146:147], v[154:155]\n\t" \
  "v_pk_fma_f32 v[180:181], v[180:181], v[148:149], v[156:157]\n\t" \
  "v_pk_fma_f32 v[182:183], v[182:183], v[150:151], v[158:159]\n\t" \
  "v_cvt_pk_bf16_f32 v60, v176, v177\n\t" \
  "v_cvt_pk_bf16_f32 v61, v178, v179\n\t" \
  "v_cvt_pk_bf16_f32 v62, v180, v181\n\t" \
  "v_cvt_pk_bf16_f32 v63, v182, v183\n\t" \
  "buffer_load_dwordx4 v203, %[sw], s96 offen offset:3072 lds\n\t" \
  ".p2align 3\n\t" \
  "ds_read_b64_tr_b16 a[152:153], v190 offset:0\n\t" \
  "ds_read_b64_tr_b16 a[154:155], v190 offset:1024\n\t" \
  "ds_read_b64_tr_b16 a[156:157], v191 offset:0\n\t" \
  "ds_read_b64_tr_b16 a[158:159], v191 offset:1024\n\t" \
  "ds_read_b128 v[144:147], v208 offset:1152\n\t" \
  "ds_read_b128 v[148:151], v208 offset:1168\n\t" \
  "ds_read_b128 v[152:155], v208 offset:1408\n\t" \
  "ds_read_b128 v[156:159], v208 offset:1424\n\t" \
  FSTP("v_writelane_b32 %[stv], s94, 32\n\t") \
  FSTP("v_writelane_b32 %[stv], s92, 33\n\t") \
  FSTP("s_memrealtime s[94:95]\n\t") \
  FSTP("s_memtime s[92:93]\n\t") \
  "s_waitcnt vmcnt(12)\n\t" \
  "s_nop 0\n\t" \
  "s_mov_b64 exec, 1\n\t" \
  "buffer_store_dword v211, off, %[ws], %[fw] sc1\n\t" \
  "s_mov_b64 exec, -1\n\t" \
  FSTP("v_writelane_b32 %[stv], s94, 34\n\t") \
  FSTP("v_writelane_b32 %[stv], s92, 35\n\t") \
  FSTP("s_memrealtime s[94:95]\n\t") \
  FSTP("s_memtime s[92:93]\n\t") \
  "s_waitcnt lgkmcnt(4)\n\t" \
  "v_mfma_f32_16x16x32_bf16 a[0:3], a[128:131], v[0:3], 0\n\t" \
  "s_add_u32 m0, %[mw], 98304\n\t" \
  ".Lhs0:\n\ts_cbranch_execz .Lhs0n\n\t" \
  "ds_read_b64_tr_b16 a[160:161], v184 offset:8192\n\t" \
  "v_mfma_f32_16x16x32_bf16 a[4:7], a[128:131], v[4:7], 0\n\t" \
  "s_mov_b32 s96, %[w2]\n\t" \
  "ds_read_b64_tr_b16 a[162:163], v184 offset:9216\n\t" \
  "v_mfma_f32_16x16x32_bf16 a[8:11], a[128:131], v[8:11], 0\n\t" \
  "buffer_load_dwordx4 v200, %[sw], s96 offen offset:0 lds\n\t" \
  "v_mfma_f32_16x16x32_bf16 a[12:15], a[128:131], v[12:15], 0\n\t" \
  "buffer_load_dwordx4 v201, %[sw], s96 offen offset:1024 lds\n\t" \
  "v_mfma_f32_16x16x32_bf16 a[28:31], a[132:135], v[12:15], 0\n\t" \
  "ds_read_b64_tr_b16 a[164:165], v185 offset:8192\n\t" \
  "v_mfma_f32_16x16x32_bf16 a[24:27], a[132:135], v[8:11], 0\n\t" \
  "ds_read_b64_tr_b16 a[166:167], v185 offset:9216\n\t" \
  "v_mfma_f32_16x16x32_bf16 a[20:23], a[132:135], v[4:7], 0\n\t" \
  "buffer_load_dwordx4 v202, %[sw], s96 offen offset:2048 lds\n\t" \
  "v_mfma_f32_16x16x32_bf16 a[16:19], a[132:135], v[0:3], 0\n\t" \
  "ds_read_b64_tr_b16 a[168:169], v186 offset:8192\n\t" \
  "v_mfma_f32_16x16x32_bf16 a[32:35], a[136:139], v[0:3], 0\n\t" \
  "s_waitcnt vmcnt(8)\n\t" \
  "ds_read_b64_tr_b16 a[170:171], v186 offset:9216\n\t" \
  "ds_read_b128 v[64:67], v206 offset:0\n\t" \
  "v_mfma_f32_16x16x32_bf16 a[36:39], a[136:139], v[4:7], 0\n\t" \
  "buffer_load_dwordx4 v203, %[sw], s96 offen offset:3072 lds\n\t" \
  "v_mfma_f32_16x16x32_bf16 a[40:43], a[136:139], v[8:11], 0\n\t" \
  "s_add_u32 m0, %[mx], 0\n\t" \
  "ds_read_b64_tr_b16 a[172:173], v187 offset:8192\n\t" \
  "ds_read_b128 v[68:71], v206 offset:2048\n\t" \
  "v_mfma_f32_16x16x32_bf16 a[44:47], a[136:139], v[12:15], 0\n\t" \
  "s_xor_b32 s96, %[c17], 0\n\t" \
  "ds_read_b64_tr_b16 a[174:175], v187 offset:9216\n\t" \
  "ds_read_b128 v[72:75], v206 offset:4096\n\t" \
  "v_mfma_f32_16x16x32_bf16 a[60:63], a[140:143], v[12:15], 0\n\t" \
  "s_add_u32 s96, s96, %[sl3]\n\t" \
  "ds_read_b64_tr_b16 a[176:177], v188 offset:8192\n\t" \
  "ds_read_b128 v[76:79], v206 offset:6144\n\t" \
  "s_waitcnt lgkmcnt(0)\n\t" \
  "v_lshlrev_b32 v176, 16, v64\n\t" \
  "v_and_b32 v177, s98, v64\n\t" \
  "v_mfma_f32_16x16x32_bf16 a[56:59], a[140:143], v[8:11], 0\n\t" \
  "buffer_load_dwordx4 v212, %[sx], s96 offen lds\n\t" \
  "v_lshlrev_b32 v178, 16, v65\n\t" \
  "v_and_b32 v179, s98, v65\n\t" \
  "v_lshlrev_b32 v180, 16, v66\n\t" \
  "v_mfma_f32_16x16x32_bf16 a[52:55], a[140:143], v[4:7], 0\n\t" \
  "ds_read_b64_tr_b16 a[178:179], v188 offset:9216\n\t" \
  "v_and_b32 v181, s98, v66\n\t" \
  "v_lshlrev_b32 v182, 16, v67\n\t" \
  "v_and_b32 v183, s98, v67\n\t" \
  "v_fma_f32 v176, v176, v160, v162\n\t" \
  "v_mfma_f32_16x16x32_bf16 a[48:51], a[140:143], v[0:3], 0\n\t" \
  "buffer_load_dwordx4 v213, %[sx], s96 offen offset:1024 lds\n\t" \
  "v_fma_f32 v177, v177, v160, v162\n\t" \
  "v_fma_f32 v178, v178, v160, v162\n\t" \
  "v_fma_f32 v179, v179, v160, v162\n\t" \
  "v_mfma_f32_16x16x32_bf16 a[64:67], a[144:147], v[0:3], 0\n\t" \
  "s_add_u32 m0, %[mx], 2048\n\t" \
  "ds_read_b64_tr_b16 a[180:181], v189 offset:8192\n\t" \
  "v_fma_f32 v180, v180, v160, v162\n\t" \
  "v_fma_f32 v181, v181, v160, v162\n\t" \
  "v_fma_f32 v182, v182, v160, v162\n\t" \
  "v_fma_f32 v183, v183, v160, v162\n\t" \
  "v_mfma_f32_16x16x32_bf16 a[68:71], a[144:147], v[4:7], 0\n\t" \
  "s_xor_b32 s96, %[c17], 131072\n\t" \
  "ds_read_b64_tr_b16 a[182:183], v189 offset:9216\n\t" \
  "v_fma_f32 v176, v176, v128, v136\n\t" \
  "v_fma_f32 v177, v177, v129, v137\n\t" \
  "v_fma_f32 v178, v178, v130, v138\n\t" \
  "v_fma_f32 v179, v179, v131, v139\n\t" \
  "v_mfma_f32_16x16x32_bf16 a[72:75], a[144:147], v[8:11], 0\n\t" \
  "s_add_u32 s96, s96, %[sl3]\n\t" \
  "buffer_load_dwordx4 v212, %[sx], s96 offen lds\n\t" \
  "v_fma_f32 v180, v180, v132, v140\n\t" \
  "v_fma_f32 v181, v181, v133, v141\n\t" \
  "v_mfma_f32_16x16x32_bf16 a[76:79], a[144:147], v[12:15], 0\n\t" \
  "ds_read_b64_tr_b16 a[184:185], v190 offset:8192\n\t" \
  "v_fma_f32 v182, v182, v134, v142\n\t" \
  "v_fma_f32 v183, v183, v135, v143\n\t" \
  "v_cvt_pk_bf16_f32 v64, v176, v177\n\t" \
  "v_cvt_pk_bf16_f32 v65, v178, v179\n\t" \
  "v_mfma_f32_16x16x32_bf16 a[92:95], a[148:151], v[12:15], 0\n\t" \
  "buffer_load_dwordx4 v213, %[sx], s96 offen offset:1024 lds\n\t" \
  "v_cvt_pk_bf16_f32 v66, v180, v181\n\t" \
  "v_cvt_pk_bf16_f32 v67, v182, v183\n\t" \
  "v_lshlrev_b32 v176, 16, v68\n\t" \
  "v_mfma_f32_16x16x32_bf16 a[88:91], a[148:151], v[8:11], 0\n\t" \
  "s_add_u32 m0, %[mx], 4096\n\t" \
  "ds_read_b64_tr_b16 a[186:187], v190 offset:9216\n\t" \
  "v_and_b32 v177, s98, v68\n\t" \
  "v_lshlrev_b32 v178, 16, v69\n\t" \
  "v_and_b32 v179, s98, v69\n\t" \
  "v_lshlrev_b32 v180, 16, v70\n\t" \
  "v_mfma_f32_16x16x32_bf16 a[84:87], a[148:151], v[4:7], 0\n\t" \
  "s_xor_b32 s96, %[c17], 262144\n\t" \
  "s_add_u32 s96, s96, %[sl3]\n\t" \
  "buffer_load_dwordx4 v212, %[sx], s96 offen lds\n\t" \
  "v_and_b32 v181, s98, v70\n\t" \
  "v_mfma_f32_16x16x32_bf16 a[80:83], a[148:151], v[0:3], 0\n\t" \
  "ds_read_b64_tr_b16 a[188:189], v191 offset:8192\n\t" \
  "v_lshlrev_b32 v182, 16, v71\n\t" \
  "v_and_b32 v183, s98, v71\n\t" \
  "v_fma_f32 v176, v176, v164, v166\n\t" \
  "v_fma_f32 v177, v177, v164, v166\n\t" \
  "v_mfma_f32_16x16x32_bf16 a[96:99], a[152:155], v[0:3], 0\n\t" \
  "buffer_load_dwordx4 v213, %[sx], s96 offen offset:1024 lds\n\t" \
  "v_fma_f32 v178, v178, v164, v166\n\t" \
  "v_fma_f32 v179, v179, v164, v166\n\t" \
  "v_fma_f32 v180, v180, v164, v166\n\t" \
  "v_mfma_f32_16x16x32_bf16 a[100:103], a[152:155], v[4:7], 0\n\t" \
  "s_add_u32 m0, %[mx], 6144\n\t" \
  "ds_read_b64_tr_b16 a[190:191], v191 offset:9216\n\t" \
  "v_fma_f32 v181, v181, v164, v166\n\t" \
  "v_fma_f32 v182, v182, v164, v166\n\t" \
  "v_fma_f32 v183, v183, v164, v166\n\t" \
  "v_fma_f32 v176, v176, v128, v136\n\t" \
  "v_mfma_f32_16x16x32_bf16 a[104:107], a[152:155], v[8:11], 0\n\t" \
  "s_xor_b32 s96, %[c17], 393216\n\t" \
  "s_add_u32 s96, s96, %[sl3]\n\t" \
  "buffer_load_dwordx4 v212, %[sx], s96 offen lds\n\t" \
  "v_fma_f32 v177, v177, v129, v137\n\t" \
  "v_mfma_f32_16x16x32_bf16 a[108:111], a[152:155], v[12:15], 0\n\t" \
  "v_fma_f32 v178, v178, v130, v138\n\t" \
  "v_fma_f32 v179, v179, v131, v139\n\t" \
  "v_fma_f32 v180, v180, v132, v140\n\t" \
  "v_fma_f32 v181, v181, v133, v141\n\t" \
  "v_fma_f32 v182, v182, v134, v142\n\t" \
  "v_mfma_f32_16x16x32_bf16 a[124:127], a[156:159], v[12:15], 0\n\t" \
  "buffer_load_dwordx4 v213, %[sx], s96 offen offset:1024 lds\n\t" \
  "v_fma_f32 v183, v183, v135, v143\n\t" \
  "v_cvt_pk_bf16_f32 v68, v176, v177\n\t" \
  "v_cvt_pk_bf16_f32 v69, v178, v179\n\t" \
  "v_mfma_f32_16x16x32_bf16 a[120:123], a[156:159], v[8:11], 0\n\t" \
  "v_cvt_pk_bf16_f32 v70, v180, v181\n\t" \
  "v_cvt_pk_bf16_f32 v71, v182, v183\n\t" \
  "v_lshlrev_b32 v176, 16, v72\n\t" \
  "v_and_b32 v177, s98, v72\n\t" \
  "v_mfma_f32_16x16x32_bf16 a[116:119], a[156:159], v[4:7], 0\n\t" \
  "v_lshlrev_b32 v178, 16, v73\n\t" \
  "v_and_b32 v179, s98, v73\n\t" \
  "v_lshlrev_b32 v180, 16, v74\n\t" \
  "v_and_b32 v181, s98, v74\n\t" \
  "v_lshlrev_b32 v182, 16, v75\n\t" \
  "v_and_b32 v183, s98, v75\n\t" \
  "v_mfma_f32_16x16x32_bf16 a[112:115], a[156:159], v[0:3], 0\n\t" \
  FSTP("v_writelane_b32 %[stv], s94, 36\n\t") \
  FSTP("v_writelane_b32 %[stv], s92, 37\n\t") \
  FSTP("s_memrealtime s[94:95]\n\t") \
  FSTP("s_memtime s[92:93]\n\t") \
  "s_waitcnt vmcnt(13) lgkmcnt(0)\n\t" \
  "s_barrier\n\t" \
  "v_mfma_f32_16x16x32_bf16 a[0:3], a[160:163], v[16:19], a[0:3]\n\t" \
  "s_add_u32 m0, %[mw], 0\n\t" \
  "ds_read_b64_tr_b16 a[128:129], v184 offset:49152\n\t" \
  "v_fma_f32 v176, v176, v168, v170\n\t" \
  "v_fma_f32 v177, v177, v168, v170\n\t" \
  "v_mfma_f32_16x16x32_bf16 a[4:7], a[160:163], v[20:23], a[4:7]\n\t" \
  "s_mov_b32 s96, %[w3]\n\t" \
  "ds_read_b64_tr_b16 a[130:131], v184 offset:50176\n\t" \
  "v_fma_f32 v178, v178, v168, v170\n\t" \
  "v_fma_f32 v179, v179, v168, v170\n\t" \
  "v_mfma_f32_16x16x32_bf16 a[8:11], a[160:163], v[24:27], a[8:11]\n\t" \
  "ds_read_b64_tr_b16 a[132:133], v185 offset:49152\n\t" \
  "v_fma_f32 v180, v180, v168, v170\n\t" \
  "v_fma_f32 v181, v181, v168, v170\n\t" \
  "v_mfma_f32_16x16x32_bf16 a[12:15], a[160:163], v[28:31], a[12:15]\n\t" \
  "ds_read_b64_tr_b16 a[134:135], v185 offset:50176\n\t" \
  "v_fma_f32 v182, v182, v168, v170\n\t" \
  "v_fma_f32 v183, v183, v168, v170\n\t" \
  "v_mfma_f32_16x16x32_bf16 a[28:31], a[164:167], v[28:31], a[28:31]\n\t" \
  "buffer_load_dwordx4 v200, %[sw], s96 offen offset:0 lds\n\t" \
  "v_fma_f32 v176, v176, v128, v136\n\t" \
  "v_mfma_f32_16x16x32_bf16 a[24:27], a[164:167], v[24:27], a[24:27]\n\t" \
  "ds_read_b64_tr_b16 a[136:137], v186 offset:49152\n\t" \
  "v_fma_f32 v177, v177, v129, v137\n\t" \
  "v_fma_f32 v178, v178, v130, v138\n\t" \
  "v_mfma_f32_16x16x32_bf16 a[20:23], a[164:167], v[20:23], a[20:23]\n\t" \
  "ds_read_b64_tr_b16 a[138:139], v186 offset:50176\n\t" \
  "v_fma_f32 v179, v179, v131, v139\n\t" \
  "v_fma_f32 v180, v180, v132, v140\n\t" \
  "v_mfma_f32_16x16x32_bf16 a[16:19], a[164:167], v[16:19], a[16:19]\n\t" \
  "buffer_load_dwordx4 v201, %[sw], s96 offen offset:1024 lds\n\t" \
  "v_fma_f32 v181, v181, v133, v141\n\t" \
  "v_mfma_f32_16x16x32_bf16 a[32:35], a[168:171], v[16:19], a[32:35]\n\t" \
  "ds_read_b64_tr_b16 a[140:141], v187 offset:49152\n\t" \
  "v_fma_f32 v182, v182, v134, v142\n\t" \
  "v_fma_f32 v183, v183, v135, v143\n\t" \
  "v_mfma_f32_16x16x32_bf16 a[36:39], a[168:171], v[20:23], a[36:39]\n\t" \
  "ds_read_b64_tr_b16 a[142:143], v187 offset:50176\n\t" \
  "v_cvt_pk_bf16_f32 v72, v176, v177\n\t" \
  "v_cvt_pk_bf16_f32 v73, v178, v179\n\t" \
  "v_mfma_f32_16x16x32_bf16 a[40:43], a[168:171], v[24:27], a[40:43]\n\t" \
  "ds_read_b64_tr_b16 a[144:145], v188 offset:49152\n\t" \
  "v_cvt_pk_bf16_f32 v74, v180, v181\n\t" \
  "v_cvt_pk_bf16_f32 v75, v182, v183\n\t" \
  "v_mfma_f32_16x16x32_bf16 a[44:47], a[168:171], v[28:31], a[44:47]\n\t" \
  "buffer_load_dwordx4 v202, %[sw], s96 offen offset:2048 lds\n\t" \
  "v_mfma_f32_16x16x32_bf16 a[60:63], a[172:175], v[28:31], a[60:63]\n\t" \
  "ds_read_b64_tr_b16 a[146:147], v188 offset:50176\n\t" \
  "ds_read_b128 v[80:83], v207 offset:0\n\t" \
  "v_mfma_f32_16x16x32_bf16 a[56:59], a[172:175], v[24:27], a[56:59]\n\t" \
  "ds_read_b64_tr_b16 a[148:149], v189 offset:49152\n\t" \
  "ds_read_b128 v[84:87], v207 offset:2048\n\t" \
  "v_mfma_f32_16x16x32_bf16 a[52:55], a[172:175], v[20:23], a[52:55]\n\t" \
  "ds_read_b64_tr_b16 a[150:151], v189 offset:50176\n\t" \
  "ds_read_b128 v[88:91], v207 offset:4096\n\t" \
  "v_mfma_f32_16x16x32_bf16 a[48:51], a[172:175], v[16:19], a[48:51]\n\t" \
  "ds_read_b64_tr_b16 a[152:153], v190 offset:49152\n\t" \
  "ds_read_b128 v[92:95], v207 offset:6144\n\t" \
  "v_lshlrev_b32 v176, 16, v76\n\t" \
  "v_and_b32 v177, s98, v76\n\t" \
  "v_mfma_f32_16x16x32_bf16 a[64:67], a[176:179], v[16:19], a[64:67]\n\t" \
  "buffer_load_dwordx4 v203, %[sw], s96 offen offset:3072 lds\n\t" \
  "v_lshlrev_b32 v178, 16, v77\n\t" \
  "v_and_b32 v179, s98, v77\n\t" \
  "v_mfma_f32_16x16x32_bf16 a[68:71], a[176:179], v[20:23], a[68:71]\n\t" \
  "ds_read_b64_tr_b16 a[154:155], v190 offset:50176\n\t" \
  "v_lshlrev_b32 v180, 16, v78\n\t" \
  "v_and_b32 v181, s98, v78\n\t" \
  "v_lshlrev_b32 v182, 16, v79\n\t" \
  "v_mfma_f32_16x16x32_bf16 a[72:75], a[176:179], v[24:27], a[72:75]\n\t" \
  "ds_read_b64_tr_b16 a[156:157], v191 offset:49152\n\t" \
  "v_and_b32 v183, s98, v79\n\t" \
  "v_fma_f32 v176, v176, v172, v174\n\t" \
  "v_fma_f32 v177, v177, v172, v174\n\t" \
  "v_mfma_f32_16x16x32_bf16 a[76:79], a[176:179], v[28:31], a[76:79]\n\t" \
  "ds_read_b64_tr_b16 a[158:159], v191 offset:50176\n\t" \
  "v_fma_f32 v178, v178, v172, v174\n\t" \
  "v_fma_f32 v179, v179, v172, v174\n\t" \
  "v_mfma_f32_16x16x32_bf16 a[92:95], a[180:183], v[28:31], a[92:95]\n\t" \
  "v_fma_f32 v180, v180, v172, v174\n\t" \
  "v_fma_f32 v181, v181, v172, v174\n\t" \
  "v_fma_f32 v182, v182, v172, v174\n\t" \
  "v_mfma_f32_16x16x32_bf16 a[88:91], a[180:183], v[24:27], a[88:91]\n\t" \
  "v_fma_f32 v183, v183, v172, v174\n\t" \
  "v_fma_f32 v176, v176, v128, v136\n\t" \
  "v_fma_f32 v177, v177, v129, v137\n\t" \
  "v_mfma_f32_16x16x32_bf16 a[84:87], a[180:183], v[20:23], a[84:87]\n\t" \
  "global_load_dword %[pf], v215, %[wq]\n\t" \
  "v_fma_f32 v178, v178, v130, v138\n\t" \
  "v_mfma_f32_16x16x32_bf16 a[80:83], a[180:183], v[16:19], a[80:83]\n\t" \
  "v_fma_f32 v179, v179, v131, v139\n\t" \
  "v_fma_f32 v180, v180, v132, v140\n\t" \
  "v_fma_f32 v181, v181, v133, v141\n\t" \
  "v_mfma_f32_16x16x32_bf16 a[96:99], a[184:187], v[16:19], a[96:99]\n\t" \
  "v_fma_f32 v182, v182, v134, v142\n\t" \
  "v_fma_f32 v183, v183, v135, v143\n\t" \
  "v_cvt_pk_bf16_f32 v76, v176, v177\n\t" \
  "v_mfma_f32_16x16x32_bf16 a[100:103], a[184:187], v[20:23], a[100:103]\n\t" \
  "v_cvt_pk_bf16_f32 v77, v178, v179\n\t" \
  "v_cvt_pk_bf16_f32 v78, v180, v181\n\t" \
  "v_cvt_pk_bf16_f32 v79, v182, v183\n\t" \
  "v_mfma_f32_16x16x32_bf16 a[104:107], a[184:187], v[24:27], a[104:107]\n\t" \
  "ds_read_b128 v[128:131], v208 offset:1536\n\t" \
  "ds_read_b128 v[132:135], v208 offset:1552\n\t" \
  "v_mfma_f32_16x16x32_bf16 a[108:111], a[184:187], v[28:31], a[108:111]\n\t" \
  "ds_read_b128 v[136:139], v208 offset:1792\n\t" \
  "ds_read_b128 v[140:143], v208 offset:1808\n\t" \
  "s_waitcnt lgkmcnt(7)\n\t" \
  "v_lshlrev_b32 v176, 16, v80\n\t" \
  "v_mfma_f32_16x16x32_bf16 a[124:127], a[188:191], v[28:31], a[124:127]\n\t" \
  "v_and_b32 v177, s98, v80\n\t" \
  "v_lshlrev_b32 v178, 16, v81\n\t" \
  "v_and_b32 v179, s98, v81\n\t" \
  "v_lshlrev_b32 v180, 16, v82\n\t" \
  "v_mfma_f32_16x16x32_bf16 a[120:123], a[188:191], v[24:27], a[120:123]\n\t" \
  "v_and_b32 v181, s98, v82\n\t" \
  "v_lshlrev_b32 v182, 16, v83\n\t" \
  "v_and_b32 v183, s98, v83\n\t" \
  "v_mfma_f32_16x16x32_bf16 a[116:119], a[188:191], v[20:23], a[116:119]\n\t" \
  "v_fma_f32 v176, v176, v160, v162\n\t" \
  "v_fma_f32 v177, v177, v160, v162\n\t" \
  "v_fma_f32 v178, v178, v160, v162\n\t" \
  "v_fma_f32 v179, v179, v160, v162\n\t" \
  "v_mfma_f32_16x16x32_bf16 a[112:115], a[188:191], v[16:19], a[112:115]\n\t" \
  FSTP("v_writelane_b32 %[stv], s94, 38\n\t") \
  FSTP("v_writelane_b32 %[stv], s92, 39\n\t") \
  FSTP("s_memrealtime s[94:95]\n\t") \
  FSTP("s_memtime s[92:93]\n\t") \
  "s_waitcnt lgkmcnt(4)\n\t" \
  "v_mfma_f32_16x16x32_bf16 a[0:3], a[128:131], v[32:35], a[0:3]\n\t" \
  "global_load_dword %[pf], v217, %[kv]\n\t" \
  "v_fma_f32 v180, v180, v160, v162\n\t" \
  "v_mfma_f32_16x16x32_bf16 a[4:7], a[128:131], v[36:39], a[4:7]\n\t" \
  "ds_read_b64_tr_b16 a[160:161], v184 offset:57344\n\t" \
  "v_fma_f32 v181, v181, v160, v162\n\t" \
  "v_fma_f32 v182, v182, v160, v162\n\t" \
  "v_mfma_f32_16x16x32_bf16 a[8:11], a[128:131], v[40:43], a[8:11]\n\t" \
  "ds_read_b64_tr_b16 a[162:163], v184 offset:58368\n\t" \
  "v_fma_f32 v183, v183, v160, v162\n\t" \
  "v_fma_f32 v176, v176, v144, v152\n\t" \
  "v_mfma_f32_16x16x32_bf16 a[12:15], a[128:131], v[44:47], a[12:15]\n\t" \
  "ds_read_b64_tr_b16 a[164:165], v185 offset:57344\n\t" \
  "v_fma_f32 v177, v177, v145, v153\n\t" \
  "v_fma_f32 v178, v178, v146, v154\n\t" \
  "v_mfma_f32_16x16x32_bf16 a[28:31], a[132:135], v[44:47], a[28:31]\n\t" \
  "ds_read_b64_tr_b16 a[166:167], v185 offset:58368\n\t" \
  "v_fma_f32 v179, v179, v147, v155\n\t" \
  "v_fma_f32 v180, v180, v148, v156\n\t" \
  "v_mfma_f32_16x16x32_bf16 a[24:27], a[132:135], v[40:43], a[24:27]\n\t" \
  "ds_read_b64_tr_b16 a[168:169], v186 offset:57344\n\t" \
  "v_fma_f32 v181, v181, v149, v157\n\t" \
  "v_fma_f32 v182, v182, v150, v158\n\t" \
  "v_mfma_f32_16x16x32_bf16 a[20:23], a[132:135], v[36:39], a[20:23]\n\t" \
  "ds_read_b64_tr_b16 a[170:171], v186 offset:58368\n\t" \
  "v_fma_f32 v183, v183, v151, v159\n\t" \
  "v_cvt_pk_bf16_f32 v80, v176, v177\n\t" \
  "v_mfma_f32_16x16x32_bf16 a[16:19], a[132:135], v[32:35], a[16:19]\n\t" \
  "ds_read_b64_tr_b16 a[172:173], v187 offset:57344\n\t" \
  "v_cvt_pk_bf16_f32 v81, v178, v179\n\t" \
  "v_cvt_pk_bf16_f32 v82, v180, v181\n\t" \
  "v_mfma_f32_16x16x32_bf16 a[32:35], a[136:139], v[32:35], a[32:35]\n\t" \
  "ds_read_b64_tr_b16 a[174:175], v187 offset:58368\n\t" \
  "v_cvt_pk_bf16_f32 v83, v182, v183\n\t" \
  "v_lshlrev_b32 v176, 16, v84\n\t" \
  "v_mfma_f32_16x16x32_bf16 a[36:39], a[136:139], v[36:39], a[36:39]\n\t" \
  "ds_read_b64_tr_b16 a[176:177], v188 offset:57344\n\t" \
  "v_and_b32 v177, s98, v84\n\t" \
  "v_lshlrev_b32 v178, 16, v85\n\t" \
  "v_mfma_f32_16x16x32_bf16 a[40:43], a[136:139], v[40:43], a[40:43]\n\t" \
  "ds_read_b64_tr_b16 a[178:179], v188 offset:58368\n\t" \
  "v_and_b32 v179, s98, v85\n\t" \
  "v_lshlrev_b32 v180, 16, v86\n\t" \
  "v_mfma_f32_16x16x32_bf16 a[44:47], a[136:139], v[44:47], a[44:47]\n\t" \
  "ds_read_b64_tr_b16 a[180:181], v189 offset:57344\n\t" \
  "v_and_b32 v181, s98, v86\n\t" \
  "v_lshlrev_b32 v182, 16, v87\n\t" \
  "v_mfma_f32_16x16x32_bf16 a[60:63], a[140:143], v[44:47], a[60:63]\n\t" \
  "ds_read_b64_tr_b16 a[182:183], v189 offset:58368\n\t" \
  "v_and_b32 v183, s98, v87\n\t" \
  "v_fma_f32 v176, v176, v164, v166\n\t" \
  "v_mfma_f32_16x16x32_bf16 a[56:59], a[140:143], v[40:43], a[56:59]\n\t" \
  "ds_read_b64_tr_b16 a[184:185], v190 offset:57344\n\t" \
  "v_fma_f32 v177, v177, v164, v166\n\t" \
  "v_fma_f32 v178, v178, v164, v166\n\t" \
  "v_mfma_f32_16x16x32_bf16 a[52:55], a[140:143], v[36:39], a[52:55]\n\t" \
  "buffer_store_short v218, v219, %[ws], 0 offen sc1\n\t" \
  "v_fma_f32 v179, v179, v164, v166\n\t" \
  "v_mfma_f32_16x16x32_bf16 a[48:51], a[140:143], v[32:35], a[48:51]\n\t" \
  "ds_read_b64_tr_b16 a[186:187], v190 offset:58368\n\t" \
  "v_fma_f32 v180, v180, v164, v166\n\t" \
  "v_fma_f32 v181, v181, v164, v166\n\t" \
  "v_mfma_f32_16x16x32_bf16 a[64:67], a[144:147], v[32:35], a[64:67]\n\t" \
  "ds_read_b64_tr_b16 a[188:189], v191 offset:57344\n\t" \
  "v_fma_f32 v182, v182, v164, v166\n\t" \
  "v_fma_f32 v183, v183, v164, v166\n\t" \
  "v_mfma_f32_16x16x32_bf16 a[68:71], a[144:147], v[36:39], a[68:71]\n\t" \
  "ds_read_b64_tr_b16 a[190:191], v191 offset:58368\n\t" \
  "v_fma_f32 v176, v176, v144, v152\n\t" \
  "v_fma_f32 v177, v177, v145, v153\n\t" \
  "v_mfma_f32_16x16x32_bf16 a[72:75], a[144:147], v[40:43], a[72:75]\n\t" \
  "v_fma_f32 v178, v178, v146, v154\n\t" \
  "v_fma_f32 v179, v179, v147, v155\n\t" \
  "v_fma_f32 v180, v180, v148, v156\n\t" \
  "v_mfma_f32_16x16x32_bf16 a[76:79], a[144:147], v[44:47], a[76:79]\n\t" \
  "v_fma_f32 v181, v181, v149, v157\n\t" \
  "v_fma_f32 v182, v182, v150, v158\n\t" \
  "v_fma_f32 v183, v183, v151, v159\n\t" \
  "v_mfma_f32_16x16x32_bf16 a[92:95], a[148:151], v[44:47], a[92:95]\n\t" \
  "v_cvt_pk_bf16_f32 v84, v176, v177\n\t" \
  "v_cvt_pk_bf16_f32 v85, v178, v179\n\t" \
  "v_cvt_pk_bf16_f32 v86, v180, v181\n\t" \
  "v_mfma_f32_16x16x32_bf16 a[88:91], a[148:151], v[40:43], a[88:91]\n\t" \
  "v_cvt_pk_bf16_f32 v87, v182, v183\n\t" \
  "v_lshlrev_b32 v176, 16, v88\n\t" \
  "v_and_b32 v177, s98, v88\n\t" \
  "v_mfma_f32_16x16x32_bf16 a[84:87], a[148:151], v[36:39], a[84:87]\n\t" \
  "v_lshlrev_b32 v178, 16, v89\n\t" \
  "v_and_b32 v179, s98, v89\n\t" \
  "v_lshlrev_b32 v180, 16, v90\n\t" \
  "v_mfma_f32_16x16x32_bf16 a[80:83], a[148:151], v[32:35], a[80:83]\n\t" \
  "v_and_b32 v181, s98, v90\n\t" \
  "v_lshlrev_b32 v182, 16, v91\n\t" \
  "v_and_b32 v183, s98, v91\n\t" \
  "v_mfma_f32_16x16x32_bf16 a[96:99], a[152:155], v[32:35], a[96:99]\n\t" \
  "v_fma_f32 v176, v176, v168, v170\n\t" \
  "v_fma_f32 v177, v177, v168, v170\n\t" \
  "v_fma_f32 v178, v178, v168, v170\n\t" \
  "v_mfma_f32_16x16x32_bf16 a[100:103], a[152:155], v[36:39], a[100:103]\n\t" \
  "v_fma_f32 v179, v179, v168, v170\n\t" \
  "v_fma_f32 v180, v180, v168, v170\n\t" \
  "v_fma_f32 v181, v181, v168, v170\n\t" \
  "v_mfma_f32_16x16x32_bf16 a[104:107], a[152:155], v[40:43], a[104:107]\n\t" \
  "v_fma_f32 v182, v182, v168, v170\n\t" \
  "v_fma_f32 v183, v183, v168, v170\n\t" \
  "v_mfma_f32_16x16x32_bf16 a[108:111], a[152:155], v[44:47], a[108:111]\n\t" \
  "v_fma_f32 v176, v176, v144, v152\n\t" \
  "v_fma_f32 v177, v177, v145, v153\n\t" \
  "v_mfma_f32_16x16x32_bf16 a[124:127], a[156:159], v[44:47], a[124:127]\n\t" \
  "v_fma_f32 v178, v178, v146, v154\n\t" \
  "v_fma_f32 v179, v179, v147, v155\n\t" \
  "v_mfma_f32_16x16x32_bf16 a[120:123], a[156:159], v[40:43], a[120:123]\n\t" \
  "v_fma_f32 v180, v180, v148, v156\n\t" \
  "v_fma_f32 v181, v181, v149, v157\n\t" \
  "v_mfma_f32_16x16x32_bf16 a[116:119], a[156:159], v[36:39], a[116:119]\n\t" \
  "v_fma_f32 v182, v182, v150, v158\n\t" \
  "v_fma_f32 v183, v183, v151, v159\n\t" \
  "v_mfma_f32_16x16x32_bf16 a[112:115], a[156:159], v[32:35], a[112:115]\n\t" \
  FSTP("v_writelane_b32 %[stv], s94, 40\n\t") \
  FSTP("v_writelane_b32 %[stv], s92, 41\n\t") \
  FSTP("s_memrealtime s[94:95]\n\t") \
  FSTP("s_memtime s[92:93]\n\t") \
  "s_waitcnt vmcnt(15) lgkmcnt(0)\n\t" \
  "s_barrier\n\t" \
  "v_mfma_f32_16x16x32_bf16 a[0:3], a[160:163], v[48:51], a[0:3]\n\t" \
  "buffer_load_dword %[fl], v210, %[ws], %[fo] offen sc1\n\t" \
  "v_cvt_pk_bf16_f32 v88, v176, v177\n\t" \
  "v_cvt_pk_bf16_f32 v89, v178, v179\n\t" \
  "v_mfma_f32_16x16x32_bf16 a[4:7], a[160:163], v[52:55], a[4:7]\n\t" \
  "s_add_u32 s96, %[rot19], 2097152\n\t" \
  "ds_read_b64_tr_b16 a[128:129], v192 offset:0\n\t" \
  "v_cvt_pk_bf16_f32 v90, v180, v181\n\t" \
  "v_cvt_pk_bf16_f32 v91, v182, v183\n\t" \
  "s_waitcnt vmcnt(8)\n\t" \
  "v_mfma_f32_16x16x32_bf16 a[8:11], a[160:163], v[56:59], a[8:11]\n\t" \
  "s_and_b32 s96, s96, 0x780000\n\t" \
  "ds_read_b64_tr_b16 a[130:131], v192 offset:1024\n\t" \
  "ds_read_b128 v[96:99], v204 offset:0\n\t" \
  "v_mfma_f32_16x16x32_bf16 a[12:15], a[160:163], v[60:63], a[12:15]\n\t" \
  "s_add_u32 m0, %[mw], 49152\n\t" \
  "ds_read_b64_tr_b16 a[132:133], v193 offset:0\n\t" \
  "ds_read_b128 v[100:103], v204 offset:2048\n\t" \
  "v_mfma_f32_16x16x32_bf16 a[28:31], a[164:167], v[60:63], a[28:31]\n\t" \
  "buffer_load_dwordx4 v200, %[sw], s96 offen offset:0 lds\n\t" \
  "v_mfma_f32_16x16x32_bf16 a[24:27], a[164:167], v[56:59], a[24:27]\n\t" \
  "ds_read_b64_tr_b16 a[134:135], v193 offset:1024\n\t" \
  "ds_read_b128 v[104:107], v204 offset:4096\n\t" \
  "v_mfma_f32_16x16x32_bf16 a[20:23], a[164:167], v[52:55], a[20:23]\n\t" \
  "ds_read_b64_tr_b16 a[136:137], v194 offset:0\n\t" \
  "ds_read_b128 v[108:111], v204 offset:6144\n\t" \
  "v_lshlrev_b32 v176, 16, v92\n\t" \
  "v_and_b32 v177, s98, v92\n\t" \
  "v_mfma_f32_16x16x32_bf16 a[16:19], a[164:167], v[48:51], a[16:19]\n\t" \
  "ds_read_b64_tr_b16 a[138:139], v194 offset:1024\n\t" \
  "v_lshlrev_b32 v178, 16, v93\n\t" \
  "v_and_b32 v179, s98, v93\n\t" \
  "v_lshlrev_b32 v180, 16, v94\n\t" \
  "v_mfma_f32_16x16x32_bf16 a[32:35], a[168:171], v[48:51], a[32:35]\n\t" \
  "ds_read_b64_tr_b16 a[140:141], v195 offset:0\n\t" \
  "v_and_b32 v181, s98, v94\n\t" \
  "v_lshlrev_b32 v182, 16, v95\n\t" \
  "v_and_b32 v183, s98, v95\n\t" \
  "v_mfma_f32_16x16x32_bf16 a[36:39], a[168:171], v[52:55], a[36:39]\n\t" \
  "buffer_load_dwordx4 v201, %[sw], s96 offen offset:1024 lds\n\t" \
  "v_fma_f32 v176, v176, v172, v174\n\t" \
  "v_fma_f32 v177, v177, v172, v174\n\t" \
  "v_mfma_f32_16x16x32_bf16 a[40:43], a[168:171], v[56:59], a[40:43]\n\t" \
  "ds_read_b64_tr_b16 a[142:143], v195 offset:1024\n\t" \
  "v_fma_f32 v178, v178, v172, v174\n\t" \
  "v_fma_f32 v179, v179, v172, v174\n\t" \
  "v_fma_f32 v180, v180, v172, v174\n\t" \
  "v_mfma_f32_16x16x32_bf16 a[44:47], a[168:171], v[60:63], a[44:47]\n\t" \
  "ds_read_b64_tr_b16 a[144:145], v196 offset:0\n\t" \
  "v_fma_f32 v181, v181, v172, v174\n\t" \
  "v_fma_f32 v182, v182, v172, v174\n\t" \
  "v_fma_f32 v183, v183, v172, v174\n\t" \
  "v_mfma_f32_16x16x32_bf16 a[60:63], a[172:175], v[60:63], a[60:63]\n\t" \
  "ds_read_b64_tr_b16 a[146:147], v196 offset:1024\n\t" \
  "v_fma_f32 v176, v176, v144, v152\n\t" \
  "v_fma_f32 v177, v177, v145, v153\n\t" \
  "v_fma_f32 v178, v178, v146, v154\n\t" \
  "v_mfma_f32_16x16x32_bf16 a[56:59], a[172:175], v[56:59], a[56:59]\n\t" \
  "ds_read_b64_tr_b16 a[148:149], v197 offset:0\n\t" \
  "v_fma_f32 v179, v179, v147, v155\n\t" \
  "v_fma_f32 v180, v180, v148, v156\n\t" \
  "v_fma_f32 v181, v181, v149, v157\n\t" \
  "v_mfma_f32_16x16x32_bf16 a[52:55], a[172:175], v[52:55], a[52:55]\n\t" \
  "buffer_load_dwordx4 v202, %[sw], s96 offen offset:2048 lds\n\t" \
  "v_fma_f32 v182, v182, v150, v158\n\t" \
  "v_mfma_f32_16x16x32_bf16 a[48:51], a[172:175], v[48:51], a[48:51]\n\t" \
  "ds_read_b64_tr_b16 a[150:151], v197 offset:1024\n\t" \
  "v_fma_f32 v183, v183, v151, v159\n\t" \
  "v_cvt_pk_bf16_f32 v92, v176, v177\n\t" \
  "v_mfma_f32_16x16x32_bf16 a[64:67], a[176:179], v[48:51], a[64:67]\n\t" \
  "ds_read_b64_tr_b16 a[152:153], v198 offset:0\n\t" \
  "v_cvt_pk_bf16_f32 v93, v178, v179\n\t" \
  "v_cvt_pk_bf16_f32 v94, v180, v181\n\t" \
  "v_mfma_f32_16x16x32_bf16 a[68:71], a[176:179], v[52:55], a[68:71]\n\t" \
  "ds_read_b64_tr_b16 a[154:155], v198 offset:1024\n\t" \
  "v_cvt_pk_bf16_f32 v95, v182, v183\n\t" \
  "v_mfma_f32_16x16x32_bf16 a[72:75], a[176:179], v[56:59], a[72:75]\n\t" \
  "buffer_load_dwordx4 v203, %[sw], s96 offen offset:3072 lds\n\t" \
  "v_mfma_f32_16x16x32_bf16 a[76:79], a[176:179], v[60:63], a[76:79]\n\t" \
  "ds_read_b64_tr_b16 a[156:157], v199 offset:0\n\t" \
  "ds_read_b128 v[144:147], v208 offset:1664\n\t" \
  "v_mfma_f32_16x16x32_bf16 a[92:95], a[180:183], v[60:63], a[92:95]\n\t" \
  "ds_read_b64_tr_b16 a[158:159], v199 offset:1024\n\t" \
  "ds_read_b128 v[148:151], v208 offset:1680\n\t" \
  "v_mfma_f32_16x16x32_bf16 a[88:91], a[180:183], v[56:59], a[88:91]\n\t" \
  "ds_read_b128 v[152:155], v208 offset:1920\n\t" \
  "ds_read_b128 v[156:159], v208 offset:1936\n\t" \
  "s_waitcnt lgkmcnt(15)\n\t" \
  "v_lshlrev_b32 v176, 16, v96\n\t" \
  "v_mfma_f32_16x16x32_bf16 a[84:87], a[180:183], v[52:55], a[84:87]\n\t" \
  "v_and_b32 v177, s98, v96\n\t" \
  "v_lshlrev_b32 v178, 16, v97\n\t" \
  "v_and_b32 v179, s98, v97\n\t" \
  "v_lshlrev_b32 v180, 16, v98\n\t" \
  "v_mfma_f32_16x16x32_bf16 a[80:83], a[180:183], v[48:51], a[80:83]\n\t" \
  "global_load_dword %[pf], v216, %[wq]\n\t" \
  "v_and_b32 v181, s98, v98\n\t" \
  "v_lshlrev_b32 v182, 16, v99\n\t" \
  "v_mfma_f32_16x16x32_bf16 a[96:99], a[184:187], v[48:51], a[96:99]\n\t" \
  "s_lshr_b32 s99, %[c17], 9\n\t" \
  "v_xor_b32 v248, s99, v214\n\t" \
  "v_xor_b32 v249, 256, v248\n\t" \
  "v_xor_b32 v250, 512, v248\n\t" \
  "v_xor_b32 v251, 768, v248\n\t" \
  "v_mfma_f32_16x16x32_bf16 a[100:103], a[184:187], v[52:55], a[100:103]\n\t" \
  "v_and_b32 v183, s98, v99\n\t" \
  "v_fma_f32 v176, v176, v160, v162\n\t" \
  "v_fma_f32 v177, v177, v160, v162\n\t" \
  "v_fma_f32 v178, v178, v160, v162\n\t" \
  "v_mfma_f32_16x16x32_bf16 a[104:107], a[184:187], v[56:59], a[104:107]\n\t" \
  "v_fma_f32 v179, v179, v160, v162\n\t" \
  "v_fma_f32 v180, v180, v160, v162\n\t" \
  "v_fma_f32 v181, v181, v160, v162\n\t" \
  "v_mfma_f32_16x16x32_bf16 a[108:111], a[184:187], v[60:63], a[108:111]\n\t" \
  "v_fma_f32 v182, v182, v160, v162\n\t" \
  "v_fma_f32 v183, v183, v160, v162\n\t" \
  "v_fma_f32 v176, v176, v128, v136\n\t" \
  "v_mfma_f32_16x16x32_bf16 a[124:127], a[188:191], v[60:63], a[124:127]\n\t" \
  "v_fma_f32 v177, v177, v129, v137\n\t" \
  "v_fma_f32 v178, v178, v130, v138\n\t" \
  "v_fma_f32 v179, v179, v131, v139\n\t" \
  "v_mfma_f32_16x16x32_bf16 a[120:123], a[188:191], v[56:59], a[120:123]\n\t" \
  "v_fma_f32 v180, v180, v132, v140\n\t" \
  "v_fma_f32 v181, v181, v133, v141\n\t" \
  "v_fma_f32 v182, v182, v134, v142\n\t" \
  "v_mfma_f32_16x16x32_bf16 a[116:119], a[188:191], v[52:55], a[116:119]\n\t" \
  "v_fma_f32 v183, v183, v135, v143\n\t" \
  "v_cvt_pk_bf16_f32 v96, v176, v177\n\t" \
  "v_cvt_pk_bf16_f32 v97, v178, v179\n\t" \
  "v_cvt_pk_bf16_f32 v98, v180, v181\n\t" \
  "v_mfma_f32_16x16x32_bf16 a[112:115], a[188:191], v[48:51], a[112:115]\n\t" \
  FSTP("v_writelane_b32 %[stv], s94, 42\n\t") \
  FSTP("v_writelane_b32 %[stv], s92, 43\n\t") \
  FSTP("s_memrealtime s[94:95]\n\t") \
  FSTP("s_memtime s[92:93]\n\t") \
  "s_waitcnt lgkmcnt(3)\n\t" \
  "v_mfma_f32_16x16x32_bf16 a[0:3], a[128:131], v[64:67], a[0:3]\n\t" \
  "s_waitcnt vmcnt(5)\n\t" \
  "v_cmp_ne_u32 vcc, v211, %[fl]\n\t" \
  "s_cbranch_vccnz .Lfq_s\n\t" \
  ".Lfq_k:\n\t" \
  "v_mfma_f32_16x16x32_bf16 a[4:7], a[128:131], v[68:71], a[4:7]\n\t" \
  "s_add_u32 s97, %[rot19], 2097152\n\t" \
  "ds_read_b64_tr_b16 a[160:161], v192 offset:8192\n\t" \
  "v_cvt_pk_bf16_f32 v99, v182, v183\n\t" \
  "v_lshlrev_b32 v176, 16, v100\n\t" \
  "v_and_b32 v177, s98, v100\n\t" \
  "v_mfma_f32_16x16x32_bf16 a[8:11], a[128:131], v[72:75], a[8:11]\n\t" \
  "s_bfe_u32 s97, s97, 0x140003\n\t" \
  "ds_read_b64_tr_b16 a[162:163], v192 offset:9216\n\t" \
  "v_lshlrev_b32 v178, 16, v101\n\t" \
  "v_and_b32 v179, s98, v101\n\t" \
  "v_lshlrev_b32 v180, 16, v102\n\t" \
  "v_mfma_f32_16x16x32_bf16 a[12:15], a[128:131], v[76:79], a[12:15]\n\t" \
  "s_add_u32 s97, s97, %[sox]\n\t" \
  "ds_read_b64_tr_b16 a[164:165], v193 offset:8192\n\t" \
  "v_and_b32 v181, s98, v102\n\t" \
  "v_lshlrev_b32 v182, 16, v103\n\t" \
  "v_and_b32 v183, s98, v103\n\t" \
  "v_mfma_f32_16x16x32_bf16 a[28:31], a[132:135], v[76:79], a[28:31]\n\t" \
  "buffer_load_dwordx4 a[192:195], v248, %[ws], s97 offen sc1\n\t" \
  "v_fma_f32 v176, v176, v164, v166\n\t" \
  "v_fma_f32 v177, v177, v164, v166\n\t" \
  "v_mfma_f32_16x16x32_bf16 a[24:27], a[132:135], v[72:75], a[24:27]\n\t" \
  "buffer_load_dwordx4 a[196:199], v249, %[ws], s97 offen sc1\n\t" \
  "v_fma_f32 v178, v178, v164, v166\n\t" \
  "v_fma_f32 v179, v179, v164, v166\n\t" \
  "v_mfma_f32_16x16x32_bf16 a[20:23], a[132:135], v[68:71], a[20:23]\n\t" \
  "ds_read_b64_tr_b16 a[166:167], v193 offset:9216\n\t" \
  "v_fma_f32 v180, v180, v164, v166\n\t" \
  "v_fma_f32 v181, v181, v164, v166\n\t" \
  "v_fma_f32 v182, v182, v164, v166\n\t" \
  "v_mfma_f32_16x16x32_bf16 a[16:19], a[132:135], v[64:67], a[16:19]\n\t" \
  "ds_read_b64_tr_b16 a[168:169], v194 offset:8192\n\t" \
  "v_fma_f32 v183, v183, v164, v166\n\t" \
  "v_fma_f32 v176, v176, v128, v136\n\t" \
  "v_fma_f32 v177, v177, v129, v137\n\t" \
  "v_mfma_f32_16x16x32_bf16 a[32:35], a[136:139], v[64:67], a[32:35]\n\t" \
  "buffer_load_dwordx4 a[200:203], v250, %[ws], s97 offen sc1\n\t" \
  "v_fma_f32 v178, v178, v130, v138\n\t" \
  ".Lhs1:\n\ts_cbranch_execz .Lhs1n\n\t" \
  "v_fma_f32 v179, v179, v131, v139\n\t" \
  "v_mfma_f32_16x16x32_bf16 a[36:39], a[136:139], v[68:71], a[36:39]\n\t" \
  "ds_read_b64_tr_b16 a[170:171], v194 offset:9216\n\t" \
  "v_fma_f32 v180, v180, v132, v140\n\t" \
  "v_fma_f32 v181, v181, v133, v141\n\t" \
  "v_mfma_f32_16x16x32_bf16 a[40:43], a[136:139], v[72:75], a[40:43]\n\t" \
  "ds_read_b64_tr_b16 a[172:173], v195 offset:8192\n\t" \
  "v_fma_f32 v182, v182, v134, v142\n\t" \
  "v_fma_f32 v183, v183, v135, v143\n\t" \
  "v_mfma_f32_16x16x32_bf16 a[44:47], a[136:139], v[76:79], a[44:47]\n\t" \
  "buffer_load_dwordx4 a[204:207], v251, %[ws], s97 offen sc1\n\t" \
  "v_cvt_pk_bf16_f32 v100, v176, v177\n\t" \
  "v_mfma_f32_16x16x32_bf16 a[60:63], a[140:143], v[76:79], a[60:63]\n\t" \
  "s_add_u32 s97, s97, %[dk]\n\t" \
  "ds_read_b64_tr_b16 a[174:175], v195 offset:9216\n\t" \
  "v_cvt_pk_bf16_f32 v101, v178, v179\n\t" \
  "v_cvt_pk_bf16_f32 v102, v180, v181\n\t" \
  "v_mfma_f32_16x16x32_bf16 a[56:59], a[140:143], v[72:75], a[56:59]\n\t" \
  "ds_read_b64_tr_b16 a[176:177], v196 offset:8192\n\t" \
  "v_cvt_pk_bf16_f32 v103, v182, v183\n\t" \
  "v_lshlrev_b32 v176, 16, v104\n\t" \
  "v_mfma_f32_16x16x32_bf16 a[52:55], a[140:143], v[68:71], a[52:55]\n\t" \
  "buffer_load_dwordx4 a[208:211], v248, %[ws], s97 offen sc1\n\t" \
  "v_and_b32 v177, s98, v104\n\t" \
  "v_mfma_f32_16x16x32_bf16 a[48:51], a[140:143], v[64:67], a[48:51]\n\t" \
  "ds_read_b64_tr_b16 a[178:179], v196 offset:9216\n\t" \
  "v_lshlrev_b32 v178, 16, v105\n\t" \
  "v_and_b32 v179, s98, v105\n\t" \
  "v_mfma_f32_16x16x32_bf16 a[64:67], a[144:147], v[64:67], a[64:67]\n\t" \
  "ds_read_b64_tr_b16 a[180:181], v197 offset:8192\n\t" \
  "v_lshlrev_b32 v180, 16, v106\n\t" \
  "v_and_b32 v181, s98, v106\n\t" \
  "v_mfma_f32_16x16x32_bf16 a[68:71], a[144:147], v[68:71], a[68:71]\n\t" \
  "buffer_load_dwordx4 a[212:215], v249, %[ws], s97 offen sc1\n\t" \
  "v_lshlrev_b32 v182, 16, v107\n\t" \
  "v_mfma_f32_16x16x32_bf16 a[72:75], a[144:147], v[72:75], a[72:75]\n\t" \
  "ds_read_b64_tr_b16 a[182:183], v197 offset:9216\n\t" \
  "v_and_b32 v183, s98, v107\n\t" \
  "v_fma_f32 v176, v176, v168, v170\n\t" \
  "v_mfma_f32_16x16x32_bf16 a[76:79], a[144:147], v[76:79], a[76:79]\n\t" \
  "ds_read_b64_tr_b16 a[184:185], v198 offset:8192\n\t" \
  "v_fma_f32 v177, v177, v168, v170\n\t" \
  "v_fma_f32 v178, v178, v168, v170\n\t" \
  "v_mfma_f32_16x16x32_bf16 a[92:95], a[148:151], v[76:79], a[92:95]\n\t" \
  "ds_read_b64_tr_b16 a[186:187], v198 offset:9216\n\t" \
  "v_fma_f32 v179, v179, v168, v170\n\t" \
  "v_fma_f32 v180, v180, v168, v170\n\t" \
  "v_mfma_f32_16x16x32_bf16 a[88:91], a[148:151], v[72:75], a[88:91]\n\t" \
  "buffer_load_dwordx4 a[216:219], v250, %[ws], s97 offen sc1\n\t" \
  "v_fma_f32 v181, v181, v168, v170\n\t" \
  "v_mfma_f32_16x16x32_bf16 a[84:87], a[148:151], v[68:71], a[84:87]\n\t" \
  "ds_read_b64_tr_b16 a[188:189], v199 offset:8192\n\t" \
  "v_fma_f32 v182, v182, v168, v170\n\t" \
  "v_fma_f32 v183, v183, v168, v170\n\t" \
  "v_mfma_f32_16x16x32_bf16 a[80:83], a[148:151], v[64:67], a[80:83]\n\t" \
  "ds_read_b64_tr_b16 a[190:191], v199 offset:9216\n\t" \
  "v_fma_f32 v176, v176, v128, v136\n\t" \
  "v_fma_f32 v177, v177, v129, v137\n\t" \
  "v_mfma_f32_16x16x32_bf16 a[96:99], a[152:155], v[64:67], a[96:99]\n\t" \
  "buffer_load_dwordx4 a[220:223], v251, %[ws], s97 offen sc1\n\t" \
  "v_fma_f32 v178, v178, v130, v138\n\t" \
  "v_mfma_f32_16x16x32_bf16 a[100:103], a[152:155], v[68:71], a[100:103]\n\t" \
  "v_fma_f32 v179, v179, v131, v139\n\t" \
  "v_fma_f32 v180, v180, v132, v140\n\t" \
  "v_fma_f32 v181, v181, v133, v141\n\t" \
  "v_mfma_f32_16x16x32_bf16 a[104:107], a[152:155], v[72:75], a[104:107]\n\t" \
  "v_fma_f32 v182, v182, v134, v142\n\t" \
  "v_fma_f32 v183, v183, v135, v143\n\t" \
  "v_cvt_pk_bf16_f32 v104, v176, v177\n\t" \
  "v_mfma_f32_16x16x32_bf16 a[108:111], a[152:155], v[76:79], a[108:111]\n\t" \
  "v_cvt_pk_bf16_f32 v105, v178, v179\n\t" \
  "v_cvt_pk_bf16_f32 v106, v180, v181\n\t" \
  "v_cvt_pk_bf16_f32 v107, v182, v183\n\t" \
  "v_mfma_f32_16x16x32_bf16 a[124:127], a[156:159], v[76:79], a[124:127]\n\t" \
  "ds_read_b128 v[112:115], v205 offset:0\n\t" \
  "ds_read_b128 v[116:119], v205 offset:2048\n\t" \
  "v_mfma_f32_16x16x32_bf16 a[120:123], a[156:159], v[72:75], a[120:123]\n\t" \
  "ds_read_b128 v[120:123], v205 offset:4096\n\t" \
  "ds_read_b128 v[124:127], v205 offset:6144\n\t" \
  "v_lshlrev_b32 v176, 16, v108\n\t" \
  "v_and_b32 v177, s98, v108\n\t" \
  "v_mfma_f32_16x16x32_bf16 a[116:119], a[156:159], v[68:71], a[116:119]\n\t" \
  "v_lshlrev_b32 v178, 16, v109\n\t" \
  "v_and_b32 v179, s98, v109\n\t" \
  "v_lshlrev_b32 v180, 16, v110\n\t" \
  "v_and_b32 v181, s98, v110\n\t" \
  "v_mfma_f32_16x16x32_bf16 a[112:115], a[156:159], v[64:67], a[112:115]\n\t" \
  FSTP("v_writelane_b32 %[stv], s94, 44\n\t") \
  FSTP("v_writelane_b32 %[stv], s92, 45\n\t") \
  FSTP("s_memrealtime s[94:95]\n\t") \
  FSTP("s_memtime s[92:93]\n\t") \
  "s_waitcnt lgkmcnt(4)\n\t" \
  "s_barrier\n\t" \
  "v_mfma_f32_16x16x32_bf16 a[0:3], a[160:163], v[80:83], a[0:3]\n\t" \
  "s_add_u32 s96, %[rot19], 2621440\n\t" \
  "ds_read_b64_tr_b16 a[128:129], v184 offset:0\n\t" \
  "v_lshlrev_b32 v182, 16, v111\n\t" \
  "v_and_b32 v183, s98, v111\n\t" \
  "v_fma_f32 v176, v176, v172, v174\n\t" \
  "v_mfma_f32_16x16x32_bf16 a[4:7], a[160:163], v[84:87], a[4:7]\n\t" \
  "s_and_b32 s96, s96, 0x780000\n\t" \
  "ds_read_b64_tr_b16 a[130:131], v184 offset:1024\n\t" \
  "v_fma_f32 v177, v177, v172, v174\n\t" \
  "v_fma_f32 v178, v178, v172, v174\n\t" \
  "v_fma_f32 v179, v179, v172, v174\n\t" \
  "v_mfma_f32_16x16x32_bf16 a[8:11], a[160:163], v[88:91], a[8:11]\n\t" \
  "s_add_u32 m0, %[mw], 98304\n\t" \
  "ds_read_b64_tr_b16 a[132:133], v185 offset:0\n\t" \
  "v_fma_f32 v180, v180, v172, v174\n\t" \
  "v_fma_f32 v181, v181, v172, v174\n\t" \
  "v_fma_f32 v182, v182, v172, v174\n\t" \
  "v_mfma_f32_16x16x32_bf16 a[12:15], a[160:163], v[92:95], a[12:15]\n\t" \
  "buffer_load_dwordx4 v200, %[sw], s96 offen offset:0 lds\n\t" \
  "v_fma_f32 v183, v183, v172, v174\n\t" \
  "v_fma_f32 v176, v176, v128, v136\n\t" \
  "v_mfma_f32_16x16x32_bf16 a[28:31], a[164:167], v[92:95], a[28:31]\n\t" \
  "buffer_load_dwordx4 v201, %[sw], s96 offen offset:1024 lds\n\t" \
  "v_fma_f32 v177, v177, v129, v137\n\t" \
  "v_fma_f32 v178, v178, v130, v138\n\t" \
  "v_mfma_f32_16x16x32_bf16 a[24:27], a[164:167], v[88:91], a[24:27]\n\t" \
  "ds_read_b64_tr_b16 a[134:135], v185 offset:1024\n\t" \
  "v_fma_f32 v179, v179, v131, v139\n\t" \
  "v_fma_f32 v180, v180, v132, v140\n\t" \
  "v_mfma_f32_16x16x32_bf16 a[20:23], a[164:167], v[84:87], a[20:23]\n\t" \
  "ds_read_b64_tr_b16 a[136:137], v186 offset:0\n\t" \
  "v_fma_f32 v181, v181, v133, v141\n\t" \
  "v_fma_f32 v182, v182, v134, v142\n\t" \
  "v_mfma_f32_16x16x32_bf16 a[16:19], a[164:167], v[80:83], a[16:19]\n\t" \
  "buffer_load_dwordx4 v202, %[sw], s96 offen offset:2048 lds\n\t" \
  "v_fma_f32 v183, v183, v135, v143\n\t" \
  "v_mfma_f32_16x16x32_bf16 a[32:35], a[168:171], v[80:83], a[32:35]\n\t" \
  "ds_read_b64_tr_b16 a[138:139], v186 offset:1024\n\t" \
  "v_cvt_pk_bf16_f32 v108, v176, v177\n\t" \
  "v_cvt_pk_bf16_f32 v109, v178, v179\n\t" \
  "v_mfma_f32_16x16x32_bf16 a[36:39], a[168:171], v[84:87], a[36:39]\n\t" \
  "ds_read_b64_tr_b16 a[140:141], v187 offset:0\n\t" \
  "v_cvt_pk_bf16_f32 v110, v180, v181\n\t" \
  "v_cvt_pk_bf16_f32 v111, v182, v183\n\t" \
  "s_waitcnt lgkmcnt(7)\n\t" \
  "v_mfma_f32_16x16x32_bf16 a[40:43], a[168:171], v[88:91], a[40:43]\n\t" \
  "buffer_load_dwordx4 v203, %[sw], s96 offen offset:3072 lds\n\t" \
  "v_lshlrev_b32 v176, 16, v112\n\t" \
  "v_mfma_f32_16x16x32_bf16 a[44:47], a[168:171], v[92:95], a[44:47]\n\t" \
  "s_add_u32 s97, %[rot19], 2621440\n\t" \
  "ds_read_b64_tr_b16 a[142:143], v187 offset:1024\n\t" \
  "v_and_b32 v177, s98, v112\n\t" \
  "v_lshlrev_b32 v178, 16, v113\n\t" \
  "v_mfma_f32_16x16x32_bf16 a[60:63], a[172:175], v[92:95], a[60:63]\n\t" \
  "s_bfe_u32 s97, s97, 0x140003\n\t" \
  "ds_read_b64_tr_b16 a[144:145], v188 offset:0\n\t" \
  "v_and_b32 v179, s98, v113\n\t" \
  "v_lshlrev_b32 v180, 16, v114\n\t" \
  "v_mfma_f32_16x16x32_bf16 a[56:59], a[172:175], v[88:91], a[56:59]\n\t" \
  "s_add_u32 s97, s97, %[sox]\n\t" \
  "ds_read_b64_tr_b16 a[146:147], v188 offset:1024\n\t" \
  "v_and_b32 v181, s98, v114\n\t" \
  "v_lshlrev_b32 v182, 16, v115\n\t" \
  "v_mfma_f32_16x16x32_bf16 a[52:55], a[172:175], v[84:87], a[52:55]\n\t" \
  "buffer_load_dwordx4 a[224:227], v248, %[ws], s97 offen sc1\n\t" \
  "v_and_b32 v183, s98, v115\n\t" \
  "v_mfma_f32_16x16x32_bf16 a[48:51], a[172:175], v[80:83], a[48:51]\n\t" \
  "ds_read_b64_tr_b16 a[148:149], v189 offset:0\n\t" \
  "v_fma_f32 v176, v176, v160, v162\n\t" \
  "v_fma_f32 v177, v177, v160, v162\n\t" \
  "v_mfma_f32_16x16x32_bf16 a[64:67], a[176:179], v[80:83], a[64:67]\n\t" \
  "ds_read_b64_tr_b16 a[150:151], v189 offset:1024\n\t" \
  "v_fma_f32 v178, v178, v160, v162\n\t" \
  "v_fma_f32 v179, v179, v160, v162\n\t" \
  "v_mfma_f32_16x16x32_bf16 a[68:71], a[176:179], v[84:87], a[68:71]\n\t" \
  "buffer_load_dwordx4 a[228:231], v249, %[ws], s97 offen sc1\n\t" \
  "v_fma_f32 v180, v180, v160, v162\n\t" \
  "v_mfma_f32_16x16x32_bf16 a[72:75], a[176:179], v[88:91], a[72:75]\n\t" \
  "ds_read_b64_tr_b16 a[152:153], v190 offset:0\n\t" \
  "v_fma_f32 v181, v181, v160, v162\n\t" \
  "v_fma_f32 v182, v182, v160, v162\n\t" \
  "v_mfma_f32_16x16x32_bf16 a[76:79], a[176:179], v[92:95], a[76:79]\n\t" \
  "ds_read_b64_tr_b16 a[154:155], v190 offset:1024\n\t" \
  "v_fma_f32 v183, v183, v160, v162\n\t" \
  "v_fma_f32 v176, v176, v144, v152\n\t" \
  "v_mfma_f32_16x16x32_bf16 a[92:95], a[180:183], v[92:95], a[92:95]\n\t" \
  "ds_read_b64_tr_b16 a[156:157], v191 offset:0\n\t" \
  "v_fma_f32 v177, v177, v145, v153\n\t" \
  "v_fma_f32 v178, v178, v146, v154\n\t" \
  "v_mfma_f32_16x16x32_bf16 a[88:91], a[180:183], v[88:91], a[88:91]\n\t" \
  "buffer_load_dwordx4 a[232:235], v250, %[ws], s97 offen sc1\n\t" \
  "v_fma_f32 v179, v179, v147, v155\n\t" \
  "v_mfma_f32_16x16x32_bf16 a[84:87], a[180:183], v[84:87], a[84:87]\n\t" \
  "ds_read_b64_tr_b16 a[158:159], v191 offset:1024\n\t" \
  "v_fma_f32 v180, v180, v148, v156\n\t" \
  "v_fma_f32 v181, v181, v149, v157\n\t" \
  "v_mfma_f32_16x16x32_bf16 a[80:83], a[180:183], v[80:83], a[80:83]\n\t" \
  "v_fma_f32 v182, v182, v150, v158\n\t" \
  "v_fma_f32 v183, v183, v151, v159\n\t" \
  "v_cvt_pk_bf16_f32 v112, v176, v177\n\t" \
  "v_mfma_f32_16x16x32_bf16 a[96:99], a[184:187], v[80:83], a[96:99]\n\t" \
  "buffer_load_dwordx4 a[236:239], v251, %[ws], s97 offen sc1\n\t" \
  "v_cvt_pk_bf16_f32 v113, v178, v179\n\t" \
  "v_mfma_f32_16x16x32_bf16 a[100:103], a[184:187], v[84:87], a[100:103]\n\t" \
  "v_cvt_pk_bf16_f32 v114, v180, v181\n\t" \
  "v_cvt_pk_bf16_f32 v115, v182, v183\n\t" \
  "v_lshlrev_b32 v176, 16, v116\n\t" \
  "v_mfma_f32_16x16x32_bf16 a[104:107], a[184:187], v[88:91], a[104:107]\n\t" \
  "v_and_b32 v177, s98, v116\n\t" \
  "v_lshlrev_b32 v178, 16, v117\n\t" \
  "v_and_b32 v179, s98, v117\n\t" \
  "v_mfma_f32_16x16x32_bf16 a[108:111], a[184:187], v[92:95], a[108:111]\n\t" \
  "v_lshlrev_b32 v180, 16, v118\n\t" \
  "v_and_b32 v181, s98, v118\n\t" \
  "v_lshlrev_b32 v182, 16, v119\n\t" \
  "v_mfma_f32_16x16x32_bf16 a[124:127], a[188:191], v[92:95], a[124:127]\n\t" \
  "v_and_b32 v183, s98, v119\n\t" \
  "v_fma_f32 v176, v176, v164, v166\n\t" \
  "v_fma_f32 v177, v177, v164, v166\n\t" \
  "v_mfma_f32_16x16x32_bf16 a[120:123], a[188:191], v[88:91], a[120:123]\n\t" \
  "v_fma_f32 v178, v178, v164, v166\n\t" \
  "v_fma_f32 v179, v179, v164, v166\n\t" \
  "v_fma_f32 v180, v180, v164, v166\n\t" \
  "v_mfma_f32_16x16x32_bf16 a[116:119], a[188:191], v[84:87], a[116:119]\n\t" \
  "v_fma_f32 v181, v181, v164, v166\n\t" \
  "v_fma_f32 v182, v182, v164, v166\n\t" \
  "v_fma_f32 v183, v183, v164, v166\n\t" \
  "v_fma_f32 v176, v176, v144, v152\n\t" \
  "v_mfma_f32_16x16x32_bf16 a[112:115], a[188:191], v[80:83], a[112:115]\n\t" \
  FSTP("v_writelane_b32 %[stv], s94, 46\n\t") \
  FSTP("v_writelane_b32 %[stv], s92, 47\n\t") \
  FSTP("s_memrealtime s[94:95]\n\t") \
  FSTP("s_memtime s[92:93]\n\t") \
  "s_waitcnt lgkmcnt(0)\n\t" \
  "v_mfma_f32_16x16x32_bf16 a[0:3], a[128:131], v[96:99], a[0:3]\n\t" \
  "s_add_u32 s96, %[rot19], 3145728\n\t" \
  "ds_read_b64_tr_b16 a[160:161], v184 offset:8192\n\t" \
  "v_fma_f32 v177, v177, v145, v153\n\t" \
  "v_fma_f32 v178, v178, v146, v154\n\t" \
  "v_fma_f32 v179, v179, v147, v155\n\t" \
  "v_mfma_f32_16x16x32_bf16 a[4:7], a[128:131], v[100:103], a[4:7]\n\t" \
  "s_and_b32 s96, s96, 0x780000\n\t" \
  "ds_read_b64_tr_b16 a[162:163], v184 offset:9216\n\t" \
  "v_fma_f32 v180, v180, v148, v156\n\t" \
  "v_fma_f32 v181, v181, v149, v157\n\t" \
  "v_fma_f32 v182, v182, v150, v158\n\t" \
  "v_mfma_f32_16x16x32_bf16 a[8:11], a[128:131], v[104:107], a[8:11]\n\t" \
  "s_add_u32 m0, %[mw], 16384\n\t" \
  "ds_read_b64_tr_b16 a[164:165], v185 offset:8192\n\t" \
  "v_fma_f32 v183, v183, v151, v159\n\t" \
  "v_cvt_pk_bf16_f32 v116, v176, v177\n\t" \
  "v_cvt_pk_bf16_f32 v117, v178, v179\n\t" \
  "v_mfma_f32_16x16x32_bf16 a[12:15], a[128:131], v[108:111], a[12:15]\n\t" \
  "buffer_load_dwordx4 v200, %[sw], s96 offen offset:0 lds\n\t" \
  "v_cvt_pk_bf16_f32 v118, v180, v181\n\t" \
  "v_mfma_f32_16x16x32_bf16 a[28:31], a[132:135], v[108:111], a[28:31]\n\t" \
  "ds_read_b64_tr_b16 a[166:167], v185 offset:9216\n\t" \
  "v_cvt_pk_bf16_f32 v119, v182, v183\n\t" \
  "v_lshlrev_b32 v176, 16, v120\n\t" \
  "v_mfma_f32_16x16x32_bf16 a[24:27], a[132:135], v[104:107], a[24:27]\n\t" \
  "ds_read_b64_tr_b16 a[168:169], v186 offset:8192\n\t" \
  "v_and_b32 v177, s98, v120\n\t" \
  "v_lshlrev_b32 v178, 16, v121\n\t" \
  "v_mfma_f32_16x16x32_bf16 a[20:23], a[132:135], v[100:103], a[20:23]\n\t" \
  "ds_read_b64_tr_b16 a[170:171], v186 offset:9216\n\t" \
  "v_and_b32 v179, s98, v121\n\t" \
  "v_lshlrev_b32 v180, 16, v122\n\t" \
  "v_mfma_f32_16x16x32_bf16 a[16:19], a[132:135], v[96:99], a[16:19]\n\t" \
  "buffer_load_dwordx4 v201, %[sw], s96 offen offset:1024 lds\n\t" \
  "v_and_b32 v181, s98, v122\n\t" \
  "v_mfma_f32_16x16x32_bf16 a[32:35], a[136:139], v[96:99], a[32:35]\n\t" \
  "ds_read_b64_tr_b16 a[172:173], v187 offset:8192\n\t" \
  "v_lshlrev_b32 v182, 16, v123\n\t" \
  "v_and_b32 v183, s98, v123\n\t" \
  "v_mfma_f32_16x16x32_bf16 a[36:39], a[136:139], v[100:103], a[36:39]\n\t" \
  "ds_read_b64_tr_b16 a[174:175], v187 offset:9216\n\t" \
  "v_fma_f32 v176, v176, v168, v170\n\t" \
  "v_fma_f32 v177, v177, v168, v170\n\t" \
  "v_mfma_f32_16x16x32_bf16 a[40:43], a[136:139], v[104:107], a[40:43]\n\t" \
  "ds_read_b64_tr_b16 a[176:177], v188 offset:8192\n\t" \
  "v_fma_f32 v178, v178, v168, v170\n\t" \
  "v_fma_f32 v179, v179, v168, v170\n\t" \
  "v_mfma_f32_16x16x32_bf16 a[44:47], a[136:139], v[108:111], a[44:47]\n\t" \
  "ds_read_b64_tr_b16 a[178:179], v188 offset:9216\n\t" \
  "v_fma_f32 v180, v180, v168, v170\n\t" \
  "v_fma_f32 v181, v181, v168, v170\n\t" \
  "v_mfma_f32_16x16x32_bf16 a[60:63], a[140:143], v[108:111], a[60:63]\n\t" \
  "ds_read_b64_tr_b16 a[180:181], v189 offset:8192\n\t" \
  "v_fma_f32 v182, v182, v168, v170\n\t" \
  "v_fma_f32 v183, v183, v168, v170\n\t" \
  "v_mfma_f32_16x16x32_bf16 a[56:59], a[140:143], v[104:107], a[56:59]\n\t" \
  "ds_read_b64_tr_b16 a[182:183], v189 offset:9216\n\t" \
  "v_fma_f32 v176, v176, v144, v152\n\t" \
  "v_fma_f32 v177, v177, v145, v153\n\t" \
  "v_mfma_f32_16x16x32_bf16 a[52:55], a[140:143], v[100:103], a[52:55]\n\t" \
  "buffer_load_dwordx4 v202, %[sw], s96 offen offset:2048 lds\n\t" \
  "v_fma_f32 v178, v178, v146, v154\n\t" \
  "v_mfma_f32_16x16x32_bf16 a[48:51], a[140:143], v[96:99], a[48:51]\n\t" \
  "ds_read_b64_tr_b16 a[184:185], v190 offset:8192\n\t" \
  "v_fma_f32 v179, v179, v147, v155\n\t" \
  "v_fma_f32 v180, v180, v148, v156\n\t" \
  "v_mfma_f32_16x16x32_bf16 a[64:67], a[144:147], v[96:99], a[64:67]\n\t" \
  "ds_read_b64_tr_b16 a[186:187], v190 offset:9216\n\t" \
  "v_fma_f32 v181, v181, v149, v157\n\t" \
  "v_fma_f32 v182, v182, v150, v158\n\t" \
  "v_mfma_f32_16x16x32_bf16 a[68:71], a[144:147], v[100:103], a[68:71]\n\t" \
  "ds_read_b64_tr_b16 a[188:189], v191 offset:8192\n\t" \
  "v_fma_f32 v183, v183, v151, v159\n\t" \
  "v_cvt_pk_bf16_f32 v120, v176, v177\n\t" \
  "v_mfma_f32_16x16x32_bf16 a[72:75], a[144:147], v[104:107], a[72:75]\n\t" \
  "ds_read_b64_tr_b16 a[190:191], v191 offset:9216\n\t" \
  "v_cvt_pk_bf16_f32 v121, v178, v179\n\t" \
  "v_cvt_pk_bf16_f32 v122, v180, v181\n\t" \
  "v_mfma_f32_16x16x32_bf16 a[76:79], a[144:147], v[108:111], a[76:79]\n\t" \
  "v_cvt_pk_bf16_f32 v123, v182, v183\n\t" \
  "v_lshlrev_b32 v176, 16, v124\n\t" \
  "v_and_b32 v177, s98, v124\n\t" \
  "v_mfma_f32_16x16x32_bf16 a[92:95], a[148:151], v[108:111], a[92:95]\n\t" \
  "v_lshlrev_b32 v178, 16, v125\n\t" \
  "v_and_b32 v179, s98, v125\n\t" \
  "v_lshlrev_b32 v180, 16, v126\n\t" \
  "v_mfma_f32_16x16x32_bf16 a[88:91], a[148:151], v[104:107], a[88:91]\n\t" \
  "buffer_load_dwordx4 v203, %[sw], s96 offen offset:3072 lds\n\t" \
  "v_and_b32 v181, s98, v126\n\t" \
  "v_mfma_f32_16x16x32_bf16 a[84:87], a[148:151], v[100:103], a[84:87]\n\t" \
  "v_lshlrev_b32 v182, 16, v127\n\t" \
  "v_and_b32 v183, s98, v127\n\t" \
  "v_fma_f32 v176, v176, v172, v174\n\t" \
  "v_mfma_f32_16x16x32_bf16 a[80:83], a[148:151], v[96:99], a[80:83]\n\t" \
  "v_fma_f32 v177, v177, v172, v174\n\t" \
  "v_fma_f32 v178, v178, v172, v174\n\t" \
  "v_fma_f32 v179, v179, v172, v174\n\t" \
  "v_mfma_f32_16x16x32_bf16 a[96:99], a[152:155], v[96:99], a[96:99]\n\t" \
  "v_mbcnt_lo_u32_b32 v217, -1, 0\n\t" \
  "v_mbcnt_hi_u32_b32 v217, -1, v217\n\t" \
  "v_cmp_gt_u32 vcc, 16, v217\n\t" \
  "v_mfma_f32_16x16x32_bf16 a[100:103], a[152:155], v[100:103], a[100:103]\n\t" \
  "v_lshlrev_b32 v217, 7, v217\n\t" \
  "v_bfrev_b32 v215, 1\n\t" \
  "v_cndmask_b32 v217, v215, v217, vcc\n\t" \
  "v_mfma_f32_16x16x32_bf16 a[104:107], a[152:155], v[104:107], a[104:107]\n\t" \
  "v_fma_f32 v180, v180, v172, v174\n\t" \
  "v_fma_f32 v181, v181, v172, v174\n\t" \
  "v_fma_f32 v182, v182, v172, v174\n\t" \
  "v_mfma_f32_16x16x32_bf16 a[108:111], a[152:155], v[108:111], a[108:111]\n\t" \
  "v_fma_f32 v183, v183, v172, v174\n\t" \
  "v_fma_f32 v176, v176, v144, v152\n\t" \
  "v_fma_f32 v177, v177, v145, v153\n\t" \
  "v_mfma_f32_16x16x32_bf16 a[124:127], a[156:159], v[108:111], a[124:127]\n\t" \
  "v_fma_f32 v178, v178, v146, v154\n\t" \
  "v_fma_f32 v179, v179, v147, v155\n\t" \
  "v_fma_f32 v180, v180, v148, v156\n\t" \
  "v_mfma_f32_16x16x32_bf16 a[120:123], a[156:159], v[104:107], a[120:123]\n\t" \
  "v_fma_f32 v181, v181, v149, v157\n\t" \
  "v_fma_f32 v182, v182, v150, v158\n\t" \
  "v_fma_f32 v183, v183, v151, v159\n\t" \
  "v_mfma_f32_16x16x32_bf16 a[116:119], a[156:159], v[100:103], a[116:119]\n\t" \
  "v_cvt_pk_bf16_f32 v124, v176, v177\n\t" \
  "v_cvt_pk_bf16_f32 v125, v178, v179\n\t" \
  "v_cvt_pk_bf16_f32 v126, v180, v181\n\t" \
  "v_cvt_pk_bf16_f32 v127, v182, v183\n\t" \
  "v_mfma_f32_16x16x32_bf16 a[112:115], a[156:159], v[96:99], a[112:115]\n\t" \
  FSTP("v_writelane_b32 %[stv], s94, 48\n\t") \
  FSTP("v_writelane_b32 %[stv], s92, 49\n\t") \
  FSTP("s_memrealtime s[94:95]\n\t") \
  FSTP("s_memtime s[92:93]\n\t") \
  "s_waitcnt vmcnt(21) lgkmcnt(0)\n\t" \
  "s_barrier\n\t" \
  "v_mfma_f32_16x16x32_bf16 a[0:3], a[160:163], v[112:115], a[0:3]\n\t" \
  "s_add_u32 s97, s97, %[dk]\n\t" \
  "ds_read_b64_tr_b16 a[128:129], v184 offset:49152\n\t" \
  "v_mfma_f32_16x16x32_bf16 a[4:7], a[160:163], v[116:119], a[4:7]\n\t" \
  "buffer_load_dwordx4 a[240:243], v248, %[ws], s97 offen sc1\n\t" \
  "v_mfma_f32_16x16x32_bf16 a[8:11], a[160:163], v[120:123], a[8:11]\n\t" \
  "ds_read_b64_tr_b16 a[130:131], v184 offset:50176\n\t" \
  "v_mfma_f32_16x16x32_bf16 a[12:15], a[160:163], v[124:127], a[12:15]\n\t" \
  "buffer_load_dwordx4 a[244:247], v249, %[ws], s97 offen sc1\n\t" \
  "v_mfma_f32_16x16x32_bf16 a[28:31], a[164:167], v[124:127], a[28:31]\n\t" \
  "ds_read_b64_tr_b16 a[132:133], v185 offset:49152\n\t" \
  "v_mfma_f32_16x16x32_bf16 a[24:27], a[164:167], v[120:123], a[24:27]\n\t" \
  "ds_read_b64_tr_b16 a[134:135], v185 offset:50176\n\t" \
  "v_mfma_f32_16x16x32_bf16 a[20:23], a[164:167], v[116:119], a[20:23]\n\t" \
  "buffer_load_dwordx4 a[248:251], v250, %[ws], s97 offen sc1\n\t" \
  "v_mfma_f32_16x16x32_bf16 a[16:19], a[164:167], v[112:115], a[16:19]\n\t" \
  "ds_read_b64_tr_b16 a[136:137], v186 offset:49152\n\t" \
  "v_mfma_f32_16x16x32_bf16 a[32:35], a[168:171], v[112:115], a[32:35]\n\t" \
  "ds_read_b64_tr_b16 a[138:139], v186 offset:50176\n\t" \
  "v_mfma_f32_16x16x32_bf16 a[36:39], a[168:171], v[116:119], a[36:39]\n\t" \
  "buffer_load_dwordx4 a[252:255], v251, %[ws], s97 offen sc1\n\t" \
  "v_mfma_f32_16x16x32_bf16 a[40:43], a[168:171], v[120:123], a[40:43]\n\t" \
  "s_add_u32 s96, %[rot19], 3670016\n\t" \
  "ds_read_b64_tr_b16 a[140:141], v187 offset:49152\n\t" \
  "v_mfma_f32_16x16x32_bf16 a[44:47], a[168:171], v[124:127], a[44:47]\n\t" \
  "s_and_b32 s96, s96, 0x780000\n\t" \
  "ds_read_b64_tr_b16 a[142:143], v187 offset:50176\n\t" \
  "v_mfma_f32_16x16x32_bf16 a[60:63], a[172:175], v[124:127], a[60:63]\n\t" \
  "s_add_u32 m0, %[mw], 32768\n\t" \
  "ds_read_b64_tr_b16 a[144:145], v188 offset:49152\n\t" \
  "v_mfma_f32_16x16x32_bf16 a[56:59], a[172:175], v[120:123], a[56:59]\n\t" \
  "buffer_load_dwordx4 v200, %[sw], s96 offen offset:0 lds\n\t" \
  "v_mfma_f32_16x16x32_bf16 a[52:55], a[172:175], v[116:119], a[52:55]\n\t" \
  "ds_read_b64_tr_b16 a[146:147], v188 offset:50176\n\t" \
  "v_mfma_f32_16x16x32_bf16 a[48:51], a[172:175], v[112:115], a[48:51]\n\t" \
  "ds_read_b64_tr_b16 a[148:149], v189 offset:49152\n\t" \
  "v_mfma_f32_16x16x32_bf16 a[64:67], a[176:179], v[112:115], a[64:67]\n\t" \
  "ds_read_b64_tr_b16 a[150:151], v189 offset:50176\n\t" \
  "v_mfma_f32_16x16x32_bf16 a[68:71], a[176:179], v[116:119], a[68:71]\n\t" \
  "buffer_load_dwordx4 v201, %[sw], s96 offen offset:1024 lds\n\t" \
  "v_mfma_f32_16x16x32_bf16 a[72:75], a[176:179], v[120:123], a[72:75]\n\t" \
  "ds_read_b64_tr_b16 a[152:153], v190 offset:49152\n\t" \
  "v_mfma_f32_16x16x32_bf16 a[76:79], a[176:179], v[124:127], a[76:79]\n\t" \
  "ds_read_b64_tr_b16 a[154:155], v190 offset:50176\n\t" \
  "v_mfma_f32_16x16x32_bf16 a[92:95], a[180:183], v[124:127], a[92:95]\n\t" \
  "ds_read_b64_tr_b16 a[156:157], v191 offset:49152\n\t" \
  "v_mfma_f32_16x16x32_bf16 a[88:91], a[180:183], v[120:123], a[88:91]\n\t" \
  "buffer_load_dwordx4 v202, %[sw], s96 offen offset:2048 lds\n\t" \
  "v_mfma_f32_16x16x32_bf16 a[84:87], a[180:183], v[116:119], a[84:87]\n\t" \
  "ds_read_b64_tr_b16 a[158:159], v191 offset:50176\n\t" \
  "v_mfma_f32_16x16x32_bf16 a[80:83], a[180:183], v[112:115], a[80:83]\n\t" \
  "v_mfma_f32_16x16x32_bf16 a[96:99], a[184:187], v[112:115], a[96:99]\n\t" \
  "v_mfma_f32_16x16x32_bf16 a[100:103], a[184:187], v[116:119], a[100:103]\n\t" \
  "buffer_load_dwordx4 v203, %[sw], s96 offen offset:3072 lds\n\t" \
  "v_mfma_f32_16x16x32_bf16 a[104:107], a[184:187], v[120:123], a[104:107]\n\t" \
  "s_add_u32 s99, %[pf1], 0\n\t" \
  "v_mfma_f32_16x16x32_bf16 a[108:111], a[184:187], v[124:127], a[108:111]\n\t" \
  "buffer_load_dword %[pf], v217, %[wo], s99 offen\n\t" \
  "v_mfma_f32_16x16x32_bf16 a[124:127], a[188:191], v[124:127], a[124:127]\n\t" \
  "v_mfma_f32_16x16x32_bf16 a[120:123], a[188:191], v[120:123], a[120:123]\n\t" \
  "v_mfma_f32_16x16x32_bf16 a[116:119], a[188:191], v[116:119], a[116:119]\n\t" \
  "v_mfma_f32_16x16x32_bf16 a[112:115], a[188:191], v[112:115], a[112:115]\n\t" \
  FSTP("v_writelane_b32 %[stv], s94, 50\n\t") \
  FSTP("v_writelane_b32 %[stv], s92, 51\n\t") \
  FSTP("s_memrealtime s[94:95]\n\t") \
  FSTP("s_memtime s[92:93]\n\t") \
  FSTS("s_memrealtime s[94:95]\n\t") \
  FSTS("s_waitcnt lgkmcnt(0)\n\t") \
  FSTS("v_writelane_b32 %[stv], s94, 14\n\t") \
  "s_waitcnt vmcnt(25) lgkmcnt(0)\n\t" \
  "v_mfma_f32_16x16x32_bf16 a[0:3], a[128:131], a[192:195], a[0:3]\n\t" \
  "s_add_u32 s96, %[rot19], 4194304\n\t" \
  "ds_read_b64_tr_b16 a[160:161], v184 offset:57344\n\t" \
  "v_mfma_f32_16x16x32_bf16 a[4:7], a[128:131], a[196:199], a[4:7]\n\t" \
  "s_and_b32 s96, s96, 0x780000\n\t" \
  "v_mfma_f32_16x16x32_bf16 a[8:11], a[128:131], a[200:203], a[8:11]\n\t" \
  "s_add_u32 m0, %[mw], 0\n\t" \
  "ds_read_b64_tr_b16 a[162:163], v184 offset:58368\n\t" \
  "v_mfma_f32_16x16x32_bf16 a[12:15], a[128:131], a[204:207], a[12:15]\n\t" \
  "buffer_load_dwordx4 v200, %[sw], s96 offen offset:0 lds\n\t" \
  "v_mfma_f32_16x16x32_bf16 a[28:31], a[132:135], a[204:207], a[28:31]\n\t" \
  "ds_read_b64_tr_b16 a[164:165], v185 offset:57344\n\t" \
  "v_mfma_f32_16x16x32_bf16 a[24:27], a[132:135], a[200:203], a[24:27]\n\t" \
  "ds_read_b64_tr_b16 a[166:167], v185 offset:58368\n\t" \
  "v_mfma_f32_16x16x32_bf16 a[20:23], a[132:135], a[196:199], a[20:23]\n\t" \
  "ds_read_b64_tr_b16 a[168:169], v186 offset:57344\n\t" \
  "v_mfma_f32_16x16x32_bf16 a[16:19], a[132:135], a[192:195], a[16:19]\n\t" \
  "buffer_load_dwordx4 v201, %[sw], s96 offen offset:1024 lds\n\t" \
  "v_mfma_f32_16x16x32_bf16 a[32:35], a[136:139], a[192:195], a[32:35]\n\t" \
  "ds_read_b64_tr_b16 a[170:171], v186 offset:58368\n\t" \
  "v_accvgpr_read_b32 v0, a0\n\t" \
  "v_accvgpr_read_b32 v1, a1\n\t" \
  "v_mfma_f32_16x16x32_bf16 a[36:39], a[136:139], a[196:199], a[36:39]\n\t" \
  "ds_read_b64_tr_b16 a[172:173], v187 offset:57344\n\t" \
  "v_accvgpr_read_b32 v2, a2\n\t" \
  "v_mfma_f32_16x16x32_bf16 a[40:43], a[136:139], a[200:203], a[40:43]\n\t" \
  "v_accvgpr_read_b32 v3, a3\n\t" \
  "v_accvgpr_read_b32 v32, a4\n\t" \
  "v_mfma_f32_16x16x32_bf16 a[44:47], a[136:139], a[204:207], a[44:47]\n\t" \
  "ds_read_b64_tr_b16 a[174:175], v187 offset:58368\n\t" \
  "v_accvgpr_read_b32 v33, a5\n\t" \
  "v_mfma_f32_16x16x32_bf16 a[60:63], a[140:143], a[204:207], a[60:63]\n\t" \
  "ds_read_b64_tr_b16 a[176:177], v188 offset:57344\n\t" \
  "v_accvgpr_read_b32 v34, a6\n\t" \
  "v_mfma_f32_16x16x32_bf16 a[56:59], a[140:143], a[200:203], a[56:59]\n\t" \
  "v_accvgpr_read_b32 v35, a7\n\t" \
  "v_accvgpr_read_b32 v64, a8\n\t" \
  "v_mfma_f32_16x16x32_bf16 a[52:55], a[140:143], a[196:199], a[52:55]\n\t" \
  "buffer_load_dwordx4 v202, %[sw], s96 offen offset:2048 lds\n\t" \
  "v_mfma_f32_16x16x32_bf16 a[48:51], a[140:143], a[192:195], a[48:51]\n\t" \
  "ds_read_b64_tr_b16 a[178:179], v188 offset:58368\n\t" \
  "v_accvgpr_read_b32 v65, a9\n\t" \
  "v_mfma_f32_16x16x32_bf16 a[64:67], a[144:147], a[192:195], a[64:67]\n\t" \
  "ds_read_b64_tr_b16 a[180:181], v189 offset:57344\n\t" \
  "v_accvgpr_read_b32 v66, a10\n\t" \
  "v_mfma_f32_16x16x32_bf16 a[68:71], a[144:147], a[196:199], a[68:71]\n\t" \
  "ds_read_b64_tr_b16 a[182:183], v189 offset:58368\n\t" \
  "v_accvgpr_read_b32 v67, a11\n\t" \
  "v_mfma_f32_16x16x32_bf16 a[72:75], a[144:147], a[200:203], a[72:75]\n\t" \
  "ds_read_b64_tr_b16 a[184:185], v190 offset:57344\n\t" \
  "v_accvgpr_read_b32 v96, a12\n\t" \
  "v_mfma_f32_16x16x32_bf16 a[76:79], a[144:147], a[204:207], a[76:79]\n\t" \
  "v_accvgpr_read_b32 v97, a13\n\t" \
  "v_accvgpr_read_b32 v98, a14\n\t" \
  "v_mfma_f32_16x16x32_bf16 a[92:95], a[148:151], a[204:207], a[92:95]\n\t" \
  "ds_read_b64_tr_b16 a[186:187], v190 offset:58368\n\t" \
  "v_accvgpr_read_b32 v99, a15\n\t" \
  "v_mfma_f32_16x16x32_bf16 a[88:91], a[148:151], a[200:203], a[88:91]\n\t" \
  "buffer_load_dwordx4 v203, %[sw], s96 offen offset:3072 lds\n\t" \
  "v_mfma_f32_16x16x32_bf16 a[84:87], a[148:151], a[196:199], a[84:87]\n\t" \
  "ds_read_b64_tr_b16 a[188:189], v191 offset:57344\n\t" \
  "v_accvgpr_read_b32 v100, a28\n\t" \
  "v_mfma_f32_16x16x32_bf16 a[80:83], a[148:151], a[192:195], a[80:83]\n\t" \
  "ds_read_b64_tr_b16 a[190:191], v191 offset:58368\n\t" \
  "v_accvgpr_read_b32 v101, a29\n\t" \
  "v_mfma_f32_16x16x32_bf16 a[96:99], a[152:155], a[192:195], a[96:99]\n\t" \
  "v_accvgpr_read_b32 v102, a30\n\t" \
  "v_accvgpr_read_b32 v103, a31\n\t" \
  "v_mfma_f32_16x16x32_bf16 a[100:103], a[152:155], a[196:199], a[100:103]\n\t" \
  "v_accvgpr_read_b32 v68, a24\n\t" \
  "v_accvgpr_read_b32 v69, a25\n\t" \
  "v_mfma_f32_16x16x32_bf16 a[104:107], a[152:155], a[200:203], a[104:107]\n\t" \
  "v_accvgpr_read_b32 v70, a26\n\t" \
  "v_accvgpr_read_b32 v71, a27\n\t" \
  "v_mfma_f32_16x16x32_bf16 a[108:111], a[152:155], a[204:207], a[108:111]\n\t" \
  "v_accvgpr_read_b32 v36, a20\n\t" \
  "v_accvgpr_read_b32 v37, a21\n\t" \
  "v_mfma_f32_16x16x32_bf16 a[124:127], a[156:159], a[204:207], a[124:127]\n\t" \
  "v_accvgpr_read_b32 v38, a22\n\t" \
  "v_accvgpr_read_b32 v39, a23\n\t" \
  "v_mfma_f32_16x16x32_bf16 a[120:123], a[156:159], a[200:203], a[120:123]\n\t" \
  "v_accvgpr_read_b32 v4, a16\n\t" \
  "v_accvgpr_read_b32 v5, a17\n\t" \
  "v_mfma_f32_16x16x32_bf16 a[116:119], a[156:159], a[196:199], a[116:119]\n\t" \
  "v_accvgpr_read_b32 v6, a18\n\t" \
  "v_accvgpr_read_b32 v7, a19\n\t" \
  "v_mfma_f32_16x16x32_bf16 a[112:115], a[156:159], a[192:195], a[112:115]\n\t" \
  "s_waitcnt vmcnt(21) lgkmcnt(0)\n\t" \
  "s_barrier\n\t" \
  "v_mfma_f32_16x16x32_bf16 a[32:35], a[168:171], a[208:211], a[32:35]\n\t" \
  "s_add_u32 s97, %[rot19], 3145728\n\t" \
  "ds_read_b64_tr_b16 a[128:129], v192 offset:0\n\t" \
  "v_mfma_f32_16x16x32_bf16 a[36:39], a[168:171], a[212:215], a[36:39]\n\t" \
  "s_bfe_u32 s97, s97, 0x140003\n\t" \
  "v_mfma_f32_16x16x32_bf16 a[40:43], a[168:171], a[216:219], a[40:43]\n\t" \
  "s_add_u32 s97, s97, %[sox]\n\t" \
  "ds_read_b64_tr_b16 a[130:131], v192 offset:1024\n\t" \
  "v_mfma_f32_16x16x32_bf16 a[44:47], a[168:171], a[220:223], a[44:47]\n\t" \
  "buffer_load_dwordx4 a[192:195], v248, %[ws], s97 offen sc1\n\t" \
  "v_mfma_f32_16x16x32_bf16 a[60:63], a[172:175], a[220:223], a[60:63]\n\t" \
  "buffer_load_dwordx4 a[196:199], v249, %[ws], s97 offen sc1\n\t" \
  "v_mfma_f32_16x16x32_bf16 a[56:59], a[172:175], a[216:219], a[56:59]\n\t" \
  "ds_read_b64_tr_b16 a[132:133], v193 offset:0\n\t" \
  "v_mfma_f32_16x16x32_bf16 a[52:55], a[172:175], a[212:215], a[52:55]\n\t" \
  "ds_read_b64_tr_b16 a[134:135], v193 offset:1024\n\t" \
  "v_mfma_f32_16x16x32_bf16 a[48:51], a[172:175], a[208:211], a[48:51]\n\t" \
  "buffer_load_dwordx4 a[200:203], v250, %[ws], s97 offen sc1\n\t" \
  "v_mfma_f32_16x16x32_bf16 a[64:67], a[176:179], a[208:211], a[64:67]\n\t" \
  "ds_read_b64_tr_b16 a[136:137], v194 offset:0\n\t" \
  "v_accvgpr_read_b32 v8, a32\n\t" \
  "v_accvgpr_read_b32 v9, a33\n\t" \
  "v_mfma_f32_16x16x32_bf16 a[68:71], a[176:179], a[212:215], a[68:71]\n\t" \
  "ds_read_b64_tr_b16 a[138:139], v194 offset:1024\n\t" \
  "v_accvgpr_read_b32 v10, a34\n\t" \
  "v_accvgpr_read_b32 v11, a35\n\t" \
  "v_mfma_f32_16x16x32_bf16 a[72:75], a[176:179], a[216:219], a[72:75]\n\t" \
  "ds_read_b64_tr_b16 a[140:141], v195 offset:0\n\t" \
  "v_accvgpr_read_b32 v40, a36\n\t" \
  "v_accvgpr_read_b32 v41, a37\n\t" \
  "v_mfma_f32_16x16x32_bf16 a[76:79], a[176:179], a[220:223], a[76:79]\n\t" \
  "buffer_load_dwordx4 a[204:207], v251, %[ws], s97 offen sc1\n\t" \
  "v_accvgpr_read_b32 v42, a38\n\t" \
  "v_mfma_f32_16x16x32_bf16 a[92:95], a[180:183], a[220:223], a[92:95]\n\t" \
  "s_add_u32 s96, %[rot19], 4718592\n\t" \
  "ds_read_b64_tr_b16 a[142:143], v195 offset:1024\n\t" \
  "v_accvgpr_read_b32 v43, a39\n\t" \
  "v_accvgpr_read_b32 v72, a40\n\t" \
  "v_mfma_f32_16x16x32_bf16 a[88:91], a[180:183], a[216:219], a[88:91]\n\t" \
  "s_and_b32 s96, s96, 0x780000\n\t" \
  "ds_read_b64_tr_b16 a[144:145], v196 offset:0\n\t" \
  "v_accvgpr_read_b32 v73, a41\n\t" \
  "v_accvgpr_read_b32 v74, a42\n\t" \
  ".Lhs2:\n\ts_cbranch_execz .Lhs2n\n\t" \
  "v_mfma_f32_16x16x32_bf16 a[84:87], a[180:183], a[212:215], a[84:87]\n\t" \
  "s_add_u32 m0, %[mw], 49152\n\t" \
  "ds_read_b64_tr_b16 a[146:147], v196 offset:1024\n\t" \
  "v_accvgpr_read_b32 v75, a43\n\t" \
  "v_accvgpr_read_b32 v104, a44\n\t" \
  "v_mfma_f32_16x16x32_bf16 a[80:83], a[180:183], a[208:211], a[80:83]\n\t" \
  "buffer_load_dwordx4 v200, %[sw], s96 offen offset:0 lds\n\t" \
  "v_accvgpr_read_b32 v105, a45\n\t" \
  "v_mfma_f32_16x16x32_bf16 a[96:99], a[184:187], a[208:211], a[96:99]\n\t" \
  "ds_read_b64_tr_b16 a[148:149], v197 offset:0\n\t" \
  "v_accvgpr_read_b32 v106, a46\n\t" \
  "v_mfma_f32_16x16x32_bf16 a[100:103], a[184:187], a[212:215], a[100:103]\n\t" \
  "ds_read_b64_tr_b16 a[150:151], v197 offset:1024\n\t" \
  "v_accvgpr_read_b32 v107, a47\n\t" \
  "v_mfma_f32_16x16x32_bf16 a[104:107], a[184:187], a[216:219], a[104:107]\n\t" \
  "ds_read_b64_tr_b16 a[152:153], v198 offset:0\n\t" \
  "v_accvgpr_read_b32 v108, a60\n\t" \
  "v_mfma_f32_16x16x32_bf16 a[108:111], a[184:187], a[220:223], a[108:111]\n\t" \
  "buffer_load_dwordx4 v201, %[sw], s96 offen offset:1024 lds\n\t" \
  "v_mfma_f32_16x16x32_bf16 a[124:127], a[188:191], a[220:223], a[124:127]\n\t" \
  "ds_read_b64_tr_b16 a[154:155], v198 offset:1024\n\t" \
  "v_accvgpr_read_b32 v109, a61\n\t" \
  "v_mfma_f32_16x16x32_bf16 a[120:123], a[188:191], a[216:219], a[120:123]\n\t" \
  "ds_read_b64_tr_b16 a[156:157], v199 offset:0\n\t" \
  "v_accvgpr_read_b32 v110, a62\n\t" \
  "v_mfma_f32_16x16x32_bf16 a[116:119], a[188:191], a[212:215], a[116:119]\n\t" \
  "v_accvgpr_read_b32 v111, a63\n\t" \
  "v_accvgpr_read_b32 v76, a56\n\t" \
  "v_mfma_f32_16x16x32_bf16 a[112:115], a[188:191], a[208:211], a[112:115]\n\t" \
  "buffer_load_dwordx4 v202, %[sw], s96 offen offset:2048 lds\n\t" \
  "v_mfma_f32_16x16x32_bf16 v[0:3], a[160:163], a[208:211], v[0:3]\n\t" \
  "ds_read_b64_tr_b16 a[158:159], v199 offset:1024\n\t" \
  "v_accvgpr_read_b32 v77, a57\n\t" \
  "v_mfma_f32_16x16x32_bf16 v[32:35], a[160:163], a[212:215], v[32:35]\n\t" \
  "v_accvgpr_read_b32 v78, a58\n\t" \
  "v_accvgpr_read_b32 v79, a59\n\t" \
  "v_mfma_f32_16x16x32_bf16 v[64:67], a[160:163], a[216:219], v[64:67]\n\t" \
  "v_accvgpr_read_b32 v44, a52\n\t" \
  "v_accvgpr_read_b32 v45, a53\n\t" \
  "v_mfma_f32_16x16x32_bf16 v[96:99], a[160:163], a[220:223], v[96:99]\n\t" \
  "buffer_load_dwordx4 v203, %[sw], s96 offen offset:3072 lds\n\t" \
  "v_mfma_f32_16x16x32_bf16 v[100:103], a[164:167], a[220:223], v[100:103]\n\t" \
  "v_accvgpr_read_b32 v46, a54\n\t" \
  "v_accvgpr_read_b32 v47, a55\n\t" \
  "v_mfma_f32_16x16x32_bf16 v[68:71], a[164:167], a[216:219], v[68:71]\n\t" \
  "v_accvgpr_read_b32 v12, a48\n\t" \
  "v_accvgpr_read_b32 v13, a49\n\t" \
  "v_mfma_f32_16x16x32_bf16 v[36:39], a[164:167], a[212:215], v[36:39]\n\t" \
  "v_accvgpr_read_b32 v14, a50\n\t" \
  "v_accvgpr_read_b32 v15, a51\n\t" \
  "v_mfma_f32_16x16x32_bf16 v[4:7], a[164:167], a[208:211], v[4:7]\n\t" \
  "s_waitcnt vmcnt(25) lgkmcnt(0)\n\t" \
  "v_mfma_f32_16x16x32_bf16 a[64:67], a[144:147], a[224:227], a[64:67]\n\t" \
  "ds_read_b64_tr_b16 a[160:161], v192 offset:8192\n\t" \
  "v_add_u32 v216, 3145728, v216\n\t" \
  "v_mfma_f32_16x16x32_bf16 a[68:71], a[144:147], a[228:231], a[68:71]\n\t" \
  "v_and_b32 v216, 0x7fffff, v216\n\t" \
  "v_mfma_f32_16x16x32_bf16 a[72:75], a[144:147], a[232:235], a[72:75]\n\t" \
  "global_load_dword %[pf], v216, %[wq]\n\t" \
  "v_mfma_f32_16x16x32_bf16 a[76:79], a[144:147], a[236:239], a[76:79]\n\t" \
  "s_add_u32 s97, s97, %[dk]\n\t" \
  "ds_read_b64_tr_b16 a[162:163], v192 offset:9216\n\t" \
  "v_mfma_f32_16x16x32_bf16 a[92:95], a[148:151], a[236:239], a[92:95]\n\t" \
  "ds_read_b64_tr_b16 a[164:165], v193 offset:8192\n\t" \
  "v_mfma_f32_16x16x32_bf16 a[88:91], a[148:151], a[232:235], a[88:91]\n\t" \
  "buffer_load_dwordx4 a[208:211], v248, %[ws], s97 offen sc1\n\t" \
  "v_mfma_f32_16x16x32_bf16 a[84:87], a[148:151], a[228:231], a[84:87]\n\t" \
  "ds_read_b64_tr_b16 a[166:167], v193 offset:9216\n\t" \
  "v_mfma_f32_16x16x32_bf16 a[80:83], a[148:151], a[224:227], a[80:83]\n\t" \
  "ds_read_b64_tr_b16 a[168:169], v194 offset:8192\n\t" \
  "v_mfma_f32_16x16x32_bf16 a[96:99], a[152:155], a[224:227], a[96:99]\n\t" \
  "ds_read_b64_tr_b16 a[170:171], v194 offset:9216\n\t" \
  "v_accvgpr_read_b32 v16, a64\n\t" \
  "v_accvgpr_read_b32 v17, a65\n\t" \
  "v_mfma_f32_16x16x32_bf16 a[100:103], a[152:155], a[228:231], a[100:103]\n\t" \
  "ds_read_b64_tr_b16 a[172:173], v195 offset:8192\n\t" \
  "v_accvgpr_read_b32 v18, a66\n\t" \
  "v_accvgpr_read_b32 v19, a67\n\t" \
  "v_mfma_f32_16x16x32_bf16 a[104:107], a[152:155], a[232:235], a[104:107]\n\t" \
  "v_accvgpr_read_b32 v48, a68\n\t" \
  "v_accvgpr_read_b32 v49, a69\n\t" \
  "v_accvgpr_read_b32 v50, a70\n\t" \
  "v_mfma_f32_16x16x32_bf16 a[108:111], a[152:155], a[236:239], a[108:111]\n\t" \
  "buffer_load_dwordx4 a[212:215], v249, %[ws], s97 offen sc1\n\t" \
  "v_mfma_f32_16x16x32_bf16 a[124:127], a[156:159], a[236:239], a[124:127]\n\t" \
  "ds_read_b64_tr_b16 a[174:175], v195 offset:9216\n\t" \
  "v_accvgpr_read_b32 v51, a71\n\t" \
  "v_mfma_f32_16x16x32_bf16 a[120:123], a[156:159], a[232:235], a[120:123]\n\t" \
  "ds_read_b64_tr_b16 a[176:177], v196 offset:8192\n\t" \
  "v_accvgpr_read_b32 v80, a72\n\t" \
  "v_mfma_f32_16x16x32_bf16 a[116:119], a[156:159], a[228:231], a[116:119]\n\t" \
  "ds_read_b64_tr_b16 a[178:179], v196 offset:9216\n\t" \
  "v_accvgpr_read_b32 v81, a73\n\t" \
  "v_mfma_f32_16x16x32_bf16 a[112:115], a[156:159], a[224:227], a[112:115]\n\t" \
  "ds_read_b64_tr_b16 a[180:181], v197 offset:8192\n\t" \
  "v_accvgpr_read_b32 v82, a74\n\t" \
  "v_mfma_f32_16x16x32_bf16 v[0:3], a[128:131], a[224:227], v[0:3]\n\t" \
  "buffer_load_dwordx4 a[216:219], v250, %[ws], s97 offen sc1\n\t" \
  "v_mfma_f32_16x16x32_bf16 v[32:35], a[128:131], a[228:231], v[32:35]\n\t" \
  "ds_read_b64_tr_b16 a[182:183], v197 offset:9216\n\t" \
  "v_accvgpr_read_b32 v83, a75\n\t" \
  "v_mfma_f32_16x16x32_bf16 v[64:67], a[128:131], a[232:235], v[64:67]\n\t" \
  "ds_read_b64_tr_b16 a[184:185], v198 offset:8192\n\t" \
  "v_accvgpr_read_b32 v112, a76\n\t" \
  "v_mfma_f32_16x16x32_bf16 v[96:99], a[128:131], a[236:239], v[96:99]\n\t" \
  "v_accvgpr_read_b32 v113, a77\n\t" \
  "v_accvgpr_read_b32 v114, a78\n\t" \
  "v_mfma_f32_16x16x32_bf16 v[100:103], a[132:135], a[236:239], v[100:103]\n\t" \
  "ds_read_b64_tr_b16 a[186:187], v198 offset:9216\n\t" \
  "v_accvgpr_read_b32 v115, a79\n\t" \
  "v_mfma_f32_16x16x32_bf16 v[68:71], a[132:135], a[232:235], v[68:71]\n\t" \
  "ds_read_b64_tr_b16 a[188:189], v199 offset:8192\n\t" \
  "v_accvgpr_read_b32 v116, a92\n\t" \
  "v_mfma_f32_16x16x32_bf16 v[36:39], a[132:135], a[228:231], v[36:39]\n\t" \
  "buffer_load_dwordx4 a[220:223], v251, %[ws], s97 offen sc1\n\t" \
  "v_mfma_f32_16x16x32_bf16 v[4:7], a[132:135], a[224:227], v[4:7]\n\t" \
  "ds_read_b64_tr_b16 a[190:191], v199 offset:9216\n\t" \
  "v_accvgpr_read_b32 v117, a93\n\t" \
  "v_mfma_f32_16x16x32_bf16 v[8:11], a[136:139], a[224:227], v[8:11]\n\t" \
  "v_accvgpr_read_b32 v118, a94\n\t" \
  "v_accvgpr_read_b32 v119, a95\n\t" \
  "v_mfma_f32_16x16x32_bf16 v[40:43], a[136:139], a[228:231], v[40:43]\n\t" \
  "v_accvgpr_read_b32 v84, a88\n\t" \
  "v_accvgpr_read_b32 v85, a89\n\t" \
  "v_mfma_f32_16x16x32_bf16 v[72:75], a[136:139], a[232:235], v[72:75]\n\t" \
  "v_accvgpr_read_b32 v86, a90\n\t" \
  "v_accvgpr_read_b32 v87, a91\n\t" \
  "v_mfma_f32_16x16x32_bf16 v[104:107], a[136:139], a[236:239], v[104:107]\n\t" \
  "v_accvgpr_read_b32 v52, a84\n\t" \
  "v_accvgpr_read_b32 v53, a85\n\t" \
  "v_mfma_f32_16x16x32_bf16 v[108:111], a[140:143], a[236:239], v[108:111]\n\t" \
  "v_accvgpr_read_b32 v54, a86\n\t" \
  "v_accvgpr_read_b32 v55, a87\n\t" \
  "v_mfma_f32_16x16x32_bf16 v[76:79], a[140:143], a[232:235], v[76:79]\n\t" \
  "v_accvgpr_read_b32 v20, a80\n\t" \
  "v_accvgpr_read_b32 v21, a81\n\t" \
  "v_mfma_f32_16x16x32_bf16 v[44:47], a[140:143], a[228:231], v[44:47]\n\t" \
  "v_accvgpr_read_b32 v22, a82\n\t" \
  "v_accvgpr_read_b32 v23, a83\n\t" \
  "v_mfma_f32_16x16x32_bf16 v[12:15], a[140:143], a[224:227], v[12:15]\n\t" \
  "s_waitcnt vmcnt(22) lgkmcnt(0)\n\t" \
  "s_barrier\n\t" \
  "v_mfma_f32_16x16x32_bf16 a[96:99], a[184:187], a[240:243], a[96:99]\n\t" \
  "s_add_u32 s97, %[rot19], 3670016\n\t" \
  "ds_read_b64_tr_b16 a[128:129], v184 offset:16384\n\t" \
  "v_mfma_f32_16x16x32_bf16 a[100:103], a[184:187], a[244:247], a[100:103]\n\t" \
  "s_bfe_u32 s97, s97, 0x140003\n\t" \
  "v_mfma_f32_16x16x32_bf16 a[104:107], a[184:187], a[248:251], a[104:107]\n\t" \
  "s_add_u32 s97, s97, %[sox]\n\t" \
  "ds_read_b64_tr_b16 a[130:131], v184 offset:17408\n\t" \
  "v_mfma_f32_16x16x32_bf16 a[108:111], a[184:187], a[252:255], a[108:111]\n\t" \
  "buffer_load_dwordx4 a[224:227], v248, %[ws], s97 offen sc1\n\t" \
  "v_mfma_f32_16x16x32_bf16 a[124:127], a[188:191], a[252:255], a[124:127]\n\t" \
  "buffer_load_dwordx4 a[228:231], v249, %[ws], s97 offen sc1\n\t" \
  "v_mfma_f32_16x16x32_bf16 a[120:123], a[188:191], a[248:251], a[120:123]\n\t" \
  "ds_read_b64_tr_b16 a[132:133], v185 offset:16384\n\t" \
  "v_mfma_f32_16x16x32_bf16 a[116:119], a[188:191], a[244:247], a[116:119]\n\t" \
  "ds_read_b64_tr_b16 a[134:135], v185 offset:17408\n\t" \
  "v_mfma_f32_16x16x32_bf16 a[112:115], a[188:191], a[240:243], a[112:115]\n\t" \
  "buffer_load_dwordx4 a[232:235], v250, %[ws], s97 offen sc1\n\t" \
  "v_mfma_f32_16x16x32_bf16 v[0:3], a[160:163], a[240:243], v[0:3]\n\t" \
  "ds_read_b64_tr_b16 a[136:137], v186 offset:16384\n\t" \
  "v_accvgpr_read_b32 v24, a96\n\t" \
  "v_accvgpr_read_b32 v25, a97\n\t" \
  "v_mfma_f32_16x16x32_bf16 v[32:35], a[160:163], a[244:247], v[32:35]\n\t" \
  "ds_read_b64_tr_b16 a[138:139], v186 offset:17408\n\t" \
  "v_accvgpr_read_b32 v26, a98\n\t" \
  "v_accvgpr_read_b32 v27, a99\n\t" \
  "v_mfma_f32_16x16x32_bf16 v[64:67], a[160:163], a[248:251], v[64:67]\n\t" \
  "ds_read_b64_tr_b16 a[140:141], v187 offset:16384\n\t" \
  "v_accvgpr_read_b32 v56, a100\n\t" \
  "v_accvgpr_read_b32 v57, a101\n\t" \
  "v_mfma_f32_16x16x32_bf16 v[96:99], a[160:163], a[252:255], v[96:99]\n\t" \
  "buffer_load_dwordx4 a[236:239], v251, %[ws], s97 offen sc1\n\t" \
  "v_accvgpr_read_b32 v58, a102\n\t" \
  "v_mfma_f32_16x16x32_bf16 v[100:103], a[164:167], a[252:255], v[100:103]\n\t" \
  "s_add_u32 s96, %[rot19], 5242880\n\t" \
  "ds_read_b64_tr_b16 a[142:143], v187 offset:17408\n\t" \
  "v_accvgpr_read_b32 v59, a103\n\t" \
  "v_accvgpr_read_b32 v88, a104\n\t" \
  "v_mfma_f32_16x16x32_bf16 v[68:71], a[164:167], a[248:251], v[68:71]\n\t" \
  "s_and_b32 s96, s96, 0x780000\n\t" \
  "ds_read_b64_tr_b16 a[144:145], v188 offset:16384\n\t" \
  "v_accvgpr_read_b32 v89, a105\n\t" \
  "v_accvgpr_read_b32 v90, a106\n\t" \
  "v_mfma_f32_16x16x32_bf16 v[36:39], a[164:167], a[244:247], v[36:39]\n\t" \
  "s_add_u32 m0, %[mw], 98304\n\t" \
  "ds_read_b64_tr_b16 a[146:147], v188 offset:17408\n\t" \
  "v_accvgpr_read_b32 v91, a107\n\t" \
  "v_accvgpr_read_b32 v120, a108\n\t" \
  "v_mfma_f32_16x16x32_bf16 v[4:7], a[164:167], a[240:243], v[4:7]\n\t" \
  "buffer_load_dwordx4 v200, %[sw], s96 offen offset:0 lds\n\t" \
  "v_accvgpr_read_b32 v121, a109\n\t" \
  "v_mfma_f32_16x16x32_bf16 v[8:11], a[168:171], a[240:243], v[8:11]\n\t" \
  "ds_read_b64_tr_b16 a[148:149], v189 offset:16384\n\t" \
  "v_accvgpr_read_b32 v122, a110\n\t" \
  "v_mfma_f32_16x16x32_bf16 v[40:43], a[168:171], a[244:247], v[40:43]\n\t" \
  "ds_read_b64_tr_b16 a[150:151], v189 offset:17408\n\t" \
  "v_accvgpr_read_b32 v123, a111\n\t" \
  "v_mfma_f32_16x16x32_bf16 v[72:75], a[168:171], a[248:251], v[72:75]\n\t" \
  "ds_read_b64_tr_b16 a[152:153], v190 offset:16384\n\t" \
  "v_accvgpr_read_b32 v124, a124\n\t" \
  "v_mfma_f32_16x16x32_bf16 v[104:107], a[168:171], a[252:255], v[104:107]\n\t" \
  "buffer_load_dwordx4 v201, %[sw], s96 offen offset:1024 lds\n\t" \
  "v_mfma_f32_16x16x32_bf16 v[108:111], a[172:175], a[252:255], v[108:111]\n\t" \
  "ds_read_b64_tr_b16 a[154:155], v190 offset:17408\n\t" \
  "v_accvgpr_read_b32 v125, a125\n\t" \
  "v_mfma_f32_16x16x32_bf16 v[76:79], a[172:175], a[248:251], v[76:79]\n\t" \
  "ds_read_b64_tr_b16 a[156:157], v191 offset:16384\n\t" \
  "v_accvgpr_read_b32 v126, a126\n\t" \
  "v_mfma_f32_16x16x32_bf16 v[44:47], a[172:175], a[244:247], v[44:47]\n\t" \
  "v_accvgpr_read_b32 v127, a127\n\t" \
  "v_accvgpr_read_b32 v92, a120\n\t" \
  "v_mfma_f32_16x16x32_bf16 v[12:15], a[172:175], a[240:243], v[12:15]\n\t" \
  "buffer_load_dwordx4 v202, %[sw], s96 offen offset:2048 lds\n\t" \
  "v_mfma_f32_16x16x32_bf16 v[16:19], a[176:179], a[240:243], v[16:19]\n\t" \
  "ds_read_b64_tr_b16 a[158:159], v191 offset:17408\n\t" \
  "v_accvgpr_read_b32 v93, a121\n\t" \
  "v_mfma_f32_16x16x32_bf16 v[48:51], a[176:179], a[244:247], v[48:51]\n\t" \
  "v_accvgpr_read_b32 v94, a122\n\t" \
  "v_accvgpr_read_b32 v95, a123\n\t" \
  "v_mfma_f32_16x16x32_bf16 v[80:83], a[176:179], a[248:251], v[80:83]\n\t" \
  "v_accvgpr_read_b32 v60, a116\n\t" \
  "v_accvgpr_read_b32 v61, a117\n\t" \
  "v_mfma_f32_16x16x32_bf16 v[112:115], a[176:179], a[252:255], v[112:115]\n\t" \
  "buffer_load_dwordx4 v203, %[sw], s96 offen offset:3072 lds\n\t" \
  "v_mfma_f32_16x16x32_bf16 v[116:119], a[180:183], a[252:255], v[116:119]\n\t" \
  "v_accvgpr_read_b32 v62, a118\n\t" \
  "v_accvgpr_read_b32 v63, a119\n\t" \
  "v_mfma_f32_16x16x32_bf16 v[84:87], a[180:183], a[248:251], v[84:87]\n\t" \
  "v_accvgpr_read_b32 v28, a112\n\t" \
  "v_accvgpr_read_b32 v29, a113\n\t" \
  "v_mfma_f32_16x16x32_bf16 v[52:55], a[180:183], a[244:247], v[52:55]\n\t" \
  "v_accvgpr_read_b32 v30, a114\n\t" \
  "v_accvgpr_read_b32 v31, a115\n\t" \
  "v_mfma_f32_16x16x32_bf16 v[20:23], a[180:183], a[240:243], v[20:23]\n\t" \
  "s_waitcnt vmcnt(17) lgkmcnt(0)\n\t" \
  "v_mfma_f32_16x16x32_bf16 v[0:3], a[128:131], a[192:195], v[0:3]\n\t" \
  "s_add_u32 s97, s97, %[dk]\n\t" \
  "ds_read_b64_tr_b16 a[160:161], v184 offset:24576\n\t" \
  "v_mfma_f32_16x16x32_bf16 v[32:35], a[128:131], a[196:199], v[32:35]\n\t" \
  "buffer_load_dwordx4 a[240:243], v248, %[ws], s97 offen sc1\n\t" \
  "v_mfma_f32_16x16x32_bf16 v[64:67], a[128:131], a[200:203], v[64:67]\n\t" \
  "ds_read_b64_tr_b16 a[162:163], v184 offset:25600\n\t" \
  "v_mfma_f32_16x16x32_bf16 v[96:99], a[128:131], a[204:207], v[96:99]\n\t" \
  "buffer_load_dwordx4 a[244:247], v249, %[ws], s97 offen sc1\n\t" \
  "v_mfma_f32_16x16x32_bf16 v[100:103], a[132:135], a[204:207], v[100:103]\n\t" \
  "ds_read_b64_tr_b16 a[164:165], v185 offset:24576\n\t" \
  "v_mfma_f32_16x16x32_bf16 v[68:71], a[132:135], a[200:203], v[68:71]\n\t" \
  "ds_read_b64_tr_b16 a[166:167], v185 offset:25600\n\t" \
  "v_mfma_f32_16x16x32_bf16 v[36:39], a[132:135], a[196:199], v[36:39]\n\t" \
  "ds_read_b64_tr_b16 a[168:169], v186 offset:24576\n\t" \
  "v_mfma_f32_16x16x32_bf16 v[4:7], a[132:135], a[192:195], v[4:7]\n\t" \
  "buffer_load_dwordx4 a[248:251], v250, %[ws], s97 offen sc1\n\t" \
  "v_mfma_f32_16x16x32_bf16 v[8:11], a[136:139], a[192:195], v[8:11]\n\t" \
  "ds_read_b64_tr_b16 a[170:171], v186 offset:25600\n\t" \
  "v_mfma_f32_16x16x32_bf16 v[40:43], a[136:139], a[196:199], v[40:43]\n\t" \
  "ds_read_b64_tr_b16 a[172:173], v187 offset:24576\n\t" \
  "v_mfma_f32_16x16x32_bf16 v[72:75], a[136:139], a[200:203], v[72:75]\n\t" \
  "v_mfma_f32_16x16x32_bf16 v[104:107], a[136:139], a[204:207], v[104:107]\n\t" \
  "buffer_load_dwordx4 a[252:255], v251, %[ws], s97 offen sc1\n\t" \
  "v_mfma_f32_16x16x32_bf16 v[108:111], a[140:143], a[204:207], v[108:111]\n\t" \
  "s_add_u32 s97, %[rot19], 4194304\n\t" \
  "ds_read_b64_tr_b16 a[174:175], v187 offset:25600\n\t" \
  "v_mfma_f32_16x16x32_bf16 v[76:79], a[140:143], a[200:203], v[76:79]\n\t" \
  "s_bfe_u32 s97, s97, 0x140003\n\t" \
  "ds_read_b64_tr_b16 a[176:177], v188 offset:24576\n\t" \
  "v_mfma_f32_16x16x32_bf16 v[44:47], a[140:143], a[196:199], v[44:47]\n\t" \
  "s_add_u32 s97, s97, %[sox]\n\t" \
  "ds_read_b64_tr_b16 a[178:179], v188 offset:25600\n\t" \
  "v_mfma_f32_16x16x32_bf16 v[12:15], a[140:143], a[192:195], v[12:15]\n\t" \
  "buffer_load_dwordx4 a[32:35], v248, %[ws], s97 offen sc1\n\t" \
  "v_mfma_f32_16x16x32_bf16 v[16:19], a[144:147], a[192:195], v[16:19]\n\t" \
  "ds_read_b64_tr_b16 a[180:181], v189 offset:24576\n\t" \
  "v_mfma_f32_16x16x32_bf16 v[48:51], a[144:147], a[196:199], v[48:51]\n\t" \
  "ds_read_b64_tr_b16 a[182:183], v189 offset:25600\n\t" \
  "v_mfma_f32_16x16x32_bf16 v[80:83], a[144:147], a[200:203], v[80:83]\n\t" \
  "ds_read_b64_tr_b16 a[184:185], v190 offset:24576\n\t" \
  "v_mfma_f32_16x16x32_bf16 v[112:115], a[144:147], a[204:207], v[112:115]\n\t" \
  "buffer_load_dwordx4 a[36:39], v249, %[ws], s97 offen sc1\n\t" \
  "v_mfma_f32_16x16x32_bf16 v[116:119], a[148:151], a[204:207], v[116:119]\n\t" \
  "ds_read_b64_tr_b16 a[186:187], v190 offset:25600\n\t" \
  "v_mfma_f32_16x16x32_bf16 v[84:87], a[148:151], a[200:203], v[84:87]\n\t" \
  "ds_read_b64_tr_b16 a[188:189], v191 offset:24576\n\t" \
  "v_mfma_f32_16x16x32_bf16 v[52:55], a[148:151], a[196:199], v[52:55]\n\t" \
  "v_mfma_f32_16x16x32_bf16 v[20:23], a[148:151], a[192:195], v[20:23]\n\t" \
  "buffer_load_dwordx4 a[40:43], v250, %[ws], s97 offen sc1\n\t" \
  "v_mfma_f32_16x16x32_bf16 v[24:27], a[152:155], a[192:195], v[24:27]\n\t" \
  "ds_read_b64_tr_b16 a[190:191], v191 offset:25600\n\t" \
  "v_mfma_f32_16x16x32_bf16 v[56:59], a[152:155], a[196:199], v[56:59]\n\t" \
  "v_mfma_f32_16x16x32_bf16 v[88:91], a[152:155], a[200:203], v[88:91]\n\t" \
  "v_mfma_f32_16x16x32_bf16 v[120:123], a[152:155], a[204:207], v[120:123]\n\t" \
  "buffer_load_dwordx4 a[44:47], v251, %[ws], s97 offen sc1\n\t" \
  "v_mfma_f32_16x16x32_bf16 v[124:127], a[156:159], a[204:207], v[124:127]\n\t" \
  "v_mfma_f32_16x16x32_bf16 v[92:95], a[156:159], a[200:203], v[92:95]\n\t" \
  "v_mfma_f32_16x16x32_bf16 v[60:63], a[156:159], a[196:199], v[60:63]\n\t" \
  "v_mfma_f32_16x16x32_bf16 v[28:31], a[156:159], a[192:195], v[28:31]\n\t" \
  "s_waitcnt vmcnt(16) lgkmcnt(0)\n\t" \
  "s_barrier\n\t" \
  "v_mfma_f32_16x16x32_bf16 v[0:3], a[160:163], a[208:211], v[0:3]\n\t" \
  "s_add_u32 s97, s97, %[dk]\n\t" \
  "ds_read_b64_tr_b16 a[128:129], v184 offset:32768\n\t" \
  "ds_read_b64_tr_b16 a[130:131], v184 offset:33792\n\t" \
  "v_mfma_f32_16x16x32_bf16 v[32:35], a[160:163], a[212:215], v[32:35]\n\t" \
  "buffer_load_dwordx4 a[48:51], v248, %[ws], s97 offen sc1\n\t" \
  "v_mfma_f32_16x16x32_bf16 v[64:67], a[160:163], a[216:219], v[64:67]\n\t" \
  "s_bfe_u32 %[hl], %[bid], 0x30003\n\t" \
  "ds_read_b64_tr_b16 a[132:133], v185 offset:32768\n\t" \
  "ds_read_b64_tr_b16 a[134:135], v185 offset:33792\n\t" \
  "v_mfma_f32_16x16x32_bf16 v[96:99], a[160:163], a[220:223], v[96:99]\n\t" \
  "buffer_load_dwordx4 a[52:55], v249, %[ws], s97 offen sc1\n\t" \
  "v_mfma_f32_16x16x32_bf16 v[100:103], a[164:167], a[220:223], v[100:103]\n\t" \
  "s_lshl_b32 %[hl18], %[hl], 18\n\t" \
  "ds_read_b64_tr_b16 a[136:137], v186 offset:32768\n\t" \
  "ds_read_b64_tr_b16 a[138:139], v186 offset:33792\n\t" \
  "v_mfma_f32_16x16x32_bf16 v[68:71], a[164:167], a[216:219], v[68:71]\n\t" \
  "s_lshr_b32 s98, %[bid], 3\n\t" \
  "ds_read_b64_tr_b16 a[140:141], v187 offset:32768\n\t" \
  "ds_read_b64_tr_b16 a[142:143], v187 offset:33792\n\t" \
  "v_mfma_f32_16x16x32_bf16 v[36:39], a[164:167], a[212:215], v[36:39]\n\t" \
  "s_lshl_b32 %[cto], %[wid], 5\n\t" \
  "ds_read_b64_tr_b16 a[144:145], v188 offset:32768\n\t" \
  "ds_read_b64_tr_b16 a[146:147], v188 offset:33792\n\t" \
  "v_mfma_f32_16x16x32_bf16 v[4:7], a[164:167], a[208:211], v[4:7]\n\t" \
  "buffer_load_dwordx4 a[56:59], v250, %[ws], s97 offen sc1\n\t" \
  "v_mfma_f32_16x16x32_bf16 v[8:11], a[168:171], a[208:211], v[8:11]\n\t" \
  "s_add_u32 %[cto], %[cto], s98\n\t" \
  "ds_read_b64_tr_b16 a[148:149], v189 offset:32768\n\t" \
  "ds_read_b64_tr_b16 a[150:151], v189 offset:33792\n\t" \
  "v_mfma_f32_16x16x32_bf16 v[40:43], a[168:171], a[212:215], v[40:43]\n\t" \
  "s_lshl_b32 %[cto], %[cto], 7\n\t" \
  "ds_read_b64_tr_b16 a[152:153], v190 offset:32768\n\t" \
  "ds_read_b64_tr_b16 a[154:155], v190 offset:33792\n\t" \
  "v_mfma_f32_16x16x32_bf16 v[72:75], a[168:171], a[216:219], v[72:75]\n\t" \
  "s_lshl2_add_u32 %[pf2], s98, %[wid]\n\t" \
  "ds_read_b64_tr_b16 a[156:157], v191 offset:32768\n\t" \
  "ds_read_b64_tr_b16 a[158:159], v191 offset:33792\n\t" \
  "v_mfma_f32_16x16x32_bf16 v[104:107], a[168:171], a[220:223], v[104:107]\n\t" \
  "buffer_load_dwordx4 a[60:63], v251, %[ws], s97 offen sc1\n\t" \
  "v_mfma_f32_16x16x32_bf16 v[108:111], a[172:175], a[220:223], v[108:111]\n\t" \
  "s_add_u32 s97, %[rot19], 4718592\n\t" \
  "ds_read_b64_tr_b16 a[64:65], v184 offset:40960\n\t" \
  "ds_read_b64_tr_b16 a[66:67], v184 offset:41984\n\t" \
  "v_mfma_f32_16x16x32_bf16 v[76:79], a[172:175], a[216:219], v[76:79]\n\t" \
  "s_bfe_u32 s97, s97, 0x140003\n\t" \
  "ds_read_b64_tr_b16 a[68:69], v185 offset:40960\n\t" \
  "ds_read_b64_tr_b16 a[70:71], v185 offset:41984\n\t" \
  "v_mfma_f32_16x16x32_bf16 v[44:47], a[172:175], a[212:215], v[44:47]\n\t" \
  "s_add_u32 s97, s97, %[sox]\n\t" \
  "v_mbcnt_lo_u32_b32 v233, -1, 0\n\t" \
  "v_mfma_f32_16x16x32_bf16 v[12:15], a[172:175], a[208:211], v[12:15]\n\t" \
  "buffer_load_dwordx4 a[96:99], v248, %[ws], s97 offen sc1\n\t" \
  "v_mfma_f32_16x16x32_bf16 v[16:19], a[176:179], a[208:211], v[16:19]\n\t" \
  "s_mul_i32 %[pf2], %[pf2], 98304\n\t" \
  "ds_read_b64_tr_b16 a[72:73], v186 offset:40960\n\t" \
  "ds_read_b64_tr_b16 a[74:75], v186 offset:41984\n\t" \
  "v_mfma_f32_16x16x32_bf16 v[48:51], a[176:179], a[212:215], v[48:51]\n\t" \
  "s_lshr_b32 s98, %[bid], 2\n\t" \
  "ds_read_b64_tr_b16 a[76:77], v187 offset:40960\n\t" \
  "ds_read_b64_tr_b16 a[78:79], v187 offset:41984\n\t" \
  "v_mfma_f32_16x16x32_bf16 v[80:83], a[176:179], a[216:219], v[80:83]\n\t" \
  "s_xor_b32 s98, s98, %[wid]\n\t" \
  "ds_read_b64_tr_b16 a[80:81], v188 offset:40960\n\t" \
  "v_mbcnt_hi_u32_b32 v233, -1, v233\n\t" \
  "v_mfma_f32_16x16x32_bf16 v[112:115], a[176:179], a[220:223], v[112:115]\n\t" \
  "buffer_load_dwordx4 a[100:103], v249, %[ws], s97 offen sc1\n\t" \
  "v_mfma_f32_16x16x32_bf16 v[116:119], a[180:183], a[220:223], v[116:119]\n\t" \
  "s_lshl_b32 s98, s98, 13\n\t" \
  "ds_read_b64_tr_b16 a[82:83], v188 offset:41984\n\t" \
  "ds_read_b64_tr_b16 a[84:85], v189 offset:40960\n\t" \
  "v_mfma_f32_16x16x32_bf16 v[84:87], a[180:183], a[216:219], v[84:87]\n\t" \
  "s_and_b32 %[po], s98, 0x2000\n\t" \
  "ds_read_b64_tr_b16 a[86:87], v189 offset:41984\n\t" \
  "v_lshrrev_b32 v129, 4, v233\n\t" \
  "v_mfma_f32_16x16x32_bf16 v[52:55], a[180:183], a[212:215], v[52:55]\n\t" \
  "s_lshr_b32 s98, %[bid], 6\n\t" \
  "ds_read_b64_tr_b16 a[88:89], v190 offset:40960\n\t" \
  "v_and_b32 v130, 15, v233\n\t" \
  "v_mfma_f32_16x16x32_bf16 v[20:23], a[180:183], a[208:211], v[20:23]\n\t" \
  "buffer_load_dwordx4 a[104:107], v250, %[ws], s97 offen sc1\n\t" \
  "v_mfma_f32_16x16x32_bf16 v[24:27], a[184:187], a[208:211], v[24:27]\n\t" \
  "s_lshl_b32 s98, s98, 22\n\t" \
  "ds_read_b64_tr_b16 a[90:91], v190 offset:41984\n\t" \
  "ds_read_b64_tr_b16 a[92:93], v191 offset:40960\n\t" \
  "v_mfma_f32_16x16x32_bf16 v[56:59], a[184:187], a[212:215], v[56:59]\n\t" \
  "s_add_u32 %[po], %[po], s98\n\t" \
  "ds_read_b64_tr_b16 a[94:95], v191 offset:41984\n\t" \
  "v_bfe_u32 v131, v233, 2, 2\n\t" \
  "v_mfma_f32_16x16x32_bf16 v[88:91], a[184:187], a[216:219], v[88:91]\n\t" \
  "s_lshl_b32 %[xcb], %[wid], 15\n\t" \
  "v_and_b32 v132, 3, v233\n\t" \
  "v_mfma_f32_16x16x32_bf16 v[120:123], a[184:187], a[220:223], v[120:123]\n\t" \
  "buffer_load_dwordx4 a[108:111], v251, %[ws], s97 offen sc1\n\t" \
  "v_mfma_f32_16x16x32_bf16 v[124:127], a[188:191], a[220:223], v[124:127]\n\t" \
  "s_add_u32 %[xcb], %[xcb], 0x1800000\n\t" \
  "v_cmp_lt_u32 vcc, 6, v233\n\t" \
  "v_mfma_f32_16x16x32_bf16 v[92:95], a[188:191], a[216:219], v[92:95]\n\t" \
  "s_add_u32 %[xcb], %[xcb], %[po]\n\t" \
  "v_mov_b32 v133, 6272\n\t" \
  "v_mfma_f32_16x16x32_bf16 v[60:63], a[188:191], a[212:215], v[60:63]\n\t" \
  "s_and_b32 s98, %[bid], 0x38\n\t" \
  "v_cndmask_b32 v133, 0, v133, vcc\n\t" \
  "v_mfma_f32_16x16x32_bf16 v[28:31], a[188:191], a[208:211], v[28:31]\n\t" \
  "s_waitcnt vmcnt(12) lgkmcnt(0)\n\t" \
  "s_barrier\n\t" \
  "v_mfma_f32_16x16x32_bf16 v[0:3], a[128:131], a[224:227], v[0:3]\n\t" \
  "s_add_u32 s97, s97, %[dk]\n\t" \
  "ds_read_b64_tr_b16 a[160:161], v184 offset:0\n\t" \
  "v_lshl_add_u32 v133, v233, 7, v133\n\t" \
  "v_mfma_f32_16x16x32_bf16 v[0:3], a[64:67], a[240:243], v[0:3]\n\t" \
  "buffer_load_dwordx4 a[112:115], v248, %[ws], s97 offen sc1\n\t" \
  "v_mfma_f32_16x16x32_bf16 v[32:35], a[64:67], a[244:247], v[32:35]\n\t" \
  "s_lshl_b32 s98, s98, 16\n\t" \
  "ds_read_b64_tr_b16 a[162:163], v184 offset:1024\n\t" \
  "v_cmp_gt_u32 vcc, 14, v233\n\t" \
  "v_mfma_f32_16x16x32_bf16 v[32:35], a[128:131], a[228:231], v[32:35]\n\t" \
  "buffer_load_dwordx4 a[116:119], v249, %[ws], s97 offen sc1\n\t" \
  "v_mfma_f32_16x16x32_bf16 v[64:67], a[128:131], a[232:235], v[64:67]\n\t" \
  "s_add_u32 %[xcb], %[xcb], s98\n\t" \
  "ds_read_b64_tr_b16 a[164:165], v185 offset:0\n\t" \
  "v_bfrev_b32 v247, 1\n\t" \
  "v_mfma_f32_16x16x32_bf16 v[64:67], a[64:67], a[248:251], v[64:67]\n\t" \
  "s_add_u32 %[otb], %[po], 0x800000\n\t" \
  "ds_read_b64_tr_b16 a[166:167], v185 offset:1024\n\t" \
  "v_cndmask_b32 v245, v247, v133, vcc\n\t" \
  "v_mfma_f32_16x16x32_bf16 v[96:99], a[64:67], a[252:255], v[96:99]\n\t" \
  "s_and_b32 s98, %[bid], 0x3\n\t" \
  "ds_read_b64_tr_b16 a[168:169], v186 offset:0\n\t" \
  "v_writelane_b32 v247, %[prb], 0\n\t" \
  "v_mfma_f32_16x16x32_bf16 v[96:99], a[128:131], a[236:239], v[96:99]\n\t" \
  "buffer_load_dwordx4 a[120:123], v250, %[ws], s97 offen sc1\n\t" \
  "v_mfma_f32_16x16x32_bf16 v[4:7], a[132:135], a[224:227], v[4:7]\n\t" \
  "s_lshl_b32 s98, s98, 20\n\t" \
  "ds_read_b64_tr_b16 a[170:171], v186 offset:1024\n\t" \
  "v_lshrrev_b32 v133, 1, v129\n\t" \
  "v_mfma_f32_16x16x32_bf16 v[4:7], a[68:71], a[240:243], v[4:7]\n\t" \
  "s_add_u32 %[otb], %[otb], s98\n\t" \
  "ds_read_b64_tr_b16 a[172:173], v187 offset:0\n\t" \
  "v_and_b32 v134, 1, v129\n\t" \
  "v_mfma_f32_16x16x32_bf16 v[36:39], a[68:71], a[244:247], v[36:39]\n\t" \
  "s_and_b32 s98, %[wid], 2\n\t" \
  "v_lshl_or_b32 v133, v134, 1, v133\n\t" \
  "v_mfma_f32_16x16x32_bf16 v[36:39], a[132:135], a[228:231], v[36:39]\n\t" \
  "buffer_load_dwordx4 a[124:127], v251, %[ws], s97 offen sc1\n\t" \
  "v_mfma_f32_16x16x32_bf16 v[68:71], a[132:135], a[232:235], v[68:71]\n\t" \
  "s_add_u32 s96, %[rot19], 5767168\n\t" \
  "ds_read_b64_tr_b16 a[174:175], v187 offset:1024\n\t" \
  "v_xor_b32 v133, v133, v130\n\t" \
  "v_mfma_f32_16x16x32_bf16 v[68:71], a[68:71], a[248:251], v[68:71]\n\t" \
  "s_and_b32 s96, s96, 0x780000\n\t" \
  "ds_read_b64_tr_b16 a[176:177], v188 offset:0\n\t" \
  "v_lshlrev_b32 v133, 4, v133\n\t" \
  "v_mfma_f32_16x16x32_bf16 v[100:103], a[68:71], a[252:255], v[100:103]\n\t" \
  "s_add_u32 m0, %[mw], 16384\n\t" \
  "ds_read_b64_tr_b16 a[178:179], v188 offset:1024\n\t" \
  "v_lshl_add_u32 v133, v130, 8, v133\n\t" \
  "v_mfma_f32_16x16x32_bf16 v[100:103], a[132:135], a[236:239], v[100:103]\n\t" \
  "buffer_load_dwordx4 v200, %[sw], s96 offen offset:0 lds\n\t" \
  "v_mfma_f32_16x16x32_bf16 v[8:11], a[136:139], a[224:227], v[8:11]\n\t" \
  "s_lshl_b32 s98, s98, 13\n\t" \
  "ds_read_b64_tr_b16 a[180:181], v189 offset:0\n\t" \
  "v_add_u32 v220, 49152, v133\n\t" \
  "v_mfma_f32_16x16x32_bf16 v[8:11], a[72:75], a[240:243], v[8:11]\n\t" \
  "s_add_u32 %[otb], %[otb], s98\n\t" \
  "ds_read_b64_tr_b16 a[182:183], v189 offset:1024\n\t" \
  "v_xor_b32 v221, 64, v220\n\t" \
  "v_mfma_f32_16x16x32_bf16 v[40:43], a[72:75], a[244:247], v[40:43]\n\t" \
  "s_and_b32 s98, %[wid], 1\n\t" \
  "ds_read_b64_tr_b16 a[184:185], v190 offset:0\n\t" \
  "v_xor_b32 v222, 128, v220\n\t" \
  "v_mfma_f32_16x16x32_bf16 v[40:43], a[136:139], a[228:231], v[40:43]\n\t" \
  "buffer_load_dwordx4 v201, %[sw], s96 offen offset:1024 lds\n\t" \
  "v_mfma_f32_16x16x32_bf16 v[72:75], a[136:139], a[232:235], v[72:75]\n\t" \
  "s_lshl_b32 s98, s98, 12\n\t" \
  "ds_read_b64_tr_b16 a[186:187], v190 offset:1024\n\t" \
  "v_xor_b32 v223, 192, v220\n\t" \
  "v_mfma_f32_16x16x32_bf16 v[72:75], a[72:75], a[248:251], v[72:75]\n\t" \
  "s_add_u32 %[otb], %[otb], s98\n\t" \
  "ds_read_b64_tr_b16 a[188:189], v191 offset:0\n\t" \
  "v_xor_b32 v133, v130, v129\n\t" \
  "v_mfma_f32_16x16x32_bf16 v[104:107], a[72:75], a[252:255], v[104:107]\n\t" \
  "s_lshr_b32 %[eso], %[c17], 2\n\t" \
  "v_lshlrev_b32 v133, 4, v133\n\t" \
  "v_mfma_f32_16x16x32_bf16 v[104:107], a[136:139], a[236:239], v[104:107]\n\t" \
  "buffer_load_dwordx4 v202, %[sw], s96 offen offset:2048 lds\n\t" \
  "v_mfma_f32_16x16x32_bf16 v[12:15], a[140:143], a[224:227], v[12:15]\n\t" \
  "s_add_u32 %[eso], %[eso], %[otb]\n\t" \
  "ds_read_b64_tr_b16 a[190:191], v191 offset:1024\n\t" \
  "v_lshl_add_u32 v224, v129, 8, v133\n\t" \
  "v_mfma_f32_16x16x32_bf16 v[12:15], a[76:79], a[240:243], v[12:15]\n\t" \
  "s_and_b32 s98, %[bid], 0x38\n\t" \
  "ds_read_b64_tr_b16 a[0:1], v184 offset:8192\n\t" \
  "v_xor_b32 v225, 64, v224\n\t" \
  "v_mfma_f32_16x16x32_bf16 v[44:47], a[76:79], a[244:247], v[44:47]\n\t" \
  "s_lshl_b32 s98, s98, 14\n\t" \
  "ds_read_b64_tr_b16 a[2:3], v184 offset:9216\n\t" \
  "v_xor_b32 v226, 128, v224\n\t" \
  "v_mfma_f32_16x16x32_bf16 v[44:47], a[140:143], a[228:231], v[44:47]\n\t" \
  "buffer_load_dwordx4 v203, %[sw], s96 offen offset:3072 lds\n\t" \
  "v_mfma_f32_16x16x32_bf16 v[76:79], a[140:143], a[232:235], v[76:79]\n\t" \
  "s_add_u32 s97, %[rot19], 5242880\n\t" \
  "ds_read_b64_tr_b16 a[4:5], v185 offset:8192\n\t" \
  "v_xor_b32 v227, 192, v224\n\t" \
  "v_mfma_f32_16x16x32_bf16 v[76:79], a[76:79], a[248:251], v[76:79]\n\t" \
  "s_bfe_u32 s97, s97, 0x140003\n\t" \
  "ds_read_b64_tr_b16 a[6:7], v185 offset:9216\n\t" \
  "v_lshlrev_b32 v133, 3, v132\n\t" \
  "v_mfma_f32_16x16x32_bf16 v[108:111], a[76:79], a[252:255], v[108:111]\n\t" \
  "s_add_u32 s97, s97, %[sox]\n\t" \
  "ds_read_b64_tr_b16 a[8:9], v186 offset:8192\n\t" \
  "v_lshl_add_u32 v133, v129, 10, v133\n\t" \
  "v_mfma_f32_16x16x32_bf16 v[108:111], a[140:143], a[236:239], v[108:111]\n\t" \
  "buffer_load_dwordx4 a[192:195], v248, %[ws], s97 offen sc1\n\t" \
  "v_mfma_f32_16x16x32_bf16 v[16:19], a[144:147], a[224:227], v[16:19]\n\t" \
  "s_add_u32 %[eso], %[eso], s98\n\t" \
  "ds_read_b64_tr_b16 a[10:11], v186 offset:9216\n\t" \
  "v_lshl_add_u32 v228, v131, 6, v133\n\t" \
  "v_mfma_f32_16x16x32_bf16 v[16:19], a[80:83], a[240:243], v[16:19]\n\t" \
  "s_lshr_b32 s98, %[bid], 1\n\t" \
  "ds_read_b64_tr_b16 a[12:13], v187 offset:8192\n\t" \
  "v_lshl_add_u32 v228, v134, 5, v228\n\t" \
  "v_mfma_f32_16x16x32_bf16 v[48:51], a[80:83], a[244:247], v[48:51]\n\t" \
  "s_xor_b32 s98, s98, %[wid]\n\t" \
  "v_add_u32 v228, 81920, v228\n\t" \
  "v_mfma_f32_16x16x32_bf16 v[48:51], a[144:147], a[228:231], v[48:51]\n\t" \
  "buffer_load_dwordx4 a[196:199], v249, %[ws], s97 offen sc1\n\t" \
  "v_mfma_f32_16x16x32_bf16 v[80:83], a[144:147], a[232:235], v[80:83]\n\t" \
  "s_lshl_b32 s98, s98, 13\n\t" \
  "ds_read_b64_tr_b16 a[14:15], v187 offset:9216\n\t" \
  "v_xor_b32 v229, 32, v228\n\t" \
  "v_mfma_f32_16x16x32_bf16 v[80:83], a[80:83], a[248:251], v[80:83]\n\t" \
  "s_and_b32 %[po], s98, 0x2000\n\t" \
  "ds_read_b64_tr_b16 a[16:17], v188 offset:8192\n\t" \
  "v_lshl_add_u32 v244, v131, 8, v133\n\t" \
  "v_mfma_f32_16x16x32_bf16 v[112:115], a[80:83], a[252:255], v[112:115]\n\t" \
  "s_lshr_b32 s98, %[bid], 5\n\t" \
  "ds_read_b64_tr_b16 a[18:19], v188 offset:9216\n\t" \
  "v_lshl_add_u32 v244, v131, 5, v244\n\t" \
  "v_mfma_f32_16x16x32_bf16 v[112:115], a[144:147], a[236:239], v[112:115]\n\t" \
  "buffer_load_dwordx4 a[200:203], v250, %[ws], s97 offen sc1\n\t" \
  "v_mfma_f32_16x16x32_bf16 v[20:23], a[148:151], a[224:227], v[20:23]\n\t" \
  "s_xor_b32 s98, s98, %[wid]\n\t" \
  "ds_read_b64_tr_b16 a[20:21], v189 offset:8192\n\t" \
  "v_lshl_add_u32 v244, v134, 7, v244\n\t" \
  "v_mfma_f32_16x16x32_bf16 v[20:23], a[84:87], a[240:243], v[20:23]\n\t" \
  "s_lshl_b32 s98, s98, 13\n\t" \
  "ds_read_b64_tr_b16 a[22:23], v189 offset:9216\n\t" \
  ".Lhs3:\n\ts_cbranch_execz .Lhs3n\n\t" \
  "v_lshlrev_b32 v133, 4, v132\n\t" \
  "v_mfma_f32_16x16x32_bf16 v[52:55], a[84:87], a[244:247], v[52:55]\n\t" \
  "s_and_b32 %[px], s98, 0x2000\n\t" \
  "ds_read_b64_tr_b16 a[24:25], v190 offset:8192\n\t" \
  "v_lshl_add_u32 v133, v129, 6, v133\n\t" \
  "v_mfma_f32_16x16x32_bf16 v[52:55], a[148:151], a[228:231], v[52:55]\n\t" \
  "buffer_load_dwordx4 a[204:207], v251, %[ws], s97 offen sc1\n\t" \
  "v_mfma_f32_16x16x32_bf16 v[84:87], a[148:151], a[232:235], v[84:87]\n\t" \
  "s_add_u32 s97, s97, %[dk]\n\t" \
  "ds_read_b64_tr_b16 a[26:27], v190 offset:9216\n\t" \
  "v_lshl_add_u32 v230, v131, 8, v133\n\t" \
  "v_mfma_f32_16x16x32_bf16 v[84:87], a[84:87], a[248:251], v[84:87]\n\t" \
  "s_lshl_b32 %[frb], %[wid], 2\n\t" \
  "ds_read_b64_tr_b16 a[28:29], v191 offset:8192\n\t" \
  "v_xor_b32 v231, 32, v230\n\t" \
  "v_mfma_f32_16x16x32_bf16 v[116:119], a[84:87], a[252:255], v[116:119]\n\t" \
  "s_add_u32 %[frb], %[frb], 0x8000\n\t" \
  "v_lshlrev_b32 v133, 1, v129\n\t" \
  "v_mfma_f32_16x16x32_bf16 v[116:119], a[148:151], a[236:239], v[116:119]\n\t" \
  "buffer_load_dwordx4 a[208:211], v248, %[ws], s97 offen sc1\n\t" \
  "v_mfma_f32_16x16x32_bf16 v[24:27], a[152:155], a[224:227], v[24:27]\n\t" \
  "s_and_b32 s98, %[bid], 0x80\n\t" \
  "ds_read_b64_tr_b16 a[30:31], v191 offset:9216\n\t" \
  "v_xor_b32 v133, v133, v130\n\t" \
  "v_mfma_f32_16x16x32_bf16 v[24:27], a[88:91], a[240:243], v[24:27]\n\t" \
  "s_lshl_b32 s98, s98, 7\n\t" \
  "v_lshlrev_b32 v133, 4, v133\n\t" \
  "v_mfma_f32_16x16x32_bf16 v[56:59], a[88:91], a[244:247], v[56:59]\n\t" \
  "s_add_u32 %[frb], %[frb], s98\n\t" \
  "v_lshl_add_u32 v240, v129, 11, v133\n\t" \
  "v_mfma_f32_16x16x32_bf16 v[56:59], a[152:155], a[228:231], v[56:59]\n\t" \
  "buffer_load_dwordx4 a[212:215], v249, %[ws], s97 offen sc1\n\t" \
  "v_mfma_f32_16x16x32_bf16 v[88:91], a[152:155], a[232:235], v[88:91]\n\t" \
  "s_and_b32 s98, %[bid], 0x4\n\t" \
  "v_xor_b32 v241, 0x80, v240\n\t" \
  "v_mfma_f32_16x16x32_bf16 v[88:91], a[88:91], a[248:251], v[88:91]\n\t" \
  "s_lshl_b32 s98, s98, 11\n\t" \
  "v_lshrrev_b32 v133, 1, v233\n\t" \
  "v_mfma_f32_16x16x32_bf16 v[120:123], a[88:91], a[252:255], v[120:123]\n\t" \
  "s_add_u32 %[frb], %[frb], s98\n\t" \
  "v_mul_u32_u24 v133, 57344, v133\n\t" \
  "v_mfma_f32_16x16x32_bf16 v[120:123], a[152:155], a[236:239], v[120:123]\n\t" \
  "buffer_load_dwordx4 a[216:219], v250, %[ws], s97 offen sc1\n\t" \
  "v_mfma_f32_16x16x32_bf16 v[28:31], a[156:159], a[224:227], v[28:31]\n\t" \
  "s_and_b32 s98, %[bid], 0x78\n\t" \
  "v_and_b32 v134, 1, v233\n\t" \
  "v_mfma_f32_16x16x32_bf16 v[28:31], a[92:95], a[240:243], v[28:31]\n\t" \
  "s_lshl_b32 s98, s98, 6\n\t" \
  "v_lshl_add_u32 v133, v134, 4, v133\n\t" \
  "v_mfma_f32_16x16x32_bf16 v[60:63], a[92:95], a[244:247], v[60:63]\n\t" \
  "s_add_u32 %[frb], %[frb], s98\n\t" \
  "v_or_b32 v232, %[idm], v133\n\t" \
  "v_mfma_f32_16x16x32_bf16 v[60:63], a[156:159], a[228:231], v[60:63]\n\t" \
  "buffer_load_dwordx4 a[220:223], v251, %[ws], s97 offen sc1\n\t" \
  "v_mfma_f32_16x16x32_bf16 v[92:95], a[156:159], a[232:235], v[92:95]\n\t" \
  "s_lshl_b32 %[fw2], %[wid], 7\n\t" \
  "v_mfma_f32_16x16x32_bf16 v[92:95], a[92:95], a[248:251], v[92:95]\n\t" \
  "s_add_u32 %[fw2], %[fw2], 0x20000\n\t" \
  "v_mfma_f32_16x16x32_bf16 v[124:127], a[92:95], a[252:255], v[124:127]\n\t" \
  "s_and_b32 s98, %[bid], 0xc0\n\t" \
  "v_mfma_f32_16x16x32_bf16 v[124:127], a[156:159], a[236:239], v[124:127]\n\t" \
  "s_waitcnt vmcnt(20) lgkmcnt(0)\n\t" \
  "s_barrier\n\t" \
  "v_mfma_f32_16x16x32_bf16 v[0:3], a[160:163], a[32:35], v[0:3]\n\t" \
  "s_add_u32 s96, %[rot19], 6291456\n\t" \
  "ds_read_b64_tr_b16 a[128:129], v184 offset:49152\n\t" \
  "v_mfma_f32_16x16x32_bf16 v[0:3], a[0:3], a[48:51], v[0:3]\n\t" \
  "s_and_b32 s96, s96, 0x780000\n\t" \
  "v_mfma_f32_16x16x32_bf16 v[32:35], a[0:3], a[52:55], v[32:35]\n\t" \
  "s_add_u32 m0, %[mw], 32768\n\t" \
  "ds_read_b64_tr_b16 a[130:131], v184 offset:50176\n\t" \
  "v_mfma_f32_16x16x32_bf16 v[32:35], a[160:163], a[36:39], v[32:35]\n\t" \
  "buffer_load_dwordx4 v200, %[sw], s96 offen offset:0 lds\n\t" \
  "v_mfma_f32_16x16x32_bf16 v[64:67], a[160:163], a[40:43], v[64:67]\n\t" \
  "buffer_load_dwordx4 v201, %[sw], s96 offen offset:1024 lds\n\t" \
  "v_mfma_f32_16x16x32_bf16 v[64:67], a[0:3], a[56:59], v[64:67]\n\t" \
  "s_lshl_b32 s98, s98, 9\n\t" \
  "ds_read_b64_tr_b16 a[132:133], v185 offset:49152\n\t" \
  "v_mfma_f32_16x16x32_bf16 v[96:99], a[0:3], a[60:63], v[96:99]\n\t" \
  "s_add_u32 %[fw2], %[fw2], s98\n\t" \
  "ds_read_b64_tr_b16 a[134:135], v185 offset:50176\n\t" \
  "v_mfma_f32_16x16x32_bf16 v[96:99], a[160:163], a[44:47], v[96:99]\n\t" \
  "s_and_b32 s98, %[bid], 0x2\n\t" \
  "ds_read_b64_tr_b16 a[136:137], v186 offset:49152\n\t" \
  "v_mfma_f32_16x16x32_bf16 v[4:7], a[164:167], a[32:35], v[4:7]\n\t" \
  "s_lshl_b32 s98, s98, 13\n\t" \
  "ds_read_b64_tr_b16 a[138:139], v186 offset:50176\n\t" \
  "v_mfma_f32_16x16x32_bf16 v[4:7], a[4:7], a[48:51], v[4:7]\n\t" \
  "buffer_load_dwordx4 v202, %[sw], s96 offen offset:2048 lds\n\t" \
  "v_mfma_f32_16x16x32_bf16 v[36:39], a[4:7], a[52:55], v[36:39]\n\t" \
  "s_add_u32 %[fw2], %[fw2], s98\n\t" \
  "ds_read_b64_tr_b16 a[140:141], v187 offset:49152\n\t" \
  "v_mfma_f32_16x16x32_bf16 v[36:39], a[164:167], a[36:39], v[36:39]\n\t" \
  "s_and_b32 s98, %[bid], 0x4\n\t" \
  "ds_read_b64_tr_b16 a[142:143], v187 offset:50176\n\t" \
  "v_mfma_f32_16x16x32_bf16 v[68:71], a[164:167], a[40:43], v[68:71]\n\t" \
  "s_lshl_b32 s98, s98, 11\n\t" \
  "ds_read_b64_tr_b16 a[144:145], v188 offset:49152\n\t" \
  "v_mfma_f32_16x16x32_bf16 v[68:71], a[4:7], a[56:59], v[68:71]\n\t" \
  "buffer_load_dwordx4 v203, %[sw], s96 offen offset:3072 lds\n\t" \
  "v_mfma_f32_16x16x32_bf16 v[100:103], a[4:7], a[60:63], v[100:103]\n\t" \
  "s_add_u32 s97, %[rot19], 5767168\n\t" \
  "ds_read_b64_tr_b16 a[146:147], v188 offset:50176\n\t" \
  "v_mfma_f32_16x16x32_bf16 v[100:103], a[164:167], a[44:47], v[100:103]\n\t" \
  "s_bfe_u32 s97, s97, 0x140003\n\t" \
  "ds_read_b64_tr_b16 a[148:149], v189 offset:49152\n\t" \
  "v_mfma_f32_16x16x32_bf16 v[8:11], a[168:171], a[32:35], v[8:11]\n\t" \
  "s_add_u32 s97, s97, %[sox]\n\t" \
  "v_mfma_f32_16x16x32_bf16 v[8:11], a[8:11], a[48:51], v[8:11]\n\t" \
  "s_add_u32 %[fw2], %[fw2], s98\n\t" \
  "ds_read_b64_tr_b16 a[150:151], v189 offset:50176\n\t" \
  "v_mfma_f32_16x16x32_bf16 v[40:43], a[8:11], a[52:55], v[40:43]\n\t" \
  "buffer_load_dwordx4 a[224:227], v248, %[ws], s97 offen sc1\n\t" \
  "v_mfma_f32_16x16x32_bf16 v[40:43], a[168:171], a[36:39], v[40:43]\n\t" \
  "s_and_b32 s98, %[bid], 0x1\n\t" \
  "ds_read_b64_tr_b16 a[152:153], v190 offset:49152\n\t" \
  "v_mfma_f32_16x16x32_bf16 v[72:75], a[168:171], a[40:43], v[72:75]\n\t" \
  "s_lshl_b32 s98, s98, 12\n\t" \
  "ds_read_b64_tr_b16 a[154:155], v190 offset:50176\n\t" \
  "v_mfma_f32_16x16x32_bf16 v[72:75], a[8:11], a[56:59], v[72:75]\n\t" \
  "s_add_u32 %[fw2], %[fw2], s98\n\t" \
  "ds_read_b64_tr_b16 a[156:157], v191 offset:49152\n\t" \
  "v_mfma_f32_16x16x32_bf16 v[104:107], a[8:11], a[60:63], v[104:107]\n\t" \
  "s_lshl2_add_u32 %[fw2], %[hl], %[fw2]\n\t" \
  "v_mfma_f32_16x16x32_bf16 v[104:107], a[168:171], a[44:47], v[104:107]\n\t" \
  "buffer_load_dwordx4 a[228:231], v249, %[ws], s97 offen sc1\n\t" \
  "v_mfma_f32_16x16x32_bf16 v[12:15], a[172:175], a[32:35], v[12:15]\n\t" \
  "s_lshl_b32 %[xso], %[wid], 15\n\t" \
  "ds_read_b64_tr_b16 a[158:159], v191 offset:50176\n\t" \
  "v_mfma_f32_16x16x32_bf16 v[12:15], a[12:15], a[48:51], v[12:15]\n\t" \
  "s_and_b32 s98, %[bid], 0x4\n\t" \
  "ds_read_b64_tr_b16 a[64:65], v184 offset:57344\n\t" \
  "v_mfma_f32_16x16x32_bf16 v[44:47], a[12:15], a[52:55], v[44:47]\n\t" \
  "s_lshl_b32 s98, s98, 19\n\t" \
  "ds_read_b64_tr_b16 a[66:67], v184 offset:58368\n\t" \
  "v_mfma_f32_16x16x32_bf16 v[44:47], a[172:175], a[36:39], v[44:47]\n\t" \
  "buffer_load_dwordx4 a[232:235], v250, %[ws], s97 offen sc1\n\t" \
  "v_mfma_f32_16x16x32_bf16 v[76:79], a[172:175], a[40:43], v[76:79]\n\t" \
  "s_add_u32 %[xso], %[xso], s98\n\t" \
  "ds_read_b64_tr_b16 a[68:69], v185 offset:57344\n\t" \
  "v_mfma_f32_16x16x32_bf16 v[76:79], a[12:15], a[56:59], v[76:79]\n\t" \
  "s_and_b32 s98, %[bid], 0xc0\n\t" \
  "ds_read_b64_tr_b16 a[70:71], v185 offset:58368\n\t" \
  "v_mfma_f32_16x16x32_bf16 v[108:111], a[12:15], a[60:63], v[108:111]\n\t" \
  "s_lshl_b32 s98, s98, 13\n\t" \
  "ds_read_b64_tr_b16 a[72:73], v186 offset:57344\n\t" \
  "v_mfma_f32_16x16x32_bf16 v[108:111], a[172:175], a[44:47], v[108:111]\n\t" \
  "s_add_u32 %[xso], %[xso], s98\n\t" \
  "v_mfma_f32_16x16x32_bf16 v[16:19], a[176:179], a[32:35], v[16:19]\n\t" \
  "buffer_load_dwordx4 a[236:239], v251, %[ws], s97 offen sc1\n\t" \
  "v_mfma_f32_16x16x32_bf16 v[16:19], a[16:19], a[48:51], v[16:19]\n\t" \
  "s_add_u32 s99, %[pf1], 2048\n\t" \
  "ds_read_b64_tr_b16 a[74:75], v186 offset:58368\n\t" \
  "v_mfma_f32_16x16x32_bf16 v[48:51], a[16:19], a[52:55], v[48:51]\n\t" \
  "s_and_b32 s98, %[bid], 0x3\n\t" \
  "ds_read_b64_tr_b16 a[76:77], v187 offset:57344\n\t" \
  "v_mfma_f32_16x16x32_bf16 v[48:51], a[176:179], a[36:39], v[48:51]\n\t" \
  "s_lshl_b32 s98, s98, 17\n\t" \
  "ds_read_b64_tr_b16 a[78:79], v187 offset:58368\n\t" \
  "v_mfma_f32_16x16x32_bf16 v[80:83], a[176:179], a[40:43], v[80:83]\n\t" \
  "buffer_load_dword %[pf], v217, %[wo], s99 offen\n\t" \
  "v_mfma_f32_16x16x32_bf16 v[80:83], a[16:19], a[56:59], v[80:83]\n\t" \
  "s_add_u32 s97, s97, %[dk]\n\t" \
  "ds_read_b64_tr_b16 a[80:81], v188 offset:57344\n\t" \
  "v_mfma_f32_16x16x32_bf16 v[112:115], a[16:19], a[60:63], v[112:115]\n\t" \
  "s_add_u32 %[xso], %[xso], s98\n\t" \
  "ds_read_b64_tr_b16 a[82:83], v188 offset:58368\n\t" \
  "v_mfma_f32_16x16x32_bf16 v[112:115], a[176:179], a[44:47], v[112:115]\n\t" \
  "s_and_b32 s98, %[bid], 0x38\n\t" \
  "ds_read_b64_tr_b16 a[84:85], v189 offset:57344\n\t" \
  "v_mfma_f32_16x16x32_bf16 v[20:23], a[180:183], a[32:35], v[20:23]\n\t" \
  "s_lshl_b32 s98, s98, 5\n\t" \
  "v_mfma_f32_16x16x32_bf16 v[20:23], a[20:23], a[48:51], v[20:23]\n\t" \
  "buffer_load_dwordx4 a[240:243], v248, %[ws], s97 offen sc1\n\t" \
  "v_mfma_f32_16x16x32_bf16 v[52:55], a[20:23], a[52:55], v[52:55]\n\t" \
  "s_add_u32 %[xso], %[xso], s98\n\t" \
  "ds_read_b64_tr_b16 a[86:87], v189 offset:58368\n\t" \
  "v_mfma_f32_16x16x32_bf16 v[52:55], a[180:183], a[36:39], v[52:55]\n\t" \
  "s_lshl_b32 %[ss], %[wid], 7\n\t" \
  "ds_read_b64_tr_b16 a[88:89], v190 offset:57344\n\t" \
  "v_mfma_f32_16x16x32_bf16 v[84:87], a[180:183], a[40:43], v[84:87]\n\t" \
  "s_add_u32 %[ss], %[ss], 0x240000\n\t" \
  "ds_read_b64_tr_b16 a[90:91], v190 offset:58368\n\t" \
  "v_mfma_f32_16x16x32_bf16 v[84:87], a[20:23], a[56:59], v[84:87]\n\t" \
  "s_and_b32 s98, %[bid], 0xff\n\t" \
  "ds_read_b64_tr_b16 a[92:93], v191 offset:57344\n\t" \
  "v_mfma_f32_16x16x32_bf16 v[116:119], a[20:23], a[60:63], v[116:119]\n\t" \
  "buffer_load_dwordx4 a[244:247], v249, %[ws], s97 offen sc1\n\t" \
  "v_mfma_f32_16x16x32_bf16 v[116:119], a[180:183], a[44:47], v[116:119]\n\t" \
  "s_lshl_b32 s98, s98, 9\n\t" \
  "ds_read_b64_tr_b16 a[94:95], v191 offset:58368\n\t" \
  "v_mfma_f32_16x16x32_bf16 v[24:27], a[184:187], a[32:35], v[24:27]\n\t" \
  "s_add_u32 %[ss], %[ss], s98\n\t" \
  "v_mfma_f32_16x16x32_bf16 v[24:27], a[24:27], a[48:51], v[24:27]\n\t" \
  "s_lshl_b32 %[mbp], %[wid], 7\n\t" \
  "v_mfma_f32_16x16x32_bf16 v[56:59], a[24:27], a[52:55], v[56:59]\n\t" \
  "buffer_load_dwordx4 a[248:251], v250, %[ws], s97 offen sc1\n\t" \
  "v_mfma_f32_16x16x32_bf16 v[56:59], a[184:187], a[36:39], v[56:59]\n\t" \
  "s_add_u32 %[mbp], %[mbp], 0x220000\n\t" \
  "v_mfma_f32_16x16x32_bf16 v[88:91], a[184:187], a[40:43], v[88:91]\n\t" \
  "s_and_b32 s98, %[bid], 0xc0\n\t" \
  "v_mfma_f32_16x16x32_bf16 v[88:91], a[24:27], a[56:59], v[88:91]\n\t" \
  "s_lshl_b32 s98, s98, 9\n\t" \
  "v_mfma_f32_16x16x32_bf16 v[120:123], a[24:27], a[60:63], v[120:123]\n\t" \
  "s_add_u32 %[mbp], %[mbp], s98\n\t" \
  "v_mfma_f32_16x16x32_bf16 v[120:123], a[184:187], a[44:47], v[120:123]\n\t" \
  "buffer_load_dwordx4 a[252:255], v251, %[ws], s97 offen sc1\n\t" \
  "v_mfma_f32_16x16x32_bf16 v[28:31], a[188:191], a[32:35], v[28:31]\n\t" \
  "s_and_b32 s98, %[bid], 0x3\n\t" \
  "v_mfma_f32_16x16x32_bf16 v[28:31], a[28:31], a[48:51], v[28:31]\n\t" \
  "s_lshl_b32 s98, s98, 13\n\t" \
  "v_mfma_f32_16x16x32_bf16 v[60:63], a[28:31], a[52:55], v[60:63]\n\t" \
  "s_add_u32 %[mbp], %[mbp], s98\n\t" \
  "v_mfma_f32_16x16x32_bf16 v[60:63], a[188:191], a[36:39], v[60:63]\n\t" \
  "s_and_b32 s98, %[bid], 0x4\n\t" \
  "v_mfma_f32_16x16x32_bf16 v[92:95], a[188:191], a[40:43], v[92:95]\n\t" \
  "s_lshl_b32 s98, s98, 10\n\t" \
  "v_mfma_f32_16x16x32_bf16 v[92:95], a[28:31], a[56:59], v[92:95]\n\t" \
  "s_add_u32 %[mbp], %[mbp], s98\n\t" \
  "v_mfma_f32_16x16x32_bf16 v[124:127], a[28:31], a[60:63], v[124:127]\n\t" \
  "s_and_b32 s98, %[bid], 0x38\n\t" \
  "v_mfma_f32_16x16x32_bf16 v[124:127], a[188:191], a[44:47], v[124:127]\n\t" \
  "s_waitcnt vmcnt(25) lgkmcnt(0)\n\t" \
  "s_barrier\n\t" \
  "v_mfma_f32_16x16x32_bf16 v[0:3], a[128:131], a[96:99], v[0:3]\n\t" \
  "s_add_u32 s96, %[rot19], 6815744\n\t" \
  "ds_read_b64_tr_b16 a[160:161], v192 offset:0\n\t" \
  "v_mfma_f32_16x16x32_bf16 v[0:3], a[64:67], a[112:115], v[0:3]\n\t" \
  "s_and_b32 s96, s96, 0x780000\n\t" \
  "v_mfma_f32_16x16x32_bf16 v[32:35], a[64:67], a[116:119], v[32:35]\n\t" \
  "s_add_u32 m0, %[mw], 49152\n\t" \
  "ds_read_b64_tr_b16 a[162:163], v192 offset:1024\n\t" \
  "v_mfma_f32_16x16x32_bf16 v[32:35], a[128:131], a[100:103], v[32:35]\n\t" \
  "buffer_load_dwordx4 v200, %[sw], s96 offen offset:0 lds\n\t" \
  "v_mfma_f32_16x16x32_bf16 v[64:67], a[128:131], a[104:107], v[64:67]\n\t" \
  "buffer_load_dwordx4 v201, %[sw], s96 offen offset:1024 lds\n\t" \
  "v_mfma_f32_16x16x32_bf16 v[64:67], a[64:67], a[120:123], v[64:67]\n\t" \
  "s_lshl_b32 s98, s98, 6\n\t" \
  "ds_read_b64_tr_b16 a[164:165], v193 offset:0\n\t" \
  "v_mfma_f32_16x16x32_bf16 v[96:99], a[64:67], a[124:127], v[96:99]\n\t" \
  "s_add_u32 %[mbp], %[mbp], s98\n\t" \
  "ds_read_b64_tr_b16 a[166:167], v193 offset:1024\n\t" \
  "v_mfma_f32_16x16x32_bf16 v[96:99], a[128:131], a[108:111], v[96:99]\n\t" \
  "s_lshr_b32 s98, %[bid], 6\n\t" \
  "ds_read_b64_tr_b16 a[168:169], v194 offset:0\n\t" \
  "v_mfma_f32_16x16x32_bf16 v[4:7], a[132:135], a[96:99], v[4:7]\n\t" \
  "s_lshl2_add_u32 %[gvo], s98, %[wid]\n\t" \
  "ds_read_b64_tr_b16 a[170:171], v194 offset:1024\n\t" \
  "v_mfma_f32_16x16x32_bf16 v[4:7], a[68:71], a[112:115], v[4:7]\n\t" \
  "buffer_load_dwordx4 v202, %[sw], s96 offen offset:2048 lds\n\t" \
  "v_mfma_f32_16x16x32_bf16 v[36:39], a[68:71], a[116:119], v[36:39]\n\t" \
  "s_mul_i32 %[gvo], %[gvo], 786432\n\t" \
  "ds_read_b64_tr_b16 a[172:173], v195 offset:0\n\t" \
  "v_mfma_f32_16x16x32_bf16 v[36:39], a[132:135], a[100:103], v[36:39]\n\t" \
  "s_and_b32 s98, %[bid], 0x38\n\t" \
  "ds_read_b64_tr_b16 a[174:175], v195 offset:1024\n\t" \
  "v_mfma_f32_16x16x32_bf16 v[68:71], a[132:135], a[104:107], v[68:71]\n\t" \
  "s_lshl_b32 s98, s98, 4\n\t" \
  "ds_read_b64_tr_b16 a[176:177], v196 offset:0\n\t" \
  "v_mfma_f32_16x16x32_bf16 v[68:71], a[68:71], a[120:123], v[68:71]\n\t" \
  "buffer_load_dwordx4 v203, %[sw], s96 offen offset:3072 lds\n\t" \
  "v_mfma_f32_16x16x32_bf16 v[100:103], a[68:71], a[124:127], v[100:103]\n\t" \
  "s_add_u32 s97, %[rot19], 6291456\n\t" \
  "ds_read_b64_tr_b16 a[178:179], v196 offset:1024\n\t" \
  "v_mfma_f32_16x16x32_bf16 v[100:103], a[132:135], a[108:111], v[100:103]\n\t" \
  "s_bfe_u32 s97, s97, 0x140003\n\t" \
  "ds_read_b64_tr_b16 a[180:181], v197 offset:0\n\t" \
  "v_mfma_f32_16x16x32_bf16 v[8:11], a[136:139], a[96:99], v[8:11]\n\t" \
  "s_add_u32 s97, s97, %[sox]\n\t" \
  "v_mfma_f32_16x16x32_bf16 v[8:11], a[72:75], a[112:115], v[8:11]\n\t" \
  "s_add_u32 %[gvo], %[gvo], s98\n\t" \
  "ds_read_b64_tr_b16 a[182:183], v197 offset:1024\n\t" \
  "v_mfma_f32_16x16x32_bf16 v[40:43], a[72:75], a[116:119], v[40:43]\n\t" \
  "buffer_load_dwordx4 a[32:35], v248, %[ws], s97 offen sc1\n\t" \
  "v_mfma_f32_16x16x32_bf16 v[40:43], a[136:139], a[100:103], v[40:43]\n\t" \
  "s_lshr_b32 s98, %[bid], 2\n\t" \
  "ds_read_b64_tr_b16 a[184:185], v198 offset:0\n\t" \
  "v_mfma_f32_16x16x32_bf16 v[72:75], a[136:139], a[104:107], v[72:75]\n\t" \
  "s_xor_b32 s98, s98, %[bid]\n\t" \
  "ds_read_b64_tr_b16 a[186:187], v198 offset:1024\n\t" \
  "v_mfma_f32_16x16x32_bf16 v[72:75], a[72:75], a[120:123], v[72:75]\n\t" \
  "s_and_b32 s98, s98, 4\n\t" \
  "ds_read_b64_tr_b16 a[188:189], v199 offset:0\n\t" \
  "v_mfma_f32_16x16x32_bf16 v[104:107], a[72:75], a[124:127], v[104:107]\n\t" \
  "s_lshl_b32 %[vo], s98, 4\n\t" \
  "v_mfma_f32_16x16x32_bf16 v[104:107], a[136:139], a[108:111], v[104:107]\n\t" \
  "buffer_load_dwordx4 a[36:39], v249, %[ws], s97 offen sc1\n\t" \
  "v_mfma_f32_16x16x32_bf16 v[12:15], a[140:143], a[96:99], v[12:15]\n\t" \
  "s_and_b32 s98, %[bid], 0x8\n\t" \
  "ds_read_b64_tr_b16 a[190:191], v199 offset:1024\n\t" \
  "v_mfma_f32_16x16x32_bf16 v[12:15], a[76:79], a[112:115], v[12:15]\n\t" \
  "s_lshl_b32 s98, s98, 2\n\t" \
  "ds_read_b64_tr_b16 a[0:1], v192 offset:8192\n\t" \
  "v_mfma_f32_16x16x32_bf16 v[44:47], a[76:79], a[116:119], v[44:47]\n\t" \
  "s_add_u32 %[vo], %[vo], s98\n\t" \
  "ds_read_b64_tr_b16 a[2:3], v192 offset:9216\n\t" \
  "v_mfma_f32_16x16x32_bf16 v[44:47], a[140:143], a[100:103], v[44:47]\n\t" \
  "buffer_load_dwordx4 a[40:43], v250, %[ws], s97 offen sc1\n\t" \
  "v_mfma_f32_16x16x32_bf16 v[76:79], a[140:143], a[104:107], v[76:79]\n\t" \
  "s_and_b32 s98, %[bid], 0x3\n\t" \
  "ds_read_b64_tr_b16 a[4:5], v193 offset:8192\n\t" \
  "v_mfma_f32_16x16x32_bf16 v[76:79], a[76:79], a[120:123], v[76:79]\n\t" \
  "s_lshl_b32 s98, s98, 3\n\t" \
  "ds_read_b64_tr_b16 a[6:7], v193 offset:9216\n\t" \
  "v_mfma_f32_16x16x32_bf16 v[108:111], a[76:79], a[124:127], v[108:111]\n\t" \
  "s_add_u32 %[vo], %[vo], s98\n\t" \
  "ds_read_b64_tr_b16 a[8:9], v194 offset:8192\n\t" \
  "v_mfma_f32_16x16x32_bf16 v[108:111], a[140:143], a[108:111], v[108:111]\n\t" \
  "s_and_b32 s98, %[bid], 0x4\n\t" \
  "v_mfma_f32_16x16x32_bf16 v[16:19], a[144:147], a[96:99], v[16:19]\n\t" \
  "buffer_load_dwordx4 a[44:47], v251, %[ws], s97 offen sc1\n\t" \
  "v_mfma_f32_16x16x32_bf16 v[16:19], a[80:83], a[112:115], v[16:19]\n\t" \
  "s_add_u32 s97, s97, %[dk]\n\t" \
  "ds_read_b64_tr_b16 a[10:11], v194 offset:9216\n\t" \
  "v_mfma_f32_16x16x32_bf16 v[48:51], a[80:83], a[116:119], v[48:51]\n\t" \
  "s_lshl_b32 s98, s98, 0\n\t" \
  "ds_read_b64_tr_b16 a[12:13], v195 offset:8192\n\t" \
  "v_mfma_f32_16x16x32_bf16 v[48:51], a[144:147], a[100:103], v[48:51]\n\t" \
  "s_add_u32 %[vo], %[vo], s98\n\t" \
  "ds_read_b64_tr_b16 a[14:15], v195 offset:9216\n\t" \
  "v_mfma_f32_16x16x32_bf16 v[80:83], a[144:147], a[104:107], v[80:83]\n\t" \
  "buffer_load_dwordx4 a[48:51], v248, %[ws], s97 offen sc1\n\t" \
  "v_mfma_f32_16x16x32_bf16 v[80:83], a[80:83], a[120:123], v[80:83]\n\t" \
  "s_add_u32 %[vo], %[vo], 0x2000\n\t" \
  "ds_read_b64_tr_b16 a[16:17], v196 offset:8192\n\t" \
  "v_mfma_f32_16x16x32_bf16 v[112:115], a[80:83], a[124:127], v[112:115]\n\t" \
  "ds_read_b64_tr_b16 a[18:19], v196 offset:9216\n\t" \
  "v_mfma_f32_16x16x32_bf16 v[112:115], a[144:147], a[108:111], v[112:115]\n\t" \
  "ds_read_b64_tr_b16 a[20:21], v197 offset:8192\n\t" \
  "v_mfma_f32_16x16x32_bf16 v[20:23], a[148:151], a[96:99], v[20:23]\n\t" \
  "s_cmp_lt_i32 %[prb], 0\n\t" \
  "s_cselect_b32 %[vo], %[prb], %[vo]\n\t" \
  "v_mfma_f32_16x16x32_bf16 v[20:23], a[84:87], a[112:115], v[20:23]\n\t" \
  "buffer_load_dwordx4 a[52:55], v249, %[ws], s97 offen sc1\n\t" \
  "v_mfma_f32_16x16x32_bf16 v[52:55], a[84:87], a[116:119], v[52:55]\n\t" \
  "ds_read_b64_tr_b16 a[22:23], v197 offset:9216\n\t" \
  "v_mfma_f32_16x16x32_bf16 v[52:55], a[148:151], a[100:103], v[52:55]\n\t" \
  "ds_read_b64_tr_b16 a[24:25], v198 offset:8192\n\t" \
  "v_mfma_f32_16x16x32_bf16 v[84:87], a[148:151], a[104:107], v[84:87]\n\t" \
  "ds_read_b64_tr_b16 a[26:27], v198 offset:9216\n\t" \
  "v_mfma_f32_16x16x32_bf16 v[84:87], a[84:87], a[120:123], v[84:87]\n\t" \
  "ds_read_b64_tr_b16 a[28:29], v199 offset:8192\n\t" \
  "v_mfma_f32_16x16x32_bf16 v[116:119], a[84:87], a[124:127], v[116:119]\n\t" \
  "buffer_load_dwordx4 a[56:59], v250, %[ws], s97 offen sc1\n\t" \
  "v_mfma_f32_16x16x32_bf16 v[116:119], a[148:151], a[108:111], v[116:119]\n\t" \
  "ds_read_b64_tr_b16 a[30:31], v199 offset:9216\n\t" \
  "v_mfma_f32_16x16x32_bf16 v[24:27], a[152:155], a[96:99], v[24:27]\n\t" \
  "v_mfma_f32_16x16x32_bf16 v[24:27], a[88:91], a[112:115], v[24:27]\n\t" \
  "v_mfma_f32_16x16x32_bf16 v[56:59], a[88:91], a[116:119], v[56:59]\n\t" \
  "buffer_load_dwordx4 a[60:63], v251, %[ws], s97 offen sc1\n\t" \
  "v_mfma_f32_16x16x32_bf16 v[56:59], a[152:155], a[100:103], v[56:59]\n\t" \
  "s_add_u32 s99, %[pf1], 4096\n\t" \
  "v_mfma_f32_16x16x32_bf16 v[88:91], a[152:155], a[104:107], v[88:91]\n\t" \
  "v_mfma_f32_16x16x32_bf16 v[88:91], a[88:91], a[120:123], v[88:91]\n\t" \
  "v_mfma_f32_16x16x32_bf16 v[120:123], a[88:91], a[124:127], v[120:123]\n\t" \
  "v_mfma_f32_16x16x32_bf16 v[120:123], a[152:155], a[108:111], v[120:123]\n\t" \
  "buffer_load_dword %[pf], v217, %[wo], s99 offen\n\t" \
  "v_mfma_f32_16x16x32_bf16 v[28:31], a[156:159], a[96:99], v[28:31]\n\t" \
  "v_mfma_f32_16x16x32_bf16 v[28:31], a[92:95], a[112:115], v[28:31]\n\t" \
  "v_mfma_f32_16x16x32_bf16 v[60:63], a[92:95], a[116:119], v[60:63]\n\t" \
  "v_mfma_f32_16x16x32_bf16 v[60:63], a[156:159], a[100:103], v[60:63]\n\t" \
  "v_mfma_f32_16x16x32_bf16 v[92:95], a[156:159], a[104:107], v[92:95]\n\t" \
  "v_mfma_f32_16x16x32_bf16 v[92:95], a[92:95], a[120:123], v[92:95]\n\t" \
  "v_mfma_f32_16x16x32_bf16 v[124:127], a[92:95], a[124:127], v[124:127]\n\t" \
  "v_mfma_f32_16x16x32_bf16 v[124:127], a[156:159], a[108:111], v[124:127]\n\t" \
  "s_waitcnt vmcnt(26) lgkmcnt(0)\n\t" \
  "s_barrier\n\t" \
  "v_mfma_f32_16x16x32_bf16 v[0:3], a[160:163], a[192:195], v[0:3]\n\t" \
  "s_add_u32 s96, %[rot19], 7340032\n\t" \
  "ds_read_b64_tr_b16 a[128:129], v184 offset:16384\n\t" \
  "v_mfma_f32_16x16x32_bf16 v[0:3], a[0:3], a[208:211], v[0:3]\n\t" \
  "s_and_b32 s96, s96, 0x780000\n\t" \
  "v_mfma_f32_16x16x32_bf16 v[32:35], a[0:3], a[212:215], v[32:35]\n\t" \
  "s_add_u32 m0, %[mw], 0\n\t" \
  "ds_read_b64_tr_b16 a[130:131], v184 offset:17408\n\t" \
  "v_mfma_f32_16x16x32_bf16 v[32:35], a[160:163], a[196:199], v[32:35]\n\t" \
  "buffer_load_dwordx4 v200, %[sw], s96 offen offset:0 lds\n\t" \
  "v_mfma_f32_16x16x32_bf16 v[64:67], a[160:163], a[200:203], v[64:67]\n\t" \
  "ds_read_b64_tr_b16 a[132:133], v185 offset:16384\n\t" \
  "v_mfma_f32_16x16x32_bf16 v[64:67], a[0:3], a[216:219], v[64:67]\n\t" \
  "buffer_load_dwordx4 v201, %[sw], s96 offen offset:1024 lds\n\t" \
  "v_mfma_f32_16x16x32_bf16 v[96:99], a[0:3], a[220:223], v[96:99]\n\t" \
  "ds_read_b64_tr_b16 a[134:135], v185 offset:17408\n\t" \
  "v_mfma_f32_16x16x32_bf16 v[96:99], a[160:163], a[204:207], v[96:99]\n\t" \
  "ds_read_b64_tr_b16 a[136:137], v186 offset:16384\n\t" \
  "v_mfma_f32_16x16x32_bf16 v[4:7], a[164:167], a[192:195], v[4:7]\n\t" \
  "ds_read_b64_tr_b16 a[138:139], v186 offset:17408\n\t" \
  "v_mfma_f32_16x16x32_bf16 v[4:7], a[4:7], a[208:211], v[4:7]\n\t" \
  "ds_read_b64_tr_b16 a[140:141], v187 offset:16384\n\t" \
  "v_mfma_f32_16x16x32_bf16 v[36:39], a[4:7], a[212:215], v[36:39]\n\t" \
  "buffer_load_dwordx4 v202, %[sw], s96 offen offset:2048 lds\n\t" \
  "v_mfma_f32_16x16x32_bf16 v[36:39], a[164:167], a[196:199], v[36:39]\n\t" \
  "ds_read_b64_tr_b16 a[142:143], v187 offset:17408\n\t" \
  "v_mfma_f32_16x16x32_bf16 v[68:71], a[164:167], a[200:203], v[68:71]\n\t" \
  "ds_read_b64_tr_b16 a[144:145], v188 offset:16384\n\t" \
  "v_mfma_f32_16x16x32_bf16 v[68:71], a[4:7], a[216:219], v[68:71]\n\t" \
  "v_mfma_f32_16x16x32_bf16 v[100:103], a[4:7], a[220:223], v[100:103]\n\t" \
  "ds_read_b64_tr_b16 a[146:147], v188 offset:17408\n\t" \
  "v_mfma_f32_16x16x32_bf16 v[100:103], a[164:167], a[204:207], v[100:103]\n\t" \
  "buffer_load_dwordx4 v203, %[sw], s96 offen offset:3072 lds\n\t" \
  "v_mfma_f32_16x16x32_bf16 v[8:11], a[168:171], a[192:195], v[8:11]\n\t" \
  "s_add_u32 s97, %[rot19], 6815744\n\t" \
  "ds_read_b64_tr_b16 a[148:149], v189 offset:16384\n\t" \
  "v_mfma_f32_16x16x32_bf16 v[8:11], a[8:11], a[208:211], v[8:11]\n\t" \
  "s_bfe_u32 s97, s97, 0x140003\n\t" \
  "ds_read_b64_tr_b16 a[150:151], v189 offset:17408\n\t" \
  "v_mfma_f32_16x16x32_bf16 v[40:43], a[8:11], a[212:215], v[40:43]\n\t" \
  "s_add_u32 s97, s97, %[sox]\n\t" \
  "ds_read_b64_tr_b16 a[152:153], v190 offset:16384\n\t" \
  "v_mfma_f32_16x16x32_bf16 v[40:43], a[168:171], a[196:199], v[40:43]\n\t" \
  "v_mfma_f32_16x16x32_bf16 v[72:75], a[168:171], a[200:203], v[72:75]\n\t" \
  "buffer_load_dwordx4 a[96:99], v248, %[ws], s97 offen sc1\n\t" \
  "v_mfma_f32_16x16x32_bf16 v[72:75], a[8:11], a[216:219], v[72:75]\n\t" \
  "ds_read_b64_tr_b16 a[154:155], v190 offset:17408\n\t" \
  "v_mfma_f32_16x16x32_bf16 v[104:107], a[8:11], a[220:223], v[104:107]\n\t" \
  "ds_read_b64_tr_b16 a[156:157], v191 offset:16384\n\t" \
  "v_mfma_f32_16x16x32_bf16 v[104:107], a[168:171], a[204:207], v[104:107]\n\t" \
  "ds_read_b64_tr_b16 a[158:159], v191 offset:17408\n\t" \
  "v_mfma_f32_16x16x32_bf16 v[12:15], a[172:175], a[192:195], v[12:15]\n\t" \
  "ds_read_b64_tr_b16 a[64:65], v184 offset:24576\n\t" \
  "v_mfma_f32_16x16x32_bf16 v[12:15], a[12:15], a[208:211], v[12:15]\n\t" \
  "buffer_load_dwordx4 a[100:103], v249, %[ws], s97 offen sc1\n\t" \
  "v_mfma_f32_16x16x32_bf16 v[44:47], a[12:15], a[212:215], v[44:47]\n\t" \
  "ds_read_b64_tr_b16 a[66:67], v184 offset:25600\n\t" \
  "v_mfma_f32_16x16x32_bf16 v[44:47], a[172:175], a[196:199], v[44:47]\n\t" \
  "ds_read_b64_tr_b16 a[68:69], v185 offset:24576\n\t" \
  "v_mfma_f32_16x16x32_bf16 v[76:79], a[172:175], a[200:203], v[76:79]\n\t" \
  "v_mfma_f32_16x16x32_bf16 v[76:79], a[12:15], a[216:219], v[76:79]\n\t" \
  "ds_read_b64_tr_b16 a[70:71], v185 offset:25600\n\t" \
  "v_mfma_f32_16x16x32_bf16 v[108:111], a[12:15], a[220:223], v[108:111]\n\t" \
  "buffer_load_dwordx4 a[104:107], v250, %[ws], s97 offen sc1\n\t" \
  "v_mfma_f32_16x16x32_bf16 v[108:111], a[172:175], a[204:207], v[108:111]\n\t" \
  "ds_read_b64_tr_b16 a[72:73], v186 offset:24576\n\t" \
  "v_mfma_f32_16x16x32_bf16 v[16:19], a[176:179], a[192:195], v[16:19]\n\t" \
  "ds_read_b64_tr_b16 a[74:75], v186 offset:25600\n\t" \
  "v_mfma_f32_16x16x32_bf16 v[16:19], a[16:19], a[208:211], v[16:19]\n\t" \
  "ds_read_b64_tr_b16 a[76:77], v187 offset:24576\n\t" \
  "v_mfma_f32_16x16x32_bf16 v[48:51], a[16:19], a[212:215], v[48:51]\n\t" \
  "v_mfma_f32_16x16x32_bf16 v[48:51], a[176:179], a[196:199], v[48:51]\n\t" \
  "buffer_load_dwordx4 a[108:111], v251, %[ws], s97 offen sc1\n\t" \
  "v_mfma_f32_16x16x32_bf16 v[80:83], a[176:179], a[200:203], v[80:83]\n\t" \
  "s_add_u32 s97, s97, %[dk]\n\t" \
  "ds_read_b64_tr_b16 a[78:79], v187 offset:25600\n\t" \
  "v_mfma_f32_16x16x32_bf16 v[80:83], a[16:19], a[216:219], v[80:83]\n\t" \
  "ds_read_b64_tr_b16 a[80:81], v188 offset:24576\n\t" \
  "v_mfma_f32_16x16x32_bf16 v[112:115], a[16:19], a[220:223], v[112:115]\n\t" \
  "ds_read_b64_tr_b16 a[82:83], v188 offset:25600\n\t" \
  "v_mfma_f32_16x16x32_bf16 v[112:115], a[176:179], a[204:207], v[112:115]\n\t" \
  "ds_read_b64_tr_b16 a[84:85], v189 offset:24576\n\t" \
  "v_mfma_f32_16x16x32_bf16 v[20:23], a[180:183], a[192:195], v[20:23]\n\t" \
  "buffer_load_dwordx4 a[112:115], v248, %[ws], s97 offen sc1\n\t" \
  "v_mfma_f32_16x16x32_bf16 v[20:23], a[20:23], a[208:211], v[20:23]\n\t" \
  "ds_read_b64_tr_b16 a[86:87], v189 offset:25600\n\t" \
  "v_mfma_f32_16x16x32_bf16 v[52:55], a[20:23], a[212:215], v[52:55]\n\t" \
  "ds_read_b64_tr_b16 a[88:89], v190 offset:24576\n\t" \
  "v_mfma_f32_16x16x32_bf16 v[52:55], a[180:183], a[196:199], v[52:55]\n\t" \
  "v_mfma_f32_16x16x32_bf16 v[84:87], a[180:183], a[200:203], v[84:87]\n\t" \
  "ds_read_b64_tr_b16 a[90:91], v190 offset:25600\n\t" \
  "v_mfma_f32_16x16x32_bf16 v[84:87], a[20:23], a[216:219], v[84:87]\n\t" \
  "buffer_load_dwordx4 a[116:119], v249, %[ws], s97 offen sc1\n\t" \
  "v_mfma_f32_16x16x32_bf16 v[116:119], a[20:23], a[220:223], v[116:119]\n\t" \
  "ds_read_b64_tr_b16 a[92:93], v191 offset:24576\n\t" \
  "v_mfma_f32_16x16x32_bf16 v[116:119], a[180:183], a[204:207], v[116:119]\n\t" \
  "ds_read_b64_tr_b16 a[94:95], v191 offset:25600\n\t" \
  "v_mfma_f32_16x16x32_bf16 v[24:27], a[184:187], a[192:195], v[24:27]\n\t" \
  "v_mfma_f32_16x16x32_bf16 v[24:27], a[24:27], a[208:211], v[24:27]\n\t" \
  "v_mfma_f32_16x16x32_bf16 v[56:59], a[24:27], a[212:215], v[56:59]\n\t" \
  "buffer_load_dwordx4 a[120:123], v250, %[ws], s97 offen sc1\n\t" \
  "v_mfma_f32_16x16x32_bf16 v[56:59], a[184:187], a[196:199], v[56:59]\n\t" \
  "v_mfma_f32_16x16x32_bf16 v[88:91], a[184:187], a[200:203], v[88:91]\n\t" \
  "v_mfma_f32_16x16x32_bf16 v[88:91], a[24:27], a[216:219], v[88:91]\n\t" \
  "v_mfma_f32_16x16x32_bf16 v[120:123], a[24:27], a[220:223], v[120:123]\n\t" \
  "v_mfma_f32_16x16x32_bf16 v[120:123], a[184:187], a[204:207], v[120:123]\n\t" \
  "buffer_load_dwordx4 a[124:127], v251, %[ws], s97 offen sc1\n\t" \
  "v_mfma_f32_16x16x32_bf16 v[28:31], a[188:191], a[192:195], v[28:31]\n\t" \
  "v_mfma_f32_16x16x32_bf16 v[28:31], a[28:31], a[208:211], v[28:31]\n\t" \
  "v_mfma_f32_16x16x32_bf16 v[60:63], a[28:31], a[212:215], v[60:63]\n\t" \
  "v_mfma_f32_16x16x32_bf16 v[60:63], a[188:191], a[196:199], v[60:63]\n\t" \
  "v_mfma_f32_16x16x32_bf16 v[92:95], a[188:191], a[200:203], v[92:95]\n\t" \
  "v_mfma_f32_16x16x32_bf16 v[92:95], a[28:31], a[216:219], v[92:95]\n\t" \
  "v_mfma_f32_16x16x32_bf16 v[124:127], a[28:31], a[220:223], v[124:127]\n\t" \
  "v_mfma_f32_16x16x32_bf16 v[124:127], a[188:191], a[204:207], v[124:127]\n\t" \
  "s_waitcnt vmcnt(25) lgkmcnt(0)\n\t" \
  "s_barrier\n\t" \
  "v_mfma_f32_16x16x32_bf16 v[0:3], a[128:131], a[224:227], v[0:3]\n\t" \
  "s_add_u32 s96, %[rot19], 7864320\n\t" \
  "ds_read_b64_tr_b16 a[160:161], v184 offset:32768\n\t" \
  "v_mfma_f32_16x16x32_bf16 v[0:3], a[64:67], a[240:243], v[0:3]\n\t" \
  "s_and_b32 s96, s96, 0x780000\n\t" \
  "v_mfma_f32_16x16x32_bf16 v[32:35], a[64:67], a[244:247], v[32:35]\n\t" \
  "s_add_u32 m0, %[mw], 98304\n\t" \
  "ds_read_b64_tr_b16 a[162:163], v184 offset:33792\n\t" \
  "v_mfma_f32_16x16x32_bf16 v[32:35], a[128:131], a[228:231], v[32:35]\n\t" \
  "buffer_load_dwordx4 v200, %[sw], s96 offen offset:0 lds\n\t" \
  "v_mfma_f32_16x16x32_bf16 v[64:67], a[128:131], a[232:235], v[64:67]\n\t" \
  "buffer_load_dwordx4 v201, %[sw], s96 offen offset:1024 lds\n\t" \
  "v_mfma_f32_16x16x32_bf16 v[64:67], a[64:67], a[248:251], v[64:67]\n\t" \
  "ds_read_b64_tr_b16 a[164:165], v185 offset:32768\n\t" \
  "v_mfma_f32_16x16x32_bf16 v[96:99], a[64:67], a[252:255], v[96:99]\n\t" \
  "ds_read_b64_tr_b16 a[166:167], v185 offset:33792\n\t" \
  "v_mfma_f32_16x16x32_bf16 v[96:99], a[128:131], a[236:239], v[96:99]\n\t" \
  "ds_read_b64_tr_b16 a[168:169], v186 offset:32768\n\t" \
  "v_mfma_f32_16x16x32_bf16 v[4:7], a[132:135], a[224:227], v[4:7]\n\t" \
  "ds_read_b64_tr_b16 a[170:171], v186 offset:33792\n\t" \
  "v_mfma_f32_16x16x32_bf16 v[4:7], a[68:71], a[240:243], v[4:7]\n\t" \
  "buffer_load_dwordx4 v202, %[sw], s96 offen offset:2048 lds\n\t" \
  "v_mfma_f32_16x16x32_bf16 v[36:39], a[68:71], a[244:247], v[36:39]\n\t" \
  "ds_read_b64_tr_b16 a[172:173], v187 offset:32768\n\t" \
  "v_mfma_f32_16x16x32_bf16 v[36:39], a[132:135], a[228:231], v[36:39]\n\t" \
  "ds_read_b64_tr_b16 a[174:175], v187 offset:33792\n\t" \
  "v_mfma_f32_16x16x32_bf16 v[68:71], a[132:135], a[232:235], v[68:71]\n\t" \
  "ds_read_b64_tr_b16 a[176:177], v188 offset:32768\n\t" \
  "v_mfma_f32_16x16x32_bf16 v[68:71], a[68:71], a[248:251], v[68:71]\n\t" \
  "buffer_load_dwordx4 v203, %[sw], s96 offen offset:3072 lds\n\t" \
  "v_mfma_f32_16x16x32_bf16 v[100:103], a[68:71], a[252:255], v[100:103]\n\t" \
  "s_add_u32 s97, %[rot19], 7340032\n\t" \
  "ds_read_b64_tr_b16 a[178:179], v188 offset:33792\n\t" \
  "v_mfma_f32_16x16x32_bf16 v[100:103], a[132:135], a[236:239], v[100:103]\n\t" \
  "s_bfe_u32 s97, s97, 0x140003\n\t" \
  "ds_read_b64_tr_b16 a[180:181], v189 offset:32768\n\t" \
  "v_mfma_f32_16x16x32_bf16 v[8:11], a[136:139], a[224:227], v[8:11]\n\t" \
  "s_add_u32 s97, s97, %[sox]\n\t" \
  "v_mfma_f32_16x16x32_bf16 v[8:11], a[72:75], a[240:243], v[8:11]\n\t" \
  "ds_read_b64_tr_b16 a[182:183], v189 offset:33792\n\t" \
  "v_mfma_f32_16x16x32_bf16 v[40:43], a[72:75], a[244:247], v[40:43]\n\t" \
  "buffer_load_dwordx4 a[192:195], v248, %[ws], s97 offen sc1\n\t" \
  "v_mfma_f32_16x16x32_bf16 v[40:43], a[136:139], a[228:231], v[40:43]\n\t" \
  "ds_read_b64_tr_b16 a[184:185], v190 offset:32768\n\t" \
  "v_mfma_f32_16x16x32_bf16 v[72:75], a[136:139], a[232:235], v[72:75]\n\t" \
  "ds_read_b64_tr_b16 a[186:187], v190 offset:33792\n\t" \
  "v_mfma_f32_16x16x32_bf16 v[72:75], a[72:75], a[248:251], v[72:75]\n\t" \
  "ds_read_b64_tr_b16 a[188:189], v191 offset:32768\n\t" \
  "v_mfma_f32_16x16x32_bf16 v[104:107], a[72:75], a[252:255], v[104:107]\n\t" \
  "v_mfma_f32_16x16x32_bf16 v[104:107], a[136:139], a[236:239], v[104:107]\n\t" \
  "buffer_load_dwordx4 a[196:199], v249, %[ws], s97 offen sc1\n\t" \
  "v_mfma_f32_16x16x32_bf16 v[12:15], a[140:143], a[224:227], v[12:15]\n\t" \
  "ds_read_b64_tr_b16 a[190:191], v191 offset:33792\n\t" \
  "v_mfma_f32_16x16x32_bf16 v[12:15], a[76:79], a[240:243], v[12:15]\n\t" \
  "ds_read_b64_tr_b16 a[0:1], v184 offset:40960\n\t" \
  "v_mfma_f32_16x16x32_bf16 v[44:47], a[76:79], a[244:247], v[44:47]\n\t" \
  "ds_read_b64_tr_b16 a[2:3], v184 offset:41984\n\t" \
  "v_mfma_f32_16x16x32_bf16 v[44:47], a[140:143], a[228:231], v[44:47]\n\t" \
  "buffer_load_dwordx4 a[200:203], v250, %[ws], s97 offen sc1\n\t" \
  "v_mfma_f32_16x16x32_bf16 v[76:79], a[140:143], a[232:235], v[76:79]\n\t" \
  "ds_read_b64_tr_b16 a[4:5], v185 offset:40960\n\t" \
  "v_mfma_f32_16x16x32_bf16 v[76:79], a[76:79], a[248:251], v[76:79]\n\t" \
  "ds_read_b64_tr_b16 a[6:7], v185 offset:41984\n\t" \
  "v_mfma_f32_16x16x32_bf16 v[108:111], a[76:79], a[252:255], v[108:111]\n\t" \
  "ds_read_b64_tr_b16 a[8:9], v186 offset:40960\n\t" \
  "v_mfma_f32_16x16x32_bf16 v[108:111], a[140:143], a[236:239], v[108:111]\n\t" \
  "v_mfma_f32_16x16x32_bf16 v[16:19], a[144:147], a[224:227], v[16:19]\n\t" \
  "buffer_load_dwordx4 a[204:207], v251, %[ws], s97 offen sc1\n\t" \
  "v_mfma_f32_16x16x32_bf16 v[16:19], a[80:83], a[240:243], v[16:19]\n\t" \
  "s_add_u32 s99, %[pf1], 6144\n\t" \
  "ds_read_b64_tr_b16 a[10:11], v186 offset:41984\n\t" \
  "v_mfma_f32_16x16x32_bf16 v[48:51], a[80:83], a[244:247], v[48:51]\n\t" \
  "ds_read_b64_tr_b16 a[12:13], v187 offset:40960\n\t" \
  "v_mfma_f32_16x16x32_bf16 v[48:51], a[144:147], a[228:231], v[48:51]\n\t" \
  ".Lhs4:\n\ts_cbranch_execz .Lhs4n\n\t" \
  "ds_read_b64_tr_b16 a[14:15], v187 offset:41984\n\t" \
  "v_mfma_f32_16x16x32_bf16 v[80:83], a[144:147], a[232:235], v[80:83]\n\t" \
  "buffer_load_dword %[pf], v217, %[wo], s99 offen\n\t" \
  "v_mfma_f32_16x16x32_bf16 v[80:83], a[80:83], a[248:251], v[80:83]\n\t" \
  "s_add_u32 s97, s97, %[dk]\n\t" \
  "ds_read_b64_tr_b16 a[16:17], v188 offset:40960\n\t" \
  "v_mfma_f32_16x16x32_bf16 v[112:115], a[80:83], a[252:255], v[112:115]\n\t" \
  "ds_read_b64_tr_b16 a[18:19], v188 offset:41984\n\t" \
  "v_mfma_f32_16x16x32_bf16 v[112:115], a[144:147], a[236:239], v[112:115]\n\t" \
  "ds_read_b64_tr_b16 a[20:21], v189 offset:40960\n\t" \
  "v_mfma_f32_16x16x32_bf16 v[20:23], a[148:151], a[224:227], v[20:23]\n\t" \
  "v_mfma_f32_16x16x32_bf16 v[20:23], a[84:87], a[240:243], v[20:23]\n\t" \
  "buffer_load_dwordx4 a[208:211], v248, %[ws], s97 offen sc1\n\t" \
  "v_mfma_f32_16x16x32_bf16 v[52:55], a[84:87], a[244:247], v[52:55]\n\t" \
  "ds_read_b64_tr_b16 a[22:23], v189 offset:41984\n\t" \
  "v_mfma_f32_16x16x32_bf16 v[52:55], a[148:151], a[228:231], v[52:55]\n\t" \
  "ds_read_b64_tr_b16 a[24:25], v190 offset:40960\n\t" \
  "v_mfma_f32_16x16x32_bf16 v[84:87], a[148:151], a[232:235], v[84:87]\n\t" \
  "ds_read_b64_tr_b16 a[26:27], v190 offset:41984\n\t" \
  "v_mfma_f32_16x16x32_bf16 v[84:87], a[84:87], a[248:251], v[84:87]\n\t" \
  "ds_read_b64_tr_b16 a[28:29], v191 offset:40960\n\t" \
  "v_mfma_f32_16x16x32_bf16 v[116:119], a[84:87], a[252:255], v[116:119]\n\t" \
  "buffer_load_dwordx4 a[212:215], v249, %[ws], s97 offen sc1\n\t" \
  "v_mfma_f32_16x16x32_bf16 v[116:119], a[148:151], a[236:239], v[116:119]\n\t" \
  "ds_read_b64_tr_b16 a[30:31], v191 offset:41984\n\t" \
  "v_mfma_f32_16x16x32_bf16 v[24:27], a[152:155], a[224:227], v[24:27]\n\t" \
  "v_mfma_f32_16x16x32_bf16 v[24:27], a[88:91], a[240:243], v[24:27]\n\t" \
  "v_mfma_f32_16x16x32_bf16 v[56:59], a[88:91], a[244:247], v[56:59]\n\t" \
  "buffer_load_dwordx4 a[216:219], v250, %[ws], s97 offen sc1\n\t" \
  "v_mfma_f32_16x16x32_bf16 v[56:59], a[152:155], a[228:231], v[56:59]\n\t" \
  "v_mfma_f32_16x16x32_bf16 v[88:91], a[152:155], a[232:235], v[88:91]\n\t" \
  "v_mfma_f32_16x16x32_bf16 v[88:91], a[88:91], a[248:251], v[88:91]\n\t" \
  "v_mfma_f32_16x16x32_bf16 v[120:123], a[88:91], a[252:255], v[120:123]\n\t" \
  "v_mfma_f32_16x16x32_bf16 v[120:123], a[152:155], a[236:239], v[120:123]\n\t" \
  "buffer_load_dwordx4 a[220:223], v251, %[ws], s97 offen sc1\n\t" \
  "v_mfma_f32_16x16x32_bf16 v[28:31], a[156:159], a[224:227], v[28:31]\n\t" \
  "v_mfma_f32_16x16x32_bf16 v[28:31], a[92:95], a[240:243], v[28:31]\n\t" \
  "v_mfma_f32_16x16x32_bf16 v[60:63], a[92:95], a[244:247], v[60:63]\n\t" \
  "v_mfma_f32_16x16x32_bf16 v[60:63], a[156:159], a[228:231], v[60:63]\n\t" \
  "v_mfma_f32_16x16x32_bf16 v[92:95], a[156:159], a[232:235], v[92:95]\n\t" \
  "v_mfma_f32_16x16x32_bf16 v[92:95], a[92:95], a[248:251], v[92:95]\n\t" \
  "v_mfma_f32_16x16x32_bf16 v[124:127], a[92:95], a[252:255], v[124:127]\n\t" \
  "v_mfma_f32_16x16x32_bf16 v[124:127], a[156:159], a[236:239], v[124:127]\n\t" \
  "s_waitcnt vmcnt(26) lgkmcnt(0)\n\t" \
  "s_barrier\n\t" \
  "v_mfma_f32_16x16x32_bf16 v[0:3], a[160:163], a[32:35], v[0:3]\n\t" \
  "s_add_u32 s97, %[rot19], 7864320\n\t" \
  "ds_read_b64_tr_b16 a[128:129], v184 offset:49152\n\t" \
  "v_mfma_f32_16x16x32_bf16 v[0:3], a[0:3], a[48:51], v[0:3]\n\t" \
  "s_bfe_u32 s97, s97, 0x140003\n\t" \
  "v_mfma_f32_16x16x32_bf16 v[32:35], a[0:3], a[52:55], v[32:35]\n\t" \
  "s_add_u32 s97, s97, %[sox]\n\t" \
  "ds_read_b64_tr_b16 a[130:131], v184 offset:50176\n\t" \
  "v_mfma_f32_16x16x32_bf16 v[32:35], a[160:163], a[36:39], v[32:35]\n\t" \
  "buffer_load_dwordx4 a[224:227], v248, %[ws], s97 offen sc1\n\t" \
  "v_mfma_f32_16x16x32_bf16 v[64:67], a[160:163], a[40:43], v[64:67]\n\t" \
  "ds_read_b64_tr_b16 a[132:133], v185 offset:49152\n\t" \
  "v_mfma_f32_16x16x32_bf16 v[64:67], a[0:3], a[56:59], v[64:67]\n\t" \
  "ds_read_b64_tr_b16 a[134:135], v185 offset:50176\n\t" \
  "v_mfma_f32_16x16x32_bf16 v[96:99], a[0:3], a[60:63], v[96:99]\n\t" \
  "buffer_load_dwordx4 a[228:231], v249, %[ws], s97 offen sc1\n\t" \
  "v_mfma_f32_16x16x32_bf16 v[96:99], a[160:163], a[44:47], v[96:99]\n\t" \
  "ds_read_b64_tr_b16 a[136:137], v186 offset:49152\n\t" \
  "v_mfma_f32_16x16x32_bf16 v[4:7], a[164:167], a[32:35], v[4:7]\n\t" \
  "ds_read_b64_tr_b16 a[138:139], v186 offset:50176\n\t" \
  "v_mfma_f32_16x16x32_bf16 v[4:7], a[4:7], a[48:51], v[4:7]\n\t" \
  "ds_read_b64_tr_b16 a[140:141], v187 offset:49152\n\t" \
  "v_mfma_f32_16x16x32_bf16 v[36:39], a[4:7], a[52:55], v[36:39]\n\t" \
  "v_mfma_f32_16x16x32_bf16 v[36:39], a[164:167], a[36:39], v[36:39]\n\t" \
  "ds_read_b64_tr_b16 a[142:143], v187 offset:50176\n\t" \
  "v_mfma_f32_16x16x32_bf16 v[68:71], a[164:167], a[40:43], v[68:71]\n\t" \
  "buffer_load_dwordx4 a[232:235], v250, %[ws], s97 offen sc1\n\t" \
  "v_mfma_f32_16x16x32_bf16 v[68:71], a[4:7], a[56:59], v[68:71]\n\t" \
  "ds_read_b64_tr_b16 a[144:145], v188 offset:49152\n\t" \
  "v_mfma_f32_16x16x32_bf16 v[100:103], a[4:7], a[60:63], v[100:103]\n\t" \
  "ds_read_b64_tr_b16 a[146:147], v188 offset:50176\n\t" \
  "v_mfma_f32_16x16x32_bf16 v[100:103], a[164:167], a[44:47], v[100:103]\n\t" \
  "ds_read_b64_tr_b16 a[148:149], v189 offset:49152\n\t" \
  "v_mfma_f32_16x16x32_bf16 v[8:11], a[168:171], a[32:35], v[8:11]\n\t" \
  "v_mfma_f32_16x16x32_bf16 v[8:11], a[8:11], a[48:51], v[8:11]\n\t" \
  "ds_read_b64_tr_b16 a[150:151], v189 offset:50176\n\t" \
  "v_mfma_f32_16x16x32_bf16 v[40:43], a[8:11], a[52:55], v[40:43]\n\t" \
  "buffer_load_dwordx4 a[236:239], v251, %[ws], s97 offen sc1\n\t" \
  "v_mfma_f32_16x16x32_bf16 v[40:43], a[168:171], a[36:39], v[40:43]\n\t" \
  "ds_read_b64_tr_b16 a[152:153], v190 offset:49152\n\t" \
  "v_mfma_f32_16x16x32_bf16 v[72:75], a[168:171], a[40:43], v[72:75]\n\t" \
  "s_add_u32 s97, s97, %[dk]\n\t" \
  "ds_read_b64_tr_b16 a[154:155], v190 offset:50176\n\t" \
  "v_mfma_f32_16x16x32_bf16 v[72:75], a[8:11], a[56:59], v[72:75]\n\t" \
  "ds_read_b64_tr_b16 a[156:157], v191 offset:49152\n\t" \
  "v_mfma_f32_16x16x32_bf16 v[104:107], a[8:11], a[60:63], v[104:107]\n\t" \
  "v_mfma_f32_16x16x32_bf16 v[104:107], a[168:171], a[44:47], v[104:107]\n\t" \
  "ds_read_b64_tr_b16 a[158:159], v191 offset:50176\n\t" \
  "v_mfma_f32_16x16x32_bf16 v[12:15], a[172:175], a[32:35], v[12:15]\n\t" \
  "buffer_load_dwordx4 a[240:243], v248, %[ws], s97 offen sc1\n\t" \
  "v_mfma_f32_16x16x32_bf16 v[12:15], a[12:15], a[48:51], v[12:15]\n\t" \
  "ds_read_b64_tr_b16 a[64:65], v184 offset:57344\n\t" \
  "v_mfma_f32_16x16x32_bf16 v[44:47], a[12:15], a[52:55], v[44:47]\n\t" \
  "ds_read_b64_tr_b16 a[66:67], v184 offset:58368\n\t" \
  "v_mfma_f32_16x16x32_bf16 v[44:47], a[172:175], a[36:39], v[44:47]\n\t" \
  "ds_read_b64_tr_b16 a[68:69], v185 offset:57344\n\t" \
  "v_mfma_f32_16x16x32_bf16 v[76:79], a[172:175], a[40:43], v[76:79]\n\t" \
  "v_mfma_f32_16x16x32_bf16 v[76:79], a[12:15], a[56:59], v[76:79]\n\t" \
  "ds_read_b64_tr_b16 a[70:71], v185 offset:58368\n\t" \
  "v_mfma_f32_16x16x32_bf16 v[108:111], a[12:15], a[60:63], v[108:111]\n\t" \
  "buffer_load_dwordx4 a[244:247], v249, %[ws], s97 offen sc1\n\t" \
  "v_mfma_f32_16x16x32_bf16 v[108:111], a[172:175], a[44:47], v[108:111]\n\t" \
  "ds_read_b64_tr_b16 a[72:73], v186 offset:57344\n\t" \
  "v_mfma_f32_16x16x32_bf16 v[16:19], a[176:179], a[32:35], v[16:19]\n\t" \
  "ds_read_b64_tr_b16 a[74:75], v186 offset:58368\n\t" \
  "v_mfma_f32_16x16x32_bf16 v[16:19], a[16:19], a[48:51], v[16:19]\n\t" \
  "ds_read_b64_tr_b16 a[76:77], v187 offset:57344\n\t" \
  "v_mfma_f32_16x16x32_bf16 v[48:51], a[16:19], a[52:55], v[48:51]\n\t" \
  "v_mfma_f32_16x16x32_bf16 v[48:51], a[176:179], a[36:39], v[48:51]\n\t" \
  "ds_read_b64_tr_b16 a[78:79], v187 offset:58368\n\t" \
  "v_mfma_f32_16x16x32_bf16 v[80:83], a[176:179], a[40:43], v[80:83]\n\t" \
  "buffer_load_dwordx4 a[248:251], v250, %[ws], s97 offen sc1\n\t" \
  "v_mfma_f32_16x16x32_bf16 v[80:83], a[16:19], a[56:59], v[80:83]\n\t" \
  "ds_read_b64_tr_b16 a[80:81], v188 offset:57344\n\t" \
  "v_mfma_f32_16x16x32_bf16 v[112:115], a[16:19], a[60:63], v[112:115]\n\t" \
  "ds_read_b64_tr_b16 a[82:83], v188 offset:58368\n\t" \
  "v_mfma_f32_16x16x32_bf16 v[112:115], a[176:179], a[44:47], v[112:115]\n\t" \
  "ds_read_b64_tr_b16 a[84:85], v189 offset:57344\n\t" \
  "v_mfma_f32_16x16x32_bf16 v[20:23], a[180:183], a[32:35], v[20:23]\n\t" \
  "v_mfma_f32_16x16x32_bf16 v[20:23], a[20:23], a[48:51], v[20:23]\n\t" \
  "ds_read_b64_tr_b16 a[86:87], v189 offset:58368\n\t" \
  "v_mfma_f32_16x16x32_bf16 v[52:55], a[20:23], a[52:55], v[52:55]\n\t" \
  "buffer_load_dwordx4 a[252:255], v251, %[ws], s97 offen sc1\n\t" \
  "v_mfma_f32_16x16x32_bf16 v[52:55], a[180:183], a[36:39], v[52:55]\n\t" \
  "ds_read_b64_tr_b16 a[88:89], v190 offset:57344\n\t" \
  "v_mfma_f32_16x16x32_bf16 v[84:87], a[180:183], a[40:43], v[84:87]\n\t" \
  "ds_read_b64_tr_b16 a[90:91], v190 offset:58368\n\t" \
  "v_mfma_f32_16x16x32_bf16 v[84:87], a[20:23], a[56:59], v[84:87]\n\t" \
  "ds_read_b64_tr_b16 a[92:93], v191 offset:57344\n\t" \
  "v_mfma_f32_16x16x32_bf16 v[116:119], a[20:23], a[60:63], v[116:119]\n\t" \
  "v_mfma_f32_16x16x32_bf16 v[116:119], a[180:183], a[44:47], v[116:119]\n\t" \
  "ds_read_b64_tr_b16 a[94:95], v191 offset:58368\n\t" \
  "v_mfma_f32_16x16x32_bf16 v[24:27], a[184:187], a[32:35], v[24:27]\n\t" \
  "buffer_load_dword %[pf], v217, %[ws], %[pto] offen sc1\n\t" \
  "v_mfma_f32_16x16x32_bf16 v[24:27], a[24:27], a[48:51], v[24:27]\n\t" \
  "v_mfma_f32_16x16x32_bf16 v[56:59], a[24:27], a[52:55], v[56:59]\n\t" \
  "s_mul_i32 s99, %[sofk], 5\n\t" \
  "v_mfma_f32_16x16x32_bf16 v[56:59], a[184:187], a[36:39], v[56:59]\n\t" \
  "s_xor_b32 s99, s99, 0\n\t" \
  "v_mfma_f32_16x16x32_bf16 v[88:91], a[184:187], a[40:43], v[88:91]\n\t" \
  "s_lshr_b32 s99, s99, 1\n\t" \
  "v_mfma_f32_16x16x32_bf16 v[88:91], a[24:27], a[56:59], v[88:91]\n\t" \
  "v_mfma_f32_16x16x32_bf16 v[120:123], a[24:27], a[60:63], v[120:123]\n\t" \
  "buffer_load_dword %[pf], v217, %[sD], s99 offen\n\t" \
  "v_mfma_f32_16x16x32_bf16 v[120:123], a[184:187], a[44:47], v[120:123]\n\t" \
  "v_mfma_f32_16x16x32_bf16 v[28:31], a[188:191], a[32:35], v[28:31]\n\t" \
  "v_mfma_f32_16x16x32_bf16 v[28:31], a[28:31], a[48:51], v[28:31]\n\t" \
  "v_mfma_f32_16x16x32_bf16 v[60:63], a[28:31], a[52:55], v[60:63]\n\t" \
  "v_mfma_f32_16x16x32_bf16 v[60:63], a[188:191], a[36:39], v[60:63]\n\t" \
  "v_mfma_f32_16x16x32_bf16 v[92:95], a[188:191], a[40:43], v[92:95]\n\t" \
  "v_mfma_f32_16x16x32_bf16 v[92:95], a[28:31], a[56:59], v[92:95]\n\t" \
  "v_mfma_f32_16x16x32_bf16 v[124:127], a[28:31], a[60:63], v[124:127]\n\t" \
  "v_mfma_f32_16x16x32_bf16 v[124:127], a[188:191], a[44:47], v[124:127]\n\t" \
  "s_waitcnt vmcnt(23) lgkmcnt(0)\n\t" \
  "s_barrier\n\t" \
  "v_mfma_f32_16x16x32_bf16 v[0:3], a[128:131], a[96:99], v[0:3]\n\t" \
  "s_mul_i32 s99, %[sofk], 5\n\t" \
  "ds_read_b64_tr_b16 a[160:161], v184 offset:0\n\t" \
  "v_mfma_f32_16x16x32_bf16 v[0:3], a[64:67], a[112:115], v[0:3]\n\t" \
  "s_xor_b32 s99, s99, 4096\n\t" \
  "v_mfma_f32_16x16x32_bf16 v[32:35], a[64:67], a[116:119], v[32:35]\n\t" \
  "s_lshr_b32 s99, s99, 1\n\t" \
  "ds_read_b64_tr_b16 a[162:163], v184 offset:1024\n\t" \
  "v_mfma_f32_16x16x32_bf16 v[32:35], a[128:131], a[100:103], v[32:35]\n\t" \
  "buffer_load_dword %[pf], v217, %[sD], s99 offen\n\t" \
  "v_mfma_f32_16x16x32_bf16 v[64:67], a[128:131], a[104:107], v[64:67]\n\t" \
  "s_add_u32 m0, %[mw], 16384\n\t" \
  "ds_read_b64_tr_b16 a[164:165], v185 offset:0\n\t" \
  "v_mfma_f32_16x16x32_bf16 v[64:67], a[64:67], a[120:123], v[64:67]\n\t" \
  "s_add_u32 s96, %[sofk], 0\n\t" \
  "ds_read_b64_tr_b16 a[166:167], v185 offset:1024\n\t" \
  "v_mfma_f32_16x16x32_bf16 v[96:99], a[64:67], a[124:127], v[96:99]\n\t" \
  "ds_read_b64_tr_b16 a[168:169], v186 offset:0\n\t" \
  "v_mfma_f32_16x16x32_bf16 v[96:99], a[128:131], a[108:111], v[96:99]\n\t" \
  "v_mfma_f32_16x16x32_bf16 v[4:7], a[132:135], a[96:99], v[4:7]\n\t" \
  "buffer_load_dwordx4 v224, %[sK], s96 offen offset:0 lds\n\t" \
  "v_mfma_f32_16x16x32_bf16 v[4:7], a[68:71], a[112:115], v[4:7]\n\t" \
  "ds_read_b64_tr_b16 a[170:171], v186 offset:1024\n\t" \
  "v_mfma_f32_16x16x32_bf16 v[36:39], a[68:71], a[116:119], v[36:39]\n\t" \
  "ds_read_b64_tr_b16 a[172:173], v187 offset:0\n\t" \
  "v_mfma_f32_16x16x32_bf16 v[36:39], a[132:135], a[100:103], v[36:39]\n\t" \
  "ds_read_b64_tr_b16 a[174:175], v187 offset:1024\n\t" \
  "v_mfma_f32_16x16x32_bf16 v[68:71], a[132:135], a[104:107], v[68:71]\n\t" \
  "ds_read_b64_tr_b16 a[176:177], v188 offset:0\n\t" \
  "v_mfma_f32_16x16x32_bf16 v[68:71], a[68:71], a[120:123], v[68:71]\n\t" \
  "v_mfma_f32_16x16x32_bf16 v[100:103], a[68:71], a[124:127], v[100:103]\n\t" \
  "ds_read_b64_tr_b16 a[178:179], v188 offset:1024\n\t" \
  "v_mfma_f32_16x16x32_bf16 v[100:103], a[132:135], a[108:111], v[100:103]\n\t" \
  "ds_read_b64_tr_b16 a[180:181], v189 offset:0\n\t" \
  "v_mfma_f32_16x16x32_bf16 v[8:11], a[136:139], a[96:99], v[8:11]\n\t" \
  "v_mfma_f32_16x16x32_bf16 v[8:11], a[72:75], a[112:115], v[8:11]\n\t" \
  "buffer_load_dwordx4 v225, %[sK], s96 offen offset:1024 lds\n\t" \
  "v_mfma_f32_16x16x32_bf16 v[40:43], a[72:75], a[116:119], v[40:43]\n\t" \
  "ds_read_b64_tr_b16 a[182:183], v189 offset:1024\n\t" \
  "v_mfma_f32_16x16x32_bf16 v[40:43], a[136:139], a[100:103], v[40:43]\n\t" \
  "ds_read_b64_tr_b16 a[184:185], v190 offset:0\n\t" \
  "v_mfma_f32_16x16x32_bf16 v[72:75], a[136:139], a[104:107], v[72:75]\n\t" \
  "ds_read_b64_tr_b16 a[186:187], v190 offset:1024\n\t" \
  "v_mfma_f32_16x16x32_bf16 v[72:75], a[72:75], a[120:123], v[72:75]\n\t" \
  "ds_read_b64_tr_b16 a[188:189], v191 offset:0\n\t" \
  "v_mfma_f32_16x16x32_bf16 v[104:107], a[72:75], a[124:127], v[104:107]\n\t" \
  "v_mfma_f32_16x16x32_bf16 v[104:107], a[136:139], a[108:111], v[104:107]\n\t" \
  "ds_read_b64_tr_b16 a[190:191], v191 offset:1024\n\t" \
  "v_mfma_f32_16x16x32_bf16 v[12:15], a[140:143], a[96:99], v[12:15]\n\t" \
  "ds_read_b64_tr_b16 a[0:1], v184 offset:8192\n\t" \
  "v_mfma_f32_16x16x32_bf16 v[12:15], a[76:79], a[112:115], v[12:15]\n\t" \
  "buffer_load_dwordx4 v226, %[sK], s96 offen offset:2048 lds\n\t" \
  "v_mfma_f32_16x16x32_bf16 v[44:47], a[76:79], a[116:119], v[44:47]\n\t" \
  "ds_read_b64_tr_b16 a[2:3], v184 offset:9216\n\t" \
  "v_mfma_f32_16x16x32_bf16 v[44:47], a[140:143], a[100:103], v[44:47]\n\t" \
  "ds_read_b64_tr_b16 a[4:5], v185 offset:8192\n\t" \
  "v_mfma_f32_16x16x32_bf16 v[76:79], a[140:143], a[104:107], v[76:79]\n\t" \
  "v_mfma_f32_16x16x32_bf16 v[76:79], a[76:79], a[120:123], v[76:79]\n\t" \
  "ds_read_b64_tr_b16 a[6:7], v185 offset:9216\n\t" \
  "v_mfma_f32_16x16x32_bf16 v[108:111], a[76:79], a[124:127], v[108:111]\n\t" \
  "ds_read_b64_tr_b16 a[8:9], v186 offset:8192\n\t" \
  "v_mfma_f32_16x16x32_bf16 v[108:111], a[140:143], a[108:111], v[108:111]\n\t" \
  "v_mfma_f32_16x16x32_bf16 v[16:19], a[144:147], a[96:99], v[16:19]\n\t" \
  "ds_read_b64_tr_b16 a[10:11], v186 offset:9216\n\t" \
  "v_mfma_f32_16x16x32_bf16 v[16:19], a[80:83], a[112:115], v[16:19]\n\t" \
  "ds_read_b64_tr_b16 a[12:13], v187 offset:8192\n\t" \
  "v_mfma_f32_16x16x32_bf16 v[48:51], a[80:83], a[116:119], v[48:51]\n\t" \
  "buffer_load_dwordx4 v227, %[sK], s96 offen offset:3072 lds\n\t" \
  "v_mfma_f32_16x16x32_bf16 v[48:51], a[144:147], a[100:103], v[48:51]\n\t" \
  "ds_read_b64_tr_b16 a[14:15], v187 offset:9216\n\t" \
  "v_mfma_f32_16x16x32_bf16 v[80:83], a[144:147], a[104:107], v[80:83]\n\t" \
  "ds_read_b64_tr_b16 a[16:17], v188 offset:8192\n\t" \
  "v_mfma_f32_16x16x32_bf16 v[80:83], a[80:83], a[120:123], v[80:83]\n\t" \
  "v_mfma_f32_16x16x32_bf16 v[112:115], a[80:83], a[124:127], v[112:115]\n\t" \
  "s_add_u32 m0, %[mw], 49152\n\t" \
  "ds_read_b64_tr_b16 a[18:19], v188 offset:9216\n\t" \
  "v_mfma_f32_16x16x32_bf16 v[112:115], a[144:147], a[108:111], v[112:115]\n\t" \
  "s_add_u32 s96, %[sofk], 0\n\t" \
  "ds_read_b64_tr_b16 a[20:21], v189 offset:8192\n\t" \
  "v_mfma_f32_16x16x32_bf16 v[20:23], a[148:151], a[96:99], v[20:23]\n\t" \
  "v_mfma_f32_16x16x32_bf16 v[20:23], a[84:87], a[112:115], v[20:23]\n\t" \
  "ds_read_b64_tr_b16 a[22:23], v189 offset:9216\n\t" \
  "v_mfma_f32_16x16x32_bf16 v[52:55], a[84:87], a[116:119], v[52:55]\n\t" \
  "buffer_load_dwordx4 v230, %[sV], s96 offen offset:0 lds\n\t" \
  "v_mfma_f32_16x16x32_bf16 v[52:55], a[148:151], a[100:103], v[52:55]\n\t" \
  "ds_read_b64_tr_b16 a[24:25], v190 offset:8192\n\t" \
  "v_mfma_f32_16x16x32_bf16 v[84:87], a[148:151], a[104:107], v[84:87]\n\t" \
  "ds_read_b64_tr_b16 a[26:27], v190 offset:9216\n\t" \
  "v_mfma_f32_16x16x32_bf16 v[84:87], a[84:87], a[120:123], v[84:87]\n\t" \
  "ds_read_b64_tr_b16 a[28:29], v191 offset:8192\n\t" \
  "v_mfma_f32_16x16x32_bf16 v[116:119], a[84:87], a[124:127], v[116:119]\n\t" \
  "v_mfma_f32_16x16x32_bf16 v[116:119], a[148:151], a[108:111], v[116:119]\n\t" \
  "ds_read_b64_tr_b16 a[30:31], v191 offset:9216\n\t" \
  "v_mfma_f32_16x16x32_bf16 v[24:27], a[152:155], a[96:99], v[24:27]\n\t" \
  "v_mfma_f32_16x16x32_bf16 v[24:27], a[88:91], a[112:115], v[24:27]\n\t" \
  "v_mfma_f32_16x16x32_bf16 v[56:59], a[88:91], a[116:119], v[56:59]\n\t" \
  "v_mfma_f32_16x16x32_bf16 v[56:59], a[152:155], a[100:103], v[56:59]\n\t" \
  "buffer_load_dwordx4 v231, %[sV], s96 offen offset:1024 lds\n\t" \
  "v_mfma_f32_16x16x32_bf16 v[88:91], a[152:155], a[104:107], v[88:91]\n\t" \
  "v_mfma_f32_16x16x32_bf16 v[88:91], a[88:91], a[120:123], v[88:91]\n\t" \
  "v_mfma_f32_16x16x32_bf16 v[120:123], a[88:91], a[124:127], v[120:123]\n\t" \
  "v_mfma_f32_16x16x32_bf16 v[120:123], a[152:155], a[108:111], v[120:123]\n\t" \
  "v_mfma_f32_16x16x32_bf16 v[28:31], a[156:159], a[96:99], v[28:31]\n\t" \
  "v_mfma_f32_16x16x32_bf16 v[28:31], a[92:95], a[112:115], v[28:31]\n\t" \
  "v_mfma_f32_16x16x32_bf16 v[60:63], a[92:95], a[116:119], v[60:63]\n\t" \
  "v_mfma_f32_16x16x32_bf16 v[60:63], a[156:159], a[100:103], v[60:63]\n\t" \
  "v_mfma_f32_16x16x32_bf16 v[92:95], a[156:159], a[104:107], v[92:95]\n\t" \
  "v_mfma_f32_16x16x32_bf16 v[92:95], a[92:95], a[120:123], v[92:95]\n\t" \
  "v_mfma_f32_16x16x32_bf16 v[124:127], a[92:95], a[124:127], v[124:127]\n\t" \
  "v_mfma_f32_16x16x32_bf16 v[124:127], a[156:159], a[108:111], v[124:127]\n\t" \
  "s_waitcnt vmcnt(17) lgkmcnt(0)\n\t" \
  "s_barrier\n\t" \
  "v_mfma_f32_16x16x32_bf16 v[0:3], a[160:163], a[192:195], v[0:3]\n\t" \
  "buffer_load_dwordx4 v230, %[sV], s96 offen offset:2048 lds\n\t" \
  "v_mfma_f32_16x16x32_bf16 v[0:3], a[0:3], a[208:211], v[0:3]\n\t" \
  "ds_read_b64_tr_b16 a[128:129], v192 offset:0\n\t" \
  "v_mfma_f32_16x16x32_bf16 v[32:35], a[0:3], a[212:215], v[32:35]\n\t" \
  "ds_read_b64_tr_b16 a[130:131], v192 offset:1024\n\t" \
  "v_mfma_f32_16x16x32_bf16 v[32:35], a[160:163], a[196:199], v[32:35]\n\t" \
  "ds_read_b64_tr_b16 a[132:133], v193 offset:0\n\t" \
  "v_mfma_f32_16x16x32_bf16 v[64:67], a[160:163], a[200:203], v[64:67]\n\t" \
  "v_mfma_f32_16x16x32_bf16 v[64:67], a[0:3], a[216:219], v[64:67]\n\t" \
  "ds_read_b64_tr_b16 a[134:135], v193 offset:1024\n\t" \
  "v_mfma_f32_16x16x32_bf16 v[96:99], a[0:3], a[220:223], v[96:99]\n\t" \
  "ds_read_b64_tr_b16 a[136:137], v194 offset:0\n\t" \
  "v_mfma_f32_16x16x32_bf16 v[96:99], a[160:163], a[204:207], v[96:99]\n\t" \
  "buffer_load_dwordx4 v231, %[sV], s96 offen offset:3072 lds\n\t" \
  "v_mfma_f32_16x16x32_bf16 v[4:7], a[164:167], a[192:195], v[4:7]\n\t" \
  "ds_read_b64_tr_b16 a[138:139], v194 offset:1024\n\t" \
  "v_mfma_f32_16x16x32_bf16 v[4:7], a[4:7], a[208:211], v[4:7]\n\t" \
  "ds_read_b64_tr_b16 a[140:141], v195 offset:0\n\t" \
  "v_mfma_f32_16x16x32_bf16 v[36:39], a[4:7], a[212:215], v[36:39]\n\t" \
  "v_mfma_f32_16x16x32_bf16 v[36:39], a[164:167], a[196:199], v[36:39]\n\t" \
  "s_add_u32 m0, %[mw], 32768\n\t" \
  "ds_read_b64_tr_b16 a[142:143], v195 offset:1024\n\t" \
  "v_mfma_f32_16x16x32_bf16 v[68:71], a[164:167], a[200:203], v[68:71]\n\t" \
  "s_add_u32 s96, %[sofk], 16384\n\t" \
  "ds_read_b64_tr_b16 a[144:145], v196 offset:0\n\t" \
  "v_mfma_f32_16x16x32_bf16 v[68:71], a[4:7], a[216:219], v[68:71]\n\t" \
  "v_mfma_f32_16x16x32_bf16 v[100:103], a[4:7], a[220:223], v[100:103]\n\t" \
  "ds_read_b64_tr_b16 a[146:147], v196 offset:1024\n\t" \
  "v_mfma_f32_16x16x32_bf16 v[100:103], a[164:167], a[204:207], v[100:103]\n\t" \
  "buffer_load_dwordx4 v224, %[sK], s96 offen offset:0 lds\n\t" \
  "v_mfma_f32_16x16x32_bf16 v[8:11], a[168:171], a[192:195], v[8:11]\n\t" \
  "ds_read_b64_tr_b16 a[148:149], v197 offset:0\n\t" \
  "v_mfma_f32_16x16x32_bf16 v[8:11], a[8:11], a[208:211], v[8:11]\n\t" \
  "ds_read_b64_tr_b16 a[150:151], v197 offset:1024\n\t" \
  "v_mfma_f32_16x16x32_bf16 v[40:43], a[8:11], a[212:215], v[40:43]\n\t" \
  "ds_read_b64_tr_b16 a[152:153], v198 offset:0\n\t" \
  "v_mfma_f32_16x16x32_bf16 v[40:43], a[168:171], a[196:199], v[40:43]\n\t" \
  "v_mfma_f32_16x16x32_bf16 v[72:75], a[168:171], a[200:203], v[72:75]\n\t" \
  "ds_read_b64_tr_b16 a[154:155], v198 offset:1024\n\t" \
  "v_mfma_f32_16x16x32_bf16 v[72:75], a[8:11], a[216:219], v[72:75]\n\t" \
  "ds_read_b64_tr_b16 a[156:157], v199 offset:0\n\t" \
  "v_mfma_f32_16x16x32_bf16 v[104:107], a[8:11], a[220:223], v[104:107]\n\t" \
  "buffer_load_dwordx4 v225, %[sK], s96 offen offset:1024 lds\n\t" \
  "v_mfma_f32_16x16x32_bf16 v[104:107], a[168:171], a[204:207], v[104:107]\n\t" \
  "ds_read_b64_tr_b16 a[158:159], v199 offset:1024\n\t" \
  "v_mfma_f32_16x16x32_bf16 v[12:15], a[172:175], a[192:195], v[12:15]\n\t" \
  "ds_read_b64_tr_b16 a[64:65], v192 offset:8192\n\t" \
  "v_mfma_f32_16x16x32_bf16 v[12:15], a[12:15], a[208:211], v[12:15]\n\t" \
  "v_mfma_f32_16x16x32_bf16 v[44:47], a[12:15], a[212:215], v[44:47]\n\t" \
  "ds_read_b64_tr_b16 a[66:67], v192 offset:9216\n\t" \
  "v_mfma_f32_16x16x32_bf16 v[44:47], a[172:175], a[196:199], v[44:47]\n\t" \
  "ds_read_b64_tr_b16 a[68:69], v193 offset:8192\n\t" \
  "v_mfma_f32_16x16x32_bf16 v[76:79], a[172:175], a[200:203], v[76:79]\n\t" \
  "v_mfma_f32_16x16x32_bf16 v[76:79], a[12:15], a[216:219], v[76:79]\n\t" \
  "ds_read_b64_tr_b16 a[70:71], v193 offset:9216\n\t" \
  "v_mfma_f32_16x16x32_bf16 v[108:111], a[12:15], a[220:223], v[108:111]\n\t" \
  "buffer_load_dwordx4 v226, %[sK], s96 offen offset:2048 lds\n\t" \
  "v_mfma_f32_16x16x32_bf16 v[108:111], a[172:175], a[204:207], v[108:111]\n\t" \
  "ds_read_b64_tr_b16 a[72:73], v194 offset:8192\n\t" \
  "v_mfma_f32_16x16x32_bf16 v[16:19], a[176:179], a[192:195], v[16:19]\n\t" \
  "ds_read_b64_tr_b16 a[74:75], v194 offset:9216\n\t" \
  "v_mfma_f32_16x16x32_bf16 v[16:19], a[16:19], a[208:211], v[16:19]\n\t" \
  "ds_read_b64_tr_b16 a[76:77], v195 offset:8192\n\t" \
  "v_mfma_f32_16x16x32_bf16 v[48:51], a[16:19], a[212:215], v[48:51]\n\t" \
  "v_mfma_f32_16x16x32_bf16 v[48:51], a[176:179], a[196:199], v[48:51]\n\t" \
  "ds_read_b64_tr_b16 a[78:79], v195 offset:9216\n\t" \
  "v_mfma_f32_16x16x32_bf16 v[80:83], a[176:179], a[200:203], v[80:83]\n\t" \
  "ds_read_b64_tr_b16 a[80:81], v196 offset:8192\n\t" \
  "v_mfma_f32_16x16x32_bf16 v[80:83], a[16:19], a[216:219], v[80:83]\n\t" \
  "buffer_load_dwordx4 v227, %[sK], s96 offen offset:3072 lds\n\t" \
  "v_mfma_f32_16x16x32_bf16 v[112:115], a[16:19], a[220:223], v[112:115]\n\t" \
  "ds_read_b64_tr_b16 a[82:83], v196 offset:9216\n\t" \
  "v_mfma_f32_16x16x32_bf16 v[112:115], a[176:179], a[204:207], v[112:115]\n\t" \
  "ds_read_b64_tr_b16 a[84:85], v197 offset:8192\n\t" \
  "v_mfma_f32_16x16x32_bf16 v[20:23], a[180:183], a[192:195], v[20:23]\n\t" \
  "v_mfma_f32_16x16x32_bf16 v[20:23], a[20:23], a[208:211], v[20:23]\n\t" \
  "s_add_u32 s99, %[pf4], 0\n\t" \
  "ds_read_b64_tr_b16 a[86:87], v197 offset:9216\n\t" \
  "v_mfma_f32_16x16x32_bf16 v[52:55], a[20:23], a[212:215], v[52:55]\n\t" \
  "ds_read_b64_tr_b16 a[88:89], v198 offset:8192\n\t" \
  "v_mfma_f32_16x16x32_bf16 v[52:55], a[180:183], a[196:199], v[52:55]\n\t" \
  "v_mfma_f32_16x16x32_bf16 v[84:87], a[180:183], a[200:203], v[84:87]\n\t" \
  "ds_read_b64_tr_b16 a[90:91], v198 offset:9216\n\t" \
  "v_mfma_f32_16x16x32_bf16 v[84:87], a[20:23], a[216:219], v[84:87]\n\t" \
  "buffer_load_dword %[pf], v245, %[sU], s99 offen\n\t" \
  "v_mfma_f32_16x16x32_bf16 v[116:119], a[20:23], a[220:223], v[116:119]\n\t" \
  "ds_read_b64_tr_b16 a[92:93], v199 offset:8192\n\t" \
  "v_mfma_f32_16x16x32_bf16 v[116:119], a[180:183], a[204:207], v[116:119]\n\t" \
  "ds_read_b64_tr_b16 a[94:95], v199 offset:9216\n\t" \
  "v_mfma_f32_16x16x32_bf16 v[24:27], a[184:187], a[192:195], v[24:27]\n\t" \
  "s_add_u32 s99, %[pf4], 14336\n\t" \
  "v_mfma_f32_16x16x32_bf16 v[24:27], a[24:27], a[208:211], v[24:27]\n\t" \
  "v_mfma_f32_16x16x32_bf16 v[56:59], a[24:27], a[212:215], v[56:59]\n\t" \
  "v_mfma_f32_16x16x32_bf16 v[56:59], a[184:187], a[196:199], v[56:59]\n\t" \
  "v_mfma_f32_16x16x32_bf16 v[88:91], a[184:187], a[200:203], v[88:91]\n\t" \
  "buffer_load_dword %[pf], v245, %[sU], s99 offen\n\t" \
  "v_mfma_f32_16x16x32_bf16 v[88:91], a[24:27], a[216:219], v[88:91]\n\t" \
  "v_mfma_f32_16x16x32_bf16 v[120:123], a[24:27], a[220:223], v[120:123]\n\t" \
  "v_mfma_f32_16x16x32_bf16 v[120:123], a[184:187], a[204:207], v[120:123]\n\t" \
  "v_mfma_f32_16x16x32_bf16 v[28:31], a[188:191], a[192:195], v[28:31]\n\t" \
  "v_mfma_f32_16x16x32_bf16 v[28:31], a[28:31], a[208:211], v[28:31]\n\t" \
  "v_mfma_f32_16x16x32_bf16 v[60:63], a[28:31], a[212:215], v[60:63]\n\t" \
  "v_mfma_f32_16x16x32_bf16 v[60:63], a[188:191], a[196:199], v[60:63]\n\t" \
  "v_mfma_f32_16x16x32_bf16 v[92:95], a[188:191], a[200:203], v[92:95]\n\t" \
  "v_mfma_f32_16x16x32_bf16 v[92:95], a[28:31], a[216:219], v[92:95]\n\t" \
  "v_mfma_f32_16x16x32_bf16 v[124:127], a[28:31], a[220:223], v[124:127]\n\t" \
  "v_mfma_f32_16x16x32_bf16 v[124:127], a[188:191], a[204:207], v[124:127]\n\t" \
  "s_waitcnt vmcnt(17)\n\t" \
  "s_waitcnt lgkmcnt(0)\n\t" \
  FSTP("s_waitcnt lgkmcnt(0)\n\t") \
  FSTP("v_writelane_b32 %[stv], s94, 52\n\t") \
  FSTP("v_writelane_b32 %[stv], s92, 53\n\t") \
  ".subsection 1\n\t" \
  ".Lfq_s:\n\t" \
  "s_mov_b32 s97, 0\n\t" \
  ".Lfq_r:\n\t" \
  "buffer_load_dword %[fl], v210, %[ws], %[fo] offen sc1\n\t" \
  "s_waitcnt vmcnt(0)\n\t" \
  "v_cmp_ne_u32 vcc, v211, %[fl]\n\t" \
  "s_cbranch_vccz .Lfq_k\n\t" \
  "s_add_u32 s97, s97, 1\n\t" \
  "s_cmp_lt_u32 s97, " FQ_CAP "\n\t" \
  "s_cbranch_scc1 .Lfq_r\n\t" \
  "buffer_store_dword v211, off, %[ws], 0 offset:256 sc1\n\t" \
  "s_branch .Lfq_k\n\t" \
  ".subsection 0\n\t"
#define FQ_CL "memory", "scc", "vcc", "s96", "s97", "s98", "s99", "v128", "v129", "v130", "v131", "v132", "v133", "v134", "v135", "v136", "v137", "v138", "v139", "v140", "v141", "v142", "v143", "v144", "v145", "v146", "v147", "v148", "v149", "v150", "v151", "v152", "v153", "v154", "v155", "v156", "v157", "v158", "v159", "v176", "v177", "v178", "v179", "v180", "v181", "v182", "v183", "v249", "v250", "v251" FQ_STC
#define FQ_X2E 0
#define FQ_W1L 1
#define FQ_W2E 0
static_assert(W_VOTE == 8192 && W_FLAGR == 32768 && W_FLAGO == 131072 && W_MODB == 2228224 && W_SIL == 2359296 && W_OT == 8388608 && W_XCH == 25165824);
#define FQ_EV 0
#define FQ_SD [sU] "s"(c.sU), [sD] "s"(c.sD),
#define FQ_EB 0
#define FQ_CTO
#define FQ_XD 1
#define STS(x)
#define AT_STV
#define STA(x)
#define AT_ASM \
  "s_cmp_eq_u32 %[pm], 0\n\t" \
  "s_cbranch_scc1 .Lsf_go\n\t" \
  "v_mfma_f32_16x16x32_bf16 v[28:31], a[156:159], a[224:227], v[28:31]\n\t" \
  "v_mov_b32 v216, 0x3f803f80\n\t" \
  "v_lshlrev_b32 v204, 7, v233\n\t" \
  "v_mfma_f32_16x16x32_bf16 v[28:31], a[92:95], a[240:243], v[28:31]\n\t" \
  "v_cmp_gt_u32 vcc, 16, v233\n\t" \
  "v_mov_b32 v217, 0x3f803f80\n\t" \
  "v_mfma_f32_16x16x32_bf16 v[24:27], a[152:155], a[224:227], v[24:27]\n\t" \
  "v_lshrrev_b32 v202, 3, v233\n\t" \
  "v_mov_b32 v218, 0x3f803f80\n\t" \
  "v_mfma_f32_16x16x32_bf16 v[24:27], a[88:91], a[240:243], v[24:27]\n\t" \
  "v_mul_u32_u24 v202, 11264, v202\n\t" \
  "v_mov_b32 v219, 0x3f803f80\n\t" \
  "v_mfma_f32_16x16x32_bf16 v[20:23], a[148:151], a[224:227], v[20:23]\n\t" \
  "v_cvt_pk_bf16_f32 v31, v30, v31\n\t" \
  "v_add_u32 v202, v202, v204\n\t" \
  "v_mfma_f32_16x16x32_bf16 v[20:23], a[84:87], a[240:243], v[20:23]\n\t" \
  "v_cvt_pk_bf16_f32 v30, v28, v29\n\t" \
  "v_lshlrev_b32 v242, 4, v233\n\t" \
  "v_mfma_f32_16x16x32_bf16 v[16:19], a[144:147], a[224:227], v[16:19]\n\t" \
  "s_mov_b32 s99, 0\n\t" \
  "v_bfrev_b32 v195, 1\n\t" \
  "v_cvt_pk_bf16_f32 v28, v24, v25\n\t" \
  "v_mfma_f32_16x16x32_bf16 v[16:19], a[80:83], a[240:243], v[16:19]\n\t" \
  "v_cvt_pk_bf16_f32 v29, v26, v27\n\t" \
  "v_add_u32 v237, 65536, v228\n\t" \
  "v_mfma_f32_16x16x32_bf16 v[12:15], a[140:143], a[224:227], v[12:15]\n\t" \
  "v_cndmask_b32 v204, v195, v204, vcc\n\t" \
  "v_cndmask_b32 v202, v195, v202, vcc\n\t" \
  "v_mfma_f32_16x16x32_bf16 v[12:15], a[76:79], a[240:243], v[12:15]\n\t" \
  "v_cvt_pk_bf16_f32 v27, v22, v23\n\t" \
  "v_add_u32 v238, 65536, v229\n\t" \
  "v_mfma_f32_16x16x32_bf16 v[8:11], a[136:139], a[224:227], v[8:11]\n\t" \
  "s_mov_b32 %[na], 0\n\t" \
  "v_permlane16_swap_b32 v28, v30\n\t" \
  "v_mfma_f32_16x16x32_bf16 v[8:11], a[72:75], a[240:243], v[8:11]\n\t" \
  "v_cvt_pk_bf16_f32 v26, v20, v21\n\t" \
  "v_cvt_pk_bf16_f32 v24, v16, v17\n\t" \
  "v_mfma_f32_16x16x32_bf16 v[4:7], a[132:135], a[224:227], v[4:7]\n\t" \
  "v_permlane16_swap_b32 v29, v31\n\t" \
  "v_mfma_f32_16x16x32_bf16 v[4:7], a[68:71], a[240:243], v[4:7]\n\t" \
  "v_cvt_pk_bf16_f32 v25, v18, v19\n\t" \
  "v_cvt_pk_bf16_f32 v23, v14, v15\n\t" \
  "v_mfma_f32_16x16x32_bf16 v[0:3], a[128:131], a[224:227], v[0:3]\n\t" \
  "v_cvt_pk_bf16_f32 v22, v12, v13\n\t" \
  "v_cvt_pk_bf16_f32 v20, v8, v9\n\t" \
  "v_mfma_f32_16x16x32_bf16 v[0:3], a[64:67], a[240:243], v[0:3]\n\t" \
  "s_waitcnt vmcnt(2)\n\t" \
  "s_barrier\n\t" \
  "v_mfma_f32_16x16x32_bf16 v[60:63], a[156:159], a[228:231], v[60:63]\n\t" \
  "ds_read_b128 a[0:3], v220 offset:0\n\t" \
  "v_cvt_pk_bf16_f32 v21, v10, v11\n\t" \
  "v_mfma_f32_16x16x32_bf16 v[60:63], a[92:95], a[244:247], v[60:63]\n\t" \
  "ds_read_b128 a[4:7], v221 offset:0\n\t" \
  "v_cvt_pk_bf16_f32 v19, v6, v7\n\t" \
  "v_mfma_f32_16x16x32_bf16 v[56:59], a[152:155], a[228:231], v[56:59]\n\t" \
  "s_add_u32 m0, %[m], 0\n\t" \
  "ds_read_b128 a[8:11], v222 offset:0\n\t" \
  "v_cvt_pk_bf16_f32 v18, v4, v5\n\t" \
  "v_mfma_f32_16x16x32_bf16 v[56:59], a[88:91], a[244:247], v[56:59]\n\t" \
  "s_xor_b32 s97, %[wob], 0\n\t" \
  "ds_read_b128 a[12:15], v223 offset:0\n\t" \
  "v_cvt_pk_bf16_f32 v16, v0, v1\n\t" \
  "v_mfma_f32_16x16x32_bf16 v[52:55], a[148:151], a[228:231], v[52:55]\n\t" \
  "ds_read_b128 a[16:19], v220 offset:4096\n\t" \
  "v_cvt_pk_bf16_f32 v17, v2, v3\n\t" \
  "v_mfma_f32_16x16x32_bf16 v[52:55], a[84:87], a[244:247], v[52:55]\n\t" \
  "ds_read_b128 a[20:23], v221 offset:4096\n\t" \
  "v_cvt_pk_bf16_f32 v63, v62, v63\n\t" \
  "v_mfma_f32_16x16x32_bf16 v[48:51], a[144:147], a[228:231], v[48:51]\n\t" \
  "ds_read_b128 a[24:27], v222 offset:4096\n\t" \
  "v_cvt_pk_bf16_f32 v62, v60, v61\n\t" \
  "v_mfma_f32_16x16x32_bf16 v[48:51], a[80:83], a[244:247], v[48:51]\n\t" \
  "ds_read_b128 a[28:31], v223 offset:4096\n\t" \
  "v_cvt_pk_bf16_f32 v60, v56, v57\n\t" \
  "v_mfma_f32_16x16x32_bf16 v[44:47], a[140:143], a[228:231], v[44:47]\n\t" \
  "ds_read_b128 a[32:35], v220 offset:8192\n\t" \
  "v_cvt_pk_bf16_f32 v61, v58, v59\n\t" \
  "v_mfma_f32_16x16x32_bf16 v[44:47], a[76:79], a[244:247], v[44:47]\n\t" \
  "buffer_load_dwordx4 v240, %[wo], s97 offen offset:0 lds\n\t" \
  "v_mfma_f32_16x16x32_bf16 v[40:43], a[136:139], a[228:231], v[40:43]\n\t" \
  "s_add_u32 s97, s97, 7168\n\t" \
  "ds_read_b128 a[36:39], v221 offset:8192\n\t" \
  "v_cvt_pk_bf16_f32 v59, v54, v55\n\t" \
  "v_mfma_f32_16x16x32_bf16 v[40:43], a[72:75], a[244:247], v[40:43]\n\t" \
  "ds_read_b128 a[40:43], v222 offset:8192\n\t" \
  "v_cvt_pk_bf16_f32 v58, v52, v53\n\t" \
  "v_mfma_f32_16x16x32_bf16 v[36:39], a[132:135], a[228:231], v[36:39]\n\t" \
  "ds_read_b128 a[44:47], v223 offset:8192\n\t" \
  "v_cvt_pk_bf16_f32 v56, v48, v49\n\t" \
  "v_mfma_f32_16x16x32_bf16 v[36:39], a[68:71], a[244:247], v[36:39]\n\t" \
  "buffer_load_dwordx4 v241, %[wo], s97 offen offset:1024 lds\n\t" \
  "v_mfma_f32_16x16x32_bf16 v[32:35], a[128:131], a[228:231], v[32:35]\n\t" \
  "s_add_u32 s97, s97, 7168\n\t" \
  "ds_read_b128 a[48:51], v220 offset:12288\n\t" \
  "v_cvt_pk_bf16_f32 v57, v50, v51\n\t" \
  "v_mfma_f32_16x16x32_bf16 v[32:35], a[64:67], a[244:247], v[32:35]\n\t" \
  "ds_read_b128 a[52:55], v221 offset:12288\n\t" \
  "v_cvt_pk_bf16_f32 v55, v46, v47\n\t" \
  "v_mfma_f32_16x16x32_bf16 v[92:95], a[156:159], a[232:235], v[92:95]\n\t" \
  "buffer_load_dwordx4 v240, %[wo], s97 offen offset:2048 lds\n\t" \
  "v_mfma_f32_16x16x32_bf16 v[92:95], a[92:95], a[248:251], v[92:95]\n\t" \
  "s_add_u32 s97, s97, 7168\n\t" \
  "ds_read_b128 a[56:59], v222 offset:12288\n\t" \
  "v_cvt_pk_bf16_f32 v54, v44, v45\n\t" \
  "v_mfma_f32_16x16x32_bf16 v[88:91], a[152:155], a[232:235], v[88:91]\n\t" \
  "ds_read_b128 a[60:63], v223 offset:12288\n\t" \
  "v_cvt_pk_bf16_f32 v52, v40, v41\n\t" \
  "v_mfma_f32_16x16x32_bf16 v[88:91], a[88:91], a[248:251], v[88:91]\n\t" \
  "v_permlane16_swap_b32 v24, v26\n\t" \
  "v_mfma_f32_16x16x32_bf16 v[84:87], a[148:151], a[232:235], v[84:87]\n\t" \
  "buffer_load_dwordx4 v241, %[wo], s97 offen offset:3072 lds\n\t" \
  "v_mfma_f32_16x16x32_bf16 v[84:87], a[84:87], a[248:251], v[84:87]\n\t" \
  "s_add_u32 m0, %[m], 16384\n\t" \
  "v_cvt_pk_bf16_f32 v53, v42, v43\n\t" \
  "v_cvt_pk_bf16_f32 v51, v38, v39\n\t" \
  "v_mfma_f32_16x16x32_bf16 v[80:83], a[144:147], a[232:235], v[80:83]\n\t" \
  "s_xor_b32 s97, %[wob], 131072\n\t" \
  "v_permlane16_swap_b32 v25, v27\n\t" \
  "v_mfma_f32_16x16x32_bf16 v[80:83], a[80:83], a[248:251], v[80:83]\n\t" \
  "v_cvt_pk_bf16_f32 v50, v36, v37\n\t" \
  "v_cvt_pk_bf16_f32 v48, v32, v33\n\t" \
  "v_mfma_f32_16x16x32_bf16 v[76:79], a[140:143], a[232:235], v[76:79]\n\t" \
  "v_permlane16_swap_b32 v20, v22\n\t" \
  "v_mfma_f32_16x16x32_bf16 v[76:79], a[76:79], a[248:251], v[76:79]\n\t" \
  "v_cvt_pk_bf16_f32 v49, v34, v35\n\t" \
  "v_cvt_pk_bf16_f32 v95, v94, v95\n\t" \
  "v_mfma_f32_16x16x32_bf16 v[72:75], a[136:139], a[232:235], v[72:75]\n\t" \
  "v_permlane16_swap_b32 v21, v23\n\t" \
  "v_mfma_f32_16x16x32_bf16 v[72:75], a[72:75], a[248:251], v[72:75]\n\t" \
  "v_cvt_pk_bf16_f32 v94, v92, v93\n\t" \
  "v_cvt_pk_bf16_f32 v92, v88, v89\n\t" \
  "v_mfma_f32_16x16x32_bf16 v[68:71], a[132:135], a[232:235], v[68:71]\n\t" \
  "v_permlane16_swap_b32 v16, v18\n\t" \
  "v_mfma_f32_16x16x32_bf16 v[68:71], a[68:71], a[248:251], v[68:71]\n\t" \
  "v_cvt_pk_bf16_f32 v93, v90, v91\n\t" \
  "v_cvt_pk_bf16_f32 v91, v86, v87\n\t" \
  "v_mfma_f32_16x16x32_bf16 v[64:67], a[128:131], a[232:235], v[64:67]\n\t" \
  "buffer_load_dwordx4 v240, %[wo], s97 offen offset:0 lds\n\t" \
  "v_mfma_f32_16x16x32_bf16 v[64:67], a[64:67], a[248:251], v[64:67]\n\t" \
  "s_add_u32 s97, s97, 7168\n\t" \
  "v_permlane16_swap_b32 v17, v19\n\t" \
  "v_mfma_f32_16x16x32_bf16 v[124:127], a[156:159], a[236:239], v[124:127]\n\t" \
  "v_cvt_pk_bf16_f32 v90, v84, v85\n\t" \
  "v_cvt_pk_bf16_f32 v88, v80, v81\n\t" \
  "v_mfma_f32_16x16x32_bf16 v[124:127], a[92:95], a[252:255], v[124:127]\n\t" \
  "v_permlane16_swap_b32 v60, v62\n\t" \
  "v_mfma_f32_16x16x32_bf16 v[120:123], a[152:155], a[236:239], v[120:123]\n\t" \
  "buffer_load_dwordx4 v241, %[wo], s97 offen offset:1024 lds\n\t" \
  "v_mfma_f32_16x16x32_bf16 v[120:123], a[88:91], a[252:255], v[120:123]\n\t" \
  ".Lhs5:\n\ts_cbranch_execz .Lhs5n\n\t" \
  "s_add_u32 s97, s97, 7168\n\t" \
  "v_cvt_pk_bf16_f32 v89, v82, v83\n\t" \
  "v_cvt_pk_bf16_f32 v87, v78, v79\n\t" \
  "v_mfma_f32_16x16x32_bf16 v[116:119], a[148:151], a[236:239], v[116:119]\n\t" \
  "v_permlane16_swap_b32 v61, v63\n\t" \
  "v_mfma_f32_16x16x32_bf16 v[116:119], a[84:87], a[252:255], v[116:119]\n\t" \
  "buffer_load_dwordx4 v240, %[wo], s97 offen offset:2048 lds\n\t" \
  "v_mfma_f32_16x16x32_bf16 v[112:115], a[144:147], a[236:239], v[112:115]\n\t" \
  "s_add_u32 s97, s97, 7168\n\t" \
  "v_cvt_pk_bf16_f32 v86, v76, v77\n\t" \
  "v_cvt_pk_bf16_f32 v84, v72, v73\n\t" \
  "v_mfma_f32_16x16x32_bf16 v[112:115], a[80:83], a[252:255], v[112:115]\n\t" \
  "v_permlane16_swap_b32 v56, v58\n\t" \
  "v_mfma_f32_16x16x32_bf16 v[108:111], a[140:143], a[236:239], v[108:111]\n\t" \
  "v_cvt_pk_bf16_f32 v85, v74, v75\n\t" \
  "v_cvt_pk_bf16_f32 v83, v70, v71\n\t" \
  "v_mfma_f32_16x16x32_bf16 v[108:111], a[76:79], a[252:255], v[108:111]\n\t" \
  "buffer_load_dwordx4 v241, %[wo], s97 offen offset:3072 lds\n\t" \
  "v_mfma_f32_16x16x32_bf16 v[104:107], a[136:139], a[236:239], v[104:107]\n\t" \
  "v_permlane16_swap_b32 v57, v59\n\t" \
  "v_mfma_f32_16x16x32_bf16 v[104:107], a[72:75], a[252:255], v[104:107]\n\t" \
  "v_cvt_pk_bf16_f32 v82, v68, v69\n\t" \
  "v_cvt_pk_bf16_f32 v80, v64, v65\n\t" \
  "v_mfma_f32_16x16x32_bf16 v[100:103], a[132:135], a[236:239], v[100:103]\n\t" \
  "v_permlane16_swap_b32 v52, v54\n\t" \
  "v_mfma_f32_16x16x32_bf16 v[100:103], a[68:71], a[252:255], v[100:103]\n\t" \
  "buffer_load_dword %[pfd], v247, %[ws], 0 offen sc1\n\t" \
  "v_mfma_f32_16x16x32_bf16 v[96:99], a[128:131], a[236:239], v[96:99]\n\t" \
  "s_add_u32 s98, %[pf4], 28672\n\t" \
  "v_cvt_pk_bf16_f32 v81, v66, v67\n\t" \
  "v_cvt_pk_bf16_f32 v127, v126, v127\n\t" \
  "v_mfma_f32_16x16x32_bf16 v[96:99], a[64:67], a[252:255], v[96:99]\n\t" \
  "s_waitcnt lgkmcnt(0)\n\t" \
  "s_barrier\n\t" \
  STS("s_memrealtime s[94:95]\n\t") \
  STS("s_memtime s[92:93]\n\t") \
  "v_mfma_f32_16x16x32_bf16 v[0:3], a[0:3], v[16:19], 0\n\t" \
  "s_add_u32 m0, %[m], 49152\n\t" \
  "buffer_load_dword %[pfd], v245, %[sU], s98 offen\n\t" \
  "v_mfma_f32_16x16x32_bf16 v[0:3], a[4:7], v[20:23], v[0:3]\n\t" \
  "s_add_u32 s96, %[sofk], 32768\n\t" \
  "ds_read_b128 a[192:195], v220 offset:16384\n\t" \
  "v_cvt_pk_bf16_f32 v126, v124, v125\n\t" \
  "v_mfma_f32_16x16x32_bf16 v[0:3], a[8:11], v[24:27], v[0:3]\n\t" \
  "ds_read_b128 a[196:199], v221 offset:16384\n\t" \
  "v_cvt_pk_bf16_f32 v124, v120, v121\n\t" \
  "v_mfma_f32_16x16x32_bf16 v[0:3], a[12:15], v[28:31], v[0:3]\n\t" \
  "ds_read_b128 a[200:203], v222 offset:16384\n\t" \
  "v_cvt_pk_bf16_f32 v125, v122, v123\n\t" \
  "v_permlane16_swap_b32 v53, v55\n\t" \
  "v_permlane16_swap_b32 v48, v50\n\t" \
  "v_permlane16_swap_b32 v49, v51\n\t" \
  "s_nop 1\n\t" \
  "v_mfma_f32_16x16x32_bf16 v[32:35], a[0:3], v[48:51], 0\n\t" \
  "ds_read_b128 a[204:207], v223 offset:16384\n\t" \
  "v_max3_f32 v194, v0, v1, v2\n\t" \
  "v_mfma_f32_16x16x32_bf16 v[32:35], a[4:7], v[52:55], v[32:35]\n\t" \
  "ds_read_b128 a[208:211], v220 offset:20480\n\t" \
  "v_max_f32 v194, v194, v3\n\t" \
  "v_mfma_f32_16x16x32_bf16 v[32:35], a[8:11], v[56:59], v[32:35]\n\t" \
  "ds_read_b128 a[212:215], v221 offset:20480\n\t" \
  "v_mov_b32 v195, v194\n\t" \
  "v_mfma_f32_16x16x32_bf16 v[32:35], a[12:15], v[60:63], v[32:35]\n\t" \
  "ds_read_b128 a[216:219], v222 offset:20480\n\t" \
  "v_cvt_pk_bf16_f32 v123, v118, v119\n\t" \
  "v_permlane32_swap_b32 v194, v195\n\t" \
  "v_max_f32 v194, v194, v195\n\t" \
  "v_mov_b32 v195, v194\n\t" \
  "s_nop 1\n\t" \
  "v_permlane16_swap_b32 v194, v195\n\t" \
  "v_max_f32 v194, v194, v195\n\t" \
  "v_sub_f32 v176, 0xc2c80000, v194\n\t" \
  "v_sub_f32 v177, 0xc2c80000, v194\n\t" \
  "v_sub_f32 v178, 0xc2c80000, v194\n\t" \
  "v_sub_f32 v179, 0xc2c80000, v194\n\t" \
  "s_nop 1\n\t" \
  "v_mfma_f32_16x16x32_bf16 v[4:7], a[16:19], v[16:19], v[176:179]\n\t" \
  "ds_read_b128 a[220:223], v223 offset:20480\n\t" \
  "v_add_f32 v0, v0, v176\n\t" \
  "v_mfma_f32_16x16x32_bf16 v[4:7], a[20:23], v[20:23], v[4:7]\n\t" \
  "ds_read_b128 a[224:227], v220 offset:24576\n\t" \
  "v_add_f32 v1, v1, v176\n\t" \
  "v_mfma_f32_16x16x32_bf16 v[4:7], a[24:27], v[24:27], v[4:7]\n\t" \
  "ds_read_b128 a[228:231], v221 offset:24576\n\t" \
  "v_add_f32 v2, v2, v176\n\t" \
  "v_mfma_f32_16x16x32_bf16 v[4:7], a[28:31], v[28:31], v[4:7]\n\t" \
  "v_add_f32 v3, v3, v176\n\t" \
  "v_max3_f32 v194, v32, v33, v34\n\t" \
  "v_mfma_f32_16x16x32_bf16 v[8:11], a[32:35], v[16:19], v[176:179]\n\t" \
  STS("v_writelane_b32 %[stv], s94, 18\n\t") \
  STS("v_writelane_b32 %[stv], s92, 19\n\t") \
  "ds_read_b128 a[232:235], v222 offset:24576\n\t" \
  "v_max_f32 v194, v194, v35\n\t" \
  "v_mfma_f32_16x16x32_bf16 v[8:11], a[36:39], v[20:23], v[8:11]\n\t" \
  "v_mov_b32 v195, v194\n\t" \
  "v_cvt_pk_bf16_f32 v122, v116, v117\n\t" \
  "v_mfma_f32_16x16x32_bf16 v[8:11], a[40:43], v[24:27], v[8:11]\n\t" \
  "v_permlane32_swap_b32 v194, v195\n\t" \
  "v_mfma_f32_16x16x32_bf16 v[8:11], a[44:47], v[28:31], v[8:11]\n\t" \
  "ds_read_b128 a[236:239], v223 offset:24576\n\t" \
  "v_max_f32 v194, v194, v195\n\t" \
  "v_mfma_f32_16x16x32_bf16 v[12:15], a[48:51], v[16:19], v[176:179]\n\t" \
  "ds_read_b128 a[240:243], v220 offset:28672\n\t" \
  "v_mov_b32 v195, v194\n\t" \
  "v_mfma_f32_16x16x32_bf16 v[12:15], a[52:55], v[20:23], v[12:15]\n\t" \
  "buffer_load_dwordx4 v224, %[sK], s96 offen offset:0 lds\n\t" \
  "v_mfma_f32_16x16x32_bf16 v[12:15], a[56:59], v[24:27], v[12:15]\n\t" \
  "v_permlane16_swap_b32 v194, v195\n\t" \
  "v_mfma_f32_16x16x32_bf16 v[12:15], a[60:63], v[28:31], v[12:15]\n\t" \
  "ds_read_b128 a[244:247], v221 offset:28672\n\t" \
  "v_max_f32 v194, v194, v195\n\t" \
  "v_permlane16_swap_b32 v92, v94\n\t" \
  "v_permlane16_swap_b32 v93, v95\n\t" \
  "v_permlane16_swap_b32 v88, v90\n\t" \
  "v_permlane16_swap_b32 v89, v91\n\t" \
  "v_permlane16_swap_b32 v84, v86\n\t" \
  "v_permlane16_swap_b32 v85, v87\n\t" \
  "v_permlane16_swap_b32 v80, v82\n\t" \
  "v_permlane16_swap_b32 v81, v83\n\t" \
  "s_nop 1\n\t" \
  "v_mfma_f32_16x16x32_bf16 v[64:67], a[0:3], v[80:83], 0\n\t" \
  "ds_read_b128 a[248:251], v222 offset:28672\n\t" \
  "v_sub_f32 v180, 0xc2c80000, v194\n\t" \
  "v_mfma_f32_16x16x32_bf16 v[64:67], a[4:7], v[84:87], v[64:67]\n\t" \
  "v_sub_f32 v181, 0xc2c80000, v194\n\t" \
  "v_sub_f32 v182, 0xc2c80000, v194\n\t" \
  "v_mfma_f32_16x16x32_bf16 v[64:67], a[8:11], v[88:91], v[64:67]\n\t" \
  "ds_read_b128 a[252:255], v223 offset:28672\n\t" \
  "v_sub_f32 v183, 0xc2c80000, v194\n\t" \
  "v_mfma_f32_16x16x32_bf16 v[64:67], a[12:15], v[92:95], v[64:67]\n\t" \
  "v_add_f32 v32, v32, v180\n\t" \
  "v_add_f32 v33, v33, v180\n\t" \
  "v_mfma_f32_16x16x32_bf16 v[36:39], a[16:19], v[48:51], v[180:183]\n\t" \
  "v_add_f32 v34, v34, v180\n\t" \
  "v_add_f32 v35, v35, v180\n\t" \
  "v_mfma_f32_16x16x32_bf16 v[36:39], a[20:23], v[52:55], v[36:39]\n\t" \
  "buffer_load_dwordx4 v225, %[sK], s96 offen offset:1024 lds\n\t" \
  "v_mfma_f32_16x16x32_bf16 v[36:39], a[24:27], v[56:59], v[36:39]\n\t" \
  "v_max3_f32 v194, v64, v65, v66\n\t" \
  "v_max_f32 v194, v194, v67\n\t" \
  "v_mfma_f32_16x16x32_bf16 v[36:39], a[28:31], v[60:63], v[36:39]\n\t" \
  "v_mov_b32 v195, v194\n\t" \
  "v_cvt_pk_bf16_f32 v120, v112, v113\n\t" \
  "v_mfma_f32_16x16x32_bf16 v[40:43], a[32:35], v[48:51], v[180:183]\n\t" \
  "v_permlane32_swap_b32 v194, v195\n\t" \
  "v_mfma_f32_16x16x32_bf16 v[40:43], a[36:39], v[52:55], v[40:43]\n\t" \
  "v_max_f32 v194, v194, v195\n\t" \
  "v_mov_b32 v195, v194\n\t" \
  "v_mfma_f32_16x16x32_bf16 v[40:43], a[40:43], v[56:59], v[40:43]\n\t" \
  "buffer_load_dwordx4 v226, %[sK], s96 offen offset:2048 lds\n\t" \
  "v_mfma_f32_16x16x32_bf16 v[40:43], a[44:47], v[60:63], v[40:43]\n\t" \
  "v_permlane16_swap_b32 v194, v195\n\t" \
  "v_mfma_f32_16x16x32_bf16 v[44:47], a[48:51], v[48:51], v[180:183]\n\t" \
  "v_max_f32 v194, v194, v195\n\t" \
  "v_sub_f32 v184, 0xc2c80000, v194\n\t" \
  "v_mfma_f32_16x16x32_bf16 v[44:47], a[52:55], v[52:55], v[44:47]\n\t" \
  "v_sub_f32 v185, 0xc2c80000, v194\n\t" \
  "v_sub_f32 v186, 0xc2c80000, v194\n\t" \
  "v_mfma_f32_16x16x32_bf16 v[44:47], a[56:59], v[56:59], v[44:47]\n\t" \
  "v_sub_f32 v187, 0xc2c80000, v194\n\t" \
  "v_add_f32 v64, v64, v184\n\t" \
  "v_mfma_f32_16x16x32_bf16 v[44:47], a[60:63], v[60:63], v[44:47]\n\t" \
  "ds_read_b64_tr_b16 a[128:129], v228 offset:0\n\t" \
  "v_add_f32 v65, v65, v184\n\t" \
  "v_cvt_pk_bf16_f32 v121, v114, v115\n\t" \
  "v_cvt_pk_bf16_f32 v119, v110, v111\n\t" \
  "v_cvt_pk_bf16_f32 v118, v108, v109\n\t" \
  "v_cvt_pk_bf16_f32 v116, v104, v105\n\t" \
  "v_cvt_pk_bf16_f32 v117, v106, v107\n\t" \
  "v_cvt_pk_bf16_f32 v115, v102, v103\n\t" \
  "v_cvt_pk_bf16_f32 v114, v100, v101\n\t" \
  "v_cvt_pk_bf16_f32 v112, v96, v97\n\t" \
  "v_cvt_pk_bf16_f32 v113, v98, v99\n\t" \
  "v_permlane16_swap_b32 v124, v126\n\t" \
  "v_permlane16_swap_b32 v125, v127\n\t" \
  "v_permlane16_swap_b32 v120, v122\n\t" \
  "v_permlane16_swap_b32 v121, v123\n\t" \
  "v_permlane16_swap_b32 v116, v118\n\t" \
  "v_permlane16_swap_b32 v117, v119\n\t" \
  "v_permlane16_swap_b32 v112, v114\n\t" \
  "v_permlane16_swap_b32 v113, v115\n\t" \
  "s_nop 1\n\t" \
  "v_mfma_f32_16x16x32_bf16 v[96:99], a[0:3], v[112:115], 0\n\t" \
  "ds_read_b64_tr_b16 a[130:131], v228 offset:4096\n\t" \
  "v_add_f32 v66, v66, v184\n\t" \
  "v_mfma_f32_16x16x32_bf16 v[96:99], a[4:7], v[116:119], v[96:99]\n\t" \
  "ds_read_b64_tr_b16 a[132:133], v228 offset:8192\n\t" \
  "v_add_f32 v67, v67, v184\n\t" \
  "v_mfma_f32_16x16x32_bf16 v[96:99], a[8:11], v[120:123], v[96:99]\n\t" \
  "buffer_load_dwordx4 v227, %[sK], s96 offen offset:3072 lds\n\t" \
  "v_mfma_f32_16x16x32_bf16 v[96:99], a[12:15], v[124:127], v[96:99]\n\t" \
  "s_add_u32 m0, %[m], 147456\n\t" \
  "v_exp_f32 v0, v0\n\t" \
  "v_mfma_f32_16x16x32_bf16 v[68:71], a[16:19], v[80:83], v[184:187]\n\t" \
  "s_add_u32 s96, %[sofk], 16384\n\t" \
  "v_exp_f32 v1, v1\n\t" \
  "v_mfma_f32_16x16x32_bf16 v[68:71], a[20:23], v[84:87], v[68:71]\n\t" \
  "v_exp_f32 v2, v2\n\t" \
  "v_mfma_f32_16x16x32_bf16 v[68:71], a[24:27], v[88:91], v[68:71]\n\t" \
  "ds_read_b64_tr_b16 a[134:135], v228 offset:12288\n\t" \
  "v_max3_f32 v194, v96, v97, v98\n\t" \
  "v_mfma_f32_16x16x32_bf16 v[68:71], a[28:31], v[92:95], v[68:71]\n\t" \
  "ds_read_b64_tr_b16 a[136:137], v229 offset:0\n\t" \
  "v_max_f32 v194, v194, v99\n\t" \
  "v_mfma_f32_16x16x32_bf16 v[72:75], a[32:35], v[80:83], v[184:187]\n\t" \
  "ds_read_b64_tr_b16 a[138:139], v229 offset:4096\n\t" \
  "v_mov_b32 v195, v194\n\t" \
  "v_mfma_f32_16x16x32_bf16 v[72:75], a[36:39], v[84:87], v[72:75]\n\t" \
  "v_exp_f32 v3, v3\n\t" \
  "v_mfma_f32_16x16x32_bf16 v[72:75], a[40:43], v[88:91], v[72:75]\n\t" \
  "v_permlane32_swap_b32 v194, v195\n\t" \
  "v_mfma_f32_16x16x32_bf16 v[72:75], a[44:47], v[92:95], v[72:75]\n\t" \
  "ds_read_b64_tr_b16 a[140:141], v229 offset:8192\n\t" \
  "v_max_f32 v194, v194, v195\n\t" \
  "v_mfma_f32_16x16x32_bf16 v[76:79], a[48:51], v[80:83], v[184:187]\n\t" \
  "ds_read_b64_tr_b16 a[142:143], v229 offset:12288\n\t" \
  "v_mov_b32 v195, v194\n\t" \
  "v_mfma_f32_16x16x32_bf16 v[76:79], a[52:55], v[84:87], v[76:79]\n\t" \
  "buffer_load_dwordx4 v230, %[sV], s96 offen offset:0 lds\n\t" \
  "v_mfma_f32_16x16x32_bf16 v[76:79], a[56:59], v[88:91], v[76:79]\n\t" \
  "v_permlane16_swap_b32 v194, v195\n\t" \
  "v_mfma_f32_16x16x32_bf16 v[76:79], a[60:63], v[92:95], v[76:79]\n\t" \
  "ds_read_b64_tr_b16 a[144:145], v228 offset:256\n\t" \
  "v_max_f32 v194, v194, v195\n\t" \
  "v_sub_f32 v188, 0xc2c80000, v194\n\t" \
  "v_sub_f32 v189, 0xc2c80000, v194\n\t" \
  "v_sub_f32 v190, 0xc2c80000, v194\n\t" \
  "v_sub_f32 v191, 0xc2c80000, v194\n\t" \
  "s_nop 1\n\t" \
  "v_mfma_f32_16x16x32_bf16 v[100:103], a[16:19], v[112:115], v[188:191]\n\t" \
  "ds_read_b64_tr_b16 a[146:147], v228 offset:4352\n\t" \
  "v_add_f32 v96, v96, v188\n\t" \
  "v_mfma_f32_16x16x32_bf16 v[100:103], a[20:23], v[116:119], v[100:103]\n\t" \
  "ds_read_b64_tr_b16 a[148:149], v228 offset:8448\n\t" \
  "v_add_f32 v97, v97, v188\n\t" \
  "v_mfma_f32_16x16x32_bf16 v[100:103], a[24:27], v[120:123], v[100:103]\n\t" \
  "v_add_f32 v98, v98, v188\n\t" \
  "v_add_f32 v99, v99, v188\n\t" \
  "v_mfma_f32_16x16x32_bf16 v[100:103], a[28:31], v[124:127], v[100:103]\n\t" \
  "buffer_load_dwordx4 v231, %[sV], s96 offen offset:1024 lds\n\t" \
  "v_mfma_f32_16x16x32_bf16 v[104:107], a[32:35], v[112:115], v[188:191]\n\t" \
  "ds_read_b64_tr_b16 a[150:151], v228 offset:12544\n\t" \
  "v_cvt_pk_bf16_f32 v128, v0, v1\n\t" \
  "v_mfma_f32_16x16x32_bf16 v[104:107], a[36:39], v[116:119], v[104:107]\n\t" \
  "v_exp_f32 v4, v4\n\t" \
  "v_mfma_f32_16x16x32_bf16 v[104:107], a[40:43], v[120:123], v[104:107]\n\t" \
  "v_exp_f32 v5, v5\n\t" \
  "v_mfma_f32_16x16x32_bf16 v[104:107], a[44:47], v[124:127], v[104:107]\n\t" \
  "v_exp_f32 v6, v6\n\t" \
  "v_mfma_f32_16x16x32_bf16 v[108:111], a[48:51], v[112:115], v[188:191]\n\t" \
  "buffer_load_dwordx4 v230, %[sV], s96 offen offset:2048 lds\n\t" \
  "v_mfma_f32_16x16x32_bf16 v[108:111], a[52:55], v[116:119], v[108:111]\n\t" \
  "v_exp_f32 v7, v7\n\t" \
  "v_mfma_f32_16x16x32_bf16 v[108:111], a[56:59], v[120:123], v[108:111]\n\t" \
  "ds_read_b64_tr_b16 a[152:153], v229 offset:256\n\t" \
  "v_cvt_pk_bf16_f32 v129, v2, v3\n\t" \
  "v_mfma_f32_16x16x32_bf16 v[108:111], a[60:63], v[124:127], v[108:111]\n\t" \
  "s_waitcnt lgkmcnt(13)\n\t" \
  "v_exp_f32 v8, v8\n\t" \
  STA("s_memrealtime s[94:95]\n\t") \
  STA("s_memtime s[92:93]\n\t") \
  "v_mfma_f32_16x16x32_bf16 v[0:3], a[192:195], v[16:19], v[176:179]\n\t" \
  "v_exp_f32 v9, v9\n\t" \
  "v_mfma_f32_16x16x32_bf16 v[0:3], a[196:199], v[20:23], v[0:3]\n\t" \
  "ds_read_b64_tr_b16 a[154:155], v229 offset:4352\n\t" \
  "v_cvt_pk_bf16_f32 v130, v4, v5\n\t" \
  "v_mfma_f32_16x16x32_bf16 v[0:3], a[200:203], v[24:27], v[0:3]\n\t" \
  "buffer_load_dwordx4 v231, %[sV], s96 offen offset:3072 lds\n\t" \
  "v_mfma_f32_16x16x32_bf16 v[0:3], a[204:207], v[28:31], v[0:3]\n\t" \
  "ds_read_b64_tr_b16 a[156:157], v229 offset:8448\n\t" \
  "v_cvt_pk_bf16_f32 v131, v6, v7\n\t" \
  "v_mfma_f32_16x16x32_bf16 v[4:7], a[220:223], v[28:31], v[176:179]\n\t" \
  "v_exp_f32 v10, v10\n\t" \
  "v_mfma_f32_16x16x32_bf16 v[4:7], a[216:219], v[24:27], v[4:7]\n\t" \
  "s_add_u32 s98, %[pf4], 43008\n\t" \
  "v_exp_f32 v11, v11\n\t" \
  "v_mfma_f32_16x16x32_bf16 v[4:7], a[212:215], v[20:23], v[4:7]\n\t" \
  "v_exp_f32 v12, v12\n\t" \
  "v_mfma_f32_16x16x32_bf16 v[4:7], a[208:211], v[16:19], v[4:7]\n\t" \
  "v_exp_f32 v13, v13\n\t" \
  "v_cvt_pk_bf16_f32 v132, v8, v9\n\t" \
  "v_cvt_pk_bf16_f32 v133, v10, v11\n\t" \
  "v_mfma_f32_16x16x32_bf16 v[8:11], a[224:227], v[16:19], v[176:179]\n\t" \
  "v_exp_f32 v14, v14\n\t" \
  "v_mfma_f32_16x16x32_bf16 v[8:11], a[228:231], v[20:23], v[8:11]\n\t" \
  "v_exp_f32 v15, v15\n\t" \
  "v_mfma_f32_16x16x32_bf16 v[8:11], a[232:235], v[24:27], v[8:11]\n\t" \
  "buffer_load_dword %[pfd], v245, %[sU], s98 offen\n\t" \
  "v_mfma_f32_16x16x32_bf16 v[8:11], a[236:239], v[28:31], v[8:11]\n\t" \
  "v_exp_f32 v32, v32\n\t" \
  "v_cvt_pk_bf16_f32 v134, v12, v13\n\t" \
  "v_cvt_pk_bf16_f32 v135, v14, v15\n\t" \
  "v_mfma_f32_16x16x32_bf16 v[12:15], a[252:255], v[28:31], v[176:179]\n\t" \
  STA("v_writelane_b32 %[stv], s94, 26\n\t") \
  STA("v_writelane_b32 %[stv], s92, 27\n\t") \
  "s_add_u32 s98, %[pf4], 0\n\t" \
  "v_exp_f32 v33, v33\n\t" \
  "v_mfma_f32_16x16x32_bf16 v[12:15], a[248:251], v[24:27], v[12:15]\n\t" \
  "v_exp_f32 v34, v34\n\t" \
  "v_mfma_f32_16x16x32_bf16 v[12:15], a[244:247], v[20:23], v[12:15]\n\t" \
  "v_exp_f32 v35, v35\n\t" \
  "v_mfma_f32_16x16x32_bf16 v[12:15], a[240:243], v[16:19], v[12:15]\n\t" \
  "buffer_load_dword %[pfd], v245, %[sG], s98 offen\n\t" \
  "v_cvt_pk_bf16_f32 v136, v32, v33\n\t" \
  "v_cvt_pk_bf16_f32 v137, v34, v35\n\t" \
  "v_mfma_f32_16x16x32_bf16 v[32:35], a[192:195], v[48:51], v[180:183]\n\t" \
  "ds_read_b64_tr_b16 a[158:159], v229 offset:12544\n\t" \
  "ds_read_b64_tr_b16 a[160:161], v228 offset:512\n\t" \
  "v_exp_f32 v36, v36\n\t" \
  "v_mfma_f32_16x16x32_bf16 v[32:35], a[196:199], v[52:55], v[32:35]\n\t" \
  "v_exp_f32 v37, v37\n\t" \
  "v_mfma_f32_16x16x32_bf16 v[32:35], a[200:203], v[56:59], v[32:35]\n\t" \
  "v_exp_f32 v38, v38\n\t" \
  "v_mfma_f32_16x16x32_bf16 v[32:35], a[204:207], v[60:63], v[32:35]\n\t" \
  "s_add_u32 s98, %[pf4], 14336\n\t" \
  "v_exp_f32 v39, v39\n\t" \
  "v_cvt_pk_bf16_f32 v138, v36, v37\n\t" \
  "v_cvt_pk_bf16_f32 v139, v38, v39\n\t" \
  "v_lshl_add_u32 v207, v233, 14, %[cto]\n\t" \
  "v_min_u32 v207, .Lfz_z-.Lat_t0-4, v207\n\t" \
  "v_mfma_f32_16x16x32_bf16 v[36:39], a[220:223], v[60:63], v[180:183]\n\t" \
  "v_exp_f32 v40, v40\n\t" \
  "v_mfma_f32_16x16x32_bf16 v[36:39], a[216:219], v[56:59], v[36:39]\n\t" \
  "buffer_load_dword %[pfd], v245, %[sG], s98 offen\n\t" \
  "v_exp_f32 v41, v41\n\t" \
  "s_getpc_b64 s[96:97]\n\t" \
  ".Lat_t0:\n\t" \
  "global_load_dword %[pfd], v207, s[96:97]\n\t" \
  "v_mfma_f32_16x16x32_bf16 v[36:39], a[212:215], v[52:55], v[36:39]\n\t" \
  "ds_read_b64_tr_b16 a[162:163], v228 offset:4608\n\t" \
  "v_cvt_pk_bf16_f32 v140, v40, v41\n\t" \
  "v_exp_f32 v42, v42\n\t" \
  "v_mfma_f32_16x16x32_bf16 v[36:39], a[208:211], v[48:51], v[36:39]\n\t" \
  "ds_read_b64_tr_b16 a[164:165], v228 offset:8704\n\t" \
  "ds_read_b64_tr_b16 a[166:167], v228 offset:12800\n\t" \
  "v_exp_f32 v43, v43\n\t" \
  "s_nop 0\n\t" \
  "v_cvt_pk_bf16_f32 v141, v42, v43\n\t" \
  "v_mfma_f32_16x16x32_bf16 v[40:43], a[224:227], v[48:51], v[180:183]\n\t" \
  "v_exp_f32 v44, v44\n\t" \
  "v_mfma_f32_16x16x32_bf16 v[40:43], a[228:231], v[52:55], v[40:43]\n\t" \
  "v_exp_f32 v45, v45\n\t" \
  "v_mfma_f32_16x16x32_bf16 v[40:43], a[232:235], v[56:59], v[40:43]\n\t" \
  "v_exp_f32 v46, v46\n\t" \
  "v_mfma_f32_16x16x32_bf16 v[40:43], a[236:239], v[60:63], v[40:43]\n\t" \
  "v_exp_f32 v47, v47\n\t" \
  "v_cvt_pk_bf16_f32 v142, v44, v45\n\t" \
  "v_cvt_pk_bf16_f32 v143, v46, v47\n\t" \
  "v_mfma_f32_16x16x32_bf16 v[44:47], a[252:255], v[60:63], v[180:183]\n\t" \
  "v_exp_f32 v64, v64\n\t" \
  "v_mfma_f32_16x16x32_bf16 v[44:47], a[248:251], v[56:59], v[44:47]\n\t" \
  "ds_read_b64_tr_b16 a[168:169], v229 offset:512\n\t" \
  "ds_read_b64_tr_b16 a[170:171], v229 offset:4608\n\t" \
  "v_exp_f32 v65, v65\n\t" \
  "v_mfma_f32_16x16x32_bf16 v[44:47], a[244:247], v[52:55], v[44:47]\n\t" \
  "v_exp_f32 v66, v66\n\t" \
  "v_mfma_f32_16x16x32_bf16 v[44:47], a[240:243], v[48:51], v[44:47]\n\t" \
  "v_exp_f32 v67, v67\n\t" \
  "v_cvt_pk_bf16_f32 v144, v64, v65\n\t" \
  "v_cvt_pk_bf16_f32 v145, v66, v67\n\t" \
  "v_mfma_f32_16x16x32_bf16 v[64:67], a[192:195], v[80:83], v[184:187]\n\t" \
  "v_exp_f32 v68, v68\n\t" \
  "v_mfma_f32_16x16x32_bf16 v[64:67], a[196:199], v[84:87], v[64:67]\n\t" \
  "v_exp_f32 v69, v69\n\t" \
  "v_mfma_f32_16x16x32_bf16 v[64:67], a[200:203], v[88:91], v[64:67]\n\t" \
  "ds_read_b64_tr_b16 a[172:173], v229 offset:8704\n\t" \
  "v_cvt_pk_bf16_f32 v146, v68, v69\n\t" \
  "v_exp_f32 v70, v70\n\t" \
  "v_mfma_f32_16x16x32_bf16 v[64:67], a[204:207], v[92:95], v[64:67]\n\t" \
  "v_exp_f32 v71, v71\n\t" \
  "s_nop 0\n\t" \
  "v_cvt_pk_bf16_f32 v147, v70, v71\n\t" \
  "v_mfma_f32_16x16x32_bf16 v[68:71], a[220:223], v[92:95], v[184:187]\n\t" \
  "v_exp_f32 v72, v72\n\t" \
  "v_mfma_f32_16x16x32_bf16 v[68:71], a[216:219], v[88:91], v[68:71]\n\t" \
  "ds_read_b64_tr_b16 a[174:175], v229 offset:12800\n\t" \
  "ds_read_b64_tr_b16 a[176:177], v228 offset:768\n\t" \
  "v_exp_f32 v73, v73\n\t" \
  "v_mfma_f32_16x16x32_bf16 v[68:71], a[212:215], v[84:87], v[68:71]\n\t" \
  "v_exp_f32 v74, v74\n\t" \
  "v_mfma_f32_16x16x32_bf16 v[68:71], a[208:211], v[80:83], v[68:71]\n\t" \
  "v_exp_f32 v75, v75\n\t" \
  "v_cvt_pk_bf16_f32 v148, v72, v73\n\t" \
  "v_cvt_pk_bf16_f32 v149, v74, v75\n\t" \
  "v_mfma_f32_16x16x32_bf16 v[72:75], a[224:227], v[80:83], v[184:187]\n\t" \
  "v_exp_f32 v76, v76\n\t" \
  "v_mfma_f32_16x16x32_bf16 v[72:75], a[228:231], v[84:87], v[72:75]\n\t" \
  "v_exp_f32 v77, v77\n\t" \
  "v_mfma_f32_16x16x32_bf16 v[72:75], a[232:235], v[88:91], v[72:75]\n\t" \
  "ds_read_b64_tr_b16 a[178:179], v228 offset:4864\n\t" \
  "v_cvt_pk_bf16_f32 v150, v76, v77\n\t" \
  "v_exp_f32 v78, v78\n\t" \
  "v_mfma_f32_16x16x32_bf16 v[72:75], a[236:239], v[92:95], v[72:75]\n\t" \
  "v_exp_f32 v79, v79\n\t" \
  "s_nop 0\n\t" \
  "v_cvt_pk_bf16_f32 v151, v78, v79\n\t" \
  "v_mfma_f32_16x16x32_bf16 v[76:79], a[252:255], v[92:95], v[184:187]\n\t" \
  "v_exp_f32 v96, v96\n\t" \
  "v_mfma_f32_16x16x32_bf16 v[76:79], a[248:251], v[88:91], v[76:79]\n\t" \
  "ds_read_b64_tr_b16 a[180:181], v228 offset:8960\n\t" \
  "ds_read_b64_tr_b16 a[182:183], v228 offset:13056\n\t" \
  "v_exp_f32 v97, v97\n\t" \
  "v_mfma_f32_16x16x32_bf16 v[76:79], a[244:247], v[84:87], v[76:79]\n\t" \
  "v_exp_f32 v98, v98\n\t" \
  "v_mfma_f32_16x16x32_bf16 v[76:79], a[240:243], v[80:83], v[76:79]\n\t" \
  "v_exp_f32 v99, v99\n\t" \
  "v_cvt_pk_bf16_f32 v152, v96, v97\n\t" \
  "v_cvt_pk_bf16_f32 v153, v98, v99\n\t" \
  "v_mfma_f32_16x16x32_bf16 v[96:99], a[192:195], v[112:115], v[188:191]\n\t" \
  "v_exp_f32 v100, v100\n\t" \
  "v_mfma_f32_16x16x32_bf16 v[96:99], a[196:199], v[116:119], v[96:99]\n\t" \
  "v_exp_f32 v101, v101\n\t" \
  "v_mfma_f32_16x16x32_bf16 v[96:99], a[200:203], v[120:123], v[96:99]\n\t" \
  "ds_read_b64_tr_b16 a[184:185], v229 offset:768\n\t" \
  "v_cvt_pk_bf16_f32 v154, v100, v101\n\t" \
  "v_exp_f32 v102, v102\n\t" \
  "v_mfma_f32_16x16x32_bf16 v[96:99], a[204:207], v[124:127], v[96:99]\n\t" \
  "v_exp_f32 v103, v103\n\t" \
  "s_nop 0\n\t" \
  "v_cvt_pk_bf16_f32 v155, v102, v103\n\t" \
  "v_mfma_f32_16x16x32_bf16 v[100:103], a[220:223], v[124:127], v[188:191]\n\t" \
  "v_exp_f32 v104, v104\n\t" \
  "v_mfma_f32_16x16x32_bf16 v[100:103], a[216:219], v[120:123], v[100:103]\n\t" \
  "ds_read_b64_tr_b16 a[186:187], v229 offset:4864\n\t" \
  "ds_read_b64_tr_b16 a[188:189], v229 offset:8960\n\t" \
  "v_exp_f32 v105, v105\n\t" \
  "v_mfma_f32_16x16x32_bf16 v[100:103], a[212:215], v[116:119], v[100:103]\n\t" \
  "v_exp_f32 v106, v106\n\t" \
  "v_mfma_f32_16x16x32_bf16 v[100:103], a[208:211], v[112:115], v[100:103]\n\t" \
  "v_exp_f32 v107, v107\n\t" \
  "v_cvt_pk_bf16_f32 v156, v104, v105\n\t" \
  "v_cvt_pk_bf16_f32 v157, v106, v107\n\t" \
  "v_mfma_f32_16x16x32_bf16 v[104:107], a[224:227], v[112:115], v[188:191]\n\t" \
  "v_exp_f32 v108, v108\n\t" \
  "v_mfma_f32_16x16x32_bf16 v[104:107], a[228:231], v[116:119], v[104:107]\n\t" \
  "v_exp_f32 v109, v109\n\t" \
  "v_mfma_f32_16x16x32_bf16 v[104:107], a[232:235], v[120:123], v[104:107]\n\t" \
  "ds_read_b64_tr_b16 a[190:191], v229 offset:13056\n\t" \
  "v_cvt_pk_bf16_f32 v158, v108, v109\n\t" \
  "v_exp_f32 v110, v110\n\t" \
  "v_mfma_f32_16x16x32_bf16 v[104:107], a[236:239], v[124:127], v[104:107]\n\t" \
  "v_exp_f32 v111, v111\n\t" \
  "s_nop 0\n\t" \
  "v_cvt_pk_bf16_f32 v159, v110, v111\n\t" \
  "v_mfma_f32_16x16x32_bf16 v[108:111], a[252:255], v[124:127], v[188:191]\n\t" \
  "v_exp_f32 v0, v0\n\t" \
  "v_mfma_f32_16x16x32_bf16 v[108:111], a[248:251], v[120:123], v[108:111]\n\t" \
  "v_exp_f32 v1, v1\n\t" \
  "v_mfma_f32_16x16x32_bf16 v[108:111], a[244:247], v[116:119], v[108:111]\n\t" \
  "v_exp_f32 v2, v2\n\t" \
  "v_mfma_f32_16x16x32_bf16 v[108:111], a[240:243], v[112:115], v[108:111]\n\t" \
  "s_waitcnt vmcnt(4) lgkmcnt(0)\n\t" \
  "s_barrier\n\t" \
  STA("s_memrealtime s[94:95]\n\t") \
  STA("s_memtime s[92:93]\n\t") \
  "v_mfma_f32_16x16x32_bf16 a[0:3], a[128:131], v[128:131], 0\n\t" \
  "v_exp_f32 v3, v3\n\t" \
  "v_mfma_f32_16x16x32_bf16 a[0:3], a[132:135], v[132:135], a[0:3]\n\t" \
  "v_exp_f32 v4, v4\n\t" \
  "v_mfma_f32_16x16x32_bf16 a[16:19], a[136:139], v[128:131], 0\n\t" \
  "ds_read_b128 a[192:195], v220 offset:0\n\t" \
  "ds_read_b128 a[196:199], v221 offset:0\n\t" \
  "v_mfma_f32_16x16x32_bf16 a[16:19], a[140:143], v[132:135], a[16:19]\n\t" \
  "v_exp_f32 v5, v5\n\t" \
  "v_mfma_f32_16x16x32_bf16 a[32:35], a[144:147], v[128:131], 0\n\t" \
  "v_exp_f32 v6, v6\n\t" \
  "v_mfma_f32_16x16x32_bf16 a[32:35], a[148:151], v[132:135], a[32:35]\n\t" \
  "v_exp_f32 v7, v7\n\t" \
  "v_mfma_f32_16x16x32_bf16 a[48:51], a[152:155], v[128:131], 0\n\t" \
  "v_exp_f32 v8, v8\n\t" \
  "v_mfma_f32_16x16x32_bf16 a[48:51], a[156:159], v[132:135], a[48:51]\n\t" \
  "v_exp_f32 v9, v9\n\t" \
  "v_mfma_f32_16x16x32_bf16 a[64:67], a[160:163], v[128:131], 0\n\t" \
  "v_exp_f32 v10, v10\n\t" \
  "v_mfma_f32_16x16x32_bf16 a[64:67], a[164:167], v[132:135], a[64:67]\n\t" \
  "v_exp_f32 v11, v11\n\t" \
  "v_mfma_f32_16x16x32_bf16 a[80:83], a[168:171], v[128:131], 0\n\t" \
  "ds_read_b128 a[200:203], v222 offset:0\n\t" \
  "ds_read_b128 a[204:207], v223 offset:0\n\t" \
  "v_mfma_f32_16x16x32_bf16 a[80:83], a[172:175], v[132:135], a[80:83]\n\t" \
  "v_exp_f32 v12, v12\n\t" \
  "v_mfma_f32_16x16x32_bf16 a[96:99], a[176:179], v[128:131], 0\n\t" \
  STA("v_writelane_b32 %[stv], s94, 28\n\t") \
  STA("v_writelane_b32 %[stv], s92, 29\n\t") \
  "v_exp_f32 v13, v13\n\t" \
  "v_mfma_f32_16x16x32_bf16 a[96:99], a[180:183], v[132:135], a[96:99]\n\t" \
  "v_exp_f32 v14, v14\n\t" \
  "v_mfma_f32_16x16x32_bf16 a[112:115], a[184:187], v[128:131], 0\n\t" \
  "v_exp_f32 v15, v15\n\t" \
  "v_mfma_f32_16x16x32_bf16 a[112:115], a[188:191], v[132:135], a[112:115]\n\t" \
  "v_exp_f32 v32, v32\n\t" \
  "v_mfma_f32_16x16x32_bf16 v[160:163], v[216:219], v[128:131], 0\n\t" \
  "v_exp_f32 v33, v33\n\t" \
  "v_mfma_f32_16x16x32_bf16 v[160:163], v[216:219], v[132:135], v[160:163]\n\t" \
  "ds_read_b128 a[208:211], v220 offset:4096\n\t" \
  "v_cvt_pk_bf16_f32 v128, v0, v1\n\t" \
  "v_mfma_f32_16x16x32_bf16 a[4:7], a[128:131], v[136:139], 0\n\t" \
  "v_exp_f32 v34, v34\n\t" \
  "v_mfma_f32_16x16x32_bf16 a[4:7], a[132:135], v[140:143], a[4:7]\n\t" \
  "ds_read_b128 a[212:215], v221 offset:4096\n\t" \
  "v_cvt_pk_bf16_f32 v129, v2, v3\n\t" \
  "v_mfma_f32_16x16x32_bf16 a[20:23], a[136:139], v[136:139], 0\n\t" \
  "s_add_u32 m0, %[m], 65536\n\t" \
  "v_exp_f32 v35, v35\n\t" \
  "v_mfma_f32_16x16x32_bf16 a[20:23], a[140:143], v[140:143], a[20:23]\n\t" \
  "s_add_u32 s96, %[sofk], 49152\n\t" \
  "v_exp_f32 v36, v36\n\t" \
  "v_mfma_f32_16x16x32_bf16 a[36:39], a[144:147], v[136:139], 0\n\t" \
  "v_exp_f32 v37, v37\n\t" \
  "v_mfma_f32_16x16x32_bf16 a[36:39], a[148:151], v[140:143], a[36:39]\n\t" \
  "ds_read_b128 a[216:219], v222 offset:4096\n\t" \
  "v_cvt_pk_bf16_f32 v130, v4, v5\n\t" \
  "v_mfma_f32_16x16x32_bf16 a[52:55], a[152:155], v[136:139], 0\n\t" \
  "v_exp_f32 v38, v38\n\t" \
  "v_mfma_f32_16x16x32_bf16 a[52:55], a[156:159], v[140:143], a[52:55]\n\t" \
  "v_exp_f32 v39, v39\n\t" \
  "v_mfma_f32_16x16x32_bf16 a[68:71], a[160:163], v[136:139], 0\n\t" \
  "v_exp_f32 v40, v40\n\t" \
  "v_mfma_f32_16x16x32_bf16 a[68:71], a[164:167], v[140:143], a[68:71]\n\t" \
  "buffer_load_dwordx4 v224, %[sK], s96 offen offset:0 lds\n\t" \
  "v_mfma_f32_16x16x32_bf16 a[84:87], a[168:171], v[136:139], 0\n\t" \
  "ds_read_b128 a[220:223], v223 offset:4096\n\t" \
  "v_cvt_pk_bf16_f32 v131, v6, v7\n\t" \
  "v_mfma_f32_16x16x32_bf16 a[84:87], a[172:175], v[140:143], a[84:87]\n\t" \
  "v_exp_f32 v41, v41\n\t" \
  "v_mfma_f32_16x16x32_bf16 a[100:103], a[176:179], v[136:139], 0\n\t" \
  "ds_read_b128 a[224:227], v220 offset:8192\n\t" \
  "v_cvt_pk_bf16_f32 v132, v8, v9\n\t" \
  "v_mfma_f32_16x16x32_bf16 a[100:103], a[180:183], v[140:143], a[100:103]\n\t" \
  "buffer_load_dwordx4 v225, %[sK], s96 offen offset:1024 lds\n\t" \
  "v_mfma_f32_16x16x32_bf16 a[116:119], a[184:187], v[136:139], 0\n\t" \
  "v_exp_f32 v42, v42\n\t" \
  "v_mfma_f32_16x16x32_bf16 a[116:119], a[188:191], v[140:143], a[116:119]\n\t" \
  "v_exp_f32 v43, v43\n\t" \
  "v_mfma_f32_16x16x32_bf16 v[164:167], v[216:219], v[136:139], 0\n\t" \
  "buffer_load_dwordx4 v226, %[sK], s96 offen offset:2048 lds\n\t" \
  "v_mfma_f32_16x16x32_bf16 v[164:167], v[216:219], v[140:143], v[164:167]\n\t" \
  "ds_read_b128 a[228:231], v221 offset:8192\n\t" \
  "v_cvt_pk_bf16_f32 v133, v10, v11\n\t" \
  "v_mfma_f32_16x16x32_bf16 a[8:11], a[128:131], v[144:147], 0\n\t" \
  "v_exp_f32 v44, v44\n\t" \
  "v_mfma_f32_16x16x32_bf16 a[8:11], a[132:135], v[148:151], a[8:11]\n\t" \
  "v_exp_f32 v45, v45\n\t" \
  "v_mfma_f32_16x16x32_bf16 a[24:27], a[136:139], v[144:147], 0\n\t" \
  "buffer_load_dwordx4 v227, %[sK], s96 offen offset:3072 lds\n\t" \
  "v_mfma_f32_16x16x32_bf16 a[24:27], a[140:143], v[148:151], a[24:27]\n\t" \
  "ds_read_b128 a[232:235], v222 offset:8192\n\t" \
  "v_cvt_pk_bf16_f32 v134, v12, v13\n\t" \
  "v_mfma_f32_16x16x32_bf16 a[40:43], a[144:147], v[144:147], 0\n\t" \
  "v_exp_f32 v46, v46\n\t" \
  "v_mfma_f32_16x16x32_bf16 a[40:43], a[148:151], v[148:151], a[40:43]\n\t" \
  "s_add_u32 m0, %[m], 114688\n\t" \
  "s_mov_b32 s96, 0\n\t" \
  "ds_read_b128 a[236:239], v223 offset:8192\n\t" \
  "v_mfma_f32_16x16x32_bf16 a[56:59], a[152:155], v[144:147], 0\n\t" \
  "buffer_load_dwordx4 v232, %[sU], s96 offen offset:0 lds\n\t" \
  "v_mfma_f32_16x16x32_bf16 a[56:59], a[156:159], v[148:151], a[56:59]\n\t" \
  "s_add_u32 m0, %[m], 81920\n\t" \
  "v_exp_f32 v47, v47\n\t" \
  "v_mfma_f32_16x16x32_bf16 a[72:75], a[160:163], v[144:147], 0\n\t" \
  "s_add_u32 s96, %[sofk], 32768\n\t" \
  "v_exp_f32 v64, v64\n\t" \
  "v_mfma_f32_16x16x32_bf16 a[72:75], a[164:167], v[148:151], a[72:75]\n\t" \
  "ds_read_b128 a[240:243], v220 offset:12288\n\t" \
  "v_cvt_pk_bf16_f32 v135, v14, v15\n\t" \
  "v_mfma_f32_16x16x32_bf16 a[88:91], a[168:171], v[144:147], 0\n\t" \
  "v_exp_f32 v65, v65\n\t" \
  "v_mfma_f32_16x16x32_bf16 a[88:91], a[172:175], v[148:151], a[88:91]\n\t" \
  "v_exp_f32 v66, v66\n\t" \
  "v_mfma_f32_16x16x32_bf16 a[104:107], a[176:179], v[144:147], 0\n\t" \
  "v_exp_f32 v67, v67\n\t" \
  "v_mfma_f32_16x16x32_bf16 a[104:107], a[180:183], v[148:151], a[104:107]\n\t" \
  "ds_read_b128 a[244:247], v221 offset:12288\n\t" \
  "v_cvt_pk_bf16_f32 v136, v32, v33\n\t" \
  "v_mfma_f32_16x16x32_bf16 a[120:123], a[184:187], v[144:147], 0\n\t" \
  "v_exp_f32 v68, v68\n\t" \
  "v_mfma_f32_16x16x32_bf16 a[120:123], a[188:191], v[148:151], a[120:123]\n\t" \
  "buffer_load_dwordx4 v230, %[sV], s96 offen offset:0 lds\n\t" \
  "v_mfma_f32_16x16x32_bf16 v[168:171], v[216:219], v[144:147], 0\n\t" \
  "v_exp_f32 v69, v69\n\t" \
  "v_mfma_f32_16x16x32_bf16 v[168:171], v[216:219], v[148:151], v[168:171]\n\t" \
  "ds_read_b128 a[248:251], v222 offset:12288\n\t" \
  "v_cvt_pk_bf16_f32 v137, v34, v35\n\t" \
  "v_mfma_f32_16x16x32_bf16 a[12:15], a[128:131], v[152:155], 0\n\t" \
  "v_exp_f32 v70, v70\n\t" \
  "v_mfma_f32_16x16x32_bf16 a[12:15], a[132:135], v[156:159], a[12:15]\n\t" \
  "buffer_load_dwordx4 v231, %[sV], s96 offen offset:1024 lds\n\t" \
  "v_mfma_f32_16x16x32_bf16 a[28:31], a[136:139], v[152:155], 0\n\t" \
  "v_exp_f32 v71, v71\n\t" \
  "v_mfma_f32_16x16x32_bf16 a[28:31], a[140:143], v[156:159], a[28:31]\n\t" \
  "ds_read_b128 a[252:255], v223 offset:12288\n\t" \
  "v_cvt_pk_bf16_f32 v138, v36, v37\n\t" \
  "v_mfma_f32_16x16x32_bf16 a[44:47], a[144:147], v[152:155], 0\n\t" \
  "buffer_load_dwordx4 v230, %[sV], s96 offen offset:2048 lds\n\t" \
  "v_mfma_f32_16x16x32_bf16 a[44:47], a[148:151], v[156:159], a[44:47]\n\t" \
  "v_exp_f32 v72, v72\n\t" \
  "v_mfma_f32_16x16x32_bf16 a[60:63], a[152:155], v[152:155], 0\n\t" \
  "v_exp_f32 v73, v73\n\t" \
  "v_mfma_f32_16x16x32_bf16 a[60:63], a[156:159], v[156:159], a[60:63]\n\t" \
  "v_exp_f32 v74, v74\n\t" \
  "v_mfma_f32_16x16x32_bf16 a[76:79], a[160:163], v[152:155], 0\n\t" \
  "buffer_load_dwordx4 v231, %[sV], s96 offen offset:3072 lds\n\t" \
  "v_mfma_f32_16x16x32_bf16 a[76:79], a[164:167], v[156:159], a[76:79]\n\t" \
  "v_exp_f32 v75, v75\n\t" \
  "v_mfma_f32_16x16x32_bf16 a[92:95], a[168:171], v[152:155], 0\n\t" \
  "v_exp_f32 v76, v76\n\t" \
  "v_mfma_f32_16x16x32_bf16 a[92:95], a[172:175], v[156:159], a[92:95]\n\t" \
  "v_cvt_pk_bf16_f32 v139, v38, v39\n\t" \
  "v_cvt_pk_bf16_f32 v140, v40, v41\n\t" \
  "v_mfma_f32_16x16x32_bf16 a[108:111], a[176:179], v[152:155], 0\n\t" \
  "s_add_u32 m0, %[m], 114688\n\t" \
  "s_mov_b32 s96, 6144\n\t" \
  "buffer_load_dwordx4 v232, %[sU], s96 offen offset:1024 lds\n\t" \
  "v_mfma_f32_16x16x32_bf16 a[108:111], a[180:183], v[156:159], a[108:111]\n\t" \
  "v_exp_f32 v77, v77\n\t" \
  "v_mfma_f32_16x16x32_bf16 a[124:127], a[184:187], v[152:155], 0\n\t" \
  "v_exp_f32 v78, v78\n\t" \
  "v_mfma_f32_16x16x32_bf16 a[124:127], a[188:191], v[156:159], a[124:127]\n\t" \
  "v_exp_f32 v79, v79\n\t" \
  "v_mfma_f32_16x16x32_bf16 v[172:175], v[216:219], v[152:155], 0\n\t" \
  "v_exp_f32 v96, v96\n\t" \
  "v_mfma_f32_16x16x32_bf16 v[172:175], v[216:219], v[156:159], v[172:175]\n\t" \
  "s_waitcnt lgkmcnt(0)\n\t" \
  "v_exp_f32 v97, v97\n\t" \
  STA("s_memrealtime s[94:95]\n\t") \
  STA("s_memtime s[92:93]\n\t") \
  "v_mfma_f32_16x16x32_bf16 v[0:3], a[192:195], v[16:19], v[176:179]\n\t" \
  ".Lhs6:\n\ts_cbranch_execz .Lhs6n\n\t" \
  "ds_read_b64_tr_b16 a[128:129], v237 offset:0\n\t" \
  "v_cvt_pk_bf16_f32 v141, v42, v43\n\t" \
  "v_mfma_f32_16x16x32_bf16 v[0:3], a[196:199], v[20:23], v[0:3]\n\t" \
  "v_exp_f32 v98, v98\n\t" \
  "v_mfma_f32_16x16x32_bf16 v[0:3], a[200:203], v[24:27], v[0:3]\n\t" \
  "ds_read_b64_tr_b16 a[130:131], v237 offset:4096\n\t" \
  "v_cvt_pk_bf16_f32 v142, v44, v45\n\t" \
  "v_mfma_f32_16x16x32_bf16 v[0:3], a[204:207], v[28:31], v[0:3]\n\t" \
  "v_exp_f32 v99, v99\n\t" \
  "v_mfma_f32_16x16x32_bf16 v[4:7], a[220:223], v[28:31], v[176:179]\n\t" \
  "ds_read_b64_tr_b16 a[132:133], v237 offset:8192\n\t" \
  "v_cvt_pk_bf16_f32 v143, v46, v47\n\t" \
  "v_mfma_f32_16x16x32_bf16 v[4:7], a[216:219], v[24:27], v[4:7]\n\t" \
  "v_exp_f32 v100, v100\n\t" \
  "v_mfma_f32_16x16x32_bf16 v[4:7], a[212:215], v[20:23], v[4:7]\n\t" \
  "ds_read_b64_tr_b16 a[134:135], v237 offset:12288\n\t" \
  "v_cvt_pk_bf16_f32 v144, v64, v65\n\t" \
  "v_mfma_f32_16x16x32_bf16 v[4:7], a[208:211], v[16:19], v[4:7]\n\t" \
  "ds_read_b64_tr_b16 a[136:137], v238 offset:0\n\t" \
  "v_cvt_pk_bf16_f32 v145, v66, v67\n\t" \
  "v_mfma_f32_16x16x32_bf16 v[8:11], a[224:227], v[16:19], v[176:179]\n\t" \
  "s_add_u32 m0, %[m], 114688\n\t" \
  "s_mov_b32 s96, 12288\n\t" \
  "ds_read_b64_tr_b16 a[138:139], v238 offset:4096\n\t" \
  "v_mfma_f32_16x16x32_bf16 v[8:11], a[228:231], v[20:23], v[8:11]\n\t" \
  "buffer_load_dwordx4 v232, %[sU], s96 offen offset:2048 lds\n\t" \
  "v_mfma_f32_16x16x32_bf16 v[8:11], a[232:235], v[24:27], v[8:11]\n\t" \
  "s_mov_b32 s96, 18432\n\t" \
  "v_exp_f32 v101, v101\n\t" \
  "v_mfma_f32_16x16x32_bf16 v[8:11], a[236:239], v[28:31], v[8:11]\n\t" \
  "ds_read_b64_tr_b16 a[140:141], v238 offset:8192\n\t" \
  "v_cvt_pk_bf16_f32 v146, v68, v69\n\t" \
  "v_mfma_f32_16x16x32_bf16 v[12:15], a[252:255], v[28:31], v[176:179]\n\t" \
  STA("v_writelane_b32 %[stv], s94, 30\n\t") \
  STA("v_writelane_b32 %[stv], s92, 31\n\t") \
  "v_exp_f32 v102, v102\n\t" \
  "v_mfma_f32_16x16x32_bf16 v[12:15], a[248:251], v[24:27], v[12:15]\n\t" \
  "ds_read_b64_tr_b16 a[142:143], v238 offset:12288\n\t" \
  "v_cvt_pk_bf16_f32 v147, v70, v71\n\t" \
  "v_mfma_f32_16x16x32_bf16 v[12:15], a[244:247], v[20:23], v[12:15]\n\t" \
  "ds_read_b64_tr_b16 a[144:145], v237 offset:256\n\t" \
  "v_cvt_pk_bf16_f32 v148, v72, v73\n\t" \
  "v_mfma_f32_16x16x32_bf16 v[12:15], a[240:243], v[16:19], v[12:15]\n\t" \
  "v_exp_f32 v103, v103\n\t" \
  "v_mfma_f32_16x16x32_bf16 v[32:35], a[192:195], v[48:51], v[180:183]\n\t" \
  "ds_read_b64_tr_b16 a[146:147], v237 offset:4352\n\t" \
  "v_cvt_pk_bf16_f32 v149, v74, v75\n\t" \
  "v_mfma_f32_16x16x32_bf16 v[32:35], a[196:199], v[52:55], v[32:35]\n\t" \
  "v_exp_f32 v104, v104\n\t" \
  "v_mfma_f32_16x16x32_bf16 v[32:35], a[200:203], v[56:59], v[32:35]\n\t" \
  "ds_read_b64_tr_b16 a[148:149], v237 offset:8448\n\t" \
  "v_cvt_pk_bf16_f32 v150, v76, v77\n\t" \
  "v_mfma_f32_16x16x32_bf16 v[32:35], a[204:207], v[60:63], v[32:35]\n\t" \
  "v_exp_f32 v105, v105\n\t" \
  "v_mfma_f32_16x16x32_bf16 v[36:39], a[220:223], v[60:63], v[180:183]\n\t" \
  "ds_read_b64_tr_b16 a[150:151], v237 offset:12544\n\t" \
  "v_cvt_pk_bf16_f32 v151, v78, v79\n\t" \
  "v_mfma_f32_16x16x32_bf16 v[36:39], a[216:219], v[56:59], v[36:39]\n\t" \
  "buffer_load_dwordx4 v232, %[sU], s96 offen offset:3072 lds\n\t" \
  "v_mfma_f32_16x16x32_bf16 v[36:39], a[212:215], v[52:55], v[36:39]\n\t" \
  "ds_read_b64_tr_b16 a[152:153], v238 offset:256\n\t" \
  "v_cvt_pk_bf16_f32 v152, v96, v97\n\t" \
  "v_mfma_f32_16x16x32_bf16 v[36:39], a[208:211], v[48:51], v[36:39]\n\t" \
  "ds_read_b64_tr_b16 a[154:155], v238 offset:4352\n\t" \
  "v_cvt_pk_bf16_f32 v153, v98, v99\n\t" \
  "v_mfma_f32_16x16x32_bf16 v[40:43], a[224:227], v[48:51], v[180:183]\n\t" \
  "v_exp_f32 v106, v106\n\t" \
  "v_mfma_f32_16x16x32_bf16 v[40:43], a[228:231], v[52:55], v[40:43]\n\t" \
  "ds_read_b64_tr_b16 a[156:157], v238 offset:8448\n\t" \
  "v_cvt_pk_bf16_f32 v154, v100, v101\n\t" \
  "v_mfma_f32_16x16x32_bf16 v[40:43], a[232:235], v[56:59], v[40:43]\n\t" \
  "v_exp_f32 v107, v107\n\t" \
  "v_mfma_f32_16x16x32_bf16 v[40:43], a[236:239], v[60:63], v[40:43]\n\t" \
  "ds_read_b64_tr_b16 a[158:159], v238 offset:12544\n\t" \
  "v_cvt_pk_bf16_f32 v155, v102, v103\n\t" \
  "v_mfma_f32_16x16x32_bf16 v[44:47], a[252:255], v[60:63], v[180:183]\n\t" \
  "ds_read_b64_tr_b16 a[160:161], v237 offset:512\n\t" \
  "v_cvt_pk_bf16_f32 v156, v104, v105\n\t" \
  "v_mfma_f32_16x16x32_bf16 v[44:47], a[248:251], v[56:59], v[44:47]\n\t" \
  "v_exp_f32 v108, v108\n\t" \
  "v_mfma_f32_16x16x32_bf16 v[44:47], a[244:247], v[52:55], v[44:47]\n\t" \
  "ds_read_b64_tr_b16 a[162:163], v237 offset:4608\n\t" \
  "v_cvt_pk_bf16_f32 v157, v106, v107\n\t" \
  "v_mfma_f32_16x16x32_bf16 v[44:47], a[240:243], v[48:51], v[44:47]\n\t" \
  "v_exp_f32 v109, v109\n\t" \
  "v_mfma_f32_16x16x32_bf16 v[64:67], a[192:195], v[80:83], v[184:187]\n\t" \
  "ds_read_b64_tr_b16 a[164:165], v237 offset:8704\n\t" \
  "v_cvt_pk_bf16_f32 v158, v108, v109\n\t" \
  "v_mfma_f32_16x16x32_bf16 v[64:67], a[196:199], v[84:87], v[64:67]\n\t" \
  "v_exp_f32 v110, v110\n\t" \
  "v_mfma_f32_16x16x32_bf16 v[64:67], a[200:203], v[88:91], v[64:67]\n\t" \
  "ds_read_b64_tr_b16 a[166:167], v237 offset:12800\n\t" \
  "ds_read_b64_tr_b16 a[168:169], v238 offset:512\n\t" \
  "v_mfma_f32_16x16x32_bf16 v[64:67], a[204:207], v[92:95], v[64:67]\n\t" \
  "s_add_u32 m0, %[m], 131072\n\t" \
  "s_mov_b32 s96, 28672\n\t" \
  "buffer_load_dwordx4 v232, %[sU], s96 offen offset:0 lds\n\t" \
  "v_mfma_f32_16x16x32_bf16 v[68:71], a[220:223], v[92:95], v[184:187]\n\t" \
  "s_mov_b32 s96, 34816\n\t" \
  "v_exp_f32 v111, v111\n\t" \
  "v_mfma_f32_16x16x32_bf16 v[68:71], a[216:219], v[88:91], v[68:71]\n\t" \
  "ds_read_b64_tr_b16 a[170:171], v238 offset:4608\n\t" \
  "v_cvt_pk_bf16_f32 v159, v110, v111\n\t" \
  "v_mfma_f32_16x16x32_bf16 v[68:71], a[212:215], v[84:87], v[68:71]\n\t" \
  "v_exp_f32 v0, v0\n\t" \
  "v_mfma_f32_16x16x32_bf16 v[68:71], a[208:211], v[80:83], v[68:71]\n\t" \
  "v_exp_f32 v1, v1\n\t" \
  "v_mfma_f32_16x16x32_bf16 v[72:75], a[224:227], v[80:83], v[184:187]\n\t" \
  "v_exp_f32 v2, v2\n\t" \
  "v_mfma_f32_16x16x32_bf16 v[72:75], a[228:231], v[84:87], v[72:75]\n\t" \
  "ds_read_b64_tr_b16 a[172:173], v238 offset:8704\n\t" \
  "ds_read_b64_tr_b16 a[174:175], v238 offset:12800\n\t" \
  "v_mfma_f32_16x16x32_bf16 v[72:75], a[232:235], v[88:91], v[72:75]\n\t" \
  "v_exp_f32 v3, v3\n\t" \
  "v_mfma_f32_16x16x32_bf16 v[72:75], a[236:239], v[92:95], v[72:75]\n\t" \
  "v_exp_f32 v4, v4\n\t" \
  "v_mfma_f32_16x16x32_bf16 v[76:79], a[252:255], v[92:95], v[184:187]\n\t" \
  "ds_read_b64_tr_b16 a[176:177], v237 offset:768\n\t" \
  "ds_read_b64_tr_b16 a[178:179], v237 offset:4864\n\t" \
  "v_mfma_f32_16x16x32_bf16 v[76:79], a[248:251], v[88:91], v[76:79]\n\t" \
  "v_exp_f32 v5, v5\n\t" \
  "v_mfma_f32_16x16x32_bf16 v[76:79], a[244:247], v[84:87], v[76:79]\n\t" \
  "v_exp_f32 v6, v6\n\t" \
  "v_mfma_f32_16x16x32_bf16 v[76:79], a[240:243], v[80:83], v[76:79]\n\t" \
  "buffer_load_dwordx4 v232, %[sU], s96 offen offset:1024 lds\n\t" \
  "v_mfma_f32_16x16x32_bf16 v[96:99], a[192:195], v[112:115], v[188:191]\n\t" \
  "ds_read_b64_tr_b16 a[180:181], v237 offset:8960\n\t" \
  "ds_read_b64_tr_b16 a[182:183], v237 offset:13056\n\t" \
  "v_mfma_f32_16x16x32_bf16 v[96:99], a[196:199], v[116:119], v[96:99]\n\t" \
  "v_exp_f32 v7, v7\n\t" \
  "v_mfma_f32_16x16x32_bf16 v[96:99], a[200:203], v[120:123], v[96:99]\n\t" \
  "v_exp_f32 v8, v8\n\t" \
  "v_mfma_f32_16x16x32_bf16 v[96:99], a[204:207], v[124:127], v[96:99]\n\t" \
  "ds_read_b64_tr_b16 a[184:185], v238 offset:768\n\t" \
  "ds_read_b64_tr_b16 a[186:187], v238 offset:4864\n\t" \
  "v_mfma_f32_16x16x32_bf16 v[100:103], a[220:223], v[124:127], v[188:191]\n\t" \
  "v_exp_f32 v9, v9\n\t" \
  "v_mfma_f32_16x16x32_bf16 v[100:103], a[216:219], v[120:123], v[100:103]\n\t" \
  "v_exp_f32 v10, v10\n\t" \
  "v_mfma_f32_16x16x32_bf16 v[100:103], a[212:215], v[116:119], v[100:103]\n\t" \
  "v_exp_f32 v11, v11\n\t" \
  "v_mfma_f32_16x16x32_bf16 v[100:103], a[208:211], v[112:115], v[100:103]\n\t" \
  "ds_read_b64_tr_b16 a[188:189], v238 offset:8960\n\t" \
  "ds_read_b64_tr_b16 a[190:191], v238 offset:13056\n\t" \
  "v_mfma_f32_16x16x32_bf16 v[104:107], a[224:227], v[112:115], v[188:191]\n\t" \
  "v_exp_f32 v12, v12\n\t" \
  "v_mfma_f32_16x16x32_bf16 v[104:107], a[228:231], v[116:119], v[104:107]\n\t" \
  "v_exp_f32 v13, v13\n\t" \
  "v_mfma_f32_16x16x32_bf16 v[104:107], a[232:235], v[120:123], v[104:107]\n\t" \
  "v_exp_f32 v14, v14\n\t" \
  "v_mfma_f32_16x16x32_bf16 v[104:107], a[236:239], v[124:127], v[104:107]\n\t" \
  "v_exp_f32 v15, v15\n\t" \
  "v_mfma_f32_16x16x32_bf16 v[108:111], a[252:255], v[124:127], v[188:191]\n\t" \
  "v_exp_f32 v32, v32\n\t" \
  "v_mfma_f32_16x16x32_bf16 v[108:111], a[248:251], v[120:123], v[108:111]\n\t" \
  "v_exp_f32 v33, v33\n\t" \
  "v_mfma_f32_16x16x32_bf16 v[108:111], a[244:247], v[116:119], v[108:111]\n\t" \
  "v_exp_f32 v34, v34\n\t" \
  "v_mfma_f32_16x16x32_bf16 v[108:111], a[240:243], v[112:115], v[108:111]\n\t" \
  "s_waitcnt vmcnt(5) lgkmcnt(0)\n\t" \
  "s_barrier\n\t" \
  STA("s_memrealtime s[94:95]\n\t") \
  STA("s_memtime s[92:93]\n\t") \
  "v_mfma_f32_16x16x32_bf16 a[0:3], a[128:131], v[128:131], a[0:3]\n\t" \
  "v_exp_f32 v35, v35\n\t" \
  "v_mfma_f32_16x16x32_bf16 a[0:3], a[132:135], v[132:135], a[0:3]\n\t" \
  "v_exp_f32 v36, v36\n\t" \
  "v_mfma_f32_16x16x32_bf16 a[16:19], a[136:139], v[128:131], a[16:19]\n\t" \
  "ds_read_b128 a[192:195], v220 offset:16384\n\t" \
  "ds_read_b128 a[196:199], v221 offset:16384\n\t" \
  "v_mfma_f32_16x16x32_bf16 a[16:19], a[140:143], v[132:135], a[16:19]\n\t" \
  "v_exp_f32 v37, v37\n\t" \
  "v_mfma_f32_16x16x32_bf16 a[32:35], a[144:147], v[128:131], a[32:35]\n\t" \
  "v_exp_f32 v38, v38\n\t" \
  "v_mfma_f32_16x16x32_bf16 a[32:35], a[148:151], v[132:135], a[32:35]\n\t" \
  "v_exp_f32 v39, v39\n\t" \
  "v_mfma_f32_16x16x32_bf16 a[48:51], a[152:155], v[128:131], a[48:51]\n\t" \
  "v_exp_f32 v40, v40\n\t" \
  "v_mfma_f32_16x16x32_bf16 a[48:51], a[156:159], v[132:135], a[48:51]\n\t" \
  "v_exp_f32 v41, v41\n\t" \
  "v_mfma_f32_16x16x32_bf16 a[64:67], a[160:163], v[128:131], a[64:67]\n\t" \
  "v_exp_f32 v42, v42\n\t" \
  "v_mfma_f32_16x16x32_bf16 a[64:67], a[164:167], v[132:135], a[64:67]\n\t" \
  "v_exp_f32 v43, v43\n\t" \
  "v_mfma_f32_16x16x32_bf16 a[80:83], a[168:171], v[128:131], a[80:83]\n\t" \
  "ds_read_b128 a[200:203], v222 offset:16384\n\t" \
  "ds_read_b128 a[204:207], v223 offset:16384\n\t" \
  "v_mfma_f32_16x16x32_bf16 a[80:83], a[172:175], v[132:135], a[80:83]\n\t" \
  "v_exp_f32 v44, v44\n\t" \
  "v_mfma_f32_16x16x32_bf16 a[96:99], a[176:179], v[128:131], a[96:99]\n\t" \
  STA("v_writelane_b32 %[stv], s94, 32\n\t") \
  STA("v_writelane_b32 %[stv], s92, 33\n\t") \
  "buffer_store_dword v233, v247, %[ws], 0 offen sc1\n\t" \
  "v_mfma_f32_16x16x32_bf16 a[96:99], a[180:183], v[132:135], a[96:99]\n\t" \
  "v_exp_f32 v45, v45\n\t" \
  "v_mfma_f32_16x16x32_bf16 a[112:115], a[184:187], v[128:131], a[112:115]\n\t" \
  "v_exp_f32 v46, v46\n\t" \
  "v_mfma_f32_16x16x32_bf16 a[112:115], a[188:191], v[132:135], a[112:115]\n\t" \
  "v_exp_f32 v47, v47\n\t" \
  "v_mfma_f32_16x16x32_bf16 v[160:163], v[216:219], v[128:131], v[160:163]\n\t" \
  "v_exp_f32 v64, v64\n\t" \
  "v_mfma_f32_16x16x32_bf16 v[160:163], v[216:219], v[132:135], v[160:163]\n\t" \
  "ds_read_b128 a[208:211], v220 offset:20480\n\t" \
  "v_cvt_pk_bf16_f32 v128, v0, v1\n\t" \
  "v_mfma_f32_16x16x32_bf16 a[4:7], a[128:131], v[136:139], a[4:7]\n\t" \
  "v_exp_f32 v65, v65\n\t" \
  "v_mfma_f32_16x16x32_bf16 a[4:7], a[132:135], v[140:143], a[4:7]\n\t" \
  "ds_read_b128 a[212:215], v221 offset:20480\n\t" \
  "v_cvt_pk_bf16_f32 v129, v2, v3\n\t" \
  "v_mfma_f32_16x16x32_bf16 a[20:23], a[136:139], v[136:139], a[20:23]\n\t" \
  "s_add_u32 m0, %[m], 49152\n\t" \
  "v_exp_f32 v66, v66\n\t" \
  "v_mfma_f32_16x16x32_bf16 a[20:23], a[140:143], v[140:143], a[20:23]\n\t" \
  "s_add_u32 s96, %[sofk], 65536\n\t" \
  "v_exp_f32 v67, v67\n\t" \
  "v_mfma_f32_16x16x32_bf16 a[36:39], a[144:147], v[136:139], a[36:39]\n\t" \
  "v_exp_f32 v68, v68\n\t" \
  "v_mfma_f32_16x16x32_bf16 a[36:39], a[148:151], v[140:143], a[36:39]\n\t" \
  "ds_read_b128 a[216:219], v222 offset:20480\n\t" \
  "v_cvt_pk_bf16_f32 v130, v4, v5\n\t" \
  "v_mfma_f32_16x16x32_bf16 a[52:55], a[152:155], v[136:139], a[52:55]\n\t" \
  "v_exp_f32 v69, v69\n\t" \
  "v_mfma_f32_16x16x32_bf16 a[52:55], a[156:159], v[140:143], a[52:55]\n\t" \
  "v_exp_f32 v70, v70\n\t" \
  "v_mfma_f32_16x16x32_bf16 a[68:71], a[160:163], v[136:139], a[68:71]\n\t" \
  "v_exp_f32 v71, v71\n\t" \
  "v_mfma_f32_16x16x32_bf16 a[68:71], a[164:167], v[140:143], a[68:71]\n\t" \
  "buffer_load_dwordx4 v224, %[sK], s96 offen offset:0 lds\n\t" \
  "v_mfma_f32_16x16x32_bf16 a[84:87], a[168:171], v[136:139], a[84:87]\n\t" \
  "ds_read_b128 a[220:223], v223 offset:20480\n\t" \
  "v_cvt_pk_bf16_f32 v131, v6, v7\n\t" \
  "v_mfma_f32_16x16x32_bf16 a[84:87], a[172:175], v[140:143], a[84:87]\n\t" \
  "v_exp_f32 v72, v72\n\t" \
  "v_mfma_f32_16x16x32_bf16 a[100:103], a[176:179], v[136:139], a[100:103]\n\t" \
  "ds_read_b128 a[224:227], v220 offset:24576\n\t" \
  "v_cvt_pk_bf16_f32 v132, v8, v9\n\t" \
  "v_mfma_f32_16x16x32_bf16 a[100:103], a[180:183], v[140:143], a[100:103]\n\t" \
  "buffer_load_dwordx4 v225, %[sK], s96 offen offset:1024 lds\n\t" \
  "v_mfma_f32_16x16x32_bf16 a[116:119], a[184:187], v[136:139], a[116:119]\n\t" \
  "v_exp_f32 v73, v73\n\t" \
  "v_mfma_f32_16x16x32_bf16 a[116:119], a[188:191], v[140:143], a[116:119]\n\t" \
  "v_exp_f32 v74, v74\n\t" \
  "v_mfma_f32_16x16x32_bf16 v[164:167], v[216:219], v[136:139], v[164:167]\n\t" \
  "buffer_load_dwordx4 v226, %[sK], s96 offen offset:2048 lds\n\t" \
  "v_mfma_f32_16x16x32_bf16 v[164:167], v[216:219], v[140:143], v[164:167]\n\t" \
  "ds_read_b128 a[228:231], v221 offset:24576\n\t" \
  "v_cvt_pk_bf16_f32 v133, v10, v11\n\t" \
  "v_mfma_f32_16x16x32_bf16 a[8:11], a[128:131], v[144:147], a[8:11]\n\t" \
  "v_exp_f32 v75, v75\n\t" \
  "v_mfma_f32_16x16x32_bf16 a[8:11], a[132:135], v[148:151], a[8:11]\n\t" \
  "v_exp_f32 v76, v76\n\t" \
  "v_mfma_f32_16x16x32_bf16 a[24:27], a[136:139], v[144:147], a[24:27]\n\t" \
  "buffer_load_dwordx4 v227, %[sK], s96 offen offset:3072 lds\n\t" \
  "v_mfma_f32_16x16x32_bf16 a[24:27], a[140:143], v[148:151], a[24:27]\n\t" \
  "ds_read_b128 a[232:235], v222 offset:24576\n\t" \
  "v_cvt_pk_bf16_f32 v134, v12, v13\n\t" \
  "v_mfma_f32_16x16x32_bf16 a[40:43], a[144:147], v[144:147], a[40:43]\n\t" \
  "v_exp_f32 v77, v77\n\t" \
  "v_mfma_f32_16x16x32_bf16 a[40:43], a[148:151], v[148:151], a[40:43]\n\t" \
  "s_add_u32 m0, %[m], 131072\n\t" \
  "s_mov_b32 s96, 40960\n\t" \
  "ds_read_b128 a[236:239], v223 offset:24576\n\t" \
  "v_mfma_f32_16x16x32_bf16 a[56:59], a[152:155], v[144:147], a[56:59]\n\t" \
  "buffer_load_dwordx4 v232, %[sU], s96 offen offset:2048 lds\n\t" \
  "v_mfma_f32_16x16x32_bf16 a[56:59], a[156:159], v[148:151], a[56:59]\n\t" \
  "s_add_u32 m0, %[m], 147456\n\t" \
  "v_exp_f32 v78, v78\n\t" \
  "v_mfma_f32_16x16x32_bf16 a[72:75], a[160:163], v[144:147], a[72:75]\n\t" \
  "s_add_u32 s96, %[sofk], 49152\n\t" \
  "v_exp_f32 v79, v79\n\t" \
  "v_mfma_f32_16x16x32_bf16 a[72:75], a[164:167], v[148:151], a[72:75]\n\t" \
  "ds_read_b128 a[240:243], v220 offset:28672\n\t" \
  "v_cvt_pk_bf16_f32 v135, v14, v15\n\t" \
  "v_mfma_f32_16x16x32_bf16 a[88:91], a[168:171], v[144:147], a[88:91]\n\t" \
  "v_exp_f32 v96, v96\n\t" \
  "v_mfma_f32_16x16x32_bf16 a[88:91], a[172:175], v[148:151], a[88:91]\n\t" \
  "v_exp_f32 v97, v97\n\t" \
  "v_mfma_f32_16x16x32_bf16 a[104:107], a[176:179], v[144:147], a[104:107]\n\t" \
  "v_exp_f32 v98, v98\n\t" \
  "v_mfma_f32_16x16x32_bf16 a[104:107], a[180:183], v[148:151], a[104:107]\n\t" \
  "ds_read_b128 a[244:247], v221 offset:28672\n\t" \
  "v_cvt_pk_bf16_f32 v136, v32, v33\n\t" \
  "v_mfma_f32_16x16x32_bf16 a[120:123], a[184:187], v[144:147], a[120:123]\n\t" \
  "v_exp_f32 v99, v99\n\t" \
  "v_mfma_f32_16x16x32_bf16 a[120:123], a[188:191], v[148:151], a[120:123]\n\t" \
  "buffer_load_dwordx4 v230, %[sV], s96 offen offset:0 lds\n\t" \
  "v_mfma_f32_16x16x32_bf16 v[168:171], v[216:219], v[144:147], v[168:171]\n\t" \
  "v_exp_f32 v100, v100\n\t" \
  "v_mfma_f32_16x16x32_bf16 v[168:171], v[216:219], v[148:151], v[168:171]\n\t" \
  "ds_read_b128 a[248:251], v222 offset:28672\n\t" \
  "v_cvt_pk_bf16_f32 v137, v34, v35\n\t" \
  "v_mfma_f32_16x16x32_bf16 a[12:15], a[128:131], v[152:155], a[12:15]\n\t" \
  "v_exp_f32 v101, v101\n\t" \
  "v_mfma_f32_16x16x32_bf16 a[12:15], a[132:135], v[156:159], a[12:15]\n\t" \
  "buffer_load_dwordx4 v231, %[sV], s96 offen offset:1024 lds\n\t" \
  "v_mfma_f32_16x16x32_bf16 a[28:31], a[136:139], v[152:155], a[28:31]\n\t" \
  "v_exp_f32 v102, v102\n\t" \
  "v_mfma_f32_16x16x32_bf16 a[28:31], a[140:143], v[156:159], a[28:31]\n\t" \
  "ds_read_b128 a[252:255], v223 offset:28672\n\t" \
  "v_cvt_pk_bf16_f32 v138, v36, v37\n\t" \
  "v_mfma_f32_16x16x32_bf16 a[44:47], a[144:147], v[152:155], a[44:47]\n\t" \
  "buffer_load_dwordx4 v230, %[sV], s96 offen offset:2048 lds\n\t" \
  "v_mfma_f32_16x16x32_bf16 a[44:47], a[148:151], v[156:159], a[44:47]\n\t" \
  "v_exp_f32 v103, v103\n\t" \
  "v_mfma_f32_16x16x32_bf16 a[60:63], a[152:155], v[152:155], a[60:63]\n\t" \
  "v_exp_f32 v104, v104\n\t" \
  "v_mfma_f32_16x16x32_bf16 a[60:63], a[156:159], v[156:159], a[60:63]\n\t" \
  "v_exp_f32 v105, v105\n\t" \
  "v_mfma_f32_16x16x32_bf16 a[76:79], a[160:163], v[152:155], a[76:79]\n\t" \
  "buffer_load_dwordx4 v231, %[sV], s96 offen offset:3072 lds\n\t" \
  "v_mfma_f32_16x16x32_bf16 a[76:79], a[164:167], v[156:159], a[76:79]\n\t" \
  "v_exp_f32 v106, v106\n\t" \
  "v_mfma_f32_16x16x32_bf16 a[92:95], a[168:171], v[152:155], a[92:95]\n\t" \
  "v_exp_f32 v107, v107\n\t" \
  "v_mfma_f32_16x16x32_bf16 a[92:95], a[172:175], v[156:159], a[92:95]\n\t" \
  "v_cvt_pk_bf16_f32 v139, v38, v39\n\t" \
  "v_cvt_pk_bf16_f32 v140, v40, v41\n\t" \
  "v_mfma_f32_16x16x32_bf16 a[108:111], a[176:179], v[152:155], a[108:111]\n\t" \
  "s_add_u32 m0, %[m], 131072\n\t" \
  "s_mov_b32 s96, 47104\n\t" \
  "buffer_load_dwordx4 v232, %[sU], s96 offen offset:3072 lds\n\t" \
  "v_mfma_f32_16x16x32_bf16 a[108:111], a[180:183], v[156:159], a[108:111]\n\t" \
  "v_exp_f32 v108, v108\n\t" \
  "v_mfma_f32_16x16x32_bf16 a[124:127], a[184:187], v[152:155], a[124:127]\n\t" \
  "v_exp_f32 v109, v109\n\t" \
  "v_mfma_f32_16x16x32_bf16 a[124:127], a[188:191], v[156:159], a[124:127]\n\t" \
  "v_exp_f32 v110, v110\n\t" \
  "v_mfma_f32_16x16x32_bf16 v[172:175], v[216:219], v[152:155], v[172:175]\n\t" \
  "v_cvt_pk_bf16_f32 v141, v42, v43\n\t" \
  "v_cvt_pk_bf16_f32 v142, v44, v45\n\t" \
  "v_mfma_f32_16x16x32_bf16 v[172:175], v[216:219], v[156:159], v[172:175]\n\t" \
  "s_waitcnt lgkmcnt(0)\n\t" \
  "s_mul_i32 s98, %[sofk], 5\n\t" \
  "s_xor_b32 s98, s98, 8192\n\t" \
  "s_lshr_b32 s98, s98, 1\n\t" \
  "v_cvt_pk_bf16_f32 v143, v46, v47\n\t" \
  STA("s_memrealtime s[94:95]\n\t") \
  STA("s_memtime s[92:93]\n\t") \
  "v_mfma_f32_16x16x32_bf16 v[0:3], a[192:195], v[16:19], v[176:179]\n\t" \
  "buffer_load_dword %[pfd], v204, %[sD], s98 offen\n\t" \
  "v_mfma_f32_16x16x32_bf16 v[0:3], a[196:199], v[20:23], v[0:3]\n\t" \
  "ds_read_b64_tr_b16 a[128:129], v228 offset:0\n\t" \
  "v_cvt_pk_bf16_f32 v144, v64, v65\n\t" \
  "v_mfma_f32_16x16x32_bf16 v[0:3], a[200:203], v[24:27], v[0:3]\n\t" \
  "ds_read_b64_tr_b16 a[130:131], v228 offset:4096\n\t" \
  "v_cvt_pk_bf16_f32 v145, v66, v67\n\t" \
  "v_mfma_f32_16x16x32_bf16 v[0:3], a[204:207], v[28:31], v[0:3]\n\t" \
  "v_exp_f32 v111, v111\n\t" \
  "v_mfma_f32_16x16x32_bf16 v[4:7], a[220:223], v[28:31], v[176:179]\n\t" \
  "ds_read_b64_tr_b16 a[132:133], v228 offset:8192\n\t" \
  "v_cvt_pk_bf16_f32 v146, v68, v69\n\t" \
  "v_mfma_f32_16x16x32_bf16 v[4:7], a[216:219], v[24:27], v[4:7]\n\t" \
  "ds_read_b64_tr_b16 a[134:135], v228 offset:12288\n\t" \
  "v_cvt_pk_bf16_f32 v147, v70, v71\n\t" \
  "v_mfma_f32_16x16x32_bf16 v[4:7], a[212:215], v[20:23], v[4:7]\n\t" \
  "v_exp_f32 v0, v0\n\t" \
  "v_mfma_f32_16x16x32_bf16 v[4:7], a[208:211], v[16:19], v[4:7]\n\t" \
  "ds_read_b64_tr_b16 a[136:137], v229 offset:0\n\t" \
  "v_cvt_pk_bf16_f32 v148, v72, v73\n\t" \
  "v_mfma_f32_16x16x32_bf16 v[8:11], a[224:227], v[16:19], v[176:179]\n\t" \
  "v_exp_f32 v1, v1\n\t" \
  "v_mfma_f32_16x16x32_bf16 v[8:11], a[228:231], v[20:23], v[8:11]\n\t" \
  "ds_read_b64_tr_b16 a[138:139], v229 offset:4096\n\t" \
  "v_cvt_pk_bf16_f32 v149, v74, v75\n\t" \
  "v_mfma_f32_16x16x32_bf16 v[8:11], a[232:235], v[24:27], v[8:11]\n\t" \
  "v_exp_f32 v2, v2\n\t" \
  "v_mfma_f32_16x16x32_bf16 v[8:11], a[236:239], v[28:31], v[8:11]\n\t" \
  "ds_read_b64_tr_b16 a[140:141], v229 offset:8192\n\t" \
  "v_cvt_pk_bf16_f32 v150, v76, v77\n\t" \
  "v_mfma_f32_16x16x32_bf16 v[12:15], a[252:255], v[28:31], v[176:179]\n\t" \
  STA("v_writelane_b32 %[stv], s94, 34\n\t") \
  STA("v_writelane_b32 %[stv], s92, 35\n\t") \
  "s_mul_i32 s98, %[sofk], 5\n\t" \
  "s_xor_b32 s98, s98, 12288\n\t" \
  "s_lshr_b32 s98, s98, 1\n\t" \
  "v_cvt_pk_bf16_f32 v151, v78, v79\n\t" \
  "v_mfma_f32_16x16x32_bf16 v[12:15], a[248:251], v[24:27], v[12:15]\n\t" \
  "buffer_load_dword %[pfd], v204, %[sD], s98 offen\n\t" \
  "v_mfma_f32_16x16x32_bf16 v[12:15], a[244:247], v[20:23], v[12:15]\n\t" \
  "ds_read_b64_tr_b16 a[142:143], v229 offset:12288\n\t" \
  "v_cvt_pk_bf16_f32 v152, v96, v97\n\t" \
  "v_mfma_f32_16x16x32_bf16 v[12:15], a[240:243], v[16:19], v[12:15]\n\t" \
  "ds_read_b64_tr_b16 a[144:145], v228 offset:256\n\t" \
  "v_cvt_pk_bf16_f32 v153, v98, v99\n\t" \
  "v_mfma_f32_16x16x32_bf16 v[32:35], a[192:195], v[48:51], v[180:183]\n\t" \
  "ds_read_b64_tr_b16 a[146:147], v228 offset:4352\n\t" \
  "v_cvt_pk_bf16_f32 v154, v100, v101\n\t" \
  "v_mfma_f32_16x16x32_bf16 v[32:35], a[196:199], v[52:55], v[32:35]\n\t" \
  "v_exp_f32 v3, v3\n\t" \
  "v_mfma_f32_16x16x32_bf16 v[32:35], a[200:203], v[56:59], v[32:35]\n\t" \
  "ds_read_b64_tr_b16 a[148:149], v228 offset:8448\n\t" \
  "v_cvt_pk_bf16_f32 v155, v102, v103\n\t" \
  "v_mfma_f32_16x16x32_bf16 v[32:35], a[204:207], v[60:63], v[32:35]\n\t" \
  "v_exp_f32 v4, v4\n\t" \
  "v_mfma_f32_16x16x32_bf16 v[36:39], a[220:223], v[60:63], v[180:183]\n\t" \
  "ds_read_b64_tr_b16 a[150:151], v228 offset:12544\n\t" \
  "v_cvt_pk_bf16_f32 v156, v104, v105\n\t" \
  "v_mfma_f32_16x16x32_bf16 v[36:39], a[216:219], v[56:59], v[36:39]\n\t" \
  "ds_read_b64_tr_b16 a[152:153], v229 offset:256\n\t" \
  "v_cvt_pk_bf16_f32 v157, v106, v107\n\t" \
  "v_mfma_f32_16x16x32_bf16 v[36:39], a[212:215], v[52:55], v[36:39]\n\t" \
  "v_exp_f32 v5, v5\n\t" \
  "v_mfma_f32_16x16x32_bf16 v[36:39], a[208:211], v[48:51], v[36:39]\n\t" \
  "ds_read_b64_tr_b16 a[154:155], v229 offset:4352\n\t" \
  "v_cvt_pk_bf16_f32 v158, v108, v109\n\t" \
  "v_mfma_f32_16x16x32_bf16 v[40:43], a[224:227], v[48:51], v[180:183]\n\t" \
  "v_exp_f32 v6, v6\n\t" \
  "v_mfma_f32_16x16x32_bf16 v[40:43], a[228:231], v[52:55], v[40:43]\n\t" \
  "ds_read_b64_tr_b16 a[156:157], v229 offset:8448\n\t" \
  "v_cvt_pk_bf16_f32 v159, v110, v111\n\t" \
  "v_mfma_f32_16x16x32_bf16 v[40:43], a[232:235], v[56:59], v[40:43]\n\t" \
  "v_exp_f32 v7, v7\n\t" \
  "v_mfma_f32_16x16x32_bf16 v[40:43], a[236:239], v[60:63], v[40:43]\n\t" \
  "v_exp_f32 v8, v8\n\t" \
  "v_mfma_f32_16x16x32_bf16 v[44:47], a[252:255], v[60:63], v[180:183]\n\t" \
  "v_exp_f32 v9, v9\n\t" \
  "v_mfma_f32_16x16x32_bf16 v[44:47], a[248:251], v[56:59], v[44:47]\n\t" \
  "ds_read_b64_tr_b16 a[158:159], v229 offset:12544\n\t" \
  "ds_read_b64_tr_b16 a[160:161], v228 offset:512\n\t" \
  "v_mfma_f32_16x16x32_bf16 v[44:47], a[244:247], v[52:55], v[44:47]\n\t" \
  "v_exp_f32 v10, v10\n\t" \
  "v_mfma_f32_16x16x32_bf16 v[44:47], a[240:243], v[48:51], v[44:47]\n\t" \
  "v_exp_f32 v11, v11\n\t" \
  "v_mfma_f32_16x16x32_bf16 v[64:67], a[192:195], v[80:83], v[184:187]\n\t" \
  "buffer_load_dword %[pfd], v247, %[ws], 0 offen sc1\n\t" \
  "v_mfma_f32_16x16x32_bf16 v[64:67], a[196:199], v[84:87], v[64:67]\n\t" \
  "ds_read_b64_tr_b16 a[162:163], v228 offset:4608\n\t" \
  "ds_read_b64_tr_b16 a[164:165], v228 offset:8704\n\t" \
  "v_mfma_f32_16x16x32_bf16 v[64:67], a[200:203], v[88:91], v[64:67]\n\t" \
  "v_exp_f32 v12, v12\n\t" \
  "v_mfma_f32_16x16x32_bf16 v[64:67], a[204:207], v[92:95], v[64:67]\n\t" \
  "v_exp_f32 v13, v13\n\t" \
  "v_mfma_f32_16x16x32_bf16 v[68:71], a[220:223], v[92:95], v[184:187]\n\t" \
  "ds_read_b64_tr_b16 a[166:167], v228 offset:12800\n\t" \
  "ds_read_b64_tr_b16 a[168:169], v229 offset:512\n\t" \
  "v_mfma_f32_16x16x32_bf16 v[68:71], a[216:219], v[88:91], v[68:71]\n\t" \
  "v_exp_f32 v14, v14\n\t" \
  "v_mfma_f32_16x16x32_bf16 v[68:71], a[212:215], v[84:87], v[68:71]\n\t" \
  "v_exp_f32 v15, v15\n\t" \
  "v_mfma_f32_16x16x32_bf16 v[68:71], a[208:211], v[80:83], v[68:71]\n\t" \
  "ds_read_b64_tr_b16 a[170:171], v229 offset:4608\n\t" \
  "ds_read_b64_tr_b16 a[172:173], v229 offset:8704\n\t" \
  "v_mfma_f32_16x16x32_bf16 v[72:75], a[224:227], v[80:83], v[184:187]\n\t" \
  "v_exp_f32 v32, v32\n\t" \
  "v_mfma_f32_16x16x32_bf16 v[72:75], a[228:231], v[84:87], v[72:75]\n\t" \
  "v_exp_f32 v33, v33\n\t" \
  "v_mfma_f32_16x16x32_bf16 v[72:75], a[232:235], v[88:91], v[72:75]\n\t" \
  "v_exp_f32 v34, v34\n\t" \
  "v_mfma_f32_16x16x32_bf16 v[72:75], a[236:239], v[92:95], v[72:75]\n\t" \
  "ds_read_b64_tr_b16 a[174:175], v229 offset:12800\n\t" \
  "ds_read_b64_tr_b16 a[176:177], v228 offset:768\n\t" \
  "v_mfma_f32_16x16x32_bf16 v[76:79], a[252:255], v[92:95], v[184:187]\n\t" \
  "v_exp_f32 v35, v35\n\t" \
  "v_mfma_f32_16x16x32_bf16 v[76:79], a[248:251], v[88:91], v[76:79]\n\t" \
  "v_exp_f32 v36, v36\n\t" \
  "v_mfma_f32_16x16x32_bf16 v[76:79], a[244:247], v[84:87], v[76:79]\n\t" \
  "ds_read_b64_tr_b16 a[178:179], v228 offset:4864\n\t" \
  "ds_read_b64_tr_b16 a[180:181], v228 offset:8960\n\t" \
  "v_mfma_f32_16x16x32_bf16 v[76:79], a[240:243], v[80:83], v[76:79]\n\t" \
  "v_exp_f32 v37, v37\n\t" \
  "v_mfma_f32_16x16x32_bf16 v[96:99], a[192:195], v[112:115], v[188:191]\n\t" \
  "v_exp_f32 v38, v38\n\t" \
  "v_mfma_f32_16x16x32_bf16 v[96:99], a[196:199], v[116:119], v[96:99]\n\t" \
  "v_exp_f32 v39, v39\n\t" \
  "v_mfma_f32_16x16x32_bf16 v[96:99], a[200:203], v[120:123], v[96:99]\n\t" \
  "ds_read_b64_tr_b16 a[182:183], v228 offset:13056\n\t" \
  "ds_read_b64_tr_b16 a[184:185], v229 offset:768\n\t" \
  "v_mfma_f32_16x16x32_bf16 v[96:99], a[204:207], v[124:127], v[96:99]\n\t" \
  "v_exp_f32 v40, v40\n\t" \
  "v_mfma_f32_16x16x32_bf16 v[100:103], a[220:223], v[124:127], v[188:191]\n\t" \
  "v_exp_f32 v41, v41\n\t" \
  "v_mfma_f32_16x16x32_bf16 v[100:103], a[216:219], v[120:123], v[100:103]\n\t" \
  "ds_read_b64_tr_b16 a[186:187], v229 offset:4864\n\t" \
  "ds_read_b64_tr_b16 a[188:189], v229 offset:8960\n\t" \
  "v_mfma_f32_16x16x32_bf16 v[100:103], a[212:215], v[116:119], v[100:103]\n\t" \
  "v_exp_f32 v42, v42\n\t" \
  "v_mfma_f32_16x16x32_bf16 v[100:103], a[208:211], v[112:115], v[100:103]\n\t" \
  "v_exp_f32 v43, v43\n\t" \
  "v_mfma_f32_16x16x32_bf16 v[104:107], a[224:227], v[112:115], v[188:191]\n\t" \
  "v_exp_f32 v44, v44\n\t" \
  "v_mfma_f32_16x16x32_bf16 v[104:107], a[228:231], v[116:119], v[104:107]\n\t" \
  "ds_read_b64_tr_b16 a[190:191], v229 offset:13056\n\t" \
  "v_mfma_f32_16x16x32_bf16 v[104:107], a[232:235], v[120:123], v[104:107]\n\t" \
  "s_getreg_b32 s98, hwreg(HW_REG_IB_STS, 0, 4)\n\t" \
  "s_cmp_lg_u32 s98, 0\n\t" \
  "s_addc_u32 %[na], %[na], 0\n\t" \
  "v_mfma_f32_16x16x32_bf16 v[104:107], a[236:239], v[124:127], v[104:107]\n\t" \
  "v_exp_f32 v45, v45\n\t" \
  "v_mfma_f32_16x16x32_bf16 v[108:111], a[252:255], v[124:127], v[188:191]\n\t" \
  "v_exp_f32 v46, v46\n\t" \
  "v_mfma_f32_16x16x32_bf16 v[108:111], a[248:251], v[120:123], v[108:111]\n\t" \
  "s_getreg_b32 s98, hwreg(HW_REG_IB_STS, 0, 4)\n\t" \
  "s_cmp_lg_u32 s98, 0\n\t" \
  "s_addc_u32 %[na], %[na], 0\n\t" \
  "v_mfma_f32_16x16x32_bf16 v[108:111], a[244:247], v[116:119], v[108:111]\n\t" \
  "v_exp_f32 v47, v47\n\t" \
  "v_mfma_f32_16x16x32_bf16 v[108:111], a[240:243], v[112:115], v[108:111]\n\t" \
  "s_waitcnt vmcnt(4) lgkmcnt(0)\n\t" \
  "s_barrier\n\t" \
  STA("s_memrealtime s[94:95]\n\t") \
  STA("s_memtime s[92:93]\n\t") \
  "v_mfma_f32_16x16x32_bf16 a[0:3], a[128:131], v[128:131], a[0:3]\n\t" \
  "v_exp_f32 v64, v64\n\t" \
  "v_mfma_f32_16x16x32_bf16 a[0:3], a[132:135], v[132:135], a[0:3]\n\t" \
  "v_exp_f32 v65, v65\n\t" \
  "v_mfma_f32_16x16x32_bf16 a[16:19], a[136:139], v[128:131], a[16:19]\n\t" \
  "ds_read_b128 a[192:195], v220 offset:0\n\t" \
  "ds_read_b128 a[196:199], v221 offset:0\n\t" \
  "v_mfma_f32_16x16x32_bf16 a[16:19], a[140:143], v[132:135], a[16:19]\n\t" \
  "v_exp_f32 v66, v66\n\t" \
  "v_mfma_f32_16x16x32_bf16 a[32:35], a[144:147], v[128:131], a[32:35]\n\t" \
  "v_exp_f32 v67, v67\n\t" \
  "v_mfma_f32_16x16x32_bf16 a[32:35], a[148:151], v[132:135], a[32:35]\n\t" \
  "v_exp_f32 v68, v68\n\t" \
  "v_mfma_f32_16x16x32_bf16 a[48:51], a[152:155], v[128:131], a[48:51]\n\t" \
  STS("v_writelane_b32 %[stv], %[na], 58\n\t") \
  "s_cmp_ge_u32 %[na], 1\n\t" \
  "s_cselect_b32 s98, 0x4000, 0\n\t" \
  "s_or_b32 s98, s98, %[tag]\n\t" \
  "s_cmp_ge_u32 %[na], 2\n\t" \
  "s_cselect_b32 %[na], 2.0, 0\n\t" \
  "s_or_b32 s98, s98, %[na]\n\t" \
  "v_mov_b32 v209, s98\n\t" \
  "v_mfma_f32_16x16x32_bf16 a[48:51], a[156:159], v[132:135], a[48:51]\n\t" \
  "v_exp_f32 v69, v69\n\t" \
  "v_mfma_f32_16x16x32_bf16 a[64:67], a[160:163], v[128:131], a[64:67]\n\t" \
  "ds_read_b128 a[200:203], v222 offset:0\n\t" \
  "ds_read_b128 a[204:207], v223 offset:0\n\t" \
  "v_mfma_f32_16x16x32_bf16 a[64:67], a[164:167], v[132:135], a[64:67]\n\t" \
  "v_exp_f32 v70, v70\n\t" \
  "v_mfma_f32_16x16x32_bf16 a[80:83], a[168:171], v[128:131], a[80:83]\n\t" \
  "ds_read_b128 a[208:211], v220 offset:4096\n\t" \
  "ds_read_b128 a[212:215], v221 offset:4096\n\t" \
  "v_mfma_f32_16x16x32_bf16 a[80:83], a[172:175], v[132:135], a[80:83]\n\t" \
  "v_bfrev_b32 v205, 1\n\t" \
  "v_writelane_b32 v205, %[vo], 0\n\t" \
  "v_mfma_f32_16x16x32_bf16 a[96:99], a[176:179], v[128:131], a[96:99]\n\t" \
  STA("v_writelane_b32 %[stv], s94, 36\n\t") \
  STA("v_writelane_b32 %[stv], s92, 37\n\t") \
  "buffer_store_dword v209, v205, %[ws], 0 offen sc1\n\t" \
  "v_mfma_f32_16x16x32_bf16 a[96:99], a[180:183], v[132:135], a[96:99]\n\t" \
  "v_exp_f32 v71, v71\n\t" \
  "v_mfma_f32_16x16x32_bf16 a[112:115], a[184:187], v[128:131], a[112:115]\n\t" \
  "v_exp_f32 v72, v72\n\t" \
  "v_mfma_f32_16x16x32_bf16 a[112:115], a[188:191], v[132:135], a[112:115]\n\t" \
  "v_exp_f32 v73, v73\n\t" \
  "v_mfma_f32_16x16x32_bf16 v[160:163], v[216:219], v[128:131], v[160:163]\n\t" \
  "v_exp_f32 v74, v74\n\t" \
  "v_mfma_f32_16x16x32_bf16 v[160:163], v[216:219], v[132:135], v[160:163]\n\t" \
  "v_exp_f32 v75, v75\n\t" \
  "v_mfma_f32_16x16x32_bf16 a[4:7], a[128:131], v[136:139], a[4:7]\n\t" \
  "v_exp_f32 v76, v76\n\t" \
  "v_mfma_f32_16x16x32_bf16 a[4:7], a[132:135], v[140:143], a[4:7]\n\t" \
  "v_exp_f32 v77, v77\n\t" \
  "v_mfma_f32_16x16x32_bf16 a[20:23], a[136:139], v[136:139], a[20:23]\n\t" \
  "s_add_u32 m0, %[m], 65536\n\t" \
  "v_exp_f32 v78, v78\n\t" \
  "v_mfma_f32_16x16x32_bf16 a[20:23], a[140:143], v[140:143], a[20:23]\n\t" \
  "s_add_u32 s96, %[sofk], 81920\n\t" \
  "v_exp_f32 v79, v79\n\t" \
  "v_mfma_f32_16x16x32_bf16 a[36:39], a[144:147], v[136:139], a[36:39]\n\t" \
  "v_exp_f32 v96, v96\n\t" \
  "v_mfma_f32_16x16x32_bf16 a[36:39], a[148:151], v[140:143], a[36:39]\n\t" \
  "ds_read_b128 a[216:219], v222 offset:4096\n\t" \
  "v_cvt_pk_bf16_f32 v128, v0, v1\n\t" \
  "v_mfma_f32_16x16x32_bf16 a[52:55], a[152:155], v[136:139], a[52:55]\n\t" \
  "v_exp_f32 v97, v97\n\t" \
  "v_mfma_f32_16x16x32_bf16 a[52:55], a[156:159], v[140:143], a[52:55]\n\t" \
  "v_exp_f32 v98, v98\n\t" \
  "v_mfma_f32_16x16x32_bf16 a[68:71], a[160:163], v[136:139], a[68:71]\n\t" \
  "v_exp_f32 v99, v99\n\t" \
  "v_mfma_f32_16x16x32_bf16 a[68:71], a[164:167], v[140:143], a[68:71]\n\t" \
  "buffer_load_dwordx4 v224, %[sK], s96 offen offset:0 lds\n\t" \
  "v_mfma_f32_16x16x32_bf16 a[84:87], a[168:171], v[136:139], a[84:87]\n\t" \
  "ds_read_b128 a[220:223], v223 offset:4096\n\t" \
  "v_cvt_pk_bf16_f32 v129, v2, v3\n\t" \
  "v_mfma_f32_16x16x32_bf16 a[84:87], a[172:175], v[140:143], a[84:87]\n\t" \
  "v_exp_f32 v100, v100\n\t" \
  "v_mfma_f32_16x16x32_bf16 a[100:103], a[176:179], v[136:139], a[100:103]\n\t" \
  "ds_read_b128 a[224:227], v220 offset:8192\n\t" \
  "v_cvt_pk_bf16_f32 v130, v4, v5\n\t" \
  "v_mfma_f32_16x16x32_bf16 a[100:103], a[180:183], v[140:143], a[100:103]\n\t" \
  "buffer_load_dwordx4 v225, %[sK], s96 offen offset:1024 lds\n\t" \
  "v_mfma_f32_16x16x32_bf16 a[116:119], a[184:187], v[136:139], a[116:119]\n\t" \
  "v_exp_f32 v101, v101\n\t" \
  "v_mfma_f32_16x16x32_bf16 a[116:119], a[188:191], v[140:143], a[116:119]\n\t" \
  "v_exp_f32 v102, v102\n\t" \
  "v_mfma_f32_16x16x32_bf16 v[164:167], v[216:219], v[136:139], v[164:167]\n\t" \
  "buffer_load_dwordx4 v226, %[sK], s96 offen offset:2048 lds\n\t" \
  "v_mfma_f32_16x16x32_bf16 v[164:167], v[216:219], v[140:143], v[164:167]\n\t" \
  "ds_read_b128 a[228:231], v221 offset:8192\n\t" \
  "v_cvt_pk_bf16_f32 v131, v6, v7\n\t" \
  "v_mfma_f32_16x16x32_bf16 a[8:11], a[128:131], v[144:147], a[8:11]\n\t" \
  "v_exp_f32 v103, v103\n\t" \
  "v_mfma_f32_16x16x32_bf16 a[8:11], a[132:135], v[148:151], a[8:11]\n\t" \
  "v_exp_f32 v104, v104\n\t" \
  "v_mfma_f32_16x16x32_bf16 a[24:27], a[136:139], v[144:147], a[24:27]\n\t" \
  "buffer_load_dwordx4 v227, %[sK], s96 offen offset:3072 lds\n\t" \
  "v_mfma_f32_16x16x32_bf16 a[24:27], a[140:143], v[148:151], a[24:27]\n\t" \
  "ds_read_b128 a[232:235], v222 offset:8192\n\t" \
  "v_cvt_pk_bf16_f32 v132, v8, v9\n\t" \
  "v_mfma_f32_16x16x32_bf16 a[40:43], a[144:147], v[144:147], a[40:43]\n\t" \
  "v_exp_f32 v105, v105\n\t" \
  "v_mfma_f32_16x16x32_bf16 a[40:43], a[148:151], v[148:151], a[40:43]\n\t" \
  "s_add_u32 m0, %[m], 32768\n\t" \
  "s_mov_b32 s96, 0\n\t" \
  ".Lhs7:\n\ts_cbranch_execz .Lhs7n\n\t" \
  "ds_read_b128 a[236:239], v223 offset:8192\n\t" \
  "v_mfma_f32_16x16x32_bf16 a[56:59], a[152:155], v[144:147], a[56:59]\n\t" \
  "buffer_load_dwordx4 v232, %[sG], s96 offen offset:0 lds\n\t" \
  "v_mfma_f32_16x16x32_bf16 a[56:59], a[156:159], v[148:151], a[56:59]\n\t" \
  "s_add_u32 m0, %[m], 81920\n\t" \
  "v_exp_f32 v106, v106\n\t" \
  "v_mfma_f32_16x16x32_bf16 a[72:75], a[160:163], v[144:147], a[72:75]\n\t" \
  "s_add_u32 s96, %[sofk], 65536\n\t" \
  "v_exp_f32 v107, v107\n\t" \
  "v_mfma_f32_16x16x32_bf16 a[72:75], a[164:167], v[148:151], a[72:75]\n\t" \
  "ds_read_b128 a[240:243], v220 offset:12288\n\t" \
  "v_cvt_pk_bf16_f32 v133, v10, v11\n\t" \
  "v_mfma_f32_16x16x32_bf16 a[88:91], a[168:171], v[144:147], a[88:91]\n\t" \
  "v_exp_f32 v108, v108\n\t" \
  "v_mfma_f32_16x16x32_bf16 a[88:91], a[172:175], v[148:151], a[88:91]\n\t" \
  "v_exp_f32 v109, v109\n\t" \
  "v_mfma_f32_16x16x32_bf16 a[104:107], a[176:179], v[144:147], a[104:107]\n\t" \
  "v_exp_f32 v110, v110\n\t" \
  "v_mfma_f32_16x16x32_bf16 a[104:107], a[180:183], v[148:151], a[104:107]\n\t" \
  "ds_read_b128 a[244:247], v221 offset:12288\n\t" \
  "v_cvt_pk_bf16_f32 v134, v12, v13\n\t" \
  "v_mfma_f32_16x16x32_bf16 a[120:123], a[184:187], v[144:147], a[120:123]\n\t" \
  "v_exp_f32 v111, v111\n\t" \
  "v_mfma_f32_16x16x32_bf16 a[120:123], a[188:191], v[148:151], a[120:123]\n\t" \
  "buffer_load_dwordx4 v230, %[sV], s96 offen offset:0 lds\n\t" \
  "v_mfma_f32_16x16x32_bf16 v[168:171], v[216:219], v[144:147], v[168:171]\n\t" \
  "ds_read_b128 a[248:251], v222 offset:12288\n\t" \
  "v_cvt_pk_bf16_f32 v135, v14, v15\n\t" \
  "v_mfma_f32_16x16x32_bf16 v[168:171], v[216:219], v[148:151], v[168:171]\n\t" \
  "ds_read_b128 a[252:255], v223 offset:12288\n\t" \
  "v_cvt_pk_bf16_f32 v136, v32, v33\n\t" \
  "v_mfma_f32_16x16x32_bf16 a[12:15], a[128:131], v[152:155], a[12:15]\n\t" \
  "v_cvt_pk_bf16_f32 v137, v34, v35\n\t" \
  "v_cvt_pk_bf16_f32 v138, v36, v37\n\t" \
  "v_mfma_f32_16x16x32_bf16 a[12:15], a[132:135], v[156:159], a[12:15]\n\t" \
  "buffer_load_dwordx4 v231, %[sV], s96 offen offset:1024 lds\n\t" \
  "v_mfma_f32_16x16x32_bf16 a[28:31], a[136:139], v[152:155], a[28:31]\n\t" \
  "v_cvt_pk_bf16_f32 v139, v38, v39\n\t" \
  "v_cvt_pk_bf16_f32 v140, v40, v41\n\t" \
  "v_mfma_f32_16x16x32_bf16 a[28:31], a[140:143], v[156:159], a[28:31]\n\t" \
  "v_cvt_pk_bf16_f32 v141, v42, v43\n\t" \
  "v_cvt_pk_bf16_f32 v142, v44, v45\n\t" \
  "v_mfma_f32_16x16x32_bf16 a[44:47], a[144:147], v[152:155], a[44:47]\n\t" \
  "buffer_load_dwordx4 v230, %[sV], s96 offen offset:2048 lds\n\t" \
  "v_mfma_f32_16x16x32_bf16 a[44:47], a[148:151], v[156:159], a[44:47]\n\t" \
  "v_cvt_pk_bf16_f32 v143, v46, v47\n\t" \
  "v_cvt_pk_bf16_f32 v144, v64, v65\n\t" \
  "v_mfma_f32_16x16x32_bf16 a[60:63], a[152:155], v[152:155], a[60:63]\n\t" \
  "v_cvt_pk_bf16_f32 v145, v66, v67\n\t" \
  "v_cvt_pk_bf16_f32 v146, v68, v69\n\t" \
  "v_mfma_f32_16x16x32_bf16 a[60:63], a[156:159], v[156:159], a[60:63]\n\t" \
  "v_cvt_pk_bf16_f32 v147, v70, v71\n\t" \
  "v_cvt_pk_bf16_f32 v148, v72, v73\n\t" \
  "v_mfma_f32_16x16x32_bf16 a[76:79], a[160:163], v[152:155], a[76:79]\n\t" \
  "buffer_load_dwordx4 v231, %[sV], s96 offen offset:3072 lds\n\t" \
  "v_mfma_f32_16x16x32_bf16 a[76:79], a[164:167], v[156:159], a[76:79]\n\t" \
  "v_cvt_pk_bf16_f32 v149, v74, v75\n\t" \
  "v_cvt_pk_bf16_f32 v150, v76, v77\n\t" \
  "v_mfma_f32_16x16x32_bf16 a[92:95], a[168:171], v[152:155], a[92:95]\n\t" \
  "s_add_u32 m0, %[m], 32768\n\t" \
  "s_mov_b32 s96, 6144\n\t" \
  "v_cvt_pk_bf16_f32 v151, v78, v79\n\t" \
  "v_mfma_f32_16x16x32_bf16 a[92:95], a[172:175], v[156:159], a[92:95]\n\t" \
  "v_mfma_f32_16x16x32_bf16 a[108:111], a[176:179], v[152:155], a[108:111]\n\t" \
  "buffer_load_dwordx4 v232, %[sG], s96 offen offset:1024 lds\n\t" \
  "v_mfma_f32_16x16x32_bf16 a[108:111], a[180:183], v[156:159], a[108:111]\n\t" \
  "v_mfma_f32_16x16x32_bf16 a[124:127], a[184:187], v[152:155], a[124:127]\n\t" \
  "v_mfma_f32_16x16x32_bf16 a[124:127], a[188:191], v[156:159], a[124:127]\n\t" \
  "s_add_u32 s98, %[pf4], 28672\n\t" \
  "v_mfma_f32_16x16x32_bf16 v[172:175], v[216:219], v[152:155], v[172:175]\n\t" \
  "buffer_load_dword %[pfd], v245, %[sG], s98 offen\n\t" \
  "v_mfma_f32_16x16x32_bf16 v[172:175], v[216:219], v[156:159], v[172:175]\n\t" \
  "s_waitcnt lgkmcnt(0)\n\t" \
  "v_cvt_pk_bf16_f32 v152, v96, v97\n\t" \
  "v_cvt_pk_bf16_f32 v153, v98, v99\n\t" \
  STA("s_memrealtime s[94:95]\n\t") \
  STA("s_memtime s[92:93]\n\t") \
  "v_mfma_f32_16x16x32_bf16 v[0:3], a[192:195], v[16:19], v[176:179]\n\t" \
  "ds_read_b64_tr_b16 a[128:129], v237 offset:0\n\t" \
  "v_cvt_pk_bf16_f32 v154, v100, v101\n\t" \
  "v_mfma_f32_16x16x32_bf16 v[0:3], a[196:199], v[20:23], v[0:3]\n\t" \
  "ds_read_b64_tr_b16 a[130:131], v237 offset:4096\n\t" \
  "v_cvt_pk_bf16_f32 v155, v102, v103\n\t" \
  "v_mfma_f32_16x16x32_bf16 v[0:3], a[200:203], v[24:27], v[0:3]\n\t" \
  "ds_read_b64_tr_b16 a[132:133], v237 offset:8192\n\t" \
  "v_cvt_pk_bf16_f32 v156, v104, v105\n\t" \
  "v_mfma_f32_16x16x32_bf16 v[0:3], a[204:207], v[28:31], v[0:3]\n\t" \
  "ds_read_b64_tr_b16 a[134:135], v237 offset:12288\n\t" \
  "v_cvt_pk_bf16_f32 v157, v106, v107\n\t" \
  "v_mfma_f32_16x16x32_bf16 v[4:7], a[220:223], v[28:31], v[176:179]\n\t" \
  "ds_read_b64_tr_b16 a[136:137], v238 offset:0\n\t" \
  "v_cvt_pk_bf16_f32 v158, v108, v109\n\t" \
  "v_mfma_f32_16x16x32_bf16 v[4:7], a[216:219], v[24:27], v[4:7]\n\t" \
  "ds_read_b64_tr_b16 a[138:139], v238 offset:4096\n\t" \
  "v_cvt_pk_bf16_f32 v159, v110, v111\n\t" \
  "v_mfma_f32_16x16x32_bf16 v[4:7], a[212:215], v[20:23], v[4:7]\n\t" \
  "v_exp_f32 v0, v0\n\t" \
  "v_mfma_f32_16x16x32_bf16 v[4:7], a[208:211], v[16:19], v[4:7]\n\t" \
  "v_exp_f32 v1, v1\n\t" \
  "v_mfma_f32_16x16x32_bf16 v[8:11], a[224:227], v[16:19], v[176:179]\n\t" \
  "v_exp_f32 v2, v2\n\t" \
  "v_mfma_f32_16x16x32_bf16 v[8:11], a[228:231], v[20:23], v[8:11]\n\t" \
  "v_exp_f32 v3, v3\n\t" \
  "v_mfma_f32_16x16x32_bf16 v[8:11], a[232:235], v[24:27], v[8:11]\n\t" \
  "v_mfma_f32_16x16x32_bf16 v[8:11], a[236:239], v[28:31], v[8:11]\n\t" \
  "s_add_u32 s98, %[pf2], 0\n\t" \
  "v_exp_f32 v4, v4\n\t" \
  "v_mfma_f32_16x16x32_bf16 v[12:15], a[252:255], v[28:31], v[176:179]\n\t" \
  STA("v_writelane_b32 %[stv], s94, 38\n\t") \
  STA("v_writelane_b32 %[stv], s92, 39\n\t") \
  "buffer_load_dword %[pfd], v202, %[sM], s98 offen\n\t" \
  "v_mfma_f32_16x16x32_bf16 v[12:15], a[248:251], v[24:27], v[12:15]\n\t" \
  "ds_read_b64_tr_b16 a[140:141], v238 offset:8192\n\t" \
  "ds_read_b64_tr_b16 a[142:143], v238 offset:12288\n\t" \
  "v_mfma_f32_16x16x32_bf16 v[12:15], a[244:247], v[20:23], v[12:15]\n\t" \
  "v_exp_f32 v5, v5\n\t" \
  "v_mfma_f32_16x16x32_bf16 v[12:15], a[240:243], v[16:19], v[12:15]\n\t" \
  "v_exp_f32 v6, v6\n\t" \
  "v_mfma_f32_16x16x32_bf16 v[32:35], a[192:195], v[48:51], v[180:183]\n\t" \
  "ds_read_b64_tr_b16 a[144:145], v237 offset:256\n\t" \
  "ds_read_b64_tr_b16 a[146:147], v237 offset:4352\n\t" \
  "v_mfma_f32_16x16x32_bf16 v[32:35], a[196:199], v[52:55], v[32:35]\n\t" \
  "v_exp_f32 v7, v7\n\t" \
  "v_mfma_f32_16x16x32_bf16 v[32:35], a[200:203], v[56:59], v[32:35]\n\t" \
  "v_exp_f32 v8, v8\n\t" \
  "v_mfma_f32_16x16x32_bf16 v[32:35], a[204:207], v[60:63], v[32:35]\n\t" \
  "v_exp_f32 v9, v9\n\t" \
  "v_mfma_f32_16x16x32_bf16 v[36:39], a[220:223], v[60:63], v[180:183]\n\t" \
  "ds_read_b64_tr_b16 a[148:149], v237 offset:8448\n\t" \
  "ds_read_b64_tr_b16 a[150:151], v237 offset:12544\n\t" \
  "v_mfma_f32_16x16x32_bf16 v[36:39], a[216:219], v[56:59], v[36:39]\n\t" \
  "v_exp_f32 v10, v10\n\t" \
  "v_mfma_f32_16x16x32_bf16 v[36:39], a[212:215], v[52:55], v[36:39]\n\t" \
  "v_exp_f32 v11, v11\n\t" \
  "v_mfma_f32_16x16x32_bf16 v[36:39], a[208:211], v[48:51], v[36:39]\n\t" \
  "ds_read_b64_tr_b16 a[152:153], v238 offset:256\n\t" \
  "ds_read_b64_tr_b16 a[154:155], v238 offset:4352\n\t" \
  "v_mfma_f32_16x16x32_bf16 v[40:43], a[224:227], v[48:51], v[180:183]\n\t" \
  "v_exp_f32 v12, v12\n\t" \
  "v_mfma_f32_16x16x32_bf16 v[40:43], a[228:231], v[52:55], v[40:43]\n\t" \
  "v_exp_f32 v13, v13\n\t" \
  "v_mfma_f32_16x16x32_bf16 v[40:43], a[232:235], v[56:59], v[40:43]\n\t" \
  "v_exp_f32 v14, v14\n\t" \
  "v_mfma_f32_16x16x32_bf16 v[40:43], a[236:239], v[60:63], v[40:43]\n\t" \
  "ds_read_b64_tr_b16 a[156:157], v238 offset:8448\n\t" \
  "ds_read_b64_tr_b16 a[158:159], v238 offset:12544\n\t" \
  "v_mfma_f32_16x16x32_bf16 v[44:47], a[252:255], v[60:63], v[180:183]\n\t" \
  "v_exp_f32 v15, v15\n\t" \
  "v_mfma_f32_16x16x32_bf16 v[44:47], a[248:251], v[56:59], v[44:47]\n\t" \
  "v_exp_f32 v32, v32\n\t" \
  "v_mfma_f32_16x16x32_bf16 v[44:47], a[244:247], v[52:55], v[44:47]\n\t" \
  "ds_read_b64_tr_b16 a[160:161], v237 offset:512\n\t" \
  "ds_read_b64_tr_b16 a[162:163], v237 offset:4608\n\t" \
  "v_mfma_f32_16x16x32_bf16 v[44:47], a[240:243], v[48:51], v[44:47]\n\t" \
  "v_exp_f32 v33, v33\n\t" \
  "v_mfma_f32_16x16x32_bf16 v[64:67], a[192:195], v[80:83], v[184:187]\n\t" \
  "v_exp_f32 v34, v34\n\t" \
  "v_mfma_f32_16x16x32_bf16 v[64:67], a[196:199], v[84:87], v[64:67]\n\t" \
  "v_exp_f32 v35, v35\n\t" \
  "v_mfma_f32_16x16x32_bf16 v[64:67], a[200:203], v[88:91], v[64:67]\n\t" \
  "ds_read_b64_tr_b16 a[164:165], v237 offset:8704\n\t" \
  "ds_read_b64_tr_b16 a[166:167], v237 offset:12800\n\t" \
  "v_mfma_f32_16x16x32_bf16 v[64:67], a[204:207], v[92:95], v[64:67]\n\t" \
  "v_exp_f32 v36, v36\n\t" \
  "v_mfma_f32_16x16x32_bf16 v[68:71], a[220:223], v[92:95], v[184:187]\n\t" \
  "v_exp_f32 v37, v37\n\t" \
  "v_mfma_f32_16x16x32_bf16 v[68:71], a[216:219], v[88:91], v[68:71]\n\t" \
  "ds_read_b64_tr_b16 a[168:169], v238 offset:512\n\t" \
  "ds_read_b64_tr_b16 a[170:171], v238 offset:4608\n\t" \
  "v_mfma_f32_16x16x32_bf16 v[68:71], a[212:215], v[84:87], v[68:71]\n\t" \
  "v_exp_f32 v38, v38\n\t" \
  "v_mfma_f32_16x16x32_bf16 v[68:71], a[208:211], v[80:83], v[68:71]\n\t" \
  "v_exp_f32 v39, v39\n\t" \
  "v_mfma_f32_16x16x32_bf16 v[72:75], a[224:227], v[80:83], v[184:187]\n\t" \
  "v_exp_f32 v40, v40\n\t" \
  "v_mfma_f32_16x16x32_bf16 v[72:75], a[228:231], v[84:87], v[72:75]\n\t" \
  "ds_read_b64_tr_b16 a[172:173], v238 offset:8704\n\t" \
  "ds_read_b64_tr_b16 a[174:175], v238 offset:12800\n\t" \
  "v_mfma_f32_16x16x32_bf16 v[72:75], a[232:235], v[88:91], v[72:75]\n\t" \
  "v_exp_f32 v41, v41\n\t" \
  "v_mfma_f32_16x16x32_bf16 v[72:75], a[236:239], v[92:95], v[72:75]\n\t" \
  "v_exp_f32 v42, v42\n\t" \
  "v_mfma_f32_16x16x32_bf16 v[76:79], a[252:255], v[92:95], v[184:187]\n\t" \
  "ds_read_b64_tr_b16 a[176:177], v237 offset:768\n\t" \
  "ds_read_b64_tr_b16 a[178:179], v237 offset:4864\n\t" \
  "v_mfma_f32_16x16x32_bf16 v[76:79], a[248:251], v[88:91], v[76:79]\n\t" \
  "v_exp_f32 v43, v43\n\t" \
  "v_mfma_f32_16x16x32_bf16 v[76:79], a[244:247], v[84:87], v[76:79]\n\t" \
  "v_exp_f32 v44, v44\n\t" \
  "v_mfma_f32_16x16x32_bf16 v[76:79], a[240:243], v[80:83], v[76:79]\n\t" \
  "v_exp_f32 v45, v45\n\t" \
  "v_mfma_f32_16x16x32_bf16 v[96:99], a[192:195], v[112:115], v[188:191]\n\t" \
  "ds_read_b64_tr_b16 a[180:181], v237 offset:8960\n\t" \
  "ds_read_b64_tr_b16 a[182:183], v237 offset:13056\n\t" \
  "v_mfma_f32_16x16x32_bf16 v[96:99], a[196:199], v[116:119], v[96:99]\n\t" \
  "v_exp_f32 v46, v46\n\t" \
  "v_mfma_f32_16x16x32_bf16 v[96:99], a[200:203], v[120:123], v[96:99]\n\t" \
  "v_exp_f32 v47, v47\n\t" \
  "v_mfma_f32_16x16x32_bf16 v[96:99], a[204:207], v[124:127], v[96:99]\n\t" \
  "ds_read_b64_tr_b16 a[184:185], v238 offset:768\n\t" \
  "ds_read_b64_tr_b16 a[186:187], v238 offset:4864\n\t" \
  "v_mfma_f32_16x16x32_bf16 v[100:103], a[220:223], v[124:127], v[188:191]\n\t" \
  "v_exp_f32 v64, v64\n\t" \
  "v_mfma_f32_16x16x32_bf16 v[100:103], a[216:219], v[120:123], v[100:103]\n\t" \
  "v_exp_f32 v65, v65\n\t" \
  "v_mfma_f32_16x16x32_bf16 v[100:103], a[212:215], v[116:119], v[100:103]\n\t" \
  "v_exp_f32 v66, v66\n\t" \
  "v_mfma_f32_16x16x32_bf16 v[100:103], a[208:211], v[112:115], v[100:103]\n\t" \
  "ds_read_b64_tr_b16 a[188:189], v238 offset:8960\n\t" \
  "ds_read_b64_tr_b16 a[190:191], v238 offset:13056\n\t" \
  "v_mfma_f32_16x16x32_bf16 v[104:107], a[224:227], v[112:115], v[188:191]\n\t" \
  "v_exp_f32 v67, v67\n\t" \
  "v_mfma_f32_16x16x32_bf16 v[104:107], a[228:231], v[116:119], v[104:107]\n\t" \
  "v_exp_f32 v68, v68\n\t" \
  "v_mfma_f32_16x16x32_bf16 v[104:107], a[232:235], v[120:123], v[104:107]\n\t" \
  "v_exp_f32 v69, v69\n\t" \
  "v_mfma_f32_16x16x32_bf16 v[104:107], a[236:239], v[124:127], v[104:107]\n\t" \
  "v_exp_f32 v70, v70\n\t" \
  "v_mfma_f32_16x16x32_bf16 v[108:111], a[252:255], v[124:127], v[188:191]\n\t" \
  "v_exp_f32 v71, v71\n\t" \
  "v_mfma_f32_16x16x32_bf16 v[108:111], a[248:251], v[120:123], v[108:111]\n\t" \
  "v_exp_f32 v72, v72\n\t" \
  "v_mfma_f32_16x16x32_bf16 v[108:111], a[244:247], v[116:119], v[108:111]\n\t" \
  "v_exp_f32 v73, v73\n\t" \
  "v_mfma_f32_16x16x32_bf16 v[108:111], a[240:243], v[112:115], v[108:111]\n\t" \
  "s_waitcnt vmcnt(3) lgkmcnt(0)\n\t" \
  "s_barrier\n\t" \
  STA("s_memrealtime s[94:95]\n\t") \
  STA("s_memtime s[92:93]\n\t") \
  "v_mfma_f32_16x16x32_bf16 a[0:3], a[128:131], v[128:131], a[0:3]\n\t" \
  "v_exp_f32 v74, v74\n\t" \
  "v_mfma_f32_16x16x32_bf16 a[0:3], a[132:135], v[132:135], a[0:3]\n\t" \
  "v_exp_f32 v75, v75\n\t" \
  "v_mfma_f32_16x16x32_bf16 a[16:19], a[136:139], v[128:131], a[16:19]\n\t" \
  "ds_read_b128 a[192:195], v220 offset:16384\n\t" \
  "ds_read_b128 a[196:199], v221 offset:16384\n\t" \
  "v_mfma_f32_16x16x32_bf16 a[16:19], a[140:143], v[132:135], a[16:19]\n\t" \
  "v_exp_f32 v76, v76\n\t" \
  "v_mfma_f32_16x16x32_bf16 a[32:35], a[144:147], v[128:131], a[32:35]\n\t" \
  "s_lshr_b32 s98, %[sofk], 6\n\t" \
  "v_xor_b32 v234, s98, v244\n\t" \
  "v_add_u32 v213, %[m], v242\n\t" \
  "v_mfma_f32_16x16x32_bf16 a[32:35], a[148:151], v[132:135], a[32:35]\n\t" \
  "v_exp_f32 v77, v77\n\t" \
  "v_mfma_f32_16x16x32_bf16 a[48:51], a[152:155], v[128:131], a[48:51]\n\t" \
  "v_exp_f32 v78, v78\n\t" \
  "v_mfma_f32_16x16x32_bf16 a[48:51], a[156:159], v[132:135], a[48:51]\n\t" \
  "v_exp_f32 v79, v79\n\t" \
  "v_mfma_f32_16x16x32_bf16 a[64:67], a[160:163], v[128:131], a[64:67]\n\t" \
  "ds_read_b128 a[200:203], v222 offset:16384\n\t" \
  "v_add_u32 v215, 98304, v213\n\t" \
  "v_mfma_f32_16x16x32_bf16 a[64:67], a[164:167], v[132:135], a[64:67]\n\t" \
  "v_exp_f32 v96, v96\n\t" \
  "v_mfma_f32_16x16x32_bf16 a[80:83], a[168:171], v[128:131], a[80:83]\n\t" \
  "v_exp_f32 v97, v97\n\t" \
  "v_mfma_f32_16x16x32_bf16 a[80:83], a[172:175], v[132:135], a[80:83]\n\t" \
  "v_exp_f32 v98, v98\n\t" \
  "v_mfma_f32_16x16x32_bf16 a[96:99], a[176:179], v[128:131], a[96:99]\n\t" \
  STA("v_writelane_b32 %[stv], s94, 40\n\t") \
  STA("v_writelane_b32 %[stv], s92, 41\n\t") \
  "ds_read_b128 a[204:207], v223 offset:16384\n\t" \
  "v_mov_b32 v243, %[tag]\n\t" \
  "v_mfma_f32_16x16x32_bf16 a[96:99], a[180:183], v[132:135], a[96:99]\n\t" \
  "v_exp_f32 v99, v99\n\t" \
  "v_mfma_f32_16x16x32_bf16 a[112:115], a[184:187], v[128:131], a[112:115]\n\t" \
  "v_exp_f32 v100, v100\n\t" \
  "v_mfma_f32_16x16x32_bf16 a[112:115], a[188:191], v[132:135], a[112:115]\n\t" \
  "ds_read_b128 a[208:211], v220 offset:20480\n\t" \
  "v_lshrrev_b32 v254, 3, v233\n\t" \
  "v_mfma_f32_16x16x32_bf16 v[160:163], v[216:219], v[128:131], v[160:163]\n\t" \
  "ds_read_b128 a[212:215], v221 offset:20480\n\t" \
  "v_min_u32 v254, 1, v254\n\t" \
  "v_mfma_f32_16x16x32_bf16 v[160:163], v[216:219], v[132:135], v[160:163]\n\t" \
  "ds_read_b128 a[216:219], v222 offset:20480\n\t" \
  "v_lshlrev_b32 v254, 31, v254\n\t" \
  "v_mfma_f32_16x16x32_bf16 a[4:7], a[128:131], v[136:139], a[4:7]\n\t" \
  "ds_read_b128 a[220:223], v223 offset:20480\n\t" \
  "v_lshl_or_b32 v254, v233, 9, v254\n\t" \
  "v_mfma_f32_16x16x32_bf16 a[4:7], a[132:135], v[140:143], a[4:7]\n\t" \
  "s_lshr_b32 m0, %[hl18], 18\n\t" \
  "s_brev_b32 s98, 1\n\t" \
  "v_writelane_b32 v254, s98, m0\n\t" \
  "v_mfma_f32_16x16x32_bf16 a[20:23], a[136:139], v[136:139], a[20:23]\n\t" \
  "s_add_u32 m0, %[m], 49152\n\t" \
  "v_exp_f32 v101, v101\n\t" \
  "v_mfma_f32_16x16x32_bf16 a[20:23], a[140:143], v[140:143], a[20:23]\n\t" \
  "s_add_u32 s96, %[sofk], 98304\n\t" \
  "v_exp_f32 v102, v102\n\t" \
  "v_mfma_f32_16x16x32_bf16 a[36:39], a[144:147], v[136:139], a[36:39]\n\t" \
  "v_exp_f32 v103, v103\n\t" \
  "v_mfma_f32_16x16x32_bf16 a[36:39], a[148:151], v[140:143], a[36:39]\n\t" \
  "v_exp_f32 v104, v104\n\t" \
  "v_mfma_f32_16x16x32_bf16 a[52:55], a[152:155], v[136:139], a[52:55]\n\t" \
  "v_exp_f32 v105, v105\n\t" \
  "v_mfma_f32_16x16x32_bf16 a[52:55], a[156:159], v[140:143], a[52:55]\n\t" \
  "v_exp_f32 v106, v106\n\t" \
  "v_mfma_f32_16x16x32_bf16 a[68:71], a[160:163], v[136:139], a[68:71]\n\t" \
  "v_exp_f32 v107, v107\n\t" \
  "v_mfma_f32_16x16x32_bf16 a[68:71], a[164:167], v[140:143], a[68:71]\n\t" \
  "buffer_load_dwordx4 v224, %[sK], s96 offen offset:0 lds\n\t" \
  "v_mfma_f32_16x16x32_bf16 a[84:87], a[168:171], v[136:139], a[84:87]\n\t" \
  "ds_read_b128 a[224:227], v220 offset:24576\n\t" \
  "v_cvt_pk_bf16_f32 v128, v0, v1\n\t" \
  "v_mfma_f32_16x16x32_bf16 a[84:87], a[172:175], v[140:143], a[84:87]\n\t" \
  "v_exp_f32 v108, v108\n\t" \
  "v_mfma_f32_16x16x32_bf16 a[100:103], a[176:179], v[136:139], a[100:103]\n\t" \
  "v_exp_f32 v109, v109\n\t" \
  "v_mfma_f32_16x16x32_bf16 a[100:103], a[180:183], v[140:143], a[100:103]\n\t" \
  "buffer_load_dwordx4 v225, %[sK], s96 offen offset:1024 lds\n\t" \
  "v_mfma_f32_16x16x32_bf16 a[116:119], a[184:187], v[136:139], a[116:119]\n\t" \
  "v_exp_f32 v110, v110\n\t" \
  "v_mfma_f32_16x16x32_bf16 a[116:119], a[188:191], v[140:143], a[116:119]\n\t" \
  "v_exp_f32 v111, v111\n\t" \
  "v_mfma_f32_16x16x32_bf16 v[164:167], v[216:219], v[136:139], v[164:167]\n\t" \
  "buffer_load_dwordx4 v226, %[sK], s96 offen offset:2048 lds\n\t" \
  "v_mfma_f32_16x16x32_bf16 v[164:167], v[216:219], v[140:143], v[164:167]\n\t" \
  "ds_read_b128 a[228:231], v221 offset:24576\n\t" \
  "v_cvt_pk_bf16_f32 v129, v2, v3\n\t" \
  "v_mfma_f32_16x16x32_bf16 a[8:11], a[128:131], v[144:147], a[8:11]\n\t" \
  "ds_read_b128 a[232:235], v222 offset:24576\n\t" \
  "v_cvt_pk_bf16_f32 v130, v4, v5\n\t" \
  "v_mfma_f32_16x16x32_bf16 a[8:11], a[132:135], v[148:151], a[8:11]\n\t" \
  "ds_read_b128 a[236:239], v223 offset:24576\n\t" \
  "v_cvt_pk_bf16_f32 v131, v6, v7\n\t" \
  "v_mfma_f32_16x16x32_bf16 a[24:27], a[136:139], v[144:147], a[24:27]\n\t" \
  "buffer_load_dwordx4 v227, %[sK], s96 offen offset:3072 lds\n\t" \
  "v_mfma_f32_16x16x32_bf16 a[24:27], a[140:143], v[148:151], a[24:27]\n\t" \
  "ds_read_b128 a[240:243], v220 offset:28672\n\t" \
  "v_cvt_pk_bf16_f32 v132, v8, v9\n\t" \
  "v_mfma_f32_16x16x32_bf16 a[40:43], a[144:147], v[144:147], a[40:43]\n\t" \
  "ds_read_b128 a[244:247], v221 offset:28672\n\t" \
  "v_cvt_pk_bf16_f32 v133, v10, v11\n\t" \
  "v_mfma_f32_16x16x32_bf16 a[40:43], a[148:151], v[148:151], a[40:43]\n\t" \
  "s_add_u32 m0, %[m], 32768\n\t" \
  "s_mov_b32 s96, 12288\n\t" \
  "v_cvt_pk_bf16_f32 v134, v12, v13\n\t" \
  "v_mfma_f32_16x16x32_bf16 a[56:59], a[152:155], v[144:147], a[56:59]\n\t" \
  "buffer_load_dwordx4 v232, %[sG], s96 offen offset:2048 lds\n\t" \
  "v_mfma_f32_16x16x32_bf16 a[56:59], a[156:159], v[148:151], a[56:59]\n\t" \
  "s_add_u32 m0, %[m], 147456\n\t" \
  "ds_read_b128 a[248:251], v222 offset:28672\n\t" \
  "v_cvt_pk_bf16_f32 v135, v14, v15\n\t" \
  "v_mfma_f32_16x16x32_bf16 a[72:75], a[160:163], v[144:147], a[72:75]\n\t" \
  "s_add_u32 s96, %[sofk], 81920\n\t" \
  "v_cvt_pk_bf16_f32 v136, v32, v33\n\t" \
  "v_cvt_pk_bf16_f32 v137, v34, v35\n\t" \
  "v_mfma_f32_16x16x32_bf16 a[72:75], a[164:167], v[148:151], a[72:75]\n\t" \
  "v_cvt_pk_bf16_f32 v138, v36, v37\n\t" \
  "v_cvt_pk_bf16_f32 v139, v38, v39\n\t" \
  "v_mfma_f32_16x16x32_bf16 a[88:91], a[168:171], v[144:147], a[88:91]\n\t" \
  "v_cvt_pk_bf16_f32 v140, v40, v41\n\t" \
  "v_cvt_pk_bf16_f32 v141, v42, v43\n\t" \
  "v_mfma_f32_16x16x32_bf16 a[88:91], a[172:175], v[148:151], a[88:91]\n\t" \
  "ds_read_b128 a[252:255], v223 offset:28672\n\t" \
  "v_cvt_pk_bf16_f32 v142, v44, v45\n\t" \
  "v_mfma_f32_16x16x32_bf16 a[104:107], a[176:179], v[144:147], a[104:107]\n\t" \
  "v_cvt_pk_bf16_f32 v143, v46, v47\n\t" \
  "v_mfma_f32_16x16x32_bf16 a[104:107], a[180:183], v[148:151], a[104:107]\n\t" \
  "v_mfma_f32_16x16x32_bf16 a[120:123], a[184:187], v[144:147], a[120:123]\n\t" \
  "v_mfma_f32_16x16x32_bf16 a[120:123], a[188:191], v[148:151], a[120:123]\n\t" \
  "buffer_load_dwordx4 v230, %[sV], s96 offen offset:0 lds\n\t" \
  "v_mfma_f32_16x16x32_bf16 v[168:171], v[216:219], v[144:147], v[168:171]\n\t" \
  "v_mfma_f32_16x16x32_bf16 v[168:171], v[216:219], v[148:151], v[168:171]\n\t" \
  "v_cvt_pk_bf16_f32 v144, v64, v65\n\t" \
  "v_cvt_pk_bf16_f32 v145, v66, v67\n\t" \
  "v_mfma_f32_16x16x32_bf16 a[12:15], a[128:131], v[152:155], a[12:15]\n\t" \
  "v_cvt_pk_bf16_f32 v146, v68, v69\n\t" \
  "v_cvt_pk_bf16_f32 v147, v70, v71\n\t" \
  "v_mfma_f32_16x16x32_bf16 a[12:15], a[132:135], v[156:159], a[12:15]\n\t" \
  "buffer_load_dwordx4 v231, %[sV], s96 offen offset:1024 lds\n\t" \
  "v_mfma_f32_16x16x32_bf16 a[28:31], a[136:139], v[152:155], a[28:31]\n\t" \
  "v_cvt_pk_bf16_f32 v148, v72, v73\n\t" \
  "v_cvt_pk_bf16_f32 v149, v74, v75\n\t" \
  "v_mfma_f32_16x16x32_bf16 a[28:31], a[140:143], v[156:159], a[28:31]\n\t" \
  "v_cvt_pk_bf16_f32 v150, v76, v77\n\t" \
  "v_cvt_pk_bf16_f32 v151, v78, v79\n\t" \
  "v_mfma_f32_16x16x32_bf16 a[44:47], a[144:147], v[152:155], a[44:47]\n\t" \
  "buffer_load_dwordx4 v230, %[sV], s96 offen offset:2048 lds\n\t" \
  "v_mfma_f32_16x16x32_bf16 a[44:47], a[148:151], v[156:159], a[44:47]\n\t" \
  "v_mfma_f32_16x16x32_bf16 a[60:63], a[152:155], v[152:155], a[60:63]\n\t" \
  "v_mfma_f32_16x16x32_bf16 a[60:63], a[156:159], v[156:159], a[60:63]\n\t" \
  "v_mfma_f32_16x16x32_bf16 a[76:79], a[160:163], v[152:155], a[76:79]\n\t" \
  "buffer_load_dwordx4 v231, %[sV], s96 offen offset:3072 lds\n\t" \
  "v_mfma_f32_16x16x32_bf16 a[76:79], a[164:167], v[156:159], a[76:79]\n\t" \
  "s_add_u32 m0, %[m], 32768\n\t" \
  "s_mov_b32 s96, 18432\n\t" \
  "v_mfma_f32_16x16x32_bf16 a[92:95], a[168:171], v[152:155], a[92:95]\n\t" \
  "v_mfma_f32_16x16x32_bf16 a[92:95], a[172:175], v[156:159], a[92:95]\n\t" \
  "v_mfma_f32_16x16x32_bf16 a[108:111], a[176:179], v[152:155], a[108:111]\n\t" \
  "buffer_load_dwordx4 v232, %[sG], s96 offen offset:3072 lds\n\t" \
  "v_mfma_f32_16x16x32_bf16 a[108:111], a[180:183], v[156:159], a[108:111]\n\t" \
  "v_mfma_f32_16x16x32_bf16 a[124:127], a[184:187], v[152:155], a[124:127]\n\t" \
  "v_mfma_f32_16x16x32_bf16 a[124:127], a[188:191], v[156:159], a[124:127]\n\t" \
  "s_add_u32 s98, %[pf4], 43008\n\t" \
  "v_mfma_f32_16x16x32_bf16 v[172:175], v[216:219], v[152:155], v[172:175]\n\t" \
  "buffer_load_dword %[pfd], v245, %[sG], s98 offen\n\t" \
  "v_mfma_f32_16x16x32_bf16 v[172:175], v[216:219], v[156:159], v[172:175]\n\t" \
  "s_waitcnt lgkmcnt(0)\n\t" \
  "v_cvt_pk_bf16_f32 v152, v96, v97\n\t" \
  "v_cvt_pk_bf16_f32 v153, v98, v99\n\t" \
  STA("s_memrealtime s[94:95]\n\t") \
  STA("s_memtime s[92:93]\n\t") \
  "v_mfma_f32_16x16x32_bf16 v[0:3], a[192:195], v[16:19], v[176:179]\n\t" \
  "ds_read_b64_tr_b16 a[128:129], v228 offset:0\n\t" \
  "v_cvt_pk_bf16_f32 v154, v100, v101\n\t" \
  "v_mfma_f32_16x16x32_bf16 v[0:3], a[196:199], v[20:23], v[0:3]\n\t" \
  "ds_read_b64_tr_b16 a[130:131], v228 offset:4096\n\t" \
  "v_cvt_pk_bf16_f32 v155, v102, v103\n\t" \
  "v_mfma_f32_16x16x32_bf16 v[0:3], a[200:203], v[24:27], v[0:3]\n\t" \
  "ds_read_b64_tr_b16 a[132:133], v228 offset:8192\n\t" \
  "v_lshrrev_b32 v252, 3, v233\n\t" \
  "v_mfma_f32_16x16x32_bf16 v[0:3], a[204:207], v[28:31], v[0:3]\n\t" \
  "ds_read_b64_tr_b16 a[134:135], v228 offset:12288\n\t" \
  "v_cvt_pk_bf16_f32 v156, v104, v105\n\t" \
  "v_mfma_f32_16x16x32_bf16 v[4:7], a[220:223], v[28:31], v[176:179]\n\t" \
  "ds_read_b64_tr_b16 a[136:137], v229 offset:0\n\t" \
  "v_cvt_pk_bf16_f32 v157, v106, v107\n\t" \
  "v_mfma_f32_16x16x32_bf16 v[4:7], a[216:219], v[24:27], v[4:7]\n\t" \
  "ds_read_b64_tr_b16 a[138:139], v229 offset:4096\n\t" \
  "v_cvt_pk_bf16_f32 v158, v108, v109\n\t" \
  "v_mfma_f32_16x16x32_bf16 v[4:7], a[212:215], v[20:23], v[4:7]\n\t" \
  "v_exp_f32 v0, v0\n\t" \
  "v_mfma_f32_16x16x32_bf16 v[4:7], a[208:211], v[16:19], v[4:7]\n\t" \
  "v_exp_f32 v1, v1\n\t" \
  "v_mfma_f32_16x16x32_bf16 v[8:11], a[224:227], v[16:19], v[176:179]\n\t" \
  "v_exp_f32 v2, v2\n\t" \
  "v_mfma_f32_16x16x32_bf16 v[8:11], a[228:231], v[20:23], v[8:11]\n\t" \
  "v_exp_f32 v3, v3\n\t" \
  "v_mfma_f32_16x16x32_bf16 v[8:11], a[232:235], v[24:27], v[8:11]\n\t" \
  "ds_read_b64_tr_b16 a[140:141], v229 offset:8192\n\t" \
  "v_mul_u32_u24 v247, 98176, v252\n\t" \
  "v_mfma_f32_16x16x32_bf16 v[8:11], a[236:239], v[28:31], v[8:11]\n\t" \
  "s_add_u32 s98, %[pf2], 24576\n\t" \
  "v_exp_f32 v4, v4\n\t" \
  "v_mfma_f32_16x16x32_bf16 v[12:15], a[252:255], v[28:31], v[176:179]\n\t" \
  STA("v_writelane_b32 %[stv], s94, 42\n\t") \
  STA("v_writelane_b32 %[stv], s92, 43\n\t") \
  "buffer_load_dword %[pfd], v202, %[sM], s98 offen\n\t" \
  "v_mfma_f32_16x16x32_bf16 v[12:15], a[248:251], v[24:27], v[12:15]\n\t" \
  "ds_read_b64_tr_b16 a[142:143], v229 offset:12288\n\t" \
  "v_cvt_pk_bf16_f32 v159, v110, v111\n\t" \
  "v_mfma_f32_16x16x32_bf16 v[12:15], a[244:247], v[20:23], v[12:15]\n\t" \
  "ds_read_b64_tr_b16 a[144:145], v228 offset:256\n\t" \
  "v_lshl_add_u32 v247, v233, 4, v247\n\t" \
  "v_mfma_f32_16x16x32_bf16 v[12:15], a[240:243], v[16:19], v[12:15]\n\t" \
  "v_exp_f32 v5, v5\n\t" \
  "v_mfma_f32_16x16x32_bf16 v[32:35], a[192:195], v[48:51], v[180:183]\n\t" \
  "ds_read_b64_tr_b16 a[146:147], v228 offset:4352\n\t" \
  "v_lshlrev_b32 v252, 4, v252\n\t" \
  "v_mfma_f32_16x16x32_bf16 v[32:35], a[196:199], v[52:55], v[32:35]\n\t" \
  "v_exp_f32 v6, v6\n\t" \
  "v_mfma_f32_16x16x32_bf16 v[32:35], a[200:203], v[56:59], v[32:35]\n\t" \
  "v_exp_f32 v7, v7\n\t" \
  "v_mfma_f32_16x16x32_bf16 v[32:35], a[204:207], v[60:63], v[32:35]\n\t" \
  "v_exp_f32 v8, v8\n\t" \
  "v_mfma_f32_16x16x32_bf16 v[36:39], a[220:223], v[60:63], v[180:183]\n\t" \
  "ds_read_b64_tr_b16 a[148:149], v228 offset:8448\n\t" \
  "ds_read_b64_tr_b16 a[150:151], v228 offset:12544\n\t" \
  "v_mfma_f32_16x16x32_bf16 v[36:39], a[216:219], v[56:59], v[36:39]\n\t" \
  "v_exp_f32 v9, v9\n\t" \
  "v_mfma_f32_16x16x32_bf16 v[36:39], a[212:215], v[52:55], v[36:39]\n\t" \
  "v_exp_f32 v10, v10\n\t" \
  "v_mfma_f32_16x16x32_bf16 v[36:39], a[208:211], v[48:51], v[36:39]\n\t" \
  "ds_read_b64_tr_b16 a[152:153], v229 offset:256\n\t" \
  "ds_read_b64_tr_b16 a[154:155], v229 offset:4352\n\t" \
  "v_mfma_f32_16x16x32_bf16 v[40:43], a[224:227], v[48:51], v[180:183]\n\t" \
  "v_exp_f32 v11, v11\n\t" \
  "v_mfma_f32_16x16x32_bf16 v[40:43], a[228:231], v[52:55], v[40:43]\n\t" \
  "v_exp_f32 v12, v12\n\t" \
  "v_mfma_f32_16x16x32_bf16 v[40:43], a[232:235], v[56:59], v[40:43]\n\t" \
  "v_exp_f32 v13, v13\n\t" \
  "v_mfma_f32_16x16x32_bf16 v[40:43], a[236:239], v[60:63], v[40:43]\n\t" \
  "ds_read_b64_tr_b16 a[156:157], v229 offset:8448\n\t" \
  "ds_read_b64_tr_b16 a[158:159], v229 offset:12544\n\t" \
  "v_mfma_f32_16x16x32_bf16 v[44:47], a[252:255], v[60:63], v[180:183]\n\t" \
  "v_exp_f32 v14, v14\n\t" \
  "v_mfma_f32_16x16x32_bf16 v[44:47], a[248:251], v[56:59], v[44:47]\n\t" \
  "v_exp_f32 v15, v15\n\t" \
  "v_mfma_f32_16x16x32_bf16 v[44:47], a[244:247], v[52:55], v[44:47]\n\t" \
  "ds_read_b64_tr_b16 a[160:161], v228 offset:512\n\t" \
  "ds_read_b64_tr_b16 a[162:163], v228 offset:4608\n\t" \
  "v_mfma_f32_16x16x32_bf16 v[44:47], a[240:243], v[48:51], v[44:47]\n\t" \
  "v_exp_f32 v32, v32\n\t" \
  "v_mfma_f32_16x16x32_bf16 v[64:67], a[192:195], v[80:83], v[184:187]\n\t" \
  "v_exp_f32 v33, v33\n\t" \
  "v_mfma_f32_16x16x32_bf16 v[64:67], a[196:199], v[84:87], v[64:67]\n\t" \
  "v_exp_f32 v34, v34\n\t" \
  "v_mfma_f32_16x16x32_bf16 v[64:67], a[200:203], v[88:91], v[64:67]\n\t" \
  "ds_read_b64_tr_b16 a[164:165], v228 offset:8704\n\t" \
  "ds_read_b64_tr_b16 a[166:167], v228 offset:12800\n\t" \
  "v_mfma_f32_16x16x32_bf16 v[64:67], a[204:207], v[92:95], v[64:67]\n\t" \
  "v_exp_f32 v35, v35\n\t" \
  "v_mfma_f32_16x16x32_bf16 v[68:71], a[220:223], v[92:95], v[184:187]\n\t" \
  "v_exp_f32 v36, v36\n\t" \
  "v_mfma_f32_16x16x32_bf16 v[68:71], a[216:219], v[88:91], v[68:71]\n\t" \
  "ds_read_b64_tr_b16 a[168:169], v229 offset:512\n\t" \
  "ds_read_b64_tr_b16 a[170:171], v229 offset:4608\n\t" \
  "v_mfma_f32_16x16x32_bf16 v[68:71], a[212:215], v[84:87], v[68:71]\n\t" \
  "v_exp_f32 v37, v37\n\t" \
  "v_mfma_f32_16x16x32_bf16 v[68:71], a[208:211], v[80:83], v[68:71]\n\t" \
  "v_exp_f32 v38, v38\n\t" \
  "v_mfma_f32_16x16x32_bf16 v[72:75], a[224:227], v[80:83], v[184:187]\n\t" \
  "v_exp_f32 v39, v39\n\t" \
  "v_mfma_f32_16x16x32_bf16 v[72:75], a[228:231], v[84:87], v[72:75]\n\t" \
  "ds_read_b64_tr_b16 a[172:173], v229 offset:8704\n\t" \
  "ds_read_b64_tr_b16 a[174:175], v229 offset:12800\n\t" \
  "v_mfma_f32_16x16x32_bf16 v[72:75], a[232:235], v[88:91], v[72:75]\n\t" \
  "v_exp_f32 v40, v40\n\t" \
  "v_mfma_f32_16x16x32_bf16 v[72:75], a[236:239], v[92:95], v[72:75]\n\t" \
  "v_exp_f32 v41, v41\n\t" \
  "v_mfma_f32_16x16x32_bf16 v[76:79], a[252:255], v[92:95], v[184:187]\n\t" \
  "ds_read_b64_tr_b16 a[176:177], v228 offset:768\n\t" \
  "ds_read_b64_tr_b16 a[178:179], v228 offset:4864\n\t" \
  "v_mfma_f32_16x16x32_bf16 v[76:79], a[248:251], v[88:91], v[76:79]\n\t" \
  "v_exp_f32 v42, v42\n\t" \
  "v_mfma_f32_16x16x32_bf16 v[76:79], a[244:247], v[84:87], v[76:79]\n\t" \
  "v_exp_f32 v43, v43\n\t" \
  "v_mfma_f32_16x16x32_bf16 v[76:79], a[240:243], v[80:83], v[76:79]\n\t" \
  "v_exp_f32 v44, v44\n\t" \
  "v_mfma_f32_16x16x32_bf16 v[96:99], a[192:195], v[112:115], v[188:191]\n\t" \
  "ds_read_b64_tr_b16 a[180:181], v228 offset:8960\n\t" \
  "ds_read_b64_tr_b16 a[182:183], v228 offset:13056\n\t" \
  "v_mfma_f32_16x16x32_bf16 v[96:99], a[196:199], v[116:119], v[96:99]\n\t" \
  "v_exp_f32 v45, v45\n\t" \
  "v_mfma_f32_16x16x32_bf16 v[96:99], a[200:203], v[120:123], v[96:99]\n\t" \
  "v_exp_f32 v46, v46\n\t" \
  "v_mfma_f32_16x16x32_bf16 v[96:99], a[204:207], v[124:127], v[96:99]\n\t" \
  "ds_read_b64_tr_b16 a[184:185], v229 offset:768\n\t" \
  "ds_read_b64_tr_b16 a[186:187], v229 offset:4864\n\t" \
  "v_mfma_f32_16x16x32_bf16 v[100:103], a[220:223], v[124:127], v[188:191]\n\t" \
  "v_exp_f32 v47, v47\n\t" \
  "v_mfma_f32_16x16x32_bf16 v[100:103], a[216:219], v[120:123], v[100:103]\n\t" \
  "v_exp_f32 v64, v64\n\t" \
  "v_mfma_f32_16x16x32_bf16 v[100:103], a[212:215], v[116:119], v[100:103]\n\t" \
  "v_exp_f32 v65, v65\n\t" \
  "v_mfma_f32_16x16x32_bf16 v[100:103], a[208:211], v[112:115], v[100:103]\n\t" \
  "ds_read_b64_tr_b16 a[188:189], v229 offset:8960\n\t" \
  "ds_read_b64_tr_b16 a[190:191], v229 offset:13056\n\t" \
  "v_mfma_f32_16x16x32_bf16 v[104:107], a[224:227], v[112:115], v[188:191]\n\t" \
  "v_exp_f32 v66, v66\n\t" \
  "v_mfma_f32_16x16x32_bf16 v[104:107], a[228:231], v[116:119], v[104:107]\n\t" \
  "v_exp_f32 v67, v67\n\t" \
  "v_mfma_f32_16x16x32_bf16 v[104:107], a[232:235], v[120:123], v[104:107]\n\t" \
  "v_exp_f32 v68, v68\n\t" \
  "v_mfma_f32_16x16x32_bf16 v[104:107], a[236:239], v[124:127], v[104:107]\n\t" \
  "v_exp_f32 v69, v69\n\t" \
  "v_mfma_f32_16x16x32_bf16 v[108:111], a[252:255], v[124:127], v[188:191]\n\t" \
  "v_exp_f32 v70, v70\n\t" \
  "v_mfma_f32_16x16x32_bf16 v[108:111], a[248:251], v[120:123], v[108:111]\n\t" \
  "v_exp_f32 v71, v71\n\t" \
  "v_mfma_f32_16x16x32_bf16 v[108:111], a[244:247], v[116:119], v[108:111]\n\t" \
  "v_exp_f32 v72, v72\n\t" \
  "v_mfma_f32_16x16x32_bf16 v[108:111], a[240:243], v[112:115], v[108:111]\n\t" \
  "s_waitcnt vmcnt(3) lgkmcnt(0)\n\t" \
  "s_barrier\n\t" \
  STA("s_memrealtime s[94:95]\n\t") \
  STA("s_memtime s[92:93]\n\t") \
  "v_mfma_f32_16x16x32_bf16 a[0:3], a[128:131], v[128:131], a[0:3]\n\t" \
  "v_exp_f32 v73, v73\n\t" \
  "v_mfma_f32_16x16x32_bf16 a[0:3], a[132:135], v[132:135], a[0:3]\n\t" \
  "v_exp_f32 v74, v74\n\t" \
  "v_mfma_f32_16x16x32_bf16 a[16:19], a[136:139], v[128:131], a[16:19]\n\t" \
  "ds_read_b128 a[192:195], v220 offset:0\n\t" \
  "ds_read_b128 a[196:199], v221 offset:0\n\t" \
  "v_mfma_f32_16x16x32_bf16 a[16:19], a[140:143], v[132:135], a[16:19]\n\t" \
  "v_exp_f32 v75, v75\n\t" \
  "v_mfma_f32_16x16x32_bf16 a[32:35], a[144:147], v[128:131], a[32:35]\n\t" \
  "v_exp_f32 v76, v76\n\t" \
  "v_mfma_f32_16x16x32_bf16 a[32:35], a[148:151], v[132:135], a[32:35]\n\t" \
  "v_exp_f32 v77, v77\n\t" \
  "v_mfma_f32_16x16x32_bf16 a[48:51], a[152:155], v[128:131], a[48:51]\n\t" \
  "v_exp_f32 v78, v78\n\t" \
  "v_mfma_f32_16x16x32_bf16 a[48:51], a[156:159], v[132:135], a[48:51]\n\t" \
  "v_exp_f32 v79, v79\n\t" \
  "v_mfma_f32_16x16x32_bf16 a[64:67], a[160:163], v[128:131], a[64:67]\n\t" \
  "v_exp_f32 v96, v96\n\t" \
  "v_mfma_f32_16x16x32_bf16 a[64:67], a[164:167], v[132:135], a[64:67]\n\t" \
  "v_exp_f32 v97, v97\n\t" \
  "v_mfma_f32_16x16x32_bf16 a[80:83], a[168:171], v[128:131], a[80:83]\n\t" \
  "ds_read_b128 a[200:203], v222 offset:0\n\t" \
  "ds_read_b128 a[204:207], v223 offset:0\n\t" \
  "v_mfma_f32_16x16x32_bf16 a[80:83], a[172:175], v[132:135], a[80:83]\n\t" \
  "v_exp_f32 v98, v98\n\t" \
  "v_mfma_f32_16x16x32_bf16 a[96:99], a[176:179], v[128:131], a[96:99]\n\t" \
  STA("v_writelane_b32 %[stv], s94, 44\n\t") \
  STA("v_writelane_b32 %[stv], s92, 45\n\t") \
  "v_exp_f32 v99, v99\n\t" \
  "v_mfma_f32_16x16x32_bf16 a[96:99], a[180:183], v[132:135], a[96:99]\n\t" \
  "v_exp_f32 v100, v100\n\t" \
  "v_mfma_f32_16x16x32_bf16 a[112:115], a[184:187], v[128:131], a[112:115]\n\t" \
  "v_exp_f32 v101, v101\n\t" \
  "v_mfma_f32_16x16x32_bf16 a[112:115], a[188:191], v[132:135], a[112:115]\n\t" \
  "v_exp_f32 v102, v102\n\t" \
  "v_mfma_f32_16x16x32_bf16 v[160:163], v[216:219], v[128:131], v[160:163]\n\t" \
  "v_exp_f32 v103, v103\n\t" \
  "v_mfma_f32_16x16x32_bf16 v[160:163], v[216:219], v[132:135], v[160:163]\n\t" \
  "ds_read_b128 a[208:211], v220 offset:4096\n\t" \
  "v_cvt_pk_bf16_f32 v128, v0, v1\n\t" \
  "v_mfma_f32_16x16x32_bf16 a[4:7], a[128:131], v[136:139], a[4:7]\n\t" \
  "v_exp_f32 v104, v104\n\t" \
  ".Lhs8:\n\ts_cbranch_execz .Lhs8n\n\t" \
  "v_mfma_f32_16x16x32_bf16 a[4:7], a[132:135], v[140:143], a[4:7]\n\t" \
  "ds_read_b128 a[212:215], v221 offset:4096\n\t" \
  "v_cvt_pk_bf16_f32 v129, v2, v3\n\t" \
  "v_mfma_f32_16x16x32_bf16 a[20:23], a[136:139], v[136:139], a[20:23]\n\t" \
  "s_add_u32 m0, %[m], 65536\n\t" \
  "v_exp_f32 v105, v105\n\t" \
  "v_mfma_f32_16x16x32_bf16 a[20:23], a[140:143], v[140:143], a[20:23]\n\t" \
  "s_add_u32 s96, %[sofk], 114688\n\t" \
  "v_exp_f32 v106, v106\n\t" \
  "v_mfma_f32_16x16x32_bf16 a[36:39], a[144:147], v[136:139], a[36:39]\n\t" \
  "v_exp_f32 v107, v107\n\t" \
  "v_mfma_f32_16x16x32_bf16 a[36:39], a[148:151], v[140:143], a[36:39]\n\t" \
  "ds_read_b128 a[216:219], v222 offset:4096\n\t" \
  "v_cvt_pk_bf16_f32 v130, v4, v5\n\t" \
  "v_mfma_f32_16x16x32_bf16 a[52:55], a[152:155], v[136:139], a[52:55]\n\t" \
  "v_exp_f32 v108, v108\n\t" \
  "v_mfma_f32_16x16x32_bf16 a[52:55], a[156:159], v[140:143], a[52:55]\n\t" \
  "v_exp_f32 v109, v109\n\t" \
  "v_mfma_f32_16x16x32_bf16 a[68:71], a[160:163], v[136:139], a[68:71]\n\t" \
  "v_exp_f32 v110, v110\n\t" \
  "v_mfma_f32_16x16x32_bf16 a[68:71], a[164:167], v[140:143], a[68:71]\n\t" \
  "buffer_load_dwordx4 v224, %[sK], s96 offen offset:0 lds\n\t" \
  "v_mfma_f32_16x16x32_bf16 a[84:87], a[168:171], v[136:139], a[84:87]\n\t" \
  "ds_read_b128 a[220:223], v223 offset:4096\n\t" \
  "v_cvt_pk_bf16_f32 v131, v6, v7\n\t" \
  "v_mfma_f32_16x16x32_bf16 a[84:87], a[172:175], v[140:143], a[84:87]\n\t" \
  "v_exp_f32 v111, v111\n\t" \
  "v_mfma_f32_16x16x32_bf16 a[100:103], a[176:179], v[136:139], a[100:103]\n\t" \
  "ds_read_b128 a[224:227], v220 offset:8192\n\t" \
  "v_cvt_pk_bf16_f32 v132, v8, v9\n\t" \
  "v_mfma_f32_16x16x32_bf16 a[100:103], a[180:183], v[140:143], a[100:103]\n\t" \
  "buffer_load_dwordx4 v225, %[sK], s96 offen offset:1024 lds\n\t" \
  "v_mfma_f32_16x16x32_bf16 a[116:119], a[184:187], v[136:139], a[116:119]\n\t" \
  "ds_read_b128 a[228:231], v221 offset:8192\n\t" \
  "v_cvt_pk_bf16_f32 v133, v10, v11\n\t" \
  "v_mfma_f32_16x16x32_bf16 a[116:119], a[188:191], v[140:143], a[116:119]\n\t" \
  "ds_read_b128 a[232:235], v222 offset:8192\n\t" \
  "v_cvt_pk_bf16_f32 v134, v12, v13\n\t" \
  "v_mfma_f32_16x16x32_bf16 v[164:167], v[216:219], v[136:139], v[164:167]\n\t" \
  "buffer_load_dwordx4 v226, %[sK], s96 offen offset:2048 lds\n\t" \
  "v_mfma_f32_16x16x32_bf16 v[164:167], v[216:219], v[140:143], v[164:167]\n\t" \
  "ds_read_b128 a[236:239], v223 offset:8192\n\t" \
  "v_cvt_pk_bf16_f32 v135, v14, v15\n\t" \
  "v_mfma_f32_16x16x32_bf16 a[8:11], a[128:131], v[144:147], a[8:11]\n\t" \
  "ds_read_b128 a[240:243], v220 offset:12288\n\t" \
  "v_cvt_pk_bf16_f32 v136, v32, v33\n\t" \
  "v_mfma_f32_16x16x32_bf16 a[8:11], a[132:135], v[148:151], a[8:11]\n\t" \
  "v_cvt_pk_bf16_f32 v137, v34, v35\n\t" \
  "v_cvt_pk_bf16_f32 v138, v36, v37\n\t" \
  "v_mfma_f32_16x16x32_bf16 a[24:27], a[136:139], v[144:147], a[24:27]\n\t" \
  "buffer_load_dwordx4 v227, %[sK], s96 offen offset:3072 lds\n\t" \
  "v_mfma_f32_16x16x32_bf16 a[24:27], a[140:143], v[148:151], a[24:27]\n\t" \
  "ds_read_b128 a[244:247], v221 offset:12288\n\t" \
  "v_cvt_pk_bf16_f32 v139, v38, v39\n\t" \
  "v_mfma_f32_16x16x32_bf16 a[40:43], a[144:147], v[144:147], a[40:43]\n\t" \
  "s_add_u32 m0, %[m], 98304\n\t" \
  "s_mov_b32 s96, 28672\n\t" \
  "v_cvt_pk_bf16_f32 v140, v40, v41\n\t" \
  "v_mfma_f32_16x16x32_bf16 a[40:43], a[148:151], v[148:151], a[40:43]\n\t" \
  "v_cvt_pk_bf16_f32 v141, v42, v43\n\t" \
  "v_cvt_pk_bf16_f32 v142, v44, v45\n\t" \
  "v_mfma_f32_16x16x32_bf16 a[56:59], a[152:155], v[144:147], a[56:59]\n\t" \
  "buffer_load_dwordx4 v232, %[sG], s96 offen offset:0 lds\n\t" \
  "v_mfma_f32_16x16x32_bf16 a[56:59], a[156:159], v[148:151], a[56:59]\n\t" \
  "s_add_u32 m0, %[m], 81920\n\t" \
  "ds_read_b128 a[248:251], v222 offset:12288\n\t" \
  "v_cvt_pk_bf16_f32 v143, v46, v47\n\t" \
  "v_mfma_f32_16x16x32_bf16 a[72:75], a[160:163], v[144:147], a[72:75]\n\t" \
  "s_add_u32 s96, %[sofk], 98304\n\t" \
  "v_mfma_f32_16x16x32_bf16 a[72:75], a[164:167], v[148:151], a[72:75]\n\t" \
  "v_mfma_f32_16x16x32_bf16 a[88:91], a[168:171], v[144:147], a[88:91]\n\t" \
  "v_mfma_f32_16x16x32_bf16 a[88:91], a[172:175], v[148:151], a[88:91]\n\t" \
  "v_mfma_f32_16x16x32_bf16 a[104:107], a[176:179], v[144:147], a[104:107]\n\t" \
  "v_mfma_f32_16x16x32_bf16 a[104:107], a[180:183], v[148:151], a[104:107]\n\t" \
  "v_mfma_f32_16x16x32_bf16 a[120:123], a[184:187], v[144:147], a[120:123]\n\t" \
  "v_mfma_f32_16x16x32_bf16 a[120:123], a[188:191], v[148:151], a[120:123]\n\t" \
  "buffer_load_dwordx4 v230, %[sV], s96 offen offset:0 lds\n\t" \
  "v_mfma_f32_16x16x32_bf16 v[168:171], v[216:219], v[144:147], v[168:171]\n\t" \
  "v_mfma_f32_16x16x32_bf16 v[168:171], v[216:219], v[148:151], v[168:171]\n\t" \
  "ds_read_b128 a[252:255], v223 offset:12288\n\t" \
  "v_cvt_pk_bf16_f32 v144, v64, v65\n\t" \
  "v_mfma_f32_16x16x32_bf16 a[12:15], a[128:131], v[152:155], a[12:15]\n\t" \
  "v_cvt_pk_bf16_f32 v145, v66, v67\n\t" \
  "v_cvt_pk_bf16_f32 v146, v68, v69\n\t" \
  "v_mfma_f32_16x16x32_bf16 a[12:15], a[132:135], v[156:159], a[12:15]\n\t" \
  "buffer_load_dwordx4 v231, %[sV], s96 offen offset:1024 lds\n\t" \
  "v_mfma_f32_16x16x32_bf16 a[28:31], a[136:139], v[152:155], a[28:31]\n\t" \
  "v_cvt_pk_bf16_f32 v147, v70, v71\n\t" \
  "v_cvt_pk_bf16_f32 v148, v72, v73\n\t" \
  "v_mfma_f32_16x16x32_bf16 a[28:31], a[140:143], v[156:159], a[28:31]\n\t" \
  "v_cvt_pk_bf16_f32 v149, v74, v75\n\t" \
  "v_cvt_pk_bf16_f32 v150, v76, v77\n\t" \
  "v_mfma_f32_16x16x32_bf16 a[44:47], a[144:147], v[152:155], a[44:47]\n\t" \
  "buffer_load_dwordx4 v230, %[sV], s96 offen offset:2048 lds\n\t" \
  "v_mfma_f32_16x16x32_bf16 a[44:47], a[148:151], v[156:159], a[44:47]\n\t" \
  "v_cvt_pk_bf16_f32 v151, v78, v79\n\t" \
  "v_mfma_f32_16x16x32_bf16 a[60:63], a[152:155], v[152:155], a[60:63]\n\t" \
  "v_mfma_f32_16x16x32_bf16 a[60:63], a[156:159], v[156:159], a[60:63]\n\t" \
  "v_mfma_f32_16x16x32_bf16 a[76:79], a[160:163], v[152:155], a[76:79]\n\t" \
  "buffer_load_dwordx4 v231, %[sV], s96 offen offset:3072 lds\n\t" \
  "v_mfma_f32_16x16x32_bf16 a[76:79], a[164:167], v[156:159], a[76:79]\n\t" \
  "v_lshrrev_b32 v205, 1, v233\n\t" \
  "v_lshlrev_b32 v205, 2, v205\n\t" \
  "v_mfma_f32_16x16x32_bf16 a[92:95], a[168:171], v[152:155], a[92:95]\n\t" \
  "v_add_u32 v205, 8192, v205\n\t" \
  "v_mfma_f32_16x16x32_bf16 a[92:95], a[172:175], v[156:159], a[92:95]\n\t" \
  "buffer_load_dword v209, v205, %[ws], 0 offen sc1\n\t" \
  "v_mfma_f32_16x16x32_bf16 a[108:111], a[176:179], v[152:155], a[108:111]\n\t" \
  "s_add_u32 m0, %[m], 98304\n\t" \
  "s_mov_b32 s96, 34816\n\t" \
  "v_mfma_f32_16x16x32_bf16 a[108:111], a[180:183], v[156:159], a[108:111]\n\t" \
  "buffer_load_dwordx4 v232, %[sG], s96 offen offset:1024 lds\n\t" \
  "v_mfma_f32_16x16x32_bf16 a[124:127], a[184:187], v[152:155], a[124:127]\n\t" \
  "v_mfma_f32_16x16x32_bf16 a[124:127], a[188:191], v[156:159], a[124:127]\n\t" \
  "s_add_u32 s98, %[pf2], 49152\n\t" \
  "v_mfma_f32_16x16x32_bf16 v[172:175], v[216:219], v[152:155], v[172:175]\n\t" \
  "buffer_load_dword %[pfd], v202, %[sM], s98 offen\n\t" \
  "v_mfma_f32_16x16x32_bf16 v[172:175], v[216:219], v[156:159], v[172:175]\n\t" \
  "s_waitcnt lgkmcnt(0)\n\t" \
  "v_cvt_pk_bf16_f32 v152, v96, v97\n\t" \
  "v_cvt_pk_bf16_f32 v153, v98, v99\n\t" \
  STA("s_memrealtime s[94:95]\n\t") \
  STA("s_memtime s[92:93]\n\t") \
  "v_mfma_f32_16x16x32_bf16 v[0:3], a[192:195], v[16:19], v[176:179]\n\t" \
  "ds_read_b64_tr_b16 a[128:129], v237 offset:0\n\t" \
  "v_cvt_pk_bf16_f32 v154, v100, v101\n\t" \
  "v_mfma_f32_16x16x32_bf16 v[0:3], a[196:199], v[20:23], v[0:3]\n\t" \
  "ds_read_b64_tr_b16 a[130:131], v237 offset:4096\n\t" \
  "v_cvt_pk_bf16_f32 v155, v102, v103\n\t" \
  "v_mfma_f32_16x16x32_bf16 v[0:3], a[200:203], v[24:27], v[0:3]\n\t" \
  "ds_read_b64_tr_b16 a[132:133], v237 offset:8192\n\t" \
  "v_cvt_pk_bf16_f32 v156, v104, v105\n\t" \
  "v_mfma_f32_16x16x32_bf16 v[0:3], a[204:207], v[28:31], v[0:3]\n\t" \
  "ds_read_b64_tr_b16 a[134:135], v237 offset:12288\n\t" \
  "v_cvt_pk_bf16_f32 v157, v106, v107\n\t" \
  "v_mfma_f32_16x16x32_bf16 v[4:7], a[220:223], v[28:31], v[176:179]\n\t" \
  "ds_read_b64_tr_b16 a[136:137], v238 offset:0\n\t" \
  "v_cvt_pk_bf16_f32 v158, v108, v109\n\t" \
  "v_mfma_f32_16x16x32_bf16 v[4:7], a[216:219], v[24:27], v[4:7]\n\t" \
  "ds_read_b64_tr_b16 a[138:139], v238 offset:4096\n\t" \
  "v_cvt_pk_bf16_f32 v159, v110, v111\n\t" \
  "v_mfma_f32_16x16x32_bf16 v[4:7], a[212:215], v[20:23], v[4:7]\n\t" \
  "v_exp_f32 v0, v0\n\t" \
  "v_mfma_f32_16x16x32_bf16 v[4:7], a[208:211], v[16:19], v[4:7]\n\t" \
  "v_exp_f32 v1, v1\n\t" \
  "v_mfma_f32_16x16x32_bf16 v[8:11], a[224:227], v[16:19], v[176:179]\n\t" \
  "v_exp_f32 v2, v2\n\t" \
  "v_mfma_f32_16x16x32_bf16 v[8:11], a[228:231], v[20:23], v[8:11]\n\t" \
  "v_exp_f32 v3, v3\n\t" \
  "v_mfma_f32_16x16x32_bf16 v[8:11], a[232:235], v[24:27], v[8:11]\n\t" \
  "ds_read_b64_tr_b16 a[140:141], v238 offset:8192\n\t" \
  "ds_read_b64_tr_b16 a[142:143], v238 offset:12288\n\t" \
  "v_mfma_f32_16x16x32_bf16 v[8:11], a[236:239], v[28:31], v[8:11]\n\t" \
  "s_add_u32 s98, %[pf2], 73728\n\t" \
  "v_exp_f32 v4, v4\n\t" \
  "v_mfma_f32_16x16x32_bf16 v[12:15], a[252:255], v[28:31], v[176:179]\n\t" \
  STA("v_writelane_b32 %[stv], s94, 46\n\t") \
  STA("v_writelane_b32 %[stv], s92, 47\n\t") \
  "buffer_load_dword %[pfd], v202, %[sM], s98 offen\n\t" \
  "v_mfma_f32_16x16x32_bf16 v[12:15], a[248:251], v[24:27], v[12:15]\n\t" \
  "ds_read_b64_tr_b16 a[144:145], v237 offset:256\n\t" \
  "ds_read_b64_tr_b16 a[146:147], v237 offset:4352\n\t" \
  "v_lshl_add_u32 v207, v233, 14, %[cto]\n\t" \
  "v_min_u32 v207, .Lfz_z-.Lat_t1-4, v207\n\t" \
  "v_mfma_f32_16x16x32_bf16 v[12:15], a[244:247], v[20:23], v[12:15]\n\t" \
  "s_getpc_b64 s[96:97]\n\t" \
  ".Lat_t1:\n\t" \
  "global_load_dword %[pfd], v207, s[96:97]\n\t" \
  "v_mfma_f32_16x16x32_bf16 v[12:15], a[240:243], v[16:19], v[12:15]\n\t" \
  "v_exp_f32 v5, v5\n\t" \
  "v_mfma_f32_16x16x32_bf16 v[32:35], a[192:195], v[48:51], v[180:183]\n\t" \
  "v_exp_f32 v6, v6\n\t" \
  "v_mfma_f32_16x16x32_bf16 v[32:35], a[196:199], v[52:55], v[32:35]\n\t" \
  "v_exp_f32 v7, v7\n\t" \
  "v_mfma_f32_16x16x32_bf16 v[32:35], a[200:203], v[56:59], v[32:35]\n\t" \
  "v_exp_f32 v8, v8\n\t" \
  "v_mfma_f32_16x16x32_bf16 v[32:35], a[204:207], v[60:63], v[32:35]\n\t" \
  "v_exp_f32 v9, v9\n\t" \
  "v_mfma_f32_16x16x32_bf16 v[36:39], a[220:223], v[60:63], v[180:183]\n\t" \
  "ds_read_b64_tr_b16 a[148:149], v237 offset:8448\n\t" \
  "ds_read_b64_tr_b16 a[150:151], v237 offset:12544\n\t" \
  "v_mfma_f32_16x16x32_bf16 v[36:39], a[216:219], v[56:59], v[36:39]\n\t" \
  "v_exp_f32 v10, v10\n\t" \
  "v_mfma_f32_16x16x32_bf16 v[36:39], a[212:215], v[52:55], v[36:39]\n\t" \
  "v_exp_f32 v11, v11\n\t" \
  "v_mfma_f32_16x16x32_bf16 v[36:39], a[208:211], v[48:51], v[36:39]\n\t" \
  "ds_read_b64_tr_b16 a[152:153], v238 offset:256\n\t" \
  "ds_read_b64_tr_b16 a[154:155], v238 offset:4352\n\t" \
  "v_mfma_f32_16x16x32_bf16 v[40:43], a[224:227], v[48:51], v[180:183]\n\t" \
  "v_exp_f32 v12, v12\n\t" \
  "v_mfma_f32_16x16x32_bf16 v[40:43], a[228:231], v[52:55], v[40:43]\n\t" \
  "v_exp_f32 v13, v13\n\t" \
  "v_mfma_f32_16x16x32_bf16 v[40:43], a[232:235], v[56:59], v[40:43]\n\t" \
  "v_exp_f32 v14, v14\n\t" \
  "v_mfma_f32_16x16x32_bf16 v[40:43], a[236:239], v[60:63], v[40:43]\n\t" \
  "ds_read_b64_tr_b16 a[156:157], v238 offset:8448\n\t" \
  "ds_read_b64_tr_b16 a[158:159], v238 offset:12544\n\t" \
  "v_mfma_f32_16x16x32_bf16 v[44:47], a[252:255], v[60:63], v[180:183]\n\t" \
  "v_exp_f32 v15, v15\n\t" \
  "v_mfma_f32_16x16x32_bf16 v[44:47], a[248:251], v[56:59], v[44:47]\n\t" \
  "v_exp_f32 v32, v32\n\t" \
  "v_mfma_f32_16x16x32_bf16 v[44:47], a[244:247], v[52:55], v[44:47]\n\t" \
  "ds_read_b64_tr_b16 a[160:161], v237 offset:512\n\t" \
  "ds_read_b64_tr_b16 a[162:163], v237 offset:4608\n\t" \
  "v_mfma_f32_16x16x32_bf16 v[44:47], a[240:243], v[48:51], v[44:47]\n\t" \
  "v_exp_f32 v33, v33\n\t" \
  "v_mfma_f32_16x16x32_bf16 v[64:67], a[192:195], v[80:83], v[184:187]\n\t" \
  "v_exp_f32 v34, v34\n\t" \
  "v_mfma_f32_16x16x32_bf16 v[64:67], a[196:199], v[84:87], v[64:67]\n\t" \
  "v_exp_f32 v35, v35\n\t" \
  "v_mfma_f32_16x16x32_bf16 v[64:67], a[200:203], v[88:91], v[64:67]\n\t" \
  "ds_read_b64_tr_b16 a[164:165], v237 offset:8704\n\t" \
  "ds_read_b64_tr_b16 a[166:167], v237 offset:12800\n\t" \
  "v_mfma_f32_16x16x32_bf16 v[64:67], a[204:207], v[92:95], v[64:67]\n\t" \
  "v_exp_f32 v36, v36\n\t" \
  "v_mfma_f32_16x16x32_bf16 v[68:71], a[220:223], v[92:95], v[184:187]\n\t" \
  "v_exp_f32 v37, v37\n\t" \
  "v_mfma_f32_16x16x32_bf16 v[68:71], a[216:219], v[88:91], v[68:71]\n\t" \
  "ds_read_b64_tr_b16 a[168:169], v238 offset:512\n\t" \
  "ds_read_b64_tr_b16 a[170:171], v238 offset:4608\n\t" \
  "v_mfma_f32_16x16x32_bf16 v[68:71], a[212:215], v[84:87], v[68:71]\n\t" \
  "v_exp_f32 v38, v38\n\t" \
  "v_mfma_f32_16x16x32_bf16 v[68:71], a[208:211], v[80:83], v[68:71]\n\t" \
  "v_exp_f32 v39, v39\n\t" \
  "v_mfma_f32_16x16x32_bf16 v[72:75], a[224:227], v[80:83], v[184:187]\n\t" \
  "v_exp_f32 v40, v40\n\t" \
  "v_mfma_f32_16x16x32_bf16 v[72:75], a[228:231], v[84:87], v[72:75]\n\t" \
  "ds_read_b64_tr_b16 a[172:173], v238 offset:8704\n\t" \
  "ds_read_b64_tr_b16 a[174:175], v238 offset:12800\n\t" \
  "v_mfma_f32_16x16x32_bf16 v[72:75], a[232:235], v[88:91], v[72:75]\n\t" \
  "ds_read_b64_tr_b16 a[176:177], v237 offset:768\n\t" \
  "ds_read_b64_tr_b16 a[178:179], v237 offset:4864\n\t" \
  "v_mfma_f32_16x16x32_bf16 v[72:75], a[236:239], v[92:95], v[72:75]\n\t" \
  "s_lshr_b32 s98, %[hl18], 18\n\t" \
  "v_xor_b32 v245, s98, v233\n\t" \
  "v_and_b32 v245, 31, v245\n\t" \
  "v_mfma_f32_16x16x32_bf16 v[76:79], a[252:255], v[92:95], v[184:187]\n\t" \
  "s_waitcnt vmcnt(4)\n\t" \
  "v_and_b32 v206, 0xbfffbfff, v209\n\t" \
  "v_cmp_ne_u32_e64 s[96:97], %[tag], v206\n\t" \
  "s_cmp_lg_u64 s[96:97], 0\n\t" \
  "s_cbranch_scc1 .Lat_v\n\t" \
  ".Lat_w:\n\t" \
  "v_mfma_f32_16x16x32_bf16 v[76:79], a[248:251], v[88:91], v[76:79]\n\t" \
  "v_exp_f32 v41, v41\n\t" \
  "v_mfma_f32_16x16x32_bf16 v[76:79], a[244:247], v[84:87], v[76:79]\n\t" \
  "ds_read_b64_tr_b16 a[180:181], v237 offset:8960\n\t" \
  "v_and_b32 v206, 1, v233\n\t" \
  "v_mfma_f32_16x16x32_bf16 v[76:79], a[240:243], v[80:83], v[76:79]\n\t" \
  "v_exp_f32 v42, v42\n\t" \
  "v_mfma_f32_16x16x32_bf16 v[96:99], a[192:195], v[112:115], v[188:191]\n\t" \
  "ds_read_b64_tr_b16 a[182:183], v237 offset:13056\n\t" \
  "v_lshlrev_b32 v206, 4, v206\n\t" \
  "v_mfma_f32_16x16x32_bf16 v[96:99], a[196:199], v[116:119], v[96:99]\n\t" \
  "ds_read_b64_tr_b16 a[184:185], v238 offset:768\n\t" \
  "v_lshrrev_b32 v206, v206, v209\n\t" \
  "v_mfma_f32_16x16x32_bf16 v[96:99], a[200:203], v[120:123], v[96:99]\n\t" \
  "v_exp_f32 v43, v43\n\t" \
  "v_mfma_f32_16x16x32_bf16 v[96:99], a[204:207], v[124:127], v[96:99]\n\t" \
  "ds_read_b64_tr_b16 a[186:187], v238 offset:4864\n\t" \
  "v_and_b32 v206, 0x4000, v206\n\t" \
  "v_mfma_f32_16x16x32_bf16 v[100:103], a[220:223], v[124:127], v[188:191]\n\t" \
  "v_exp_f32 v44, v44\n\t" \
  "v_mfma_f32_16x16x32_bf16 v[100:103], a[216:219], v[120:123], v[100:103]\n\t" \
  "ds_read_b64_tr_b16 a[188:189], v238 offset:8960\n\t" \
  "v_cmp_ne_u32_e64 s[96:97], 0, v206\n\t" \
  STS("s_nop 4\n\t") \
  STS("v_writelane_b32 %[stv], s96, 60\n\t") \
  STS("v_writelane_b32 %[stv], s97, 61\n\t") \
  "v_mfma_f32_16x16x32_bf16 v[100:103], a[212:215], v[116:119], v[100:103]\n\t" \
  "s_bcnt1_i32_b32 %[nb], s96\n\t" \
  "s_bcnt1_i32_b32 %[na], s97\n\t" \
  "s_lshr_b32 s96, s96, 16\n\t" \
  "s_lshr_b32 s98, %[hl18], 18\n\t" \
  "s_mul_i32 s98, s98, 127\n\t" \
  "v_add_lshl_u32 v245, v245, s98, 2\n\t" \
  "v_mfma_f32_16x16x32_bf16 v[100:103], a[208:211], v[112:115], v[100:103]\n\t" \
  "s_lshr_b32 s97, s97, 16\n\t" \
  "ds_read_b64_tr_b16 a[190:191], v238 offset:13056\n\t" \
  "s_bcnt1_i32_b32 s96, s96\n\t" \
  "v_mfma_f32_16x16x32_bf16 v[104:107], a[224:227], v[112:115], v[188:191]\n\t" \
  "s_bcnt1_i32_b32 s97, s97\n\t" \
  "s_sub_u32 s96, s97, s96\n\t" \
  "s_sub_u32 %[nb], %[na], %[nb]\n\t" \
  "s_ashr_i32 %[nb], %[nb], 31\n\t" \
  "v_mfma_f32_16x16x32_bf16 v[104:107], a[228:231], v[116:119], v[104:107]\n\t" \
  "s_xor_b32 s96, s96, %[nb]\n\t" \
  "v_exp_f32 v45, v45\n\t" \
  "v_mfma_f32_16x16x32_bf16 v[104:107], a[232:235], v[120:123], v[104:107]\n\t" \
  "s_sub_u32 s96, s96, %[nb]\n\t" \
  "v_exp_f32 v46, v46\n\t" \
  "v_mfma_f32_16x16x32_bf16 v[104:107], a[236:239], v[124:127], v[104:107]\n\t" \
  "s_sub_u32 %[na], 6, s96\n\t" \
  "v_exp_f32 v47, v47\n\t" \
  "v_mfma_f32_16x16x32_bf16 v[108:111], a[252:255], v[124:127], v[188:191]\n\t" \
  "s_ashr_i32 %[na], %[na], 31\n\t" \
  "v_exp_f32 v64, v64\n\t" \
  "v_mfma_f32_16x16x32_bf16 v[108:111], a[248:251], v[120:123], v[108:111]\n\t" \
  "s_and_b32 %[nb], %[nb], 0x2000\n\t" \
  "v_exp_f32 v65, v65\n\t" \
  "v_mfma_f32_16x16x32_bf16 v[108:111], a[244:247], v[116:119], v[108:111]\n\t" \
  "s_xor_b32 s96, %[po], %[nb]\n\t" \
  "v_exp_f32 v66, v66\n\t" \
  "v_mfma_f32_16x16x32_bf16 v[108:111], a[240:243], v[112:115], v[108:111]\n\t" \
  "s_waitcnt lgkmcnt(0)\n\t" \
  "s_barrier\n\t" \
  STA("s_memrealtime s[94:95]\n\t") \
  STA("s_memtime s[92:93]\n\t") \
  "v_mfma_f32_16x16x32_bf16 a[0:3], a[128:131], v[128:131], a[0:3]\n\t" \
  "s_and_b32 s96, s96, %[na]\n\t" \
  "v_exp_f32 v67, v67\n\t" \
  "v_mfma_f32_16x16x32_bf16 a[0:3], a[132:135], v[132:135], a[0:3]\n\t" \
  "s_xor_b32 %[eso], %[eso], s96\n\t" \
  STS("v_writelane_b32 %[stv], %[na], 57\n\t") \
  STS("v_writelane_b32 %[stv], %[nb], 59\n\t") \
  "v_exp_f32 v68, v68\n\t" \
  "v_mfma_f32_16x16x32_bf16 a[16:19], a[136:139], v[128:131], a[16:19]\n\t" \
  "s_xor_b32 s96, %[px], %[nb]\n\t" \
  "ds_read_b128 a[192:195], v220 offset:16384\n\t" \
  "s_and_b32 s96, s96, %[na]\n\t" \
  "v_mfma_f32_16x16x32_bf16 a[16:19], a[140:143], v[132:135], a[16:19]\n\t" \
  "s_xor_b32 %[xcb], %[xcb], s96\n\t" \
  "v_exp_f32 v69, v69\n\t" \
  "v_mfma_f32_16x16x32_bf16 a[32:35], a[144:147], v[128:131], a[32:35]\n\t" \
  "s_xor_b32 %[frb], %[frb], %[nb]\n\t" \
  "v_exp_f32 v70, v70\n\t" \
  "v_mfma_f32_16x16x32_bf16 a[32:35], a[148:151], v[132:135], a[32:35]\n\t" \
  "s_xor_b32 %[fw2], %[fw2], %[nb]\n\t" \
  "v_exp_f32 v71, v71\n\t" \
  "v_mfma_f32_16x16x32_bf16 a[48:51], a[152:155], v[128:131], a[48:51]\n\t" \
  "ds_read_b128 a[196:199], v221 offset:16384\n\t" \
  "ds_read_b128 a[200:203], v222 offset:16384\n\t" \
  "v_mfma_f32_16x16x32_bf16 a[48:51], a[156:159], v[132:135], a[48:51]\n\t" \
  "v_exp_f32 v72, v72\n\t" \
  "v_mfma_f32_16x16x32_bf16 a[64:67], a[160:163], v[128:131], a[64:67]\n\t" \
  "v_exp_f32 v73, v73\n\t" \
  "v_mfma_f32_16x16x32_bf16 a[64:67], a[164:167], v[132:135], a[64:67]\n\t" \
  "v_exp_f32 v74, v74\n\t" \
  "v_mfma_f32_16x16x32_bf16 a[80:83], a[168:171], v[128:131], a[80:83]\n\t" \
  "v_exp_f32 v75, v75\n\t" \
  "v_mfma_f32_16x16x32_bf16 a[80:83], a[172:175], v[132:135], a[80:83]\n\t" \
  "v_exp_f32 v76, v76\n\t" \
  "v_mfma_f32_16x16x32_bf16 a[96:99], a[176:179], v[128:131], a[96:99]\n\t" \
  STA("v_writelane_b32 %[stv], s94, 48\n\t") \
  STA("v_writelane_b32 %[stv], s92, 49\n\t") \
  "v_exp_f32 v77, v77\n\t" \
  "v_mfma_f32_16x16x32_bf16 a[96:99], a[180:183], v[132:135], a[96:99]\n\t" \
  "v_exp_f32 v78, v78\n\t" \
  "v_mfma_f32_16x16x32_bf16 a[112:115], a[184:187], v[128:131], a[112:115]\n\t" \
  "ds_read_b128 a[204:207], v223 offset:16384\n\t" \
  "ds_read_b128 a[208:211], v220 offset:20480\n\t" \
  "v_mfma_f32_16x16x32_bf16 a[112:115], a[188:191], v[132:135], a[112:115]\n\t" \
  "v_exp_f32 v79, v79\n\t" \
  "v_mfma_f32_16x16x32_bf16 v[160:163], v[216:219], v[128:131], v[160:163]\n\t" \
  "v_exp_f32 v96, v96\n\t" \
  "v_mfma_f32_16x16x32_bf16 v[160:163], v[216:219], v[132:135], v[160:163]\n\t" \
  "v_exp_f32 v97, v97\n\t" \
  "v_mfma_f32_16x16x32_bf16 a[4:7], a[128:131], v[136:139], a[4:7]\n\t" \
  "v_exp_f32 v98, v98\n\t" \
  "v_mfma_f32_16x16x32_bf16 a[4:7], a[132:135], v[140:143], a[4:7]\n\t" \
  "ds_read_b128 a[212:215], v221 offset:20480\n\t" \
  "v_cvt_pk_bf16_f32 v128, v0, v1\n\t" \
  "v_mfma_f32_16x16x32_bf16 a[20:23], a[136:139], v[136:139], a[20:23]\n\t" \
  "s_add_u32 m0, %[m], 147456\n\t" \
  "v_exp_f32 v99, v99\n\t" \
  "v_mfma_f32_16x16x32_bf16 a[20:23], a[140:143], v[140:143], a[20:23]\n\t" \
  "s_add_u32 s96, %[sofk], 114688\n\t" \
  "v_exp_f32 v100, v100\n\t" \
  "v_mfma_f32_16x16x32_bf16 a[36:39], a[144:147], v[136:139], a[36:39]\n\t" \
  "v_exp_f32 v101, v101\n\t" \
  "v_mfma_f32_16x16x32_bf16 a[36:39], a[148:151], v[140:143], a[36:39]\n\t" \
  "ds_read_b128 a[216:219], v222 offset:20480\n\t" \
  "v_cvt_pk_bf16_f32 v129, v2, v3\n\t" \
  "v_mfma_f32_16x16x32_bf16 a[52:55], a[152:155], v[136:139], a[52:55]\n\t" \
  "v_exp_f32 v102, v102\n\t" \
  "v_mfma_f32_16x16x32_bf16 a[52:55], a[156:159], v[140:143], a[52:55]\n\t" \
  "v_exp_f32 v103, v103\n\t" \
  "v_mfma_f32_16x16x32_bf16 a[68:71], a[160:163], v[136:139], a[68:71]\n\t" \
  "v_exp_f32 v104, v104\n\t" \
  "v_mfma_f32_16x16x32_bf16 a[68:71], a[164:167], v[140:143], a[68:71]\n\t" \
  "buffer_load_dwordx4 v230, %[sV], s96 offen offset:0 lds\n\t" \
  "v_mfma_f32_16x16x32_bf16 a[84:87], a[168:171], v[136:139], a[84:87]\n\t" \
  "ds_read_b128 a[220:223], v223 offset:20480\n\t" \
  "v_cvt_pk_bf16_f32 v130, v4, v5\n\t" \
  "v_mfma_f32_16x16x32_bf16 a[84:87], a[172:175], v[140:143], a[84:87]\n\t" \
  "v_exp_f32 v105, v105\n\t" \
  "v_mfma_f32_16x16x32_bf16 a[100:103], a[176:179], v[136:139], a[100:103]\n\t" \
  "ds_read_b128 a[224:227], v220 offset:24576\n\t" \
  "v_cvt_pk_bf16_f32 v131, v6, v7\n\t" \
  "v_mfma_f32_16x16x32_bf16 a[100:103], a[180:183], v[140:143], a[100:103]\n\t" \
  "buffer_load_dwordx4 v231, %[sV], s96 offen offset:1024 lds\n\t" \
  "v_mfma_f32_16x16x32_bf16 a[116:119], a[184:187], v[136:139], a[116:119]\n\t" \
  "v_exp_f32 v106, v106\n\t" \
  "v_mfma_f32_16x16x32_bf16 a[116:119], a[188:191], v[140:143], a[116:119]\n\t" \
  "v_exp_f32 v107, v107\n\t" \
  "v_mfma_f32_16x16x32_bf16 v[164:167], v[216:219], v[136:139], v[164:167]\n\t" \
  "buffer_load_dwordx4 v230, %[sV], s96 offen offset:2048 lds\n\t" \
  "v_mfma_f32_16x16x32_bf16 v[164:167], v[216:219], v[140:143], v[164:167]\n\t" \
  "ds_read_b128 a[228:231], v221 offset:24576\n\t" \
  "v_cvt_pk_bf16_f32 v132, v8, v9\n\t" \
  "v_mfma_f32_16x16x32_bf16 a[8:11], a[128:131], v[144:147], a[8:11]\n\t" \
  "v_exp_f32 v108, v108\n\t" \
  "v_mfma_f32_16x16x32_bf16 a[8:11], a[132:135], v[148:151], a[8:11]\n\t" \
  "v_exp_f32 v109, v109\n\t" \
  "v_mfma_f32_16x16x32_bf16 a[24:27], a[136:139], v[144:147], a[24:27]\n\t" \
  "buffer_load_dwordx4 v231, %[sV], s96 offen offset:3072 lds\n\t" \
  "v_mfma_f32_16x16x32_bf16 a[24:27], a[140:143], v[148:151], a[24:27]\n\t" \
  "s_add_u32 m0, %[m], 49152\n\t" \
  "ds_read_b128 a[232:235], v222 offset:24576\n\t" \
  "v_cvt_pk_bf16_f32 v133, v10, v11\n\t" \
  "v_mfma_f32_16x16x32_bf16 a[40:43], a[144:147], v[144:147], a[40:43]\n\t" \
  "s_xor_b32 s97, %[wob], 262144\n\t" \
  "v_exp_f32 v110, v110\n\t" \
  "v_mfma_f32_16x16x32_bf16 a[40:43], a[148:151], v[148:151], a[40:43]\n\t" \
  "v_exp_f32 v111, v111\n\t" \
  "v_mfma_f32_16x16x32_bf16 a[56:59], a[152:155], v[144:147], a[56:59]\n\t" \
  "ds_read_b128 a[236:239], v223 offset:24576\n\t" \
  "v_cvt_pk_bf16_f32 v134, v12, v13\n\t" \
  "v_mfma_f32_16x16x32_bf16 a[56:59], a[156:159], v[148:151], a[56:59]\n\t" \
  "ds_read_b128 a[240:243], v220 offset:28672\n\t" \
  "v_cvt_pk_bf16_f32 v135, v14, v15\n\t" \
  "v_mfma_f32_16x16x32_bf16 a[72:75], a[160:163], v[144:147], a[72:75]\n\t" \
  "ds_read_b128 a[244:247], v221 offset:28672\n\t" \
  "v_cvt_pk_bf16_f32 v136, v32, v33\n\t" \
  "v_mfma_f32_16x16x32_bf16 a[72:75], a[164:167], v[148:151], a[72:75]\n\t" \
  "ds_read_b128 a[248:251], v222 offset:28672\n\t" \
  "v_cvt_pk_bf16_f32 v137, v34, v35\n\t" \
  "v_mfma_f32_16x16x32_bf16 a[88:91], a[168:171], v[144:147], a[88:91]\n\t" \
  "v_cvt_pk_bf16_f32 v138, v36, v37\n\t" \
  "v_cvt_pk_bf16_f32 v139, v38, v39\n\t" \
  "v_mfma_f32_16x16x32_bf16 a[88:91], a[172:175], v[148:151], a[88:91]\n\t" \
  "ds_read_b128 a[252:255], v223 offset:28672\n\t" \
  "v_cvt_pk_bf16_f32 v140, v40, v41\n\t" \
  "v_mfma_f32_16x16x32_bf16 a[104:107], a[176:179], v[144:147], a[104:107]\n\t" \
  "buffer_load_dwordx4 v240, %[wo], s97 offen offset:0 lds\n\t" \
  "v_mfma_f32_16x16x32_bf16 a[104:107], a[180:183], v[148:151], a[104:107]\n\t" \
  "s_add_u32 s97, s97, 7168\n\t" \
  "v_cvt_pk_bf16_f32 v141, v42, v43\n\t" \
  "v_cvt_pk_bf16_f32 v142, v44, v45\n\t" \
  "v_mfma_f32_16x16x32_bf16 a[120:123], a[184:187], v[144:147], a[120:123]\n\t" \
  "v_cvt_pk_bf16_f32 v143, v46, v47\n\t" \
  "v_mfma_f32_16x16x32_bf16 a[120:123], a[188:191], v[148:151], a[120:123]\n\t" \
  "buffer_load_dwordx4 v241, %[wo], s97 offen offset:1024 lds\n\t" \
  "v_mfma_f32_16x16x32_bf16 v[168:171], v[216:219], v[144:147], v[168:171]\n\t" \
  "s_add_u32 s97, s97, 7168\n\t" \
  "v_mfma_f32_16x16x32_bf16 v[168:171], v[216:219], v[148:151], v[168:171]\n\t" \
  "v_cvt_pk_bf16_f32 v144, v64, v65\n\t" \
  "v_cvt_pk_bf16_f32 v145, v66, v67\n\t" \
  "v_mfma_f32_16x16x32_bf16 a[12:15], a[128:131], v[152:155], a[12:15]\n\t" \
  "v_cvt_pk_bf16_f32 v146, v68, v69\n\t" \
  "v_cvt_pk_bf16_f32 v147, v70, v71\n\t" \
  "v_mfma_f32_16x16x32_bf16 a[12:15], a[132:135], v[156:159], a[12:15]\n\t" \
  "buffer_load_dwordx4 v240, %[wo], s97 offen offset:2048 lds\n\t" \
  "v_mfma_f32_16x16x32_bf16 a[28:31], a[136:139], v[152:155], a[28:31]\n\t" \
  "s_add_u32 s97, s97, 7168\n\t" \
  "v_cvt_pk_bf16_f32 v148, v72, v73\n\t" \
  "v_cvt_pk_bf16_f32 v149, v74, v75\n\t" \
  "v_mfma_f32_16x16x32_bf16 a[28:31], a[140:143], v[156:159], a[28:31]\n\t" \
  "v_cvt_pk_bf16_f32 v150, v76, v77\n\t" \
  "v_cvt_pk_bf16_f32 v151, v78, v79\n\t" \
  "v_mfma_f32_16x16x32_bf16 a[44:47], a[144:147], v[152:155], a[44:47]\n\t" \
  "buffer_load_dwordx4 v241, %[wo], s97 offen offset:3072 lds\n\t" \
  "v_mfma_f32_16x16x32_bf16 a[44:47], a[148:151], v[156:159], a[44:47]\n\t" \
  "v_mfma_f32_16x16x32_bf16 a[60:63], a[152:155], v[152:155], a[60:63]\n\t" \
  "s_add_u32 m0, %[m], 98304\n\t" \
  "s_mov_b32 s96, 40960\n\t" \
  "v_mfma_f32_16x16x32_bf16 a[60:63], a[156:159], v[156:159], a[60:63]\n\t" \
  "v_mfma_f32_16x16x32_bf16 a[76:79], a[160:163], v[152:155], a[76:79]\n\t" \
  "v_mfma_f32_16x16x32_bf16 a[76:79], a[164:167], v[156:159], a[76:79]\n\t" \
  "buffer_load_dwordx4 v232, %[sG], s96 offen offset:2048 lds\n\t" \
  "v_mfma_f32_16x16x32_bf16 a[92:95], a[168:171], v[152:155], a[92:95]\n\t" \
  "s_mov_b32 s96, 47104\n\t" \
  "v_mfma_f32_16x16x32_bf16 a[92:95], a[172:175], v[156:159], a[92:95]\n\t" \
  "v_mfma_f32_16x16x32_bf16 a[108:111], a[176:179], v[152:155], a[108:111]\n\t" \
  "v_mfma_f32_16x16x32_bf16 a[108:111], a[180:183], v[156:159], a[108:111]\n\t" \
  "buffer_load_dwordx4 v232, %[sG], s96 offen offset:3072 lds\n\t" \
  "v_mfma_f32_16x16x32_bf16 a[124:127], a[184:187], v[152:155], a[124:127]\n\t" \
  "v_mfma_f32_16x16x32_bf16 a[124:127], a[188:191], v[156:159], a[124:127]\n\t" \
  "v_mfma_f32_16x16x32_bf16 v[172:175], v[216:219], v[152:155], v[172:175]\n\t" \
  "v_mfma_f32_16x16x32_bf16 v[172:175], v[216:219], v[156:159], v[172:175]\n\t" \
  "s_waitcnt lgkmcnt(0)\n\t" \
  "s_barrier\n\t" \
  STA("s_memrealtime s[94:95]\n\t") \
  STA("s_memtime s[92:93]\n\t") \
  "v_mfma_f32_16x16x32_bf16 v[0:3], a[192:195], v[16:19], v[176:179]\n\t" \
  "ds_read_b64_tr_b16 a[128:129], v228 offset:0\n\t" \
  "v_cvt_pk_bf16_f32 v152, v96, v97\n\t" \
  "v_mfma_f32_16x16x32_bf16 v[0:3], a[196:199], v[20:23], v[0:3]\n\t" \
  "ds_read_b64_tr_b16 a[130:131], v228 offset:4096\n\t" \
  "v_cvt_pk_bf16_f32 v153, v98, v99\n\t" \
  "v_mfma_f32_16x16x32_bf16 v[0:3], a[200:203], v[24:27], v[0:3]\n\t" \
  "ds_read_b64_tr_b16 a[132:133], v228 offset:8192\n\t" \
  "v_cvt_pk_bf16_f32 v154, v100, v101\n\t" \
  "v_mfma_f32_16x16x32_bf16 v[0:3], a[204:207], v[28:31], v[0:3]\n\t" \
  "ds_read_b64_tr_b16 a[134:135], v228 offset:12288\n\t" \
  "v_cvt_pk_bf16_f32 v155, v102, v103\n\t" \
  "v_mfma_f32_16x16x32_bf16 v[4:7], a[220:223], v[28:31], v[176:179]\n\t" \
  "s_add_u32 m0, %[m], 65536\n\t" \
  "ds_read_b64_tr_b16 a[136:137], v229 offset:0\n\t" \
  "v_cvt_pk_bf16_f32 v156, v104, v105\n\t" \
  "v_mfma_f32_16x16x32_bf16 v[4:7], a[216:219], v[24:27], v[4:7]\n\t" \
  "s_xor_b32 s97, %[wob], 393216\n\t" \
  "v_exp_f32 v0, v0\n\t" \
  "v_mfma_f32_16x16x32_bf16 v[4:7], a[212:215], v[20:23], v[4:7]\n\t" \
  "v_exp_f32 v1, v1\n\t" \
  "v_mfma_f32_16x16x32_bf16 v[4:7], a[208:211], v[16:19], v[4:7]\n\t" \
  "v_exp_f32 v2, v2\n\t" \
  "v_mfma_f32_16x16x32_bf16 v[8:11], a[224:227], v[16:19], v[176:179]\n\t" \
  "v_exp_f32 v3, v3\n\t" \
  "v_mfma_f32_16x16x32_bf16 v[8:11], a[228:231], v[20:23], v[8:11]\n\t" \
  "ds_read_b64_tr_b16 a[138:139], v229 offset:4096\n\t" \
  "v_cvt_pk_bf16_f32 v157, v106, v107\n\t" \
  "v_mfma_f32_16x16x32_bf16 v[8:11], a[232:235], v[24:27], v[8:11]\n\t" \
  "ds_read_b64_tr_b16 a[140:141], v229 offset:8192\n\t" \
  "v_cvt_pk_bf16_f32 v158, v108, v109\n\t" \
  "v_mfma_f32_16x16x32_bf16 v[8:11], a[236:239], v[28:31], v[8:11]\n\t" \
  "v_exp_f32 v4, v4\n\t" \
  "v_mfma_f32_16x16x32_bf16 v[12:15], a[252:255], v[28:31], v[176:179]\n\t" \
  STA("v_writelane_b32 %[stv], s94, 50\n\t") \
  STA("v_writelane_b32 %[stv], s92, 51\n\t") \
  "buffer_load_dwordx4 v240, %[wo], s97 offen offset:0 lds\n\t" \
  "v_mfma_f32_16x16x32_bf16 v[12:15], a[248:251], v[24:27], v[12:15]\n\t" \
  "s_add_u32 s97, s97, 7168\n\t" \
  "ds_read_b64_tr_b16 a[142:143], v229 offset:12288\n\t" \
  "v_cvt_pk_bf16_f32 v159, v110, v111\n\t" \
  "v_mfma_f32_16x16x32_bf16 v[12:15], a[244:247], v[20:23], v[12:15]\n\t" \
  "v_exp_f32 v5, v5\n\t" \
  "v_mfma_f32_16x16x32_bf16 v[12:15], a[240:243], v[16:19], v[12:15]\n\t" \
  "v_exp_f32 v6, v6\n\t" \
  "v_mfma_f32_16x16x32_bf16 v[32:35], a[192:195], v[48:51], v[180:183]\n\t" \
  "buffer_load_dwordx4 v241, %[wo], s97 offen offset:1024 lds\n\t" \
  "v_mfma_f32_16x16x32_bf16 v[32:35], a[196:199], v[52:55], v[32:35]\n\t" \
  "s_add_u32 s97, s97, 7168\n\t" \
  "ds_read_b64_tr_b16 a[144:145], v228 offset:256\n\t" \
  "ds_read_b64_tr_b16 a[146:147], v228 offset:4352\n\t" \
  "v_mfma_f32_16x16x32_bf16 v[32:35], a[200:203], v[56:59], v[32:35]\n\t" \
  "v_exp_f32 v7, v7\n\t" \
  "v_mfma_f32_16x16x32_bf16 v[32:35], a[204:207], v[60:63], v[32:35]\n\t" \
  "v_exp_f32 v8, v8\n\t" \
  "v_mfma_f32_16x16x32_bf16 v[36:39], a[220:223], v[60:63], v[180:183]\n\t" \
  "buffer_load_dwordx4 v240, %[wo], s97 offen offset:2048 lds\n\t" \
  "v_mfma_f32_16x16x32_bf16 v[36:39], a[216:219], v[56:59], v[36:39]\n\t" \
  "s_add_u32 s97, s97, 7168\n\t" \
  "ds_read_b64_tr_b16 a[148:149], v228 offset:8448\n\t" \
  "ds_read_b64_tr_b16 a[150:151], v228 offset:12544\n\t" \
  "v_mfma_f32_16x16x32_bf16 v[36:39], a[212:215], v[52:55], v[36:39]\n\t" \
  "v_exp_f32 v9, v9\n\t" \
  "v_mfma_f32_16x16x32_bf16 v[36:39], a[208:211], v[48:51], v[36:39]\n\t" \
  "ds_read_b64_tr_b16 a[152:153], v229 offset:256\n\t" \
  "ds_read_b64_tr_b16 a[154:155], v229 offset:4352\n\t" \
  "v_mfma_f32_16x16x32_bf16 v[40:43], a[224:227], v[48:51], v[180:183]\n\t" \
  "buffer_load_dwordx4 v241, %[wo], s97 offen offset:3072 lds\n\t" \
  "v_mfma_f32_16x16x32_bf16 v[40:43], a[228:231], v[52:55], v[40:43]\n\t" \
  "v_exp_f32 v10, v10\n\t" \
  "v_mfma_f32_16x16x32_bf16 v[40:43], a[232:235], v[56:59], v[40:43]\n\t" \
  "v_exp_f32 v11, v11\n\t" \
  "v_mfma_f32_16x16x32_bf16 v[40:43], a[236:239], v[60:63], v[40:43]\n\t" \
  "ds_read_b64_tr_b16 a[156:157], v229 offset:8448\n\t" \
  "ds_read_b64_tr_b16 a[158:159], v229 offset:12544\n\t" \
  "v_mfma_f32_16x16x32_bf16 v[44:47], a[252:255], v[60:63], v[180:183]\n\t" \
  "v_exp_f32 v12, v12\n\t" \
  "v_mfma_f32_16x16x32_bf16 v[44:47], a[248:251], v[56:59], v[44:47]\n\t" \
  "v_exp_f32 v13, v13\n\t" \
  "v_mfma_f32_16x16x32_bf16 v[44:47], a[244:247], v[52:55], v[44:47]\n\t" \
  "ds_read_b64_tr_b16 a[160:161], v228 offset:512\n\t" \
  "ds_read_b64_tr_b16 a[162:163], v228 offset:4608\n\t" \
  "v_mfma_f32_16x16x32_bf16 v[44:47], a[240:243], v[48:51], v[44:47]\n\t" \
  "v_exp_f32 v14, v14\n\t" \
  "v_mfma_f32_16x16x32_bf16 v[64:67], a[192:195], v[80:83], v[184:187]\n\t" \
  "v_exp_f32 v15, v15\n\t" \
  "v_mfma_f32_16x16x32_bf16 v[64:67], a[196:199], v[84:87], v[64:67]\n\t" \
  "v_exp_f32 v32, v32\n\t" \
  "v_mfma_f32_16x16x32_bf16 v[64:67], a[200:203], v[88:91], v[64:67]\n\t" \
  "ds_read_b64_tr_b16 a[164:165], v228 offset:8704\n\t" \
  "ds_read_b64_tr_b16 a[166:167], v228 offset:12800\n\t" \
  "v_mfma_f32_16x16x32_bf16 v[64:67], a[204:207], v[92:95], v[64:67]\n\t" \
  "v_exp_f32 v33, v33\n\t" \
  "v_mfma_f32_16x16x32_bf16 v[68:71], a[220:223], v[92:95], v[184:187]\n\t" \
  "v_exp_f32 v34, v34\n\t" \
  "v_mfma_f32_16x16x32_bf16 v[68:71], a[216:219], v[88:91], v[68:71]\n\t" \
  "ds_read_b64_tr_b16 a[168:169], v229 offset:512\n\t" \
  "ds_read_b64_tr_b16 a[170:171], v229 offset:4608\n\t" \
  "v_mfma_f32_16x16x32_bf16 v[68:71], a[212:215], v[84:87], v[68:71]\n\t" \
  "v_exp_f32 v35, v35\n\t" \
  "v_mfma_f32_16x16x32_bf16 v[68:71], a[208:211], v[80:83], v[68:71]\n\t" \
  "v_exp_f32 v36, v36\n\t" \
  "v_mfma_f32_16x16x32_bf16 v[72:75], a[224:227], v[80:83], v[184:187]\n\t" \
  "v_exp_f32 v37, v37\n\t" \
  "v_mfma_f32_16x16x32_bf16 v[72:75], a[228:231], v[84:87], v[72:75]\n\t" \
  "ds_read_b64_tr_b16 a[172:173], v229 offset:8704\n\t" \
  "ds_read_b64_tr_b16 a[174:175], v229 offset:12800\n\t" \
  "v_mfma_f32_16x16x32_bf16 v[72:75], a[232:235], v[88:91], v[72:75]\n\t" \
  "v_exp_f32 v38, v38\n\t" \
  "v_mfma_f32_16x16x32_bf16 v[72:75], a[236:239], v[92:95], v[72:75]\n\t" \
  "v_exp_f32 v39, v39\n\t" \
  "v_mfma_f32_16x16x32_bf16 v[76:79], a[252:255], v[92:95], v[184:187]\n\t" \
  "ds_read_b64_tr_b16 a[176:177], v228 offset:768\n\t" \
  "ds_read_b64_tr_b16 a[178:179], v228 offset:4864\n\t" \
  ".Lhs9:\n\ts_cbranch_execz .Lhs9n\n\t" \
  "v_mfma_f32_16x16x32_bf16 v[76:79], a[248:251], v[88:91], v[76:79]\n\t" \
  "v_exp_f32 v40, v40\n\t" \
  "v_mfma_f32_16x16x32_bf16 v[76:79], a[244:247], v[84:87], v[76:79]\n\t" \
  "v_exp_f32 v41, v41\n\t" \
  "v_mfma_f32_16x16x32_bf16 v[76:79], a[240:243], v[80:83], v[76:79]\n\t" \
  "v_exp_f32 v42, v42\n\t" \
  "v_mfma_f32_16x16x32_bf16 v[96:99], a[192:195], v[112:115], v[188:191]\n\t" \
  "ds_read_b64_tr_b16 a[180:181], v228 offset:8960\n\t" \
  "ds_read_b64_tr_b16 a[182:183], v228 offset:13056\n\t" \
  "v_mfma_f32_16x16x32_bf16 v[96:99], a[196:199], v[116:119], v[96:99]\n\t" \
  "v_exp_f32 v43, v43\n\t" \
  "v_mfma_f32_16x16x32_bf16 v[96:99], a[200:203], v[120:123], v[96:99]\n\t" \
  "v_exp_f32 v44, v44\n\t" \
  "v_mfma_f32_16x16x32_bf16 v[96:99], a[204:207], v[124:127], v[96:99]\n\t" \
  "ds_read_b64_tr_b16 a[184:185], v229 offset:768\n\t" \
  "ds_read_b64_tr_b16 a[186:187], v229 offset:4864\n\t" \
  "v_mfma_f32_16x16x32_bf16 v[100:103], a[220:223], v[124:127], v[188:191]\n\t" \
  "v_exp_f32 v45, v45\n\t" \
  "v_mfma_f32_16x16x32_bf16 v[100:103], a[216:219], v[120:123], v[100:103]\n\t" \
  "v_exp_f32 v46, v46\n\t" \
  "v_mfma_f32_16x16x32_bf16 v[100:103], a[212:215], v[116:119], v[100:103]\n\t" \
  "v_exp_f32 v47, v47\n\t" \
  "v_mfma_f32_16x16x32_bf16 v[100:103], a[208:211], v[112:115], v[100:103]\n\t" \
  "ds_read_b64_tr_b16 a[188:189], v229 offset:8960\n\t" \
  "ds_read_b64_tr_b16 a[190:191], v229 offset:13056\n\t" \
  "v_mfma_f32_16x16x32_bf16 v[104:107], a[224:227], v[112:115], v[188:191]\n\t" \
  "v_exp_f32 v64, v64\n\t" \
  "v_mfma_f32_16x16x32_bf16 v[104:107], a[228:231], v[116:119], v[104:107]\n\t" \
  "v_exp_f32 v65, v65\n\t" \
  "v_mfma_f32_16x16x32_bf16 v[104:107], a[232:235], v[120:123], v[104:107]\n\t" \
  "v_exp_f32 v66, v66\n\t" \
  "v_mfma_f32_16x16x32_bf16 v[104:107], a[236:239], v[124:127], v[104:107]\n\t" \
  "v_exp_f32 v67, v67\n\t" \
  "v_mfma_f32_16x16x32_bf16 v[108:111], a[252:255], v[124:127], v[188:191]\n\t" \
  "v_exp_f32 v68, v68\n\t" \
  "v_mfma_f32_16x16x32_bf16 v[108:111], a[248:251], v[120:123], v[108:111]\n\t" \
  "v_exp_f32 v69, v69\n\t" \
  "v_mfma_f32_16x16x32_bf16 v[108:111], a[244:247], v[116:119], v[108:111]\n\t" \
  "v_exp_f32 v70, v70\n\t" \
  "v_mfma_f32_16x16x32_bf16 v[108:111], a[240:243], v[112:115], v[108:111]\n\t" \
  "s_waitcnt vmcnt(6) lgkmcnt(0)\n\t" \
  "s_barrier\n\t" \
  STA("s_memrealtime s[94:95]\n\t") \
  STA("s_memtime s[92:93]\n\t") \
  "v_mfma_f32_16x16x32_bf16 a[0:3], a[128:131], v[128:131], a[0:3]\n\t" \
  "s_add_u32 m0, %[m], 81920\n\t" \
  "v_exp_f32 v71, v71\n\t" \
  "v_mfma_f32_16x16x32_bf16 a[0:3], a[132:135], v[132:135], a[0:3]\n\t" \
  "s_xor_b32 s97, %[wob], 524288\n\t" \
  "v_exp_f32 v72, v72\n\t" \
  "v_mfma_f32_16x16x32_bf16 a[16:19], a[136:139], v[128:131], a[16:19]\n\t" \
  "ds_read_b64_tr_b16 a[192:193], v237 offset:0\n\t" \
  "ds_read_b64_tr_b16 a[194:195], v237 offset:4096\n\t" \
  "v_mfma_f32_16x16x32_bf16 a[16:19], a[140:143], v[132:135], a[16:19]\n\t" \
  "v_exp_f32 v73, v73\n\t" \
  "v_mfma_f32_16x16x32_bf16 a[32:35], a[144:147], v[128:131], a[32:35]\n\t" \
  "ds_read_b128 v[176:179], v215 offset:16384\n\t" \
  "ds_read_b64_tr_b16 a[196:197], v237 offset:8192\n\t" \
  "v_mfma_f32_16x16x32_bf16 a[32:35], a[148:151], v[132:135], a[32:35]\n\t" \
  "v_exp_f32 v74, v74\n\t" \
  "v_mfma_f32_16x16x32_bf16 a[48:51], a[152:155], v[128:131], a[48:51]\n\t" \
  "buffer_load_dwordx4 v240, %[wo], s97 offen offset:0 lds\n\t" \
  "v_mfma_f32_16x16x32_bf16 a[48:51], a[156:159], v[132:135], a[48:51]\n\t" \
  "s_add_u32 s97, s97, 7168\n\t" \
  "ds_read_b128 v[180:183], v215 offset:17408\n\t" \
  "ds_read_b64_tr_b16 a[198:199], v237 offset:12288\n\t" \
  "v_mfma_f32_16x16x32_bf16 a[64:67], a[160:163], v[128:131], a[64:67]\n\t" \
  "buffer_load_dwordx4 v241, %[wo], s97 offen offset:1024 lds\n\t" \
  "v_mfma_f32_16x16x32_bf16 a[64:67], a[164:167], v[132:135], a[64:67]\n\t" \
  "s_add_u32 s97, s97, 7168\n\t" \
  "ds_read_b64_tr_b16 a[200:201], v238 offset:0\n\t" \
  "ds_read_b128 v[184:187], v215 offset:18432\n\t" \
  "v_mfma_f32_16x16x32_bf16 a[80:83], a[168:171], v[128:131], a[80:83]\n\t" \
  "ds_read_b64_tr_b16 a[202:203], v238 offset:4096\n\t" \
  "ds_read_b128 v[188:191], v215 offset:19456\n\t" \
  "v_mfma_f32_16x16x32_bf16 a[80:83], a[172:175], v[132:135], a[80:83]\n\t" \
  "ds_read_b64_tr_b16 a[204:205], v238 offset:8192\n\t" \
  "ds_read_b128 v[192:195], v215 offset:32768\n\t" \
  "v_mfma_f32_16x16x32_bf16 a[96:99], a[176:179], v[128:131], a[96:99]\n\t" \
  STA("v_writelane_b32 %[stv], s94, 52\n\t") \
  STA("v_writelane_b32 %[stv], s92, 53\n\t") \
  "buffer_load_dwordx4 v240, %[wo], s97 offen offset:2048 lds\n\t" \
  "v_mfma_f32_16x16x32_bf16 a[96:99], a[180:183], v[132:135], a[96:99]\n\t" \
  "s_add_u32 s97, s97, 7168\n\t" \
  "ds_read_b64_tr_b16 a[206:207], v238 offset:12288\n\t" \
  "ds_read_b128 v[196:199], v215 offset:33792\n\t" \
  "v_mfma_f32_16x16x32_bf16 a[112:115], a[184:187], v[128:131], a[112:115]\n\t" \
  "ds_read_b64_tr_b16 a[208:209], v237 offset:256\n\t" \
  "ds_read_b128 v[200:203], v215 offset:34816\n\t" \
  "v_mfma_f32_16x16x32_bf16 a[112:115], a[188:191], v[132:135], a[112:115]\n\t" \
  "ds_read_b64_tr_b16 a[210:211], v237 offset:4352\n\t" \
  "ds_read_b64_tr_b16 a[212:213], v237 offset:8448\n\t" \
  "v_mfma_f32_16x16x32_bf16 v[160:163], v[216:219], v[128:131], v[160:163]\n\t" \
  "buffer_load_dwordx4 v241, %[wo], s97 offen offset:3072 lds\n\t" \
  "v_mfma_f32_16x16x32_bf16 v[160:163], v[216:219], v[132:135], v[160:163]\n\t" \
  "ds_read_b128 v[204:207], v215 offset:35840\n\t" \
  "v_cvt_pk_bf16_f32 v128, v0, v1\n\t" \
  "v_mfma_f32_16x16x32_bf16 a[4:7], a[128:131], v[136:139], a[4:7]\n\t" \
  "ds_read_b64_tr_b16 a[214:215], v237 offset:12544\n\t" \
  "v_cvt_pk_bf16_f32 v129, v2, v3\n\t" \
  "v_mfma_f32_16x16x32_bf16 a[4:7], a[132:135], v[140:143], a[4:7]\n\t" \
  "ds_read_b64_tr_b16 a[216:217], v238 offset:256\n\t" \
  "v_cvt_pk_bf16_f32 v130, v4, v5\n\t" \
  "v_mfma_f32_16x16x32_bf16 a[20:23], a[136:139], v[136:139], a[20:23]\n\t" \
  "s_add_u32 m0, %[m], 114688\n\t" \
  "ds_read_b64_tr_b16 a[218:219], v238 offset:4352\n\t" \
  "v_cvt_pk_bf16_f32 v131, v6, v7\n\t" \
  "v_mfma_f32_16x16x32_bf16 a[20:23], a[140:143], v[140:143], a[20:23]\n\t" \
  "s_xor_b32 s97, %[wob], 786432\n\t" \
  "ds_read_b64_tr_b16 a[220:221], v238 offset:8448\n\t" \
  "v_cvt_pk_bf16_f32 v132, v8, v9\n\t" \
  "v_mfma_f32_16x16x32_bf16 a[36:39], a[144:147], v[136:139], a[36:39]\n\t" \
  "s_waitcnt lgkmcnt(13)\n\t" \
  "ds_read_b64_tr_b16 a[222:223], v238 offset:12544\n\t" \
  "v_cvt_pk_bf16_f32 v133, v10, v11\n\t" \
  "v_mfma_f32_16x16x32_bf16 a[36:39], a[148:151], v[140:143], a[36:39]\n\t" \
  "ds_read_b64_tr_b16 a[224:225], v237 offset:512\n\t" \
  "v_cvt_pk_bf16_f32 v134, v12, v13\n\t" \
  "v_mfma_f32_16x16x32_bf16 a[52:55], a[152:155], v[136:139], a[52:55]\n\t" \
  "ds_read_b64_tr_b16 a[226:227], v237 offset:4608\n\t" \
  "v_cvt_pk_bf16_f32 v135, v14, v15\n\t" \
  "v_mfma_f32_16x16x32_bf16 a[52:55], a[156:159], v[140:143], a[52:55]\n\t" \
  "ds_read_b64_tr_b16 a[228:229], v237 offset:8704\n\t" \
  "ds_read_b64_tr_b16 a[230:231], v237 offset:12800\n\t" \
  "v_mfma_f32_16x16x32_bf16 a[68:71], a[160:163], v[136:139], a[68:71]\n\t" \
  "v_exp_f32 v75, v75\n\t" \
  "v_mfma_f32_16x16x32_bf16 a[68:71], a[164:167], v[140:143], a[68:71]\n\t" \
  "ds_read_b64_tr_b16 a[232:233], v238 offset:512\n\t" \
  "ds_read_b64_tr_b16 a[234:235], v238 offset:4608\n\t" \
  "v_mfma_f32_16x16x32_bf16 a[84:87], a[168:171], v[136:139], a[84:87]\n\t" \
  "buffer_load_dwordx4 v240, %[wo], s97 offen offset:0 lds\n\t" \
  "v_mfma_f32_16x16x32_bf16 a[84:87], a[172:175], v[140:143], a[84:87]\n\t" \
  "s_add_u32 s97, s97, 7168\n\t" \
  "v_exp_f32 v76, v76\n\t" \
  "v_mfma_f32_16x16x32_bf16 a[100:103], a[176:179], v[136:139], a[100:103]\n\t" \
  "ds_read_b64_tr_b16 a[236:237], v238 offset:8704\n\t" \
  "ds_read_b64_tr_b16 a[238:239], v238 offset:12800\n\t" \
  "v_mfma_f32_16x16x32_bf16 a[100:103], a[180:183], v[140:143], a[100:103]\n\t" \
  "buffer_load_dwordx4 v241, %[wo], s97 offen offset:1024 lds\n\t" \
  "v_mfma_f32_16x16x32_bf16 a[116:119], a[184:187], v[136:139], a[116:119]\n\t" \
  "s_add_u32 s97, s97, 7168\n\t" \
  "ds_read_b64_tr_b16 a[240:241], v237 offset:768\n\t" \
  "ds_read_b64_tr_b16 a[242:243], v237 offset:4864\n\t" \
  "v_mfma_f32_16x16x32_bf16 a[116:119], a[188:191], v[140:143], a[116:119]\n\t" \
  "v_exp_f32 v77, v77\n\t" \
  "v_mfma_f32_16x16x32_bf16 v[164:167], v[216:219], v[136:139], v[164:167]\n\t" \
  "buffer_load_dwordx4 v240, %[wo], s97 offen offset:2048 lds\n\t" \
  "v_mfma_f32_16x16x32_bf16 v[164:167], v[216:219], v[140:143], v[164:167]\n\t" \
  "s_add_u32 s97, s97, 7168\n\t" \
  "ds_read_b64_tr_b16 a[244:245], v237 offset:8960\n\t" \
  "v_cvt_pk_bf16_f32 v136, v32, v33\n\t" \
  "v_mfma_f32_16x16x32_bf16 a[0:3], a[192:195], v[128:131], a[0:3]\n\t" \
  "ds_read_b64_tr_b16 a[246:247], v237 offset:13056\n\t" \
  "v_cvt_pk_bf16_f32 v137, v34, v35\n\t" \
  "v_mfma_f32_16x16x32_bf16 a[0:3], a[196:199], v[132:135], a[0:3]\n\t" \
  "ds_read_b64_tr_b16 a[248:249], v238 offset:768\n\t" \
  "v_cvt_pk_bf16_f32 v138, v36, v37\n\t" \
  "v_mfma_f32_16x16x32_bf16 v[160:163], v[216:219], v[128:131], v[160:163]\n\t" \
  "buffer_load_dwordx4 v241, %[wo], s97 offen offset:3072 lds\n\t" \
  "v_mfma_f32_16x16x32_bf16 v[160:163], v[216:219], v[132:135], v[160:163]\n\t" \
  "s_add_u32 m0, %[m], 131072\n\t" \
  "ds_read_b64_tr_b16 a[250:251], v238 offset:4864\n\t" \
  "v_cvt_pk_bf16_f32 v139, v38, v39\n\t" \
  "v_mfma_f32_16x16x32_bf16 a[16:19], a[200:203], v[128:131], a[16:19]\n\t" \
  "s_waitcnt lgkmcnt(15)\n\t" \
  "ds_read_b64_tr_b16 a[252:253], v238 offset:8960\n\t" \
  "v_cvt_pk_bf16_f32 v140, v40, v41\n\t" \
  "v_mfma_f32_16x16x32_bf16 a[16:19], a[204:207], v[132:135], a[16:19]\n\t" \
  "s_xor_b32 s97, %[wob], 917504\n\t" \
  "ds_read_b64_tr_b16 a[254:255], v238 offset:13056\n\t" \
  "v_cvt_pk_bf16_f32 v141, v42, v43\n\t" \
  "v_xor_b32 v235, 64, v234\n\t" \
  "v_mfma_f32_16x16x32_bf16 a[32:35], a[208:211], v[128:131], a[32:35]\n\t" \
  "v_exp_f32 v78, v78\n\t" \
  "v_cvt_pk_bf16_f32 v142, v44, v45\n\t" \
  "v_mfma_f32_16x16x32_bf16 a[32:35], a[212:215], v[132:135], a[32:35]\n\t" \
  "ds_read_b64_tr_b16 v[48:49], v235 offset:0\n\t" \
  "v_cvt_pk_bf16_f32 v143, v46, v47\n\t" \
  "v_mfma_f32_16x16x32_bf16 a[48:51], a[216:219], v[128:131], a[48:51]\n\t" \
  "s_waitcnt lgkmcnt(15)\n\t" \
  "v_exp_f32 v79, v79\n\t" \
  "v_mfma_f32_16x16x32_bf16 a[48:51], a[220:223], v[132:135], a[48:51]\n\t" \
  "ds_read_b64_tr_b16 v[50:51], v235 offset:4096\n\t" \
  "ds_read_b64_tr_b16 v[52:53], v235 offset:8192\n\t" \
  "v_mfma_f32_16x16x32_bf16 a[64:67], a[224:227], v[128:131], a[64:67]\n\t" \
  "s_waitcnt lgkmcnt(15)\n\t" \
  "buffer_load_dwordx4 v240, %[wo], s97 offen offset:0 lds\n\t" \
  "v_mfma_f32_16x16x32_bf16 a[64:67], a[228:231], v[132:135], a[64:67]\n\t" \
  "s_waitcnt lgkmcnt(13)\n\t" \
  "v_rcp_f32 v208, v160\n\t" \
  "v_mfma_f32_16x16x32_bf16 a[80:83], a[232:235], v[128:131], a[80:83]\n\t" \
  "s_waitcnt lgkmcnt(11)\n\t" \
  "v_cmp_lt_f32 vcc, 0x5f800000, v160\n\t" \
  "s_cbranch_vccnz .Lat_x0\n\t" \
  ".Lat_y0:\n\t" \
  "v_accvgpr_read_b32 v16, a0\n\t" \
  "v_mfma_f32_16x16x32_bf16 a[80:83], a[236:239], v[132:135], a[80:83]\n\t" \
  "s_waitcnt lgkmcnt(9)\n\t" \
  "ds_read_b64_tr_b16 v[54:55], v235 offset:12288\n\t" \
  "v_accvgpr_read_b32 v17, a1\n\t" \
  "v_accvgpr_read_b32 v18, a2\n\t" \
  "v_mfma_f32_16x16x32_bf16 a[96:99], a[240:243], v[128:131], a[96:99]\n\t" \
  "s_waitcnt lgkmcnt(8)\n\t" \
  "v_accvgpr_read_b32 v19, a3\n\t" \
  "v_accvgpr_read_b32 v20, a16\n\t" \
  "v_accvgpr_read_b32 v21, a17\n\t" \
  "v_accvgpr_read_b32 v22, a18\n\t" \
  "v_accvgpr_read_b32 v23, a19\n\t" \
  "v_pk_mul_f32 v[16:17], v[16:17], v[208:209] op_sel_hi:[1,0]\n\t" \
  "v_pk_mul_f32 v[18:19], v[18:19], v[208:209] op_sel_hi:[1,0]\n\t" \
  "v_pk_mul_f32 v[20:21], v[20:21], v[208:209] op_sel_hi:[1,0]\n\t" \
  "v_pk_mul_f32 v[22:23], v[22:23], v[208:209] op_sel_hi:[1,0]\n\t" \
  "v_cvt_pk_bf16_f32 v0, v16, v17\n\t" \
  "v_mfma_f32_16x16x32_bf16 a[96:99], a[244:247], v[132:135], a[96:99]\n\t" \
  "s_waitcnt lgkmcnt(6)\n\t" \
  "ds_read_b64_tr_b16 v[56:57], v235 offset:16384\n\t" \
  "v_cvt_pk_bf16_f32 v1, v18, v19\n\t" \
  "v_cvt_pk_bf16_f32 v2, v20, v21\n\t" \
  "v_cvt_pk_bf16_f32 v3, v22, v23\n\t" \
  "v_mfma_f32_16x16x32_bf16 a[112:115], a[248:251], v[128:131], a[112:115]\n\t" \
  "s_waitcnt lgkmcnt(5)\n\t" \
  "ds_read_b64_tr_b16 v[58:59], v235 offset:20480\n\t" \
  "v_max3_f32 v209, |v0|, |v1|, |v2|\n\t" \
  "v_mfma_f32_16x16x32_bf16 a[112:115], a[252:255], v[132:135], a[112:115]\n\t" \
  "s_waitcnt vmcnt(9)\n\t" \
  "s_barrier\n\t" \
  "v_mfma_f32_16x16x32_bf16 a[8:11], a[128:131], v[144:147], a[8:11]\n\t" \
  "s_add_u32 s97, s97, 7168\n\t" \
  "buffer_load_dwordx4 v241, %[wo], s97 offen offset:1024 lds\n\t" \
  "v_mfma_f32_16x16x32_bf16 a[8:11], a[132:135], v[148:151], a[8:11]\n\t" \
  "s_add_u32 s97, s97, 7168\n\t" \
  "v_cmp_lt_f32 vcc, 0x33800000, v209\n\t" \
  "s_cbranch_vccz .Lat_x4\n\t" \
  ".Lat_y4:\n\t" \
  "v_accvgpr_read_b32 v24, a32\n\t" \
  "v_mfma_f32_16x16x32_bf16 a[24:27], a[136:139], v[144:147], a[24:27]\n\t" \
  "buffer_load_dwordx4 v240, %[wo], s97 offen offset:2048 lds\n\t" \
  "v_accvgpr_read_b32 v25, a33\n\t" \
  "v_accvgpr_read_b32 v26, a34\n\t" \
  "v_mfma_f32_16x16x32_bf16 a[24:27], a[140:143], v[148:151], a[24:27]\n\t" \
  "s_add_u32 s97, s97, 7168\n\t" \
  "v_accvgpr_read_b32 v27, a35\n\t" \
  "v_accvgpr_read_b32 v28, a48\n\t" \
  "v_accvgpr_read_b32 v29, a49\n\t" \
  "v_accvgpr_read_b32 v30, a50\n\t" \
  "v_accvgpr_read_b32 v31, a51\n\t" \
  "v_pk_mul_f32 v[24:25], v[24:25], v[208:209] op_sel_hi:[1,0]\n\t" \
  "v_pk_mul_f32 v[26:27], v[26:27], v[208:209] op_sel_hi:[1,0]\n\t" \
  "v_pk_mul_f32 v[28:29], v[28:29], v[208:209] op_sel_hi:[1,0]\n\t" \
  "v_pk_mul_f32 v[30:31], v[30:31], v[208:209] op_sel_hi:[1,0]\n\t" \
  "v_cvt_pk_bf16_f32 v4, v24, v25\n\t" \
  "v_mfma_f32_16x16x32_bf16 a[40:43], a[144:147], v[144:147], a[40:43]\n\t" \
  "s_xor_b32 s98, %[eso], 0\n\t" \
  "ds_read_b64_tr_b16 v[60:61], v235 offset:24576\n\t" \
  "v_cvt_pk_bf16_f32 v5, v26, v27\n\t" \
  "v_cvt_pk_bf16_f32 v6, v28, v29\n\t" \
  "v_cvt_pk_bf16_f32 v7, v30, v31\n\t" \
  "v_mfma_f32_16x16x32_bf16 a[40:43], a[148:151], v[148:151], a[40:43]\n\t" \
  "buffer_load_dwordx4 v241, %[wo], s97 offen offset:3072 lds\n\t" \
  "v_accvgpr_read_b32 v16, a64\n\t" \
  "v_accvgpr_read_b32 v17, a65\n\t" \
  "v_accvgpr_read_b32 v18, a66\n\t" \
  "v_mfma_f32_16x16x32_bf16 a[56:59], a[152:155], v[144:147], a[56:59]\n\t" \
  "v_exp_f32 v96, v96\n\t" \
  "v_mfma_f32_16x16x32_bf16 a[56:59], a[156:159], v[148:151], a[56:59]\n\t" \
  "buffer_store_dwordx4 v[0:3], v242, %[ws], s98 offen offset:0 sc1\n\t" \
  "v_mfma_f32_16x16x32_bf16 a[72:75], a[160:163], v[144:147], a[72:75]\n\t" \
  "v_accvgpr_read_b32 v19, a67\n\t" \
  "v_accvgpr_read_b32 v20, a80\n\t" \
  "v_accvgpr_read_b32 v21, a81\n\t" \
  "v_accvgpr_read_b32 v22, a82\n\t" \
  "v_accvgpr_read_b32 v23, a83\n\t" \
  "v_pk_mul_f32 v[16:17], v[16:17], v[208:209] op_sel_hi:[1,0]\n\t" \
  "v_pk_mul_f32 v[18:19], v[18:19], v[208:209] op_sel_hi:[1,0]\n\t" \
  "v_pk_mul_f32 v[20:21], v[20:21], v[208:209] op_sel_hi:[1,0]\n\t" \
  "v_pk_mul_f32 v[22:23], v[22:23], v[208:209] op_sel_hi:[1,0]\n\t" \
  "v_cvt_pk_bf16_f32 v8, v16, v17\n\t" \
  "v_mfma_f32_16x16x32_bf16 a[72:75], a[164:167], v[148:151], a[72:75]\n\t" \
  "s_xor_b32 s98, %[eso], 0\n\t" \
  "ds_read_b64_tr_b16 v[62:63], v235 offset:28672\n\t" \
  "v_xor_b32 v236, 96, v234\n\t" \
  "v_mfma_f32_16x16x32_bf16 a[88:91], a[168:171], v[144:147], a[88:91]\n\t" \
  "buffer_store_dwordx4 v[4:7], v242, %[ws], s98 offen offset:1024 sc1\n\t" \
  "v_mfma_f32_16x16x32_bf16 a[88:91], a[172:175], v[148:151], a[88:91]\n\t" \
  "ds_read_b64_tr_b16 v[80:81], v236 offset:0\n\t" \
  "v_cvt_pk_bf16_f32 v9, v18, v19\n\t" \
  "v_cvt_pk_bf16_f32 v10, v20, v21\n\t" \
  "v_cvt_pk_bf16_f32 v11, v22, v23\n\t" \
  "v_mfma_f32_16x16x32_bf16 a[104:107], a[176:179], v[144:147], a[104:107]\n\t" \
  "ds_read_b64_tr_b16 v[82:83], v236 offset:4096\n\t" \
  "v_accvgpr_read_b32 v24, a96\n\t" \
  "v_mfma_f32_16x16x32_bf16 a[104:107], a[180:183], v[148:151], a[104:107]\n\t" \
  "ds_read_b64_tr_b16 v[84:85], v236 offset:8192\n\t" \
  "v_accvgpr_read_b32 v25, a97\n\t" \
  "v_accvgpr_read_b32 v26, a98\n\t" \
  "v_mfma_f32_16x16x32_bf16 a[120:123], a[184:187], v[144:147], a[120:123]\n\t" \
  "v_exp_f32 v97, v97\n\t" \
  "v_mfma_f32_16x16x32_bf16 a[120:123], a[188:191], v[148:151], a[120:123]\n\t" \
  "v_accvgpr_read_b32 v27, a99\n\t" \
  "v_accvgpr_read_b32 v28, a112\n\t" \
  "v_accvgpr_read_b32 v29, a113\n\t" \
  "v_accvgpr_read_b32 v30, a114\n\t" \
  "v_accvgpr_read_b32 v31, a115\n\t" \
  "v_pk_mul_f32 v[24:25], v[24:25], v[208:209] op_sel_hi:[1,0]\n\t" \
  "v_pk_mul_f32 v[26:27], v[26:27], v[208:209] op_sel_hi:[1,0]\n\t" \
  "v_pk_mul_f32 v[28:29], v[28:29], v[208:209] op_sel_hi:[1,0]\n\t" \
  "v_pk_mul_f32 v[30:31], v[30:31], v[208:209] op_sel_hi:[1,0]\n\t" \
  "v_cvt_pk_bf16_f32 v12, v24, v25\n\t" \
  "v_mfma_f32_16x16x32_bf16 v[168:171], v[216:219], v[144:147], v[168:171]\n\t" \
  "v_exp_f32 v98, v98\n\t" \
  "v_mfma_f32_16x16x32_bf16 v[168:171], v[216:219], v[148:151], v[168:171]\n\t" \
  "s_xor_b32 s98, %[eso], 0\n\t" \
  "v_exp_f32 v99, v99\n\t" \
  STS("s_memrealtime s[94:95]\n\t") \
  STS("s_memtime s[92:93]\n\t") \
  "v_mfma_f32_16x16x32_bf16 a[4:7], a[192:195], v[136:139], a[4:7]\n\t" \
  "buffer_store_dwordx4 v[8:11], v242, %[ws], s98 offen offset:2048 sc1\n\t" \
  "v_cvt_pk_bf16_f32 v144, v64, v65\n\t" \
  "v_mfma_f32_16x16x32_bf16 a[4:7], a[196:199], v[140:143], a[4:7]\n\t" \
  "ds_read_b64_tr_b16 v[86:87], v236 offset:12288\n\t" \
  "v_cvt_pk_bf16_f32 v13, v26, v27\n\t" \
  "v_cvt_pk_bf16_f32 v14, v28, v29\n\t" \
  "v_cvt_pk_bf16_f32 v15, v30, v31\n\t" \
  "v_cvt_pk_bf16_f32 v145, v66, v67\n\t" \
  "v_mfma_f32_16x16x32_bf16 v[164:167], v[216:219], v[136:139], v[164:167]\n\t" \
  "v_exp_f32 v100, v100\n\t" \
  "v_cvt_pk_bf16_f32 v146, v68, v69\n\t" \
  "v_mfma_f32_16x16x32_bf16 v[164:167], v[216:219], v[140:143], v[164:167]\n\t" \
  "ds_read_b64_tr_b16 v[88:89], v236 offset:16384\n\t" \
  "v_cvt_pk_bf16_f32 v147, v70, v71\n\t" \
  "v_mfma_f32_16x16x32_bf16 a[20:23], a[200:203], v[136:139], a[20:23]\n\t" \
  "v_exp_f32 v101, v101\n\t" \
  "v_cvt_pk_bf16_f32 v148, v72, v73\n\t" \
  "v_mfma_f32_16x16x32_bf16 a[20:23], a[204:207], v[140:143], a[20:23]\n\t" \
  "v_exp_f32 v102, v102\n\t" \
  "v_cvt_pk_bf16_f32 v149, v74, v75\n\t" \
  "v_mfma_f32_16x16x32_bf16 a[36:39], a[208:211], v[136:139], a[36:39]\n\t" \
  "ds_read_b64_tr_b16 v[90:91], v236 offset:20480\n\t" \
  "v_cvt_pk_bf16_f32 v150, v76, v77\n\t" \
  "v_mfma_f32_16x16x32_bf16 a[36:39], a[212:215], v[140:143], a[36:39]\n\t" \
  "s_xor_b32 s98, %[eso], 0\n\t" \
  "v_exp_f32 v103, v103\n\t" \
  "v_cvt_pk_bf16_f32 v151, v78, v79\n\t" \
  "v_mfma_f32_16x16x32_bf16 a[52:55], a[216:219], v[136:139], a[52:55]\n\t" \
  "buffer_store_dwordx4 v[12:15], v242, %[ws], s98 offen offset:3072 sc1\n\t" \
  "v_mfma_f32_16x16x32_bf16 a[52:55], a[220:223], v[140:143], a[52:55]\n\t" \
  "ds_read_b64_tr_b16 v[92:93], v236 offset:24576\n\t" \
  "ds_read_b64_tr_b16 v[94:95], v236 offset:28672\n\t" \
  "v_mfma_f32_16x16x32_bf16 a[68:71], a[224:227], v[136:139], a[68:71]\n\t" \
  "v_rcp_f32 v210, v164\n\t" \
  "v_mfma_f32_16x16x32_bf16 a[68:71], a[228:231], v[140:143], a[68:71]\n\t" \
  "v_cmp_lt_f32 vcc, 0x5f800000, v164\n\t" \
  "s_cbranch_vccnz .Lat_x1\n\t" \
  ".Lat_y1:\n\t" \
  "v_accvgpr_read_b32 v16, a4\n\t" \
  "v_mfma_f32_16x16x32_bf16 a[84:87], a[232:235], v[136:139], a[84:87]\n\t" \
  STS("v_writelane_b32 %[stv], s94, 20\n\t") \
  STS("v_writelane_b32 %[stv], s92, 21\n\t") \
  "v_accvgpr_read_b32 v17, a5\n\t" \
  "v_accvgpr_read_b32 v18, a6\n\t" \
  "v_mfma_f32_16x16x32_bf16 a[84:87], a[236:239], v[140:143], a[84:87]\n\t" \
  "v_accvgpr_read_b32 v19, a7\n\t" \
  "v_accvgpr_read_b32 v20, a20\n\t" \
  "v_accvgpr_read_b32 v21, a21\n\t" \
  "v_accvgpr_read_b32 v22, a22\n\t" \
  "v_accvgpr_read_b32 v23, a23\n\t" \
  "v_pk_mul_f32 v[16:17], v[16:17], v[210:211] op_sel_hi:[1,0]\n\t" \
  "v_pk_mul_f32 v[18:19], v[18:19], v[210:211] op_sel_hi:[1,0]\n\t" \
  "v_pk_mul_f32 v[20:21], v[20:21], v[210:211] op_sel_hi:[1,0]\n\t" \
  "v_pk_mul_f32 v[22:23], v[22:23], v[210:211] op_sel_hi:[1,0]\n\t" \
  "v_cvt_pk_bf16_f32 v32, v16, v17\n\t" \
  "v_mfma_f32_16x16x32_bf16 a[100:103], a[240:243], v[136:139], a[100:103]\n\t" \
  "v_xor_b32 v235, 128, v234\n\t" \
  "v_cvt_pk_bf16_f32 v33, v18, v19\n\t" \
  "v_cvt_pk_bf16_f32 v34, v20, v21\n\t" \
  "v_cvt_pk_bf16_f32 v35, v22, v23\n\t" \
  "v_mfma_f32_16x16x32_bf16 a[100:103], a[244:247], v[140:143], a[100:103]\n\t" \
  "s_xor_b32 s98, %[eso], 32768\n\t" \
  "ds_read_b64_tr_b16 v[112:113], v235 offset:0\n\t" \
  "v_accvgpr_read_b32 v24, a36\n\t" \
  "v_mfma_f32_16x16x32_bf16 a[116:119], a[248:251], v[136:139], a[116:119]\n\t" \
  "buffer_store_dwordx4 v[32:35], v242, %[ws], s98 offen offset:0 sc1\n\t" \
  "v_accvgpr_read_b32 v25, a37\n\t" \
  "v_accvgpr_read_b32 v26, a38\n\t" \
  "v_mfma_f32_16x16x32_bf16 a[116:119], a[252:255], v[140:143], a[116:119]\n\t" \
  "v_exp_f32 v104, v104\n\t" \
  "v_mfma_f32_16x16x32_bf16 a[12:15], a[128:131], v[152:155], a[12:15]\n\t" \
  "v_accvgpr_read_b32 v27, a39\n\t" \
  "v_accvgpr_read_b32 v28, a52\n\t" \
  "v_accvgpr_read_b32 v29, a53\n\t" \
  "v_accvgpr_read_b32 v30, a54\n\t" \
  "v_accvgpr_read_b32 v31, a55\n\t" \
  "v_pk_mul_f32 v[24:25], v[24:25], v[210:211] op_sel_hi:[1,0]\n\t" \
  "v_pk_mul_f32 v[26:27], v[26:27], v[210:211] op_sel_hi:[1,0]\n\t" \
  "v_pk_mul_f32 v[28:29], v[28:29], v[210:211] op_sel_hi:[1,0]\n\t" \
  "v_pk_mul_f32 v[30:31], v[30:31], v[210:211] op_sel_hi:[1,0]\n\t" \
  "v_cvt_pk_bf16_f32 v36, v24, v25\n\t" \
  "v_mfma_f32_16x16x32_bf16 a[12:15], a[132:135], v[156:159], a[12:15]\n\t" \
  "ds_read_b64_tr_b16 v[114:115], v235 offset:4096\n\t" \
  "v_cvt_pk_bf16_f32 v37, v26, v27\n\t" \
  "v_mfma_f32_16x16x32_bf16 a[28:31], a[136:139], v[152:155], a[28:31]\n\t" \
  "v_exp_f32 v105, v105\n\t" \
  "v_mfma_f32_16x16x32_bf16 a[28:31], a[140:143], v[156:159], a[28:31]\n\t" \
  "ds_read_b64_tr_b16 v[116:117], v235 offset:8192\n\t" \
  "v_cvt_pk_bf16_f32 v38, v28, v29\n\t" \
  "v_mfma_f32_16x16x32_bf16 a[44:47], a[144:147], v[152:155], a[44:47]\n\t" \
  "ds_read_b64_tr_b16 v[118:119], v235 offset:12288\n\t" \
  "v_cvt_pk_bf16_f32 v39, v30, v31\n\t" \
  "v_mfma_f32_16x16x32_bf16 a[44:47], a[148:151], v[156:159], a[44:47]\n\t" \
  "s_xor_b32 s98, %[eso], 32768\n\t" \
  "ds_read_b64_tr_b16 v[120:121], v235 offset:16384\n\t" \
  "v_accvgpr_read_b32 v16, a68\n\t" \
  "v_accvgpr_read_b32 v17, a69\n\t" \
  "v_accvgpr_read_b32 v18, a70\n\t" \
  "v_mfma_f32_16x16x32_bf16 a[60:63], a[152:155], v[152:155], a[60:63]\n\t" \
  "buffer_store_dwordx4 v[36:39], v242, %[ws], s98 offen offset:1024 sc1\n\t" \
  "v_mfma_f32_16x16x32_bf16 a[60:63], a[156:159], v[156:159], a[60:63]\n\t" \
  "v_accvgpr_read_b32 v19, a71\n\t" \
  "v_accvgpr_read_b32 v20, a84\n\t" \
  "v_accvgpr_read_b32 v21, a85\n\t" \
  "v_accvgpr_read_b32 v22, a86\n\t" \
  "v_accvgpr_read_b32 v23, a87\n\t" \
  "v_pk_mul_f32 v[16:17], v[16:17], v[210:211] op_sel_hi:[1,0]\n\t" \
  "v_pk_mul_f32 v[18:19], v[18:19], v[210:211] op_sel_hi:[1,0]\n\t" \
  "v_pk_mul_f32 v[20:21], v[20:21], v[210:211] op_sel_hi:[1,0]\n\t" \
  "v_pk_mul_f32 v[22:23], v[22:23], v[210:211] op_sel_hi:[1,0]\n\t" \
  "v_cvt_pk_bf16_f32 v40, v16, v17\n\t" \
  "v_mfma_f32_16x16x32_bf16 a[76:79], a[160:163], v[152:155], a[76:79]\n\t" \
  "v_exp_f32 v106, v106\n\t" \
  "v_mfma_f32_16x16x32_bf16 a[76:79], a[164:167], v[156:159], a[76:79]\n\t" \
  "v_exp_f32 v107, v107\n\t" \
  "v_mfma_f32_16x16x32_bf16 a[92:95], a[168:171], v[152:155], a[92:95]\n\t" \
  "ds_read_b64_tr_b16 v[122:123], v235 offset:20480\n\t" \
  "v_cvt_pk_bf16_f32 v41, v18, v19\n\t" \
  "v_mfma_f32_16x16x32_bf16 a[92:95], a[172:175], v[156:159], a[92:95]\n\t" \
  "ds_read_b64_tr_b16 v[124:125], v235 offset:24576\n\t" \
  "v_cvt_pk_bf16_f32 v42, v20, v21\n\t" \
  "v_cvt_pk_bf16_f32 v43, v22, v23\n\t" \
  "v_mfma_f32_16x16x32_bf16 a[108:111], a[176:179], v[152:155], a[108:111]\n\t" \
  "ds_read_b64_tr_b16 v[126:127], v235 offset:28672\n\t" \
  "v_accvgpr_read_b32 v24, a100\n\t" \
  "v_mfma_f32_16x16x32_bf16 a[108:111], a[180:183], v[156:159], a[108:111]\n\t" \
  "s_xor_b32 s98, %[eso], 32768\n\t" \
  "v_accvgpr_read_b32 v25, a101\n\t" \
  "v_accvgpr_read_b32 v26, a102\n\t" \
  "v_mfma_f32_16x16x32_bf16 a[124:127], a[184:187], v[152:155], a[124:127]\n\t" \
  "buffer_store_dwordx4 v[40:43], v242, %[ws], s98 offen offset:2048 sc1\n\t" \
  "v_mfma_f32_16x16x32_bf16 a[124:127], a[188:191], v[156:159], a[124:127]\n\t" \
  "v_accvgpr_read_b32 v27, a103\n\t" \
  "v_accvgpr_read_b32 v28, a116\n\t" \
  "v_accvgpr_read_b32 v29, a117\n\t" \
  "v_accvgpr_read_b32 v30, a118\n\t" \
  "v_accvgpr_read_b32 v31, a119\n\t" \
  "v_pk_mul_f32 v[24:25], v[24:25], v[210:211] op_sel_hi:[1,0]\n\t" \
  "v_pk_mul_f32 v[26:27], v[26:27], v[210:211] op_sel_hi:[1,0]\n\t" \
  "v_pk_mul_f32 v[28:29], v[28:29], v[210:211] op_sel_hi:[1,0]\n\t" \
  "v_pk_mul_f32 v[30:31], v[30:31], v[210:211] op_sel_hi:[1,0]\n\t" \
  "v_cvt_pk_bf16_f32 v44, v24, v25\n\t" \
  "v_mfma_f32_16x16x32_bf16 v[172:175], v[216:219], v[152:155], v[172:175]\n\t" \
  "v_exp_f32 v108, v108\n\t" \
  "v_mfma_f32_16x16x32_bf16 v[172:175], v[216:219], v[156:159], v[172:175]\n\t" \
  "ds_read_b64_tr_b16 a[128:129], v234 offset:0\n\t" \
  "v_cvt_pk_bf16_f32 v152, v96, v97\n\t" \
  "v_mfma_f32_16x16x32_bf16 a[8:11], a[192:195], v[144:147], a[8:11]\n\t" \
  "ds_read_b64_tr_b16 a[130:131], v234 offset:4096\n\t" \
  "v_cvt_pk_bf16_f32 v153, v98, v99\n\t" \
  "v_mfma_f32_16x16x32_bf16 a[8:11], a[196:199], v[148:151], a[8:11]\n\t" \
  "ds_read_b64_tr_b16 a[132:133], v234 offset:8192\n\t" \
  "v_cvt_pk_bf16_f32 v45, v26, v27\n\t" \
  "v_cvt_pk_bf16_f32 v46, v28, v29\n\t" \
  "v_cvt_pk_bf16_f32 v47, v30, v31\n\t" \
  "v_cvt_pk_bf16_f32 v154, v100, v101\n\t" \
  "v_mfma_f32_16x16x32_bf16 v[168:171], v[216:219], v[144:147], v[168:171]\n\t" \
  "v_exp_f32 v109, v109\n\t" \
  "v_cvt_pk_bf16_f32 v155, v102, v103\n\t" \
  "v_mfma_f32_16x16x32_bf16 v[168:171], v[216:219], v[148:151], v[168:171]\n\t" \
  "s_xor_b32 s98, %[eso], 32768\n\t" \
  "ds_read_b64_tr_b16 a[134:135], v234 offset:12288\n\t" \
  "v_cvt_pk_bf16_f32 v156, v104, v105\n\t" \
  "v_mfma_f32_16x16x32_bf16 a[24:27], a[200:203], v[144:147], a[24:27]\n\t" \
  "buffer_store_dwordx4 v[44:47], v242, %[ws], s98 offen offset:3072 sc1\n\t" \
  "v_cvt_pk_bf16_f32 v157, v106, v107\n\t" \
  "v_mfma_f32_16x16x32_bf16 a[24:27], a[204:207], v[148:151], a[24:27]\n\t" \
  "ds_read_b64_tr_b16 a[136:137], v234 offset:16384\n\t" \
  "v_cvt_pk_bf16_f32 v158, v108, v109\n\t" \
  "v_mfma_f32_16x16x32_bf16 a[40:43], a[208:211], v[144:147], a[40:43]\n\t" \
  "v_exp_f32 v110, v110\n\t" \
  "v_mfma_f32_16x16x32_bf16 a[40:43], a[212:215], v[148:151], a[40:43]\n\t" \
  "ds_read_b64_tr_b16 a[138:139], v234 offset:20480\n\t" \
  "ds_read_b64_tr_b16 a[140:141], v234 offset:24576\n\t" \
  "v_mfma_f32_16x16x32_bf16 a[56:59], a[216:219], v[144:147], a[56:59]\n\t" \
  "v_exp_f32 v111, v111\n\t" \
  "v_mfma_f32_16x16x32_bf16 a[56:59], a[220:223], v[148:151], a[56:59]\n\t" \
  "ds_read_b64_tr_b16 a[142:143], v234 offset:28672\n\t" \
  "v_xor_b32 v236, 32, v234\n\t" \
  "v_mfma_f32_16x16x32_bf16 a[72:75], a[224:227], v[144:147], a[72:75]\n\t" \
  "ds_read_b64_tr_b16 a[144:145], v236 offset:0\n\t" \
  "v_cvt_pk_bf16_f32 v159, v110, v111\n\t" \
  "v_mfma_f32_16x16x32_bf16 a[72:75], a[228:231], v[148:151], a[72:75]\n\t" \
  "ds_read_b64_tr_b16 a[146:147], v236 offset:4096\n\t" \
  "ds_read_b64_tr_b16 a[148:149], v236 offset:8192\n\t" \
  "v_mfma_f32_16x16x32_bf16 a[88:91], a[232:235], v[144:147], a[88:91]\n\t" \
  "v_rcp_f32 v212, v168\n\t" \
  "v_mfma_f32_16x16x32_bf16 a[88:91], a[236:239], v[148:151], a[88:91]\n\t" \
  "ds_read_b64_tr_b16 a[150:151], v236 offset:12288\n\t" \
  "ds_read_b64_tr_b16 a[152:153], v236 offset:16384\n\t" \
  "v_mfma_f32_16x16x32_bf16 a[104:107], a[240:243], v[144:147], a[104:107]\n\t" \
  "v_cmp_lt_f32 vcc, 0x5f800000, v168\n\t" \
  "s_cbranch_vccnz .Lat_x2\n\t" \
  ".Lat_y2:\n\t" \
  "v_accvgpr_read_b32 v16, a8\n\t" \
  "v_mfma_f32_16x16x32_bf16 a[104:107], a[244:247], v[148:151], a[104:107]\n\t" \
  "ds_read_b64_tr_b16 a[154:155], v236 offset:20480\n\t" \
  "v_accvgpr_read_b32 v17, a9\n\t" \
  "v_accvgpr_read_b32 v18, a10\n\t" \
  "v_mfma_f32_16x16x32_bf16 a[120:123], a[248:251], v[144:147], a[120:123]\n\t" \
  "v_accvgpr_read_b32 v19, a11\n\t" \
  "v_accvgpr_read_b32 v20, a24\n\t" \
  "v_accvgpr_read_b32 v21, a25\n\t" \
  "v_accvgpr_read_b32 v22, a26\n\t" \
  "v_accvgpr_read_b32 v23, a27\n\t" \
  "v_pk_mul_f32 v[16:17], v[16:17], v[212:213] op_sel_hi:[1,0]\n\t" \
  "v_pk_mul_f32 v[18:19], v[18:19], v[212:213] op_sel_hi:[1,0]\n\t" \
  "v_pk_mul_f32 v[20:21], v[20:21], v[212:213] op_sel_hi:[1,0]\n\t" \
  "v_pk_mul_f32 v[22:23], v[22:23], v[212:213] op_sel_hi:[1,0]\n\t" \
  "v_cvt_pk_bf16_f32 v64, v16, v17\n\t" \
  "v_mfma_f32_16x16x32_bf16 a[120:123], a[252:255], v[148:151], a[120:123]\n\t" \
  "ds_read_b64_tr_b16 a[156:157], v236 offset:24576\n\t" \
  "v_cvt_pk_bf16_f32 v65, v18, v19\n\t" \
  "v_cvt_pk_bf16_f32 v66, v20, v21\n\t" \
  "v_cvt_pk_bf16_f32 v67, v22, v23\n\t" \
  "v_accvgpr_read_b32 v24, a40\n\t" \
  "v_mfma_f32_16x16x32_bf16 a[12:15], a[192:195], v[152:155], a[12:15]\n\t" \
  "s_xor_b32 s98, %[eso], 65536\n\t" \
  "ds_read_b64_tr_b16 a[158:159], v236 offset:28672\n\t" \
  "v_accvgpr_read_b32 v25, a41\n\t" \
  "v_accvgpr_read_b32 v26, a42\n\t" \
  "v_mfma_f32_16x16x32_bf16 a[12:15], a[196:199], v[156:159], a[12:15]\n\t" \
  "buffer_store_dwordx4 v[64:67], v242, %[ws], s98 offen offset:0 sc1\n\t" \
  "v_mfma_f32_16x16x32_bf16 v[172:175], v[216:219], v[152:155], v[172:175]\n\t" \
  "v_accvgpr_read_b32 v27, a43\n\t" \
  "v_accvgpr_read_b32 v28, a56\n\t" \
  "v_accvgpr_read_b32 v29, a57\n\t" \
  "v_accvgpr_read_b32 v30, a58\n\t" \
  "v_accvgpr_read_b32 v31, a59\n\t" \
  "v_pk_mul_f32 v[24:25], v[24:25], v[212:213] op_sel_hi:[1,0]\n\t" \
  "v_pk_mul_f32 v[26:27], v[26:27], v[212:213] op_sel_hi:[1,0]\n\t" \
  "v_pk_mul_f32 v[28:29], v[28:29], v[212:213] op_sel_hi:[1,0]\n\t" \
  "v_pk_mul_f32 v[30:31], v[30:31], v[212:213] op_sel_hi:[1,0]\n\t" \
  "v_cvt_pk_bf16_f32 v68, v24, v25\n\t" \
  "v_mfma_f32_16x16x32_bf16 v[172:175], v[216:219], v[156:159], v[172:175]\n\t" \
  "v_xor_b32 v235, 192, v234\n\t" \
  "v_cvt_pk_bf16_f32 v69, v26, v27\n\t" \
  "v_mfma_f32_16x16x32_bf16 a[28:31], a[200:203], v[152:155], a[28:31]\n\t" \
  "ds_read_b64_tr_b16 a[160:161], v235 offset:0\n\t" \
  "v_cvt_pk_bf16_f32 v70, v28, v29\n\t" \
  "v_cvt_pk_bf16_f32 v71, v30, v31\n\t" \
  ".Lhs10:\n\ts_cbranch_execz .Lhs10n\n\t" \
  "v_mfma_f32_16x16x32_bf16 a[28:31], a[204:207], v[156:159], a[28:31]\n\t" \
  "s_xor_b32 s98, %[eso], 65536\n\t" \
  "ds_read_b64_tr_b16 a[162:163], v235 offset:4096\n\t" \
  "v_accvgpr_read_b32 v16, a72\n\t" \
  "v_accvgpr_read_b32 v17, a73\n\t" \
  "v_accvgpr_read_b32 v18, a74\n\t" \
  "v_mfma_f32_16x16x32_bf16 a[44:47], a[208:211], v[152:155], a[44:47]\n\t" \
  "buffer_store_dwordx4 v[68:71], v242, %[ws], s98 offen offset:1024 sc1\n\t" \
  "v_mfma_f32_16x16x32_bf16 a[44:47], a[212:215], v[156:159], a[44:47]\n\t" \
  "v_accvgpr_read_b32 v19, a75\n\t" \
  "v_accvgpr_read_b32 v20, a88\n\t" \
  "v_accvgpr_read_b32 v21, a89\n\t" \
  "v_accvgpr_read_b32 v22, a90\n\t" \
  "v_accvgpr_read_b32 v23, a91\n\t" \
  "v_pk_mul_f32 v[16:17], v[16:17], v[212:213] op_sel_hi:[1,0]\n\t" \
  "v_pk_mul_f32 v[18:19], v[18:19], v[212:213] op_sel_hi:[1,0]\n\t" \
  "v_pk_mul_f32 v[20:21], v[20:21], v[212:213] op_sel_hi:[1,0]\n\t" \
  "v_pk_mul_f32 v[22:23], v[22:23], v[212:213] op_sel_hi:[1,0]\n\t" \
  "v_cvt_pk_bf16_f32 v72, v16, v17\n\t" \
  "v_mfma_f32_16x16x32_bf16 a[60:63], a[216:219], v[152:155], a[60:63]\n\t" \
  "ds_read_b64_tr_b16 a[164:165], v235 offset:8192\n\t" \
  "v_cvt_pk_bf16_f32 v73, v18, v19\n\t" \
  "v_mfma_f32_16x16x32_bf16 a[60:63], a[220:223], v[156:159], a[60:63]\n\t" \
  "ds_read_b64_tr_b16 a[166:167], v235 offset:12288\n\t" \
  "v_cvt_pk_bf16_f32 v74, v20, v21\n\t" \
  "v_mfma_f32_16x16x32_bf16 a[76:79], a[224:227], v[152:155], a[76:79]\n\t" \
  "ds_read_b64_tr_b16 a[168:169], v235 offset:16384\n\t" \
  "v_cvt_pk_bf16_f32 v75, v22, v23\n\t" \
  "v_mfma_f32_16x16x32_bf16 a[76:79], a[228:231], v[156:159], a[76:79]\n\t" \
  "s_xor_b32 s98, %[eso], 65536\n\t" \
  "ds_read_b64_tr_b16 a[170:171], v235 offset:20480\n\t" \
  "v_accvgpr_read_b32 v24, a104\n\t" \
  "v_mfma_f32_16x16x32_bf16 a[92:95], a[232:235], v[152:155], a[92:95]\n\t" \
  "buffer_store_dwordx4 v[72:75], v242, %[ws], s98 offen offset:2048 sc1\n\t" \
  "v_mfma_f32_16x16x32_bf16 a[92:95], a[236:239], v[156:159], a[92:95]\n\t" \
  "ds_read_b64_tr_b16 a[172:173], v235 offset:24576\n\t" \
  "v_accvgpr_read_b32 v25, a105\n\t" \
  "v_accvgpr_read_b32 v26, a106\n\t" \
  "v_mfma_f32_16x16x32_bf16 a[108:111], a[240:243], v[152:155], a[108:111]\n\t" \
  "ds_read_b64_tr_b16 a[174:175], v235 offset:28672\n\t" \
  "v_xor_b32 v236, 224, v234\n\t" \
  "v_mfma_f32_16x16x32_bf16 a[108:111], a[244:247], v[156:159], a[108:111]\n\t" \
  "v_accvgpr_read_b32 v27, a107\n\t" \
  "v_accvgpr_read_b32 v28, a120\n\t" \
  "v_accvgpr_read_b32 v29, a121\n\t" \
  "v_accvgpr_read_b32 v30, a122\n\t" \
  "v_accvgpr_read_b32 v31, a123\n\t" \
  "v_pk_mul_f32 v[24:25], v[24:25], v[212:213] op_sel_hi:[1,0]\n\t" \
  "v_pk_mul_f32 v[26:27], v[26:27], v[212:213] op_sel_hi:[1,0]\n\t" \
  "v_pk_mul_f32 v[28:29], v[28:29], v[212:213] op_sel_hi:[1,0]\n\t" \
  "v_pk_mul_f32 v[30:31], v[30:31], v[212:213] op_sel_hi:[1,0]\n\t" \
  "v_cvt_pk_bf16_f32 v76, v24, v25\n\t" \
  "v_mfma_f32_16x16x32_bf16 a[124:127], a[248:251], v[152:155], a[124:127]\n\t" \
  "ds_read_b64_tr_b16 a[176:177], v236 offset:0\n\t" \
  "v_cvt_pk_bf16_f32 v77, v26, v27\n\t" \
  "v_mfma_f32_16x16x32_bf16 a[124:127], a[252:255], v[156:159], a[124:127]\n\t" \
  "s_waitcnt lgkmcnt(15)\n\t" \
  "ds_read_b64_tr_b16 a[178:179], v236 offset:4096\n\t" \
  "v_cvt_pk_bf16_f32 v78, v28, v29\n\t" \
  "v_mfma_f32_16x16x32_bf16 a[32:35], v[48:51], v[0:3], 0\n\t" \
  "ds_read_b64_tr_b16 a[180:181], v236 offset:8192\n\t" \
  "v_cvt_pk_bf16_f32 v79, v30, v31\n\t" \
  "v_mfma_f32_16x16x32_bf16 a[32:35], v[52:55], v[4:7], a[32:35]\n\t" \
  "s_xor_b32 s98, %[eso], 65536\n\t" \
  "ds_read_b64_tr_b16 a[182:183], v236 offset:12288\n\t" \
  "ds_read_b64_tr_b16 a[184:185], v236 offset:16384\n\t" \
  "v_mfma_f32_16x16x32_bf16 a[32:35], v[56:59], v[8:11], a[32:35]\n\t" \
  "buffer_store_dwordx4 v[76:79], v242, %[ws], s98 offen offset:3072 sc1\n\t" \
  "v_mfma_f32_16x16x32_bf16 a[32:35], v[60:63], v[12:15], a[32:35]\n\t" \
  "ds_read_b64_tr_b16 a[186:187], v236 offset:20480\n\t" \
  "ds_read_b64_tr_b16 a[188:189], v236 offset:24576\n\t" \
  "v_mfma_f32_16x16x32_bf16 a[48:51], v[92:95], v[12:15], 0\n\t" \
  "ds_read_b64_tr_b16 a[190:191], v236 offset:28672\n\t" \
  "v_xor_b32 v236, 160, v234\n\t" \
  "v_mfma_f32_16x16x32_bf16 a[48:51], v[88:91], v[8:11], a[48:51]\n\t" \
  "ds_read_b64_tr_b16 v[96:97], v236 offset:0\n\t" \
  "ds_read_b64_tr_b16 v[98:99], v236 offset:4096\n\t" \
  "v_mfma_f32_16x16x32_bf16 a[48:51], v[84:87], v[4:7], a[48:51]\n\t" \
  "v_rcp_f32 v214, v172\n\t" \
  "v_mfma_f32_16x16x32_bf16 a[48:51], v[80:83], v[0:3], a[48:51]\n\t" \
  "v_cmp_lt_f32 vcc, 0x5f800000, v172\n\t" \
  "s_cbranch_vccnz .Lat_x3\n\t" \
  ".Lat_y3:\n\t" \
  "v_mfma_f32_16x16x32_bf16 a[64:67], v[112:115], v[0:3], 0\n\t" \
  "ds_read_b64_tr_b16 v[100:101], v236 offset:8192\n\t" \
  "v_accvgpr_read_b32 v16, a12\n\t" \
  "v_mfma_f32_16x16x32_bf16 a[64:67], v[116:119], v[4:7], a[64:67]\n\t" \
  "ds_read_b64_tr_b16 v[102:103], v236 offset:12288\n\t" \
  "v_accvgpr_read_b32 v17, a13\n\t" \
  "v_mfma_f32_16x16x32_bf16 a[64:67], v[120:123], v[8:11], a[64:67]\n\t" \
  "ds_read_b64_tr_b16 v[104:105], v236 offset:16384\n\t" \
  "v_accvgpr_read_b32 v18, a14\n\t" \
  "v_mfma_f32_16x16x32_bf16 a[64:67], v[124:127], v[12:15], a[64:67]\n\t" \
  "ds_read_b64_tr_b16 v[106:107], v236 offset:20480\n\t" \
  "v_accvgpr_read_b32 v19, a15\n\t" \
  "v_mfma_f32_16x16x32_bf16 a[0:3], a[140:143], v[12:15], 0\n\t" \
  "ds_read_b64_tr_b16 v[108:109], v236 offset:24576\n\t" \
  "v_accvgpr_read_b32 v20, a28\n\t" \
  "v_mfma_f32_16x16x32_bf16 a[0:3], a[136:139], v[8:11], a[0:3]\n\t" \
  "ds_read_b64_tr_b16 v[110:111], v236 offset:28672\n\t" \
  "v_accvgpr_read_b32 v21, a29\n\t" \
  "v_mfma_f32_16x16x32_bf16 a[0:3], a[132:135], v[4:7], a[0:3]\n\t" \
  "ds_read_b128 a[224:227], v213 offset:32768\n\t" \
  "v_accvgpr_read_b32 v22, a30\n\t" \
  "v_mfma_f32_16x16x32_bf16 a[0:3], a[128:131], v[0:3], a[0:3]\n\t" \
  "ds_read_b128 a[228:231], v213 offset:33792\n\t" \
  "v_accvgpr_read_b32 v23, a31\n\t" \
  "v_mfma_f32_16x16x32_bf16 a[16:19], a[144:147], v[0:3], 0\n\t" \
  "s_waitcnt lgkmcnt(15)\n\t" \
  "ds_read_b128 a[232:235], v213 offset:34816\n\t" \
  "v_mul_f32 v16, v16, v214\n\t" \
  "v_mfma_f32_16x16x32_bf16 a[16:19], a[148:151], v[4:7], a[16:19]\n\t" \
  "ds_read_b128 a[236:239], v213 offset:35840\n\t" \
  "v_mul_f32 v17, v17, v214\n\t" \
  "v_mfma_f32_16x16x32_bf16 a[16:19], a[152:155], v[8:11], a[16:19]\n\t" \
  "ds_read_b128 a[240:243], v215 offset:0\n\t" \
  "v_mul_f32 v18, v18, v214\n\t" \
  "v_mfma_f32_16x16x32_bf16 a[16:19], a[156:159], v[12:15], a[16:19]\n\t" \
  "ds_read_b128 a[244:247], v215 offset:1024\n\t" \
  "v_mul_f32 v19, v19, v214\n\t" \
  "v_mfma_f32_16x16x32_bf16 a[96:99], a[172:175], v[12:15], 0\n\t" \
  "buffer_load_dwordx4 v[224:227], v252, %[ws], %[ss] offen sc1\n\t" \
  "v_mfma_f32_16x16x32_bf16 a[96:99], a[168:171], v[8:11], a[96:99]\n\t" \
  "ds_read_b128 a[248:251], v215 offset:2048\n\t" \
  "v_mul_f32 v20, v20, v214\n\t" \
  "v_mfma_f32_16x16x32_bf16 a[96:99], a[164:167], v[4:7], a[96:99]\n\t" \
  "ds_read_b128 a[252:255], v215 offset:3072\n\t" \
  "v_mul_f32 v21, v21, v214\n\t" \
  "v_mfma_f32_16x16x32_bf16 a[96:99], a[160:163], v[0:3], a[96:99]\n\t" \
  "v_mul_f32 v22, v22, v214\n\t" \
  "v_mul_f32 v23, v23, v214\n\t" \
  "v_mfma_f32_16x16x32_bf16 a[112:115], a[176:179], v[0:3], 0\n\t" \
  "s_waitcnt lgkmcnt(15)\n\t" \
  "buffer_load_dwordx4 v[128:131], v247, %[sM], %[gvo] offen\n\t" \
  "v_mfma_f32_16x16x32_bf16 a[112:115], a[180:183], v[4:7], a[112:115]\n\t" \
  "s_add_u32 s98, %[gvo], 12288\n\t" \
  "v_cvt_pk_bf16_f32 v160, v16, v17\n\t" \
  "v_cvt_pk_bf16_f32 v161, v18, v19\n\t" \
  "v_mfma_f32_16x16x32_bf16 a[112:115], a[184:187], v[8:11], a[112:115]\n\t" \
  "v_cvt_pk_bf16_f32 v162, v20, v21\n\t" \
  "v_cvt_pk_bf16_f32 v163, v22, v23\n\t" \
  "v_mfma_f32_16x16x32_bf16 a[112:115], a[188:191], v[12:15], a[112:115]\n\t" \
  "s_waitcnt lgkmcnt(8)\n\t" \
  "s_waitcnt lgkmcnt(4)\n\t" \
  "ds_write_b128 v213, v[160:163] offset:32768\n\t" \
  "v_mfma_f32_16x16x32_bf16 a[80:83], v[108:111], v[12:15], 0\n\t" \
  "buffer_load_dwordx4 v[132:135], v247, %[sM], s98 offen\n\t" \
  "v_mfma_f32_16x16x32_bf16 a[80:83], v[104:107], v[8:11], a[80:83]\n\t" \
  "s_add_u32 s98, %[gvo], 24576\n\t" \
  "v_accvgpr_read_b32 v24, a44\n\t" \
  "v_accvgpr_read_b32 v25, a45\n\t" \
  "v_mfma_f32_16x16x32_bf16 a[80:83], v[100:103], v[4:7], a[80:83]\n\t" \
  "v_accvgpr_read_b32 v26, a46\n\t" \
  "v_accvgpr_read_b32 v27, a47\n\t" \
  "v_mfma_f32_16x16x32_bf16 a[80:83], v[96:99], v[0:3], a[80:83]\n\t" \
  "v_accvgpr_read_b32 v28, a60\n\t" \
  "v_accvgpr_read_b32 v29, a61\n\t" \
  "v_mfma_f32_16x16x32_bf16 a[36:39], v[48:51], v[32:35], 0\n\t" \
  "buffer_load_dwordx4 v[136:139], v247, %[sM], s98 offen\n\t" \
  "v_mfma_f32_16x16x32_bf16 a[36:39], v[52:55], v[36:39], a[36:39]\n\t" \
  "s_add_u32 s98, %[gvo], 36864\n\t" \
  "v_accvgpr_read_b32 v30, a62\n\t" \
  "v_accvgpr_read_b32 v31, a63\n\t" \
  "v_mfma_f32_16x16x32_bf16 a[36:39], v[56:59], v[40:43], a[36:39]\n\t" \
  "v_mul_f32 v24, v24, v214\n\t" \
  "v_mul_f32 v25, v25, v214\n\t" \
  "v_mfma_f32_16x16x32_bf16 a[36:39], v[60:63], v[44:47], a[36:39]\n\t" \
  "v_mul_f32 v26, v26, v214\n\t" \
  "v_mul_f32 v27, v27, v214\n\t" \
  "s_waitcnt vmcnt(8)\n\t" \
  "buffer_store_dword v243, v254, %[ws], %[fw2] offen offset:0 sc1\n\t" \
  "v_mfma_f32_16x16x32_bf16 a[52:55], v[92:95], v[44:47], 0\n\t" \
  "buffer_load_dwordx4 v[140:143], v247, %[sM], s98 offen\n\t" \
  "v_mfma_f32_16x16x32_bf16 a[52:55], v[88:91], v[40:43], a[52:55]\n\t" \
  "s_add_u32 s98, %[gvo], 49152\n\t" \
  "v_mul_f32 v28, v28, v214\n\t" \
  "v_mul_f32 v29, v29, v214\n\t" \
  "v_mfma_f32_16x16x32_bf16 a[52:55], v[84:87], v[36:39], a[52:55]\n\t" \
  "v_mul_f32 v30, v30, v214\n\t" \
  "v_mul_f32 v31, v31, v214\n\t" \
  "v_mfma_f32_16x16x32_bf16 a[52:55], v[80:83], v[32:35], a[52:55]\n\t" \
  "v_cvt_pk_bf16_f32 v164, v24, v25\n\t" \
  "v_cvt_pk_bf16_f32 v165, v26, v27\n\t" \
  "v_mfma_f32_16x16x32_bf16 a[68:71], v[112:115], v[32:35], 0\n\t" \
  "buffer_load_dwordx4 v[144:147], v247, %[sM], s98 offen\n\t" \
  "v_mfma_f32_16x16x32_bf16 a[68:71], v[116:119], v[36:39], a[68:71]\n\t" \
  "s_add_u32 s98, %[gvo], 61440\n\t" \
  "v_cvt_pk_bf16_f32 v166, v28, v29\n\t" \
  "v_cvt_pk_bf16_f32 v167, v30, v31\n\t" \
  "v_mfma_f32_16x16x32_bf16 a[68:71], v[120:123], v[40:43], a[68:71]\n\t" \
  "ds_write_b128 v213, v[164:167] offset:33792\n\t" \
  "v_mfma_f32_16x16x32_bf16 a[68:71], v[124:127], v[44:47], a[68:71]\n\t" \
  "v_accvgpr_read_b32 v16, a76\n\t" \
  "v_accvgpr_read_b32 v17, a77\n\t" \
  "v_mfma_f32_16x16x32_bf16 a[4:7], a[140:143], v[44:47], 0\n\t" \
  "buffer_load_dwordx4 v[148:151], v247, %[sM], s98 offen\n\t" \
  "v_mfma_f32_16x16x32_bf16 a[4:7], a[136:139], v[40:43], a[4:7]\n\t" \
  "s_add_u32 s98, %[gvo], 73728\n\t" \
  "v_accvgpr_read_b32 v18, a78\n\t" \
  "v_accvgpr_read_b32 v19, a79\n\t" \
  "v_mfma_f32_16x16x32_bf16 a[4:7], a[132:135], v[36:39], a[4:7]\n\t" \
  "v_accvgpr_read_b32 v20, a92\n\t" \
  "v_accvgpr_read_b32 v21, a93\n\t" \
  "v_mfma_f32_16x16x32_bf16 a[4:7], a[128:131], v[32:35], a[4:7]\n\t" \
  "v_accvgpr_read_b32 v22, a94\n\t" \
  "v_accvgpr_read_b32 v23, a95\n\t" \
  "v_mfma_f32_16x16x32_bf16 a[20:23], a[144:147], v[32:35], 0\n\t" \
  "buffer_load_dwordx4 v[152:155], v247, %[sM], s98 offen\n\t" \
  "v_mfma_f32_16x16x32_bf16 a[20:23], a[148:151], v[36:39], a[20:23]\n\t" \
  "s_add_u32 s98, %[gvo], 86016\n\t" \
  "v_mul_f32 v16, v16, v214\n\t" \
  "v_mul_f32 v17, v17, v214\n\t" \
  "v_mfma_f32_16x16x32_bf16 a[20:23], a[152:155], v[40:43], a[20:23]\n\t" \
  "v_mul_f32 v18, v18, v214\n\t" \
  "v_mul_f32 v19, v19, v214\n\t" \
  "v_mfma_f32_16x16x32_bf16 a[20:23], a[156:159], v[44:47], a[20:23]\n\t" \
  "v_mul_f32 v20, v20, v214\n\t" \
  "v_mul_f32 v21, v21, v214\n\t" \
  "v_mfma_f32_16x16x32_bf16 a[100:103], a[172:175], v[44:47], 0\n\t" \
  "buffer_load_dwordx4 v[156:159], v247, %[sM], s98 offen\n\t" \
  "v_mfma_f32_16x16x32_bf16 a[100:103], a[168:171], v[40:43], a[100:103]\n\t" \
  "v_mul_f32 v22, v22, v214\n\t" \
  "v_mul_f32 v23, v23, v214\n\t" \
  "v_mfma_f32_16x16x32_bf16 a[100:103], a[164:167], v[36:39], a[100:103]\n\t" \
  "v_cvt_pk_bf16_f32 v168, v16, v17\n\t" \
  "v_cvt_pk_bf16_f32 v169, v18, v19\n\t" \
  "v_mfma_f32_16x16x32_bf16 a[100:103], a[160:163], v[32:35], a[100:103]\n\t" \
  "v_cvt_pk_bf16_f32 v170, v20, v21\n\t" \
  "v_cvt_pk_bf16_f32 v171, v22, v23\n\t" \
  "v_mfma_f32_16x16x32_bf16 a[116:119], a[176:179], v[32:35], 0\n\t" \
  "ds_write_b128 v213, v[168:171] offset:34816\n\t" \
  "v_mfma_f32_16x16x32_bf16 a[116:119], a[180:183], v[36:39], a[116:119]\n\t" \
  "v_accvgpr_read_b32 v24, a108\n\t" \
  "v_accvgpr_read_b32 v25, a109\n\t" \
  "v_mfma_f32_16x16x32_bf16 a[116:119], a[184:187], v[40:43], a[116:119]\n\t" \
  "v_accvgpr_read_b32 v26, a110\n\t" \
  "v_accvgpr_read_b32 v27, a111\n\t" \
  "v_mfma_f32_16x16x32_bf16 a[116:119], a[188:191], v[44:47], a[116:119]\n\t" \
  "v_accvgpr_read_b32 v28, a124\n\t" \
  "v_accvgpr_read_b32 v29, a125\n\t" \
  "v_mfma_f32_16x16x32_bf16 a[84:87], v[108:111], v[44:47], 0\n\t" \
  "s_add_u32 m0, %[m], 147456\n\t" \
  "v_accvgpr_read_b32 v30, a126\n\t" \
  "v_accvgpr_read_b32 v31, a127\n\t" \
  "v_mfma_f32_16x16x32_bf16 a[84:87], v[104:107], v[40:43], a[84:87]\n\t" \
  "s_xor_b32 s97, %[wob], 655360\n\t" \
  "v_mul_f32 v24, v24, v214\n\t" \
  "v_mul_f32 v25, v25, v214\n\t" \
  "v_mfma_f32_16x16x32_bf16 a[84:87], v[100:103], v[36:39], a[84:87]\n\t" \
  "v_mul_f32 v26, v26, v214\n\t" \
  "v_mul_f32 v27, v27, v214\n\t" \
  "v_mfma_f32_16x16x32_bf16 a[84:87], v[96:99], v[32:35], a[84:87]\n\t" \
  "v_mul_f32 v28, v28, v214\n\t" \
  "v_mul_f32 v29, v29, v214\n\t" \
  "v_mfma_f32_16x16x32_bf16 a[40:43], v[48:51], v[64:67], 0\n\t" \
  "v_mul_f32 v30, v30, v214\n\t" \
  "v_mul_f32 v31, v31, v214\n\t" \
  "v_mfma_f32_16x16x32_bf16 a[40:43], v[52:55], v[68:71], a[40:43]\n\t" \
  "v_cvt_pk_bf16_f32 v172, v24, v25\n\t" \
  "v_cvt_pk_bf16_f32 v173, v26, v27\n\t" \
  "v_mfma_f32_16x16x32_bf16 a[40:43], v[56:59], v[72:75], a[40:43]\n\t" \
  "buffer_load_dwordx4 v240, %[wo], s97 offen offset:0 lds\n\t" \
  "v_mfma_f32_16x16x32_bf16 a[40:43], v[60:63], v[76:79], a[40:43]\n\t" \
  "s_add_u32 s97, s97, 7168\n\t" \
  "v_cvt_pk_bf16_f32 v174, v28, v29\n\t" \
  "v_cvt_pk_bf16_f32 v175, v30, v31\n\t" \
  "v_mfma_f32_16x16x32_bf16 a[56:59], v[92:95], v[76:79], 0\n\t" \
  "ds_write_b128 v213, v[172:175] offset:35840\n\t" \
  "v_mfma_f32_16x16x32_bf16 a[56:59], v[88:91], v[72:75], a[56:59]\n\t" \
  "buffer_load_dwordx4 v241, %[wo], s97 offen offset:1024 lds\n\t" \
  "v_mfma_f32_16x16x32_bf16 a[56:59], v[84:87], v[68:71], a[56:59]\n\t" \
  "s_add_u32 s97, s97, 7168\n\t" \
  "v_mfma_f32_16x16x32_bf16 a[56:59], v[80:83], v[64:67], a[56:59]\n\t" \
  "v_mfma_f32_16x16x32_bf16 a[72:75], v[112:115], v[64:67], 0\n\t" \
  "buffer_load_dwordx4 v240, %[wo], s97 offen offset:2048 lds\n\t" \
  "v_mfma_f32_16x16x32_bf16 a[72:75], v[116:119], v[68:71], a[72:75]\n\t" \
  "s_add_u32 s97, s97, 7168\n\t" \
  "v_mfma_f32_16x16x32_bf16 a[72:75], v[120:123], v[72:75], a[72:75]\n\t" \
  "v_mfma_f32_16x16x32_bf16 a[72:75], v[124:127], v[76:79], a[72:75]\n\t" \
  "buffer_load_dwordx4 v241, %[wo], s97 offen offset:3072 lds\n\t" \
  STS("s_memrealtime s[94:95]\n\t") \
  STS("s_memtime s[92:93]\n\t") \
  STS("s_waitcnt lgkmcnt(0)\n\t") \
  STS("v_writelane_b32 %[stv], s94, 22\n\t") \
  STS("v_writelane_b32 %[stv], s92, 23\n\t") \
  ".subsection 1\n\t" \
  "s_endpgm\n\t" \
  ".Lat_v:\n\t" \
  "s_mov_b32 s98, 0\n\t" \
  ".Lat_v1:\n\t" \
  "s_sleep 4\n\t" \
  "buffer_load_dword v209, v205, %[ws], 0 offen sc1\n\t" \
  "s_waitcnt vmcnt(0)\n\t" \
  "v_and_b32 v206, 0xbfffbfff, v209\n\t" \
  "v_cmp_ne_u32_e64 s[96:97], %[tag], v206\n\t" \
  "s_cmp_lg_u64 s[96:97], 0\n\t" \
  "s_cbranch_scc0 .Lat_w\n\t" \
  "s_add_u32 s98, s98, 1\n\t" \
  "s_cmp_lt_u32 s98, %[cap]\n\t" \
  "s_cbranch_scc1 .Lat_v1\n\t" \
  "v_mov_b32 v206, %[tag]\n\t" \
  "buffer_store_dword v206, off, %[ws], 0 offset:256 sc1\n\t" \
  "s_branch .Lat_w\n\t" \
  ".Lat_x0:\n\t" \
  "v_lshlrev_b32 v209, 12, v233\n\t" \
  "v_bfrev_b32 v254, 1\n\t" \
  "s_mov_b64 exec, 15\n\t" \
  "ds_write_b32 v209, v243 offset:49152\n\t" \
  "s_mov_b64 exec, -1\n\t" \
  "s_waitcnt lgkmcnt(0)\n\t" \
  "s_branch .Lat_y0\n\t" \
  ".Lat_x4:\n\t" \
  "v_lshlrev_b32 v209, 12, v233\n\t" \
  "v_bfrev_b32 v254, 1\n\t" \
  "s_mov_b64 exec, 15\n\t" \
  "ds_write_b32 v209, v243 offset:49152\n\t" \
  "s_mov_b64 exec, -1\n\t" \
  "s_waitcnt lgkmcnt(0)\n\t" \
  "s_branch .Lat_y4\n\t" \
  ".Lat_x1:\n\t" \
  "v_lshlrev_b32 v209, 12, v233\n\t" \
  "v_bfrev_b32 v254, 1\n\t" \
  "s_mov_b64 exec, 15\n\t" \
  "ds_write_b32 v209, v243 offset:49152\n\t" \
  "s_mov_b64 exec, -1\n\t" \
  "s_waitcnt lgkmcnt(0)\n\t" \
  "s_branch .Lat_y1\n\t" \
  ".Lat_x2:\n\t" \
  "v_lshlrev_b32 v209, 12, v233\n\t" \
  "v_bfrev_b32 v254, 1\n\t" \
  "s_mov_b64 exec, 15\n\t" \
  "ds_write_b32 v209, v243 offset:49152\n\t" \
  "s_mov_b64 exec, -1\n\t" \
  "s_waitcnt lgkmcnt(0)\n\t" \
  "s_branch .Lat_y2\n\t" \
  ".Lat_x3:\n\t" \
  "v_lshlrev_b32 v209, 12, v233\n\t" \
  "v_bfrev_b32 v254, 1\n\t" \
  "s_mov_b64 exec, 15\n\t" \
  "ds_write_b32 v209, v243 offset:49152\n\t" \
  "s_mov_b64 exec, -1\n\t" \
  "s_waitcnt lgkmcnt(0)\n\t" \
  "s_branch .Lat_y3\n\t" \
  ".subsection 2\n\t" \
  ".Lsf_go:\n\t" \
  "v_mfma_f32_16x16x32_bf16 v[28:31], a[156:159], a[224:227], v[28:31]\n\t" \
  "v_mov_b32 v216, 0x3f803f80\n\t" \
  "v_lshlrev_b32 v204, 7, v233\n\t" \
  "v_mfma_f32_16x16x32_bf16 v[28:31], a[92:95], a[240:243], v[28:31]\n\t" \
  "v_cmp_gt_u32 vcc, 16, v233\n\t" \
  "v_lshrrev_b32 v202, 3, v233\n\t" \
  "v_mfma_f32_16x16x32_bf16 v[24:27], a[152:155], a[224:227], v[24:27]\n\t" \
  "v_mul_u32_u24 v202, 11264, v202\n\t" \
  "v_add_u32 v202, v202, v204\n\t" \
  "v_mfma_f32_16x16x32_bf16 v[24:27], a[88:91], a[240:243], v[24:27]\n\t" \
  "v_bfrev_b32 v195, 1\n\t" \
  "v_mov_b32 v217, 0x3f803f80\n\t" \
  "v_mfma_f32_16x16x32_bf16 v[20:23], a[148:151], a[224:227], v[20:23]\n\t" \
  "v_cvt_pk_bf16_f32 v31, v30, v31\n\t" \
  "v_cvt_pk_bf16_f32 v30, v28, v29\n\t" \
  "v_mfma_f32_16x16x32_bf16 v[20:23], a[84:87], a[240:243], v[20:23]\n\t" \
  "v_cndmask_b32 v204, v195, v204, vcc\n\t" \
  "v_cndmask_b32 v202, v195, v202, vcc\n\t" \
  "v_mfma_f32_16x16x32_bf16 v[16:19], a[144:147], a[224:227], v[16:19]\n\t" \
  "v_cvt_pk_bf16_f32 v28, v24, v25\n\t" \
  "v_cvt_pk_bf16_f32 v29, v26, v27\n\t" \
  "v_mfma_f32_16x16x32_bf16 v[16:19], a[80:83], a[240:243], v[16:19]\n\t" \
  "v_permlane16_swap_b32 v28, v30\n\t" \
  "v_permlane16_swap_b32 v29, v31\n\t" \
  "v_mfma_f32_16x16x32_bf16 v[12:15], a[140:143], a[224:227], v[12:15]\n\t" \
  "v_cvt_pk_bf16_f32 v27, v22, v23\n\t" \
  "v_cvt_pk_bf16_f32 v26, v20, v21\n\t" \
  "v_mfma_f32_16x16x32_bf16 v[12:15], a[76:79], a[240:243], v[12:15]\n\t" \
  "v_mov_b32 v218, 0x3f803f80\n\t" \
  "v_mov_b32 v219, 0x3f803f80\n\t" \
  "v_mfma_f32_16x16x32_bf16 v[8:11], a[136:139], a[224:227], v[8:11]\n\t" \
  "v_cvt_pk_bf16_f32 v24, v16, v17\n\t" \
  "v_cvt_pk_bf16_f32 v25, v18, v19\n\t" \
  "v_mfma_f32_16x16x32_bf16 v[8:11], a[72:75], a[240:243], v[8:11]\n\t" \
  "v_permlane16_swap_b32 v24, v26\n\t" \
  "v_permlane16_swap_b32 v25, v27\n\t" \
  "v_mfma_f32_16x16x32_bf16 v[4:7], a[132:135], a[224:227], v[4:7]\n\t" \
  "v_cvt_pk_bf16_f32 v23, v14, v15\n\t" \
  "v_cvt_pk_bf16_f32 v22, v12, v13\n\t" \
  "v_mfma_f32_16x16x32_bf16 v[4:7], a[68:71], a[240:243], v[4:7]\n\t" \
  "v_mov_b32 v196, 1.0\n\t" \
  "v_mov_b32 v197, 1.0\n\t" \
  "v_mfma_f32_16x16x32_bf16 v[0:3], a[128:131], a[224:227], v[0:3]\n\t" \
  "v_cvt_pk_bf16_f32 v20, v8, v9\n\t" \
  "v_cvt_pk_bf16_f32 v21, v10, v11\n\t" \
  "v_mfma_f32_16x16x32_bf16 v[0:3], a[64:67], a[240:243], v[0:3]\n\t" \
  "v_permlane16_swap_b32 v20, v22\n\t" \
  "v_permlane16_swap_b32 v21, v23\n\t" \
  "s_waitcnt vmcnt(2)\n\t" \
  "s_barrier\n\t" \
  "v_mfma_f32_16x16x32_bf16 v[60:63], a[156:159], a[228:231], v[60:63]\n\t" \
  "v_cvt_pk_bf16_f32 v19, v6, v7\n\t" \
  "v_cvt_pk_bf16_f32 v18, v4, v5\n\t" \
  "v_mfma_f32_16x16x32_bf16 v[60:63], a[92:95], a[244:247], v[60:63]\n\t" \
  "v_cvt_pk_bf16_f32 v16, v0, v1\n\t" \
  "v_cvt_pk_bf16_f32 v17, v2, v3\n\t" \
  "v_mfma_f32_16x16x32_bf16 v[56:59], a[152:155], a[228:231], v[56:59]\n\t" \
  "v_permlane16_swap_b32 v16, v18\n\t" \
  "v_permlane16_swap_b32 v17, v19\n\t" \
  "v_mfma_f32_16x16x32_bf16 v[56:59], a[88:91], a[244:247], v[56:59]\n\t" \
  "v_mov_b32 v198, 1.0\n\t" \
  "v_mov_b32 v199, 1.0\n\t" \
  "v_mfma_f32_16x16x32_bf16 v[52:55], a[148:151], a[228:231], v[52:55]\n\t" \
  "v_cvt_pk_bf16_f32 v63, v62, v63\n\t" \
  "v_cvt_pk_bf16_f32 v62, v60, v61\n\t" \
  "v_mfma_f32_16x16x32_bf16 v[52:55], a[84:87], a[244:247], v[52:55]\n\t" \
  "v_lshlrev_b32 v242, 4, v233\n\t" \
  "s_mov_b32 s99, 0\n\t" \
  "v_mfma_f32_16x16x32_bf16 v[48:51], a[144:147], a[228:231], v[48:51]\n\t" \
  "v_cvt_pk_bf16_f32 v60, v56, v57\n\t" \
  "v_cvt_pk_bf16_f32 v61, v58, v59\n\t" \
  "v_mfma_f32_16x16x32_bf16 v[48:51], a[80:83], a[244:247], v[48:51]\n\t" \
  "v_permlane16_swap_b32 v60, v62\n\t" \
  "v_permlane16_swap_b32 v61, v63\n\t" \
  "v_mfma_f32_16x16x32_bf16 v[44:47], a[140:143], a[228:231], v[44:47]\n\t" \
  "v_cvt_pk_bf16_f32 v59, v54, v55\n\t" \
  "v_cvt_pk_bf16_f32 v58, v52, v53\n\t" \
  "v_mfma_f32_16x16x32_bf16 v[44:47], a[76:79], a[244:247], v[44:47]\n\t" \
  "v_add_u32 v237, 65536, v228\n\t" \
  "v_add_u32 v238, 65536, v229\n\t" \
  "v_mfma_f32_16x16x32_bf16 v[40:43], a[136:139], a[228:231], v[40:43]\n\t" \
  "v_cvt_pk_bf16_f32 v56, v48, v49\n\t" \
  "v_cvt_pk_bf16_f32 v57, v50, v51\n\t" \
  "v_mfma_f32_16x16x32_bf16 v[40:43], a[72:75], a[244:247], v[40:43]\n\t" \
  "v_permlane16_swap_b32 v56, v58\n\t" \
  "v_permlane16_swap_b32 v57, v59\n\t" \
  "v_mfma_f32_16x16x32_bf16 v[36:39], a[132:135], a[228:231], v[36:39]\n\t" \
  "v_cvt_pk_bf16_f32 v55, v46, v47\n\t" \
  "v_cvt_pk_bf16_f32 v54, v44, v45\n\t" \
  "v_mfma_f32_16x16x32_bf16 v[36:39], a[68:71], a[244:247], v[36:39]\n\t" \
  "ds_read_b128 a[0:3], v220 offset:0\n\t" \
  "ds_read_b128 a[4:7], v221 offset:0\n\t" \
  "v_mfma_f32_16x16x32_bf16 v[32:35], a[128:131], a[228:231], v[32:35]\n\t" \
  "v_cvt_pk_bf16_f32 v52, v40, v41\n\t" \
  "v_cvt_pk_bf16_f32 v53, v42, v43\n\t" \
  "v_mfma_f32_16x16x32_bf16 v[32:35], a[64:67], a[244:247], v[32:35]\n\t" \
  "v_permlane16_swap_b32 v52, v54\n\t" \
  "v_permlane16_swap_b32 v53, v55\n\t" \
  "v_mfma_f32_16x16x32_bf16 v[92:95], a[156:159], a[232:235], v[92:95]\n\t" \
  "v_cvt_pk_bf16_f32 v51, v38, v39\n\t" \
  "v_cvt_pk_bf16_f32 v50, v36, v37\n\t" \
  "v_mfma_f32_16x16x32_bf16 v[92:95], a[92:95], a[248:251], v[92:95]\n\t" \
  "s_add_u32 m0, %[m], 0\n\t" \
  "ds_read_b128 a[8:11], v222 offset:0\n\t" \
  "v_mfma_f32_16x16x32_bf16 v[88:91], a[152:155], a[232:235], v[88:91]\n\t" \
  "v_cvt_pk_bf16_f32 v48, v32, v33\n\t" \
  "v_cvt_pk_bf16_f32 v49, v34, v35\n\t" \
  "v_mfma_f32_16x16x32_bf16 v[88:91], a[88:91], a[248:251], v[88:91]\n\t" \
  "v_permlane16_swap_b32 v48, v50\n\t" \
  "v_permlane16_swap_b32 v49, v51\n\t" \
  "v_mfma_f32_16x16x32_bf16 v[84:87], a[148:151], a[232:235], v[84:87]\n\t" \
  "v_cvt_pk_bf16_f32 v95, v94, v95\n\t" \
  "v_cvt_pk_bf16_f32 v94, v92, v93\n\t" \
  "v_mfma_f32_16x16x32_bf16 v[84:87], a[84:87], a[248:251], v[84:87]\n\t" \
  "ds_read_b128 a[12:15], v223 offset:0\n\t" \
  "s_xor_b32 s97, %[wob], 0\n\t" \
  "v_mfma_f32_16x16x32_bf16 v[80:83], a[144:147], a[232:235], v[80:83]\n\t" \
  "v_cvt_pk_bf16_f32 v92, v88, v89\n\t" \
  "v_cvt_pk_bf16_f32 v93, v90, v91\n\t" \
  "v_mfma_f32_16x16x32_bf16 v[80:83], a[80:83], a[248:251], v[80:83]\n\t" \
  "v_permlane16_swap_b32 v92, v94\n\t" \
  "v_permlane16_swap_b32 v93, v95\n\t" \
  "v_mfma_f32_16x16x32_bf16 v[76:79], a[140:143], a[232:235], v[76:79]\n\t" \
  "v_cvt_pk_bf16_f32 v91, v86, v87\n\t" \
  "v_cvt_pk_bf16_f32 v90, v84, v85\n\t" \
  "v_mfma_f32_16x16x32_bf16 v[76:79], a[76:79], a[248:251], v[76:79]\n\t" \
  "ds_read_b128 a[16:19], v220 offset:4096\n\t" \
  "ds_read_b128 a[20:23], v221 offset:4096\n\t" \
  "v_mfma_f32_16x16x32_bf16 v[72:75], a[136:139], a[232:235], v[72:75]\n\t" \
  "v_cvt_pk_bf16_f32 v88, v80, v81\n\t" \
  "v_cvt_pk_bf16_f32 v89, v82, v83\n\t" \
  "v_mfma_f32_16x16x32_bf16 v[72:75], a[72:75], a[248:251], v[72:75]\n\t" \
  "v_permlane16_swap_b32 v88, v90\n\t" \
  "v_permlane16_swap_b32 v89, v91\n\t" \
  "v_mfma_f32_16x16x32_bf16 v[68:71], a[132:135], a[232:235], v[68:71]\n\t" \
  "v_cvt_pk_bf16_f32 v87, v78, v79\n\t" \
  "v_cvt_pk_bf16_f32 v86, v76, v77\n\t" \
  "v_mfma_f32_16x16x32_bf16 v[68:71], a[68:71], a[248:251], v[68:71]\n\t" \
  "ds_read_b128 a[24:27], v222 offset:4096\n\t" \
  "buffer_load_dwordx4 v240, %[wo], s97 offen offset:0 lds\n\t" \
  "v_mfma_f32_16x16x32_bf16 v[64:67], a[128:131], a[232:235], v[64:67]\n\t" \
  "v_cvt_pk_bf16_f32 v84, v72, v73\n\t" \
  "v_cvt_pk_bf16_f32 v85, v74, v75\n\t" \
  "v_mfma_f32_16x16x32_bf16 v[64:67], a[64:67], a[248:251], v[64:67]\n\t" \
  "v_permlane16_swap_b32 v84, v86\n\t" \
  "v_permlane16_swap_b32 v85, v87\n\t" \
  "v_mfma_f32_16x16x32_bf16 v[124:127], a[156:159], a[236:239], v[124:127]\n\t" \
  "v_cvt_pk_bf16_f32 v83, v70, v71\n\t" \
  "v_cvt_pk_bf16_f32 v82, v68, v69\n\t" \
  "v_mfma_f32_16x16x32_bf16 v[124:127], a[92:95], a[252:255], v[124:127]\n\t" \
  "ds_read_b128 a[28:31], v223 offset:4096\n\t" \
  "ds_read_b128 a[32:35], v220 offset:8192\n\t" \
  "v_mfma_f32_16x16x32_bf16 v[120:123], a[152:155], a[236:239], v[120:123]\n\t" \
  "v_cvt_pk_bf16_f32 v80, v64, v65\n\t" \
  "v_cvt_pk_bf16_f32 v81, v66, v67\n\t" \
  "v_mfma_f32_16x16x32_bf16 v[120:123], a[88:91], a[252:255], v[120:123]\n\t" \
  "v_permlane16_swap_b32 v80, v82\n\t" \
  "v_permlane16_swap_b32 v81, v83\n\t" \
  "v_mfma_f32_16x16x32_bf16 v[116:119], a[148:151], a[236:239], v[116:119]\n\t" \
  "v_cvt_pk_bf16_f32 v127, v126, v127\n\t" \
  "v_cvt_pk_bf16_f32 v126, v124, v125\n\t" \
  "v_mfma_f32_16x16x32_bf16 v[116:119], a[84:87], a[252:255], v[116:119]\n\t" \
  "s_add_u32 s97, s97, 7168\n\t" \
  "buffer_load_dwordx4 v241, %[wo], s97 offen offset:1024 lds\n\t" \
  "v_mfma_f32_16x16x32_bf16 v[112:115], a[144:147], a[236:239], v[112:115]\n\t" \
  "v_cvt_pk_bf16_f32 v124, v120, v121\n\t" \
  "v_cvt_pk_bf16_f32 v125, v122, v123\n\t" \
  "v_mfma_f32_16x16x32_bf16 v[112:115], a[80:83], a[252:255], v[112:115]\n\t" \
  "v_permlane16_swap_b32 v124, v126\n\t" \
  "v_permlane16_swap_b32 v125, v127\n\t" \
  "ds_read_b128 a[36:39], v221 offset:8192\n\t" \
  "ds_read_b128 a[40:43], v222 offset:8192\n\t" \
  "ds_read_b128 a[44:47], v223 offset:8192\n\t" \
  "ds_read_b128 a[48:51], v220 offset:12288\n\t" \
  "ds_read_b128 a[52:55], v221 offset:12288\n\t" \
  "ds_read_b128 a[56:59], v222 offset:12288\n\t" \
  "ds_read_b128 a[60:63], v223 offset:12288\n\t" \
  "v_mfma_f32_16x16x32_bf16 v[108:111], a[140:143], a[236:239], v[108:111]\n\t" \
  "v_cvt_pk_bf16_f32 v123, v118, v119\n\t" \
  "v_cvt_pk_bf16_f32 v122, v116, v117\n\t" \
  "v_mfma_f32_16x16x32_bf16 v[108:111], a[76:79], a[252:255], v[108:111]\n\t" \
  "v_cvt_pk_bf16_f32 v120, v112, v113\n\t" \
  "v_cvt_pk_bf16_f32 v121, v114, v115\n\t" \
  "v_mfma_f32_16x16x32_bf16 v[104:107], a[136:139], a[236:239], v[104:107]\n\t" \
  "v_permlane16_swap_b32 v120, v122\n\t" \
  "v_permlane16_swap_b32 v121, v123\n\t" \
  "v_mfma_f32_16x16x32_bf16 v[104:107], a[72:75], a[252:255], v[104:107]\n\t" \
  "s_add_u32 s97, s97, 7168\n\t" \
  "buffer_load_dwordx4 v240, %[wo], s97 offen offset:2048 lds\n\t" \
  "v_mfma_f32_16x16x32_bf16 v[100:103], a[132:135], a[236:239], v[100:103]\n\t" \
  "v_cvt_pk_bf16_f32 v119, v110, v111\n\t" \
  "v_cvt_pk_bf16_f32 v118, v108, v109\n\t" \
  "v_mfma_f32_16x16x32_bf16 v[100:103], a[68:71], a[252:255], v[100:103]\n\t" \
  "s_add_u32 s97, s97, 7168\n\t" \
  "buffer_load_dwordx4 v241, %[wo], s97 offen offset:3072 lds\n\t" \
  "v_mfma_f32_16x16x32_bf16 v[96:99], a[128:131], a[236:239], v[96:99]\n\t" \
  "v_cvt_pk_bf16_f32 v116, v104, v105\n\t" \
  "v_cvt_pk_bf16_f32 v117, v106, v107\n\t" \
  "v_mfma_f32_16x16x32_bf16 v[96:99], a[64:67], a[252:255], v[96:99]\n\t" \
  "v_permlane16_swap_b32 v116, v118\n\t" \
  "v_permlane16_swap_b32 v117, v119\n\t" \
  "s_add_u32 m0, %[m], 16384\n\t" \
  "s_xor_b32 s97, %[wob], 131072\n\t" \
  "buffer_load_dwordx4 v240, %[wo], s97 offen offset:0 lds\n\t" \
  "s_add_u32 s97, s97, 7168\n\t" \
  "buffer_load_dwordx4 v241, %[wo], s97 offen offset:1024 lds\n\t" \
  "s_add_u32 s97, s97, 7168\n\t" \
  "buffer_load_dwordx4 v240, %[wo], s97 offen offset:2048 lds\n\t" \
  "s_add_u32 s97, s97, 7168\n\t" \
  "buffer_load_dwordx4 v241, %[wo], s97 offen offset:3072 lds\n\t" \
  "s_waitcnt lgkmcnt(0)\n\t" \
  "s_barrier\n\t" \
  "v_mfma_f32_16x16x32_bf16 v[0:3], a[0:3], v[16:19], 0\n\t" \
  "v_cvt_pk_bf16_f32 v115, v102, v103\n\t" \
  "v_cvt_pk_bf16_f32 v114, v100, v101\n\t" \
  "v_mfma_f32_16x16x32_bf16 v[0:3], a[4:7], v[20:23], v[0:3]\n\t" \
  "v_cvt_pk_bf16_f32 v112, v96, v97\n\t" \
  "v_cvt_pk_bf16_f32 v113, v98, v99\n\t" \
  "v_mfma_f32_16x16x32_bf16 v[0:3], a[8:11], v[24:27], v[0:3]\n\t" \
  "v_permlane16_swap_b32 v112, v114\n\t" \
  "v_permlane16_swap_b32 v113, v115\n\t" \
  "v_mfma_f32_16x16x32_bf16 v[0:3], a[12:15], v[28:31], v[0:3]\n\t" \
  "s_add_u32 s98, %[pf4], 28672\n\t" \
  "buffer_load_dword %[pfd], v245, %[sU], s98 offen\n\t" \
  "v_mfma_f32_16x16x32_bf16 v[32:35], a[0:3], v[48:51], 0\n\t" \
  "ds_read_b128 a[192:195], v220 offset:16384\n\t" \
  "s_add_u32 m0, %[m], 49152\n\t" \
  "v_mfma_f32_16x16x32_bf16 v[32:35], a[4:7], v[52:55], v[32:35]\n\t" \
  "ds_read_b128 a[196:199], v221 offset:16384\n\t" \
  "ds_read_b128 a[200:203], v222 offset:16384\n\t" \
  "v_mfma_f32_16x16x32_bf16 v[32:35], a[8:11], v[56:59], v[32:35]\n\t" \
  "v_max3_f32 v194, v0, v1, v2\n\t" \
  "v_max_f32 v194, v194, v3\n\t" \
  "v_mfma_f32_16x16x32_bf16 v[32:35], a[12:15], v[60:63], v[32:35]\n\t" \
  "v_mov_b32 v195, v194\n\t" \
  "s_add_u32 s96, %[sofk], 32768\n\t" \
  "s_nop 0\n\t" \
  "v_permlane32_swap_b32 v194, v195\n\t" \
  "v_max_f32 v194, v194, v195\n\t" \
  "v_mov_b32 v195, v194\n\t" \
  "s_nop 1\n\t" \
  "v_permlane16_swap_b32 v194, v195\n\t" \
  "v_max_f32 v194, v194, v195\n\t" \
  "v_sub_f32 v176, 0xc2000000, v194\n\t" \
  "v_sub_f32 v177, 0xc2000000, v194\n\t" \
  "v_sub_f32 v178, 0xc2000000, v194\n\t" \
  "v_sub_f32 v179, 0xc2000000, v194\n\t" \
  "s_nop 1\n\t" \
  "v_mfma_f32_16x16x32_bf16 v[4:7], a[16:19], v[16:19], v[176:179]\n\t" \
  "v_add_f32 v0, v0, v176\n\t" \
  "v_add_f32 v1, v1, v176\n\t" \
  "v_mfma_f32_16x16x32_bf16 v[4:7], a[20:23], v[20:23], v[4:7]\n\t" \
  "v_add_f32 v2, v2, v176\n\t" \
  "v_add_f32 v3, v3, v176\n\t" \
  "v_mfma_f32_16x16x32_bf16 v[4:7], a[24:27], v[24:27], v[4:7]\n\t" \
  "ds_read_b128 a[204:207], v223 offset:16384\n\t" \
  "v_max3_f32 v194, v32, v33, v34\n\t" \
  "v_mfma_f32_16x16x32_bf16 v[4:7], a[28:31], v[28:31], v[4:7]\n\t" \
  "v_max_f32 v194, v194, v35\n\t" \
  "v_mov_b32 v195, v194\n\t" \
  "v_mfma_f32_16x16x32_bf16 v[8:11], a[32:35], v[16:19], v[176:179]\n\t" \
  "ds_read_b128 a[208:211], v220 offset:20480\n\t" \
  "v_permlane32_swap_b32 v194, v195\n\t" \
  "v_mfma_f32_16x16x32_bf16 v[8:11], a[36:39], v[20:23], v[8:11]\n\t" \
  "v_max_f32 v194, v194, v195\n\t" \
  "v_mov_b32 v195, v194\n\t" \
  "v_mfma_f32_16x16x32_bf16 v[8:11], a[40:43], v[24:27], v[8:11]\n\t" \
  "ds_read_b128 a[212:215], v221 offset:20480\n\t" \
  "v_permlane16_swap_b32 v194, v195\n\t" \
  "v_mfma_f32_16x16x32_bf16 v[8:11], a[44:47], v[28:31], v[8:11]\n\t" \
  "v_max_f32 v194, v194, v195\n\t" \
  "v_sub_f32 v180, 0xc2000000, v194\n\t" \
  "v_mfma_f32_16x16x32_bf16 v[12:15], a[48:51], v[16:19], v[176:179]\n\t" \
  "v_sub_f32 v181, 0xc2000000, v194\n\t" \
  "v_sub_f32 v182, 0xc2000000, v194\n\t" \
  "v_mfma_f32_16x16x32_bf16 v[12:15], a[52:55], v[20:23], v[12:15]\n\t" \
  "v_sub_f32 v183, 0xc2000000, v194\n\t" \
  "v_add_f32 v32, v32, v180\n\t" \
  "v_mfma_f32_16x16x32_bf16 v[12:15], a[56:59], v[24:27], v[12:15]\n\t" \
  "v_add_f32 v33, v33, v180\n\t" \
  "v_add_f32 v34, v34, v180\n\t" \
  "v_mfma_f32_16x16x32_bf16 v[12:15], a[60:63], v[28:31], v[12:15]\n\t" \
  "v_add_f32 v35, v35, v180\n\t" \
  "v_max3_f32 v192, v4, v5, v6\n\t" \
  "v_mfma_f32_16x16x32_bf16 v[64:67], a[0:3], v[80:83], 0\n\t" \
  "buffer_load_dwordx4 v224, %[sK], s96 offen offset:0 lds\n\t" \
  "ds_read_b128 a[216:219], v222 offset:20480\n\t" \
  "v_mfma_f32_16x16x32_bf16 v[64:67], a[4:7], v[84:87], v[64:67]\n\t" \
  "ds_read_b128 a[220:223], v223 offset:20480\n\t" \
  "v_max3_f32 v203, v7, v8, v9\n\t" \
  "v_mfma_f32_16x16x32_bf16 v[64:67], a[8:11], v[88:91], v[64:67]\n\t" \
  "v_max3_f32 v192, v192, v10, v11\n\t" \
  "ds_read_b128 a[224:227], v220 offset:24576\n\t" \
  "v_mfma_f32_16x16x32_bf16 v[64:67], a[12:15], v[92:95], v[64:67]\n\t" \
  "buffer_load_dwordx4 v225, %[sK], s96 offen offset:1024 lds\n\t" \
  "ds_read_b128 a[228:231], v221 offset:24576\n\t" \
  "v_mfma_f32_16x16x32_bf16 v[36:39], a[16:19], v[48:51], v[180:183]\n\t" \
  "v_max3_f32 v203, v203, v12, v13\n\t" \
  "v_max3_f32 v192, v192, v14, v15\n\t" \
  "v_mfma_f32_16x16x32_bf16 v[36:39], a[20:23], v[52:55], v[36:39]\n\t" \
  "v_max_f32 v192, v192, v203\n\t" \
  "ds_read_b128 a[232:235], v222 offset:24576\n\t" \
  "v_mfma_f32_16x16x32_bf16 v[36:39], a[24:27], v[56:59], v[36:39]\n\t" \
  "v_cmp_lt_f32 vcc, 0x42800000, v192\n\t" \
  "v_max3_f32 v194, v64, v65, v66\n\t" \
  "v_mfma_f32_16x16x32_bf16 v[36:39], a[28:31], v[60:63], v[36:39]\n\t" \
  "s_cbranch_vccnz .Lsf_c00\n\t" \
  ".Lsf_r00:\n\t" \
  "v_max_f32 v194, v194, v67\n\t" \
  "v_mfma_f32_16x16x32_bf16 v[40:43], a[32:35], v[48:51], v[180:183]\n\t" \
  "v_mov_b32 v195, v194\n\t" \
  "ds_read_b128 a[236:239], v223 offset:24576\n\t" \
  "v_mfma_f32_16x16x32_bf16 v[40:43], a[36:39], v[52:55], v[40:43]\n\t" \
  "v_permlane32_swap_b32 v194, v195\n\t" \
  "v_max_f32 v194, v194, v195\n\t" \
  "v_mfma_f32_16x16x32_bf16 v[40:43], a[40:43], v[56:59], v[40:43]\n\t" \
  "v_mov_b32 v195, v194\n\t" \
  "buffer_load_dwordx4 v226, %[sK], s96 offen offset:2048 lds\n\t" \
  "v_mfma_f32_16x16x32_bf16 v[40:43], a[44:47], v[60:63], v[40:43]\n\t" \
  "v_permlane16_swap_b32 v194, v195\n\t" \
  "v_max_f32 v194, v194, v195\n\t" \
  "v_mfma_f32_16x16x32_bf16 v[44:47], a[48:51], v[48:51], v[180:183]\n\t" \
  "v_sub_f32 v184, 0xc2000000, v194\n\t" \
  "v_sub_f32 v185, 0xc2000000, v194\n\t" \
  "v_mfma_f32_16x16x32_bf16 v[44:47], a[52:55], v[52:55], v[44:47]\n\t" \
  "v_sub_f32 v186, 0xc2000000, v194\n\t" \
  "v_sub_f32 v187, 0xc2000000, v194\n\t" \
  "v_mfma_f32_16x16x32_bf16 v[44:47], a[56:59], v[56:59], v[44:47]\n\t" \
  "v_add_f32 v64, v64, v184\n\t" \
  "v_add_f32 v65, v65, v184\n\t" \
  "v_mfma_f32_16x16x32_bf16 v[44:47], a[60:63], v[60:63], v[44:47]\n\t" \
  "v_add_f32 v66, v66, v184\n\t" \
  "v_add_f32 v67, v67, v184\n\t" \
  "v_mfma_f32_16x16x32_bf16 v[96:99], a[0:3], v[112:115], 0\n\t" \
  "ds_read_b128 a[240:243], v220 offset:28672\n\t" \
  "ds_read_b128 a[244:247], v221 offset:28672\n\t" \
  "v_mfma_f32_16x16x32_bf16 v[96:99], a[4:7], v[116:119], v[96:99]\n\t" \
  "v_max3_f32 v192, v36, v37, v38\n\t" \
  "ds_read_b128 a[248:251], v222 offset:28672\n\t" \
  "v_mfma_f32_16x16x32_bf16 v[96:99], a[8:11], v[120:123], v[96:99]\n\t" \
  "buffer_load_dwordx4 v227, %[sK], s96 offen offset:3072 lds\n\t" \
  "ds_read_b128 a[252:255], v223 offset:28672\n\t" \
  "v_mfma_f32_16x16x32_bf16 v[96:99], a[12:15], v[124:127], v[96:99]\n\t" \
  "v_max3_f32 v203, v39, v40, v41\n\t" \
  "v_max3_f32 v192, v192, v42, v43\n\t" \
  "v_mfma_f32_16x16x32_bf16 v[68:71], a[16:19], v[80:83], v[184:187]\n\t" \
  "ds_read_b64_tr_b16 a[128:129], v228 offset:0\n\t" \
  "v_max3_f32 v203, v203, v44, v45\n\t" \
  "v_mfma_f32_16x16x32_bf16 v[68:71], a[20:23], v[84:87], v[68:71]\n\t" \
  "v_max3_f32 v192, v192, v46, v47\n\t" \
  "v_max_f32 v192, v192, v203\n\t" \
  "v_mfma_f32_16x16x32_bf16 v[68:71], a[24:27], v[88:91], v[68:71]\n\t" \
  "v_cmp_lt_f32 vcc, 0x42800000, v192\n\t" \
  "s_add_u32 m0, %[m], 147456\n\t" \
  "v_mfma_f32_16x16x32_bf16 v[68:71], a[28:31], v[92:95], v[68:71]\n\t" \
  "s_cbranch_vccnz .Lsf_c01\n\t" \
  ".Lsf_r01:\n\t" \
  "ds_read_b64_tr_b16 a[130:131], v228 offset:4096\n\t" \
  "v_mfma_f32_16x16x32_bf16 v[72:75], a[32:35], v[80:83], v[184:187]\n\t" \
  "v_max3_f32 v194, v96, v97, v98\n\t" \
  "v_max_f32 v194, v194, v99\n\t" \
  "v_mfma_f32_16x16x32_bf16 v[72:75], a[36:39], v[84:87], v[72:75]\n\t" \
  "v_mov_b32 v195, v194\n\t" \
  "ds_read_b64_tr_b16 a[132:133], v228 offset:8192\n\t" \
  "v_mfma_f32_16x16x32_bf16 v[72:75], a[40:43], v[88:91], v[72:75]\n\t" \
  "v_permlane32_swap_b32 v194, v195\n\t" \
  "v_max_f32 v194, v194, v195\n\t" \
  "v_mfma_f32_16x16x32_bf16 v[72:75], a[44:47], v[92:95], v[72:75]\n\t" \
  "v_mov_b32 v195, v194\n\t" \
  "s_add_u32 s96, %[sofk], 16384\n\t" \
  "v_mfma_f32_16x16x32_bf16 v[76:79], a[48:51], v[80:83], v[184:187]\n\t" \
  "v_permlane16_swap_b32 v194, v195\n\t" \
  "v_max_f32 v194, v194, v195\n\t" \
  "v_mfma_f32_16x16x32_bf16 v[76:79], a[52:55], v[84:87], v[76:79]\n\t" \
  "v_sub_f32 v188, 0xc2000000, v194\n\t" \
  "v_sub_f32 v189, 0xc2000000, v194\n\t" \
  "v_mfma_f32_16x16x32_bf16 v[76:79], a[56:59], v[88:91], v[76:79]\n\t" \
  "v_sub_f32 v190, 0xc2000000, v194\n\t" \
  "v_sub_f32 v191, 0xc2000000, v194\n\t" \
  "v_mfma_f32_16x16x32_bf16 v[76:79], a[60:63], v[92:95], v[76:79]\n\t" \
  "v_add_f32 v96, v96, v188\n\t" \
  "v_add_f32 v97, v97, v188\n\t" \
  "v_mfma_f32_16x16x32_bf16 v[100:103], a[16:19], v[112:115], v[188:191]\n\t" \
  "v_add_f32 v98, v98, v188\n\t" \
  "v_add_f32 v99, v99, v188\n\t" \
  "v_mfma_f32_16x16x32_bf16 v[100:103], a[20:23], v[116:119], v[100:103]\n\t" \
  "ds_read_b64_tr_b16 a[134:135], v228 offset:12288\n\t" \
  "v_max3_f32 v192, v68, v69, v70\n\t" \
  "v_mfma_f32_16x16x32_bf16 v[100:103], a[24:27], v[120:123], v[100:103]\n\t" \
  "ds_read_b64_tr_b16 a[136:137], v229 offset:0\n\t" \
  "v_max3_f32 v203, v71, v72, v73\n\t" \
  "v_mfma_f32_16x16x32_bf16 v[100:103], a[28:31], v[124:127], v[100:103]\n\t" \
  "v_max3_f32 v192, v192, v74, v75\n\t" \
  "buffer_load_dwordx4 v230, %[sV], s96 offen offset:0 lds\n\t" \
  "v_mfma_f32_16x16x32_bf16 v[104:107], a[32:35], v[112:115], v[188:191]\n\t" \
  "ds_read_b64_tr_b16 a[138:139], v229 offset:4096\n\t" \
  "ds_read_b64_tr_b16 a[140:141], v229 offset:8192\n\t" \
  "v_mfma_f32_16x16x32_bf16 v[104:107], a[36:39], v[116:119], v[104:107]\n\t" \
  "v_max3_f32 v203, v203, v76, v77\n\t" \
  "v_max3_f32 v192, v192, v78, v79\n\t" \
  "v_mfma_f32_16x16x32_bf16 v[104:107], a[40:43], v[120:123], v[104:107]\n\t" \
  "v_max_f32 v192, v192, v203\n\t" \
  "ds_read_b64_tr_b16 a[142:143], v229 offset:12288\n\t" \
  "v_mfma_f32_16x16x32_bf16 v[104:107], a[44:47], v[124:127], v[104:107]\n\t" \
  "v_cmp_lt_f32 vcc, 0x42800000, v192\n\t" \
  "buffer_load_dwordx4 v231, %[sV], s96 offen offset:1024 lds\n\t" \
  "v_mfma_f32_16x16x32_bf16 v[108:111], a[48:51], v[112:115], v[188:191]\n\t" \
  "s_cbranch_vccnz .Lsf_c02\n\t" \
  ".Lsf_r02:\n\t" \
  "v_max3_f32 v192, v100, v101, v102\n\t" \
  "v_mfma_f32_16x16x32_bf16 v[108:111], a[52:55], v[116:119], v[108:111]\n\t" \
  "ds_read_b64_tr_b16 a[144:145], v228 offset:256\n\t" \
  "ds_read_b64_tr_b16 a[146:147], v228 offset:4352\n\t" \
  "v_mfma_f32_16x16x32_bf16 v[108:111], a[56:59], v[120:123], v[108:111]\n\t" \
  "v_max3_f32 v203, v103, v104, v105\n\t" \
  "v_max3_f32 v192, v192, v106, v107\n\t" \
  "v_mfma_f32_16x16x32_bf16 v[108:111], a[60:63], v[124:127], v[108:111]\n\t" \
  "buffer_load_dwordx4 v230, %[sV], s96 offen offset:2048 lds\n\t" \
  "ds_read_b64_tr_b16 a[148:149], v228 offset:8448\n\t" \
  "v_exp_f32 v0, v0\n\t" \
  "v_exp_f32 v1, v1\n\t" \
  "v_exp_f32 v2, v2\n\t" \
  "v_exp_f32 v3, v3\n\t" \
  "v_cvt_pk_bf16_f32 v128, v0, v1\n\t" \
  "v_cvt_pk_bf16_f32 v129, v2, v3\n\t" \
  "s_waitcnt lgkmcnt(11)\n\t" \
  "v_mfma_f32_16x16x32_bf16 v[0:3], a[192:195], v[16:19], v[176:179]\n\t" \
  "v_exp_f32 v4, v4\n\t" \
  "v_mfma_f32_16x16x32_bf16 v[0:3], a[196:199], v[20:23], v[0:3]\n\t" \
  "v_max3_f32 v203, v203, v108, v109\n\t" \
  "v_max3_f32 v192, v192, v110, v111\n\t" \
  "v_mfma_f32_16x16x32_bf16 v[0:3], a[200:203], v[24:27], v[0:3]\n\t" \
  "v_max_f32 v192, v192, v203\n\t" \
  "ds_read_b64_tr_b16 a[150:151], v228 offset:12544\n\t" \
  "v_mfma_f32_16x16x32_bf16 v[0:3], a[204:207], v[28:31], v[0:3]\n\t" \
  "v_cmp_lt_f32 vcc, 0x42800000, v192\n\t" \
  "buffer_load_dwordx4 v231, %[sV], s96 offen offset:3072 lds\n\t" \
  "v_exp_f32 v5, v5\n\t" \
  "v_exp_f32 v6, v6\n\t" \
  "v_exp_f32 v7, v7\n\t" \
  "v_cvt_pk_bf16_f32 v130, v4, v5\n\t" \
  "v_cvt_pk_bf16_f32 v131, v6, v7\n\t" \
  "v_mfma_f32_16x16x32_bf16 v[4:7], a[220:223], v[28:31], v[176:179]\n\t" \
  "s_cbranch_vccnz .Lsf_c03\n\t" \
  ".Lsf_r03:\n\t" \
  "ds_read_b64_tr_b16 a[152:153], v229 offset:256\n\t" \
  "v_mfma_f32_16x16x32_bf16 v[4:7], a[216:219], v[24:27], v[4:7]\n\t" \
  "s_cmp_lg_u32 s99, 0\n\t" \
  "s_cbranch_scc1 .Lsf_p0\n\t" \
  ".Lsf_q0:\n\t" \
  "v_mfma_f32_16x16x32_bf16 v[4:7], a[212:215], v[20:23], v[4:7]\n\t" \
  "v_max3_f32 v192, v0, v1, v2\n\t" \
  "ds_read_b64_tr_b16 a[154:155], v229 offset:4352\n\t" \
  "v_mfma_f32_16x16x32_bf16 v[4:7], a[208:211], v[16:19], v[4:7]\n\t" \
  "v_exp_f32 v8, v8\n\t" \
  "v_exp_f32 v9, v9\n\t" \
  "v_exp_f32 v10, v10\n\t" \
  "v_exp_f32 v11, v11\n\t" \
  "v_cvt_pk_bf16_f32 v132, v8, v9\n\t" \
  "v_cvt_pk_bf16_f32 v133, v10, v11\n\t" \
  "v_mfma_f32_16x16x32_bf16 v[8:11], a[224:227], v[16:19], v[176:179]\n\t" \
  "s_add_u32 s98, %[pf4], 43008\n\t" \
  "buffer_load_dword %[pfd], v245, %[sU], s98 offen\n\t" \
  "v_mfma_f32_16x16x32_bf16 v[8:11], a[228:231], v[20:23], v[8:11]\n\t" \
  "v_max3_f32 v203, v3, v4, v5\n\t" \
  "v_max3_f32 v192, v192, v6, v7\n\t" \
  "v_mfma_f32_16x16x32_bf16 v[8:11], a[232:235], v[24:27], v[8:11]\n\t" \
  "v_exp_f32 v12, v12\n\t" \
  "v_mfma_f32_16x16x32_bf16 v[8:11], a[236:239], v[28:31], v[8:11]\n\t" \
  "ds_read_b64_tr_b16 a[156:157], v229 offset:8448\n\t" \
  "ds_read_b64_tr_b16 a[158:159], v229 offset:12544\n\t" \
  "v_exp_f32 v13, v13\n\t" \
  "v_exp_f32 v14, v14\n\t" \
  "v_exp_f32 v15, v15\n\t" \
  "v_cvt_pk_bf16_f32 v134, v12, v13\n\t" \
  "v_cvt_pk_bf16_f32 v135, v14, v15\n\t" \
  "v_mfma_f32_16x16x32_bf16 v[12:15], a[252:255], v[28:31], v[176:179]\n\t" \
  "v_exp_f32 v32, v32\n\t" \
  "v_mfma_f32_16x16x32_bf16 v[12:15], a[248:251], v[24:27], v[12:15]\n\t" \
  "v_max3_f32 v203, v203, v8, v9\n\t" \
  "v_max3_f32 v192, v192, v10, v11\n\t" \
  "v_mfma_f32_16x16x32_bf16 v[12:15], a[244:247], v[20:23], v[12:15]\n\t" \
  "s_add_u32 s98, %[pf4], 0\n\t" \
  "buffer_load_dword %[pfd], v245, %[sG], s98 offen\n\t" \
  "v_mfma_f32_16x16x32_bf16 v[12:15], a[240:243], v[16:19], v[12:15]\n\t" \
  "v_exp_f32 v33, v33\n\t" \
  "v_exp_f32 v34, v34\n\t" \
  "v_exp_f32 v35, v35\n\t" \
  "v_cvt_pk_bf16_f32 v136, v32, v33\n\t" \
  "v_cvt_pk_bf16_f32 v137, v34, v35\n\t" \
  "v_mfma_f32_16x16x32_bf16 v[32:35], a[192:195], v[48:51], v[180:183]\n\t" \
  "ds_read_b64_tr_b16 a[160:161], v228 offset:512\n\t" \
  "ds_read_b64_tr_b16 a[162:163], v228 offset:4608\n\t" \
  "v_mfma_f32_16x16x32_bf16 v[32:35], a[196:199], v[52:55], v[32:35]\n\t" \
  "v_exp_f32 v36, v36\n\t" \
  "v_mfma_f32_16x16x32_bf16 v[32:35], a[200:203], v[56:59], v[32:35]\n\t" \
  "v_max3_f32 v203, v203, v12, v13\n\t" \
  "v_max3_f32 v192, v192, v14, v15\n\t" \
  "v_mfma_f32_16x16x32_bf16 v[32:35], a[204:207], v[60:63], v[32:35]\n\t" \
  "v_max_f32 v192, v192, v203\n\t" \
  "ds_read_b64_tr_b16 a[164:165], v228 offset:8704\n\t" \
  "v_exp_f32 v37, v37\n\t" \
  "v_exp_f32 v38, v38\n\t" \
  "v_exp_f32 v39, v39\n\t" \
  "v_cvt_pk_bf16_f32 v138, v36, v37\n\t" \
  "v_cvt_pk_bf16_f32 v139, v38, v39\n\t" \
  "v_mfma_f32_16x16x32_bf16 v[36:39], a[220:223], v[60:63], v[180:183]\n\t" \
  "v_cmp_lt_f32 vcc, 0x42800000, v192\n\t" \
  "ds_read_b64_tr_b16 a[166:167], v228 offset:12800\n\t" \
  "v_mfma_f32_16x16x32_bf16 v[36:39], a[216:219], v[56:59], v[36:39]\n\t" \
  "s_cbranch_vccnz .Lsf_c10\n\t" \
  ".Lsf_r10:\n\t" \
  "v_max3_f32 v192, v32, v33, v34\n\t" \
  "v_mfma_f32_16x16x32_bf16 v[36:39], a[212:215], v[52:55], v[36:39]\n\t" \
  "s_add_u32 s98, %[pf4], 14336\n\t" \
  "buffer_load_dword %[pfd], v245, %[sG], s98 offen\n\t" \
  "v_mfma_f32_16x16x32_bf16 v[36:39], a[208:211], v[48:51], v[36:39]\n\t" \
  "v_exp_f32 v40, v40\n\t" \
  "v_exp_f32 v41, v41\n\t" \
  "v_exp_f32 v42, v42\n\t" \
  "v_exp_f32 v43, v43\n\t" \
  "v_cvt_pk_bf16_f32 v140, v40, v41\n\t" \
  "v_cvt_pk_bf16_f32 v141, v42, v43\n\t" \
  "v_mfma_f32_16x16x32_bf16 v[40:43], a[224:227], v[48:51], v[180:183]\n\t" \
  "v_exp_f32 v44, v44\n\t" \
  "v_mfma_f32_16x16x32_bf16 v[40:43], a[228:231], v[52:55], v[40:43]\n\t" \
  "v_max3_f32 v203, v35, v36, v37\n\t" \
  "v_max3_f32 v192, v192, v38, v39\n\t" \
  "v_mfma_f32_16x16x32_bf16 v[40:43], a[232:235], v[56:59], v[40:43]\n\t" \
  "v_exp_f32 v45, v45\n\t" \
  "v_mfma_f32_16x16x32_bf16 v[40:43], a[236:239], v[60:63], v[40:43]\n\t" \
  "v_cvt_pk_bf16_f32 v142, v44, v45\n\t" \
  "ds_read_b64_tr_b16 a[168:169], v229 offset:512\n\t" \
  "v_exp_f32 v46, v46\n\t" \
  "v_exp_f32 v47, v47\n\t" \
  "s_nop 0\n\t" \
  "v_cvt_pk_bf16_f32 v143, v46, v47\n\t" \
  "v_mfma_f32_16x16x32_bf16 v[44:47], a[252:255], v[60:63], v[180:183]\n\t" \
  "v_exp_f32 v64, v64\n\t" \
  "v_mfma_f32_16x16x32_bf16 v[44:47], a[248:251], v[56:59], v[44:47]\n\t" \
  "ds_read_b64_tr_b16 a[170:171], v229 offset:4608\n\t" \
  "v_max3_f32 v203, v203, v40, v41\n\t" \
  "v_mfma_f32_16x16x32_bf16 v[44:47], a[244:247], v[52:55], v[44:47]\n\t" \
  "v_max3_f32 v192, v192, v42, v43\n\t" \
  "ds_read_b64_tr_b16 a[172:173], v229 offset:8704\n\t" \
  "v_mfma_f32_16x16x32_bf16 v[44:47], a[240:243], v[48:51], v[44:47]\n\t" \
  "v_exp_f32 v65, v65\n\t" \
  "v_exp_f32 v66, v66\n\t" \
  "v_exp_f32 v67, v67\n\t" \
  "v_cvt_pk_bf16_f32 v144, v64, v65\n\t" \
  "v_cvt_pk_bf16_f32 v145, v66, v67\n\t" \
  "v_mfma_f32_16x16x32_bf16 v[64:67], a[192:195], v[80:83], v[184:187]\n\t" \
  "v_exp_f32 v68, v68\n\t" \
  "v_mfma_f32_16x16x32_bf16 v[64:67], a[196:199], v[84:87], v[64:67]\n\t" \
  "v_max3_f32 v203, v203, v44, v45\n\t" \
  "v_max3_f32 v192, v192, v46, v47\n\t" \
  "v_mfma_f32_16x16x32_bf16 v[64:67], a[200:203], v[88:91], v[64:67]\n\t" \
  "v_max_f32 v192, v192, v203\n\t" \
  "ds_read_b64_tr_b16 a[174:175], v229 offset:12800\n\t" \
  "v_mfma_f32_16x16x32_bf16 v[64:67], a[204:207], v[92:95], v[64:67]\n\t" \
  "v_cmp_lt_f32 vcc, 0x42800000, v192\n\t" \
  "ds_read_b64_tr_b16 a[176:177], v228 offset:768\n\t" \
  "v_exp_f32 v69, v69\n\t" \
  "v_exp_f32 v70, v70\n\t" \
  "v_exp_f32 v71, v71\n\t" \
  "v_cvt_pk_bf16_f32 v146, v68, v69\n\t" \
  "v_cvt_pk_bf16_f32 v147, v70, v71\n\t" \
  "v_mfma_f32_16x16x32_bf16 v[68:71], a[220:223], v[92:95], v[184:187]\n\t" \
  "s_cbranch_vccnz .Lsf_c11\n\t" \
  ".Lsf_r11:\n\t" \
  "v_max3_f32 v192, v64, v65, v66\n\t" \
  "v_mfma_f32_16x16x32_bf16 v[68:71], a[216:219], v[88:91], v[68:71]\n\t" \
  "v_exp_f32 v72, v72\n\t" \
  "v_mfma_f32_16x16x32_bf16 v[68:71], a[212:215], v[84:87], v[68:71]\n\t" \
  "v_exp_f32 v73, v73\n\t" \
  "v_mfma_f32_16x16x32_bf16 v[68:71], a[208:211], v[80:83], v[68:71]\n\t" \
  "v_cvt_pk_bf16_f32 v148, v72, v73\n\t" \
  "ds_read_b64_tr_b16 a[178:179], v228 offset:4864\n\t" \
  "v_exp_f32 v74, v74\n\t" \
  "v_exp_f32 v75, v75\n\t" \
  "s_nop 0\n\t" \
  "v_cvt_pk_bf16_f32 v149, v74, v75\n\t" \
  "v_mfma_f32_16x16x32_bf16 v[72:75], a[224:227], v[80:83], v[184:187]\n\t" \
  "v_exp_f32 v76, v76\n\t" \
  "v_mfma_f32_16x16x32_bf16 v[72:75], a[228:231], v[84:87], v[72:75]\n\t" \
  "v_max3_f32 v203, v67, v68, v69\n\t" \
  "v_max3_f32 v192, v192, v70, v71\n\t" \
  "v_mfma_f32_16x16x32_bf16 v[72:75], a[232:235], v[88:91], v[72:75]\n\t" \
  "v_exp_f32 v77, v77\n\t" \
  "v_mfma_f32_16x16x32_bf16 v[72:75], a[236:239], v[92:95], v[72:75]\n\t" \
  "v_cvt_pk_bf16_f32 v150, v76, v77\n\t" \
  "ds_read_b64_tr_b16 a[180:181], v228 offset:8960\n\t" \
  "v_exp_f32 v78, v78\n\t" \
  "v_exp_f32 v79, v79\n\t" \
  "s_nop 0\n\t" \
  "v_cvt_pk_bf16_f32 v151, v78, v79\n\t" \
  "v_mfma_f32_16x16x32_bf16 v[76:79], a[252:255], v[92:95], v[184:187]\n\t" \
  "v_exp_f32 v96, v96\n\t" \
  "v_mfma_f32_16x16x32_bf16 v[76:79], a[248:251], v[88:91], v[76:79]\n\t" \
  "ds_read_b64_tr_b16 a[182:183], v228 offset:13056\n\t" \
  "v_max3_f32 v203, v203, v72, v73\n\t" \
  "v_mfma_f32_16x16x32_bf16 v[76:79], a[244:247], v[84:87], v[76:79]\n\t" \
  "v_max3_f32 v192, v192, v74, v75\n\t" \
  "ds_read_b64_tr_b16 a[184:185], v229 offset:768\n\t" \
  "v_mfma_f32_16x16x32_bf16 v[76:79], a[240:243], v[80:83], v[76:79]\n\t" \
  "v_exp_f32 v97, v97\n\t" \
  "v_exp_f32 v98, v98\n\t" \
  "v_exp_f32 v99, v99\n\t" \
  "v_cvt_pk_bf16_f32 v152, v96, v97\n\t" \
  "v_cvt_pk_bf16_f32 v153, v98, v99\n\t" \
  "v_mfma_f32_16x16x32_bf16 v[96:99], a[192:195], v[112:115], v[188:191]\n\t" \
  "v_exp_f32 v100, v100\n\t" \
  "v_mfma_f32_16x16x32_bf16 v[96:99], a[196:199], v[116:119], v[96:99]\n\t" \
  "v_max3_f32 v203, v203, v76, v77\n\t" \
  "v_max3_f32 v192, v192, v78, v79\n\t" \
  "v_mfma_f32_16x16x32_bf16 v[96:99], a[200:203], v[120:123], v[96:99]\n\t" \
  "v_max_f32 v192, v192, v203\n\t" \
  "ds_read_b64_tr_b16 a[186:187], v229 offset:4864\n\t" \
  "v_mfma_f32_16x16x32_bf16 v[96:99], a[204:207], v[124:127], v[96:99]\n\t" \
  "v_cmp_lt_f32 vcc, 0x42800000, v192\n\t" \
  "ds_read_b64_tr_b16 a[188:189], v229 offset:8960\n\t" \
  "v_exp_f32 v101, v101\n\t" \
  "v_exp_f32 v102, v102\n\t" \
  "v_exp_f32 v103, v103\n\t" \
  "v_cvt_pk_bf16_f32 v154, v100, v101\n\t" \
  "v_cvt_pk_bf16_f32 v155, v102, v103\n\t" \
  "v_mfma_f32_16x16x32_bf16 v[100:103], a[220:223], v[124:127], v[188:191]\n\t" \
  "s_cbranch_vccnz .Lsf_c12\n\t" \
  ".Lsf_r12:\n\t" \
  "v_max3_f32 v192, v96, v97, v98\n\t" \
  "v_mfma_f32_16x16x32_bf16 v[100:103], a[216:219], v[120:123], v[100:103]\n\t" \
  "v_exp_f32 v104, v104\n\t" \
  "v_mfma_f32_16x16x32_bf16 v[100:103], a[212:215], v[116:119], v[100:103]\n\t" \
  "v_exp_f32 v105, v105\n\t" \
  "v_mfma_f32_16x16x32_bf16 v[100:103], a[208:211], v[112:115], v[100:103]\n\t" \
  "v_cvt_pk_bf16_f32 v156, v104, v105\n\t" \
  "ds_read_b64_tr_b16 a[190:191], v229 offset:13056\n\t" \
  "v_exp_f32 v106, v106\n\t" \
  "v_exp_f32 v107, v107\n\t" \
  "s_nop 0\n\t" \
  "v_cvt_pk_bf16_f32 v157, v106, v107\n\t" \
  "v_mfma_f32_16x16x32_bf16 v[104:107], a[224:227], v[112:115], v[188:191]\n\t" \
  "v_exp_f32 v108, v108\n\t" \
  "v_mfma_f32_16x16x32_bf16 v[104:107], a[228:231], v[116:119], v[104:107]\n\t" \
  "v_exp_f32 v109, v109\n\t" \
  "v_mfma_f32_16x16x32_bf16 v[104:107], a[232:235], v[120:123], v[104:107]\n\t" \
  "v_cvt_pk_bf16_f32 v158, v108, v109\n\t" \
  "v_max3_f32 v203, v99, v100, v101\n\t" \
  "v_mfma_f32_16x16x32_bf16 v[104:107], a[236:239], v[124:127], v[104:107]\n\t" \
  "v_max3_f32 v192, v192, v102, v103\n\t" \
  "v_exp_f32 v110, v110\n\t" \
  "v_exp_f32 v111, v111\n\t" \
  "s_nop 0\n\t" \
  "v_cvt_pk_bf16_f32 v159, v110, v111\n\t" \
  "v_mfma_f32_16x16x32_bf16 v[108:111], a[252:255], v[124:127], v[188:191]\n\t" \
  "v_exp_f32 v0, v0\n\t" \
  "v_mfma_f32_16x16x32_bf16 v[108:111], a[248:251], v[120:123], v[108:111]\n\t" \
  "v_exp_f32 v1, v1\n\t" \
  "v_mfma_f32_16x16x32_bf16 v[108:111], a[244:247], v[116:119], v[108:111]\n\t" \
  "v_max3_f32 v203, v203, v104, v105\n\t" \
  "v_max3_f32 v192, v192, v106, v107\n\t" \
  "v_mfma_f32_16x16x32_bf16 v[108:111], a[240:243], v[112:115], v[108:111]\n\t" \
  "v_exp_f32 v2, v2\n\t" \
  "s_waitcnt vmcnt(3) lgkmcnt(0)\n\t" \
  "s_barrier\n\t" \
  "v_mfma_f32_16x16x32_bf16 a[0:3], a[128:131], v[128:131], 0\n\t" \
  "v_exp_f32 v3, v3\n\t" \
  "v_mfma_f32_16x16x32_bf16 a[0:3], a[132:135], v[132:135], a[0:3]\n\t" \
  "v_exp_f32 v4, v4\n\t" \
  "v_mfma_f32_16x16x32_bf16 a[16:19], a[136:139], v[128:131], 0\n\t" \
  "ds_read_b128 a[192:195], v220 offset:0\n\t" \
  "v_max3_f32 v203, v203, v108, v109\n\t" \
  "v_mfma_f32_16x16x32_bf16 a[16:19], a[140:143], v[132:135], a[16:19]\n\t" \
  "v_max3_f32 v192, v192, v110, v111\n\t" \
  "v_max_f32 v192, v192, v203\n\t" \
  "v_mfma_f32_16x16x32_bf16 a[32:35], a[144:147], v[128:131], 0\n\t" \
  "v_cmp_lt_f32 vcc, 0x42800000, v192\n\t" \
  "ds_read_b128 a[196:199], v221 offset:0\n\t" \
  "v_mfma_f32_16x16x32_bf16 a[32:35], a[148:151], v[132:135], a[32:35]\n\t" \
  "s_cbranch_vccnz .Lsf_c13\n\t" \
  ".Lsf_r13:\n\t" \
  "ds_read_b128 a[200:203], v222 offset:0\n\t" \
  "v_mfma_f32_16x16x32_bf16 a[48:51], a[152:155], v[128:131], 0\n\t" \
  "v_exp_f32 v5, v5\n\t" \
  "v_mfma_f32_16x16x32_bf16 a[48:51], a[156:159], v[132:135], a[48:51]\n\t" \
  "v_exp_f32 v6, v6\n\t" \
  "v_mfma_f32_16x16x32_bf16 a[64:67], a[160:163], v[128:131], 0\n\t" \
  "v_exp_f32 v7, v7\n\t" \
  "v_mfma_f32_16x16x32_bf16 a[64:67], a[164:167], v[132:135], a[64:67]\n\t" \
  "v_exp_f32 v8, v8\n\t" \
  "v_mfma_f32_16x16x32_bf16 a[80:83], a[168:171], v[128:131], 0\n\t" \
  "v_exp_f32 v9, v9\n\t" \
  "v_mfma_f32_16x16x32_bf16 a[80:83], a[172:175], v[132:135], a[80:83]\n\t" \
  "v_exp_f32 v10, v10\n\t" \
  "v_mfma_f32_16x16x32_bf16 a[96:99], a[176:179], v[128:131], 0\n\t" \
  "v_exp_f32 v11, v11\n\t" \
  "v_mfma_f32_16x16x32_bf16 a[96:99], a[180:183], v[132:135], a[96:99]\n\t" \
  "v_exp_f32 v12, v12\n\t" \
  "v_mfma_f32_16x16x32_bf16 a[112:115], a[184:187], v[128:131], 0\n\t" \
  "ds_read_b128 a[204:207], v223 offset:0\n\t" \
  "ds_read_b128 a[208:211], v220 offset:4096\n\t" \
  "v_mfma_f32_16x16x32_bf16 a[112:115], a[188:191], v[132:135], a[112:115]\n\t" \
  "v_exp_f32 v13, v13\n\t" \
  "v_mfma_f32_16x16x32_bf16 v[160:163], v[216:219], v[128:131], 0\n\t" \
  "v_exp_f32 v14, v14\n\t" \
  "v_mfma_f32_16x16x32_bf16 v[160:163], v[216:219], v[132:135], v[160:163]\n\t" \
  "v_cvt_pk_bf16_f32 v128, v0, v1\n\t" \
  "v_cvt_pk_bf16_f32 v129, v2, v3\n\t" \
  "v_mfma_f32_16x16x32_bf16 a[4:7], a[128:131], v[136:139], 0\n\t" \
  "v_cvt_pk_bf16_f32 v130, v4, v5\n\t" \
  "v_cvt_pk_bf16_f32 v131, v6, v7\n\t" \
  "v_mfma_f32_16x16x32_bf16 a[4:7], a[132:135], v[140:143], a[4:7]\n\t" \
  "v_cvt_pk_bf16_f32 v132, v8, v9\n\t" \
  "v_cvt_pk_bf16_f32 v133, v10, v11\n\t" \
  "v_mfma_f32_16x16x32_bf16 a[20:23], a[136:139], v[136:139], 0\n\t" \
  "v_cvt_pk_bf16_f32 v134, v12, v13\n\t" \
  "ds_read_b128 a[212:215], v221 offset:4096\n\t" \
  "v_mfma_f32_16x16x32_bf16 a[20:23], a[140:143], v[140:143], a[20:23]\n\t" \
  "v_exp_f32 v15, v15\n\t" \
  "v_mfma_f32_16x16x32_bf16 a[36:39], a[144:147], v[136:139], 0\n\t" \
  "v_cvt_pk_bf16_f32 v135, v14, v15\n\t" \
  "s_add_u32 m0, %[m], 65536\n\t" \
  "v_mfma_f32_16x16x32_bf16 a[36:39], a[148:151], v[140:143], a[36:39]\n\t" \
  "v_exp_f32 v32, v32\n\t" \
  "v_mfma_f32_16x16x32_bf16 a[52:55], a[152:155], v[136:139], 0\n\t" \
  "ds_read_b128 a[216:219], v222 offset:4096\n\t" \
  "s_add_u32 s96, %[sofk], 49152\n\t" \
  "v_mfma_f32_16x16x32_bf16 a[52:55], a[156:159], v[140:143], a[52:55]\n\t" \
  "v_exp_f32 v33, v33\n\t" \
  "v_mfma_f32_16x16x32_bf16 a[68:71], a[160:163], v[136:139], 0\n\t" \
  "v_exp_f32 v34, v34\n\t" \
  "v_mfma_f32_16x16x32_bf16 a[68:71], a[164:167], v[140:143], a[68:71]\n\t" \
  "ds_read_b128 a[220:223], v223 offset:4096\n\t" \
  "buffer_load_dwordx4 v224, %[sK], s96 offen offset:0 lds\n\t" \
  "v_mfma_f32_16x16x32_bf16 a[84:87], a[168:171], v[136:139], 0\n\t" \
  "v_exp_f32 v35, v35\n\t" \
  "v_mfma_f32_16x16x32_bf16 a[84:87], a[172:175], v[140:143], a[84:87]\n\t" \
  "v_exp_f32 v36, v36\n\t" \
  "v_mfma_f32_16x16x32_bf16 a[100:103], a[176:179], v[136:139], 0\n\t" \
  "v_exp_f32 v37, v37\n\t" \
  "v_mfma_f32_16x16x32_bf16 a[100:103], a[180:183], v[140:143], a[100:103]\n\t" \
  "ds_read_b128 a[224:227], v220 offset:8192\n\t" \
  "buffer_load_dwordx4 v225, %[sK], s96 offen offset:1024 lds\n\t" \
  "v_mfma_f32_16x16x32_bf16 a[116:119], a[184:187], v[136:139], 0\n\t" \
  "v_exp_f32 v38, v38\n\t" \
  "v_mfma_f32_16x16x32_bf16 a[116:119], a[188:191], v[140:143], a[116:119]\n\t" \
  "v_exp_f32 v39, v39\n\t" \
  "v_mfma_f32_16x16x32_bf16 v[164:167], v[216:219], v[136:139], 0\n\t" \
  "buffer_load_dwordx4 v226, %[sK], s96 offen offset:2048 lds\n\t" \
  "ds_read_b128 a[228:231], v221 offset:8192\n\t" \
  "v_mfma_f32_16x16x32_bf16 v[164:167], v[216:219], v[140:143], v[164:167]\n\t" \
  "v_cvt_pk_bf16_f32 v136, v32, v33\n\t" \
  "v_cvt_pk_bf16_f32 v137, v34, v35\n\t" \
  "v_mfma_f32_16x16x32_bf16 a[8:11], a[128:131], v[144:147], 0\n\t" \
  "v_cvt_pk_bf16_f32 v138, v36, v37\n\t" \
  "v_cvt_pk_bf16_f32 v139, v38, v39\n\t" \
  "v_mfma_f32_16x16x32_bf16 a[8:11], a[132:135], v[148:151], a[8:11]\n\t" \
  "v_exp_f32 v40, v40\n\t" \
  "v_mfma_f32_16x16x32_bf16 a[24:27], a[136:139], v[144:147], 0\n\t" \
  "buffer_load_dwordx4 v227, %[sK], s96 offen offset:3072 lds\n\t" \
  "ds_read_b128 a[232:235], v222 offset:8192\n\t" \
  "v_mfma_f32_16x16x32_bf16 a[24:27], a[140:143], v[148:151], a[24:27]\n\t" \
  "v_exp_f32 v41, v41\n\t" \
  "v_mfma_f32_16x16x32_bf16 a[40:43], a[144:147], v[144:147], 0\n\t" \
  "v_cvt_pk_bf16_f32 v140, v40, v41\n\t" \
  "ds_read_b128 a[236:239], v223 offset:8192\n\t" \
  "v_mfma_f32_16x16x32_bf16 a[40:43], a[148:151], v[148:151], a[40:43]\n\t" \
  "v_exp_f32 v42, v42\n\t" \
  "v_mfma_f32_16x16x32_bf16 a[56:59], a[152:155], v[144:147], 0\n\t" \
  "v_exp_f32 v43, v43\n\t" \
  "v_mfma_f32_16x16x32_bf16 a[56:59], a[156:159], v[148:151], a[56:59]\n\t" \
  "v_cvt_pk_bf16_f32 v141, v42, v43\n\t" \
  "ds_read_b128 a[240:243], v220 offset:12288\n\t" \
  "v_mfma_f32_16x16x32_bf16 a[72:75], a[160:163], v[144:147], 0\n\t" \
  "s_add_u32 m0, %[m], 114688\n\t" \
  "s_mov_b32 s96, 0\n\t" \
  "buffer_load_dwordx4 v232, %[sU], s96 offen offset:0 lds\n\t" \
  "v_mfma_f32_16x16x32_bf16 a[72:75], a[164:167], v[148:151], a[72:75]\n\t" \
  "v_exp_f32 v44, v44\n\t" \
  "v_mfma_f32_16x16x32_bf16 a[88:91], a[168:171], v[144:147], 0\n\t" \
  "s_add_u32 m0, %[m], 81920\n\t" \
  "s_add_u32 s96, %[sofk], 32768\n\t" \
  "v_mfma_f32_16x16x32_bf16 a[88:91], a[172:175], v[148:151], a[88:91]\n\t" \
  "v_exp_f32 v45, v45\n\t" \
  "v_mfma_f32_16x16x32_bf16 a[104:107], a[176:179], v[144:147], 0\n\t" \
  "v_cvt_pk_bf16_f32 v142, v44, v45\n\t" \
  "ds_read_b128 a[244:247], v221 offset:12288\n\t" \
  "v_mfma_f32_16x16x32_bf16 a[104:107], a[180:183], v[148:151], a[104:107]\n\t" \
  "v_exp_f32 v46, v46\n\t" \
  "v_mfma_f32_16x16x32_bf16 a[120:123], a[184:187], v[144:147], 0\n\t" \
  "v_exp_f32 v47, v47\n\t" \
  "v_mfma_f32_16x16x32_bf16 a[120:123], a[188:191], v[148:151], a[120:123]\n\t" \
  "v_cvt_pk_bf16_f32 v143, v46, v47\n\t" \
  "buffer_load_dwordx4 v230, %[sV], s96 offen offset:0 lds\n\t" \
  "v_mfma_f32_16x16x32_bf16 v[168:171], v[216:219], v[144:147], 0\n\t" \
  "v_exp_f32 v64, v64\n\t" \
  "v_mfma_f32_16x16x32_bf16 v[168:171], v[216:219], v[148:151], v[168:171]\n\t" \
  "v_exp_f32 v65, v65\n\t" \
  "v_mfma_f32_16x16x32_bf16 a[12:15], a[128:131], v[152:155], 0\n\t" \
  "v_cvt_pk_bf16_f32 v144, v64, v65\n\t" \
  "ds_read_b128 a[248:251], v222 offset:12288\n\t" \
  "v_mfma_f32_16x16x32_bf16 a[12:15], a[132:135], v[156:159], a[12:15]\n\t" \
  "v_exp_f32 v66, v66\n\t" \
  "v_mfma_f32_16x16x32_bf16 a[28:31], a[136:139], v[152:155], 0\n\t" \
  "v_exp_f32 v67, v67\n\t" \
  "v_mfma_f32_16x16x32_bf16 a[28:31], a[140:143], v[156:159], a[28:31]\n\t" \
  "v_cvt_pk_bf16_f32 v145, v66, v67\n\t" \
  "buffer_load_dwordx4 v231, %[sV], s96 offen offset:1024 lds\n\t" \
  "v_mfma_f32_16x16x32_bf16 a[44:47], a[144:147], v[152:155], 0\n\t" \
  "ds_read_b128 a[252:255], v223 offset:12288\n\t" \
  "buffer_load_dwordx4 v230, %[sV], s96 offen offset:2048 lds\n\t" \
  "v_mfma_f32_16x16x32_bf16 a[44:47], a[148:151], v[156:159], a[44:47]\n\t" \
  "v_exp_f32 v68, v68\n\t" \
  "v_mfma_f32_16x16x32_bf16 a[60:63], a[152:155], v[152:155], 0\n\t" \
  "v_exp_f32 v69, v69\n\t" \
  "v_mfma_f32_16x16x32_bf16 a[60:63], a[156:159], v[156:159], a[60:63]\n\t" \
  "v_cvt_pk_bf16_f32 v146, v68, v69\n\t" \
  "buffer_load_dwordx4 v231, %[sV], s96 offen offset:3072 lds\n\t" \
  "v_mfma_f32_16x16x32_bf16 a[76:79], a[160:163], v[152:155], 0\n\t" \
  "v_exp_f32 v70, v70\n\t" \
  "v_mfma_f32_16x16x32_bf16 a[76:79], a[164:167], v[156:159], a[76:79]\n\t" \
  "v_exp_f32 v71, v71\n\t" \
  "v_mfma_f32_16x16x32_bf16 a[92:95], a[168:171], v[152:155], 0\n\t" \
  "v_cvt_pk_bf16_f32 v147, v70, v71\n\t" \
  "v_mfma_f32_16x16x32_bf16 a[92:95], a[172:175], v[156:159], a[92:95]\n\t" \
  "v_exp_f32 v72, v72\n\t" \
  "v_mfma_f32_16x16x32_bf16 a[108:111], a[176:179], v[152:155], 0\n\t" \
  "v_exp_f32 v73, v73\n\t" \
  "v_mfma_f32_16x16x32_bf16 a[108:111], a[180:183], v[156:159], a[108:111]\n\t" \
  "v_cvt_pk_bf16_f32 v148, v72, v73\n\t" \
  "v_mfma_f32_16x16x32_bf16 a[124:127], a[184:187], v[152:155], 0\n\t" \
  "s_add_u32 m0, %[m], 114688\n\t" \
  "s_mov_b32 s96, 6144\n\t" \
  "buffer_load_dwordx4 v232, %[sU], s96 offen offset:1024 lds\n\t" \
  "v_mfma_f32_16x16x32_bf16 a[124:127], a[188:191], v[156:159], a[124:127]\n\t" \
  "v_exp_f32 v74, v74\n\t" \
  "v_mfma_f32_16x16x32_bf16 v[172:175], v[216:219], v[152:155], 0\n\t" \
  "v_exp_f32 v75, v75\n\t" \
  "v_mfma_f32_16x16x32_bf16 v[172:175], v[216:219], v[156:159], v[172:175]\n\t" \
  "v_cvt_pk_bf16_f32 v149, v74, v75\n\t" \
  "s_cmp_lg_u32 s99, 0\n\t" \
  "s_cbranch_scc1 .Lsf_p1\n\t" \
  ".Lsf_q1:\n\t" \
  "s_waitcnt lgkmcnt(0)\n\t" \
  "v_mfma_f32_16x16x32_bf16 v[0:3], a[192:195], v[16:19], v[176:179]\n\t" \
  "v_exp_f32 v76, v76\n\t" \
  "v_mfma_f32_16x16x32_bf16 v[0:3], a[196:199], v[20:23], v[0:3]\n\t" \
  "v_exp_f32 v77, v77\n\t" \
  "v_mfma_f32_16x16x32_bf16 v[0:3], a[200:203], v[24:27], v[0:3]\n\t" \
  "v_cvt_pk_bf16_f32 v150, v76, v77\n\t" \
  "ds_read_b64_tr_b16 a[128:129], v237 offset:0\n\t" \
  "v_mfma_f32_16x16x32_bf16 v[0:3], a[204:207], v[28:31], v[0:3]\n\t" \
  "v_exp_f32 v78, v78\n\t" \
  "v_mfma_f32_16x16x32_bf16 v[4:7], a[220:223], v[28:31], v[176:179]\n\t" \
  "ds_read_b64_tr_b16 a[130:131], v237 offset:4096\n\t" \
  "ds_read_b64_tr_b16 a[132:133], v237 offset:8192\n\t" \
  "v_mfma_f32_16x16x32_bf16 v[4:7], a[216:219], v[24:27], v[4:7]\n\t" \
  "v_exp_f32 v79, v79\n\t" \
  "v_mfma_f32_16x16x32_bf16 v[4:7], a[212:215], v[20:23], v[4:7]\n\t" \
  "v_cvt_pk_bf16_f32 v151, v78, v79\n\t" \
  "v_max3_f32 v192, v0, v1, v2\n\t" \
  "v_mfma_f32_16x16x32_bf16 v[4:7], a[208:211], v[16:19], v[4:7]\n\t" \
  "v_exp_f32 v96, v96\n\t" \
  "v_mfma_f32_16x16x32_bf16 v[8:11], a[224:227], v[16:19], v[176:179]\n\t" \
  "ds_read_b64_tr_b16 a[134:135], v237 offset:12288\n\t" \
  "ds_read_b64_tr_b16 a[136:137], v238 offset:0\n\t" \
  "v_mfma_f32_16x16x32_bf16 v[8:11], a[228:231], v[20:23], v[8:11]\n\t" \
  "s_add_u32 m0, %[m], 114688\n\t" \
  "s_mov_b32 s96, 12288\n\t" \
  "buffer_load_dwordx4 v232, %[sU], s96 offen offset:2048 lds\n\t" \
  "v_mfma_f32_16x16x32_bf16 v[8:11], a[232:235], v[24:27], v[8:11]\n\t" \
  "v_max3_f32 v203, v3, v4, v5\n\t" \
  "v_max3_f32 v192, v192, v6, v7\n\t" \
  "v_mfma_f32_16x16x32_bf16 v[8:11], a[236:239], v[28:31], v[8:11]\n\t" \
  "ds_read_b64_tr_b16 a[138:139], v238 offset:4096\n\t" \
  "ds_read_b64_tr_b16 a[140:141], v238 offset:8192\n\t" \
  "v_mfma_f32_16x16x32_bf16 v[12:15], a[252:255], v[28:31], v[176:179]\n\t" \
  "v_exp_f32 v97, v97\n\t" \
  "v_mfma_f32_16x16x32_bf16 v[12:15], a[248:251], v[24:27], v[12:15]\n\t" \
  "v_cvt_pk_bf16_f32 v152, v96, v97\n\t" \
  "ds_read_b64_tr_b16 a[142:143], v238 offset:12288\n\t" \
  "v_mfma_f32_16x16x32_bf16 v[12:15], a[244:247], v[20:23], v[12:15]\n\t" \
  "v_max3_f32 v203, v203, v8, v9\n\t" \
  "v_max3_f32 v192, v192, v10, v11\n\t" \
  "v_mfma_f32_16x16x32_bf16 v[12:15], a[240:243], v[16:19], v[12:15]\n\t" \
  "v_exp_f32 v98, v98\n\t" \
  "v_mfma_f32_16x16x32_bf16 v[32:35], a[192:195], v[48:51], v[180:183]\n\t" \
  "ds_read_b64_tr_b16 a[144:145], v237 offset:256\n\t" \
  "ds_read_b64_tr_b16 a[146:147], v237 offset:4352\n\t" \
  "v_mfma_f32_16x16x32_bf16 v[32:35], a[196:199], v[52:55], v[32:35]\n\t" \
  "v_exp_f32 v99, v99\n\t" \
  "v_mfma_f32_16x16x32_bf16 v[32:35], a[200:203], v[56:59], v[32:35]\n\t" \
  "v_cvt_pk_bf16_f32 v153, v98, v99\n\t" \
  "v_max3_f32 v203, v203, v12, v13\n\t" \
  "v_mfma_f32_16x16x32_bf16 v[32:35], a[204:207], v[60:63], v[32:35]\n\t" \
  "v_max3_f32 v192, v192, v14, v15\n\t" \
  "v_max_f32 v192, v192, v203\n\t" \
  "v_mfma_f32_16x16x32_bf16 v[36:39], a[220:223], v[60:63], v[180:183]\n\t" \
  "v_cmp_lt_f32 vcc, 0x42800000, v192\n\t" \
  "ds_read_b64_tr_b16 a[148:149], v237 offset:8448\n\t" \
  "v_mfma_f32_16x16x32_bf16 v[36:39], a[216:219], v[56:59], v[36:39]\n\t" \
  "s_cbranch_vccnz .Lsf_c20\n\t" \
  ".Lsf_r20:\n\t" \
  "ds_read_b64_tr_b16 a[150:151], v237 offset:12544\n\t" \
  "v_mfma_f32_16x16x32_bf16 v[36:39], a[212:215], v[52:55], v[36:39]\n\t" \
  "v_max3_f32 v192, v32, v33, v34\n\t" \
  "ds_read_b64_tr_b16 a[152:153], v238 offset:256\n\t" \
  "v_mfma_f32_16x16x32_bf16 v[36:39], a[208:211], v[48:51], v[36:39]\n\t" \
  "s_mov_b32 s96, 18432\n\t" \
  "buffer_load_dwordx4 v232, %[sU], s96 offen offset:3072 lds\n\t" \
  "v_mfma_f32_16x16x32_bf16 v[40:43], a[224:227], v[48:51], v[180:183]\n\t" \
  "v_exp_f32 v100, v100\n\t" \
  "v_mfma_f32_16x16x32_bf16 v[40:43], a[228:231], v[52:55], v[40:43]\n\t" \
  "ds_read_b64_tr_b16 a[154:155], v238 offset:4352\n\t" \
  "ds_read_b64_tr_b16 a[156:157], v238 offset:8448\n\t" \
  "v_mfma_f32_16x16x32_bf16 v[40:43], a[232:235], v[56:59], v[40:43]\n\t" \
  "v_max3_f32 v203, v35, v36, v37\n\t" \
  "v_max3_f32 v192, v192, v38, v39\n\t" \
  "v_mfma_f32_16x16x32_bf16 v[40:43], a[236:239], v[60:63], v[40:43]\n\t" \
  "v_exp_f32 v101, v101\n\t" \
  "v_mfma_f32_16x16x32_bf16 v[44:47], a[252:255], v[60:63], v[180:183]\n\t" \
  "v_cvt_pk_bf16_f32 v154, v100, v101\n\t" \
  "ds_read_b64_tr_b16 a[158:159], v238 offset:12544\n\t" \
  "v_mfma_f32_16x16x32_bf16 v[44:47], a[248:251], v[56:59], v[44:47]\n\t" \
  "v_exp_f32 v102, v102\n\t" \
  "v_mfma_f32_16x16x32_bf16 v[44:47], a[244:247], v[52:55], v[44:47]\n\t" \
  "ds_read_b64_tr_b16 a[160:161], v237 offset:512\n\t" \
  "v_max3_f32 v203, v203, v40, v41\n\t" \
  "v_mfma_f32_16x16x32_bf16 v[44:47], a[240:243], v[48:51], v[44:47]\n\t" \
  "v_max3_f32 v192, v192, v42, v43\n\t" \
  "ds_read_b64_tr_b16 a[162:163], v237 offset:4608\n\t" \
  "v_mfma_f32_16x16x32_bf16 v[64:67], a[192:195], v[80:83], v[184:187]\n\t" \
  "v_exp_f32 v103, v103\n\t" \
  "v_mfma_f32_16x16x32_bf16 v[64:67], a[196:199], v[84:87], v[64:67]\n\t" \
  "v_cvt_pk_bf16_f32 v155, v102, v103\n\t" \
  "ds_read_b64_tr_b16 a[164:165], v237 offset:8704\n\t" \
  "v_mfma_f32_16x16x32_bf16 v[64:67], a[200:203], v[88:91], v[64:67]\n\t" \
  "v_max3_f32 v203, v203, v44, v45\n\t" \
  "v_max3_f32 v192, v192, v46, v47\n\t" \
  "v_mfma_f32_16x16x32_bf16 v[64:67], a[204:207], v[92:95], v[64:67]\n\t" \
  "v_max_f32 v192, v192, v203\n\t" \
  "ds_read_b64_tr_b16 a[166:167], v237 offset:12800\n\t" \
  "v_mfma_f32_16x16x32_bf16 v[68:71], a[220:223], v[92:95], v[184:187]\n\t" \
  "v_cmp_lt_f32 vcc, 0x42800000, v192\n\t" \
  "ds_read_b64_tr_b16 a[168:169], v238 offset:512\n\t" \
  "v_mfma_f32_16x16x32_bf16 v[68:71], a[216:219], v[88:91], v[68:71]\n\t" \
  "s_cbranch_vccnz .Lsf_c21\n\t" \
  ".Lsf_r21:\n\t" \
  "ds_read_b64_tr_b16 a[170:171], v238 offset:4608\n\t" \
  "v_mfma_f32_16x16x32_bf16 v[68:71], a[212:215], v[84:87], v[68:71]\n\t" \
  "s_add_u32 m0, %[m], 131072\n\t" \
  "s_mov_b32 s96, 28672\n\t" \
  "buffer_load_dwordx4 v232, %[sU], s96 offen offset:0 lds\n\t" \
  "v_mfma_f32_16x16x32_bf16 v[68:71], a[208:211], v[80:83], v[68:71]\n\t" \
  "v_max3_f32 v192, v64, v65, v66\n\t" \
  "ds_read_b64_tr_b16 a[172:173], v238 offset:8704\n\t" \
  "v_mfma_f32_16x16x32_bf16 v[72:75], a[224:227], v[80:83], v[184:187]\n\t" \
  "v_exp_f32 v104, v104\n\t" \
  "v_mfma_f32_16x16x32_bf16 v[72:75], a[228:231], v[84:87], v[72:75]\n\t" \
  "v_exp_f32 v105, v105\n\t" \
  "v_mfma_f32_16x16x32_bf16 v[72:75], a[232:235], v[88:91], v[72:75]\n\t" \
  "v_cvt_pk_bf16_f32 v156, v104, v105\n\t" \
  "v_max3_f32 v203, v67, v68, v69\n\t" \
  "v_mfma_f32_16x16x32_bf16 v[72:75], a[236:239], v[92:95], v[72:75]\n\t" \
  "v_max3_f32 v192, v192, v70, v71\n\t" \
  "ds_read_b64_tr_b16 a[174:175], v238 offset:12800\n\t" \
  "v_mfma_f32_16x16x32_bf16 v[76:79], a[252:255], v[92:95], v[184:187]\n\t" \
  "ds_read_b64_tr_b16 a[176:177], v237 offset:768\n\t" \
  "ds_read_b64_tr_b16 a[178:179], v237 offset:4864\n\t" \
  "v_mfma_f32_16x16x32_bf16 v[76:79], a[248:251], v[88:91], v[76:79]\n\t" \
  "v_exp_f32 v106, v106\n\t" \
  "v_mfma_f32_16x16x32_bf16 v[76:79], a[244:247], v[84:87], v[76:79]\n\t" \
  "v_max3_f32 v203, v203, v72, v73\n\t" \
  "v_max3_f32 v192, v192, v74, v75\n\t" \
  "v_mfma_f32_16x16x32_bf16 v[76:79], a[240:243], v[80:83], v[76:79]\n\t" \
  "ds_read_b64_tr_b16 a[180:181], v237 offset:8960\n\t" \
  "ds_read_b64_tr_b16 a[182:183], v237 offset:13056\n\t" \
  "v_mfma_f32_16x16x32_bf16 v[96:99], a[192:195], v[112:115], v[188:191]\n\t" \
  "v_exp_f32 v107, v107\n\t" \
  "v_mfma_f32_16x16x32_bf16 v[96:99], a[196:199], v[116:119], v[96:99]\n\t" \
  "v_cvt_pk_bf16_f32 v157, v106, v107\n\t" \
  "ds_read_b64_tr_b16 a[184:185], v238 offset:768\n\t" \
  "v_mfma_f32_16x16x32_bf16 v[96:99], a[200:203], v[120:123], v[96:99]\n\t" \
  "s_mov_b32 s96, 34816\n\t" \
  "buffer_load_dwordx4 v232, %[sU], s96 offen offset:1024 lds\n\t" \
  "v_mfma_f32_16x16x32_bf16 v[96:99], a[204:207], v[124:127], v[96:99]\n\t" \
  "v_max3_f32 v203, v203, v76, v77\n\t" \
  "v_max3_f32 v192, v192, v78, v79\n\t" \
  "v_mfma_f32_16x16x32_bf16 v[100:103], a[220:223], v[124:127], v[188:191]\n\t" \
  "v_max_f32 v192, v192, v203\n\t" \
  "ds_read_b64_tr_b16 a[186:187], v238 offset:4864\n\t" \
  "v_mfma_f32_16x16x32_bf16 v[100:103], a[216:219], v[120:123], v[100:103]\n\t" \
  "v_cmp_lt_f32 vcc, 0x42800000, v192\n\t" \
  "ds_read_b64_tr_b16 a[188:189], v238 offset:8960\n\t" \
  "v_mfma_f32_16x16x32_bf16 v[100:103], a[212:215], v[116:119], v[100:103]\n\t" \
  "s_cbranch_vccnz .Lsf_c22\n\t" \
  ".Lsf_r22:\n\t" \
  "v_max3_f32 v192, v96, v97, v98\n\t" \
  "v_mfma_f32_16x16x32_bf16 v[100:103], a[208:211], v[112:115], v[100:103]\n\t" \
  "v_exp_f32 v108, v108\n\t" \
  "v_mfma_f32_16x16x32_bf16 v[104:107], a[224:227], v[112:115], v[188:191]\n\t" \
  "v_exp_f32 v109, v109\n\t" \
  "v_mfma_f32_16x16x32_bf16 v[104:107], a[228:231], v[116:119], v[104:107]\n\t" \
  "v_cvt_pk_bf16_f32 v158, v108, v109\n\t" \
  "ds_read_b64_tr_b16 a[190:191], v238 offset:13056\n\t" \
  "v_mfma_f32_16x16x32_bf16 v[104:107], a[232:235], v[120:123], v[104:107]\n\t" \
  "v_exp_f32 v110, v110\n\t" \
  "v_mfma_f32_16x16x32_bf16 v[104:107], a[236:239], v[124:127], v[104:107]\n\t" \
  "v_max3_f32 v203, v99, v100, v101\n\t" \
  "v_max3_f32 v192, v192, v102, v103\n\t" \
  "v_exp_f32 v111, v111\n\t" \
  "s_nop 0\n\t" \
  "v_cvt_pk_bf16_f32 v159, v110, v111\n\t" \
  "v_mfma_f32_16x16x32_bf16 v[108:111], a[252:255], v[124:127], v[188:191]\n\t" \
  "v_exp_f32 v0, v0\n\t" \
  "v_mfma_f32_16x16x32_bf16 v[108:111], a[248:251], v[120:123], v[108:111]\n\t" \
  "v_exp_f32 v1, v1\n\t" \
  "v_mfma_f32_16x16x32_bf16 v[108:111], a[244:247], v[116:119], v[108:111]\n\t" \
  "v_max3_f32 v203, v203, v104, v105\n\t" \
  "v_max3_f32 v192, v192, v106, v107\n\t" \
  "v_mfma_f32_16x16x32_bf16 v[108:111], a[240:243], v[112:115], v[108:111]\n\t" \
  "v_exp_f32 v2, v2\n\t" \
  "s_waitcnt vmcnt(5) lgkmcnt(0)\n\t" \
  "s_barrier\n\t" \
  "v_mfma_f32_16x16x32_bf16 a[0:3], a[128:131], v[128:131], a[0:3]\n\t" \
  "v_exp_f32 v3, v3\n\t" \
  "v_mfma_f32_16x16x32_bf16 a[0:3], a[132:135], v[132:135], a[0:3]\n\t" \
  "v_exp_f32 v4, v4\n\t" \
  "v_mfma_f32_16x16x32_bf16 a[16:19], a[136:139], v[128:131], a[16:19]\n\t" \
  "ds_read_b128 a[192:195], v220 offset:16384\n\t" \
  "v_max3_f32 v203, v203, v108, v109\n\t" \
  "v_mfma_f32_16x16x32_bf16 a[16:19], a[140:143], v[132:135], a[16:19]\n\t" \
  "v_max3_f32 v192, v192, v110, v111\n\t" \
  "v_max_f32 v192, v192, v203\n\t" \
  "v_mfma_f32_16x16x32_bf16 a[32:35], a[144:147], v[128:131], a[32:35]\n\t" \
  "v_cmp_lt_f32 vcc, 0x42800000, v192\n\t" \
  "ds_read_b128 a[196:199], v221 offset:16384\n\t" \
  "v_mfma_f32_16x16x32_bf16 a[32:35], a[148:151], v[132:135], a[32:35]\n\t" \
  "s_cbranch_vccnz .Lsf_c23\n\t" \
  ".Lsf_r23:\n\t" \
  "ds_read_b128 a[200:203], v222 offset:16384\n\t" \
  "v_mfma_f32_16x16x32_bf16 a[48:51], a[152:155], v[128:131], a[48:51]\n\t" \
  "v_exp_f32 v5, v5\n\t" \
  "v_mfma_f32_16x16x32_bf16 a[48:51], a[156:159], v[132:135], a[48:51]\n\t" \
  "v_exp_f32 v6, v6\n\t" \
  "v_mfma_f32_16x16x32_bf16 a[64:67], a[160:163], v[128:131], a[64:67]\n\t" \
  "v_exp_f32 v7, v7\n\t" \
  "v_mfma_f32_16x16x32_bf16 a[64:67], a[164:167], v[132:135], a[64:67]\n\t" \
  "v_exp_f32 v8, v8\n\t" \
  "v_mfma_f32_16x16x32_bf16 a[80:83], a[168:171], v[128:131], a[80:83]\n\t" \
  "v_exp_f32 v9, v9\n\t" \
  "v_mfma_f32_16x16x32_bf16 a[80:83], a[172:175], v[132:135], a[80:83]\n\t" \
  "v_exp_f32 v10, v10\n\t" \
  "v_mfma_f32_16x16x32_bf16 a[96:99], a[176:179], v[128:131], a[96:99]\n\t" \
  "v_exp_f32 v11, v11\n\t" \
  "v_mfma_f32_16x16x32_bf16 a[96:99], a[180:183], v[132:135], a[96:99]\n\t" \
  "v_exp_f32 v12, v12\n\t" \
  "v_mfma_f32_16x16x32_bf16 a[112:115], a[184:187], v[128:131], a[112:115]\n\t" \
  "ds_read_b128 a[204:207], v223 offset:16384\n\t" \
  "ds_read_b128 a[208:211], v220 offset:20480\n\t" \
  "v_mfma_f32_16x16x32_bf16 a[112:115], a[188:191], v[132:135], a[112:115]\n\t" \
  "v_exp_f32 v13, v13\n\t" \
  "v_mfma_f32_16x16x32_bf16 v[160:163], v[216:219], v[128:131], v[160:163]\n\t" \
  "v_exp_f32 v14, v14\n\t" \
  "v_mfma_f32_16x16x32_bf16 v[160:163], v[216:219], v[132:135], v[160:163]\n\t" \
  "v_cvt_pk_bf16_f32 v128, v0, v1\n\t" \
  "v_cvt_pk_bf16_f32 v129, v2, v3\n\t" \
  "v_mfma_f32_16x16x32_bf16 a[4:7], a[128:131], v[136:139], a[4:7]\n\t" \
  "v_cvt_pk_bf16_f32 v130, v4, v5\n\t" \
  "v_cvt_pk_bf16_f32 v131, v6, v7\n\t" \
  "v_mfma_f32_16x16x32_bf16 a[4:7], a[132:135], v[140:143], a[4:7]\n\t" \
  "v_cvt_pk_bf16_f32 v132, v8, v9\n\t" \
  "v_cvt_pk_bf16_f32 v133, v10, v11\n\t" \
  "v_mfma_f32_16x16x32_bf16 a[20:23], a[136:139], v[136:139], a[20:23]\n\t" \
  "v_cvt_pk_bf16_f32 v134, v12, v13\n\t" \
  "ds_read_b128 a[212:215], v221 offset:20480\n\t" \
  "v_mfma_f32_16x16x32_bf16 a[20:23], a[140:143], v[140:143], a[20:23]\n\t" \
  "v_exp_f32 v15, v15\n\t" \
  "v_mfma_f32_16x16x32_bf16 a[36:39], a[144:147], v[136:139], a[36:39]\n\t" \
  "v_cvt_pk_bf16_f32 v135, v14, v15\n\t" \
  "s_add_u32 m0, %[m], 49152\n\t" \
  "v_mfma_f32_16x16x32_bf16 a[36:39], a[148:151], v[140:143], a[36:39]\n\t" \
  "v_exp_f32 v32, v32\n\t" \
  "v_mfma_f32_16x16x32_bf16 a[52:55], a[152:155], v[136:139], a[52:55]\n\t" \
  "ds_read_b128 a[216:219], v222 offset:20480\n\t" \
  "s_add_u32 s96, %[sofk], 65536\n\t" \
  "v_mfma_f32_16x16x32_bf16 a[52:55], a[156:159], v[140:143], a[52:55]\n\t" \
  "v_exp_f32 v33, v33\n\t" \
  "v_mfma_f32_16x16x32_bf16 a[68:71], a[160:163], v[136:139], a[68:71]\n\t" \
  "v_exp_f32 v34, v34\n\t" \
  "v_mfma_f32_16x16x32_bf16 a[68:71], a[164:167], v[140:143], a[68:71]\n\t" \
  "ds_read_b128 a[220:223], v223 offset:20480\n\t" \
  "buffer_load_dwordx4 v224, %[sK], s96 offen offset:0 lds\n\t" \
  "v_mfma_f32_16x16x32_bf16 a[84:87], a[168:171], v[136:139], a[84:87]\n\t" \
  "v_exp_f32 v35, v35\n\t" \
  "v_mfma_f32_16x16x32_bf16 a[84:87], a[172:175], v[140:143], a[84:87]\n\t" \
  "v_exp_f32 v36, v36\n\t" \
  "v_mfma_f32_16x16x32_bf16 a[100:103], a[176:179], v[136:139], a[100:103]\n\t" \
  "v_exp_f32 v37, v37\n\t" \
  "v_mfma_f32_16x16x32_bf16 a[100:103], a[180:183], v[140:143], a[100:103]\n\t" \
  "ds_read_b128 a[224:227], v220 offset:24576\n\t" \
  "buffer_load_dwordx4 v225, %[sK], s96 offen offset:1024 lds\n\t" \
  "v_mfma_f32_16x16x32_bf16 a[116:119], a[184:187], v[136:139], a[116:119]\n\t" \
  "v_exp_f32 v38, v38\n\t" \
  "v_mfma_f32_16x16x32_bf16 a[116:119], a[188:191], v[140:143], a[116:119]\n\t" \
  "v_exp_f32 v39, v39\n\t" \
  "v_mfma_f32_16x16x32_bf16 v[164:167], v[216:219], v[136:139], v[164:167]\n\t" \
  "buffer_load_dwordx4 v226, %[sK], s96 offen offset:2048 lds\n\t" \
  "ds_read_b128 a[228:231], v221 offset:24576\n\t" \
  "v_mfma_f32_16x16x32_bf16 v[164:167], v[216:219], v[140:143], v[164:167]\n\t" \
  "v_cvt_pk_bf16_f32 v136, v32, v33\n\t" \
  "v_cvt_pk_bf16_f32 v137, v34, v35\n\t" \
  "v_mfma_f32_16x16x32_bf16 a[8:11], a[128:131], v[144:147], a[8:11]\n\t" \
  "v_cvt_pk_bf16_f32 v138, v36, v37\n\t" \
  "v_cvt_pk_bf16_f32 v139, v38, v39\n\t" \
  "v_mfma_f32_16x16x32_bf16 a[8:11], a[132:135], v[148:151], a[8:11]\n\t" \
  "v_exp_f32 v40, v40\n\t" \
  "v_mfma_f32_16x16x32_bf16 a[24:27], a[136:139], v[144:147], a[24:27]\n\t" \
  "buffer_load_dwordx4 v227, %[sK], s96 offen offset:3072 lds\n\t" \
  "ds_read_b128 a[232:235], v222 offset:24576\n\t" \
  "v_mfma_f32_16x16x32_bf16 a[24:27], a[140:143], v[148:151], a[24:27]\n\t" \
  "v_exp_f32 v41, v41\n\t" \
  "v_mfma_f32_16x16x32_bf16 a[40:43], a[144:147], v[144:147], a[40:43]\n\t" \
  "v_cvt_pk_bf16_f32 v140, v40, v41\n\t" \
  "ds_read_b128 a[236:239], v223 offset:24576\n\t" \
  "v_mfma_f32_16x16x32_bf16 a[40:43], a[148:151], v[148:151], a[40:43]\n\t" \
  "v_exp_f32 v42, v42\n\t" \
  "v_mfma_f32_16x16x32_bf16 a[56:59], a[152:155], v[144:147], a[56:59]\n\t" \
  "v_exp_f32 v43, v43\n\t" \
  "v_mfma_f32_16x16x32_bf16 a[56:59], a[156:159], v[148:151], a[56:59]\n\t" \
  "v_cvt_pk_bf16_f32 v141, v42, v43\n\t" \
  "ds_read_b128 a[240:243], v220 offset:28672\n\t" \
  "v_mfma_f32_16x16x32_bf16 a[72:75], a[160:163], v[144:147], a[72:75]\n\t" \
  "s_add_u32 m0, %[m], 131072\n\t" \
  "s_mov_b32 s96, 40960\n\t" \
  "buffer_load_dwordx4 v232, %[sU], s96 offen offset:2048 lds\n\t" \
  "v_mfma_f32_16x16x32_bf16 a[72:75], a[164:167], v[148:151], a[72:75]\n\t" \
  "v_exp_f32 v44, v44\n\t" \
  "v_mfma_f32_16x16x32_bf16 a[88:91], a[168:171], v[144:147], a[88:91]\n\t" \
  "s_add_u32 m0, %[m], 147456\n\t" \
  "s_add_u32 s96, %[sofk], 49152\n\t" \
  "v_mfma_f32_16x16x32_bf16 a[88:91], a[172:175], v[148:151], a[88:91]\n\t" \
  "v_exp_f32 v45, v45\n\t" \
  "v_mfma_f32_16x16x32_bf16 a[104:107], a[176:179], v[144:147], a[104:107]\n\t" \
  "v_cvt_pk_bf16_f32 v142, v44, v45\n\t" \
  "ds_read_b128 a[244:247], v221 offset:28672\n\t" \
  "v_mfma_f32_16x16x32_bf16 a[104:107], a[180:183], v[148:151], a[104:107]\n\t" \
  "v_exp_f32 v46, v46\n\t" \
  "v_mfma_f32_16x16x32_bf16 a[120:123], a[184:187], v[144:147], a[120:123]\n\t" \
  "v_exp_f32 v47, v47\n\t" \
  "v_mfma_f32_16x16x32_bf16 a[120:123], a[188:191], v[148:151], a[120:123]\n\t" \
  "v_cvt_pk_bf16_f32 v143, v46, v47\n\t" \
  "buffer_load_dwordx4 v230, %[sV], s96 offen offset:0 lds\n\t" \
  "v_mfma_f32_16x16x32_bf16 v[168:171], v[216:219], v[144:147], v[168:171]\n\t" \
  "v_exp_f32 v64, v64\n\t" \
  "v_mfma_f32_16x16x32_bf16 v[168:171], v[216:219], v[148:151], v[168:171]\n\t" \
  "v_exp_f32 v65, v65\n\t" \
  "v_mfma_f32_16x16x32_bf16 a[12:15], a[128:131], v[152:155], a[12:15]\n\t" \
  "v_cvt_pk_bf16_f32 v144, v64, v65\n\t" \
  "ds_read_b128 a[248:251], v222 offset:28672\n\t" \
  "v_mfma_f32_16x16x32_bf16 a[12:15], a[132:135], v[156:159], a[12:15]\n\t" \
  "v_exp_f32 v66, v66\n\t" \
  "v_mfma_f32_16x16x32_bf16 a[28:31], a[136:139], v[152:155], a[28:31]\n\t" \
  "v_exp_f32 v67, v67\n\t" \
  "v_mfma_f32_16x16x32_bf16 a[28:31], a[140:143], v[156:159], a[28:31]\n\t" \
  "v_cvt_pk_bf16_f32 v145, v66, v67\n\t" \
  "buffer_load_dwordx4 v231, %[sV], s96 offen offset:1024 lds\n\t" \
  "v_mfma_f32_16x16x32_bf16 a[44:47], a[144:147], v[152:155], a[44:47]\n\t" \
  "ds_read_b128 a[252:255], v223 offset:28672\n\t" \
  "buffer_load_dwordx4 v230, %[sV], s96 offen offset:2048 lds\n\t" \
  "v_mfma_f32_16x16x32_bf16 a[44:47], a[148:151], v[156:159], a[44:47]\n\t" \
  "v_exp_f32 v68, v68\n\t" \
  "v_mfma_f32_16x16x32_bf16 a[60:63], a[152:155], v[152:155], a[60:63]\n\t" \
  "v_exp_f32 v69, v69\n\t" \
  "v_mfma_f32_16x16x32_bf16 a[60:63], a[156:159], v[156:159], a[60:63]\n\t" \
  "v_cvt_pk_bf16_f32 v146, v68, v69\n\t" \
  "buffer_load_dwordx4 v231, %[sV], s96 offen offset:3072 lds\n\t" \
  "v_mfma_f32_16x16x32_bf16 a[76:79], a[160:163], v[152:155], a[76:79]\n\t" \
  "v_exp_f32 v70, v70\n\t" \
  "v_mfma_f32_16x16x32_bf16 a[76:79], a[164:167], v[156:159], a[76:79]\n\t" \
  "v_exp_f32 v71, v71\n\t" \
  "v_mfma_f32_16x16x32_bf16 a[92:95], a[168:171], v[152:155], a[92:95]\n\t" \
  "v_cvt_pk_bf16_f32 v147, v70, v71\n\t" \
  "v_mfma_f32_16x16x32_bf16 a[92:95], a[172:175], v[156:159], a[92:95]\n\t" \
  "v_exp_f32 v72, v72\n\t" \
  "v_mfma_f32_16x16x32_bf16 a[108:111], a[176:179], v[152:155], a[108:111]\n\t" \
  "v_exp_f32 v73, v73\n\t" \
  "v_mfma_f32_16x16x32_bf16 a[108:111], a[180:183], v[156:159], a[108:111]\n\t" \
  "v_cvt_pk_bf16_f32 v148, v72, v73\n\t" \
  "v_mfma_f32_16x16x32_bf16 a[124:127], a[184:187], v[152:155], a[124:127]\n\t" \
  "s_add_u32 m0, %[m], 131072\n\t" \
  "s_mov_b32 s96, 47104\n\t" \
  "buffer_load_dwordx4 v232, %[sU], s96 offen offset:3072 lds\n\t" \
  "v_mfma_f32_16x16x32_bf16 a[124:127], a[188:191], v[156:159], a[124:127]\n\t" \
  "v_exp_f32 v74, v74\n\t" \
  "v_mfma_f32_16x16x32_bf16 v[172:175], v[216:219], v[152:155], v[172:175]\n\t" \
  "v_exp_f32 v75, v75\n\t" \
  "v_mfma_f32_16x16x32_bf16 v[172:175], v[216:219], v[156:159], v[172:175]\n\t" \
  "v_cvt_pk_bf16_f32 v149, v74, v75\n\t" \
  "s_cmp_lg_u32 s99, 0\n\t" \
  "s_cbranch_scc1 .Lsf_p2\n\t" \
  ".Lsf_q2:\n\t" \
  "s_waitcnt lgkmcnt(0)\n\t" \
  "v_mfma_f32_16x16x32_bf16 v[0:3], a[192:195], v[16:19], v[176:179]\n\t" \
  "s_mul_i32 s98, %[sofk], 5\n\t" \
  "s_xor_b32 s98, s98, 8192\n\t" \
  "s_lshr_b32 s98, s98, 1\n\t" \
  "buffer_load_dword %[pfd], v204, %[sD], s98 offen\n\t" \
  "v_mfma_f32_16x16x32_bf16 v[0:3], a[196:199], v[20:23], v[0:3]\n\t" \
  "v_exp_f32 v76, v76\n\t" \
  "v_mfma_f32_16x16x32_bf16 v[0:3], a[200:203], v[24:27], v[0:3]\n\t" \
  "ds_read_b64_tr_b16 a[128:129], v228 offset:0\n\t" \
  "ds_read_b64_tr_b16 a[130:131], v228 offset:4096\n\t" \
  "v_mfma_f32_16x16x32_bf16 v[0:3], a[204:207], v[28:31], v[0:3]\n\t" \
  "v_exp_f32 v77, v77\n\t" \
  "v_mfma_f32_16x16x32_bf16 v[4:7], a[220:223], v[28:31], v[176:179]\n\t" \
  "v_cvt_pk_bf16_f32 v150, v76, v77\n\t" \
  "ds_read_b64_tr_b16 a[132:133], v228 offset:8192\n\t" \
  "v_mfma_f32_16x16x32_bf16 v[4:7], a[216:219], v[24:27], v[4:7]\n\t" \
  "v_exp_f32 v78, v78\n\t" \
  "v_mfma_f32_16x16x32_bf16 v[4:7], a[212:215], v[20:23], v[4:7]\n\t" \
  "v_exp_f32 v79, v79\n\t" \
  "v_mfma_f32_16x16x32_bf16 v[4:7], a[208:211], v[16:19], v[4:7]\n\t" \
  "v_cvt_pk_bf16_f32 v151, v78, v79\n\t" \
  "v_max3_f32 v192, v0, v1, v2\n\t" \
  "v_mfma_f32_16x16x32_bf16 v[8:11], a[224:227], v[16:19], v[176:179]\n\t" \
  "ds_read_b64_tr_b16 a[134:135], v228 offset:12288\n\t" \
  "ds_read_b64_tr_b16 a[136:137], v229 offset:0\n\t" \
  "v_mfma_f32_16x16x32_bf16 v[8:11], a[228:231], v[20:23], v[8:11]\n\t" \
  "v_exp_f32 v96, v96\n\t" \
  "v_mfma_f32_16x16x32_bf16 v[8:11], a[232:235], v[24:27], v[8:11]\n\t" \
  "v_max3_f32 v203, v3, v4, v5\n\t" \
  "v_max3_f32 v192, v192, v6, v7\n\t" \
  "v_mfma_f32_16x16x32_bf16 v[8:11], a[236:239], v[28:31], v[8:11]\n\t" \
  "ds_read_b64_tr_b16 a[138:139], v229 offset:4096\n\t" \
  "ds_read_b64_tr_b16 a[140:141], v229 offset:8192\n\t" \
  "v_mfma_f32_16x16x32_bf16 v[12:15], a[252:255], v[28:31], v[176:179]\n\t" \
  "v_exp_f32 v97, v97\n\t" \
  "v_mfma_f32_16x16x32_bf16 v[12:15], a[248:251], v[24:27], v[12:15]\n\t" \
  "v_cvt_pk_bf16_f32 v152, v96, v97\n\t" \
  "ds_read_b64_tr_b16 a[142:143], v229 offset:12288\n\t" \
  "v_mfma_f32_16x16x32_bf16 v[12:15], a[244:247], v[20:23], v[12:15]\n\t" \
  "v_max3_f32 v203, v203, v8, v9\n\t" \
  "v_max3_f32 v192, v192, v10, v11\n\t" \
  "v_mfma_f32_16x16x32_bf16 v[12:15], a[240:243], v[16:19], v[12:15]\n\t" \
  "s_mul_i32 s98, %[sofk], 5\n\t" \
  "s_xor_b32 s98, s98, 12288\n\t" \
  "s_lshr_b32 s98, s98, 1\n\t" \
  "buffer_load_dword %[pfd], v204, %[sD], s98 offen\n\t" \
  "v_mfma_f32_16x16x32_bf16 v[32:35], a[192:195], v[48:51], v[180:183]\n\t" \
  "ds_read_b64_tr_b16 a[144:145], v228 offset:256\n\t" \
  "ds_read_b64_tr_b16 a[146:147], v228 offset:4352\n\t" \
  "v_mfma_f32_16x16x32_bf16 v[32:35], a[196:199], v[52:55], v[32:35]\n\t" \
  "v_exp_f32 v98, v98\n\t" \
  "v_mfma_f32_16x16x32_bf16 v[32:35], a[200:203], v[56:59], v[32:35]\n\t" \
  "v_max3_f32 v203, v203, v12, v13\n\t" \
  "v_max3_f32 v192, v192, v14, v15\n\t" \
  "v_mfma_f32_16x16x32_bf16 v[32:35], a[204:207], v[60:63], v[32:35]\n\t" \
  "v_max_f32 v192, v192, v203\n\t" \
  "ds_read_b64_tr_b16 a[148:149], v228 offset:8448\n\t" \
  "v_mfma_f32_16x16x32_bf16 v[36:39], a[220:223], v[60:63], v[180:183]\n\t" \
  "v_cmp_lt_f32 vcc, 0x42800000, v192\n\t" \
  "ds_read_b64_tr_b16 a[150:151], v228 offset:12544\n\t" \
  "v_mfma_f32_16x16x32_bf16 v[36:39], a[216:219], v[56:59], v[36:39]\n\t" \
  "s_cbranch_vccnz .Lsf_c30\n\t" \
  ".Lsf_r30:\n\t" \
  "ds_read_b64_tr_b16 a[152:153], v229 offset:256\n\t" \
  "v_mfma_f32_16x16x32_bf16 v[36:39], a[212:215], v[52:55], v[36:39]\n\t" \
  "v_max3_f32 v192, v32, v33, v34\n\t" \
  "ds_read_b64_tr_b16 a[154:155], v229 offset:4352\n\t" \
  "v_mfma_f32_16x16x32_bf16 v[36:39], a[208:211], v[48:51], v[36:39]\n\t" \
  "v_exp_f32 v99, v99\n\t" \
  "v_mfma_f32_16x16x32_bf16 v[40:43], a[224:227], v[48:51], v[180:183]\n\t" \
  "v_cvt_pk_bf16_f32 v153, v98, v99\n\t" \
  "ds_read_b64_tr_b16 a[156:157], v229 offset:8448\n\t" \
  "v_mfma_f32_16x16x32_bf16 v[40:43], a[228:231], v[52:55], v[40:43]\n\t" \
  "v_exp_f32 v100, v100\n\t" \
  "v_mfma_f32_16x16x32_bf16 v[40:43], a[232:235], v[56:59], v[40:43]\n\t" \
  "v_exp_f32 v101, v101\n\t" \
  "v_mfma_f32_16x16x32_bf16 v[40:43], a[236:239], v[60:63], v[40:43]\n\t" \
  "v_cvt_pk_bf16_f32 v154, v100, v101\n\t" \
  "v_max3_f32 v203, v35, v36, v37\n\t" \
  "v_mfma_f32_16x16x32_bf16 v[44:47], a[252:255], v[60:63], v[180:183]\n\t" \
  "v_max3_f32 v192, v192, v38, v39\n\t" \
  "ds_read_b64_tr_b16 a[158:159], v229 offset:12544\n\t" \
  "v_mfma_f32_16x16x32_bf16 v[44:47], a[248:251], v[56:59], v[44:47]\n\t" \
  "v_exp_f32 v102, v102\n\t" \
  "v_mfma_f32_16x16x32_bf16 v[44:47], a[244:247], v[52:55], v[44:47]\n\t" \
  "ds_read_b64_tr_b16 a[160:161], v228 offset:512\n\t" \
  "v_max3_f32 v203, v203, v40, v41\n\t" \
  "v_mfma_f32_16x16x32_bf16 v[44:47], a[240:243], v[48:51], v[44:47]\n\t" \
  "v_max3_f32 v192, v192, v42, v43\n\t" \
  "ds_read_b64_tr_b16 a[162:163], v228 offset:4608\n\t" \
  "v_mfma_f32_16x16x32_bf16 v[64:67], a[192:195], v[80:83], v[184:187]\n\t" \
  "v_exp_f32 v103, v103\n\t" \
  "v_mfma_f32_16x16x32_bf16 v[64:67], a[196:199], v[84:87], v[64:67]\n\t" \
  "v_cvt_pk_bf16_f32 v155, v102, v103\n\t" \
  "ds_read_b64_tr_b16 a[164:165], v228 offset:8704\n\t" \
  "v_mfma_f32_16x16x32_bf16 v[64:67], a[200:203], v[88:91], v[64:67]\n\t" \
  "v_max3_f32 v203, v203, v44, v45\n\t" \
  "v_max3_f32 v192, v192, v46, v47\n\t" \
  "v_mfma_f32_16x16x32_bf16 v[64:67], a[204:207], v[92:95], v[64:67]\n\t" \
  "v_max_f32 v192, v192, v203\n\t" \
  "ds_read_b64_tr_b16 a[166:167], v228 offset:12800\n\t" \
  "v_mfma_f32_16x16x32_bf16 v[68:71], a[220:223], v[92:95], v[184:187]\n\t" \
  "v_cmp_lt_f32 vcc, 0x42800000, v192\n\t" \
  "ds_read_b64_tr_b16 a[168:169], v229 offset:512\n\t" \
  "v_mfma_f32_16x16x32_bf16 v[68:71], a[216:219], v[88:91], v[68:71]\n\t" \
  "s_cbranch_vccnz .Lsf_c31\n\t" \
  ".Lsf_r31:\n\t" \
  "ds_read_b64_tr_b16 a[170:171], v229 offset:4608\n\t" \
  "v_mfma_f32_16x16x32_bf16 v[68:71], a[212:215], v[84:87], v[68:71]\n\t" \
  "v_max3_f32 v192, v64, v65, v66\n\t" \
  "ds_read_b64_tr_b16 a[172:173], v229 offset:8704\n\t" \
  "v_mfma_f32_16x16x32_bf16 v[68:71], a[208:211], v[80:83], v[68:71]\n\t" \
  "v_exp_f32 v104, v104\n\t" \
  "v_mfma_f32_16x16x32_bf16 v[72:75], a[224:227], v[80:83], v[184:187]\n\t" \
  "v_exp_f32 v105, v105\n\t" \
  "v_mfma_f32_16x16x32_bf16 v[72:75], a[228:231], v[84:87], v[72:75]\n\t" \
  "v_cvt_pk_bf16_f32 v156, v104, v105\n\t" \
  "ds_read_b64_tr_b16 a[174:175], v229 offset:12800\n\t" \
  "v_mfma_f32_16x16x32_bf16 v[72:75], a[232:235], v[88:91], v[72:75]\n\t" \
  "v_exp_f32 v106, v106\n\t" \
  "v_mfma_f32_16x16x32_bf16 v[72:75], a[236:239], v[92:95], v[72:75]\n\t" \
  "v_max3_f32 v203, v67, v68, v69\n\t" \
  "v_max3_f32 v192, v192, v70, v71\n\t" \
  "v_mfma_f32_16x16x32_bf16 v[76:79], a[252:255], v[92:95], v[184:187]\n\t" \
  "ds_read_b64_tr_b16 a[176:177], v228 offset:768\n\t" \
  "ds_read_b64_tr_b16 a[178:179], v228 offset:4864\n\t" \
  "v_mfma_f32_16x16x32_bf16 v[76:79], a[248:251], v[88:91], v[76:79]\n\t" \
  "v_exp_f32 v107, v107\n\t" \
  "v_mfma_f32_16x16x32_bf16 v[76:79], a[244:247], v[84:87], v[76:79]\n\t" \
  "v_cvt_pk_bf16_f32 v157, v106, v107\n\t" \
  "v_max3_f32 v203, v203, v72, v73\n\t" \
  "v_mfma_f32_16x16x32_bf16 v[76:79], a[240:243], v[80:83], v[76:79]\n\t" \
  "v_max3_f32 v192, v192, v74, v75\n\t" \
  "ds_read_b64_tr_b16 a[180:181], v228 offset:8960\n\t" \
  "v_mfma_f32_16x16x32_bf16 v[96:99], a[192:195], v[112:115], v[188:191]\n\t" \
  "v_exp_f32 v108, v108\n\t" \
  "v_mfma_f32_16x16x32_bf16 v[96:99], a[196:199], v[116:119], v[96:99]\n\t" \
  "v_exp_f32 v109, v109\n\t" \
  "v_mfma_f32_16x16x32_bf16 v[96:99], a[200:203], v[120:123], v[96:99]\n\t" \
  "v_cvt_pk_bf16_f32 v158, v108, v109\n\t" \
  "ds_read_b64_tr_b16 a[182:183], v228 offset:13056\n\t" \
  "v_mfma_f32_16x16x32_bf16 v[96:99], a[204:207], v[124:127], v[96:99]\n\t" \
  "v_max3_f32 v203, v203, v76, v77\n\t" \
  "v_max3_f32 v192, v192, v78, v79\n\t" \
  "v_mfma_f32_16x16x32_bf16 v[100:103], a[220:223], v[124:127], v[188:191]\n\t" \
  "v_max_f32 v192, v192, v203\n\t" \
  "ds_read_b64_tr_b16 a[184:185], v229 offset:768\n\t" \
  "v_mfma_f32_16x16x32_bf16 v[100:103], a[216:219], v[120:123], v[100:103]\n\t" \
  "v_cmp_lt_f32 vcc, 0x42800000, v192\n\t" \
  "ds_read_b64_tr_b16 a[186:187], v229 offset:4864\n\t" \
  "v_mfma_f32_16x16x32_bf16 v[100:103], a[212:215], v[116:119], v[100:103]\n\t" \
  "s_cbranch_vccnz .Lsf_c32\n\t" \
  ".Lsf_r32:\n\t" \
  "v_max3_f32 v192, v96, v97, v98\n\t" \
  "v_mfma_f32_16x16x32_bf16 v[100:103], a[208:211], v[112:115], v[100:103]\n\t" \
  "ds_read_b64_tr_b16 a[188:189], v229 offset:8960\n\t" \
  "ds_read_b64_tr_b16 a[190:191], v229 offset:13056\n\t" \
  "v_mfma_f32_16x16x32_bf16 v[104:107], a[224:227], v[112:115], v[188:191]\n\t" \
  "v_exp_f32 v110, v110\n\t" \
  "v_mfma_f32_16x16x32_bf16 v[104:107], a[228:231], v[116:119], v[104:107]\n\t" \
  "v_exp_f32 v111, v111\n\t" \
  "v_mfma_f32_16x16x32_bf16 v[104:107], a[232:235], v[120:123], v[104:107]\n\t" \
  "v_cvt_pk_bf16_f32 v159, v110, v111\n\t" \
  "v_max3_f32 v203, v99, v100, v101\n\t" \
  "v_mfma_f32_16x16x32_bf16 v[104:107], a[236:239], v[124:127], v[104:107]\n\t" \
  "v_max3_f32 v192, v192, v102, v103\n\t" \
  "v_mfma_f32_16x16x32_bf16 v[108:111], a[252:255], v[124:127], v[188:191]\n\t" \
  "v_exp_f32 v0, v0\n\t" \
  "v_mfma_f32_16x16x32_bf16 v[108:111], a[248:251], v[120:123], v[108:111]\n\t" \
  "v_exp_f32 v1, v1\n\t" \
  "v_mfma_f32_16x16x32_bf16 v[108:111], a[244:247], v[116:119], v[108:111]\n\t" \
  "v_exp_f32 v2, v2\n\t" \
  "v_mfma_f32_16x16x32_bf16 v[108:111], a[240:243], v[112:115], v[108:111]\n\t" \
  "v_max3_f32 v203, v203, v104, v105\n\t" \
  "v_max3_f32 v192, v192, v106, v107\n\t" \
  "s_waitcnt vmcnt(3) lgkmcnt(0)\n\t" \
  "s_barrier\n\t" \
  "v_mfma_f32_16x16x32_bf16 a[0:3], a[128:131], v[128:131], a[0:3]\n\t" \
  "v_exp_f32 v3, v3\n\t" \
  "v_mfma_f32_16x16x32_bf16 a[0:3], a[132:135], v[132:135], a[0:3]\n\t" \
  "v_exp_f32 v4, v4\n\t" \
  "v_mfma_f32_16x16x32_bf16 a[16:19], a[136:139], v[128:131], a[16:19]\n\t" \
  "ds_read_b128 a[192:195], v220 offset:0\n\t" \
  "v_max3_f32 v203, v203, v108, v109\n\t" \
  "v_mfma_f32_16x16x32_bf16 a[16:19], a[140:143], v[132:135], a[16:19]\n\t" \
  "v_max3_f32 v192, v192, v110, v111\n\t" \
  "v_max_f32 v192, v192, v203\n\t" \
  "v_mfma_f32_16x16x32_bf16 a[32:35], a[144:147], v[128:131], a[32:35]\n\t" \
  "v_cmp_lt_f32 vcc, 0x42800000, v192\n\t" \
  "ds_read_b128 a[196:199], v221 offset:0\n\t" \
  "v_mfma_f32_16x16x32_bf16 a[32:35], a[148:151], v[132:135], a[32:35]\n\t" \
  "s_cbranch_vccnz .Lsf_c33\n\t" \
  ".Lsf_r33:\n\t" \
  "ds_read_b128 a[200:203], v222 offset:0\n\t" \
  "v_mfma_f32_16x16x32_bf16 a[48:51], a[152:155], v[128:131], a[48:51]\n\t" \
  "v_exp_f32 v5, v5\n\t" \
  "v_mfma_f32_16x16x32_bf16 a[48:51], a[156:159], v[132:135], a[48:51]\n\t" \
  "v_exp_f32 v6, v6\n\t" \
  "v_mfma_f32_16x16x32_bf16 a[64:67], a[160:163], v[128:131], a[64:67]\n\t" \
  "v_exp_f32 v7, v7\n\t" \
  "v_mfma_f32_16x16x32_bf16 a[64:67], a[164:167], v[132:135], a[64:67]\n\t" \
  "v_exp_f32 v8, v8\n\t" \
  "v_mfma_f32_16x16x32_bf16 a[80:83], a[168:171], v[128:131], a[80:83]\n\t" \
  "v_exp_f32 v9, v9\n\t" \
  "v_mfma_f32_16x16x32_bf16 a[80:83], a[172:175], v[132:135], a[80:83]\n\t" \
  "v_exp_f32 v10, v10\n\t" \
  "v_mfma_f32_16x16x32_bf16 a[96:99], a[176:179], v[128:131], a[96:99]\n\t" \
  "v_exp_f32 v11, v11\n\t" \
  "v_mfma_f32_16x16x32_bf16 a[96:99], a[180:183], v[132:135], a[96:99]\n\t" \
  "v_exp_f32 v12, v12\n\t" \
  "v_mfma_f32_16x16x32_bf16 a[112:115], a[184:187], v[128:131], a[112:115]\n\t" \
  "ds_read_b128 a[204:207], v223 offset:0\n\t" \
  "ds_read_b128 a[208:211], v220 offset:4096\n\t" \
  "v_mfma_f32_16x16x32_bf16 a[112:115], a[188:191], v[132:135], a[112:115]\n\t" \
  "v_exp_f32 v13, v13\n\t" \
  "v_mfma_f32_16x16x32_bf16 v[160:163], v[216:219], v[128:131], v[160:163]\n\t" \
  "v_exp_f32 v14, v14\n\t" \
  "v_mfma_f32_16x16x32_bf16 v[160:163], v[216:219], v[132:135], v[160:163]\n\t" \
  "v_cvt_pk_bf16_f32 v128, v0, v1\n\t" \
  "v_cvt_pk_bf16_f32 v129, v2, v3\n\t" \
  "v_mfma_f32_16x16x32_bf16 a[4:7], a[128:131], v[136:139], a[4:7]\n\t" \
  "v_cvt_pk_bf16_f32 v130, v4, v5\n\t" \
  "v_cvt_pk_bf16_f32 v131, v6, v7\n\t" \
  "v_mfma_f32_16x16x32_bf16 a[4:7], a[132:135], v[140:143], a[4:7]\n\t" \
  "v_cvt_pk_bf16_f32 v132, v8, v9\n\t" \
  "v_cvt_pk_bf16_f32 v133, v10, v11\n\t" \
  "v_mfma_f32_16x16x32_bf16 a[20:23], a[136:139], v[136:139], a[20:23]\n\t" \
  "v_cvt_pk_bf16_f32 v134, v12, v13\n\t" \
  "ds_read_b128 a[212:215], v221 offset:4096\n\t" \
  "v_mfma_f32_16x16x32_bf16 a[20:23], a[140:143], v[140:143], a[20:23]\n\t" \
  "v_exp_f32 v15, v15\n\t" \
  "v_mfma_f32_16x16x32_bf16 a[36:39], a[144:147], v[136:139], a[36:39]\n\t" \
  "v_cvt_pk_bf16_f32 v135, v14, v15\n\t" \
  "s_add_u32 m0, %[m], 65536\n\t" \
  "v_mfma_f32_16x16x32_bf16 a[36:39], a[148:151], v[140:143], a[36:39]\n\t" \
  "v_exp_f32 v32, v32\n\t" \
  "v_mfma_f32_16x16x32_bf16 a[52:55], a[152:155], v[136:139], a[52:55]\n\t" \
  "ds_read_b128 a[216:219], v222 offset:4096\n\t" \
  "s_add_u32 s96, %[sofk], 81920\n\t" \
  "v_mfma_f32_16x16x32_bf16 a[52:55], a[156:159], v[140:143], a[52:55]\n\t" \
  "v_exp_f32 v33, v33\n\t" \
  "v_mfma_f32_16x16x32_bf16 a[68:71], a[160:163], v[136:139], a[68:71]\n\t" \
  "v_exp_f32 v34, v34\n\t" \
  "v_mfma_f32_16x16x32_bf16 a[68:71], a[164:167], v[140:143], a[68:71]\n\t" \
  "ds_read_b128 a[220:223], v223 offset:4096\n\t" \
  "buffer_load_dwordx4 v224, %[sK], s96 offen offset:0 lds\n\t" \
  "v_mfma_f32_16x16x32_bf16 a[84:87], a[168:171], v[136:139], a[84:87]\n\t" \
  "v_exp_f32 v35, v35\n\t" \
  "v_mfma_f32_16x16x32_bf16 a[84:87], a[172:175], v[140:143], a[84:87]\n\t" \
  "v_exp_f32 v36, v36\n\t" \
  "v_mfma_f32_16x16x32_bf16 a[100:103], a[176:179], v[136:139], a[100:103]\n\t" \
  "v_exp_f32 v37, v37\n\t" \
  "v_mfma_f32_16x16x32_bf16 a[100:103], a[180:183], v[140:143], a[100:103]\n\t" \
  "ds_read_b128 a[224:227], v220 offset:8192\n\t" \
  "buffer_load_dwordx4 v225, %[sK], s96 offen offset:1024 lds\n\t" \
  "v_mfma_f32_16x16x32_bf16 a[116:119], a[184:187], v[136:139], a[116:119]\n\t" \
  "v_exp_f32 v38, v38\n\t" \
  "v_mfma_f32_16x16x32_bf16 a[116:119], a[188:191], v[140:143], a[116:119]\n\t" \
  "v_exp_f32 v39, v39\n\t" \
  "v_mfma_f32_16x16x32_bf16 v[164:167], v[216:219], v[136:139], v[164:167]\n\t" \
  "buffer_load_dwordx4 v226, %[sK], s96 offen offset:2048 lds\n\t" \
  "ds_read_b128 a[228:231], v221 offset:8192\n\t" \
  "v_mfma_f32_16x16x32_bf16 v[164:167], v[216:219], v[140:143], v[164:167]\n\t" \
  "v_cvt_pk_bf16_f32 v136, v32, v33\n\t" \
  "v_cvt_pk_bf16_f32 v137, v34, v35\n\t" \
  "v_mfma_f32_16x16x32_bf16 a[8:11], a[128:131], v[144:147], a[8:11]\n\t" \
  "v_cvt_pk_bf16_f32 v138, v36, v37\n\t" \
  "v_cvt_pk_bf16_f32 v139, v38, v39\n\t" \
  "v_mfma_f32_16x16x32_bf16 a[8:11], a[132:135], v[148:151], a[8:11]\n\t" \
  "v_exp_f32 v40, v40\n\t" \
  "v_mfma_f32_16x16x32_bf16 a[24:27], a[136:139], v[144:147], a[24:27]\n\t" \
  "buffer_load_dwordx4 v227, %[sK], s96 offen offset:3072 lds\n\t" \
  "ds_read_b128 a[232:235], v222 offset:8192\n\t" \
  "v_mfma_f32_16x16x32_bf16 a[24:27], a[140:143], v[148:151], a[24:27]\n\t" \
  "v_exp_f32 v41, v41\n\t" \
  "v_mfma_f32_16x16x32_bf16 a[40:43], a[144:147], v[144:147], a[40:43]\n\t" \
  "v_cvt_pk_bf16_f32 v140, v40, v41\n\t" \
  "ds_read_b128 a[236:239], v223 offset:8192\n\t" \
  "v_mfma_f32_16x16x32_bf16 a[40:43], a[148:151], v[148:151], a[40:43]\n\t" \
  "v_exp_f32 v42, v42\n\t" \
  "v_mfma_f32_16x16x32_bf16 a[56:59], a[152:155], v[144:147], a[56:59]\n\t" \
  "v_exp_f32 v43, v43\n\t" \
  "v_mfma_f32_16x16x32_bf16 a[56:59], a[156:159], v[148:151], a[56:59]\n\t" \
  "v_cvt_pk_bf16_f32 v141, v42, v43\n\t" \
  "ds_read_b128 a[240:243], v220 offset:12288\n\t" \
  "v_mfma_f32_16x16x32_bf16 a[72:75], a[160:163], v[144:147], a[72:75]\n\t" \
  "s_add_u32 m0, %[m], 32768\n\t" \
  "s_mov_b32 s96, 0\n\t" \
  "buffer_load_dwordx4 v232, %[sG], s96 offen offset:0 lds\n\t" \
  "v_mfma_f32_16x16x32_bf16 a[72:75], a[164:167], v[148:151], a[72:75]\n\t" \
  "v_exp_f32 v44, v44\n\t" \
  "v_mfma_f32_16x16x32_bf16 a[88:91], a[168:171], v[144:147], a[88:91]\n\t" \
  "s_add_u32 m0, %[m], 81920\n\t" \
  "s_add_u32 s96, %[sofk], 65536\n\t" \
  "v_mfma_f32_16x16x32_bf16 a[88:91], a[172:175], v[148:151], a[88:91]\n\t" \
  "v_exp_f32 v45, v45\n\t" \
  "v_mfma_f32_16x16x32_bf16 a[104:107], a[176:179], v[144:147], a[104:107]\n\t" \
  "v_cvt_pk_bf16_f32 v142, v44, v45\n\t" \
  "ds_read_b128 a[244:247], v221 offset:12288\n\t" \
  "v_mfma_f32_16x16x32_bf16 a[104:107], a[180:183], v[148:151], a[104:107]\n\t" \
  "v_exp_f32 v46, v46\n\t" \
  "v_mfma_f32_16x16x32_bf16 a[120:123], a[184:187], v[144:147], a[120:123]\n\t" \
  "v_exp_f32 v47, v47\n\t" \
  "v_mfma_f32_16x16x32_bf16 a[120:123], a[188:191], v[148:151], a[120:123]\n\t" \
  "v_cvt_pk_bf16_f32 v143, v46, v47\n\t" \
  "buffer_load_dwordx4 v230, %[sV], s96 offen offset:0 lds\n\t" \
  "v_mfma_f32_16x16x32_bf16 v[168:171], v[216:219], v[144:147], v[168:171]\n\t" \
  "v_exp_f32 v64, v64\n\t" \
  "v_mfma_f32_16x16x32_bf16 v[168:171], v[216:219], v[148:151], v[168:171]\n\t" \
  "v_exp_f32 v65, v65\n\t" \
  "v_mfma_f32_16x16x32_bf16 a[12:15], a[128:131], v[152:155], a[12:15]\n\t" \
  "v_cvt_pk_bf16_f32 v144, v64, v65\n\t" \
  "ds_read_b128 a[248:251], v222 offset:12288\n\t" \
  "v_mfma_f32_16x16x32_bf16 a[12:15], a[132:135], v[156:159], a[12:15]\n\t" \
  "v_exp_f32 v66, v66\n\t" \
  "v_mfma_f32_16x16x32_bf16 a[28:31], a[136:139], v[152:155], a[28:31]\n\t" \
  "v_exp_f32 v67, v67\n\t" \
  "v_mfma_f32_16x16x32_bf16 a[28:31], a[140:143], v[156:159], a[28:31]\n\t" \
  "v_cvt_pk_bf16_f32 v145, v66, v67\n\t" \
  "buffer_load_dwordx4 v231, %[sV], s96 offen offset:1024 lds\n\t" \
  "v_mfma_f32_16x16x32_bf16 a[44:47], a[144:147], v[152:155], a[44:47]\n\t" \
  "ds_read_b128 a[252:255], v223 offset:12288\n\t" \
  "buffer_load_dwordx4 v230, %[sV], s96 offen offset:2048 lds\n\t" \
  "v_mfma_f32_16x16x32_bf16 a[44:47], a[148:151], v[156:159], a[44:47]\n\t" \
  "v_exp_f32 v68, v68\n\t" \
  "v_mfma_f32_16x16x32_bf16 a[60:63], a[152:155], v[152:155], a[60:63]\n\t" \
  "v_exp_f32 v69, v69\n\t" \
  "v_mfma_f32_16x16x32_bf16 a[60:63], a[156:159], v[156:159], a[60:63]\n\t" \
  "v_cvt_pk_bf16_f32 v146, v68, v69\n\t" \
  "buffer_load_dwordx4 v231, %[sV], s96 offen offset:3072 lds\n\t" \
  "v_mfma_f32_16x16x32_bf16 a[76:79], a[160:163], v[152:155], a[76:79]\n\t" \
  "v_exp_f32 v70, v70\n\t" \
  "v_mfma_f32_16x16x32_bf16 a[76:79], a[164:167], v[156:159], a[76:79]\n\t" \
  "v_exp_f32 v71, v71\n\t" \
  "v_mfma_f32_16x16x32_bf16 a[92:95], a[168:171], v[152:155], a[92:95]\n\t" \
  "v_cvt_pk_bf16_f32 v147, v70, v71\n\t" \
  "v_mfma_f32_16x16x32_bf16 a[92:95], a[172:175], v[156:159], a[92:95]\n\t" \
  "v_exp_f32 v72, v72\n\t" \
  "v_mfma_f32_16x16x32_bf16 a[108:111], a[176:179], v[152:155], a[108:111]\n\t" \
  "v_exp_f32 v73, v73\n\t" \
  "v_mfma_f32_16x16x32_bf16 a[108:111], a[180:183], v[156:159], a[108:111]\n\t" \
  "v_cvt_pk_bf16_f32 v148, v72, v73\n\t" \
  "v_mfma_f32_16x16x32_bf16 a[124:127], a[184:187], v[152:155], a[124:127]\n\t" \
  "s_add_u32 m0, %[m], 32768\n\t" \
  "s_mov_b32 s96, 6144\n\t" \
  "buffer_load_dwordx4 v232, %[sG], s96 offen offset:1024 lds\n\t" \
  "v_mfma_f32_16x16x32_bf16 a[124:127], a[188:191], v[156:159], a[124:127]\n\t" \
  "v_exp_f32 v74, v74\n\t" \
  "v_mfma_f32_16x16x32_bf16 v[172:175], v[216:219], v[152:155], v[172:175]\n\t" \
  "v_exp_f32 v75, v75\n\t" \
  "v_mfma_f32_16x16x32_bf16 v[172:175], v[216:219], v[156:159], v[172:175]\n\t" \
  "v_cvt_pk_bf16_f32 v149, v74, v75\n\t" \
  "s_cmp_lg_u32 s99, 0\n\t" \
  "s_cbranch_scc1 .Lsf_p3\n\t" \
  ".Lsf_q3:\n\t" \
  "s_waitcnt lgkmcnt(0)\n\t" \
  "v_mfma_f32_16x16x32_bf16 v[0:3], a[192:195], v[16:19], v[176:179]\n\t" \
  "s_add_u32 s98, %[pf4], 28672\n\t" \
  "buffer_load_dword %[pfd], v245, %[sG], s98 offen\n\t" \
  "v_mfma_f32_16x16x32_bf16 v[0:3], a[196:199], v[20:23], v[0:3]\n\t" \
  "v_exp_f32 v76, v76\n\t" \
  "v_mfma_f32_16x16x32_bf16 v[0:3], a[200:203], v[24:27], v[0:3]\n\t" \
  "ds_read_b64_tr_b16 a[128:129], v237 offset:0\n\t" \
  "ds_read_b64_tr_b16 a[130:131], v237 offset:4096\n\t" \
  "v_mfma_f32_16x16x32_bf16 v[0:3], a[204:207], v[28:31], v[0:3]\n\t" \
  "v_exp_f32 v77, v77\n\t" \
  "v_mfma_f32_16x16x32_bf16 v[4:7], a[220:223], v[28:31], v[176:179]\n\t" \
  "v_cvt_pk_bf16_f32 v150, v76, v77\n\t" \
  "ds_read_b64_tr_b16 a[132:133], v237 offset:8192\n\t" \
  "v_mfma_f32_16x16x32_bf16 v[4:7], a[216:219], v[24:27], v[4:7]\n\t" \
  "v_exp_f32 v78, v78\n\t" \
  "v_mfma_f32_16x16x32_bf16 v[4:7], a[212:215], v[20:23], v[4:7]\n\t" \
  "v_exp_f32 v79, v79\n\t" \
  "v_mfma_f32_16x16x32_bf16 v[4:7], a[208:211], v[16:19], v[4:7]\n\t" \
  "v_cvt_pk_bf16_f32 v151, v78, v79\n\t" \
  "v_max3_f32 v192, v0, v1, v2\n\t" \
  "v_mfma_f32_16x16x32_bf16 v[8:11], a[224:227], v[16:19], v[176:179]\n\t" \
  "ds_read_b64_tr_b16 a[134:135], v237 offset:12288\n\t" \
  "ds_read_b64_tr_b16 a[136:137], v238 offset:0\n\t" \
  "v_mfma_f32_16x16x32_bf16 v[8:11], a[228:231], v[20:23], v[8:11]\n\t" \
  "v_exp_f32 v96, v96\n\t" \
  "v_mfma_f32_16x16x32_bf16 v[8:11], a[232:235], v[24:27], v[8:11]\n\t" \
  "v_max3_f32 v203, v3, v4, v5\n\t" \
  "v_max3_f32 v192, v192, v6, v7\n\t" \
  "v_mfma_f32_16x16x32_bf16 v[8:11], a[236:239], v[28:31], v[8:11]\n\t" \
  "ds_read_b64_tr_b16 a[138:139], v238 offset:4096\n\t" \
  "ds_read_b64_tr_b16 a[140:141], v238 offset:8192\n\t" \
  "v_mfma_f32_16x16x32_bf16 v[12:15], a[252:255], v[28:31], v[176:179]\n\t" \
  "v_exp_f32 v97, v97\n\t" \
  "v_mfma_f32_16x16x32_bf16 v[12:15], a[248:251], v[24:27], v[12:15]\n\t" \
  "v_cvt_pk_bf16_f32 v152, v96, v97\n\t" \
  "ds_read_b64_tr_b16 a[142:143], v238 offset:12288\n\t" \
  "v_mfma_f32_16x16x32_bf16 v[12:15], a[244:247], v[20:23], v[12:15]\n\t" \
  "v_max3_f32 v203, v203, v8, v9\n\t" \
  "v_max3_f32 v192, v192, v10, v11\n\t" \
  "v_mfma_f32_16x16x32_bf16 v[12:15], a[240:243], v[16:19], v[12:15]\n\t" \
  "s_add_u32 s98, %[pf2], 0\n\t" \
  "buffer_load_dword %[pfd], v202, %[sM], s98 offen\n\t" \
  "v_mfma_f32_16x16x32_bf16 v[32:35], a[192:195], v[48:51], v[180:183]\n\t" \
  "ds_read_b64_tr_b16 a[144:145], v237 offset:256\n\t" \
  "ds_read_b64_tr_b16 a[146:147], v237 offset:4352\n\t" \
  "v_mfma_f32_16x16x32_bf16 v[32:35], a[196:199], v[52:55], v[32:35]\n\t" \
  "v_exp_f32 v98, v98\n\t" \
  "v_mfma_f32_16x16x32_bf16 v[32:35], a[200:203], v[56:59], v[32:35]\n\t" \
  "v_max3_f32 v203, v203, v12, v13\n\t" \
  "v_max3_f32 v192, v192, v14, v15\n\t" \
  "v_mfma_f32_16x16x32_bf16 v[32:35], a[204:207], v[60:63], v[32:35]\n\t" \
  "v_max_f32 v192, v192, v203\n\t" \
  "ds_read_b64_tr_b16 a[148:149], v237 offset:8448\n\t" \
  "v_mfma_f32_16x16x32_bf16 v[36:39], a[220:223], v[60:63], v[180:183]\n\t" \
  "v_cmp_lt_f32 vcc, 0x42800000, v192\n\t" \
  "ds_read_b64_tr_b16 a[150:151], v237 offset:12544\n\t" \
  "v_mfma_f32_16x16x32_bf16 v[36:39], a[216:219], v[56:59], v[36:39]\n\t" \
  "s_cbranch_vccnz .Lsf_c40\n\t" \
  ".Lsf_r40:\n\t" \
  "ds_read_b64_tr_b16 a[152:153], v238 offset:256\n\t" \
  "v_mfma_f32_16x16x32_bf16 v[36:39], a[212:215], v[52:55], v[36:39]\n\t" \
  "v_max3_f32 v192, v32, v33, v34\n\t" \
  "ds_read_b64_tr_b16 a[154:155], v238 offset:4352\n\t" \
  "v_mfma_f32_16x16x32_bf16 v[36:39], a[208:211], v[48:51], v[36:39]\n\t" \
  "v_exp_f32 v99, v99\n\t" \
  "v_mfma_f32_16x16x32_bf16 v[40:43], a[224:227], v[48:51], v[180:183]\n\t" \
  "v_cvt_pk_bf16_f32 v153, v98, v99\n\t" \
  "ds_read_b64_tr_b16 a[156:157], v238 offset:8448\n\t" \
  "v_mfma_f32_16x16x32_bf16 v[40:43], a[228:231], v[52:55], v[40:43]\n\t" \
  "v_exp_f32 v100, v100\n\t" \
  "v_mfma_f32_16x16x32_bf16 v[40:43], a[232:235], v[56:59], v[40:43]\n\t" \
  "v_exp_f32 v101, v101\n\t" \
  "v_mfma_f32_16x16x32_bf16 v[40:43], a[236:239], v[60:63], v[40:43]\n\t" \
  "v_cvt_pk_bf16_f32 v154, v100, v101\n\t" \
  "v_max3_f32 v203, v35, v36, v37\n\t" \
  "v_mfma_f32_16x16x32_bf16 v[44:47], a[252:255], v[60:63], v[180:183]\n\t" \
  "v_max3_f32 v192, v192, v38, v39\n\t" \
  "ds_read_b64_tr_b16 a[158:159], v238 offset:12544\n\t" \
  "v_mfma_f32_16x16x32_bf16 v[44:47], a[248:251], v[56:59], v[44:47]\n\t" \
  "v_exp_f32 v102, v102\n\t" \
  "v_mfma_f32_16x16x32_bf16 v[44:47], a[244:247], v[52:55], v[44:47]\n\t" \
  "ds_read_b64_tr_b16 a[160:161], v237 offset:512\n\t" \
  "v_max3_f32 v203, v203, v40, v41\n\t" \
  "v_mfma_f32_16x16x32_bf16 v[44:47], a[240:243], v[48:51], v[44:47]\n\t" \
  "v_max3_f32 v192, v192, v42, v43\n\t" \
  "ds_read_b64_tr_b16 a[162:163], v237 offset:4608\n\t" \
  "v_mfma_f32_16x16x32_bf16 v[64:67], a[192:195], v[80:83], v[184:187]\n\t" \
  "v_exp_f32 v103, v103\n\t" \
  "v_mfma_f32_16x16x32_bf16 v[64:67], a[196:199], v[84:87], v[64:67]\n\t" \
  "v_cvt_pk_bf16_f32 v155, v102, v103\n\t" \
  "ds_read_b64_tr_b16 a[164:165], v237 offset:8704\n\t" \
  "v_mfma_f32_16x16x32_bf16 v[64:67], a[200:203], v[88:91], v[64:67]\n\t" \
  "v_max3_f32 v203, v203, v44, v45\n\t" \
  "v_max3_f32 v192, v192, v46, v47\n\t" \
  "v_mfma_f32_16x16x32_bf16 v[64:67], a[204:207], v[92:95], v[64:67]\n\t" \
  "v_max_f32 v192, v192, v203\n\t" \
  "ds_read_b64_tr_b16 a[166:167], v237 offset:12800\n\t" \
  "v_mfma_f32_16x16x32_bf16 v[68:71], a[220:223], v[92:95], v[184:187]\n\t" \
  "v_cmp_lt_f32 vcc, 0x42800000, v192\n\t" \
  "ds_read_b64_tr_b16 a[168:169], v238 offset:512\n\t" \
  "v_mfma_f32_16x16x32_bf16 v[68:71], a[216:219], v[88:91], v[68:71]\n\t" \
  "s_cbranch_vccnz .Lsf_c41\n\t" \
  ".Lsf_r41:\n\t" \
  "ds_read_b64_tr_b16 a[170:171], v238 offset:4608\n\t" \
  "v_mfma_f32_16x16x32_bf16 v[68:71], a[212:215], v[84:87], v[68:71]\n\t" \
  "v_max3_f32 v192, v64, v65, v66\n\t" \
  "ds_read_b64_tr_b16 a[172:173], v238 offset:8704\n\t" \
  "v_mfma_f32_16x16x32_bf16 v[68:71], a[208:211], v[80:83], v[68:71]\n\t" \
  "v_exp_f32 v104, v104\n\t" \
  "v_mfma_f32_16x16x32_bf16 v[72:75], a[224:227], v[80:83], v[184:187]\n\t" \
  "v_exp_f32 v105, v105\n\t" \
  "v_mfma_f32_16x16x32_bf16 v[72:75], a[228:231], v[84:87], v[72:75]\n\t" \
  "v_cvt_pk_bf16_f32 v156, v104, v105\n\t" \
  "ds_read_b64_tr_b16 a[174:175], v238 offset:12800\n\t" \
  "v_mfma_f32_16x16x32_bf16 v[72:75], a[232:235], v[88:91], v[72:75]\n\t" \
  "v_exp_f32 v106, v106\n\t" \
  "v_mfma_f32_16x16x32_bf16 v[72:75], a[236:239], v[92:95], v[72:75]\n\t" \
  "v_max3_f32 v203, v67, v68, v69\n\t" \
  "v_max3_f32 v192, v192, v70, v71\n\t" \
  "v_mfma_f32_16x16x32_bf16 v[76:79], a[252:255], v[92:95], v[184:187]\n\t" \
  "ds_read_b64_tr_b16 a[176:177], v237 offset:768\n\t" \
  "ds_read_b64_tr_b16 a[178:179], v237 offset:4864\n\t" \
  "v_mfma_f32_16x16x32_bf16 v[76:79], a[248:251], v[88:91], v[76:79]\n\t" \
  "v_exp_f32 v107, v107\n\t" \
  "v_mfma_f32_16x16x32_bf16 v[76:79], a[244:247], v[84:87], v[76:79]\n\t" \
  "v_cvt_pk_bf16_f32 v157, v106, v107\n\t" \
  "v_max3_f32 v203, v203, v72, v73\n\t" \
  "v_mfma_f32_16x16x32_bf16 v[76:79], a[240:243], v[80:83], v[76:79]\n\t" \
  "v_max3_f32 v192, v192, v74, v75\n\t" \
  "ds_read_b64_tr_b16 a[180:181], v237 offset:8960\n\t" \
  "v_mfma_f32_16x16x32_bf16 v[96:99], a[192:195], v[112:115], v[188:191]\n\t" \
  "v_exp_f32 v108, v108\n\t" \
  "v_mfma_f32_16x16x32_bf16 v[96:99], a[196:199], v[116:119], v[96:99]\n\t" \
  "v_exp_f32 v109, v109\n\t" \
  "v_mfma_f32_16x16x32_bf16 v[96:99], a[200:203], v[120:123], v[96:99]\n\t" \
  "v_cvt_pk_bf16_f32 v158, v108, v109\n\t" \
  "ds_read_b64_tr_b16 a[182:183], v237 offset:13056\n\t" \
  "v_mfma_f32_16x16x32_bf16 v[96:99], a[204:207], v[124:127], v[96:99]\n\t" \
  "v_max3_f32 v203, v203, v76, v77\n\t" \
  "v_max3_f32 v192, v192, v78, v79\n\t" \
  "v_mfma_f32_16x16x32_bf16 v[100:103], a[220:223], v[124:127], v[188:191]\n\t" \
  "v_max_f32 v192, v192, v203\n\t" \
  "ds_read_b64_tr_b16 a[184:185], v238 offset:768\n\t" \
  "v_mfma_f32_16x16x32_bf16 v[100:103], a[216:219], v[120:123], v[100:103]\n\t" \
  "v_cmp_lt_f32 vcc, 0x42800000, v192\n\t" \
  "ds_read_b64_tr_b16 a[186:187], v238 offset:4864\n\t" \
  "v_mfma_f32_16x16x32_bf16 v[100:103], a[212:215], v[116:119], v[100:103]\n\t" \
  "s_cbranch_vccnz .Lsf_c42\n\t" \
  ".Lsf_r42:\n\t" \
  "v_max3_f32 v192, v96, v97, v98\n\t" \
  "v_mfma_f32_16x16x32_bf16 v[100:103], a[208:211], v[112:115], v[100:103]\n\t" \
  "ds_read_b64_tr_b16 a[188:189], v238 offset:8960\n\t" \
  "ds_read_b64_tr_b16 a[190:191], v238 offset:13056\n\t" \
  "v_mfma_f32_16x16x32_bf16 v[104:107], a[224:227], v[112:115], v[188:191]\n\t" \
  "v_exp_f32 v110, v110\n\t" \
  "v_mfma_f32_16x16x32_bf16 v[104:107], a[228:231], v[116:119], v[104:107]\n\t" \
  "v_exp_f32 v111, v111\n\t" \
  "v_mfma_f32_16x16x32_bf16 v[104:107], a[232:235], v[120:123], v[104:107]\n\t" \
  "v_cvt_pk_bf16_f32 v159, v110, v111\n\t" \
  "v_max3_f32 v203, v99, v100, v101\n\t" \
  "v_mfma_f32_16x16x32_bf16 v[104:107], a[236:239], v[124:127], v[104:107]\n\t" \
  "v_max3_f32 v192, v192, v102, v103\n\t" \
  "v_mfma_f32_16x16x32_bf16 v[108:111], a[252:255], v[124:127], v[188:191]\n\t" \
  "v_exp_f32 v0, v0\n\t" \
  "v_mfma_f32_16x16x32_bf16 v[108:111], a[248:251], v[120:123], v[108:111]\n\t" \
  "v_exp_f32 v1, v1\n\t" \
  "v_mfma_f32_16x16x32_bf16 v[108:111], a[244:247], v[116:119], v[108:111]\n\t" \
  "v_exp_f32 v2, v2\n\t" \
  "v_mfma_f32_16x16x32_bf16 v[108:111], a[240:243], v[112:115], v[108:111]\n\t" \
  "v_max3_f32 v203, v203, v104, v105\n\t" \
  "v_max3_f32 v192, v192, v106, v107\n\t" \
  "s_waitcnt vmcnt(3) lgkmcnt(0)\n\t" \
  "s_barrier\n\t" \
  "v_mfma_f32_16x16x32_bf16 a[0:3], a[128:131], v[128:131], a[0:3]\n\t" \
  "v_exp_f32 v3, v3\n\t" \
  "v_mfma_f32_16x16x32_bf16 a[0:3], a[132:135], v[132:135], a[0:3]\n\t" \
  "v_exp_f32 v4, v4\n\t" \
  "v_mfma_f32_16x16x32_bf16 a[16:19], a[136:139], v[128:131], a[16:19]\n\t" \
  "ds_read_b128 a[192:195], v220 offset:16384\n\t" \
  "v_max3_f32 v203, v203, v108, v109\n\t" \
  "v_mfma_f32_16x16x32_bf16 a[16:19], a[140:143], v[132:135], a[16:19]\n\t" \
  "v_max3_f32 v192, v192, v110, v111\n\t" \
  "v_max_f32 v192, v192, v203\n\t" \
  "v_mfma_f32_16x16x32_bf16 a[32:35], a[144:147], v[128:131], a[32:35]\n\t" \
  "v_cmp_lt_f32 vcc, 0x42800000, v192\n\t" \
  "ds_read_b128 a[196:199], v221 offset:16384\n\t" \
  "v_mfma_f32_16x16x32_bf16 a[32:35], a[148:151], v[132:135], a[32:35]\n\t" \
  "s_cbranch_vccnz .Lsf_c43\n\t" \
  ".Lsf_r43:\n\t" \
  "ds_read_b128 a[200:203], v222 offset:16384\n\t" \
  "v_mfma_f32_16x16x32_bf16 a[48:51], a[152:155], v[128:131], a[48:51]\n\t" \
  "s_lshr_b32 s98, %[sofk], 6\n\t" \
  "v_xor_b32 v234, s98, v244\n\t" \
  "v_mfma_f32_16x16x32_bf16 a[48:51], a[156:159], v[132:135], a[48:51]\n\t" \
  "v_add_u32 v213, %[m], v242\n\t" \
  "v_add_u32 v215, 98304, v213\n\t" \
  "v_mfma_f32_16x16x32_bf16 a[64:67], a[160:163], v[128:131], a[64:67]\n\t" \
  "v_exp_f32 v5, v5\n\t" \
  "v_mfma_f32_16x16x32_bf16 a[64:67], a[164:167], v[132:135], a[64:67]\n\t" \
  "v_exp_f32 v6, v6\n\t" \
  "v_mfma_f32_16x16x32_bf16 a[80:83], a[168:171], v[128:131], a[80:83]\n\t" \
  "v_mov_b32 v243, %[tag]\n\t" \
  "v_lshrrev_b32 v254, 3, v233\n\t" \
  "v_mfma_f32_16x16x32_bf16 a[80:83], a[172:175], v[132:135], a[80:83]\n\t" \
  "v_exp_f32 v7, v7\n\t" \
  "v_mfma_f32_16x16x32_bf16 a[96:99], a[176:179], v[128:131], a[96:99]\n\t" \
  "v_min_u32 v254, 1, v254\n\t" \
  "ds_read_b128 a[204:207], v223 offset:16384\n\t" \
  "v_mfma_f32_16x16x32_bf16 a[96:99], a[180:183], v[132:135], a[96:99]\n\t" \
  "v_exp_f32 v8, v8\n\t" \
  "v_mfma_f32_16x16x32_bf16 a[112:115], a[184:187], v[128:131], a[112:115]\n\t" \
  "v_exp_f32 v9, v9\n\t" \
  "v_mfma_f32_16x16x32_bf16 a[112:115], a[188:191], v[132:135], a[112:115]\n\t" \
  "v_lshlrev_b32 v254, 31, v254\n\t" \
  "v_lshl_or_b32 v254, v233, 9, v254\n\t" \
  "v_mfma_f32_16x16x32_bf16 v[160:163], v[216:219], v[128:131], v[160:163]\n\t" \
  "v_exp_f32 v10, v10\n\t" \
  "v_mfma_f32_16x16x32_bf16 v[160:163], v[216:219], v[132:135], v[160:163]\n\t" \
  "v_cvt_pk_bf16_f32 v128, v0, v1\n\t" \
  "v_cvt_pk_bf16_f32 v129, v2, v3\n\t" \
  "v_mfma_f32_16x16x32_bf16 a[4:7], a[128:131], v[136:139], a[4:7]\n\t" \
  "v_cvt_pk_bf16_f32 v130, v4, v5\n\t" \
  "v_cvt_pk_bf16_f32 v131, v6, v7\n\t" \
  "v_mfma_f32_16x16x32_bf16 a[4:7], a[132:135], v[140:143], a[4:7]\n\t" \
  "v_cvt_pk_bf16_f32 v132, v8, v9\n\t" \
  "ds_read_b128 a[208:211], v220 offset:20480\n\t" \
  "s_lshr_b32 m0, %[hl18], 18\n\t" \
  "s_brev_b32 s98, 1\n\t" \
  "v_writelane_b32 v254, s98, m0\n\t" \
  "v_mfma_f32_16x16x32_bf16 a[20:23], a[136:139], v[136:139], a[20:23]\n\t" \
  "ds_read_b128 a[212:215], v221 offset:20480\n\t" \
  "s_add_u32 m0, %[m], 49152\n\t" \
  "v_mfma_f32_16x16x32_bf16 a[20:23], a[140:143], v[140:143], a[20:23]\n\t" \
  "v_exp_f32 v11, v11\n\t" \
  "v_mfma_f32_16x16x32_bf16 a[36:39], a[144:147], v[136:139], a[36:39]\n\t" \
  "v_cvt_pk_bf16_f32 v133, v10, v11\n\t" \
  "ds_read_b128 a[216:219], v222 offset:20480\n\t" \
  "v_mfma_f32_16x16x32_bf16 a[36:39], a[148:151], v[140:143], a[36:39]\n\t" \
  "v_exp_f32 v12, v12\n\t" \
  "v_mfma_f32_16x16x32_bf16 a[52:55], a[152:155], v[136:139], a[52:55]\n\t" \
  "v_exp_f32 v13, v13\n\t" \
  "v_mfma_f32_16x16x32_bf16 a[52:55], a[156:159], v[140:143], a[52:55]\n\t" \
  "v_cvt_pk_bf16_f32 v134, v12, v13\n\t" \
  "s_add_u32 s96, %[sofk], 98304\n\t" \
  "v_mfma_f32_16x16x32_bf16 a[68:71], a[160:163], v[136:139], a[68:71]\n\t" \
  "v_exp_f32 v14, v14\n\t" \
  "v_mfma_f32_16x16x32_bf16 a[68:71], a[164:167], v[140:143], a[68:71]\n\t" \
  "ds_read_b128 a[220:223], v223 offset:20480\n\t" \
  "buffer_load_dwordx4 v224, %[sK], s96 offen offset:0 lds\n\t" \
  "v_mfma_f32_16x16x32_bf16 a[84:87], a[168:171], v[136:139], a[84:87]\n\t" \
  "v_exp_f32 v15, v15\n\t" \
  "v_mfma_f32_16x16x32_bf16 a[84:87], a[172:175], v[140:143], a[84:87]\n\t" \
  "v_cvt_pk_bf16_f32 v135, v14, v15\n\t" \
  "ds_read_b128 a[224:227], v220 offset:24576\n\t" \
  "v_mfma_f32_16x16x32_bf16 a[100:103], a[176:179], v[136:139], a[100:103]\n\t" \
  "v_exp_f32 v32, v32\n\t" \
  "v_mfma_f32_16x16x32_bf16 a[100:103], a[180:183], v[140:143], a[100:103]\n\t" \
  "v_exp_f32 v33, v33\n\t" \
  "v_mfma_f32_16x16x32_bf16 a[116:119], a[184:187], v[136:139], a[116:119]\n\t" \
  "v_exp_f32 v34, v34\n\t" \
  "v_mfma_f32_16x16x32_bf16 a[116:119], a[188:191], v[140:143], a[116:119]\n\t" \
  "buffer_load_dwordx4 v225, %[sK], s96 offen offset:1024 lds\n\t" \
  "buffer_load_dwordx4 v226, %[sK], s96 offen offset:2048 lds\n\t" \
  "v_mfma_f32_16x16x32_bf16 v[164:167], v[216:219], v[136:139], v[164:167]\n\t" \
  "v_exp_f32 v35, v35\n\t" \
  "v_mfma_f32_16x16x32_bf16 v[164:167], v[216:219], v[140:143], v[164:167]\n\t" \
  "v_cvt_pk_bf16_f32 v136, v32, v33\n\t" \
  "v_cvt_pk_bf16_f32 v137, v34, v35\n\t" \
  "v_mfma_f32_16x16x32_bf16 a[8:11], a[128:131], v[144:147], a[8:11]\n\t" \
  "ds_read_b128 a[228:231], v221 offset:24576\n\t" \
  "buffer_load_dwordx4 v227, %[sK], s96 offen offset:3072 lds\n\t" \
  "v_mfma_f32_16x16x32_bf16 a[8:11], a[132:135], v[148:151], a[8:11]\n\t" \
  "v_exp_f32 v36, v36\n\t" \
  "v_mfma_f32_16x16x32_bf16 a[24:27], a[136:139], v[144:147], a[24:27]\n\t" \
  "v_exp_f32 v37, v37\n\t" \
  "v_mfma_f32_16x16x32_bf16 a[24:27], a[140:143], v[148:151], a[24:27]\n\t" \
  "v_cvt_pk_bf16_f32 v138, v36, v37\n\t" \
  "ds_read_b128 a[232:235], v222 offset:24576\n\t" \
  "v_mfma_f32_16x16x32_bf16 a[40:43], a[144:147], v[144:147], a[40:43]\n\t" \
  "v_exp_f32 v38, v38\n\t" \
  "v_mfma_f32_16x16x32_bf16 a[40:43], a[148:151], v[148:151], a[40:43]\n\t" \
  "v_exp_f32 v39, v39\n\t" \
  "v_mfma_f32_16x16x32_bf16 a[56:59], a[152:155], v[144:147], a[56:59]\n\t" \
  "v_cvt_pk_bf16_f32 v139, v38, v39\n\t" \
  "ds_read_b128 a[236:239], v223 offset:24576\n\t" \
  "v_mfma_f32_16x16x32_bf16 a[56:59], a[156:159], v[148:151], a[56:59]\n\t" \
  "s_add_u32 m0, %[m], 32768\n\t" \
  "s_mov_b32 s96, 12288\n\t" \
  "buffer_load_dwordx4 v232, %[sG], s96 offen offset:2048 lds\n\t" \
  "v_mfma_f32_16x16x32_bf16 a[72:75], a[160:163], v[144:147], a[72:75]\n\t" \
  "v_exp_f32 v40, v40\n\t" \
  "v_mfma_f32_16x16x32_bf16 a[72:75], a[164:167], v[148:151], a[72:75]\n\t" \
  "s_add_u32 m0, %[m], 147456\n\t" \
  "ds_read_b128 a[240:243], v220 offset:28672\n\t" \
  "v_mfma_f32_16x16x32_bf16 a[88:91], a[168:171], v[144:147], a[88:91]\n\t" \
  "v_exp_f32 v41, v41\n\t" \
  "v_mfma_f32_16x16x32_bf16 a[88:91], a[172:175], v[148:151], a[88:91]\n\t" \
  "v_cvt_pk_bf16_f32 v140, v40, v41\n\t" \
  "s_add_u32 s96, %[sofk], 81920\n\t" \
  "v_mfma_f32_16x16x32_bf16 a[104:107], a[176:179], v[144:147], a[104:107]\n\t" \
  "v_exp_f32 v42, v42\n\t" \
  "v_mfma_f32_16x16x32_bf16 a[104:107], a[180:183], v[148:151], a[104:107]\n\t" \
  "v_exp_f32 v43, v43\n\t" \
  "v_mfma_f32_16x16x32_bf16 a[120:123], a[184:187], v[144:147], a[120:123]\n\t" \
  "v_cvt_pk_bf16_f32 v141, v42, v43\n\t" \
  "ds_read_b128 a[244:247], v221 offset:28672\n\t" \
  "v_mfma_f32_16x16x32_bf16 a[120:123], a[188:191], v[148:151], a[120:123]\n\t" \
  "v_exp_f32 v44, v44\n\t" \
  "v_mfma_f32_16x16x32_bf16 v[168:171], v[216:219], v[144:147], v[168:171]\n\t" \
  "v_exp_f32 v45, v45\n\t" \
  "v_mfma_f32_16x16x32_bf16 v[168:171], v[216:219], v[148:151], v[168:171]\n\t" \
  "v_cvt_pk_bf16_f32 v142, v44, v45\n\t" \
  "buffer_load_dwordx4 v230, %[sV], s96 offen offset:0 lds\n\t" \
  "v_mfma_f32_16x16x32_bf16 a[12:15], a[128:131], v[152:155], a[12:15]\n\t" \
  "v_exp_f32 v46, v46\n\t" \
  "v_mfma_f32_16x16x32_bf16 a[12:15], a[132:135], v[156:159], a[12:15]\n\t" \
  "ds_read_b128 a[248:251], v222 offset:28672\n\t" \
  "buffer_load_dwordx4 v231, %[sV], s96 offen offset:1024 lds\n\t" \
  "v_mfma_f32_16x16x32_bf16 a[28:31], a[136:139], v[152:155], a[28:31]\n\t" \
  "v_exp_f32 v47, v47\n\t" \
  "v_mfma_f32_16x16x32_bf16 a[28:31], a[140:143], v[156:159], a[28:31]\n\t" \
  "v_cvt_pk_bf16_f32 v143, v46, v47\n\t" \
  "ds_read_b128 a[252:255], v223 offset:28672\n\t" \
  "v_mfma_f32_16x16x32_bf16 a[44:47], a[144:147], v[152:155], a[44:47]\n\t" \
  "v_exp_f32 v64, v64\n\t" \
  "v_mfma_f32_16x16x32_bf16 a[44:47], a[148:151], v[156:159], a[44:47]\n\t" \
  "v_exp_f32 v65, v65\n\t" \
  "v_mfma_f32_16x16x32_bf16 a[60:63], a[152:155], v[152:155], a[60:63]\n\t" \
  "v_cvt_pk_bf16_f32 v144, v64, v65\n\t" \
  "buffer_load_dwordx4 v230, %[sV], s96 offen offset:2048 lds\n\t" \
  "v_mfma_f32_16x16x32_bf16 a[60:63], a[156:159], v[156:159], a[60:63]\n\t" \
  "v_exp_f32 v66, v66\n\t" \
  "v_mfma_f32_16x16x32_bf16 a[76:79], a[160:163], v[152:155], a[76:79]\n\t" \
  "v_exp_f32 v67, v67\n\t" \
  "v_mfma_f32_16x16x32_bf16 a[76:79], a[164:167], v[156:159], a[76:79]\n\t" \
  "v_cvt_pk_bf16_f32 v145, v66, v67\n\t" \
  "buffer_load_dwordx4 v231, %[sV], s96 offen offset:3072 lds\n\t" \
  "v_mfma_f32_16x16x32_bf16 a[92:95], a[168:171], v[152:155], a[92:95]\n\t" \
  "v_exp_f32 v68, v68\n\t" \
  "v_mfma_f32_16x16x32_bf16 a[92:95], a[172:175], v[156:159], a[92:95]\n\t" \
  "v_exp_f32 v69, v69\n\t" \
  "v_mfma_f32_16x16x32_bf16 a[108:111], a[176:179], v[152:155], a[108:111]\n\t" \
  "v_cvt_pk_bf16_f32 v146, v68, v69\n\t" \
  "v_mfma_f32_16x16x32_bf16 a[108:111], a[180:183], v[156:159], a[108:111]\n\t" \
  "s_add_u32 m0, %[m], 32768\n\t" \
  "s_mov_b32 s96, 18432\n\t" \
  "buffer_load_dwordx4 v232, %[sG], s96 offen offset:3072 lds\n\t" \
  "v_mfma_f32_16x16x32_bf16 a[124:127], a[184:187], v[152:155], a[124:127]\n\t" \
  "v_exp_f32 v70, v70\n\t" \
  "v_mfma_f32_16x16x32_bf16 a[124:127], a[188:191], v[156:159], a[124:127]\n\t" \
  "v_exp_f32 v71, v71\n\t" \
  "v_mfma_f32_16x16x32_bf16 v[172:175], v[216:219], v[152:155], v[172:175]\n\t" \
  "v_cvt_pk_bf16_f32 v147, v70, v71\n\t" \
  "v_mfma_f32_16x16x32_bf16 v[172:175], v[216:219], v[156:159], v[172:175]\n\t" \
  "v_exp_f32 v72, v72\n\t" \
  "s_cmp_lg_u32 s99, 0\n\t" \
  "s_cbranch_scc1 .Lsf_p4\n\t" \
  ".Lsf_q4:\n\t" \
  "s_waitcnt lgkmcnt(0)\n\t" \
  "v_mfma_f32_16x16x32_bf16 v[0:3], a[192:195], v[16:19], v[176:179]\n\t" \
  "s_add_u32 s98, %[pf4], 43008\n\t" \
  "buffer_load_dword %[pfd], v245, %[sG], s98 offen\n\t" \
  "v_mfma_f32_16x16x32_bf16 v[0:3], a[196:199], v[20:23], v[0:3]\n\t" \
  "ds_read_b64_tr_b16 a[128:129], v228 offset:0\n\t" \
  "v_lshrrev_b32 v252, 3, v233\n\t" \
  "v_mfma_f32_16x16x32_bf16 v[0:3], a[200:203], v[24:27], v[0:3]\n\t" \
  "v_exp_f32 v73, v73\n\t" \
  "v_mfma_f32_16x16x32_bf16 v[0:3], a[204:207], v[28:31], v[0:3]\n\t" \
  "v_cvt_pk_bf16_f32 v148, v72, v73\n\t" \
  "ds_read_b64_tr_b16 a[130:131], v228 offset:4096\n\t" \
  "v_mfma_f32_16x16x32_bf16 v[4:7], a[220:223], v[28:31], v[176:179]\n\t" \
  "ds_read_b64_tr_b16 a[132:133], v228 offset:8192\n\t" \
  "v_mul_u32_u24 v247, 98176, v252\n\t" \
  "v_mfma_f32_16x16x32_bf16 v[4:7], a[216:219], v[24:27], v[4:7]\n\t" \
  "v_exp_f32 v74, v74\n\t" \
  "v_mfma_f32_16x16x32_bf16 v[4:7], a[212:215], v[20:23], v[4:7]\n\t" \
  "v_max3_f32 v192, v0, v1, v2\n\t" \
  "ds_read_b64_tr_b16 a[134:135], v228 offset:12288\n\t" \
  "v_mfma_f32_16x16x32_bf16 v[4:7], a[208:211], v[16:19], v[4:7]\n\t" \
  "v_exp_f32 v75, v75\n\t" \
  "v_mfma_f32_16x16x32_bf16 v[8:11], a[224:227], v[16:19], v[176:179]\n\t" \
  "v_cvt_pk_bf16_f32 v149, v74, v75\n\t" \
  "ds_read_b64_tr_b16 a[136:137], v229 offset:0\n\t" \
  "v_mfma_f32_16x16x32_bf16 v[8:11], a[228:231], v[20:23], v[8:11]\n\t" \
  "v_lshl_add_u32 v247, v233, 4, v247\n\t" \
  "ds_read_b64_tr_b16 a[138:139], v229 offset:4096\n\t" \
  "v_mfma_f32_16x16x32_bf16 v[8:11], a[232:235], v[24:27], v[8:11]\n\t" \
  "v_max3_f32 v203, v3, v4, v5\n\t" \
  "v_max3_f32 v192, v192, v6, v7\n\t" \
  "v_mfma_f32_16x16x32_bf16 v[8:11], a[236:239], v[28:31], v[8:11]\n\t" \
  "v_exp_f32 v76, v76\n\t" \
  "v_mfma_f32_16x16x32_bf16 v[12:15], a[252:255], v[28:31], v[176:179]\n\t" \
  "ds_read_b64_tr_b16 a[140:141], v229 offset:8192\n\t" \
  "v_lshlrev_b32 v252, 4, v252\n\t" \
  "v_mfma_f32_16x16x32_bf16 v[12:15], a[248:251], v[24:27], v[12:15]\n\t" \
  "s_add_u32 s98, %[pf2], 24576\n\t" \
  "buffer_load_dword %[pfd], v202, %[sM], s98 offen\n\t" \
  "v_mfma_f32_16x16x32_bf16 v[12:15], a[244:247], v[20:23], v[12:15]\n\t" \
  "v_max3_f32 v203, v203, v8, v9\n\t" \
  "v_max3_f32 v192, v192, v10, v11\n\t" \
  "v_mfma_f32_16x16x32_bf16 v[12:15], a[240:243], v[16:19], v[12:15]\n\t" \
  "ds_read_b64_tr_b16 a[142:143], v229 offset:12288\n\t" \
  "ds_read_b64_tr_b16 a[144:145], v228 offset:256\n\t" \
  "v_mfma_f32_16x16x32_bf16 v[32:35], a[192:195], v[48:51], v[180:183]\n\t" \
  "v_exp_f32 v77, v77\n\t" \
  "v_mfma_f32_16x16x32_bf16 v[32:35], a[196:199], v[52:55], v[32:35]\n\t" \
  "v_cvt_pk_bf16_f32 v150, v76, v77\n\t" \
  "ds_read_b64_tr_b16 a[146:147], v228 offset:4352\n\t" \
  "v_mfma_f32_16x16x32_bf16 v[32:35], a[200:203], v[56:59], v[32:35]\n\t" \
  "v_max3_f32 v203, v203, v12, v13\n\t" \
  "v_max3_f32 v192, v192, v14, v15\n\t" \
  "v_mfma_f32_16x16x32_bf16 v[32:35], a[204:207], v[60:63], v[32:35]\n\t" \
  "v_max_f32 v192, v192, v203\n\t" \
  "ds_read_b64_tr_b16 a[148:149], v228 offset:8448\n\t" \
  "v_mfma_f32_16x16x32_bf16 v[36:39], a[220:223], v[60:63], v[180:183]\n\t" \
  "v_cmp_lt_f32 vcc, 0x42800000, v192\n\t" \
  "ds_read_b64_tr_b16 a[150:151], v228 offset:12544\n\t" \
  "v_mfma_f32_16x16x32_bf16 v[36:39], a[216:219], v[56:59], v[36:39]\n\t" \
  "s_cbranch_vccnz .Lsf_c50\n\t" \
  ".Lsf_r50:\n\t" \
  "ds_read_b64_tr_b16 a[152:153], v229 offset:256\n\t" \
  "v_mfma_f32_16x16x32_bf16 v[36:39], a[212:215], v[52:55], v[36:39]\n\t" \
  "v_max3_f32 v192, v32, v33, v34\n\t" \
  "ds_read_b64_tr_b16 a[154:155], v229 offset:4352\n\t" \
  "v_mfma_f32_16x16x32_bf16 v[36:39], a[208:211], v[48:51], v[36:39]\n\t" \
  "v_exp_f32 v78, v78\n\t" \
  "v_mfma_f32_16x16x32_bf16 v[40:43], a[224:227], v[48:51], v[180:183]\n\t" \
  "v_exp_f32 v79, v79\n\t" \
  "v_mfma_f32_16x16x32_bf16 v[40:43], a[228:231], v[52:55], v[40:43]\n\t" \
  "v_cvt_pk_bf16_f32 v151, v78, v79\n\t" \
  "ds_read_b64_tr_b16 a[156:157], v229 offset:8448\n\t" \
  "v_mfma_f32_16x16x32_bf16 v[40:43], a[232:235], v[56:59], v[40:43]\n\t" \
  "v_exp_f32 v96, v96\n\t" \
  "v_mfma_f32_16x16x32_bf16 v[40:43], a[236:239], v[60:63], v[40:43]\n\t" \
  "v_max3_f32 v203, v35, v36, v37\n\t" \
  "v_max3_f32 v192, v192, v38, v39\n\t" \
  "v_mfma_f32_16x16x32_bf16 v[44:47], a[252:255], v[60:63], v[180:183]\n\t" \
  "v_exp_f32 v97, v97\n\t" \
  "v_mfma_f32_16x16x32_bf16 v[44:47], a[248:251], v[56:59], v[44:47]\n\t" \
  "v_cvt_pk_bf16_f32 v152, v96, v97\n\t" \
  "ds_read_b64_tr_b16 a[158:159], v229 offset:12544\n\t" \
  "v_mfma_f32_16x16x32_bf16 v[44:47], a[244:247], v[52:55], v[44:47]\n\t" \
  "ds_read_b64_tr_b16 a[160:161], v228 offset:512\n\t" \
  "v_max3_f32 v203, v203, v40, v41\n\t" \
  "v_mfma_f32_16x16x32_bf16 v[44:47], a[240:243], v[48:51], v[44:47]\n\t" \
  "v_max3_f32 v192, v192, v42, v43\n\t" \
  "ds_read_b64_tr_b16 a[162:163], v228 offset:4608\n\t" \
  "v_mfma_f32_16x16x32_bf16 v[64:67], a[192:195], v[80:83], v[184:187]\n\t" \
  "v_exp_f32 v98, v98\n\t" \
  "v_mfma_f32_16x16x32_bf16 v[64:67], a[196:199], v[84:87], v[64:67]\n\t" \
  "v_exp_f32 v99, v99\n\t" \
  "v_mfma_f32_16x16x32_bf16 v[64:67], a[200:203], v[88:91], v[64:67]\n\t" \
  "v_cvt_pk_bf16_f32 v153, v98, v99\n\t" \
  "ds_read_b64_tr_b16 a[164:165], v228 offset:8704\n\t" \
  "v_mfma_f32_16x16x32_bf16 v[64:67], a[204:207], v[92:95], v[64:67]\n\t" \
  "v_max3_f32 v203, v203, v44, v45\n\t" \
  "v_max3_f32 v192, v192, v46, v47\n\t" \
  "v_mfma_f32_16x16x32_bf16 v[68:71], a[220:223], v[92:95], v[184:187]\n\t" \
  "v_max_f32 v192, v192, v203\n\t" \
  "ds_read_b64_tr_b16 a[166:167], v228 offset:12800\n\t" \
  "v_mfma_f32_16x16x32_bf16 v[68:71], a[216:219], v[88:91], v[68:71]\n\t" \
  "v_cmp_lt_f32 vcc, 0x42800000, v192\n\t" \
  "ds_read_b64_tr_b16 a[168:169], v229 offset:512\n\t" \
  "v_mfma_f32_16x16x32_bf16 v[68:71], a[212:215], v[84:87], v[68:71]\n\t" \
  "s_cbranch_vccnz .Lsf_c51\n\t" \
  ".Lsf_r51:\n\t" \
  "v_max3_f32 v192, v64, v65, v66\n\t" \
  "v_mfma_f32_16x16x32_bf16 v[68:71], a[208:211], v[80:83], v[68:71]\n\t" \
  "ds_read_b64_tr_b16 a[170:171], v229 offset:4608\n\t" \
  "ds_read_b64_tr_b16 a[172:173], v229 offset:8704\n\t" \
  "v_mfma_f32_16x16x32_bf16 v[72:75], a[224:227], v[80:83], v[184:187]\n\t" \
  "v_exp_f32 v100, v100\n\t" \
  "v_mfma_f32_16x16x32_bf16 v[72:75], a[228:231], v[84:87], v[72:75]\n\t" \
  "v_exp_f32 v101, v101\n\t" \
  "v_mfma_f32_16x16x32_bf16 v[72:75], a[232:235], v[88:91], v[72:75]\n\t" \
  "v_cvt_pk_bf16_f32 v154, v100, v101\n\t" \
  "v_max3_f32 v203, v67, v68, v69\n\t" \
  "v_mfma_f32_16x16x32_bf16 v[72:75], a[236:239], v[92:95], v[72:75]\n\t" \
  "v_max3_f32 v192, v192, v70, v71\n\t" \
  "ds_read_b64_tr_b16 a[174:175], v229 offset:12800\n\t" \
  "v_mfma_f32_16x16x32_bf16 v[76:79], a[252:255], v[92:95], v[184:187]\n\t" \
  "ds_read_b64_tr_b16 a[176:177], v228 offset:768\n\t" \
  "ds_read_b64_tr_b16 a[178:179], v228 offset:4864\n\t" \
  "v_mfma_f32_16x16x32_bf16 v[76:79], a[248:251], v[88:91], v[76:79]\n\t" \
  "v_exp_f32 v102, v102\n\t" \
  "v_mfma_f32_16x16x32_bf16 v[76:79], a[244:247], v[84:87], v[76:79]\n\t" \
  "v_max3_f32 v203, v203, v72, v73\n\t" \
  "v_max3_f32 v192, v192, v74, v75\n\t" \
  "v_mfma_f32_16x16x32_bf16 v[76:79], a[240:243], v[80:83], v[76:79]\n\t" \
  "v_exp_f32 v103, v103\n\t" \
  "v_mfma_f32_16x16x32_bf16 v[96:99], a[192:195], v[112:115], v[188:191]\n\t" \
  "v_cvt_pk_bf16_f32 v155, v102, v103\n\t" \
  "ds_read_b64_tr_b16 a[180:181], v228 offset:8960\n\t" \
  "v_mfma_f32_16x16x32_bf16 v[96:99], a[196:199], v[116:119], v[96:99]\n\t" \
  "v_exp_f32 v104, v104\n\t" \
  "v_mfma_f32_16x16x32_bf16 v[96:99], a[200:203], v[120:123], v[96:99]\n\t" \
  "ds_read_b64_tr_b16 a[182:183], v228 offset:13056\n\t" \
  "v_max3_f32 v203, v203, v76, v77\n\t" \
  "v_mfma_f32_16x16x32_bf16 v[96:99], a[204:207], v[124:127], v[96:99]\n\t" \
  "v_max3_f32 v192, v192, v78, v79\n\t" \
  "v_max_f32 v192, v192, v203\n\t" \
  "v_mfma_f32_16x16x32_bf16 v[100:103], a[220:223], v[124:127], v[188:191]\n\t" \
  "v_cmp_lt_f32 vcc, 0x42800000, v192\n\t" \
  "ds_read_b64_tr_b16 a[184:185], v229 offset:768\n\t" \
  "v_mfma_f32_16x16x32_bf16 v[100:103], a[216:219], v[120:123], v[100:103]\n\t" \
  "s_cbranch_vccnz .Lsf_c52\n\t" \
  ".Lsf_r52:\n\t" \
  "ds_read_b64_tr_b16 a[186:187], v229 offset:4864\n\t" \
  "v_mfma_f32_16x16x32_bf16 v[100:103], a[212:215], v[116:119], v[100:103]\n\t" \
  "v_max3_f32 v192, v96, v97, v98\n\t" \
  "ds_read_b64_tr_b16 a[188:189], v229 offset:8960\n\t" \
  "v_mfma_f32_16x16x32_bf16 v[100:103], a[208:211], v[112:115], v[100:103]\n\t" \
  "v_exp_f32 v105, v105\n\t" \
  "v_exp_f32 v106, v106\n\t" \
  "v_exp_f32 v107, v107\n\t" \
  "v_cvt_pk_bf16_f32 v156, v104, v105\n\t" \
  "v_cvt_pk_bf16_f32 v157, v106, v107\n\t" \
  "v_mfma_f32_16x16x32_bf16 v[104:107], a[224:227], v[112:115], v[188:191]\n\t" \
  "v_exp_f32 v108, v108\n\t" \
  "v_mfma_f32_16x16x32_bf16 v[104:107], a[228:231], v[116:119], v[104:107]\n\t" \
  "ds_read_b64_tr_b16 a[190:191], v229 offset:13056\n\t" \
  "v_max3_f32 v203, v99, v100, v101\n\t" \
  "v_mfma_f32_16x16x32_bf16 v[104:107], a[232:235], v[120:123], v[104:107]\n\t" \
  "v_max3_f32 v192, v192, v102, v103\n\t" \
  "v_mfma_f32_16x16x32_bf16 v[104:107], a[236:239], v[124:127], v[104:107]\n\t" \
  "v_exp_f32 v109, v109\n\t" \
  "v_exp_f32 v110, v110\n\t" \
  "v_exp_f32 v111, v111\n\t" \
  "v_cvt_pk_bf16_f32 v158, v108, v109\n\t" \
  "v_cvt_pk_bf16_f32 v159, v110, v111\n\t" \
  "v_mfma_f32_16x16x32_bf16 v[108:111], a[252:255], v[124:127], v[188:191]\n\t" \
  "v_exp_f32 v0, v0\n\t" \
  "v_mfma_f32_16x16x32_bf16 v[108:111], a[248:251], v[120:123], v[108:111]\n\t" \
  "v_exp_f32 v1, v1\n\t" \
  "v_mfma_f32_16x16x32_bf16 v[108:111], a[244:247], v[116:119], v[108:111]\n\t" \
  "v_max3_f32 v203, v203, v104, v105\n\t" \
  "v_max3_f32 v192, v192, v106, v107\n\t" \
  "v_mfma_f32_16x16x32_bf16 v[108:111], a[240:243], v[112:115], v[108:111]\n\t" \
  "v_exp_f32 v2, v2\n\t" \
  "s_waitcnt vmcnt(3) lgkmcnt(0)\n\t" \
  "s_barrier\n\t" \
  "v_mfma_f32_16x16x32_bf16 a[0:3], a[128:131], v[128:131], a[0:3]\n\t" \
  "v_exp_f32 v3, v3\n\t" \
  "v_mfma_f32_16x16x32_bf16 a[0:3], a[132:135], v[132:135], a[0:3]\n\t" \
  "v_exp_f32 v4, v4\n\t" \
  "v_mfma_f32_16x16x32_bf16 a[16:19], a[136:139], v[128:131], a[16:19]\n\t" \
  "ds_read_b128 a[192:195], v220 offset:0\n\t" \
  "v_max3_f32 v203, v203, v108, v109\n\t" \
  "v_mfma_f32_16x16x32_bf16 a[16:19], a[140:143], v[132:135], a[16:19]\n\t" \
  "v_max3_f32 v192, v192, v110, v111\n\t" \
  "v_max_f32 v192, v192, v203\n\t" \
  "v_mfma_f32_16x16x32_bf16 a[32:35], a[144:147], v[128:131], a[32:35]\n\t" \
  "v_cmp_lt_f32 vcc, 0x42800000, v192\n\t" \
  "ds_read_b128 a[196:199], v221 offset:0\n\t" \
  "v_mfma_f32_16x16x32_bf16 a[32:35], a[148:151], v[132:135], a[32:35]\n\t" \
  "s_cbranch_vccnz .Lsf_c53\n\t" \
  ".Lsf_r53:\n\t" \
  "ds_read_b128 a[200:203], v222 offset:0\n\t" \
  "v_mfma_f32_16x16x32_bf16 a[48:51], a[152:155], v[128:131], a[48:51]\n\t" \
  "v_exp_f32 v5, v5\n\t" \
  "v_mfma_f32_16x16x32_bf16 a[48:51], a[156:159], v[132:135], a[48:51]\n\t" \
  "v_exp_f32 v6, v6\n\t" \
  "v_mfma_f32_16x16x32_bf16 a[64:67], a[160:163], v[128:131], a[64:67]\n\t" \
  "v_exp_f32 v7, v7\n\t" \
  "v_mfma_f32_16x16x32_bf16 a[64:67], a[164:167], v[132:135], a[64:67]\n\t" \
  "v_exp_f32 v8, v8\n\t" \
  "v_mfma_f32_16x16x32_bf16 a[80:83], a[168:171], v[128:131], a[80:83]\n\t" \
  "v_exp_f32 v9, v9\n\t" \
  "v_mfma_f32_16x16x32_bf16 a[80:83], a[172:175], v[132:135], a[80:83]\n\t" \
  "v_exp_f32 v10, v10\n\t" \
  "v_mfma_f32_16x16x32_bf16 a[96:99], a[176:179], v[128:131], a[96:99]\n\t" \
  "v_exp_f32 v11, v11\n\t" \
  "v_mfma_f32_16x16x32_bf16 a[96:99], a[180:183], v[132:135], a[96:99]\n\t" \
  "v_exp_f32 v12, v12\n\t" \
  "v_mfma_f32_16x16x32_bf16 a[112:115], a[184:187], v[128:131], a[112:115]\n\t" \
  "ds_read_b128 a[204:207], v223 offset:0\n\t" \
  "ds_read_b128 a[208:211], v220 offset:4096\n\t" \
  "v_mfma_f32_16x16x32_bf16 a[112:115], a[188:191], v[132:135], a[112:115]\n\t" \
  "v_exp_f32 v13, v13\n\t" \
  "v_mfma_f32_16x16x32_bf16 v[160:163], v[216:219], v[128:131], v[160:163]\n\t" \
  "v_exp_f32 v14, v14\n\t" \
  "v_mfma_f32_16x16x32_bf16 v[160:163], v[216:219], v[132:135], v[160:163]\n\t" \
  "v_cvt_pk_bf16_f32 v128, v0, v1\n\t" \
  "v_cvt_pk_bf16_f32 v129, v2, v3\n\t" \
  "v_mfma_f32_16x16x32_bf16 a[4:7], a[128:131], v[136:139], a[4:7]\n\t" \
  "v_cvt_pk_bf16_f32 v130, v4, v5\n\t" \
  "v_cvt_pk_bf16_f32 v131, v6, v7\n\t" \
  "v_mfma_f32_16x16x32_bf16 a[4:7], a[132:135], v[140:143], a[4:7]\n\t" \
  "v_cvt_pk_bf16_f32 v132, v8, v9\n\t" \
  "v_cvt_pk_bf16_f32 v133, v10, v11\n\t" \
  "v_mfma_f32_16x16x32_bf16 a[20:23], a[136:139], v[136:139], a[20:23]\n\t" \
  "v_cvt_pk_bf16_f32 v134, v12, v13\n\t" \
  "ds_read_b128 a[212:215], v221 offset:4096\n\t" \
  "v_mfma_f32_16x16x32_bf16 a[20:23], a[140:143], v[140:143], a[20:23]\n\t" \
  "v_exp_f32 v15, v15\n\t" \
  "v_mfma_f32_16x16x32_bf16 a[36:39], a[144:147], v[136:139], a[36:39]\n\t" \
  "v_cvt_pk_bf16_f32 v135, v14, v15\n\t" \
  "s_add_u32 m0, %[m], 65536\n\t" \
  "v_mfma_f32_16x16x32_bf16 a[36:39], a[148:151], v[140:143], a[36:39]\n\t" \
  "v_exp_f32 v32, v32\n\t" \
  "v_mfma_f32_16x16x32_bf16 a[52:55], a[152:155], v[136:139], a[52:55]\n\t" \
  "ds_read_b128 a[216:219], v222 offset:4096\n\t" \
  "s_add_u32 s96, %[sofk], 114688\n\t" \
  "v_mfma_f32_16x16x32_bf16 a[52:55], a[156:159], v[140:143], a[52:55]\n\t" \
  "v_exp_f32 v33, v33\n\t" \
  "v_mfma_f32_16x16x32_bf16 a[68:71], a[160:163], v[136:139], a[68:71]\n\t" \
  "v_exp_f32 v34, v34\n\t" \
  "v_mfma_f32_16x16x32_bf16 a[68:71], a[164:167], v[140:143], a[68:71]\n\t" \
  "ds_read_b128 a[220:223], v223 offset:4096\n\t" \
  "buffer_load_dwordx4 v224, %[sK], s96 offen offset:0 lds\n\t" \
  "v_mfma_f32_16x16x32_bf16 a[84:87], a[168:171], v[136:139], a[84:87]\n\t" \
  "v_exp_f32 v35, v35\n\t" \
  "v_mfma_f32_16x16x32_bf16 a[84:87], a[172:175], v[140:143], a[84:87]\n\t" \
  "v_exp_f32 v36, v36\n\t" \
  "v_mfma_f32_16x16x32_bf16 a[100:103], a[176:179], v[136:139], a[100:103]\n\t" \
  "v_exp_f32 v37, v37\n\t" \
  "v_mfma_f32_16x16x32_bf16 a[100:103], a[180:183], v[140:143], a[100:103]\n\t" \
  "ds_read_b128 a[224:227], v220 offset:8192\n\t" \
  "buffer_load_dwordx4 v225, %[sK], s96 offen offset:1024 lds\n\t" \
  "v_mfma_f32_16x16x32_bf16 a[116:119], a[184:187], v[136:139], a[116:119]\n\t" \
  "v_exp_f32 v38, v38\n\t" \
  "v_mfma_f32_16x16x32_bf16 a[116:119], a[188:191], v[140:143], a[116:119]\n\t" \
  "v_exp_f32 v39, v39\n\t" \
  "v_mfma_f32_16x16x32_bf16 v[164:167], v[216:219], v[136:139], v[164:167]\n\t" \
  "buffer_load_dwordx4 v226, %[sK], s96 offen offset:2048 lds\n\t" \
  "ds_read_b128 a[228:231], v221 offset:8192\n\t" \
  "v_mfma_f32_16x16x32_bf16 v[164:167], v[216:219], v[140:143], v[164:167]\n\t" \
  "v_cvt_pk_bf16_f32 v136, v32, v33\n\t" \
  "v_cvt_pk_bf16_f32 v137, v34, v35\n\t" \
  "v_mfma_f32_16x16x32_bf16 a[8:11], a[128:131], v[144:147], a[8:11]\n\t" \
  "v_cvt_pk_bf16_f32 v138, v36, v37\n\t" \
  "v_cvt_pk_bf16_f32 v139, v38, v39\n\t" \
  "v_mfma_f32_16x16x32_bf16 a[8:11], a[132:135], v[148:151], a[8:11]\n\t" \
  "v_exp_f32 v40, v40\n\t" \
  "v_mfma_f32_16x16x32_bf16 a[24:27], a[136:139], v[144:147], a[24:27]\n\t" \
  "buffer_load_dwordx4 v227, %[sK], s96 offen offset:3072 lds\n\t" \
  "ds_read_b128 a[232:235], v222 offset:8192\n\t" \
  "v_mfma_f32_16x16x32_bf16 a[24:27], a[140:143], v[148:151], a[24:27]\n\t" \
  "v_exp_f32 v41, v41\n\t" \
  "v_mfma_f32_16x16x32_bf16 a[40:43], a[144:147], v[144:147], a[40:43]\n\t" \
  "v_cvt_pk_bf16_f32 v140, v40, v41\n\t" \
  "ds_read_b128 a[236:239], v223 offset:8192\n\t" \
  "v_mfma_f32_16x16x32_bf16 a[40:43], a[148:151], v[148:151], a[40:43]\n\t" \
  "v_exp_f32 v42, v42\n\t" \
  "v_mfma_f32_16x16x32_bf16 a[56:59], a[152:155], v[144:147], a[56:59]\n\t" \
  "v_exp_f32 v43, v43\n\t" \
  "v_mfma_f32_16x16x32_bf16 a[56:59], a[156:159], v[148:151], a[56:59]\n\t" \
  "v_cvt_pk_bf16_f32 v141, v42, v43\n\t" \
  "ds_read_b128 a[240:243], v220 offset:12288\n\t" \
  "v_mfma_f32_16x16x32_bf16 a[72:75], a[160:163], v[144:147], a[72:75]\n\t" \
  "s_add_u32 m0, %[m], 98304\n\t" \
  "s_mov_b32 s96, 28672\n\t" \
  "buffer_load_dwordx4 v232, %[sG], s96 offen offset:0 lds\n\t" \
  "v_mfma_f32_16x16x32_bf16 a[72:75], a[164:167], v[148:151], a[72:75]\n\t" \
  "v_exp_f32 v44, v44\n\t" \
  "v_mfma_f32_16x16x32_bf16 a[88:91], a[168:171], v[144:147], a[88:91]\n\t" \
  "s_add_u32 m0, %[m], 81920\n\t" \
  "s_add_u32 s96, %[sofk], 98304\n\t" \
  "v_mfma_f32_16x16x32_bf16 a[88:91], a[172:175], v[148:151], a[88:91]\n\t" \
  "v_exp_f32 v45, v45\n\t" \
  "v_mfma_f32_16x16x32_bf16 a[104:107], a[176:179], v[144:147], a[104:107]\n\t" \
  "v_cvt_pk_bf16_f32 v142, v44, v45\n\t" \
  "ds_read_b128 a[244:247], v221 offset:12288\n\t" \
  "v_mfma_f32_16x16x32_bf16 a[104:107], a[180:183], v[148:151], a[104:107]\n\t" \
  "v_exp_f32 v46, v46\n\t" \
  "v_mfma_f32_16x16x32_bf16 a[120:123], a[184:187], v[144:147], a[120:123]\n\t" \
  "v_exp_f32 v47, v47\n\t" \
  "v_mfma_f32_16x16x32_bf16 a[120:123], a[188:191], v[148:151], a[120:123]\n\t" \
  "v_cvt_pk_bf16_f32 v143, v46, v47\n\t" \
  "buffer_load_dwordx4 v230, %[sV], s96 offen offset:0 lds\n\t" \
  "v_mfma_f32_16x16x32_bf16 v[168:171], v[216:219], v[144:147], v[168:171]\n\t" \
  "v_exp_f32 v64, v64\n\t" \
  "v_mfma_f32_16x16x32_bf16 v[168:171], v[216:219], v[148:151], v[168:171]\n\t" \
  "v_exp_f32 v65, v65\n\t" \
  "v_mfma_f32_16x16x32_bf16 a[12:15], a[128:131], v[152:155], a[12:15]\n\t" \
  "v_cvt_pk_bf16_f32 v144, v64, v65\n\t" \
  "ds_read_b128 a[248:251], v222 offset:12288\n\t" \
  "v_mfma_f32_16x16x32_bf16 a[12:15], a[132:135], v[156:159], a[12:15]\n\t" \
  "v_exp_f32 v66, v66\n\t" \
  "v_mfma_f32_16x16x32_bf16 a[28:31], a[136:139], v[152:155], a[28:31]\n\t" \
  "v_exp_f32 v67, v67\n\t" \
  "v_mfma_f32_16x16x32_bf16 a[28:31], a[140:143], v[156:159], a[28:31]\n\t" \
  "v_cvt_pk_bf16_f32 v145, v66, v67\n\t" \
  "buffer_load_dwordx4 v231, %[sV], s96 offen offset:1024 lds\n\t" \
  "v_mfma_f32_16x16x32_bf16 a[44:47], a[144:147], v[152:155], a[44:47]\n\t" \
  "ds_read_b128 a[252:255], v223 offset:12288\n\t" \
  "buffer_load_dwordx4 v230, %[sV], s96 offen offset:2048 lds\n\t" \
  "v_mfma_f32_16x16x32_bf16 a[44:47], a[148:151], v[156:159], a[44:47]\n\t" \
  "v_exp_f32 v68, v68\n\t" \
  "v_mfma_f32_16x16x32_bf16 a[60:63], a[152:155], v[152:155], a[60:63]\n\t" \
  "v_exp_f32 v69, v69\n\t" \
  "v_mfma_f32_16x16x32_bf16 a[60:63], a[156:159], v[156:159], a[60:63]\n\t" \
  "v_cvt_pk_bf16_f32 v146, v68, v69\n\t" \
  "buffer_load_dwordx4 v231, %[sV], s96 offen offset:3072 lds\n\t" \
  "v_mfma_f32_16x16x32_bf16 a[76:79], a[160:163], v[152:155], a[76:79]\n\t" \
  "v_exp_f32 v70, v70\n\t" \
  "v_mfma_f32_16x16x32_bf16 a[76:79], a[164:167], v[156:159], a[76:79]\n\t" \
  "v_exp_f32 v71, v71\n\t" \
  "v_mfma_f32_16x16x32_bf16 a[92:95], a[168:171], v[152:155], a[92:95]\n\t" \
  "v_cvt_pk_bf16_f32 v147, v70, v71\n\t" \
  "v_mfma_f32_16x16x32_bf16 a[92:95], a[172:175], v[156:159], a[92:95]\n\t" \
  "v_lshrrev_b32 v205, 1, v233\n\t" \
  "v_lshlrev_b32 v205, 2, v205\n\t" \
  "v_mfma_f32_16x16x32_bf16 a[108:111], a[176:179], v[152:155], a[108:111]\n\t" \
  "v_add_u32 v205, 8192, v205\n\t" \
  "buffer_load_dword v209, v205, %[ws], 0 offen sc1\n\t" \
  "v_mfma_f32_16x16x32_bf16 a[108:111], a[180:183], v[156:159], a[108:111]\n\t" \
  "s_add_u32 m0, %[m], 98304\n\t" \
  "s_mov_b32 s96, 34816\n\t" \
  "buffer_load_dwordx4 v232, %[sG], s96 offen offset:1024 lds\n\t" \
  "v_mfma_f32_16x16x32_bf16 a[124:127], a[184:187], v[152:155], a[124:127]\n\t" \
  "v_exp_f32 v72, v72\n\t" \
  "v_mfma_f32_16x16x32_bf16 a[124:127], a[188:191], v[156:159], a[124:127]\n\t" \
  "v_exp_f32 v73, v73\n\t" \
  "v_mfma_f32_16x16x32_bf16 v[172:175], v[216:219], v[152:155], v[172:175]\n\t" \
  "v_cvt_pk_bf16_f32 v148, v72, v73\n\t" \
  "v_mfma_f32_16x16x32_bf16 v[172:175], v[216:219], v[156:159], v[172:175]\n\t" \
  "v_exp_f32 v74, v74\n\t" \
  "s_cmp_lg_u32 s99, 0\n\t" \
  "s_cbranch_scc1 .Lsf_p5\n\t" \
  ".Lsf_q5:\n\t" \
  "s_waitcnt lgkmcnt(0)\n\t" \
  "v_mfma_f32_16x16x32_bf16 v[0:3], a[192:195], v[16:19], v[176:179]\n\t" \
  "s_add_u32 s98, %[pf2], 49152\n\t" \
  "buffer_load_dword %[pfd], v202, %[sM], s98 offen\n\t" \
  "v_mfma_f32_16x16x32_bf16 v[0:3], a[196:199], v[20:23], v[0:3]\n\t" \
  "v_exp_f32 v75, v75\n\t" \
  "v_mfma_f32_16x16x32_bf16 v[0:3], a[200:203], v[24:27], v[0:3]\n\t" \
  "v_cvt_pk_bf16_f32 v149, v74, v75\n\t" \
  "ds_read_b64_tr_b16 a[128:129], v237 offset:0\n\t" \
  "v_mfma_f32_16x16x32_bf16 v[0:3], a[204:207], v[28:31], v[0:3]\n\t" \
  "v_exp_f32 v76, v76\n\t" \
  "v_mfma_f32_16x16x32_bf16 v[4:7], a[220:223], v[28:31], v[176:179]\n\t" \
  "ds_read_b64_tr_b16 a[130:131], v237 offset:4096\n\t" \
  "ds_read_b64_tr_b16 a[132:133], v237 offset:8192\n\t" \
  "v_mfma_f32_16x16x32_bf16 v[4:7], a[216:219], v[24:27], v[4:7]\n\t" \
  "v_exp_f32 v77, v77\n\t" \
  "v_mfma_f32_16x16x32_bf16 v[4:7], a[212:215], v[20:23], v[4:7]\n\t" \
  "v_cvt_pk_bf16_f32 v150, v76, v77\n\t" \
  "v_max3_f32 v192, v0, v1, v2\n\t" \
  "v_mfma_f32_16x16x32_bf16 v[4:7], a[208:211], v[16:19], v[4:7]\n\t" \
  "v_exp_f32 v78, v78\n\t" \
  "v_mfma_f32_16x16x32_bf16 v[8:11], a[224:227], v[16:19], v[176:179]\n\t" \
  "ds_read_b64_tr_b16 a[134:135], v237 offset:12288\n\t" \
  "ds_read_b64_tr_b16 a[136:137], v238 offset:0\n\t" \
  "v_mfma_f32_16x16x32_bf16 v[8:11], a[228:231], v[20:23], v[8:11]\n\t" \
  "v_exp_f32 v79, v79\n\t" \
  "v_mfma_f32_16x16x32_bf16 v[8:11], a[232:235], v[24:27], v[8:11]\n\t" \
  "v_cvt_pk_bf16_f32 v151, v78, v79\n\t" \
  "v_max3_f32 v203, v3, v4, v5\n\t" \
  "v_mfma_f32_16x16x32_bf16 v[8:11], a[236:239], v[28:31], v[8:11]\n\t" \
  "v_max3_f32 v192, v192, v6, v7\n\t" \
  "ds_read_b64_tr_b16 a[138:139], v238 offset:4096\n\t" \
  "v_mfma_f32_16x16x32_bf16 v[12:15], a[252:255], v[28:31], v[176:179]\n\t" \
  "ds_read_b64_tr_b16 a[140:141], v238 offset:8192\n\t" \
  "ds_read_b64_tr_b16 a[142:143], v238 offset:12288\n\t" \
  "v_mfma_f32_16x16x32_bf16 v[12:15], a[248:251], v[24:27], v[12:15]\n\t" \
  "v_exp_f32 v96, v96\n\t" \
  "v_mfma_f32_16x16x32_bf16 v[12:15], a[244:247], v[20:23], v[12:15]\n\t" \
  "v_max3_f32 v203, v203, v8, v9\n\t" \
  "v_max3_f32 v192, v192, v10, v11\n\t" \
  "v_mfma_f32_16x16x32_bf16 v[12:15], a[240:243], v[16:19], v[12:15]\n\t" \
  "s_add_u32 s98, %[pf2], 73728\n\t" \
  "buffer_load_dword %[pfd], v202, %[sM], s98 offen\n\t" \
  "v_mfma_f32_16x16x32_bf16 v[32:35], a[192:195], v[48:51], v[180:183]\n\t" \
  "ds_read_b64_tr_b16 a[144:145], v237 offset:256\n\t" \
  "ds_read_b64_tr_b16 a[146:147], v237 offset:4352\n\t" \
  "v_mfma_f32_16x16x32_bf16 v[32:35], a[196:199], v[52:55], v[32:35]\n\t" \
  "v_exp_f32 v97, v97\n\t" \
  "v_mfma_f32_16x16x32_bf16 v[32:35], a[200:203], v[56:59], v[32:35]\n\t" \
  "v_cvt_pk_bf16_f32 v152, v96, v97\n\t" \
  "v_max3_f32 v203, v203, v12, v13\n\t" \
  "v_mfma_f32_16x16x32_bf16 v[32:35], a[204:207], v[60:63], v[32:35]\n\t" \
  "v_max3_f32 v192, v192, v14, v15\n\t" \
  "v_max_f32 v192, v192, v203\n\t" \
  "v_mfma_f32_16x16x32_bf16 v[36:39], a[220:223], v[60:63], v[180:183]\n\t" \
  "v_cmp_lt_f32 vcc, 0x42800000, v192\n\t" \
  "ds_read_b64_tr_b16 a[148:149], v237 offset:8448\n\t" \
  "v_mfma_f32_16x16x32_bf16 v[36:39], a[216:219], v[56:59], v[36:39]\n\t" \
  "s_cbranch_vccnz .Lsf_c60\n\t" \
  ".Lsf_r60:\n\t" \
  "ds_read_b64_tr_b16 a[150:151], v237 offset:12544\n\t" \
  "v_mfma_f32_16x16x32_bf16 v[36:39], a[212:215], v[52:55], v[36:39]\n\t" \
  "v_max3_f32 v192, v32, v33, v34\n\t" \
  "ds_read_b64_tr_b16 a[152:153], v238 offset:256\n\t" \
  "v_mfma_f32_16x16x32_bf16 v[36:39], a[208:211], v[48:51], v[36:39]\n\t" \
  "v_exp_f32 v98, v98\n\t" \
  "v_mfma_f32_16x16x32_bf16 v[40:43], a[224:227], v[48:51], v[180:183]\n\t" \
  "v_exp_f32 v99, v99\n\t" \
  "v_mfma_f32_16x16x32_bf16 v[40:43], a[228:231], v[52:55], v[40:43]\n\t" \
  "v_cvt_pk_bf16_f32 v153, v98, v99\n\t" \
  "ds_read_b64_tr_b16 a[154:155], v238 offset:4352\n\t" \
  "v_mfma_f32_16x16x32_bf16 v[40:43], a[232:235], v[56:59], v[40:43]\n\t" \
  "v_exp_f32 v100, v100\n\t" \
  "v_mfma_f32_16x16x32_bf16 v[40:43], a[236:239], v[60:63], v[40:43]\n\t" \
  "v_max3_f32 v203, v35, v36, v37\n\t" \
  "v_max3_f32 v192, v192, v38, v39\n\t" \
  "v_mfma_f32_16x16x32_bf16 v[44:47], a[252:255], v[60:63], v[180:183]\n\t" \
  "ds_read_b64_tr_b16 a[156:157], v238 offset:8448\n\t" \
  "ds_read_b64_tr_b16 a[158:159], v238 offset:12544\n\t" \
  "v_mfma_f32_16x16x32_bf16 v[44:47], a[248:251], v[56:59], v[44:47]\n\t" \
  "v_exp_f32 v101, v101\n\t" \
  "v_mfma_f32_16x16x32_bf16 v[44:47], a[244:247], v[52:55], v[44:47]\n\t" \
  "v_cvt_pk_bf16_f32 v154, v100, v101\n\t" \
  "ds_read_b64_tr_b16 a[160:161], v237 offset:512\n\t" \
  "v_mfma_f32_16x16x32_bf16 v[44:47], a[240:243], v[48:51], v[44:47]\n\t" \
  "v_max3_f32 v203, v203, v40, v41\n\t" \
  "v_max3_f32 v192, v192, v42, v43\n\t" \
  "v_mfma_f32_16x16x32_bf16 v[64:67], a[192:195], v[80:83], v[184:187]\n\t" \
  "ds_read_b64_tr_b16 a[162:163], v237 offset:4608\n\t" \
  "ds_read_b64_tr_b16 a[164:165], v237 offset:8704\n\t" \
  "v_mfma_f32_16x16x32_bf16 v[64:67], a[196:199], v[84:87], v[64:67]\n\t" \
  "v_exp_f32 v102, v102\n\t" \
  "v_mfma_f32_16x16x32_bf16 v[64:67], a[200:203], v[88:91], v[64:67]\n\t" \
  "v_max3_f32 v203, v203, v44, v45\n\t" \
  "v_max3_f32 v192, v192, v46, v47\n\t" \
  "v_mfma_f32_16x16x32_bf16 v[64:67], a[204:207], v[92:95], v[64:67]\n\t" \
  "v_max_f32 v192, v192, v203\n\t" \
  "ds_read_b64_tr_b16 a[166:167], v237 offset:12800\n\t" \
  "v_mfma_f32_16x16x32_bf16 v[68:71], a[220:223], v[92:95], v[184:187]\n\t" \
  "v_cmp_lt_f32 vcc, 0x42800000, v192\n\t" \
  "ds_read_b64_tr_b16 a[168:169], v238 offset:512\n\t" \
  "v_mfma_f32_16x16x32_bf16 v[68:71], a[216:219], v[88:91], v[68:71]\n\t" \
  "s_cbranch_vccnz .Lsf_c61\n\t" \
  ".Lsf_r61:\n\t" \
  "ds_read_b64_tr_b16 a[170:171], v238 offset:4608\n\t" \
  "v_mfma_f32_16x16x32_bf16 v[68:71], a[212:215], v[84:87], v[68:71]\n\t" \
  "v_max3_f32 v192, v64, v65, v66\n\t" \
  "ds_read_b64_tr_b16 a[172:173], v238 offset:8704\n\t" \
  "v_mfma_f32_16x16x32_bf16 v[68:71], a[208:211], v[80:83], v[68:71]\n\t" \
  "v_exp_f32 v103, v103\n\t" \
  "v_mfma_f32_16x16x32_bf16 v[72:75], a[224:227], v[80:83], v[184:187]\n\t" \
  "v_cvt_pk_bf16_f32 v155, v102, v103\n\t" \
  "ds_read_b64_tr_b16 a[174:175], v238 offset:12800\n\t" \
  "v_mfma_f32_16x16x32_bf16 v[72:75], a[228:231], v[84:87], v[72:75]\n\t" \
  "v_exp_f32 v104, v104\n\t" \
  "v_mfma_f32_16x16x32_bf16 v[72:75], a[232:235], v[88:91], v[72:75]\n\t" \
  "s_lshr_b32 s98, %[hl18], 18\n\t" \
  "v_xor_b32 v245, s98, v233\n\t" \
  "v_mfma_f32_16x16x32_bf16 v[72:75], a[236:239], v[92:95], v[72:75]\n\t" \
  "v_max3_f32 v203, v67, v68, v69\n\t" \
  "v_max3_f32 v192, v192, v70, v71\n\t" \
  "v_mfma_f32_16x16x32_bf16 v[76:79], a[252:255], v[92:95], v[184:187]\n\t" \
  "ds_read_b64_tr_b16 a[176:177], v237 offset:768\n\t" \
  "ds_read_b64_tr_b16 a[178:179], v237 offset:4864\n\t" \
  "v_mfma_f32_16x16x32_bf16 v[76:79], a[248:251], v[88:91], v[76:79]\n\t" \
  "s_waitcnt vmcnt(3)\n\t" \
  "v_and_b32 v206, 0xbfffbfff, v209\n\t" \
  "v_cmp_ne_u32_e64 s[96:97], %[tag], v206\n\t" \
  "s_cmp_lg_u64 s[96:97], 0\n\t" \
  "s_cbranch_scc1 .Lsf_v\n\t" \
  ".Lsf_w:\n\t" \
  "v_mfma_f32_16x16x32_bf16 v[76:79], a[244:247], v[84:87], v[76:79]\n\t" \
  "v_and_b32 v206, 1, v233\n\t" \
  "v_max3_f32 v203, v203, v72, v73\n\t" \
  "v_mfma_f32_16x16x32_bf16 v[76:79], a[240:243], v[80:83], v[76:79]\n\t" \
  "v_max3_f32 v192, v192, v74, v75\n\t" \
  "v_lshlrev_b32 v206, 4, v206\n\t" \
  "v_mfma_f32_16x16x32_bf16 v[96:99], a[192:195], v[112:115], v[188:191]\n\t" \
  "v_lshrrev_b32 v206, v206, v209\n\t" \
  "v_and_b32 v245, 31, v245\n\t" \
  "v_mfma_f32_16x16x32_bf16 v[96:99], a[196:199], v[116:119], v[96:99]\n\t" \
  "ds_read_b64_tr_b16 a[180:181], v237 offset:8960\n\t" \
  "v_and_b32 v206, 0x4000, v206\n\t" \
  "v_mfma_f32_16x16x32_bf16 v[96:99], a[200:203], v[120:123], v[96:99]\n\t" \
  "v_cmp_ne_u32_e64 s[96:97], 0, v206\n\t" \
  "v_mfma_f32_16x16x32_bf16 v[96:99], a[204:207], v[124:127], v[96:99]\n\t" \
  "ds_read_b64_tr_b16 a[182:183], v237 offset:13056\n\t" \
  "v_max3_f32 v203, v203, v76, v77\n\t" \
  "v_mfma_f32_16x16x32_bf16 v[100:103], a[220:223], v[124:127], v[188:191]\n\t" \
  "v_max3_f32 v192, v192, v78, v79\n\t" \
  "v_max_f32 v192, v192, v203\n\t" \
  "v_mfma_f32_16x16x32_bf16 v[100:103], a[216:219], v[120:123], v[100:103]\n\t" \
  "v_cmp_lt_f32 vcc, 0x42800000, v192\n\t" \
  "s_bcnt1_i32_b32 %[nb], s96\n\t" \
  "v_mfma_f32_16x16x32_bf16 v[100:103], a[212:215], v[116:119], v[100:103]\n\t" \
  "s_cbranch_vccnz .Lsf_c62\n\t" \
  ".Lsf_r62:\n\t" \
  "ds_read_b64_tr_b16 a[184:185], v238 offset:768\n\t" \
  "v_mfma_f32_16x16x32_bf16 v[100:103], a[208:211], v[112:115], v[100:103]\n\t" \
  "s_bcnt1_i32_b32 %[na], s97\n\t" \
  "s_lshr_b32 s96, s96, 16\n\t" \
  "v_exp_f32 v105, v105\n\t" \
  "v_exp_f32 v106, v106\n\t" \
  "v_exp_f32 v107, v107\n\t" \
  "v_cvt_pk_bf16_f32 v156, v104, v105\n\t" \
  "v_cvt_pk_bf16_f32 v157, v106, v107\n\t" \
  "v_mfma_f32_16x16x32_bf16 v[104:107], a[224:227], v[112:115], v[188:191]\n\t" \
  "s_lshr_b32 s98, %[hl18], 18\n\t" \
  "s_mul_i32 s98, s98, 127\n\t" \
  "v_add_lshl_u32 v245, v245, s98, 2\n\t" \
  "v_mfma_f32_16x16x32_bf16 v[104:107], a[228:231], v[116:119], v[104:107]\n\t" \
  "ds_read_b64_tr_b16 a[186:187], v238 offset:4864\n\t" \
  "s_lshr_b32 s97, s97, 16\n\t" \
  "v_mfma_f32_16x16x32_bf16 v[104:107], a[232:235], v[120:123], v[104:107]\n\t" \
  "v_max3_f32 v192, v96, v97, v98\n\t" \
  "s_bcnt1_i32_b32 s96, s96\n\t" \
  "v_mfma_f32_16x16x32_bf16 v[104:107], a[236:239], v[124:127], v[104:107]\n\t" \
  "ds_read_b64_tr_b16 a[188:189], v238 offset:8960\n\t" \
  "s_bcnt1_i32_b32 s97, s97\n\t" \
  "v_exp_f32 v108, v108\n\t" \
  "v_exp_f32 v109, v109\n\t" \
  "v_exp_f32 v110, v110\n\t" \
  "v_exp_f32 v111, v111\n\t" \
  "v_cvt_pk_bf16_f32 v158, v108, v109\n\t" \
  "v_cvt_pk_bf16_f32 v159, v110, v111\n\t" \
  "v_mfma_f32_16x16x32_bf16 v[108:111], a[252:255], v[124:127], v[188:191]\n\t" \
  "s_sub_u32 s96, s97, s96\n\t" \
  "ds_read_b64_tr_b16 a[190:191], v238 offset:13056\n\t" \
  "v_mfma_f32_16x16x32_bf16 v[108:111], a[248:251], v[120:123], v[108:111]\n\t" \
  "s_sub_u32 %[nb], %[na], %[nb]\n\t" \
  "v_max3_f32 v203, v99, v100, v101\n\t" \
  "v_mfma_f32_16x16x32_bf16 v[108:111], a[244:247], v[116:119], v[108:111]\n\t" \
  "v_max3_f32 v192, v192, v102, v103\n\t" \
  "s_ashr_i32 %[nb], %[nb], 31\n\t" \
  "v_mfma_f32_16x16x32_bf16 v[108:111], a[240:243], v[112:115], v[108:111]\n\t" \
  "s_xor_b32 s96, s96, %[nb]\n\t" \
  "s_sub_u32 s96, s96, %[nb]\n\t" \
  "s_waitcnt lgkmcnt(0)\n\t" \
  "s_barrier\n\t" \
  "v_mfma_f32_16x16x32_bf16 a[0:3], a[128:131], v[128:131], a[0:3]\n\t" \
  "s_sub_u32 %[na], 6, s96\n\t" \
  "v_max3_f32 v203, v203, v104, v105\n\t" \
  "v_mfma_f32_16x16x32_bf16 a[0:3], a[132:135], v[132:135], a[0:3]\n\t" \
  "v_max3_f32 v192, v192, v106, v107\n\t" \
  "s_ashr_i32 %[na], %[na], 31\n\t" \
  "v_mfma_f32_16x16x32_bf16 a[16:19], a[136:139], v[128:131], a[16:19]\n\t" \
  "s_and_b32 %[nb], %[nb], 0x2000\n\t" \
  "s_xor_b32 s96, %[po], %[nb]\n\t" \
  "v_mfma_f32_16x16x32_bf16 a[16:19], a[140:143], v[132:135], a[16:19]\n\t" \
  "s_and_b32 s96, s96, %[na]\n\t" \
  "ds_read_b128 a[192:195], v220 offset:16384\n\t" \
  "v_mfma_f32_16x16x32_bf16 a[32:35], a[144:147], v[128:131], a[32:35]\n\t" \
  "v_max3_f32 v203, v203, v108, v109\n\t" \
  "v_max3_f32 v192, v192, v110, v111\n\t" \
  "v_mfma_f32_16x16x32_bf16 a[32:35], a[148:151], v[132:135], a[32:35]\n\t" \
  "v_max_f32 v192, v192, v203\n\t" \
  "ds_read_b128 a[196:199], v221 offset:16384\n\t" \
  "v_mfma_f32_16x16x32_bf16 a[48:51], a[152:155], v[128:131], a[48:51]\n\t" \
  "v_cmp_lt_f32 vcc, 0x42800000, v192\n\t" \
  "ds_read_b128 a[200:203], v222 offset:16384\n\t" \
  "v_mfma_f32_16x16x32_bf16 a[48:51], a[156:159], v[132:135], a[48:51]\n\t" \
  "s_cbranch_vccnz .Lsf_c63\n\t" \
  ".Lsf_r63:\n\t" \
  "ds_read_b128 a[204:207], v223 offset:16384\n\t" \
  "v_mfma_f32_16x16x32_bf16 a[64:67], a[160:163], v[128:131], a[64:67]\n\t" \
  "s_xor_b32 %[eso], %[eso], s96\n\t" \
  "v_mfma_f32_16x16x32_bf16 a[64:67], a[164:167], v[132:135], a[64:67]\n\t" \
  "s_xor_b32 s96, %[px], %[nb]\n\t" \
  "s_and_b32 s96, s96, %[na]\n\t" \
  "v_mfma_f32_16x16x32_bf16 a[80:83], a[168:171], v[128:131], a[80:83]\n\t" \
  "s_xor_b32 %[xcb], %[xcb], s96\n\t" \
  "s_xor_b32 %[frb], %[frb], %[nb]\n\t" \
  "v_mfma_f32_16x16x32_bf16 a[80:83], a[172:175], v[132:135], a[80:83]\n\t" \
  "s_xor_b32 %[fw2], %[fw2], %[nb]\n\t" \
  "ds_read_b128 a[208:211], v220 offset:20480\n\t" \
  "v_mfma_f32_16x16x32_bf16 a[96:99], a[176:179], v[128:131], a[96:99]\n\t" \
  "v_exp_f32 v0, v0\n\t" \
  "v_mfma_f32_16x16x32_bf16 a[96:99], a[180:183], v[132:135], a[96:99]\n\t" \
  "v_exp_f32 v1, v1\n\t" \
  "v_mfma_f32_16x16x32_bf16 a[112:115], a[184:187], v[128:131], a[112:115]\n\t" \
  "v_exp_f32 v2, v2\n\t" \
  "v_mfma_f32_16x16x32_bf16 a[112:115], a[188:191], v[132:135], a[112:115]\n\t" \
  "v_exp_f32 v3, v3\n\t" \
  "v_mfma_f32_16x16x32_bf16 v[160:163], v[216:219], v[128:131], v[160:163]\n\t" \
  "v_exp_f32 v4, v4\n\t" \
  "v_mfma_f32_16x16x32_bf16 v[160:163], v[216:219], v[132:135], v[160:163]\n\t" \
  "v_cvt_pk_bf16_f32 v128, v0, v1\n\t" \
  "v_cvt_pk_bf16_f32 v129, v2, v3\n\t" \
  "v_mfma_f32_16x16x32_bf16 a[4:7], a[128:131], v[136:139], a[4:7]\n\t" \
  "v_exp_f32 v5, v5\n\t" \
  "v_mfma_f32_16x16x32_bf16 a[4:7], a[132:135], v[140:143], a[4:7]\n\t" \
  "v_cvt_pk_bf16_f32 v130, v4, v5\n\t" \
  "ds_read_b128 a[212:215], v221 offset:20480\n\t" \
  "v_mfma_f32_16x16x32_bf16 a[20:23], a[136:139], v[136:139], a[20:23]\n\t" \
  "v_exp_f32 v6, v6\n\t" \
  "v_mfma_f32_16x16x32_bf16 a[20:23], a[140:143], v[140:143], a[20:23]\n\t" \
  "v_exp_f32 v7, v7\n\t" \
  "v_mfma_f32_16x16x32_bf16 a[36:39], a[144:147], v[136:139], a[36:39]\n\t" \
  "v_cvt_pk_bf16_f32 v131, v6, v7\n\t" \
  "s_add_u32 m0, %[m], 147456\n\t" \
  "v_mfma_f32_16x16x32_bf16 a[36:39], a[148:151], v[140:143], a[36:39]\n\t" \
  "v_exp_f32 v8, v8\n\t" \
  "v_mfma_f32_16x16x32_bf16 a[52:55], a[152:155], v[136:139], a[52:55]\n\t" \
  "ds_read_b128 a[216:219], v222 offset:20480\n\t" \
  "s_add_u32 s96, %[sofk], 114688\n\t" \
  "v_mfma_f32_16x16x32_bf16 a[52:55], a[156:159], v[140:143], a[52:55]\n\t" \
  "v_exp_f32 v9, v9\n\t" \
  "v_mfma_f32_16x16x32_bf16 a[68:71], a[160:163], v[136:139], a[68:71]\n\t" \
  "v_cvt_pk_bf16_f32 v132, v8, v9\n\t" \
  "ds_read_b128 a[220:223], v223 offset:20480\n\t" \
  "v_mfma_f32_16x16x32_bf16 a[68:71], a[164:167], v[140:143], a[68:71]\n\t" \
  "v_exp_f32 v10, v10\n\t" \
  "v_mfma_f32_16x16x32_bf16 a[84:87], a[168:171], v[136:139], a[84:87]\n\t" \
  "v_exp_f32 v11, v11\n\t" \
  "v_mfma_f32_16x16x32_bf16 a[84:87], a[172:175], v[140:143], a[84:87]\n\t" \
  "v_cvt_pk_bf16_f32 v133, v10, v11\n\t" \
  "buffer_load_dwordx4 v230, %[sV], s96 offen offset:0 lds\n\t" \
  "v_mfma_f32_16x16x32_bf16 a[100:103], a[176:179], v[136:139], a[100:103]\n\t" \
  "v_exp_f32 v12, v12\n\t" \
  "v_mfma_f32_16x16x32_bf16 a[100:103], a[180:183], v[140:143], a[100:103]\n\t" \
  "ds_read_b128 a[224:227], v220 offset:24576\n\t" \
  "buffer_load_dwordx4 v231, %[sV], s96 offen offset:1024 lds\n\t" \
  "v_mfma_f32_16x16x32_bf16 a[116:119], a[184:187], v[136:139], a[116:119]\n\t" \
  "v_exp_f32 v13, v13\n\t" \
  "v_mfma_f32_16x16x32_bf16 a[116:119], a[188:191], v[140:143], a[116:119]\n\t" \
  "v_cvt_pk_bf16_f32 v134, v12, v13\n\t" \
  "buffer_load_dwordx4 v230, %[sV], s96 offen offset:2048 lds\n\t" \
  "v_mfma_f32_16x16x32_bf16 v[164:167], v[216:219], v[136:139], v[164:167]\n\t" \
  "v_exp_f32 v14, v14\n\t" \
  "v_mfma_f32_16x16x32_bf16 v[164:167], v[216:219], v[140:143], v[164:167]\n\t" \
  "v_exp_f32 v15, v15\n\t" \
  "v_mfma_f32_16x16x32_bf16 a[8:11], a[128:131], v[144:147], a[8:11]\n\t" \
  "v_cvt_pk_bf16_f32 v135, v14, v15\n\t" \
  "ds_read_b128 a[228:231], v221 offset:24576\n\t" \
  "v_mfma_f32_16x16x32_bf16 a[8:11], a[132:135], v[148:151], a[8:11]\n\t" \
  "v_exp_f32 v32, v32\n\t" \
  "v_mfma_f32_16x16x32_bf16 a[24:27], a[136:139], v[144:147], a[24:27]\n\t" \
  "buffer_load_dwordx4 v231, %[sV], s96 offen offset:3072 lds\n\t" \
  "ds_read_b128 a[232:235], v222 offset:24576\n\t" \
  "v_mfma_f32_16x16x32_bf16 a[24:27], a[140:143], v[148:151], a[24:27]\n\t" \
  "v_exp_f32 v33, v33\n\t" \
  "v_mfma_f32_16x16x32_bf16 a[40:43], a[144:147], v[144:147], a[40:43]\n\t" \
  "v_cvt_pk_bf16_f32 v136, v32, v33\n\t" \
  "s_add_u32 m0, %[m], 49152\n\t" \
  "v_mfma_f32_16x16x32_bf16 a[40:43], a[148:151], v[148:151], a[40:43]\n\t" \
  "v_exp_f32 v34, v34\n\t" \
  "v_mfma_f32_16x16x32_bf16 a[56:59], a[152:155], v[144:147], a[56:59]\n\t" \
  "v_exp_f32 v35, v35\n\t" \
  "v_mfma_f32_16x16x32_bf16 a[56:59], a[156:159], v[148:151], a[56:59]\n\t" \
  "v_cvt_pk_bf16_f32 v137, v34, v35\n\t" \
  "ds_read_b128 a[236:239], v223 offset:24576\n\t" \
  "v_mfma_f32_16x16x32_bf16 a[72:75], a[160:163], v[144:147], a[72:75]\n\t" \
  "v_exp_f32 v36, v36\n\t" \
  "v_mfma_f32_16x16x32_bf16 a[72:75], a[164:167], v[148:151], a[72:75]\n\t" \
  "s_xor_b32 s97, %[wob], 262144\n\t" \
  "ds_read_b128 a[240:243], v220 offset:28672\n\t" \
  "v_mfma_f32_16x16x32_bf16 a[88:91], a[168:171], v[144:147], a[88:91]\n\t" \
  "v_exp_f32 v37, v37\n\t" \
  "v_mfma_f32_16x16x32_bf16 a[88:91], a[172:175], v[148:151], a[88:91]\n\t" \
  "v_cvt_pk_bf16_f32 v138, v36, v37\n\t" \
  "buffer_load_dwordx4 v240, %[wo], s97 offen offset:0 lds\n\t" \
  "v_mfma_f32_16x16x32_bf16 a[104:107], a[176:179], v[144:147], a[104:107]\n\t" \
  "v_exp_f32 v38, v38\n\t" \
  "v_mfma_f32_16x16x32_bf16 a[104:107], a[180:183], v[148:151], a[104:107]\n\t" \
  "v_exp_f32 v39, v39\n\t" \
  "v_mfma_f32_16x16x32_bf16 a[120:123], a[184:187], v[144:147], a[120:123]\n\t" \
  "v_cvt_pk_bf16_f32 v139, v38, v39\n\t" \
  "ds_read_b128 a[244:247], v221 offset:28672\n\t" \
  "v_mfma_f32_16x16x32_bf16 a[120:123], a[188:191], v[148:151], a[120:123]\n\t" \
  "v_exp_f32 v40, v40\n\t" \
  "v_mfma_f32_16x16x32_bf16 v[168:171], v[216:219], v[144:147], v[168:171]\n\t" \
  "v_exp_f32 v41, v41\n\t" \
  "v_mfma_f32_16x16x32_bf16 v[168:171], v[216:219], v[148:151], v[168:171]\n\t" \
  "v_cvt_pk_bf16_f32 v140, v40, v41\n\t" \
  "ds_read_b128 a[248:251], v222 offset:28672\n\t" \
  "v_mfma_f32_16x16x32_bf16 a[12:15], a[128:131], v[152:155], a[12:15]\n\t" \
  "s_add_u32 s97, s97, 7168\n\t" \
  "buffer_load_dwordx4 v241, %[wo], s97 offen offset:1024 lds\n\t" \
  "v_mfma_f32_16x16x32_bf16 a[12:15], a[132:135], v[156:159], a[12:15]\n\t" \
  "v_exp_f32 v42, v42\n\t" \
  "v_mfma_f32_16x16x32_bf16 a[28:31], a[136:139], v[152:155], a[28:31]\n\t" \
  "v_exp_f32 v43, v43\n\t" \
  "v_mfma_f32_16x16x32_bf16 a[28:31], a[140:143], v[156:159], a[28:31]\n\t" \
  "v_cvt_pk_bf16_f32 v141, v42, v43\n\t" \
  "ds_read_b128 a[252:255], v223 offset:28672\n\t" \
  "v_mfma_f32_16x16x32_bf16 a[44:47], a[144:147], v[152:155], a[44:47]\n\t" \
  "s_add_u32 s97, s97, 7168\n\t" \
  "buffer_load_dwordx4 v240, %[wo], s97 offen offset:2048 lds\n\t" \
  "v_mfma_f32_16x16x32_bf16 a[44:47], a[148:151], v[156:159], a[44:47]\n\t" \
  "v_exp_f32 v44, v44\n\t" \
  "v_mfma_f32_16x16x32_bf16 a[60:63], a[152:155], v[152:155], a[60:63]\n\t" \
  "s_add_u32 s97, s97, 7168\n\t" \
  "buffer_load_dwordx4 v241, %[wo], s97 offen offset:3072 lds\n\t" \
  "v_mfma_f32_16x16x32_bf16 a[60:63], a[156:159], v[156:159], a[60:63]\n\t" \
  "v_exp_f32 v45, v45\n\t" \
  "v_mfma_f32_16x16x32_bf16 a[76:79], a[160:163], v[152:155], a[76:79]\n\t" \
  "v_cvt_pk_bf16_f32 v142, v44, v45\n\t" \
  "v_mfma_f32_16x16x32_bf16 a[76:79], a[164:167], v[156:159], a[76:79]\n\t" \
  "v_exp_f32 v46, v46\n\t" \
  "v_mfma_f32_16x16x32_bf16 a[92:95], a[168:171], v[152:155], a[92:95]\n\t" \
  "s_add_u32 m0, %[m], 98304\n\t" \
  "s_mov_b32 s96, 40960\n\t" \
  "buffer_load_dwordx4 v232, %[sG], s96 offen offset:2048 lds\n\t" \
  "v_mfma_f32_16x16x32_bf16 a[92:95], a[172:175], v[156:159], a[92:95]\n\t" \
  "v_exp_f32 v47, v47\n\t" \
  "v_mfma_f32_16x16x32_bf16 a[108:111], a[176:179], v[152:155], a[108:111]\n\t" \
  "v_cvt_pk_bf16_f32 v143, v46, v47\n\t" \
  "v_mfma_f32_16x16x32_bf16 a[108:111], a[180:183], v[156:159], a[108:111]\n\t" \
  "s_mov_b32 s96, 47104\n\t" \
  "buffer_load_dwordx4 v232, %[sG], s96 offen offset:3072 lds\n\t" \
  "v_mfma_f32_16x16x32_bf16 a[124:127], a[184:187], v[152:155], a[124:127]\n\t" \
  "v_exp_f32 v64, v64\n\t" \
  "v_mfma_f32_16x16x32_bf16 a[124:127], a[188:191], v[156:159], a[124:127]\n\t" \
  "v_exp_f32 v65, v65\n\t" \
  "v_mfma_f32_16x16x32_bf16 v[172:175], v[216:219], v[152:155], v[172:175]\n\t" \
  "v_cvt_pk_bf16_f32 v144, v64, v65\n\t" \
  "v_mfma_f32_16x16x32_bf16 v[172:175], v[216:219], v[156:159], v[172:175]\n\t" \
  "v_exp_f32 v66, v66\n\t" \
  "s_cmp_lg_u32 s99, 0\n\t" \
  "s_cbranch_scc1 .Lsf_p6\n\t" \
  ".Lsf_q6:\n\t" \
  "s_waitcnt lgkmcnt(0)\n\t" \
  "s_barrier\n\t" \
  "v_mfma_f32_16x16x32_bf16 v[0:3], a[192:195], v[16:19], v[176:179]\n\t" \
  "v_exp_f32 v67, v67\n\t" \
  "v_mfma_f32_16x16x32_bf16 v[0:3], a[196:199], v[20:23], v[0:3]\n\t" \
  "v_cvt_pk_bf16_f32 v145, v66, v67\n\t" \
  "ds_read_b64_tr_b16 a[128:129], v228 offset:0\n\t" \
  "v_mfma_f32_16x16x32_bf16 v[0:3], a[200:203], v[24:27], v[0:3]\n\t" \
  "v_exp_f32 v68, v68\n\t" \
  "v_mfma_f32_16x16x32_bf16 v[0:3], a[204:207], v[28:31], v[0:3]\n\t" \
  "v_exp_f32 v69, v69\n\t" \
  "v_mfma_f32_16x16x32_bf16 v[4:7], a[220:223], v[28:31], v[176:179]\n\t" \
  "v_cvt_pk_bf16_f32 v146, v68, v69\n\t" \
  "ds_read_b64_tr_b16 a[130:131], v228 offset:4096\n\t" \
  "v_mfma_f32_16x16x32_bf16 v[4:7], a[216:219], v[24:27], v[4:7]\n\t" \
  "ds_read_b64_tr_b16 a[132:133], v228 offset:8192\n\t" \
  "s_add_u32 m0, %[m], 65536\n\t" \
  "v_mfma_f32_16x16x32_bf16 v[4:7], a[212:215], v[20:23], v[4:7]\n\t" \
  "v_max3_f32 v192, v0, v1, v2\n\t" \
  "ds_read_b64_tr_b16 a[134:135], v228 offset:12288\n\t" \
  "v_mfma_f32_16x16x32_bf16 v[4:7], a[208:211], v[16:19], v[4:7]\n\t" \
  "v_exp_f32 v70, v70\n\t" \
  "v_mfma_f32_16x16x32_bf16 v[8:11], a[224:227], v[16:19], v[176:179]\n\t" \
  "ds_read_b64_tr_b16 a[136:137], v229 offset:0\n\t" \
  "s_xor_b32 s97, %[wob], 393216\n\t" \
  "v_mfma_f32_16x16x32_bf16 v[8:11], a[228:231], v[20:23], v[8:11]\n\t" \
  "v_exp_f32 v71, v71\n\t" \
  "v_mfma_f32_16x16x32_bf16 v[8:11], a[232:235], v[24:27], v[8:11]\n\t" \
  "v_cvt_pk_bf16_f32 v147, v70, v71\n\t" \
  "v_max3_f32 v203, v3, v4, v5\n\t" \
  "v_mfma_f32_16x16x32_bf16 v[8:11], a[236:239], v[28:31], v[8:11]\n\t" \
  "v_max3_f32 v192, v192, v6, v7\n\t" \
  "ds_read_b64_tr_b16 a[138:139], v229 offset:4096\n\t" \
  "v_mfma_f32_16x16x32_bf16 v[12:15], a[252:255], v[28:31], v[176:179]\n\t" \
  "ds_read_b64_tr_b16 a[140:141], v229 offset:8192\n\t" \
  "buffer_load_dwordx4 v240, %[wo], s97 offen offset:0 lds\n\t" \
  "v_mfma_f32_16x16x32_bf16 v[12:15], a[248:251], v[24:27], v[12:15]\n\t" \
  "v_exp_f32 v72, v72\n\t" \
  "v_mfma_f32_16x16x32_bf16 v[12:15], a[244:247], v[20:23], v[12:15]\n\t" \
  "v_max3_f32 v203, v203, v8, v9\n\t" \
  "v_max3_f32 v192, v192, v10, v11\n\t" \
  "v_mfma_f32_16x16x32_bf16 v[12:15], a[240:243], v[16:19], v[12:15]\n\t" \
  "ds_read_b64_tr_b16 a[142:143], v229 offset:12288\n\t" \
  "ds_read_b64_tr_b16 a[144:145], v228 offset:256\n\t" \
  "v_mfma_f32_16x16x32_bf16 v[32:35], a[192:195], v[48:51], v[180:183]\n\t" \
  "ds_read_b64_tr_b16 a[146:147], v228 offset:4352\n\t" \
  "ds_read_b64_tr_b16 a[148:149], v228 offset:8448\n\t" \
  "v_mfma_f32_16x16x32_bf16 v[32:35], a[196:199], v[52:55], v[32:35]\n\t" \
  "v_exp_f32 v73, v73\n\t" \
  "v_mfma_f32_16x16x32_bf16 v[32:35], a[200:203], v[56:59], v[32:35]\n\t" \
  "v_cvt_pk_bf16_f32 v148, v72, v73\n\t" \
  "v_max3_f32 v203, v203, v12, v13\n\t" \
  "v_mfma_f32_16x16x32_bf16 v[32:35], a[204:207], v[60:63], v[32:35]\n\t" \
  "v_max3_f32 v192, v192, v14, v15\n\t" \
  "v_max_f32 v192, v192, v203\n\t" \
  "v_mfma_f32_16x16x32_bf16 v[36:39], a[220:223], v[60:63], v[180:183]\n\t" \
  "v_cmp_lt_f32 vcc, 0x42800000, v192\n\t" \
  "ds_read_b64_tr_b16 a[150:151], v228 offset:12544\n\t" \
  "v_mfma_f32_16x16x32_bf16 v[36:39], a[216:219], v[56:59], v[36:39]\n\t" \
  "s_cbranch_vccnz .Lsf_c70\n\t" \
  ".Lsf_r70:\n\t" \
  "ds_read_b64_tr_b16 a[152:153], v229 offset:256\n\t" \
  "v_mfma_f32_16x16x32_bf16 v[36:39], a[212:215], v[52:55], v[36:39]\n\t" \
  "s_add_u32 s97, s97, 7168\n\t" \
  "buffer_load_dwordx4 v241, %[wo], s97 offen offset:1024 lds\n\t" \
  "v_mfma_f32_16x16x32_bf16 v[36:39], a[208:211], v[48:51], v[36:39]\n\t" \
  "v_max3_f32 v192, v32, v33, v34\n\t" \
  "ds_read_b64_tr_b16 a[154:155], v229 offset:4352\n\t" \
  "v_mfma_f32_16x16x32_bf16 v[40:43], a[224:227], v[48:51], v[180:183]\n\t" \
  "s_add_u32 s97, s97, 7168\n\t" \
  "buffer_load_dwordx4 v240, %[wo], s97 offen offset:2048 lds\n\t" \
  "v_mfma_f32_16x16x32_bf16 v[40:43], a[228:231], v[52:55], v[40:43]\n\t" \
  "s_add_u32 s97, s97, 7168\n\t" \
  "buffer_load_dwordx4 v241, %[wo], s97 offen offset:3072 lds\n\t" \
  "v_mfma_f32_16x16x32_bf16 v[40:43], a[232:235], v[56:59], v[40:43]\n\t" \
  "v_max3_f32 v203, v35, v36, v37\n\t" \
  "v_max3_f32 v192, v192, v38, v39\n\t" \
  "v_mfma_f32_16x16x32_bf16 v[40:43], a[236:239], v[60:63], v[40:43]\n\t" \
  "ds_read_b64_tr_b16 a[156:157], v229 offset:8448\n\t" \
  "ds_read_b64_tr_b16 a[158:159], v229 offset:12544\n\t" \
  "v_mfma_f32_16x16x32_bf16 v[44:47], a[252:255], v[60:63], v[180:183]\n\t" \
  "v_exp_f32 v74, v74\n\t" \
  "v_mfma_f32_16x16x32_bf16 v[44:47], a[248:251], v[56:59], v[44:47]\n\t" \
  "v_exp_f32 v75, v75\n\t" \
  "v_mfma_f32_16x16x32_bf16 v[44:47], a[244:247], v[52:55], v[44:47]\n\t" \
  "v_cvt_pk_bf16_f32 v149, v74, v75\n\t" \
  "ds_read_b64_tr_b16 a[160:161], v228 offset:512\n\t" \
  "v_mfma_f32_16x16x32_bf16 v[44:47], a[240:243], v[48:51], v[44:47]\n\t" \
  "v_max3_f32 v203, v203, v40, v41\n\t" \
  "v_max3_f32 v192, v192, v42, v43\n\t" \
  "v_mfma_f32_16x16x32_bf16 v[64:67], a[192:195], v[80:83], v[184:187]\n\t" \
  "ds_read_b64_tr_b16 a[162:163], v228 offset:4608\n\t" \
  "ds_read_b64_tr_b16 a[164:165], v228 offset:8704\n\t" \
  "v_mfma_f32_16x16x32_bf16 v[64:67], a[196:199], v[84:87], v[64:67]\n\t" \
  "v_exp_f32 v76, v76\n\t" \
  "v_mfma_f32_16x16x32_bf16 v[64:67], a[200:203], v[88:91], v[64:67]\n\t" \
  "v_max3_f32 v203, v203, v44, v45\n\t" \
  "v_max3_f32 v192, v192, v46, v47\n\t" \
  "v_mfma_f32_16x16x32_bf16 v[64:67], a[204:207], v[92:95], v[64:67]\n\t" \
  "v_max_f32 v192, v192, v203\n\t" \
  "ds_read_b64_tr_b16 a[166:167], v228 offset:12800\n\t" \
  "v_mfma_f32_16x16x32_bf16 v[68:71], a[220:223], v[92:95], v[184:187]\n\t" \
  "v_cmp_lt_f32 vcc, 0x42800000, v192\n\t" \
  "ds_read_b64_tr_b16 a[168:169], v229 offset:512\n\t" \
  "v_mfma_f32_16x16x32_bf16 v[68:71], a[216:219], v[88:91], v[68:71]\n\t" \
  "s_cbranch_vccnz .Lsf_c71\n\t" \
  ".Lsf_r71:\n\t" \
  "ds_read_b64_tr_b16 a[170:171], v229 offset:4608\n\t" \
  "v_mfma_f32_16x16x32_bf16 v[68:71], a[212:215], v[84:87], v[68:71]\n\t" \
  "v_max3_f32 v192, v64, v65, v66\n\t" \
  "ds_read_b64_tr_b16 a[172:173], v229 offset:8704\n\t" \
  "v_mfma_f32_16x16x32_bf16 v[68:71], a[208:211], v[80:83], v[68:71]\n\t" \
  "v_exp_f32 v77, v77\n\t" \
  "v_mfma_f32_16x16x32_bf16 v[72:75], a[224:227], v[80:83], v[184:187]\n\t" \
  "v_cvt_pk_bf16_f32 v150, v76, v77\n\t" \
  "ds_read_b64_tr_b16 a[174:175], v229 offset:12800\n\t" \
  "v_mfma_f32_16x16x32_bf16 v[72:75], a[228:231], v[84:87], v[72:75]\n\t" \
  "v_exp_f32 v78, v78\n\t" \
  "v_mfma_f32_16x16x32_bf16 v[72:75], a[232:235], v[88:91], v[72:75]\n\t" \
  "v_exp_f32 v79, v79\n\t" \
  "v_mfma_f32_16x16x32_bf16 v[72:75], a[236:239], v[92:95], v[72:75]\n\t" \
  "v_cvt_pk_bf16_f32 v151, v78, v79\n\t" \
  "v_max3_f32 v203, v67, v68, v69\n\t" \
  "v_mfma_f32_16x16x32_bf16 v[76:79], a[252:255], v[92:95], v[184:187]\n\t" \
  "v_max3_f32 v192, v192, v70, v71\n\t" \
  "ds_read_b64_tr_b16 a[176:177], v228 offset:768\n\t" \
  "v_mfma_f32_16x16x32_bf16 v[76:79], a[248:251], v[88:91], v[76:79]\n\t" \
  "v_exp_f32 v96, v96\n\t" \
  "v_mfma_f32_16x16x32_bf16 v[76:79], a[244:247], v[84:87], v[76:79]\n\t" \
  "ds_read_b64_tr_b16 a[178:179], v228 offset:4864\n\t" \
  "v_max3_f32 v203, v203, v72, v73\n\t" \
  "v_mfma_f32_16x16x32_bf16 v[76:79], a[240:243], v[80:83], v[76:79]\n\t" \
  "v_max3_f32 v192, v192, v74, v75\n\t" \
  "ds_read_b64_tr_b16 a[180:181], v228 offset:8960\n\t" \
  "v_exp_f32 v97, v97\n\t" \
  "v_exp_f32 v98, v98\n\t" \
  "v_exp_f32 v99, v99\n\t" \
  "v_cvt_pk_bf16_f32 v152, v96, v97\n\t" \
  "v_cvt_pk_bf16_f32 v153, v98, v99\n\t" \
  "v_mfma_f32_16x16x32_bf16 v[96:99], a[192:195], v[112:115], v[188:191]\n\t" \
  "ds_read_b64_tr_b16 a[182:183], v228 offset:13056\n\t" \
  "v_max3_f32 v203, v203, v76, v77\n\t" \
  "v_mfma_f32_16x16x32_bf16 v[96:99], a[196:199], v[116:119], v[96:99]\n\t" \
  "v_max3_f32 v192, v192, v78, v79\n\t" \
  "v_max_f32 v192, v192, v203\n\t" \
  "v_mfma_f32_16x16x32_bf16 v[96:99], a[200:203], v[120:123], v[96:99]\n\t" \
  "v_cmp_lt_f32 vcc, 0x42800000, v192\n\t" \
  "ds_read_b64_tr_b16 a[184:185], v229 offset:768\n\t" \
  "v_mfma_f32_16x16x32_bf16 v[96:99], a[204:207], v[124:127], v[96:99]\n\t" \
  "s_cbranch_vccnz .Lsf_c72\n\t" \
  ".Lsf_r72:\n\t" \
  "ds_read_b64_tr_b16 a[186:187], v229 offset:4864\n\t" \
  "v_exp_f32 v100, v100\n\t" \
  "v_exp_f32 v101, v101\n\t" \
  "v_exp_f32 v102, v102\n\t" \
  "v_exp_f32 v103, v103\n\t" \
  "v_cvt_pk_bf16_f32 v154, v100, v101\n\t" \
  "v_cvt_pk_bf16_f32 v155, v102, v103\n\t" \
  "v_mfma_f32_16x16x32_bf16 v[100:103], a[220:223], v[124:127], v[188:191]\n\t" \
  "v_exp_f32 v104, v104\n\t" \
  "v_mfma_f32_16x16x32_bf16 v[100:103], a[216:219], v[120:123], v[100:103]\n\t" \
  "v_max3_f32 v192, v96, v97, v98\n\t" \
  "ds_read_b64_tr_b16 a[188:189], v229 offset:8960\n\t" \
  "v_mfma_f32_16x16x32_bf16 v[100:103], a[212:215], v[116:119], v[100:103]\n\t" \
  "v_exp_f32 v105, v105\n\t" \
  "v_mfma_f32_16x16x32_bf16 v[100:103], a[208:211], v[112:115], v[100:103]\n\t" \
  "v_cvt_pk_bf16_f32 v156, v104, v105\n\t" \
  "ds_read_b64_tr_b16 a[190:191], v229 offset:13056\n\t" \
  "v_exp_f32 v106, v106\n\t" \
  "v_exp_f32 v107, v107\n\t" \
  "s_nop 0\n\t" \
  "v_cvt_pk_bf16_f32 v157, v106, v107\n\t" \
  "v_mfma_f32_16x16x32_bf16 v[104:107], a[224:227], v[112:115], v[188:191]\n\t" \
  "v_exp_f32 v108, v108\n\t" \
  "v_mfma_f32_16x16x32_bf16 v[104:107], a[228:231], v[116:119], v[104:107]\n\t" \
  "v_exp_f32 v109, v109\n\t" \
  "v_mfma_f32_16x16x32_bf16 v[104:107], a[232:235], v[120:123], v[104:107]\n\t" \
  "v_cvt_pk_bf16_f32 v158, v108, v109\n\t" \
  "v_max3_f32 v203, v99, v100, v101\n\t" \
  "v_mfma_f32_16x16x32_bf16 v[104:107], a[236:239], v[124:127], v[104:107]\n\t" \
  "v_max3_f32 v192, v192, v102, v103\n\t" \
  "v_exp_f32 v110, v110\n\t" \
  "v_exp_f32 v111, v111\n\t" \
  "s_nop 0\n\t" \
  "v_cvt_pk_bf16_f32 v159, v110, v111\n\t" \
  "v_mfma_f32_16x16x32_bf16 v[108:111], a[252:255], v[124:127], v[188:191]\n\t" \
  "v_exp_f32 v0, v0\n\t" \
  "v_mfma_f32_16x16x32_bf16 v[108:111], a[248:251], v[120:123], v[108:111]\n\t" \
  "v_exp_f32 v1, v1\n\t" \
  "v_mfma_f32_16x16x32_bf16 v[108:111], a[244:247], v[116:119], v[108:111]\n\t" \
  "v_max3_f32 v203, v203, v104, v105\n\t" \
  "v_max3_f32 v192, v192, v106, v107\n\t" \
  "v_mfma_f32_16x16x32_bf16 v[108:111], a[240:243], v[112:115], v[108:111]\n\t" \
  "v_exp_f32 v2, v2\n\t" \
  "s_waitcnt vmcnt(6) lgkmcnt(0)\n\t" \
  "s_barrier\n\t" \
  "v_mfma_f32_16x16x32_bf16 a[0:3], a[128:131], v[128:131], a[0:3]\n\t" \
  "ds_read_b64_tr_b16 a[192:193], v237 offset:0\n\t" \
  "s_add_u32 m0, %[m], 81920\n\t" \
  "v_mfma_f32_16x16x32_bf16 a[0:3], a[132:135], v[132:135], a[0:3]\n\t" \
  "v_exp_f32 v3, v3\n\t" \
  "v_mfma_f32_16x16x32_bf16 a[16:19], a[136:139], v[128:131], a[16:19]\n\t" \
  "v_max3_f32 v203, v203, v108, v109\n\t" \
  "v_max3_f32 v192, v192, v110, v111\n\t" \
  "v_mfma_f32_16x16x32_bf16 a[16:19], a[140:143], v[132:135], a[16:19]\n\t" \
  "v_max_f32 v192, v192, v203\n\t" \
  "ds_read_b64_tr_b16 a[194:195], v237 offset:4096\n\t" \
  "v_mfma_f32_16x16x32_bf16 a[32:35], a[144:147], v[128:131], a[32:35]\n\t" \
  "v_cmp_lt_f32 vcc, 0x42800000, v192\n\t" \
  "ds_read_b64_tr_b16 a[196:197], v237 offset:8192\n\t" \
  "v_mfma_f32_16x16x32_bf16 a[32:35], a[148:151], v[132:135], a[32:35]\n\t" \
  "s_cbranch_vccnz .Lsf_c73\n\t" \
  ".Lsf_r73:\n\t" \
  "s_xor_b32 s97, %[wob], 524288\n\t" \
  "v_mfma_f32_16x16x32_bf16 a[48:51], a[152:155], v[128:131], a[48:51]\n\t" \
  "ds_read_b64_tr_b16 a[198:199], v237 offset:12288\n\t" \
  "ds_read_b64_tr_b16 a[200:201], v238 offset:0\n\t" \
  "v_mfma_f32_16x16x32_bf16 a[48:51], a[156:159], v[132:135], a[48:51]\n\t" \
  "buffer_load_dwordx4 v240, %[wo], s97 offen offset:0 lds\n\t" \
  "ds_read_b64_tr_b16 a[202:203], v238 offset:4096\n\t" \
  "v_mfma_f32_16x16x32_bf16 a[64:67], a[160:163], v[128:131], a[64:67]\n\t" \
  "ds_read_b64_tr_b16 a[204:205], v238 offset:8192\n\t" \
  "ds_read_b64_tr_b16 a[206:207], v238 offset:12288\n\t" \
  "v_mfma_f32_16x16x32_bf16 a[64:67], a[164:167], v[132:135], a[64:67]\n\t" \
  "v_exp_f32 v4, v4\n\t" \
  "v_mfma_f32_16x16x32_bf16 a[80:83], a[168:171], v[128:131], a[80:83]\n\t" \
  "s_add_u32 s97, s97, 7168\n\t" \
  "buffer_load_dwordx4 v241, %[wo], s97 offen offset:1024 lds\n\t" \
  "v_mfma_f32_16x16x32_bf16 a[80:83], a[172:175], v[132:135], a[80:83]\n\t" \
  "ds_read_b64_tr_b16 a[208:209], v237 offset:256\n\t" \
  "ds_read_b64_tr_b16 a[210:211], v237 offset:4352\n\t" \
  "v_mfma_f32_16x16x32_bf16 a[96:99], a[176:179], v[128:131], a[96:99]\n\t" \
  "v_exp_f32 v5, v5\n\t" \
  "v_mfma_f32_16x16x32_bf16 a[96:99], a[180:183], v[132:135], a[96:99]\n\t" \
  "s_add_u32 s97, s97, 7168\n\t" \
  "buffer_load_dwordx4 v240, %[wo], s97 offen offset:2048 lds\n\t" \
  "v_mfma_f32_16x16x32_bf16 a[112:115], a[184:187], v[128:131], a[112:115]\n\t" \
  "ds_read_b64_tr_b16 a[212:213], v237 offset:8448\n\t" \
  "ds_read_b64_tr_b16 a[214:215], v237 offset:12544\n\t" \
  "v_mfma_f32_16x16x32_bf16 a[112:115], a[188:191], v[132:135], a[112:115]\n\t" \
  "s_add_u32 s97, s97, 7168\n\t" \
  "buffer_load_dwordx4 v241, %[wo], s97 offen offset:3072 lds\n\t" \
  "v_mfma_f32_16x16x32_bf16 v[160:163], v[216:219], v[128:131], v[160:163]\n\t" \
  "v_exp_f32 v6, v6\n\t" \
  "v_mfma_f32_16x16x32_bf16 v[160:163], v[216:219], v[132:135], v[160:163]\n\t" \
  "v_cvt_pk_bf16_f32 v128, v0, v1\n\t" \
  "v_cvt_pk_bf16_f32 v129, v2, v3\n\t" \
  "v_mfma_f32_16x16x32_bf16 a[4:7], a[128:131], v[136:139], a[4:7]\n\t" \
  "v_cvt_pk_bf16_f32 v130, v4, v5\n\t" \
  "ds_read_b64_tr_b16 a[216:217], v238 offset:256\n\t" \
  "v_mfma_f32_16x16x32_bf16 a[4:7], a[132:135], v[140:143], a[4:7]\n\t" \
  "ds_read_b64_tr_b16 a[218:219], v238 offset:4352\n\t" \
  "ds_read_b64_tr_b16 a[220:221], v238 offset:8448\n\t" \
  "v_mfma_f32_16x16x32_bf16 a[20:23], a[136:139], v[136:139], a[20:23]\n\t" \
  "ds_read_b64_tr_b16 a[222:223], v238 offset:12544\n\t" \
  "ds_read_b64_tr_b16 a[224:225], v237 offset:512\n\t" \
  "v_mfma_f32_16x16x32_bf16 a[20:23], a[140:143], v[140:143], a[20:23]\n\t" \
  "v_exp_f32 v7, v7\n\t" \
  "v_mfma_f32_16x16x32_bf16 a[36:39], a[144:147], v[136:139], a[36:39]\n\t" \
  "v_cvt_pk_bf16_f32 v131, v6, v7\n\t" \
  "s_add_u32 m0, %[m], 114688\n\t" \
  "v_mfma_f32_16x16x32_bf16 a[36:39], a[148:151], v[140:143], a[36:39]\n\t" \
  "ds_read_b64_tr_b16 a[226:227], v237 offset:4608\n\t" \
  "ds_read_b64_tr_b16 a[228:229], v237 offset:8704\n\t" \
  "v_mfma_f32_16x16x32_bf16 a[52:55], a[152:155], v[136:139], a[52:55]\n\t" \
  "s_xor_b32 s97, %[wob], 786432\n\t" \
  "ds_read_b64_tr_b16 a[230:231], v237 offset:12800\n\t" \
  "v_mfma_f32_16x16x32_bf16 a[52:55], a[156:159], v[140:143], a[52:55]\n\t" \
  "v_exp_f32 v8, v8\n\t" \
  "v_mfma_f32_16x16x32_bf16 a[68:71], a[160:163], v[136:139], a[68:71]\n\t" \
  "v_exp_f32 v9, v9\n\t" \
  "v_mfma_f32_16x16x32_bf16 a[68:71], a[164:167], v[140:143], a[68:71]\n\t" \
  "v_cvt_pk_bf16_f32 v132, v8, v9\n\t" \
  "ds_read_b64_tr_b16 a[232:233], v238 offset:512\n\t" \
  "v_mfma_f32_16x16x32_bf16 a[84:87], a[168:171], v[136:139], a[84:87]\n\t" \
  "v_exp_f32 v10, v10\n\t" \
  "v_mfma_f32_16x16x32_bf16 a[84:87], a[172:175], v[140:143], a[84:87]\n\t" \
  "ds_read_b64_tr_b16 a[234:235], v238 offset:4608\n\t" \
  "ds_read_b64_tr_b16 a[236:237], v238 offset:8704\n\t" \
  "v_mfma_f32_16x16x32_bf16 a[100:103], a[176:179], v[136:139], a[100:103]\n\t" \
  "v_exp_f32 v11, v11\n\t" \
  "v_mfma_f32_16x16x32_bf16 a[100:103], a[180:183], v[140:143], a[100:103]\n\t" \
  "v_cvt_pk_bf16_f32 v133, v10, v11\n\t" \
  "ds_read_b64_tr_b16 a[238:239], v238 offset:12800\n\t" \
  "v_mfma_f32_16x16x32_bf16 a[116:119], a[184:187], v[136:139], a[116:119]\n\t" \
  "ds_read_b64_tr_b16 a[240:241], v237 offset:768\n\t" \
  "ds_read_b64_tr_b16 a[242:243], v237 offset:4864\n\t" \
  "v_mfma_f32_16x16x32_bf16 a[116:119], a[188:191], v[140:143], a[116:119]\n\t" \
  "v_exp_f32 v12, v12\n\t" \
  "v_mfma_f32_16x16x32_bf16 v[164:167], v[216:219], v[136:139], v[164:167]\n\t" \
  "v_exp_f32 v13, v13\n\t" \
  "v_mfma_f32_16x16x32_bf16 v[164:167], v[216:219], v[140:143], v[164:167]\n\t" \
  "v_cvt_pk_bf16_f32 v134, v12, v13\n\t" \
  "ds_read_b64_tr_b16 a[244:245], v237 offset:8960\n\t" \
  "v_mfma_f32_16x16x32_bf16 a[8:11], a[128:131], v[144:147], a[8:11]\n\t" \
  "ds_read_b64_tr_b16 a[246:247], v237 offset:13056\n\t" \
  "ds_read_b64_tr_b16 a[248:249], v238 offset:768\n\t" \
  "v_mfma_f32_16x16x32_bf16 a[8:11], a[132:135], v[148:151], a[8:11]\n\t" \
  "v_exp_f32 v14, v14\n\t" \
  "v_mfma_f32_16x16x32_bf16 a[24:27], a[136:139], v[144:147], a[24:27]\n\t" \
  "v_exp_f32 v15, v15\n\t" \
  "v_mfma_f32_16x16x32_bf16 a[24:27], a[140:143], v[148:151], a[24:27]\n\t" \
  "v_cvt_pk_bf16_f32 v135, v14, v15\n\t" \
  "ds_read_b64_tr_b16 a[250:251], v238 offset:4864\n\t" \
  "v_mfma_f32_16x16x32_bf16 a[40:43], a[144:147], v[144:147], a[40:43]\n\t" \
  "ds_read_b64_tr_b16 a[252:253], v238 offset:8960\n\t" \
  "ds_read_b64_tr_b16 a[254:255], v238 offset:13056\n\t" \
  "v_mfma_f32_16x16x32_bf16 a[40:43], a[148:151], v[148:151], a[40:43]\n\t" \
  "v_exp_f32 v32, v32\n\t" \
  "v_mfma_f32_16x16x32_bf16 a[56:59], a[152:155], v[144:147], a[56:59]\n\t" \
  "v_xor_b32 v235, 64, v234\n\t" \
  "ds_read_b64_tr_b16 v[48:49], v235 offset:0\n\t" \
  "v_mfma_f32_16x16x32_bf16 a[56:59], a[156:159], v[148:151], a[56:59]\n\t" \
  "v_exp_f32 v33, v33\n\t" \
  "v_mfma_f32_16x16x32_bf16 a[72:75], a[160:163], v[144:147], a[72:75]\n\t" \
  "v_cvt_pk_bf16_f32 v136, v32, v33\n\t" \
  "ds_read_b64_tr_b16 v[50:51], v235 offset:4096\n\t" \
  "v_mfma_f32_16x16x32_bf16 a[72:75], a[164:167], v[148:151], a[72:75]\n\t" \
  "v_exp_f32 v34, v34\n\t" \
  "v_mfma_f32_16x16x32_bf16 a[88:91], a[168:171], v[144:147], a[88:91]\n\t" \
  "v_exp_f32 v35, v35\n\t" \
  "v_mfma_f32_16x16x32_bf16 a[88:91], a[172:175], v[148:151], a[88:91]\n\t" \
  "v_cvt_pk_bf16_f32 v137, v34, v35\n\t" \
  "ds_read_b64_tr_b16 v[52:53], v235 offset:8192\n\t" \
  "v_mfma_f32_16x16x32_bf16 a[104:107], a[176:179], v[144:147], a[104:107]\n\t" \
  "v_exp_f32 v36, v36\n\t" \
  "v_mfma_f32_16x16x32_bf16 a[104:107], a[180:183], v[148:151], a[104:107]\n\t" \
  "v_exp_f32 v37, v37\n\t" \
  "v_mfma_f32_16x16x32_bf16 a[120:123], a[184:187], v[144:147], a[120:123]\n\t" \
  "v_cvt_pk_bf16_f32 v138, v36, v37\n\t" \
  "ds_read_b64_tr_b16 v[54:55], v235 offset:12288\n\t" \
  "v_mfma_f32_16x16x32_bf16 a[120:123], a[188:191], v[148:151], a[120:123]\n\t" \
  "v_exp_f32 v38, v38\n\t" \
  "v_mfma_f32_16x16x32_bf16 v[168:171], v[216:219], v[144:147], v[168:171]\n\t" \
  "v_exp_f32 v39, v39\n\t" \
  "v_mfma_f32_16x16x32_bf16 v[168:171], v[216:219], v[148:151], v[168:171]\n\t" \
  "v_cvt_pk_bf16_f32 v139, v38, v39\n\t" \
  "ds_read_b64_tr_b16 v[56:57], v235 offset:16384\n\t" \
  "v_mfma_f32_16x16x32_bf16 a[12:15], a[128:131], v[152:155], a[12:15]\n\t" \
  "v_exp_f32 v40, v40\n\t" \
  "v_mfma_f32_16x16x32_bf16 a[12:15], a[132:135], v[156:159], a[12:15]\n\t" \
  "v_exp_f32 v41, v41\n\t" \
  "v_mfma_f32_16x16x32_bf16 a[28:31], a[136:139], v[152:155], a[28:31]\n\t" \
  "v_cvt_pk_bf16_f32 v140, v40, v41\n\t" \
  "ds_read_b64_tr_b16 v[58:59], v235 offset:20480\n\t" \
  "v_mfma_f32_16x16x32_bf16 a[28:31], a[140:143], v[156:159], a[28:31]\n\t" \
  "v_exp_f32 v42, v42\n\t" \
  "v_mfma_f32_16x16x32_bf16 a[44:47], a[144:147], v[152:155], a[44:47]\n\t" \
  "ds_read_b64_tr_b16 v[60:61], v235 offset:24576\n\t" \
  "ds_read_b64_tr_b16 v[62:63], v235 offset:28672\n\t" \
  "v_mfma_f32_16x16x32_bf16 a[44:47], a[148:151], v[156:159], a[44:47]\n\t" \
  "v_exp_f32 v43, v43\n\t" \
  "v_mfma_f32_16x16x32_bf16 a[60:63], a[152:155], v[152:155], a[60:63]\n\t" \
  "v_cvt_pk_bf16_f32 v141, v42, v43\n\t" \
  "v_xor_b32 v236, 96, v234\n\t" \
  "v_mfma_f32_16x16x32_bf16 a[60:63], a[156:159], v[156:159], a[60:63]\n\t" \
  "v_exp_f32 v44, v44\n\t" \
  "v_mfma_f32_16x16x32_bf16 a[76:79], a[160:163], v[152:155], a[76:79]\n\t" \
  "v_exp_f32 v45, v45\n\t" \
  "v_mfma_f32_16x16x32_bf16 a[76:79], a[164:167], v[156:159], a[76:79]\n\t" \
  "v_cvt_pk_bf16_f32 v142, v44, v45\n\t" \
  "ds_read_b64_tr_b16 v[80:81], v236 offset:0\n\t" \
  "v_mfma_f32_16x16x32_bf16 a[92:95], a[168:171], v[152:155], a[92:95]\n\t" \
  "v_exp_f32 v46, v46\n\t" \
  "v_mfma_f32_16x16x32_bf16 a[92:95], a[172:175], v[156:159], a[92:95]\n\t" \
  "v_exp_f32 v47, v47\n\t" \
  "v_mfma_f32_16x16x32_bf16 a[108:111], a[176:179], v[152:155], a[108:111]\n\t" \
  "v_cvt_pk_bf16_f32 v143, v46, v47\n\t" \
  "ds_read_b64_tr_b16 v[82:83], v236 offset:4096\n\t" \
  "v_mfma_f32_16x16x32_bf16 a[108:111], a[180:183], v[156:159], a[108:111]\n\t" \
  "v_exp_f32 v64, v64\n\t" \
  "v_mfma_f32_16x16x32_bf16 a[124:127], a[184:187], v[152:155], a[124:127]\n\t" \
  "v_exp_f32 v65, v65\n\t" \
  "v_mfma_f32_16x16x32_bf16 a[124:127], a[188:191], v[156:159], a[124:127]\n\t" \
  "v_cvt_pk_bf16_f32 v144, v64, v65\n\t" \
  "ds_read_b64_tr_b16 v[84:85], v236 offset:8192\n\t" \
  "v_mfma_f32_16x16x32_bf16 v[172:175], v[216:219], v[152:155], v[172:175]\n\t" \
  "v_exp_f32 v66, v66\n\t" \
  "v_mfma_f32_16x16x32_bf16 v[172:175], v[216:219], v[156:159], v[172:175]\n\t" \
  "ds_read_b64_tr_b16 v[86:87], v236 offset:12288\n\t" \
  "ds_read_b64_tr_b16 v[88:89], v236 offset:16384\n\t" \
  "s_cmp_lg_u32 s99, 0\n\t" \
  "s_cbranch_scc1 .Lsf_p7\n\t" \
  ".Lsf_q7:\n\t" \
  "s_waitcnt lgkmcnt(15)\n\t" \
  "v_mfma_f32_16x16x32_bf16 a[0:3], a[192:195], v[128:131], a[0:3]\n\t" \
  "v_exp_f32 v67, v67\n\t" \
  "v_mfma_f32_16x16x32_bf16 a[0:3], a[196:199], v[132:135], a[0:3]\n\t" \
  "v_cvt_pk_bf16_f32 v145, v66, v67\n\t" \
  "ds_read_b128 v[176:179], v215 offset:16384\n\t" \
  "v_mfma_f32_16x16x32_bf16 v[160:163], v[216:219], v[128:131], v[160:163]\n\t" \
  "v_exp_f32 v68, v68\n\t" \
  "v_mfma_f32_16x16x32_bf16 v[160:163], v[216:219], v[132:135], v[160:163]\n\t" \
  "ds_read_b64_tr_b16 v[90:91], v236 offset:20480\n\t" \
  "ds_read_b128 v[180:183], v215 offset:17408\n\t" \
  "v_mfma_f32_16x16x32_bf16 a[16:19], a[200:203], v[128:131], a[16:19]\n\t" \
  "ds_read_b64_tr_b16 v[92:93], v236 offset:24576\n\t" \
  "ds_read_b128 v[184:187], v215 offset:18432\n\t" \
  "v_mfma_f32_16x16x32_bf16 a[16:19], a[204:207], v[132:135], a[16:19]\n\t" \
  "v_exp_f32 v69, v69\n\t" \
  "v_mfma_f32_16x16x32_bf16 a[32:35], a[208:211], v[128:131], a[32:35]\n\t" \
  "v_cvt_pk_bf16_f32 v146, v68, v69\n\t" \
  "ds_read_b128 v[188:191], v215 offset:19456\n\t" \
  "v_mfma_f32_16x16x32_bf16 a[32:35], a[212:215], v[132:135], a[32:35]\n\t" \
  "s_waitcnt lgkmcnt(0)\n\t" \
  "buffer_load_dwordx4 v240, %[wo], s97 offen offset:0 lds\n\t" \
  "v_mfma_f32_16x16x32_bf16 a[48:51], a[216:219], v[128:131], a[48:51]\n\t" \
  "s_add_u32 s97, s97, 7168\n\t" \
  "buffer_load_dwordx4 v241, %[wo], s97 offen offset:1024 lds\n\t" \
  "v_mfma_f32_16x16x32_bf16 a[48:51], a[220:223], v[132:135], a[48:51]\n\t" \
  "s_add_u32 s97, s97, 7168\n\t" \
  "buffer_load_dwordx4 v240, %[wo], s97 offen offset:2048 lds\n\t" \
  "v_mfma_f32_16x16x32_bf16 a[64:67], a[224:227], v[128:131], a[64:67]\n\t" \
  "s_add_u32 s97, s97, 7168\n\t" \
  "buffer_load_dwordx4 v241, %[wo], s97 offen offset:3072 lds\n\t" \
  "v_mfma_f32_16x16x32_bf16 a[64:67], a[228:231], v[132:135], a[64:67]\n\t" \
  "s_add_u32 m0, %[m], 131072\n\t" \
  "s_xor_b32 s97, %[wob], 917504\n\t" \
  "v_mfma_f32_16x16x32_bf16 a[80:83], a[232:235], v[128:131], a[80:83]\n\t" \
  "ds_read_b64_tr_b16 v[94:95], v236 offset:28672\n\t" \
  "ds_read_b128 v[192:195], v215 offset:32768\n\t" \
  "v_mfma_f32_16x16x32_bf16 a[80:83], a[236:239], v[132:135], a[80:83]\n\t" \
  "v_rcp_f32 v208, v160\n\t" \
  "v_mfma_f32_16x16x32_bf16 a[96:99], a[240:243], v[128:131], a[96:99]\n\t" \
  "v_accvgpr_read_b32 v16, a0\n\t" \
  "v_accvgpr_read_b32 v17, a1\n\t" \
  "v_mfma_f32_16x16x32_bf16 a[96:99], a[244:247], v[132:135], a[96:99]\n\t" \
  "v_accvgpr_read_b32 v18, a2\n\t" \
  "v_accvgpr_read_b32 v19, a3\n\t" \
  "v_mfma_f32_16x16x32_bf16 a[112:115], a[248:251], v[128:131], a[112:115]\n\t" \
  "v_accvgpr_read_b32 v20, a16\n\t" \
  "v_accvgpr_read_b32 v21, a17\n\t" \
  "v_mfma_f32_16x16x32_bf16 a[112:115], a[252:255], v[132:135], a[112:115]\n\t" \
  "v_accvgpr_read_b32 v22, a18\n\t" \
  "v_accvgpr_read_b32 v23, a19\n\t" \
  "s_waitcnt vmcnt(8)\n\t" \
  "s_barrier\n\t" \
  "v_mfma_f32_16x16x32_bf16 a[4:7], a[192:195], v[136:139], a[4:7]\n\t" \
  "v_xor_b32 v235, 128, v234\n\t" \
  "ds_read_b128 v[196:199], v215 offset:33792\n\t" \
  "v_mfma_f32_16x16x32_bf16 a[4:7], a[196:199], v[140:143], a[4:7]\n\t" \
  "v_mul_f32 v16, v16, v208\n\t" \
  "v_mul_f32 v17, v17, v208\n\t" \
  "v_mfma_f32_16x16x32_bf16 v[164:167], v[216:219], v[136:139], v[164:167]\n\t" \
  "v_mul_f32 v18, v18, v208\n\t" \
  "v_mul_f32 v19, v19, v208\n\t" \
  "v_mfma_f32_16x16x32_bf16 v[164:167], v[216:219], v[140:143], v[164:167]\n\t" \
  "v_mul_f32 v20, v20, v208\n\t" \
  "v_mul_f32 v21, v21, v208\n\t" \
  "v_mfma_f32_16x16x32_bf16 a[20:23], a[200:203], v[136:139], a[20:23]\n\t" \
  "v_mul_f32 v22, v22, v208\n\t" \
  "v_mul_f32 v23, v23, v208\n\t" \
  "v_mfma_f32_16x16x32_bf16 a[20:23], a[204:207], v[140:143], a[20:23]\n\t" \
  "ds_read_b128 v[200:203], v215 offset:34816\n\t" \
  "ds_read_b64_tr_b16 v[112:113], v235 offset:0\n\t" \
  "v_mfma_f32_16x16x32_bf16 a[36:39], a[208:211], v[136:139], a[36:39]\n\t" \
  "ds_read_b128 v[204:207], v215 offset:35840\n\t" \
  "s_waitcnt lgkmcnt(0)\n\t" \
  "v_mfma_f32_16x16x32_bf16 a[36:39], a[212:215], v[140:143], a[36:39]\n\t" \
  "buffer_load_dwordx4 v240, %[wo], s97 offen offset:0 lds\n\t" \
  "ds_read_b64_tr_b16 v[114:115], v235 offset:4096\n\t" \
  "v_mfma_f32_16x16x32_bf16 a[52:55], a[216:219], v[136:139], a[52:55]\n\t" \
  "s_add_u32 s97, s97, 7168\n\t" \
  "buffer_load_dwordx4 v241, %[wo], s97 offen offset:1024 lds\n\t" \
  "v_mfma_f32_16x16x32_bf16 a[52:55], a[220:223], v[140:143], a[52:55]\n\t" \
  "s_add_u32 s97, s97, 7168\n\t" \
  "buffer_load_dwordx4 v240, %[wo], s97 offen offset:2048 lds\n\t" \
  "v_mfma_f32_16x16x32_bf16 a[68:71], a[224:227], v[136:139], a[68:71]\n\t" \
  "s_add_u32 s97, s97, 7168\n\t" \
  "buffer_load_dwordx4 v241, %[wo], s97 offen offset:3072 lds\n\t" \
  "v_mfma_f32_16x16x32_bf16 a[68:71], a[228:231], v[140:143], a[68:71]\n\t" \
  "v_cvt_pk_bf16_f32 v0, v16, v17\n\t" \
  "v_cvt_pk_bf16_f32 v1, v18, v19\n\t" \
  "v_mfma_f32_16x16x32_bf16 a[84:87], a[232:235], v[136:139], a[84:87]\n\t" \
  "v_cvt_pk_bf16_f32 v2, v20, v21\n\t" \
  "v_cvt_pk_bf16_f32 v3, v22, v23\n\t" \
  "v_mfma_f32_16x16x32_bf16 a[84:87], a[236:239], v[140:143], a[84:87]\n\t" \
  "s_xor_b32 s98, %[eso], 0\n\t" \
  "buffer_store_dwordx4 v[0:3], v242, %[ws], s98 offen offset:0 sc1\n\t" \
  "v_mfma_f32_16x16x32_bf16 a[100:103], a[240:243], v[136:139], a[100:103]\n\t" \
  "v_accvgpr_read_b32 v24, a32\n\t" \
  "v_accvgpr_read_b32 v25, a33\n\t" \
  "v_mfma_f32_16x16x32_bf16 a[100:103], a[244:247], v[140:143], a[100:103]\n\t" \
  "v_accvgpr_read_b32 v26, a34\n\t" \
  "v_accvgpr_read_b32 v27, a35\n\t" \
  "v_mfma_f32_16x16x32_bf16 a[116:119], a[248:251], v[136:139], a[116:119]\n\t" \
  "v_accvgpr_read_b32 v28, a48\n\t" \
  "v_accvgpr_read_b32 v29, a49\n\t" \
  "v_mfma_f32_16x16x32_bf16 a[116:119], a[252:255], v[140:143], a[116:119]\n\t" \
  "v_accvgpr_read_b32 v30, a50\n\t" \
  "v_accvgpr_read_b32 v31, a51\n\t" \
  "v_exp_f32 v70, v70\n\t" \
  "v_exp_f32 v71, v71\n\t" \
  "s_nop 0\n\t" \
  "v_cvt_pk_bf16_f32 v147, v70, v71\n\t" \
  "s_nop 1\n\t" \
  "v_mfma_f32_16x16x32_bf16 a[8:11], a[192:195], v[144:147], a[8:11]\n\t" \
  "ds_read_b64_tr_b16 v[116:117], v235 offset:8192\n\t" \
  "ds_read_b64_tr_b16 v[118:119], v235 offset:12288\n\t" \
  "v_exp_f32 v72, v72\n\t" \
  "v_exp_f32 v73, v73\n\t" \
  "v_exp_f32 v74, v74\n\t" \
  "v_exp_f32 v75, v75\n\t" \
  "v_exp_f32 v76, v76\n\t" \
  "v_exp_f32 v77, v77\n\t" \
  "v_exp_f32 v78, v78\n\t" \
  "v_exp_f32 v79, v79\n\t" \
  "v_cvt_pk_bf16_f32 v148, v72, v73\n\t" \
  "v_cvt_pk_bf16_f32 v149, v74, v75\n\t" \
  "v_cvt_pk_bf16_f32 v150, v76, v77\n\t" \
  "v_cvt_pk_bf16_f32 v151, v78, v79\n\t" \
  "s_nop 1\n\t" \
  "v_mfma_f32_16x16x32_bf16 a[8:11], a[196:199], v[148:151], a[8:11]\n\t" \
  "v_mul_f32 v24, v24, v208\n\t" \
  "v_mul_f32 v25, v25, v208\n\t" \
  "v_mfma_f32_16x16x32_bf16 v[168:171], v[216:219], v[144:147], v[168:171]\n\t" \
  "v_mul_f32 v26, v26, v208\n\t" \
  "v_mul_f32 v27, v27, v208\n\t" \
  "v_mfma_f32_16x16x32_bf16 v[168:171], v[216:219], v[148:151], v[168:171]\n\t" \
  "v_mul_f32 v28, v28, v208\n\t" \
  "v_mul_f32 v29, v29, v208\n\t" \
  "v_mfma_f32_16x16x32_bf16 a[24:27], a[200:203], v[144:147], a[24:27]\n\t" \
  "v_mul_f32 v30, v30, v208\n\t" \
  "v_mul_f32 v31, v31, v208\n\t" \
  "v_mfma_f32_16x16x32_bf16 a[24:27], a[204:207], v[148:151], a[24:27]\n\t" \
  "ds_read_b64_tr_b16 v[120:121], v235 offset:16384\n\t" \
  "v_cvt_pk_bf16_f32 v4, v24, v25\n\t" \
  "v_mfma_f32_16x16x32_bf16 a[40:43], a[208:211], v[144:147], a[40:43]\n\t" \
  "v_cvt_pk_bf16_f32 v5, v26, v27\n\t" \
  "v_cvt_pk_bf16_f32 v6, v28, v29\n\t" \
  "v_mfma_f32_16x16x32_bf16 a[40:43], a[212:215], v[148:151], a[40:43]\n\t" \
  "v_cvt_pk_bf16_f32 v7, v30, v31\n\t" \
  "ds_read_b64_tr_b16 v[122:123], v235 offset:20480\n\t" \
  "v_mfma_f32_16x16x32_bf16 a[56:59], a[216:219], v[144:147], a[56:59]\n\t" \
  "s_xor_b32 s98, %[eso], 0\n\t" \
  "buffer_store_dwordx4 v[4:7], v242, %[ws], s98 offen offset:1024 sc1\n\t" \
  "v_mfma_f32_16x16x32_bf16 a[56:59], a[220:223], v[148:151], a[56:59]\n\t" \
  "v_accvgpr_read_b32 v16, a64\n\t" \
  "v_accvgpr_read_b32 v17, a65\n\t" \
  "v_mfma_f32_16x16x32_bf16 a[72:75], a[224:227], v[144:147], a[72:75]\n\t" \
  "v_accvgpr_read_b32 v18, a66\n\t" \
  "v_accvgpr_read_b32 v19, a67\n\t" \
  "v_mfma_f32_16x16x32_bf16 a[72:75], a[228:231], v[148:151], a[72:75]\n\t" \
  "v_accvgpr_read_b32 v20, a80\n\t" \
  "v_accvgpr_read_b32 v21, a81\n\t" \
  "v_mfma_f32_16x16x32_bf16 a[88:91], a[232:235], v[144:147], a[88:91]\n\t" \
  "v_accvgpr_read_b32 v22, a82\n\t" \
  "v_accvgpr_read_b32 v23, a83\n\t" \
  "v_mfma_f32_16x16x32_bf16 a[88:91], a[236:239], v[148:151], a[88:91]\n\t" \
  "ds_read_b64_tr_b16 v[124:125], v235 offset:24576\n\t" \
  "v_mul_f32 v16, v16, v208\n\t" \
  "v_mfma_f32_16x16x32_bf16 a[104:107], a[240:243], v[144:147], a[104:107]\n\t" \
  "v_mul_f32 v17, v17, v208\n\t" \
  "v_mul_f32 v18, v18, v208\n\t" \
  "v_mfma_f32_16x16x32_bf16 a[104:107], a[244:247], v[148:151], a[104:107]\n\t" \
  "v_mul_f32 v19, v19, v208\n\t" \
  "v_mul_f32 v20, v20, v208\n\t" \
  "v_mfma_f32_16x16x32_bf16 a[120:123], a[248:251], v[144:147], a[120:123]\n\t" \
  "v_mul_f32 v21, v21, v208\n\t" \
  "v_mul_f32 v22, v22, v208\n\t" \
  "v_mfma_f32_16x16x32_bf16 a[120:123], a[252:255], v[148:151], a[120:123]\n\t" \
  "v_mul_f32 v23, v23, v208\n\t" \
  "ds_read_b64_tr_b16 v[126:127], v235 offset:28672\n\t" \
  "v_exp_f32 v96, v96\n\t" \
  "v_exp_f32 v97, v97\n\t" \
  "v_exp_f32 v98, v98\n\t" \
  "v_exp_f32 v99, v99\n\t" \
  "v_exp_f32 v100, v100\n\t" \
  "v_exp_f32 v101, v101\n\t" \
  "v_exp_f32 v102, v102\n\t" \
  "v_exp_f32 v103, v103\n\t" \
  "v_cvt_pk_bf16_f32 v152, v96, v97\n\t" \
  "v_cvt_pk_bf16_f32 v153, v98, v99\n\t" \
  "v_cvt_pk_bf16_f32 v154, v100, v101\n\t" \
  "v_cvt_pk_bf16_f32 v155, v102, v103\n\t" \
  "s_nop 1\n\t" \
  "v_mfma_f32_16x16x32_bf16 a[12:15], a[192:195], v[152:155], a[12:15]\n\t" \
  "v_cvt_pk_bf16_f32 v8, v16, v17\n\t" \
  "v_cvt_pk_bf16_f32 v9, v18, v19\n\t" \
  "v_exp_f32 v104, v104\n\t" \
  "v_exp_f32 v105, v105\n\t" \
  "v_exp_f32 v106, v106\n\t" \
  "v_exp_f32 v107, v107\n\t" \
  "v_exp_f32 v108, v108\n\t" \
  "v_exp_f32 v109, v109\n\t" \
  "v_exp_f32 v110, v110\n\t" \
  "v_exp_f32 v111, v111\n\t" \
  "v_cvt_pk_bf16_f32 v156, v104, v105\n\t" \
  "v_cvt_pk_bf16_f32 v157, v106, v107\n\t" \
  "v_cvt_pk_bf16_f32 v158, v108, v109\n\t" \
  "v_cvt_pk_bf16_f32 v159, v110, v111\n\t" \
  "s_nop 1\n\t" \
  "v_mfma_f32_16x16x32_bf16 a[12:15], a[196:199], v[156:159], a[12:15]\n\t" \
  "v_cvt_pk_bf16_f32 v10, v20, v21\n\t" \
  "v_cvt_pk_bf16_f32 v11, v22, v23\n\t" \
  "v_mfma_f32_16x16x32_bf16 v[172:175], v[216:219], v[152:155], v[172:175]\n\t" \
  "s_xor_b32 s98, %[eso], 0\n\t" \
  "buffer_store_dwordx4 v[8:11], v242, %[ws], s98 offen offset:2048 sc1\n\t" \
  "v_mfma_f32_16x16x32_bf16 v[172:175], v[216:219], v[156:159], v[172:175]\n\t" \
  "ds_read_b64_tr_b16 a[128:129], v234 offset:0\n\t" \
  "ds_read_b64_tr_b16 a[130:131], v234 offset:4096\n\t" \
  "v_mfma_f32_16x16x32_bf16 a[28:31], a[200:203], v[152:155], a[28:31]\n\t" \
  "v_accvgpr_read_b32 v24, a96\n\t" \
  "v_accvgpr_read_b32 v25, a97\n\t" \
  "v_mfma_f32_16x16x32_bf16 a[28:31], a[204:207], v[156:159], a[28:31]\n\t" \
  "v_accvgpr_read_b32 v26, a98\n\t" \
  "v_accvgpr_read_b32 v27, a99\n\t" \
  "v_mfma_f32_16x16x32_bf16 a[44:47], a[208:211], v[152:155], a[44:47]\n\t" \
  "v_accvgpr_read_b32 v28, a112\n\t" \
  "v_accvgpr_read_b32 v29, a113\n\t" \
  "v_mfma_f32_16x16x32_bf16 a[44:47], a[212:215], v[156:159], a[44:47]\n\t" \
  "v_accvgpr_read_b32 v30, a114\n\t" \
  "v_accvgpr_read_b32 v31, a115\n\t" \
  "v_mfma_f32_16x16x32_bf16 a[60:63], a[216:219], v[152:155], a[60:63]\n\t" \
  "ds_read_b64_tr_b16 a[132:133], v234 offset:8192\n\t" \
  "ds_read_b64_tr_b16 a[134:135], v234 offset:12288\n\t" \
  "v_mfma_f32_16x16x32_bf16 a[60:63], a[220:223], v[156:159], a[60:63]\n\t" \
  "v_mul_f32 v24, v24, v208\n\t" \
  "v_mul_f32 v25, v25, v208\n\t" \
  "v_mfma_f32_16x16x32_bf16 a[76:79], a[224:227], v[152:155], a[76:79]\n\t" \
  "v_mul_f32 v26, v26, v208\n\t" \
  "v_mul_f32 v27, v27, v208\n\t" \
  "v_mfma_f32_16x16x32_bf16 a[76:79], a[228:231], v[156:159], a[76:79]\n\t" \
  "v_mul_f32 v28, v28, v208\n\t" \
  "v_mul_f32 v29, v29, v208\n\t" \
  "v_mfma_f32_16x16x32_bf16 a[92:95], a[232:235], v[152:155], a[92:95]\n\t" \
  "v_mul_f32 v30, v30, v208\n\t" \
  "v_mul_f32 v31, v31, v208\n\t" \
  "v_mfma_f32_16x16x32_bf16 a[92:95], a[236:239], v[156:159], a[92:95]\n\t" \
  "ds_read_b64_tr_b16 a[136:137], v234 offset:16384\n\t" \
  "ds_read_b64_tr_b16 a[138:139], v234 offset:20480\n\t" \
  "v_mfma_f32_16x16x32_bf16 a[108:111], a[240:243], v[152:155], a[108:111]\n\t" \
  "v_cvt_pk_bf16_f32 v12, v24, v25\n\t" \
  "v_cvt_pk_bf16_f32 v13, v26, v27\n\t" \
  "v_mfma_f32_16x16x32_bf16 a[108:111], a[244:247], v[156:159], a[108:111]\n\t" \
  "v_cvt_pk_bf16_f32 v14, v28, v29\n\t" \
  "v_cvt_pk_bf16_f32 v15, v30, v31\n\t" \
  "v_mfma_f32_16x16x32_bf16 a[124:127], a[248:251], v[152:155], a[124:127]\n\t" \
  "s_xor_b32 s98, %[eso], 0\n\t" \
  "buffer_store_dwordx4 v[12:15], v242, %[ws], s98 offen offset:3072 sc1\n\t" \
  "v_mfma_f32_16x16x32_bf16 a[124:127], a[252:255], v[156:159], a[124:127]\n\t" \
  "ds_read_b64_tr_b16 a[140:141], v234 offset:24576\n\t" \
  "ds_read_b64_tr_b16 a[142:143], v234 offset:28672\n\t" \
  "v_mfma_f32_16x16x32_bf16 a[32:35], v[48:51], v[0:3], 0\n\t" \
  "v_rcp_f32 v210, v164\n\t" \
  "v_mfma_f32_16x16x32_bf16 a[32:35], v[52:55], v[4:7], a[32:35]\n\t" \
  "v_accvgpr_read_b32 v16, a4\n\t" \
  "v_accvgpr_read_b32 v17, a5\n\t" \
  "v_mfma_f32_16x16x32_bf16 a[32:35], v[56:59], v[8:11], a[32:35]\n\t" \
  "v_accvgpr_read_b32 v18, a6\n\t" \
  "v_accvgpr_read_b32 v19, a7\n\t" \
  "v_mfma_f32_16x16x32_bf16 a[32:35], v[60:63], v[12:15], a[32:35]\n\t" \
  "v_accvgpr_read_b32 v20, a20\n\t" \
  "v_accvgpr_read_b32 v21, a21\n\t" \
  "v_mfma_f32_16x16x32_bf16 a[48:51], v[92:95], v[12:15], 0\n\t" \
  "v_accvgpr_read_b32 v22, a22\n\t" \
  "v_accvgpr_read_b32 v23, a23\n\t" \
  "v_mfma_f32_16x16x32_bf16 a[48:51], v[88:91], v[8:11], a[48:51]\n\t" \
  "v_xor_b32 v236, 32, v234\n\t" \
  "ds_read_b64_tr_b16 a[144:145], v236 offset:0\n\t" \
  "v_mfma_f32_16x16x32_bf16 a[48:51], v[84:87], v[4:7], a[48:51]\n\t" \
  "v_mul_f32 v16, v16, v210\n\t" \
  "v_mul_f32 v17, v17, v210\n\t" \
  "v_mfma_f32_16x16x32_bf16 a[48:51], v[80:83], v[0:3], a[48:51]\n\t" \
  "v_mul_f32 v18, v18, v210\n\t" \
  "v_mul_f32 v19, v19, v210\n\t" \
  "s_waitcnt lgkmcnt(15)\n\t" \
  "v_mfma_f32_16x16x32_bf16 a[64:67], v[112:115], v[0:3], 0\n\t" \
  "v_mul_f32 v20, v20, v210\n\t" \
  "v_mul_f32 v21, v21, v210\n\t" \
  "s_waitcnt lgkmcnt(13)\n\t" \
  "v_mfma_f32_16x16x32_bf16 a[64:67], v[116:119], v[4:7], a[64:67]\n\t" \
  "v_mul_f32 v22, v22, v210\n\t" \
  "v_mul_f32 v23, v23, v210\n\t" \
  "s_waitcnt lgkmcnt(11)\n\t" \
  "v_mfma_f32_16x16x32_bf16 a[64:67], v[120:123], v[8:11], a[64:67]\n\t" \
  "ds_read_b64_tr_b16 a[146:147], v236 offset:4096\n\t" \
  "ds_read_b64_tr_b16 a[148:149], v236 offset:8192\n\t" \
  "s_waitcnt lgkmcnt(11)\n\t" \
  "v_mfma_f32_16x16x32_bf16 a[64:67], v[124:127], v[12:15], a[64:67]\n\t" \
  "v_cvt_pk_bf16_f32 v32, v16, v17\n\t" \
  "v_cvt_pk_bf16_f32 v33, v18, v19\n\t" \
  "ds_read_b64_tr_b16 a[150:151], v236 offset:12288\n\t" \
  "ds_read_b64_tr_b16 a[152:153], v236 offset:16384\n\t" \
  "ds_read_b64_tr_b16 a[154:155], v236 offset:20480\n\t" \
  "ds_read_b64_tr_b16 a[156:157], v236 offset:24576\n\t" \
  "ds_read_b64_tr_b16 a[158:159], v236 offset:28672\n\t" \
  "s_waitcnt lgkmcnt(8)\n\t" \
  "v_mfma_f32_16x16x32_bf16 a[0:3], a[140:143], v[12:15], 0\n\t" \
  "v_cvt_pk_bf16_f32 v34, v20, v21\n\t" \
  "v_cvt_pk_bf16_f32 v35, v22, v23\n\t" \
  "v_mfma_f32_16x16x32_bf16 a[0:3], a[136:139], v[8:11], a[0:3]\n\t" \
  "s_xor_b32 s98, %[eso], 32768\n\t" \
  "buffer_store_dwordx4 v[32:35], v242, %[ws], s98 offen offset:0 sc1\n\t" \
  "v_mfma_f32_16x16x32_bf16 a[0:3], a[132:135], v[4:7], a[0:3]\n\t" \
  "v_accvgpr_read_b32 v24, a36\n\t" \
  "v_accvgpr_read_b32 v25, a37\n\t" \
  "v_mfma_f32_16x16x32_bf16 a[0:3], a[128:131], v[0:3], a[0:3]\n\t" \
  "v_accvgpr_read_b32 v26, a38\n\t" \
  "v_accvgpr_read_b32 v27, a39\n\t" \
  "v_xor_b32 v235, 192, v234\n\t" \
  "ds_read_b64_tr_b16 a[160:161], v235 offset:0\n\t" \
  "ds_read_b64_tr_b16 a[162:163], v235 offset:4096\n\t" \
  "ds_read_b64_tr_b16 a[164:165], v235 offset:8192\n\t" \
  "ds_read_b64_tr_b16 a[166:167], v235 offset:12288\n\t" \
  "ds_read_b64_tr_b16 a[168:169], v235 offset:16384\n\t" \
  "ds_read_b64_tr_b16 a[170:171], v235 offset:20480\n\t" \
  "ds_read_b64_tr_b16 a[172:173], v235 offset:24576\n\t" \
  "ds_read_b64_tr_b16 a[174:175], v235 offset:28672\n\t" \
  "s_waitcnt lgkmcnt(14)\n\t" \
  "v_mfma_f32_16x16x32_bf16 a[16:19], a[144:147], v[0:3], 0\n\t" \
  "v_accvgpr_read_b32 v28, a52\n\t" \
  "v_accvgpr_read_b32 v29, a53\n\t" \
  "s_waitcnt lgkmcnt(12)\n\t" \
  "v_mfma_f32_16x16x32_bf16 a[16:19], a[148:151], v[4:7], a[16:19]\n\t" \
  "v_accvgpr_read_b32 v30, a54\n\t" \
  "v_accvgpr_read_b32 v31, a55\n\t" \
  "s_waitcnt lgkmcnt(10)\n\t" \
  "v_mfma_f32_16x16x32_bf16 a[16:19], a[152:155], v[8:11], a[16:19]\n\t" \
  "v_mul_f32 v24, v24, v210\n\t" \
  "v_mul_f32 v25, v25, v210\n\t" \
  "s_waitcnt lgkmcnt(8)\n\t" \
  "v_mfma_f32_16x16x32_bf16 a[16:19], a[156:159], v[12:15], a[16:19]\n\t" \
  "v_mul_f32 v26, v26, v210\n\t" \
  "v_mul_f32 v27, v27, v210\n\t" \
  "v_xor_b32 v236, 224, v234\n\t" \
  "ds_read_b64_tr_b16 a[176:177], v236 offset:0\n\t" \
  "ds_read_b64_tr_b16 a[178:179], v236 offset:4096\n\t" \
  "ds_read_b64_tr_b16 a[180:181], v236 offset:8192\n\t" \
  "ds_read_b64_tr_b16 a[182:183], v236 offset:12288\n\t" \
  "ds_read_b64_tr_b16 a[184:185], v236 offset:16384\n\t" \
  "ds_read_b64_tr_b16 a[186:187], v236 offset:20480\n\t" \
  "ds_read_b64_tr_b16 a[188:189], v236 offset:24576\n\t" \
  "ds_read_b64_tr_b16 a[190:191], v236 offset:28672\n\t" \
  "s_waitcnt lgkmcnt(8)\n\t" \
  "v_mfma_f32_16x16x32_bf16 a[96:99], a[172:175], v[12:15], 0\n\t" \
  "v_mul_f32 v28, v28, v210\n\t" \
  "v_mul_f32 v29, v29, v210\n\t" \
  "v_mfma_f32_16x16x32_bf16 a[96:99], a[168:171], v[8:11], a[96:99]\n\t" \
  "v_mul_f32 v30, v30, v210\n\t" \
  "v_mul_f32 v31, v31, v210\n\t" \
  "v_mfma_f32_16x16x32_bf16 a[96:99], a[164:167], v[4:7], a[96:99]\n\t" \
  "v_cvt_pk_bf16_f32 v36, v24, v25\n\t" \
  "v_cvt_pk_bf16_f32 v37, v26, v27\n\t" \
  "v_mfma_f32_16x16x32_bf16 a[96:99], a[160:163], v[0:3], a[96:99]\n\t" \
  "v_cvt_pk_bf16_f32 v38, v28, v29\n\t" \
  "v_cvt_pk_bf16_f32 v39, v30, v31\n\t" \
  "v_xor_b32 v236, 160, v234\n\t" \
  "ds_read_b64_tr_b16 v[96:97], v236 offset:0\n\t" \
  "ds_read_b64_tr_b16 v[98:99], v236 offset:4096\n\t" \
  "ds_read_b64_tr_b16 v[100:101], v236 offset:8192\n\t" \
  "ds_read_b64_tr_b16 v[102:103], v236 offset:12288\n\t" \
  "ds_read_b64_tr_b16 v[104:105], v236 offset:16384\n\t" \
  "ds_read_b64_tr_b16 v[106:107], v236 offset:20480\n\t" \
  "ds_read_b64_tr_b16 v[108:109], v236 offset:24576\n\t" \
  "ds_read_b64_tr_b16 v[110:111], v236 offset:28672\n\t" \
  "s_waitcnt lgkmcnt(14)\n\t" \
  "v_mfma_f32_16x16x32_bf16 a[112:115], a[176:179], v[0:3], 0\n\t" \
  "s_xor_b32 s98, %[eso], 32768\n\t" \
  "buffer_store_dwordx4 v[36:39], v242, %[ws], s98 offen offset:1024 sc1\n\t" \
  "s_waitcnt lgkmcnt(12)\n\t" \
  "v_mfma_f32_16x16x32_bf16 a[112:115], a[180:183], v[4:7], a[112:115]\n\t" \
  "v_accvgpr_read_b32 v16, a68\n\t" \
  "v_accvgpr_read_b32 v17, a69\n\t" \
  "s_waitcnt lgkmcnt(10)\n\t" \
  "v_mfma_f32_16x16x32_bf16 a[112:115], a[184:187], v[8:11], a[112:115]\n\t" \
  "v_accvgpr_read_b32 v18, a70\n\t" \
  "v_accvgpr_read_b32 v19, a71\n\t" \
  "s_waitcnt lgkmcnt(8)\n\t" \
  "v_mfma_f32_16x16x32_bf16 a[112:115], a[188:191], v[12:15], a[112:115]\n\t" \
  "v_accvgpr_read_b32 v20, a84\n\t" \
  "v_accvgpr_read_b32 v21, a85\n\t" \
  "s_waitcnt lgkmcnt(0)\n\t" \
  "v_mfma_f32_16x16x32_bf16 a[80:83], v[108:111], v[12:15], 0\n\t" \
  "v_accvgpr_read_b32 v22, a86\n\t" \
  "v_accvgpr_read_b32 v23, a87\n\t" \
  "v_mfma_f32_16x16x32_bf16 a[80:83], v[104:107], v[8:11], a[80:83]\n\t" \
  "v_mul_f32 v16, v16, v210\n\t" \
  "v_mul_f32 v17, v17, v210\n\t" \
  "v_mfma_f32_16x16x32_bf16 a[80:83], v[100:103], v[4:7], a[80:83]\n\t" \
  "v_mul_f32 v18, v18, v210\n\t" \
  "v_mul_f32 v19, v19, v210\n\t" \
  "v_mfma_f32_16x16x32_bf16 a[80:83], v[96:99], v[0:3], a[80:83]\n\t" \
  "v_mul_f32 v20, v20, v210\n\t" \
  "v_mul_f32 v21, v21, v210\n\t" \
  "v_mul_f32 v22, v22, v210\n\t" \
  "v_mul_f32 v23, v23, v210\n\t" \
  "v_cvt_pk_bf16_f32 v40, v16, v17\n\t" \
  "v_cvt_pk_bf16_f32 v41, v18, v19\n\t" \
  "v_cvt_pk_bf16_f32 v42, v20, v21\n\t" \
  "v_cvt_pk_bf16_f32 v43, v22, v23\n\t" \
  "v_accvgpr_read_b32 v24, a100\n\t" \
  "v_accvgpr_read_b32 v25, a101\n\t" \
  "v_accvgpr_read_b32 v26, a102\n\t" \
  "v_accvgpr_read_b32 v27, a103\n\t" \
  "v_accvgpr_read_b32 v28, a116\n\t" \
  "v_accvgpr_read_b32 v29, a117\n\t" \
  "v_accvgpr_read_b32 v30, a118\n\t" \
  "v_accvgpr_read_b32 v31, a119\n\t" \
  "v_mul_f32 v24, v24, v210\n\t" \
  "v_mul_f32 v25, v25, v210\n\t" \
  "v_mul_f32 v26, v26, v210\n\t" \
  "v_mul_f32 v27, v27, v210\n\t" \
  "v_mul_f32 v28, v28, v210\n\t" \
  "v_mul_f32 v29, v29, v210\n\t" \
  "v_mul_f32 v30, v30, v210\n\t" \
  "v_mul_f32 v31, v31, v210\n\t" \
  "v_cvt_pk_bf16_f32 v44, v24, v25\n\t" \
  "v_cvt_pk_bf16_f32 v45, v26, v27\n\t" \
  "v_cvt_pk_bf16_f32 v46, v28, v29\n\t" \
  "v_cvt_pk_bf16_f32 v47, v30, v31\n\t" \
  "v_mfma_f32_16x16x32_bf16 a[36:39], v[48:51], v[32:35], 0\n\t" \
  "s_xor_b32 s98, %[eso], 32768\n\t" \
  "buffer_store_dwordx4 v[40:43], v242, %[ws], s98 offen offset:2048 sc1\n\t" \
  "v_mfma_f32_16x16x32_bf16 a[36:39], v[52:55], v[36:39], a[36:39]\n\t" \
  "s_xor_b32 s98, %[eso], 32768\n\t" \
  "buffer_store_dwordx4 v[44:47], v242, %[ws], s98 offen offset:3072 sc1\n\t" \
  "v_mfma_f32_16x16x32_bf16 a[36:39], v[56:59], v[40:43], a[36:39]\n\t" \
  "v_rcp_f32 v212, v168\n\t" \
  "v_mfma_f32_16x16x32_bf16 a[36:39], v[60:63], v[44:47], a[36:39]\n\t" \
  "v_accvgpr_read_b32 v16, a8\n\t" \
  "v_accvgpr_read_b32 v17, a9\n\t" \
  "s_waitcnt vmcnt(0)\n\t" \
  "buffer_store_dword v243, v254, %[ws], %[fw2] offen offset:0 sc1\n\t" \
  "v_mfma_f32_16x16x32_bf16 a[52:55], v[92:95], v[44:47], 0\n\t" \
  "v_accvgpr_read_b32 v18, a10\n\t" \
  "v_accvgpr_read_b32 v19, a11\n\t" \
  "v_mfma_f32_16x16x32_bf16 a[52:55], v[88:91], v[40:43], a[52:55]\n\t" \
  "v_accvgpr_read_b32 v20, a24\n\t" \
  "v_accvgpr_read_b32 v21, a25\n\t" \
  "v_mfma_f32_16x16x32_bf16 a[52:55], v[84:87], v[36:39], a[52:55]\n\t" \
  "v_accvgpr_read_b32 v22, a26\n\t" \
  "v_accvgpr_read_b32 v23, a27\n\t" \
  "v_mfma_f32_16x16x32_bf16 a[52:55], v[80:83], v[32:35], a[52:55]\n\t" \
  "v_mul_f32 v16, v16, v212\n\t" \
  "v_mul_f32 v17, v17, v212\n\t" \
  "ds_read_b128 a[224:227], v213 offset:32768\n\t" \
  "ds_read_b128 a[228:231], v213 offset:33792\n\t" \
  "ds_read_b128 a[232:235], v213 offset:34816\n\t" \
  "ds_read_b128 a[236:239], v213 offset:35840\n\t" \
  "ds_read_b128 a[240:243], v215 offset:0\n\t" \
  "ds_read_b128 a[244:247], v215 offset:1024\n\t" \
  "ds_read_b128 a[248:251], v215 offset:2048\n\t" \
  "ds_read_b128 a[252:255], v215 offset:3072\n\t" \
  "v_mfma_f32_16x16x32_bf16 a[68:71], v[112:115], v[32:35], 0\n\t" \
  "v_mul_f32 v18, v18, v212\n\t" \
  "v_mul_f32 v19, v19, v212\n\t" \
  "v_mfma_f32_16x16x32_bf16 a[68:71], v[116:119], v[36:39], a[68:71]\n\t" \
  "v_mul_f32 v20, v20, v212\n\t" \
  "v_mul_f32 v21, v21, v212\n\t" \
  "v_mfma_f32_16x16x32_bf16 a[68:71], v[120:123], v[40:43], a[68:71]\n\t" \
  "v_mul_f32 v22, v22, v212\n\t" \
  "v_mul_f32 v23, v23, v212\n\t" \
  "v_mfma_f32_16x16x32_bf16 a[68:71], v[124:127], v[44:47], a[68:71]\n\t" \
  "v_cvt_pk_bf16_f32 v64, v16, v17\n\t" \
  "v_cvt_pk_bf16_f32 v65, v18, v19\n\t" \
  "v_mfma_f32_16x16x32_bf16 a[4:7], a[140:143], v[44:47], 0\n\t" \
  "v_cvt_pk_bf16_f32 v66, v20, v21\n\t" \
  "v_cvt_pk_bf16_f32 v67, v22, v23\n\t" \
  "v_mfma_f32_16x16x32_bf16 a[4:7], a[136:139], v[40:43], a[4:7]\n\t" \
  "s_xor_b32 s98, %[eso], 65536\n\t" \
  "buffer_store_dwordx4 v[64:67], v242, %[ws], s98 offen offset:0 sc1\n\t" \
  "v_mfma_f32_16x16x32_bf16 a[4:7], a[132:135], v[36:39], a[4:7]\n\t" \
  "v_accvgpr_read_b32 v24, a40\n\t" \
  "v_accvgpr_read_b32 v25, a41\n\t" \
  "v_mfma_f32_16x16x32_bf16 a[4:7], a[128:131], v[32:35], a[4:7]\n\t" \
  "v_accvgpr_read_b32 v26, a42\n\t" \
  "v_accvgpr_read_b32 v27, a43\n\t" \
  "v_mfma_f32_16x16x32_bf16 a[20:23], a[144:147], v[32:35], 0\n\t" \
  "v_accvgpr_read_b32 v28, a56\n\t" \
  "v_accvgpr_read_b32 v29, a57\n\t" \
  "v_mfma_f32_16x16x32_bf16 a[20:23], a[148:151], v[36:39], a[20:23]\n\t" \
  "v_accvgpr_read_b32 v30, a58\n\t" \
  "v_accvgpr_read_b32 v31, a59\n\t" \
  "v_mfma_f32_16x16x32_bf16 a[20:23], a[152:155], v[40:43], a[20:23]\n\t" \
  "v_mul_f32 v24, v24, v212\n\t" \
  "v_mul_f32 v25, v25, v212\n\t" \
  "v_mfma_f32_16x16x32_bf16 a[20:23], a[156:159], v[44:47], a[20:23]\n\t" \
  "v_mul_f32 v26, v26, v212\n\t" \
  "v_mul_f32 v27, v27, v212\n\t" \
  "v_mfma_f32_16x16x32_bf16 a[100:103], a[172:175], v[44:47], 0\n\t" \
  "v_mul_f32 v28, v28, v212\n\t" \
  "v_mul_f32 v29, v29, v212\n\t" \
  "v_mfma_f32_16x16x32_bf16 a[100:103], a[168:171], v[40:43], a[100:103]\n\t" \
  "v_mul_f32 v30, v30, v212\n\t" \
  "v_mul_f32 v31, v31, v212\n\t" \
  "v_mfma_f32_16x16x32_bf16 a[100:103], a[164:167], v[36:39], a[100:103]\n\t" \
  "v_cvt_pk_bf16_f32 v68, v24, v25\n\t" \
  "v_cvt_pk_bf16_f32 v69, v26, v27\n\t" \
  "v_mfma_f32_16x16x32_bf16 a[100:103], a[160:163], v[32:35], a[100:103]\n\t" \
  "v_cvt_pk_bf16_f32 v70, v28, v29\n\t" \
  "v_cvt_pk_bf16_f32 v71, v30, v31\n\t" \
  "v_mfma_f32_16x16x32_bf16 a[116:119], a[176:179], v[32:35], 0\n\t" \
  "s_xor_b32 s98, %[eso], 65536\n\t" \
  "buffer_store_dwordx4 v[68:71], v242, %[ws], s98 offen offset:1024 sc1\n\t" \
  "v_mfma_f32_16x16x32_bf16 a[116:119], a[180:183], v[36:39], a[116:119]\n\t" \
  "v_accvgpr_read_b32 v16, a72\n\t" \
  "v_accvgpr_read_b32 v17, a73\n\t" \
  "v_mfma_f32_16x16x32_bf16 a[116:119], a[184:187], v[40:43], a[116:119]\n\t" \
  "v_accvgpr_read_b32 v18, a74\n\t" \
  "v_accvgpr_read_b32 v19, a75\n\t" \
  "v_mfma_f32_16x16x32_bf16 a[116:119], a[188:191], v[44:47], a[116:119]\n\t" \
  "v_accvgpr_read_b32 v20, a88\n\t" \
  "v_accvgpr_read_b32 v21, a89\n\t" \
  "v_mfma_f32_16x16x32_bf16 a[84:87], v[108:111], v[44:47], 0\n\t" \
  "v_accvgpr_read_b32 v22, a90\n\t" \
  "v_accvgpr_read_b32 v23, a91\n\t" \
  "v_mfma_f32_16x16x32_bf16 a[84:87], v[104:107], v[40:43], a[84:87]\n\t" \
  "v_mul_f32 v16, v16, v212\n\t" \
  "v_mul_f32 v17, v17, v212\n\t" \
  "v_mfma_f32_16x16x32_bf16 a[84:87], v[100:103], v[36:39], a[84:87]\n\t" \
  "v_mul_f32 v18, v18, v212\n\t" \
  "v_mul_f32 v19, v19, v212\n\t" \
  "v_mfma_f32_16x16x32_bf16 a[84:87], v[96:99], v[32:35], a[84:87]\n\t" \
  "v_mul_f32 v20, v20, v212\n\t" \
  "v_mul_f32 v21, v21, v212\n\t" \
  "v_mul_f32 v22, v22, v212\n\t" \
  "v_mul_f32 v23, v23, v212\n\t" \
  "v_cvt_pk_bf16_f32 v72, v16, v17\n\t" \
  "v_cvt_pk_bf16_f32 v73, v18, v19\n\t" \
  "v_cvt_pk_bf16_f32 v74, v20, v21\n\t" \
  "v_cvt_pk_bf16_f32 v75, v22, v23\n\t" \
  "v_accvgpr_read_b32 v24, a104\n\t" \
  "v_accvgpr_read_b32 v25, a105\n\t" \
  "v_accvgpr_read_b32 v26, a106\n\t" \
  "v_accvgpr_read_b32 v27, a107\n\t" \
  "v_accvgpr_read_b32 v28, a120\n\t" \
  "v_accvgpr_read_b32 v29, a121\n\t" \
  "v_accvgpr_read_b32 v30, a122\n\t" \
  "v_accvgpr_read_b32 v31, a123\n\t" \
  "v_mul_f32 v24, v24, v212\n\t" \
  "v_mul_f32 v25, v25, v212\n\t" \
  "v_mul_f32 v26, v26, v212\n\t" \
  "v_mul_f32 v27, v27, v212\n\t" \
  "v_mul_f32 v28, v28, v212\n\t" \
  "v_mul_f32 v29, v29, v212\n\t" \
  "v_mul_f32 v30, v30, v212\n\t" \
  "v_mul_f32 v31, v31, v212\n\t" \
  "v_cvt_pk_bf16_f32 v76, v24, v25\n\t" \
  "v_cvt_pk_bf16_f32 v77, v26, v27\n\t" \
  "v_cvt_pk_bf16_f32 v78, v28, v29\n\t" \
  "v_cvt_pk_bf16_f32 v79, v30, v31\n\t" \
  "v_mfma_f32_16x16x32_bf16 a[40:43], v[48:51], v[64:67], 0\n\t" \
  "s_xor_b32 s98, %[eso], 65536\n\t" \
  "buffer_store_dwordx4 v[72:75], v242, %[ws], s98 offen offset:2048 sc1\n\t" \
  "v_mfma_f32_16x16x32_bf16 a[40:43], v[52:55], v[68:71], a[40:43]\n\t" \
  "s_xor_b32 s98, %[eso], 65536\n\t" \
  "buffer_store_dwordx4 v[76:79], v242, %[ws], s98 offen offset:3072 sc1\n\t" \
  "v_mfma_f32_16x16x32_bf16 a[40:43], v[56:59], v[72:75], a[40:43]\n\t" \
  "buffer_load_dwordx4 v[224:227], v252, %[ws], %[ss] offen sc1\n\t" \
  "buffer_load_dwordx4 v[128:131], v247, %[sM], %[gvo] offen\n\t" \
  "v_mfma_f32_16x16x32_bf16 a[40:43], v[60:63], v[76:79], a[40:43]\n\t" \
  "v_rcp_f32 v214, v172\n\t" \
  "v_mfma_f32_16x16x32_bf16 a[56:59], v[92:95], v[76:79], 0\n\t" \
  "v_accvgpr_read_b32 v16, a12\n\t" \
  "v_accvgpr_read_b32 v17, a13\n\t" \
  "v_mfma_f32_16x16x32_bf16 a[56:59], v[88:91], v[72:75], a[56:59]\n\t" \
  "v_accvgpr_read_b32 v18, a14\n\t" \
  "v_accvgpr_read_b32 v19, a15\n\t" \
  "v_mfma_f32_16x16x32_bf16 a[56:59], v[84:87], v[68:71], a[56:59]\n\t" \
  "v_accvgpr_read_b32 v20, a28\n\t" \
  "v_accvgpr_read_b32 v21, a29\n\t" \
  "v_mfma_f32_16x16x32_bf16 a[56:59], v[80:83], v[64:67], a[56:59]\n\t" \
  "v_accvgpr_read_b32 v22, a30\n\t" \
  "v_accvgpr_read_b32 v23, a31\n\t" \
  "v_mfma_f32_16x16x32_bf16 a[72:75], v[112:115], v[64:67], 0\n\t" \
  "v_mul_f32 v16, v16, v214\n\t" \
  "v_mul_f32 v17, v17, v214\n\t" \
  "v_mul_f32 v18, v18, v214\n\t" \
  "v_mul_f32 v19, v19, v214\n\t" \
  "v_mul_f32 v20, v20, v214\n\t" \
  "v_mul_f32 v21, v21, v214\n\t" \
  "v_mul_f32 v22, v22, v214\n\t" \
  "v_mul_f32 v23, v23, v214\n\t" \
  "v_cvt_pk_bf16_f32 v160, v16, v17\n\t" \
  "v_cvt_pk_bf16_f32 v161, v18, v19\n\t" \
  "v_cvt_pk_bf16_f32 v162, v20, v21\n\t" \
  "v_cvt_pk_bf16_f32 v163, v22, v23\n\t" \
  "s_waitcnt lgkmcnt(4)\n\t" \
  "ds_write_b128 v213, v[160:163] offset:32768\n\t" \
  "v_accvgpr_read_b32 v24, a44\n\t" \
  "v_accvgpr_read_b32 v25, a45\n\t" \
  "v_accvgpr_read_b32 v26, a46\n\t" \
  "v_accvgpr_read_b32 v27, a47\n\t" \
  "v_accvgpr_read_b32 v28, a60\n\t" \
  "v_accvgpr_read_b32 v29, a61\n\t" \
  "v_accvgpr_read_b32 v30, a62\n\t" \
  "v_accvgpr_read_b32 v31, a63\n\t" \
  "v_mul_f32 v24, v24, v214\n\t" \
  "v_mul_f32 v25, v25, v214\n\t" \
  "v_mul_f32 v26, v26, v214\n\t" \
  "v_mul_f32 v27, v27, v214\n\t" \
  "v_mul_f32 v28, v28, v214\n\t" \
  "v_mul_f32 v29, v29, v214\n\t" \
  "v_mul_f32 v30, v30, v214\n\t" \
  "v_mul_f32 v31, v31, v214\n\t" \
  "v_cvt_pk_bf16_f32 v164, v24, v25\n\t" \
  "v_cvt_pk_bf16_f32 v165, v26, v27\n\t" \
  "v_cvt_pk_bf16_f32 v166, v28, v29\n\t" \
  "v_cvt_pk_bf16_f32 v167, v30, v31\n\t" \
  "ds_write_b128 v213, v[164:167] offset:33792\n\t" \
  "v_accvgpr_read_b32 v16, a76\n\t" \
  "v_accvgpr_read_b32 v17, a77\n\t" \
  "v_accvgpr_read_b32 v18, a78\n\t" \
  "v_accvgpr_read_b32 v19, a79\n\t" \
  "v_accvgpr_read_b32 v20, a92\n\t" \
  "v_accvgpr_read_b32 v21, a93\n\t" \
  "v_accvgpr_read_b32 v22, a94\n\t" \
  "v_accvgpr_read_b32 v23, a95\n\t" \
  "v_mul_f32 v16, v16, v214\n\t" \
  "v_mul_f32 v17, v17, v214\n\t" \
  "v_mul_f32 v18, v18, v214\n\t" \
  "v_mul_f32 v19, v19, v214\n\t" \
  "v_mul_f32 v20, v20, v214\n\t" \
  "v_mul_f32 v21, v21, v214\n\t" \
  "v_mul_f32 v22, v22, v214\n\t" \
  "v_mul_f32 v23, v23, v214\n\t" \
  "v_cvt_pk_bf16_f32 v168, v16, v17\n\t" \
  "v_cvt_pk_bf16_f32 v169, v18, v19\n\t" \
  "v_cvt_pk_bf16_f32 v170, v20, v21\n\t" \
  "v_cvt_pk_bf16_f32 v171, v22, v23\n\t" \
  "ds_write_b128 v213, v[168:171] offset:34816\n\t" \
  "v_accvgpr_read_b32 v24, a108\n\t" \
  "v_accvgpr_read_b32 v25, a109\n\t" \
  "v_accvgpr_read_b32 v26, a110\n\t" \
  "v_accvgpr_read_b32 v27, a111\n\t" \
  "v_accvgpr_read_b32 v28, a124\n\t" \
  "v_accvgpr_read_b32 v29, a125\n\t" \
  "v_accvgpr_read_b32 v30, a126\n\t" \
  "v_accvgpr_read_b32 v31, a127\n\t" \
  "v_mul_f32 v24, v24, v214\n\t" \
  "v_mul_f32 v25, v25, v214\n\t" \
  "v_mul_f32 v26, v26, v214\n\t" \
  "v_mul_f32 v27, v27, v214\n\t" \
  "v_mul_f32 v28, v28, v214\n\t" \
  "v_mul_f32 v29, v29, v214\n\t" \
  "v_mul_f32 v30, v30, v214\n\t" \
  "v_mul_f32 v31, v31, v214\n\t" \
  "v_cvt_pk_bf16_f32 v172, v24, v25\n\t" \
  "v_cvt_pk_bf16_f32 v173, v26, v27\n\t" \
  "v_cvt_pk_bf16_f32 v174, v28, v29\n\t" \
  "v_cvt_pk_bf16_f32 v175, v30, v31\n\t" \
  "ds_write_b128 v213, v[172:175] offset:35840\n\t" \
  "v_mfma_f32_16x16x32_bf16 a[72:75], v[116:119], v[68:71], a[72:75]\n\t" \
  "s_add_u32 s98, %[gvo], 12288\n\t" \
  "buffer_load_dwordx4 v[132:135], v247, %[sM], s98 offen\n\t" \
  "v_mfma_f32_16x16x32_bf16 a[72:75], v[120:123], v[72:75], a[72:75]\n\t" \
  "s_add_u32 s98, %[gvo], 24576\n\t" \
  "buffer_load_dwordx4 v[136:139], v247, %[sM], s98 offen\n\t" \
  "v_mfma_f32_16x16x32_bf16 a[72:75], v[124:127], v[76:79], a[72:75]\n\t" \
  "s_add_u32 s98, %[gvo], 36864\n\t" \
  "buffer_load_dwordx4 v[140:143], v247, %[sM], s98 offen\n\t" \
  "s_add_u32 m0, %[m], 147456\n\t" \
  "s_xor_b32 s97, %[wob], 655360\n\t" \
  "buffer_load_dwordx4 v240, %[wo], s97 offen offset:0 lds\n\t" \
  "s_add_u32 s97, s97, 7168\n\t" \
  "buffer_load_dwordx4 v241, %[wo], s97 offen offset:1024 lds\n\t" \
  "s_add_u32 s97, s97, 7168\n\t" \
  "buffer_load_dwordx4 v240, %[wo], s97 offen offset:2048 lds\n\t" \
  "s_add_u32 s97, s97, 7168\n\t" \
  "buffer_load_dwordx4 v241, %[wo], s97 offen offset:3072 lds\n\t" \
  "s_add_u32 s98, %[gvo], 49152\n\t" \
  "buffer_load_dwordx4 v[144:147], v247, %[sM], s98 offen\n\t" \
  "s_add_u32 s98, %[gvo], 61440\n\t" \
  "buffer_load_dwordx4 v[148:151], v247, %[sM], s98 offen\n\t" \
  "s_add_u32 s98, %[gvo], 73728\n\t" \
  "buffer_load_dwordx4 v[152:155], v247, %[sM], s98 offen\n\t" \
  "s_add_u32 s98, %[gvo], 86016\n\t" \
  "buffer_load_dwordx4 v[156:159], v247, %[sM], s98 offen\n\t" \
  "s_waitcnt vmcnt(0)\n\t" \
  "s_branch .Lat_end\n\t" \
  ".Lsf_c00:\n\t" \
  "s_mov_b32 vcc_lo, 0\n\t" \
  "s_branch .Lsf_s0\n\t" \
  ".Lsf_c10:\n\t" \
  "s_mov_b32 vcc_lo, 1\n\t" \
  "s_branch .Lsf_s0\n\t" \
  ".Lsf_c20:\n\t" \
  "s_mov_b32 vcc_lo, 2\n\t" \
  "s_branch .Lsf_s0\n\t" \
  ".Lsf_c30:\n\t" \
  "s_mov_b32 vcc_lo, 3\n\t" \
  "s_branch .Lsf_s0\n\t" \
  ".Lsf_c40:\n\t" \
  "s_mov_b32 vcc_lo, 4\n\t" \
  "s_branch .Lsf_s0\n\t" \
  ".Lsf_c50:\n\t" \
  "s_mov_b32 vcc_lo, 5\n\t" \
  "s_branch .Lsf_s0\n\t" \
  ".Lsf_c60:\n\t" \
  "s_mov_b32 vcc_lo, 6\n\t" \
  "s_branch .Lsf_s0\n\t" \
  ".Lsf_c70:\n\t" \
  "s_mov_b32 vcc_lo, 7\n\t" \
  "s_branch .Lsf_s0\n\t" \
  ".Lsf_s0:\n\t" \
  "s_nop 7\n\t" \
  "s_nop 1\n\t" \
  "v_mov_b32 v193, v192\n\t" \
  "s_nop 1\n\t" \
  "v_permlane32_swap_b32 v192, v193\n\t" \
  "s_nop 1\n\t" \
  "v_max_f32 v192, v192, v193\n\t" \
  "s_nop 1\n\t" \
  "v_mov_b32 v193, v192\n\t" \
  "s_nop 1\n\t" \
  "v_permlane16_swap_b32 v192, v193\n\t" \
  "s_nop 1\n\t" \
  "v_max_f32 v192, v192, v193\n\t" \
  "v_max_f32 v192, 0, v192\n\t" \
  "v_sub_f32 v0, v0, v192\n\t" \
  "v_sub_f32 v1, v1, v192\n\t" \
  "v_sub_f32 v2, v2, v192\n\t" \
  "v_sub_f32 v3, v3, v192\n\t" \
  "v_sub_f32 v4, v4, v192\n\t" \
  "v_sub_f32 v5, v5, v192\n\t" \
  "v_sub_f32 v6, v6, v192\n\t" \
  "v_sub_f32 v7, v7, v192\n\t" \
  "v_sub_f32 v8, v8, v192\n\t" \
  "v_sub_f32 v9, v9, v192\n\t" \
  "v_sub_f32 v10, v10, v192\n\t" \
  "v_sub_f32 v11, v11, v192\n\t" \
  "v_sub_f32 v12, v12, v192\n\t" \
  "v_sub_f32 v13, v13, v192\n\t" \
  "v_sub_f32 v14, v14, v192\n\t" \
  "v_sub_f32 v15, v15, v192\n\t" \
  "v_sub_f32 v176, v176, v192\n\t" \
  "v_sub_f32 v177, v177, v192\n\t" \
  "v_sub_f32 v178, v178, v192\n\t" \
  "v_sub_f32 v179, v179, v192\n\t" \
  "v_exp_f32 v196, -v192\n\t" \
  "s_mov_b32 s99, 1\n\t" \
  "s_nop 1\n\t" \
  "s_cselect_b32 vcc_hi, 1, 0\n\t" \
  "s_cmp_eq_u32 vcc_lo, 0\n\t" \
  "s_cbranch_scc1 .Lsf_b00\n\t" \
  "s_cmp_eq_u32 vcc_lo, 1\n\t" \
  "s_cbranch_scc1 .Lsf_b10\n\t" \
  "s_cmp_eq_u32 vcc_lo, 2\n\t" \
  "s_cbranch_scc1 .Lsf_b20\n\t" \
  "s_cmp_eq_u32 vcc_lo, 3\n\t" \
  "s_cbranch_scc1 .Lsf_b30\n\t" \
  "s_cmp_eq_u32 vcc_lo, 4\n\t" \
  "s_cbranch_scc1 .Lsf_b40\n\t" \
  "s_cmp_eq_u32 vcc_lo, 5\n\t" \
  "s_cbranch_scc1 .Lsf_b50\n\t" \
  "s_cmp_eq_u32 vcc_lo, 6\n\t" \
  "s_cbranch_scc1 .Lsf_b60\n\t" \
  "s_cmp_eq_u32 vcc_lo, 7\n\t" \
  "s_cbranch_scc1 .Lsf_b70\n\t" \
  ".Lsf_b00:\n\t" \
  "s_cmp_lg_u32 vcc_hi, 0\n\t" \
  "s_branch .Lsf_r00\n\t" \
  ".Lsf_b10:\n\t" \
  "s_cmp_lg_u32 vcc_hi, 0\n\t" \
  "s_branch .Lsf_r10\n\t" \
  ".Lsf_b20:\n\t" \
  "s_cmp_lg_u32 vcc_hi, 0\n\t" \
  "s_branch .Lsf_r20\n\t" \
  ".Lsf_b30:\n\t" \
  "s_cmp_lg_u32 vcc_hi, 0\n\t" \
  "s_branch .Lsf_r30\n\t" \
  ".Lsf_b40:\n\t" \
  "s_cmp_lg_u32 vcc_hi, 0\n\t" \
  "s_branch .Lsf_r40\n\t" \
  ".Lsf_b50:\n\t" \
  "s_cmp_lg_u32 vcc_hi, 0\n\t" \
  "s_branch .Lsf_r50\n\t" \
  ".Lsf_b60:\n\t" \
  "s_cmp_lg_u32 vcc_hi, 0\n\t" \
  "s_branch .Lsf_r60\n\t" \
  ".Lsf_b70:\n\t" \
  "s_cmp_lg_u32 vcc_hi, 0\n\t" \
  "s_branch .Lsf_r70\n\t" \
  ".Lsf_c01:\n\t" \
  "s_mov_b32 vcc_lo, 0\n\t" \
  "s_branch .Lsf_s1\n\t" \
  ".Lsf_c11:\n\t" \
  "s_mov_b32 vcc_lo, 1\n\t" \
  "s_branch .Lsf_s1\n\t" \
  ".Lsf_c21:\n\t" \
  "s_mov_b32 vcc_lo, 2\n\t" \
  "s_branch .Lsf_s1\n\t" \
  ".Lsf_c31:\n\t" \
  "s_mov_b32 vcc_lo, 3\n\t" \
  "s_branch .Lsf_s1\n\t" \
  ".Lsf_c41:\n\t" \
  "s_mov_b32 vcc_lo, 4\n\t" \
  "s_branch .Lsf_s1\n\t" \
  ".Lsf_c51:\n\t" \
  "s_mov_b32 vcc_lo, 5\n\t" \
  "s_branch .Lsf_s1\n\t" \
  ".Lsf_c61:\n\t" \
  "s_mov_b32 vcc_lo, 6\n\t" \
  "s_branch .Lsf_s1\n\t" \
  ".Lsf_c71:\n\t" \
  "s_mov_b32 vcc_lo, 7\n\t" \
  "s_branch .Lsf_s1\n\t" \
  ".Lsf_s1:\n\t" \
  "s_nop 7\n\t" \
  "s_nop 1\n\t" \
  "v_mov_b32 v193, v192\n\t" \
  "s_nop 1\n\t" \
  "v_permlane32_swap_b32 v192, v193\n\t" \
  "s_nop 1\n\t" \
  "v_max_f32 v192, v192, v193\n\t" \
  "s_nop 1\n\t" \
  "v_mov_b32 v193, v192\n\t" \
  "s_nop 1\n\t" \
  "v_permlane16_swap_b32 v192, v193\n\t" \
  "s_nop 1\n\t" \
  "v_max_f32 v192, v192, v193\n\t" \
  "v_max_f32 v192, 0, v192\n\t" \
  "v_sub_f32 v32, v32, v192\n\t" \
  "v_sub_f32 v33, v33, v192\n\t" \
  "v_sub_f32 v34, v34, v192\n\t" \
  "v_sub_f32 v35, v35, v192\n\t" \
  "v_sub_f32 v36, v36, v192\n\t" \
  "v_sub_f32 v37, v37, v192\n\t" \
  "v_sub_f32 v38, v38, v192\n\t" \
  "v_sub_f32 v39, v39, v192\n\t" \
  "v_sub_f32 v40, v40, v192\n\t" \
  "v_sub_f32 v41, v41, v192\n\t" \
  "v_sub_f32 v42, v42, v192\n\t" \
  "v_sub_f32 v43, v43, v192\n\t" \
  "v_sub_f32 v44, v44, v192\n\t" \
  "v_sub_f32 v45, v45, v192\n\t" \
  "v_sub_f32 v46, v46, v192\n\t" \
  "v_sub_f32 v47, v47, v192\n\t" \
  "v_sub_f32 v180, v180, v192\n\t" \
  "v_sub_f32 v181, v181, v192\n\t" \
  "v_sub_f32 v182, v182, v192\n\t" \
  "v_sub_f32 v183, v183, v192\n\t" \
  "v_exp_f32 v197, -v192\n\t" \
  "s_mov_b32 s99, 1\n\t" \
  "s_nop 1\n\t" \
  "s_cselect_b32 vcc_hi, 1, 0\n\t" \
  "s_cmp_eq_u32 vcc_lo, 0\n\t" \
  "s_cbranch_scc1 .Lsf_b01\n\t" \
  "s_cmp_eq_u32 vcc_lo, 1\n\t" \
  "s_cbranch_scc1 .Lsf_b11\n\t" \
  "s_cmp_eq_u32 vcc_lo, 2\n\t" \
  "s_cbranch_scc1 .Lsf_b21\n\t" \
  "s_cmp_eq_u32 vcc_lo, 3\n\t" \
  "s_cbranch_scc1 .Lsf_b31\n\t" \
  "s_cmp_eq_u32 vcc_lo, 4\n\t" \
  "s_cbranch_scc1 .Lsf_b41\n\t" \
  "s_cmp_eq_u32 vcc_lo, 5\n\t" \
  "s_cbranch_scc1 .Lsf_b51\n\t" \
  "s_cmp_eq_u32 vcc_lo, 6\n\t" \
  "s_cbranch_scc1 .Lsf_b61\n\t" \
  "s_cmp_eq_u32 vcc_lo, 7\n\t" \
  "s_cbranch_scc1 .Lsf_b71\n\t" \
  ".Lsf_b01:\n\t" \
  "s_cmp_lg_u32 vcc_hi, 0\n\t" \
  "s_branch .Lsf_r01\n\t" \
  ".Lsf_b11:\n\t" \
  "s_cmp_lg_u32 vcc_hi, 0\n\t" \
  "s_branch .Lsf_r11\n\t" \
  ".Lsf_b21:\n\t" \
  "s_cmp_lg_u32 vcc_hi, 0\n\t" \
  "s_branch .Lsf_r21\n\t" \
  ".Lsf_b31:\n\t" \
  "s_cmp_lg_u32 vcc_hi, 0\n\t" \
  "s_branch .Lsf_r31\n\t" \
  ".Lsf_b41:\n\t" \
  "s_cmp_lg_u32 vcc_hi, 0\n\t" \
  "s_branch .Lsf_r41\n\t" \
  ".Lsf_b51:\n\t" \
  "s_cmp_lg_u32 vcc_hi, 0\n\t" \
  "s_branch .Lsf_r51\n\t" \
  ".Lsf_b61:\n\t" \
  "s_cmp_lg_u32 vcc_hi, 0\n\t" \
  "s_branch .Lsf_r61\n\t" \
  ".Lsf_b71:\n\t" \
  "s_cmp_lg_u32 vcc_hi, 0\n\t" \
  "s_branch .Lsf_r71\n\t" \
  ".Lsf_c02:\n\t" \
  "s_mov_b32 vcc_lo, 0\n\t" \
  "s_branch .Lsf_s2\n\t" \
  ".Lsf_c12:\n\t" \
  "s_mov_b32 vcc_lo, 1\n\t" \
  "s_branch .Lsf_s2\n\t" \
  ".Lsf_c22:\n\t" \
  "s_mov_b32 vcc_lo, 2\n\t" \
  "s_branch .Lsf_s2\n\t" \
  ".Lsf_c32:\n\t" \
  "s_mov_b32 vcc_lo, 3\n\t" \
  "s_branch .Lsf_s2\n\t" \
  ".Lsf_c42:\n\t" \
  "s_mov_b32 vcc_lo, 4\n\t" \
  "s_branch .Lsf_s2\n\t" \
  ".Lsf_c52:\n\t" \
  "s_mov_b32 vcc_lo, 5\n\t" \
  "s_branch .Lsf_s2\n\t" \
  ".Lsf_c62:\n\t" \
  "s_mov_b32 vcc_lo, 6\n\t" \
  "s_branch .Lsf_s2\n\t" \
  ".Lsf_c72:\n\t" \
  "s_mov_b32 vcc_lo, 7\n\t" \
  "s_branch .Lsf_s2\n\t" \
  ".Lsf_s2:\n\t" \
  "s_nop 7\n\t" \
  "s_nop 1\n\t" \
  "v_mov_b32 v193, v192\n\t" \
  "s_nop 1\n\t" \
  "v_permlane32_swap_b32 v192, v193\n\t" \
  "s_nop 1\n\t" \
  "v_max_f32 v192, v192, v193\n\t" \
  "s_nop 1\n\t" \
  "v_mov_b32 v193, v192\n\t" \
  "s_nop 1\n\t" \
  "v_permlane16_swap_b32 v192, v193\n\t" \
  "s_nop 1\n\t" \
  "v_max_f32 v192, v192, v193\n\t" \
  "v_max_f32 v192, 0, v192\n\t" \
  "v_sub_f32 v64, v64, v192\n\t" \
  "v_sub_f32 v65, v65, v192\n\t" \
  "v_sub_f32 v66, v66, v192\n\t" \
  "v_sub_f32 v67, v67, v192\n\t" \
  "v_sub_f32 v68, v68, v192\n\t" \
  "v_sub_f32 v69, v69, v192\n\t" \
  "v_sub_f32 v70, v70, v192\n\t" \
  "v_sub_f32 v71, v71, v192\n\t" \
  "v_sub_f32 v72, v72, v192\n\t" \
  "v_sub_f32 v73, v73, v192\n\t" \
  "v_sub_f32 v74, v74, v192\n\t" \
  "v_sub_f32 v75, v75, v192\n\t" \
  "v_sub_f32 v76, v76, v192\n\t" \
  "v_sub_f32 v77, v77, v192\n\t" \
  "v_sub_f32 v78, v78, v192\n\t" \
  "v_sub_f32 v79, v79, v192\n\t" \
  "v_sub_f32 v184, v184, v192\n\t" \
  "v_sub_f32 v185, v185, v192\n\t" \
  "v_sub_f32 v186, v186, v192\n\t" \
  "v_sub_f32 v187, v187, v192\n\t" \
  "v_exp_f32 v198, -v192\n\t" \
  "s_mov_b32 s99, 1\n\t" \
  "s_nop 1\n\t" \
  "s_cselect_b32 vcc_hi, 1, 0\n\t" \
  "s_cmp_eq_u32 vcc_lo, 0\n\t" \
  "s_cbranch_scc1 .Lsf_b02\n\t" \
  "s_cmp_eq_u32 vcc_lo, 1\n\t" \
  "s_cbranch_scc1 .Lsf_b12\n\t" \
  "s_cmp_eq_u32 vcc_lo, 2\n\t" \
  "s_cbranch_scc1 .Lsf_b22\n\t" \
  "s_cmp_eq_u32 vcc_lo, 3\n\t" \
  "s_cbranch_scc1 .Lsf_b32\n\t" \
  "s_cmp_eq_u32 vcc_lo, 4\n\t" \
  "s_cbranch_scc1 .Lsf_b42\n\t" \
  "s_cmp_eq_u32 vcc_lo, 5\n\t" \
  "s_cbranch_scc1 .Lsf_b52\n\t" \
  "s_cmp_eq_u32 vcc_lo, 6\n\t" \
  "s_cbranch_scc1 .Lsf_b62\n\t" \
  "s_cmp_eq_u32 vcc_lo, 7\n\t" \
  "s_cbranch_scc1 .Lsf_b72\n\t" \
  ".Lsf_b02:\n\t" \
  "s_cmp_lg_u32 vcc_hi, 0\n\t" \
  "s_branch .Lsf_r02\n\t" \
  ".Lsf_b12:\n\t" \
  "s_cmp_lg_u32 vcc_hi, 0\n\t" \
  "s_branch .Lsf_r12\n\t" \
  ".Lsf_b22:\n\t" \
  "s_cmp_lg_u32 vcc_hi, 0\n\t" \
  "s_branch .Lsf_r22\n\t" \
  ".Lsf_b32:\n\t" \
  "s_cmp_lg_u32 vcc_hi, 0\n\t" \
  "s_branch .Lsf_r32\n\t" \
  ".Lsf_b42:\n\t" \
  "s_cmp_lg_u32 vcc_hi, 0\n\t" \
  "s_branch .Lsf_r42\n\t" \
  ".Lsf_b52:\n\t" \
  "s_cmp_lg_u32 vcc_hi, 0\n\t" \
  "s_branch .Lsf_r52\n\t" \
  ".Lsf_b62:\n\t" \
  "s_cmp_lg_u32 vcc_hi, 0\n\t" \
  "s_branch .Lsf_r62\n\t" \
  ".Lsf_b72:\n\t" \
  "s_cmp_lg_u32 vcc_hi, 0\n\t" \
  "s_branch .Lsf_r72\n\t" \
  ".Lsf_c03:\n\t" \
  "s_mov_b32 vcc_lo, 0\n\t" \
  "s_branch .Lsf_s3\n\t" \
  ".Lsf_c13:\n\t" \
  "s_mov_b32 vcc_lo, 1\n\t" \
  "s_branch .Lsf_s3\n\t" \
  ".Lsf_c23:\n\t" \
  "s_mov_b32 vcc_lo, 2\n\t" \
  "s_branch .Lsf_s3\n\t" \
  ".Lsf_c33:\n\t" \
  "s_mov_b32 vcc_lo, 3\n\t" \
  "s_branch .Lsf_s3\n\t" \
  ".Lsf_c43:\n\t" \
  "s_mov_b32 vcc_lo, 4\n\t" \
  "s_branch .Lsf_s3\n\t" \
  ".Lsf_c53:\n\t" \
  "s_mov_b32 vcc_lo, 5\n\t" \
  "s_branch .Lsf_s3\n\t" \
  ".Lsf_c63:\n\t" \
  "s_mov_b32 vcc_lo, 6\n\t" \
  "s_branch .Lsf_s3\n\t" \
  ".Lsf_c73:\n\t" \
  "s_mov_b32 vcc_lo, 7\n\t" \
  "s_branch .Lsf_s3\n\t" \
  ".Lsf_s3:\n\t" \
  "s_nop 7\n\t" \
  "s_nop 1\n\t" \
  "v_mov_b32 v193, v192\n\t" \
  "s_nop 1\n\t" \
  "v_permlane32_swap_b32 v192, v193\n\t" \
  "s_nop 1\n\t" \
  "v_max_f32 v192, v192, v193\n\t" \
  "s_nop 1\n\t" \
  "v_mov_b32 v193, v192\n\t" \
  "s_nop 1\n\t" \
  "v_permlane16_swap_b32 v192, v193\n\t" \
  "s_nop 1\n\t" \
  "v_max_f32 v192, v192, v193\n\t" \
  "v_max_f32 v192, 0, v192\n\t" \
  "v_sub_f32 v96, v96, v192\n\t" \
  "v_sub_f32 v97, v97, v192\n\t" \
  "v_sub_f32 v98, v98, v192\n\t" \
  "v_sub_f32 v99, v99, v192\n\t" \
  "v_sub_f32 v100, v100, v192\n\t" \
  "v_sub_f32 v101, v101, v192\n\t" \
  "v_sub_f32 v102, v102, v192\n\t" \
  "v_sub_f32 v103, v103, v192\n\t" \
  "v_sub_f32 v104, v104, v192\n\t" \
  "v_sub_f32 v105, v105, v192\n\t" \
  "v_sub_f32 v106, v106, v192\n\t" \
  "v_sub_f32 v107, v107, v192\n\t" \
  "v_sub_f32 v108, v108, v192\n\t" \
  "v_sub_f32 v109, v109, v192\n\t" \
  "v_sub_f32 v110, v110, v192\n\t" \
  "v_sub_f32 v111, v111, v192\n\t" \
  "v_sub_f32 v188, v188, v192\n\t" \
  "v_sub_f32 v189, v189, v192\n\t" \
  "v_sub_f32 v190, v190, v192\n\t" \
  "v_sub_f32 v191, v191, v192\n\t" \
  "v_exp_f32 v199, -v192\n\t" \
  "s_mov_b32 s99, 1\n\t" \
  "s_nop 1\n\t" \
  "s_cselect_b32 vcc_hi, 1, 0\n\t" \
  "s_cmp_eq_u32 vcc_lo, 0\n\t" \
  "s_cbranch_scc1 .Lsf_b03\n\t" \
  "s_cmp_eq_u32 vcc_lo, 1\n\t" \
  "s_cbranch_scc1 .Lsf_b13\n\t" \
  "s_cmp_eq_u32 vcc_lo, 2\n\t" \
  "s_cbranch_scc1 .Lsf_b23\n\t" \
  "s_cmp_eq_u32 vcc_lo, 3\n\t" \
  "s_cbranch_scc1 .Lsf_b33\n\t" \
  "s_cmp_eq_u32 vcc_lo, 4\n\t" \
  "s_cbranch_scc1 .Lsf_b43\n\t" \
  "s_cmp_eq_u32 vcc_lo, 5\n\t" \
  "s_cbranch_scc1 .Lsf_b53\n\t" \
  "s_cmp_eq_u32 vcc_lo, 6\n\t" \
  "s_cbranch_scc1 .Lsf_b63\n\t" \
  "s_cmp_eq_u32 vcc_lo, 7\n\t" \
  "s_cbranch_scc1 .Lsf_b73\n\t" \
  ".Lsf_b03:\n\t" \
  "s_cmp_lg_u32 vcc_hi, 0\n\t" \
  "s_branch .Lsf_r03\n\t" \
  ".Lsf_b13:\n\t" \
  "s_cmp_lg_u32 vcc_hi, 0\n\t" \
  "s_branch .Lsf_r13\n\t" \
  ".Lsf_b23:\n\t" \
  "s_cmp_lg_u32 vcc_hi, 0\n\t" \
  "s_branch .Lsf_r23\n\t" \
  ".Lsf_b33:\n\t" \
  "s_cmp_lg_u32 vcc_hi, 0\n\t" \
  "s_branch .Lsf_r33\n\t" \
  ".Lsf_b43:\n\t" \
  "s_cmp_lg_u32 vcc_hi, 0\n\t" \
  "s_branch .Lsf_r43\n\t" \
  ".Lsf_b53:\n\t" \
  "s_cmp_lg_u32 vcc_hi, 0\n\t" \
  "s_branch .Lsf_r53\n\t" \
  ".Lsf_b63:\n\t" \
  "s_cmp_lg_u32 vcc_hi, 0\n\t" \
  "s_branch .Lsf_r63\n\t" \
  ".Lsf_b73:\n\t" \
  "s_cmp_lg_u32 vcc_hi, 0\n\t" \
  "s_branch .Lsf_r73\n\t" \
  ".Lsf_p0:\n\t" \
  "s_mov_b32 vcc_lo, 0\n\t" \
  "s_branch .Lsf_rs\n\t" \
  ".Lsf_p1:\n\t" \
  "s_mov_b32 vcc_lo, 1\n\t" \
  "s_branch .Lsf_rs\n\t" \
  ".Lsf_p2:\n\t" \
  "s_mov_b32 vcc_lo, 2\n\t" \
  "s_branch .Lsf_rs\n\t" \
  ".Lsf_p3:\n\t" \
  "s_mov_b32 vcc_lo, 3\n\t" \
  "s_branch .Lsf_rs\n\t" \
  ".Lsf_p4:\n\t" \
  "s_mov_b32 vcc_lo, 4\n\t" \
  "s_branch .Lsf_rs\n\t" \
  ".Lsf_p5:\n\t" \
  "s_mov_b32 vcc_lo, 5\n\t" \
  "s_branch .Lsf_rs\n\t" \
  ".Lsf_p6:\n\t" \
  "s_mov_b32 vcc_lo, 6\n\t" \
  "s_branch .Lsf_rs\n\t" \
  ".Lsf_p7:\n\t" \
  "s_mov_b32 vcc_lo, 7\n\t" \
  "s_branch .Lsf_rs\n\t" \
  ".Lsf_rs:\n\t" \
  "s_nop 7\n\t" \
  "s_nop 7\n\t" \
  "v_accvgpr_read_b32 v193, a0\n\t" \
  "v_mul_f32 v193, v196, v193\n\t" \
  "v_accvgpr_write_b32 a0, v193\n\t" \
  "v_accvgpr_read_b32 v193, a1\n\t" \
  "v_mul_f32 v193, v196, v193\n\t" \
  "v_accvgpr_write_b32 a1, v193\n\t" \
  "v_accvgpr_read_b32 v193, a2\n\t" \
  "v_mul_f32 v193, v196, v193\n\t" \
  "v_accvgpr_write_b32 a2, v193\n\t" \
  "v_accvgpr_read_b32 v193, a3\n\t" \
  "v_mul_f32 v193, v196, v193\n\t" \
  "v_accvgpr_write_b32 a3, v193\n\t" \
  "v_accvgpr_read_b32 v193, a4\n\t" \
  "v_mul_f32 v193, v197, v193\n\t" \
  "v_accvgpr_write_b32 a4, v193\n\t" \
  "v_accvgpr_read_b32 v193, a5\n\t" \
  "v_mul_f32 v193, v197, v193\n\t" \
  "v_accvgpr_write_b32 a5, v193\n\t" \
  "v_accvgpr_read_b32 v193, a6\n\t" \
  "v_mul_f32 v193, v197, v193\n\t" \
  "v_accvgpr_write_b32 a6, v193\n\t" \
  "v_accvgpr_read_b32 v193, a7\n\t" \
  "v_mul_f32 v193, v197, v193\n\t" \
  "v_accvgpr_write_b32 a7, v193\n\t" \
  "v_accvgpr_read_b32 v193, a8\n\t" \
  "v_mul_f32 v193, v198, v193\n\t" \
  "v_accvgpr_write_b32 a8, v193\n\t" \
  "v_accvgpr_read_b32 v193, a9\n\t" \
  "v_mul_f32 v193, v198, v193\n\t" \
  "v_accvgpr_write_b32 a9, v193\n\t" \
  "v_accvgpr_read_b32 v193, a10\n\t" \
  "v_mul_f32 v193, v198, v193\n\t" \
  "v_accvgpr_write_b32 a10, v193\n\t" \
  "v_accvgpr_read_b32 v193, a11\n\t" \
  "v_mul_f32 v193, v198, v193\n\t" \
  "v_accvgpr_write_b32 a11, v193\n\t" \
  "v_accvgpr_read_b32 v193, a12\n\t" \
  "v_mul_f32 v193, v199, v193\n\t" \
  "v_accvgpr_write_b32 a12, v193\n\t" \
  "v_accvgpr_read_b32 v193, a13\n\t" \
  "v_mul_f32 v193, v199, v193\n\t" \
  "v_accvgpr_write_b32 a13, v193\n\t" \
  "v_accvgpr_read_b32 v193, a14\n\t" \
  "v_mul_f32 v193, v199, v193\n\t" \
  "v_accvgpr_write_b32 a14, v193\n\t" \
  "v_accvgpr_read_b32 v193, a15\n\t" \
  "v_mul_f32 v193, v199, v193\n\t" \
  "v_accvgpr_write_b32 a15, v193\n\t" \
  "v_accvgpr_read_b32 v193, a16\n\t" \
  "v_mul_f32 v193, v196, v193\n\t" \
  "v_accvgpr_write_b32 a16, v193\n\t" \
  "v_accvgpr_read_b32 v193, a17\n\t" \
  "v_mul_f32 v193, v196, v193\n\t" \
  "v_accvgpr_write_b32 a17, v193\n\t" \
  "v_accvgpr_read_b32 v193, a18\n\t" \
  "v_mul_f32 v193, v196, v193\n\t" \
  "v_accvgpr_write_b32 a18, v193\n\t" \
  "v_accvgpr_read_b32 v193, a19\n\t" \
  "v_mul_f32 v193, v196, v193\n\t" \
  "v_accvgpr_write_b32 a19, v193\n\t" \
  "v_accvgpr_read_b32 v193, a20\n\t" \
  "v_mul_f32 v193, v197, v193\n\t" \
  "v_accvgpr_write_b32 a20, v193\n\t" \
  "v_accvgpr_read_b32 v193, a21\n\t" \
  "v_mul_f32 v193, v197, v193\n\t" \
  "v_accvgpr_write_b32 a21, v193\n\t" \
  "v_accvgpr_read_b32 v193, a22\n\t" \
  "v_mul_f32 v193, v197, v193\n\t" \
  "v_accvgpr_write_b32 a22, v193\n\t" \
  "v_accvgpr_read_b32 v193, a23\n\t" \
  "v_mul_f32 v193, v197, v193\n\t" \
  "v_accvgpr_write_b32 a23, v193\n\t" \
  "v_accvgpr_read_b32 v193, a24\n\t" \
  "v_mul_f32 v193, v198, v193\n\t" \
  "v_accvgpr_write_b32 a24, v193\n\t" \
  "v_accvgpr_read_b32 v193, a25\n\t" \
  "v_mul_f32 v193, v198, v193\n\t" \
  "v_accvgpr_write_b32 a25, v193\n\t" \
  "v_accvgpr_read_b32 v193, a26\n\t" \
  "v_mul_f32 v193, v198, v193\n\t" \
  "v_accvgpr_write_b32 a26, v193\n\t" \
  "v_accvgpr_read_b32 v193, a27\n\t" \
  "v_mul_f32 v193, v198, v193\n\t" \
  "v_accvgpr_write_b32 a27, v193\n\t" \
  "v_accvgpr_read_b32 v193, a28\n\t" \
  "v_mul_f32 v193, v199, v193\n\t" \
  "v_accvgpr_write_b32 a28, v193\n\t" \
  "v_accvgpr_read_b32 v193, a29\n\t" \
  "v_mul_f32 v193, v199, v193\n\t" \
  "v_accvgpr_write_b32 a29, v193\n\t" \
  "v_accvgpr_read_b32 v193, a30\n\t" \
  "v_mul_f32 v193, v199, v193\n\t" \
  "v_accvgpr_write_b32 a30, v193\n\t" \
  "v_accvgpr_read_b32 v193, a31\n\t" \
  "v_mul_f32 v193, v199, v193\n\t" \
  "v_accvgpr_write_b32 a31, v193\n\t" \
  "v_accvgpr_read_b32 v193, a32\n\t" \
  "v_mul_f32 v193, v196, v193\n\t" \
  "v_accvgpr_write_b32 a32, v193\n\t" \
  "v_accvgpr_read_b32 v193, a33\n\t" \
  "v_mul_f32 v193, v196, v193\n\t" \
  "v_accvgpr_write_b32 a33, v193\n\t" \
  "v_accvgpr_read_b32 v193, a34\n\t" \
  "v_mul_f32 v193, v196, v193\n\t" \
  "v_accvgpr_write_b32 a34, v193\n\t" \
  "v_accvgpr_read_b32 v193, a35\n\t" \
  "v_mul_f32 v193, v196, v193\n\t" \
  "v_accvgpr_write_b32 a35, v193\n\t" \
  "v_accvgpr_read_b32 v193, a36\n\t" \
  "v_mul_f32 v193, v197, v193\n\t" \
  "v_accvgpr_write_b32 a36, v193\n\t" \
  "v_accvgpr_read_b32 v193, a37\n\t" \
  "v_mul_f32 v193, v197, v193\n\t" \
  "v_accvgpr_write_b32 a37, v193\n\t" \
  "v_accvgpr_read_b32 v193, a38\n\t" \
  "v_mul_f32 v193, v197, v193\n\t" \
  "v_accvgpr_write_b32 a38, v193\n\t" \
  "v_accvgpr_read_b32 v193, a39\n\t" \
  "v_mul_f32 v193, v197, v193\n\t" \
  "v_accvgpr_write_b32 a39, v193\n\t" \
  "v_accvgpr_read_b32 v193, a40\n\t" \
  "v_mul_f32 v193, v198, v193\n\t" \
  "v_accvgpr_write_b32 a40, v193\n\t" \
  "v_accvgpr_read_b32 v193, a41\n\t" \
  "v_mul_f32 v193, v198, v193\n\t" \
  "v_accvgpr_write_b32 a41, v193\n\t" \
  "v_accvgpr_read_b32 v193, a42\n\t" \
  "v_mul_f32 v193, v198, v193\n\t" \
  "v_accvgpr_write_b32 a42, v193\n\t" \
  "v_accvgpr_read_b32 v193, a43\n\t" \
  "v_mul_f32 v193, v198, v193\n\t" \
  "v_accvgpr_write_b32 a43, v193\n\t" \
  "v_accvgpr_read_b32 v193, a44\n\t" \
  "v_mul_f32 v193, v199, v193\n\t" \
  "v_accvgpr_write_b32 a44, v193\n\t" \
  "v_accvgpr_read_b32 v193, a45\n\t" \
  "v_mul_f32 v193, v199, v193\n\t" \
  "v_accvgpr_write_b32 a45, v193\n\t" \
  "v_accvgpr_read_b32 v193, a46\n\t" \
  "v_mul_f32 v193, v199, v193\n\t" \
  "v_accvgpr_write_b32 a46, v193\n\t" \
  "v_accvgpr_read_b32 v193, a47\n\t" \
  "v_mul_f32 v193, v199, v193\n\t" \
  "v_accvgpr_write_b32 a47, v193\n\t" \
  "v_accvgpr_read_b32 v193, a48\n\t" \
  "v_mul_f32 v193, v196, v193\n\t" \
  "v_accvgpr_write_b32 a48, v193\n\t" \
  "v_accvgpr_read_b32 v193, a49\n\t" \
  "v_mul_f32 v193, v196, v193\n\t" \
  "v_accvgpr_write_b32 a49, v193\n\t" \
  "v_accvgpr_read_b32 v193, a50\n\t" \
  "v_mul_f32 v193, v196, v193\n\t" \
  "v_accvgpr_write_b32 a50, v193\n\t" \
  "v_accvgpr_read_b32 v193, a51\n\t" \
  "v_mul_f32 v193, v196, v193\n\t" \
  "v_accvgpr_write_b32 a51, v193\n\t" \
  "v_accvgpr_read_b32 v193, a52\n\t" \
  "v_mul_f32 v193, v197, v193\n\t" \
  "v_accvgpr_write_b32 a52, v193\n\t" \
  "v_accvgpr_read_b32 v193, a53\n\t" \
  "v_mul_f32 v193, v197, v193\n\t" \
  "v_accvgpr_write_b32 a53, v193\n\t" \
  "v_accvgpr_read_b32 v193, a54\n\t" \
  "v_mul_f32 v193, v197, v193\n\t" \
  "v_accvgpr_write_b32 a54, v193\n\t" \
  "v_accvgpr_read_b32 v193, a55\n\t" \
  "v_mul_f32 v193, v197, v193\n\t" \
  "v_accvgpr_write_b32 a55, v193\n\t" \
  "v_accvgpr_read_b32 v193, a56\n\t" \
  "v_mul_f32 v193, v198, v193\n\t" \
  "v_accvgpr_write_b32 a56, v193\n\t" \
  "v_accvgpr_read_b32 v193, a57\n\t" \
  "v_mul_f32 v193, v198, v193\n\t" \
  "v_accvgpr_write_b32 a57, v193\n\t" \
  "v_accvgpr_read_b32 v193, a58\n\t" \
  "v_mul_f32 v193, v198, v193\n\t" \
  "v_accvgpr_write_b32 a58, v193\n\t" \
  "v_accvgpr_read_b32 v193, a59\n\t" \
  "v_mul_f32 v193, v198, v193\n\t" \
  "v_accvgpr_write_b32 a59, v193\n\t" \
  "v_accvgpr_read_b32 v193, a60\n\t" \
  "v_mul_f32 v193, v199, v193\n\t" \
  "v_accvgpr_write_b32 a60, v193\n\t" \
  "v_accvgpr_read_b32 v193, a61\n\t" \
  "v_mul_f32 v193, v199, v193\n\t" \
  "v_accvgpr_write_b32 a61, v193\n\t" \
  "v_accvgpr_read_b32 v193, a62\n\t" \
  "v_mul_f32 v193, v199, v193\n\t" \
  "v_accvgpr_write_b32 a62, v193\n\t" \
  "v_accvgpr_read_b32 v193, a63\n\t" \
  "v_mul_f32 v193, v199, v193\n\t" \
  "v_accvgpr_write_b32 a63, v193\n\t" \
  "v_accvgpr_read_b32 v193, a64\n\t" \
  "v_mul_f32 v193, v196, v193\n\t" \
  "v_accvgpr_write_b32 a64, v193\n\t" \
  "v_accvgpr_read_b32 v193, a65\n\t" \
  "v_mul_f32 v193, v196, v193\n\t" \
  "v_accvgpr_write_b32 a65, v193\n\t" \
  "v_accvgpr_read_b32 v193, a66\n\t" \
  "v_mul_f32 v193, v196, v193\n\t" \
  "v_accvgpr_write_b32 a66, v193\n\t" \
  "v_accvgpr_read_b32 v193, a67\n\t" \
  "v_mul_f32 v193, v196, v193\n\t" \
  "v_accvgpr_write_b32 a67, v193\n\t" \
  "v_accvgpr_read_b32 v193, a68\n\t" \
  "v_mul_f32 v193, v197, v193\n\t" \
  "v_accvgpr_write_b32 a68, v193\n\t" \
  "v_accvgpr_read_b32 v193, a69\n\t" \
  "v_mul_f32 v193, v197, v193\n\t" \
  "v_accvgpr_write_b32 a69, v193\n\t" \
  "v_accvgpr_read_b32 v193, a70\n\t" \
  "v_mul_f32 v193, v197, v193\n\t" \
  "v_accvgpr_write_b32 a70, v193\n\t" \
  "v_accvgpr_read_b32 v193, a71\n\t" \
  "v_mul_f32 v193, v197, v193\n\t" \
  "v_accvgpr_write_b32 a71, v193\n\t" \
  "v_accvgpr_read_b32 v193, a72\n\t" \
  "v_mul_f32 v193, v198, v193\n\t" \
  "v_accvgpr_write_b32 a72, v193\n\t" \
  "v_accvgpr_read_b32 v193, a73\n\t" \
  "v_mul_f32 v193, v198, v193\n\t" \
  "v_accvgpr_write_b32 a73, v193\n\t" \
  "v_accvgpr_read_b32 v193, a74\n\t" \
  "v_mul_f32 v193, v198, v193\n\t" \
  "v_accvgpr_write_b32 a74, v193\n\t" \
  "v_accvgpr_read_b32 v193, a75\n\t" \
  "v_mul_f32 v193, v198, v193\n\t" \
  "v_accvgpr_write_b32 a75, v193\n\t" \
  "v_accvgpr_read_b32 v193, a76\n\t" \
  "v_mul_f32 v193, v199, v193\n\t" \
  "v_accvgpr_write_b32 a76, v193\n\t" \
  "v_accvgpr_read_b32 v193, a77\n\t" \
  "v_mul_f32 v193, v199, v193\n\t" \
  "v_accvgpr_write_b32 a77, v193\n\t" \
  "v_accvgpr_read_b32 v193, a78\n\t" \
  "v_mul_f32 v193, v199, v193\n\t" \
  "v_accvgpr_write_b32 a78, v193\n\t" \
  "v_accvgpr_read_b32 v193, a79\n\t" \
  "v_mul_f32 v193, v199, v193\n\t" \
  "v_accvgpr_write_b32 a79, v193\n\t" \
  "v_accvgpr_read_b32 v193, a80\n\t" \
  "v_mul_f32 v193, v196, v193\n\t" \
  "v_accvgpr_write_b32 a80, v193\n\t" \
  "v_accvgpr_read_b32 v193, a81\n\t" \
  "v_mul_f32 v193, v196, v193\n\t" \
  "v_accvgpr_write_b32 a81, v193\n\t" \
  "v_accvgpr_read_b32 v193, a82\n\t" \
  "v_mul_f32 v193, v196, v193\n\t" \
  "v_accvgpr_write_b32 a82, v193\n\t" \
  "v_accvgpr_read_b32 v193, a83\n\t" \
  "v_mul_f32 v193, v196, v193\n\t" \
  "v_accvgpr_write_b32 a83, v193\n\t" \
  "v_accvgpr_read_b32 v193, a84\n\t" \
  "v_mul_f32 v193, v197, v193\n\t" \
  "v_accvgpr_write_b32 a84, v193\n\t" \
  "v_accvgpr_read_b32 v193, a85\n\t" \
  "v_mul_f32 v193, v197, v193\n\t" \
  "v_accvgpr_write_b32 a85, v193\n\t" \
  "v_accvgpr_read_b32 v193, a86\n\t" \
  "v_mul_f32 v193, v197, v193\n\t" \
  "v_accvgpr_write_b32 a86, v193\n\t" \
  "v_accvgpr_read_b32 v193, a87\n\t" \
  "v_mul_f32 v193, v197, v193\n\t" \
  "v_accvgpr_write_b32 a87, v193\n\t" \
  "v_accvgpr_read_b32 v193, a88\n\t" \
  "v_mul_f32 v193, v198, v193\n\t" \
  "v_accvgpr_write_b32 a88, v193\n\t" \
  "v_accvgpr_read_b32 v193, a89\n\t" \
  "v_mul_f32 v193, v198, v193\n\t" \
  "v_accvgpr_write_b32 a89, v193\n\t" \
  "v_accvgpr_read_b32 v193, a90\n\t" \
  "v_mul_f32 v193, v198, v193\n\t" \
  "v_accvgpr_write_b32 a90, v193\n\t" \
  "v_accvgpr_read_b32 v193, a91\n\t" \
  "v_mul_f32 v193, v198, v193\n\t" \
  "v_accvgpr_write_b32 a91, v193\n\t" \
  "v_accvgpr_read_b32 v193, a92\n\t" \
  "v_mul_f32 v193, v199, v193\n\t" \
  "v_accvgpr_write_b32 a92, v193\n\t" \
  "v_accvgpr_read_b32 v193, a93\n\t" \
  "v_mul_f32 v193, v199, v193\n\t" \
  "v_accvgpr_write_b32 a93, v193\n\t" \
  "v_accvgpr_read_b32 v193, a94\n\t" \
  "v_mul_f32 v193, v199, v193\n\t" \
  "v_accvgpr_write_b32 a94, v193\n\t" \
  "v_accvgpr_read_b32 v193, a95\n\t" \
  "v_mul_f32 v193, v199, v193\n\t" \
  "v_accvgpr_write_b32 a95, v193\n\t" \
  "v_accvgpr_read_b32 v193, a96\n\t" \
  "v_mul_f32 v193, v196, v193\n\t" \
  "v_accvgpr_write_b32 a96, v193\n\t" \
  "v_accvgpr_read_b32 v193, a97\n\t" \
  "v_mul_f32 v193, v196, v193\n\t" \
  "v_accvgpr_write_b32 a97, v193\n\t" \
  "v_accvgpr_read_b32 v193, a98\n\t" \
  "v_mul_f32 v193, v196, v193\n\t" \
  "v_accvgpr_write_b32 a98, v193\n\t" \
  "v_accvgpr_read_b32 v193, a99\n\t" \
  "v_mul_f32 v193, v196, v193\n\t" \
  "v_accvgpr_write_b32 a99, v193\n\t" \
  "v_accvgpr_read_b32 v193, a100\n\t" \
  "v_mul_f32 v193, v197, v193\n\t" \
  "v_accvgpr_write_b32 a100, v193\n\t" \
  "v_accvgpr_read_b32 v193, a101\n\t" \
  "v_mul_f32 v193, v197, v193\n\t" \
  "v_accvgpr_write_b32 a101, v193\n\t" \
  "v_accvgpr_read_b32 v193, a102\n\t" \
  "v_mul_f32 v193, v197, v193\n\t" \
  "v_accvgpr_write_b32 a102, v193\n\t" \
  "v_accvgpr_read_b32 v193, a103\n\t" \
  "v_mul_f32 v193, v197, v193\n\t" \
  "v_accvgpr_write_b32 a103, v193\n\t" \
  "v_accvgpr_read_b32 v193, a104\n\t" \
  "v_mul_f32 v193, v198, v193\n\t" \
  "v_accvgpr_write_b32 a104, v193\n\t" \
  "v_accvgpr_read_b32 v193, a105\n\t" \
  "v_mul_f32 v193, v198, v193\n\t" \
  "v_accvgpr_write_b32 a105, v193\n\t" \
  "v_accvgpr_read_b32 v193, a106\n\t" \
  "v_mul_f32 v193, v198, v193\n\t" \
  "v_accvgpr_write_b32 a106, v193\n\t" \
  "v_accvgpr_read_b32 v193, a107\n\t" \
  "v_mul_f32 v193, v198, v193\n\t" \
  "v_accvgpr_write_b32 a107, v193\n\t" \
  "v_accvgpr_read_b32 v193, a108\n\t" \
  "v_mul_f32 v193, v199, v193\n\t" \
  "v_accvgpr_write_b32 a108, v193\n\t" \
  "v_accvgpr_read_b32 v193, a109\n\t" \
  "v_mul_f32 v193, v199, v193\n\t" \
  "v_accvgpr_write_b32 a109, v193\n\t" \
  "v_accvgpr_read_b32 v193, a110\n\t" \
  "v_mul_f32 v193, v199, v193\n\t" \
  "v_accvgpr_write_b32 a110, v193\n\t" \
  "v_accvgpr_read_b32 v193, a111\n\t" \
  "v_mul_f32 v193, v199, v193\n\t" \
  "v_accvgpr_write_b32 a111, v193\n\t" \
  "v_accvgpr_read_b32 v193, a112\n\t" \
  "v_mul_f32 v193, v196, v193\n\t" \
  "v_accvgpr_write_b32 a112, v193\n\t" \
  "v_accvgpr_read_b32 v193, a113\n\t" \
  "v_mul_f32 v193, v196, v193\n\t" \
  "v_accvgpr_write_b32 a113, v193\n\t" \
  "v_accvgpr_read_b32 v193, a114\n\t" \
  "v_mul_f32 v193, v196, v193\n\t" \
  "v_accvgpr_write_b32 a114, v193\n\t" \
  "v_accvgpr_read_b32 v193, a115\n\t" \
  "v_mul_f32 v193, v196, v193\n\t" \
  "v_accvgpr_write_b32 a115, v193\n\t" \
  "v_accvgpr_read_b32 v193, a116\n\t" \
  "v_mul_f32 v193, v197, v193\n\t" \
  "v_accvgpr_write_b32 a116, v193\n\t" \
  "v_accvgpr_read_b32 v193, a117\n\t" \
  "v_mul_f32 v193, v197, v193\n\t" \
  "v_accvgpr_write_b32 a117, v193\n\t" \
  "v_accvgpr_read_b32 v193, a118\n\t" \
  "v_mul_f32 v193, v197, v193\n\t" \
  "v_accvgpr_write_b32 a118, v193\n\t" \
  "v_accvgpr_read_b32 v193, a119\n\t" \
  "v_mul_f32 v193, v197, v193\n\t" \
  "v_accvgpr_write_b32 a119, v193\n\t" \
  "v_accvgpr_read_b32 v193, a120\n\t" \
  "v_mul_f32 v193, v198, v193\n\t" \
  "v_accvgpr_write_b32 a120, v193\n\t" \
  "v_accvgpr_read_b32 v193, a121\n\t" \
  "v_mul_f32 v193, v198, v193\n\t" \
  "v_accvgpr_write_b32 a121, v193\n\t" \
  "v_accvgpr_read_b32 v193, a122\n\t" \
  "v_mul_f32 v193, v198, v193\n\t" \
  "v_accvgpr_write_b32 a122, v193\n\t" \
  "v_accvgpr_read_b32 v193, a123\n\t" \
  "v_mul_f32 v193, v198, v193\n\t" \
  "v_accvgpr_write_b32 a123, v193\n\t" \
  "v_accvgpr_read_b32 v193, a124\n\t" \
  "v_mul_f32 v193, v199, v193\n\t" \
  "v_accvgpr_write_b32 a124, v193\n\t" \
  "v_accvgpr_read_b32 v193, a125\n\t" \
  "v_mul_f32 v193, v199, v193\n\t" \
  "v_accvgpr_write_b32 a125, v193\n\t" \
  "v_accvgpr_read_b32 v193, a126\n\t" \
  "v_mul_f32 v193, v199, v193\n\t" \
  "v_accvgpr_write_b32 a126, v193\n\t" \
  "v_accvgpr_read_b32 v193, a127\n\t" \
  "v_mul_f32 v193, v199, v193\n\t" \
  "v_accvgpr_write_b32 a127, v193\n\t" \
  "v_mul_f32 v160, v196, v160\n\t" \
  "v_mul_f32 v161, v196, v161\n\t" \
  "v_mul_f32 v162, v196, v162\n\t" \
  "v_mul_f32 v163, v196, v163\n\t" \
  "v_mov_b32 v196, 1.0\n\t" \
  "v_mul_f32 v164, v197, v164\n\t" \
  "v_mul_f32 v165, v197, v165\n\t" \
  "v_mul_f32 v166, v197, v166\n\t" \
  "v_mul_f32 v167, v197, v167\n\t" \
  "v_mov_b32 v197, 1.0\n\t" \
  "v_mul_f32 v168, v198, v168\n\t" \
  "v_mul_f32 v169, v198, v169\n\t" \
  "v_mul_f32 v170, v198, v170\n\t" \
  "v_mul_f32 v171, v198, v171\n\t" \
  "v_mov_b32 v198, 1.0\n\t" \
  "v_mul_f32 v172, v199, v172\n\t" \
  "v_mul_f32 v173, v199, v173\n\t" \
  "v_mul_f32 v174, v199, v174\n\t" \
  "v_mul_f32 v175, v199, v175\n\t" \
  "v_mov_b32 v199, 1.0\n\t" \
  "s_mov_b32 s99, 0\n\t" \
  "s_nop 1\n\t" \
  "s_cmp_eq_u32 vcc_lo, 0\n\t" \
  "s_cbranch_scc1 .Lsf_q0\n\t" \
  "s_cmp_eq_u32 vcc_lo, 1\n\t" \
  "s_cbranch_scc1 .Lsf_q1\n\t" \
  "s_cmp_eq_u32 vcc_lo, 2\n\t" \
  "s_cbranch_scc1 .Lsf_q2\n\t" \
  "s_cmp_eq_u32 vcc_lo, 3\n\t" \
  "s_cbranch_scc1 .Lsf_q3\n\t" \
  "s_cmp_eq_u32 vcc_lo, 4\n\t" \
  "s_cbranch_scc1 .Lsf_q4\n\t" \
  "s_cmp_eq_u32 vcc_lo, 5\n\t" \
  "s_cbranch_scc1 .Lsf_q5\n\t" \
  "s_cmp_eq_u32 vcc_lo, 6\n\t" \
  "s_cbranch_scc1 .Lsf_q6\n\t" \
  "s_cmp_eq_u32 vcc_lo, 7\n\t" \
  "s_cbranch_scc1 .Lsf_q7\n\t" \
  "s_endpgm\n\t" \
  ".Lsf_v:\n\t" \
  "s_mov_b32 s98, 0\n\t" \
  ".Lsf_v1:\n\t" \
  "s_sleep 4\n\t" \
  "buffer_load_dword v209, v205, %[ws], 0 offen sc1\n\t" \
  "s_waitcnt vmcnt(0)\n\t" \
  "v_and_b32 v206, 0xbfffbfff, v209\n\t" \
  "v_cmp_ne_u32_e64 s[96:97], %[tag], v206\n\t" \
  "s_cmp_lg_u64 s[96:97], 0\n\t" \
  "s_cbranch_scc0 .Lsf_w\n\t" \
  "s_add_u32 s98, s98, 1\n\t" \
  "s_cmp_lt_u32 s98, %[cap]\n\t" \
  "s_cbranch_scc1 .Lsf_v1\n\t" \
  "v_mov_b32 v206, %[tag]\n\t" \
  "buffer_store_dword v206, off, %[ws], 0 offset:256 sc1\n\t" \
  "s_branch .Lsf_w\n\t" \
  ".subsection 0\n\t" \
  ".Lat_end:\n\t" \
  ""
#define AT_CLOB "v208", "v209", "v210", "v211", "v212", "v214", "v215", "v216", "v217", "v218", "v219", "v235", "v236", "v237", "v238", "v239", "s92", "s93", "s94", "s95", "s96", "s97", "s98", "s99", "vcc", "scc", "memory"
#define AT_PF1
#define AT_NU 1
#define AT_BN 0
#define AT_XF "+{v[0:15]}"
#define AT_WF "+{v[16:47]}"
#define TL_ASM \
  "v_pk_fma_f32 v[0:1], v[128:129], v[96:97], v[0:1]\n\t" \
  "v_pk_fma_f32 v[2:3], v[130:131], v[98:99], v[2:3]\n\t" \
  "v_pk_fma_f32 v[16:17], v[132:133], v[112:113], v[16:17]\n\t" \
  "v_pk_fma_f32 v[18:19], v[134:135], v[114:115], v[18:19]\n\t" \
  "v_pk_fma_f32 v[4:5], v[128:129], v[100:101], v[4:5]\n\t" \
  "v_pk_fma_f32 v[6:7], v[130:131], v[102:103], v[6:7]\n\t" \
  "v_pk_fma_f32 v[20:21], v[132:133], v[116:117], v[20:21]\n\t" \
  "v_pk_fma_f32 v[22:23], v[134:135], v[118:119], v[22:23]\n\t" \
  "v_cvt_pk_bf16_f32 v160, v0, v1\n\t" \
  "v_cvt_pk_bf16_f32 v161, v2, v3\n\t" \
  "v_cvt_pk_bf16_f32 v162, v16, v17\n\t" \
  "v_cvt_pk_bf16_f32 v163, v18, v19\n\t" \
  "v_pk_fma_f32 v[8:9], v[128:129], v[104:105], v[8:9]\n\t" \
  "v_pk_fma_f32 v[10:11], v[130:131], v[106:107], v[10:11]\n\t" \
  "v_pk_fma_f32 v[24:25], v[132:133], v[120:121], v[24:25]\n\t" \
  "v_pk_fma_f32 v[26:27], v[134:135], v[122:123], v[26:27]\n\t" \
  "v_cvt_pk_bf16_f32 v164, v4, v5\n\t" \
  "v_cvt_pk_bf16_f32 v165, v6, v7\n\t" \
  "v_cvt_pk_bf16_f32 v166, v20, v21\n\t" \
  "v_cvt_pk_bf16_f32 v167, v22, v23\n\t" \
  "v_pk_fma_f32 v[12:13], v[128:129], v[108:109], v[12:13]\n\t" \
  "v_pk_fma_f32 v[14:15], v[130:131], v[110:111], v[14:15]\n\t" \
  "v_pk_fma_f32 v[28:29], v[132:133], v[124:125], v[28:29]\n\t" \
  "v_pk_fma_f32 v[30:31], v[134:135], v[126:127], v[30:31]\n\t" \
  "v_cvt_pk_bf16_f32 v168, v8, v9\n\t" \
  "v_cvt_pk_bf16_f32 v169, v10, v11\n\t" \
  "v_cvt_pk_bf16_f32 v170, v24, v25\n\t" \
  "v_cvt_pk_bf16_f32 v171, v26, v27\n\t" \
  "v_cvt_pk_bf16_f32 v172, v12, v13\n\t" \
  "v_cvt_pk_bf16_f32 v173, v14, v15\n\t" \
  "v_cvt_pk_bf16_f32 v174, v28, v29\n\t" \
  "v_cvt_pk_bf16_f32 v175, v30, v31\n\t" \
  "v_dot2_f32_bf16 v152, v160, v160, 0\n\t" \
  "v_dot2_f32_bf16 v153, v164, v164, 0\n\t" \
  "v_dot2_f32_bf16 v154, v168, v168, 0\n\t" \
  "v_dot2_f32_bf16 v155, v172, v172, 0\n\t" \
  "v_dot2_f32_bf16 v152, v161, v161, v152\n\t" \
  "v_dot2_f32_bf16 v153, v165, v165, v153\n\t" \
  "v_dot2_f32_bf16 v154, v169, v169, v154\n\t" \
  "v_dot2_f32_bf16 v155, v173, v173, v155\n\t" \
  "v_dot2_f32_bf16 v152, v162, v162, v152\n\t" \
  "v_dot2_f32_bf16 v153, v166, v166, v153\n\t" \
  "v_dot2_f32_bf16 v154, v170, v170, v154\n\t" \
  "v_dot2_f32_bf16 v155, v174, v174, v155\n\t" \
  "v_dot2_f32_bf16 v152, v163, v163, v152\n\t" \
  "v_dot2_f32_bf16 v153, v167, v167, v153\n\t" \
  "v_dot2_f32_bf16 v154, v171, v171, v154\n\t" \
  "v_dot2_f32_bf16 v155, v175, v175, v155\n\t" \
  "v_mfma_f32_16x16x32_bf16 v[156:159], a[208:211], v[160:163], 0\n\t" \
  "s_lshl_b32 s64, %[wid], 10\n\t" \
  "v_add_u32 v234, s64, v242\n\t" \
  "v_mov_b32 v250, s87\n\t" \
  "v_mfma_f32_16x16x32_bf16 v[156:159], a[212:215], v[164:167], v[156:159]\n\t" \
  "s_lshl_b32 s61, %[bid], 11\n\t" \
  "v_permlane32_swap_b32 v152, v154\n\t" \
  "v_mfma_f32_16x16x32_bf16 v[156:159], a[216:219], v[168:171], v[156:159]\n\t" \
  "s_and_b32 s61, s61, 0x2000\n\t" \
  "v_permlane32_swap_b32 v153, v155\n\t" \
  "v_mfma_f32_16x16x32_bf16 v[156:159], a[220:223], v[172:175], v[156:159]\n\t" \
  "s_xor_b32 s61, s61, %[nb]\n\t" \
  "v_add_f32 v152, v152, v154\n\t" \
  "v_add_f32 v153, v153, v155\n\t" \
  "v_writelane_b32 v250, s88, 1\n\t" \
  "s_and_b32 s80, %[bid], 3\n\t" \
  "v_permlane16_swap_b32 v152, v153\n\t" \
  "s_lshr_b32 s60, %[bid], 6\n\t" \
  "s_lshl2_add_u32 s60, s60, s80\n\t" \
  "v_add_f32 v158, v157, v158\n\t" \
  "v_add_f32 v157, v152, v153\n\t" \
  "v_mov_b32 v159, v250\n\t" \
  "ds_write_b128 v234, v[156:159]\n\t" \
  "s_lshl_b32 s62, s60, 15\n\t" \
  "s_add_u32 s62, s62, s61\n\t" \
  "s_add_u32 s62, s62, 524288\n\t" \
  "s_and_b32 s63, %[bid], 0x20\n\t" \
  "s_lshl_b32 s63, s63, 9\n\t" \
  "s_add_u32 s77, s62, s63\n\t" \
  "s_and_b32 s63, %[bid], 0x18\n\t" \
  "s_lshl_b32 s63, s63, 8\n\t" \
  "s_add_u32 s77, s77, s63\n\t" \
  "s_add_u32 s77, s77, s64\n\t" \
  "v_lshl_add_u32 v248, %[wid], 3, v242\n\t" \
  "v_mov_b32 v217, v243\n\t" \
  "v_mov_b32 v219, v243\n\t" \
  "s_cmp_gt_u32 %[wid], 1\n\t" \
  "s_waitcnt lgkmcnt(0)\n\t" \
  "s_barrier\n\t" \
  "s_cbranch_scc1 .Ltl_np\n\t" \
  "ds_read2st64_b64 v[208:211], v248 offset1:2\n\t" \
  "ds_read2st64_b64 v[212:215], v248 offset0:4 offset1:6\n\t" \
  "s_waitcnt lgkmcnt(1)\n\t" \
  "v_pk_add_f32 v[208:209], v[208:209], v[210:211]\n\t" \
  "s_waitcnt lgkmcnt(0)\n\t" \
  "v_pk_add_f32 v[212:213], v[212:213], v[214:215]\n\t" \
  "s_nop 0\n\t" \
  "v_add_f32 v216, v208, v212\n\t" \
  "v_add_f32 v218, v209, v213\n\t" \
  "buffer_store_dwordx4 v[216:219], v242, %[ws], s77 offen sc1\n\t" \
  ".Ltl_np:\n\t" \
  STP("s_memrealtime s[90:91]\n\t") \
  STP("s_memtime s[82:83]\n\t") \
  STP("s_waitcnt lgkmcnt(0)\n\t") \
  STP("s_or_b32 s90, s90, 1\n\t") \
  STP("v_writelane_b32 %[stv], s90, 40\n\t") \
  STP("v_writelane_b32 %[stv], s82, 41\n\t") \
  "s_and_b32 s63, %[wid], 2\n\t" \
  "s_lshl_b32 s63, s63, 13\n\t" \
  "s_add_u32 s62, s62, s63\n\t" \
  "s_and_b32 s63, %[wid], 1\n\t" \
  "s_lshl_b32 s63, s63, 12\n\t" \
  "s_add_u32 s62, s62, s63\n\t" \
  "ds_read_b128 v[128:131], v237 offset:2688\n\t" \
  "ds_read_b128 v[132:135], v237 offset:2752\n\t" \
  "ds_read_b128 v[136:139], v237 offset:2816\n\t" \
  "ds_read_b128 v[140:143], v237 offset:2880\n\t" \
  "ds_read_b128 v[144:147], v237 offset:2944\n\t" \
  "ds_read_b128 v[148:151], v237 offset:3008\n\t" \
  "s_mov_b32 s75, 0x05040100\n\t" \
  "s_mov_b32 s76, 0x07060302\n\t" \
  "s_mov_b32 s72, 0\n\t" \
  "s_mov_b32 s81, 0xffff0000\n\t" \
  "s_bfe_u32 s79, %[bid], 0x30003\n\t" \
  "s_and_b32 s63, %[bid], 0x4\n\t" \
  "s_lshl_b32 s63, s63, 2\n\t" \
  "s_add_u32 s60, s60, s63\n\t" \
  "v_lshlrev_b32 v0, 16, v160\n\t" \
  "v_and_b32 v1, 0xffff0000, v160\n\t" \
  "v_lshlrev_b32 v2, 16, v161\n\t" \
  "v_and_b32 v3, 0xffff0000, v161\n\t" \
  "v_lshlrev_b32 v16, 16, v162\n\t" \
  "v_and_b32 v17, 0xffff0000, v162\n\t" \
  "v_lshlrev_b32 v18, 16, v163\n\t" \
  "v_and_b32 v19, 0xffff0000, v163\n\t" \
  "v_lshlrev_b32 v4, 16, v164\n\t" \
  "v_and_b32 v5, 0xffff0000, v164\n\t" \
  "v_lshlrev_b32 v6, 16, v165\n\t" \
  "v_and_b32 v7, 0xffff0000, v165\n\t" \
  "v_lshlrev_b32 v20, 16, v166\n\t" \
  "v_and_b32 v21, 0xffff0000, v166\n\t" \
  "v_lshlrev_b32 v22, 16, v167\n\t" \
  "v_and_b32 v23, 0xffff0000, v167\n\t" \
  "v_lshlrev_b32 v8, 16, v168\n\t" \
  "v_and_b32 v9, 0xffff0000, v168\n\t" \
  "v_lshlrev_b32 v10, 16, v169\n\t" \
  "v_and_b32 v11, 0xffff0000, v169\n\t" \
  "v_lshlrev_b32 v24, 16, v170\n\t" \
  "v_and_b32 v25, 0xffff0000, v170\n\t" \
  "v_lshlrev_b32 v26, 16, v171\n\t" \
  "v_and_b32 v27, 0xffff0000, v171\n\t" \
  "v_lshlrev_b32 v12, 16, v172\n\t" \
  "v_and_b32 v13, 0xffff0000, v172\n\t" \
  "buffer_load_dwordx4 v[208:211], v242, %[ws], s62 offen offset:0 sc1\n\t" \
  "buffer_load_dwordx4 v[212:215], v242, %[ws], s62 offen offset:1024 sc1\n\t" \
  "buffer_load_dwordx4 v[216:219], v242, %[ws], s62 offen offset:2048 sc1\n\t" \
  "buffer_load_dwordx4 v[220:223], v242, %[ws], s62 offen offset:3072 sc1\n\t" \
  "s_branch .Lht0\n\t" \
  ".p2align 12\n\t" \
  ".Lht0:\n\t" \
  "s_cbranch_execz .Lht0n\n\t" \
  "v_lshlrev_b32 v14, 16, v173\n\t" \
  "v_and_b32 v15, 0xffff0000, v173\n\t" \
  "v_lshlrev_b32 v28, 16, v174\n\t" \
  "v_and_b32 v29, 0xffff0000, v174\n\t" \
  "v_lshlrev_b32 v30, 16, v175\n\t" \
  "v_and_b32 v31, 0xffff0000, v175\n\t" \
  "s_lshl_b32 s63, s60, 6\n\t" \
  "s_sub_u32 s63, 0x7ff, s63\n\t" \
  "v_sub_u32 v248, s63, v244\n\t" \
  "v_and_b32 v246, 31, v244\n\t" \
  "s_and_b32 s63, %[bid], 0x18\n\t" \
  "s_lshl_b32 s63, s63, 7\n\t" \
  "s_add_u32 s65, s61, s63\n\t" \
  "s_and_b32 s63, %[bid], 0x3\n\t" \
  "s_lshl_b32 s63, s63, 8\n\t" \
  "s_add_u32 s65, s65, s63\n\t" \
  "s_add_u32 s65, s65, 65536\n\t" \
  "v_lshl_add_u32 v235, v246, 3, s65\n\t" \
  "s_lshl_b32 s68, 1, s60\n\t" \
  "s_lshl2_add_u32 s65, s79, %[wid]\n\t" \
  "s_lshl_b32 s63, s65, 13\n\t" \
  "s_and_b32 s63, s63, 0x2000\n\t" \
  "s_xor_b32 s63, s63, %[nb]\n\t" \
  "s_lshr_b32 s65, s65, 1\n\t" \
  "s_lshl_b32 s65, s65, 8\n\t" \
  "s_add_u32 s65, s65, s63\n\t" \
  "s_lshl3_add_u32 s65, s60, s65\n\t" \
  "s_add_u32 s65, s65, 65536\n\t" \
  "v_bfrev_b32 v249, 1\n\t" \
  "s_nop 0\n\t" \
  "v_writelane_b32 v249, 0, 63\n\t" \
  "v_mov_b32 v159, v243\n\t" \
  "s_add_u32 s63, s61, 262144\n\t" \
  "v_and_b32 v236, -32, v234\n\t" \
  "v_add_u32 v236, s63, v236\n\t" \
  "v_mov_b32 v113, v243\n\t" \
  "v_mov_b32 v115, v243\n\t" \
  "v_mov_b32 v117, v243\n\t" \
  "v_mov_b32 v119, v243\n\t" \
  "v_mov_b32 v121, v243\n\t" \
  "v_mov_b32 v123, v243\n\t" \
  "v_mov_b32 v125, v243\n\t" \
  "v_mov_b32 v127, v243\n\t" \
  "v_and_b32 v239, 48, v244\n\t" \
  "v_lshl_add_u32 v239, %[wid], 7, v239\n\t" \
  "v_and_b32 v157, 15, v244\n\t" \
  "v_bfrev_b32 v156, 1\n\t" \
  "s_lshl_b32 s66, s60, 13\n\t" \
  "s_and_b32 s66, s66, 0x3c000\n\t" \
  "s_and_b32 s63, %[bid], 0x1\n\t" \
  "s_lshl_b32 s63, s63, 12\n\t" \
  "s_add_u32 s66, s66, s63\n\t" \
  "s_and_b32 s63, %[bid], 0x38\n\t" \
  "s_lshl_b32 s63, s63, 6\n\t" \
  "s_add_u32 s66, s66, s63\n\t" \
  "s_add_u32 s66, s66, 262144\n\t" \
  "s_xor_b32 s77, s66, 0x2000\n\t" \
  "s_lshl_b32 s67, s60, 12\n\t" \
  "s_and_b32 s67, s67, 0x1c000\n\t" \
  "s_and_b32 s63, %[bid], 0x20\n\t" \
  "s_lshl_b32 s63, s63, 8\n\t" \
  "s_add_u32 s67, s67, s63\n\t" \
  "s_xor_b32 s67, s67, %[nb]\n\t" \
  "s_and_b32 s63, %[bid], 0x3\n\t" \
  "s_lshl_b32 s63, s63, 11\n\t" \
  "s_add_u32 s67, s67, s63\n\t" \
  "s_and_b32 s63, %[bid], 0x18\n\t" \
  "s_lshl_b32 s63, s63, 6\n\t" \
  "s_add_u32 s67, s67, s63\n\t" \
  "s_add_u32 s67, s67, 1966080\n\t" \
  "v_perm_b32 v240, v34, v32, s75\n\t" \
  "v_perm_b32 v34, v34, v32, s76\n\t" \
  "v_perm_b32 v32, v35, v33, s75\n\t" \
  "v_perm_b32 v35, v35, v33, s76\n\t" \
  "v_perm_b32 v33, v38, v36, s75\n\t" \
  "v_perm_b32 v38, v38, v36, s76\n\t" \
  "v_perm_b32 v36, v39, v37, s75\n\t" \
  "v_perm_b32 v39, v39, v37, s76\n\t" \
  "v_perm_b32 v37, v42, v40, s75\n\t" \
  "v_perm_b32 v42, v42, v40, s76\n\t" \
  "v_perm_b32 v40, v43, v41, s75\n\t" \
  "v_perm_b32 v43, v43, v41, s76\n\t" \
  "v_perm_b32 v41, v46, v44, s75\n\t" \
  "v_perm_b32 v46, v46, v44, s76\n\t" \
  "v_perm_b32 v44, v47, v45, s75\n\t" \
  "v_perm_b32 v47, v47, v45, s76\n\t" \
  "v_perm_b32 v45, v50, v48, s75\n\t" \
  "v_perm_b32 v50, v50, v48, s76\n\t" \
  "v_perm_b32 v48, v51, v49, s75\n\t" \
  "v_perm_b32 v51, v51, v49, s76\n\t" \
  "v_perm_b32 v49, v54, v52, s75\n\t" \
  "v_perm_b32 v54, v54, v52, s76\n\t" \
  "v_perm_b32 v52, v55, v53, s75\n\t" \
  "v_perm_b32 v55, v55, v53, s76\n\t" \
  "v_perm_b32 v53, v58, v56, s75\n\t" \
  "v_perm_b32 v58, v58, v56, s76\n\t" \
  "v_perm_b32 v56, v59, v57, s75\n\t" \
  "v_perm_b32 v59, v59, v57, s76\n\t" \
  "v_perm_b32 v57, v62, v60, s75\n\t" \
  "v_perm_b32 v62, v62, v60, s76\n\t" \
  "v_perm_b32 v60, v63, v61, s75\n\t" \
  "v_perm_b32 v63, v63, v61, s76\n\t" \
  "s_waitcnt vmcnt(0)\n\t" \
  ".Ltl_c0:\n\t" \
  "v_xor_b32 v152, %[tag], v209\n\t" \
  "v_bitop3_b32 v152, v152, v211, %[tag] bitop3:0xf6\n\t" \
  "v_bitop3_b32 v152, v152, v213, %[tag] bitop3:0xf6\n\t" \
  "v_bitop3_b32 v152, v152, v215, %[tag] bitop3:0xf6\n\t" \
  "v_bitop3_b32 v152, v152, v217, %[tag] bitop3:0xf6\n\t" \
  "v_bitop3_b32 v152, v152, v219, %[tag] bitop3:0xf6\n\t" \
  "v_bitop3_b32 v152, v152, v221, %[tag] bitop3:0xf6\n\t" \
  "v_bitop3_b32 v152, v152, v223, %[tag] bitop3:0xf6\n\t" \
  "v_cmp_ne_u32 vcc, 0, v152\n\t" \
  "v_add_f32 v152, v208, v216\n\t" \
  "v_add_f32 v153, v210, v218\n\t" \
  "v_add_f32 v154, v212, v220\n\t" \
  "v_add_f32 v155, v214, v222\n\t" \
  "s_cbranch_vccnz .Ltl_s0\n\t" \
  ".Ltl_k0:\n\t" \
  "ds_write_b128 v234, v[152:155] offset:4096\n\t" \
  "buffer_load_dwordx2 v[216:217], v235, %[ws], 0 offen sc1\n\t" \
  "s_waitcnt lgkmcnt(0)\n\t" \
  "s_barrier\n\t" \
  STP("s_memrealtime s[90:91]\n\t") \
  STP("s_memtime s[82:83]\n\t") \
  STP("s_waitcnt lgkmcnt(0)\n\t") \
  STP("s_or_b32 s90, s90, 1\n\t") \
  STP("v_writelane_b32 %[stv], s90, 42\n\t") \
  STP("v_writelane_b32 %[stv], s82, 43\n\t") \
  "ds_read_b128 v[96:99], v242 offset:4096\n\t" \
  "ds_read_b128 v[100:103], v242 offset:5120\n\t" \
  "ds_read_b128 v[104:107], v242 offset:6144\n\t" \
  "ds_read_b128 v[108:111], v242 offset:7168\n\t" \
  "s_waitcnt lgkmcnt(2)\n\t" \
  "v_pk_add_f32 v[96:97], v[96:97], v[100:101]\n\t" \
  "v_pk_add_f32 v[98:99], v[98:99], v[102:103]\n\t" \
  "s_waitcnt lgkmcnt(0)\n\t" \
  "v_pk_add_f32 v[104:105], v[104:105], v[108:109]\n\t" \
  "v_pk_add_f32 v[106:107], v[106:107], v[110:111]\n\t" \
  "v_pk_add_f32 v[96:97], v[96:97], v[104:105]\n\t" \
  "v_pk_add_f32 v[98:99], v[98:99], v[106:107]\n\t" \
  "v_mul_f32 v96, 0x3a800000, v96\n\t" \
  "v_mul_f32 v97, 0x3a800000, v97\n\t" \
  "v_readlane_b32 s87, v99, 0\n\t" \
  "v_fma_f32 v97, -v96, v96, v97\n\t" \
  "v_readlane_b32 s88, v99, 1\n\t" \
  "v_max_f32 v97, 0, v97\n\t" \
  "v_add_f32 v97, 0x3727c5ac, v97\n\t" \
  "v_rsq_f32 v208, v97\n\t" \
  "v_fma_f32 v98, -v96, s87, v98\n\t" \
  "v_fma_f32 v98, v208, v98, s88\n\t" \
  "v_cvt_pk_bf16_f32 v98, v98, v98\n\t" \
  "v_ashrrev_i32 v99, 31, v98\n\t" \
  "v_or_b32 v99, 0x80000000, v99\n\t" \
  "v_xor_b32 v98, v98, v99\n\t" \
  "v_and_or_b32 v158, v98, s81, v248\n\t" \
  "v_mul_f32 v209, v208, -v96\n\t" \
  "v_mov_b32 v212, v208\n\t" \
  "v_max_u32_dpp v158, v158, v158 quad_perm:[1,0,3,2] row_mask:0xf bank_mask:0xf\n\t" \
  "v_mov_b32 v213, v209\n\t" \
  "v_permlane32_swap_b32 v208, v212\n\t" \
  "v_max_u32_dpp v158, v158, v158 quad_perm:[2,3,0,1] row_mask:0xf bank_mask:0xf\n\t" \
  "v_permlane32_swap_b32 v209, v213\n\t" \
  "v_mov_b32 v210, v208\n\t" \
  "v_max_u32_dpp v158, v158, v158 row_half_mirror row_mask:0xf bank_mask:0xf\n\t" \
  "v_mov_b32 v211, v209\n\t" \
  "v_mov_b32 v214, v212\n\t" \
  "v_max_u32_dpp v158, v158, v158 row_mirror row_mask:0xf bank_mask:0xf\n\t" \
  "v_mov_b32 v215, v213\n\t" \
  "v_permlane16_swap_b32 v208, v210\n\t" \
  "v_max_u32_dpp v158, v158, v158 row_bcast:15 row_mask:0xa bank_mask:0xf\n\t" \
  "v_permlane16_swap_b32 v209, v211\n\t" \
  "v_permlane16_swap_b32 v212, v214\n\t" \
  "v_max_u32_dpp v158, v158, v158 row_bcast:31 row_mask:0xc bank_mask:0xf\n\t" \
  "buffer_store_dwordx2 v[158:159], v249, %[ws], s65 offen sc1\n\t" \
  "v_readlane_b32 s69, v158, 63\n\t" \
  STP("s_memrealtime s[90:91]\n\t") \
  STP("s_memtime s[82:83]\n\t") \
  STP("s_waitcnt lgkmcnt(0)\n\t") \
  STP("s_or_b32 s90, s90, 1\n\t") \
  STP("v_writelane_b32 %[stv], s90, 44\n\t") \
  STP("v_writelane_b32 %[stv], s82, 45\n\t") \
  "v_permlane16_swap_b32 v213, v215\n\t" \
  "s_andn2_b32 s73, 63, s69\n\t" \
  "s_waitcnt vmcnt(1)\n\t" \
  "v_cmp_eq_u32 vcc, %[tag], v217\n\t" \
  "s_mov_b32 s74, 0x100\n\t" \
  "s_nop 0\n\t" \
  "v_cndmask_b32 v152, 0, v216, vcc\n\t" \
  "s_bcnt1_i32_b32 s78, vcc_lo\n\t" \
  "v_cmp_lt_u32 vcc, s69, v152\n\t" \
  "s_cbranch_vccz .Ltl_cd\n\t" \
  ".Ltl_ce:\n\t" \
  "v_max_u32_dpp v152, v152, v152 quad_perm:[1,0,3,2] row_mask:0xf bank_mask:0xf\n\t" \
  "s_lshl_b32 s63, s60, 17\n\t" \
  "s_lshl_b32 s64, s79, 8\n\t" \
  "v_max_u32_dpp v152, v152, v152 quad_perm:[2,3,0,1] row_mask:0xf bank_mask:0xf\n\t" \
  "s_add_u32 s63, s63, s64\n\t" \
  "s_lshl_b32 s64, %[wid], 6\n\t" \
  "v_max_u32_dpp v152, v152, v152 row_half_mirror row_mask:0xf bank_mask:0xf\n\t" \
  "s_add_u32 s63, s63, s64\n\t" \
  "v_lshl_add_u32 v238, v157, 11, s63\n\t" \
  "v_max_u32_dpp v152, v152, v152 row_mirror row_mask:0xf bank_mask:0xf\n\t" \
  "s_nop 1\n\t" \
  "v_max_u32_dpp v152, v152, v152 row_bcast:15 row_mask:0xa bank_mask:0xf\n\t" \
  "s_nop 0\n\t" \
  "v_readlane_b32 s70, v152, 31\n\t" \
  "s_max_u32 s70, s70, s69\n\t" \
  "s_andn2_b32 s63, 0x780, s70\n\t" \
  "s_lshl_b32 s63, s63, 7\n\t" \
  "s_andn2_b32 s71, 64, s70\n\t" \
  "s_lshl_b32 s71, s71, 6\n\t" \
  "s_or_b32 s71, s71, s63\n\t" \
  "buffer_load_dwordx2 v[216:217], v235, %[ws], 0 offen sc1\n\t" \
  "buffer_load_dwordx4 v[218:221], v236, %[ws], s71 offen sc1\n\t" \
  "buffer_load_dwordx4 v[222:225], v236, %[ws], s71 offen offset:16 sc1\n\t" \
  "v_bfe_u32 v153, v244, 4, 1\n\t" \
  "v_lshl_add_u32 v238, v153, 5, v238\n\t" \
  "v_lshrrev_b32 v154, 5, v244\n\t" \
  "v_lshl_add_u32 v238, v154, 4, v238\n\t" \
  "s_mov_b64 s[96:97], %[out]\n\t" \
  "s_and_b32 s97, s97, 0xffff\n\t" \
  "s_mov_b32 s98, 0x400000\n\t" \
  "s_mov_b32 s99, 0x20000\n\t" \
  "s_lshr_b32 s64, %[bid], 3\n\t" \
  "s_and_b32 s63, s64, 27\n\t" \
  "s_cmp_lt_u32 s64, 24\n\t" \
  "s_cselect_b32 s64, s64, s63\n\t" \
  "s_mul_i32 s63, s80, 56\n\t" \
  "s_add_u32 s64, s64, s63\n\t" \
  "s_and_b32 s63, %[bid], 4\n\t" \
  "s_mul_i32 s63, s63, 7\n\t" \
  "s_add_u32 s64, s64, s63\n\t" \
  "s_lshl_b32 s63, %[wid], 12\n\t" \
  "s_and_b32 s63, s63, 0x2000\n\t" \
  "s_xor_b32 s85, s63, %[nb]\n\t" \
  "s_lshr_b32 s63, s64, 2\n\t" \
  "s_lshl_b32 s63, s63, 14\n\t" \
  "s_add_u32 s85, s85, s63\n\t" \
  "s_and_b32 s63, s64, 3\n\t" \
  "s_lshl_b32 s63, s63, 11\n\t" \
  "s_add_u32 s85, s85, s63\n\t" \
  "s_add_u32 s85, s85, 1048576\n\t" \
  "v_and_b32 v247, 0x7f0, v234\n\t" \
  "v_lshlrev_b32 v245, 6, v154\n\t" \
  "v_lshl_add_u32 v245, v153, 4, v245\n\t" \
  "v_and_b32 v153, 1, v244\n\t" \
  "v_lshl_add_u32 v245, v153, 5, v245\n\t" \
  "v_lshl_add_u32 v245, %[wid], 7, v245\n\t" \
  "v_and_b32 v153, 14, v244\n\t" \
  "v_lshl_add_u32 v245, v153, 8, v245\n\t" \
  "v_lshlrev_b32 v246, 2, v246\n\t" \
  "v_add_u32 v248, 0x80, v246\n\t" \
  "s_mov_b32 s72, 0\n\t" \
  "s_lshr_b32 s62, %[bid], 5\n\t" \
  "s_bfe_u32 s63, %[bid], 0x10002\n\t" \
  "s_sub_u32 s62, s62, s63\n\t" \
  "s_cmp_ge_u32 s78, 0\n\t" \
  "s_cselect_b32 s84, 1, 0\n\t" \
  "s_cmp_eq_u32 s62, 6\n\t" \
  "s_cselect_b32 s84, 0, s84\n\t" \
  "s_cmp_lt_u32 s78, 33\n\t" \
  "s_cselect_b32 s78, 0, 0x80000000\n\t" \
  "s_cmp_lg_u32 s84, 0\n\t" \
  "s_cbranch_scc1 .Lht1\n\t" \
  ".Ltl_h2:\n\t" \
  "v_pk_fma_f32 v[112:113], v[0:1], v[208:209], v[208:209] op_sel:[0,0,1] op_sel_hi:[1,0,1]\n\t" \
  "v_pk_fma_f32 v[114:115], v[2:3], v[208:209], v[208:209] op_sel:[0,0,1] op_sel_hi:[1,0,1]\n\t" \
  "v_pk_fma_f32 v[116:117], v[16:17], v[208:209], v[208:209] op_sel:[0,0,1] op_sel_hi:[1,0,1]\n\t" \
  "v_pk_fma_f32 v[118:119], v[18:19], v[208:209], v[208:209] op_sel:[0,0,1] op_sel_hi:[1,0,1]\n\t" \
  "v_pk_fma_f32 v[112:113], v[112:113], v[136:137], v[128:129]\n\t" \
  "v_pk_fma_f32 v[114:115], v[114:115], v[138:139], v[130:131]\n\t" \
  "v_pk_fma_f32 v[116:117], v[116:117], v[140:141], v[132:133]\n\t" \
  "v_pk_fma_f32 v[118:119], v[118:119], v[142:143], v[134:135]\n\t" \
  "v_cvt_pk_bf16_f32 v96, v112, v113\n\t" \
  "v_cvt_pk_bf16_f32 v97, v114, v115\n\t" \
  "v_cvt_pk_bf16_f32 v98, v116, v117\n\t" \
  "v_cvt_pk_bf16_f32 v99, v118, v119\n\t" \
  "v_pk_fma_f32 v[120:121], v[4:5], v[210:211], v[210:211] op_sel:[0,0,1] op_sel_hi:[1,0,1]\n\t" \
  "v_pk_fma_f32 v[122:123], v[6:7], v[210:211], v[210:211] op_sel:[0,0,1] op_sel_hi:[1,0,1]\n\t" \
  "v_pk_fma_f32 v[124:125], v[20:21], v[210:211], v[210:211] op_sel:[0,0,1] op_sel_hi:[1,0,1]\n\t" \
  "v_pk_fma_f32 v[126:127], v[22:23], v[210:211], v[210:211] op_sel:[0,0,1] op_sel_hi:[1,0,1]\n\t" \
  "v_pk_fma_f32 v[120:121], v[120:121], v[136:137], v[128:129]\n\t" \
  "v_pk_fma_f32 v[122:123], v[122:123], v[138:139], v[130:131]\n\t" \
  "v_pk_fma_f32 v[124:125], v[124:125], v[140:141], v[132:133]\n\t" \
  "v_pk_fma_f32 v[126:127], v[126:127], v[142:143], v[134:135]\n\t" \
  "v_cvt_pk_bf16_f32 v100, v120, v121\n\t" \
  "v_cvt_pk_bf16_f32 v101, v122, v123\n\t" \
  "v_cvt_pk_bf16_f32 v102, v124, v125\n\t" \
  "v_cvt_pk_bf16_f32 v103, v126, v127\n\t" \
  "v_pk_fma_f32 v[112:113], v[8:9], v[212:213], v[212:213] op_sel:[0,0,1] op_sel_hi:[1,0,1]\n\t" \
  "v_pk_fma_f32 v[114:115], v[10:11], v[212:213], v[212:213] op_sel:[0,0,1] op_sel_hi:[1,0,1]\n\t" \
  "v_pk_fma_f32 v[116:117], v[24:25], v[212:213], v[212:213] op_sel:[0,0,1] op_sel_hi:[1,0,1]\n\t" \
  "v_pk_fma_f32 v[118:119], v[26:27], v[212:213], v[212:213] op_sel:[0,0,1] op_sel_hi:[1,0,1]\n\t" \
  "v_pk_fma_f32 v[112:113], v[112:113], v[136:137], v[128:129]\n\t" \
  "v_pk_fma_f32 v[114:115], v[114:115], v[138:139], v[130:131]\n\t" \
  "v_pk_fma_f32 v[116:117], v[116:117], v[140:141], v[132:133]\n\t" \
  "v_pk_fma_f32 v[118:119], v[118:119], v[142:143], v[134:135]\n\t" \
  "v_cvt_pk_bf16_f32 v104, v112, v113\n\t" \
  "v_cvt_pk_bf16_f32 v105, v114, v115\n\t" \
  "v_cvt_pk_bf16_f32 v106, v116, v117\n\t" \
  "v_cvt_pk_bf16_f32 v107, v118, v119\n\t" \
  "v_pk_fma_f32 v[120:121], v[12:13], v[214:215], v[214:215] op_sel:[0,0,1] op_sel_hi:[1,0,1]\n\t" \
  "v_pk_fma_f32 v[122:123], v[14:15], v[214:215], v[214:215] op_sel:[0,0,1] op_sel_hi:[1,0,1]\n\t" \
  "v_pk_fma_f32 v[124:125], v[28:29], v[214:215], v[214:215] op_sel:[0,0,1] op_sel_hi:[1,0,1]\n\t" \
  "v_pk_fma_f32 v[126:127], v[30:31], v[214:215], v[214:215] op_sel:[0,0,1] op_sel_hi:[1,0,1]\n\t" \
  "v_pk_fma_f32 v[120:121], v[120:121], v[136:137], v[128:129]\n\t" \
  "v_pk_fma_f32 v[122:123], v[122:123], v[138:139], v[130:131]\n\t" \
  "v_pk_fma_f32 v[124:125], v[124:125], v[140:141], v[132:133]\n\t" \
  "v_pk_fma_f32 v[126:127], v[126:127], v[142:143], v[134:135]\n\t" \
  "v_cvt_pk_bf16_f32 v108, v120, v121\n\t" \
  "v_cvt_pk_bf16_f32 v109, v122, v123\n\t" \
  "v_cvt_pk_bf16_f32 v110, v124, v125\n\t" \
  "v_cvt_pk_bf16_f32 v111, v126, v127\n\t" \
  "v_sub_u32 v152, s74, v157\n\t" \
  "v_bfrev_b32 v155, 1\n\t" \
  "v_cmp_eq_u32 vcc, 0, v152\n\t" \
  "v_lshlrev_b32 v160, 16, v96\n\t" \
  "v_and_b32 v161, 0xffff0000, v96\n\t" \
  "v_lshlrev_b32 v162, 16, v97\n\t" \
  "v_and_b32 v163, 0xffff0000, v97\n\t" \
  "v_lshlrev_b32 v164, 16, v98\n\t" \
  "v_and_b32 v165, 0xffff0000, v98\n\t" \
  "v_lshlrev_b32 v166, 16, v99\n\t" \
  "v_and_b32 v167, 0xffff0000, v99\n\t" \
  "v_pk_fma_f32 v[160:161], v[144:145], v[160:161], v[0:1]\n\t" \
  "v_pk_fma_f32 v[162:163], v[146:147], v[162:163], v[2:3]\n\t" \
  "v_pk_fma_f32 v[164:165], v[148:149], v[164:165], v[16:17]\n\t" \
  "v_pk_fma_f32 v[166:167], v[150:151], v[166:167], v[18:19]\n\t" \
  "v_cndmask_b32 v228, v238, v155, vcc\n\t" \
  "v_cvt_pk_bf16_f32 v112, v160, v161\n\t" \
  "v_cvt_pk_bf16_f32 v114, v164, v165\n\t" \
  "v_cvt_pk_bf16_f32 v113, v162, v163\n\t" \
  "v_cvt_pk_bf16_f32 v115, v166, v167\n\t" \
  "v_or_b32 v232, s78, v228\n\t" \
  "s_mov_b32 s64, 0\n\t" \
  "v_permlane16_swap_b32 v112, v114\n\t" \
  "v_permlane16_swap_b32 v113, v115\n\t" \
  "buffer_store_dwordx4 v[112:115], v232, s[96:99], s64 offen sc1\n\t" \
  "v_cmp_eq_u32 vcc, 16, v152\n\t" \
  "v_lshlrev_b32 v160, 16, v100\n\t" \
  "v_and_b32 v161, 0xffff0000, v100\n\t" \
  "v_lshlrev_b32 v162, 16, v101\n\t" \
  "v_and_b32 v163, 0xffff0000, v101\n\t" \
  "v_lshlrev_b32 v164, 16, v102\n\t" \
  "v_and_b32 v165, 0xffff0000, v102\n\t" \
  "v_lshlrev_b32 v166, 16, v103\n\t" \
  "v_and_b32 v167, 0xffff0000, v103\n\t" \
  "v_pk_fma_f32 v[160:161], v[144:145], v[160:161], v[4:5]\n\t" \
  "v_pk_fma_f32 v[162:163], v[146:147], v[162:163], v[6:7]\n\t" \
  "v_pk_fma_f32 v[164:165], v[148:149], v[164:165], v[20:21]\n\t" \
  "v_pk_fma_f32 v[166:167], v[150:151], v[166:167], v[22:23]\n\t" \
  "v_cndmask_b32 v229, v238, v155, vcc\n\t" \
  "v_cvt_pk_bf16_f32 v116, v160, v161\n\t" \
  "v_cvt_pk_bf16_f32 v118, v164, v165\n\t" \
  "v_cvt_pk_bf16_f32 v117, v162, v163\n\t" \
  "v_cvt_pk_bf16_f32 v119, v166, v167\n\t" \
  "v_or_b32 v232, s78, v229\n\t" \
  "s_mov_b32 s64, 32768\n\t" \
  "v_permlane16_swap_b32 v116, v118\n\t" \
  "v_permlane16_swap_b32 v117, v119\n\t" \
  "buffer_store_dwordx4 v[116:119], v232, s[96:99], s64 offen sc1\n\t" \
  "v_cmp_eq_u32 vcc, 32, v152\n\t" \
  "v_lshlrev_b32 v160, 16, v104\n\t" \
  "v_and_b32 v161, 0xffff0000, v104\n\t" \
  "v_lshlrev_b32 v162, 16, v105\n\t" \
  "v_and_b32 v163, 0xffff0000, v105\n\t" \
  "v_lshlrev_b32 v164, 16, v106\n\t" \
  "v_and_b32 v165, 0xffff0000, v106\n\t" \
  "v_lshlrev_b32 v166, 16, v107\n\t" \
  "v_and_b32 v167, 0xffff0000, v107\n\t" \
  "v_pk_fma_f32 v[160:161], v[144:145], v[160:161], v[8:9]\n\t" \
  "v_pk_fma_f32 v[162:163], v[146:147], v[162:163], v[10:11]\n\t" \
  "v_pk_fma_f32 v[164:165], v[148:149], v[164:165], v[24:25]\n\t" \
  "v_pk_fma_f32 v[166:167], v[150:151], v[166:167], v[26:27]\n\t" \
  "v_cndmask_b32 v230, v238, v155, vcc\n\t" \
  "v_cvt_pk_bf16_f32 v120, v160, v161\n\t" \
  "v_cvt_pk_bf16_f32 v122, v164, v165\n\t" \
  "v_cvt_pk_bf16_f32 v121, v162, v163\n\t" \
  "v_cvt_pk_bf16_f32 v123, v166, v167\n\t" \
  "v_or_b32 v232, s78, v230\n\t" \
  "s_mov_b32 s64, 65536\n\t" \
  "v_permlane16_swap_b32 v120, v122\n\t" \
  "v_permlane16_swap_b32 v121, v123\n\t" \
  "buffer_store_dwordx4 v[120:123], v232, s[96:99], s64 offen sc1\n\t" \
  "v_cmp_eq_u32 vcc, 48, v152\n\t" \
  "v_lshlrev_b32 v160, 16, v108\n\t" \
  "v_and_b32 v161, 0xffff0000, v108\n\t" \
  "v_lshlrev_b32 v162, 16, v109\n\t" \
  "v_and_b32 v163, 0xffff0000, v109\n\t" \
  "v_lshlrev_b32 v164, 16, v110\n\t" \
  "v_and_b32 v165, 0xffff0000, v110\n\t" \
  "v_lshlrev_b32 v166, 16, v111\n\t" \
  "v_and_b32 v167, 0xffff0000, v111\n\t" \
  "v_pk_fma_f32 v[160:161], v[144:145], v[160:161], v[12:13]\n\t" \
  "v_pk_fma_f32 v[162:163], v[146:147], v[162:163], v[14:15]\n\t" \
  "v_pk_fma_f32 v[164:165], v[148:149], v[164:165], v[28:29]\n\t" \
  "v_pk_fma_f32 v[166:167], v[150:151], v[166:167], v[30:31]\n\t" \
  "v_cndmask_b32 v231, v238, v155, vcc\n\t" \
  "v_cvt_pk_bf16_f32 v124, v160, v161\n\t" \
  "v_cvt_pk_bf16_f32 v126, v164, v165\n\t" \
  "v_cvt_pk_bf16_f32 v125, v162, v163\n\t" \
  "v_cvt_pk_bf16_f32 v127, v166, v167\n\t" \
  "v_or_b32 v232, s78, v231\n\t" \
  "s_mov_b32 s64, 98304\n\t" \
  "v_permlane16_swap_b32 v124, v126\n\t" \
  "v_permlane16_swap_b32 v125, v127\n\t" \
  "buffer_store_dwordx4 v[124:127], v232, s[96:99], s64 offen sc1\n\t" \
  STP("s_memrealtime s[90:91]\n\t") \
  STP("s_memtime s[82:83]\n\t") \
  STP("s_waitcnt lgkmcnt(0)\n\t") \
  STP("s_or_b32 s90, s90, 1\n\t") \
  STP("v_writelane_b32 %[stv], s90, 46\n\t") \
  STP("v_writelane_b32 %[stv], s82, 47\n\t") \
  "s_cmp_lg_u32 s84, 0\n\t" \
  "s_cbranch_scc1 .Ltl_oe\n\t" \
  "s_waitcnt vmcnt(4)\n\t" \
  "s_branch .Ltl_c1\n\t" \
  ".Ltl_s0:\n\t" \
  "s_sleep 1\n\t" \
  "buffer_load_dwordx4 v[208:211], v242, %[ws], s62 offen offset:0 sc1\n\t" \
  "buffer_load_dwordx4 v[212:215], v242, %[ws], s62 offen offset:1024 sc1\n\t" \
  "buffer_load_dwordx4 v[216:219], v242, %[ws], s62 offen offset:2048 sc1\n\t" \
  "buffer_load_dwordx4 v[220:223], v242, %[ws], s62 offen offset:3072 sc1\n\t" \
  "s_add_u32 s72, s72, 1\n\t" \
  "s_cmp_lt_u32 s72, %[cap]\n\t" \
  "s_waitcnt vmcnt(0)\n\t" \
  "s_cbranch_scc1 .Ltl_c0\n\t" \
  "buffer_store_dword v243, off, %[ws], 0 offset:256 sc1\n\t" \
  "s_branch .Ltl_k0\n\t" \
  ".Ltl_cd:\n\t" \
  "s_mov_b32 s74, s73\n\t" \
  "s_and_b32 s63, s73, 15\n\t" \
  "v_cmp_eq_u32 vcc, s63, v157\n\t" \
  "s_cmp_lt_u32 s73, 32\n\t" \
  "s_cbranch_scc1 .Ltl_d0\n\t" \
  "s_cmp_lt_u32 s73, 48\n\t" \
  "s_cbranch_scc1 .Ltl_e2\n\t" \
  ".Ltl_e3:\n\t" \
  "v_pk_fma_f32 v[96:97], v[12:13], v[214:215], v[214:215] op_sel:[0,0,1] op_sel_hi:[1,0,1]\n\t" \
  "v_pk_fma_f32 v[98:99], v[14:15], v[214:215], v[214:215] op_sel:[0,0,1] op_sel_hi:[1,0,1]\n\t" \
  "v_pk_fma_f32 v[100:101], v[28:29], v[214:215], v[214:215] op_sel:[0,0,1] op_sel_hi:[1,0,1]\n\t" \
  "v_pk_fma_f32 v[102:103], v[30:31], v[214:215], v[214:215] op_sel:[0,0,1] op_sel_hi:[1,0,1]\n\t" \
  "v_mov_b32 v120, v172\n\t" \
  "v_mov_b32 v122, v173\n\t" \
  "v_mov_b32 v124, v174\n\t" \
  "v_mov_b32 v126, v175\n\t" \
  "s_branch .Ltl_cs\n\t" \
  ".Ltl_e2:\n\t" \
  "v_pk_fma_f32 v[96:97], v[8:9], v[212:213], v[212:213] op_sel:[0,0,1] op_sel_hi:[1,0,1]\n\t" \
  "v_pk_fma_f32 v[98:99], v[10:11], v[212:213], v[212:213] op_sel:[0,0,1] op_sel_hi:[1,0,1]\n\t" \
  "v_pk_fma_f32 v[100:101], v[24:25], v[212:213], v[212:213] op_sel:[0,0,1] op_sel_hi:[1,0,1]\n\t" \
  "v_pk_fma_f32 v[102:103], v[26:27], v[212:213], v[212:213] op_sel:[0,0,1] op_sel_hi:[1,0,1]\n\t" \
  "v_mov_b32 v120, v168\n\t" \
  "v_mov_b32 v122, v169\n\t" \
  "v_mov_b32 v124, v170\n\t" \
  "v_mov_b32 v126, v171\n\t" \
  "s_branch .Ltl_cs\n\t" \
  ".Ltl_d0:\n\t" \
  "s_cmp_lt_u32 s73, 16\n\t" \
  "s_cbranch_scc1 .Ltl_e0\n\t" \
  ".Ltl_e1:\n\t" \
  "v_pk_fma_f32 v[96:97], v[4:5], v[210:211], v[210:211] op_sel:[0,0,1] op_sel_hi:[1,0,1]\n\t" \
  "v_pk_fma_f32 v[98:99], v[6:7], v[210:211], v[210:211] op_sel:[0,0,1] op_sel_hi:[1,0,1]\n\t" \
  "v_pk_fma_f32 v[100:101], v[20:21], v[210:211], v[210:211] op_sel:[0,0,1] op_sel_hi:[1,0,1]\n\t" \
  "v_pk_fma_f32 v[102:103], v[22:23], v[210:211], v[210:211] op_sel:[0,0,1] op_sel_hi:[1,0,1]\n\t" \
  "v_mov_b32 v120, v164\n\t" \
  "v_mov_b32 v122, v165\n\t" \
  "v_mov_b32 v124, v166\n\t" \
  "v_mov_b32 v126, v167\n\t" \
  "s_branch .Ltl_cs\n\t" \
  ".Ltl_e0:\n\t" \
  "v_pk_fma_f32 v[96:97], v[0:1], v[208:209], v[208:209] op_sel:[0,0,1] op_sel_hi:[1,0,1]\n\t" \
  "v_pk_fma_f32 v[98:99], v[2:3], v[208:209], v[208:209] op_sel:[0,0,1] op_sel_hi:[1,0,1]\n\t" \
  "v_pk_fma_f32 v[100:101], v[16:17], v[208:209], v[208:209] op_sel:[0,0,1] op_sel_hi:[1,0,1]\n\t" \
  "v_pk_fma_f32 v[102:103], v[18:19], v[208:209], v[208:209] op_sel:[0,0,1] op_sel_hi:[1,0,1]\n\t" \
  "v_mov_b32 v120, v160\n\t" \
  "v_mov_b32 v122, v161\n\t" \
  "v_mov_b32 v124, v162\n\t" \
  "v_mov_b32 v126, v163\n\t" \
  ".Ltl_cs:\n\t" \
  "v_pk_fma_f32 v[96:97], v[96:97], v[136:137], v[128:129]\n\t" \
  "v_pk_fma_f32 v[98:99], v[98:99], v[138:139], v[130:131]\n\t" \
  "v_pk_fma_f32 v[100:101], v[100:101], v[140:141], v[132:133]\n\t" \
  "v_pk_fma_f32 v[102:103], v[102:103], v[142:143], v[134:135]\n\t" \
  "v_cndmask_b32 v239, v156, v239, vcc\n\t" \
  "v_cvt_pk_bf16_f32 v112, v96, v97\n\t" \
  "v_cvt_pk_bf16_f32 v114, v98, v99\n\t" \
  "v_cvt_pk_bf16_f32 v116, v100, v101\n\t" \
  "v_cvt_pk_bf16_f32 v118, v102, v103\n\t" \
  "buffer_store_dwordx4 v[112:115], v239, %[ws], s66 offen offset:0 sc1\n\t" \
  "buffer_store_dwordx4 v[116:119], v239, %[ws], s66 offen offset:64 sc1\n\t" \
  "buffer_store_dwordx4 v[112:115], v239, %[ws], s77 offen offset:0 sc1\n\t" \
  "buffer_store_dwordx4 v[116:119], v239, %[ws], s77 offen offset:64 sc1\n\t" \
  "buffer_store_dwordx4 v[120:123], v239, %[ws], s67 offen offset:0 sc1\n\t" \
  "buffer_store_dwordx4 v[124:127], v239, %[ws], s67 offen offset:64 sc1\n\t" \
  "s_branch .Ltl_ce\n\t" \
  ".p2align 12\n\t" \
  ".Lht1:\n\t" \
  "s_cbranch_execz .Lht1n\n\t" \
  "s_waitcnt vmcnt(0)\n\t" \
  ".Ltl_c1:\n\t" \
  "v_cmp_eq_u32 vcc, %[tag], v217\n\t" \
  "v_xor_b32 v153, %[tag], v219\n\t" \
  "v_bitop3_b32 v153, v153, v221, %[tag] bitop3:0xf6\n\t" \
  "v_cndmask_b32 v152, 0, v216, vcc\n\t" \
  "v_bitop3_b32 v153, v153, v223, %[tag] bitop3:0xf6\n\t" \
  "v_bitop3_b32 v153, v153, v225, %[tag] bitop3:0xf6\n\t" \
  "v_max_u32_dpp v152, v152, v152 quad_perm:[1,0,3,2] row_mask:0xf bank_mask:0xf\n\t" \
  "s_or_b32 s77, vcc_lo, s68\n\t" \
  "v_cmp_ne_u32 vcc, 0, v153\n\t" \
  "v_max_u32_dpp v152, v152, v152 quad_perm:[2,3,0,1] row_mask:0xf bank_mask:0xf\n\t" \
  "s_nop 1\n\t" \
  "v_max_u32_dpp v152, v152, v152 row_half_mirror row_mask:0xf bank_mask:0xf\n\t" \
  "s_nop 1\n\t" \
  "v_max_u32_dpp v152, v152, v152 row_mirror row_mask:0xf bank_mask:0xf\n\t" \
  "s_nop 1\n\t" \
  "v_max_u32_dpp v152, v152, v152 row_bcast:15 row_mask:0xa bank_mask:0xf\n\t" \
  "s_nop 0\n\t" \
  "v_readlane_b32 s70, v152, 31\n\t" \
  "s_max_u32 s70, s70, s69\n\t" \
  "s_andn2_b32 s63, 0x780, s70\n\t" \
  "s_lshl_b32 s63, s63, 7\n\t" \
  "s_andn2_b32 s64, 64, s70\n\t" \
  "s_lshl_b32 s64, s64, 6\n\t" \
  "s_or_b32 s64, s64, s63\n\t" \
  "s_xor_b32 s63, s64, s71\n\t" \
  "s_mov_b32 s71, s64\n\t" \
  "s_or_b32 vcc_lo, vcc_lo, vcc_hi\n\t" \
  "s_or_b32 s65, s63, vcc_lo\n\t" \
  "s_not_b32 s93, s77\n\t" \
  "s_or_b32 s63, s65, s93\n\t" \
  "s_cbranch_scc1 .Ltl_s1\n\t" \
  ".Ltl_k1:\n\t" \
  STP("s_memrealtime s[90:91]\n\t") \
  STP("s_memtime s[82:83]\n\t") \
  STP("s_waitcnt lgkmcnt(0)\n\t") \
  STP("s_or_b32 s90, s90, 1\n\t") \
  STP("v_writelane_b32 %[stv], s90, 48\n\t") \
  STP("v_writelane_b32 %[stv], s82, 49\n\t") \
  "s_cmp_eq_u32 s62, 6\n\t" \
  "s_cbranch_scc1 .Ltl_nf\n\t" \
  "v_dot2_f32_bf16 v96, v64, v218, 0\n\t" \
  "v_dot2_f32_bf16 v104, v254, v218, 0\n\t" \
  "v_dot2_f32_bf16 v97, v68, v218, 0\n\t" \
  "v_dot2_f32_bf16 v105, v180, v218, 0\n\t" \
  "v_dot2_f32_bf16 v98, v65, v218, 0\n\t" \
  "v_dot2_f32_bf16 v106, v176, v218, 0\n\t" \
  "v_dot2_f32_bf16 v99, v69, v218, 0\n\t" \
  "v_dot2_f32_bf16 v107, v181, v218, 0\n\t" \
  "v_dot2_f32_bf16 v100, v66, v218, 0\n\t" \
  "v_dot2_f32_bf16 v108, v177, v218, 0\n\t" \
  "v_dot2_f32_bf16 v101, v70, v218, 0\n\t" \
  "v_dot2_f32_bf16 v109, v182, v218, 0\n\t" \
  "v_dot2_f32_bf16 v102, v67, v218, 0\n\t" \
  "v_dot2_f32_bf16 v110, v178, v218, 0\n\t" \
  "v_dot2_f32_bf16 v103, v71, v218, 0\n\t" \
  "v_dot2_f32_bf16 v111, v183, v218, 0\n\t" \
  "v_dot2_f32_bf16 v96, v72, v220, v96\n\t" \
  "v_dot2_f32_bf16 v104, v179, v220, v104\n\t" \
  "v_dot2_f32_bf16 v97, v76, v220, v97\n\t" \
  "v_dot2_f32_bf16 v105, v188, v220, v105\n\t" \
  "v_dot2_f32_bf16 v98, v73, v220, v98\n\t" \
  "v_dot2_f32_bf16 v106, v184, v220, v106\n\t" \
  "v_dot2_f32_bf16 v99, v77, v220, v99\n\t" \
  "v_dot2_f32_bf16 v107, v189, v220, v107\n\t" \
  "v_dot2_f32_bf16 v100, v74, v220, v100\n\t" \
  "v_dot2_f32_bf16 v108, v185, v220, v108\n\t" \
  "v_dot2_f32_bf16 v101, v78, v220, v101\n\t" \
  "v_dot2_f32_bf16 v109, v190, v220, v109\n\t" \
  "v_dot2_f32_bf16 v102, v75, v220, v102\n\t" \
  "v_dot2_f32_bf16 v110, v186, v220, v110\n\t" \
  "v_dot2_f32_bf16 v103, v79, v220, v103\n\t" \
  "v_dot2_f32_bf16 v111, v191, v220, v111\n\t" \
  "v_dot2_f32_bf16 v96, v80, v222, v96\n\t" \
  "v_dot2_f32_bf16 v104, v187, v222, v104\n\t" \
  "v_dot2_f32_bf16 v97, v84, v222, v97\n\t" \
  "v_dot2_f32_bf16 v105, v196, v222, v105\n\t" \
  "v_dot2_f32_bf16 v98, v81, v222, v98\n\t" \
  "v_dot2_f32_bf16 v106, v192, v222, v106\n\t" \
  "v_dot2_f32_bf16 v99, v85, v222, v99\n\t" \
  "v_dot2_f32_bf16 v107, v197, v222, v107\n\t" \
  "v_dot2_f32_bf16 v100, v82, v222, v100\n\t" \
  "v_dot2_f32_bf16 v108, v193, v222, v108\n\t" \
  "v_dot2_f32_bf16 v101, v86, v222, v101\n\t" \
  "v_dot2_f32_bf16 v109, v198, v222, v109\n\t" \
  "v_dot2_f32_bf16 v102, v83, v222, v102\n\t" \
  "v_dot2_f32_bf16 v110, v194, v222, v110\n\t" \
  "v_dot2_f32_bf16 v103, v87, v222, v103\n\t" \
  "v_dot2_f32_bf16 v111, v199, v222, v111\n\t" \
  "v_dot2_f32_bf16 v96, v88, v224, v96\n\t" \
  "v_dot2_f32_bf16 v104, v195, v224, v104\n\t" \
  "v_dot2_f32_bf16 v97, v92, v224, v97\n\t" \
  "v_dot2_f32_bf16 v105, v204, v224, v105\n\t" \
  "v_dot2_f32_bf16 v98, v89, v224, v98\n\t" \
  "v_dot2_f32_bf16 v106, v200, v224, v106\n\t" \
  "v_dot2_f32_bf16 v99, v93, v224, v99\n\t" \
  "v_dot2_f32_bf16 v107, v205, v224, v107\n\t" \
  "v_dot2_f32_bf16 v100, v90, v224, v100\n\t" \
  "v_dot2_f32_bf16 v108, v201, v224, v108\n\t" \
  "v_dot2_f32_bf16 v101, v94, v224, v101\n\t" \
  "v_dot2_f32_bf16 v109, v206, v224, v109\n\t" \
  "v_dot2_f32_bf16 v102, v91, v224, v102\n\t" \
  "v_dot2_f32_bf16 v110, v202, v224, v110\n\t" \
  "v_dot2_f32_bf16 v103, v95, v224, v103\n\t" \
  "v_dot2_f32_bf16 v111, v207, v224, v111\n\t" \
  "v_permlane32_swap_b32 v96, v104\n\t" \
  "v_permlane32_swap_b32 v97, v105\n\t" \
  "v_permlane32_swap_b32 v98, v106\n\t" \
  "v_permlane32_swap_b32 v99, v107\n\t" \
  "v_permlane32_swap_b32 v100, v108\n\t" \
  "v_permlane32_swap_b32 v101, v109\n\t" \
  "v_permlane32_swap_b32 v102, v110\n\t" \
  "v_permlane32_swap_b32 v103, v111\n\t" \
  "v_pk_add_f32 v[96:97], v[96:97], v[104:105]\n\t" \
  "v_pk_add_f32 v[100:101], v[100:101], v[108:109]\n\t" \
  "v_pk_add_f32 v[98:99], v[98:99], v[106:107]\n\t" \
  "v_pk_add_f32 v[102:103], v[102:103], v[110:111]\n\t" \
  "v_permlane16_swap_b32 v96, v100\n\t" \
  "v_permlane16_swap_b32 v97, v101\n\t" \
  "v_permlane16_swap_b32 v98, v102\n\t" \
  "v_permlane16_swap_b32 v99, v103\n\t" \
  "v_add_f32 v96, v96, v100\n\t" \
  "v_add_f32 v97, v97, v101\n\t" \
  "v_add_f32 v98, v98, v102\n\t" \
  "v_add_f32 v99, v99, v103\n\t" \
  "v_add_f32_dpp v96, v96, v96 row_ror:8 row_mask:0xf bank_mask:0xf\n\t" \
  "v_add_f32_dpp v97, v97, v97 row_ror:8 row_mask:0xf bank_mask:0xf\n\t" \
  "v_add_f32_dpp v98, v98, v98 row_ror:8 row_mask:0xf bank_mask:0xf\n\t" \
  "v_add_f32_dpp v99, v99, v99 row_ror:8 row_mask:0xf bank_mask:0xf\n\t" \
  "v_add_f32_dpp v96, v96, v96 row_ror:4 row_mask:0xf bank_mask:0xf\n\t" \
  "v_add_f32_dpp v97, v97, v97 row_ror:4 row_mask:0xf bank_mask:0xf\n\t" \
  "v_add_f32_dpp v98, v98, v98 row_ror:4 row_mask:0xf bank_mask:0xf\n\t" \
  "v_add_f32_dpp v99, v99, v99 row_ror:4 row_mask:0xf bank_mask:0xf\n\t" \
  "v_add_f32_dpp v96, v96, v96 row_ror:2 row_mask:0xf bank_mask:0xf\n\t" \
  "v_add_f32_dpp v97, v97, v97 row_ror:2 row_mask:0xf bank_mask:0xf\n\t" \
  "v_add_f32_dpp v98, v98, v98 row_ror:2 row_mask:0xf bank_mask:0xf\n\t" \
  "v_add_f32_dpp v99, v99, v99 row_ror:2 row_mask:0xf bank_mask:0xf\n\t" \
  "ds_write_b128 v245, v[96:99] offset:8192\n\t" \
  "v_mov_b32 v169, v243\n\t" \
  "v_mov_b32 v171, v243\n\t" \
  "s_waitcnt lgkmcnt(0)\n\t" \
  "s_barrier\n\t" \
  "ds_read2st64_b32 v[96:97], v246 offset0:32 offset1:33\n\t" \
  "ds_read2st64_b32 v[98:99], v248 offset0:32 offset1:33\n\t" \
  "s_waitcnt lgkmcnt(0)\n\t" \
  "v_pk_add_f32 v[96:97], v[96:97], v[98:99]\n\t" \
  "s_nop 0\n\t" \
  "v_add_f32 v96, v96, v97\n\t" \
  "v_cvt_pk_bf16_f32 v96, v96, v96\n\t" \
  "v_lshlrev_b32 v97, 16, v96\n\t" \
  "v_lshlrev_b32 v96, 16, v96\n\t" \
  "s_nop 1\n\t" \
  "v_permlane16_swap_b32 v96, v97\n\t" \
  "v_mul_f32 v98, 0xbfb8aa3b, v96\n\t" \
  "v_exp_f32 v98, v98\n\t" \
  "s_nop 0\n\t" \
  "v_add_f32 v98, 1.0, v98\n\t" \
  "v_rcp_f32 v98, v98\n\t" \
  "s_nop 0\n\t" \
  "v_mul_f32 v96, v98, v96\n\t" \
  "v_cvt_pk_bf16_f32 v96, v96, v96\n\t" \
  "v_lshlrev_b32 v96, 16, v96\n\t" \
  "v_mul_f32 v96, v97, v96\n\t" \
  "s_nop 1\n\t" \
  "v_mov_b32_dpp v97, v96 quad_perm:[1,0,3,2] row_mask:0xf bank_mask:0xf\n\t" \
  "v_cvt_pk_bf16_f32 v96, v96, v97\n\t" \
  "s_nop 0\n\t" \
  "v_readlane_b32 s75, v96, 0\n\t" \
  "v_readlane_b32 s76, v96, 2\n\t" \
  "v_readlane_b32 s63, v96, 4\n\t" \
  "v_readlane_b32 s64, v96, 6\n\t" \
  "v_readlane_b32 s65, v96, 8\n\t" \
  "v_readlane_b32 s77, v96, 10\n\t" \
  "v_readlane_b32 s87, v96, 12\n\t" \
  "v_readlane_b32 s88, v96, 14\n\t" \
  "v_dot2_f32_bf16 v100, v240, s75, 0\n\t" \
  "v_dot2_f32_bf16 v101, v34, s75, 0\n\t" \
  "v_dot2_f32_bf16 v102, v32, s75, 0\n\t" \
  "v_dot2_f32_bf16 v103, v35, s75, 0\n\t" \
  "v_dot2_f32_bf16 v100, v33, s76, v100\n\t" \
  "v_dot2_f32_bf16 v101, v38, s76, v101\n\t" \
  "v_dot2_f32_bf16 v102, v36, s76, v102\n\t" \
  "v_dot2_f32_bf16 v103, v39, s76, v103\n\t" \
  "v_dot2_f32_bf16 v100, v37, s63, v100\n\t" \
  "v_dot2_f32_bf16 v101, v42, s63, v101\n\t" \
  "v_dot2_f32_bf16 v102, v40, s63, v102\n\t" \
  "v_dot2_f32_bf16 v103, v43, s63, v103\n\t" \
  "v_dot2_f32_bf16 v100, v41, s64, v100\n\t" \
  "v_dot2_f32_bf16 v101, v46, s64, v101\n\t" \
  "v_dot2_f32_bf16 v102, v44, s64, v102\n\t" \
  "v_dot2_f32_bf16 v103, v47, s64, v103\n\t" \
  "v_dot2_f32_bf16 v100, v45, s65, v100\n\t" \
  "v_dot2_f32_bf16 v101, v50, s65, v101\n\t" \
  "v_dot2_f32_bf16 v102, v48, s65, v102\n\t" \
  "v_dot2_f32_bf16 v103, v51, s65, v103\n\t" \
  "v_dot2_f32_bf16 v100, v49, s77, v100\n\t" \
  "v_dot2_f32_bf16 v101, v54, s77, v101\n\t" \
  "v_dot2_f32_bf16 v102, v52, s77, v102\n\t" \
  "v_dot2_f32_bf16 v103, v55, s77, v103\n\t" \
  "v_dot2_f32_bf16 v100, v53, s87, v100\n\t" \
  "v_dot2_f32_bf16 v101, v58, s87, v101\n\t" \
  "v_dot2_f32_bf16 v102, v56, s87, v102\n\t" \
  "v_dot2_f32_bf16 v103, v59, s87, v103\n\t" \
  "v_dot2_f32_bf16 v100, v57, s88, v100\n\t" \
  "v_dot2_f32_bf16 v101, v62, s88, v101\n\t" \
  "v_dot2_f32_bf16 v102, v60, s88, v102\n\t" \
  "v_dot2_f32_bf16 v103, v63, s88, v103\n\t" \
  "s_nop 0\n\t" \
  "v_cvt_pk_bf16_f32 v168, v100, v101\n\t" \
  "s_nop 0\n\t" \
  "v_cvt_pk_bf16_f32 v170, v102, v103\n\t" \
  "buffer_store_dwordx4 v[168:171], v247, %[ws], s85 offen sc1\n\t" \
  ".Ltl_nf:\n\t" \
  STP("s_memrealtime s[90:91]\n\t") \
  STP("s_memtime s[82:83]\n\t") \
  STP("s_waitcnt lgkmcnt(0)\n\t") \
  STP("s_or_b32 s90, s90, 1\n\t") \
  STP("v_writelane_b32 %[stv], s90, 50\n\t") \
  STP("v_writelane_b32 %[stv], s82, 51\n\t") \
  "s_cmp_lg_u32 s84, 0\n\t" \
  "s_cbranch_scc1 .Ltl_df\n\t" \
  "s_cmp_lg_u32 s78, 0\n\t" \
  "s_cbranch_scc1 .Ltl_o1\n\t" \
  "s_cmp_lt_u32 s74, 64\n\t" \
  "s_cbranch_scc1 .Ltl_o1\n\t" \
  ".Ltl_oe:\n\t" \
  "s_or_b32 s63, %[bid], %[wid]\n\t" \
  "s_cbranch_scc0 .Ltl_ep\n\t" \
  ".Ltl_ee:\n\t" \
  "s_cmp_eq_u32 s62, 6\n\t" \
  "s_cbranch_scc0 .Ltl_end\n\t" \
  "s_lshl_b32 s77, s79, 9\n\t" \
  "s_lshl_b32 s63, s80, 7\n\t" \
  "s_add_u32 s77, s77, s63\n\t" \
  "v_lshl_add_u32 v37, v157, 3, s77\n\t" \
  "s_add_u32 s63, s71, s61\n\t" \
  "s_add_u32 s63, s63, 262144\n\t" \
  "v_add_u32 v33, s63, v37\n\t" \
  "s_andn2_b32 s63, 0x700, s70\n\t" \
  "s_lshl_b32 s63, s63, 6\n\t" \
  "s_andn2_b32 s64, 0xc0, s70\n\t" \
  "s_lshl_b32 s64, s64, 5\n\t" \
  "s_add_u32 s63, s63, s64\n\t" \
  "s_add_u32 s63, s63, s61\n\t" \
  "s_add_u32 s63, s63, 1966080\n\t" \
  "v_and_b32 v34, 0x7ff, v37\n\t" \
  "v_add_u32 v34, s63, v34\n\t" \
  "s_and_b32 s77, s77, 0x7ff\n\t" \
  "s_add_u32 s77, s77, s61\n\t" \
  "s_lshl_b32 s63, %[wid], 15\n\t" \
  "s_add_u32 s77, s77, s63\n\t" \
  "v_and_b32 v36, 0x70, v242\n\t" \
  "v_lshl_add_u32 v32, v154, 14, v36\n\t" \
  "v_and_b32 v35, 24, v244\n\t" \
  "v_lshl_add_u32 v32, v35, 8, v32\n\t" \
  "s_add_u32 s60, s77, 1048576\n\t" \
  "s_add_u32 s65, s77, 1179648\n\t" \
  "s_add_u32 s66, s77, 1310720\n\t" \
  "s_add_u32 s67, s77, 1441792\n\t" \
  "s_add_u32 s68, s77, 1572864\n\t" \
  "s_add_u32 s73, s77, 1703936\n\t" \
  "s_add_u32 s74, s77, 1835008\n\t" \
  "s_lshl_b32 s63, s80, 12\n\t" \
  "s_add_u32 s63, s63, 147456\n\t" \
  "v_lshl_add_u32 v40, v157, 3, s63\n\t" \
  "ds_read_b64 v[40:41], v40 offset:2944\n\t" \
  "s_ashr_i32 s63, s70, 31\n\t" \
  "s_orn2_b32 s63, 0x80000000, s63\n\t" \
  "s_xor_b32 s63, s63, s70\n\t" \
  "s_and_b32 s63, s63, 0xffff0000\n\t" \
  "v_mov_b32 v38, 0xbfb8aa3b\n\t" \
  "v_mul_f32 v38, s63, v38\n\t" \
  "v_exp_f32 v38, v38\n\t" \
  "v_lshrrev_b32 v37, 1, v37\n\t" \
  "v_add_f32 v38, 1.0, v38\n\t" \
  "v_rcp_f32 v38, v38\n\t" \
  "s_andn2_b32 s64, 0x7ff, s70\n\t" \
  "s_lshl_b32 s64, s64, 11\n\t" \
  "v_cvt_pk_bf16_f32 v38, v38, v38\n\t" \
  "v_lshlrev_b32 v38, 16, v38\n\t" \
  "v_lshrrev_b32 v153, 4, v244\n\t" \
  "v_lshl_add_u32 v36, v153, 2, v36\n\t" \
  "v_lshl_add_u32 v36, %[wid], 7, v36\n\t" \
  "v_and_b32 v35, 8, v244\n\t" \
  "v_lshl_add_u32 v36, v35, 7, v36\n\t" \
  "v_lshlrev_b32 v35, 3, v157\n\t" \
  "v_add_u32 v35, 0x3000, v35\n\t" \
  "v_min_u32 v153, 1, v153\n\t" \
  "v_lshl_or_b32 v37, v153, 31, v37\n\t" \
  "s_mov_b32 s75, 0x3f80\n\t" \
  "s_mov_b32 s76, 0x3f800000\n\t" \
  "s_mov_b32 s72, 0\n\t" \
  "buffer_load_dwordx2 v[42:43], v33, %[ws], 0 offen sc1\n\t" \
  "buffer_load_dwordx2 v[44:45], v34, %[ws], 0 offen sc1\n\t" \
  "buffer_load_dwordx4 v[0:3], v32, %[ws], s60 offen sc1\n\t" \
  "buffer_load_dwordx4 v[4:7], v32, %[ws], s65 offen sc1\n\t" \
  "buffer_load_dwordx4 v[8:11], v32, %[ws], s66 offen sc1\n\t" \
  "buffer_load_dwordx4 v[12:15], v32, %[ws], s67 offen sc1\n\t" \
  "buffer_load_dwordx4 v[16:19], v32, %[ws], s68 offen sc1\n\t" \
  "buffer_load_dwordx4 v[20:23], v32, %[ws], s73 offen sc1\n\t" \
  "buffer_load_dwordx4 v[24:27], v32, %[ws], s74 offen sc1\n\t" \
  "s_waitcnt vmcnt(0)\n\t" \
  ".Ltl_c2:\n\t" \
  "v_xor_b32 v152, %[tag], v43\n\t" \
  "v_bitop3_b32 v152, v152, v45, %[tag] bitop3:0xf6\n\t" \
  "v_bitop3_b32 v152, v152, v1, %[tag] bitop3:0xf6\n\t" \
  "v_bitop3_b32 v152, v152, v3, %[tag] bitop3:0xf6\n\t" \
  "v_bitop3_b32 v152, v152, v5, %[tag] bitop3:0xf6\n\t" \
  "v_bitop3_b32 v152, v152, v7, %[tag] bitop3:0xf6\n\t" \
  "v_bitop3_b32 v152, v152, v9, %[tag] bitop3:0xf6\n\t" \
  "v_bitop3_b32 v152, v152, v11, %[tag] bitop3:0xf6\n\t" \
  "v_bitop3_b32 v152, v152, v13, %[tag] bitop3:0xf6\n\t" \
  "v_bitop3_b32 v152, v152, v15, %[tag] bitop3:0xf6\n\t" \
  "v_bitop3_b32 v152, v152, v17, %[tag] bitop3:0xf6\n\t" \
  "v_bitop3_b32 v152, v152, v19, %[tag] bitop3:0xf6\n\t" \
  "v_bitop3_b32 v152, v152, v21, %[tag] bitop3:0xf6\n\t" \
  "v_bitop3_b32 v152, v152, v23, %[tag] bitop3:0xf6\n\t" \
  "v_bitop3_b32 v152, v152, v25, %[tag] bitop3:0xf6\n\t" \
  "v_bitop3_b32 v152, v152, v27, %[tag] bitop3:0xf6\n\t" \
  "v_cmp_ne_u32 vcc, 0, v152\n\t" \
  "s_cbranch_vccnz .Ltl_s2\n\t" \
  ".Ltl_k2:\n\t" \
  "v_dot2_f32_bf16 v48, v0, s75, 0\n\t" \
  "v_dot2_f32_bf16 v49, v0, s76, 0\n\t" \
  "v_dot2_f32_bf16 v50, v2, s75, 0\n\t" \
  "v_dot2_f32_bf16 v51, v2, s76, 0\n\t" \
  "v_dot2_f32_bf16 v48, v4, s75, v48\n\t" \
  "v_dot2_f32_bf16 v49, v4, s76, v49\n\t" \
  "v_dot2_f32_bf16 v50, v6, s75, v50\n\t" \
  "v_dot2_f32_bf16 v51, v6, s76, v51\n\t" \
  "v_dot2_f32_bf16 v48, v8, s75, v48\n\t" \
  "v_dot2_f32_bf16 v49, v8, s76, v49\n\t" \
  "v_dot2_f32_bf16 v50, v10, s75, v50\n\t" \
  "v_dot2_f32_bf16 v51, v10, s76, v51\n\t" \
  "v_dot2_f32_bf16 v48, v12, s75, v48\n\t" \
  "v_dot2_f32_bf16 v49, v12, s76, v49\n\t" \
  "v_dot2_f32_bf16 v50, v14, s75, v50\n\t" \
  "v_dot2_f32_bf16 v51, v14, s76, v51\n\t" \
  "v_dot2_f32_bf16 v48, v16, s75, v48\n\t" \
  "v_dot2_f32_bf16 v49, v16, s76, v49\n\t" \
  "v_dot2_f32_bf16 v50, v18, s75, v50\n\t" \
  "v_dot2_f32_bf16 v51, v18, s76, v51\n\t" \
  "v_dot2_f32_bf16 v48, v20, s75, v48\n\t" \
  "v_dot2_f32_bf16 v49, v20, s76, v49\n\t" \
  "v_dot2_f32_bf16 v50, v22, s75, v50\n\t" \
  "v_dot2_f32_bf16 v51, v22, s76, v51\n\t" \
  "v_dot2_f32_bf16 v48, v24, s75, v48\n\t" \
  "v_dot2_f32_bf16 v49, v24, s76, v49\n\t" \
  "v_dot2_f32_bf16 v50, v26, s75, v50\n\t" \
  "v_dot2_f32_bf16 v51, v26, s76, v51\n\t" \
  "s_nop 1\n\t" \
  "v_permlane32_swap_b32 v48, v50\n\t" \
  "v_permlane32_swap_b32 v49, v51\n\t" \
  "v_add_f32 v48, v48, v50\n\t" \
  "v_add_f32 v49, v49, v51\n\t" \
  "v_and_b32 v43, 0xffff0000, v42\n\t" \
  "v_lshlrev_b32 v42, 16, v42\n\t" \
  "v_permlane16_swap_b32 v48, v49\n\t" \
  "v_add_f32 v48, v48, v49\n\t" \
  "v_and_b32 v45, 0xffff0000, v44\n\t" \
  "v_lshlrev_b32 v44, 16, v44\n\t" \
  "v_add_f32_dpp v48, v48, v48 row_ror:8 row_mask:0xf bank_mask:0xf\n\t" \
  "ds_write_b32 v36, v48 offset:12288\n\t" \
  "s_cmp_lg_u32 %[wid], 0\n\t" \
  "s_waitcnt lgkmcnt(0)\n\t" \
  "s_barrier\n\t" \
  "s_cbranch_scc1 .Ltl_end\n\t" \
  "ds_read2_b64 v[0:3], v35 offset1:16\n\t" \
  "ds_read2_b64 v[4:7], v35 offset0:32 offset1:48\n\t" \
  "s_waitcnt lgkmcnt(1)\n\t" \
  "v_pk_add_f32 v[0:1], v[0:1], v[2:3]\n\t" \
  "s_waitcnt lgkmcnt(0)\n\t" \
  "v_pk_add_f32 v[4:5], v[4:5], v[6:7]\n\t" \
  "s_nop 0\n\t" \
  "v_pk_add_f32 v[0:1], v[0:1], v[4:5]\n\t" \
  "s_nop 0\n\t" \
  "v_cvt_pk_bf16_f32 v0, v0, v1\n\t" \
  "v_and_b32 v1, 0xffff0000, v0\n\t" \
  "v_lshlrev_b32 v0, 16, v0\n\t" \
  "v_mul_f32 v0, v38, v0\n\t" \
  "v_mul_f32 v1, v38, v1\n\t" \
  "v_cvt_pk_bf16_f32 v0, v0, v1\n\t" \
  "v_and_b32 v1, 0xffff0000, v0\n\t" \
  "v_lshlrev_b32 v0, 16, v0\n\t" \
  "v_add_f32 v0, v0, v42\n\t" \
  "v_add_f32 v1, v1, v43\n\t" \
  "v_cvt_pk_bf16_f32 v0, v0, v1\n\t" \
  "v_and_b32 v1, 0xffff0000, v0\n\t" \
  "v_lshlrev_b32 v0, 16, v0\n\t" \
  "v_mul_f32 v0, v0, v40\n\t" \
  "v_mul_f32 v1, v1, v41\n\t" \
  "v_cvt_pk_bf16_f32 v0, v0, v1\n\t" \
  "v_and_b32 v1, 0xffff0000, v0\n\t" \
  "v_lshlrev_b32 v0, 16, v0\n\t" \
  "v_add_f32 v0, v0, v44\n\t" \
  "v_add_f32 v1, v1, v45\n\t" \
  "v_cvt_pk_bf16_f32 v0, v0, v1\n\t" \
  "buffer_store_dword v0, v37, s[96:99], s64 offen sc0 sc1\n\t" \
  "s_branch .Ltl_end\n\t" \
  ".Ltl_s1:\n\t" \
  "s_sleep 1\n\t" \
  "buffer_load_dwordx2 v[216:217], v235, %[ws], 0 offen sc1\n\t" \
  "buffer_load_dwordx4 v[218:221], v236, %[ws], s71 offen sc1\n\t" \
  "buffer_load_dwordx4 v[222:225], v236, %[ws], s71 offen offset:16 sc1\n\t" \
  "s_add_u32 s72, s72, 1\n\t" \
  "s_cmp_lt_u32 s72, %[cap]\n\t" \
  "s_waitcnt vmcnt(0)\n\t" \
  "s_cbranch_scc1 .Ltl_c1\n\t" \
  "buffer_store_dword v243, off, %[ws], 0 offset:256 sc1\n\t" \
  "s_branch .Ltl_k1\n\t" \
  ".Ltl_o1:\n\t" \
  "s_cmp_eq_u32 s70, s69\n\t" \
  "s_cselect_b32 s63, 0x80000000, 0\n\t" \
  "s_xor_b32 s77, s78, 0x80000000\n\t" \
  "v_or_b32 v152, s63, v238\n\t" \
  "v_or_b32 v153, s77, v238\n\t" \
  "v_cmp_gt_i32 vcc, 0, v228\n\t" \
  "s_mov_b32 s64, 0\n\t" \
  "s_nop 0\n\t" \
  "v_cndmask_b32 v228, v153, v152, vcc\n\t" \
  "buffer_store_dwordx4 v[112:115], v228, s[96:99], s64 offen sc1\n\t" \
  "v_cmp_gt_i32 vcc, 0, v229\n\t" \
  "s_mov_b32 s64, 32768\n\t" \
  "s_nop 0\n\t" \
  "v_cndmask_b32 v229, v153, v152, vcc\n\t" \
  "buffer_store_dwordx4 v[116:119], v229, s[96:99], s64 offen sc1\n\t" \
  "v_cmp_gt_i32 vcc, 0, v230\n\t" \
  "s_mov_b32 s64, 65536\n\t" \
  "s_nop 0\n\t" \
  "v_cndmask_b32 v230, v153, v152, vcc\n\t" \
  "buffer_store_dwordx4 v[120:123], v230, s[96:99], s64 offen sc1\n\t" \
  "v_cmp_gt_i32 vcc, 0, v231\n\t" \
  "s_mov_b32 s64, 98304\n\t" \
  "s_nop 0\n\t" \
  "v_cndmask_b32 v231, v153, v152, vcc\n\t" \
  "buffer_store_dwordx4 v[124:127], v231, s[96:99], s64 offen sc1\n\t" \
  "s_branch .Ltl_oe\n\t" \
  ".Ltl_df:\n\t" \
  "s_mov_b32 s78, 0\n\t" \
  "s_cmp_eq_u32 s70, s69\n\t" \
  "s_cselect_b32 s74, s74, 0x100\n\t" \
  "s_branch .Ltl_h2\n\t" \
  ".Ltl_ep:\n\t" \
  "s_mov_b32 s63, 2490368\n\t" \
  "buffer_store_dword v243, v249, %[ws], s63 offen sc1\n\t" \
  "s_branch .Ltl_ee\n\t" \
  ".Ltl_s2:\n\t" \
  "s_sleep 1\n\t" \
  "buffer_load_dwordx2 v[42:43], v33, %[ws], 0 offen sc1\n\t" \
  "buffer_load_dwordx2 v[44:45], v34, %[ws], 0 offen sc1\n\t" \
  "buffer_load_dwordx4 v[0:3], v32, %[ws], s60 offen sc1\n\t" \
  "buffer_load_dwordx4 v[4:7], v32, %[ws], s65 offen sc1\n\t" \
  "buffer_load_dwordx4 v[8:11], v32, %[ws], s66 offen sc1\n\t" \
  "buffer_load_dwordx4 v[12:15], v32, %[ws], s67 offen sc1\n\t" \
  "buffer_load_dwordx4 v[16:19], v32, %[ws], s68 offen sc1\n\t" \
  "buffer_load_dwordx4 v[20:23], v32, %[ws], s73 offen sc1\n\t" \
  "buffer_load_dwordx4 v[24:27], v32, %[ws], s74 offen sc1\n\t" \
  "s_add_u32 s72, s72, 1\n\t" \
  "s_cmp_lt_u32 s72, %[cap]\n\t" \
  "s_waitcnt vmcnt(0)\n\t" \
  "s_cbranch_scc1 .Ltl_c2\n\t" \
  "buffer_store_dword v243, off, %[ws], 0 offset:256 sc1\n\t" \
  "s_branch .Ltl_k2\n\t" \
  ".Ltl_end:\n\t" \
  ""
#define TL_CLOB "v208", "v209", "v210", "v211", "v212", "v213", "v214", "v215", "v216", "v217", "v218", "v219", "v220", "v221", "v222", "v223", "v224", "v225", "v226", "v227", "v228", "v229", "v230", "v231", "v232", "v233", "v234", "v235", "v236", "v238", "v239", "v245", "v246", "v247", "v248", "v249", "v250", "s60", "s61", "s62", "s63", "s64", "s65", "s66", "s67", "s68", "s69", "s70", "s71", "s72", "s73", "s74", "s75", "s76", "s77", "s78", "s79", "s80", "s81", "s82", "s83", "s84", "s85", "s86", "s90", "s91", "s92", "s93", "s94", "s96", "s97", "s98", "s99", "vcc", "scc", "memory"
static_assert(W_EPOCH == 2490368 && W_GKEY == 65536 && W_CANDH == 262144 && W_STAT == 524288 && W_YG == 1048576 && W_CANDX == 1966080);
struct WoP { srd_t srd; unsigned wv[2], m, hl, base, wa; };
struct W4C {
  KVP kv;
  unsigned pf1, pf2, pf4, cto, po, px, vo, eso, hl, hl18, frb, xcb, xso, ss, mbp, gvo, l16, gvm, vs, c3;
  WoP wo; u16* out;
  srd_t sG, sU, sD, sX, sR, sM;
  unsigned gv, wa0, fw2, tagv, fbv, tlb, pre, otb, na, gra, fl; u32x32 q[4], up; f32x32 wm; u32x16 o3;
};
struct Ptrs { const u16 *wo, *w_r, *wg, *wu, *wd; u16* out; };
DI void attn_setup(const Ctx& k, W4C& c, const u16* kc, const u16* vc, const u16* pm, srd_t srdx, const Ptrs& a) {
  const unsigned wid = k.wid, fb = k.xcd * 28 + ffn_j(k);
  c.kv.sofk = KRS * 16u * wid;
  const size_t kvoff = ((size_t)k.kvh * SKV + (SKV - WIN)) * DH;
  c.kv.srdK = mk_srd(kc + kvoff, WIN * DH * 2); c.kv.srdV = mk_srd(vc + kvoff, WIN * DH * 2);
  const unsigned go = wid * 256 * FF + fb * 16, nG = (DM * FF - go) * 2;
  c.sG = mk_srd(a.wg + go, nG); c.sU = mk_srd(a.wu + go, nG);
  c.sM = mk_srd(pm + 2048 + k.xcd * 512, DM * 6 * DM * 2 - 4096 - k.xcd * 1024);
  c.wo.srd = mk_srd(a.wo, QD * DM * 2); c.sX = srdx;
  c.sD = mk_srd(a.wd + (size_t)fb * 16 * DM, ffn_idle(k) ? 0u : 32768u); c.sR = mk_srd(a.w_r, DM * 2);
  c.out = a.out;
  asm volatile("" : "+s"(c.out), "+s"(c.kv.srdK), "+s"(c.kv.srdV), "+s"(c.sG), "+s"(c.sU), "+s"(c.sM), "+s"(c.wo.srd), "+s"(c.sD), "+s"(c.sR));
  c.wo.m = wid * 4096; c.wo.base = (k.kvh * 1024 + k.hl * 128 + wid * 16) * (DM * 2) + k.hl * 256;
  c.pf1 = (k.kvh * 1024 + k.j * 32 + wid * 8) * (DM * 2) + (k.xcd & 1) * 8192;
  c.pf4 = k.j * (8 * FF * 2) - ffn_j(k) * 32;
}
DI void attention(Ctx& k, H4& g, W4C& c STAMP_ARG) {
  unsigned nb;
  asm volatile(AT_ASM
               : "+{v[0:31]}"(g.q[0]), "+{v[32:63]}"(g.q[1]), "+{v[64:95]}"(g.q[2]), "+{v[96:127]}"(g.q[3]), [pfd] "+{v246}"(g.pfd), "=&{v[128:159]}"(c.wm), "=&{v[160:175]}"(c.o3), "+{v224}"(c.kv.vksrc[0]), "+{v225}"(c.kv.vksrc[1]), "+{v226}"(c.kv.vksrc[2]), "+{v227}"(c.kv.vksrc[3]), "=&{v[176:207]}"(c.up),
                 "=&{v234}"(c.wo.wa), "=&{v213}"(c.gra), "+{v245}"(g.fvo), "=&{v243}"(c.tagv), "=&{v254}"(c.fbv),
                 "=&{v242}"(c.l16), "+{v247}"(c.tlb), "=&{v252}"(c.vs), [na] "=&s"(c.na), [nb] "=&s"(nb), [eso] "+s"(c.eso), [xcb] "+s"(c.xcb), [frb] "+s"(c.frb), [fw2] "+s"(c.fw2) AT_STV
               : "{v220}"(c.kv.vkb[0]), "{v221}"(c.kv.vkb[1]), "{v222}"(c.kv.vkb[2]), "{v223}"(c.kv.vkb[3]),
                 "{v228}"(c.kv.vvb[0]), "{v229}"(c.kv.vvb[1]), "{v230}"(c.kv.vvsrc[0]), "{v231}"(c.kv.vvsrc[1]), "{v232}"(c.gv), "{v233}"(c.gvm), "{v240}"(c.wo.wv[0]), "{v241}"(c.wo.wv[1]), "{v244}"(c.wa0), "{v248}"(c.c3),
                 [ws] "s"(k.ws), [sK] "s"(c.kv.srdK), [sV] "s"(c.kv.srdV), [sG] "s"(c.sG), [sU] "s"(c.sU), [wo] "s"(c.wo.srd), [sM] "s"(c.sM), [sD] "s"(c.sD), [sR] "s"(c.sR),
                 [m] "s"(c.wo.m), [sofk] "s"(c.wo.m), [hl18] "s"(c.hl18), [wob] "s"(c.wo.base), [pf2] "s"(c.pf2), AT_PF1 [pf4] "s"(c.pf4),
                 [tag] "s"(k.tag), [cap] "i"(kSpinCap), [cto] "s"(c.cto), [po] "s"(c.po), [px] "s"(c.px), [vo] "s"(c.vo), [pm] "s"(k.pm), [ss] "s"(c.ss), [gvo] "s"(c.gvo)
               : AT_CLOB, ACLOB_LO, ACLOB_HI);
  k.nb = nb; c.fl = g.fvo;
#pragma unroll
  for (int i = 0; i < 4; ++i) c.q[i] = g.q[i];
  GAP();
}
constexpr unsigned SCR = 9 * UNIT;
#define STAMPF(I) do { } while (0)
#define FQ_STR2(x) #x
#define FQ_STR(x) FQ_STR2(x)
#define FQ_CAP FQ_STR(SPINCAP)
DI bool tags2(const u32x4& a, unsigned tag) { return (a[1] == tag) & (a[3] == tag); }
DI void bcast4(float v, float (&o)[4]) {
  auto a = __builtin_amdgcn_permlane32_swap(__float_as_uint(v), __float_as_uint(v), false, false);
  auto b = __builtin_amdgcn_permlane16_swap(a[0], a[0], false, false);
  auto c = __builtin_amdgcn_permlane16_swap(a[1], a[1], false, false);
  o[0] = __uint_as_float(b[0]); o[1] = __uint_as_float(b[1]); o[2] = __uint_as_float(c[0]); o[3] = __uint_as_float(c[1]);
}
DI void x_raw2(unsigned m0, srd_t srd, unsigned v0, unsigned v1, unsigned so) {
  asm volatile("s_mov_b32 m0, %0\n\ts_nop 0\n\tbuffer_load_dwordx4 %1, %3, %4 offen lds\n\tbuffer_load_dwordx4 %2, %3, %4 offen offset:1024 lds"
               :: "s"(m0), "v"(v0), "v"(v1), "s"(srd), "s"(so) : "memory");
}
DI void w_touch2(H4& g, const u16* wq, unsigned base, unsigned t) {
  touch(g.pfd, wq, base + ((g.rot + t) & 15u) * kWT);
}
constexpr unsigned TB0 = 6, TBN = 6;
#define SE 0
#define F1 0
#define FT 0
#define MK 0
#define PGA 0
#define WT 0
#define LT 0
#define EL 0
#define PT0 0
#define XH 0
#define S3 0
#define X1L 0
#define PBK 1
#define IL 1
#define GE 0
#define PPOS 2
DI void front(Ctx& k, H4& g, W4C& c STAMP_ARG) {
  argp_t ap = args();
  const u16 *px = ap->x, *kc = ap->kc, *vc = ap->vc, *wqp = ap->wq, *pc_ = ap->c, *pm = ap->w_mod; char* pw = ap->ws;
  GAP();
  const unsigned lane = lane_id(), wid = k.wid, tid = wid * 64 + lane;
  g.pfd = 0;
  {
    unsigned long long pc; unsigned len;
    asm volatile("s_getpc_b64 %0\n.Lfz_a:\n\ts_mov_b32 %1, .Lfz_z-.Lfz_a-4" : "=s"(pc), "=s"(len));
    touch(g.pfd, (const void*)pc, min(((lane * 4 + wid) * 32 + k.j) * 128, len));
  }
  GAP();
  k.ws = mk_srd((char*)(((uintptr_t)pw + 0x1fffff) & ~(uintptr_t)0x1fffff), W_END);
  const srd_t srdx = mk_srd(px, S * DM * 2), srdm = mk_srd(pm, DM * 6 * DM * 2), srdc = mk_srd(pc_, DM * 2);
  asm volatile("" : "+s"(kc), "+s"(vc));
  const unsigned head = k.kvh * 8 + k.hl, row64 = k.rblk * 256 + k.kvh * 64, rot = k.kvh * 4, u0 = k.hl >> 1, kt = rot + ((u0 + wid) & 3), k0 = k.hl & 1, cq = k.kvh ^ 3;
  g.srdW = mk_srd(wqp + head * DH, (DM * QD - head * DH) * 2); g.rot = rot;
  u32x32 xa;
  const unsigned st1 = W_ST1 + (k.rblk << 12);
  u32x4 xs[4];
  const unsigned cbk = (k.xcd & 1) * 32 + k.j, kp = k.kvh;
  const unsigned s3 = S3 && (k.j & 14) == 6, od = s3 & k.j, rg = tid >> (2 + s3), cm = 3 + 4 * s3;
  u32x4 wm[4];
  u32x2 cv = ald8<0>(srdc, rg * 8, kp * 512 + od * 256);
  { const unsigned vo = rg * (4 * 6 * DM * 2) + (tid & cm) * 16, so = kp * (256 * 6 * DM * 2) + cbk * 64 + od * (128 * 6 * DM * 2 - 64);
#pragma unroll
    for (int i = 0; i < 4; ++i) wm[i] = ald16<0>(srdm, vo, so + i * (6 * DM * 2)); }
  unsigned E = ald4<0, true>(k.ws, EL == 0 || lane == 0 ? W_EPOCH : lane == 1 ? W_HT + ((k.rblk & 3) << 20) : lane == 2 ? st1 + (k.kvh * 8 + k.hl) * 128 : W_FLAGH + (k.rblk * 4 + wid) * 128, 0u);
  { const unsigned so = (row64 + k.hl * 8 + 2 * wid) * (DM * 2);
    xs[0] = ald16<0>(srdx, lane * 16, so); xs[1] = ald16<1024>(srdx, lane * 16, so); xs[2] = ald16<2048>(srdx, lane * 16, so); xs[3] = ald16<3072>(srdx, lane * 16, so); }
  unsigned vx = (lane >> 3) * (DM * 2) + (((lane & 7) ^ (lane >> 4)) << 4), vx1 = (vx ^ 64) + 15360, sl[4];
  g.mX = k.ldsb + wid * 8192;
#pragma unroll
  for (int u = 0; u < 4; ++u) sl[u] = (k.rblk * 256 + wid * 16) * (DM * 2) + (rot + ((u0 + u) & 3)) * 128;
#define XRAW(u, rb) x_raw2(g.mX + (u) * GSLOT + (rb) * 2048, srdx, vx, vx1, sl[u] + (((rb) ^ cq) << 17))
  const unsigned hx = XH ? 2 * k0 : 0;
  if (!(LT & 2)) { XRAW(0, hx); XRAW(0, hx + 1); }
  const unsigned tw2 = (k.j * 2 + (wid >> 1)) * (QD * 2) + k.kvh * 2048 + (lane & 7) * 256 + (wid & 1) * 128;
  if (!(LT & 1)) w_touch2(g, wqp, tw2, (lane >> 3) & 3);
  constexpr int NL = (LT & 1) + 2 * (LT & 2);
  GAP();
  g.mW = k.ldsb + 32768 + wid * 4096;
#define X1() do { XRAW(1, 0); XRAW(1, 1); XRAW(1, 2); XRAW(1, 3); } while (0)
#define X01() do { XRAW(0, 2 - hx); XRAW(0, 3 - hx); if (!X1L) X1(); } while (0)
#define X2() do { XRAW(2, 0); XRAW(2, 1); XRAW(2, 2); XRAW(2, 3); } while (0)
  const unsigned tb = k.ldsb + SCR + 1024;
  u32x16 wbs; u32x8 vw, ma; u32x4 mb; const u16* kvt; unsigned pso, fw, w2, w3, gso, gpo;
  auto setup_w = [&]() {
    w_voffs(g.voffW, QD, wid, lane, k0);
    gso = st1 + (wid * 16 + (lane & 15)) * 16; gpo = W_MODA + ((lane >> 5) * 1024 + kt * 64 + (lane & 31) * 2) * 8;
    asm volatile("" : W4(g.voffW), "+v"(gso), "+v"(gpo));
    GAP();
  };
  auto setup_b = [&]() {
    g.voffX = wid * 8192 + ((lane >> 4) ^ cq) * 256 + (lane & 15) * 16; g.soX = W_HT + ((k.rblk & 3) << 20) + (((k.xcd ^ wid) & 1) << 13) + wid * 8192; g.fo = W_FLAGH + (k.rblk * 4 + wid) * 128;
    h4_init_addr(g, k.ldsb, wid, lane, k0);
    const unsigned xr = g.mX + (lane & 15) * 128 + (((4 * k0 + (lane >> 4)) ^ ((lane >> 1) & 7)) << 4);
    vw = u32x8{g.voffW[0], g.voffW[1], g.voffW[2], g.voffW[3], xr, xr ^ 64, xr + 2 * GSLOT, (xr ^ 64) + 2 * GSLOT};
#pragma unroll
    for (int i = 0; i < 8; ++i) { wbs[i] = g.wb[0][i]; wbs[8 + i] = g.wb[1][i]; }
    ma = u32x8{tb + (lane >> 4) * 32, g.voffX, (lane & 31) * 4, 0u, vx, vx1, g.xb1,
               WT == 1 ? tw2 + ((rot + 4 + (k.xcd & 1) + 2 * min(lane >> 3, 5u)) & 15u) * kWT : WT == 2 ? tw2 + ((rot + 4 + (k.xcd & 1) + 2 * min(lane >> 3, 2u)) & 15u) * kWT
                       : tw2 - (lane & 7) * 256 + (1 + 4 * (lane & 1)) * 256 + ((rot + 4 + min(lane >> 1, 11u)) & 15u) * kWT};
    mb = u32x4{WT == 2 ? tw2 + ((rot + 10 + (k.xcd & 1) + 2 * min(lane >> 3, 2u)) & 15u) * kWT : (tw2 & ~(((lane & 3) == 1) ? 256u : 0u)) + ((rot + TB0 + min(lane >> 3, TBN - 1u)) & 15u) * kWT, ((k.j & 15) * 64 + wid * 16 + (lane & 15)) * 128, 0u, W_SIL + k.bid * 512 + tid * 2};
    kvt = ((k.j & 16) ? vc : kc) + ((size_t)k.kvh * SKV + (SKV - WIN)) * DH;
    pso = g.soX + (rot + u0) * kXT + cq * 256 - GSLOT - k.ldsb; fw = g.fo + head * 4; w2 = (rot + ((u0 + 2) & 3)) * kWT; w3 = (rot + ((u0 + 3) & 3)) * kWT;
    asm volatile("" : "+{v[184:199]}"(wbs), "+{v[200:207]}"(vw), "+{v[208:215]}"(ma), "+{v[216:219]}"(mb), "+s"(kvt), "+s"(pso), "+s"(fw), "+s"(w2), "+s"(w3));
    GAP();
  };
  if constexpr (SE & 2) { setup_w(); setup_b(); }
  unsigned tag;
  HOP_GO(2, wid);
  unsigned la = k.ldsb + SCR + wid * 256 + (4 * (lane & 7) + 2 * (lane >> 5) + ((lane >> 4) & 1)) * 8 + (lane & 8) * 512, lr = k.ldsb + SCR + lane * 8;
  unsigned pa_ = (W_MODA + od * (W_MODC - W_MODA - 256) + (kp * 2048 + cbk * 32 + lane * 2) * 8) | (lane < (16u << s3) ? 0u : 0x80000000u);
  unsigned ps_ = (st1 + (k.kvh * 64 + k.hl * 8 + 2 * wid + (lane >> 5)) * 16) | ((lane & 31) == 0 ? 0u : 0x80000000u);
  asm volatile("" : "+v"(la), "+v"(lr), "+v"(pa_), "+v"(ps_));
  GAP();
  asm volatile("s_waitcnt vmcnt(%1)" : "+v"(cv) : "i"(14 + PT0 - NL) : "memory");
  unsigned sc[2];
#pragma unroll
  for (int i = 0; i < 2; ++i) { const float a = lo16f(cv[i]), b = hi16f(cv[i]); sc[i] = cvtpk(a * __builtin_amdgcn_rcpf(1.f + __expf(-a)), b * __builtin_amdgcn_rcpf(1.f + __expf(-b))); }
  asm volatile("" : "+v"(sc[0]), "+v"(sc[1]));
  GAP();
  STAMPF(27);
  asm volatile("s_waitcnt vmcnt(%4)" : W4(wm) : "i"(10 + PT0 - NL) : "memory");
  STAMP(1);
  if (LT & 2) { XRAW(0, 0); XRAW(0, 1); }
  if (LT & 1) w_touch2(g, wqp, tw2, (lane >> 3) & 3);
  GAP();
  {
    float a[8];
#pragma unroll
    for (int d = 0; d < 4; ++d) {
      a[2 * d] = dot2(plo(wm[2][d], wm[3][d]), sc[1], dot2z(plo(wm[0][d], wm[1][d]), sc[0]));
      a[2 * d + 1] = dot2(phi(wm[2][d], wm[3][d]), sc[1], dot2z(phi(wm[0][d], wm[1][d]), sc[0]));
    }
    float c0 = fold16(fold32(a[0], a[4]), fold32(a[2], a[6])), c1 = fold16(fold32(a[1], a[5]), fold32(a[3], a[7]));
    c0 += dpp_f<0x128>(c0); c1 += dpp_f<0x128>(c1);
    if (S3) { const float kf = s3 ? 0.f : 1.f; c0 = fmaf(dpp_f<0x124>(c0), kf, c0); c1 = fmaf(dpp_f<0x124>(c1), kf, c1); }
    else    { c0 += dpp_f<0x124>(c0); c1 += dpp_f<0x124>(c1); }
    lds_st(la, f32x2{c0, c1});
    asm volatile("s_waitcnt vmcnt(%1)" : "+v"(E) : "i"(9 + PT0) : "memory");
    tag = ((rfl(E) | TAGM) + TAGC) & ~TAGM;
    k.tag = tag;
    unsigned tv = tag;
    asm volatile("s_waitcnt lgkmcnt(0)\n\ts_barrier" : "+v"(tv) :: "memory");
    if (wid == 0) {
      const f32x2 s = (lds_ld<f32x2>(lr) + lds_ld<f32x2>(lr + 256)) + (lds_ld<f32x2>(lr + 512) + lds_ld<f32x2>(lr + 768));
      st16(k.ws, pa_, u32x4{__float_as_uint(s[0]), tv, __float_as_uint(s[1]), tv});
    }
  }
  GAP();
  asm volatile("s_waitcnt vmcnt(%5)\n\ts_cmp_eq_u32 %4, 0\n\ts_cbranch_scc1 .Lxs%=\n\ts_waitcnt vmcnt(%6)\n.Lxs%=:" : W4(xs) : "s"(wid), "i"(6 + PT0), "i"(5 + PT0) : "memory", "scc");
  STAMP(2);
  {
    float sm[2], sq[2];
#pragma unroll
    for (int r = 0; r < 2; ++r) { sm[r] = dot2z(xs[2 * r][0], 0x3f803f80u); sq[r] = dot2z(xs[2 * r][0], xs[2 * r][0]); }
#pragma unroll
    for (int e = 1; e < 8; ++e)
#pragma unroll
      for (int r = 0; r < 2; ++r) { const unsigned w = xs[2 * r + (e >> 2)][e & 3]; sm[r] = dot2(w, 0x3f803f80u, sm[r]); sq[r] = dot2(w, w, sq[r]); }
    const float z = sum_row16(fold16(fold32(sm[0], sm[1]), fold32(sq[0], sq[1]))) * (1.f / DM);
    auto zz = __builtin_amdgcn_permlane16_swap(__float_as_uint(z), __float_as_uint(z), false, false);
    const float mean = __uint_as_float(zz[0]);
    const float rstd = __builtin_amdgcn_rsqf(fmaxf(fmaf(-mean, mean, __uint_as_float(zz[1])), 0.f) + 1e-5f);
    st16(k.ws, ps_, u32x4{__float_as_uint(rstd), tag, __float_as_uint(-mean * rstd), tag});
  }
  GAP();
  STAMP(3);
  Ptrs pa;
  { argp_t aq = args(); pa = Ptrs{aq->wo, aq->w_r, aq->wg, aq->wu, aq->wd, aq->out}; }
  if (IL) { XRAW(0, 2 - hx); XRAW(0, 3 - hx); if (IL & 2) XRAW(1, 0); GAP(); } else X01();
  unsigned cb;
  asm volatile("buffer_load_ushort %0, %1, %2, %3 offen" : "=v"(cb) : "v"(tid * 2), "s"(srdc), "s"((k.j >> 3) * 512) : "memory");
  if constexpr (!(SE & 2)) setup_w();
  if (IL) { if (!(IL & 2)) XRAW(1, 0); XRAW(1, 1); if (IL & 2) XRAW(1, 2); } else w_dma4(g.mW, g.srdW, g.voffW, (rot + u0) * kWT);
#define W1() w_dma4(g.mW + GSLOT, g.srdW, g.voffW, (rot + ((u0 + 1) & 3)) * kWT)
#define W2() w_dma4(g.mW + 2 * GSLOT, g.srdW, g.voffW, (rot + ((u0 + 2) & 3)) * kWT)
  if constexpr (FQ_W1L == 0) W1();
  if constexpr (FQ_X2E == 2) X2();
  if constexpr (FQ_W2E == 2) W2();
  u32x4 st[4], p[4], p2[4];
  const bool k3 = S3 && (kt & 7) == 3;
  const unsigned g2 = gpo | (k3 ? 0u : 0x80000000u);
#define POLL() do { \
    asm volatile("buffer_load_dwordx4 %0, %4, %5, %6 offen sc1\n\tbuffer_load_dwordx4 %1, %4, %5, %7 offen sc1\n\tbuffer_load_dwordx4 %2, %4, %5, %8 offen sc1\n\tbuffer_load_dwordx4 %3, %4, %5, %9 offen sc1" \
                 : "=&{v[160:163]}"(st[0]), "=&{v[164:167]}"(st[1]), "=&{v[168:171]}"(st[2]), "=&{v[172:175]}"(st[3]) : "v"(gso), "s"(k.ws), "s"(cq << 10), "s"((cq ^ 1) << 10), "s"((cq ^ 2) << 10), "s"((cq ^ 3) << 10) : "memory"); \
    asm volatile("buffer_load_dwordx4 %0, %4, %5, 0 offen sc1\n\tbuffer_load_dwordx4 %1, %4, %5, %6 offen sc1\n\tbuffer_load_dwordx4 %2, %4, %5, %7 offen sc1\n\tbuffer_load_dwordx4 %3, %4, %5, %8 offen sc1" \
                 : "=&{v[128:131]}"(p[0]), "=&{v[132:135]}"(p[1]), "=&{v[136:139]}"(p[2]), "=&{v[140:143]}"(p[3]) : "v"(gpo), "s"(k.ws), "s"(16384u), "s"(32768u), "s"(49152u) : "memory"); \
    if (S3) asm volatile("buffer_load_dwordx4 %0, %4, %5, %6 offen sc1\n\tbuffer_load_dwordx4 %1, %4, %5, %7 offen sc1\n\tbuffer_load_dwordx4 %2, %4, %5, %8 offen sc1\n\tbuffer_load_dwordx4 %3, %4, %5, %9 offen sc1" \
                 : "=&{v[144:147]}"(p2[0]), "=&{v[148:151]}"(p2[1]), "=&{v[152:155]}"(p2[2]), "=&{v[156:159]}"(p2[3]) : "v"(g2), "s"(k.ws), "s"(W_MODC - W_MODA), "s"(W_MODC - W_MODA + 16384u), "s"(W_MODC - W_MODA + 32768u), "s"(W_MODC - W_MODA + 49152u) : "memory"); \
    GAP(); } while (0)
#define POLL_WAIT(N) asm volatile("s_waitcnt vmcnt(%12)" : "+{v[160:163]}"(st[0]), "+{v[164:167]}"(st[1]), "+{v[168:171]}"(st[2]), "+{v[172:175]}"(st[3]), "+{v[128:131]}"(p[0]), "+{v[132:135]}"(p[1]), "+{v[136:139]}"(p[2]), \
                                  "+{v[140:143]}"(p[3]), "+{v[144:147]}"(p2[0]), "+{v[148:151]}"(p2[1]), "+{v[152:155]}"(p2[2]), "+{v[156:159]}"(p2[3]) : "i"(N) : "memory")
  GAP();
  if (PBK) { HOP_FRONT(); }
  if constexpr (PPOS == 0) POLL();
  if constexpr (!(SE & 2)) setup_b();
  if constexpr (PPOS == 2 && !IL) POLL();
  if (X1L) X1();
  if (IL) { if (!(IL & 2)) XRAW(1, 2); XRAW(1, 3); if (IL & 2) w_dma4(g.mW, g.srdW, g.voffW, (rot + u0) * kWT); GAP(); }
  attn_setup(k, c, kc, vc, pm, srdx, pa);
  GAP();
  if (IL && !(IL & 2)) { w_dma4(g.mW, g.srdW, g.voffW, (rot + u0) * kWT); GAP(); }
  if (IL & 4) POLL();
  ma[3] = tag;
  static_assert(W_XCH == W_OT + (16u << 20));
  const unsigned vp = k.xcd * 2 + ((k.j >> 1) & 1);
  unsigned o_rot = rot << 19, o_sox = g.soX - GSLOT - k.ldsb, o_dk = 4096u - k0 * 8192u, o_c17 = cq << 17, o_w1 = (rot + ((u0 + 1) & 3)) * kWT, o_idm = ffn_idle(k) ? 0x80000000u : 0u;
  unsigned o_prb = ((k.j & 1) ? W_OT + (vp << 21) : W_PROBE + ((vp & 1) << 13) + vp * 128) | (ffn_idle(k) && wid == 0 ? 0u : 0x80000000u), o_pto = (blockIdx.x * 4 + wid) * 2048;
  unsigned ta = tb + wid * 512 + (((lane ^ 32) * 8) ^ (k0 * 128)); float one = lane >= 32 ? 1.f : 0.f;
  asm volatile("" : "+{v[208:215]}"(ma), "+s"(o_rot), "+s"(o_sox), "+s"(o_dk), "+s"(o_c17), "+s"(o_w1), "+s"(o_idm), "+s"(o_prb), "+s"(o_pto), "+s"(sl[2]), "+s"(sl[3]), "+s"(c.pf1), "+s"(c.pf4), "+s"(c.wo.base),
                    "+s"(k.pm), "+v"(ta), "+v"(one));
  GAP();
  if (IL && !(IL & 4)) POLL();
  if constexpr (FQ_W1L == 2) W1();
  if constexpr (FQ_X2E == 1) X2();
  if constexpr (FQ_W2E == 1) W2();
  GAP();
  STAMPF(5);
  HOP_GO(0, wid);
  if (!PBK) { HOP_FRONT(); }
  STAMPF(6);
  {
    POLL_WAIT((X1L ? 8 : 0) + (FQ_X2E == 1 ? 8 : 0) + (FQ_W1L == 2 ? 4 : 0) + (FQ_W2E == 1 ? 4 : 0));
    asm volatile("ds_read_b128 v[0:3], %3\n\tds_read_b128 v[4:7], %3 offset:2048\n\tds_read_b128 v[8:11], %3 offset:4096\n\tds_read_b128 v[12:15], %3 offset:6144\n\t"
                 "ds_read_b128 v[16:19], %4\n\tds_read_b128 v[20:23], %4 offset:2048\n\tds_read_b128 v[24:27], %4 offset:4096\n\tds_read_b128 v[28:31], %4 offset:6144"
                 : "=&{v[0:31]}"(xa), "+v"(g.pfd), "+v"(cb) : "v"(vw[4]), "v"(vw[5]) : "memory");
    STAMPF(7);
    static_assert(W_MODC - W_MODA == 0x60000);
#define FR_C2
#define FR_L3
    unsigned bad, sp = 0;
    asm volatile(".Lfr_c:\n\t" "v_xor_b32 %[b], %[t], v161\n\t" "v_bitop3_b32 %[b], %[b], v163, %[t] bitop3:0xf6\n\t" "v_bitop3_b32 %[b], %[b], v165, %[t] bitop3:0xf6\n\t" "v_bitop3_b32 %[b], %[b], v167, %[t] bitop3:0xf6\n\t" "v_bitop3_b32 %[b], %[b], v169, %[t] bitop3:0xf6\n\t" "v_bitop3_b32 %[b], %[b], v171, %[t] bitop3:0xf6\n\t" "v_bitop3_b32 %[b], %[b], v173, %[t] bitop3:0xf6\n\t" "v_bitop3_b32 %[b], %[b], v175, %[t] bitop3:0xf6\n\t" "v_bitop3_b32 %[b], %[b], v129, %[t] bitop3:0xf6\n\t" "v_bitop3_b32 %[b], %[b], v131, %[t] bitop3:0xf6\n\t" "v_bitop3_b32 %[b], %[b], v133, %[t] bitop3:0xf6\n\t" "v_bitop3_b32 %[b], %[b], v135, %[t] bitop3:0xf6\n\t" "v_bitop3_b32 %[b], %[b], v137, %[t] bitop3:0xf6\n\t" "v_bitop3_b32 %[b], %[b], v139, %[t] bitop3:0xf6\n\t" "v_bitop3_b32 %[b], %[b], v141, %[t] bitop3:0xf6\n\t" "v_bitop3_b32 %[b], %[b], v143, %[t] bitop3:0xf6\n\t" FR_C2 "v_cmp_ne_u32 vcc, 0, %[b]\n\ts_cbranch_vccnz .Lfr_s\n.Lfr_k:\n\t.subsection 1\n.Lfr_s:\n\ts_sleep 1\n\t" "buffer_load_dwordx4 v[160:163], %[gs], %[ws], %[q0] offen sc1\n\t" "buffer_load_dwordx4 v[164:167], %[gs], %[ws], %[q1] offen sc1\n\t" "buffer_load_dwordx4 v[168:171], %[gs], %[ws], %[q2] offen sc1\n\t" "buffer_load_dwordx4 v[172:175], %[gs], %[ws], %[q3] offen sc1\n\t" "s_mov_b32 vcc_lo, 0\n\t" "buffer_load_dwordx4 v[128:131], %[gp], %[ws], vcc_lo offen sc1\n\t" "s_mov_b32 vcc_lo, 16384\n\t" "buffer_load_dwordx4 v[132:135], %[gp], %[ws], vcc_lo offen sc1\n\t" "s_mov_b32 vcc_lo, 32768\n\t" "buffer_load_dwordx4 v[136:139], %[gp], %[ws], vcc_lo offen sc1\n\t" "s_mov_b32 vcc_lo, 49152\n\t" "buffer_load_dwordx4 v[140:143], %[gp], %[ws], vcc_lo offen sc1\n\t" FR_L3
                 "s_add_u32 %[sp], %[sp], 1\n\ts_cmp_lt_u32 %[sp], %[cap]\n\ts_waitcnt vmcnt(0)\n\ts_cbranch_scc1 .Lfr_c\n\tbuffer_store_dword v211, off, %[ws], 0 offset:256 sc1\n\ts_branch .Lfr_k\n\t.subsection 0"
                 : "+{v[160:163]}"(st[0]), "+{v[164:167]}"(st[1]), "+{v[168:171]}"(st[2]), "+{v[172:175]}"(st[3]), "+{v[128:131]}"(p[0]), "+{v[132:135]}"(p[1]), "+{v[136:139]}"(p[2]), "+{v[140:143]}"(p[3]),
                   "+{v[144:147]}"(p2[0]), "+{v[148:151]}"(p2[1]), "+{v[152:155]}"(p2[2]), "+{v[156:159]}"(p2[3]), [b] "=&v"(bad), [sp] "+s"(sp)
                 : [t] "s"(tag), [t2] "s"(k3 ? tag : 0u), [gs] "v"(gso), [gp] "v"(gpo), [g2] "v"(g2), [ws] "s"(k.ws), [q0] "s"(cq << 10), [q1] "s"((cq ^ 1) << 10), [q2] "s"((cq ^ 2) << 10), [q3] "s"((cq ^ 3) << 10),
                   [cap] "i"(kSpinCap), "{v[208:215]}"(ma) : "vcc", "scc", "memory");
    STAMP(4);
    f32x2 m[4];
#pragma unroll
    for (int i = 0; i < 4; ++i) { m[i] = f32x2{__uint_as_float(p[i][0]), __uint_as_float(p[i][2])}; if (S3) m[i] += f32x2{__uint_as_float(p2[i][0]), __uint_as_float(p2[i][2])}; }
    f32x2 s = (m[0] + m[1]) + (m[2] + m[3]);
    s = up2(cvtpk(s[0], s[1])) + one;
    lds_st(ta, up2(cvtpk(s[0], s[1])) * QSCALE);
    GAP();
    mb[2] = __float_as_uint(silu_bf(lo16f(cb))) >> 16;
    asm volatile("" : "+{v[216:219]}"(mb));
    STAMPF(12);
    if constexpr (FQ_EB) asm volatile("s_waitcnt lgkmcnt(1)" ::: "memory"); else BARRIER_LGKM();
    STAMPF(13);
  }
  g.q[0] = xa;
  asm volatile(FQ_ASM : "+{v[0:31]}"(g.q[0]), "=&{v[32:63]}"(g.q[1]), "=&{v[64:95]}"(g.q[2]), "=&{v[96:127]}"(g.q[3]), [fl] "=&v"(g.fl), [pf] "+{v246}"(g.pfd), "+{v[200:207]}"(vw), "+{v[208:215]}"(ma), "+{v[216:219]}"(mb),
                 "=&{v220}"(c.kv.vkb[0]), "=&{v221}"(c.kv.vkb[1]), "=&{v222}"(c.kv.vkb[2]), "=&{v223}"(c.kv.vkb[3]), "=&{v224}"(c.kv.vksrc[0]), "=&{v225}"(c.kv.vksrc[1]), "=&{v226}"(c.kv.vksrc[2]), "=&{v227}"(c.kv.vksrc[3]),
                 "=&{v228}"(c.kv.vvb[0]), "=&{v229}"(c.kv.vvb[1]), "=&{v230}"(c.kv.vvsrc[0]), "=&{v231}"(c.kv.vvsrc[1]), "=&{v232}"(c.gv), "=&{v240}"(c.wo.wv[0]), "=&{v241}"(c.wo.wv[1]), "=&{v244}"(c.wa0),
                 "=&{v245}"(g.fvo), "=&{v247}"(c.tlb), "=&{v233}"(c.gvm), "=&{v248}"(c.c3),
                 [hl] "=&s"(c.hl), [hl18] "=&s"(c.hl18), [cto] "=&s"(c.cto), [pf2] "=&s"(c.pf2), [po] "=&s"(c.po), [px] "=&s"(c.px), [vo] "=&s"(c.vo), [eso] "=&s"(c.eso), [otb] "=&s"(c.otb),
                 [xcb] "=&s"(c.xcb), [frb] "=&s"(c.frb), [fw2] "=&s"(c.fw2), [xso] "=&s"(c.xso), [ss] "=&s"(c.ss), [mbp] "=&s"(c.mbp), [gvo] "=&s"(c.gvo) FQ_STV
               : "{v[160:163]}"(st[0]), "{v[164:167]}"(st[1]), "{v[168:171]}"(st[2]), "{v[172:175]}"(st[3]), "{v[184:199]}"(wbs),
                 [ws] "s"(k.ws), [sw] "s"(g.srdW), [sx] "s"(srdx), [sK] "s"(c.kv.srdK), [sV] "s"(c.kv.srdV), FQ_SD [mw] "s"(g.mW), [mx] "s"(g.mX),
                 [rot19] "s"(o_rot), [w2] "s"(w2), [w3] "s"(w3), [sox] "s"(o_sox), [dk] "s"(o_dk), [fo] "s"(g.fo), [fw] "s"(fw), [pso] "s"(pso), [c17] "s"(o_c17), [sl2] "s"(sl[2]), [w1] "s"(o_w1),
                 [sl3] "s"(sl[3]), [wq] "s"(wqp), [kv] "s"(kvt), [sofk] "s"(c.wo.m), [pf4] "s"(c.pf4), [pf1] "s"(c.pf1), [wo] "s"(c.wo.srd),
                 [bid] "s"(k.bid), [wid] "s"(wid), [idm] "s"(o_idm), [prb] "s"(o_prb), [pto] "s"(o_pto)
               : FQ_CL, ACLOB_LO, ACLOB_HI);
  GAP();
}
DI void oproj(Ctx& k, W4C& c STAMP_ARG) {
  const unsigned wid = k.wid, tag = k.tag; unsigned gad, ln;
  u32x32 UP = c.up, DN = c.q[1]; f32x32 A3 = __builtin_bit_cast(f32x32, c.q[3]), XT = __builtin_bit_cast(f32x32, c.q[0]); float cww, bbw;
  asm volatile(OP_ASM
               : "+{v[176:207]}"(UP), "+{v[160:175]}"(c.o3), "+{v[32:63]}"(DN), "+{v[96:127]}"(A3), "+{v[0:31]}"(XT), "+{v[64:95]}"(c.q[2]), "+{v[128:159]}"(c.wm), "+{v224}"(c.kv.vksrc[0]), "+{v225}"(c.kv.vksrc[1]), "+{v226}"(c.kv.vksrc[2]), "+{v227}"(c.kv.vksrc[3]), "=&{s87}"(cww), "=&{s88}"(bbw),
                 "=&{v237}"(gad), "=&{v244}"(ln), "+{v213}"(c.gra), "+{v240}"(c.wo.wv[0]), "+{v241}"(c.wo.wv[1]), "+{v247}"(c.tlb), "+{v252}"(c.vs), "+{v254}"(c.fbv), [pm] "+s"(k.pm)
               : "{v234}"(c.wo.wa), "{v242}"(c.l16), "{v243}"(c.tagv), "{v245}"(c.fl),
                 [ws] "s"(k.ws), [wo] "s"(c.wo.srd), [sd] "s"(c.sD), [sx] "s"(c.sX), [sr] "s"(c.sR),
                 [tag] "s"(tag), [wid] "s"(wid), [hl] "s"(c.hl), [eso] "s"(c.eso), [na] "s"(c.na), [frb] "s"(c.frb), [xcb] "s"(c.xcb), [xso] "s"(c.xso),
                 [m] "s"(c.wo.m), [wob] "s"(c.wo.base), [ss] "s"(c.ss), [mbp] "s"(c.mbp), [cap] "i"(kSpinCap), [fw2] "s"(c.fw2)
               : OP_CLOB, ACLOB_LO, ACLOB_HI);
  if (__builtin_expect(k.pm == 2, 0)) {
    asm volatile("s_getpc_b64 vcc\n.Lrs_a:\n\ts_sub_u32 vcc_lo, vcc_lo, .Lrs_a-.Lfz_a\n\ts_subb_u32 vcc_hi, vcc_hi, 0\n\ts_and_b32 vcc_lo, vcc_lo, 0xfffff000\n\ts_setpc_b64 vcc"
                 :: "{s[0:1]}"(__builtin_amdgcn_kernarg_segment_ptr()), "{s2}"(blockIdx.x), "{v0}"((wid * 64 + lane_id()) | 0x10000u) : "vcc", "scc", "memory");
    __builtin_unreachable();
  }
  asm volatile(TL_ASM
               : "+{v[176:207]}"(UP), "+{v[32:63]}"(DN), "+{v[96:127]}"(A3), "+{v[0:31]}"(XT), "+{v[64:95]}"(c.q[2]), "+{v[128:159]}"(c.wm), "+{v[160:175]}"(c.o3), "+{s87}"(cww), "+{s88}"(bbw), "+{v240}"(c.wo.wv[0])
               : "{v237}"(gad), "{v254}"(c.fbv), "{v242}"(c.l16), "{v243}"(c.tagv), "{v244}"(ln),
                 [ws] "s"(k.ws), [tag] "s"(tag), [wid] "s"(wid), [bid] "s"(k.bid), [nb] "s"(k.nb), [out] "s"(c.out), [cap] "i"(kSpinCap)
               : TL_CLOB);
  STAMPO(26);
}
#define PERM 0
__global__ __launch_bounds__(256, 1) __attribute__((aligned(4096))) void k_fused(Args a) {
  Ctx k;
  const unsigned bid = PERM ? ((blockIdx.x << 3) | (blockIdx.x >> 5)) & 255u : blockIdx.x;
  unsigned t0;
  asm volatile("v_readfirstlane_b32 %0, %1" : "=s"(t0) : "v"(threadIdx.x));
  const unsigned wid = (t0 >> 6) & 3;
  k.pm = 1 - (t0 >> 16);
  k.wid = wid; k.bid = bid; k.xcd = ((bid & 3) << 1) | ((bid >> 2) & 1); k.j = bid >> 3; k.kvh = k.xcd >> 1; k.hl = k.j & 7; k.rblk = (k.xcd & 1) * 4 + (k.j >> 3);
  k.cs = (k.xcd & 1) << 13; k.nb = 0;
  STAMP_DECL;
  STAMP(0);
#define SPINTO(i) do { } while (0)
  W4C c;
  SPINTO(0);
  {
    H4 g;
    front(k, g, c STAMP_PASS);
    STAMP(8);
    SPINTO(1);
    attention(k, g, c STAMP_PASS);
  }
  SPINTO(2);
  oproj(k, c STAMP_PASS);
  SPINTO(3);
  STAMP_FLUSH();
  asm volatile(HOP_DEFS);
}

extern "C" void kernel_launch(void* const* d_in, const int* in_sizes, int n_in, void* d_out, int out_size, void* d_ws, size_t ws_size,
                              hipStream_t stream) {
  Args a;
  a.x = (const u16*)d_in[0]; a.kc = (const u16*)d_in[1]; a.vc = (const u16*)d_in[2]; a.wq = (const u16*)d_in[3]; a.wo = (const u16*)d_in[4];
  a.w_r = (const u16*)d_in[6]; a.wg = (const u16*)d_in[7]; a.wu = (const u16*)d_in[8]; a.wd = (const u16*)d_in[9]; a.c = (const u16*)d_in[10];
  a.w_mod = (const u16*)d_in[11]; a.out = (u16*)d_out; a.ws = (char*)d_ws;
  (void)hipFuncSetAttribute((const void*)k_fused, hipFuncAttributeMaxDynamicSharedMemorySize, 163840);
  k_fused<<<256, 256, 163840, stream>>>(a);
}
```

```cpp
#include <hip/hip_runtime.h>
#include <hip/hip_bf16.h>
#include <stdint.h>
#include <utility>

typedef unsigned short u16;
using bf16x8 = __attribute__((ext_vector_type(8))) short;
using s16x4  = __attribute__((ext_vector_type(4))) short;
using f32x16 = __attribute__((ext_vector_type(16))) float;
using u32x16 = __attribute__((ext_vector_type(16))) unsigned;
using f32x4  = __attribute__((ext_vector_type(4))) float;
using f32x2  = __attribute__((ext_vector_type(2))) float;
using u32x4  = __attribute__((ext_vector_type(4))) unsigned;
using u32x2  = __attribute__((ext_vector_type(2))) unsigned;
using u32x8 = __attribute__((ext_vector_type(8))) unsigned;
using f32x8 = __attribute__((ext_vector_type(8))) float;
using f32x32 = __attribute__((ext_vector_type(32))) float;
using u32x32 = __attribute__((ext_vector_type(32))) unsigned;
typedef __bf16 bf16x2_t __attribute__((ext_vector_type(2)));
#define DI __device__ __forceinline__
#define NTMASK 0
#define NT_SEL(bit) ((NTMASK >> (bit)) & 1)
#if NT_SEL(0)
#define NTX " nt"
#else
#define NTX ""
#endif
#if NT_SEL(1)
#define NTM " nt"
#else
#define NTM ""
#endif
#if NT_SEL(2)
#define NTQ " nt"
#else
#define NTQ ""
#endif
#if NT_SEL(3)
#define NTO " nt"
#else
#define NTO ""
#endif
#if NT_SEL(4)
#define NTG " nt"
#else
#define NTG ""
#endif
#if NT_SEL(5)
#define NTD " nt"
#else
#define NTD ""
#endif
#if NT_SEL(6)
#define NTT " nt"
#else
#define NTT ""
#endif
#if NT_SEL(7)
#define NTF " nt"
#else
#define NTF ""
#endif

constexpr int S = 2048, DM = 1024, HQ = 32, HKV = 4, DH = 128, SKV = 2048, WIN = 512, FF = 3584;
constexpr int QD = HQ * DH;
constexpr float QSCALE = 0.12751743082f;

DI float bf2f(u16 v) { return __uint_as_float(((unsigned)v) << 16); }
DI u16 f2bf(float x) { unsigned u = __float_as_uint(x); u += 0x7fffu + ((u >> 16) & 1u); return (u16)(u >> 16); }
DI unsigned cvtpk(float lo, float hi) { f32x2 v = {lo, hi}; bf16x2_t b = __builtin_convertvector(v, bf16x2_t); return __builtin_bit_cast(unsigned, b); }
DI float lo16f(unsigned w) { return __uint_as_float(w << 16); }
DI float hi16f(unsigned w) { return __uint_as_float(w & 0xffff0000u); }
DI float rbf(float x) { return lo16f(cvtpk(x, x)); }
DI f32x2 up2(unsigned w) { return f32x2{lo16f(w), hi16f(w)}; }
DI float dot2(unsigned a, unsigned b, float c) { return __builtin_amdgcn_fdot2_f32_bf16(__builtin_bit_cast(bf16x2_t, a), __builtin_bit_cast(bf16x2_t, b), c, false); }
DI float dot2z(unsigned a, unsigned b) { return __builtin_amdgcn_fdot2_f32_bf16(__builtin_bit_cast(bf16x2_t, a), __builtin_bit_cast(bf16x2_t, b), 0.f, false); }
DI unsigned plo(unsigned a, unsigned b) { return __builtin_amdgcn_perm(b, a, 0x05040100u); }
DI unsigned phi(unsigned a, unsigned b) { return __builtin_amdgcn_perm(b, a, 0x07060302u); }
DI unsigned rfl(unsigned x) { return __builtin_amdgcn_readfirstlane(x); }
template <int CTRL> DI float dpp_f(float v) { return __int_as_float(__builtin_amdgcn_update_dpp(0, __float_as_int(v), CTRL, 0xf, 0xf, true)); }
template <int CTRL> DI unsigned dpp_u(unsigned v) { return (unsigned)__builtin_amdgcn_update_dpp(0, (int)v, CTRL, 0xf, 0xf, true); }
DI float fold16(float a, float b) { auto r = __builtin_amdgcn_permlane16_swap(__float_as_uint(a), __float_as_uint(b), false, false); return __uint_as_float(r[0]) + __uint_as_float(r[1]); }
DI float fold32(float a, float b) { auto r = __builtin_amdgcn_permlane32_swap(__float_as_uint(a), __float_as_uint(b), false, false); return __uint_as_float(r[0]) + __uint_as_float(r[1]); }
DI float sum16(float v) { return fold16(v, v); }
DI float sum32(float v) { return fold32(v, v); }
DI float sum_row16(float v) { v += dpp_f<0xB1>(v); v += dpp_f<0x4E>(v); v += dpp_f<0x141>(v); v += dpp_f<0x140>(v); return v; }
DI float wave_sum(float v) { return sum32(sum16(sum_row16(v))); }
DI unsigned wave_maxu(unsigned v) {
  v = max(v, dpp_u<0xB1>(v)); v = max(v, dpp_u<0x4E>(v)); v = max(v, dpp_u<0x141>(v)); v = max(v, dpp_u<0x140>(v));
  { auto r = __builtin_amdgcn_permlane16_swap(v, v, false, false); v = max(r[0], r[1]); }
  { auto r = __builtin_amdgcn_permlane32_swap(v, v, false, false); v = max(r[0], r[1]); }
  return v;
}
#define GAP() __builtin_amdgcn_sched_barrier(0)
template <int N> using iseq = std::make_integer_sequence<int, N>;
template <int... Is> using ilist = std::integer_sequence<int, Is...>;
typedef __amdgpu_buffer_rsrc_t srd_t;
#define SRD3 0x00020000
DI srd_t mk_srd(const void* p, unsigned bytes) { return __builtin_amdgcn_make_buffer_rsrc((void*)p, (short)0, (int)bytes, SRD3); }
DI unsigned lane_id() { unsigned l; asm volatile("v_mbcnt_lo_u32_b32 %0, -1, 0\n\tv_mbcnt_hi_u32_b32 %0, -1, %0" : "=v"(l)); return l; }
struct Args { const u16 *x, *kc, *vc, *wq, *wo, *w_r, *wg, *wu, *wd, *c, *w_mod; u16* out; char* ws; };
typedef const __attribute__((address_space(4))) Args* argp_t;
DI argp_t args() { argp_t p = (argp_t)__builtin_amdgcn_kernarg_segment_ptr(); asm volatile("" : "+s"(p)); return p; }
#define BARRIER_LGKM() do { asm volatile("s_waitcnt lgkmcnt(0)\n\ts_barrier" ::: "memory"); } while (0)

constexpr unsigned W_EPOCH = 0x260000, W_TMO = 256;
constexpr unsigned W_FLAGH = 0x261000;
constexpr unsigned W_VOTE = 0x2000, W_PROBE = 0x4000;
constexpr unsigned W_GKEY = 0x10000;
constexpr unsigned W_FLAGR = 0x8000;
constexpr unsigned W_FLAGO = 0x20000;
constexpr unsigned W_CANDH = 0x40000;
constexpr unsigned W_STAT = 0x80000;
constexpr unsigned W_YG = 0x100000;
constexpr unsigned W_CANDX = 0x1E0000;
constexpr unsigned W_ST1 = 0x200000;
constexpr unsigned W_MODA = 0x210000;
constexpr unsigned W_MODC = 0x270000;
constexpr unsigned W_MODB = 0x220000;
constexpr unsigned W_SIL = 0x240000;
constexpr unsigned W_HT = 4u << 20;
constexpr unsigned W_OT = 8u << 20;
constexpr unsigned W_XCH = 24u << 20;
constexpr unsigned W_END = 40u << 20;
constexpr unsigned TAGM = 0x40004000u, TAGC = 0x9E3739B9u;
#define SPINCAP 262144
constexpr unsigned kSpinCap = SPINCAP;
#define SL0 1
#define SL1 1
#define SL2 1
#define SL3 1
template <int SL = 1> DI bool spin_fail(unsigned& spins, srd_t ws, unsigned tag) {
  __builtin_amdgcn_s_sleep(SL);
  if (++spins > kSpinCap) { __builtin_amdgcn_raw_buffer_store_b32(tag, ws, W_TMO, 0, 16); return true; }
  return (spins & 255u) == 0u && rfl(__builtin_amdgcn_raw_buffer_load_b32(ws, W_TMO, 0, 16)) == tag;
}
DI u32x4 ld16(srd_t ws, unsigned off) { return __builtin_amdgcn_raw_buffer_load_b128(ws, off, 0, 16); }
DI u32x2 ld8(srd_t ws, unsigned off) { return __builtin_amdgcn_raw_buffer_load_b64(ws, off, 0, 16); }
DI void st16(srd_t ws, unsigned off, u32x4 v) { __builtin_amdgcn_raw_buffer_store_b128(v, ws, off, 0, 16); }
DI void st8(srd_t ws, unsigned off, u32x2 v) { __builtin_amdgcn_raw_buffer_store_b64(v, ws, off, 0, 16); }
DI void st4(srd_t ws, unsigned off, unsigned v) { __builtin_amdgcn_raw_buffer_store_b32(v, ws, off, 0, 16); }
DI void wait_flags(srd_t ws, unsigned off, unsigned tag) {
  const unsigned o = off + (lane_id() & 31u) * 4u;
  for (unsigned spins = 0;;) {
    const unsigned f = __builtin_amdgcn_raw_buffer_load_b32(ws, o, 0, 16);
    if (__all(f == tag)) break;
    if (spin_fail(spins, ws, tag)) break;
  }
  asm volatile("" ::: "memory");
}
template <int OFF, bool DEV = false> DI u32x4 ald16(srd_t s, unsigned vo, unsigned so) {
  u32x4 r;
  if constexpr (DEV) asm volatile("buffer_load_dwordx4 %0, %1, %2, %3 offen offset:%4 sc1" : "=v"(r) : "v"(vo), "s"(s), "s"(so), "i"(OFF) : "memory");
  else               asm volatile("buffer_load_dwordx4 %0, %1, %2, %3 offen offset:%4" NTF : "=v"(r) : "v"(vo), "s"(s), "s"(so), "i"(OFF) : "memory");
  return r;
}
template <int OFF> DI u32x2 ald8(srd_t s, unsigned vo, unsigned so) {
  u32x2 r; asm volatile("buffer_load_dwordx2 %0, %1, %2, %3 offen offset:%4" : "=v"(r) : "v"(vo), "s"(s), "s"(so), "i"(OFF) : "memory"); return r;
}
DI u32x2 ald8d(srd_t s, unsigned vo) { u32x2 r; asm volatile("buffer_load_dwordx2 %0, %1, %2, 0 offen sc1" : "=v"(r) : "v"(vo), "s"(s) : "memory"); return r; }
template <int OFF, bool DEV = false> DI unsigned ald4(srd_t s, unsigned vo, unsigned so) {
  unsigned r;
  if constexpr (DEV) asm volatile("buffer_load_dword %0, %1, %2, %3 offen offset:%4 sc1" : "=v"(r) : "v"(vo), "s"(s), "s"(so), "i"(OFF) : "memory");
  else               asm volatile("buffer_load_dword %0, %1, %2, %3 offen offset:%4" : "=v"(r) : "v"(vo), "s"(s), "s"(so), "i"(OFF) : "memory");
  return r;
}
DI void touch(unsigned& d, const void* base, unsigned vo) { asm volatile("global_load_dword %0, %1, %2" : "+v"(d) : "v"(vo), "s"(base) : "memory"); }
DI void touchq(unsigned& d, const void* base, unsigned vo) { asm volatile("global_load_dword %0, %1, %2" NTQ : "+v"(d) : "v"(vo), "s"(base) : "memory"); }
template <class T> DI T lds_ld(unsigned a) { return *reinterpret_cast<const __attribute__((address_space(3))) T*>(a); }
template <class T> DI void lds_st(unsigned a, T v) { *reinterpret_cast<__attribute__((address_space(3))) T*>(a) = v; }
DI float silu_bf(float v) { return rbf(v * __builtin_amdgcn_rcpf(1.f + __expf(-v))); }
struct Ctx { static constexpr unsigned ldsb = 0; unsigned pm, bid, wid, xcd, j, kvh, hl, rblk, tag, cs, nb; srd_t ws; };
#define TAIL2 1
DI unsigned ffn_j(const Ctx& k) { return TAIL2 ? (k.j < 24 ? k.j : 24 + (k.j & 3)) : min(k.j, 27u); }
DI bool ffn_idle(const Ctx& k) { return TAIL2 ? (k.j >> 2) == 6 + (k.xcd & 1) : k.j >= 28; }
constexpr unsigned UNIT = 16384;
#define STAMPA(I) do { } while (0)
#define STAMPO(I) do { } while (0)
#define STAMP_DECL
#define STAMP(I) do { } while (0)
#define STAMP_FLUSH() do { } while (0)
#define STAMP_ARG
#define STAMP_PASS
#define ACLOB_LO "a0","a1","a2","a3","a4","a5","a6","a7","a8","a9","a10","a11","a12","a13","a14","a15","a16","a17","a18","a19","a20","a21","a22","a23","a24","a25","a26","a27","a28","a29","a30","a31","a32","a33","a34","a35","a36","a37","a38","a39","a40","a41","a42","a43","a44","a45","a46","a47","a48","a49","a50","a51","a52","a53","a54","a55","a56","a57","a58","a59","a60","a61","a62","a63","a64","a65","a66","a67","a68","a69","a70","a71","a72","a73","a74","a75","a76","a77","a78","a79","a80","a81","a82","a83","a84","a85","a86","a87","a88","a89","a90","a91","a92","a93","a94","a95","a96","a97","a98","a99","a100","a101","a102","a103","a104","a105","a106","a107","a108","a109","a110","a111","a112","a113","a114","a115","a116","a117","a118","a119","a120","a121","a122","a123","a124","a125","a126","a127"
#define ACLOB_HI "a128","a129","a130","a131","a132","a133","a134","a135","a136","a137","a138","a139","a140","a141","a142","a143","a144","a145","a146","a147","a148","a149","a150","a151","a152","a153","a154","a155","a156","a157","a158","a159","a160","a161","a162","a163","a164","a165","a166","a167","a168","a169","a170","a171","a172","a173","a174","a175","a176","a177","a178","a179","a180","a181","a182","a183","a184","a185","a186","a187","a188","a189","a190","a191","a192","a193","a194","a195","a196","a197","a198","a199","a200","a201","a202","a203","a204","a205","a206","a207","a208","a209","a210","a211","a212","a213","a214","a215","a216","a217","a218","a219","a220","a221","a222","a223","a224","a225","a226","a227","a228","a229","a230","a231","a232","a233","a234","a235","a236","a237","a238","a239","a240","a241","a242","a243","a244","a245","a246","a247","a248","a249","a250","a251","a252","a253","a254","a255"
#define HOP_DEFS ".set .Lhc00, .Lhs0\n\t.set .Lhs0n, .Lhs4\n\t.set .Lhs4n, .Lhs8\n\t.set .Lhs8n, .Lhr0\n\t.set .Lhc01, .Lhs1\n\t.set .Lhs1n, .Lhs5\n\t.set .Lhs5n, .Lhs9\n\t.set .Lhs9n, .Lhr0\n\t.set .Lhc02, .Lhs2\n\t.set .Lhs2n, .Lhs6\n\t.set .Lhs6n, .Lhs10\n\t.set .Lhs10n, .Lhr0\n\t.set .Lhc03, .Lhs3\n\t.set .Lhs3n, .Lhs7\n\t.set .Lhs7n, .Lhr0\n\t.set .Lhc10, .Lhs11\n\t.set .Lhs11n, .Lhs15\n\t.set .Lhs15n, .Lhr1\n\t.set .Lhc11, .Lhs12\n\t.set .Lhs12n, .Lht0\n\t.set .Lht0n, .Lhr1\n\t.set .Lhc12, .Lhs13\n\t.set .Lhs13n, .Lht1\n\t.set .Lht1n, .Lhr1\n\t.set .Lhc13, .Lhs14\n\t.set .Lhs14n, .Lhr1\n\t.set .Lhc20, .Lhf\n\t.set .Lhfn, .Lhr2\n\t.set .Lhc21, .Lhr2\n\t.set .Lhc22, .Lhr2\n\t.set .Lhc23, .Lhr2"
#define HOP_PAGE(n) asm volatile("s_branch .Lht" #n "\n\t.p2align 12\n.Lht" #n ":\n\ts_cbranch_execz .Lht" #n "n")
#define PAGE_ALIGN() asm volatile("s_branch .Lpa%=\n\t.p2align 12\n.Lpa%=:" ::: "memory")
#define HOP_FRONT() asm volatile("s_branch .Lhf\n\t.p2align 12\n.Lhf:\n\ts_cbranch_execz .Lhfn")
#define HOP_GO(g, w) asm volatile("s_mov_b64 exec, 0\n\ts_cmp_eq_u32 %0, 0\n\ts_cbranch_scc1 .Lhc" #g "0\n\ts_cmp_eq_u32 %0, 1\n\ts_cbranch_scc1 .Lhc" #g "1\n\ts_cmp_eq_u32 %0, 2\n\ts_cbranch_scc1 .Lhc" #g "2\n\ts_branch .Lhc" #g "3\n.Lhr" #g ":\n\ts_mov_b64 exec, -1" :: "s"(w) : "scc", "memory")
#define STP(x)
#define OP_ASM \
  "s_waitcnt lgkmcnt(0)\n\t" \
  "s_barrier\n\t" \
  STP("s_memrealtime s[94:95]\n\t") \
  STP("s_memtime s[92:93]\n\t") \
  "ds_read_b32 v228, v213 offset:49152\n\t" \
  "v_mfma_f32_16x16x32_bf16 a[8:11], a[140:143], v[76:79], 0\n\t" \
  "v_add_u32 v238, 14336, v240\n\t" \
  "v_add_u32 v239, 21504, v241\n\t" \
  "v_mfma_f32_16x16x32_bf16 a[8:11], a[136:139], v[72:75], a[8:11]\n\t" \
  "ds_read_b128 a[12:15], v242 offset:32768\n\t" \
  "v_add_u32 v241, 7168, v241\n\t" \
  "v_mfma_f32_16x16x32_bf16 a[8:11], a[132:135], v[68:71], a[8:11]\n\t" \
  "s_mov_b32 s79, 0x05040100\n\t" \
  "ds_read_b128 a[28:31], v242 offset:33792\n\t" \
  "s_mov_b32 s80, 0x07060302\n\t" \
  "v_mfma_f32_16x16x32_bf16 a[8:11], a[128:131], v[64:67], a[8:11]\n\t" \
  "ds_read_b128 a[44:47], v242 offset:34816\n\t" \
  "v_mfma_f32_16x16x32_bf16 a[24:27], a[144:147], v[64:67], 0\n\t" \
  "ds_read_b128 a[60:63], v242 offset:35840\n\t" \
  "v_mfma_f32_16x16x32_bf16 a[24:27], a[148:151], v[68:71], a[24:27]\n\t" \
  "v_mfma_f32_16x16x32_bf16 a[24:27], a[152:155], v[72:75], a[24:27]\n\t" \
  "s_waitcnt vmcnt(14)\n\t" \
  "buffer_store_dword v243, v254, %[ws], %[fw2] offen offset:32 sc1\n\t" \
  "v_mfma_f32_16x16x32_bf16 a[24:27], a[156:159], v[76:79], a[24:27]\n\t" \
  "ds_read_b128 a[76:79], v242 offset:36864\n\t" \
  "v_mfma_f32_16x16x32_bf16 a[104:107], a[172:175], v[76:79], 0\n\t" \
  "ds_read_b128 a[92:95], v242 offset:37888\n\t" \
  "v_mfma_f32_16x16x32_bf16 a[104:107], a[168:171], v[72:75], a[104:107]\n\t" \
  "ds_read_b128 a[108:111], v242 offset:38912\n\t" \
  "s_waitcnt lgkmcnt(7)\n\t" \
  "v_cmp_eq_u32 vcc, %[tag], v228\n\t" \
  "s_and_b32 vcc_lo, vcc_lo, %[pm]\n\t" \
  "s_cbranch_scc1 .Lop_rd\n\t" \
  "v_mfma_f32_16x16x32_bf16 a[104:107], a[164:167], v[68:71], a[104:107]\n\t" \
  "ds_read_b128 a[124:127], v242 offset:39936\n\t" \
  "v_mfma_f32_16x16x32_bf16 a[104:107], a[160:163], v[64:67], a[104:107]\n\t" \
  "ds_read_b128 a[208:211], v242 offset:40960\n\t" \
  "v_mfma_f32_16x16x32_bf16 a[120:123], a[176:179], v[64:67], 0\n\t" \
  "buffer_load_dword v246, v245, %[ws], %[fw2] offen sc1\n\t" \
  "v_mfma_f32_16x16x32_bf16 a[120:123], a[180:183], v[68:71], a[120:123]\n\t" \
  "ds_read_b128 a[212:215], v242 offset:41984\n\t" \
  "v_mfma_f32_16x16x32_bf16 a[120:123], a[184:187], v[72:75], a[120:123]\n\t" \
  "ds_read_b128 a[216:219], v242 offset:43008\n\t" \
  "v_mfma_f32_16x16x32_bf16 a[120:123], a[188:191], v[76:79], a[120:123]\n\t" \
  "s_xor_b32 s73, %[eso], 131072\n\t" \
  "ds_read_b128 a[220:223], v242 offset:44032\n\t" \
  "v_mfma_f32_16x16x32_bf16 a[88:91], v[108:111], v[76:79], 0\n\t" \
  "s_add_u32 m0, %[m], 0\n\t" \
  "ds_read_b128 a[192:195], v242 offset:45056\n\t" \
  "v_mfma_f32_16x16x32_bf16 a[88:91], v[104:107], v[72:75], a[88:91]\n\t" \
  "ds_read_b128 a[196:199], v242 offset:46080\n\t" \
  "v_mfma_f32_16x16x32_bf16 a[88:91], v[100:103], v[68:71], a[88:91]\n\t" \
  "ds_read_b128 a[200:203], v242 offset:47104\n\t" \
  "ds_read_b128 a[204:207], v242 offset:48128\n\t" \
  "v_mfma_f32_16x16x32_bf16 a[88:91], v[96:99], v[64:67], a[88:91]\n\t" \
  "s_waitcnt lgkmcnt(12)\n\t" \
  "s_mov_b64 exec, 0\n\t" \
  "s_cmp_eq_u32 %[wid], 0\n\t" \
  "s_cbranch_scc1 .Lhc10\n\t" \
  "s_cmp_eq_u32 %[wid], 1\n\t" \
  "s_cbranch_scc1 .Lhc11\n\t" \
  "s_cmp_eq_u32 %[wid], 2\n\t" \
  "s_cbranch_scc1 .Lhc12\n\t" \
  "s_branch .Lhc13\n\t" \
  ".Lhr1:\n\t" \
  "s_mov_b64 exec, -1\n\t" \
  "s_waitcnt vmcnt(6)\n\t" \
  "s_waitcnt vmcnt(0)\n\t" \
  "v_cmp_ne_u32 vcc, %[tag], v246\n\t" \
  "s_and_b32 vcc_lo, vcc_lo, 0x6\n\t" \
  "s_cbranch_scc1 .Lop_s0\n\t" \
  ".Lop_k0:\n\t" \
  "buffer_load_dwordx4 v[16:19], v242, %[ws], s73 offen offset:0 sc1\n\t" \
  "v_perm_b32 v248, v132, v128, s79\n\t" \
  "v_perm_b32 v128, v132, v128, s80\n\t" \
  "v_dot2_f32_bf16 v132, v248, v224, 0\n\t" \
  "v_dot2_f32_bf16 v128, v128, v224, 0\n\t" \
  "v_perm_b32 v249, v133, v129, s79\n\t" \
  "v_perm_b32 v129, v133, v129, s80\n\t" \
  "v_dot2_f32_bf16 v133, v249, v224, 0\n\t" \
  "v_dot2_f32_bf16 v129, v129, v224, 0\n\t" \
  "buffer_load_dwordx4 v[20:23], v242, %[ws], s73 offen offset:1024 sc1\n\t" \
  "v_perm_b32 v248, v134, v130, s79\n\t" \
  "v_perm_b32 v130, v134, v130, s80\n\t" \
  "v_dot2_f32_bf16 v134, v248, v224, 0\n\t" \
  "v_dot2_f32_bf16 v130, v130, v224, 0\n\t" \
  "v_perm_b32 v249, v135, v131, s79\n\t" \
  "v_perm_b32 v131, v135, v131, s80\n\t" \
  "v_dot2_f32_bf16 v135, v249, v224, 0\n\t" \
  "v_dot2_f32_bf16 v131, v131, v224, 0\n\t" \
  "buffer_load_dwordx4 v[24:27], v242, %[ws], s73 offen offset:2048 sc1\n\t" \
  "v_perm_b32 v248, v140, v136, s79\n\t" \
  "v_perm_b32 v136, v140, v136, s80\n\t" \
  "v_dot2_f32_bf16 v132, v248, v225, v132\n\t" \
  "v_dot2_f32_bf16 v128, v136, v225, v128\n\t" \
  "v_perm_b32 v249, v141, v137, s79\n\t" \
  "v_perm_b32 v137, v141, v137, s80\n\t" \
  "v_dot2_f32_bf16 v133, v249, v225, v133\n\t" \
  "v_dot2_f32_bf16 v129, v137, v225, v129\n\t" \
  "buffer_load_dwordx4 v[28:31], v242, %[ws], s73 offen offset:3072 sc1\n\t" \
  "v_perm_b32 v248, v142, v138, s79\n\t" \
  "v_perm_b32 v138, v142, v138, s80\n\t" \
  "v_dot2_f32_bf16 v134, v248, v225, v134\n\t" \
  "v_dot2_f32_bf16 v130, v138, v225, v130\n\t" \
  "v_perm_b32 v249, v143, v139, s79\n\t" \
  "v_perm_b32 v139, v143, v139, s80\n\t" \
  "v_dot2_f32_bf16 v135, v249, v225, v135\n\t" \
  "v_dot2_f32_bf16 v131, v139, v225, v131\n\t" \
  "s_xor_b32 s73, %[eso], 163840\n\t" \
  "buffer_load_dwordx4 v[0:3], v242, %[ws], s73 offen offset:0 sc1\n\t" \
  "v_perm_b32 v248, v148, v144, s79\n\t" \
  "v_perm_b32 v144, v148, v144, s80\n\t" \
  "v_dot2_f32_bf16 v132, v248, v226, v132\n\t" \
  "v_dot2_f32_bf16 v128, v144, v226, v128\n\t" \
  "v_perm_b32 v249, v149, v145, s79\n\t" \
  "v_perm_b32 v145, v149, v145, s80\n\t" \
  "v_dot2_f32_bf16 v133, v249, v226, v133\n\t" \
  "v_dot2_f32_bf16 v129, v145, v226, v129\n\t" \
  "buffer_load_dwordx4 v[4:7], v242, %[ws], s73 offen offset:1024 sc1\n\t" \
  "v_perm_b32 v248, v150, v146, s79\n\t" \
  "v_perm_b32 v146, v150, v146, s80\n\t" \
  "v_dot2_f32_bf16 v134, v248, v226, v134\n\t" \
  "v_dot2_f32_bf16 v130, v146, v226, v130\n\t" \
  "v_perm_b32 v249, v151, v147, s79\n\t" \
  "v_perm_b32 v147, v151, v147, s80\n\t" \
  "v_dot2_f32_bf16 v135, v249, v226, v135\n\t" \
  "v_dot2_f32_bf16 v131, v147, v226, v131\n\t" \
  "buffer_load_dwordx4 v[8:11], v242, %[ws], s73 offen offset:2048 sc1\n\t" \
  "v_perm_b32 v248, v156, v152, s79\n\t" \
  "v_perm_b32 v152, v156, v152, s80\n\t" \
  "v_dot2_f32_bf16 v132, v248, v227, v132\n\t" \
  "v_dot2_f32_bf16 v128, v152, v227, v128\n\t" \
  "v_perm_b32 v249, v157, v153, s79\n\t" \
  "v_perm_b32 v153, v157, v153, s80\n\t" \
  "v_dot2_f32_bf16 v133, v249, v227, v133\n\t" \
  "v_dot2_f32_bf16 v129, v153, v227, v129\n\t" \
  "buffer_load_dwordx4 v[12:15], v242, %[ws], s73 offen offset:3072 sc1\n\t" \
  "v_perm_b32 v248, v158, v154, s79\n\t" \
  "v_perm_b32 v154, v158, v154, s80\n\t" \
  "v_dot2_f32_bf16 v134, v248, v227, v134\n\t" \
  "v_dot2_f32_bf16 v130, v154, v227, v130\n\t" \
  "v_perm_b32 v249, v159, v155, s79\n\t" \
  "v_perm_b32 v155, v159, v155, s80\n\t" \
  "v_dot2_f32_bf16 v135, v249, v227, v135\n\t" \
  "v_dot2_f32_bf16 v131, v155, v227, v131\n\t" \
  "v_mfma_f32_16x16x32_bf16 v[96:99], a[128:131], a[12:15], 0\n\t" \
  "s_xor_b32 s73, %[eso], 262144\n\t" \
  "s_xor_b32 s71, %[wob], 1048576\n\t" \
  "v_mfma_f32_16x16x32_bf16 v[96:99], a[132:135], a[28:31], v[96:99]\n\t" \
  "v_mfma_f32_16x16x32_bf16 v[96:99], a[136:139], a[44:47], v[96:99]\n\t" \
  "v_mfma_f32_16x16x32_bf16 v[96:99], a[140:143], a[60:63], v[96:99]\n\t" \
  "v_mfma_f32_16x16x32_bf16 v[112:115], a[156:159], a[60:63], 0\n\t" \
  "buffer_load_dwordx4 v[48:51], v242, %[ws], s73 offen offset:0 sc1\n\t" \
  "v_mfma_f32_16x16x32_bf16 v[112:115], a[152:155], a[44:47], v[112:115]\n\t" \
  "v_mfma_f32_16x16x32_bf16 v[112:115], a[148:151], a[28:31], v[112:115]\n\t" \
  "v_mfma_f32_16x16x32_bf16 v[112:115], a[144:147], a[12:15], v[112:115]\n\t" \
  "s_waitcnt lgkmcnt(8)\n\t" \
  "v_mfma_f32_16x16x32_bf16 v[100:103], a[128:131], a[76:79], 0\n\t" \
  "buffer_load_dwordx4 v[52:55], v242, %[ws], s73 offen offset:1024 sc1\n\t" \
  "v_mfma_f32_16x16x32_bf16 v[100:103], a[132:135], a[92:95], v[100:103]\n\t" \
  "v_mfma_f32_16x16x32_bf16 v[100:103], a[136:139], a[108:111], v[100:103]\n\t" \
  "v_mfma_f32_16x16x32_bf16 v[100:103], a[140:143], a[124:127], v[100:103]\n\t" \
  "v_mfma_f32_16x16x32_bf16 v[116:119], a[156:159], a[124:127], 0\n\t" \
  "v_mfma_f32_16x16x32_bf16 v[116:119], a[152:155], a[108:111], v[116:119]\n\t" \
  "buffer_load_dwordx4 v[56:59], v242, %[ws], s73 offen offset:2048 sc1\n\t" \
  "v_mfma_f32_16x16x32_bf16 v[116:119], a[148:151], a[92:95], v[116:119]\n\t" \
  "v_mfma_f32_16x16x32_bf16 v[116:119], a[144:147], a[76:79], v[116:119]\n\t" \
  "s_waitcnt lgkmcnt(4)\n\t" \
  "v_mfma_f32_16x16x32_bf16 v[104:107], a[128:131], a[208:211], 0\n\t" \
  "v_xor_b32 v236, 0xc000, v234\n\t" \
  "v_mfma_f32_16x16x32_bf16 v[104:107], a[132:135], a[212:215], v[104:107]\n\t" \
  "buffer_load_dwordx4 v[60:63], v242, %[ws], s73 offen offset:3072 sc1\n\t" \
  "s_xor_b32 s73, %[eso], 294912\n\t" \
  "v_mfma_f32_16x16x32_bf16 v[104:107], a[136:139], a[216:219], v[104:107]\n\t" \
  "ds_read_b64_tr_b16 a[160:161], v236 offset:0\n\t" \
  "v_mfma_f32_16x16x32_bf16 v[104:107], a[140:143], a[220:223], v[104:107]\n\t" \
  "ds_read_b64_tr_b16 a[162:163], v236 offset:4096\n\t" \
  "v_mfma_f32_16x16x32_bf16 v[120:123], a[156:159], a[220:223], 0\n\t" \
  "ds_read_b64_tr_b16 a[164:165], v236 offset:8192\n\t" \
  "v_mfma_f32_16x16x32_bf16 v[120:123], a[152:155], a[216:219], v[120:123]\n\t" \
  "ds_read_b64_tr_b16 a[166:167], v236 offset:12288\n\t" \
  "v_mfma_f32_16x16x32_bf16 v[120:123], a[148:151], a[212:215], v[120:123]\n\t" \
  "buffer_load_dwordx4 v[80:83], v242, %[ws], s73 offen offset:0 sc1\n\t" \
  "v_mfma_f32_16x16x32_bf16 v[120:123], a[144:147], a[208:211], v[120:123]\n\t" \
  "s_waitcnt lgkmcnt(4)\n\t" \
  "ds_read_b64_tr_b16 a[168:169], v236 offset:16384\n\t" \
  "v_mfma_f32_16x16x32_bf16 v[108:111], a[128:131], a[192:195], 0\n\t" \
  "ds_read_b64_tr_b16 a[170:171], v236 offset:20480\n\t" \
  "v_mfma_f32_16x16x32_bf16 v[108:111], a[132:135], a[196:199], v[108:111]\n\t" \
  "ds_read_b64_tr_b16 a[172:173], v236 offset:24576\n\t" \
  "v_mfma_f32_16x16x32_bf16 v[108:111], a[136:139], a[200:203], v[108:111]\n\t" \
  "buffer_load_dwordx4 v[84:87], v242, %[ws], s73 offen offset:1024 sc1\n\t" \
  "v_mfma_f32_16x16x32_bf16 v[108:111], a[140:143], a[204:207], v[108:111]\n\t" \
  "ds_read_b64_tr_b16 a[174:175], v236 offset:28672\n\t" \
  "v_xor_b32 v237, 0xc020, v234\n\t" \
  "v_mfma_f32_16x16x32_bf16 v[124:127], a[156:159], a[204:207], 0\n\t" \
  "ds_read_b64_tr_b16 a[176:177], v237 offset:0\n\t" \
  "v_mfma_f32_16x16x32_bf16 v[124:127], a[152:155], a[200:203], v[124:127]\n\t" \
  "ds_read_b64_tr_b16 a[178:179], v237 offset:4096\n\t" \
  "v_mfma_f32_16x16x32_bf16 v[124:127], a[148:151], a[196:199], v[124:127]\n\t" \
  "buffer_load_dwordx4 v[88:91], v242, %[ws], s73 offen offset:2048 sc1\n\t" \
  "v_mfma_f32_16x16x32_bf16 v[124:127], a[144:147], a[192:195], v[124:127]\n\t" \
  "s_waitcnt vmcnt(11) lgkmcnt(6)\n\t" \
  "ds_read_b64_tr_b16 a[180:181], v237 offset:8192\n\t" \
  "ds_read_b64_tr_b16 a[182:183], v237 offset:12288\n\t" \
  "buffer_load_dword v246, v245, %[ws], %[fw2] offen sc1\n\t" \
  STP("v_writelane_b32 %[stv], s94, 24\n\t") \
  STP("v_writelane_b32 %[stv], s92, 25\n\t") \
  STP("s_memrealtime s[94:95]\n\t") \
  STP("s_memtime s[92:93]\n\t") \
  "v_mfma_f32_16x16x32_bf16 a[0:3], a[160:163], v[16:19], a[0:3]\n\t" \
  "ds_read_b64_tr_b16 a[184:185], v237 offset:16384\n\t" \
  "v_mfma_f32_16x16x32_bf16 a[0:3], a[164:167], v[20:23], a[0:3]\n\t" \
  "s_waitcnt lgkmcnt(5)\n\t" \
  "ds_read_b64_tr_b16 a[186:187], v237 offset:20480\n\t" \
  "v_mfma_f32_16x16x32_bf16 a[0:3], a[168:171], v[24:27], a[0:3]\n\t" \
  "ds_read_b64_tr_b16 a[188:189], v237 offset:24576\n\t" \
  "v_mfma_f32_16x16x32_bf16 a[0:3], a[172:175], v[28:31], a[0:3]\n\t" \
  "s_waitcnt vmcnt(8)\n\t" \
  "ds_read_b64_tr_b16 a[190:191], v237 offset:28672\n\t" \
  "v_xor_b32 v235, 0xc040, v234\n\t" \
  "v_mfma_f32_16x16x32_bf16 a[4:7], a[172:175], v[12:15], a[4:7]\n\t" \
  "buffer_load_dwordx4 v[92:95], v242, %[ws], s73 offen offset:3072 sc1\n\t" \
  "v_mfma_f32_16x16x32_bf16 a[4:7], a[168:171], v[8:11], a[4:7]\n\t" \
  "ds_read_b64_tr_b16 a[128:129], v235 offset:0\n\t" \
  "ds_read_b64_tr_b16 a[130:131], v235 offset:4096\n\t" \
  "v_mfma_f32_16x16x32_bf16 a[4:7], a[164:167], v[4:7], a[4:7]\n\t" \
  "ds_read_b64_tr_b16 a[132:133], v235 offset:8192\n\t" \
  "v_mfma_f32_16x16x32_bf16 a[4:7], a[160:163], v[0:3], a[4:7]\n\t" \
  "s_waitcnt lgkmcnt(7)\n\t" \
  "ds_read_b64_tr_b16 a[134:135], v235 offset:12288\n\t" \
  "v_mfma_f32_16x16x32_bf16 a[16:19], a[176:179], v[16:19], a[16:19]\n\t" \
  "buffer_load_dwordx4 v240, %[wo], s71 offen offset:0 lds\n\t" \
  "v_mfma_f32_16x16x32_bf16 a[16:19], a[180:183], v[20:23], a[16:19]\n\t" \
  "s_waitcnt lgkmcnt(4)\n\t" \
  "ds_read_b64_tr_b16 a[136:137], v235 offset:16384\n\t" \
  "ds_read_b64_tr_b16 a[138:139], v235 offset:20480\n\t" \
  "v_mfma_f32_16x16x32_bf16 a[16:19], a[184:187], v[24:27], a[16:19]\n\t" \
  "ds_read_b64_tr_b16 a[140:141], v235 offset:24576\n\t" \
  "v_mfma_f32_16x16x32_bf16 a[16:19], a[188:191], v[28:31], a[16:19]\n\t" \
  "ds_read_b64_tr_b16 a[142:143], v235 offset:28672\n\t" \
  "v_xor_b32 v236, 0xc060, v234\n\t" \
  "v_mfma_f32_16x16x32_bf16 a[20:23], a[188:191], v[12:15], a[20:23]\n\t" \
  "ds_read_b64_tr_b16 a[160:161], v236 offset:0\n\t" \
  "v_mfma_f32_16x16x32_bf16 a[20:23], a[184:187], v[8:11], a[20:23]\n\t" \
  "buffer_load_dwordx4 v241, %[wo], s71 offen offset:1024 lds\n\t" \
  "v_mfma_f32_16x16x32_bf16 a[20:23], a[180:183], v[4:7], a[20:23]\n\t" \
  "ds_read_b64_tr_b16 a[162:163], v236 offset:4096\n\t" \
  "ds_read_b64_tr_b16 a[164:165], v236 offset:8192\n\t" \
  "v_mfma_f32_16x16x32_bf16 a[20:23], a[176:179], v[0:3], a[20:23]\n\t" \
  "s_waitcnt lgkmcnt(7)\n\t" \
  "ds_read_b64_tr_b16 a[166:167], v236 offset:12288\n\t" \
  "v_mfma_f32_16x16x32_bf16 a[32:35], a[128:131], v[16:19], a[32:35]\n\t" \
  "s_xor_b32 s73, %[eso], 327680\n\t" \
  "ds_read_b64_tr_b16 a[168:169], v236 offset:16384\n\t" \
  "v_mfma_f32_16x16x32_bf16 a[32:35], a[132:135], v[20:23], a[32:35]\n\t" \
  "s_waitcnt lgkmcnt(5)\n\t" \
  "buffer_load_dwordx4 v238, %[wo], s71 offen offset:2048 lds\n\t" \
  "v_mfma_f32_16x16x32_bf16 a[32:35], a[136:139], v[24:27], a[32:35]\n\t" \
  "ds_read_b64_tr_b16 a[170:171], v236 offset:20480\n\t" \
  "ds_read_b64_tr_b16 a[172:173], v236 offset:24576\n\t" \
  "v_mfma_f32_16x16x32_bf16 a[32:35], a[140:143], v[28:31], a[32:35]\n\t" \
  "ds_read_b64_tr_b16 a[174:175], v236 offset:28672\n\t" \
  "v_xor_b32 v237, 0xc080, v234\n\t" \
  "v_mfma_f32_16x16x32_bf16 a[36:39], a[140:143], v[12:15], a[36:39]\n\t" \
  "ds_read_b64_tr_b16 a[176:177], v237 offset:0\n\t" \
  "v_mfma_f32_16x16x32_bf16 a[36:39], a[136:139], v[8:11], a[36:39]\n\t" \
  "ds_read_b64_tr_b16 a[178:179], v237 offset:4096\n\t" \
  "v_mfma_f32_16x16x32_bf16 a[36:39], a[132:135], v[4:7], a[36:39]\n\t" \
  "buffer_load_dwordx4 v239, %[wo], s71 offen offset:3072 lds\n\t" \
  "s_add_u32 m0, %[m], 16384\n\t" \
  "v_mfma_f32_16x16x32_bf16 a[36:39], a[128:131], v[0:3], a[36:39]\n\t" \
  "s_waitcnt lgkmcnt(6)\n\t" \
  "s_xor_b32 s71, %[wob], 1179648\n\t" \
  "ds_read_b64_tr_b16 a[180:181], v237 offset:8192\n\t" \
  "v_mfma_f32_16x16x32_bf16 a[48:51], a[160:163], v[16:19], a[48:51]\n\t" \
  ".Lhs11:\n\ts_cbranch_execz .Lhs11n\n\t" \
  "ds_read_b64_tr_b16 a[182:183], v237 offset:12288\n\t" \
  "ds_read_b64_tr_b16 a[184:185], v237 offset:16384\n\t" \
  "v_mfma_f32_16x16x32_bf16 a[48:51], a[164:167], v[20:23], a[48:51]\n\t" \
  "s_waitcnt lgkmcnt(5)\n\t" \
  "ds_read_b64_tr_b16 a[186:187], v237 offset:20480\n\t" \
  "s_waitcnt vmcnt(5)\n\t" \
  "v_cmp_ne_u32 vcc, %[tag], v246\n\t" \
  "s_and_b32 vcc_lo, vcc_lo, 0xe00\n\t" \
  "s_cbranch_scc1 .Lop_s1\n\t" \
  ".Lop_k1:\n\t" \
  "v_mfma_f32_16x16x32_bf16 a[48:51], a[168:171], v[24:27], a[48:51]\n\t" \
  "buffer_load_dwordx4 v[64:67], v242, %[ws], s73 offen offset:0 sc1\n\t" \
  "v_mfma_f32_16x16x32_bf16 a[48:51], a[172:175], v[28:31], a[48:51]\n\t" \
  "ds_read_b64_tr_b16 a[188:189], v237 offset:24576\n\t" \
  "ds_read_b64_tr_b16 a[190:191], v237 offset:28672\n\t" \
  "v_mfma_f32_16x16x32_bf16 a[52:55], a[172:175], v[12:15], a[52:55]\n\t" \
  "v_xor_b32 v235, 0xc0a0, v234\n\t" \
  "v_mfma_f32_16x16x32_bf16 a[52:55], a[168:171], v[8:11], a[52:55]\n\t" \
  "ds_read_b64_tr_b16 a[144:145], v235 offset:0\n\t" \
  "ds_read_b64_tr_b16 a[146:147], v235 offset:4096\n\t" \
  "v_mfma_f32_16x16x32_bf16 a[52:55], a[164:167], v[4:7], a[52:55]\n\t" \
  "buffer_load_dwordx4 v[68:71], v242, %[ws], s73 offen offset:1024 sc1\n\t" \
  "v_mfma_f32_16x16x32_bf16 a[52:55], a[160:163], v[0:3], a[52:55]\n\t" \
  "s_waitcnt lgkmcnt(6)\n\t" \
  "ds_read_b64_tr_b16 a[148:149], v235 offset:8192\n\t" \
  "ds_read_b64_tr_b16 a[150:151], v235 offset:12288\n\t" \
  "v_mfma_f32_16x16x32_bf16 a[64:67], a[176:179], v[16:19], a[64:67]\n\t" \
  "ds_read_b64_tr_b16 a[152:153], v235 offset:16384\n\t" \
  "v_mfma_f32_16x16x32_bf16 a[64:67], a[180:183], v[20:23], a[64:67]\n\t" \
  "s_waitcnt lgkmcnt(5)\n\t" \
  "ds_read_b64_tr_b16 a[154:155], v235 offset:20480\n\t" \
  "v_mfma_f32_16x16x32_bf16 a[64:67], a[184:187], v[24:27], a[64:67]\n\t" \
  "ds_read_b64_tr_b16 a[156:157], v235 offset:24576\n\t" \
  "v_mfma_f32_16x16x32_bf16 a[64:67], a[188:191], v[28:31], a[64:67]\n\t" \
  "buffer_load_dwordx4 v[72:75], v242, %[ws], s73 offen offset:2048 sc1\n\t" \
  "v_mfma_f32_16x16x32_bf16 a[68:71], a[188:191], v[12:15], a[68:71]\n\t" \
  "ds_read_b64_tr_b16 a[158:159], v235 offset:28672\n\t" \
  "v_xor_b32 v236, 0xc0c0, v234\n\t" \
  "v_mfma_f32_16x16x32_bf16 a[68:71], a[184:187], v[8:11], a[68:71]\n\t" \
  "ds_read_b64_tr_b16 a[128:129], v236 offset:0\n\t" \
  "ds_read_b64_tr_b16 a[130:131], v236 offset:4096\n\t" \
  "v_mfma_f32_16x16x32_bf16 a[68:71], a[180:183], v[4:7], a[68:71]\n\t" \
  "ds_read_b64_tr_b16 a[132:133], v236 offset:8192\n\t" \
  "v_mfma_f32_16x16x32_bf16 a[68:71], a[176:179], v[0:3], a[68:71]\n\t" \
  "s_waitcnt lgkmcnt(7)\n\t" \
  "s_barrier\n\t" \
  "v_mfma_f32_16x16x32_bf16 a[80:83], a[144:147], v[16:19], a[80:83]\n\t" \
  "buffer_load_dwordx4 v[76:79], v242, %[ws], s73 offen offset:3072 sc1\n\t" \
  "v_mfma_f32_16x16x32_bf16 a[80:83], a[148:151], v[20:23], a[80:83]\n\t" \
  "s_waitcnt lgkmcnt(3)\n\t" \
  "ds_read_b64_tr_b16 a[134:135], v236 offset:12288\n\t" \
  "ds_read_b64_tr_b16 a[136:137], v236 offset:16384\n\t" \
  "v_mfma_f32_16x16x32_bf16 a[80:83], a[152:155], v[24:27], a[80:83]\n\t" \
  "ds_read_b64_tr_b16 a[138:139], v236 offset:20480\n\t" \
  "ds_read_b64_tr_b16 a[140:141], v236 offset:24576\n\t" \
  "v_mfma_f32_16x16x32_bf16 a[80:83], a[156:159], v[28:31], a[80:83]\n\t" \
  "ds_read_b64_tr_b16 a[142:143], v236 offset:28672\n\t" \
  "v_xor_b32 v237, 0xc0e0, v234\n\t" \
  "v_mfma_f32_16x16x32_bf16 a[84:87], a[156:159], v[12:15], a[84:87]\n\t" \
  "buffer_load_dwordx4 v240, %[wo], s71 offen offset:0 lds\n\t" \
  "v_mfma_f32_16x16x32_bf16 a[84:87], a[152:155], v[8:11], a[84:87]\n\t" \
  "ds_read_b64_tr_b16 a[12:13], v237 offset:0\n\t" \
  "ds_read_b64_tr_b16 a[14:15], v237 offset:4096\n\t" \
  "v_mfma_f32_16x16x32_bf16 a[84:87], a[148:151], v[4:7], a[84:87]\n\t" \
  "ds_read_b64_tr_b16 a[28:29], v237 offset:8192\n\t" \
  "v_mfma_f32_16x16x32_bf16 a[84:87], a[144:147], v[0:3], a[84:87]\n\t" \
  "s_waitcnt lgkmcnt(7)\n\t" \
  "ds_read_b64_tr_b16 a[30:31], v237 offset:12288\n\t" \
  "v_mfma_f32_16x16x32_bf16 a[96:99], a[128:131], v[16:19], a[96:99]\n\t" \
  "buffer_load_dwordx4 v241, %[wo], s71 offen offset:1024 lds\n\t" \
  "v_mfma_f32_16x16x32_bf16 a[96:99], a[132:135], v[20:23], a[96:99]\n\t" \
  "s_waitcnt lgkmcnt(4)\n\t" \
  "ds_read_b64_tr_b16 a[44:45], v237 offset:16384\n\t" \
  "ds_read_b64_tr_b16 a[46:47], v237 offset:20480\n\t" \
  "v_mfma_f32_16x16x32_bf16 a[96:99], a[136:139], v[24:27], a[96:99]\n\t" \
  "ds_read_b64_tr_b16 a[60:61], v237 offset:24576\n\t" \
  "v_lshrrev_b32 v244, 4, v242\n\t" \
  "v_mfma_f32_16x16x32_bf16 a[96:99], a[140:143], v[28:31], a[96:99]\n\t" \
  "s_lshl_b32 s77, %[hl], 2\n\t" \
  "ds_read_b64_tr_b16 a[62:63], v237 offset:28672\n\t" \
  "v_xor_b32 v235, 0x14000, v234\n\t" \
  "v_mfma_f32_16x16x32_bf16 a[100:103], a[140:143], v[12:15], a[100:103]\n\t" \
  "buffer_load_dwordx4 v238, %[wo], s71 offen offset:2048 lds\n\t" \
  "v_mfma_f32_16x16x32_bf16 a[100:103], a[136:139], v[8:11], a[100:103]\n\t" \
  "s_add_u32 s77, s77, %[wid]\n\t" \
  "ds_read_b64_tr_b16 a[160:161], v235 offset:0\n\t" \
  "ds_read_b64_tr_b16 a[162:163], v235 offset:4096\n\t" \
  "v_mfma_f32_16x16x32_bf16 a[100:103], a[132:135], v[4:7], a[100:103]\n\t" \
  "s_lshl_b32 s77, s77, 6\n\t" \
  "ds_read_b64_tr_b16 a[164:165], v235 offset:8192\n\t" \
  "v_add_u32 v230, 0x10000, v242\n\t" \
  "v_mfma_f32_16x16x32_bf16 a[100:103], a[128:131], v[0:3], a[100:103]\n\t" \
  "s_waitcnt lgkmcnt(7)\n\t" \
  "ds_read_b64_tr_b16 a[166:167], v235 offset:12288\n\t" \
  "v_xor_b32 v236, 0x24000, v234\n\t" \
  "v_mfma_f32_16x16x32_bf16 a[112:115], a[12:15], v[16:19], a[112:115]\n\t" \
  "ds_read_b64_tr_b16 a[168:169], v236 offset:0\n\t" \
  "v_add_u32 v232, 0x20000, v242\n\t" \
  "v_mfma_f32_16x16x32_bf16 a[112:115], a[28:31], v[20:23], a[112:115]\n\t" \
  "s_waitcnt lgkmcnt(5)\n\t" \
  "buffer_load_dwordx4 v239, %[wo], s71 offen offset:3072 lds\n\t" \
  "s_add_u32 m0, %[m], 98304\n\t" \
  "v_mfma_f32_16x16x32_bf16 a[112:115], a[44:47], v[24:27], a[112:115]\n\t" \
  "s_xor_b32 s71, %[wob], 1310720\n\t" \
  "ds_read_b64_tr_b16 a[170:171], v236 offset:4096\n\t" \
  "ds_read_b64_tr_b16 a[172:173], v236 offset:8192\n\t" \
  "v_mfma_f32_16x16x32_bf16 a[112:115], a[60:63], v[28:31], a[112:115]\n\t" \
  "s_xor_b32 s73, %[eso], 393216\n\t" \
  "ds_read_b64_tr_b16 a[174:175], v236 offset:12288\n\t" \
  "s_add_u32 s70, %[m], 147456\n\t" \
  "v_mfma_f32_16x16x32_bf16 a[116:119], a[60:63], v[12:15], a[116:119]\n\t" \
  "s_lshl_b32 s69, s77, 2\n\t" \
  "v_xor_b32 v237, 0x14020, v234\n\t" \
  "s_add_u32 s69, s69, 2228224\n\t" \
  "v_mfma_f32_16x16x32_bf16 a[116:119], a[44:47], v[8:11], a[116:119]\n\t" \
  "buffer_load_dwordx4 v240, %[wo], s71 offen offset:0 lds\n\t" \
  "v_mfma_f32_16x16x32_bf16 a[116:119], a[28:31], v[4:7], a[116:119]\n\t" \
  "s_bfe_u32 s68, %[eso], 0x2000f\n\t" \
  "ds_read_b64_tr_b16 a[176:177], v237 offset:0\n\t" \
  "v_mfma_f32_16x16x32_bf16 a[116:119], a[12:15], v[0:3], a[116:119]\n\t" \
  "s_waitcnt lgkmcnt(5)\n\t" \
  "ds_read_b64_tr_b16 a[178:179], v237 offset:4096\n\t" \
  "s_lshl_b32 s60, s68, 17\n\t" \
  STP("v_writelane_b32 %[stv], s94, 26\n\t") \
  STP("v_writelane_b32 %[stv], s92, 27\n\t") \
  STP("s_memrealtime s[94:95]\n\t") \
  STP("s_memtime s[92:93]\n\t") \
  "v_mfma_f32_16x16x32_bf16 a[0:3], a[160:163], v[48:51], a[0:3]\n\t" \
  "ds_read_b64_tr_b16 a[180:181], v237 offset:8192\n\t" \
  "v_mfma_f32_16x16x32_bf16 a[0:3], a[164:167], v[52:55], a[0:3]\n\t" \
  "s_waitcnt lgkmcnt(3)\n\t" \
  "ds_read_b64_tr_b16 a[182:183], v237 offset:12288\n\t" \
  "v_xor_b32 v235, 0x24020, v234\n\t" \
  "v_mfma_f32_16x16x32_bf16 a[0:3], a[168:171], v[56:59], a[0:3]\n\t" \
  "buffer_load_dwordx4 v[16:19], v242, %[ws], s73 offen offset:0 sc1\n\t" \
  "v_mfma_f32_16x16x32_bf16 a[0:3], a[172:175], v[60:63], a[0:3]\n\t" \
  "s_waitcnt vmcnt(14)\n\t" \
  "ds_read_b64_tr_b16 a[184:185], v235 offset:0\n\t" \
  "s_add_u32 s60, s60, %[xcb]\n\t" \
  "v_mfma_f32_16x16x32_bf16 a[4:7], a[172:175], v[92:95], a[4:7]\n\t" \
  "s_lshl_b32 s63, %[wid], 10\n\t" \
  "ds_read_b64_tr_b16 a[186:187], v235 offset:4096\n\t" \
  "v_and_b32 v250, 7, v244\n\t" \
  "v_mfma_f32_16x16x32_bf16 a[4:7], a[168:171], v[88:91], a[4:7]\n\t" \
  "s_add_u32 s60, s60, s63\n\t" \
  "ds_read_b64_tr_b16 a[188:189], v235 offset:8192\n\t" \
  "v_lshlrev_b32 v250, 5, v250\n\t" \
  "v_mfma_f32_16x16x32_bf16 a[4:7], a[164:167], v[84:87], a[4:7]\n\t" \
  "buffer_load_dwordx4 v[20:23], v242, %[ws], s73 offen offset:1024 sc1\n\t" \
  "v_mfma_f32_16x16x32_bf16 a[4:7], a[160:163], v[80:83], a[4:7]\n\t" \
  "s_waitcnt vmcnt(7)\n\t" \
  "ds_read_b64_tr_b16 a[190:191], v235 offset:12288\n\t" \
  "v_lshrrev_b32 v251, 4, v244\n\t" \
  "v_mfma_f32_16x16x32_bf16 a[8:11], a[160:163], v[64:67], a[8:11]\n\t" \
  "s_xor_b32 s61, s60, 135168\n\t" \
  "v_xor_b32 v236, 0x14040, v234\n\t" \
  "v_lshl_add_u32 v250, v251, 3, v250\n\t" \
  "v_mfma_f32_16x16x32_bf16 a[8:11], a[164:167], v[68:71], a[8:11]\n\t" \
  "ds_read_b64_tr_b16 a[144:145], v236 offset:0\n\t" \
  "v_mfma_f32_16x16x32_bf16 a[8:11], a[168:171], v[72:75], a[8:11]\n\t" \
  "buffer_load_dwordx4 v[24:27], v242, %[ws], s73 offen offset:2048 sc1\n\t" \
  "v_mfma_f32_16x16x32_bf16 a[8:11], a[172:175], v[76:79], a[8:11]\n\t" \
  "s_waitcnt lgkmcnt(5)\n\t" \
  "ds_read_b64_tr_b16 a[146:147], v236 offset:4096\n\t" \
  "s_xor_b32 s62, s60, 278528\n\t" \
  "v_mfma_f32_16x16x32_bf16 a[16:19], a[176:179], v[48:51], a[16:19]\n\t" \
  "s_lshl_b32 s65, s68, 7\n\t" \
  "ds_read_b64_tr_b16 a[148:149], v236 offset:8192\n\t" \
  "v_mfma_f32_16x16x32_bf16 a[16:19], a[180:183], v[52:55], a[16:19]\n\t" \
  "s_waitcnt lgkmcnt(3)\n\t" \
  "ds_read_b64_tr_b16 a[150:151], v236 offset:12288\n\t" \
  "v_xor_b32 v237, 0x24040, v234\n\t" \
  "v_mfma_f32_16x16x32_bf16 a[16:19], a[184:187], v[56:59], a[16:19]\n\t" \
  "s_add_u32 s65, s65, %[frb]\n\t" \
  "ds_read_b64_tr_b16 a[152:153], v237 offset:0\n\t" \
  "v_mfma_f32_16x16x32_bf16 a[16:19], a[188:191], v[60:63], a[16:19]\n\t" \
  "buffer_load_dwordx4 v[28:31], v242, %[ws], s73 offen offset:3072 sc1\n\t" \
  "s_xor_b32 s73, %[eso], 425984\n\t" \
  "v_mfma_f32_16x16x32_bf16 a[20:23], a[188:191], v[92:95], a[20:23]\n\t" \
  "s_xor_b32 s66, s65, 144\n\t" \
  "ds_read_b64_tr_b16 a[154:155], v237 offset:4096\n\t" \
  "v_mfma_f32_16x16x32_bf16 a[20:23], a[184:187], v[88:91], a[20:23]\n\t" \
  "ds_read_b64_tr_b16 a[156:157], v237 offset:8192\n\t" \
  "v_mfma_f32_16x16x32_bf16 a[20:23], a[180:183], v[84:87], a[20:23]\n\t" \
  "ds_read_b64_tr_b16 a[158:159], v237 offset:12288\n\t" \
  "v_mfma_f32_16x16x32_bf16 a[20:23], a[176:179], v[80:83], a[20:23]\n\t" \
  "buffer_load_dwordx4 v[0:3], v242, %[ws], s73 offen offset:0 sc1\n\t" \
  "v_mfma_f32_16x16x32_bf16 a[24:27], a[176:179], v[64:67], a[24:27]\n\t" \
  "s_xor_b32 s67, s65, 288\n\t" \
  "v_xor_b32 v235, 0x14060, v234\n\t" \
  "v_mfma_f32_16x16x32_bf16 a[24:27], a[180:183], v[68:71], a[24:27]\n\t" \
  "s_xor_b32 s68, s65, 384\n\t" \
  "ds_read_b64_tr_b16 a[128:129], v235 offset:0\n\t" \
  "v_mfma_f32_16x16x32_bf16 a[24:27], a[184:187], v[72:75], a[24:27]\n\t" \
  "s_lshr_b32 s75, %[m], 4\n\t" \
  "s_bitset1_b32 s75, 17\n\t" \
  "v_add_u32 v250, s75, v250\n\t" \
  "v_mfma_f32_16x16x32_bf16 a[24:27], a[188:191], v[76:79], a[24:27]\n\t" \
  "s_waitcnt lgkmcnt(5)\n\t" \
  "ds_read_b64_tr_b16 a[130:131], v235 offset:4096\n\t" \
  "s_andn2_b32 s68, s68, 12\n\t" \
  "v_mfma_f32_16x16x32_bf16 a[32:35], a[144:147], v[48:51], a[32:35]\n\t" \
  "buffer_load_dwordx4 v[4:7], v242, %[ws], s73 offen offset:1024 sc1\n\t" \
  "v_mfma_f32_16x16x32_bf16 a[32:35], a[148:151], v[52:55], a[32:35]\n\t" \
  "s_waitcnt lgkmcnt(2)\n\t" \
  "ds_read_b64_tr_b16 a[132:133], v235 offset:8192\n\t" \
  "s_andn2_b32 s64, 0x2000, %[na]\n\t" \
  "v_mfma_f32_16x16x32_bf16 a[32:35], a[152:155], v[56:59], a[32:35]\n\t" \
  "v_permlane32_swap_b32 v132, v134\n\t" \
  "v_mfma_f32_16x16x32_bf16 a[32:35], a[156:159], v[60:63], a[32:35]\n\t" \
  "ds_read_b64_tr_b16 a[134:135], v235 offset:12288\n\t" \
  "v_xor_b32 v236, 0x24060, v234\n\t" \
  "v_mfma_f32_16x16x32_bf16 a[36:39], a[156:159], v[92:95], a[36:39]\n\t" \
  "buffer_load_dwordx4 v[8:11], v242, %[ws], s73 offen offset:2048 sc1\n\t" \
  "v_mfma_f32_16x16x32_bf16 a[36:39], a[152:155], v[88:91], a[36:39]\n\t" \
  "s_xor_b32 s63, s60, 393216\n\t" \
  "ds_read_b64_tr_b16 a[136:137], v236 offset:0\n\t" \
  "ds_read_b64_tr_b16 a[138:139], v236 offset:4096\n\t" \
  "v_mfma_f32_16x16x32_bf16 a[36:39], a[148:151], v[84:87], a[36:39]\n\t" \
  "s_andn2_b32 s63, s63, 98304\n\t" \
  "v_permlane32_swap_b32 v128, v130\n\t" \
  "v_mfma_f32_16x16x32_bf16 a[36:39], a[144:147], v[80:83], a[36:39]\n\t" \
  "ds_read_b64_tr_b16 a[140:141], v236 offset:8192\n\t" \
  "ds_read_b64_tr_b16 a[142:143], v236 offset:12288\n\t" \
  "v_mfma_f32_16x16x32_bf16 a[40:43], a[144:147], v[64:67], a[40:43]\n\t" \
  "buffer_load_dwordx4 v[12:15], v242, %[ws], s73 offen offset:3072 sc1\n\t" \
  "s_xor_b32 s73, %[eso], 458752\n\t" \
  "v_mfma_f32_16x16x32_bf16 a[40:43], a[148:151], v[68:71], a[40:43]\n\t" \
  "v_permlane32_swap_b32 v133, v135\n\t" \
  "v_mfma_f32_16x16x32_bf16 a[40:43], a[152:155], v[72:75], a[40:43]\n\t" \
  "v_permlane32_swap_b32 v129, v131\n\t" \
  "v_mfma_f32_16x16x32_bf16 a[40:43], a[156:159], v[76:79], a[40:43]\n\t" \
  "s_waitcnt lgkmcnt(4)\n\t" \
  "v_xor_b32 v237, 0x14080, v234\n\t" \
  "v_add_f32 v132, v132, v134\n\t" \
  "v_mfma_f32_16x16x32_bf16 a[48:51], a[128:131], v[48:51], a[48:51]\n\t" \
  "ds_read_b64_tr_b16 a[76:77], v237 offset:0\n\t" \
  "v_add_f32 v128, v128, v130\n\t" \
  "v_mfma_f32_16x16x32_bf16 a[48:51], a[132:135], v[52:55], a[48:51]\n\t" \
  "s_waitcnt lgkmcnt(1)\n\t" \
  "buffer_load_dwordx4 v[32:35], v242, %[ws], s73 offen offset:0 sc1\n\t" \
  "v_mfma_f32_16x16x32_bf16 a[48:51], a[136:139], v[56:59], a[48:51]\n\t" \
  "ds_read_b64_tr_b16 a[78:79], v237 offset:4096\n\t" \
  "ds_read_b64_tr_b16 a[92:93], v237 offset:8192\n\t" \
  "v_mfma_f32_16x16x32_bf16 a[48:51], a[140:143], v[60:63], a[48:51]\n\t" \
  "s_barrier\n\t" \
  "v_mfma_f32_16x16x32_bf16 a[52:55], a[140:143], v[92:95], a[52:55]\n\t" \
  "ds_read_b64_tr_b16 a[94:95], v237 offset:12288\n\t" \
  "v_xor_b32 v235, 0x24080, v234\n\t" \
  "v_mfma_f32_16x16x32_bf16 a[52:55], a[136:139], v[88:91], a[52:55]\n\t" \
  "buffer_load_dwordx4 v[36:39], v242, %[ws], s73 offen offset:1024 sc1\n\t" \
  "buffer_load_dword v246, v245, %[ws], %[fw2] offen sc1\n\t" \
  "v_mfma_f32_16x16x32_bf16 a[52:55], a[132:135], v[84:87], a[52:55]\n\t" \
  "ds_read_b64_tr_b16 a[108:109], v235 offset:0\n\t" \
  "ds_read_b64_tr_b16 a[110:111], v235 offset:4096\n\t" \
  "v_add_f32 v133, v133, v135\n\t" \
  "v_mfma_f32_16x16x32_bf16 a[52:55], a[128:131], v[80:83], a[52:55]\n\t" \
  "ds_read_b64_tr_b16 a[124:125], v235 offset:8192\n\t" \
  "ds_read_b64_tr_b16 a[126:127], v235 offset:12288\n\t" \
  "v_mfma_f32_16x16x32_bf16 a[56:59], a[128:131], v[64:67], a[56:59]\n\t" \
  "s_bitcmp1_b32 %[wid], 0\n\t" \
  "s_cselect_b32 s72, s64, 0\n\t" \
  "s_xor_b32 s63, s63, s72\n\t" \
  "v_mfma_f32_16x16x32_bf16 a[56:59], a[132:135], v[68:71], a[56:59]\n\t" \
  "s_xor_b32 s64, s64, s63\n\t" \
  "v_add_f32 v129, v129, v131\n\t" \
  "v_xor_b32 v236, 0x140a0, v234\n\t" \
  "v_mfma_f32_16x16x32_bf16 a[56:59], a[136:139], v[72:75], a[56:59]\n\t" \
  "ds_read_b64_tr_b16 a[144:145], v236 offset:0\n\t" \
  "v_mfma_f32_16x16x32_bf16 a[56:59], a[140:143], v[76:79], a[56:59]\n\t" \
  "s_waitcnt lgkmcnt(5)\n\t" \
  "buffer_load_dwordx4 v[40:43], v242, %[ws], s73 offen offset:2048 sc1\n\t" \
  "v_mfma_f32_16x16x32_bf16 a[64:67], a[76:79], v[48:51], a[64:67]\n\t" \
  "ds_read_b64_tr_b16 a[146:147], v236 offset:4096\n\t" \
  "v_permlane16_swap_b32 v132, v133\n\t" \
  "v_mfma_f32_16x16x32_bf16 a[64:67], a[92:95], v[52:55], a[64:67]\n\t" \
  "s_waitcnt lgkmcnt(2)\n\t" \
  "ds_read_b64_tr_b16 a[148:149], v236 offset:8192\n\t" \
  "v_mfma_f32_16x16x32_bf16 a[64:67], a[108:111], v[56:59], a[64:67]\n\t" \
  "ds_read_b64_tr_b16 a[150:151], v236 offset:12288\n\t" \
  "v_xor_b32 v237, 0x240a0, v234\n\t" \
  "v_mfma_f32_16x16x32_bf16 a[64:67], a[124:127], v[60:63], a[64:67]\n\t" \
  "buffer_load_dwordx4 v[44:47], v242, %[ws], s73 offen offset:3072 sc1\n\t" \
  "v_mfma_f32_16x16x32_bf16 a[68:71], a[124:127], v[92:95], a[68:71]\n\t" \
  "ds_read_b64_tr_b16 a[152:153], v237 offset:0\n\t" \
  "v_permlane16_swap_b32 v128, v129\n\t" \
  "v_mfma_f32_16x16x32_bf16 a[68:71], a[108:111], v[88:91], a[68:71]\n\t" \
  "ds_read_b64_tr_b16 a[154:155], v237 offset:4096\n\t" \
  "v_add_f32 v132, v132, v133\n\t" \
  "v_mfma_f32_16x16x32_bf16 a[68:71], a[92:95], v[84:87], a[68:71]\n\t" \
  "ds_read_b64_tr_b16 a[156:157], v237 offset:8192\n\t" \
  "v_add_f32 v128, v128, v129\n\t" \
  "v_mfma_f32_16x16x32_bf16 a[68:71], a[76:79], v[80:83], a[68:71]\n\t" \
  "ds_read_b64_tr_b16 a[158:159], v237 offset:12288\n\t" \
  "v_add_f32_dpp v248, v132, v132 row_ror:8 row_mask:0xf bank_mask:0xf\n\t" \
  "v_mfma_f32_16x16x32_bf16 a[72:75], a[76:79], v[64:67], a[72:75]\n\t" \
  "buffer_load_dwordx4 v241, %[wo], s71 offen offset:1024 lds\n\t" \
  "v_mfma_f32_16x16x32_bf16 a[72:75], a[92:95], v[68:71], a[72:75]\n\t" \
  "v_xor_b32 v235, 0x140c0, v234\n\t" \
  "v_add_f32_dpp v249, v128, v128 row_ror:8 row_mask:0xf bank_mask:0xf\n\t" \
  "v_mfma_f32_16x16x32_bf16 a[72:75], a[108:111], v[72:75], a[72:75]\n\t" \
  "ds_read_b64_tr_b16 a[12:13], v235 offset:0\n\t" \
  "v_mfma_f32_16x16x32_bf16 a[72:75], a[124:127], v[76:79], a[72:75]\n\t" \
  "s_waitcnt lgkmcnt(5)\n\t" \
  "ds_read_b64_tr_b16 a[14:15], v235 offset:4096\n\t" \
  "v_mfma_f32_16x16x32_bf16 a[80:83], a[144:147], v[48:51], a[80:83]\n\t" \
  "buffer_load_dwordx4 v238, %[wo], s71 offen offset:2048 lds\n\t" \
  "v_mfma_f32_16x16x32_bf16 a[80:83], a[148:151], v[52:55], a[80:83]\n\t" \
  "s_waitcnt lgkmcnt(2)\n\t" \
  "ds_read_b64_tr_b16 a[28:29], v235 offset:8192\n\t" \
  "v_mfma_f32_16x16x32_bf16 a[80:83], a[152:155], v[56:59], a[80:83]\n\t" \
  "s_xor_b32 s74, %[eso], 98304\n\t" \
  "ds_read_b64_tr_b16 a[30:31], v235 offset:12288\n\t" \
  "v_xor_b32 v236, 0x240c0, v234\n\t" \
  "v_mfma_f32_16x16x32_bf16 a[80:83], a[156:159], v[60:63], a[80:83]\n\t" \
  "ds_read_b64_tr_b16 a[44:45], v236 offset:0\n\t" \
  "v_mfma_f32_16x16x32_bf16 a[84:87], a[156:159], v[92:95], a[84:87]\n\t" \
  "ds_read_b64_tr_b16 a[46:47], v236 offset:4096\n\t" \
  "v_mfma_f32_16x16x32_bf16 a[84:87], a[152:155], v[88:91], a[84:87]\n\t" \
  "buffer_load_dwordx4 v239, %[wo], s71 offen offset:3072 lds\n\t" \
  "s_add_u32 m0, %[m], 32768\n\t" \
  "v_mfma_f32_16x16x32_bf16 a[84:87], a[148:151], v[84:87], a[84:87]\n\t" \
  "s_xor_b32 s71, %[wob], 1441792\n\t" \
  "ds_read_b64_tr_b16 a[60:61], v236 offset:8192\n\t" \
  "v_mfma_f32_16x16x32_bf16 a[84:87], a[144:147], v[80:83], a[84:87]\n\t" \
  "buffer_store_dwordx4 v[160:163], v242, %[ws], s74 offen offset:0 sc1\n\t" \
  "v_mfma_f32_16x16x32_bf16 a[88:91], a[144:147], v[64:67], a[88:91]\n\t" \
  "s_xor_b32 s74, %[eso], 98304\n\t" \
  "ds_read_b64_tr_b16 a[62:63], v236 offset:12288\n\t" \
  "v_xor_b32 v237, 0x140e0, v234\n\t" \
  "v_mfma_f32_16x16x32_bf16 a[88:91], a[148:151], v[68:71], a[88:91]\n\t" \
  "ds_read_b64_tr_b16 a[76:77], v237 offset:0\n\t" \
  "v_mfma_f32_16x16x32_bf16 a[88:91], a[152:155], v[72:75], a[88:91]\n\t" \
  "ds_read_b64_tr_b16 a[78:79], v237 offset:4096\n\t" \
  "v_mfma_f32_16x16x32_bf16 a[88:91], a[156:159], v[76:79], a[88:91]\n\t" \
  "s_waitcnt lgkmcnt(6)\n\t" \
  "buffer_load_dwordx4 v240, %[wo], s71 offen offset:0 lds\n\t" \
  "v_mfma_f32_16x16x32_bf16 a[96:99], a[12:15], v[48:51], a[96:99]\n\t" \
  "ds_read_b64_tr_b16 a[92:93], v237 offset:8192\n\t" \
  "v_mfma_f32_16x16x32_bf16 a[96:99], a[28:31], v[52:55], a[96:99]\n\t" \
  "s_waitcnt lgkmcnt(3)\n\t" \
  "buffer_store_dwordx4 v[164:167], v242, %[ws], s74 offen offset:1024 sc1\n\t" \
  "v_mfma_f32_16x16x32_bf16 a[96:99], a[44:47], v[56:59], a[96:99]\n\t" \
  "s_xor_b32 s74, %[eso], 98304\n\t" \
  "ds_read_b64_tr_b16 a[94:95], v237 offset:12288\n\t" \
  "v_xor_b32 v235, 0x240e0, v234\n\t" \
  "v_mfma_f32_16x16x32_bf16 a[96:99], a[60:63], v[60:63], a[96:99]\n\t" \
  "ds_read_b64_tr_b16 a[108:109], v235 offset:0\n\t" \
  "v_mfma_f32_16x16x32_bf16 a[100:103], a[60:63], v[92:95], a[100:103]\n\t" \
  "ds_read_b64_tr_b16 a[110:111], v235 offset:4096\n\t" \
  "v_mfma_f32_16x16x32_bf16 a[100:103], a[44:47], v[88:91], a[100:103]\n\t" \
  "buffer_load_dwordx4 v241, %[wo], s71 offen offset:1024 lds\n\t" \
  "v_mfma_f32_16x16x32_bf16 a[100:103], a[28:31], v[84:87], a[100:103]\n\t" \
  "ds_read_b64_tr_b16 a[124:125], v235 offset:8192\n\t" \
  "v_mfma_f32_16x16x32_bf16 a[100:103], a[12:15], v[80:83], a[100:103]\n\t" \
  "buffer_store_dwordx4 v[168:171], v242, %[ws], s74 offen offset:2048 sc1\n\t" \
  "s_waitcnt vmcnt(10)\n\t" \
  "v_cmp_ne_u32 vcc, %[tag], v246\n\t" \
  "s_and_b32 vcc_lo, vcc_lo, 0xfe00\n\t" \
  "s_cbranch_scc1 .Lop_s2\n\t" \
  ".Lop_k2:\n\t" \
  "v_mfma_f32_16x16x32_bf16 a[104:107], a[12:15], v[64:67], a[104:107]\n\t" \
  "s_xor_b32 s74, %[eso], 98304\n\t" \
  "ds_read_b64_tr_b16 a[126:127], v235 offset:12288\n\t" \
  "v_xor_b32 v236, 0x1c000, v234\n\t" \
  "v_mfma_f32_16x16x32_bf16 a[104:107], a[28:31], v[68:71], a[104:107]\n\t" \
  "ds_read_b64_tr_b16 a[128:129], v236 offset:0\n\t" \
  "v_mfma_f32_16x16x32_bf16 a[104:107], a[44:47], v[72:75], a[104:107]\n\t" \
  "ds_read_b64_tr_b16 a[130:131], v236 offset:4096\n\t" \
  "v_mfma_f32_16x16x32_bf16 a[104:107], a[60:63], v[76:79], a[104:107]\n\t" \
  "s_waitcnt lgkmcnt(6)\n\t" \
  "ds_read_b64_tr_b16 a[132:133], v236 offset:8192\n\t" \
  "v_mfma_f32_16x16x32_bf16 a[112:115], a[76:79], v[48:51], a[112:115]\n\t" \
  "buffer_store_dwordx4 v[172:175], v242, %[ws], s74 offen offset:3072 sc1\n\t" \
  "v_mfma_f32_16x16x32_bf16 a[112:115], a[92:95], v[52:55], a[112:115]\n\t" \
  "s_waitcnt lgkmcnt(3)\n\t" \
  "ds_read_b64_tr_b16 a[134:135], v236 offset:12288\n\t" \
  "v_mfma_f32_16x16x32_bf16 a[112:115], a[108:111], v[56:59], a[112:115]\n\t" \
  "buffer_load_dwordx4 v238, %[wo], s71 offen offset:2048 lds\n\t" \
  "v_mfma_f32_16x16x32_bf16 a[112:115], a[124:127], v[60:63], a[112:115]\n\t" \
  "ds_read_b64_tr_b16 a[136:137], v236 offset:16384\n\t" \
  "v_mfma_f32_16x16x32_bf16 a[116:119], a[124:127], v[92:95], a[116:119]\n\t" \
  "ds_read_b64_tr_b16 a[138:139], v236 offset:20480\n\t" \
  "v_mfma_f32_16x16x32_bf16 a[116:119], a[108:111], v[88:91], a[116:119]\n\t" \
  "ds_read_b64_tr_b16 a[140:141], v236 offset:24576\n\t" \
  "v_mfma_f32_16x16x32_bf16 a[116:119], a[92:95], v[84:87], a[116:119]\n\t" \
  "buffer_load_dwordx4 v239, %[wo], s71 offen offset:3072 lds\n\t" \
  "v_mfma_f32_16x16x32_bf16 a[116:119], a[76:79], v[80:83], a[116:119]\n\t" \
  "s_xor_b32 s73, %[eso], 524288\n\t" \
  "ds_read_b64_tr_b16 a[142:143], v236 offset:28672\n\t" \
  "v_mfma_f32_16x16x32_bf16 a[120:123], a[76:79], v[64:67], a[120:123]\n\t" \
  "v_xor_b32 v237, 0x1c020, v234\n\t" \
  "v_mfma_f32_16x16x32_bf16 a[120:123], a[92:95], v[68:71], a[120:123]\n\t" \
  "ds_read_b64_tr_b16 a[144:145], v237 offset:0\n\t" \
  "v_mfma_f32_16x16x32_bf16 a[120:123], a[108:111], v[72:75], a[120:123]\n\t" \
  "ds_read_b64_tr_b16 a[146:147], v237 offset:4096\n\t" \
  "v_mfma_f32_16x16x32_bf16 a[120:123], a[124:127], v[76:79], a[120:123]\n\t" \
  "s_waitcnt lgkmcnt(6)\n\t" \
  "buffer_load_dwordx4 v[48:51], v242, %[ws], s73 offen offset:0 sc1\n\t" \
  STP("v_writelane_b32 %[stv], s94, 28\n\t") \
  STP("v_writelane_b32 %[stv], s92, 29\n\t") \
  STP("s_memrealtime s[94:95]\n\t") \
  STP("s_memtime s[92:93]\n\t") \
  "v_mfma_f32_16x16x32_bf16 a[0:3], a[128:131], v[16:19], a[0:3]\n\t" \
  "ds_read_b64_tr_b16 a[148:149], v237 offset:8192\n\t" \
  "v_mfma_f32_16x16x32_bf16 a[0:3], a[132:135], v[20:23], a[0:3]\n\t" \
  "s_waitcnt lgkmcnt(3)\n\t" \
  "ds_read_b64_tr_b16 a[150:151], v237 offset:12288\n\t" \
  "v_mfma_f32_16x16x32_bf16 a[0:3], a[136:139], v[24:27], a[0:3]\n\t" \
  "ds_read_b64_tr_b16 a[152:153], v237 offset:16384\n\t" \
  "v_mfma_f32_16x16x32_bf16 a[0:3], a[140:143], v[28:31], a[0:3]\n\t" \
  "buffer_load_dwordx4 v[52:55], v242, %[ws], s73 offen offset:1024 sc1\n\t" \
  "v_mfma_f32_16x16x32_bf16 a[4:7], a[140:143], v[12:15], a[4:7]\n\t" \
  "ds_read_b64_tr_b16 a[154:155], v237 offset:20480\n\t" \
  "v_mfma_f32_16x16x32_bf16 a[4:7], a[136:139], v[8:11], a[4:7]\n\t" \
  "ds_read_b64_tr_b16 a[156:157], v237 offset:24576\n\t" \
  "v_mfma_f32_16x16x32_bf16 a[4:7], a[132:135], v[4:7], a[4:7]\n\t" \
  "ds_read_b64_tr_b16 a[158:159], v237 offset:28672\n\t" \
  "v_mfma_f32_16x16x32_bf16 a[4:7], a[128:131], v[0:3], a[4:7]\n\t" \
  "s_waitcnt vmcnt(13)\n\t" \
  "buffer_load_dwordx4 v[56:59], v242, %[ws], s73 offen offset:2048 sc1\n\t" \
  "v_mfma_f32_16x16x32_bf16 a[8:11], a[128:131], v[32:35], a[8:11]\n\t" \
  "v_xor_b32 v235, 0x1c040, v234\n\t" \
  "v_mfma_f32_16x16x32_bf16 a[8:11], a[132:135], v[36:39], a[8:11]\n\t" \
  "ds_read_b64_tr_b16 a[12:13], v235 offset:0\n\t" \
  "v_mfma_f32_16x16x32_bf16 a[8:11], a[136:139], v[40:43], a[8:11]\n\t" \
  "ds_read_b64_tr_b16 a[14:15], v235 offset:4096\n\t" \
  "v_mfma_f32_16x16x32_bf16 a[8:11], a[140:143], v[44:47], a[8:11]\n\t" \
  "s_waitcnt lgkmcnt(6)\n\t" \
  "ds_read_b64_tr_b16 a[28:29], v235 offset:8192\n\t" \
  "v_mfma_f32_16x16x32_bf16 a[16:19], a[144:147], v[16:19], a[16:19]\n\t" \
  "buffer_load_dwordx4 v[60:63], v242, %[ws], s73 offen offset:3072 sc1\n\t" \
  "s_xor_b32 s73, %[eso], 557056\n\t" \
  "v_mfma_f32_16x16x32_bf16 a[16:19], a[148:151], v[20:23], a[16:19]\n\t" \
  "s_waitcnt lgkmcnt(3)\n\t" \
  "ds_read_b64_tr_b16 a[30:31], v235 offset:12288\n\t" \
  "v_mfma_f32_16x16x32_bf16 a[16:19], a[152:155], v[24:27], a[16:19]\n\t" \
  "ds_read_b64_tr_b16 a[44:45], v235 offset:16384\n\t" \
  "v_mfma_f32_16x16x32_bf16 a[16:19], a[156:159], v[28:31], a[16:19]\n\t" \
  "ds_read_b64_tr_b16 a[46:47], v235 offset:20480\n\t" \
  "v_mfma_f32_16x16x32_bf16 a[20:23], a[156:159], v[12:15], a[20:23]\n\t" \
  "buffer_load_dwordx4 v[80:83], v242, %[ws], s73 offen offset:0 sc1\n\t" \
  "v_mfma_f32_16x16x32_bf16 a[20:23], a[152:155], v[8:11], a[20:23]\n\t" \
  "ds_read_b64_tr_b16 a[60:61], v235 offset:24576\n\t" \
  "v_mfma_f32_16x16x32_bf16 a[20:23], a[148:151], v[4:7], a[20:23]\n\t" \
  "ds_read_b64_tr_b16 a[62:63], v235 offset:28672\n\t" \
  "v_mfma_f32_16x16x32_bf16 a[20:23], a[144:147], v[0:3], a[20:23]\n\t" \
  "v_mfma_f32_16x16x32_bf16 a[24:27], a[144:147], v[32:35], a[24:27]\n\t" \
  "v_xor_b32 v236, 0x1c060, v234\n\t" \
  "v_mfma_f32_16x16x32_bf16 a[24:27], a[148:151], v[36:39], a[24:27]\n\t" \
  "buffer_load_dwordx4 v[84:87], v242, %[ws], s73 offen offset:1024 sc1\n\t" \
  "v_mfma_f32_16x16x32_bf16 a[24:27], a[152:155], v[40:43], a[24:27]\n\t" \
  "ds_read_b64_tr_b16 a[76:77], v236 offset:0\n\t" \
  "v_mfma_f32_16x16x32_bf16 a[24:27], a[156:159], v[44:47], a[24:27]\n\t" \
  "s_waitcnt lgkmcnt(5)\n\t" \
  "ds_read_b64_tr_b16 a[78:79], v236 offset:4096\n\t" \
  "v_mfma_f32_16x16x32_bf16 a[32:35], a[12:15], v[16:19], a[32:35]\n\t" \
  "ds_read_b64_tr_b16 a[92:93], v236 offset:8192\n\t" \
  "v_mfma_f32_16x16x32_bf16 a[32:35], a[28:31], v[20:23], a[32:35]\n\t" \
  "s_waitcnt lgkmcnt(3)\n\t" \
  "buffer_load_dwordx4 v[88:91], v242, %[ws], s73 offen offset:2048 sc1\n\t" \
  "v_mfma_f32_16x16x32_bf16 a[32:35], a[44:47], v[24:27], a[32:35]\n\t" \
  "ds_read_b64_tr_b16 a[94:95], v236 offset:12288\n\t" \
  "v_mfma_f32_16x16x32_bf16 a[32:35], a[60:63], v[28:31], a[32:35]\n\t" \
  "ds_read_b64_tr_b16 a[108:109], v236 offset:16384\n\t" \
  "v_mfma_f32_16x16x32_bf16 a[36:39], a[60:63], v[12:15], a[36:39]\n\t" \
  "ds_read_b64_tr_b16 a[110:111], v236 offset:20480\n\t" \
  "v_mfma_f32_16x16x32_bf16 a[36:39], a[44:47], v[8:11], a[36:39]\n\t" \
  "buffer_load_dwordx4 v[92:95], v242, %[ws], s73 offen offset:3072 sc1\n\t" \
  "s_xor_b32 s73, %[eso], 589824\n\t" \
  "v_mfma_f32_16x16x32_bf16 a[36:39], a[28:31], v[4:7], a[36:39]\n\t" \
  "ds_read_b64_tr_b16 a[124:125], v236 offset:24576\n\t" \
  "v_mfma_f32_16x16x32_bf16 a[36:39], a[12:15], v[0:3], a[36:39]\n\t" \
  "ds_read_b64_tr_b16 a[126:127], v236 offset:28672\n\t" \
  "v_mfma_f32_16x16x32_bf16 a[40:43], a[12:15], v[32:35], a[40:43]\n\t" \
  "v_xor_b32 v237, 0x1c080, v234\n\t" \
  "v_mfma_f32_16x16x32_bf16 a[40:43], a[28:31], v[36:39], a[40:43]\n\t" \
  "ds_read_b64_tr_b16 v[128:129], v237 offset:0\n\t" \
  "v_mfma_f32_16x16x32_bf16 a[40:43], a[44:47], v[40:43], a[40:43]\n\t" \
  "buffer_load_dwordx4 v[64:67], v242, %[ws], s73 offen offset:0 sc1\n\t" \
  "v_mfma_f32_16x16x32_bf16 a[40:43], a[60:63], v[44:47], a[40:43]\n\t" \
  "s_waitcnt lgkmcnt(5)\n\t" \
  "ds_read_b64_tr_b16 v[130:131], v237 offset:4096\n\t" \
  "v_mfma_f32_16x16x32_bf16 a[48:51], a[76:79], v[16:19], a[48:51]\n\t" \
  "s_add_u32 m0, %[m], 81920\n\t" \
  "ds_read_b64_tr_b16 v[132:133], v237 offset:8192\n\t" \
  "v_mfma_f32_16x16x32_bf16 a[48:51], a[92:95], v[20:23], a[48:51]\n\t" \
  "s_waitcnt lgkmcnt(3)\n\t" \
  "ds_read_b64_tr_b16 v[134:135], v237 offset:12288\n\t" \
  "v_mfma_f32_16x16x32_bf16 a[48:51], a[108:111], v[24:27], a[48:51]\n\t" \
  "buffer_load_dwordx4 v[68:71], v242, %[ws], s73 offen offset:1024 sc1\n\t" \
  "v_mfma_f32_16x16x32_bf16 a[48:51], a[124:127], v[28:31], a[48:51]\n\t" \
  "s_barrier\n\t" \
  "v_mfma_f32_16x16x32_bf16 a[52:55], a[124:127], v[12:15], a[52:55]\n\t" \
  "s_xor_b32 s71, %[wob], 1572864\n\t" \
  "ds_write_b64 v250, v[248:249] offset:19456\n\t" \
  "v_mfma_f32_16x16x32_bf16 a[52:55], a[108:111], v[8:11], a[52:55]\n\t" \
  "ds_read_b64_tr_b16 v[136:137], v237 offset:16384\n\t" \
  "ds_read_b64_tr_b16 v[138:139], v237 offset:20480\n\t" \
  "v_mfma_f32_16x16x32_bf16 a[52:55], a[92:95], v[4:7], a[52:55]\n\t" \
  "ds_read_b64_tr_b16 v[140:141], v237 offset:24576\n\t" \
  "v_mfma_f32_16x16x32_bf16 a[52:55], a[76:79], v[0:3], a[52:55]\n\t" \
  "buffer_load_dwordx4 v[72:75], v242, %[ws], s73 offen offset:2048 sc1\n\t" \
  "v_mfma_f32_16x16x32_bf16 a[56:59], a[76:79], v[32:35], a[56:59]\n\t" \
  "ds_read_b64_tr_b16 v[142:143], v237 offset:28672\n\t" \
  "v_xor_b32 v235, 0x1c0a0, v234\n\t" \
  "v_mfma_f32_16x16x32_bf16 a[56:59], a[92:95], v[36:39], a[56:59]\n\t" \
  "ds_read_b64_tr_b16 a[12:13], v235 offset:0\n\t" \
  "v_mfma_f32_16x16x32_bf16 a[56:59], a[108:111], v[40:43], a[56:59]\n\t" \
  "ds_read_b64_tr_b16 a[14:15], v235 offset:4096\n\t" \
  "v_mfma_f32_16x16x32_bf16 a[56:59], a[124:127], v[44:47], a[56:59]\n\t" \
  "s_waitcnt lgkmcnt(7)\n\t" \
  "buffer_load_dwordx4 v[76:79], v242, %[ws], s73 offen offset:3072 sc1\n\t" \
  "v_mfma_f32_16x16x32_bf16 a[64:67], v[128:131], v[16:19], a[64:67]\n\t" \
  "ds_read_b64_tr_b16 a[28:29], v235 offset:8192\n\t" \
  "v_mfma_f32_16x16x32_bf16 a[64:67], v[132:135], v[20:23], a[64:67]\n\t" \
  "s_waitcnt lgkmcnt(3)\n\t" \
  "ds_read_b64_tr_b16 a[30:31], v235 offset:12288\n\t" \
  "v_mfma_f32_16x16x32_bf16 a[64:67], v[136:139], v[24:27], a[64:67]\n\t" \
  "ds_read_b64_tr_b16 a[44:45], v235 offset:16384\n\t" \
  "v_mfma_f32_16x16x32_bf16 a[64:67], v[140:143], v[28:31], a[64:67]\n\t" \
  "buffer_load_dwordx4 v240, %[wo], s71 offen offset:0 lds\n\t" \
  "v_mfma_f32_16x16x32_bf16 a[68:71], v[140:143], v[12:15], a[68:71]\n\t" \
  "ds_read_b64_tr_b16 a[46:47], v235 offset:20480\n\t" \
  "v_mfma_f32_16x16x32_bf16 a[68:71], v[136:139], v[8:11], a[68:71]\n\t" \
  "ds_read_b64_tr_b16 a[60:61], v235 offset:24576\n\t" \
  "v_mfma_f32_16x16x32_bf16 a[68:71], v[132:135], v[4:7], a[68:71]\n\t" \
  "ds_read_b64_tr_b16 a[62:63], v235 offset:28672\n\t" \
  "v_mfma_f32_16x16x32_bf16 a[68:71], v[128:131], v[0:3], a[68:71]\n\t" \
  "v_mfma_f32_16x16x32_bf16 a[72:75], v[128:131], v[32:35], a[72:75]\n\t" \
  "buffer_load_dwordx4 v241, %[wo], s71 offen offset:1024 lds\n\t" \
  "v_mfma_f32_16x16x32_bf16 a[72:75], v[132:135], v[36:39], a[72:75]\n\t" \
  "v_xor_b32 v236, 0x1c0c0, v234\n\t" \
  "v_mfma_f32_16x16x32_bf16 a[72:75], v[136:139], v[40:43], a[72:75]\n\t" \
  "ds_read_b64_tr_b16 v[144:145], v236 offset:0\n\t" \
  "v_mfma_f32_16x16x32_bf16 a[72:75], v[140:143], v[44:47], a[72:75]\n\t" \
  "s_waitcnt lgkmcnt(5)\n\t" \
  "ds_read_b64_tr_b16 v[146:147], v236 offset:4096\n\t" \
  "v_mfma_f32_16x16x32_bf16 a[80:83], a[12:15], v[16:19], a[80:83]\n\t" \
  "buffer_load_dwordx4 v238, %[wo], s71 offen offset:2048 lds\n\t" \
  "v_mfma_f32_16x16x32_bf16 a[80:83], a[28:31], v[20:23], a[80:83]\n\t" \
  "s_waitcnt lgkmcnt(2)\n\t" \
  "ds_read_b64_tr_b16 v[148:149], v236 offset:8192\n\t" \
  "v_mfma_f32_16x16x32_bf16 a[80:83], a[44:47], v[24:27], a[80:83]\n\t" \
  "ds_read_b64_tr_b16 v[150:151], v236 offset:12288\n\t" \
  "v_mfma_f32_16x16x32_bf16 a[80:83], a[60:63], v[28:31], a[80:83]\n\t" \
  "s_barrier\n\t" \
  "v_mfma_f32_16x16x32_bf16 a[84:87], a[60:63], v[12:15], a[84:87]\n\t" \
  "s_waitcnt vmcnt(17)\n\t" \
  "buffer_store_dword v243, v254, %[ws], %[fw2] offen offset:64 sc1\n\t" \
  "v_mfma_f32_16x16x32_bf16 a[84:87], a[44:47], v[8:11], a[84:87]\n\t" \
  "ds_read_b64_tr_b16 v[152:153], v236 offset:16384\n\t" \
  "ds_read_b64_tr_b16 v[154:155], v236 offset:20480\n\t" \
  "v_mfma_f32_16x16x32_bf16 a[84:87], a[28:31], v[4:7], a[84:87]\n\t" \
  "buffer_load_dwordx4 v239, %[wo], s71 offen offset:3072 lds\n\t" \
  "v_mfma_f32_16x16x32_bf16 a[84:87], a[12:15], v[0:3], a[84:87]\n\t" \
  "ds_read_b64_tr_b16 v[156:157], v236 offset:24576\n\t" \
  "ds_read_b64_tr_b16 v[158:159], v236 offset:28672\n\t" \
  "v_mfma_f32_16x16x32_bf16 a[88:91], a[12:15], v[32:35], a[88:91]\n\t" \
  "v_and_b32 v250, 3, v244\n\t" \
  "v_xor_b32 v237, 0x1c0e0, v234\n\t" \
  "v_mfma_f32_16x16x32_bf16 a[88:91], a[28:31], v[36:39], a[88:91]\n\t" \
  "ds_read_b64_tr_b16 v[128:129], v237 offset:0\n\t" \
  "v_bfe_u32 v251, v244, 4, 1\n\t" \
  ".Lhs12:\n\ts_cbranch_execz .Lhs12n\n\t" \
  "v_mfma_f32_16x16x32_bf16 a[88:91], a[44:47], v[40:43], a[88:91]\n\t" \
  "ds_read_b64_tr_b16 v[130:131], v237 offset:4096\n\t" \
  "v_lshl_add_u32 v250, v251, 2, v250\n\t" \
  "v_mfma_f32_16x16x32_bf16 a[88:91], a[60:63], v[44:47], a[88:91]\n\t" \
  "s_waitcnt lgkmcnt(6)\n\t" \
  "ds_read_b64_tr_b16 v[132:133], v237 offset:8192\n\t" \
  "v_bfe_u32 v251, v244, 2, 2\n\t" \
  "v_mfma_f32_16x16x32_bf16 a[96:99], v[144:147], v[16:19], a[96:99]\n\t" \
  "ds_read_b64_tr_b16 v[134:135], v237 offset:12288\n\t" \
  "v_lshlrev_b32 v251, 8, v251\n\t" \
  "v_mfma_f32_16x16x32_bf16 a[96:99], v[148:151], v[20:23], a[96:99]\n\t" \
  "s_waitcnt lgkmcnt(4)\n\t" \
  "ds_read_b64_tr_b16 v[136:137], v237 offset:16384\n\t" \
  "v_lshl_add_u32 v251, v250, 3, v251\n\t" \
  "v_mfma_f32_16x16x32_bf16 a[96:99], v[152:155], v[24:27], a[96:99]\n\t" \
  "ds_read_b64_tr_b16 v[138:139], v237 offset:20480\n\t" \
  "v_mfma_f32_16x16x32_bf16 a[96:99], v[156:159], v[28:31], a[96:99]\n\t" \
  "ds_read_b64_tr_b16 v[140:141], v237 offset:24576\n\t" \
  "v_mfma_f32_16x16x32_bf16 a[100:103], v[156:159], v[12:15], a[100:103]\n\t" \
  "ds_read_b64_tr_b16 v[142:143], v237 offset:28672\n\t" \
  "v_mfma_f32_16x16x32_bf16 a[100:103], v[152:155], v[8:11], a[100:103]\n\t" \
  "s_lshr_b32 s75, %[m], 6\n\t" \
  "s_bitset1_b32 s75, 17\n\t" \
  "v_add_u32 v251, s75, v251\n\t" \
  "v_mfma_f32_16x16x32_bf16 a[100:103], v[148:151], v[4:7], a[100:103]\n\t" \
  "ds_read_b64 v[224:225], v251 offset:19456\n\t" \
  "v_lshlrev_b32 v228, 4, v250\n\t" \
  "v_mfma_f32_16x16x32_bf16 a[100:103], v[144:147], v[0:3], a[100:103]\n\t" \
  "v_mov_b32 v249, %[tag]\n\t" \
  "v_mov_b32 v251, %[tag]\n\t" \
  "v_mfma_f32_16x16x32_bf16 a[104:107], v[144:147], v[32:35], a[104:107]\n\t" \
  "s_mov_b32 s82, 0x000f000f\n\t" \
  "v_xor_b32 v235, 0x0, v234\n\t" \
  "s_mov_b32 s83, 0\n\t" \
  "v_mfma_f32_16x16x32_bf16 a[104:107], v[148:151], v[36:39], a[104:107]\n\t" \
  "ds_read_b64_tr_b16 a[12:13], v235 offset:0\n\t" \
  "v_mfma_f32_16x16x32_bf16 a[104:107], v[152:155], v[40:43], a[104:107]\n\t" \
  "ds_read_b64_tr_b16 a[14:15], v235 offset:4096\n\t" \
  "v_mfma_f32_16x16x32_bf16 a[104:107], v[156:159], v[44:47], a[104:107]\n\t" \
  "s_waitcnt lgkmcnt(7)\n\t" \
  "ds_read_b64_tr_b16 a[28:29], v235 offset:8192\n\t" \
  "v_mfma_f32_16x16x32_bf16 a[112:115], v[128:131], v[16:19], a[112:115]\n\t" \
  "ds_read_b64_tr_b16 a[30:31], v235 offset:12288\n\t" \
  "v_mfma_f32_16x16x32_bf16 a[112:115], v[132:135], v[20:23], a[112:115]\n\t" \
  "s_waitcnt lgkmcnt(5)\n\t" \
  "ds_read_b64_tr_b16 a[44:45], v235 offset:16384\n\t" \
  "v_mfma_f32_16x16x32_bf16 a[112:115], v[136:139], v[24:27], a[112:115]\n\t" \
  "s_waitcnt lgkmcnt(5)\n\t" \
  "v_add_f32_dpp v226, v224, v224 row_ror:4 row_mask:0xf bank_mask:0xf\n\t" \
  "v_add_f32_dpp v227, v225, v225 row_ror:4 row_mask:0xf bank_mask:0xf\n\t" \
  "v_mfma_f32_16x16x32_bf16 a[112:115], v[140:143], v[28:31], a[112:115]\n\t" \
  "ds_read_b64_tr_b16 a[46:47], v235 offset:20480\n\t" \
  "v_add_f32_dpp v248, v226, v226 row_ror:8 row_mask:0xf bank_mask:0xf\n\t" \
  "v_mfma_f32_16x16x32_bf16 a[116:119], v[140:143], v[12:15], a[116:119]\n\t" \
  "ds_read_b64_tr_b16 a[60:61], v235 offset:24576\n\t" \
  "v_add_f32_dpp v250, v227, v227 row_ror:8 row_mask:0xf bank_mask:0xf\n\t" \
  "v_mfma_f32_16x16x32_bf16 a[116:119], v[136:139], v[8:11], a[116:119]\n\t" \
  "s_mov_b64 exec, s[82:83]\n\t" \
  "buffer_store_dwordx4 v[248:251], v228, %[ws], %[mbp] offen sc1\n\t" \
  "s_mov_b64 exec, -1\n\t" \
  "v_mfma_f32_16x16x32_bf16 a[116:119], v[132:135], v[4:7], a[116:119]\n\t" \
  "ds_read_b64_tr_b16 a[62:63], v235 offset:28672\n\t" \
  "v_mfma_f32_16x16x32_bf16 a[116:119], v[128:131], v[0:3], a[116:119]\n\t" \
  "s_xor_b32 s73, %[eso], 655360\n\t" \
  "v_mfma_f32_16x16x32_bf16 a[120:123], v[128:131], v[32:35], a[120:123]\n\t" \
  "v_xor_b32 v236, 0x20, v234\n\t" \
  "v_mfma_f32_16x16x32_bf16 a[120:123], v[132:135], v[36:39], a[120:123]\n\t" \
  "ds_read_b64_tr_b16 a[76:77], v236 offset:0\n\t" \
  "v_mfma_f32_16x16x32_bf16 a[120:123], v[136:139], v[40:43], a[120:123]\n\t" \
  "ds_read_b64_tr_b16 a[78:79], v236 offset:4096\n\t" \
  "v_mov_b32 v160, 0\n\t" \
  "v_mfma_f32_16x16x32_bf16 a[120:123], v[140:143], v[44:47], a[120:123]\n\t" \
  "s_waitcnt vmcnt(14) lgkmcnt(6)\n\t" \
  "buffer_load_dwordx4 v[16:19], v242, %[ws], s73 offen offset:0 sc1\n\t" \
  "v_mfma_f32_16x16x32_bf16 a[0:3], a[12:15], v[48:51], a[0:3]\n\t" \
  "ds_read_b64_tr_b16 a[92:93], v236 offset:8192\n\t" \
  "v_mov_b32 v161, 0\n\t" \
  "v_mfma_f32_16x16x32_bf16 a[0:3], a[28:31], v[52:55], a[0:3]\n\t" \
  "s_waitcnt lgkmcnt(3)\n\t" \
  "buffer_load_dwordx4 v[20:23], v242, %[ws], s73 offen offset:1024 sc1\n\t" \
  "v_mfma_f32_16x16x32_bf16 a[0:3], a[44:47], v[56:59], a[0:3]\n\t" \
  "ds_read_b64_tr_b16 a[94:95], v236 offset:12288\n\t" \
  "v_mov_b32 v162, 0\n\t" \
  "v_mfma_f32_16x16x32_bf16 a[0:3], a[60:63], v[60:63], a[0:3]\n\t" \
  "s_waitcnt vmcnt(12)\n\t" \
  "ds_read_b64_tr_b16 a[108:109], v236 offset:16384\n\t" \
  "v_mov_b32 v163, 0\n\t" \
  "v_mfma_f32_16x16x32_bf16 a[4:7], a[60:63], v[92:95], a[4:7]\n\t" \
  "ds_read_b64_tr_b16 a[110:111], v236 offset:20480\n\t" \
  "v_mov_b32 v164, 0x3f803f80\n\t" \
  "v_mfma_f32_16x16x32_bf16 a[4:7], a[44:47], v[88:91], a[4:7]\n\t" \
  "buffer_load_dwordx4 v[24:27], v242, %[ws], s73 offen offset:2048 sc1\n\t" \
  "v_mfma_f32_16x16x32_bf16 a[4:7], a[28:31], v[84:87], a[4:7]\n\t" \
  "ds_read_b64_tr_b16 a[124:125], v236 offset:24576\n\t" \
  "v_add_u32 v165, s70, v242\n\t" \
  "v_mfma_f32_16x16x32_bf16 a[4:7], a[12:15], v[80:83], a[4:7]\n\t" \
  "s_waitcnt vmcnt(9)\n\t" \
  "ds_read_b64_tr_b16 a[126:127], v236 offset:28672\n\t" \
  "v_mfma_f32_16x16x32_bf16 a[8:11], a[12:15], v[64:67], a[8:11]\n\t" \
  "v_xor_b32 v237, 0x40, v234\n\t" \
  "v_mfma_f32_16x16x32_bf16 a[8:11], a[28:31], v[68:71], a[8:11]\n\t" \
  "buffer_load_dwordx4 v[28:31], v242, %[ws], s73 offen offset:3072 sc1\n\t" \
  "s_xor_b32 s73, %[eso], 688128\n\t" \
  "v_mfma_f32_16x16x32_bf16 a[8:11], a[44:47], v[72:75], a[8:11]\n\t" \
  "ds_read_b64_tr_b16 v[144:145], v237 offset:0\n\t" \
  "v_mfma_f32_16x16x32_bf16 a[8:11], a[60:63], v[76:79], a[8:11]\n\t" \
  "s_waitcnt lgkmcnt(5)\n\t" \
  "ds_read_b64_tr_b16 v[146:147], v237 offset:4096\n\t" \
  "v_mfma_f32_16x16x32_bf16 a[16:19], a[76:79], v[48:51], a[16:19]\n\t" \
  "ds_read_b64_tr_b16 v[148:149], v237 offset:8192\n\t" \
  "v_mfma_f32_16x16x32_bf16 a[16:19], a[92:95], v[52:55], a[16:19]\n\t" \
  "s_waitcnt lgkmcnt(3)\n\t" \
  "ds_read_b64_tr_b16 v[150:151], v237 offset:12288\n\t" \
  "v_mfma_f32_16x16x32_bf16 a[16:19], a[108:111], v[56:59], a[16:19]\n\t" \
  "buffer_load_dwordx4 v[0:3], v242, %[ws], s73 offen offset:0 sc1\n\t" \
  "v_mfma_f32_16x16x32_bf16 a[16:19], a[124:127], v[60:63], a[16:19]\n\t" \
  "ds_read_b64_tr_b16 v[152:153], v237 offset:16384\n\t" \
  "ds_write_b128 v165, v[160:163]\n\t" \
  "v_mfma_f32_16x16x32_bf16 a[20:23], a[124:127], v[92:95], a[20:23]\n\t" \
  "ds_read_b64_tr_b16 v[154:155], v237 offset:20480\n\t" \
  "v_and_b32 v171, 15, v244\n\t" \
  "v_mfma_f32_16x16x32_bf16 a[20:23], a[108:111], v[88:91], a[20:23]\n\t" \
  "ds_read_b64_tr_b16 v[156:157], v237 offset:24576\n\t" \
  "v_lshrrev_b32 v172, 4, v244\n\t" \
  "v_mfma_f32_16x16x32_bf16 a[20:23], a[92:95], v[84:87], a[20:23]\n\t" \
  "buffer_load_dwordx4 v[4:7], v242, %[ws], s73 offen offset:1024 sc1\n\t" \
  "v_mfma_f32_16x16x32_bf16 a[20:23], a[76:79], v[80:83], a[20:23]\n\t" \
  "ds_read_b64_tr_b16 v[158:159], v237 offset:28672\n\t" \
  "v_lshlrev_b32 v170, 13, v172\n\t" \
  "v_mfma_f32_16x16x32_bf16 a[24:27], a[76:79], v[64:67], a[24:27]\n\t" \
  "v_xor_b32 v235, 0x60, v234\n\t" \
  "v_lshl_add_u32 v170, v171, 4, v170\n\t" \
  "v_mfma_f32_16x16x32_bf16 a[24:27], a[92:95], v[68:71], a[24:27]\n\t" \
  "ds_read_b64_tr_b16 v[128:129], v235 offset:0\n\t" \
  "v_lshlrev_b32 v174, 7, v172\n\t" \
  "v_mfma_f32_16x16x32_bf16 a[24:27], a[108:111], v[72:75], a[24:27]\n\t" \
  "buffer_load_dwordx4 v[8:11], v242, %[ws], s73 offen offset:2048 sc1\n\t" \
  "v_mfma_f32_16x16x32_bf16 a[24:27], a[124:127], v[76:79], a[24:27]\n\t" \
  "s_waitcnt lgkmcnt(6)\n\t" \
  "ds_read_b64_tr_b16 v[130:131], v235 offset:4096\n\t" \
  "v_lshl_add_u32 v174, v171, 3, v174\n\t" \
  "v_mfma_f32_16x16x32_bf16 a[32:35], v[144:147], v[48:51], a[32:35]\n\t" \
  "ds_read_b64_tr_b16 v[132:133], v235 offset:8192\n\t" \
  "v_add_u32 v174, s70, v174\n\t" \
  "v_mfma_f32_16x16x32_bf16 a[32:35], v[148:151], v[52:55], a[32:35]\n\t" \
  "s_waitcnt lgkmcnt(3)\n\t" \
  "ds_read_b64_tr_b16 v[134:135], v235 offset:12288\n\t" \
  "v_mfma_f32_16x16x32_bf16 a[32:35], v[152:155], v[56:59], a[32:35]\n\t" \
  "ds_read_b64_tr_b16 v[136:137], v235 offset:16384\n\t" \
  "v_mfma_f32_16x16x32_bf16 a[32:35], v[156:159], v[60:63], a[32:35]\n\t" \
  "buffer_load_dwordx4 v[12:15], v242, %[ws], s73 offen offset:3072 sc1\n\t" \
  "s_xor_b32 s73, %[eso], 720896\n\t" \
  "v_mfma_f32_16x16x32_bf16 a[36:39], v[156:159], v[92:95], a[36:39]\n\t" \
  "ds_read_b64_tr_b16 v[138:139], v235 offset:20480\n\t" \
  "v_mfma_f32_16x16x32_bf16 a[36:39], v[152:155], v[88:91], a[36:39]\n\t" \
  "ds_read_b64_tr_b16 v[140:141], v235 offset:24576\n\t" \
  "v_mfma_f32_16x16x32_bf16 a[36:39], v[148:151], v[84:87], a[36:39]\n\t" \
  "ds_read_b64_tr_b16 v[142:143], v235 offset:28672\n\t" \
  "ds_write_b128 v165, v[160:163] offset:1024\n\t" \
  "v_mfma_f32_16x16x32_bf16 a[36:39], v[144:147], v[80:83], a[36:39]\n\t" \
  "buffer_load_dwordx4 v[32:35], v242, %[ws], s73 offen offset:0 sc1\n\t" \
  "v_mfma_f32_16x16x32_bf16 a[40:43], v[144:147], v[64:67], a[40:43]\n\t" \
  "v_lshlrev_b32 v171, 2, v171\n\t" \
  "v_xor_b32 v236, 0x80, v234\n\t" \
  "v_mfma_f32_16x16x32_bf16 a[40:43], v[148:151], v[68:71], a[40:43]\n\t" \
  "v_cmp_eq_u32 vcc, 2, v172\n\t" \
  "s_nop 1\n\t" \
  "v_cndmask_b32 v172, 0, 1.0, vcc\n\t" \
  "v_mfma_f32_16x16x32_bf16 a[40:43], v[152:155], v[72:75], a[40:43]\n\t" \
  "ds_read_b64_tr_b16 a[208:209], v236 offset:0\n\t" \
  "v_mfma_f32_16x16x32_bf16 a[40:43], v[156:159], v[76:79], a[40:43]\n\t" \
  "s_waitcnt lgkmcnt(6)\n\t" \
  "ds_read_b64_tr_b16 a[210:211], v236 offset:4096\n\t" \
  "v_mfma_f32_16x16x32_bf16 a[48:51], v[128:131], v[48:51], a[48:51]\n\t" \
  "s_add_u32 m0, %[m], 114688\n\t" \
  "buffer_load_dwordx4 v[36:39], v242, %[ws], s73 offen offset:1024 sc1\n\t" \
  "v_mfma_f32_16x16x32_bf16 a[48:51], v[132:135], v[52:55], a[48:51]\n\t" \
  "s_waitcnt lgkmcnt(3)\n\t" \
  "ds_read_b64_tr_b16 a[212:213], v236 offset:8192\n\t" \
  "ds_write_b128 v165, v[160:163] offset:1536\n\t" \
  "v_mfma_f32_16x16x32_bf16 a[48:51], v[136:139], v[56:59], a[48:51]\n\t" \
  "ds_read_b64_tr_b16 a[214:215], v236 offset:12288\n\t" \
  "s_waitcnt lgkmcnt(1)\n\t" \
  "v_bfe_u32 v160, v244, 3, 1\n\t" \
  "v_mfma_f32_16x16x32_bf16 a[48:51], v[140:143], v[60:63], a[48:51]\n\t" \
  "s_barrier\n\t" \
  "v_mfma_f32_16x16x32_bf16 a[52:55], v[140:143], v[92:95], a[52:55]\n\t" \
  "buffer_load_dwordx4 v[40:43], v242, %[ws], s73 offen offset:2048 sc1\n\t" \
  "v_bfe_u32 v173, v244, 1, 2\n\t" \
  "v_mfma_f32_16x16x32_bf16 a[52:55], v[136:139], v[88:91], a[52:55]\n\t" \
  "s_xor_b32 s71, %[wob], 1703936\n\t" \
  "ds_read_b64_tr_b16 a[216:217], v236 offset:16384\n\t" \
  "ds_read_b64_tr_b16 a[218:219], v236 offset:20480\n\t" \
  "v_mfma_f32_16x16x32_bf16 a[52:55], v[132:135], v[84:87], a[52:55]\n\t" \
  "ds_read_b64_tr_b16 a[220:221], v236 offset:24576\n\t" \
  "v_lshlrev_b32 v173, 4, v173\n\t" \
  "v_mfma_f32_16x16x32_bf16 a[52:55], v[128:131], v[80:83], a[52:55]\n\t" \
  "ds_read_b64_tr_b16 a[222:223], v236 offset:28672\n\t" \
  "v_lshl_add_u32 v173, v160, 3, v173\n\t" \
  "v_mfma_f32_16x16x32_bf16 a[56:59], v[128:131], v[64:67], a[56:59]\n\t" \
  "buffer_load_dwordx4 v[44:47], v242, %[ws], s73 offen offset:3072 sc1\n\t" \
  "v_mfma_f32_16x16x32_bf16 a[56:59], v[132:135], v[68:71], a[56:59]\n\t" \
  "v_xor_b32 v237, 0xa0, v234\n\t" \
  "v_and_b32 v160, 1, v244\n\t" \
  "v_mfma_f32_16x16x32_bf16 a[56:59], v[136:139], v[72:75], a[56:59]\n\t" \
  "ds_read_b64_tr_b16 v[144:145], v237 offset:0\n\t" \
  "v_lshl_add_u32 v173, v160, 2, v173\n\t" \
  "v_mfma_f32_16x16x32_bf16 a[56:59], v[140:143], v[76:79], a[56:59]\n\t" \
  "s_waitcnt lgkmcnt(5)\n\t" \
  "ds_read_b64_tr_b16 v[146:147], v237 offset:4096\n\t" \
  "v_add_u32 v173, s70, v173\n\t" \
  "v_mfma_f32_16x16x32_bf16 a[64:67], a[208:211], v[48:51], a[64:67]\n\t" \
  "ds_read_b64_tr_b16 v[148:149], v237 offset:8192\n\t" \
  "v_mfma_f32_16x16x32_bf16 a[64:67], a[212:215], v[52:55], a[64:67]\n\t" \
  "s_waitcnt lgkmcnt(3)\n\t" \
  "buffer_load_dwordx4 v240, %[wo], s71 offen offset:0 lds\n\t" \
  "v_mfma_f32_16x16x32_bf16 a[64:67], a[216:219], v[56:59], a[64:67]\n\t" \
  "ds_read_b64_tr_b16 v[150:151], v237 offset:12288\n\t" \
  "v_mfma_f32_16x16x32_bf16 a[64:67], a[220:223], v[60:63], a[64:67]\n\t" \
  "ds_read_b64_tr_b16 v[152:153], v237 offset:16384\n\t" \
  "v_mfma_f32_16x16x32_bf16 a[68:71], a[220:223], v[92:95], a[68:71]\n\t" \
  "ds_read_b64_tr_b16 v[154:155], v237 offset:20480\n\t" \
  "v_mfma_f32_16x16x32_bf16 a[68:71], a[216:219], v[88:91], a[68:71]\n\t" \
  "buffer_load_dwordx4 v241, %[wo], s71 offen offset:1024 lds\n\t" \
  "v_mfma_f32_16x16x32_bf16 a[68:71], a[212:215], v[84:87], a[68:71]\n\t" \
  "s_mov_b64 exec, 0xffff\n\t" \
  "ds_write_b32 v173, v164 offset:960\n\t" \
  "s_mov_b64 exec, -1\n\t" \
  "v_mfma_f32_16x16x32_bf16 a[68:71], a[208:211], v[80:83], a[68:71]\n\t" \
  "ds_read_b64_tr_b16 v[156:157], v237 offset:24576\n\t" \
  "ds_read_b64_tr_b16 v[158:159], v237 offset:28672\n\t" \
  "v_lshrrev_b32 v229, 4, v244\n\t" \
  "v_mfma_f32_16x16x32_bf16 a[72:75], a[208:211], v[64:67], a[72:75]\n\t" \
  "v_and_b32 v231, 15, v244\n\t" \
  "v_xor_b32 v248, v231, v229\n\t" \
  "v_mfma_f32_16x16x32_bf16 a[72:75], a[212:215], v[68:71], a[72:75]\n\t" \
  "v_lshlrev_b32 v248, 4, v248\n\t" \
  "v_xor_b32 v235, 0xc0, v234\n\t" \
  "v_mfma_f32_16x16x32_bf16 a[72:75], a[216:219], v[72:75], a[72:75]\n\t" \
  "buffer_load_dwordx4 v238, %[wo], s71 offen offset:2048 lds\n\t" \
  "v_lshl_add_u32 v248, v229, 11, v248\n\t" \
  "v_mfma_f32_16x16x32_bf16 a[72:75], a[220:223], v[76:79], a[72:75]\n\t" \
  "s_waitcnt lgkmcnt(5)\n\t" \
  "ds_read_b64_tr_b16 a[192:193], v235 offset:0\n\t" \
  "v_xor_b32 v249, 64, v248\n\t" \
  "v_mfma_f32_16x16x32_bf16 a[80:83], v[144:147], v[48:51], a[80:83]\n\t" \
  "ds_read_b64_tr_b16 a[194:195], v235 offset:4096\n\t" \
  "v_xor_b32 v250, 0x80, v248\n\t" \
  "v_mfma_f32_16x16x32_bf16 a[80:83], v[148:151], v[52:55], a[80:83]\n\t" \
  "s_waitcnt lgkmcnt(2)\n\t" \
  "ds_read_b64_tr_b16 a[196:197], v235 offset:8192\n\t" \
  "v_xor_b32 v251, 0xc0, v248\n\t" \
  "v_mfma_f32_16x16x32_bf16 a[80:83], v[152:155], v[56:59], a[80:83]\n\t" \
  "buffer_load_dwordx4 v239, %[wo], s71 offen offset:3072 lds\n\t" \
  "s_add_u32 m0, %[m], 131072\n\t" \
  "v_mfma_f32_16x16x32_bf16 a[80:83], v[156:159], v[60:63], a[80:83]\n\t" \
  "s_xor_b32 s71, %[wob], 1835008\n\t" \
  "ds_read_b64_tr_b16 a[198:199], v235 offset:12288\n\t" \
  "v_lshrrev_b32 v227, 5, v244\n\t" \
  "s_lshl_b32 s75, %[wid], 2\n\t" \
  "v_mfma_f32_16x16x32_bf16 a[84:87], v[156:159], v[92:95], a[84:87]\n\t" \
  "ds_read_b64_tr_b16 a[200:201], v235 offset:16384\n\t" \
  "v_add_u32 v227, s75, v227\n\t" \
  "v_mfma_f32_16x16x32_bf16 a[84:87], v[152:155], v[88:91], a[84:87]\n\t" \
  "ds_read_b64_tr_b16 a[202:203], v235 offset:20480\n\t" \
  "v_xor_b32 v227, v227, v231\n\t" \
  "v_mfma_f32_16x16x32_bf16 a[84:87], v[148:151], v[84:87], a[84:87]\n\t" \
  "buffer_load_dwordx4 v240, %[wo], s71 offen offset:0 lds\n\t" \
  "v_mfma_f32_16x16x32_bf16 a[84:87], v[144:147], v[80:83], a[84:87]\n\t" \
  "ds_read_b64_tr_b16 a[204:205], v235 offset:24576\n\t" \
  "ds_read_b64_tr_b16 a[206:207], v235 offset:28672\n\t" \
  "v_mfma_f32_16x16x32_bf16 a[88:91], v[144:147], v[64:67], a[88:91]\n\t" \
  "v_xor_b32 v236, 0xe0, v234\n\t" \
  "v_lshlrev_b32 v227, 4, v227\n\t" \
  "v_mfma_f32_16x16x32_bf16 a[88:91], v[148:151], v[68:71], a[88:91]\n\t" \
  "ds_read_b64_tr_b16 a[208:209], v236 offset:0\n\t" \
  "v_lshl_add_u32 v227, v231, 8, v227\n\t" \
  "v_mfma_f32_16x16x32_bf16 a[88:91], v[152:155], v[72:75], a[88:91]\n\t" \
  "ds_read_b64_tr_b16 a[210:211], v236 offset:4096\n\t" \
  "v_and_b32 v229, 1, v229\n\t" \
  "v_mfma_f32_16x16x32_bf16 a[88:91], v[156:159], v[76:79], a[88:91]\n\t" \
  "s_waitcnt lgkmcnt(6)\n\t" \
  "buffer_load_dwordx4 v241, %[wo], s71 offen offset:1024 lds\n\t" \
  "v_mfma_f32_16x16x32_bf16 a[96:99], a[192:195], v[48:51], a[96:99]\n\t" \
  "ds_read_b64_tr_b16 a[212:213], v236 offset:8192\n\t" \
  "v_perm_b32 v254, v180, v176, s79\n\t" \
  "v_mfma_f32_16x16x32_bf16 a[96:99], a[196:199], v[52:55], a[96:99]\n\t" \
  "s_waitcnt lgkmcnt(3)\n\t" \
  "ds_read_b64_tr_b16 a[214:215], v236 offset:12288\n\t" \
  "v_perm_b32 v180, v180, v176, s80\n\t" \
  "v_mfma_f32_16x16x32_bf16 a[96:99], a[200:203], v[56:59], a[96:99]\n\t" \
  "ds_read_b64_tr_b16 a[216:217], v236 offset:16384\n\t" \
  "v_lshl_add_u32 v227, v229, 3, v227\n\t" \
  "v_mfma_f32_16x16x32_bf16 a[96:99], a[204:207], v[60:63], a[96:99]\n\t" \
  "buffer_load_dwordx4 v238, %[wo], s71 offen offset:2048 lds\n\t" \
  "v_mfma_f32_16x16x32_bf16 a[100:103], a[204:207], v[92:95], a[100:103]\n\t" \
  "ds_read_b64_tr_b16 a[218:219], v236 offset:20480\n\t" \
  "v_add_u32 v231, 131072, v227\n\t" \
  "v_mfma_f32_16x16x32_bf16 a[100:103], a[200:203], v[88:91], a[100:103]\n\t" \
  "ds_read_b64_tr_b16 a[220:221], v236 offset:24576\n\t" \
  "v_perm_b32 v176, v181, v177, s79\n\t" \
  "v_mfma_f32_16x16x32_bf16 a[100:103], a[196:199], v[84:87], a[100:103]\n\t" \
  "ds_read_b64_tr_b16 a[222:223], v236 offset:28672\n\t" \
  "v_xor_b32 v233, 32, v231\n\t" \
  "v_mfma_f32_16x16x32_bf16 a[100:103], a[192:195], v[80:83], a[100:103]\n\t" \
  "s_lshl_b32 s75, %[wid], 9\n\t" \
  "v_perm_b32 v181, v181, v177, s80\n\t" \
  "v_lshl_add_u32 v252, v244, 3, s75\n\t" \
  "v_mfma_f32_16x16x32_bf16 a[104:107], a[192:195], v[64:67], a[104:107]\n\t" \
  "buffer_load_dwordx4 v239, %[wo], s71 offen offset:3072 lds\n\t" \
  "v_mfma_f32_16x16x32_bf16 a[104:107], a[196:199], v[68:71], a[104:107]\n\t" \
  "v_xor_b32 v237, 0x18000, v234\n\t" \
  "v_perm_b32 v177, v182, v178, s79\n\t" \
  "v_mfma_f32_16x16x32_bf16 a[104:107], a[200:203], v[72:75], a[104:107]\n\t" \
  "ds_read_b64_tr_b16 v[128:129], v237 offset:0\n\t" \
  "v_mfma_f32_16x16x32_bf16 a[104:107], a[204:207], v[76:79], a[104:107]\n\t" \
  "s_waitcnt lgkmcnt(5)\n\t" \
  "ds_read_b64_tr_b16 v[130:131], v237 offset:4096\n\t" \
  "v_perm_b32 v182, v182, v178, s80\n\t" \
  "v_mfma_f32_16x16x32_bf16 a[112:115], a[208:211], v[48:51], a[112:115]\n\t" \
  "ds_read_b64_tr_b16 v[132:133], v237 offset:8192\n\t" \
  "v_mfma_f32_16x16x32_bf16 a[112:115], a[212:215], v[52:55], a[112:115]\n\t" \
  "s_waitcnt lgkmcnt(3)\n\t" \
  "ds_read_b64_tr_b16 v[134:135], v237 offset:12288\n\t" \
  "v_xor_b32 v235, 0x8000, v234\n\t" \
  "v_mfma_f32_16x16x32_bf16 a[112:115], a[216:219], v[56:59], a[112:115]\n\t" \
  "ds_read_b64_tr_b16 v[136:137], v235 offset:0\n\t" \
  "v_mfma_f32_16x16x32_bf16 a[112:115], a[220:223], v[60:63], a[112:115]\n\t" \
  "ds_read_b64_tr_b16 v[138:139], v235 offset:4096\n\t" \
  "v_perm_b32 v178, v183, v179, s79\n\t" \
  "v_mfma_f32_16x16x32_bf16 a[116:119], a[220:223], v[92:95], a[116:119]\n\t" \
  "ds_read_b64_tr_b16 v[140:141], v235 offset:8192\n\t" \
  "v_mfma_f32_16x16x32_bf16 a[116:119], a[216:219], v[88:91], a[116:119]\n\t" \
  "ds_read_b64_tr_b16 v[142:143], v235 offset:12288\n\t" \
  "v_mfma_f32_16x16x32_bf16 a[116:119], a[212:215], v[84:87], a[116:119]\n\t" \
  "v_mfma_f32_16x16x32_bf16 a[116:119], a[208:211], v[80:83], a[116:119]\n\t" \
  "s_xor_b32 s73, %[eso], 786432\n\t" \
  "v_perm_b32 v183, v183, v179, s80\n\t" \
  "v_mfma_f32_16x16x32_bf16 a[120:123], a[208:211], v[64:67], a[120:123]\n\t" \
  "v_xor_b32 v236, 0x18020, v234\n\t" \
  "v_mfma_f32_16x16x32_bf16 a[120:123], a[212:215], v[68:71], a[120:123]\n\t" \
  "ds_read_b64_tr_b16 v[144:145], v236 offset:0\n\t" \
  "v_mfma_f32_16x16x32_bf16 a[120:123], a[216:219], v[72:75], a[120:123]\n\t" \
  "ds_read_b64_tr_b16 v[146:147], v236 offset:4096\n\t" \
  "v_perm_b32 v179, v188, v184, s79\n\t" \
  "v_mfma_f32_16x16x32_bf16 a[120:123], a[220:223], v[76:79], a[120:123]\n\t" \
  "s_waitcnt vmcnt(16) lgkmcnt(6)\n\t" \
  "buffer_load_dwordx4 v[48:51], v242, %[ws], s73 offen offset:0 sc1\n\t" \
  "v_mfma_f32_16x16x32_bf16 a[0:3], v[128:131], v[16:19], a[0:3]\n\t" \
  "ds_read_b64_tr_b16 v[148:149], v236 offset:8192\n\t" \
  "v_mfma_f32_16x16x32_bf16 a[0:3], v[132:135], v[20:23], a[0:3]\n\t" \
  "s_waitcnt lgkmcnt(3)\n\t" \
  "buffer_load_dwordx4 v[52:55], v242, %[ws], s73 offen offset:1024 sc1\n\t" \
  "v_mfma_f32_16x16x32_bf16 a[0:3], v[136:139], v[24:27], a[0:3]\n\t" \
  "ds_read_b64_tr_b16 v[150:151], v236 offset:12288\n\t" \
  "v_xor_b32 v237, 0x8020, v234\n\t" \
  "v_mfma_f32_16x16x32_bf16 a[0:3], v[140:143], v[28:31], a[0:3]\n\t" \
  "s_waitcnt vmcnt(14)\n\t" \
  "ds_read_b64_tr_b16 v[152:153], v237 offset:0\n\t" \
  "v_perm_b32 v188, v188, v184, s80\n\t" \
  "v_mfma_f32_16x16x32_bf16 a[4:7], v[140:143], v[12:15], a[4:7]\n\t" \
  "ds_read_b64_tr_b16 v[154:155], v237 offset:4096\n\t" \
  "v_mfma_f32_16x16x32_bf16 a[4:7], v[136:139], v[8:11], a[4:7]\n\t" \
  "buffer_load_dwordx4 v[56:59], v242, %[ws], s73 offen offset:2048 sc1\n\t" \
  "v_mfma_f32_16x16x32_bf16 a[4:7], v[132:135], v[4:7], a[4:7]\n\t" \
  "ds_read_b64_tr_b16 v[156:157], v237 offset:8192\n\t" \
  "v_perm_b32 v184, v189, v185, s79\n\t" \
  "v_mfma_f32_16x16x32_bf16 a[4:7], v[128:131], v[0:3], a[4:7]\n\t" \
  "s_waitcnt vmcnt(11)\n\t" \
  "ds_read_b64_tr_b16 v[158:159], v237 offset:12288\n\t" \
  "v_mfma_f32_16x16x32_bf16 a[8:11], v[128:131], v[32:35], a[8:11]\n\t" \
  "v_xor_b32 v235, 0x18040, v234\n\t" \
  "v_mfma_f32_16x16x32_bf16 a[8:11], v[132:135], v[36:39], a[8:11]\n\t" \
  "buffer_load_dwordx4 v[60:63], v242, %[ws], s73 offen offset:3072 sc1\n\t" \
  "s_xor_b32 s73, %[eso], 819200\n\t" \
  "v_mfma_f32_16x16x32_bf16 a[8:11], v[136:139], v[40:43], a[8:11]\n\t" \
  "ds_read_b64_tr_b16 a[208:209], v235 offset:0\n\t" \
  "v_perm_b32 v189, v189, v185, s80\n\t" \
  "v_mfma_f32_16x16x32_bf16 a[8:11], v[140:143], v[44:47], a[8:11]\n\t" \
  "s_waitcnt lgkmcnt(5)\n\t" \
  "ds_read_b64_tr_b16 a[210:211], v235 offset:4096\n\t" \
  "buffer_load_dword v246, v245, %[ws], %[fw2] offen sc1\n\t" \
  "v_mfma_f32_16x16x32_bf16 a[16:19], v[144:147], v[16:19], a[16:19]\n\t" \
  "ds_read_b64_tr_b16 a[212:213], v235 offset:8192\n\t" \
  "v_mfma_f32_16x16x32_bf16 a[16:19], v[148:151], v[20:23], a[16:19]\n\t" \
  "s_waitcnt lgkmcnt(3)\n\t" \
  "ds_read_b64_tr_b16 a[214:215], v235 offset:12288\n\t" \
  "v_xor_b32 v236, 0x8040, v234\n\t" \
  "v_mfma_f32_16x16x32_bf16 a[16:19], v[152:155], v[24:27], a[16:19]\n\t" \
  "ds_read_b64_tr_b16 a[216:217], v236 offset:0\n\t" \
  "v_perm_b32 v185, v190, v186, s79\n\t" \
  "v_mfma_f32_16x16x32_bf16 a[16:19], v[156:159], v[28:31], a[16:19]\n\t" \
  "buffer_load_dwordx4 v[80:83], v242, %[ws], s73 offen offset:0 sc1\n\t" \
  "v_mfma_f32_16x16x32_bf16 a[20:23], v[156:159], v[12:15], a[20:23]\n\t" \
  "ds_read_b64_tr_b16 a[218:219], v236 offset:4096\n\t" \
  "v_mfma_f32_16x16x32_bf16 a[20:23], v[152:155], v[8:11], a[20:23]\n\t" \
  "ds_read_b64_tr_b16 a[220:221], v236 offset:8192\n\t" \
  "v_perm_b32 v190, v190, v186, s80\n\t" \
  "v_mfma_f32_16x16x32_bf16 a[20:23], v[148:151], v[4:7], a[20:23]\n\t" \
  "ds_read_b64_tr_b16 a[222:223], v236 offset:12288\n\t" \
  "v_mfma_f32_16x16x32_bf16 a[20:23], v[144:147], v[0:3], a[20:23]\n\t" \
  "buffer_load_dwordx4 v[84:87], v242, %[ws], s73 offen offset:1024 sc1\n\t" \
  "v_mfma_f32_16x16x32_bf16 a[24:27], v[144:147], v[32:35], a[24:27]\n\t" \
  "v_xor_b32 v237, 0x18060, v234\n\t" \
  "v_mfma_f32_16x16x32_bf16 a[24:27], v[148:151], v[36:39], a[24:27]\n\t" \
  "ds_read_b64_tr_b16 a[192:193], v237 offset:0\n\t" \
  "v_perm_b32 v186, v191, v187, s79\n\t" \
  "v_mfma_f32_16x16x32_bf16 a[24:27], v[152:155], v[40:43], a[24:27]\n\t" \
  "ds_read_b64_tr_b16 a[194:195], v237 offset:4096\n\t" \
  "v_mfma_f32_16x16x32_bf16 a[24:27], v[156:159], v[44:47], a[24:27]\n\t" \
  "s_waitcnt lgkmcnt(6)\n\t" \
  "ds_read_b64_tr_b16 a[196:197], v237 offset:8192\n\t" \
  "v_mfma_f32_16x16x32_bf16 a[32:35], a[208:211], v[16:19], a[32:35]\n\t" \
  "buffer_load_dwordx4 v[88:91], v242, %[ws], s73 offen offset:2048 sc1\n\t" \
  "v_mfma_f32_16x16x32_bf16 a[32:35], a[212:215], v[20:23], a[32:35]\n\t" \
  "s_waitcnt lgkmcnt(3)\n\t" \
  "ds_read_b64_tr_b16 a[198:199], v237 offset:12288\n\t" \
  "v_xor_b32 v235, 0x8060, v234\n\t" \
  "v_mfma_f32_16x16x32_bf16 a[32:35], a[216:219], v[24:27], a[32:35]\n\t" \
  "ds_read_b64_tr_b16 a[200:201], v235 offset:0\n\t" \
  "v_perm_b32 v191, v191, v187, s80\n\t" \
  "v_mfma_f32_16x16x32_bf16 a[32:35], a[220:223], v[28:31], a[32:35]\n\t" \
  "ds_read_b64_tr_b16 a[202:203], v235 offset:4096\n\t" \
  "v_mfma_f32_16x16x32_bf16 a[36:39], a[220:223], v[12:15], a[36:39]\n\t" \
  "buffer_load_dwordx4 v[92:95], v242, %[ws], s73 offen offset:3072 sc1\n\t" \
  "s_xor_b32 s73, %[eso], 851968\n\t" \
  "v_mfma_f32_16x16x32_bf16 a[36:39], a[216:219], v[8:11], a[36:39]\n\t" \
  "ds_read_b64_tr_b16 a[204:205], v235 offset:8192\n\t" \
  "v_perm_b32 v187, v196, v192, s79\n\t" \
  "v_mfma_f32_16x16x32_bf16 a[36:39], a[212:215], v[4:7], a[36:39]\n\t" \
  "ds_read_b64_tr_b16 a[206:207], v235 offset:12288\n\t" \
  "v_mfma_f32_16x16x32_bf16 a[36:39], a[208:211], v[0:3], a[36:39]\n\t" \
  "v_mfma_f32_16x16x32_bf16 a[40:43], a[208:211], v[32:35], a[40:43]\n\t" \
  "buffer_load_dwordx4 v[64:67], v242, %[ws], s73 offen offset:0 sc1\n\t" \
  "v_mfma_f32_16x16x32_bf16 a[40:43], a[212:215], v[36:39], a[40:43]\n\t" \
  "v_perm_b32 v196, v196, v192, s80\n\t" \
  "v_mfma_f32_16x16x32_bf16 a[40:43], a[216:219], v[40:43], a[40:43]\n\t" \
  "v_mfma_f32_16x16x32_bf16 a[40:43], a[220:223], v[44:47], a[40:43]\n\t" \
  "s_waitcnt lgkmcnt(4)\n\t" \
  "v_xor_b32 v236, 0x18080, v234\n\t" \
  "v_mfma_f32_16x16x32_bf16 a[48:51], a[192:195], v[16:19], a[48:51]\n\t" \
  "s_add_u32 m0, %[m], 0\n\t" \
  "ds_read_b64_tr_b16 a[208:209], v236 offset:0\n\t" \
  "v_perm_b32 v192, v197, v193, s79\n\t" \
  "v_mfma_f32_16x16x32_bf16 a[48:51], a[196:199], v[20:23], a[48:51]\n\t" \
  "s_waitcnt lgkmcnt(1)\n\t" \
  "buffer_load_dwordx4 v[68:71], v242, %[ws], s73 offen offset:1024 sc1\n\t" \
  "v_mfma_f32_16x16x32_bf16 a[48:51], a[200:203], v[24:27], a[48:51]\n\t" \
  "ds_read_b64_tr_b16 a[210:211], v236 offset:4096\n\t" \
  "v_mfma_f32_16x16x32_bf16 a[48:51], a[204:207], v[28:31], a[48:51]\n\t" \
  "s_waitcnt vmcnt(15)\n\t" \
  "s_barrier\n\t" \
  "v_mfma_f32_16x16x32_bf16 a[52:55], a[204:207], v[12:15], a[52:55]\n\t" \
  "s_xor_b32 s71, %[wob], 1966080\n\t" \
  "ds_read_b64_tr_b16 a[212:213], v236 offset:8192\n\t" \
  "v_perm_b32 v197, v197, v193, s80\n\t" \
  "v_mfma_f32_16x16x32_bf16 a[52:55], a[200:203], v[8:11], a[52:55]\n\t" \
  "buffer_load_dwordx4 v[72:75], v242, %[ws], s73 offen offset:2048 sc1\n\t" \
  "v_mfma_f32_16x16x32_bf16 a[52:55], a[196:199], v[4:7], a[52:55]\n\t" \
  "ds_read_b64_tr_b16 a[214:215], v236 offset:12288\n\t" \
  "v_xor_b32 v237, 0x8080, v234\n\t" \
  "v_mfma_f32_16x16x32_bf16 a[52:55], a[192:195], v[0:3], a[52:55]\n\t" \
  "ds_read_b64_tr_b16 a[216:217], v237 offset:0\n\t" \
  "v_perm_b32 v193, v198, v194, s79\n\t" \
  "v_mfma_f32_16x16x32_bf16 a[56:59], a[192:195], v[32:35], a[56:59]\n\t" \
  "ds_read_b64_tr_b16 a[218:219], v237 offset:4096\n\t" \
  "v_mfma_f32_16x16x32_bf16 a[56:59], a[196:199], v[36:39], a[56:59]\n\t" \
  "ds_read_b64_tr_b16 a[220:221], v237 offset:8192\n\t" \
  "v_mfma_f32_16x16x32_bf16 a[56:59], a[200:203], v[40:43], a[56:59]\n\t" \
  "buffer_load_dwordx4 v[76:79], v242, %[ws], s73 offen offset:3072 sc1\n\t" \
  "ds_read_b64_tr_b16 a[222:223], v237 offset:12288\n\t" \
  "v_mfma_f32_16x16x32_bf16 a[56:59], a[204:207], v[44:47], a[56:59]\n\t" \
  "s_waitcnt lgkmcnt(4)\n\t" \
  "v_xor_b32 v235, 0x180a0, v234\n\t" \
  "v_perm_b32 v198, v198, v194, s80\n\t" \
  "v_mfma_f32_16x16x32_bf16 a[64:67], a[208:211], v[16:19], a[64:67]\n\t" \
  "s_add_u32 s75, s69, 0\n\t" \
  "ds_read_b64_tr_b16 a[192:193], v235 offset:0\n\t" \
  "v_mfma_f32_16x16x32_bf16 a[64:67], a[212:215], v[20:23], a[64:67]\n\t" \
  "s_waitcnt lgkmcnt(1)\n\t" \
  "s_waitcnt vmcnt(8)\n\t" \
  "v_cmp_ne_u32 vcc, %[tag], v246\n\t" \
  "s_and_b32 vcc_lo, vcc_lo, 0xfe0000\n\t" \
  "s_cbranch_scc1 .Lop_s3\n\t" \
  ".Lop_k3:\n\t" \
  "v_mfma_f32_16x16x32_bf16 a[64:67], a[216:219], v[24:27], a[64:67]\n\t" \
  "buffer_load_dwordx4 v240, %[wo], s71 offen offset:0 lds\n\t" \
  "v_mfma_f32_16x16x32_bf16 a[64:67], a[220:223], v[28:31], a[64:67]\n\t" \
  "ds_read_b64_tr_b16 a[194:195], v235 offset:4096\n\t" \
  "v_perm_b32 v194, v199, v195, s79\n\t" \
  "v_mfma_f32_16x16x32_bf16 a[68:71], a[220:223], v[12:15], a[68:71]\n\t" \
  "ds_read_b64_tr_b16 a[196:197], v235 offset:8192\n\t" \
  "v_mfma_f32_16x16x32_bf16 a[68:71], a[216:219], v[8:11], a[68:71]\n\t" \
  "ds_read_b64_tr_b16 a[198:199], v235 offset:12288\n\t" \
  "v_xor_b32 v236, 0x80a0, v234\n\t" \
  "v_mfma_f32_16x16x32_bf16 a[68:71], a[212:215], v[4:7], a[68:71]\n\t" \
  "buffer_load_dwordx4 v[208:211], v170, %[ws], s75 offen sc1\n\t" \
  "v_mfma_f32_16x16x32_bf16 a[68:71], a[208:211], v[0:3], a[68:71]\n\t" \
  "s_add_u32 s75, s69, 32768\n\t" \
  "ds_read_b64_tr_b16 a[200:201], v236 offset:0\n\t" \
  "v_perm_b32 v199, v199, v195, s80\n\t" \
  "v_mfma_f32_16x16x32_bf16 a[72:75], a[208:211], v[32:35], a[72:75]\n\t" \
  "ds_read_b64_tr_b16 a[202:203], v236 offset:4096\n\t" \
  "v_mfma_f32_16x16x32_bf16 a[72:75], a[212:215], v[36:39], a[72:75]\n\t" \
  "ds_read_b64_tr_b16 a[204:205], v236 offset:8192\n\t" \
  "v_mfma_f32_16x16x32_bf16 a[72:75], a[216:219], v[40:43], a[72:75]\n\t" \
  "ds_read_b64_tr_b16 a[206:207], v236 offset:12288\n\t" \
  "v_perm_b32 v195, v204, v200, s79\n\t" \
  "v_mfma_f32_16x16x32_bf16 a[72:75], a[220:223], v[44:47], a[72:75]\n\t" \
  "s_waitcnt lgkmcnt(4)\n\t" \
  "buffer_load_dwordx4 v[212:215], v170, %[ws], s75 offen sc1\n\t" \
  "v_mfma_f32_16x16x32_bf16 a[80:83], a[192:195], v[16:19], a[80:83]\n\t" \
  "s_add_u32 s75, s69, 65536\n\t" \
  "v_xor_b32 v237, 0x180c0, v234\n\t" \
  "v_mfma_f32_16x16x32_bf16 a[80:83], a[196:199], v[20:23], a[80:83]\n\t" \
  "s_waitcnt lgkmcnt(0)\n\t" \
  "ds_read_b64_tr_b16 a[208:209], v237 offset:0\n\t" \
  "v_mfma_f32_16x16x32_bf16 a[80:83], a[200:203], v[24:27], a[80:83]\n\t" \
  "ds_read_b64_tr_b16 a[210:211], v237 offset:4096\n\t" \
  "v_perm_b32 v204, v204, v200, s80\n\t" \
  "v_mfma_f32_16x16x32_bf16 a[80:83], a[204:207], v[28:31], a[80:83]\n\t" \
  "buffer_load_dwordx4 v[216:219], v170, %[ws], s75 offen sc1\n\t" \
  "v_mfma_f32_16x16x32_bf16 a[84:87], a[204:207], v[12:15], a[84:87]\n\t" \
  "s_add_u32 s75, s69, 98304\n\t" \
  "ds_read_b64_tr_b16 a[212:213], v237 offset:8192\n\t" \
  "v_mfma_f32_16x16x32_bf16 a[84:87], a[200:203], v[8:11], a[84:87]\n\t" \
  "ds_read_b64_tr_b16 a[214:215], v237 offset:12288\n\t" \
  "v_xor_b32 v235, 0x80c0, v234\n\t" \
  "v_mfma_f32_16x16x32_bf16 a[84:87], a[196:199], v[4:7], a[84:87]\n\t" \
  "ds_read_b64_tr_b16 a[216:217], v235 offset:0\n\t" \
  "v_perm_b32 v200, v205, v201, s79\n\t" \
  "v_mfma_f32_16x16x32_bf16 a[84:87], a[192:195], v[0:3], a[84:87]\n\t" \
  "ds_read_b64_tr_b16 a[218:219], v235 offset:4096\n\t" \
  "v_mfma_f32_16x16x32_bf16 a[88:91], a[192:195], v[32:35], a[88:91]\n\t" \
  "buffer_load_dwordx4 v[220:223], v170, %[ws], s75 offen sc1\n\t" \
  "v_mfma_f32_16x16x32_bf16 a[88:91], a[196:199], v[36:39], a[88:91]\n\t" \
  "ds_read_b64_tr_b16 a[220:221], v235 offset:8192\n\t" \
  "v_mfma_f32_16x16x32_bf16 a[88:91], a[200:203], v[40:43], a[88:91]\n\t" \
  "ds_read_b64_tr_b16 a[222:223], v235 offset:12288\n\t" \
  "v_perm_b32 v205, v205, v201, s80\n\t" \
  "v_mfma_f32_16x16x32_bf16 a[88:91], a[204:207], v[44:47], a[88:91]\n\t" \
  "s_waitcnt lgkmcnt(4)\n\t" \
  "v_xor_b32 v236, 0x180e0, v234\n\t" \
  "v_mfma_f32_16x16x32_bf16 a[96:99], a[208:211], v[16:19], a[96:99]\n\t" \
  "buffer_load_dwordx4 v241, %[wo], s71 offen offset:1024 lds\n\t" \
  "v_mfma_f32_16x16x32_bf16 a[96:99], a[212:215], v[20:23], a[96:99]\n\t" \
  "s_waitcnt lgkmcnt(0)\n\t" \
  "ds_read_b64_tr_b16 a[192:193], v236 offset:0\n\t" \
  "v_perm_b32 v201, v206, v202, s79\n\t" \
  "v_mfma_f32_16x16x32_bf16 a[96:99], a[216:219], v[24:27], a[96:99]\n\t" \
  "ds_read_b64_tr_b16 a[194:195], v236 offset:4096\n\t" \
  "v_mfma_f32_16x16x32_bf16 a[96:99], a[220:223], v[28:31], a[96:99]\n\t" \
  "ds_read_b64_tr_b16 a[196:197], v236 offset:8192\n\t" \
  "v_mfma_f32_16x16x32_bf16 a[100:103], a[220:223], v[12:15], a[100:103]\n\t" \
  "buffer_load_dwordx4 v238, %[wo], s71 offen offset:2048 lds\n\t" \
  "v_mfma_f32_16x16x32_bf16 a[100:103], a[216:219], v[8:11], a[100:103]\n\t" \
  "ds_read_b64_tr_b16 a[198:199], v236 offset:12288\n\t" \
  "v_xor_b32 v237, 0x80e0, v234\n\t" \
  ".Lhs13:\n\ts_cbranch_execz .Lhs13n\n\t" \
  "v_mfma_f32_16x16x32_bf16 a[100:103], a[212:215], v[4:7], a[100:103]\n\t" \
  "ds_read_b64_tr_b16 a[200:201], v237 offset:0\n\t" \
  "v_perm_b32 v206, v206, v202, s80\n\t" \
  "v_mfma_f32_16x16x32_bf16 a[100:103], a[208:211], v[0:3], a[100:103]\n\t" \
  "ds_read_b64_tr_b16 a[202:203], v237 offset:4096\n\t" \
  "v_mfma_f32_16x16x32_bf16 a[104:107], a[208:211], v[32:35], a[104:107]\n\t" \
  "ds_read_b64_tr_b16 a[204:205], v237 offset:8192\n\t" \
  "v_mfma_f32_16x16x32_bf16 a[104:107], a[212:215], v[36:39], a[104:107]\n\t" \
  "buffer_load_dwordx4 v239, %[wo], s71 offen offset:3072 lds\n\t" \
  "s_add_u32 m0, %[m], 16384\n\t" \
  "buffer_load_dword v224, v171, %[sr], s77 offen\n\t" \
  "v_mfma_f32_16x16x32_bf16 a[104:107], a[216:219], v[40:43], a[104:107]\n\t" \
  "s_xor_b32 s71, %[eso], 1015808\n\t" \
  "ds_read_b64_tr_b16 a[206:207], v237 offset:12288\n\t" \
  "v_perm_b32 v202, v207, v203, s79\n\t" \
  "v_mfma_f32_16x16x32_bf16 a[104:107], a[220:223], v[44:47], a[104:107]\n\t" \
  "s_waitcnt lgkmcnt(4)\n\t" \
  "v_xor_b32 v235, 0x14000, v234\n\t" \
  "v_mfma_f32_16x16x32_bf16 a[112:115], a[192:195], v[16:19], a[112:115]\n\t" \
  "ds_read_b64_tr_b16 a[208:209], v235 offset:0\n\t" \
  "v_mfma_f32_16x16x32_bf16 a[112:115], a[196:199], v[20:23], a[112:115]\n\t" \
  "s_waitcnt lgkmcnt(1)\n\t" \
  "ds_read_b64_tr_b16 a[210:211], v235 offset:4096\n\t" \
  "v_perm_b32 v207, v207, v203, s80\n\t" \
  "v_mfma_f32_16x16x32_bf16 a[112:115], a[200:203], v[24:27], a[112:115]\n\t" \
  "ds_read_b64_tr_b16 a[212:213], v235 offset:8192\n\t" \
  "v_mfma_f32_16x16x32_bf16 a[112:115], a[204:207], v[28:31], a[112:115]\n\t" \
  "buffer_load_dwordx4 v242, %[ws], s71 offen offset:0 sc1 lds\n\t" \
  "v_mfma_f32_16x16x32_bf16 a[116:119], a[204:207], v[12:15], a[116:119]\n\t" \
  "ds_read_b64_tr_b16 a[214:215], v235 offset:12288\n\t" \
  "v_mfma_f32_16x16x32_bf16 a[116:119], a[200:203], v[8:11], a[116:119]\n\t" \
  "ds_read_b64_tr_b16 a[216:217], v235 offset:32768\n\t" \
  "v_mfma_f32_16x16x32_bf16 a[116:119], a[196:199], v[4:7], a[116:119]\n\t" \
  "ds_read_b64_tr_b16 a[218:219], v235 offset:36864\n\t" \
  "v_mfma_f32_16x16x32_bf16 a[116:119], a[192:195], v[0:3], a[116:119]\n\t" \
  "buffer_load_dwordx4 v242, %[ws], s71 offen offset:1024 sc1 lds\n\t" \
  "v_mfma_f32_16x16x32_bf16 a[120:123], a[192:195], v[32:35], a[120:123]\n\t" \
  "s_xor_b32 s73, %[eso], 917504\n\t" \
  "ds_read_b64_tr_b16 a[220:221], v235 offset:40960\n\t" \
  "v_mfma_f32_16x16x32_bf16 a[120:123], a[196:199], v[36:39], a[120:123]\n\t" \
  "ds_read_b64_tr_b16 a[222:223], v235 offset:45056\n\t" \
  "v_mfma_f32_16x16x32_bf16 a[120:123], a[200:203], v[40:43], a[120:123]\n\t" \
  "v_mfma_f32_16x16x32_bf16 a[120:123], a[204:207], v[44:47], a[120:123]\n\t" \
  "s_waitcnt lgkmcnt(4)\n\t" \
  "buffer_load_dwordx4 v[16:19], v242, %[ws], s73 offen offset:0 sc1\n\t" \
  STP("v_writelane_b32 %[stv], s94, 30\n\t") \
  STP("v_writelane_b32 %[stv], s92, 31\n\t") \
  STP("s_memrealtime s[94:95]\n\t") \
  STP("s_memtime s[92:93]\n\t") \
  "v_mfma_f32_16x16x32_bf16 a[0:3], a[208:211], v[48:51], a[0:3]\n\t" \
  "v_xor_b32 v236, 0x14020, v234\n\t" \
  "v_mfma_f32_16x16x32_bf16 a[0:3], a[212:215], v[52:55], a[0:3]\n\t" \
  "s_waitcnt lgkmcnt(0)\n\t" \
  "ds_read_b64_tr_b16 a[192:193], v236 offset:0\n\t" \
  "v_mfma_f32_16x16x32_bf16 a[0:3], a[216:219], v[56:59], a[0:3]\n\t" \
  "ds_read_b64_tr_b16 a[194:195], v236 offset:4096\n\t" \
  "v_mfma_f32_16x16x32_bf16 a[0:3], a[220:223], v[60:63], a[0:3]\n\t" \
  "s_waitcnt vmcnt(16)\n\t" \
  "ds_read_b64_tr_b16 a[196:197], v236 offset:8192\n\t" \
  "v_mfma_f32_16x16x32_bf16 a[4:7], a[220:223], v[92:95], a[4:7]\n\t" \
  "buffer_load_dwordx4 v[20:23], v242, %[ws], s73 offen offset:1024 sc1\n\t" \
  "v_mfma_f32_16x16x32_bf16 a[4:7], a[216:219], v[88:91], a[4:7]\n\t" \
  "ds_read_b64_tr_b16 a[198:199], v236 offset:12288\n\t" \
  "v_mfma_f32_16x16x32_bf16 a[4:7], a[212:215], v[84:87], a[4:7]\n\t" \
  "ds_read_b64_tr_b16 a[200:201], v236 offset:32768\n\t" \
  "v_mfma_f32_16x16x32_bf16 a[4:7], a[208:211], v[80:83], a[4:7]\n\t" \
  "s_waitcnt vmcnt(13)\n\t" \
  "ds_read_b64_tr_b16 a[202:203], v236 offset:36864\n\t" \
  "v_mfma_f32_16x16x32_bf16 a[8:11], a[208:211], v[64:67], a[8:11]\n\t" \
  "buffer_load_dwordx4 v[24:27], v242, %[ws], s73 offen offset:2048 sc1\n\t" \
  "v_mfma_f32_16x16x32_bf16 a[8:11], a[212:215], v[68:71], a[8:11]\n\t" \
  "ds_read_b64_tr_b16 a[204:205], v236 offset:40960\n\t" \
  "v_mfma_f32_16x16x32_bf16 a[8:11], a[216:219], v[72:75], a[8:11]\n\t" \
  "ds_read_b64_tr_b16 a[206:207], v236 offset:45056\n\t" \
  "v_mfma_f32_16x16x32_bf16 a[8:11], a[220:223], v[76:79], a[8:11]\n\t" \
  "s_waitcnt lgkmcnt(4)\n\t" \
  "v_xor_b32 v237, 0x14040, v234\n\t" \
  "v_mfma_f32_16x16x32_bf16 a[16:19], a[192:195], v[48:51], a[16:19]\n\t" \
  "ds_read_b64_tr_b16 a[208:209], v237 offset:0\n\t" \
  "v_mfma_f32_16x16x32_bf16 a[16:19], a[196:199], v[52:55], a[16:19]\n\t" \
  "s_waitcnt lgkmcnt(1)\n\t" \
  "buffer_load_dwordx4 v[28:31], v242, %[ws], s73 offen offset:3072 sc1\n\t" \
  "s_xor_b32 s73, %[eso], 950272\n\t" \
  "v_mfma_f32_16x16x32_bf16 a[16:19], a[200:203], v[56:59], a[16:19]\n\t" \
  "ds_read_b64_tr_b16 a[210:211], v237 offset:4096\n\t" \
  "v_mfma_f32_16x16x32_bf16 a[16:19], a[204:207], v[60:63], a[16:19]\n\t" \
  "ds_read_b64_tr_b16 a[212:213], v237 offset:8192\n\t" \
  "v_mfma_f32_16x16x32_bf16 a[20:23], a[204:207], v[92:95], a[20:23]\n\t" \
  "ds_read_b64_tr_b16 a[214:215], v237 offset:12288\n\t" \
  "v_bfrev_b32 v225, 1\n\t" \
  "v_mfma_f32_16x16x32_bf16 a[20:23], a[200:203], v[88:91], a[20:23]\n\t" \
  "buffer_load_dwordx4 v[0:3], v242, %[ws], s73 offen offset:0 sc1\n\t" \
  "v_mfma_f32_16x16x32_bf16 a[20:23], a[196:199], v[84:87], a[20:23]\n\t" \
  "ds_read_b64_tr_b16 a[216:217], v237 offset:32768\n\t" \
  "v_bfrev_b32 v226, 1\n\t" \
  "v_mfma_f32_16x16x32_bf16 a[20:23], a[192:195], v[80:83], a[20:23]\n\t" \
  "ds_read_b64_tr_b16 a[218:219], v237 offset:36864\n\t" \
  "v_writelane_b32 v225, s65, 0\n\t" \
  "v_mfma_f32_16x16x32_bf16 a[24:27], a[192:195], v[64:67], a[24:27]\n\t" \
  "ds_read_b64_tr_b16 a[220:221], v237 offset:40960\n\t" \
  "v_writelane_b32 v225, s66, 1\n\t" \
  "v_mfma_f32_16x16x32_bf16 a[24:27], a[196:199], v[68:71], a[24:27]\n\t" \
  "buffer_load_dwordx4 v[4:7], v242, %[ws], s73 offen offset:1024 sc1\n\t" \
  "v_mfma_f32_16x16x32_bf16 a[24:27], a[200:203], v[72:75], a[24:27]\n\t" \
  "ds_read_b64_tr_b16 a[222:223], v237 offset:45056\n\t" \
  "v_writelane_b32 v226, s67, 2\n\t" \
  "v_mfma_f32_16x16x32_bf16 a[24:27], a[204:207], v[76:79], a[24:27]\n\t" \
  "s_waitcnt lgkmcnt(4)\n\t" \
  "s_waitcnt vmcnt(8)\n\t" \
  "v_xor_b32 v246, v243, v209\n\t" \
  "v_bitop3_b32 v246, v246, v211, v243 bitop3:0xf6\n\t" \
  "v_mfma_f32_16x16x32_bf16 a[32:35], a[208:211], v[48:51], a[32:35]\n\t" \
  "v_bitop3_b32 v246, v246, v213, v243 bitop3:0xf6\n\t" \
  "v_xor_b32 v235, 0x14060, v234\n\t" \
  "v_mfma_f32_16x16x32_bf16 a[32:35], a[212:215], v[52:55], a[32:35]\n\t" \
  "s_waitcnt lgkmcnt(0)\n\t" \
  "ds_read_b64_tr_b16 a[192:193], v235 offset:0\n\t" \
  "v_bitop3_b32 v246, v246, v215, v243 bitop3:0xf6\n\t" \
  "v_mfma_f32_16x16x32_bf16 a[32:35], a[216:219], v[56:59], a[32:35]\n\t" \
  "buffer_load_dwordx4 v[8:11], v242, %[ws], s73 offen offset:2048 sc1\n\t" \
  "v_mfma_f32_16x16x32_bf16 a[32:35], a[220:223], v[60:63], a[32:35]\n\t" \
  "s_mov_b32 s81, 0\n\t" \
  "ds_read_b64_tr_b16 a[194:195], v235 offset:4096\n\t" \
  "v_bitop3_b32 v246, v246, v217, v243 bitop3:0xf6\n\t" \
  "v_mfma_f32_16x16x32_bf16 a[36:39], a[220:223], v[92:95], a[36:39]\n\t" \
  "ds_read_b64_tr_b16 a[196:197], v235 offset:8192\n\t" \
  "v_bitop3_b32 v246, v246, v219, v243 bitop3:0xf6\n\t" \
  "v_mfma_f32_16x16x32_bf16 a[36:39], a[216:219], v[88:91], a[36:39]\n\t" \
  "ds_read_b64_tr_b16 a[198:199], v235 offset:12288\n\t" \
  "v_bitop3_b32 v246, v246, v221, v243 bitop3:0xf6\n\t" \
  "v_mfma_f32_16x16x32_bf16 a[36:39], a[212:215], v[84:87], a[36:39]\n\t" \
  "buffer_load_dwordx4 v[12:15], v242, %[ws], s73 offen offset:3072 sc1\n\t" \
  "s_xor_b32 s73, %[eso], 983040\n\t" \
  "v_mfma_f32_16x16x32_bf16 a[36:39], a[208:211], v[80:83], a[36:39]\n\t" \
  "ds_read_b64_tr_b16 a[200:201], v235 offset:32768\n\t" \
  "v_bitop3_b32 v246, v246, v223, v243 bitop3:0xf6\n\t" \
  "v_mfma_f32_16x16x32_bf16 a[40:43], a[208:211], v[64:67], a[40:43]\n\t" \
  "ds_read_b64_tr_b16 a[202:203], v235 offset:36864\n\t" \
  "v_mfma_f32_16x16x32_bf16 a[40:43], a[212:215], v[68:71], a[40:43]\n\t" \
  "ds_read_b64_tr_b16 a[204:205], v235 offset:40960\n\t" \
  "v_mfma_f32_16x16x32_bf16 a[40:43], a[216:219], v[72:75], a[40:43]\n\t" \
  "ds_read_b64_tr_b16 a[206:207], v235 offset:45056\n\t" \
  "v_mfma_f32_16x16x32_bf16 a[40:43], a[220:223], v[76:79], a[40:43]\n\t" \
  "s_waitcnt lgkmcnt(4)\n\t" \
  "buffer_load_dwordx4 v[32:35], v242, %[ws], s73 offen offset:0 sc1\n\t" \
  "v_mfma_f32_16x16x32_bf16 a[48:51], a[192:195], v[48:51], a[48:51]\n\t" \
  "v_cmp_ne_u32 vcc, 0, v246\n\t" \
  "s_cbranch_vccnz .Lop_s4\n\t" \
  ".Lop_k4:\n\t" \
  "v_mfma_f32_16x16x32_bf16 a[48:51], a[196:199], v[52:55], a[48:51]\n\t" \
  "s_waitcnt lgkmcnt(0)\n\t" \
  "v_add_f32 v208, v208, v212\n\t" \
  "v_add_f32 v216, v216, v220\n\t" \
  "v_add_f32 v210, v210, v214\n\t" \
  "v_mfma_f32_16x16x32_bf16 a[48:51], a[200:203], v[56:59], a[48:51]\n\t" \
  "v_xor_b32 v236, 0x14080, v234\n\t" \
  "v_add_f32 v218, v218, v222\n\t" \
  "v_add_f32 v208, v208, v216\n\t" \
  "v_mfma_f32_16x16x32_bf16 a[48:51], a[204:207], v[60:63], a[48:51]\n\t" \
  "s_barrier\n\t" \
  "v_mfma_f32_16x16x32_bf16 a[52:55], a[204:207], v[92:95], a[52:55]\n\t" \
  "buffer_load_dwordx4 v[36:39], v242, %[ws], s73 offen offset:1024 sc1\n\t" \
  "v_add_f32 v210, v210, v218\n\t" \
  "v_mfma_f32_16x16x32_bf16 a[52:55], a[200:203], v[88:91], a[52:55]\n\t" \
  "ds_read_b64_tr_b16 a[208:209], v236 offset:0\n\t" \
  "ds_read_b64_tr_b16 a[210:211], v236 offset:4096\n\t" \
  "v_lshlrev_b32 v163, 16, v224\n\t" \
  "v_mfma_f32_16x16x32_bf16 a[52:55], a[196:199], v[84:87], a[52:55]\n\t" \
  "ds_read_b64_tr_b16 a[212:213], v236 offset:8192\n\t" \
  "ds_read_b64_tr_b16 a[214:215], v236 offset:12288\n\t" \
  "v_and_b32 v164, 0xffff0000, v224\n\t" \
  "v_mfma_f32_16x16x32_bf16 a[52:55], a[192:195], v[80:83], a[52:55]\n\t" \
  "buffer_load_dwordx4 v[40:43], v242, %[ws], s73 offen offset:2048 sc1\n\t" \
  "v_cvt_pk_bf16_f32 v162, v208, v210\n\t" \
  "v_mfma_f32_16x16x32_bf16 a[56:59], a[192:195], v[64:67], a[56:59]\n\t" \
  "ds_read_b64_tr_b16 a[216:217], v236 offset:32768\n\t" \
  "ds_read_b64_tr_b16 a[218:219], v236 offset:36864\n\t" \
  "v_lshlrev_b32 v160, 16, v162\n\t" \
  "v_mfma_f32_16x16x32_bf16 a[56:59], a[196:199], v[68:71], a[56:59]\n\t" \
  "ds_read_b64_tr_b16 a[220:221], v236 offset:40960\n\t" \
  "v_and_b32 v161, 0xffff0000, v162\n\t" \
  "v_mfma_f32_16x16x32_bf16 a[56:59], a[200:203], v[72:75], a[56:59]\n\t" \
  "ds_read_b64_tr_b16 a[222:223], v236 offset:45056\n\t" \
  "v_add_f32 v160, v160, v172\n\t" \
  "v_mfma_f32_16x16x32_bf16 a[56:59], a[204:207], v[76:79], a[56:59]\n\t" \
  "s_waitcnt lgkmcnt(4)\n\t" \
  "v_add_f32 v161, v161, v172\n\t" \
  "v_xor_b32 v237, 0x140a0, v234\n\t" \
  "v_mfma_f32_16x16x32_bf16 a[64:67], a[208:211], v[48:51], a[64:67]\n\t" \
  "buffer_load_dwordx4 v[44:47], v242, %[ws], s73 offen offset:3072 sc1\n\t" \
  "v_mfma_f32_16x16x32_bf16 a[64:67], a[212:215], v[52:55], a[64:67]\n\t" \
  "s_waitcnt lgkmcnt(0)\n\t" \
  "ds_read_b64_tr_b16 a[192:193], v237 offset:0\n\t" \
  "v_cvt_pk_bf16_f32 v162, v160, v161\n\t" \
  "v_mfma_f32_16x16x32_bf16 a[64:67], a[216:219], v[56:59], a[64:67]\n\t" \
  "ds_read_b64_tr_b16 a[194:195], v237 offset:4096\n\t" \
  "v_lshlrev_b32 v160, 16, v162\n\t" \
  "v_mfma_f32_16x16x32_bf16 a[64:67], a[220:223], v[60:63], a[64:67]\n\t" \
  "ds_read_b64_tr_b16 a[196:197], v237 offset:8192\n\t" \
  "v_and_b32 v161, 0xffff0000, v162\n\t" \
  "v_mfma_f32_16x16x32_bf16 a[68:71], a[220:223], v[92:95], a[68:71]\n\t" \
  "buffer_load_dwordx4 v242, %[ws], s71 offen offset:2048 sc1 lds\n\t" \
  "v_mfma_f32_16x16x32_bf16 a[68:71], a[216:219], v[88:91], a[68:71]\n\t" \
  "ds_read_b64_tr_b16 a[198:199], v237 offset:12288\n\t" \
  "ds_write_b64 v174, v[160:161] offset:2560\n\t" \
  "v_mfma_f32_16x16x32_bf16 a[68:71], a[212:215], v[84:87], a[68:71]\n\t" \
  "ds_read_b64_tr_b16 a[200:201], v237 offset:32768\n\t" \
  "v_mul_f32 v166, v160, v163\n\t" \
  "v_mfma_f32_16x16x32_bf16 a[68:71], a[208:211], v[80:83], a[68:71]\n\t" \
  "ds_read_b64_tr_b16 a[202:203], v237 offset:36864\n\t" \
  "v_mul_f32 v167, v161, v164\n\t" \
  "v_mfma_f32_16x16x32_bf16 a[72:75], a[208:211], v[64:67], a[72:75]\n\t" \
  "ds_read_b64_tr_b16 a[204:205], v237 offset:40960\n\t" \
  "v_cvt_pk_bf16_f32 v168, v166, v167\n\t" \
  "v_mfma_f32_16x16x32_bf16 a[72:75], a[212:215], v[68:71], a[72:75]\n\t" \
  "buffer_load_dwordx4 v242, %[ws], s71 offen offset:3072 sc1 lds\n\t" \
  "s_add_u32 m0, %[m], 32768\n\t" \
  "v_mfma_f32_16x16x32_bf16 a[72:75], a[216:219], v[72:75], a[72:75]\n\t" \
  "s_xor_b32 s71, %[eso], 360448\n\t" \
  "ds_read_b64_tr_b16 a[206:207], v237 offset:45056\n\t" \
  "v_add_f32 v162, v166, v167\n\t" \
  "v_mfma_f32_16x16x32_bf16 a[72:75], a[220:223], v[76:79], a[72:75]\n\t" \
  "s_waitcnt lgkmcnt(5)\n\t" \
  "v_lshlrev_b32 v163, 16, v168\n\t" \
  "v_xor_b32 v235, 0x140c0, v234\n\t" \
  "v_mfma_f32_16x16x32_bf16 a[80:83], a[192:195], v[48:51], a[80:83]\n\t" \
  "ds_read_b64_tr_b16 a[208:209], v235 offset:0\n\t" \
  "v_and_b32 v164, 0xffff0000, v168\n\t" \
  "v_mfma_f32_16x16x32_bf16 a[80:83], a[196:199], v[52:55], a[80:83]\n\t" \
  "s_waitcnt lgkmcnt(1)\n\t" \
  "buffer_load_dwordx4 v242, %[ws], s71 offen offset:0 sc1 lds\n\t" \
  "v_mfma_f32_16x16x32_bf16 a[80:83], a[200:203], v[56:59], a[80:83]\n\t" \
  "ds_read_b64_tr_b16 a[210:211], v235 offset:4096\n\t" \
  "v_add_f32_dpp v162, v162, v162 row_ror:8 row_mask:0xf bank_mask:0xf\n\t" \
  "v_mfma_f32_16x16x32_bf16 a[80:83], a[204:207], v[60:63], a[80:83]\n\t" \
  "ds_read_b64_tr_b16 a[212:213], v235 offset:8192\n\t" \
  "v_sub_f32 v166, v166, v163\n\t" \
  "v_mfma_f32_16x16x32_bf16 a[84:87], a[204:207], v[92:95], a[84:87]\n\t" \
  "ds_read_b64_tr_b16 a[214:215], v235 offset:12288\n\t" \
  "v_sub_f32 v167, v167, v164\n\t" \
  "v_mfma_f32_16x16x32_bf16 a[84:87], a[200:203], v[88:91], a[84:87]\n\t" \
  "buffer_load_dwordx4 v242, %[ws], s71 offen offset:1024 sc1 lds\n\t" \
  "v_mfma_f32_16x16x32_bf16 a[84:87], a[196:199], v[84:87], a[84:87]\n\t" \
  "ds_read_b64_tr_b16 a[216:217], v235 offset:32768\n\t" \
  "v_add_f32_dpp v162, v162, v162 row_ror:4 row_mask:0xf bank_mask:0xf\n\t" \
  "v_mfma_f32_16x16x32_bf16 a[84:87], a[192:195], v[80:83], a[84:87]\n\t" \
  "ds_read_b64_tr_b16 a[218:219], v235 offset:36864\n\t" \
  "v_cvt_pk_bf16_f32 v169, v166, v167\n\t" \
  "v_mfma_f32_16x16x32_bf16 a[88:91], a[192:195], v[64:67], a[88:91]\n\t" \
  "s_mov_b32 s82, 0\n\t" \
  "ds_read_b64_tr_b16 a[220:221], v235 offset:40960\n\t" \
  "v_add_f32_dpp v162, v162, v162 row_ror:2 row_mask:0xf bank_mask:0xf\n\t" \
  "v_mfma_f32_16x16x32_bf16 a[88:91], a[196:199], v[68:71], a[88:91]\n\t" \
  "s_mov_b32 s83, 0xffff\n\t" \
  "ds_read_b64_tr_b16 a[222:223], v235 offset:45056\n\t" \
  "v_add_f32_dpp v162, v162, v162 row_ror:1 row_mask:0xf bank_mask:0xf\n\t" \
  "v_mfma_f32_16x16x32_bf16 a[88:91], a[200:203], v[72:75], a[88:91]\n\t" \
  "buffer_load_dwordx4 v242, %[ws], s71 offen offset:2048 sc1 lds\n\t" \
  "v_mfma_f32_16x16x32_bf16 a[88:91], a[204:207], v[76:79], a[88:91]\n\t" \
  "s_waitcnt lgkmcnt(4)\n\t" \
  "v_xor_b32 v236, 0x140e0, v234\n\t" \
  "v_mfma_f32_16x16x32_bf16 a[96:99], a[208:211], v[48:51], a[96:99]\n\t" \
  "ds_read_b64_tr_b16 a[192:193], v236 offset:0\n\t" \
  "v_mfma_f32_16x16x32_bf16 a[96:99], a[212:215], v[52:55], a[96:99]\n\t" \
  "s_waitcnt lgkmcnt(1)\n\t" \
  "s_mov_b64 exec, s[82:83]\n\t" \
  "ds_write_b32 v173, v168 offset:1040\n\t" \
  "ds_write_b32 v173, v169 offset:1120\n\t" \
  "s_mov_b64 exec, -1\n\t" \
  "v_mfma_f32_16x16x32_bf16 a[96:99], a[216:219], v[56:59], a[96:99]\n\t" \
  "buffer_load_dwordx4 v242, %[ws], s71 offen offset:3072 sc1 lds\n\t" \
  "s_add_u32 m0, %[m], 98304\n\t" \
  "v_mfma_f32_16x16x32_bf16 a[96:99], a[220:223], v[60:63], a[96:99]\n\t" \
  "s_xor_b32 s71, %[eso], 491520\n\t" \
  "ds_read_b64_tr_b16 a[194:195], v236 offset:4096\n\t" \
  "v_readlane_b32 s87, v162, 32\n\t" \
  "v_mfma_f32_16x16x32_bf16 a[100:103], a[220:223], v[92:95], a[100:103]\n\t" \
  "ds_read_b64_tr_b16 a[196:197], v236 offset:8192\n\t" \
  "v_readlane_b32 s88, v162, 16\n\t" \
  "v_mfma_f32_16x16x32_bf16 a[100:103], a[216:219], v[88:91], a[100:103]\n\t" \
  "ds_read_b64_tr_b16 a[198:199], v236 offset:12288\n\t" \
  "v_mfma_f32_16x16x32_bf16 a[100:103], a[212:215], v[84:87], a[100:103]\n\t" \
  "s_xor_b32 s73, %[eso], 196608\n\t" \
  "ds_read_b64_tr_b16 a[200:201], v236 offset:32768\n\t" \
  "v_mfma_f32_16x16x32_bf16 a[100:103], a[208:211], v[80:83], a[100:103]\n\t" \
  "buffer_load_dwordx4 v242, %[ws], s71 offen offset:0 sc1 lds\n\t" \
  "v_mfma_f32_16x16x32_bf16 a[104:107], a[208:211], v[64:67], a[104:107]\n\t" \
  "ds_read_b64_tr_b16 a[202:203], v236 offset:36864\n\t" \
  "v_mfma_f32_16x16x32_bf16 a[104:107], a[212:215], v[68:71], a[104:107]\n\t" \
  "ds_read_b64_tr_b16 a[204:205], v236 offset:40960\n\t" \
  "v_mfma_f32_16x16x32_bf16 a[104:107], a[216:219], v[72:75], a[104:107]\n\t" \
  "ds_read_b64_tr_b16 a[206:207], v236 offset:45056\n\t" \
  "v_mfma_f32_16x16x32_bf16 a[104:107], a[220:223], v[76:79], a[104:107]\n\t" \
  "s_waitcnt lgkmcnt(4)\n\t" \
  "buffer_load_dwordx4 v[208:211], v242, %[ws], s73 offen offset:0 sc1\n\t" \
  "v_mfma_f32_16x16x32_bf16 a[112:115], a[192:195], v[48:51], a[112:115]\n\t" \
  "v_xor_b32 v237, 0x20040, v234\n\t" \
  "v_mfma_f32_16x16x32_bf16 a[112:115], a[196:199], v[52:55], a[112:115]\n\t" \
  "s_waitcnt lgkmcnt(0)\n\t" \
  "ds_read_b64_tr_b16 a[208:209], v237 offset:0\n\t" \
  "v_mfma_f32_16x16x32_bf16 a[112:115], a[200:203], v[56:59], a[112:115]\n\t" \
  "ds_read_b64_tr_b16 a[210:211], v237 offset:4096\n\t" \
  "v_mfma_f32_16x16x32_bf16 a[112:115], a[204:207], v[60:63], a[112:115]\n\t" \
  "buffer_load_dwordx4 v[212:215], v242, %[ws], s73 offen offset:1024 sc1\n\t" \
  "v_mfma_f32_16x16x32_bf16 a[116:119], a[204:207], v[92:95], a[116:119]\n\t" \
  "ds_read_b64_tr_b16 a[212:213], v237 offset:8192\n\t" \
  "v_mfma_f32_16x16x32_bf16 a[116:119], a[200:203], v[88:91], a[116:119]\n\t" \
  "ds_read_b64_tr_b16 a[214:215], v237 offset:12288\n\t" \
  "v_xor_b32 v235, 0x40, v234\n\t" \
  "v_mfma_f32_16x16x32_bf16 a[116:119], a[196:199], v[84:87], a[116:119]\n\t" \
  "ds_read_b64_tr_b16 a[216:217], v235 offset:0\n\t" \
  "v_mfma_f32_16x16x32_bf16 a[116:119], a[192:195], v[80:83], a[116:119]\n\t" \
  "ds_read_b64_tr_b16 a[218:219], v235 offset:4096\n\t" \
  "v_mfma_f32_16x16x32_bf16 a[120:123], a[192:195], v[64:67], a[120:123]\n\t" \
  "buffer_load_dwordx4 v[216:219], v242, %[ws], s73 offen offset:2048 sc1\n\t" \
  "v_mfma_f32_16x16x32_bf16 a[120:123], a[196:199], v[68:71], a[120:123]\n\t" \
  "s_mov_b32 s74, 16384\n\t" \
  "ds_read_b64_tr_b16 a[220:221], v235 offset:8192\n\t" \
  "v_mfma_f32_16x16x32_bf16 a[120:123], a[200:203], v[72:75], a[120:123]\n\t" \
  "ds_read_b64_tr_b16 a[222:223], v235 offset:12288\n\t" \
  "v_mfma_f32_16x16x32_bf16 a[120:123], a[204:207], v[76:79], a[120:123]\n\t" \
  "s_waitcnt vmcnt(18) lgkmcnt(4)\n\t" \
  "v_xor_b32 v236, 0x20060, v234\n\t" \
  STP("v_writelane_b32 %[stv], s94, 32\n\t") \
  STP("v_writelane_b32 %[stv], s92, 33\n\t") \
  STP("s_memrealtime s[94:95]\n\t") \
  STP("s_memtime s[92:93]\n\t") \
  "v_mfma_f32_16x16x32_bf16 a[32:35], a[208:211], v[16:19], a[32:35]\n\t" \
  "buffer_load_dwordx4 v[220:223], v242, %[ws], s73 offen offset:3072 sc1\n\t" \
  "v_mfma_f32_16x16x32_bf16 a[32:35], a[212:215], v[20:23], a[32:35]\n\t" \
  "s_waitcnt lgkmcnt(0)\n\t" \
  "ds_read_b64_tr_b16 a[192:193], v236 offset:0\n\t" \
  "v_mfma_f32_16x16x32_bf16 a[32:35], a[216:219], v[24:27], a[32:35]\n\t" \
  "ds_read_b64_tr_b16 a[194:195], v236 offset:4096\n\t" \
  "v_mfma_f32_16x16x32_bf16 a[32:35], a[220:223], v[28:31], a[32:35]\n\t" \
  "s_waitcnt vmcnt(15)\n\t" \
  "ds_read_b64_tr_b16 a[196:197], v236 offset:8192\n\t" \
  "v_mfma_f32_16x16x32_bf16 a[36:39], a[220:223], v[12:15], a[36:39]\n\t" \
  "ds_read_b64_tr_b16 a[198:199], v236 offset:12288\n\t" \
  "v_xor_b32 v237, 0x60, v234\n\t" \
  "v_mfma_f32_16x16x32_bf16 a[36:39], a[216:219], v[8:11], a[36:39]\n\t" \
  "buffer_load_dwordx4 v242, %[ws], s71 offen offset:1024 sc1 lds\n\t" \
  "v_mfma_f32_16x16x32_bf16 a[36:39], a[212:215], v[4:7], a[36:39]\n\t" \
  "ds_read_b64_tr_b16 a[200:201], v237 offset:0\n\t" \
  "v_mfma_f32_16x16x32_bf16 a[36:39], a[208:211], v[0:3], a[36:39]\n\t" \
  "s_waitcnt vmcnt(12)\n\t" \
  "ds_read_b64_tr_b16 a[202:203], v237 offset:4096\n\t" \
  "v_mfma_f32_16x16x32_bf16 a[40:43], a[208:211], v[32:35], a[40:43]\n\t" \
  "ds_read_b64_tr_b16 a[204:205], v237 offset:8192\n\t" \
  "v_mfma_f32_16x16x32_bf16 a[40:43], a[212:215], v[36:39], a[40:43]\n\t" \
  "buffer_load_dwordx4 v242, %[ws], s71 offen offset:2048 sc1 lds\n\t" \
  "v_mfma_f32_16x16x32_bf16 a[40:43], a[216:219], v[40:43], a[40:43]\n\t" \
  "ds_read_b64_tr_b16 a[206:207], v237 offset:12288\n\t" \
  "v_xor_b32 v235, 0x20000, v234\n\t" \
  "v_mfma_f32_16x16x32_bf16 a[40:43], a[220:223], v[44:47], a[40:43]\n\t" \
  "s_waitcnt lgkmcnt(4)\n\t" \
  "ds_read_b64_tr_b16 v[64:65], v235 offset:0\n\t" \
  "v_mfma_f32_16x16x32_bf16 a[48:51], a[192:195], v[16:19], a[48:51]\n\t" \
  "ds_read_b64_tr_b16 v[66:67], v235 offset:4096\n\t" \
  "v_mfma_f32_16x16x32_bf16 a[48:51], a[196:199], v[20:23], a[48:51]\n\t" \
  "s_waitcnt lgkmcnt(2)\n\t" \
  "buffer_load_dwordx4 v242, %[ws], s71 offen offset:3072 sc1 lds\n\t" \
  "v_mfma_f32_16x16x32_bf16 a[48:51], a[200:203], v[24:27], a[48:51]\n\t" \
  "ds_read_b64_tr_b16 v[68:69], v235 offset:8192\n\t" \
  "v_mfma_f32_16x16x32_bf16 a[48:51], a[204:207], v[28:31], a[48:51]\n\t" \
  "ds_read_b64_tr_b16 v[70:71], v235 offset:12288\n\t" \
  "v_xor_b32 v236, 0x0, v234\n\t" \
  "v_mfma_f32_16x16x32_bf16 a[52:55], a[204:207], v[12:15], a[52:55]\n\t" \
  "ds_read_b64_tr_b16 v[72:73], v236 offset:0\n\t" \
  "v_accvgpr_read_b32 v160, a32\n\t" \
  "v_mfma_f32_16x16x32_bf16 a[52:55], a[200:203], v[8:11], a[52:55]\n\t" \
  "buffer_load_dwordx2 v[48:49], v252, %[sd], s74 offen offset:0\n\t" \
  "v_mfma_f32_16x16x32_bf16 a[52:55], a[196:199], v[4:7], a[52:55]\n\t" \
  "ds_read_b64_tr_b16 v[74:75], v236 offset:4096\n\t" \
  "ds_read_b64_tr_b16 v[76:77], v236 offset:8192\n\t" \
  "v_mfma_f32_16x16x32_bf16 a[52:55], a[192:195], v[0:3], a[52:55]\n\t" \
  "ds_read_b64_tr_b16 v[78:79], v236 offset:12288\n\t" \
  "v_accvgpr_read_b32 v161, a33\n\t" \
  "v_mfma_f32_16x16x32_bf16 a[56:59], a[192:195], v[32:35], a[56:59]\n\t" \
  "v_xor_b32 v237, 0x20020, v234\n\t" \
  "v_accvgpr_read_b32 v162, a34\n\t" \
  "v_mfma_f32_16x16x32_bf16 a[56:59], a[196:199], v[36:39], a[56:59]\n\t" \
  "buffer_load_dwordx2 v[50:51], v252, %[sd], s74 offen offset:2048\n\t" \
  "s_mov_b32 s74, 20480\n\t" \
  "v_mfma_f32_16x16x32_bf16 a[56:59], a[200:203], v[40:43], a[56:59]\n\t" \
  "ds_read_b64_tr_b16 v[80:81], v237 offset:0\n\t" \
  "v_accvgpr_read_b32 v163, a35\n\t" \
  "v_mfma_f32_16x16x32_bf16 a[56:59], a[204:207], v[44:47], a[56:59]\n\t" \
  "s_waitcnt lgkmcnt(5)\n\t" \
  "ds_read_b64_tr_b16 v[82:83], v237 offset:4096\n\t" \
  "v_accvgpr_read_b32 v164, a48\n\t" \
  "v_mfma_f32_16x16x32_bf16 a[0:3], v[64:67], v[16:19], a[0:3]\n\t" \
  "ds_read_b64_tr_b16 v[84:85], v237 offset:8192\n\t" \
  "v_accvgpr_read_b32 v165, a49\n\t" \
  "v_mfma_f32_16x16x32_bf16 a[0:3], v[68:71], v[20:23], a[0:3]\n\t" \
  "s_waitcnt lgkmcnt(3)\n\t" \
  "buffer_load_dwordx2 v[52:53], v252, %[sd], s74 offen offset:0\n\t" \
  "v_accvgpr_read_b32 v166, a50\n\t" \
  "v_mfma_f32_16x16x32_bf16 a[0:3], v[72:75], v[24:27], a[0:3]\n\t" \
  "ds_read_b64_tr_b16 v[86:87], v237 offset:12288\n\t" \
  "v_accvgpr_read_b32 v167, a51\n\t" \
  "v_cvt_pk_bf16_f32 v168, v160, v161\n\t" \
  "v_mfma_f32_16x16x32_bf16 a[0:3], v[76:79], v[28:31], a[0:3]\n\t" \
  "v_xor_b32 v235, 0x20, v234\n\t" \
  "v_cvt_pk_bf16_f32 v169, v162, v163\n\t" \
  "v_cvt_pk_bf16_f32 v170, v164, v165\n\t" \
  "v_mfma_f32_16x16x32_bf16 a[4:7], v[76:79], v[12:15], a[4:7]\n\t" \
  "ds_read_b64_tr_b16 v[88:89], v235 offset:0\n\t" \
  "v_cvt_pk_bf16_f32 v171, v166, v167\n\t" \
  "s_xor_b32 s76, s60, 1024\n\t" \
  "v_mfma_f32_16x16x32_bf16 a[4:7], v[72:75], v[8:11], a[4:7]\n\t" \
  "buffer_store_dwordx4 v[168:171], v242, %[ws], s76 offen sc1\n\t" \
  "v_accvgpr_read_b32 v160, a36\n\t" \
  "v_mfma_f32_16x16x32_bf16 a[4:7], v[68:71], v[4:7], a[4:7]\n\t" \
  "ds_read_b64_tr_b16 v[90:91], v235 offset:4096\n\t" \
  "ds_read_b64_tr_b16 v[92:93], v235 offset:8192\n\t" \
  "v_accvgpr_read_b32 v161, a37\n\t" \
  "v_mfma_f32_16x16x32_bf16 a[4:7], v[64:67], v[0:3], a[4:7]\n\t" \
  "buffer_load_dwordx2 v[54:55], v252, %[sd], s74 offen offset:2048\n\t" \
  "s_mov_b32 s74, 24576\n\t" \
  "v_mfma_f32_16x16x32_bf16 a[8:11], v[64:67], v[32:35], a[8:11]\n\t" \
  "ds_read_b64_tr_b16 v[94:95], v235 offset:12288\n\t" \
  "v_accvgpr_read_b32 v162, a38\n\t" \
  "v_accvgpr_read_b32 v163, a39\n\t" \
  "v_mfma_f32_16x16x32_bf16 a[8:11], v[68:71], v[36:39], a[8:11]\n\t" \
  "v_accvgpr_read_b32 v164, a52\n\t" \
  "v_accvgpr_read_b32 v165, a53\n\t" \
  "v_accvgpr_read_b32 v166, a54\n\t" \
  "v_mfma_f32_16x16x32_bf16 a[8:11], v[72:75], v[40:43], a[8:11]\n\t" \
  "v_accvgpr_read_b32 v167, a55\n\t" \
  "v_cvt_pk_bf16_f32 v174, v160, v161\n\t" \
  "v_cvt_pk_bf16_f32 v175, v162, v163\n\t" \
  "v_mfma_f32_16x16x32_bf16 a[8:11], v[76:79], v[44:47], a[8:11]\n\t" \
  "s_waitcnt lgkmcnt(4)\n\t" \
  "buffer_load_dwordx2 v[56:57], v252, %[sd], s74 offen offset:0\n\t" \
  "v_cvt_pk_bf16_f32 v172, v164, v165\n\t" \
  "v_mfma_f32_16x16x32_bf16 a[16:19], v[80:83], v[16:19], a[16:19]\n\t" \
  "v_xor_b32 v236, 0x20080, v234\n\t" \
  "v_cvt_pk_bf16_f32 v173, v166, v167\n\t" \
  "s_xor_b32 s76, s61, 1024\n\t" \
  "v_mfma_f32_16x16x32_bf16 a[16:19], v[84:87], v[20:23], a[16:19]\n\t" \
  "s_waitcnt lgkmcnt(0)\n\t" \
  "buffer_store_dwordx4 v[172:175], v242, %[ws], s76 offen sc1\n\t" \
  "v_mfma_f32_16x16x32_bf16 a[16:19], v[88:91], v[24:27], a[16:19]\n\t" \
  "ds_read_b64_tr_b16 a[208:209], v236 offset:0\n\t" \
  "ds_read_b64_tr_b16 a[210:211], v236 offset:4096\n\t" \
  "v_mfma_f32_16x16x32_bf16 a[16:19], v[92:95], v[28:31], a[16:19]\n\t" \
  "s_barrier\n\t" \
  "v_mfma_f32_16x16x32_bf16 a[20:23], v[92:95], v[12:15], a[20:23]\n\t" \
  "ds_read_b64_tr_b16 a[212:213], v236 offset:8192\n\t" \
  "ds_read_b64_tr_b16 a[214:215], v236 offset:12288\n\t" \
  "v_mfma_f32_16x16x32_bf16 a[20:23], v[88:91], v[8:11], a[20:23]\n\t" \
  "buffer_load_dwordx2 v[58:59], v252, %[sd], s74 offen offset:2048\n\t" \
  "s_mov_b32 s74, 28672\n\t" \
  "v_mfma_f32_16x16x32_bf16 a[20:23], v[84:87], v[4:7], a[20:23]\n\t" \
  "v_xor_b32 v237, 0x80, v234\n\t" \
  "v_accvgpr_read_b32 v160, a0\n\t" \
  "v_mfma_f32_16x16x32_bf16 a[20:23], v[80:83], v[0:3], a[20:23]\n\t" \
  "ds_read_b64_tr_b16 a[216:217], v237 offset:0\n\t" \
  "ds_read_b64_tr_b16 a[218:219], v237 offset:4096\n\t" \
  "v_mfma_f32_16x16x32_bf16 a[24:27], v[80:83], v[32:35], a[24:27]\n\t" \
  "ds_read_b64_tr_b16 a[220:221], v237 offset:8192\n\t" \
  "ds_read_b64_tr_b16 a[222:223], v237 offset:12288\n\t" \
  "v_mfma_f32_16x16x32_bf16 a[24:27], v[84:87], v[36:39], a[24:27]\n\t" \
  "buffer_load_dwordx2 v[60:61], v252, %[sd], s74 offen offset:0\n\t" \
  "v_mfma_f32_16x16x32_bf16 a[24:27], v[88:91], v[40:43], a[24:27]\n\t" \
  "v_xor_b32 v235, 0x200a0, v234\n\t" \
  "v_accvgpr_read_b32 v161, a1\n\t" \
  "v_mfma_f32_16x16x32_bf16 a[24:27], v[92:95], v[44:47], a[24:27]\n\t" \
  "s_waitcnt lgkmcnt(4)\n\t" \
  "ds_read_b64_tr_b16 a[192:193], v235 offset:0\n\t" \
  "v_accvgpr_read_b32 v162, a2\n\t" \
  "v_accvgpr_read_b32 v163, a3\n\t" \
  "v_mfma_f32_16x16x32_bf16 a[64:67], a[208:211], v[16:19], a[64:67]\n\t" \
  "ds_read_b64_tr_b16 a[194:195], v235 offset:4096\n\t" \
  "v_accvgpr_read_b32 v164, a16\n\t" \
  "v_accvgpr_read_b32 v165, a17\n\t" \
  "v_mfma_f32_16x16x32_bf16 a[64:67], a[212:215], v[20:23], a[64:67]\n\t" \
  "s_waitcnt lgkmcnt(2)\n\t" \
  "buffer_load_dwordx2 v[62:63], v252, %[sd], s74 offen offset:2048\n\t" \
  "v_accvgpr_read_b32 v166, a18\n\t" \
  "v_mfma_f32_16x16x32_bf16 a[64:67], a[216:219], v[24:27], a[64:67]\n\t" \
  "ds_read_b64_tr_b16 a[196:197], v235 offset:8192\n\t" \
  "v_accvgpr_read_b32 v167, a19\n\t" \
  "v_cvt_pk_bf16_f32 v168, v160, v161\n\t" \
  "v_mfma_f32_16x16x32_bf16 a[64:67], a[220:223], v[28:31], a[64:67]\n\t" \
  "ds_read_b64_tr_b16 a[198:199], v235 offset:12288\n\t" \
  "v_xor_b32 v236, 0xa0, v234\n\t" \
  "v_cvt_pk_bf16_f32 v169, v162, v163\n\t" \
  "v_mfma_f32_16x16x32_bf16 a[68:71], a[220:223], v[12:15], a[68:71]\n\t" \
  "ds_read_b64_tr_b16 a[200:201], v236 offset:0\n\t" \
  "v_cvt_pk_bf16_f32 v170, v164, v165\n\t" \
  "v_cvt_pk_bf16_f32 v171, v166, v167\n\t" \
  "v_mfma_f32_16x16x32_bf16 a[68:71], a[216:219], v[8:11], a[68:71]\n\t" \
  "s_xor_b32 s76, s60, 0\n\t" \
  "ds_read_b64_tr_b16 a[202:203], v236 offset:4096\n\t" \
  "buffer_store_dwordx4 v[168:171], v242, %[ws], s76 offen sc1\n\t" \
  "v_mfma_f32_16x16x32_bf16 a[68:71], a[212:215], v[4:7], a[68:71]\n\t" \
  "ds_read_b64_tr_b16 a[204:205], v236 offset:8192\n\t" \
  "v_accvgpr_read_b32 v160, a4\n\t" \
  "v_accvgpr_read_b32 v161, a5\n\t" \
  "v_mfma_f32_16x16x32_bf16 a[68:71], a[208:211], v[0:3], a[68:71]\n\t" \
  "ds_read_b64_tr_b16 a[206:207], v236 offset:12288\n\t" \
  "v_accvgpr_read_b32 v162, a6\n\t" \
  "v_accvgpr_read_b32 v163, a7\n\t" \
  "v_mfma_f32_16x16x32_bf16 a[72:75], a[208:211], v[32:35], a[72:75]\n\t" \
  "v_accvgpr_read_b32 v164, a20\n\t" \
  "v_accvgpr_read_b32 v165, a21\n\t" \
  "v_accvgpr_read_b32 v166, a22\n\t" \
  "v_mfma_f32_16x16x32_bf16 a[72:75], a[212:215], v[36:39], a[72:75]\n\t" \
  "v_accvgpr_read_b32 v167, a23\n\t" \
  "v_cvt_pk_bf16_f32 v174, v160, v161\n\t" \
  "v_cvt_pk_bf16_f32 v175, v162, v163\n\t" \
  "v_mfma_f32_16x16x32_bf16 a[72:75], a[216:219], v[40:43], a[72:75]\n\t" \
  "v_cvt_pk_bf16_f32 v172, v164, v165\n\t" \
  "v_cvt_pk_bf16_f32 v173, v166, v167\n\t" \
  "s_xor_b32 s76, s61, 0\n\t" \
  "v_mfma_f32_16x16x32_bf16 a[72:75], a[220:223], v[44:47], a[72:75]\n\t" \
  "s_waitcnt lgkmcnt(4)\n\t" \
  "buffer_store_dwordx4 v[172:175], v242, %[ws], s76 offen sc1\n\t" \
  "v_mfma_f32_16x16x32_bf16 a[80:83], a[192:195], v[16:19], a[80:83]\n\t" \
  "v_xor_b32 v237, 0x200c0, v234\n\t" \
  "v_mfma_f32_16x16x32_bf16 a[80:83], a[196:199], v[20:23], a[80:83]\n\t" \
  "s_waitcnt lgkmcnt(0)\n\t" \
  "ds_read_b64_tr_b16 a[208:209], v237 offset:0\n\t" \
  "v_mfma_f32_16x16x32_bf16 a[80:83], a[200:203], v[24:27], a[80:83]\n\t" \
  "ds_read_b64_tr_b16 a[210:211], v237 offset:4096\n\t" \
  "v_mfma_f32_16x16x32_bf16 a[80:83], a[204:207], v[28:31], a[80:83]\n\t" \
  "ds_read_b64_tr_b16 a[212:213], v237 offset:8192\n\t" \
  "v_mfma_f32_16x16x32_bf16 a[84:87], a[204:207], v[12:15], a[84:87]\n\t" \
  "ds_read_b64_tr_b16 a[214:215], v237 offset:12288\n\t" \
  "v_xor_b32 v235, 0xc0, v234\n\t" \
  "v_mfma_f32_16x16x32_bf16 a[84:87], a[200:203], v[8:11], a[84:87]\n\t" \
  "ds_read_b64_tr_b16 a[216:217], v235 offset:0\n\t" \
  "v_accvgpr_read_b32 v160, a64\n\t" \
  "v_mfma_f32_16x16x32_bf16 a[84:87], a[196:199], v[4:7], a[84:87]\n\t" \
  "ds_read_b64_tr_b16 a[218:219], v235 offset:4096\n\t" \
  "v_accvgpr_read_b32 v161, a65\n\t" \
  "v_mfma_f32_16x16x32_bf16 a[84:87], a[192:195], v[0:3], a[84:87]\n\t" \
  "ds_read_b64_tr_b16 a[220:221], v235 offset:8192\n\t" \
  "v_accvgpr_read_b32 v162, a66\n\t" \
  "v_mfma_f32_16x16x32_bf16 a[88:91], a[192:195], v[32:35], a[88:91]\n\t" \
  "ds_read_b64_tr_b16 a[222:223], v235 offset:12288\n\t" \
  "v_accvgpr_read_b32 v163, a67\n\t" \
  "v_mfma_f32_16x16x32_bf16 a[88:91], a[196:199], v[36:39], a[88:91]\n\t" \
  "v_accvgpr_read_b32 v164, a80\n\t" \
  "v_accvgpr_read_b32 v165, a81\n\t" \
  "v_mfma_f32_16x16x32_bf16 a[88:91], a[200:203], v[40:43], a[88:91]\n\t" \
  "v_accvgpr_read_b32 v166, a82\n\t" \
  "v_accvgpr_read_b32 v167, a83\n\t" \
  "v_mfma_f32_16x16x32_bf16 a[88:91], a[204:207], v[44:47], a[88:91]\n\t" \
  "s_waitcnt lgkmcnt(4)\n\t" \
  "v_xor_b32 v236, 0x200e0, v234\n\t" \
  "v_cvt_pk_bf16_f32 v168, v160, v161\n\t" \
  "v_mfma_f32_16x16x32_bf16 a[96:99], a[208:211], v[16:19], a[96:99]\n\t" \
  ".Lhs14:\n\ts_cbranch_execz .Lhs14n\n\t" \
  "ds_read_b64_tr_b16 a[192:193], v236 offset:0\n\t" \
  "v_cvt_pk_bf16_f32 v169, v162, v163\n\t" \
  "v_mfma_f32_16x16x32_bf16 a[96:99], a[212:215], v[20:23], a[96:99]\n\t" \
  "s_waitcnt lgkmcnt(1)\n\t" \
  "ds_read_b64_tr_b16 a[194:195], v236 offset:4096\n\t" \
  "v_cvt_pk_bf16_f32 v170, v164, v165\n\t" \
  "v_mfma_f32_16x16x32_bf16 a[96:99], a[216:219], v[24:27], a[96:99]\n\t" \
  "ds_read_b64_tr_b16 a[196:197], v236 offset:8192\n\t" \
  "v_cvt_pk_bf16_f32 v171, v166, v167\n\t" \
  "s_xor_b32 s76, s60, 2048\n\t" \
  "v_mfma_f32_16x16x32_bf16 a[96:99], a[220:223], v[28:31], a[96:99]\n\t" \
  "buffer_store_dwordx4 v[168:171], v242, %[ws], s76 offen sc1\n\t" \
  "v_mfma_f32_16x16x32_bf16 a[100:103], a[220:223], v[12:15], a[100:103]\n\t" \
  "ds_read_b64_tr_b16 a[198:199], v236 offset:12288\n\t" \
  "v_accvgpr_read_b32 v160, a68\n\t" \
  "v_mfma_f32_16x16x32_bf16 a[100:103], a[216:219], v[8:11], a[100:103]\n\t" \
  "v_xor_b32 v237, 0xe0, v234\n\t" \
  "v_accvgpr_read_b32 v161, a69\n\t" \
  "v_mfma_f32_16x16x32_bf16 a[100:103], a[212:215], v[4:7], a[100:103]\n\t" \
  "ds_read_b64_tr_b16 a[200:201], v237 offset:0\n\t" \
  "v_accvgpr_read_b32 v162, a70\n\t" \
  "v_accvgpr_read_b32 v163, a71\n\t" \
  "v_mfma_f32_16x16x32_bf16 a[100:103], a[208:211], v[0:3], a[100:103]\n\t" \
  "ds_read_b64_tr_b16 a[202:203], v237 offset:4096\n\t" \
  "v_accvgpr_read_b32 v164, a84\n\t" \
  "v_accvgpr_read_b32 v165, a85\n\t" \
  "v_mfma_f32_16x16x32_bf16 a[104:107], a[208:211], v[32:35], a[104:107]\n\t" \
  "ds_read_b64_tr_b16 a[204:205], v237 offset:8192\n\t" \
  "v_accvgpr_read_b32 v166, a86\n\t" \
  "v_accvgpr_read_b32 v167, a87\n\t" \
  "v_mfma_f32_16x16x32_bf16 a[104:107], a[212:215], v[36:39], a[104:107]\n\t" \
  "ds_read_b64_tr_b16 a[206:207], v237 offset:12288\n\t" \
  "v_xor_b32 v235, 0xc000, v234\n\t" \
  "v_cvt_pk_bf16_f32 v174, v160, v161\n\t" \
  "v_mfma_f32_16x16x32_bf16 a[104:107], a[216:219], v[40:43], a[104:107]\n\t" \
  "ds_read_b64_tr_b16 a[32:33], v235 offset:0\n\t" \
  "ds_read_b64_tr_b16 a[34:35], v235 offset:4096\n\t" \
  "ds_read_b64_tr_b16 a[48:49], v235 offset:8192\n\t" \
  "v_mfma_f32_16x16x32_bf16 a[104:107], a[220:223], v[44:47], a[104:107]\n\t" \
  "s_waitcnt lgkmcnt(7)\n\t" \
  "ds_read_b64_tr_b16 a[50:51], v235 offset:12288\n\t" \
  "ds_read_b64_tr_b16 a[36:37], v235 offset:16384\n\t" \
  "ds_read_b64_tr_b16 a[38:39], v235 offset:20480\n\t" \
  "v_mfma_f32_16x16x32_bf16 a[112:115], a[192:195], v[16:19], a[112:115]\n\t" \
  "ds_read_b64_tr_b16 a[52:53], v235 offset:24576\n\t" \
  "ds_read_b64_tr_b16 a[54:55], v235 offset:28672\n\t" \
  "v_xor_b32 v236, 0xc020, v234\n\t" \
  "v_mfma_f32_16x16x32_bf16 a[112:115], a[196:199], v[20:23], a[112:115]\n\t" \
  "s_waitcnt lgkmcnt(8)\n\t" \
  "ds_read_b64_tr_b16 a[0:1], v236 offset:0\n\t" \
  "ds_read_b64_tr_b16 a[2:3], v236 offset:4096\n\t" \
  "ds_read_b64_tr_b16 a[16:17], v236 offset:8192\n\t" \
  "v_mfma_f32_16x16x32_bf16 a[112:115], a[200:203], v[24:27], a[112:115]\n\t" \
  "ds_read_b64_tr_b16 a[18:19], v236 offset:12288\n\t" \
  "ds_read_b64_tr_b16 a[4:5], v236 offset:16384\n\t" \
  "ds_read_b64_tr_b16 a[6:7], v236 offset:20480\n\t" \
  "v_mfma_f32_16x16x32_bf16 a[112:115], a[204:207], v[28:31], a[112:115]\n\t" \
  "ds_read_b64_tr_b16 a[20:21], v236 offset:24576\n\t" \
  "ds_read_b64_tr_b16 a[22:23], v236 offset:28672\n\t" \
  "v_xor_b32 v237, 0xc040, v234\n\t" \
  "v_mfma_f32_16x16x32_bf16 a[116:119], a[204:207], v[12:15], a[116:119]\n\t" \
  "ds_read_b64_tr_b16 a[64:65], v237 offset:0\n\t" \
  "ds_read_b64_tr_b16 a[66:67], v237 offset:4096\n\t" \
  "v_cvt_pk_bf16_f32 v175, v162, v163\n\t" \
  "v_mfma_f32_16x16x32_bf16 a[116:119], a[200:203], v[8:11], a[116:119]\n\t" \
  "ds_read_b64_tr_b16 a[80:81], v237 offset:8192\n\t" \
  "v_cvt_pk_bf16_f32 v172, v164, v165\n\t" \
  "v_cvt_pk_bf16_f32 v173, v166, v167\n\t" \
  "v_mfma_f32_16x16x32_bf16 a[116:119], a[196:199], v[4:7], a[116:119]\n\t" \
  "s_xor_b32 s76, s61, 2048\n\t" \
  "ds_read_b64_tr_b16 a[82:83], v237 offset:12288\n\t" \
  "ds_read_b64_tr_b16 a[68:69], v237 offset:16384\n\t" \
  "ds_read_b64_tr_b16 a[70:71], v237 offset:20480\n\t" \
  "v_mfma_f32_16x16x32_bf16 a[116:119], a[192:195], v[0:3], a[116:119]\n\t" \
  "buffer_store_dwordx4 v[172:175], v242, %[ws], s76 offen sc1\n\t" \
  "ds_read_b64_tr_b16 a[84:85], v237 offset:24576\n\t" \
  "v_mfma_f32_16x16x32_bf16 a[120:123], a[192:195], v[32:35], a[120:123]\n\t" \
  "ds_read_b64_tr_b16 a[86:87], v237 offset:28672\n\t" \
  "v_xor_b32 v235, 0xc060, v234\n\t" \
  "v_mfma_f32_16x16x32_bf16 a[120:123], a[196:199], v[36:39], a[120:123]\n\t" \
  "ds_read_b64_tr_b16 a[208:209], v235 offset:0\n\t" \
  "ds_read_b64_tr_b16 a[210:211], v235 offset:4096\n\t" \
  "ds_read_b64_tr_b16 a[212:213], v235 offset:8192\n\t" \
  "v_mfma_f32_16x16x32_bf16 a[120:123], a[200:203], v[40:43], a[120:123]\n\t" \
  "ds_read_b64_tr_b16 a[214:215], v235 offset:12288\n\t" \
  "ds_read_b64_tr_b16 a[216:217], v235 offset:16384\n\t" \
  "ds_read_b64_tr_b16 a[218:219], v235 offset:20480\n\t" \
  "v_mfma_f32_16x16x32_bf16 a[120:123], a[204:207], v[44:47], a[120:123]\n\t" \
  "s_waitcnt vmcnt(17) lgkmcnt(15)\n\t" \
  "ds_read_b64_tr_b16 a[220:221], v235 offset:24576\n\t" \
  "ds_read_b64_tr_b16 a[222:223], v235 offset:28672\n\t" \
  "v_accvgpr_read_b32 v160, a96\n\t" \
  "v_mfma_f32_16x16x32_bf16 a[8:11], a[32:35], v[208:211], a[8:11]\n\t" \
  "v_accvgpr_read_b32 v161, a97\n\t" \
  "v_accvgpr_read_b32 v162, a98\n\t" \
  "v_accvgpr_read_b32 v163, a99\n\t" \
  "v_mfma_f32_16x16x32_bf16 a[8:11], a[48:51], v[212:215], a[8:11]\n\t" \
  "v_accvgpr_read_b32 v164, a112\n\t" \
  "v_accvgpr_read_b32 v165, a113\n\t" \
  "v_accvgpr_read_b32 v166, a114\n\t" \
  "v_mfma_f32_16x16x32_bf16 a[8:11], a[36:39], v[216:219], a[8:11]\n\t" \
  "v_xor_b32 v236, 0xc080, v234\n\t" \
  "v_accvgpr_read_b32 v167, a115\n\t" \
  "v_cvt_pk_bf16_f32 v168, v160, v161\n\t" \
  "v_mfma_f32_16x16x32_bf16 a[8:11], a[52:55], v[220:223], a[8:11]\n\t" \
  "ds_read_b64_tr_b16 v[32:33], v236 offset:0\n\t" \
  "v_cvt_pk_bf16_f32 v169, v162, v163\n\t" \
  "v_cvt_pk_bf16_f32 v170, v164, v165\n\t" \
  "v_mfma_f32_16x16x32_bf16 a[24:27], a[0:3], v[208:211], a[24:27]\n\t" \
  "ds_read_b64_tr_b16 v[34:35], v236 offset:4096\n\t" \
  "v_cvt_pk_bf16_f32 v171, v166, v167\n\t" \
  "s_xor_b32 s76, s60, 3072\n\t" \
  "v_mfma_f32_16x16x32_bf16 a[24:27], a[16:19], v[212:215], a[24:27]\n\t" \
  "s_waitcnt lgkmcnt(15)\n\t" \
  "buffer_store_dwordx4 v[168:171], v242, %[ws], s76 offen sc1\n\t" \
  "v_accvgpr_read_b32 v160, a100\n\t" \
  "v_mfma_f32_16x16x32_bf16 a[24:27], a[4:7], v[216:219], a[24:27]\n\t" \
  "ds_read_b64_tr_b16 v[36:37], v236 offset:8192\n\t" \
  "ds_read_b64_tr_b16 v[38:39], v236 offset:12288\n\t" \
  "v_accvgpr_read_b32 v161, a101\n\t" \
  "v_mfma_f32_16x16x32_bf16 a[24:27], a[20:23], v[220:223], a[24:27]\n\t" \
  "s_waitcnt lgkmcnt(15)\n\t" \
  "ds_read_b64_tr_b16 v[40:41], v236 offset:16384\n\t" \
  "v_accvgpr_read_b32 v162, a102\n\t" \
  "v_accvgpr_read_b32 v163, a103\n\t" \
  "v_mfma_f32_16x16x32_bf16 a[40:43], a[64:67], v[208:211], a[40:43]\n\t" \
  "ds_read_b64_tr_b16 v[42:43], v236 offset:20480\n\t" \
  "v_accvgpr_read_b32 v164, a116\n\t" \
  "v_accvgpr_read_b32 v165, a117\n\t" \
  "v_mfma_f32_16x16x32_bf16 a[40:43], a[80:83], v[212:215], a[40:43]\n\t" \
  "s_waitcnt lgkmcnt(14)\n\t" \
  "ds_read_b64_tr_b16 v[44:45], v236 offset:24576\n\t" \
  "v_accvgpr_read_b32 v166, a118\n\t" \
  "v_accvgpr_read_b32 v167, a119\n\t" \
  "v_mfma_f32_16x16x32_bf16 a[40:43], a[68:71], v[216:219], a[40:43]\n\t" \
  "ds_read_b64_tr_b16 v[46:47], v236 offset:28672\n\t" \
  "v_xor_b32 v237, 0xc0a0, v234\n\t" \
  "v_cvt_pk_bf16_f32 v174, v160, v161\n\t" \
  "v_mfma_f32_16x16x32_bf16 a[40:43], a[84:87], v[220:223], a[40:43]\n\t" \
  "s_waitcnt lgkmcnt(12)\n\t" \
  "ds_read_b64_tr_b16 a[192:193], v237 offset:0\n\t" \
  "ds_read_b64_tr_b16 a[194:195], v237 offset:4096\n\t" \
  "ds_read_b64_tr_b16 a[196:197], v237 offset:8192\n\t" \
  "v_mfma_f32_16x16x32_bf16 a[56:59], a[208:211], v[208:211], a[56:59]\n\t" \
  "s_add_u32 m0, %[m], 0\n\t" \
  "ds_read_b64_tr_b16 a[198:199], v237 offset:12288\n\t" \
  "ds_read_b64_tr_b16 a[200:201], v237 offset:16384\n\t" \
  "ds_read_b64_tr_b16 a[202:203], v237 offset:20480\n\t" \
  "v_mfma_f32_16x16x32_bf16 a[56:59], a[212:215], v[212:215], a[56:59]\n\t" \
  "s_waitcnt lgkmcnt(14)\n\t" \
  "ds_read_b64_tr_b16 a[204:205], v237 offset:24576\n\t" \
  "ds_read_b64_tr_b16 a[206:207], v237 offset:28672\n\t" \
  "v_xor_b32 v235, 0xc0c0, v234\n\t" \
  "v_mfma_f32_16x16x32_bf16 a[56:59], a[216:219], v[216:219], a[56:59]\n\t" \
  "ds_read_b64_tr_b16 v[0:1], v235 offset:0\n\t" \
  "ds_read_b64_tr_b16 v[2:3], v235 offset:4096\n\t" \
  "ds_read_b64_tr_b16 v[4:5], v235 offset:8192\n\t" \
  "v_mfma_f32_16x16x32_bf16 a[56:59], a[220:223], v[220:223], a[56:59]\n\t" \
  "s_waitcnt lgkmcnt(15)\n\t" \
  "s_barrier\n\t" \
  "v_mfma_f32_16x16x32_bf16 a[72:75], v[32:35], v[208:211], a[72:75]\n\t" \
  "s_xor_b32 s71, %[eso], 753664\n\t" \
  "ds_read_b64_tr_b16 v[6:7], v235 offset:12288\n\t" \
  "v_cvt_pk_bf16_f32 v175, v162, v163\n\t" \
  "v_cvt_pk_bf16_f32 v172, v164, v165\n\t" \
  "v_mfma_f32_16x16x32_bf16 a[72:75], v[36:39], v[212:215], a[72:75]\n\t" \
  "s_waitcnt lgkmcnt(12)\n\t" \
  "ds_read_b64_tr_b16 v[8:9], v235 offset:16384\n\t" \
  "v_cvt_pk_bf16_f32 v173, v166, v167\n\t" \
  "s_xor_b32 s76, s61, 3072\n\t" \
  "v_mfma_f32_16x16x32_bf16 a[72:75], v[40:43], v[216:219], a[72:75]\n\t" \
  "ds_read_b64_tr_b16 v[10:11], v235 offset:20480\n\t" \
  "ds_read_b64_tr_b16 v[12:13], v235 offset:24576\n\t" \
  "ds_read_b64_tr_b16 v[14:15], v235 offset:28672\n\t" \
  "v_mfma_f32_16x16x32_bf16 a[72:75], v[44:47], v[220:223], a[72:75]\n\t" \
  "s_waitcnt lgkmcnt(12)\n\t" \
  "v_xor_b32 v236, 0xc0e0, v234\n\t" \
  "v_mfma_f32_16x16x32_bf16 a[88:91], a[192:195], v[208:211], a[88:91]\n\t" \
  "ds_read_b64_tr_b16 v[16:17], v236 offset:0\n\t" \
  "ds_read_b64_tr_b16 v[18:19], v236 offset:4096\n\t" \
  "ds_read_b64_tr_b16 v[20:21], v236 offset:8192\n\t" \
  "v_mfma_f32_16x16x32_bf16 a[88:91], a[196:199], v[212:215], a[88:91]\n\t" \
  "s_waitcnt lgkmcnt(11)\n\t" \
  "buffer_store_dwordx4 v[172:175], v242, %[ws], s76 offen sc1\n\t" \
  "v_accvgpr_read_b32 v160, a8\n\t" \
  "v_mfma_f32_16x16x32_bf16 a[88:91], a[200:203], v[216:219], a[88:91]\n\t" \
  "ds_read_b64_tr_b16 v[22:23], v236 offset:12288\n\t" \
  "v_accvgpr_read_b32 v161, a9\n\t" \
  "v_accvgpr_read_b32 v162, a10\n\t" \
  "v_mfma_f32_16x16x32_bf16 a[88:91], a[204:207], v[220:223], a[88:91]\n\t" \
  "s_waitcnt lgkmcnt(8)\n\t" \
  "ds_read_b64_tr_b16 v[24:25], v236 offset:16384\n\t" \
  "v_accvgpr_read_b32 v163, a11\n\t" \
  "v_accvgpr_read_b32 v164, a24\n\t" \
  "v_mfma_f32_16x16x32_bf16 a[104:107], v[0:3], v[208:211], a[104:107]\n\t" \
  "ds_read_b64_tr_b16 v[26:27], v236 offset:20480\n\t" \
  "v_accvgpr_read_b32 v165, a25\n\t" \
  "v_accvgpr_read_b32 v166, a26\n\t" \
  "v_mfma_f32_16x16x32_bf16 a[104:107], v[4:7], v[212:215], a[104:107]\n\t" \
  "s_waitcnt lgkmcnt(6)\n\t" \
  "ds_read_b64_tr_b16 v[28:29], v236 offset:24576\n\t" \
  "v_accvgpr_read_b32 v167, a27\n\t" \
  "v_cvt_pk_bf16_f32 v168, v160, v161\n\t" \
  "v_mfma_f32_16x16x32_bf16 a[104:107], v[8:11], v[216:219], a[104:107]\n\t" \
  "ds_read_b64_tr_b16 v[30:31], v236 offset:28672\n\t" \
  "ds_read_b128 a[208:211], v242 offset:16384\n\t" \
  "v_cvt_pk_bf16_f32 v169, v162, v163\n\t" \
  "v_mfma_f32_16x16x32_bf16 a[104:107], v[12:15], v[220:223], a[104:107]\n\t" \
  "s_waitcnt lgkmcnt(5)\n\t" \
  "ds_read_b128 a[212:215], v242 offset:17408\n\t" \
  "v_cvt_pk_bf16_f32 v170, v164, v165\n\t" \
  "v_cvt_pk_bf16_f32 v171, v166, v167\n\t" \
  "v_mfma_f32_16x16x32_bf16 a[120:123], v[16:19], v[208:211], a[120:123]\n\t" \
  "s_xor_b32 s76, s62, 0\n\t" \
  "ds_read_b128 a[216:219], v242 offset:18432\n\t" \
  "ds_read_b128 a[220:223], v242 offset:19456\n\t" \
  "v_mfma_f32_16x16x32_bf16 a[120:123], v[20:23], v[212:215], a[120:123]\n\t" \
  "s_waitcnt lgkmcnt(4)\n\t" \
  "buffer_store_dwordx4 v[168:171], v242, %[ws], s76 offen sc1\n\t" \
  "v_accvgpr_read_b32 v160, a40\n\t" \
  "v_mfma_f32_16x16x32_bf16 a[120:123], v[24:27], v[216:219], a[120:123]\n\t" \
  "ds_read_b128 v[32:35], v242 offset:20480\n\t" \
  "v_accvgpr_read_b32 v161, a41\n\t" \
  "v_accvgpr_read_b32 v162, a42\n\t" \
  "v_mfma_f32_16x16x32_bf16 a[120:123], v[28:31], v[220:223], a[120:123]\n\t" \
  "s_waitcnt lgkmcnt(1)\n\t" \
  "ds_read_b128 v[36:39], v242 offset:21504\n\t" \
  "v_accvgpr_read_b32 v163, a43\n\t" \
  "v_accvgpr_read_b32 v164, a56\n\t" \
  STP("v_writelane_b32 %[stv], s94, 34\n\t") \
  STP("v_writelane_b32 %[stv], s92, 35\n\t") \
  STP("s_memrealtime s[94:95]\n\t") \
  STP("s_memtime s[92:93]\n\t") \
  "v_mfma_f32_16x16x32_bf16 v[96:99], v[64:67], a[208:211], v[96:99]\n\t" \
  "ds_read_b128 v[40:43], v242 offset:22528\n\t" \
  "v_accvgpr_read_b32 v165, a57\n\t" \
  "v_accvgpr_read_b32 v166, a58\n\t" \
  "v_mfma_f32_16x16x32_bf16 v[96:99], v[68:71], a[212:215], v[96:99]\n\t" \
  "ds_read_b128 v[44:47], v242 offset:23552\n\t" \
  "v_accvgpr_read_b32 v167, a59\n\t" \
  "v_cvt_pk_bf16_f32 v172, v160, v161\n\t" \
  "v_mfma_f32_16x16x32_bf16 v[96:99], v[72:75], a[216:219], v[96:99]\n\t" \
  "ds_read_b128 a[192:195], v242 offset:24576\n\t" \
  "v_cvt_pk_bf16_f32 v173, v162, v163\n\t" \
  "v_cvt_pk_bf16_f32 v174, v164, v165\n\t" \
  "v_mfma_f32_16x16x32_bf16 v[96:99], v[76:79], a[220:223], v[96:99]\n\t" \
  "ds_read_b128 a[196:199], v242 offset:25600\n\t" \
  "v_cvt_pk_bf16_f32 v175, v166, v167\n\t" \
  "s_xor_b32 s76, s62, 1024\n\t" \
  "v_mfma_f32_16x16x32_bf16 v[112:115], v[92:95], a[220:223], v[112:115]\n\t" \
  "buffer_store_dwordx4 v[172:175], v242, %[ws], s76 offen sc1\n\t" \
  "v_accvgpr_read_b32 v160, a72\n\t" \
  "v_mfma_f32_16x16x32_bf16 v[112:115], v[88:91], a[216:219], v[112:115]\n\t" \
  "ds_read_b128 a[200:203], v242 offset:26624\n\t" \
  "v_accvgpr_read_b32 v161, a73\n\t" \
  "v_accvgpr_read_b32 v162, a74\n\t" \
  "v_mfma_f32_16x16x32_bf16 v[112:115], v[84:87], a[212:215], v[112:115]\n\t" \
  "ds_read_b128 a[204:207], v242 offset:27648\n\t" \
  "v_accvgpr_read_b32 v163, a75\n\t" \
  "v_accvgpr_read_b32 v164, a88\n\t" \
  "v_mfma_f32_16x16x32_bf16 v[112:115], v[80:83], a[208:211], v[112:115]\n\t" \
  "s_waitcnt lgkmcnt(4)\n\t" \
  "v_accvgpr_read_b32 v165, a89\n\t" \
  "v_accvgpr_read_b32 v166, a90\n\t" \
  "v_accvgpr_read_b32 v167, a91\n\t" \
  "v_mfma_f32_16x16x32_bf16 v[100:103], v[64:67], v[32:35], v[100:103]\n\t" \
  "buffer_load_dwordx4 v242, %[ws], s71 offen offset:0 sc1 lds\n\t" \
  "v_cvt_pk_bf16_f32 v168, v160, v161\n\t" \
  "v_mfma_f32_16x16x32_bf16 v[100:103], v[68:71], v[36:39], v[100:103]\n\t" \
  "ds_read_b128 a[208:211], v242 offset:28672\n\t" \
  "v_cvt_pk_bf16_f32 v169, v162, v163\n\t" \
  "v_cvt_pk_bf16_f32 v170, v164, v165\n\t" \
  "v_mfma_f32_16x16x32_bf16 v[100:103], v[72:75], v[40:43], v[100:103]\n\t" \
  "ds_read_b128 a[212:215], v242 offset:29696\n\t" \
  "v_cvt_pk_bf16_f32 v171, v166, v167\n\t" \
  "s_xor_b32 s76, s62, 2048\n\t" \
  "v_mfma_f32_16x16x32_bf16 v[100:103], v[76:79], v[44:47], v[100:103]\n\t" \
  "buffer_store_dwordx4 v[168:171], v242, %[ws], s76 offen sc1\n\t" \
  "v_accvgpr_read_b32 v160, a104\n\t" \
  "v_mfma_f32_16x16x32_bf16 v[116:119], v[92:95], v[44:47], v[116:119]\n\t" \
  "s_mov_b32 s74, 0\n\t" \
  "ds_read_b128 a[216:219], v242 offset:30720\n\t" \
  "v_accvgpr_read_b32 v161, a105\n\t" \
  "v_accvgpr_read_b32 v162, a106\n\t" \
  "v_mfma_f32_16x16x32_bf16 v[116:119], v[88:91], v[40:43], v[116:119]\n\t" \
  "buffer_load_dwordx4 v242, %[ws], s71 offen offset:1024 sc1 lds\n\t" \
  "v_accvgpr_read_b32 v163, a107\n\t" \
  "v_mfma_f32_16x16x32_bf16 v[116:119], v[84:87], v[36:39], v[116:119]\n\t" \
  "ds_read_b128 a[220:223], v242 offset:31744\n\t" \
  "v_accvgpr_read_b32 v164, a120\n\t" \
  "v_accvgpr_read_b32 v165, a121\n\t" \
  "v_mfma_f32_16x16x32_bf16 v[116:119], v[80:83], v[32:35], v[116:119]\n\t" \
  "s_waitcnt lgkmcnt(4)\n\t" \
  "s_barrier\n\t" \
  "v_mfma_f32_16x16x32_bf16 v[104:107], v[64:67], a[192:195], v[104:107]\n\t" \
  "ds_read_b128 v[208:211], v242 offset:32768\n\t" \
  "v_accvgpr_read_b32 v166, a122\n\t" \
  "v_accvgpr_read_b32 v167, a123\n\t" \
  "v_mfma_f32_16x16x32_bf16 v[104:107], v[68:71], a[196:199], v[104:107]\n\t" \
  "ds_read_b128 v[212:215], v242 offset:33792\n\t" \
  "v_cvt_pk_bf16_f32 v172, v160, v161\n\t" \
  "v_cvt_pk_bf16_f32 v173, v162, v163\n\t" \
  "v_mfma_f32_16x16x32_bf16 v[104:107], v[72:75], a[200:203], v[104:107]\n\t" \
  "ds_read_b128 v[216:219], v242 offset:34816\n\t" \
  "v_cvt_pk_bf16_f32 v174, v164, v165\n\t" \
  "v_cvt_pk_bf16_f32 v175, v166, v167\n\t" \
  "v_mfma_f32_16x16x32_bf16 v[104:107], v[76:79], a[204:207], v[104:107]\n\t" \
  "s_xor_b32 s76, s62, 3072\n\t" \
  "ds_read_b128 v[220:223], v242 offset:35840\n\t" \
  "buffer_store_dwordx4 v[172:175], v242, %[ws], s76 offen sc1\n\t" \
  "v_mfma_f32_16x16x32_bf16 v[120:123], v[92:95], a[204:207], v[120:123]\n\t" \
  "v_mfma_f32_16x16x32_bf16 v[120:123], v[88:91], a[200:203], v[120:123]\n\t" \
  "buffer_load_dwordx4 v242, %[ws], s71 offen offset:2048 sc1 lds\n\t" \
  "v_mfma_f32_16x16x32_bf16 v[120:123], v[84:87], a[196:199], v[120:123]\n\t" \
  "v_mfma_f32_16x16x32_bf16 v[120:123], v[80:83], a[192:195], v[120:123]\n\t" \
  "s_waitcnt lgkmcnt(4)\n\t" \
  "buffer_load_dwordx4 v242, %[ws], s71 offen offset:3072 sc1 lds\n\t" \
  "s_add_u32 m0, %[m], 131072\n\t" \
  "v_mfma_f32_16x16x32_bf16 v[108:111], v[64:67], a[208:211], v[108:111]\n\t" \
  "s_add_u32 s71, %[xso], 0\n\t" \
  "ds_read_b128 a[192:195], v242 offset:36864\n\t" \
  "v_mfma_f32_16x16x32_bf16 v[108:111], v[68:71], a[212:215], v[108:111]\n\t" \
  "ds_read_b128 a[196:199], v242 offset:37888\n\t" \
  "v_mfma_f32_16x16x32_bf16 v[108:111], v[72:75], a[216:219], v[108:111]\n\t" \
  "ds_read_b128 a[200:203], v242 offset:38912\n\t" \
  "v_mfma_f32_16x16x32_bf16 v[108:111], v[76:79], a[220:223], v[108:111]\n\t" \
  "buffer_load_dwordx4 v248, %[sx], s71 offen offset:0 lds\n\t" \
  "s_add_u32 s71, %[xso], 7168\n\t" \
  "v_mfma_f32_16x16x32_bf16 v[124:127], v[92:95], a[220:223], v[124:127]\n\t" \
  "ds_read_b128 a[204:207], v242 offset:39936\n\t" \
  "v_mfma_f32_16x16x32_bf16 v[124:127], v[88:91], a[216:219], v[124:127]\n\t" \
  "v_mfma_f32_16x16x32_bf16 v[124:127], v[84:87], a[212:215], v[124:127]\n\t" \
  "v_xor_b32 v237, 0x14000, v234\n\t" \
  "v_mfma_f32_16x16x32_bf16 v[124:127], v[80:83], a[208:211], v[124:127]\n\t" \
  "s_waitcnt lgkmcnt(4)\n\t" \
  "buffer_load_dwordx4 v249, %[sx], s71 offen offset:1024 lds\n\t" \
  "s_add_u32 s71, %[xso], 14336\n\t" \
  "v_mfma_f32_16x16x32_bf16 v[96:99], a[160:163], v[208:211], v[96:99]\n\t" \
  "ds_read_b64_tr_b16 a[96:97], v237 offset:0\n\t" \
  "v_mfma_f32_16x16x32_bf16 v[96:99], a[164:167], v[212:215], v[96:99]\n\t" \
  "ds_read_b64_tr_b16 a[98:99], v237 offset:4096\n\t" \
  "v_mfma_f32_16x16x32_bf16 v[96:99], a[168:171], v[216:219], v[96:99]\n\t" \
  "s_waitcnt vmcnt(10)\n\t" \
  "buffer_store_dword v243, v225, %[ws], 0 offen sc1\n\t" \
  "v_mfma_f32_16x16x32_bf16 v[96:99], a[172:175], v[220:223], v[96:99]\n\t" \
  "ds_read_b64_tr_b16 a[112:113], v237 offset:8192\n\t" \
  "v_mfma_f32_16x16x32_bf16 v[112:115], a[188:191], v[220:223], v[112:115]\n\t" \
  "buffer_load_dwordx4 v250, %[sx], s71 offen offset:2048 lds\n\t" \
  "s_add_u32 s71, %[xso], 21504\n\t" \
  "v_mfma_f32_16x16x32_bf16 v[112:115], a[184:187], v[216:219], v[112:115]\n\t" \
  "ds_read_b64_tr_b16 a[114:115], v237 offset:12288\n\t" \
  "v_mfma_f32_16x16x32_bf16 v[112:115], a[180:183], v[212:215], v[112:115]\n\t" \
  "ds_read_b64_tr_b16 a[100:101], v237 offset:32768\n\t" \
  "v_mfma_f32_16x16x32_bf16 v[112:115], a[176:179], v[208:211], v[112:115]\n\t" \
  "s_waitcnt lgkmcnt(5)\n\t" \
  "ds_read_b64_tr_b16 a[102:103], v237 offset:36864\n\t" \
  "v_mfma_f32_16x16x32_bf16 v[100:103], a[160:163], a[192:195], v[100:103]\n\t" \
  "ds_read_b64_tr_b16 a[116:117], v237 offset:40960\n\t" \
  "v_mfma_f32_16x16x32_bf16 v[100:103], a[164:167], a[196:199], v[100:103]\n\t" \
  "buffer_load_dwordx4 v251, %[sx], s71 offen offset:3072 lds\n\t" \
  "s_add_u32 m0, %[m], 49152\n\t" \
  "v_mfma_f32_16x16x32_bf16 v[100:103], a[168:171], a[200:203], v[100:103]\n\t" \
  "s_xor_b32 s71, %[eso], 622592\n\t" \
  "ds_read_b64_tr_b16 a[118:119], v237 offset:45056\n\t" \
  "ds_read_b128 a[208:211], v242 offset:40960\n\t" \
  "ds_read_b128 a[212:215], v242 offset:41984\n\t" \
  "v_mfma_f32_16x16x32_bf16 v[100:103], a[172:175], a[204:207], v[100:103]\n\t" \
  "ds_read_b128 a[216:219], v242 offset:43008\n\t" \
  "ds_read_b128 a[220:223], v242 offset:44032\n\t" \
  "v_mfma_f32_16x16x32_bf16 v[116:119], a[188:191], a[204:207], v[116:119]\n\t" \
  "ds_read_b128 v[208:211], v242 offset:45056\n\t" \
  "v_mfma_f32_16x16x32_bf16 v[116:119], a[184:187], a[200:203], v[116:119]\n\t" \
  "buffer_load_dwordx4 v242, %[ws], s71 offen offset:0 sc1 lds\n\t" \
  "v_mfma_f32_16x16x32_bf16 v[116:119], a[180:183], a[196:199], v[116:119]\n\t" \
  "ds_read_b128 v[212:215], v242 offset:46080\n\t" \
  "ds_read_b128 v[216:219], v242 offset:47104\n\t" \
  "v_mfma_f32_16x16x32_bf16 v[116:119], a[176:179], a[192:195], v[116:119]\n\t" \
  "s_waitcnt lgkmcnt(3)\n\t" \
  "s_barrier\n\t" \
  "v_mfma_f32_16x16x32_bf16 v[104:107], a[160:163], a[208:211], v[104:107]\n\t" \
  "ds_read_b128 v[220:223], v242 offset:48128\n\t" \
  "v_xor_b32 v235, 0x14020, v234\n\t" \
  "v_mfma_f32_16x16x32_bf16 v[104:107], a[164:167], a[212:215], v[104:107]\n\t" \
  "buffer_load_dwordx4 v242, %[ws], s71 offen offset:1024 sc1 lds\n\t" \
  "ds_read_b64_tr_b16 a[64:65], v235 offset:0\n\t" \
  "v_mfma_f32_16x16x32_bf16 v[104:107], a[168:171], a[216:219], v[104:107]\n\t" \
  "ds_read_b64_tr_b16 a[66:67], v235 offset:4096\n\t" \
  "ds_read_b64_tr_b16 a[68:69], v235 offset:8192\n\t" \
  "ds_read_b64_tr_b16 a[70:71], v235 offset:12288\n\t" \
  "v_mfma_f32_16x16x32_bf16 v[104:107], a[172:175], a[220:223], v[104:107]\n\t" \
  "ds_read_b64_tr_b16 a[80:81], v235 offset:32768\n\t" \
  "ds_read_b64_tr_b16 a[82:83], v235 offset:36864\n\t" \
  "v_mfma_f32_16x16x32_bf16 v[120:123], a[188:191], a[220:223], v[120:123]\n\t" \
  "ds_read_b64_tr_b16 a[84:85], v235 offset:40960\n\t" \
  "ds_read_b64_tr_b16 a[86:87], v235 offset:45056\n\t" \
  "v_mfma_f32_16x16x32_bf16 v[120:123], a[184:187], a[216:219], v[120:123]\n\t" \
  "ds_read_b128 a[192:195], v230 offset:32768\n\t" \
  "ds_read_b128 a[196:199], v230 offset:33792\n\t" \
  "v_and_b32 v235, 16, v242\n\t" \
  "v_mfma_f32_16x16x32_bf16 v[120:123], a[180:183], a[212:215], v[120:123]\n\t" \
  "buffer_load_dwordx4 v242, %[ws], s71 offen offset:2048 sc1 lds\n\t" \
  "v_accvgpr_read_b32 v224, a224\n\t" \
  "v_mfma_f32_16x16x32_bf16 v[120:123], a[176:179], a[208:211], v[120:123]\n\t" \
  "s_waitcnt lgkmcnt(10)\n\t" \
  "ds_read_b128 a[200:203], v230 offset:34816\n\t" \
  "ds_read_b128 a[204:207], v230 offset:35840\n\t" \
  "v_mov_b32 v229, 0x3f80\n\t" \
  "v_mfma_f32_16x16x32_bf16 v[108:111], a[160:163], v[208:211], v[108:111]\n\t" \
  "ds_read_b128 v[0:3], v230 offset:36864\n\t" \
  "v_accvgpr_read_b32 v227, a228\n\t" \
  "v_lshlrev_b32 v229, v235, v229\n\t" \
  "v_mfma_f32_16x16x32_bf16 v[108:111], a[164:167], v[212:215], v[108:111]\n\t" \
  "s_mov_b32 s82, 0x300003\n\t" \
  "ds_read_b128 v[4:7], v230 offset:37888\n\t" \
  "v_perm_b32 v64, v227, v224, s79\n\t" \
  "v_mfma_f32_16x16x32_bf16 v[108:111], a[168:171], v[216:219], v[108:111]\n\t" \
  "buffer_load_dwordx4 v242, %[ws], s71 offen offset:3072 sc1 lds\n\t" \
  "s_add_u32 m0, %[m], 65536\n\t" \
  "v_mfma_f32_16x16x32_bf16 v[108:111], a[172:175], v[220:223], v[108:111]\n\t" \
  "s_xor_b32 s71, %[eso], 884736\n\t" \
  "s_mov_b32 s83, 0x30000300\n\t" \
  "ds_read_b128 v[8:11], v230 offset:38912\n\t" \
  "v_mfma_f32_16x16x32_bf16 v[124:127], a[188:191], v[220:223], v[124:127]\n\t" \
  "s_mov_b32 s84, 0xc0000c\n\t" \
  "ds_read_b128 v[12:15], v230 offset:39936\n\t" \
  "v_perm_b32 v68, v227, v224, s80\n\t" \
  "v_mfma_f32_16x16x32_bf16 v[124:127], a[184:187], v[216:219], v[124:127]\n\t" \
  "s_mov_b32 s85, 0xc0000c00\n\t" \
  "v_accvgpr_read_b32 v224, a225\n\t" \
  "v_cndmask_b32 v238, 0, v229, s[82:83]\n\t" \
  "v_mfma_f32_16x16x32_bf16 v[124:127], a[180:183], v[212:215], v[124:127]\n\t" \
  "v_cndmask_b32 v239, 0, v229, s[84:85]\n\t" \
  "v_accvgpr_read_b32 v227, a229\n\t" \
  "v_mfma_f32_16x16x32_bf16 v[124:127], a[176:179], v[208:211], v[124:127]\n\t" \
  "s_waitcnt lgkmcnt(4)\n\t" \
  "buffer_load_dwordx4 v242, %[ws], s71 offen offset:0 sc1 lds\n\t" \
  "v_mfma_f32_16x16x32_bf16 v[96:99], a[128:131], a[192:195], v[96:99]\n\t" \
  "ds_read_b128 v[208:211], v230 offset:40960\n\t" \
  "v_and_b32 v236, 15, v244\n\t" \
  "v_mfma_f32_16x16x32_bf16 v[96:99], a[132:135], a[196:199], v[96:99]\n\t" \
  "ds_read_b128 v[212:215], v230 offset:41984\n\t" \
  "v_lshrrev_b32 v229, 4, v244\n\t" \
  "v_mfma_f32_16x16x32_bf16 v[96:99], a[136:139], a[200:203], v[96:99]\n\t" \
  "ds_read_b128 v[216:219], v230 offset:43008\n\t" \
  "v_perm_b32 v65, v227, v224, s79\n\t" \
  "v_mfma_f32_16x16x32_bf16 v[96:99], a[140:143], a[204:207], v[96:99]\n\t" \
  "s_waitcnt vmcnt(12)\n\t" \
  "buffer_store_dword v243, v226, %[ws], 0 offen sc1\n\t" \
  "v_mfma_f32_16x16x32_bf16 v[112:115], a[156:159], a[204:207], v[112:115]\n\t" \
  "ds_read_b128 v[220:223], v230 offset:44032\n\t" \
  "v_lshlrev_b32 v235, 2, v236\n\t" \
  "v_mfma_f32_16x16x32_bf16 v[112:115], a[152:155], a[200:203], v[112:115]\n\t" \
  "buffer_load_dwordx4 v242, %[ws], s71 offen offset:1024 sc1 lds\n\t" \
  "v_mfma_f32_16x16x32_bf16 v[112:115], a[148:151], a[196:199], v[112:115]\n\t" \
  "v_perm_b32 v69, v227, v224, s80\n\t" \
  "v_mul_u32_u24 v236, 80, v236\n\t" \
  "v_mfma_f32_16x16x32_bf16 v[112:115], a[144:147], a[192:195], v[112:115]\n\t" \
  "s_waitcnt lgkmcnt(4)\n\t" \
  "v_lshlrev_b32 v237, 4, v229\n\t" \
  "v_accvgpr_read_b32 v224, a226\n\t" \
  "v_mfma_f32_16x16x32_bf16 v[100:103], a[128:131], v[0:3], v[100:103]\n\t" \
  "buffer_load_dwordx4 v242, %[ws], s71 offen offset:2048 sc1 lds\n\t" \
  "v_mfma_f32_16x16x32_bf16 v[100:103], a[132:135], v[4:7], v[100:103]\n\t" \
  "ds_read_b128 a[192:195], v230 offset:45056\n\t" \
  "v_add_u32 v237, s70, v237\n\t" \
  "v_mfma_f32_16x16x32_bf16 v[100:103], a[136:139], v[8:11], v[100:103]\n\t" \
  "ds_read_b128 a[196:199], v230 offset:46080\n\t" \
  "v_add_u32 v236, v237, v236\n\t" \
  "v_mfma_f32_16x16x32_bf16 v[100:103], a[140:143], v[12:15], v[100:103]\n\t" \
  "ds_read_b128 a[200:203], v230 offset:47104\n\t" \
  "v_accvgpr_read_b32 v227, a230\n\t" \
  "v_mfma_f32_16x16x32_bf16 v[116:119], a[156:159], v[12:15], v[116:119]\n\t" \
  "buffer_load_dwordx4 v242, %[ws], s71 offen offset:3072 sc1 lds\n\t" \
  "s_add_u32 m0, %[m], 16384\n\t" \
  "v_mfma_f32_16x16x32_bf16 v[116:119], a[152:155], v[8:11], v[116:119]\n\t" \
  "s_xor_b32 s71, %[eso], 229376\n\t" \
  "ds_read_b128 a[204:207], v230 offset:48128\n\t" \
  "ds_read_b128 a[208:211], v236 offset:960\n\t" \
  "v_mfma_f32_16x16x32_bf16 v[116:119], a[148:151], v[4:7], v[116:119]\n\t" \
  "ds_read_b128 a[212:215], v236 offset:640\n\t" \
  "v_perm_b32 v66, v227, v224, s79\n\t" \
  "v_mfma_f32_16x16x32_bf16 v[116:119], a[144:147], v[0:3], v[116:119]\n\t" \
  "s_waitcnt vmcnt(14) lgkmcnt(6)\n\t" \
  "s_barrier\n\t" \
  "v_mfma_f32_16x16x32_bf16 v[104:107], a[128:131], v[208:211], v[104:107]\n\t" \
  "buffer_load_dword v246, v235, %[ws], s68 offen sc1\n\t" \
  "v_mfma_f32_16x16x32_bf16 v[104:107], a[132:135], v[212:215], v[104:107]\n\t" \
  "ds_read_b128 a[160:163], v242 offset:0\n\t" \
  "v_perm_b32 v70, v227, v224, s80\n\t" \
  "v_mfma_f32_16x16x32_bf16 v[104:107], a[136:139], v[216:219], v[104:107]\n\t" \
  "buffer_load_dwordx2 v[32:33], v252, %[sd], s74 offen offset:0\n\t" \
  "v_mfma_f32_16x16x32_bf16 v[104:107], a[140:143], v[220:223], v[104:107]\n\t" \
  "ds_read_b128 a[164:167], v242 offset:1024\n\t" \
  "v_accvgpr_read_b32 v224, a227\n\t" \
  "v_mfma_f32_16x16x32_bf16 v[120:123], a[156:159], v[220:223], v[120:123]\n\t" \
  "ds_read_b128 a[168:171], v242 offset:2048\n\t" \
  "v_accvgpr_read_b32 v227, a231\n\t" \
  "v_mfma_f32_16x16x32_bf16 v[120:123], a[152:155], v[216:219], v[120:123]\n\t" \
  "ds_read_b128 a[172:175], v242 offset:3072\n\t" \
  "v_perm_b32 v67, v227, v224, s79\n\t" \
  "v_mfma_f32_16x16x32_bf16 v[120:123], a[148:151], v[212:215], v[120:123]\n\t" \
  "buffer_load_dwordx2 v[34:35], v252, %[sd], s74 offen offset:2048\n\t" \
  "s_mov_b32 s74, 4096\n\t" \
  "v_mfma_f32_16x16x32_bf16 v[120:123], a[144:147], v[208:211], v[120:123]\n\t" \
  "s_waitcnt lgkmcnt(6)\n\t" \
  "ds_read_b128 a[216:219], v236 offset:320\n\t" \
  "v_perm_b32 v71, v227, v224, s80\n\t" \
  "v_mfma_f32_16x16x32_bf16 v[108:111], a[128:131], a[192:195], v[108:111]\n\t" \
  "ds_read_b128 v[208:211], v242 offset:4096\n\t" \
  "v_accvgpr_read_b32 v224, a232\n\t" \
  "v_mfma_f32_16x16x32_bf16 v[108:111], a[132:135], a[196:199], v[108:111]\n\t" \
  "ds_read_b128 v[212:215], v242 offset:5120\n\t" \
  "v_accvgpr_read_b32 v227, a236\n\t" \
  "v_mfma_f32_16x16x32_bf16 v[108:111], a[136:139], a[200:203], v[108:111]\n\t" \
  "buffer_load_dwordx2 v[36:37], v252, %[sd], s74 offen offset:0\n\t" \
  "v_mfma_f32_16x16x32_bf16 v[108:111], a[140:143], a[204:207], v[108:111]\n\t" \
  "ds_read_b128 v[216:219], v242 offset:6144\n\t" \
  "v_perm_b32 v72, v227, v224, s79\n\t" \
  "v_mfma_f32_16x16x32_bf16 v[124:127], a[156:159], a[204:207], v[124:127]\n\t" \
  "ds_read_b128 v[220:223], v242 offset:7168\n\t" \
  "v_perm_b32 v76, v227, v224, s80\n\t" \
  "v_mfma_f32_16x16x32_bf16 v[124:127], a[152:155], a[200:203], v[124:127]\n\t" \
  "buffer_load_dwordx2 v[38:39], v252, %[sd], s74 offen offset:2048\n\t" \
  "s_mov_b32 s74, 8192\n\t" \
  "v_mfma_f32_16x16x32_bf16 v[124:127], a[148:151], a[196:199], v[124:127]\n\t" \
  "ds_read_b128 a[220:223], v236 offset:0\n\t" \
  "v_accvgpr_read_b32 v224, a233\n\t" \
  "v_mfma_f32_16x16x32_bf16 v[124:127], a[144:147], a[192:195], v[124:127]\n\t" \
  "s_waitcnt lgkmcnt(6)\n\t" \
  "v_accvgpr_read_b32 v227, a237\n\t" \
  "v_perm_b32 v73, v227, v224, s79\n\t" \
  "v_mfma_f32_16x16x32_bf16 v[96:99], v[128:131], a[160:163], v[96:99]\n\t" \
  "ds_read_b128 a[128:131], v242 offset:8192\n\t" \
  "v_mfma_f32_16x16x32_bf16 v[96:99], v[132:135], a[164:167], v[96:99]\n\t" \
  "buffer_load_dwordx2 v[40:41], v252, %[sd], s74 offen offset:0\n\t" \
  "v_mfma_f32_16x16x32_bf16 v[96:99], v[136:139], a[168:171], v[96:99]\n\t" \
  "ds_read_b128 a[132:135], v242 offset:9216\n\t" \
  "v_perm_b32 v77, v227, v224, s80\n\t" \
  "v_mfma_f32_16x16x32_bf16 v[96:99], v[140:143], a[172:175], v[96:99]\n\t" \
  "ds_read_b128 a[136:139], v242 offset:10240\n\t" \
  ".Lhs15:\n\ts_cbranch_execz .Lhs15n\n\t" \
  "v_mfma_f32_16x16x32_bf16 v[112:115], v[156:159], a[172:175], v[112:115]\n\t" \
  "ds_read_b128 a[140:143], v242 offset:11264\n\t" \
  "v_accvgpr_read_b32 v224, a234\n\t" \
  "v_mfma_f32_16x16x32_bf16 v[112:115], v[152:155], a[168:171], v[112:115]\n\t" \
  "buffer_load_dwordx2 v[42:43], v252, %[sd], s74 offen offset:2048\n\t" \
  "s_mov_b32 s74, 12288\n\t" \
  "v_mfma_f32_16x16x32_bf16 v[112:115], v[148:151], a[164:167], v[112:115]\n\t" \
  "v_mfma_f32_16x16x32_bf16 v[112:115], v[144:147], a[160:163], v[112:115]\n\t" \
  "s_waitcnt lgkmcnt(5)\n\t" \
  "v_accvgpr_read_b32 v227, a238\n\t" \
  "v_mfma_f32_16x16x32_bf16 v[100:103], v[128:131], v[208:211], v[100:103]\n\t" \
  "ds_read_b128 a[144:147], v242 offset:12288\n\t" \
  "v_accvgpr_write_b32 a192, 0\n\t" \
  "v_mfma_f32_16x16x32_bf16 v[100:103], v[132:135], v[212:215], v[100:103]\n\t" \
  "buffer_load_dwordx2 v[44:45], v252, %[sd], s74 offen offset:0\n\t" \
  "v_mfma_f32_16x16x32_bf16 v[100:103], v[136:139], v[216:219], v[100:103]\n\t" \
  "ds_read_b128 a[148:151], v242 offset:13312\n\t" \
  "v_accvgpr_write_b32 a193, 0\n\t" \
  "v_mfma_f32_16x16x32_bf16 v[100:103], v[140:143], v[220:223], v[100:103]\n\t" \
  "ds_read_b128 a[152:155], v242 offset:14336\n\t" \
  "v_accvgpr_write_b32 a194, v238\n\t" \
  "v_mfma_f32_16x16x32_bf16 v[116:119], v[156:159], v[220:223], v[116:119]\n\t" \
  "ds_read_b128 a[156:159], v242 offset:15360\n\t" \
  "v_accvgpr_write_b32 a195, v239\n\t" \
  "v_mfma_f32_16x16x32_bf16 v[116:119], v[152:155], v[216:219], v[116:119]\n\t" \
  "buffer_load_dwordx2 v[46:47], v252, %[sd], s74 offen offset:2048\n\t" \
  "v_mfma_f32_16x16x32_bf16 v[116:119], v[148:151], v[212:215], v[116:119]\n\t" \
  "v_accvgpr_write_b32 a196, v238\n\t" \
  "v_accvgpr_write_b32 a197, v239\n\t" \
  "v_mfma_f32_16x16x32_bf16 v[116:119], v[144:147], v[208:211], v[116:119]\n\t" \
  "s_waitcnt vmcnt(14) lgkmcnt(4)\n\t" \
  "s_barrier\n\t" \
  "v_mfma_f32_16x16x32_bf16 v[104:107], v[128:131], a[128:131], v[104:107]\n\t" \
  "ds_read_b128 v[16:19], v242 offset:49152\n\t" \
  "v_accvgpr_write_b32 a198, 0\n\t" \
  "v_mfma_f32_16x16x32_bf16 v[104:107], v[132:135], a[132:135], v[104:107]\n\t" \
  "ds_read_b128 v[20:23], v242 offset:50176\n\t" \
  "v_accvgpr_write_b32 a199, 0\n\t" \
  "v_mfma_f32_16x16x32_bf16 v[104:107], v[136:139], a[136:139], v[104:107]\n\t" \
  "buffer_load_dwordx4 v242, %[ws], s71 offen offset:0 sc1 lds\n\t" \
  "v_mfma_f32_16x16x32_bf16 v[104:107], v[140:143], a[140:143], v[104:107]\n\t" \
  "ds_read_b128 v[24:27], v242 offset:51200\n\t" \
  "ds_read_b64 v[208:209], v231 offset:0\n\t" \
  "v_mfma_f32_16x16x32_bf16 v[120:123], v[156:159], a[140:143], v[120:123]\n\t" \
  "ds_read_b128 v[28:31], v242 offset:52224\n\t" \
  "v_perm_b32 v74, v227, v224, s79\n\t" \
  "v_mfma_f32_16x16x32_bf16 v[120:123], v[152:155], a[136:139], v[120:123]\n\t" \
  "ds_read_b64 v[210:211], v231 offset:4096\n\t" \
  "v_perm_b32 v78, v227, v224, s80\n\t" \
  "v_mfma_f32_16x16x32_bf16 v[120:123], v[148:151], a[132:135], v[120:123]\n\t" \
  "buffer_load_dwordx4 v242, %[ws], s71 offen offset:1024 sc1 lds\n\t" \
  "v_mfma_f32_16x16x32_bf16 v[120:123], v[144:147], a[128:131], v[120:123]\n\t" \
  "s_waitcnt lgkmcnt(6)\n\t" \
  "ds_read_b64 v[212:213], v231 offset:8192\n\t" \
  "v_accvgpr_read_b32 v224, a235\n\t" \
  "v_mfma_f32_16x16x32_bf16 v[108:111], v[128:131], a[144:147], v[108:111]\n\t" \
  "ds_read_b128 a[8:11], v242 offset:53248\n\t" \
  "ds_read_b64 v[214:215], v231 offset:12288\n\t" \
  "v_mfma_f32_16x16x32_bf16 v[108:111], v[132:135], a[148:151], v[108:111]\n\t" \
  "ds_read_b128 a[24:27], v242 offset:54272\n\t" \
  "ds_read_b64 v[216:217], v233 offset:0\n\t" \
  "v_mfma_f32_16x16x32_bf16 v[108:111], v[136:139], a[152:155], v[108:111]\n\t" \
  "ds_read_b128 a[40:43], v242 offset:55296\n\t" \
  "v_accvgpr_read_b32 v227, a239\n\t" \
  "v_mfma_f32_16x16x32_bf16 v[108:111], v[140:143], a[156:159], v[108:111]\n\t" \
  "buffer_load_dwordx4 v242, %[ws], s71 offen offset:2048 sc1 lds\n\t" \
  "v_mfma_f32_16x16x32_bf16 v[124:127], v[156:159], a[156:159], v[124:127]\n\t" \
  "ds_read_b64 v[218:219], v233 offset:4096\n\t" \
  "ds_read_b64 v[220:221], v233 offset:8192\n\t" \
  "v_mfma_f32_16x16x32_bf16 v[124:127], v[152:155], a[152:155], v[124:127]\n\t" \
  "ds_read_b128 a[56:59], v242 offset:56320\n\t" \
  "v_perm_b32 v75, v227, v224, s79\n\t" \
  "ds_read_b64 v[222:223], v233 offset:12288\n\t" \
  "buffer_load_dword v228, v235, %[ws], s68 offen sc1\n\t" \
  "v_mfma_f32_16x16x32_bf16 v[124:127], v[148:151], a[148:151], v[124:127]\n\t" \
  "v_perm_b32 v79, v227, v224, s80\n\t" \
  "v_accvgpr_read_b32 v224, a240\n\t" \
  "v_mfma_f32_16x16x32_bf16 v[124:127], v[144:147], a[144:147], v[124:127]\n\t" \
  "s_waitcnt lgkmcnt(11)\n\t" \
  "v_accvgpr_read_b32 v227, a244\n\t" \
  "v_perm_b32 v80, v227, v224, s79\n\t" \
  "v_mfma_f32_16x16x32_bf16 v[96:99], a[12:15], v[16:19], v[96:99]\n\t" \
  "buffer_load_dwordx4 v242, %[ws], s71 offen offset:3072 sc1 lds\n\t" \
  "v_mfma_f32_16x16x32_bf16 v[96:99], a[28:31], v[20:23], v[96:99]\n\t" \
  "ds_read_b128 v[128:131], v242 offset:57344\n\t" \
  "v_perm_b32 v84, v227, v224, s80\n\t" \
  "v_mfma_f32_16x16x32_bf16 v[96:99], a[44:47], v[24:27], v[96:99]\n\t" \
  "ds_read_b128 v[132:135], v242 offset:58368\n\t" \
  "v_accvgpr_read_b32 v224, a241\n\t" \
  "v_mfma_f32_16x16x32_bf16 v[96:99], a[60:63], v[28:31], v[96:99]\n\t" \
  "ds_read_b128 a[72:75], v242 offset:59392\n\t" \
  "s_waitcnt vmcnt(13)\n\t" \
  "v_cmp_ne_u32 vcc, %[tag], v246\n\t" \
  "s_and_b32 vcc_lo, vcc_lo, 0xff\n\t" \
  "s_or_b32 s81, s81, vcc_lo\n\t" \
  "v_mfma_f32_16x16x32_bf16 v[112:115], a[124:127], v[28:31], v[112:115]\n\t" \
  "ds_read_b128 a[88:91], v242 offset:60416\n\t" \
  "v_mfma_f32_16x16x32_bf16 v[112:115], a[108:111], v[24:27], v[112:115]\n\t" \
  "s_and_b32 s72, s81, 15\n\t" \
  "s_cselect_b32 s72, -1, 0\n\t" \
  "v_lshl_or_b32 v247, s72, 31, v242\n\t" \
  "s_add_u32 s72, s63, 0\n\t" \
  "v_mfma_f32_16x16x32_bf16 v[112:115], a[92:95], v[20:23], v[112:115]\n\t" \
  "buffer_load_dwordx4 a[128:131], v247, %[ws], s72 offen sc1\n\t" \
  "v_mfma_f32_16x16x32_bf16 v[112:115], a[76:79], v[16:19], v[112:115]\n\t" \
  "s_waitcnt lgkmcnt(5)\n\t" \
  "s_add_u32 s72, s64, 32768\n\t" \
  "v_accvgpr_read_b32 v227, a245\n\t" \
  "v_mfma_f32_16x16x32_bf16 v[100:103], a[12:15], a[8:11], v[100:103]\n\t" \
  "ds_read_b128 a[104:107], v242 offset:61440\n\t" \
  "v_perm_b32 v81, v227, v224, s79\n\t" \
  "v_mfma_f32_16x16x32_bf16 v[100:103], a[28:31], a[24:27], v[100:103]\n\t" \
  "buffer_load_dwordx4 a[140:143], v247, %[ws], s72 offen sc1\n\t" \
  "v_mfma_f32_16x16x32_bf16 v[100:103], a[44:47], a[40:43], v[100:103]\n\t" \
  "s_add_u32 s72, s63, 65536\n\t" \
  "ds_read_b128 a[120:123], v242 offset:62464\n\t" \
  "v_perm_b32 v85, v227, v224, s80\n\t" \
  "v_mfma_f32_16x16x32_bf16 v[100:103], a[60:63], a[56:59], v[100:103]\n\t" \
  "ds_read_b128 a[176:179], v242 offset:63488\n\t" \
  "v_mfma_f32_16x16x32_bf16 v[116:119], a[124:127], a[56:59], v[116:119]\n\t" \
  "ds_read_b128 a[180:183], v242 offset:64512\n\t" \
  "v_accvgpr_read_b32 v224, a242\n\t" \
  "v_mfma_f32_16x16x32_bf16 v[116:119], a[108:111], a[40:43], v[116:119]\n\t" \
  "buffer_load_dwordx4 a[152:155], v247, %[ws], s72 offen sc1\n\t" \
  "s_add_u32 s72, s64, 98304\n\t" \
  "v_mfma_f32_16x16x32_bf16 v[116:119], a[92:95], a[24:27], v[116:119]\n\t" \
  "s_waitcnt lgkmcnt(8)\n\t" \
  "v_lshlrev_b32 v0, 16, v208\n\t" \
  "v_and_b32 v1, 0xffff0000, v208\n\t" \
  "v_mfma_f32_16x16x32_bf16 v[116:119], a[76:79], a[8:11], v[116:119]\n\t" \
  "s_waitcnt lgkmcnt(4)\n\t" \
  "s_barrier\n\t" \
  "v_mfma_f32_16x16x32_bf16 v[104:107], a[12:15], v[128:131], v[104:107]\n\t" \
  "ds_read_b128 a[184:187], v230 offset:0\n\t" \
  "v_lshlrev_b32 v2, 16, v209\n\t" \
  "v_mfma_f32_16x16x32_bf16 v[104:107], a[28:31], v[132:135], v[104:107]\n\t" \
  "ds_read_b128 a[188:191], v230 offset:1024\n\t" \
  "v_and_b32 v3, 0xffff0000, v209\n\t" \
  "v_mfma_f32_16x16x32_bf16 v[104:107], a[44:47], a[72:75], v[104:107]\n\t" \
  "buffer_load_dwordx4 a[164:167], v247, %[ws], s72 offen sc1\n\t" \
  "v_mfma_f32_16x16x32_bf16 v[104:107], a[60:63], a[88:91], v[104:107]\n\t" \
  "s_and_b32 s72, s81, 240\n\t" \
  "s_cselect_b32 s72, -1, 0\n\t" \
  "v_lshl_or_b32 v247, s72, 31, v242\n\t" \
  "s_add_u32 s72, s63, 4096\n\t" \
  "v_mfma_f32_16x16x32_bf16 v[120:123], a[124:127], a[88:91], v[120:123]\n\t" \
  "ds_read_b128 v[136:139], v230 offset:2048\n\t" \
  "v_lshlrev_b32 v4, 16, v210\n\t" \
  "v_mfma_f32_16x16x32_bf16 v[120:123], a[108:111], a[72:75], v[120:123]\n\t" \
  "ds_read_b128 v[140:143], v230 offset:3072\n\t" \
  "v_and_b32 v5, 0xffff0000, v210\n\t" \
  "v_mfma_f32_16x16x32_bf16 v[120:123], a[92:95], v[132:135], v[120:123]\n\t" \
  "buffer_load_dwordx4 a[132:135], v247, %[ws], s72 offen sc1\n\t" \
  "s_add_u32 s72, s64, 36864\n\t" \
  "v_mfma_f32_16x16x32_bf16 v[120:123], a[76:79], v[128:131], v[120:123]\n\t" \
  "s_waitcnt lgkmcnt(4)\n\t" \
  "v_lshlrev_b32 v6, 16, v211\n\t" \
  "v_and_b32 v7, 0xffff0000, v211\n\t" \
  "v_mfma_f32_16x16x32_bf16 v[108:111], a[12:15], a[104:107], v[108:111]\n\t" \
  "ds_read_b128 v[128:131], v230 offset:4096\n\t" \
  "v_accvgpr_read_b32 v227, a246\n\t" \
  "v_mfma_f32_16x16x32_bf16 v[108:111], a[28:31], a[120:123], v[108:111]\n\t" \
  "ds_read_b128 v[132:135], v230 offset:5120\n\t" \
  "v_lshlrev_b32 v8, 16, v212\n\t" \
  "v_mfma_f32_16x16x32_bf16 v[108:111], a[44:47], a[176:179], v[108:111]\n\t" \
  "ds_read_b128 v[144:147], v230 offset:6144\n\t" \
  "v_perm_b32 v82, v227, v224, s79\n\t" \
  "v_mfma_f32_16x16x32_bf16 v[108:111], a[60:63], a[180:183], v[108:111]\n\t" \
  "buffer_load_dwordx4 a[144:147], v247, %[ws], s72 offen sc1\n\t" \
  "s_add_u32 s72, s63, 69632\n\t" \
  "v_mfma_f32_16x16x32_bf16 v[124:127], a[124:127], a[180:183], v[124:127]\n\t" \
  "ds_read_b128 v[148:151], v230 offset:7168\n\t" \
  "v_and_b32 v9, 0xffff0000, v212\n\t" \
  "v_mfma_f32_16x16x32_bf16 v[124:127], a[108:111], a[176:179], v[124:127]\n\t" \
  "v_perm_b32 v86, v227, v224, s80\n\t" \
  "v_lshlrev_b32 v10, 16, v213\n\t" \
  "v_mfma_f32_16x16x32_bf16 v[124:127], a[92:95], a[120:123], v[124:127]\n\t" \
  "v_accvgpr_read_b32 v224, a243\n\t" \
  "v_and_b32 v11, 0xffff0000, v213\n\t" \
  "v_mfma_f32_16x16x32_bf16 v[124:127], a[76:79], a[104:107], v[124:127]\n\t" \
  "s_waitcnt lgkmcnt(4)\n\t" \
  "buffer_load_dwordx4 a[156:159], v247, %[ws], s72 offen sc1\n\t" \
  "s_add_u32 s72, s64, 102400\n\t" \
  "v_mfma_f32_16x16x32_bf16 v[96:99], a[96:99], a[184:187], v[96:99]\n\t" \
  "ds_read_b128 v[152:155], v230 offset:8192\n\t" \
  "v_lshlrev_b32 v12, 16, v214\n\t" \
  "v_mfma_f32_16x16x32_bf16 v[96:99], a[112:115], a[188:191], v[96:99]\n\t" \
  "ds_read_b128 v[156:159], v230 offset:9216\n\t" \
  "v_accvgpr_read_b32 v227, a247\n\t" \
  "v_mfma_f32_16x16x32_bf16 v[96:99], a[100:103], v[136:139], v[96:99]\n\t" \
  "ds_read_b128 a[8:11], v230 offset:10240\n\t" \
  "v_and_b32 v13, 0xffff0000, v214\n\t" \
  "v_mfma_f32_16x16x32_bf16 v[96:99], a[116:119], v[140:143], v[96:99]\n\t" \
  "buffer_load_dwordx4 a[168:171], v247, %[ws], s72 offen sc1\n\t" \
  "v_mfma_f32_16x16x32_bf16 v[112:115], a[84:87], v[140:143], v[112:115]\n\t" \
  "ds_read_b128 a[24:27], v230 offset:11264\n\t" \
  "v_perm_b32 v83, v227, v224, s79\n\t" \
  "v_mfma_f32_16x16x32_bf16 v[112:115], a[80:83], v[136:139], v[112:115]\n\t" \
  "v_lshlrev_b32 v14, 16, v215\n\t" \
  "v_perm_b32 v87, v227, v224, s80\n\t" \
  "v_mfma_f32_16x16x32_bf16 v[112:115], a[68:71], a[188:191], v[112:115]\n\t" \
  "v_and_b32 v15, 0xffff0000, v215\n\t" \
  "v_accvgpr_read_b32 v224, a248\n\t" \
  "v_mfma_f32_16x16x32_bf16 v[112:115], a[64:67], a[184:187], v[112:115]\n\t" \
  "s_waitcnt lgkmcnt(4)\n\t" \
  "v_lshlrev_b32 v16, 16, v216\n\t" \
  "v_and_b32 v17, 0xffff0000, v216\n\t" \
  "v_mfma_f32_16x16x32_bf16 v[100:103], a[96:99], v[128:131], v[100:103]\n\t" \
  "ds_read_b128 a[40:43], v230 offset:12288\n\t" \
  "v_accvgpr_read_b32 v227, a252\n\t" \
  "v_mfma_f32_16x16x32_bf16 v[100:103], a[112:115], v[132:135], v[100:103]\n\t" \
  "ds_read_b128 a[56:59], v230 offset:13312\n\t" \
  "v_lshlrev_b32 v18, 16, v217\n\t" \
  "v_mfma_f32_16x16x32_bf16 v[100:103], a[100:103], v[144:147], v[100:103]\n\t" \
  "ds_read_b128 a[72:75], v230 offset:14336\n\t" \
  "v_perm_b32 v88, v227, v224, s79\n\t" \
  "v_mfma_f32_16x16x32_bf16 v[100:103], a[116:119], v[148:151], v[100:103]\n\t" \
  "ds_read_b128 a[88:91], v230 offset:15360\n\t" \
  "v_and_b32 v19, 0xffff0000, v217\n\t" \
  "s_waitcnt vmcnt(9)\n\t" \
  "v_cmp_ne_u32 vcc, %[tag], v228\n\t" \
  "s_and_b32 vcc_lo, vcc_lo, 0xf00\n\t" \
  "s_or_b32 s81, s81, vcc_lo\n\t" \
  "v_mfma_f32_16x16x32_bf16 v[116:119], a[84:87], v[148:151], v[116:119]\n\t" \
  "s_and_b32 s72, s81, 3840\n\t" \
  "s_cselect_b32 s72, -1, 0\n\t" \
  "v_lshl_or_b32 v247, s72, 31, v242\n\t" \
  "s_add_u32 s72, s63, 16384\n\t" \
  "v_mfma_f32_16x16x32_bf16 v[116:119], a[80:83], v[144:147], v[116:119]\n\t" \
  "buffer_load_dwordx4 a[136:139], v247, %[ws], s72 offen sc1\n\t" \
  "v_mfma_f32_16x16x32_bf16 v[116:119], a[68:71], v[132:135], v[116:119]\n\t" \
  "s_add_u32 s72, s64, 49152\n\t" \
  "v_perm_b32 v92, v227, v224, s80\n\t" \
  "v_lshlrev_b32 v20, 16, v218\n\t" \
  "v_mfma_f32_16x16x32_bf16 v[116:119], a[64:67], v[128:131], v[116:119]\n\t" \
  "s_waitcnt vmcnt(9) lgkmcnt(4)\n\t" \
  "s_barrier\n\t" \
  "v_mfma_f32_16x16x32_bf16 v[104:107], a[96:99], v[152:155], v[104:107]\n\t" \
  "buffer_load_dwordx4 a[148:151], v247, %[ws], s72 offen sc1\n\t" \
  "v_mfma_f32_16x16x32_bf16 v[104:107], a[112:115], v[156:159], v[104:107]\n\t" \
  "s_add_u32 s72, s63, 81920\n\t" \
  "ds_read_b128 v[128:131], v242 offset:16384\n\t" \
  "v_accvgpr_read_b32 v224, a249\n\t" \
  "v_mfma_f32_16x16x32_bf16 v[104:107], a[100:103], a[8:11], v[104:107]\n\t" \
  "ds_read_b128 v[132:135], v242 offset:17408\n\t" \
  "v_and_b32 v21, 0xffff0000, v218\n\t" \
  "v_mfma_f32_16x16x32_bf16 v[104:107], a[116:119], a[24:27], v[104:107]\n\t" \
  "buffer_load_dwordx4 a[160:163], v247, %[ws], s72 offen sc1\n\t" \
  "v_mfma_f32_16x16x32_bf16 v[120:123], a[84:87], a[24:27], v[120:123]\n\t" \
  "s_add_u32 s72, s64, 114688\n\t" \
  "ds_read_b128 a[12:15], v242 offset:18432\n\t" \
  "v_lshlrev_b32 v22, 16, v219\n\t" \
  "v_mfma_f32_16x16x32_bf16 v[120:123], a[80:83], a[8:11], v[120:123]\n\t" \
  "ds_read_b128 a[28:31], v242 offset:19456\n\t" \
  "v_accvgpr_read_b32 v227, a253\n\t" \
  "v_mfma_f32_16x16x32_bf16 v[120:123], a[68:71], v[156:159], v[120:123]\n\t" \
  "v_and_b32 v23, 0xffff0000, v219\n\t" \
  "v_perm_b32 v89, v227, v224, s79\n\t" \
  "v_mfma_f32_16x16x32_bf16 v[120:123], a[64:67], v[152:155], v[120:123]\n\t" \
  "s_waitcnt lgkmcnt(4)\n\t" \
  "buffer_load_dwordx4 a[172:175], v247, %[ws], s72 offen sc1\n\t" \
  "v_mfma_f32_16x16x32_bf16 v[108:111], a[96:99], a[40:43], v[108:111]\n\t" \
  "ds_read_b128 a[44:47], v242 offset:20480\n\t" \
  "v_lshlrev_b32 v24, 16, v220\n\t" \
  "v_mfma_f32_16x16x32_bf16 v[108:111], a[112:115], a[56:59], v[108:111]\n\t" \
  "ds_read_b128 a[60:63], v242 offset:21504\n\t" \
  "v_perm_b32 v93, v227, v224, s80\n\t" \
  "v_mfma_f32_16x16x32_bf16 v[108:111], a[100:103], a[72:75], v[108:111]\n\t" \
  "ds_read_b128 a[76:79], v242 offset:22528\n\t" \
  "v_and_b32 v25, 0xffff0000, v220\n\t" \
  "v_mfma_f32_16x16x32_bf16 v[108:111], a[116:119], a[88:91], v[108:111]\n\t" \
  "ds_read_b128 a[92:95], v242 offset:23552\n\t" \
  "v_accvgpr_read_b32 v224, a250\n\t" \
  "v_mfma_f32_16x16x32_bf16 v[124:127], a[84:87], a[88:91], v[124:127]\n\t" \
  "v_lshlrev_b32 v26, 16, v221\n\t" \
  "v_and_b32 v27, 0xffff0000, v221\n\t" \
  "v_mfma_f32_16x16x32_bf16 v[124:127], a[80:83], a[72:75], v[124:127]\n\t" \
  "v_accvgpr_read_b32 v227, a254\n\t" \
  "v_lshlrev_b32 v28, 16, v222\n\t" \
  "v_mfma_f32_16x16x32_bf16 v[124:127], a[68:71], a[56:59], v[124:127]\n\t" \
  "v_perm_b32 v90, v227, v224, s79\n\t" \
  "v_and_b32 v29, 0xffff0000, v222\n\t" \
  "v_mfma_f32_16x16x32_bf16 v[124:127], a[64:67], a[40:43], v[124:127]\n\t" \
  "s_waitcnt lgkmcnt(4)\n\t" \
  "v_perm_b32 v94, v227, v224, s80\n\t" \
  "v_lshlrev_b32 v30, 16, v223\n\t" \
  "v_mfma_f32_16x16x32_bf16 v[96:99], a[32:35], v[128:131], v[96:99]\n\t" \
  "ds_read_b128 a[104:107], v242 offset:24576\n\t" \
  "v_accvgpr_read_b32 v224, a251\n\t" \
  "v_mfma_f32_16x16x32_bf16 v[96:99], a[48:51], v[132:135], v[96:99]\n\t" \
  "ds_read_b128 a[108:111], v242 offset:25600\n\t" \
  "v_and_b32 v31, 0xffff0000, v223\n\t" \
  "v_mfma_f32_16x16x32_bf16 v[96:99], a[36:39], a[12:15], v[96:99]\n\t" \
  "buffer_load_dword v246, v235, %[ws], s68 offen sc1\n\t" \
  "v_mfma_f32_16x16x32_bf16 v[96:99], a[52:55], a[28:31], v[96:99]\n\t" \
  "ds_read_b128 a[120:123], v242 offset:26624\n\t" \
  "v_accvgpr_read_b32 v227, a255\n\t" \
  "v_mfma_f32_16x16x32_bf16 v[112:115], a[20:23], a[28:31], v[112:115]\n\t" \
  "ds_read_b128 a[124:127], v242 offset:27648\n\t" \
  "v_perm_b32 v91, v227, v224, s79\n\t" \
  "v_mfma_f32_16x16x32_bf16 v[112:115], a[4:7], a[12:15], v[112:115]\n\t" \
  "v_mfma_f32_16x16x32_bf16 v[112:115], a[16:19], v[132:135], v[112:115]\n\t" \
  "v_perm_b32 v95, v227, v224, s80\n\t" \
  "v_mfma_f32_16x16x32_bf16 v[112:115], a[0:3], v[128:131], v[112:115]\n\t" \
  "s_waitcnt lgkmcnt(4)\n\t" \
  "v_mfma_f32_16x16x32_bf16 v[100:103], a[32:35], a[44:47], v[100:103]\n\t" \
  "ds_read_b128 a[176:179], v242 offset:28672\n\t" \
  "v_mfma_f32_16x16x32_bf16 v[100:103], a[48:51], a[60:63], v[100:103]\n\t" \
  "ds_read_b128 a[180:183], v242 offset:29696\n\t" \
  "v_mfma_f32_16x16x32_bf16 v[100:103], a[36:39], a[76:79], v[100:103]\n\t" \
  "ds_read_b128 a[184:187], v242 offset:30720\n\t" \
  "v_mfma_f32_16x16x32_bf16 v[100:103], a[52:55], a[92:95], v[100:103]\n\t" \
  "ds_read_b128 a[188:191], v242 offset:31744\n\t" \
  "v_mfma_f32_16x16x32_bf16 v[116:119], a[20:23], a[92:95], v[116:119]\n\t" \
  "v_mfma_f32_16x16x32_bf16 v[116:119], a[4:7], a[76:79], v[116:119]\n\t" \
  "v_mfma_f32_16x16x32_bf16 v[116:119], a[16:19], a[60:63], v[116:119]\n\t" \
  "v_mfma_f32_16x16x32_bf16 v[116:119], a[0:3], a[44:47], v[116:119]\n\t" \
  "s_waitcnt lgkmcnt(4)\n\t" \
  "v_mfma_f32_16x16x32_bf16 v[104:107], a[32:35], a[104:107], v[104:107]\n\t" \
  "ds_read_b128 v[128:131], v237 offset:2560\n\t" \
  "v_mfma_f32_16x16x32_bf16 v[104:107], a[48:51], a[108:111], v[104:107]\n\t" \
  "ds_read_b128 v[132:135], v237 offset:2624\n\t" \
  "v_mfma_f32_16x16x32_bf16 v[104:107], a[36:39], a[120:123], v[104:107]\n\t" \
  "v_mfma_f32_16x16x32_bf16 v[104:107], a[52:55], a[124:127], v[104:107]\n\t" \
  "v_mfma_f32_16x16x32_bf16 v[120:123], a[20:23], a[124:127], v[120:123]\n\t" \
  "v_mfma_f32_16x16x32_bf16 v[120:123], a[4:7], a[120:123], v[120:123]\n\t" \
  "v_mfma_f32_16x16x32_bf16 v[120:123], a[16:19], a[108:111], v[120:123]\n\t" \
  "v_mfma_f32_16x16x32_bf16 v[120:123], a[0:3], a[104:107], v[120:123]\n\t" \
  "s_waitcnt lgkmcnt(2)\n\t" \
  "v_mfma_f32_16x16x32_bf16 v[108:111], a[32:35], a[176:179], v[108:111]\n\t" \
  "v_mfma_f32_16x16x32_bf16 v[108:111], a[48:51], a[180:183], v[108:111]\n\t" \
  "v_mfma_f32_16x16x32_bf16 v[108:111], a[36:39], a[184:187], v[108:111]\n\t" \
  "v_mfma_f32_16x16x32_bf16 v[108:111], a[52:55], a[188:191], v[108:111]\n\t" \
  "v_mfma_f32_16x16x32_bf16 v[124:127], a[20:23], a[188:191], v[124:127]\n\t" \
  "v_mfma_f32_16x16x32_bf16 v[124:127], a[4:7], a[184:187], v[124:127]\n\t" \
  "v_mfma_f32_16x16x32_bf16 v[124:127], a[16:19], a[180:183], v[124:127]\n\t" \
  "v_mfma_f32_16x16x32_bf16 v[124:127], a[0:3], a[176:179], v[124:127]\n\t" \
  STP("v_writelane_b32 %[stv], s94, 36\n\t") \
  STP("v_writelane_b32 %[stv], s92, 37\n\t") \
  STP("s_memrealtime s[94:95]\n\t") \
  STP("s_memtime s[92:93]\n\t") \
  "s_cmp_lg_u32 s81, 0\n\t" \
  "s_cbranch_scc1 .Lop_s99\n\t" \
  ".Lop_d2:\n\t" \
  "s_waitcnt vmcnt(9)\n\t" \
  "v_mfma_f32_16x16x32_bf16 v[96:99], a[196:199], a[128:131], v[96:99]\n\t" \
  "v_mfma_f32_16x16x32_bf16 v[100:103], a[196:199], a[140:143], v[100:103]\n\t" \
  "v_mfma_f32_16x16x32_bf16 v[104:107], a[196:199], a[152:155], v[104:107]\n\t" \
  "v_mfma_f32_16x16x32_bf16 v[108:111], a[196:199], a[164:167], v[108:111]\n\t" \
  "s_waitcnt vmcnt(5)\n\t" \
  "v_mfma_f32_16x16x32_bf16 v[112:115], a[194:197], a[130:133], v[112:115]\n\t" \
  "v_mfma_f32_16x16x32_bf16 v[116:119], a[194:197], a[142:145], v[116:119]\n\t" \
  "v_mfma_f32_16x16x32_bf16 v[120:123], a[194:197], a[154:157], v[120:123]\n\t" \
  "v_mfma_f32_16x16x32_bf16 v[124:127], a[194:197], a[166:169], v[124:127]\n\t" \
  "s_waitcnt vmcnt(1)\n\t" \
  "v_mfma_f32_16x16x32_bf16 v[96:99], a[194:197], a[134:137], v[96:99]\n\t" \
  "v_mfma_f32_16x16x32_bf16 v[100:103], a[194:197], a[146:149], v[100:103]\n\t" \
  "v_mfma_f32_16x16x32_bf16 v[104:107], a[194:197], a[158:161], v[104:107]\n\t" \
  "v_mfma_f32_16x16x32_bf16 v[108:111], a[194:197], a[170:173], v[108:111]\n\t" \
  "v_mfma_f32_16x16x32_bf16 v[112:115], a[192:195], a[136:139], v[112:115]\n\t" \
  "v_mfma_f32_16x16x32_bf16 v[116:119], a[192:195], a[148:151], v[116:119]\n\t" \
  "v_mfma_f32_16x16x32_bf16 v[120:123], a[192:195], a[160:163], v[120:123]\n\t" \
  "v_mfma_f32_16x16x32_bf16 v[124:127], a[192:195], a[172:175], v[124:127]\n\t" \
  "s_waitcnt vmcnt(0) lgkmcnt(0)\n\t" \
  STP("v_writelane_b32 %[stv], s94, 38\n\t") \
  STP("v_writelane_b32 %[stv], s92, 39\n\t") \
  "s_nop 7\n\t" \
  ".subsection 1\n\t" \
  ".Lop_s0:\n\t" \
  "s_mov_b32 s78, 0\n\t" \
  ".Lop_r0:\n\t" \
  "s_sleep 2\n\t" \
  "buffer_load_dword v246, v245, %[ws], %[fw2] offen sc1\n\t" \
  "s_waitcnt vmcnt(0)\n\t" \
  "v_cmp_ne_u32 vcc, %[tag], v246\n\t" \
  "s_and_b32 vcc_lo, vcc_lo, 0x6\n\t" \
  "s_cbranch_scc0 .Lop_k0\n\t" \
  "s_add_u32 s78, s78, 1\n\t" \
  "s_cmp_lt_u32 s78, %[cap]\n\t" \
  "s_cbranch_scc1 .Lop_r0\n\t" \
  "buffer_store_dword v243, off, %[ws], 0 offset:256 sc1\n\t" \
  "s_branch .Lop_k0\n\t" \
  ".Lop_s1:\n\t" \
  "s_mov_b32 s78, 0\n\t" \
  ".Lop_r1:\n\t" \
  "s_sleep 2\n\t" \
  "buffer_load_dword v246, v245, %[ws], %[fw2] offen sc1\n\t" \
  "s_waitcnt vmcnt(0)\n\t" \
  "v_cmp_ne_u32 vcc, %[tag], v246\n\t" \
  "s_and_b32 vcc_lo, vcc_lo, 0xe00\n\t" \
  "s_cbranch_scc0 .Lop_k1\n\t" \
  "s_add_u32 s78, s78, 1\n\t" \
  "s_cmp_lt_u32 s78, %[cap]\n\t" \
  "s_cbranch_scc1 .Lop_r1\n\t" \
  "buffer_store_dword v243, off, %[ws], 0 offset:256 sc1\n\t" \
  "s_branch .Lop_k1\n\t" \
  ".Lop_s2:\n\t" \
  "s_mov_b32 s78, 0\n\t" \
  ".Lop_r2:\n\t" \
  "s_sleep 2\n\t" \
  "buffer_load_dword v246, v245, %[ws], %[fw2] offen sc1\n\t" \
  "s_waitcnt vmcnt(0)\n\t" \
  "v_cmp_ne_u32 vcc, %[tag], v246\n\t" \
  "s_and_b32 vcc_lo, vcc_lo, 0xfe00\n\t" \
  "s_cbranch_scc0 .Lop_k2\n\t" \
  "s_add_u32 s78, s78, 1\n\t" \
  "s_cmp_lt_u32 s78, %[cap]\n\t" \
  "s_cbranch_scc1 .Lop_r2\n\t" \
  "buffer_store_dword v243, off, %[ws], 0 offset:256 sc1\n\t" \
  "s_branch .Lop_k2\n\t" \
  ".Lop_s3:\n\t" \
  "s_mov_b32 s78, 0\n\t" \
  ".Lop_r3:\n\t" \
  "s_sleep 2\n\t" \
  "buffer_load_dword v246, v245, %[ws], %[fw2] offen sc1\n\t" \
  "s_waitcnt vmcnt(0)\n\t" \
  "v_cmp_ne_u32 vcc, %[tag], v246\n\t" \
  "s_and_b32 vcc_lo, vcc_lo, 0xfe0000\n\t" \
  "s_cbranch_scc0 .Lop_k3\n\t" \
  "s_add_u32 s78, s78, 1\n\t" \
  "s_cmp_lt_u32 s78, %[cap]\n\t" \
  "s_cbranch_scc1 .Lop_r3\n\t" \
  "buffer_store_dword v243, off, %[ws], 0 offset:256 sc1\n\t" \
  "s_branch .Lop_k3\n\t" \
  ".Lop_s4:\n\t" \
  "s_mov_b32 s78, 0\n\t" \
  ".Lop_r4:\n\t" \
  "s_sleep 4\n\t" \
  "s_add_u32 s75, s69, 0\n\t" \
  "buffer_load_dwordx4 v[208:211], v170, %[ws], s75 offen sc1\n\t" \
  "s_add_u32 s75, s69, 32768\n\t" \
  "buffer_load_dwordx4 v[212:215], v170, %[ws], s75 offen sc1\n\t" \
  "s_add_u32 s75, s69, 65536\n\t" \
  "buffer_load_dwordx4 v[216:219], v170, %[ws], s75 offen sc1\n\t" \
  "s_add_u32 s75, s69, 98304\n\t" \
  "buffer_load_dwordx4 v[220:223], v170, %[ws], s75 offen sc1\n\t" \
  "s_waitcnt vmcnt(0)\n\t" \
  "v_xor_b32 v246, v243, v209\n\t" \
  "v_bitop3_b32 v246, v246, v211, v243 bitop3:0xf6\n\t" \
  "v_bitop3_b32 v246, v246, v213, v243 bitop3:0xf6\n\t" \
  "v_bitop3_b32 v246, v246, v215, v243 bitop3:0xf6\n\t" \
  "v_bitop3_b32 v246, v246, v217, v243 bitop3:0xf6\n\t" \
  "v_bitop3_b32 v246, v246, v219, v243 bitop3:0xf6\n\t" \
  "v_bitop3_b32 v246, v246, v221, v243 bitop3:0xf6\n\t" \
  "v_bitop3_b32 v246, v246, v223, v243 bitop3:0xf6\n\t" \
  "v_cmp_ne_u32 vcc, 0, v246\n\t" \
  "s_cbranch_vccz .Lop_k4\n\t" \
  "s_add_u32 s78, s78, 1\n\t" \
  "s_cmp_lt_u32 s78, %[cap]\n\t" \
  "s_cbranch_scc1 .Lop_r4\n\t" \
  "buffer_store_dword v243, off, %[ws], 0 offset:256 sc1\n\t" \
  "s_branch .Lop_k4\n\t" \
  ".Lop_rd:\n\t" \
  "s_waitcnt vmcnt(0) lgkmcnt(0)\n\t" \
  "s_barrier\n\t" \
  "s_mov_b32 %[pm], 2\n\t" \
  "s_branch .Lop_end\n\t" \
  ".Lop_s99:\n\t" \
  "s_waitcnt vmcnt(0)\n\t" \
  "v_cmp_ne_u32 vcc, %[tag], v246\n\t" \
  "s_and_b32 vcc_lo, vcc_lo, 0xfff\n\t" \
  "s_cbranch_scc0 .Lop_k99\n\t" \
  "s_mov_b32 s78, 0\n\t" \
  ".Lop_r99:\n\t" \
  "buffer_load_dword v246, v235, %[ws], s68 offen sc1\n\t" \
  "s_waitcnt vmcnt(0)\n\t" \
  "v_cmp_ne_u32 vcc, %[tag], v246\n\t" \
  "s_and_b32 vcc_lo, vcc_lo, 0xfff\n\t" \
  "s_cbranch_scc0 .Lop_k99\n\t" \
  "s_add_u32 s78, s78, 1\n\t" \
  "s_cmp_lt_u32 s78, %[cap]\n\t" \
  "s_cbranch_scc1 .Lop_r99\n\t" \
  "buffer_store_dword v243, off, %[ws], 0 offset:256 sc1\n\t" \
  "s_branch .Lop_k99\n\t" \
  ".Lop_k99:\n\t" \
  "s_and_b32 s72, s81, 15\n\t" \
  "s_cbranch_scc0 .Lop_m0\n\t" \
  "s_add_u32 s72, s63, 0\n\t" \
  "buffer_load_dwordx4 a[128:131], v242, %[ws], s72 offen sc1\n\t" \
  "s_add_u32 s72, s64, 32768\n\t" \
  "buffer_load_dwordx4 a[140:143], v242, %[ws], s72 offen sc1\n\t" \
  "s_add_u32 s72, s63, 65536\n\t" \
  "buffer_load_dwordx4 a[152:155], v242, %[ws], s72 offen sc1\n\t" \
  "s_add_u32 s72, s64, 98304\n\t" \
  "buffer_load_dwordx4 a[164:167], v242, %[ws], s72 offen sc1\n\t" \
  ".Lop_m0:\n\t" \
  "s_and_b32 s72, s81, 240\n\t" \
  "s_cbranch_scc0 .Lop_m1\n\t" \
  "s_add_u32 s72, s63, 4096\n\t" \
  "buffer_load_dwordx4 a[132:135], v242, %[ws], s72 offen sc1\n\t" \
  "s_add_u32 s72, s64, 36864\n\t" \
  "buffer_load_dwordx4 a[144:147], v242, %[ws], s72 offen sc1\n\t" \
  "s_add_u32 s72, s63, 69632\n\t" \
  "buffer_load_dwordx4 a[156:159], v242, %[ws], s72 offen sc1\n\t" \
  "s_add_u32 s72, s64, 102400\n\t" \
  "buffer_load_dwordx4 a[168:171], v242, %[ws], s72 offen sc1\n\t" \
  ".Lop_m1:\n\t" \
  "s_and_b32 s72, s81, 3840\n\t" \
  "s_cbranch_scc0 .Lop_m2\n\t" \
  "s_add_u32 s72, s63, 16384\n\t" \
  "buffer_load_dwordx4 a[136:139], v242, %[ws], s72 offen sc1\n\t" \
  "s_add_u32 s72, s64, 49152\n\t" \
  "buffer_load_dwordx4 a[148:151], v242, %[ws], s72 offen sc1\n\t" \
  "s_add_u32 s72, s63, 81920\n\t" \
  "buffer_load_dwordx4 a[160:163], v242, %[ws], s72 offen sc1\n\t" \
  "s_add_u32 s72, s64, 114688\n\t" \
  "buffer_load_dwordx4 a[172:175], v242, %[ws], s72 offen sc1\n\t" \
  ".Lop_m2:\n\t" \
  "s_waitcnt vmcnt(0)\n\t" \
  "s_branch .Lop_d2\n\t" \
  ".Lfz_z:\n\t" \
  ".fill 128, 4, 0xBF800000\n\t" \
  ".subsection 0\n\t" \
  ".Lop_end:\n\t" \
  ""
#define OP_CLOB "v208", "v209", "v210", "v211", "v212", "v214", "v215", "v216", "v217", "v218", "v219", "v220", "v221", "v222", "v223", "v228", "v229", "v230", "v231", "v232", "v233", "v235", "v236", "v238", "v239", "v246", "v248", "v249", "v250", "v251", "s60", "s61", "s62", "s63", "s64", "s65", "s66", "s67", "s68", "s69", "s70", "s71", "s72", "s73", "s74", "s75", "s76", "s77", "s78", "s79", "s80", "s81", "s82", "s83", "s84", "s85", "s92", "s93", "s94", "s95", "vcc", "scc", "memory"
#define ST_ASM "s_nop 1\n\t" "v_mfma_f32_16x16x32_bf16 v[80:83], a[208:211], v[160:163], 0\n\t" "v_mfma_f32_16x16x32_bf16 v[80:83], a[212:215], v[164:167], v[80:83]\n\t" "v_mfma_f32_16x16x32_bf16 v[80:83], a[216:219], v[168:171], v[80:83]\n\t" "v_mfma_f32_16x16x32_bf16 v[80:83], a[220:223], v[172:175], v[80:83]\n\t" "v_dot2_f32_bf16 v64, v160, v160, 0\n\t" "v_dot2_f32_bf16 v65, v164, v164, 0\n\t" "v_dot2_f32_bf16 v66, v168, v168, 0\n\t" "v_dot2_f32_bf16 v67, v172, v172, 0\n\t" "v_dot2_f32_bf16 v64, v161, v161, v64\n\t" "v_dot2_f32_bf16 v65, v165, v165, v65\n\t" "v_dot2_f32_bf16 v66, v169, v169, v66\n\t" "v_dot2_f32_bf16 v67, v173, v173, v67\n\t" "v_dot2_f32_bf16 v64, v162, v162, v64\n\t" "v_dot2_f32_bf16 v65, v166, v166, v65\n\t" "v_dot2_f32_bf16 v66, v170, v170, v66\n\t" "v_dot2_f32_bf16 v67, v174, v174, v67\n\t" "v_dot2_f32_bf16 v64, v163, v163, v64\n\t" "v_dot2_f32_bf16 v65, v167, v167, v65\n\t" "v_dot2_f32_bf16 v66, v171, v171, v66\n\t" "v_dot2_f32_bf16 v67, v175, v175, v67\n\t" "s_nop 2\n\t"
static_assert(W_MODB == 2228224);
constexpr int GSLOT = 49152;
constexpr unsigned KRS = 256;
struct KVP { srd_t srdK, srdV; unsigned vkb[4], vvb[2], vksrc[4], vvsrc[2], sofk; };
struct H4 {
  srd_t srdW;
  unsigned voffX, voffW[4];
  unsigned xb1, wb[2][8];
  unsigned mX, mW, soX, rot;
  unsigned pfd, fo, fl, fvo;
  u32x32 q[4];
};
#define W4(a) "+v"(a[0]), "+v"(a[1]), "+v"(a[2]), "+v"(a[3])
#define W8(a) W4(a), "+v"(a[4]), "+v"(a[5]), "+v"(a[6]), "+v"(a[7])
constexpr unsigned kXT = 65536, kWT = 64 * QD * 2;
DI void w_dma4(unsigned m0, srd_t srd, const unsigned (&vo)[4], unsigned so) {
  asm volatile("s_mov_b32 m0, %0\n\ts_nop 0\n\tbuffer_load_dwordx4 %1, %5, %6 offen lds\n\tbuffer_load_dwordx4 %2, %5, %6 offen offset:1024 lds\n\t"
               "buffer_load_dwordx4 %3, %5, %6 offen offset:2048 lds\n\tbuffer_load_dwordx4 %4, %5, %6 offen offset:3072 lds"
               :: "s"(m0), "v"(vo[0]), "v"(vo[1]), "v"(vo[2]), "v"(vo[3]), "s"(srd), "s"(so) : "memory");
}
DI void w_voffs(unsigned (&vo)[4], int ldw, unsigned wid, unsigned lane, unsigned k0) {
#pragma unroll
  for (int j = 0; j < 4; ++j) {
    const unsigned k = (4 * wid + j) * 4 + (lane >> 4), c16 = lane & 15, sw = (k & 3) | (((k >> 3) & 1) << 2);
    vo[j] = (k ^ (32 * k0)) * ldw * 2 + ((((c16 >> 1) ^ sw) << 1) + (c16 & 1)) * 16 - 1024 * j;
  }
}
DI void h4_init_addr(H4& c, unsigned ldsbase, unsigned wid, unsigned lane, unsigned k0) {
  const unsigned r16 = lane & 15, g = lane >> 4, q = (lane >> 2) & 3, p = lane & 3;
  c.xb1 = ldsbase + GSLOT + wid * 8192 + k0 * 4096 + g * 1024 + r16 * 16;
#pragma unroll
  for (int sl = 0; sl < 2; ++sl)
#pragma unroll
    for (int nb = 0; nb < 8; ++nb) { c.wb[sl][nb] = ldsbase + 2 * sl * GSLOT + 32768 + (8 * g + q) * 256 + ((nb ^ (q | ((g & 1) << 2))) << 5) + p * 8; asm volatile("" : "+v"(c.wb[sl][nb])); }
}
#define FSTP(x)
#define FSTS(x)
#define FQ_STV
#define FQ_STC
#define FQ_ASM \
  "s_mov_b32 s98, 0xffff0000\n\t" \
  ".p2align 3\n\t" \
  "ds_read_b128 v[128:131], v208 offset:0\n\t" \
  "ds_read_b128 v[132:135], v208 offset:16\n\t" \
  "ds_read_b128 v[136:139], v208 offset:256\n\t" \
  "ds_read_b128 v[140:143], v208 offset:272\n\t" \
  "ds_read_b128 v[32:35], v204 offset:49152\n\t" \
  "ds_read_b128 v[36:39], v204 offset:51200\n\t" \
  "ds_read_b128 v[40:43], v204 offset:53248\n\t" \
  "ds_read_b128 v[44:47], v204 offset:55296\n\t" \
  "ds_read_b128 v[144:147], v208 offset:128\n\t" \
  "ds_read_b128 v[148:151], v208 offset:144\n\t" \
  "ds_read_b128 v[152:155], v208 offset:384\n\t" \
  "ds_read_b128 v[156:159], v208 offset:400\n\t" \
  "ds_read_b128 v[48:51], v205 offset:49152\n\t" \
  "ds_read_b128 v[52:55], v205 offset:51200\n\t" \
  "ds_read_b128 v[56:59], v205 offset:53248\n\t" \
  "ds_read_b128 v[60:63], v205 offset:55296\n\t" \
  "v_lshlrev_b32 v176, 16, v0\n\t" \
  "v_and_b32 v177, s98, v0\n\t" \
  "v_lshlrev_b32 v178, 16, v1\n\t" \
  "v_and_b32 v179, s98, v1\n\t" \
  "v_lshlrev_b32 v180, 16, v2\n\t" \
  "v_and_b32 v181, s98, v2\n\t" \
  "v_lshlrev_b32 v182, 16, v3\n\t" \
  "v_and_b32 v183, s98, v3\n\t" \
  "v_pk_fma_f32 v[176:177], v[176:177], v[160:161], v[162:163] op_sel_hi:[1,0,0]\n\t" \
  "v_pk_fma_f32 v[178:179], v[178:179], v[160:161], v[162:163] op_sel_hi:[1,0,0]\n\t" \
  "v_pk_fma_f32 v[180:181], v[180:181], v[160:161], v[162:163] op_sel_hi:[1,0,0]\n\t" \
  "v_pk_fma_f32 v[182:183], v[182:183], v[160:161], v[162:163] op_sel_hi:[1,0,0]\n\t" \
  "s_waitcnt lgkmcnt(12)\n\t" \
  "s_nop 0\n\t" \
  "s_xor_b32 s97, %[pso], 0\n\t" \
  ".p2align 3\n\t" \
  "v_pk_fma_f32 v[176:177], v[176:177], v[128:129], v[136:137]\n\t" \
  "v_pk_fma_f32 v[178:179], v[178:179], v[130:131], v[138:139]\n\t" \
  "v_pk_fma_f32 v[180:181], v[180:181], v[132:133], v[140:141]\n\t" \
  "v_pk_fma_f32 v[182:183], v[182:183], v[134:135], v[142:143]\n\t" \
  "v_cvt_pk_bf16_f32 v0, v176, v177\n\t" \
  "v_cvt_pk_bf16_f32 v1, v178, v179\n\t" \
  "v_cvt_pk_bf16_f32 v2, v180, v181\n\t" \
  "v_cvt_pk_bf16_f32 v3, v182, v183\n\t" \
  "buffer_store_dwordx4 v[0:3], v214, %[ws], s97 offen sc1\n\t" \
  "v_lshlrev_b32 v176, 16, v4\n\t" \
  "v_and_b32 v177, s98, v4\n\t" \
  "v_lshlrev_b32 v178, 16, v5\n\t" \
  "v_and_b32 v179, s98, v5\n\t" \
  "v_lshlrev_b32 v180, 16, v6\n\t" \
  "v_and_b32 v181, s98, v6\n\t" \
  "v_lshlrev_b32 v182, 16, v7\n\t" \
  "v_and_b32 v183, s98, v7\n\t" \
  "v_pk_fma_f32 v[176:177], v[176:177], v[164:165], v[166:167] op_sel_hi:[1,0,0]\n\t" \
  "v_pk_fma_f32 v[178:179], v[178:179], v[164:165], v[166:167] op_sel_hi:[1,0,0]\n\t" \
  "v_pk_fma_f32 v[180:181], v[180:181], v[164:165], v[166:167] op_sel_hi:[1,0,0]\n\t" \
  "v_pk_fma_f32 v[182:183], v[182:183], v[164:165], v[166:167] op_sel_hi:[1,0,0]\n\t" \
  "s_xor_b32 s97, %[pso], 256\n\t" \
  ".p2align 3\n\t" \
  "v_pk_fma_f32 v[176:177], v[176:177], v[128:129], v[136:137]\n\t" \
  "v_pk_fma_f32 v[178:179], v[178:179], v[130:131], v[138:139]\n\t" \
  "v_pk_fma_f32 v[180:181], v[180:181], v[132:133], v[140:141]\n\t" \
  "v_pk_fma_f32 v[182:183], v[182:183], v[134:135], v[142:143]\n\t" \
  "v_cvt_pk_bf16_f32 v4, v176, v177\n\t" \
  "v_cvt_pk_bf16_f32 v5, v178, v179\n\t" \
  "v_cvt_pk_bf16_f32 v6, v180, v181\n\t" \
  "v_cvt_pk_bf16_f32 v7, v182, v183\n\t" \
  "buffer_store_dwordx4 v[4:7], v214, %[ws], s97 offen sc1\n\t" \
  "v_lshlrev_b32 v176, 16, v8\n\t" \
  "v_and_b32 v177, s98, v8\n\t" \
  "v_lshlrev_b32 v178, 16, v9\n\t" \
  "v_and_b32 v179, s98, v9\n\t" \
  "v_lshlrev_b32 v180, 16, v10\n\t" \
  "v_and_b32 v181, s98, v10\n\t" \
  "v_lshlrev_b32 v182, 16, v11\n\t" \
  "v_and_b32 v183, s98, v11\n\t" \
  "v_pk_fma_f32 v[176:177], v[176:177], v[168:169], v[170:171] op_sel_hi:[1,0,0]\n\t" \
  "v_pk_fma_f32 v[178:179], v[178:179], v[168:169], v[170:171] op_sel_hi:[1,0,0]\n\t" \
  "v_pk_fma_f32 v[180:181], v[180:181], v[168:169], v[170:171] op_sel_hi:[1,0,0]\n\t" \
  "v_pk_fma_f32 v[182:183], v[182:183], v[168:169], v[170:171] op_sel_hi:[1,0,0]\n\t" \
  "s_xor_b32 s97, %[pso], 512\n\t" \
  ".p2align 3\n\t" \
  "v_pk_fma_f32 v[176:177], v[176:177], v[128:129], v[136:137]\n\t" \
  "v_pk_fma_f32 v[178:179], v[178:179], v[130:131], v[138:139]\n\t" \
  "v_pk_fma_f32 v[180:181], v[180:181], v[132:133], v[140:141]\n\t" \
  "v_pk_fma_f32 v[182:183], v[182:183], v[134:135], v[142:143]\n\t" \
  "v_cvt_pk_bf16_f32 v8, v176, v177\n\t" \
  "v_cvt_pk_bf16_f32 v9, v178, v179\n\t" \
  "v_cvt_pk_bf16_f32 v10, v180, v181\n\t" \
  "v_cvt_pk_bf16_f32 v11, v182, v183\n\t" \
  "buffer_store_dwordx4 v[8:11], v214, %[ws], s97 offen sc1\n\t" \
  "v_lshlrev_b32 v176, 16, v12\n\t" \
  "v_and_b32 v177, s98, v12\n\t" \
  "v_lshlrev_b32 v178, 16, v13\n\t" \
  "v_and_b32 v179, s98, v13\n\t" \
  "v_lshlrev_b32 v180, 16, v14\n\t" \
  "v_and_b32 v181, s98, v14\n\t" \
  "v_lshlrev_b32 v182, 16, v15\n\t" \
  "v_and_b32 v183, s98, v15\n\t" \
  "v_pk_fma_f32 v[176:177], v[176:177], v[172:173], v[174:175] op_sel_hi:[1,0,0]\n\t" \
  "v_pk_fma_f32 v[178:179], v[178:179], v[172:173], v[174:175] op_sel_hi:[1,0,0]\n\t" \
  "v_pk_fma_f32 v[180:181], v[180:181], v[172:173], v[174:175] op_sel_hi:[1,0,0]\n\t" \
  "v_pk_fma_f32 v[182:183], v[182:183], v[172:173], v[174:175] op_sel_hi:[1,0,0]\n\t" \
  "s_xor_b32 s97, %[pso], 768\n\t" \
  ".p2align 3\n\t" \
  "v_pk_fma_f32 v[176:177], v[176:177], v[128:129], v[136:137]\n\t" \
  "v_pk_fma_f32 v[178:179], v[178:179], v[130:131], v[138:139]\n\t" \
  "v_pk_fma_f32 v[180:181], v[180:181], v[132:133], v[140:141]\n\t" \
  "v_pk_fma_f32 v[182:183], v[182:183], v[134:135], v[142:143]\n\t" \
  "v_cvt_pk_bf16_f32 v12, v176, v177\n\t" \
  "v_cvt_pk_bf16_f32 v13, v178, v179\n\t" \
  "v_cvt_pk_bf16_f32 v14, v180, v181\n\t" \
  "v_cvt_pk_bf16_f32 v15, v182, v183\n\t" \
  "buffer_store_dwordx4 v[12:15], v214, %[ws], s97 offen sc1\n\t" \
  "ds_read_b128 v[128:131], v208 offset:512\n\t" \
  "ds_read_b128 v[132:135], v208 offset:528\n\t" \
  "ds_read_b128 v[136:139], v208 offset:768\n\t" \
  "ds_read_b128 v[140:143], v208 offset:784\n\t" \
  FSTP("s_memrealtime s[94:95]\n\t") \
  FSTP("s_memtime s[92:93]\n\t") \
  "v_lshlrev_b32 v176, 16, v16\n\t" \
  "v_and_b32 v177, s98, v16\n\t" \
  "v_lshlrev_b32 v178, 16, v17\n\t" \
  "v_and_b32 v179, s98, v17\n\t" \
  "v_lshlrev_b32 v180, 16, v18\n\t" \
  "v_and_b32 v181, s98, v18\n\t" \
  "v_lshlrev_b32 v182, 16, v19\n\t" \
  "v_and_b32 v183, s98, v19\n\t" \
  "v_pk_fma_f32 v[176:177], v[176:177], v[160:161], v[162:163] op_sel_hi:[1,0,0]\n\t" \
  "v_pk_fma_f32 v[178:179], v[178:179], v[160:161], v[162:163] op_sel_hi:[1,0,0]\n\t" \
  "v_pk_fma_f32 v[180:181], v[180:181], v[160:161], v[162:163] op_sel_hi:[1,0,0]\n\t" \
  "v_pk_fma_f32 v[182:183], v[182:183], v[160:161], v[162:163] op_sel_hi:[1,0,0]\n\t" \
  "s_waitcnt lgkmcnt(8)\n\t" \
  "s_nop 0\n\t" \
  "v_pk_fma_f32 v[176:177], v[176:177], v[144:145], v[152:153]\n\t" \
  "v_pk_fma_f32 v[178:179], v[178:179], v[146:147], v[154:155]\n\t" \
  "v_pk_fma_f32 v[180:181], v[180:181], v[148:149], v[156:157]\n\t" \
  "v_pk_fma_f32 v[182:183], v[182:183], v[150:151], v[158:159]\n\t" \
  "v_cvt_pk_bf16_f32 v16, v176, v177\n\t" \
  "v_cvt_pk_bf16_f32 v17, v178, v179\n\t" \
  "v_cvt_pk_bf16_f32 v18, v180, v181\n\t" \
  "v_cvt_pk_bf16_f32 v19, v182, v183\n\t" \
  "s_add_u32 m0, %[mx], 98304\n\t" \
  "s_xor_b32 s96, %[c17], 0\n\t" \
  ".p2align 3\n\t" \
  "s_add_u32 s96, s96, %[sl2]\n\t" \
  "buffer_load_dwordx4 v212, %[sx], s96 offen lds\n\t" \
  ".p2align 3\n\t" \
  "v_lshlrev_b32 v176, 16, v20\n\t" \
  "v_and_b32 v177, s98, v20\n\t" \
  "v_lshlrev_b32 v178, 16, v21\n\t" \
  "v_and_b32 v179, s98, v21\n\t" \
  "v_lshlrev_b32 v180, 16, v22\n\t" \
  "v_and_b32 v181, s98, v22\n\t" \
  "v_lshlrev_b32 v182, 16, v23\n\t" \
  "v_and_b32 v183, s98, v23\n\t" \
  "v_pk_fma_f32 v[176:177], v[176:177], v[164:165], v[166:167] op_sel_hi:[1,0,0]\n\t" \
  "v_pk_fma_f32 v[178:179], v[178:179], v[164:165], v[166:167] op_sel_hi:[1,0,0]\n\t" \
  "v_pk_fma_f32 v[180:181], v[180:181], v[164:165], v[166:167] op_sel_hi:[1,0,0]\n\t" \
  "v_pk_fma_f32 v[182:183], v[182:183], v[164:165], v[166:167] op_sel_hi:[1,0,0]\n\t" \
  "v_pk_fma_f32 v[176:177], v[176:177], v[144:145], v[152:153]\n\t" \
  "v_pk_fma_f32 v[178:179], v[178:179], v[146:147], v[154:155]\n\t" \
  "v_pk_fma_f32 v[180:181], v[180:181], v[148:149], v[156:157]\n\t" \
  "v_pk_fma_f32 v[182:183], v[182:183], v[150:151], v[158:159]\n\t" \
  "v_cvt_pk_bf16_f32 v20, v176, v177\n\t" \
  "v_cvt_pk_bf16_f32 v21, v178, v179\n\t" \
  "v_cvt_pk_bf16_f32 v22, v180, v181\n\t" \
  "v_cvt_pk_bf16_f32 v23, v182, v183\n\t" \
  "buffer_load_dwordx4 v213, %[sx], s96 offen offset:1024 lds\n\t" \
  ".p2align 3\n\t" \
  "v_lshlrev_b32 v176, 16, v24\n\t" \
  "v_and_b32 v177, s98, v24\n\t" \
  "v_lshlrev_b32 v178, 16, v25\n\t" \
  "v_and_b32 v179, s98, v25\n\t" \
  "v_lshlrev_b32 v180, 16, v26\n\t" \
  "v_and_b32 v181, s98, v26\n\t" \
  "v_lshlrev_b32 v182, 16, v27\n\t" \
  "v_and_b32 v183, s98, v27\n\t" \
  "v_pk_fma_f32 v[176:177], v[176:177], v[168:169], v[170:171] op_sel_hi:[1,0,0]\n\t" \
  "v_pk_fma_f32 v[178:179], v[178:179], v[168:169], v[170:171] op_sel_hi:[1,0,0]\n\t" \
  "v_pk_fma_f32 v[180:181], v[180:181], v[168:169], v[170:171] op_sel_hi:[1,0,0]\n\t" \
  "v_pk_fma_f32 v[182:183], v[182:183], v[168:169], v[170:171] op_sel_hi:[1,0,0]\n\t" \
  "v_pk_fma_f32 v[176:177], v[176:177], v[144:145], v[152:153]\n\t" \
  "v_pk_fma_f32 v[178:179], v[178:179], v[146:147], v[154:155]\n\t" \
  "v_pk_fma_f32 v[180:181], v[180:181], v[148:149], v[156:157]\n\t" \
  "v_pk_fma_f32 v[182:183], v[182:183], v[150:151], v[158:159]\n\t" \
  "v_cvt_pk_bf16_f32 v24, v176, v177\n\t" \
  "v_cvt_pk_bf16_f32 v25, v178, v179\n\t" \
  "v_cvt_pk_bf16_f32 v26, v180, v181\n\t" \
  "v_cvt_pk_bf16_f32 v27, v182, v183\n\t" \
  "s_add_u32 m0, %[mx], 100352\n\t" \
  "s_xor_b32 s96, %[c17], 131072\n\t" \
  ".p2align 3\n\t" \
  "s_add_u32 s96, s96, %[sl2]\n\t" \
  "buffer_load_dwordx4 v212, %[sx], s96 offen lds\n\t" \
  ".p2align 3\n\t" \
  "v_lshlrev_b32 v176, 16, v28\n\t" \
  "v_and_b32 v177, s98, v28\n\t" \
  "v_lshlrev_b32 v178, 16, v29\n\t" \
  "v_and_b32 v179, s98, v29\n\t" \
  "v_lshlrev_b32 v180, 16, v30\n\t" \
  "v_and_b32 v181, s98, v30\n\t" \
  "v_lshlrev_b32 v182, 16, v31\n\t" \
  "v_and_b32 v183, s98, v31\n\t" \
  "v_pk_fma_f32 v[176:177], v[176:177], v[172:173], v[174:175] op_sel_hi:[1,0,0]\n\t" \
  "v_pk_fma_f32 v[178:179], v[178:179], v[172:173], v[174:175] op_sel_hi:[1,0,0]\n\t" \
  "v_pk_fma_f32 v[180:181], v[180:181], v[172:173], v[174:175] op_sel_hi:[1,0,0]\n\t" \
  "v_pk_fma_f32 v[182:183], v[182:183], v[172:173], v[174:175] op_sel_hi:[1,0,0]\n\t" \
  "v_pk_fma_f32 v[176:177], v[176:177], v[144:145], v[152:153]\n\t" \
  "v_pk_fma_f32 v[178:179], v[178:179], v[146:147], v[154:155]\n\t" \
  "v_pk_fma_f32 v[180:181], v[180:181], v[148:149], v[156:157]\n\t" \
  "v_pk_fma_f32 v[182:183], v[182:183], v[150:151], v[158:159]\n\t" \
  "v_cvt_pk_bf16_f32 v28, v176, v177\n\t" \
  "v_cvt_pk_bf16_f32 v29, v178, v179\n\t" \
  "v_cvt_pk_bf16_f32 v30, v180, v181\n\t" \
  "v_cvt_pk_bf16_f32 v31, v182, v183\n\t" \
  "buffer_load_dwordx4 v213, %[sx], s96 offen offset:1024 lds\n\t" \
  ".p2align 3\n\t" \
  "ds_read_b128 v[144:147], v208 offset:640\n\t" \
  "ds_read_b128 v[148:151], v208 offset:656\n\t" \
  "ds_read_b128 v[152:155], v208 offset:896\n\t" \
  "ds_read_b128 v[156:159], v208 offset:912\n\t" \
  FSTP("v_writelane_b32 %[stv], s94, 28\n\t") \
  FSTP("v_writelane_b32 %[stv], s92, 29\n\t") \
  FSTP("s_memrealtime s[94:95]\n\t") \
  FSTP("s_memtime s[92:93]\n\t") \
  "v_lshlrev_b32 v176, 16, v32\n\t" \
  "v_and_b32 v177, s98, v32\n\t" \
  "v_lshlrev_b32 v178, 16, v33\n\t" \
  "v_and_b32 v179, s98, v33\n\t" \
  "v_lshlrev_b32 v180, 16, v34\n\t" \
  "v_and_b32 v181, s98, v34\n\t" \
  "v_lshlrev_b32 v182, 16, v35\n\t" \
  "v_and_b32 v183, s98, v35\n\t" \
  "v_pk_fma_f32 v[176:177], v[176:177], v[160:161], v[162:163] op_sel_hi:[1,0,0]\n\t" \
  "v_pk_fma_f32 v[178:179], v[178:179], v[160:161], v[162:163] op_sel_hi:[1,0,0]\n\t" \
  "v_pk_fma_f32 v[180:181], v[180:181], v[160:161], v[162:163] op_sel_hi:[1,0,0]\n\t" \
  "v_pk_fma_f32 v[182:183], v[182:183], v[160:161], v[162:163] op_sel_hi:[1,0,0]\n\t" \
  "s_waitcnt lgkmcnt(4)\n\t" \
  "s_nop 0\n\t" \
  "v_pk_fma_f32 v[176:177], v[176:177], v[128:129], v[136:137]\n\t" \
  "v_pk_fma_f32 v[178:179], v[178:179], v[130:131], v[138:139]\n\t" \
  "v_pk_fma_f32 v[180:181], v[180:181], v[132:133], v[140:141]\n\t" \
  "v_pk_fma_f32 v[182:183], v[182:183], v[134:135], v[142:143]\n\t" \
  "v_cvt_pk_bf16_f32 v32, v176, v177\n\t" \
  "v_cvt_pk_bf16_f32 v33, v178, v179\n\t" \
  "v_cvt_pk_bf16_f32 v34, v180, v181\n\t" \
  "v_cvt_pk_bf16_f32 v35, v182, v183\n\t" \
  "s_add_u32 m0, %[mx], 102400\n\t" \
  "s_xor_b32 s96, %[c17], 262144\n\t" \
  ".p2align 3\n\t" \
  "s_add_u32 s96, s96, %[sl2]\n\t" \
  "buffer_load_dwordx4 v212, %[sx], s96 offen lds\n\t" \
  ".p2align 3\n\t" \
  "v_lshlrev_b32 v176, 16, v36\n\t" \
  "v_and_b32 v177, s98, v36\n\t" \
  "v_lshlrev_b32 v178, 16, v37\n\t" \
  "v_and_b32 v179, s98, v37\n\t" \
  "v_lshlrev_b32 v180, 16, v38\n\t" \
  "v_and_b32 v181, s98, v38\n\t" \
  "v_lshlrev_b32 v182, 16, v39\n\t" \
  "v_and_b32 v183, s98, v39\n\t" \
  "v_pk_fma_f32 v[176:177], v[176:177], v[164:165], v[166:167] op_sel_hi:[1,0,0]\n\t" \
  "v_pk_fma_f32 v[178:179], v[178:179], v[164:165], v[166:167] op_sel_hi:[1,0,0]\n\t" \
  "v_pk_fma_f32 v[180:181], v[180:181], v[164:165], v[166:167] op_sel_hi:[1,0,0]\n\t" \
  "v_pk_fma_f32 v[182:183], v[182:183], v[164:165], v[166:167] op_sel_hi:[1,0,0]\n\t" \
  "v_pk_fma_f32 v[176:177], v[176:177], v[128:129], v[136:137]\n\t" \
  "v_pk_fma_f32 v[178:179], v[178:179], v[130:131], v[138:139]\n\t" \
  "v_pk_fma_f32 v[180:181], v[180:181], v[132:133], v[140:141]\n\t" \
  "v_pk_fma_f32 v[182:183], v[182:183], v[134:135], v[142:143]\n\t" \
  "v_cvt_pk_bf16_f32 v36, v176, v177\n\t" \
  "v_cvt_pk_bf16_f32 v37, v178, v179\n\t" \
  "v_cvt_pk_bf16_f32 v38, v180, v181\n\t" \
  "v_cvt_pk_bf16_f32 v39, v182, v183\n\t" \
  "buffer_load_dwordx4 v213, %[sx], s96 offen offset:1024 lds\n\t" \
  ".p2align 3\n\t" \
  "v_lshlrev_b32 v176, 16, v40\n\t" \
  "v_and_b32 v177, s98, v40\n\t" \
  "v_lshlrev_b32 v178, 16, v41\n\t" \
  "v_and_b32 v179, s98, v41\n\t" \
  "v_lshlrev_b32 v180, 16, v42\n\t" \
  "v_and_b32 v181, s98, v42\n\t" \
  "v_lshlrev_b32 v182, 16, v43\n\t" \
  "v_and_b32 v183, s98, v43\n\t" \
  "v_pk_fma_f32 v[176:177], v[176:177], v[168:169], v[170:171] op_sel_hi:[1,0,0]\n\t" \
  "v_pk_fma_f32 v[178:179], v[178:179], v[168:169], v[170:171] op_sel_hi:[1,0,0]\n\t" \
  "v_pk_fma_f32 v[180:181], v[180:181], v[168:169], v[170:171] op_sel_hi:[1,0,0]\n\t" \
  "v_pk_fma_f32 v[182:183], v[182:183], v[168:169], v[170:171] op_sel_hi:[1,0,0]\n\t" \
  "v_pk_fma_f32 v[176:177], v[176:177], v[128:129], v[136:137]\n\t" \
  "v_pk_fma_f32 v[178:179], v[178:179], v[130:131], v[138:139]\n\t" \
  "v_pk_fma_f32 v[180:181], v[180:181], v[132:133], v[140:141]\n\t" \
  "v_pk_fma_f32 v[182:183], v[182:183], v[134:135], v[142:143]\n\t" \
  "v_cvt_pk_bf16_f32 v40, v176, v177\n\t" \
  "v_cvt_pk_bf16_f32 v41, v178, v179\n\t" \
  "v_cvt_pk_bf16_f32 v42, v180, v181\n\t" \
  "v_cvt_pk_bf16_f32 v43, v182, v183\n\t" \
  "s_add_u32 m0, %[mx], 104448\n\t" \
  "s_xor_b32 s96, %[c17], 393216\n\t" \
  ".p2align 3\n\t" \
  "s_add_u32 s96, s96, %[sl2]\n\t" \
  "buffer_load_dwordx4 v212, %[sx], s96 offen lds\n\t" \
  ".p2align 3\n\t" \
  "v_lshlrev_b32 v176, 16, v44\n\t" \
  "v_and_b32 v177, s98, v44\n\t" \
  "v_lshlrev_b32 v178, 16, v45\n\t" \
  "v_and_b32 v179, s98, v45\n\t" \
  "v_lshlrev_b32 v180, 16, v46\n\t" \
  "v_and_b32 v181, s98, v46\n\t" \
  "v_lshlrev_b32 v182, 16, v47\n\t" \
  "v_and_b32 v183, s98, v47\n\t" \
  "v_pk_fma_f32 v[176:177], v[176:177], v[172:173], v[174:175] op_sel_hi:[1,0,0]\n\t" \
  "v_pk_fma_f32 v[178:179], v[178:179], v[172:173], v[174:175] op_sel_hi:[1,0,0]\n\t" \
  "v_pk_fma_f32 v[180:181], v[180:181], v[172:173], v[174:175] op_sel_hi:[1,0,0]\n\t" \
  "v_pk_fma_f32 v[182:183], v[182:183], v[172:173], v[174:175] op_sel_hi:[1,0,0]\n\t" \
  "v_pk_fma_f32 v[176:177], v[176:177], v[128:129], v[136:137]\n\t" \
  "v_pk_fma_f32 v[178:179], v[178:179], v[130:131], v[138:139]\n\t" \
  "v_pk_fma_f32 v[180:181], v[180:181], v[132:133], v[140:141]\n\t" \
  "v_pk_fma_f32 v[182:183], v[182:183], v[134:135], v[142:143]\n\t" \
  "v_cvt_pk_bf16_f32 v44, v176, v177\n\t" \
  "v_cvt_pk_bf16_f32 v45, v178, v179\n\t" \
  "v_cvt_pk_bf16_f32 v46, v180, v181\n\t" \
  "v_cvt_pk_bf16_f32 v47, v182, v183\n\t" \
  "buffer_load_dwordx4 v213, %[sx], s96 offen offset:1024 lds\n\t" \
  ".p2align 3\n\t" \
  "ds_read_b128 v[128:131], v208 offset:1024\n\t" \
  "ds_read_b128 v[132:135], v208 offset:1040\n\t" \
  "ds_read_b128 v[136:139], v208 offset:1280\n\t" \
  "ds_read_b128 v[140:143], v208 offset:1296\n\t" \
  FSTP("v_writelane_b32 %[stv], s94, 30\n\t") \
  FSTP("v_writelane_b32 %[stv], s92, 31\n\t") \
  FSTP("s_memrealtime s[94:95]\n\t") \
  FSTP("s_memtime s[92:93]\n\t") \
  "v_lshlrev_b32 v176, 16, v48\n\t" \
  "v_and_b32 v177, s98, v48\n\t" \
  "v_lshlrev_b32 v178, 16, v49\n\t" \
  "v_and_b32 v179, s98, v49\n\t" \
  "v_lshlrev_b32 v180, 16, v50\n\t" \
  "v_and_b32 v181, s98, v50\n\t" \
  "v_lshlrev_b32 v182, 16, v51\n\t" \
  "v_and_b32 v183, s98, v51\n\t" \
  "v_pk_fma_f32 v[176:177], v[176:177], v[160:161], v[162:163] op_sel_hi:[1,0,0]\n\t" \
  "v_pk_fma_f32 v[178:179], v[178:179], v[160:161], v[162:163] op_sel_hi:[1,0,0]\n\t" \
  "v_pk_fma_f32 v[180:181], v[180:181], v[160:161], v[162:163] op_sel_hi:[1,0,0]\n\t" \
  "v_pk_fma_f32 v[182:183], v[182:183], v[160:161], v[162:163] op_sel_hi:[1,0,0]\n\t" \
  "s_waitcnt lgkmcnt(4)\n\t" \
  "s_nop 0\n\t" \
  "v_pk_fma_f32 v[176:177], v[176:177], v[144:145], v[152:153]\n\t" \
  "v_pk_fma_f32 v[178:179], v[178:179], v[146:147], v[154:155]\n\t" \
  "v_pk_fma_f32 v[180:181], v[180:181], v[148:149], v[156:157]\n\t" \
  "v_pk_fma_f32 v[182:183], v[182:183], v[150:151], v[158:159]\n\t" \
  "v_cvt_pk_bf16_f32 v48, v176, v177\n\t" \
  "v_cvt_pk_bf16_f32 v49, v178, v179\n\t" \
  "v_cvt_pk_bf16_f32 v50, v180, v181\n\t" \
  "v_cvt_pk_bf16_f32 v51, v182, v183\n\t" \
  "s_add_u32 m0, %[mw], 49152\n\t" \
  "s_mov_b32 s96, %[w1]\n\t" \
  ".p2align 3\n\t" \
  "buffer_load_dwordx4 v200, %[sw], s96 offen offset:0 lds\n\t" \
  ".p2align 3\n\t" \
  "ds_read_b64_tr_b16 a[128:129], v184 offset:0\n\t" \
  "ds_read_b64_tr_b16 a[130:131], v184 offset:1024\n\t" \
  "ds_read_b64_tr_b16 a[132:133], v185 offset:0\n\t" \
  "ds_read_b64_tr_b16 a[134:135], v185 offset:1024\n\t" \
  "v_lshlrev_b32 v176, 16, v52\n\t" \
  "v_and_b32 v177, s98, v52\n\t" \
  "v_lshlrev_b32 v178, 16, v53\n\t" \
  "v_and_b32 v179, s98, v53\n\t" \
  "v_lshlrev_b32 v180, 16, v54\n\t" \
  "v_and_b32 v181, s98, v54\n\t" \
  "v_lshlrev_b32 v182, 16, v55\n\t" \
  "v_and_b32 v183, s98, v55\n\t" \
  "v_pk_fma_f32 v[176:177], v[176:177], v[164:165], v[166:167] op_sel_hi:[1,0,0]\n\t" \
  "v_pk_fma_f32 v[178:179], v[178:179], v[164:165], v[166:167] op_sel_hi:[1,0,0]\n\t" \
  "v_pk_fma_f32 v[180:181], v[180:181], v[164:165], v[166:167] op_sel_hi:[1,0,0]\n\t" \
  "v_pk_fma_f32 v[182:183], v[182:183], v[164:165], v[166:167] op_sel_hi:[1,0,0]\n\t" \
  "v_pk_fma_f32 v[176:177], v[176:177], v[144:145], v[152:153]\n\t" \
  "v_pk_fma_f32 v[178:179], v[178:179], v[146:147], v[154:155]\n\t" \
  "v_pk_fma_f32 v[180:181], v[180:181], v[148:149], v[156:157]\n\t" \
  "v_pk_fma_f32 v[182:183], v[182:183], v[150:151], v[158:159]\n\t" \
  "v_cvt_pk_bf16_f32 v52, v176, v177\n\t" \
  "v_cvt_pk_bf16_f32 v53, v178, v179\n\t" \
  "v_cvt_pk_bf16_f32 v54, v180, v181\n\t" \
  "v_cvt_pk_bf16_f32 v55, v182, v183\n\t" \
  "buffer_load_dwordx4 v201, %[sw], s96 offen offset:1024 lds\n\t" \
  ".p2align 3\n\t" \
  "ds_read_b64_tr_b16 a[136:137], v186 offset:0\n\t" \
  "ds_read_b64_tr_b16 a[138:139], v186 offset:1024\n\t" \
  "ds_read_b64_tr_b16 a[140:141], v187 offset:0\n\t" \
  "ds_read_b64_tr_b16 a[142:143], v187 offset:1024\n\t" \
  "v_lshlrev_b32 v176, 16, v56\n\t" \
  "v_and_b32 v177, s98, v56\n\t" \
  "v_lshlrev_b32 v178, 16, v57\n\t" \
  "v_and_b32 v179, s98, v57\n\t" \
  "v_lshlrev_b32 v180, 16, v58\n\t" \
  "v_and_b32 v181, s98, v58\n\t" \
  "v_lshlrev_b32 v182, 16, v59\n\t" \
  "v_and_b32 v183, s98, v59\n\t" \
  "v_pk_fma_f32 v[176:177], v[176:177], v[168:169], v[170:171] op_sel_hi:[1,0,0]\n\t" \
  "v_pk_fma_f32 v[178:179], v[178:179], v[168:169], v[170:171] op_sel_hi:[1,0,0]\n\t" \
  "v_pk_fma_f32 v[180:181], v[180:181], v[168:169], v[170:171] op_sel_hi:[1,0,0]\n\t" \
  "v_pk_fma_f32 v[182:183], v[182:183], v[168:169], v[170:171] op_sel_hi:[1,0,0]\n\t" \
  "v_pk_fma_f32 v[176:177], v[176:177], v[144:145], v[152:153]\n\t" \
  "v_pk_fma_f32 v[178:179], v[178:179], v[146:147], v[154:155]\n\t" \
  "v_pk_fma_f32 v[180:181], v[180:181], v[148:149], v[156:157]\n\t" \
  "v_pk_fma_f32 v[182:183], v[182:183], v[150:151], v[158:159]\n\t" \
  "v_cvt_pk_bf16_f32 v56, v176, v177\n\t" \
  "v_cvt_pk_bf16_f32 v57, v178, v179\n\t" \
  "v_cvt_pk_bf16_f32 v58, v180, v181\n\t" \
  "v_cvt_pk_bf16_f32 v59, v182, v183\n\t" \
  "buffer_load_dwordx4 v202, %[sw], s96 offen offset:2048 lds\n\t" \
  ".p2align 3\n\t" \
  "ds_read_b64_tr_b16 a[144:145], v188 offset:0\n\t" \
  "ds_read_b64_tr_b16 a[146:147], v188 offset:1024\n\t" \
  "ds_read_b64_tr_b16 a[148:149], v189 offset:0\n\t" \
  "ds_read_b64_tr_b16 a[150:151], v189 offset:1024\n\t" \
  "v_lshlrev_b32 v176, 16, v60\n\t" \
  "v_and_b32 v177, s98, v60\n\t" \
  "v_lshlrev_b32 v178, 16, v61\n\t" \
  "v_and_b32 v179, s98, v61\n\t" \
  "v_lshlrev_b32 v180, 16, v62\n\t" \
  "v_and_b32 v181, s98, v62\n\t" \
  "v_lshlrev_b32 v182, 16, v63\n\t" \
  "v_and_b32 v183, s98, v63\n\t" \
  "v_pk_fma_f32 v[176:177], v[176:177], v[172:173], v[174:175] op_sel_hi:[1,0,0]\n\t" \
  "v_pk_fma_f32 v[178:179], v[178:179], v[172:173], v[174:175] op_sel_hi:[1,0,0]\n\t" \
  "v_pk_fma_f32 v[180:181], v[180:181], v[172:173], v[174:175] op_sel_hi:[1,0,0]\n\t" \
  "v_pk_fma_f32 v[182:183], v[182:183], v[172:173], v[174:175] op_sel_hi:[1,0,0]\n\t" \
  "v_pk_fma_f32 v[176:177], v[176:177], v[144:145], v[152:153]\n\t" \
  "v_pk_fma_f32 v[178:179], v[178:179], v[146:147], v[154:155]\n\t" \
  "v_pk_fma_f32 v[180:181], v[180:181], v[148:149], v[156:157]\n\t" \
  "v_pk_fma_f32 v[182:183], v[182:183], v[150:151], v[158:159]\n\t" \
  "v_cvt_pk_bf16_f32 v60, v176, v177\n\t" \
  "v_cvt_pk_bf16_f32 v61, v178, v179\n\t" \
  "v_cvt_pk_bf16_f32 v62, v180, v181\n\t" \
  "v_cvt_pk_bf16_f32 v63, v182, v183\n\t" \
  "buffer_load_dwordx4 v203, %[sw], s96 offen offset:3072 lds\n\t" \
  ".p2align 3\n\t" \
  "ds_read_b64_tr_b16 a[152:153], v190 offset:0\n\t" \
  "ds_read_b64_tr_b16 a[154:155], v190 offset:1024\n\t" \
  "ds_read_b64_tr_b16 a[156:157], v191 offset:0\n\t" \
  "ds_read_b64_tr_b16 a[158:159], v191 offset:1024\n\t" \
  "ds_read_b128 v[144:147], v208 offset:1152\n\t" \
  "ds_read_b128 v[148:151], v208 offset:1168\n\t" \
  "ds_read_b128 v[152:155], v208 offset:1408\n\t" \
  "ds_read_b128 v[156:159], v208 offset:1424\n\t" \
  FSTP("v_writelane_b32 %[stv], s94, 32\n\t") \
  FSTP("v_writelane_b32 %[stv], s92, 33\n\t") \
  FSTP("s_memrealtime s[94:95]\n\t") \
  FSTP("s_memtime s[92:93]\n\t") \
  "s_waitcnt vmcnt(12)\n\t" \
  "s_nop 0\n\t" \
  "s_mov_b64 exec, 1\n\t" \
  "buffer_store_dword v211, off, %[ws], %[fw] sc1\n\t" \
  "s_mov_b64 exec, -1\n\t" \
  FSTP("v_writelane_b32 %[stv], s94, 34\n\t") \
  FSTP("v_writelane_b32 %[stv], s92, 35\n\t") \
  FSTP("s_memrealtime s[94:95]\n\t") \
  FSTP("s_memtime s[92:93]\n\t") \
  "s_waitcnt lgkmcnt(4)\n\t" \
  "v_mfma_f32_16x16x32_bf16 a[0:3], a[128:131], v[0:3], 0\n\t" \
  ".Lhs0:\n\ts_cbranch_execz .Lhs0n\n\t" \
  "s_add_u32 m0, %[mw], 98304\n\t" \
  "ds_read_b64_tr_b16 a[160:161], v184 offset:8192\n\t" \
  "v_mfma_f32_16x16x32_bf16 a[4:7], a[128:131], v[4:7], 0\n\t" \
  "s_mov_b32 s96, %[w2]\n\t" \
  "ds_read_b64_tr_b16 a[162:163], v184 offset:9216\n\t" \
  "v_mfma_f32_16x16x32_bf16 a[8:11], a[128:131], v[8:11], 0\n\t" \
  "buffer_load_dwordx4 v200, %[sw], s96 offen offset:0 lds\n\t" \
  "v_mfma_f32_16x16x32_bf16 a[12:15], a[128:131], v[12:15], 0\n\t" \
  "buffer_load_dwordx4 v201, %[sw], s96 offen offset:1024 lds\n\t" \
  "v_mfma_f32_16x16x32_bf16 a[28:31], a[132:135], v[12:15], 0\n\t" \
  "ds_read_b64_tr_b16 a[164:165], v185 offset:8192\n\t" \
  "v_mfma_f32_16x16x32_bf16 a[24:27], a[132:135], v[8:11], 0\n\t" \
  "ds_read_b64_tr_b16 a[166:167], v185 offset:9216\n\t" \
  "v_mfma_f32_16x16x32_bf16 a[20:23], a[132:135], v[4:7], 0\n\t" \
  "buffer_load_dwordx4 v202, %[sw], s96 offen offset:2048 lds\n\t" \
  "v_mfma_f32_16x16x32_bf16 a[16:19], a[132:135], v[0:3], 0\n\t" \
  "ds_read_b64_tr_b16 a[168:169], v186 offset:8192\n\t" \
  "v_mfma_f32_16x16x32_bf16 a[32:35], a[136:139], v[0:3], 0\n\t" \
  "s_waitcnt vmcnt(8)\n\t" \
  "ds_read_b64_tr_b16 a[170:171], v186 offset:9216\n\t" \
  "ds_read_b128 v[64:67], v206 offset:0\n\t" \
  "v_mfma_f32_16x16x32_bf16 a[36:39], a[136:139], v[4:7], 0\n\t" \
  "buffer_load_dwordx4 v203, %[sw], s96 offen offset:3072 lds\n\t" \
  "v_mfma_f32_16x16x32_bf16 a[40:43], a[136:139], v[8:11], 0\n\t" \
  "s_add_u32 m0, %[mx], 0\n\t" \
  "ds_read_b64_tr_b16 a[172:173], v187 offset:8192\n\t" \
  "ds_read_b128 v[68:71], v206 offset:2048\n\t" \
  "v_mfma_f32_16x16x32_bf16 a[44:47], a[136:139], v[12:15], 0\n\t" \
  "s_xor_b32 s96, %[c17], 0\n\t" \
  "ds_read_b64_tr_b16 a[174:175], v187 offset:9216\n\t" \
  "ds_read_b128 v[72:75], v206 offset:4096\n\t" \
  "v_mfma_f32_16x16x32_bf16 a[60:63], a[140:143], v[12:15], 0\n\t" \
  "s_add_u32 s96, s96, %[sl3]\n\t" \
  "ds_read_b64_tr_b16 a[176:177], v188 offset:8192\n\t" \
  "ds_read_b128 v[76:79], v206 offset:6144\n\t" \
  "s_waitcnt lgkmcnt(0)\n\t" \
  "v_lshlrev_b32 v176, 16, v64\n\t" \
  "v_and_b32 v177, s98, v64\n\t" \
  "v_mfma_f32_16x16x32_bf16 a[56:59], a[140:143], v[8:11], 0\n\t" \
  "buffer_load_dwordx4 v212, %[sx], s96 offen lds\n\t" \
  "v_lshlrev_b32 v178, 16, v65\n\t" \
  "v_and_b32 v179, s98, v65\n\t" \
  "v_lshlrev_b32 v180, 16, v66\n\t" \
  "v_mfma_f32_16x16x32_bf16 a[52:55], a[140:143], v[4:7], 0\n\t" \
  "ds_read_b64_tr_b16 a[178:179], v188 offset:9216\n\t" \
  "v_and_b32 v181, s98, v66\n\t" \
  "v_lshlrev_b32 v182, 16, v67\n\t" \
  "v_and_b32 v183, s98, v67\n\t" \
  "v_fma_f32 v176, v176, v160, v162\n\t" \
  "v_mfma_f32_16x16x32_bf16 a[48:51], a[140:143], v[0:3], 0\n\t" \
  "buffer_load_dwordx4 v213, %[sx], s96 offen offset:1024 lds\n\t" \
  "v_fma_f32 v177, v177, v160, v162\n\t" \
  "v_fma_f32 v178, v178, v160, v162\n\t" \
  "v_fma_f32 v179, v179, v160, v162\n\t" \
  "v_mfma_f32_16x16x32_bf16 a[64:67], a[144:147], v[0:3], 0\n\t" \
  "s_add_u32 m0, %[mx], 2048\n\t" \
  "ds_read_b64_tr_b16 a[180:181], v189 offset:8192\n\t" \
  "v_fma_f32 v180, v180, v160, v162\n\t" \
  "v_fma_f32 v181, v181, v160, v162\n\t" \
  "v_fma_f32 v182, v182, v160, v162\n\t" \
  "v_fma_f32 v183, v183, v160, v162\n\t" \
  "v_mfma_f32_16x16x32_bf16 a[68:71], a[144:147], v[4:7], 0\n\t" \
  "s_xor_b32 s96, %[c17], 131072\n\t" \
  "ds_read_b64_tr_b16 a[182:183], v189 offset:9216\n\t" \
  "v_fma_f32 v176, v176, v128, v136\n\t" \
  "v_fma_f32 v177, v177, v129, v137\n\t" \
  "v_fma_f32 v178, v178, v130, v138\n\t" \
  "v_fma_f32 v179, v179, v131, v139\n\t" \
  "v_mfma_f32_16x16x32_bf16 a[72:75], a[144:147], v[8:11], 0\n\t" \
  "s_add_u32 s96, s96, %[sl3]\n\t" \
  "buffer_load_dwordx4 v212, %[sx], s96 offen lds\n\t" \
  "v_fma_f32 v180, v180, v132, v140\n\t" \
  "v_fma_f32 v181, v181, v133, v141\n\t" \
  "v_mfma_f32_16x16x32_bf16 a[76:79], a[144:147], v[12:15], 0\n\t" \
  "ds_read_b64_tr_b16 a[184:185], v190 offset:8192\n\t" \
  "v_fma_f32 v182, v182, v134, v142\n\t" \
  "v_fma_f32 v183, v183, v135, v143\n\t" \
  "v_cvt_pk_bf16_f32 v64, v176, v177\n\t" \
  "v_cvt_pk_bf16_f32 v65, v178, v179\n\t" \
  "v_mfma_f32_16x16x32_bf16 a[92:95], a[148:151], v[12:15], 0\n\t" \
  "buffer_load_dwordx4 v213, %[sx], s96 offen offset:1024 lds\n\t" \
  "v_cvt_pk_bf16_f32 v66, v180, v181\n\t" \
  "v_cvt_pk_bf16_f32 v67, v182, v183\n\t" \
  "v_lshlrev_b32 v176, 16, v68\n\t" \
  "v_mfma_f32_16x16x32_bf16 a[88:91], a[148:151], v[8:11], 0\n\t" \
  "s_add_u32 m0, %[mx], 4096\n\t" \
  "ds_read_b64_tr_b16 a[186:187], v190 offset:9216\n\t" \
  "v_and_b32 v177, s98, v68\n\t" \
  "v_lshlrev_b32 v178, 16, v69\n\t" \
  "v_and_b32 v179, s98, v69\n\t" \
  "v_lshlrev_b32 v180, 16, v70\n\t" \
  "v_mfma_f32_16x16x32_bf16 a[84:87], a[148:151], v[4:7], 0\n\t" \
  "s_xor_b32 s96, %[c17], 262144\n\t" \
  "s_add_u32 s96, s96, %[sl3]\n\t" \
  "buffer_load_dwordx4 v212, %[sx], s96 offen lds\n\t" \
  "v_and_b32 v181, s98, v70\n\t" \
  "v_mfma_f32_16x16x32_bf16 a[80:83], a[148:151], v[0:3], 0\n\t" \
  "ds_read_b64_tr_b16 a[188:189], v191 offset:8192\n\t" \
  "v_lshlrev_b32 v182, 16, v71\n\t" \
  "v_and_b32 v183, s98, v71\n\t" \
  "v_fma_f32 v176, v176, v164, v166\n\t" \
  "v_fma_f32 v177, v177, v164, v166\n\t" \
  "v_mfma_f32_16x16x32_bf16 a[96:99], a[152:155], v[0:3], 0\n\t" \
  "buffer_load_dwordx4 v213, %[sx], s96 offen offset:1024 lds\n\t" \
  "v_fma_f32 v178, v178, v164, v166\n\t" \
  "v_fma_f32 v179, v179, v164, v166\n\t" \
  "v_fma_f32 v180, v180, v164, v166\n\t" \
  "v_mfma_f32_16x16x32_bf16 a[100:103], a[152:155], v[4:7], 0\n\t" \
  "s_add_u32 m0, %[mx], 6144\n\t" \
  "ds_read_b64_tr_b16 a[190:191], v191 offset:9216\n\t" \
  "v_fma_f32 v181, v181, v164, v166\n\t" \
  "v_fma_f32 v182, v182, v164, v166\n\t" \
  "v_fma_f32 v183, v183, v164, v166\n\t" \
  "v_fma_f32 v176, v176, v128, v136\n\t" \
  "v_mfma_f32_16x16x32_bf16 a[104:107], a[152:155], v[8:11], 0\n\t" \
  "s_xor_b32 s96, %[c17], 393216\n\t" \
  "s_add_u32 s96, s96, %[sl3]\n\t" \
  "buffer_load_dwordx4 v212, %[sx], s96 offen lds\n\t" \
  "v_fma_f32 v177, v177, v129, v137\n\t" \
  "v_mfma_f32_16x16x32_bf16 a[108:111], a[152:155], v[12:15], 0\n\t" \
  "v_fma_f32 v178, v178, v130, v138\n\t" \
  "v_fma_f32 v179, v179, v131, v139\n\t" \
  "v_fma_f32 v180, v180, v132, v140\n\t" \
  "v_fma_f32 v181, v181, v133, v141\n\t" \
  "v_fma_f32 v182, v182, v134, v142\n\t" \
  "v_mfma_f32_16x16x32_bf16 a[124:127], a[156:159], v[12:15], 0\n\t" \
  "buffer_load_dwordx4 v213, %[sx], s96 offen offset:1024 lds\n\t" \
  "v_fma_f32 v183, v183, v135, v143\n\t" \
  "v_cvt_pk_bf16_f32 v68, v176, v177\n\t" \
  "v_cvt_pk_bf16_f32 v69, v178, v179\n\t" \
  "v_mfma_f32_16x16x32_bf16 a[120:123], a[156:159], v[8:11], 0\n\t" \
  "v_cvt_pk_bf16_f32 v70, v180, v181\n\t" \
  "v_cvt_pk_bf16_f32 v71, v182, v183\n\t" \
  "v_lshlrev_b32 v176, 16, v72\n\t" \
  "v_and_b32 v177, s98, v72\n\t" \
  "v_mfma_f32_16x16x32_bf16 a[116:119], a[156:159], v[4:7], 0\n\t" \
  "v_lshlrev_b32 v178, 16, v73\n\t" \
  "v_and_b32 v179, s98, v73\n\t" \
  "v_lshlrev_b32 v180, 16, v74\n\t" \
  "v_and_b32 v181, s98, v74\n\t" \
  "v_lshlrev_b32 v182, 16, v75\n\t" \
  "v_and_b32 v183, s98, v75\n\t" \
  "v_mfma_f32_16x16x32_bf16 a[112:115], a[156:159], v[0:3], 0\n\t" \
  FSTP("v_writelane_b32 %[stv], s94, 36\n\t") \
  FSTP("v_writelane_b32 %[stv], s92, 37\n\t") \
  FSTP("s_memrealtime s[94:95]\n\t") \
  FSTP("s_memtime s[92:93]\n\t") \
  "s_waitcnt vmcnt(13) lgkmcnt(0)\n\t" \
  "s_barrier\n\t" \
  "v_mfma_f32_16x16x32_bf16 a[0:3], a[160:163], v[16:19], a[0:3]\n\t" \
  "s_add_u32 m0, %[mw], 0\n\t" \
  "ds_read_b64_tr_b16 a[128:129], v184 offset:49152\n\t" \
  "v_fma_f32 v176, v176, v168, v170\n\t" \
  "v_fma_f32 v177, v177, v168, v170\n\t" \
  "v_mfma_f32_16x16x32_bf16 a[4:7], a[160:163], v[20:23], a[4:7]\n\t" \
  "s_mov_b32 s96, %[w3]\n\t" \
  "ds_read_b64_tr_b16 a[130:131], v184 offset:50176\n\t" \
  "v_fma_f32 v178, v178, v168, v170\n\t" \
  "v_fma_f32 v179, v179, v168, v170\n\t" \
  "v_mfma_f32_16x16x32_bf16 a[8:11], a[160:163], v[24:27], a[8:11]\n\t" \
  "ds_read_b64_tr_b16 a[132:133], v185 offset:49152\n\t" \
  "v_fma_f32 v180, v180, v168, v170\n\t" \
  "v_fma_f32 v181, v181, v168, v170\n\t" \
  "v_mfma_f32_16x16x32_bf16 a[12:15], a[160:163], v[28:31], a[12:15]\n\t" \
  "ds_read_b64_tr_b16 a[134:135], v185 offset:50176\n\t" \
  "v_fma_f32 v182, v182, v168, v170\n\t" \
  "v_fma_f32 v183, v183, v168, v170\n\t" \
  "v_mfma_f32_16x16x32_bf16 a[28:31], a[164:167], v[28:31], a[28:31]\n\t" \
  "buffer_load_dwordx4 v200, %[sw], s96 offen offset:0 lds\n\t" \
  "v_fma_f32 v176, v176, v128, v136\n\t" \
  "v_mfma_f32_16x16x32_bf16 a[24:27], a[164:167], v[24:27], a[24:27]\n\t" \
  "ds_read_b64_tr_b16 a[136:137], v186 offset:49152\n\t" \
  "v_fma_f32 v177, v177, v129, v137\n\t" \
  "v_fma_f32 v178, v178, v130, v138\n\t" \
  "v_mfma_f32_16x16x32_bf16 a[20:23], a[164:167], v[20:23], a[20:23]\n\t" \
  "ds_read_b64_tr_b16 a[138:139], v186 offset:50176\n\t" \
  "v_fma_f32 v179, v179, v131, v139\n\t" \
  "v_fma_f32 v180, v180, v132, v140\n\t" \
  "v_mfma_f32_16x16x32_bf16 a[16:19], a[164:167], v[16:19], a[16:19]\n\t" \
  "buffer_load_dwordx4 v201, %[sw], s96 offen offset:1024 lds\n\t" \
  "v_fma_f32 v181, v181, v133, v141\n\t" \
  "v_mfma_f32_16x16x32_bf16 a[32:35], a[168:171], v[16:19], a[32:35]\n\t" \
  "ds_read_b64_tr_b16 a[140:141], v187 offset:49152\n\t" \
  "v_fma_f32 v182, v182, v134, v142\n\t" \
  "v_fma_f32 v183, v183, v135, v143\n\t" \
  "v_mfma_f32_16x16x32_bf16 a[36:39], a[168:171], v[20:23], a[36:39]\n\t" \
  "ds_read_b64_tr_b16 a[142:143], v187 offset:50176\n\t" \
  "v_cvt_pk_bf16_f32 v72, v176, v177\n\t" \
  "v_cvt_pk_bf16_f32 v73, v178, v179\n\t" \
  "v_mfma_f32_16x16x32_bf16 a[40:43], a[168:171], v[24:27], a[40:43]\n\t" \
  "ds_read_b64_tr_b16 a[144:145], v188 offset:49152\n\t" \
  "v_cvt_pk_bf16_f32 v74, v180, v181\n\t" \
  "v_cvt_pk_bf16_f32 v75, v182, v183\n\t" \
  "v_mfma_f32_16x16x32_bf16 a[44:47], a[168:171], v[28:31], a[44:47]\n\t" \
  "buffer_load_dwordx4 v202, %[sw], s96 offen offset:2048 lds\n\t" \
  "v_mfma_f32_16x16x32_bf16 a[60:63], a[172:175], v[28:31], a[60:63]\n\t" \
  "ds_read_b64_tr_b16 a[146:147], v188 offset:50176\n\t" \
  "ds_read_b128 v[80:83], v207 offset:0\n\t" \
  "v_mfma_f32_16x16x32_bf16 a[56:59], a[172:175], v[24:27], a[56:59]\n\t" \
  "ds_read_b64_tr_b16 a[148:149], v189 offset:49152\n\t" \
  "ds_read_b128 v[84:87], v207 offset:2048\n\t" \
  "v_mfma_f32_16x16x32_bf16 a[52:55], a[172:175], v[20:23], a[52:55]\n\t" \
  "ds_read_b64_tr_b16 a[150:151], v189 offset:50176\n\t" \
  "ds_read_b128 v[88:91], v207 offset:4096\n\t" \
  "v_mfma_f32_16x16x32_bf16 a[48:51], a[172:175], v[16:19], a[48:51]\n\t" \
  "ds_read_b64_tr_b16 a[152:153], v190 offset:49152\n\t" \
  "ds_read_b128 v[92:95], v207 offset:6144\n\t" \
  "v_lshlrev_b32 v176, 16, v76\n\t" \
  "v_and_b32 v177, s98, v76\n\t" \
  "v_mfma_f32_16x16x32_bf16 a[64:67], a[176:179], v[16:19], a[64:67]\n\t" \
  "buffer_load_dwordx4 v203, %[sw], s96 offen offset:3072 lds\n\t" \
  "v_lshlrev_b32 v178, 16, v77\n\t" \
  "v_and_b32 v179, s98, v77\n\t" \
  "v_mfma_f32_16x16x32_bf16 a[68:71], a[176:179], v[20:23], a[68:71]\n\t" \
  "ds_read_b64_tr_b16 a[154:155], v190 offset:50176\n\t" \
  "v_lshlrev_b32 v180, 16, v78\n\t" \
  "v_and_b32 v181, s98, v78\n\t" \
  "v_lshlrev_b32 v182, 16, v79\n\t" \
  "v_mfma_f32_16x16x32_bf16 a[72:75], a[176:179], v[24:27], a[72:75]\n\t" \
  "ds_read_b64_tr_b16 a[156:157], v191 offset:49152\n\t" \
  "v_and_b32 v183, s98, v79\n\t" \
  "v_fma_f32 v176, v176, v172, v174\n\t" \
  "v_fma_f32 v177, v177, v172, v174\n\t" \
  "v_mfma_f32_16x16x32_bf16 a[76:79], a[176:179], v[28:31], a[76:79]\n\t" \
  "ds_read_b64_tr_b16 a[158:159], v191 offset:50176\n\t" \
  "v_fma_f32 v178, v178, v172, v174\n\t" \
  "v_fma_f32 v179, v179, v172, v174\n\t" \
  "v_mfma_f32_16x16x32_bf16 a[92:95], a[180:183], v[28:31], a[92:95]\n\t" \
  "v_fma_f32 v180, v180, v172, v174\n\t" \
  "v_fma_f32 v181, v181, v172, v174\n\t" \
  "v_fma_f32 v182, v182, v172, v174\n\t" \
  "v_mfma_f32_16x16x32_bf16 a[88:91], a[180:183], v[24:27], a[88:91]\n\t" \
  "v_fma_f32 v183, v183, v172, v174\n\t" \
  "v_fma_f32 v176, v176, v128, v136\n\t" \
  "v_fma_f32 v177, v177, v129, v137\n\t" \
  "v_mfma_f32_16x16x32_bf16 a[84:87], a[180:183], v[20:23], a[84:87]\n\t" \
  "global_load_dword %[pf], v215, %[wq]\n\t" \
  "v_fma_f32 v178, v178, v130, v138\n\t" \
  "v_mfma_f32_16x16x32_bf16 a[80:83], a[180:183], v[16:19], a[80:83]\n\t" \
  "v_fma_f32 v179, v179, v131, v139\n\t" \
  "v_fma_f32 v180, v180, v132, v140\n\t" \
  "v_fma_f32 v181, v181, v133, v141\n\t" \
  "v_mfma_f32_16x16x32_bf16 a[96:99], a[184:187], v[16:19], a[96:99]\n\t" \
  "v_fma_f32 v182, v182, v134, v142\n\t" \
  "v_fma_f32 v183, v183, v135, v143\n\t" \
  "v_cvt_pk_bf16_f32 v76, v176, v177\n\t" \
  "v_mfma_f32_16x16x32_bf16 a[100:103], a[184:187], v[20:23], a[100:103]\n\t" \
  "v_cvt_pk_bf16_f32 v77, v178, v179\n\t" \
  "v_cvt_pk_bf16_f32 v78, v180, v181\n\t" \
  "v_cvt_pk_bf16_f32 v79, v182, v183\n\t" \
  "v_mfma_f32_16x16x32_bf16 a[104:107], a[184:187], v[24:27], a[104:107]\n\t" \
  "ds_read_b128 v[128:131], v208 offset:1536\n\t" \
  "ds_read_b128 v[132:135], v208 offset:1552\n\t" \
  "v_mfma_f32_16x16x32_bf16 a[108:111], a[184:187], v[28:31], a[108:111]\n\t" \
  "ds_read_b128 v[136:139], v208 offset:1792\n\t" \
  "ds_read_b128 v[140:143], v208 offset:1808\n\t" \
  "s_waitcnt lgkmcnt(7)\n\t" \
  "v_lshlrev_b32 v176, 16, v80\n\t" \
  "v_mfma_f32_16x16x32_bf16 a[124:127], a[188:191], v[28:31], a[124:127]\n\t" \
  "v_and_b32 v177, s98, v80\n\t" \
  "v_lshlrev_b32 v178, 16, v81\n\t" \
  "v_and_b32 v179, s98, v81\n\t" \
  "v_lshlrev_b32 v180, 16, v82\n\t" \
  "v_mfma_f32_16x16x32_bf16 a[120:123], a[188:191], v[24:27], a[120:123]\n\t" \
  "v_and_b32 v181, s98, v82\n\t" \
  "v_lshlrev_b32 v182, 16, v83\n\t" \
  "v_and_b32 v183, s98, v83\n\t" \
  "v_mfma_f32_16x16x32_bf16 a[116:119], a[188:191], v[20:23], a[116:119]\n\t" \
  "v_fma_f32 v176, v176, v160, v162\n\t" \
  "v_fma_f32 v177, v177, v160, v162\n\t" \
  "v_fma_f32 v178, v178, v160, v162\n\t" \
  "v_fma_f32 v179, v179, v160, v162\n\t" \
  "v_mfma_f32_16x16x32_bf16 a[112:115], a[188:191], v[16:19], a[112:115]\n\t" \
  FSTP("v_writelane_b32 %[stv], s94, 38\n\t") \
  FSTP("v_writelane_b32 %[stv], s92, 39\n\t") \
  FSTP("s_memrealtime s[94:95]\n\t") \
  FSTP("s_memtime s[92:93]\n\t") \
  "s_waitcnt lgkmcnt(4)\n\t" \
  "v_mfma_f32_16x16x32_bf16 a[0:3], a[128:131], v[32:35], a[0:3]\n\t" \
  "global_load_dword %[pf], v217, %[kv]\n\t" \
  "v_fma_f32 v180, v180, v160, v162\n\t" \
  "v_mfma_f32_16x16x32_bf16 a[4:7], a[128:131], v[36:39], a[4:7]\n\t" \
  "ds_read_b64_tr_b16 a[160:161], v184 offset:57344\n\t" \
  "v_fma_f32 v181, v181, v160, v162\n\t" \
  "v_fma_f32 v182, v182, v160, v162\n\t" \
  "v_mfma_f32_16x16x32_bf16 a[8:11], a[128:131], v[40:43], a[8:11]\n\t" \
  "ds_read_b64_tr_b16 a[162:163], v184 offset:58368\n\t" \
  "v_fma_f32 v183, v183, v160, v162\n\t" \
  "v_fma_f32 v176, v176, v144, v152\n\t" \
  "v_mfma_f32_16x16x32_bf16 a[12:15], a[128:131], v[44:47], a[12:15]\n\t" \
  "ds_read_b64_tr_b16 a[164:165], v185 offset:57344\n\t" \
  "v_fma_f32 v177, v177, v145, v153\n\t" \
  "v_fma_f32 v178, v178, v146, v154\n\t" \
  "v_mfma_f32_16x16x32_bf16 a[28:31], a[132:135], v[44:47], a[28:31]\n\t" \
  "ds_read_b64_tr_b16 a[166:167], v185 offset:58368\n\t" \
  "v_fma_f32 v179, v179, v147, v155\n\t" \
  "v_fma_f32 v180, v180, v148, v156\n\t" \
  "v_mfma_f32_16x16x32_bf16 a[24:27], a[132:135], v[40:43], a[24:27]\n\t" \
  "ds_read_b64_tr_b16 a[168:169], v186 offset:57344\n\t" \
  "v_fma_f32 v181, v181, v149, v157\n\t" \
  "v_fma_f32 v182, v182, v150, v158\n\t" \
  "v_mfma_f32_16x16x32_bf16 a[20:23], a[132:135], v[36:39], a[20:23]\n\t" \
  "ds_read_b64_tr_b16 a[170:171], v186 offset:58368\n\t" \
  "v_fma_f32 v183, v183, v151, v159\n\t" \
  "v_cvt_pk_bf16_f32 v80, v176, v177\n\t" \
  "v_mfma_f32_16x16x32_bf16 a[16:19], a[132:135], v[32:35], a[16:19]\n\t" \
  "ds_read_b64_tr_b16 a[172:173], v187 offset:57344\n\t" \
  "v_cvt_pk_bf16_f32 v81, v178, v179\n\t" \
  "v_cvt_pk_bf16_f32 v82, v180, v181\n\t" \
  "v_mfma_f32_16x16x32_bf16 a[32:35], a[136:139], v[32:35], a[32:35]\n\t" \
  "ds_read_b64_tr_b16 a[174:175], v187 offset:58368\n\t" \
  "v_cvt_pk_bf16_f32 v83, v182, v183\n\t" \
  "v_lshlrev_b32 v176, 16, v84\n\t" \
  "v_mfma_f32_16x16x32_bf16 a[36:39], a[136:139], v[36:39], a[36:39]\n\t" \
  "ds_read_b64_tr_b16 a[176:177], v188 offset:57344\n\t" \
  "v_and_b32 v177, s98, v84\n\t" \
  "v_lshlrev_b32 v178, 16, v85\n\t" \
  "v_mfma_f32_16x16x32_bf16 a[40:43], a[136:139], v[40:43], a[40:43]\n\t" \
  "ds_read_b64_tr_b16 a[178:179], v188 offset:58368\n\t" \
  "v_and_b32 v179, s98, v85\n\t" \
  "v_lshlrev_b32 v180, 16, v86\n\t" \
  "v_mfma_f32_16x16x32_bf16 a[44:47], a[136:139], v[44:47], a[44:47]\n\t" \
  "ds_read_b64_tr_b16 a[180:181], v189 offset:57344\n\t" \
  "v_and_b32 v181, s98, v86\n\t" \
  "v_lshlrev_b32 v182, 16, v87\n\t" \
  "v_mfma_f32_16x16x32_bf16 a[60:63], a[140:143], v[44:47], a[60:63]\n\t" \
  "ds_read_b64_tr_b16 a[182:183], v189 offset:58368\n\t" \
  "v_and_b32 v183, s98, v87\n\t" \
  "v_fma_f32 v176, v176, v164, v166\n\t" \
  "v_mfma_f32_16x16x32_bf16 a[56:59], a[140:143], v[40:43], a[56:59]\n\t" \
  "ds_read_b64_tr_b16 a[184:185], v190 offset:57344\n\t" \
  "v_fma_f32 v177, v177, v164, v166\n\t" \
  "v_fma_f32 v178, v178, v164, v166\n\t" \
  "v_mfma_f32_16x16x32_bf16 a[52:55], a[140:143], v[36:39], a[52:55]\n\t" \
  "buffer_store_short v218, v219, %[ws], 0 offen sc1\n\t" \
  "v_fma_f32 v179, v179, v164, v166\n\t" \
  "v_mfma_f32_16x16x32_bf16 a[48:51], a[140:143], v[32:35], a[48:51]\n\t" \
  "ds_read_b64_tr_b16 a[186:187], v190 offset:58368\n\t" \
  "v_fma_f32 v180, v180, v164, v166\n\t" \
  "v_fma_f32 v181, v181, v164, v166\n\t" \
  "v_mfma_f32_16x16x32_bf16 a[64:67], a[144:147], v[32:35], a[64:67]\n\t" \
  "ds_read_b64_tr_b16 a[188:189], v191 offset:57344\n\t" \
  "v_fma_f32 v182, v182, v164, v166\n\t" \
  "v_fma_f32 v183, v183, v164, v166\n\t" \
  "v_mfma_f32_16x16x32_bf16 a[68:71], a[144:147], v[36:39], a[68:71]\n\t" \
  "ds_read_b64_tr_b16 a[190:191], v191 offset:58368\n\t" \
  "v_fma_f32 v176, v176, v144, v152\n\t" \
  "v_fma_f32 v177, v177, v145, v153\n\t" \
  "v_mfma_f32_16x16x32_bf16 a[72:75], a[144:147], v[40:43], a[72:75]\n\t" \
  "v_fma_f32 v178, v178, v146, v154\n\t" \
  "v_fma_f32 v179, v179, v147, v155\n\t" \
  "v_fma_f32 v180, v180, v148, v156\n\t" \
  "v_mfma_f32_16x16x32_bf16 a[76:79], a[144:147], v[44:47], a[76:79]\n\t" \
  "v_fma_f32 v181, v181, v149, v157\n\t" \
  "v_fma_f32 v182, v182, v150, v158\n\t" \
  "v_fma_f32 v183, v183, v151, v159\n\t" \
  "v_mfma_f32_16x16x32_bf16 a[92:95], a[148:151], v[44:47], a[92:95]\n\t" \
  "v_cvt_pk_bf16_f32 v84, v176, v177\n\t" \
  "v_cvt_pk_bf16_f32 v85, v178, v179\n\t" \
  "v_cvt_pk_bf16_f32 v86, v180, v181\n\t" \
  "v_mfma_f32_16x16x32_bf16 a[88:91], a[148:151], v[40:43], a[88:91]\n\t" \
  "v_cvt_pk_bf16_f32 v87, v182, v183\n\t" \
  "v_lshlrev_b32 v176, 16, v88\n\t" \
  "v_and_b32 v177, s98, v88\n\t" \
  "v_mfma_f32_16x16x32_bf16 a[84:87], a[148:151], v[36:39], a[84:87]\n\t" \
  "v_lshlrev_b32 v178, 16, v89\n\t" \
  "v_and_b32 v179, s98, v89\n\t" \
  "v_lshlrev_b32 v180, 16, v90\n\t" \
  "v_mfma_f32_16x16x32_bf16 a[80:83], a[148:151], v[32:35], a[80:83]\n\t" \
  "v_and_b32 v181, s98, v90\n\t" \
  "v_lshlrev_b32 v182, 16, v91\n\t" \
  "v_and_b32 v183, s98, v91\n\t" \
  "v_mfma_f32_16x16x32_bf16 a[96:99], a[152:155], v[32:35], a[96:99]\n\t" \
  "v_fma_f32 v176, v176, v168, v170\n\t" \
  "v_fma_f32 v177, v177, v168, v170\n\t" \
  "v_fma_f32 v178, v178, v168, v170\n\t" \
  "v_mfma_f32_16x16x32_bf16 a[100:103], a[152:155], v[36:39], a[100:103]\n\t" \
  "v_fma_f32 v179, v179, v168, v170\n\t" \
  "v_fma_f32 v180, v180, v168, v170\n\t" \
  "v_fma_f32 v181, v181, v168, v170\n\t" \
  "v_mfma_f32_16x16x32_bf16 a[104:107], a[152:155], v[40:43], a[104:107]\n\t" \
  "v_fma_f32 v182, v182, v168, v170\n\t" \
  "v_fma_f32 v183, v183, v168, v170\n\t" \
  "v_mfma_f32_16x16x32_bf16 a[108:111], a[152:155], v[44:47], a[108:111]\n\t" \
  "v_fma_f32 v176, v176, v144, v152\n\t" \
  "v_fma_f32 v177, v177, v145, v153\n\t" \
  "v_mfma_f32_16x16x32_bf16 a[124:127], a[156:159], v[44:47], a[124:127]\n\t" \
  "v_fma_f32 v178, v178, v146, v154\n\t" \
  "v_fma_f32 v179, v179, v147, v155\n\t" \
  "v_mfma_f32_16x16x32_bf16 a[120:123], a[156:159], v[40:43], a[120:123]\n\t" \
  "v_fma_f32 v180, v180, v148, v156\n\t" \
  "v_fma_f32 v181, v181, v149, v157\n\t" \
  "v_mfma_f32_16x16x32_bf16 a[116:119], a[156:159], v[36:39], a[116:119]\n\t" \
  "v_fma_f32 v182, v182, v150, v158\n\t" \
  "v_fma_f32 v183, v183, v151, v159\n\t" \
  "v_mfma_f32_16x16x32_bf16 a[112:115], a[156:159], v[32:35], a[112:115]\n\t" \
  FSTP("v_writelane_b32 %[stv], s94, 40\n\t") \
  FSTP("v_writelane_b32 %[stv], s92, 41\n\t") \
  FSTP("s_memrealtime s[94:95]\n\t") \
  FSTP("s_memtime s[92:93]\n\t") \
  "s_waitcnt vmcnt(15) lgkmcnt(0)\n\t" \
  "s_barrier\n\t" \
  "v_mfma_f32_16x16x32_bf16 a[0:3], a[160:163], v[48:51], a[0:3]\n\t" \
  "buffer_load_dword %[fl], v210, %[ws], %[fo] offen sc1\n\t" \
  "v_cvt_pk_bf16_f32 v88, v176, v177\n\t" \
  "v_cvt_pk_bf16_f32 v89, v178, v179\n\t" \
  "v_mfma_f32_16x16x32_bf16 a[4:7], a[160:163], v[52:55], a[4:7]\n\t" \
  "s_add_u32 s96, %[rot19], 2097152\n\t" \
  "ds_read_b64_tr_b16 a[128:129], v192 offset:0\n\t" \
  "v_cvt_pk_bf16_f32 v90, v180, v181\n\t" \
  "v_cvt_pk_bf16_f32 v91, v182, v183\n\t" \
  "s_waitcnt vmcnt(8)\n\t" \
  "v_mfma_f32_16x16x32_bf16 a[8:11], a[160:163], v[56:59], a[8:11]\n\t" \
  "s_and_b32 s96, s96, 0x780000\n\t" \
  "ds_read_b64_tr_b16 a[130:131], v192 offset:1024\n\t" \
  "ds_read_b128 v[96:99], v204 offset:0\n\t" \
  "v_mfma_f32_16x16x32_bf16 a[12:15], a[160:163], v[60:63], a[12:15]\n\t" \
  "s_add_u32 m0, %[mw], 49152\n\t" \
  "ds_read_b64_tr_b16 a[132:133], v193 offset:0\n\t" \
  "ds_read_b128 v[100:103], v204 offset:2048\n\t" \
  "v_mfma_f32_16x16x32_bf16 a[28:31], a[164:167], v[60:63], a[28:31]\n\t" \
  "buffer_load_dwordx4 v200, %[sw], s96 offen offset:0 lds\n\t" \
  "v_mfma_f32_16x16x32_bf16 a[24:27], a[164:167], v[56:59], a[24:27]\n\t" \
  "ds_read_b64_tr_b16 a[134:135], v193 offset:1024\n\t" \
  "ds_read_b128 v[104:107], v204 offset:4096\n\t" \
  "v_mfma_f32_16x16x32_bf16 a[20:23], a[164:167], v[52:55], a[20:23]\n\t" \
  "ds_read_b64_tr_b16 a[136:137], v194 offset:0\n\t" \
  "ds_read_b128 v[108:111], v204 offset:6144\n\t" \
  "v_lshlrev_b32 v176, 16, v92\n\t" \
  "v_and_b32 v177, s98, v92\n\t" \
  "v_mfma_f32_16x16x32_bf16 a[16:19], a[164:167], v[48:51], a[16:19]\n\t" \
  "ds_read_b64_tr_b16 a[138:139], v194 offset:1024\n\t" \
  "v_lshlrev_b32 v178, 16, v93\n\t" \
  "v_and_b32 v179, s98, v93\n\t" \
  "v_lshlrev_b32 v180, 16, v94\n\t" \
  "v_mfma_f32_16x16x32_bf16 a[32:35], a[168:171], v[48:51], a[32:35]\n\t" \
  "ds_read_b64_tr_b16 a[140:141], v195 offset:0\n\t" \
  "v_and_b32 v181, s98, v94\n\t" \
  "v_lshlrev_b32 v182, 16, v95\n\t" \
  "v_and_b32 v183, s98, v95\n\t" \
  "v_mfma_f32_16x16x32_bf16 a[36:39], a[168:171], v[52:55], a[36:39]\n\t" \
  "buffer_load_dwordx4 v201, %[sw], s96 offen offset:1024 lds\n\t" \
  "v_fma_f32 v176, v176, v172, v174\n\t" \
  "v_fma_f32 v177, v177, v172, v174\n\t" \
  "v_mfma_f32_16x16x32_bf16 a[40:43], a[168:171], v[56:59], a[40:43]\n\t" \
  "ds_read_b64_tr_b16 a[142:143], v195 offset:1024\n\t" \
  "v_fma_f32 v178, v178, v172, v174\n\t" \
  "v_fma_f32 v179, v179, v172, v174\n\t" \
  "v_fma_f32 v180, v180, v172, v174\n\t" \
  "v_mfma_f32_16x16x32_bf16 a[44:47], a[168:171], v[60:63], a[44:47]\n\t" \
  "ds_read_b64_tr_b16 a[144:145], v196 offset:0\n\t" \
  "v_fma_f32 v181, v181, v172, v174\n\t" \
  "v_fma_f32 v182, v182, v172, v174\n\t" \
  "v_fma_f32 v183, v183, v172, v174\n\t" \
  "v_mfma_f32_16x16x32_bf16 a[60:63], a[172:175], v[60:63], a[60:63]\n\t" \
  "ds_read_b64_tr_b16 a[146:147], v196 offset:1024\n\t" \
  "v_fma_f32 v176, v176, v144, v152\n\t" \
  "v_fma_f32 v177, v177, v145, v153\n\t" \
  "v_fma_f32 v178, v178, v146, v154\n\t" \
  "v_mfma_f32_16x16x32_bf16 a[56:59], a[172:175], v[56:59], a[56:59]\n\t" \
  "ds_read_b64_tr_b16 a[148:149], v197 offset:0\n\t" \
  "v_fma_f32 v179, v179, v147, v155\n\t" \
  "v_fma_f32 v180, v180, v148, v156\n\t" \
  "v_fma_f32 v181, v181, v149, v157\n\t" \
  "v_mfma_f32_16x16x32_bf16 a[52:55], a[172:175], v[52:55], a[52:55]\n\t" \
  "buffer_load_dwordx4 v202, %[sw], s96 offen offset:2048 lds\n\t" \
  "v_fma_f32 v182, v182, v150, v158\n\t" \
  "v_mfma_f32_16x16x32_bf16 a[48:51], a[172:175], v[48:51], a[48:51]\n\t" \
  "ds_read_b64_tr_b16 a[150:151], v197 offset:1024\n\t" \
  "v_fma_f32 v183, v183, v151, v159\n\t" \
  "v_cvt_pk_bf16_f32 v92, v176, v177\n\t" \
  "v_mfma_f32_16x16x32_bf16 a[64:67], a[176:179], v[48:51], a[64:67]\n\t" \
  "ds_read_b64_tr_b16 a[152:153], v198 offset:0\n\t" \
  "v_cvt_pk_bf16_f32 v93, v178, v179\n\t" \
  "v_cvt_pk_bf16_f32 v94, v180, v181\n\t" \
  "v_mfma_f32_16x16x32_bf16 a[68:71], a[176:179], v[52:55], a[68:71]\n\t" \
  "ds_read_b64_tr_b16 a[154:155], v198 offset:1024\n\t" \
  "v_cvt_pk_bf16_f32 v95, v182, v183\n\t" \
  "v_mfma_f32_16x16x32_bf16 a[72:75], a[176:179], v[56:59], a[72:75]\n\t" \
  "buffer_load_dwordx4 v203, %[sw], s96 offen offset:3072 lds\n\t" \
  "v_mfma_f32_16x16x32_bf16 a[76:79], a[176:179], v[60:63], a[76:79]\n\t" \
  "ds_read_b64_tr_b16 a[156:157], v199 offset:0\n\t" \
  "ds_read_b128 v[144:147], v208 offset:1664\n\t" \
  "v_mfma_f32_16x16x32_bf16 a[92:95], a[180:183], v[60:63], a[92:95]\n\t" \
  "ds_read_b64_tr_b16 a[158:159], v199 offset:1024\n\t" \
  "ds_read_b128 v[148:151], v208 offset:1680\n\t" \
  "v_mfma_f32_16x16x32_bf16 a[88:91], a[180:183], v[56:59], a[88:91]\n\t" \
  "ds_read_b128 v[152:155], v208 offset:1920\n\t" \
  "ds_read_b128 v[156:159], v208 offset:1936\n\t" \
  "s_waitcnt lgkmcnt(15)\n\t" \
  "v_lshlrev_b32 v176, 16, v96\n\t" \
  "v_mfma_f32_16x16x32_bf16 a[84:87], a[180:183], v[52:55], a[84:87]\n\t" \
  "v_and_b32 v177, s98, v96\n\t" \
  "v_lshlrev_b32 v178, 16, v97\n\t" \
  "v_and_b32 v179, s98, v97\n\t" \
  "v_lshlrev_b32 v180, 16, v98\n\t" \
  "v_mfma_f32_16x16x32_bf16 a[80:83], a[180:183], v[48:51], a[80:83]\n\t" \
  "global_load_dword %[pf], v216, %[wq]\n\t" \
  "v_and_b32 v181, s98, v98\n\t" \
  "v_lshlrev_b32 v182, 16, v99\n\t" \
  "v_mfma_f32_16x16x32_bf16 a[96:99], a[184:187], v[48:51], a[96:99]\n\t" \
  "s_lshr_b32 s99, %[c17], 9\n\t" \
  "v_xor_b32 v248, s99, v214\n\t" \
  "v_xor_b32 v249, 256, v248\n\t" \
  "v_xor_b32 v250, 512, v248\n\t" \
  "v_xor_b32 v251, 768, v248\n\t" \
  "v_mfma_f32_16x16x32_bf16 a[100:103], a[184:187], v[52:55], a[100:103]\n\t" \
  "v_and_b32 v183, s98, v99\n\t" \
  "v_fma_f32 v176, v176, v160, v162\n\t" \
  "v_fma_f32 v177, v177, v160, v162\n\t" \
  "v_fma_f32 v178, v178, v160, v162\n\t" \
  "v_mfma_f32_16x16x32_bf16 a[104:107], a[184:187], v[56:59], a[104:107]\n\t" \
  "v_fma_f32 v179, v179, v160, v162\n\t" \
  "v_fma_f32 v180, v180, v160, v162\n\t" \
  "v_fma_f32 v181, v181, v160, v162\n\t" \
  "v_mfma_f32_16x16x32_bf16 a[108:111], a[184:187], v[60:63], a[108:111]\n\t" \
  "v_fma_f32 v182, v182, v160, v162\n\t" \
  "v_fma_f32 v183, v183, v160, v162\n\t" \
  "v_fma_f32 v176, v176, v128, v136\n\t" \
  "v_mfma_f32_16x16x32_bf16 a[124:127], a[188:191], v[60:63], a[124:127]\n\t" \
  "v_fma_f32 v177, v177, v129, v137\n\t" \
  "v_fma_f32 v178, v178, v130, v138\n\t" \
  "v_fma_f32 v179, v179, v131, v139\n\t" \
  "v_mfma_f32_16x16x32_bf16 a[120:123], a[188:191], v[56:59], a[120:123]\n\t" \
  "v_fma_f32 v180, v180, v132, v140\n\t" \
  "v_fma_f32 v181, v181, v133, v141\n\t" \
  "v_fma_f32 v182, v182, v134, v142\n\t" \
  "v_mfma_f32_16x16x32_bf16 a[116:119], a[188:191], v[52:55], a[116:119]\n\t" \
  "v_fma_f32 v183, v183, v135, v143\n\t" \
  "v_cvt_pk_bf16_f32 v96, v176, v177\n\t" \
  "v_cvt_pk_bf16_f32 v97, v178, v179\n\t" \
  "v_cvt_pk_bf16_f32 v98, v180, v181\n\t" \
  "v_mfma_f32_16x16x32_bf16 a[112:115], a[188:191], v[48:51], a[112:115]\n\t" \
  FSTP("v_writelane_b32 %[stv], s94, 42\n\t") \
  FSTP("v_writelane_b32 %[stv], s92, 43\n\t") \
  FSTP("s_memrealtime s[94:95]\n\t") \
  FSTP("s_memtime s[92:93]\n\t") \
  "s_waitcnt lgkmcnt(3)\n\t" \
  "v_mfma_f32_16x16x32_bf16 a[0:3], a[128:131], v[64:67], a[0:3]\n\t" \
  "s_waitcnt vmcnt(5)\n\t" \
  "v_cmp_ne_u32 vcc, v211, %[fl]\n\t" \
  "s_cbranch_vccnz .Lfq_s\n\t" \
  ".Lfq_k:\n\t" \
  "v_mfma_f32_16x16x32_bf16 a[4:7], a[128:131], v[68:71], a[4:7]\n\t" \
  "s_add_u32 s97, %[rot19], 2097152\n\t" \
  "ds_read_b64_tr_b16 a[160:161], v192 offset:8192\n\t" \
  "v_cvt_pk_bf16_f32 v99, v182, v183\n\t" \
  "v_lshlrev_b32 v176, 16, v100\n\t" \
  "v_and_b32 v177, s98, v100\n\t" \
  "v_mfma_f32_16x16x32_bf16 a[8:11], a[128:131], v[72:75], a[8:11]\n\t" \
  "s_bfe_u32 s97, s97, 0x140003\n\t" \
  "ds_read_b64_tr_b16 a[162:163], v192 offset:9216\n\t" \
  "v_lshlrev_b32 v178, 16, v101\n\t" \
  "v_and_b32 v179, s98, v101\n\t" \
  "v_lshlrev_b32 v180, 16, v102\n\t" \
  "v_mfma_f32_16x16x32_bf16 a[12:15], a[128:131], v[76:79], a[12:15]\n\t" \
  "s_add_u32 s97, s97, %[sox]\n\t" \
  "ds_read_b64_tr_b16 a[164:165], v193 offset:8192\n\t" \
  "v_and_b32 v181, s98, v102\n\t" \
  "v_lshlrev_b32 v182, 16, v103\n\t" \
  "v_and_b32 v183, s98, v103\n\t" \
  "v_mfma_f32_16x16x32_bf16 a[28:31], a[132:135], v[76:79], a[28:31]\n\t" \
  "buffer_load_dwordx4 a[192:195], v248, %[ws], s97 offen sc1\n\t" \
  "v_fma_f32 v176, v176, v164, v166\n\t" \
  "v_fma_f32 v177, v177, v164, v166\n\t" \
  "v_mfma_f32_16x16x32_bf16 a[24:27], a[132:135], v[72:75], a[24:27]\n\t" \
  "buffer_load_dwordx4 a[196:199], v249, %[ws], s97 offen sc1\n\t" \
  "v_fma_f32 v178, v178, v164, v166\n\t" \
  "v_fma_f32 v179, v179, v164, v166\n\t" \
  "v_mfma_f32_16x16x32_bf16 a[20:23], a[132:135], v[68:71], a[20:23]\n\t" \
  "ds_read_b64_tr_b16 a[166:167], v193 offset:9216\n\t" \
  "v_fma_f32 v180, v180, v164, v166\n\t" \
  "v_fma_f32 v181, v181, v164, v166\n\t" \
  "v_fma_f32 v182, v182, v164, v166\n\t" \
  "v_mfma_f32_16x16x32_bf16 a[16:19], a[132:135], v[64:67], a[16:19]\n\t" \
  "ds_read_b64_tr_b16 a[168:169], v194 offset:8192\n\t" \
  "v_fma_f32 v183, v183, v164, v166\n\t" \
  "v_fma_f32 v176, v176, v128, v136\n\t" \
  "v_fma_f32 v177, v177, v129, v137\n\t" \
  "v_mfma_f32_16x16x32_bf16 a[32:35], a[136:139], v[64:67], a[32:35]\n\t" \
  "buffer_load_dwordx4 a[200:203], v250, %[ws], s97 offen sc1\n\t" \
  ".Lhs1:\n\ts_cbranch_execz .Lhs1n\n\t" \
  "v_fma_f32 v178, v178, v130, v138\n\t" \
  "v_fma_f32 v179, v179, v131, v139\n\t" \
  "v_mfma_f32_16x16x32_bf16 a[36:39], a[136:139], v[68:71], a[36:39]\n\t" \
  "ds_read_b64_tr_b16 a[170:171], v194 offset:9216\n\t" \
  "v_fma_f32 v180, v180, v132, v140\n\t" \
  "v_fma_f32 v181, v181, v133, v141\n\t" \
  "v_mfma_f32_16x16x32_bf16 a[40:43], a[136:139], v[72:75], a[40:43]\n\t" \
  "ds_read_b64_tr_b16 a[172:173], v195 offset:8192\n\t" \
  "v_fma_f32 v182, v182, v134, v142\n\t" \
  "v_fma_f32 v183, v183, v135, v143\n\t" \
  "v_mfma_f32_16x16x32_bf16 a[44:47], a[136:139], v[76:79], a[44:47]\n\t" \
  "buffer_load_dwordx4 a[204:207], v251, %[ws], s97 offen sc1\n\t" \
  "v_cvt_pk_bf16_f32 v100, v176, v177\n\t" \
  "v_mfma_f32_16x16x32_bf16 a[60:63], a[140:143], v[76:79], a[60:63]\n\t" \
  "s_add_u32 s97, s97, %[dk]\n\t" \
  "ds_read_b64_tr_b16 a[174:175], v195 offset:9216\n\t" \
  "v_cvt_pk_bf16_f32 v101, v178, v179\n\t" \
  "v_cvt_pk_bf16_f32 v102, v180, v181\n\t" \
  "v_mfma_f32_16x16x32_bf16 a[56:59], a[140:143], v[72:75], a[56:59]\n\t" \
  "ds_read_b64_tr_b16 a[176:177], v196 offset:8192\n\t" \
  "v_cvt_pk_bf16_f32 v103, v182, v183\n\t" \
  "v_lshlrev_b32 v176, 16, v104\n\t" \
  "v_mfma_f32_16x16x32_bf16 a[52:55], a[140:143], v[68:71], a[52:55]\n\t" \
  "buffer_load_dwordx4 a[208:211], v248, %[ws], s97 offen sc1\n\t" \
  "v_and_b32 v177, s98, v104\n\t" \
  "v_mfma_f32_16x16x32_bf16 a[48:51], a[140:143], v[64:67], a[48:51]\n\t" \
  "ds_read_b64_tr_b16 a[178:179], v196 offset:9216\n\t" \
  "v_lshlrev_b32 v178, 16, v105\n\t" \
  "v_and_b32 v179, s98, v105\n\t" \
  "v_mfma_f32_16x16x32_bf16 a[64:67], a[144:147], v[64:67], a[64:67]\n\t" \
  "ds_read_b64_tr_b16 a[180:181], v197 offset:8192\n\t" \
  "v_lshlrev_b32 v180, 16, v106\n\t" \
  "v_and_b32 v181, s98, v106\n\t" \
  "v_mfma_f32_16x16x32_bf16 a[68:71], a[144:147], v[68:71], a[68:71]\n\t" \
  "buffer_load_dwordx4 a[212:215], v249, %[ws], s97 offen sc1\n\t" \
  "v_lshlrev_b32 v182, 16, v107\n\t" \
  "v_mfma_f32_16x16x32_bf16 a[72:75], a[144:147], v[72:75], a[72:75]\n\t" \
  "ds_read_b64_tr_b16 a[182:183], v197 offset:9216\n\t" \
  "v_and_b32 v183, s98, v107\n\t" \
  "v_fma_f32 v176, v176, v168, v170\n\t" \
  "v_mfma_f32_16x16x32_bf16 a[76:79], a[144:147], v[76:79], a[76:79]\n\t" \
  "ds_read_b64_tr_b16 a[184:185], v198 offset:8192\n\t" \
  "v_fma_f32 v177, v177, v168, v170\n\t" \
  "v_fma_f32 v178, v178, v168, v170\n\t" \
  "v_mfma_f32_16x16x32_bf16 a[92:95], a[148:151], v[76:79], a[92:95]\n\t" \
  "ds_read_b64_tr_b16 a[186:187], v198 offset:9216\n\t" \
  "v_fma_f32 v179, v179, v168, v170\n\t" \
  "v_fma_f32 v180, v180, v168, v170\n\t" \
  "v_mfma_f32_16x16x32_bf16 a[88:91], a[148:151], v[72:75], a[88:91]\n\t" \
  "buffer_load_dwordx4 a[216:219], v250, %[ws], s97 offen sc1\n\t" \
  "v_fma_f32 v181, v181, v168, v170\n\t" \
  "v_mfma_f32_16x16x32_bf16 a[84:87], a[148:151], v[68:71], a[84:87]\n\t" \
  "ds_read_b64_tr_b16 a[188:189], v199 offset:8192\n\t" \
  "v_fma_f32 v182, v182, v168, v170\n\t" \
  "v_fma_f32 v183, v183, v168, v170\n\t" \
  "v_mfma_f32_16x16x32_bf16 a[80:83], a[148:151], v[64:67], a[80:83]\n\t" \
  "ds_read_b64_tr_b16 a[190:191], v199 offset:9216\n\t" \
  "v_fma_f32 v176, v176, v128, v136\n\t" \
  "v_fma_f32 v177, v177, v129, v137\n\t" \
  "v_mfma_f32_16x16x32_bf16 a[96:99], a[152:155], v[64:67], a[96:99]\n\t" \
  "buffer_load_dwordx4 a[220:223], v251, %[ws], s97 offen sc1\n\t" \
  "v_fma_f32 v178, v178, v130, v138\n\t" \
  "v_mfma_f32_16x16x32_bf16 a[100:103], a[152:155], v[68:71], a[100:103]\n\t" \
  "v_fma_f32 v179, v179, v131, v139\n\t" \
  "v_fma_f32 v180, v180, v132, v140\n\t" \
  "v_fma_f32 v181, v181, v133, v141\n\t" \
  "v_mfma_f32_16x16x32_bf16 a[104:107], a[152:155], v[72:75], a[104:107]\n\t" \
  "v_fma_f32 v182, v182, v134, v142\n\t" \
  "v_fma_f32 v183, v183, v135, v143\n\t" \
  "v_cvt_pk_bf16_f32 v104, v176, v177\n\t" \
  "v_mfma_f32_16x16x32_bf16 a[108:111], a[152:155], v[76:79], a[108:111]\n\t" \
  "v_cvt_pk_bf16_f32 v105, v178, v179\n\t" \
  "v_cvt_pk_bf16_f32 v106, v180, v181\n\t" \
  "v_cvt_pk_bf16_f32 v107, v182, v183\n\t" \
  "v_mfma_f32_16x16x32_bf16 a[124:127], a[156:159], v[76:79], a[124:127]\n\t" \
  "ds_read_b128 v[112:115], v205 offset:0\n\t" \
  "ds_read_b128 v[116:119], v205 offset:2048\n\t" \
  "v_mfma_f32_16x16x32_bf16 a[120:123], a[156:159], v[72:75], a[120:123]\n\t" \
  "ds_read_b128 v[120:123], v205 offset:4096\n\t" \
  "ds_read_b128 v[124:127], v205 offset:6144\n\t" \
  "v_lshlrev_b32 v176, 16, v108\n\t" \
  "v_and_b32 v177, s98, v108\n\t" \
  "v_mfma_f32_16x16x32_bf16 a[116:119], a[156:159], v[68:71], a[116:119]\n\t" \
  "v_lshlrev_b32 v178, 16, v109\n\t" \
  "v_and_b32 v179, s98, v109\n\t" \
  "v_lshlrev_b32 v180, 16, v110\n\t" \
  "v_and_b32 v181, s98, v110\n\t" \
  "v_mfma_f32_16x16x32_bf16 a[112:115], a[156:159], v[64:67], a[112:115]\n\t" \
  FSTP("v_writelane_b32 %[stv], s94, 44\n\t") \
  FSTP("v_writelane_b32 %[stv], s92, 45\n\t") \
  FSTP("s_memrealtime s[94:95]\n\t") \
  FSTP("s_memtime s[92:93]\n\t") \
  "s_waitcnt lgkmcnt(4)\n\t" \
  "s_barrier\n\t" \
  "v_mfma_f32_16x16x32_bf16 a[0:3], a[160:163], v[80:83], a[0:3]\n\t" \
  "s_add_u32 s96, %[rot19], 2621440\n\t" \
  "ds_read_b64_tr_b16 a[128:129], v184 offset:0\n\t" \
  "v_lshlrev_b32 v182, 16, v111\n\t" \
  "v_and_b32 v183, s98, v111\n\t" \
  "v_fma_f32 v176, v176, v172, v174\n\t" \
  "v_mfma_f32_16x16x32_bf16 a[4:7], a[160:163], v[84:87], a[4:7]\n\t" \
  "s_and_b32 s96, s96, 0x780000\n\t" \
  "ds_read_b64_tr_b16 a[130:131], v184 offset:1024\n\t" \
  "v_fma_f32 v177, v177, v172, v174\n\t" \
  "v_fma_f32 v178, v178, v172, v174\n\t" \
  "v_fma_f32 v179, v179, v172, v174\n\t" \
  "v_mfma_f32_16x16x32_bf16 a[8:11], a[160:163], v[88:91], a[8:11]\n\t" \
  "s_add_u32 m0, %[mw], 98304\n\t" \
  "ds_read_b64_tr_b16 a[132:133], v185 offset:0\n\t" \
  "v_fma_f32 v180, v180, v172, v174\n\t" \
  "v_fma_f32 v181, v181, v172, v174\n\t" \
  "v_fma_f32 v182, v182, v172, v174\n\t" \
  "v_mfma_f32_16x16x32_bf16 a[12:15], a[160:163], v[92:95], a[12:15]\n\t" \
  "buffer_load_dwordx4 v200, %[sw], s96 offen offset:0 lds\n\t" \
  "v_fma_f32 v183, v183, v172, v174\n\t" \
  "v_fma_f32 v176, v176, v128, v136\n\t" \
  "v_mfma_f32_16x16x32_bf16 a[28:31], a[164:167], v[92:95], a[28:31]\n\t" \
  "buffer_load_dwordx4 v201, %[sw], s96 offen offset:1024 lds\n\t" \
  "v_fma_f32 v177, v177, v129, v137\n\t" \
  "v_fma_f32 v178, v178, v130, v138\n\t" \
  "v_mfma_f32_16x16x32_bf16 a[24:27], a[164:167], v[88:91], a[24:27]\n\t" \
  "ds_read_b64_tr_b16 a[134:135], v185 offset:1024\n\t" \
  "v_fma_f32 v179, v179, v131, v139\n\t" \
  "v_fma_f32 v180, v180, v132, v140\n\t" \
  "v_mfma_f32_16x16x32_bf16 a[20:23], a[164:167], v[84:87], a[20:23]\n\t" \
  "ds_read_b64_tr_b16 a[136:137], v186 offset:0\n\t" \
  "v_fma_f32 v181, v181, v133, v141\n\t" \
  "v_fma_f32 v182, v182, v134, v142\n\t" \
  "v_mfma_f32_16x16x32_bf16 a[16:19], a[164:167], v[80:83], a[16:19]\n\t" \
  "buffer_load_dwordx4 v202, %[sw], s96 offen offset:2048 lds\n\t" \
  "v_fma_f32 v183, v183, v135, v143\n\t" \
  "v_mfma_f32_16x16x32_bf16 a[32:35], a[168:171], v[80:83], a[32:35]\n\t" \
  "ds_read_b64_tr_b16 a[138:139], v186 offset:1024\n\t" \
  "v_cvt_pk_bf16_f32 v108, v176, v177\n\t" \
  "v_cvt_pk_bf16_f32 v109, v178, v179\n\t" \
  "v_mfma_f32_16x16x32_bf16 a[36:39], a[168:171], v[84:87], a[36:39]\n\t" \
  "ds_read_b64_tr_b16 a[140:141], v187 offset:0\n\t" \
  "v_cvt_pk_bf16_f32 v110, v180, v181\n\t" \
  "v_cvt_pk_bf16_f32 v111, v182, v183\n\t" \
  "s_waitcnt lgkmcnt(7)\n\t" \
  "v_mfma_f32_16x16x32_bf16 a[40:43], a[168:171], v[88:91], a[40:43]\n\t" \
  "buffer_load_dwordx4 v203, %[sw], s96 offen offset:3072 lds\n\t" \
  "v_lshlrev_b32 v176, 16, v112\n\t" \
  "v_mfma_f32_16x16x32_bf16 a[44:47], a[168:171], v[92:95], a[44:47]\n\t" \
  "s_add_u32 s97, %[rot19], 2621440\n\t" \
  "ds_read_b64_tr_b16 a[142:143], v187 offset:1024\n\t" \
  "v_and_b32 v177, s98, v112\n\t" \
  "v_lshlrev_b32 v178, 16, v113\n\t" \
  "v_mfma_f32_16x16x32_bf16 a[60:63], a[172:175], v[92:95], a[60:63]\n\t" \
  "s_bfe_u32 s97, s97, 0x140003\n\t" \
  "ds_read_b64_tr_b16 a[144:145], v188 offset:0\n\t" \
  "v_and_b32 v179, s98, v113\n\t" \
  "v_lshlrev_b32 v180, 16, v114\n\t" \
  "v_mfma_f32_16x16x32_bf16 a[56:59], a[172:175], v[88:91], a[56:59]\n\t" \
  "s_add_u32 s97, s97, %[sox]\n\t" \
  "ds_read_b64_tr_b16 a[146:147], v188 offset:1024\n\t" \
  "v_and_b32 v181, s98, v114\n\t" \
  "v_lshlrev_b32 v182, 16, v115\n\t" \
  "v_mfma_f32_16x16x32_bf16 a[52:55], a[172:175], v[84:87], a[52:55]\n\t" \
  "buffer_load_dwordx4 a[224:227], v248, %[ws], s97 offen sc1\n\t" \
  "v_and_b32 v183, s98, v115\n\t" \
  "v_mfma_f32_16x16x32_bf16 a[48:51], a[172:175], v[80:83], a[48:51]\n\t" \
  "ds_read_b64_tr_b16 a[148:149], v189 offset:0\n\t" \
  "v_fma_f32 v176, v176, v160, v162\n\t" \
  "v_fma_f32 v177, v177, v160, v162\n\t" \
  "v_mfma_f32_16x16x32_bf16 a[64:67], a[176:179], v[80:83], a[64:67]\n\t" \
  "ds_read_b64_tr_b16 a[150:151], v189 offset:1024\n\t" \
  "v_fma_f32 v178, v178, v160, v162\n\t" \
  "v_fma_f32 v179, v179, v160, v162\n\t" \
  "v_mfma_f32_16x16x32_bf16 a[68:71], a[176:179], v[84:87], a[68:71]\n\t" \
  "buffer_load_dwordx4 a[228:231], v249, %[ws], s97 offen sc1\n\t" \
  "v_fma_f32 v180, v180, v160, v162\n\t" \
  "v_mfma_f32_16x16x32_bf16 a[72:75], a[176:179], v[88:91], a[72:75]\n\t" \
  "ds_read_b64_tr_b16 a[152:153], v190 offset:0\n\t" \
  "v_fma_f32 v181, v181, v160, v162\n\t" \
  "v_fma_f32 v182, v182, v160, v162\n\t" \
  "v_mfma_f32_16x16x32_bf16 a[76:79], a[176:179], v[92:95], a[76:79]\n\t" \
  "ds_read_b64_tr_b16 a[154:155], v190 offset:1024\n\t" \
  "v_fma_f32 v183, v183, v160, v162\n\t" \
  "v_fma_f32 v176, v176, v144, v152\n\t" \
  "v_mfma_f32_16x16x32_bf16 a[92:95], a[180:183], v[92:95], a[92:95]\n\t" \
  "ds_read_b64_tr_b16 a[156:157], v191 offset:0\n\t" \
  "v_fma_f32 v177, v177, v145, v153\n\t" \
  "v_fma_f32 v178, v178, v146, v154\n\t" \
  "v_mfma_f32_16x16x32_bf16 a[88:91], a[180:183], v[88:91], a[88:91]\n\t" \
  "buffer_load_dwordx4 a[232:235], v250, %[ws], s97 offen sc1\n\t" \
  "v_fma_f32 v179, v179, v147, v155\n\t" \
  "v_mfma_f32_16x16x32_bf16 a[84:87], a[180:183], v[84:87], a[84:87]\n\t" \
  "ds_read_b64_tr_b16 a[158:159], v191 offset:1024\n\t" \
  "v_fma_f32 v180, v180, v148, v156\n\t" \
  "v_fma_f32 v181, v181, v149, v157\n\t" \
  "v_mfma_f32_16x16x32_bf16 a[80:83], a[180:183], v[80:83], a[80:83]\n\t" \
  "v_fma_f32 v182, v182, v150, v158\n\t" \
  "v_fma_f32 v183, v183, v151, v159\n\t" \
  "v_cvt_pk_bf16_f32 v112, v176, v177\n\t" \
  "v_mfma_f32_16x16x32_bf16 a[96:99], a[184:187], v[80:83], a[96:99]\n\t" \
  "buffer_load_dwordx4 a[236:239], v251, %[ws], s97 offen sc1\n\t" \
  "v_cvt_pk_bf16_f32 v113, v178, v179\n\t" \
  "v_mfma_f32_16x16x32_bf16 a[100:103], a[184:187], v[84:87], a[100:103]\n\t" \
  "v_cvt_pk_bf16_f32 v114, v180, v181\n\t" \
  "v_cvt_pk_bf16_f32 v115, v182, v183\n\t" \
  "v_lshlrev_b32 v176, 16, v116\n\t" \
  "v_mfma_f32_16x16x32_bf16 a[104:107], a[184:187], v[88:91], a[104:107]\n\t" \
  "v_and_b32 v177, s98, v116\n\t" \
  "v_lshlrev_b32 v178, 16, v117\n\t" \
  "v_and_b32 v179, s98, v117\n\t" \
  "v_mfma_f32_16x16x32_bf16 a[108:111], a[184:187], v[92:95], a[108:111]\n\t" \
  "v_lshlrev_b32 v180, 16, v118\n\t" \
  "v_and_b32 v181, s98, v118\n\t" \
  "v_lshlrev_b32 v182, 16, v119\n\t" \
  "v_mfma_f32_16x16x32_bf16 a[124:127], a[188:191], v[92:95], a[124:127]\n\t" \
  "v_and_b32 v183, s98, v119\n\t" \
  "v_fma_f32 v176, v176, v164, v166\n\t" \
  "v_fma_f32 v177, v177, v164, v166\n\t" \
  "v_mfma_f32_16x16x32_bf16 a[120:123], a[188:191], v[88:91], a[120:123]\n\t" \
  "v_fma_f32 v178, v178, v164, v166\n\t" \
  "v_fma_f32 v179, v179, v164, v166\n\t" \
  "v_fma_f32 v180, v180, v164, v166\n\t" \
  "v_mfma_f32_16x16x32_bf16 a[116:119], a[188:191], v[84:87], a[116:119]\n\t" \
  "v_fma_f32 v181, v181, v164, v166\n\t" \
  "v_fma_f32 v182, v182, v164, v166\n\t" \
  "v_fma_f32 v183, v183, v164, v166\n\t" \
  "v_fma_f32 v176, v176, v144, v152\n\t" \
  "v_mfma_f32_16x16x32_bf16 a[112:115], a[188:191], v[80:83], a[112:115]\n\t" \
  FSTP("v_writelane_b32 %[stv], s94, 46\n\t") \
  FSTP("v_writelane_b32 %[stv], s92, 47\n\t") \
  FSTP("s_memrealtime s[94:95]\n\t") \
  FSTP("s_memtime s[92:93]\n\t") \
  "s_waitcnt lgkmcnt(0)\n\t" \
  "v_mfma_f32_16x16x32_bf16 a[0:3], a[128:131], v[96:99], a[0:3]\n\t" \
  "s_add_u32 s96, %[rot19], 3145728\n\t" \
  "ds_read_b64_tr_b16 a[160:161], v184 offset:8192\n\t" \
  "v_fma_f32 v177, v177, v145, v153\n\t" \
  "v_fma_f32 v178, v178, v146, v154\n\t" \
  "v_fma_f32 v179, v179, v147, v155\n\t" \
  "v_mfma_f32_16x16x32_bf16 a[4:7], a[128:131], v[100:103], a[4:7]\n\t" \
  "s_and_b32 s96, s96, 0x780000\n\t" \
  "ds_read_b64_tr_b16 a[162:163], v184 offset:9216\n\t" \
  "v_fma_f32 v180, v180, v148, v156\n\t" \
  "v_fma_f32 v181, v181, v149, v157\n\t" \
  "v_fma_f32 v182, v182, v150, v158\n\t" \
  "v_mfma_f32_16x16x32_bf16 a[8:11], a[128:131], v[104:107], a[8:11]\n\t" \
  "s_add_u32 m0, %[mw], 16384\n\t" \
  "ds_read_b64_tr_b16 a[164:165], v185 offset:8192\n\t" \
  "v_fma_f32 v183, v183, v151, v159\n\t" \
  "v_cvt_pk_bf16_f32 v116, v176, v177\n\t" \
  "v_cvt_pk_bf16_f32 v117, v178, v179\n\t" \
  "v_mfma_f32_16x16x32_bf16 a[12:15], a[128:131], v[108:111], a[12:15]\n\t" \
  "buffer_load_dwordx4 v200, %[sw], s96 offen offset:0 lds\n\t" \
  "v_cvt_pk_bf16_f32 v118, v180, v181\n\t" \
  "v_mfma_f32_16x16x32_bf16 a[28:31], a[132:135], v[108:111], a[28:31]\n\t" \
  "ds_read_b64_tr_b16 a[166:167], v185 offset:9216\n\t" \
  "v_cvt_pk_bf16_f32 v119, v182, v183\n\t" \
  "v_lshlrev_b32 v176, 16, v120\n\t" \
  "v_mfma_f32_16x16x32_bf16 a[24:27], a[132:135], v[104:107], a[24:27]\n\t" \
  "ds_read_b64_tr_b16 a[168:169], v186 offset:8192\n\t" \
  "v_and_b32 v177, s98, v120\n\t" \
  "v_lshlrev_b32 v178, 16, v121\n\t" \
  "v_mfma_f32_16x16x32_bf16 a[20:23], a[132:135], v[100:103], a[20:23]\n\t" \
  "ds_read_b64_tr_b16 a[170:171], v186 offset:9216\n\t" \
  "v_and_b32 v179, s98, v121\n\t" \
  "v_lshlrev_b32 v180, 16, v122\n\t" \
  "v_mfma_f32_16x16x32_bf16 a[16:19], a[132:135], v[96:99], a[16:19]\n\t" \
  "buffer_load_dwordx4 v201, %[sw], s96 offen offset:1024 lds\n\t" \
  "v_and_b32 v181, s98, v122\n\t" \
  "v_mfma_f32_16x16x32_bf16 a[32:35], a[136:139], v[96:99], a[32:35]\n\t" \
  "ds_read_b64_tr_b16 a[172:173], v187 offset:8192\n\t" \
  "v_lshlrev_b32 v182, 16, v123\n\t" \
  "v_and_b32 v183, s98, v123\n\t" \
  "v_mfma_f32_16x16x32_bf16 a[36:39], a[136:139], v[100:103], a[36:39]\n\t" \
  "ds_read_b64_tr_b16 a[174:175], v187 offset:9216\n\t" \
  "v_fma_f32 v176, v176, v168, v170\n\t" \
  "v_fma_f32 v177, v177, v168, v170\n\t" \
  "v_mfma_f32_16x16x32_bf16 a[40:43], a[136:139], v[104:107], a[40:43]\n\t" \
  "ds_read_b64_tr_b16 a[176:177], v188 offset:8192\n\t" \
  "v_fma_f32 v178, v178, v168, v170\n\t" \
  "v_fma_f32 v179, v179, v168, v170\n\t" \
  "v_mfma_f32_16x16x32_bf16 a[44:47], a[136:139], v[108:111], a[44:47]\n\t" \
  "ds_read_b64_tr_b16 a[178:179], v188 offset:9216\n\t" \
  "v_fma_f32 v180, v180, v168, v170\n\t" \
  "v_fma_f32 v181, v181, v168, v170\n\t" \
  "v_mfma_f32_16x16x32_bf16 a[60:63], a[140:143], v[108:111], a[60:63]\n\t" \
  "ds_read_b64_tr_b16 a[180:181], v189 offset:8192\n\t" \
  "v_fma_f32 v182, v182, v168, v170\n\t" \
  "v_fma_f32 v183, v183, v168, v170\n\t" \
  "v_mfma_f32_16x16x32_bf16 a[56:59], a[140:143], v[104:107], a[56:59]\n\t" \
  "ds_read_b64_tr_b16 a[182:183], v189 offset:9216\n\t" \
  "v_fma_f32 v176, v176, v144, v152\n\t" \
  "v_fma_f32 v177, v177, v145, v153\n\t" \
  "v_mfma_f32_16x16x32_bf16 a[52:55], a[140:143], v[100:103], a[52:55]\n\t" \
  "buffer_load_dwordx4 v202, %[sw], s96 offen offset:2048 lds\n\t" \
  "v_fma_f32 v178, v178, v146, v154\n\t" \
  "v_mfma_f32_16x16x32_bf16 a[48:51], a[140:143], v[96:99], a[48:51]\n\t" \
  "ds_read_b64_tr_b16 a[184:185], v190 offset:8192\n\t" \
  "v_fma_f32 v179, v179, v147, v155\n\t" \
  "v_fma_f32 v180, v180, v148, v156\n\t" \
  "v_mfma_f32_16x16x32_bf16 a[64:67], a[144:147], v[96:99], a[64:67]\n\t" \
  "ds_read_b64_tr_b16 a[186:187], v190 offset:9216\n\t" \
  "v_fma_f32 v181, v181, v149, v157\n\t" \
  "v_fma_f32 v182, v182, v150, v158\n\t" \
  "v_mfma_f32_16x16x32_bf16 a[68:71], a[144:147], v[100:103], a[68:71]\n\t" \
  "ds_read_b64_tr_b16 a[188:189], v191 offset:8192\n\t" \
  "v_fma_f32 v183, v183, v151, v159\n\t" \
  "v_cvt_pk_bf16_f32 v120, v176, v177\n\t" \
  "v_mfma_f32_16x16x32_bf16 a[72:75], a[144:147], v[104:107], a[72:75]\n\t" \
  "ds_read_b64_tr_b16 a[190:191], v191 offset:9216\n\t" \
  "v_cvt_pk_bf16_f32 v121, v178, v179\n\t" \
  "v_cvt_pk_bf16_f32 v122, v180, v181\n\t" \
  "v_mfma_f32_16x16x32_bf16 a[76:79], a[144:147], v[108:111], a[76:79]\n\t" \
  "v_cvt_pk_bf16_f32 v123, v182, v183\n\t" \
  "v_lshlrev_b32 v176, 16, v124\n\t" \
  "v_and_b32 v177, s98, v124\n\t" \
  "v_mfma_f32_16x16x32_bf16 a[92:95], a[148:151], v[108:111], a[92:95]\n\t" \
  "v_lshlrev_b32 v178, 16, v125\n\t" \
  "v_and_b32 v179, s98, v125\n\t" \
  "v_lshlrev_b32 v180, 16, v126\n\t" \
  "v_mfma_f32_16x16x32_bf16 a[88:91], a[148:151], v[104:107], a[88:91]\n\t" \
  "buffer_load_dwordx4 v203, %[sw], s96 offen offset:3072 lds\n\t" \
  "v_and_b32 v181, s98, v126\n\t" \
  "v_mfma_f32_16x16x32_bf16 a[84:87], a[148:151], v[100:103], a[84:87]\n\t" \
  "v_lshlrev_b32 v182, 16, v127\n\t" \
  "v_and_b32 v183, s98, v127\n\t" \
  "v_fma_f32 v176, v176, v172, v174\n\t" \
  "v_mfma_f32_16x16x32_bf16 a[80:83], a[148:151], v[96:99], a[80:83]\n\t" \
  "v_fma_f32 v177, v177, v172, v174\n\t" \
  "v_fma_f32 v178, v178, v172, v174\n\t" \
  "v_fma_f32 v179, v179, v172, v174\n\t" \
  "v_mfma_f32_16x16x32_bf16 a[96:99], a[152:155], v[96:99], a[96:99]\n\t" \
  "v_mbcnt_lo_u32_b32 v217, -1, 0\n\t" \
  "v_mbcnt_hi_u32_b32 v217, -1, v217\n\t" \
  "v_cmp_gt_u32 vcc, 16, v217\n\t" \
  "v_mfma_f32_16x16x32_bf16 a[100:103], a[152:155], v[100:103], a[100:103]\n\t" \
  "v_lshlrev_b32 v217, 7, v217\n\t" \
  "v_bfrev_b32 v215, 1\n\t" \
  "v_cndmask_b32 v217, v215, v217, vcc\n\t" \
  "v_mfma_f32_16x16x32_bf16 a[104:107], a[152:155], v[104:107], a[104:107]\n\t" \
  "v_fma_f32 v180, v180, v172, v174\n\t" \
  "v_fma_f32 v181, v181, v172, v174\n\t" \
  "v_fma_f32 v182, v182, v172, v174\n\t" \
  "v_mfma_f32_16x16x32_bf16 a[108:111], a[152:155], v[108:111], a[108:111]\n\t" \
  "v_fma_f32 v183, v183, v172, v174\n\t" \
  "v_fma_f32 v176, v176, v144, v152\n\t" \
  "v_fma_f32 v177, v177, v145, v153\n\t" \
  "v_mfma_f32_16x16x32_bf16 a[124:127], a[156:159], v[108:111], a[124:127]\n\t" \
  "v_fma_f32 v178, v178, v146, v154\n\t" \
  "v_fma_f32 v179, v179, v147, v155\n\t" \
  "v_fma_f32 v180, v180, v148, v156\n\t" \
  "v_mfma_f32_16x16x32_bf16 a[120:123], a[156:159], v[104:107], a[120:123]\n\t" \
  "v_fma_f32 v181, v181, v149, v157\n\t" \
  "v_fma_f32 v182, v182, v150, v158\n\t" \
  "v_fma_f32 v183, v183, v151, v159\n\t" \
  "v_mfma_f32_16x16x32_bf16 a[116:119], a[156:159], v[100:103], a[116:119]\n\t" \
  "v_cvt_pk_bf16_f32 v124, v176, v177\n\t" \
  "v_cvt_pk_bf16_f32 v125, v178, v179\n\t" \
  "v_cvt_pk_bf16_f32 v126, v180, v181\n\t" \
  "v_cvt_pk_bf16_f32 v127, v182, v183\n\t" \
  "v_mfma_f32_16x16x32_bf16 a[112:115], a[156:159], v[96:99], a[112:115]\n\t" \
  FSTP("v_writelane_b32 %[stv], s94, 48\n\t") \
  FSTP("v_writelane_b32 %[stv], s92, 49\n\t") \
  FSTP("s_memrealtime s[94:95]\n\t") \
  FSTP("s_memtime s[92:93]\n\t") \
  "s_waitcnt vmcnt(21) lgkmcnt(0)\n\t" \
  "s_barrier\n\t" \
  "v_mfma_f32_16x16x32_bf16 a[0:3], a[160:163], v[112:115], a[0:3]\n\t" \
  "s_add_u32 s97, s97, %[dk]\n\t" \
  "ds_read_b64_tr_b16 a[128:129], v184 offset:49152\n\t" \
  "v_mfma_f32_16x16x32_bf16 a[4:7], a[160:163], v[116:119], a[4:7]\n\t" \
  "buffer_load_dwordx4 a[240:243], v248, %[ws], s97 offen sc1\n\t" \
  "v_mfma_f32_16x16x32_bf16 a[8:11], a[160:163], v[120:123], a[8:11]\n\t" \
  "ds_read_b64_tr_b16 a[130:131], v184 offset:50176\n\t" \
  "v_mfma_f32_16x16x32_bf16 a[12:15], a[160:163], v[124:127], a[12:15]\n\t" \
  "buffer_load_dwordx4 a[244:247], v249, %[ws], s97 offen sc1\n\t" \
  "v_mfma_f32_16x16x32_bf16 a[28:31], a[164:167], v[124:127], a[28:31]\n\t" \
  "ds_read_b64_tr_b16 a[132:133], v185 offset:49152\n\t" \
  "v_mfma_f32_16x16x32_bf16 a[24:27], a[164:167], v[120:123], a[24:27]\n\t" \
  "ds_read_b64_tr_b16 a[134:135], v185 offset:50176\n\t" \
  "v_mfma_f32_16x16x32_bf16 a[20:23], a[164:167], v[116:119], a[20:23]\n\t" \
  "buffer_load_dwordx4 a[248:251], v250, %[ws], s97 offen sc1\n\t" \
  "v_mfma_f32_16x16x32_bf16 a[16:19], a[164:167], v[112:115], a[16:19]\n\t" \
  "ds_read_b64_tr_b16 a[136:137], v186 offset:49152\n\t" \
  "v_mfma_f32_16x16x32_bf16 a[32:35], a[168:171], v[112:115], a[32:35]\n\t" \
  "ds_read_b64_tr_b16 a[138:139], v186 offset:50176\n\t" \
  "v_mfma_f32_16x16x32_bf16 a[36:39], a[168:171], v[116:119], a[36:39]\n\t" \
  "buffer_load_dwordx4 a[252:255], v251, %[ws], s97 offen sc1\n\t" \
  "v_mfma_f32_16x16x32_bf16 a[40:43], a[168:171], v[120:123], a[40:43]\n\t" \
  "s_add_u32 s96, %[rot19], 3670016\n\t" \
  "ds_read_b64_tr_b16 a[140:141], v187 offset:49152\n\t" \
  "v_mfma_f32_16x16x32_bf16 a[44:47], a[168:171], v[124:127], a[44:47]\n\t" \
  "s_and_b32 s96, s96, 0x780000\n\t" \
  "ds_read_b64_tr_b16 a[142:143], v187 offset:50176\n\t" \
  "v_mfma_f32_16x16x32_bf16 a[60:63], a[172:175], v[124:127], a[60:63]\n\t" \
  "s_add_u32 m0, %[mw], 32768\n\t" \
  "ds_read_b64_tr_b16 a[144:145], v188 offset:49152\n\t" \
  "v_mfma_f32_16x16x32_bf16 a[56:59], a[172:175], v[120:123], a[56:59]\n\t" \
  "buffer_load_dwordx4 v200, %[sw], s96 offen offset:0 lds\n\t" \
  "v_mfma_f32_16x16x32_bf16 a[52:55], a[172:175], v[116:119], a[52:55]\n\t" \
  "ds_read_b64_tr_b16 a[146:147], v188 offset:50176\n\t" \
  "v_mfma_f32_16x16x32_bf16 a[48:51], a[172:175], v[112:115], a[48:51]\n\t" \
  "ds_read_b64_tr_b16 a[148:149], v189 offset:49152\n\t" \
  "v_mfma_f32_16x16x32_bf16 a[64:67], a[176:179], v[112:115], a[64:67]\n\t" \
  "ds_read_b64_tr_b16 a[150:151], v189 offset:50176\n\t" \
  "v_mfma_f32_16x16x32_bf16 a[68:71], a[176:179], v[116:119], a[68:71]\n\t" \
  "buffer_load_dwordx4 v201, %[sw], s96 offen offset:1024 lds\n\t" \
  "v_mfma_f32_16x16x32_bf16 a[72:75], a[176:179], v[120:123], a[72:75]\n\t" \
  "ds_read_b64_tr_b16 a[152:153], v190 offset:49152\n\t" \
  "v_mfma_f32_16x16x32_bf16 a[76:79], a[176:179], v[124:127], a[76:79]\n\t" \
  "ds_read_b64_tr_b16 a[154:155], v190 offset:50176\n\t" \
  "v_mfma_f32_16x16x32_bf16 a[92:95], a[180:183], v[124:127], a[92:95]\n\t" \
  "ds_read_b64_tr_b16 a[156:157], v191 offset:49152\n\t" \
  "v_mfma_f32_16x16x32_bf16 a[88:91], a[180:183], v[120:123], a[88:91]\n\t" \
  "buffer_load_dwordx4 v202, %[sw], s96 offen offset:2048 lds\n\t" \
  "v_mfma_f32_16x16x32_bf16 a[84:87], a[180:183], v[116:119], a[84:87]\n\t" \
  "ds_read_b64_tr_b16 a[158:159], v191 offset:50176\n\t" \
  "v_mfma_f32_16x16x32_bf16 a[80:83], a[180:183], v[112:115], a[80:83]\n\t" \
  "v_mfma_f32_16x16x32_bf16 a[96:99], a[184:187], v[112:115], a[96:99]\n\t" \
  "v_mfma_f32_16x16x32_bf16 a[100:103], a[184:187], v[116:119], a[100:103]\n\t" \
  "buffer_load_dwordx4 v203, %[sw], s96 offen offset:3072 lds\n\t" \
  "v_mfma_f32_16x16x32_bf16 a[104:107], a[184:187], v[120:123], a[104:107]\n\t" \
  "s_add_u32 s99, %[pf1], 0\n\t" \
  "v_mfma_f32_16x16x32_bf16 a[108:111], a[184:187], v[124:127], a[108:111]\n\t" \
  "buffer_load_dword %[pf], v217, %[wo], s99 offen\n\t" \
  "v_mfma_f32_16x16x32_bf16 a[124:127], a[188:191], v[124:127], a[124:127]\n\t" \
  "v_mfma_f32_16x16x32_bf16 a[120:123], a[188:191], v[120:123], a[120:123]\n\t" \
  "v_mfma_f32_16x16x32_bf16 a[116:119], a[188:191], v[116:119], a[116:119]\n\t" \
  "v_mfma_f32_16x16x32_bf16 a[112:115], a[188:191], v[112:115], a[112:115]\n\t" \
  FSTP("v_writelane_b32 %[stv], s94, 50\n\t") \
  FSTP("v_writelane_b32 %[stv], s92, 51\n\t") \
  FSTP("s_memrealtime s[94:95]\n\t") \
  FSTP("s_memtime s[92:93]\n\t") \
  FSTS("s_memrealtime s[94:95]\n\t") \
  FSTS("s_waitcnt lgkmcnt(0)\n\t") \
  FSTS("v_writelane_b32 %[stv], s94, 14\n\t") \
  "s_waitcnt vmcnt(25) lgkmcnt(0)\n\t" \
  "v_mfma_f32_16x16x32_bf16 a[0:3], a[128:131], a[192:195], a[0:3]\n\t" \
  "s_add_u32 s96, %[rot19], 4194304\n\t" \
  "ds_read_b64_tr_b16 a[160:161], v184 offset:57344\n\t" \
  "v_mfma_f32_16x16x32_bf16 a[4:7], a[128:131], a[196:199], a[4:7]\n\t" \
  "s_and_b32 s96, s96, 0x780000\n\t" \
  "v_mfma_f32_16x16x32_bf16 a[8:11], a[128:131], a[200:203], a[8:11]\n\t" \
  "s_add_u32 m0, %[mw], 0\n\t" \
  "ds_read_b64_tr_b16 a[162:163], v184 offset:58368\n\t" \
  "v_mfma_f32_16x16x32_bf16 a[12:15], a[128:131], a[204:207], a[12:15]\n\t" \
  "buffer_load_dwordx4 v200, %[sw], s96 offen offset:0 lds\n\t" \
  "v_mfma_f32_16x16x32_bf16 a[28:31], a[132:135], a[204:207], a[28:31]\n\t" \
  "ds_read_b64_tr_b16 a[164:165], v185 offset:57344\n\t" \
  "v_mfma_f32_16x16x32_bf16 a[24:27], a[132:135], a[200:203], a[24:27]\n\t" \
  "ds_read_b64_tr_b16 a[166:167], v185 offset:58368\n\t" \
  "v_mfma_f32_16x16x32_bf16 a[20:23], a[132:135], a[196:199], a[20:23]\n\t" \
  "ds_read_b64_tr_b16 a[168:169], v186 offset:57344\n\t" \
  "v_mfma_f32_16x16x32_bf16 a[16:19], a[132:135], a[192:195], a[16:19]\n\t" \
  "buffer_load_dwordx4 v201, %[sw], s96 offen offset:1024 lds\n\t" \
  "v_mfma_f32_16x16x32_bf16 a[32:35], a[136:139], a[192:195], a[32:35]\n\t" \
  "ds_read_b64_tr_b16 a[170:171], v186 offset:58368\n\t" \
  "v_accvgpr_read_b32 v0, a0\n\t" \
  "v_accvgpr_read_b32 v1, a1\n\t" \
  "v_mfma_f32_16x16x32_bf16 a[36:39], a[136:139], a[196:199], a[36:39]\n\t" \
  "ds_read_b64_tr_b16 a[172:173], v187 offset:57344\n\t" \
  "v_accvgpr_read_b32 v2, a2\n\t" \
  "v_mfma_f32_16x16x32_bf16 a[40:43], a[136:139], a[200:203], a[40:43]\n\t" \
  "v_accvgpr_read_b32 v3, a3\n\t" \
  "v_accvgpr_read_b32 v32, a4\n\t" \
  "v_mfma_f32_16x16x32_bf16 a[44:47], a[136:139], a[204:207], a[44:47]\n\t" \
  "ds_read_b64_tr_b16 a[174:175], v187 offset:58368\n\t" \
  "v_accvgpr_read_b32 v33, a5\n\t" \
  "v_mfma_f32_16x16x32_bf16 a[60:63], a[140:143], a[204:207], a[60:63]\n\t" \
  "ds_read_b64_tr_b16 a[176:177], v188 offset:57344\n\t" \
  "v_accvgpr_read_b32 v34, a6\n\t" \
  "v_mfma_f32_16x16x32_bf16 a[56:59], a[140:143], a[200:203], a[56:59]\n\t" \
  "v_accvgpr_read_b32 v35, a7\n\t" \
  "v_accvgpr_read_b32 v64, a8\n\t" \
  "v_mfma_f32_16x16x32_bf16 a[52:55], a[140:143], a[196:199], a[52:55]\n\t" \
  "buffer_load_dwordx4 v202, %[sw], s96 offen offset:2048 lds\n\t" \
  "v_mfma_f32_16x16x32_bf16 a[48:51], a[140:143], a[192:195], a[48:51]\n\t" \
  "ds_read_b64_tr_b16 a[178:179], v188 offset:58368\n\t" \
  "v_accvgpr_read_b32 v65, a9\n\t" \
  "v_mfma_f32_16x16x32_bf16 a[64:67], a[144:147], a[192:195], a[64:67]\n\t" \
  "ds_read_b64_tr_b16 a[180:181], v189 offset:57344\n\t" \
  "v_accvgpr_read_b32 v66, a10\n\t" \
  "v_mfma_f32_16x16x32_bf16 a[68:71], a[144:147], a[196:199], a[68:71]\n\t" \
  "ds_read_b64_tr_b16 a[182:183], v189 offset:58368\n\t" \
  "v_accvgpr_read_b32 v67, a11\n\t" \
  "v_mfma_f32_16x16x32_bf16 a[72:75], a[144:147], a[200:203], a[72:75]\n\t" \
  "ds_read_b64_tr_b16 a[184:185], v190 offset:57344\n\t" \
  "v_accvgpr_read_b32 v96, a12\n\t" \
  "v_mfma_f32_16x16x32_bf16 a[76:79], a[144:147], a[204:207], a[76:79]\n\t" \
  "v_accvgpr_read_b32 v97, a13\n\t" \
  "v_accvgpr_read_b32 v98, a14\n\t" \
  "v_mfma_f32_16x16x32_bf16 a[92:95], a[148:151], a[204:207], a[92:95]\n\t" \
  "ds_read_b64_tr_b16 a[186:187], v190 offset:58368\n\t" \
  "v_accvgpr_read_b32 v99, a15\n\t" \
  "v_mfma_f32_16x16x32_bf16 a[88:91], a[148:151], a[200:203], a[88:91]\n\t" \
  "buffer_load_dwordx4 v203, %[sw], s96 offen offset:3072 lds\n\t" \
  "v_mfma_f32_16x16x32_bf16 a[84:87], a[148:151], a[196:199], a[84:87]\n\t" \
  "ds_read_b64_tr_b16 a[188:189], v191 offset:57344\n\t" \
  "v_accvgpr_read_b32 v100, a28\n\t" \
  "v_mfma_f32_16x16x32_bf16 a[80:83], a[148:151], a[192:195], a[80:83]\n\t" \
  "ds_read_b64_tr_b16 a[190:191], v191 offset:58368\n\t" \
  "v_accvgpr_read_b32 v101, a29\n\t" \
  "v_mfma_f32_16x16x32_bf16 a[96:99], a[152:155], a[192:195], a[96:99]\n\t" \
  "v_accvgpr_read_b32 v102, a30\n\t" \
  "v_accvgpr_read_b32 v103, a31\n\t" \
  "v_mfma_f32_16x16x32_bf16 a[100:103], a[152:155], a[196:199], a[100:103]\n\t" \
  "v_accvgpr_read_b32 v68, a24\n\t" \
  "v_accvgpr_read_b32 v69, a25\n\t" \
  "v_mfma_f32_16x16x32_bf16 a[104:107], a[152:155], a[200:203], a[104:107]\n\t" \
  "v_accvgpr_read_b32 v70, a26\n\t" \
  "v_accvgpr_read_b32 v71, a27\n\t" \
  "v_mfma_f32_16x16x32_bf16 a[108:111], a[152:155], a[204:207], a[108:111]\n\t" \
  "v_accvgpr_read_b32 v36, a20\n\t" \
  "v_accvgpr_read_b32 v37, a21\n\t" \
  "v_mfma_f32_16x16x32_bf16 a[124:127], a[156:159], a[204:207], a[124:127]\n\t" \
  "v_accvgpr_read_b32 v38, a22\n\t" \
  "v_accvgpr_read_b32 v39, a23\n\t" \
  "v_mfma_f32_16x16x32_bf16 a[120:123], a[156:159], a[200:203], a[120:123]\n\t" \
  "v_accvgpr_read_b32 v4, a16\n\t" \
  "v_accvgpr_read_b32 v5, a17\n\t" \
  "v_mfma_f32_16x16x32_bf16 a[116:119], a[156:159], a[196:199], a[116:119]\n\t" \
  "v_accvgpr_read_b32 v6, a18\n\t" \
  "v_accvgpr_read_b32 v7, a19\n\t" \
  "v_mfma_f32_16x16x32_bf16 a[112:115], a[156:159], a[192:195], a[112:115]\n\t" \
  "s_waitcnt vmcnt(21) lgkmcnt(0)\n\t" \
  "s_barrier\n\t" \
  "v_mfma_f32_16x16x32_bf16 a[32:35], a[168:171], a[208:211], a[32:35]\n\t" \
  "s_add_u32 s97, %[rot19], 3145728\n\t" \
  "ds_read_b64_tr_b16 a[128:129], v192 offset:0\n\t" \
  "v_mfma_f32_16x16x32_bf16 a[36:39], a[168:171], a[212:215], a[36:39]\n\t" \
  "s_bfe_u32 s97, s97, 0x140003\n\t" \
  "v_mfma_f32_16x16x32_bf16 a[40:43], a[168:171], a[216:219], a[40:43]\n\t" \
  "s_add_u32 s97, s97, %[sox]\n\t" \
  "ds_read_b64_tr_b16 a[130:131], v192 offset:1024\n\t" \
  "v_mfma_f32_16x16x32_bf16 a[44:47], a[168:171], a[220:223], a[44:47]\n\t" \
  "buffer_load_dwordx4 a[192:195], v248, %[ws], s97 offen sc1\n\t" \
  "v_mfma_f32_16x16x32_bf16 a[60:63], a[172:175], a[220:223], a[60:63]\n\t" \
  "buffer_load_dwordx4 a[196:199], v249, %[ws], s97 offen sc1\n\t" \
  "v_mfma_f32_16x16x32_bf16 a[56:59], a[172:175], a[216:219], a[56:59]\n\t" \
  "ds_read_b64_tr_b16 a[132:133], v193 offset:0\n\t" \
  "v_mfma_f32_16x16x32_bf16 a[52:55], a[172:175], a[212:215], a[52:55]\n\t" \
  "ds_read_b64_tr_b16 a[134:135], v193 offset:1024\n\t" \
  "v_mfma_f32_16x16x32_bf16 a[48:51], a[172:175], a[208:211], a[48:51]\n\t" \
  "buffer_load_dwordx4 a[200:203], v250, %[ws], s97 offen sc1\n\t" \
  "v_mfma_f32_16x16x32_bf16 a[64:67], a[176:179], a[208:211], a[64:67]\n\t" \
  "ds_read_b64_tr_b16 a[136:137], v194 offset:0\n\t" \
  "v_accvgpr_read_b32 v8, a32\n\t" \
  "v_accvgpr_read_b32 v9, a33\n\t" \
  "v_mfma_f32_16x16x32_bf16 a[68:71], a[176:179], a[212:215], a[68:71]\n\t" \
  "ds_read_b64_tr_b16 a[138:139], v194 offset:1024\n\t" \
  "v_accvgpr_read_b32 v10, a34\n\t" \
  "v_accvgpr_read_b32 v11, a35\n\t" \
  "v_mfma_f32_16x16x32_bf16 a[72:75], a[176:179], a[216:219], a[72:75]\n\t" \
  "ds_read_b64_tr_b16 a[140:141], v195 offset:0\n\t" \
  "v_accvgpr_read_b32 v40, a36\n\t" \
  "v_accvgpr_read_b32 v41, a37\n\t" \
  "v_mfma_f32_16x16x32_bf16 a[76:79], a[176:179], a[220:223], a[76:79]\n\t" \
  "buffer_load_dwordx4 a[204:207], v251, %[ws], s97 offen sc1\n\t" \
  "v_accvgpr_read_b32 v42, a38\n\t" \
  "v_mfma_f32_16x16x32_bf16 a[92:95], a[180:183], a[220:223], a[92:95]\n\t" \
  "s_add_u32 s96, %[rot19], 4718592\n\t" \
  "ds_read_b64_tr_b16 a[142:143], v195 offset:1024\n\t" \
  "v_accvgpr_read_b32 v43, a39\n\t" \
  "v_accvgpr_read_b32 v72, a40\n\t" \
  "v_mfma_f32_16x16x32_bf16 a[88:91], a[180:183], a[216:219], a[88:91]\n\t" \
  "s_and_b32 s96, s96, 0x780000\n\t" \
  "ds_read_b64_tr_b16 a[144:145], v196 offset:0\n\t" \
  "v_accvgpr_read_b32 v73, a41\n\t" \
  ".Lhs2:\n\ts_cbranch_execz .Lhs2n\n\t" \
  "v_accvgpr_read_b32 v74, a42\n\t" \
  "v_mfma_f32_16x16x32_bf16 a[84:87], a[180:183], a[212:215], a[84:87]\n\t" \
  "s_add_u32 m0, %[mw], 49152\n\t" \
  "ds_read_b64_tr_b16 a[146:147], v196 offset:1024\n\t" \
  "v_accvgpr_read_b32 v75, a43\n\t" \
  "v_accvgpr_read_b32 v104, a44\n\t" \
  "v_mfma_f32_16x16x32_bf16 a[80:83], a[180:183], a[208:211], a[80:83]\n\t" \
  "buffer_load_dwordx4 v200, %[sw], s96 offen offset:0 lds\n\t" \
  "v_accvgpr_read_b32 v105, a45\n\t" \
  "v_mfma_f32_16x16x32_bf16 a[96:99], a[184:187], a[208:211], a[96:99]\n\t" \
  "ds_read_b64_tr_b16 a[148:149], v197 offset:0\n\t" \
  "v_accvgpr_read_b32 v106, a46\n\t" \
  "v_mfma_f32_16x16x32_bf16 a[100:103], a[184:187], a[212:215], a[100:103]\n\t" \
  "ds_read_b64_tr_b16 a[150:151], v197 offset:1024\n\t" \
  "v_accvgpr_read_b32 v107, a47\n\t" \
  "v_mfma_f32_16x16x32_bf16 a[104:107], a[184:187], a[216:219], a[104:107]\n\t" \
  "ds_read_b64_tr_b16 a[152:153], v198 offset:0\n\t" \
  "v_accvgpr_read_b32 v108, a60\n\t" \
  "v_mfma_f32_16x16x32_bf16 a[108:111], a[184:187], a[220:223], a[108:111]\n\t" \
  "buffer_load_dwordx4 v201, %[sw], s96 offen offset:1024 lds\n\t" \
  "v_mfma_f32_16x16x32_bf16 a[124:127], a[188:191], a[220:223], a[124:127]\n\t" \
  "ds_read_b64_tr_b16 a[154:155], v198 offset:1024\n\t" \
  "v_accvgpr_read_b32 v109, a61\n\t" \
  "v_mfma_f32_16x16x32_bf16 a[120:123], a[188:191], a[216:219], a[120:123]\n\t" \
  "ds_read_b64_tr_b16 a[156:157], v199 offset:0\n\t" \
  "v_accvgpr_read_b32 v110, a62\n\t" \
  "v_mfma_f32_16x16x32_bf16 a[116:119], a[188:191], a[212:215], a[116:119]\n\t" \
  "v_accvgpr_read_b32 v111, a63\n\t" \
  "v_accvgpr_read_b32 v76, a56\n\t" \
  "v_mfma_f32_16x16x32_bf16 a[112:115], a[188:191], a[208:211], a[112:115]\n\t" \
  "buffer_load_dwordx4 v202, %[sw], s96 offen offset:2048 lds\n\t" \
  "v_mfma_f32_16x16x32_bf16 v[0:3], a[160:163], a[208:211], v[0:3]\n\t" \
  "ds_read_b64_tr_b16 a[158:159], v199 offset:1024\n\t" \
  "v_accvgpr_read_b32 v77, a57\n\t" \
  "v_mfma_f32_16x16x32_bf16 v[32:35], a[160:163], a[212:215], v[32:35]\n\t" \
  "v_accvgpr_read_b32 v78, a58\n\t" \
  "v_accvgpr_read_b32 v79, a59\n\t" \
  "v_mfma_f32_16x16x32_bf16 v[64:67], a[160:163], a[216:219], v[64:67]\n\t" \
  "v_accvgpr_read_b32 v44, a52\n\t" \
  "v_accvgpr_read_b32 v45, a53\n\t" \
  "v_mfma_f32_16x16x32_bf16 v[96:99], a[160:163], a[220:223], v[96:99]\n\t" \
  "buffer_load_dwordx4 v203, %[sw], s96 offen offset:3072 lds\n\t" \
  "v_mfma_f32_16x16x32_bf16 v[100:103], a[164:167], a[220:223], v[100:103]\n\t" \
  "v_accvgpr_read_b32 v46, a54\n\t" \
  "v_accvgpr_read_b32 v47, a55\n\t" \
  "v_mfma_f32_16x16x32_bf16 v[68:71], a[164:167], a[216:219], v[68:71]\n\t" \
  "v_accvgpr_read_b32 v12, a48\n\t" \
  "v_accvgpr_read_b32 v13, a49\n\t" \
  "v_mfma_f32_16x16x32_bf16 v[36:39], a[164:167], a[212:215], v[36:39]\n\t" \
  "v_accvgpr_read_b32 v14, a50\n\t" \
  "v_accvgpr_read_b32 v15, a51\n\t" \
  "v_mfma_f32_16x16x32_bf16 v[4:7], a[164:167], a[208:211], v[4:7]\n\t" \
  "s_waitcnt vmcnt(25) lgkmcnt(0)\n\t" \
  "v_mfma_f32_16x16x32_bf16 a[64:67], a[144:147], a[224:227], a[64:67]\n\t" \
  "ds_read_b64_tr_b16 a[160:161], v192 offset:8192\n\t" \
  "v_add_u32 v216, 3145728, v216\n\t" \
  "v_mfma_f32_16x16x32_bf16 a[68:71], a[144:147], a[228:231], a[68:71]\n\t" \
  "v_and_b32 v216, 0x7fffff, v216\n\t" \
  "v_mfma_f32_16x16x32_bf16 a[72:75], a[144:147], a[232:235], a[72:75]\n\t" \
  "global_load_dword %[pf], v216, %[wq]\n\t" \
  "v_mfma_f32_16x16x32_bf16 a[76:79], a[144:147], a[236:239], a[76:79]\n\t" \
  "s_add_u32 s97, s97, %[dk]\n\t" \
  "ds_read_b64_tr_b16 a[162:163], v192 offset:9216\n\t" \
  "v_mfma_f32_16x16x32_bf16 a[92:95], a[148:151], a[236:239], a[92:95]\n\t" \
  "ds_read_b64_tr_b16 a[164:165], v193 offset:8192\n\t" \
  "v_mfma_f32_16x16x32_bf16 a[88:91], a[148:151], a[232:235], a[88:91]\n\t" \
  "buffer_load_dwordx4 a[208:211], v248, %[ws], s97 offen sc1\n\t" \
  "v_mfma_f32_16x16x32_bf16 a[84:87], a[148:151], a[228:231], a[84:87]\n\t" \
  "ds_read_b64_tr_b16 a[166:167], v193 offset:9216\n\t" \
  "v_mfma_f32_16x16x32_bf16 a[80:83], a[148:151], a[224:227], a[80:83]\n\t" \
  "ds_read_b64_tr_b16 a[168:169], v194 offset:8192\n\t" \
  "v_mfma_f32_16x16x32_bf16 a[96:99], a[152:155], a[224:227], a[96:99]\n\t" \
  "ds_read_b64_tr_b16 a[170:171], v194 offset:9216\n\t" \
  "v_accvgpr_read_b32 v16, a64\n\t" \
  "v_accvgpr_read_b32 v17, a65\n\t" \
  "v_mfma_f32_16x16x32_bf16 a[100:103], a[152:155], a[228:231], a[100:103]\n\t" \
  "ds_read_b64_tr_b16 a[172:173], v195 offset:8192\n\t" \
  "v_accvgpr_read_b32 v18, a66\n\t" \
  "v_accvgpr_read_b32 v19, a67\n\t" \
  "v_mfma_f32_16x16x32_bf16 a[104:107], a[152:155], a[232:235], a[104:107]\n\t" \
  "v_accvgpr_read_b32 v48, a68\n\t" \
  "v_accvgpr_read_b32 v49, a69\n\t" \
  "v_accvgpr_read_b32 v50, a70\n\t" \
  "v_mfma_f32_16x16x32_bf16 a[108:111], a[152:155], a[236:239], a[108:111]\n\t" \
  "buffer_load_dwordx4 a[212:215], v249, %[ws], s97 offen sc1\n\t" \
  "v_mfma_f32_16x16x32_bf16 a[124:127], a[156:159], a[236:239], a[124:127]\n\t" \
  "ds_read_b64_tr_b16 a[174:175], v195 offset:9216\n\t" \
  "v_accvgpr_read_b32 v51, a71\n\t" \
  "v_mfma_f32_16x16x32_bf16 a[120:123], a[156:159], a[232:235], a[120:123]\n\t" \
  "ds_read_b64_tr_b16 a[176:177], v196 offset:8192\n\t" \
  "v_accvgpr_read_b32 v80, a72\n\t" \
  "v_mfma_f32_16x16x32_bf16 a[116:119], a[156:159], a[228:231], a[116:119]\n\t" \
  "ds_read_b64_tr_b16 a[178:179], v196 offset:9216\n\t" \
  "v_accvgpr_read_b32 v81, a73\n\t" \
  "v_mfma_f32_16x16x32_bf16 a[112:115], a[156:159], a[224:227], a[112:115]\n\t" \
  "ds_read_b64_tr_b16 a[180:181], v197 offset:8192\n\t" \
  "v_accvgpr_read_b32 v82, a74\n\t" \
  "v_mfma_f32_16x16x32_bf16 v[0:3], a[128:131], a[224:227], v[0:3]\n\t" \
  "buffer_load_dwordx4 a[216:219], v250, %[ws], s97 offen sc1\n\t" \
  "v_mfma_f32_16x16x32_bf16 v[32:35], a[128:131], a[228:231], v[32:35]\n\t" \
  "ds_read_b64_tr_b16 a[182:183], v197 offset:9216\n\t" \
  "v_accvgpr_read_b32 v83, a75\n\t" \
  "v_mfma_f32_16x16x32_bf16 v[64:67], a[128:131], a[232:235], v[64:67]\n\t" \
  "ds_read_b64_tr_b16 a[184:185], v198 offset:8192\n\t" \
  "v_accvgpr_read_b32 v112, a76\n\t" \
  "v_mfma_f32_16x16x32_bf16 v[96:99], a[128:131], a[236:239], v[96:99]\n\t" \
  "v_accvgpr_read_b32 v113, a77\n\t" \
  "v_accvgpr_read_b32 v114, a78\n\t" \
  "v_mfma_f32_16x16x32_bf16 v[100:103], a[132:135], a[236:239], v[100:103]\n\t" \
  "ds_read_b64_tr_b16 a[186:187], v198 offset:9216\n\t" \
  "v_accvgpr_read_b32 v115, a79\n\t" \
  "v_mfma_f32_16x16x32_bf16 v[68:71], a[132:135], a[232:235], v[68:71]\n\t" \
  "ds_read_b64_tr_b16 a[188:189], v199 offset:8192\n\t" \
  "v_accvgpr_read_b32 v116, a92\n\t" \
  "v_mfma_f32_16x16x32_bf16 v[36:39], a[132:135], a[228:231], v[36:39]\n\t" \
  "buffer_load_dwordx4 a[220:223], v251, %[ws], s97 offen sc1\n\t" \
  "v_mfma_f32_16x16x32_bf16 v[4:7], a[132:135], a[224:227], v[4:7]\n\t" \
  "ds_read_b64_tr_b16 a[190:191], v199 offset:9216\n\t" \
  "v_accvgpr_read_b32 v117, a93\n\t" \
  "v_mfma_f32_16x16x32_bf16 v[8:11], a[136:139], a[224:227], v[8:11]\n\t" \
  "v_accvgpr_read_b32 v118, a94\n\t" \
  "v_accvgpr_read_b32 v119, a95\n\t" \
  "v_mfma_f32_16x16x32_bf16 v[40:43], a[136:139], a[228:231], v[40:43]\n\t" \
  "v_accvgpr_read_b32 v84, a88\n\t" \
  "v_accvgpr_read_b32 v85, a89\n\t" \
  "v_mfma_f32_16x16x32_bf16 v[72:75], a[136:139], a[232:235], v[72:75]\n\t" \
  "v_accvgpr_read_b32 v86, a90\n\t" \
  "v_accvgpr_read_b32 v87, a91\n\t" \
  "v_mfma_f32_16x16x32_bf16 v[104:107], a[136:139], a[236:239], v[104:107]\n\t" \
  "v_accvgpr_read_b32 v52, a84\n\t" \
  "v_accvgpr_read_b32 v53, a85\n\t" \
  "v_mfma_f32_16x16x32_bf16 v[108:111], a[140:143], a[236:239], v[108:111]\n\t" \
  "v_accvgpr_read_b32 v54, a86\n\t" \
  "v_accvgpr_read_b32 v55, a87\n\t" \
  "v_mfma_f32_16x16x32_bf16 v[76:79], a[140:143], a[232:235], v[76:79]\n\t" \
  "v_accvgpr_read_b32 v20, a80\n\t" \
  "v_accvgpr_read_b32 v21, a81\n\t" \
  "v_mfma_f32_16x16x32_bf16 v[44:47], a[140:143], a[228:231], v[44:47]\n\t" \
  "v_accvgpr_read_b32 v22, a82\n\t" \
  "v_accvgpr_read_b32 v23, a83\n\t" \
  "v_mfma_f32_16x16x32_bf16 v[12:15], a[140:143], a[224:227], v[12:15]\n\t" \
  "s_waitcnt vmcnt(22) lgkmcnt(0)\n\t" \
  "s_barrier\n\t" \
  "v_mfma_f32_16x16x32_bf16 a[96:99], a[184:187], a[240:243], a[96:99]\n\t" \
  "s_add_u32 s97, %[rot19], 3670016\n\t" \
  "ds_read_b64_tr_b16 a[128:129], v184 offset:16384\n\t" \
  "v_mfma_f32_16x16x32_bf16 a[100:103], a[184:187], a[244:247], a[100:103]\n\t" \
  "s_bfe_u32 s97, s97, 0x140003\n\t" \
  "v_mfma_f32_16x16x32_bf16 a[104:107], a[184:187], a[248:251], a[104:107]\n\t" \
  "s_add_u32 s97, s97, %[sox]\n\t" \
  "ds_read_b64_tr_b16 a[130:131], v184 offset:17408\n\t" \
  "v_mfma_f32_16x16x32_bf16 a[108:111], a[184:187], a[252:255], a[108:111]\n\t" \
  "buffer_load_dwordx4 a[224:227], v248, %[ws], s97 offen sc1\n\t" \
  "v_mfma_f32_16x16x32_bf16 a[124:127], a[188:191], a[252:255], a[124:127]\n\t" \
  "buffer_load_dwordx4 a[228:231], v249, %[ws], s97 offen sc1\n\t" \
  "v_mfma_f32_16x16x32_bf16 a[120:123], a[188:191], a[248:251], a[120:123]\n\t" \
  "ds_read_b64_tr_b16 a[132:133], v185 offset:16384\n\t" \
  "v_mfma_f32_16x16x32_bf16 a[116:119], a[188:191], a[244:247], a[116:119]\n\t" \
  "ds_read_b64_tr_b16 a[134:135], v185 offset:17408\n\t" \
  "v_mfma_f32_16x16x32_bf16 a[112:115], a[188:191], a[240:243], a[112:115]\n\t" \
  "buffer_load_dwordx4 a[232:235], v250, %[ws], s97 offen sc1\n\t" \
  "v_mfma_f32_16x16x32_bf16 v[0:3], a[160:163], a[240:243], v[0:3]\n\t" \
  "ds_read_b64_tr_b16 a[136:137], v186 offset:16384\n\t" \
  "v_accvgpr_read_b32 v24, a96\n\t" \
  "v_accvgpr_read_b32 v25, a97\n\t" \
  "v_mfma_f32_16x16x32_bf16 v[32:35], a[160:163], a[244:247], v[32:35]\n\t" \
  "ds_read_b64_tr_b16 a[138:139], v186 offset:17408\n\t" \
  "v_accvgpr_read_b32 v26, a98\n\t" \
  "v_accvgpr_read_b32 v27, a99\n\t" \
  "v_mfma_f32_16x16x32_bf16 v[64:67], a[160:163], a[248:251], v[64:67]\n\t" \
  "ds_read_b64_tr_b16 a[140:141], v187 offset:16384\n\t" \
  "v_accvgpr_read_b32 v56, a100\n\t" \
  "v_accvgpr_read_b32 v57, a101\n\t" \
  "v_mfma_f32_16x16x32_bf16 v[96:99], a[160:163], a[252:255], v[96:99]\n\t" \
  "buffer_load_dwordx4 a[236:239], v251, %[ws], s97 offen sc1\n\t" \
  "v_accvgpr_read_b32 v58, a102\n\t" \
  "v_mfma_f32_16x16x32_bf16 v[100:103], a[164:167], a[252:255], v[100:103]\n\t" \
  "s_add_u32 s96, %[rot19], 5242880\n\t" \
  "ds_read_b64_tr_b16 a[142:143], v187 offset:17408\n\t" \
  "v_accvgpr_read_b32 v59, a103\n\t" \
  "v_accvgpr_read_b32 v88, a104\n\t" \
  "v_mfma_f32_16x16x32_bf16 v[68:71], a[164:167], a[248:251], v[68:71]\n\t" \
  "s_and_b32 s96, s96, 0x780000\n\t" \
  "ds_read_b64_tr_b16 a[144:145], v188 offset:16384\n\t" \
  "v_accvgpr_read_b32 v89, a105\n\t" \
  "v_accvgpr_read_b32 v90, a106\n\t" \
  "v_mfma_f32_16x16x32_bf16 v[36:39], a[164:167], a[244:247], v[36:39]\n\t" \
  "s_add_u32 m0, %[mw], 98304\n\t" \
  "ds_read_b64_tr_b16 a[146:147], v188 offset:17408\n\t" \
  "v_accvgpr_read_b32 v91, a107\n\t" \
  "v_accvgpr_read_b32 v120, a108\n\t" \
  "v_mfma_f32_16x16x32_bf16 v[4:7], a[164:167], a[240:243], v[4:7]\n\t" \
  "buffer_load_dwordx4 v200, %[sw], s96 offen offset:0 lds\n\t" \
  "v_accvgpr_read_b32 v121, a109\n\t" \
  "v_mfma_f32_16x16x32_bf16 v[8:11], a[168:171], a[240:243], v[8:11]\n\t" \
  "ds_read_b64_tr_b16 a[148:149], v189 offset:16384\n\t" \
  "v_accvgpr_read_b32 v122, a110\n\t" \
  "v_mfma_f32_16x16x32_bf16 v[40:43], a[168:171], a[244:247], v[40:43]\n\t" \
  "ds_read_b64_tr_b16 a[150:151], v189 offset:17408\n\t" \
  "v_accvgpr_read_b32 v123, a111\n\t" \
  "v_mfma_f32_16x16x32_bf16 v[72:75], a[168:171], a[248:251], v[72:75]\n\t" \
  "ds_read_b64_tr_b16 a[152:153], v190 offset:16384\n\t" \
  "v_accvgpr_read_b32 v124, a124\n\t" \
  "v_mfma_f32_16x16x32_bf16 v[104:107], a[168:171], a[252:255], v[104:107]\n\t" \
  "buffer_load_dwordx4 v201, %[sw], s96 offen offset:1024 lds\n\t" \
  "v_mfma_f32_16x16x32_bf16 v[108:111], a[172:175], a[252:255], v[108:111]\n\t" \
  "ds_read_b64_tr_b16 a[154:155], v190 offset:17408\n\t" \
  "v_accvgpr_read_b32 v125, a125\n\t" \
  "v_mfma_f32_16x16x32_bf16 v[76:79], a[172:175], a[248:251], v[76:79]\n\t" \
  "ds_read_b64_tr_b16 a[156:157], v191 offset:16384\n\t" \
  "v_accvgpr_read_b32 v126, a126\n\t" \
  "v_mfma_f32_16x16x32_bf16 v[44:47], a[172:175], a[244:247], v[44:47]\n\t" \
  "v_accvgpr_read_b32 v127, a127\n\t" \
  "v_accvgpr_read_b32 v92, a120\n\t" \
  "v_mfma_f32_16x16x32_bf16 v[12:15], a[172:175], a[240:243], v[12:15]\n\t" \
  "buffer_load_dwordx4 v202, %[sw], s96 offen offset:2048 lds\n\t" \
  "v_mfma_f32_16x16x32_bf16 v[16:19], a[176:179], a[240:243], v[16:19]\n\t" \
  "ds_read_b64_tr_b16 a[158:159], v191 offset:17408\n\t" \
  "v_accvgpr_read_b32 v93, a121\n\t" \
  "v_mfma_f32_16x16x32_bf16 v[48:51], a[176:179], a[244:247], v[48:51]\n\t" \
  "v_accvgpr_read_b32 v94, a122\n\t" \
  "v_accvgpr_read_b32 v95, a123\n\t" \
  "v_mfma_f32_16x16x32_bf16 v[80:83], a[176:179], a[248:251], v[80:83]\n\t" \
  "v_accvgpr_read_b32 v60, a116\n\t" \
  "v_accvgpr_read_b32 v61, a117\n\t" \
  "v_mfma_f32_16x16x32_bf16 v[112:115], a[176:179], a[252:255], v[112:115]\n\t" \
  "buffer_load_dwordx4 v203, %[sw], s96 offen offset:3072 lds\n\t" \
  "v_mfma_f32_16x16x32_bf16 v[116:119], a[180:183], a[252:255], v[116:119]\n\t" \
  "v_accvgpr_read_b32 v62, a118\n\t" \
  "v_accvgpr_read_b32 v63, a119\n\t" \
  "v_mfma_f32_16x16x32_bf16 v[84:87], a[180:183], a[248:251], v[84:87]\n\t" \
  "v_accvgpr_read_b32 v28, a112\n\t" \
  "v_accvgpr_read_b32 v29, a113\n\t" \
  "v_mfma_f32_16x16x32_bf16 v[52:55], a[180:183], a[244:247], v[52:55]\n\t" \
  "v_accvgpr_read_b32 v30, a114\n\t" \
  "v_accvgpr_read_b32 v31, a115\n\t" \
  "v_mfma_f32_16x16x32_bf16 v[20:23], a[180:183], a[240:243], v[20:23]\n\t" \
  "s_waitcnt vmcnt(17) lgkmcnt(0)\n\t" \
  "v_mfma_f32_16x16x32_bf16 v[0:3], a[128:131], a[192:195], v[0:3]\n\t" \
  "s_add_u32 s97, s97, %[dk]\n\t" \
  "ds_read_b64_tr_b16 a[160:161], v184 offset:24576\n\t" \
  "v_mfma_f32_16x16x32_bf16 v[32:35], a[128:131], a[196:199], v[32:35]\n\t" \
  "buffer_load_dwordx4 a[240:243], v248, %[ws], s97 offen sc1\n\t" \
  "v_mfma_f32_16x16x32_bf16 v[64:67], a[128:131], a[200:203], v[64:67]\n\t" \
  "ds_read_b64_tr_b16 a[162:163], v184 offset:25600\n\t" \
  "v_mfma_f32_16x16x32_bf16 v[96:99], a[128:131], a[204:207], v[96:99]\n\t" \
  "buffer_load_dwordx4 a[244:247], v249, %[ws], s97 offen sc1\n\t" \
  "v_mfma_f32_16x16x32_bf16 v[100:103], a[132:135], a[204:207], v[100:103]\n\t" \
  "ds_read_b64_tr_b16 a[164:165], v185 offset:24576\n\t" \
  "v_mfma_f32_16x16x32_bf16 v[68:71], a[132:135], a[200:203], v[68:71]\n\t" \
  "ds_read_b64_tr_b16 a[166:167], v185 offset:25600\n\t" \
  "v_mfma_f32_16x16x32_bf16 v[36:39], a[132:135], a[196:199], v[36:39]\n\t" \
  "ds_read_b64_tr_b16 a[168:169], v186 offset:24576\n\t" \
  "v_mfma_f32_16x16x32_bf16 v[4:7], a[132:135], a[192:195], v[4:7]\n\t" \
  "buffer_load_dwordx4 a[248:251], v250, %[ws], s97 offen sc1\n\t" \
  "v_mfma_f32_16x16x32_bf16 v[8:11], a[136:139], a[192:195], v[8:11]\n\t" \
  "ds_read_b64_tr_b16 a[170:171], v186 offset:25600\n\t" \
  "v_mfma_f32_16x16x32_bf16 v[40:43], a[136:139], a[196:199], v[40:43]\n\t" \
  "ds_read_b64_tr_b16 a[172:173], v187 offset:24576\n\t" \
  "v_mfma_f32_16x16x32_bf16 v[72:75], a[136:139], a[200:203], v[72:75]\n\t" \
  "v_mfma_f32_16x16x32_bf16 v[104:107], a[136:139], a[204:207], v[104:107]\n\t" \
  "buffer_load_dwordx4 a[252:255], v251, %[ws], s97 offen sc1\n\t" \
  "v_mfma_f32_16x16x32_bf16 v[108:111], a[140:143], a[204:207], v[108:111]\n\t" \
  "s_add_u32 s97, %[rot19], 4194304\n\t" \
  "ds_read_b64_tr_b16 a[174:175], v187 offset:25600\n\t" \
  "v_mfma_f32_16x16x32_bf16 v[76:79], a[140:143], a[200:203], v[76:79]\n\t" \
  "s_bfe_u32 s97, s97, 0x140003\n\t" \
  "ds_read_b64_tr_b16 a[176:177], v188 offset:24576\n\t" \
  "v_mfma_f32_16x16x32_bf16 v[44:47], a[140:143], a[196:199], v[44:47]\n\t" \
  "s_add_u32 s97, s97, %[sox]\n\t" \
  "ds_read_b64_tr_b16 a[178:179], v188 offset:25600\n\t" \
  "v_mfma_f32_16x16x32_bf16 v[12:15], a[140:143], a[192:195], v[12:15]\n\t" \
  "buffer_load_dwordx4 a[32:35], v248, %[ws], s97 offen sc1\n\t" \
  "v_mfma_f32_16x16x32_bf16 v[16:19], a[144:147], a[192:195], v[16:19]\n\t" \
  "ds_read_b64_tr_b16 a[180:181], v189 offset:24576\n\t" \
  "v_mfma_f32_16x16x32_bf16 v[48:51], a[144:147], a[196:199], v[48:51]\n\t" \
  "ds_read_b64_tr_b16 a[182:183], v189 offset:25600\n\t" \
  "v_mfma_f32_16x16x32_bf16 v[80:83], a[144:147], a[200:203], v[80:83]\n\t" \
  "ds_read_b64_tr_b16 a[184:185], v190 offset:24576\n\t" \
  "v_mfma_f32_16x16x32_bf16 v[112:115], a[144:147], a[204:207], v[112:115]\n\t" \
  "buffer_load_dwordx4 a[36:39], v249, %[ws], s97 offen sc1\n\t" \
  "v_mfma_f32_16x16x32_bf16 v[116:119], a[148:151], a[204:207], v[116:119]\n\t" \
  "ds_read_b64_tr_b16 a[186:187], v190 offset:25600\n\t" \
  "v_mfma_f32_16x16x32_bf16 v[84:87], a[148:151], a[200:203], v[84:87]\n\t" \
  "ds_read_b64_tr_b16 a[188:189], v191 offset:24576\n\t" \
  "v_mfma_f32_16x16x32_bf16 v[52:55], a[148:151], a[196:199], v[52:55]\n\t" \
  "v_mfma_f32_16x16x32_bf16 v[20:23], a[148:151], a[192:195], v[20:23]\n\t" \
  "buffer_load_dwordx4 a[40:43], v250, %[ws], s97 offen sc1\n\t" \
  "v_mfma_f32_16x16x32_bf16 v[24:27], a[152:155], a[192:195], v[24:27]\n\t" \
  "ds_read_b64_tr_b16 a[190:191], v191 offset:25600\n\t" \
  "v_mfma_f32_16x16x32_bf16 v[56:59], a[152:155], a[196:199], v[56:59]\n\t" \
  "v_mfma_f32_16x16x32_bf16 v[88:91], a[152:155], a[200:203], v[88:91]\n\t" \
  "v_mfma_f32_16x16x32_bf16 v[120:123], a[152:155], a[204:207], v[120:123]\n\t" \
  "buffer_load_dwordx4 a[44:47], v251, %[ws], s97 offen sc1\n\t" \
  "v_mfma_f32_16x16x32_bf16 v[124:127], a[156:159], a[204:207], v[124:127]\n\t" \
  "v_mfma_f32_16x16x32_bf16 v[92:95], a[156:159], a[200:203], v[92:95]\n\t" \
  "v_mfma_f32_16x16x32_bf16 v[60:63], a[156:159], a[196:199], v[60:63]\n\t" \
  "v_mfma_f32_16x16x32_bf16 v[28:31], a[156:159], a[192:195], v[28:31]\n\t" \
  "s_waitcnt vmcnt(16) lgkmcnt(0)\n\t" \
  "s_barrier\n\t" \
  "v_mfma_f32_16x16x32_bf16 v[0:3], a[160:163], a[208:211], v[0:3]\n\t" \
  "s_add_u32 s97, s97, %[dk]\n\t" \
  "ds_read_b64_tr_b16 a[128:129], v184 offset:32768\n\t" \
  "ds_read_b64_tr_b16 a[130:131], v184 offset:33792\n\t" \
  "v_mfma_f32_16x16x32_bf16 v[32:35], a[160:163], a[212:215], v[32:35]\n\t" \
  "buffer_load_dwordx4 a[48:51], v248, %[ws], s97 offen sc1\n\t" \
  "v_mfma_f32_16x16x32_bf16 v[64:67], a[160:163], a[216:219], v[64:67]\n\t" \
  "s_bfe_u32 %[hl], %[bid], 0x30003\n\t" \
  "ds_read_b64_tr_b16 a[132:133], v185 offset:32768\n\t" \
  "ds_read_b64_tr_b16 a[134:135], v185 offset:33792\n\t" \
  "v_mfma_f32_16x16x32_bf16 v[96:99], a[160:163], a[220:223], v[96:99]\n\t" \
  "buffer_load_dwordx4 a[52:55], v249, %[ws], s97 offen sc1\n\t" \
  "v_mfma_f32_16x16x32_bf16 v[100:103], a[164:167], a[220:223], v[100:103]\n\t" \
  "s_lshl_b32 %[hl18], %[hl], 18\n\t" \
  "ds_read_b64_tr_b16 a[136:137], v186 offset:32768\n\t" \
  "ds_read_b64_tr_b16 a[138:139], v186 offset:33792\n\t" \
  "v_mfma_f32_16x16x32_bf16 v[68:71], a[164:167], a[216:219], v[68:71]\n\t" \
  "s_lshr_b32 s98, %[bid], 3\n\t" \
  "ds_read_b64_tr_b16 a[140:141], v187 offset:32768\n\t" \
  "ds_read_b64_tr_b16 a[142:143], v187 offset:33792\n\t" \
  "v_mfma_f32_16x16x32_bf16 v[36:39], a[164:167], a[212:215], v[36:39]\n\t" \
  "s_lshl_b32 %[cto], %[wid], 5\n\t" \
  "ds_read_b64_tr_b16 a[144:145], v188 offset:32768\n\t" \
  "ds_read_b64_tr_b16 a[146:147], v188 offset:33792\n\t" \
  "v_mfma_f32_16x16x32_bf16 v[4:7], a[164:167], a[208:211], v[4:7]\n\t" \
  "buffer_load_dwordx4 a[56:59], v250, %[ws], s97 offen sc1\n\t" \
  "v_mfma_f32_16x16x32_bf16 v[8:11], a[168:171], a[208:211], v[8:11]\n\t" \
  "s_add_u32 %[cto], %[cto], s98\n\t" \
  "ds_read_b64_tr_b16 a[148:149], v189 offset:32768\n\t" \
  "ds_read_b64_tr_b16 a[150:151], v189 offset:33792\n\t" \
  "v_mfma_f32_16x16x32_bf16 v[40:43], a[168:171], a[212:215], v[40:43]\n\t" \
  "s_lshl_b32 %[cto], %[cto], 7\n\t" \
  "ds_read_b64_tr_b16 a[152:153], v190 offset:32768\n\t" \
  "ds_read_b64_tr_b16 a[154:155], v190 offset:33792\n\t" \
  "v_mfma_f32_16x16x32_bf16 v[72:75], a[168:171], a[216:219], v[72:75]\n\t" \
  "s_lshl2_add_u32 %[pf2], s98, %[wid]\n\t" \
  "ds_read_b64_tr_b16 a[156:157], v191 offset:32768\n\t" \
  "ds_read_b64_tr_b16 a[158:159], v191 offset:33792\n\t" \
  "v_mfma_f32_16x16x32_bf16 v[104:107], a[168:171], a[220:223], v[104:107]\n\t" \
  "buffer_load_dwordx4 a[60:63], v251, %[ws], s97 offen sc1\n\t" \
  "v_mfma_f32_16x16x32_bf16 v[108:111], a[172:175], a[220:223], v[108:111]\n\t" \
  "s_add_u32 s97, %[rot19], 4718592\n\t" \
  "ds_read_b64_tr_b16 a[64:65], v184 offset:40960\n\t" \
  "ds_read_b64_tr_b16 a[66:67], v184 offset:41984\n\t" \
  "v_mfma_f32_16x16x32_bf16 v[76:79], a[172:175], a[216:219], v[76:79]\n\t" \
  "s_bfe_u32 s97, s97, 0x140003\n\t" \
  "ds_read_b64_tr_b16 a[68:69], v185 offset:40960\n\t" \
  "ds_read_b64_tr_b16 a[70:71], v185 offset:41984\n\t" \
  "v_mfma_f32_16x16x32_bf16 v[44:47], a[172:175], a[212:215], v[44:47]\n\t" \
  "s_add_u32 s97, s97, %[sox]\n\t" \
  "v_mbcnt_lo_u32_b32 v233, -1, 0\n\t" \
  "v_mfma_f32_16x16x32_bf16 v[12:15], a[172:175], a[208:211], v[12:15]\n\t" \
  "buffer_load_dwordx4 a[96:99], v248, %[ws], s97 offen sc1\n\t" \
  "v_mfma_f32_16x16x32_bf16 v[16:19], a[176:179], a[208:211], v[16:19]\n\t" \
  "s_mul_i32 %[pf2], %[pf2], 98304\n\t" \
  "ds_read_b64_tr_b16 a[72:73], v186 offset:40960\n\t" \
  "ds_read_b64_tr_b16 a[74:75], v186 offset:41984\n\t" \
  "v_mfma_f32_16x16x32_bf16 v[48:51], a[176:179], a[212:215], v[48:51]\n\t" \
  "s_lshr_b32 s98, %[bid], 2\n\t" \
  "ds_read_b64_tr_b16 a[76:77], v187 offset:40960\n\t" \
  "ds_read_b64_tr_b16 a[78:79], v187 offset:41984\n\t" \
  "v_mfma_f32_16x16x32_bf16 v[80:83], a[176:179], a[216:219], v[80:83]\n\t" \
  "s_xor_b32 s98, s98, %[wid]\n\t" \
  "ds_read_b64_tr_b16 a[80:81], v188 offset:40960\n\t" \
  "v_mbcnt_hi_u32_b32 v233, -1, v233\n\t" \
  "v_mfma_f32_16x16x32_bf16 v[112:115], a[176:179], a[220:223], v[112:115]\n\t" \
  "buffer_load_dwordx4 a[100:103], v249, %[ws], s97 offen sc1\n\t" \
  "v_mfma_f32_16x16x32_bf16 v[116:119], a[180:183], a[220:223], v[116:119]\n\t" \
  "s_lshl_b32 s98, s98, 13\n\t" \
  "ds_read_b64_tr_b16 a[82:83], v188 offset:41984\n\t" \
  "ds_read_b64_tr_b16 a[84:85], v189 offset:40960\n\t" \
  "v_mfma_f32_16x16x32_bf16 v[84:87], a[180:183], a[216:219], v[84:87]\n\t" \
  "s_and_b32 %[po], s98, 0x2000\n\t" \
  "ds_read_b64_tr_b16 a[86:87], v189 offset:41984\n\t" \
  "v_lshrrev_b32 v129, 4, v233\n\t" \
  "v_mfma_f32_16x16x32_bf16 v[52:55], a[180:183], a[212:215], v[52:55]\n\t" \
  "s_lshr_b32 s98, %[bid], 6\n\t" \
  "ds_read_b64_tr_b16 a[88:89], v190 offset:40960\n\t" \
  "v_and_b32 v130, 15, v233\n\t" \
  "v_mfma_f32_16x16x32_bf16 v[20:23], a[180:183], a[208:211], v[20:23]\n\t" \
  "buffer_load_dwordx4 a[104:107], v250, %[ws], s97 offen sc1\n\t" \
  "v_mfma_f32_16x16x32_bf16 v[24:27], a[184:187], a[208:211], v[24:27]\n\t" \
  "s_lshl_b32 s98, s98, 22\n\t" \
  "ds_read_b64_tr_b16 a[90:91], v190 offset:41984\n\t" \
  "ds_read_b64_tr_b16 a[92:93], v191 offset:40960\n\t" \
  "v_mfma_f32_16x16x32_bf16 v[56:59], a[184:187], a[212:215], v[56:59]\n\t" \
  "s_add_u32 %[po], %[po], s98\n\t" \
  "ds_read_b64_tr_b16 a[94:95], v191 offset:41984\n\t" \
  "v_bfe_u32 v131, v233, 2, 2\n\t" \
  "v_mfma_f32_16x16x32_bf16 v[88:91], a[184:187], a[216:219], v[88:91]\n\t" \
  "s_lshl_b32 %[xcb], %[wid], 15\n\t" \
  "v_and_b32 v132, 3, v233\n\t" \
  "v_mfma_f32_16x16x32_bf16 v[120:123], a[184:187], a[220:223], v[120:123]\n\t" \
  "buffer_load_dwordx4 a[108:111], v251, %[ws], s97 offen sc1\n\t" \
  "v_mfma_f32_16x16x32_bf16 v[124:127], a[188:191], a[220:223], v[124:127]\n\t" \
  "s_add_u32 %[xcb], %[xcb], 0x1800000\n\t" \
  "v_cmp_lt_u32 vcc, 6, v233\n\t" \
  "v_mfma_f32_16x16x32_bf16 v[92:95], a[188:191], a[216:219], v[92:95]\n\t" \
  "s_add_u32 %[xcb], %[xcb], %[po]\n\t" \
  "v_mov_b32 v133, 6272\n\t" \
  "v_mfma_f32_16x16x32_bf16 v[60:63], a[188:191], a[212:215], v[60:63]\n\t" \
  "s_and_b32 s98, %[bid], 0x38\n\t" \
  "v_cndmask_b32 v133, 0, v133, vcc\n\t" \
  "v_mfma_f32_16x16x32_bf16 v[28:31], a[188:191], a[208:211], v[28:31]\n\t" \
  "s_waitcnt vmcnt(12) lgkmcnt(0)\n\t" \
  "s_barrier\n\t" \
  "v_mfma_f32_16x16x32_bf16 v[0:3], a[128:131], a[224:227], v[0:3]\n\t" \
  "s_add_u32 s97, s97, %[dk]\n\t" \
  "ds_read_b64_tr_b16 a[160:161], v184 offset:0\n\t" \
  "v_lshl_add_u32 v133, v233, 7, v133\n\t" \
  "v_mfma_f32_16x16x32_bf16 v[0:3], a[64:67], a[240:243], v[0:3]\n\t" \
  "buffer_load_dwordx4 a[112:115], v248, %[ws], s97 offen sc1\n\t" \
  "v_mfma_f32_16x16x32_bf16 v[32:35], a[64:67], a[244:247], v[32:35]\n\t" \
  "s_lshl_b32 s98, s98, 16\n\t" \
  "ds_read_b64_tr_b16 a[162:163], v184 offset:1024\n\t" \
  "v_cmp_gt_u32 vcc, 14, v233\n\t" \
  "v_mfma_f32_16x16x32_bf16 v[32:35], a[128:131], a[228:231], v[32:35]\n\t" \
  "buffer_load_dwordx4 a[116:119], v249, %[ws], s97 offen sc1\n\t" \
  "v_mfma_f32_16x16x32_bf16 v[64:67], a[128:131], a[232:235], v[64:67]\n\t" \
  "s_add_u32 %[xcb], %[xcb], s98\n\t" \
  "ds_read_b64_tr_b16 a[164:165], v185 offset:0\n\t" \
  "v_bfrev_b32 v247, 1\n\t" \
  "v_mfma_f32_16x16x32_bf16 v[64:67], a[64:67], a[248:251], v[64:67]\n\t" \
  "s_add_u32 %[otb], %[po], 0x800000\n\t" \
  "ds_read_b64_tr_b16 a[166:167], v185 offset:1024\n\t" \
  "v_cndmask_b32 v245, v247, v133, vcc\n\t" \
  "v_mfma_f32_16x16x32_bf16 v[96:99], a[64:67], a[252:255], v[96:99]\n\t" \
  "s_and_b32 s98, %[bid], 0x3\n\t" \
  "ds_read_b64_tr_b16 a[168:169], v186 offset:0\n\t" \
  "v_writelane_b32 v247, %[prb], 0\n\t" \
  "v_mfma_f32_16x16x32_bf16 v[96:99], a[128:131], a[236:239], v[96:99]\n\t" \
  "buffer_load_dwordx4 a[120:123], v250, %[ws], s97 offen sc1\n\t" \
  "v_mfma_f32_16x16x32_bf16 v[4:7], a[132:135], a[224:227], v[4:7]\n\t" \
  "s_lshl_b32 s98, s98, 20\n\t" \
  "ds_read_b64_tr_b16 a[170:171], v186 offset:1024\n\t" \
  "v_lshrrev_b32 v133, 1, v129\n\t" \
  "v_mfma_f32_16x16x32_bf16 v[4:7], a[68:71], a[240:243], v[4:7]\n\t" \
  "s_add_u32 %[otb], %[otb], s98\n\t" \
  "ds_read_b64_tr_b16 a[172:173], v187 offset:0\n\t" \
  "v_and_b32 v134, 1, v129\n\t" \
  "v_mfma_f32_16x16x32_bf16 v[36:39], a[68:71], a[244:247], v[36:39]\n\t" \
  "s_and_b32 s98, %[wid], 2\n\t" \
  "v_lshl_or_b32 v133, v134, 1, v133\n\t" \
  "v_mfma_f32_16x16x32_bf16 v[36:39], a[132:135], a[228:231], v[36:39]\n\t" \
  "buffer_load_dwordx4 a[124:127], v251, %[ws], s97 offen sc1\n\t" \
  "v_mfma_f32_16x16x32_bf16 v[68:71], a[132:135], a[232:235], v[68:71]\n\t" \
  "s_add_u32 s96, %[rot19], 5767168\n\t" \
  "ds_read_b64_tr_b16 a[174:175], v187 offset:1024\n\t" \
  "v_xor_b32 v133, v133, v130\n\t" \
  "v_mfma_f32_16x16x32_bf16 v[68:71], a[68:71], a[248:251], v[68:71]\n\t" \
  "s_and_b32 s96, s96, 0x780000\n\t" \
  "ds_read_b64_tr_b16 a[176:177], v188 offset:0\n\t" \
  "v_lshlrev_b32 v133, 4, v133\n\t" \
  "v_mfma_f32_16x16x32_bf16 v[100:103], a[68:71], a[252:255], v[100:103]\n\t" \
  "s_add_u32 m0, %[mw], 16384\n\t" \
  "ds_read_b64_tr_b16 a[178:179], v188 offset:1024\n\t" \
  "v_lshl_add_u32 v133, v130, 8, v133\n\t" \
  "v_mfma_f32_16x16x32_bf16 v[100:103], a[132:135], a[236:239], v[100:103]\n\t" \
  "buffer_load_dwordx4 v200, %[sw], s96 offen offset:0 lds\n\t" \
  "v_mfma_f32_16x16x32_bf16 v[8:11], a[136:139], a[224:227], v[8:11]\n\t" \
  "s_lshl_b32 s98, s98, 13\n\t" \
  "ds_read_b64_tr_b16 a[180:181], v189 offset:0\n\t" \
  "v_add_u32 v220, 49152, v133\n\t" \
  "v_mfma_f32_16x16x32_bf16 v[8:11], a[72:75], a[240:243], v[8:11]\n\t" \
  "s_add_u32 %[otb], %[otb], s98\n\t" \
  "ds_read_b64_tr_b16 a[182:183], v189 offset:1024\n\t" \
  "v_xor_b32 v221, 64, v220\n\t" \
  "v_mfma_f32_16x16x32_bf16 v[40:43], a[72:75], a[244:247], v[40:43]\n\t" \
  "s_and_b32 s98, %[wid], 1\n\t" \
  "ds_read_b64_tr_b16 a[184:185], v190 offset:0\n\t" \
  "v_xor_b32 v222, 128, v220\n\t" \
  "v_mfma_f32_16x16x32_bf16 v[40:43], a[136:139], a[228:231], v[40:43]\n\t" \
  "buffer_load_dwordx4 v201, %[sw], s96 offen offset:1024 lds\n\t" \
  "v_mfma_f32_16x16x32_bf16 v[72:75], a[136:139], a[232:235], v[72:75]\n\t" \
  "s_lshl_b32 s98, s98, 12\n\t" \
  "ds_read_b64_tr_b16 a[186:187], v190 offset:1024\n\t" \
  "v_xor_b32 v223, 192, v220\n\t" \
  "v_mfma_f32_16x16x32_bf16 v[72:75], a[72:75], a[248:251], v[72:75]\n\t" \
  "s_add_u32 %[otb], %[otb], s98\n\t" \
  "ds_read_b64_tr_b16 a[188:189], v191 offset:0\n\t" \
  "v_xor_b32 v133, v130, v129\n\t" \
  "v_mfma_f32_16x16x32_bf16 v[104:107], a[72:75], a[252:255], v[104:107]\n\t" \
  "s_lshr_b32 %[eso], %[c17], 2\n\t" \
  "v_lshlrev_b32 v133, 4, v133\n\t" \
  "v_mfma_f32_16x16x32_bf16 v[104:107], a[136:139], a[236:239], v[104:107]\n\t" \
  "buffer_load_dwordx4 v202, %[sw], s96 offen offset:2048 lds\n\t" \
  "v_mfma_f32_16x16x32_bf16 v[12:15], a[140:143], a[224:227], v[12:15]\n\t" \
  "s_add_u32 %[eso], %[eso], %[otb]\n\t" \
  "ds_read_b64_tr_b16 a[190:191], v191 offset:1024\n\t" \
  "v_lshl_add_u32 v224, v129, 8, v133\n\t" \
  "v_mfma_f32_16x16x32_bf16 v[12:15], a[76:79], a[240:243], v[12:15]\n\t" \
  "s_and_b32 s98, %[bid], 0x38\n\t" \
  "ds_read_b64_tr_b16 a[0:1], v184 offset:8192\n\t" \
  "v_xor_b32 v225, 64, v224\n\t" \
  "v_mfma_f32_16x16x32_bf16 v[44:47], a[76:79], a[244:247], v[44:47]\n\t" \
  "s_lshl_b32 s98, s98, 14\n\t" \
  "ds_read_b64_tr_b16 a[2:3], v184 offset:9216\n\t" \
  "v_xor_b32 v226, 128, v224\n\t" \
  "v_mfma_f32_16x16x32_bf16 v[44:47], a[140:143], a[228:231], v[44:47]\n\t" \
  "buffer_load_dwordx4 v203, %[sw], s96 offen offset:3072 lds\n\t" \
  "v_mfma_f32_16x16x32_bf16 v[76:79], a[140:143], a[232:235], v[76:79]\n\t" \
  "s_add_u32 s97, %[rot19], 5242880\n\t" \
  "ds_read_b64_tr_b16 a[4:5], v185 offset:8192\n\t" \
  "v_xor_b32 v227, 192, v224\n\t" \
  "v_mfma_f32_16x16x32_bf16 v[76:79], a[76:79], a[248:251], v[76:79]\n\t" \
  "s_bfe_u32 s97, s97, 0x140003\n\t" \
  "ds_read_b64_tr_b16 a[6:7], v185 offset:9216\n\t" \
  "v_lshlrev_b32 v133, 3, v132\n\t" \
  "v_mfma_f32_16x16x32_bf16 v[108:111], a[76:79], a[252:255], v[108:111]\n\t" \
  "s_add_u32 s97, s97, %[sox]\n\t" \
  "ds_read_b64_tr_b16 a[8:9], v186 offset:8192\n\t" \
  "v_lshl_add_u32 v133, v129, 10, v133\n\t" \
  "v_mfma_f32_16x16x32_bf16 v[108:111], a[140:143], a[236:239], v[108:111]\n\t" \
  "buffer_load_dwordx4 a[192:195], v248, %[ws], s97 offen sc1\n\t" \
  "v_mfma_f32_16x16x32_bf16 v[16:19], a[144:147], a[224:227], v[16:19]\n\t" \
  "s_add_u32 %[eso], %[eso], s98\n\t" \
  "ds_read_b64_tr_b16 a[10:11], v186 offset:9216\n\t" \
  "v_lshl_add_u32 v228, v131, 6, v133\n\t" \
  "v_mfma_f32_16x16x32_bf16 v[16:19], a[80:83], a[240:243], v[16:19]\n\t" \
  "s_lshr_b32 s98, %[bid], 1\n\t" \
  "ds_read_b64_tr_b16 a[12:13], v187 offset:8192\n\t" \
  "v_lshl_add_u32 v228, v134, 5, v228\n\t" \
  "v_mfma_f32_16x16x32_bf16 v[48:51], a[80:83], a[244:247], v[48:51]\n\t" \
  "s_xor_b32 s98, s98, %[wid]\n\t" \
  "v_add_u32 v228, 81920, v228\n\t" \
  "v_mfma_f32_16x16x32_bf16 v[48:51], a[144:147], a[228:231], v[48:51]\n\t" \
  "buffer_load_dwordx4 a[196:199], v249, %[ws], s97 offen sc1\n\t" \
  "v_mfma_f32_16x16x32_bf16 v[80:83], a[144:147], a[232:235], v[80:83]\n\t" \
  "s_lshl_b32 s98, s98, 13\n\t" \
  "ds_read_b64_tr_b16 a[14:15], v187 offset:9216\n\t" \
  "v_xor_b32 v229, 32, v228\n\t" \
  "v_mfma_f32_16x16x32_bf16 v[80:83], a[80:83], a[248:251], v[80:83]\n\t" \
  "s_and_b32 %[po], s98, 0x2000\n\t" \
  "ds_read_b64_tr_b16 a[16:17], v188 offset:8192\n\t" \
  "v_lshl_add_u32 v244, v131, 8, v133\n\t" \
  "v_mfma_f32_16x16x32_bf16 v[112:115], a[80:83], a[252:255], v[112:115]\n\t" \
  "s_lshr_b32 s98, %[bid], 5\n\t" \
  "ds_read_b64_tr_b16 a[18:19], v188 offset:9216\n\t" \
  "v_lshl_add_u32 v244, v131, 5, v244\n\t" \
  "v_mfma_f32_16x16x32_bf16 v[112:115], a[144:147], a[236:239], v[112:115]\n\t" \
  "buffer_load_dwordx4 a[200:203], v250, %[ws], s97 offen sc1\n\t" \
  "v_mfma_f32_16x16x32_bf16 v[20:23], a[148:151], a[224:227], v[20:23]\n\t" \
  "s_xor_b32 s98, s98, %[wid]\n\t" \
  "ds_read_b64_tr_b16 a[20:21], v189 offset:8192\n\t" \
  "v_lshl_add_u32 v244, v134, 7, v244\n\t" \
  "v_mfma_f32_16x16x32_bf16 v[20:23], a[84:87], a[240:243], v[20:23]\n\t" \
  "s_lshl_b32 s98, s98, 13\n\t" \
  ".Lhs3:\n\ts_cbranch_execz .Lhs3n\n\t" \
  "ds_read_b64_tr_b16 a[22:23], v189 offset:9216\n\t" \
  "v_lshlrev_b32 v133, 4, v132\n\t" \
  "v_mfma_f32_16x16x32_bf16 v[52:55], a[84:87], a[244:247], v[52:55]\n\t" \
  "s_and_b32 %[px], s98, 0x2000\n\t" \
  "ds_read_b64_tr_b16 a[24:25], v190 offset:8192\n\t" \
  "v_lshl_add_u32 v133, v129, 6, v133\n\t" \
  "v_mfma_f32_16x16x32_bf16 v[52:55], a[148:151], a[228:231], v[52:55]\n\t" \
  "buffer_load_dwordx4 a[204:207], v251, %[ws], s97 offen sc1\n\t" \
  "v_mfma_f32_16x16x32_bf16 v[84:87], a[148:151], a[232:235], v[84:87]\n\t" \
  "s_add_u32 s97, s97, %[dk]\n\t" \
  "ds_read_b64_tr_b16 a[26:27], v190 offset:9216\n\t" \
  "v_lshl_add_u32 v230, v131, 8, v133\n\t" \
  "v_mfma_f32_16x16x32_bf16 v[84:87], a[84:87], a[248:251], v[84:87]\n\t" \
  "s_lshl_b32 %[frb], %[wid], 2\n\t" \
  "ds_read_b64_tr_b16 a[28:29], v191 offset:8192\n\t" \
  "v_xor_b32 v231, 32, v230\n\t" \
  "v_mfma_f32_16x16x32_bf16 v[116:119], a[84:87], a[252:255], v[116:119]\n\t" \
  "s_add_u32 %[frb], %[frb], 0x8000\n\t" \
  "v_lshlrev_b32 v133, 1, v129\n\t" \
  "v_mfma_f32_16x16x32_bf16 v[116:119], a[148:151], a[236:239], v[116:119]\n\t" \
  "buffer_load_dwordx4 a[208:211], v248, %[ws], s97 offen sc1\n\t" \
  "v_mfma_f32_16x16x32_bf16 v[24:27], a[152:155], a[224:227], v[24:27]\n\t" \
  "s_and_b32 s98, %[bid], 0x80\n\t" \
  "ds_read_b64_tr_b16 a[30:31], v191 offset:9216\n\t" \
  "v_xor_b32 v133, v133, v130\n\t" \
  "v_mfma_f32_16x16x32_bf16 v[24:27], a[88:91], a[240:243], v[24:27]\n\t" \
  "s_lshl_b32 s98, s98, 7\n\t" \
  "v_lshlrev_b32 v133, 4, v133\n\t" \
  "v_mfma_f32_16x16x32_bf16 v[56:59], a[88:91], a[244:247], v[56:59]\n\t" \
  "s_add_u32 %[frb], %[frb], s98\n\t" \
  "v_lshl_add_u32 v240, v129, 11, v133\n\t" \
  "v_mfma_f32_16x16x32_bf16 v[56:59], a[152:155], a[228:231], v[56:59]\n\t" \
  "buffer_load_dwordx4 a[212:215], v249, %[ws], s97 offen sc1\n\t" \
  "v_mfma_f32_16x16x32_bf16 v[88:91], a[152:155], a[232:235], v[88:91]\n\t" \
  "s_and_b32 s98, %[bid], 0x4\n\t" \
  "v_xor_b32 v241, 0x80, v240\n\t" \
  "v_mfma_f32_16x16x32_bf16 v[88:91], a[88:91], a[248:251], v[88:91]\n\t" \
  "s_lshl_b32 s98, s98, 11\n\t" \
  "v_lshrrev_b32 v133, 1, v233\n\t" \
  "v_mfma_f32_16x16x32_bf16 v[120:123], a[88:91], a[252:255], v[120:123]\n\t" \
  "s_add_u32 %[frb], %[frb], s98\n\t" \
  "v_mul_u32_u24 v133, 57344, v133\n\t" \
  "v_mfma_f32_16x16x32_bf16 v[120:123], a[152:155], a[236:239], v[120:123]\n\t" \
  "buffer_load_dwordx4 a[216:219], v250, %[ws], s97 offen sc1\n\t" \
  "v_mfma_f32_16x16x32_bf16 v[28:31], a[156:159], a[224:227], v[28:31]\n\t" \
  "s_and_b32 s98, %[bid], 0x78\n\t" \
  "v_and_b32 v134, 1, v233\n\t" \
  "v_mfma_f32_16x16x32_bf16 v[28:31], a[92:95], a[240:243], v[28:31]\n\t" \
  "s_lshl_b32 s98, s98, 6\n\t" \
  "v_lshl_add_u32 v133, v134, 4, v133\n\t" \
  "v_mfma_f32_16x16x32_bf16 v[60:63], a[92:95], a[244:247], v[60:63]\n\t" \
  "s_add_u32 %[frb], %[frb], s98\n\t" \
  "v_or_b32 v232, %[idm], v133\n\t" \
  "v_mfma_f32_16x16x32_bf16 v[60:63], a[156:159], a[228:231], v[60:63]\n\t" \
  "buffer_load_dwordx4 a[220:223], v251, %[ws], s97 offen sc1\n\t" \
  "v_mfma_f32_16x16x32_bf16 v[92:95], a[156:159], a[232:235], v[92:95]\n\t" \
  "s_lshl_b32 %[fw2], %[wid], 7\n\t" \
  "v_mfma_f32_16x16x32_bf16 v[92:95], a[92:95], a[248:251], v[92:95]\n\t" \
  "s_add_u32 %[fw2], %[fw2], 0x20000\n\t" \
  "v_mfma_f32_16x16x32_bf16 v[124:127], a[92:95], a[252:255], v[124:127]\n\t" \
  "s_and_b32 s98, %[bid], 0xc0\n\t" \
  "v_mfma_f32_16x16x32_bf16 v[124:127], a[156:159], a[236:239], v[124:127]\n\t" \
  "s_waitcnt vmcnt(20) lgkmcnt(0)\n\t" \
  "s_barrier\n\t" \
  "v_mfma_f32_16x16x32_bf16 v[0:3], a[160:163], a[32:35], v[0:3]\n\t" \
  "s_add_u32 s96, %[rot19], 6291456\n\t" \
  "ds_read_b64_tr_b16 a[128:129], v184 offset:49152\n\t" \
  "v_mfma_f32_16x16x32_bf16 v[0:3], a[0:3], a[48:51], v[0:3]\n\t" \
  "s_and_b32 s96, s96, 0x780000\n\t" \
  "v_mfma_f32_16x16x32_bf16 v[32:35], a[0:3], a[52:55], v[32:35]\n\t" \
  "s_add_u32 m0, %[mw], 32768\n\t" \
  "ds_read_b64_tr_b16 a[130:131], v184 offset:50176\n\t" \
  "v_mfma_f32_16x16x32_bf16 v[32:35], a[160:163], a[36:39], v[32:35]\n\t" \
  "buffer_load_dwordx4 v200, %[sw], s96 offen offset:0 lds\n\t" \
  "v_mfma_f32_16x16x32_bf16 v[64:67], a[160:163], a[40:43], v[64:67]\n\t" \
  "buffer_load_dwordx4 v201, %[sw], s96 offen offset:1024 lds\n\t" \
  "v_mfma_f32_16x16x32_bf16 v[64:67], a[0:3], a[56:59], v[64:67]\n\t" \
  "s_lshl_b32 s98, s98, 9\n\t" \
  "ds_read_b64_tr_b16 a[132:133], v185 offset:49152\n\t" \
  "v_mfma_f32_16x16x32_bf16 v[96:99], a[0:3], a[60:63], v[96:99]\n\t" \
  "s_add_u32 %[fw2], %[fw2], s98\n\t" \
  "ds_read_b64_tr_b16 a[134:135], v185 offset:50176\n\t" \
  "v_mfma_f32_16x16x32_bf16 v[96:99], a[160:163], a[44:47], v[96:99]\n\t" \
  "s_and_b32 s98, %[bid], 0x2\n\t" \
  "ds_read_b64_tr_b16 a[136:137], v186 offset:49152\n\t" \
  "v_mfma_f32_16x16x32_bf16 v[4:7], a[164:167], a[32:35], v[4:7]\n\t" \
  "s_lshl_b32 s98, s98, 13\n\t" \
  "ds_read_b64_tr_b16 a[138:139], v186 offset:50176\n\t" \
  "v_mfma_f32_16x16x32_bf16 v[4:7], a[4:7], a[48:51], v[4:7]\n\t" \
  "buffer_load_dwordx4 v202, %[sw], s96 offen offset:2048 lds\n\t" \
  "v_mfma_f32_16x16x32_bf16 v[36:39], a[4:7], a[52:55], v[36:39]\n\t" \
  "s_add_u32 %[fw2], %[fw2], s98\n\t" \
  "ds_read_b64_tr_b16 a[140:141], v187 offset:49152\n\t" \
  "v_mfma_f32_16x16x32_bf16 v[36:39], a[164:167], a[36:39], v[36:39]\n\t" \
  "s_and_b32 s98, %[bid], 0x4\n\t" \
  "ds_read_b64_tr_b16 a[142:143], v187 offset:50176\n\t" \
  "v_mfma_f32_16x16x32_bf16 v[68:71], a[164:167], a[40:43], v[68:71]\n\t" \
  "s_lshl_b32 s98, s98, 11\n\t" \
  "ds_read_b64_tr_b16 a[144:145], v188 offset:49152\n\t" \
  "v_mfma_f32_16x16x32_bf16 v[68:71], a[4:7], a[56:59], v[68:71]\n\t" \
  "buffer_load_dwordx4 v203, %[sw], s96 offen offset:3072 lds\n\t" \
  "v_mfma_f32_16x16x32_bf16 v[100:103], a[4:7], a[60:63], v[100:103]\n\t" \
  "s_add_u32 s97, %[rot19], 5767168\n\t" \
  "ds_read_b64_tr_b16 a[146:147], v188 offset:50176\n\t" \
  "v_mfma_f32_16x16x32_bf16 v[100:103], a[164:167], a[44:47], v[100:103]\n\t" \
  "s_bfe_u32 s97, s97, 0x140003\n\t" \
  "ds_read_b64_tr_b16 a[148:149], v189 offset:49152\n\t" \
  "v_mfma_f32_16x16x32_bf16 v[8:11], a[168:171], a[32:35], v[8:11]\n\t" \
  "s_add_u32 s97, s97, %[sox]\n\t" \
  "v_mfma_f32_16x16x32_bf16 v[8:11], a[8:11], a[48:51], v[8:11]\n\t" \
  "s_add_u32 %[fw2], %[fw2], s98\n\t" \
  "ds_read_b64_tr_b16 a[150:151], v189 offset:50176\n\t" \
  "v_mfma_f32_16x16x32_bf16 v[40:43], a[8:11], a[52:55], v[40:43]\n\t" \
  "buffer_load_dwordx4 a[224:227], v248, %[ws], s97 offen sc1\n\t" \
  "v_mfma_f32_16x16x32_bf16 v[40:43], a[168:171], a[36:39], v[40:43]\n\t" \
  "s_and_b32 s98, %[bid], 0x1\n\t" \
  "ds_read_b64_tr_b16 a[152:153], v190 offset:49152\n\t" \
  "v_mfma_f32_16x16x32_bf16 v[72:75], a[168:171], a[40:43], v[72:75]\n\t" \
  "s_lshl_b32 s98, s98, 12\n\t" \
  "ds_read_b64_tr_b16 a[154:155], v190 offset:50176\n\t" \
  "v_mfma_f32_16x16x32_bf16 v[72:75], a[8:11], a[56:59], v[72:75]\n\t" \
  "s_add_u32 %[fw2], %[fw2], s98\n\t" \
  "ds_read_b64_tr_b16 a[156:157], v191 offset:49152\n\t" \
  "v_mfma_f32_16x16x32_bf16 v[104:107], a[8:11], a[60:63], v[104:107]\n\t" \
  "s_lshl2_add_u32 %[fw2], %[hl], %[fw2]\n\t" \
  "v_mfma_f32_16x16x32_bf16 v[104:107], a[168:171], a[44:47], v[104:107]\n\t" \
  "buffer_load_dwordx4 a[228:231], v249, %[ws], s97 offen sc1\n\t" \
  "v_mfma_f32_16x16x32_bf16 v[12:15], a[172:175], a[32:35], v[12:15]\n\t" \
  "s_lshl_b32 %[xso], %[wid], 15\n\t" \
  "ds_read_b64_tr_b16 a[158:159], v191 offset:50176\n\t" \
  "v_mfma_f32_16x16x32_bf16 v[12:15], a[12:15], a[48:51], v[12:15]\n\t" \
  "s_and_b32 s98, %[bid], 0x4\n\t" \
  "ds_read_b64_tr_b16 a[64:65], v184 offset:57344\n\t" \
  "v_mfma_f32_16x16x32_bf16 v[44:47], a[12:15], a[52:55], v[44:47]\n\t" \
  "s_lshl_b32 s98, s98, 19\n\t" \
  "ds_read_b64_tr_b16 a[66:67], v184 offset:58368\n\t" \
  "v_mfma_f32_16x16x32_bf16 v[44:47], a[172:175], a[36:39], v[44:47]\n\t" \
  "buffer_load_dwordx4 a[232:235], v250, %[ws], s97 offen sc1\n\t" \
  "v_mfma_f32_16x16x32_bf16 v[76:79], a[172:175], a[40:43], v[76:79]\n\t" \
  "s_add_u32 %[xso], %[xso], s98\n\t" \
  "ds_read_b64_tr_b16 a[68:69], v185 offset:57344\n\t" \
  "v_mfma_f32_16x16x32_bf16 v[76:79], a[12:15], a[56:59], v[76:79]\n\t" \
  "s_and_b32 s98, %[bid], 0xc0\n\t" \
  "ds_read_b64_tr_b16 a[70:71], v185 offset:58368\n\t" \
  "v_mfma_f32_16x16x32_bf16 v[108:111], a[12:15], a[60:63], v[108:111]\n\t" \
  "s_lshl_b32 s98, s98, 13\n\t" \
  "ds_read_b64_tr_b16 a[72:73], v186 offset:57344\n\t" \
  "v_mfma_f32_16x16x32_bf16 v[108:111], a[172:175], a[44:47], v[108:111]\n\t" \
  "s_add_u32 %[xso], %[xso], s98\n\t" \
  "v_mfma_f32_16x16x32_bf16 v[16:19], a[176:179], a[32:35], v[16:19]\n\t" \
  "buffer_load_dwordx4 a[236:239], v251, %[ws], s97 offen sc1\n\t" \
  "v_mfma_f32_16x16x32_bf16 v[16:19], a[16:19], a[48:51], v[16:19]\n\t" \
  "s_add_u32 s99, %[pf1], 2048\n\t" \
  "ds_read_b64_tr_b16 a[74:75], v186 offset:58368\n\t" \
  "v_mfma_f32_16x16x32_bf16 v[48:51], a[16:19], a[52:55], v[48:51]\n\t" \
  "s_and_b32 s98, %[bid], 0x3\n\t" \
  "ds_read_b64_tr_b16 a[76:77], v187 offset:57344\n\t" \
  "v_mfma_f32_16x16x32_bf16 v[48:51], a[176:179], a[36:39], v[48:51]\n\t" \
  "s_lshl_b32 s98, s98, 17\n\t" \
  "ds_read_b64_tr_b16 a[78:79], v187 offset:58368\n\t" \
  "v_mfma_f32_16x16x32_bf16 v[80:83], a[176:179], a[40:43], v[80:83]\n\t" \
  "buffer_load_dword %[pf], v217, %[wo], s99 offen\n\t" \
  "v_mfma_f32_16x16x32_bf16 v[80:83], a[16:19], a[56:59], v[80:83]\n\t" \
  "s_add_u32 s97, s97, %[dk]\n\t" \
  "ds_read_b64_tr_b16 a[80:81], v188 offset:57344\n\t" \
  "v_mfma_f32_16x16x32_bf16 v[112:115], a[16:19], a[60:63], v[112:115]\n\t" \
  "s_add_u32 %[xso], %[xso], s98\n\t" \
  "ds_read_b64_tr_b16 a[82:83], v188 offset:58368\n\t" \
  "v_mfma_f32_16x16x32_bf16 v[112:115], a[176:179], a[44:47], v[112:115]\n\t" \
  "s_and_b32 s98, %[bid], 0x38\n\t" \
  "ds_read_b64_tr_b16 a[84:85], v189 offset:57344\n\t" \
  "v_mfma_f32_16x16x32_bf16 v[20:23], a[180:183], a[32:35], v[20:23]\n\t" \
  "s_lshl_b32 s98, s98, 5\n\t" \
  "v_mfma_f32_16x16x32_bf16 v[20:23], a[20:23], a[48:51], v[20:23]\n\t" \
  "buffer_load_dwordx4 a[240:243], v248, %[ws], s97 offen sc1\n\t" \
  "v_mfma_f32_16x16x32_bf16 v[52:55], a[20:23], a[52:55], v[52:55]\n\t" \
  "s_add_u32 %[xso], %[xso], s98\n\t" \
  "ds_read_b64_tr_b16 a[86:87], v189 offset:58368\n\t" \
  "v_mfma_f32_16x16x32_bf16 v[52:55], a[180:183], a[36:39], v[52:55]\n\t" \
  "s_lshl_b32 %[ss], %[wid], 7\n\t" \
  "ds_read_b64_tr_b16 a[88:89], v190 offset:57344\n\t" \
  "v_mfma_f32_16x16x32_bf16 v[84:87], a[180:183], a[40:43], v[84:87]\n\t" \
  "s_add_u32 %[ss], %[ss], 0x240000\n\t" \
  "ds_read_b64_tr_b16 a[90:91], v190 offset:58368\n\t" \
  "v_mfma_f32_16x16x32_bf16 v[84:87], a[20:23], a[56:59], v[84:87]\n\t" \
  "s_and_b32 s98, %[bid], 0xff\n\t" \
  "ds_read_b64_tr_b16 a[92:93], v191 offset:57344\n\t" \
  "v_mfma_f32_16x16x32_bf16 v[116:119], a[20:23], a[60:63], v[116:119]\n\t" \
  "buffer_load_dwordx4 a[244:247], v249, %[ws], s97 offen sc1\n\t" \
  "v_mfma_f32_16x16x32_bf16 v[116:119], a[180:183], a[44:47], v[116:119]\n\t" \
  "s_lshl_b32 s98, s98, 9\n\t" \
  "ds_read_b64_tr_b16 a[94:95], v191 offset:58368\n\t" \
  "v_mfma_f32_16x16x32_bf16 v[24:27], a[184:187], a[32:35], v[24:27]\n\t" \
  "s_add_u32 %[ss], %[ss], s98\n\t" \
  "v_mfma_f32_16x16x32_bf16 v[24:27], a[24:27], a[48:51], v[24:27]\n\t" \
  "s_lshl_b32 %[mbp], %[wid], 7\n\t" \
  "v_mfma_f32_16x16x32_bf16 v[56:59], a[24:27], a[52:55], v[56:59]\n\t" \
  "buffer_load_dwordx4 a[248:251], v250, %[ws], s97 offen sc1\n\t" \
  "v_mfma_f32_16x16x32_bf16 v[56:59], a[184:187], a[36:39], v[56:59]\n\t" \
  "s_add_u32 %[mbp], %[mbp], 0x220000\n\t" \
  "v_mfma_f32_16x16x32_bf16 v[88:91], a[184:187], a[40:43], v[88:91]\n\t" \
  "s_and_b32 s98, %[bid], 0xc0\n\t" \
  "v_mfma_f32_16x16x32_bf16 v[88:91], a[24:27], a[56:59], v[88:91]\n\t" \
  "s_lshl_b32 s98, s98, 9\n\t" \
  "v_mfma_f32_16x16x32_bf16 v[120:123], a[24:27], a[60:63], v[120:123]\n\t" \
  "s_add_u32 %[mbp], %[mbp], s98\n\t" \
  "v_mfma_f32_16x16x32_bf16 v[120:123], a[184:187], a[44:47], v[120:123]\n\t" \
  "buffer_load_dwordx4 a[252:255], v251, %[ws], s97 offen sc1\n\t" \
  "v_mfma_f32_16x16x32_bf16 v[28:31], a[188:191], a[32:35], v[28:31]\n\t" \
  "s_and_b32 s98, %[bid], 0x3\n\t" \
  "v_mfma_f32_16x16x32_bf16 v[28:31], a[28:31], a[48:51], v[28:31]\n\t" \
  "s_lshl_b32 s98, s98, 13\n\t" \
  "v_mfma_f32_16x16x32_bf16 v[60:63], a[28:31], a[52:55], v[60:63]\n\t" \
  "s_add_u32 %[mbp], %[mbp], s98\n\t" \
  "v_mfma_f32_16x16x32_bf16 v[60:63], a[188:191], a[36:39], v[60:63]\n\t" \
  "s_and_b32 s98, %[bid], 0x4\n\t" \
  "v_mfma_f32_16x16x32_bf16 v[92:95], a[188:191], a[40:43], v[92:95]\n\t" \
  "s_lshl_b32 s98, s98, 10\n\t" \
  "v_mfma_f32_16x16x32_bf16 v[92:95], a[28:31], a[56:59], v[92:95]\n\t" \
  "s_add_u32 %[mbp], %[mbp], s98\n\t" \
  "v_mfma_f32_16x16x32_bf16 v[124:127], a[28:31], a[60:63], v[124:127]\n\t" \
  "s_and_b32 s98, %[bid], 0x38\n\t" \
  "v_mfma_f32_16x16x32_bf16 v[124:127], a[188:191], a[44:47], v[124:127]\n\t" \
  "s_waitcnt vmcnt(25) lgkmcnt(0)\n\t" \
  "s_barrier\n\t" \
  "v_mfma_f32_16x16x32_bf16 v[0:3], a[128:131], a[96:99], v[0:3]\n\t" \
  "s_add_u32 s96, %[rot19], 6815744\n\t" \
  "ds_read_b64_tr_b16 a[160:161], v192 offset:0\n\t" \
  "v_mfma_f32_16x16x32_bf16 v[0:3], a[64:67], a[112:115], v[0:3]\n\t" \
  "s_and_b32 s96, s96, 0x780000\n\t" \
  "v_mfma_f32_16x16x32_bf16 v[32:35], a[64:67], a[116:119], v[32:35]\n\t" \
  "s_add_u32 m0, %[mw], 49152\n\t" \
  "ds_read_b64_tr_b16 a[162:163], v192 offset:1024\n\t" \
  "v_mfma_f32_16x16x32_bf16 v[32:35], a[128:131], a[100:103], v[32:35]\n\t" \
  "buffer_load_dwordx4 v200, %[sw], s96 offen offset:0 lds\n\t" \
  "v_mfma_f32_16x16x32_bf16 v[64:67], a[128:131], a[104:107], v[64:67]\n\t" \
  "buffer_load_dwordx4 v201, %[sw], s96 offen offset:1024 lds\n\t" \
  "v_mfma_f32_16x16x32_bf16 v[64:67], a[64:67], a[120:123], v[64:67]\n\t" \
  "s_lshl_b32 s98, s98, 6\n\t" \
  "ds_read_b64_tr_b16 a[164:165], v193 offset:0\n\t" \
  "v_mfma_f32_16x16x32_bf16 v[96:99], a[64:67], a[124:127], v[96:99]\n\t" \
  "s_add_u32 %[mbp], %[mbp], s98\n\t" \
  "ds_read_b64_tr_b16 a[166:167], v193 offset:1024\n\t" \
  "v_mfma_f32_16x16x32_bf16 v[96:99], a[128:131], a[108:111], v[96:99]\n\t" \
  "s_lshr_b32 s98, %[bid], 6\n\t" \
  "ds_read_b64_tr_b16 a[168:169], v194 offset:0\n\t" \
  "v_mfma_f32_16x16x32_bf16 v[4:7], a[132:135], a[96:99], v[4:7]\n\t" \
  "s_lshl2_add_u32 %[gvo], s98, %[wid]\n\t" \
  "ds_read_b64_tr_b16 a[170:171], v194 offset:1024\n\t" \
  "v_mfma_f32_16x16x32_bf16 v[4:7], a[68:71], a[112:115], v[4:7]\n\t" \
  "buffer_load_dwordx4 v202, %[sw], s96 offen offset:2048 lds\n\t" \
  "v_mfma_f32_16x16x32_bf16 v[36:39], a[68:71], a[116:119], v[36:39]\n\t" \
  "s_mul_i32 %[gvo], %[gvo], 786432\n\t" \
  "ds_read_b64_tr_b16 a[172:173], v195 offset:0\n\t" \
  "v_mfma_f32_16x16x32_bf16 v[36:39], a[132:135], a[100:103], v[36:39]\n\t" \
  "s_and_b32 s98, %[bid], 0x38\n\t" \
  "ds_read_b64_tr_b16 a[174:175], v195 offset:1024\n\t" \
  "v_mfma_f32_16x16x32_bf16 v[68:71], a[132:135], a[104:107], v[68:71]\n\t" \
  "s_lshl_b32 s98, s98, 4\n\t" \
  "ds_read_b64_tr_b16 a[176:177], v196 offset:0\n\t" \
  "v_mfma_f32_16x16x32_bf16 v[68:71], a[68:71], a[120:123], v[68:71]\n\t" \
  "buffer_load_dwordx4 v203, %[sw], s96 offen offset:3072 lds\n\t" \
  "v_mfma_f32_16x16x32_bf16 v[100:103], a[68:71], a[124:127], v[100:103]\n\t" \
  "s_add_u32 s97, %[rot19], 6291456\n\t" \
  "ds_read_b64_tr_b16 a[178:179], v196 offset:1024\n\t" \
  "v_mfma_f32_16x16x32_bf16 v[100:103], a[132:135], a[108:111], v[100:103]\n\t" \
  "s_bfe_u32 s97, s97, 0x140003\n\t" \
  "ds_read_b64_tr_b16 a[180:181], v197 offset:0\n\t" \
  "v_mfma_f32_16x16x32_bf16 v[8:11], a[136:139], a[96:99], v[8:11]\n\t" \
  "s_add_u32 s97, s97, %[sox]\n\t" \
  "v_mfma_f32_16x16x32_bf16 v[8:11], a[72:75], a[112:115], v[8:11]\n\t" \
  "s_add_u32 %[gvo], %[gvo], s98\n\t" \
  "ds_read_b64_tr_b16 a[182:183], v197 offset:1024\n\t" \
  "v_mfma_f32_16x16x32_bf16 v[40:43], a[72:75], a[116:119], v[40:43]\n\t" \
  "buffer_load_dwordx4 a[32:35], v248, %[ws], s97 offen sc1\n\t" \
  "v_mfma_f32_16x16x32_bf16 v[40:43], a[136:139], a[100:103], v[40:43]\n\t" \
  "s_lshr_b32 s98, %[bid], 2\n\t" \
  "ds_read_b64_tr_b16 a[184:185], v198 offset:0\n\t" \
  "v_mfma_f32_16x16x32_bf16 v[72:75], a[136:139], a[104:107], v[72:75]\n\t" \
  "s_xor_b32 s98, s98, %[bid]\n\t" \
  "ds_read_b64_tr_b16 a[186:187], v198 offset:1024\n\t" \
  "v_mfma_f32_16x16x32_bf16 v[72:75], a[72:75], a[120:123], v[72:75]\n\t" \
  "s_and_b32 s98, s98, 4\n\t" \
  "ds_read_b64_tr_b16 a[188:189], v199 offset:0\n\t" \
  "v_mfma_f32_16x16x32_bf16 v[104:107], a[72:75], a[124:127], v[104:107]\n\t" \
  "s_lshl_b32 %[vo], s98, 4\n\t" \
  "v_mfma_f32_16x16x32_bf16 v[104:107], a[136:139], a[108:111], v[104:107]\n\t" \
  "buffer_load_dwordx4 a[36:39], v249, %[ws], s97 offen sc1\n\t" \
  "v_mfma_f32_16x16x32_bf16 v[12:15], a[140:143], a[96:99], v[12:15]\n\t" \
  "s_and_b32 s98, %[bid], 0x8\n\t" \
  "ds_read_b64_tr_b16 a[190:191], v199 offset:1024\n\t" \
  "v_mfma_f32_16x16x32_bf16 v[12:15], a[76:79], a[112:115], v[12:15]\n\t" \
  "s_lshl_b32 s98, s98, 2\n\t" \
  "ds_read_b64_tr_b16 a[0:1], v192 offset:8192\n\t" \
  "v_mfma_f32_16x16x32_bf16 v[44:47], a[76:79], a[116:119], v[44:47]\n\t" \
  "s_add_u32 %[vo], %[vo], s98\n\t" \
  "ds_read_b64_tr_b16 a[2:3], v192 offset:9216\n\t" \
  "v_mfma_f32_16x16x32_bf16 v[44:47], a[140:143], a[100:103], v[44:47]\n\t" \
  "buffer_load_dwordx4 a[40:43], v250, %[ws], s97 offen sc1\n\t" \
  "v_mfma_f32_16x16x32_bf16 v[76:79], a[140:143], a[104:107], v[76:79]\n\t" \
  "s_and_b32 s98, %[bid], 0x3\n\t" \
  "ds_read_b64_tr_b16 a[4:5], v193 offset:8192\n\t" \
  "v_mfma_f32_16x16x32_bf16 v[76:79], a[76:79], a[120:123], v[76:79]\n\t" \
  "s_lshl_b32 s98, s98, 3\n\t" \
  "ds_read_b64_tr_b16 a[6:7], v193 offset:9216\n\t" \
  "v_mfma_f32_16x16x32_bf16 v[108:111], a[76:79], a[124:127], v[108:111]\n\t" \
  "s_add_u32 %[vo], %[vo], s98\n\t" \
  "ds_read_b64_tr_b16 a[8:9], v194 offset:8192\n\t" \
  "v_mfma_f32_16x16x32_bf16 v[108:111], a[140:143], a[108:111], v[108:111]\n\t" \
  "s_and_b32 s98, %[bid], 0x4\n\t" \
  "v_mfma_f32_16x16x32_bf16 v[16:19], a[144:147], a[96:99], v[16:19]\n\t" \
  "buffer_load_dwordx4 a[44:47], v251, %[ws], s97 offen sc1\n\t" \
  "v_mfma_f32_16x16x32_bf16 v[16:19], a[80:83], a[112:115], v[16:19]\n\t" \
  "s_add_u32 s97, s97, %[dk]\n\t" \
  "ds_read_b64_tr_b16 a[10:11], v194 offset:9216\n\t" \
  "v_mfma_f32_16x16x32_bf16 v[48:51], a[80:83], a[116:119], v[48:51]\n\t" \
  "s_lshl_b32 s98, s98, 0\n\t" \
  "ds_read_b64_tr_b16 a[12:13], v195 offset:8192\n\t" \
  "v_mfma_f32_16x16x32_bf16 v[48:51], a[144:147], a[100:103], v[48:51]\n\t" \
  "s_add_u32 %[vo], %[vo], s98\n\t" \
  "ds_read_b64_tr_b16 a[14:15], v195 offset:9216\n\t" \
  "v_mfma_f32_16x16x32_bf16 v[80:83], a[144:147], a[104:107], v[80:83]\n\t" \
  "buffer_load_dwordx4 a[48:51], v248, %[ws], s97 offen sc1\n\t" \
  "v_mfma_f32_16x16x32_bf16 v[80:83], a[80:83], a[120:123], v[80:83]\n\t" \
  "s_add_u32 %[vo], %[vo], 0x2000\n\t" \
  "ds_read_b64_tr_b16 a[16:17], v196 offset:8192\n\t" \
  "v_mfma_f32_16x16x32_bf16 v[112:115], a[80:83], a[124:127], v[112:115]\n\t" \
  "ds_read_b64_tr_b16 a[18:19], v196 offset:9216\n\t" \
  "v_mfma_f32_16x16x32_bf16 v[112:115], a[144:147], a[108:111], v[112:115]\n\t" \
  "ds_read_b64_tr_b16 a[20:21], v197 offset:8192\n\t" \
  "v_mfma_f32_16x16x32_bf16 v[20:23], a[148:151], a[96:99], v[20:23]\n\t" \
  "s_cmp_lt_i32 %[prb], 0\n\t" \
  "s_cselect_b32 %[vo], %[prb], %[vo]\n\t" \
  "v_mfma_f32_16x16x32_bf16 v[20:23], a[84:87], a[112:115], v[20:23]\n\t" \
  "buffer_load_dwordx4 a[52:55], v249, %[ws], s97 offen sc1\n\t" \
  "v_mfma_f32_16x16x32_bf16 v[52:55], a[84:87], a[116:119], v[52:55]\n\t" \
  "ds_read_b64_tr_b16 a[22:23], v197 offset:9216\n\t" \
  "v_mfma_f32_16x16x32_bf16 v[52:55], a[148:151], a[100:103], v[52:55]\n\t" \
  "ds_read_b64_tr_b16 a[24:25], v198 offset:8192\n\t" \
  "v_mfma_f32_16x16x32_bf16 v[84:87], a[148:151], a[104:107], v[84:87]\n\t" \
  "ds_read_b64_tr_b16 a[26:27], v198 offset:9216\n\t" \
  "v_mfma_f32_16x16x32_bf16 v[84:87], a[84:87], a[120:123], v[84:87]\n\t" \
  "ds_read_b64_tr_b16 a[28:29], v199 offset:8192\n\t" \
  "v_mfma_f32_16x16x32_bf16 v[116:119], a[84:87], a[124:127], v[116:119]\n\t" \
  "buffer_load_dwordx4 a[56:59], v250, %[ws], s97 offen sc1\n\t" \
  "v_mfma_f32_16x16x32_bf16 v[116:119], a[148:151], a[108:111], v[116:119]\n\t" \
  "ds_read_b64_tr_b16 a[30:31], v199 offset:9216\n\t" \
  "v_mfma_f32_16x16x32_bf16 v[24:27], a[152:155], a[96:99], v[24:27]\n\t" \
  "v_mfma_f32_16x16x32_bf16 v[24:27], a[88:91], a[112:115], v[24:27]\n\t" \
  "v_mfma_f32_16x16x32_bf16 v[56:59], a[88:91], a[116:119], v[56:59]\n\t" \
  "buffer_load_dwordx4 a[60:63], v251, %[ws], s97 offen sc1\n\t" \
  "v_mfma_f32_16x16x32_bf16 v[56:59], a[152:155], a[100:103], v[56:59]\n\t" \
  "s_add_u32 s99, %[pf1], 4096\n\t" \
  "v_mfma_f32_16x16x32_bf16 v[88:91], a[152:155], a[104:107], v[88:91]\n\t" \
  "v_mfma_f32_16x16x32_bf16 v[88:91], a[88:91], a[120:123], v[88:91]\n\t" \
  "v_mfma_f32_16x16x32_bf16 v[120:123], a[88:91], a[124:127], v[120:123]\n\t" \
  "v_mfma_f32_16x16x32_bf16 v[120:123], a[152:155], a[108:111], v[120:123]\n\t" \
  "buffer_load_dword %[pf], v217, %[wo], s99 offen\n\t" \
  "v_mfma_f32_16x16x32_bf16 v[28:31], a[156:159], a[96:99], v[28:31]\n\t" \
  "v_mfma_f32_16x16x32_bf16 v[28:31], a[92:95], a[112:115], v[28:31]\n\t" \
  "v_mfma_f32_16x16x32_bf16 v[60:63], a[92:95], a[116:119], v[60:63]\n\t" \
  "v_mfma_f32_16x16x32_bf16 v[60:63], a[156:159], a[100:103], v[60:63]\n\t" \
  "v_mfma_f32_16x16x32_bf16 v[92:95], a[156:159], a[104:107], v[92:95]\n\t" \
  "v_mfma_f32_16x16x32_bf16 v[92:95], a[92:95], a[120:123], v[92:95]\n\t" \
  "v_mfma_f32_16x16x32_bf16 v[124:127], a[92:95], a[124:127], v[124:127]\n\t" \
  "v_mfma_f32_16x16x32_bf16 v[124:127], a[156:159], a[108:111], v[124:127]\n\t" \
  "s_waitcnt vmcnt(26) lgkmcnt(0)\n\t" \
  "s_barrier\n\t" \
  "v_mfma_f32_16x16x32_bf16 v[0:3], a[160:163], a[192:195], v[0:3]\n\t" \
  "s_add_u32 s96, %[rot19], 7340032\n\t" \
  "ds_read_b64_tr_b16 a[128:129], v184 offset:16384\n\t" \
  "v_mfma_f32_16x16x32_bf16 v[0:3], a[0:3], a[208:211], v[0:3]\n\t" \
  "s_and_b32 s96, s96, 0x780000\n\t" \
  "v_mfma_f32_16x16x32_bf16 v[32:35], a[0:3], a[212:215], v[32:35]\n\t" \
  "s_add_u32 m0, %[mw], 0\n\t" \
  "ds_read_b64_tr_b16 a[130:131], v184 offset:17408\n\t" \
  "v_mfma_f32_16x16x32_bf16 v[32:35], a[160:163], a[196:199], v[32:35]\n\t" \
  "buffer_load_dwordx4 v200, %[sw], s96 offen offset:0 lds\n\t" \
  "v_mfma_f32_16x16x32_bf16 v[64:67], a[160:163], a[200:203], v[64:67]\n\t" \
  "ds_read_b64_tr_b16 a[132:133], v185 offset:16384\n\t" \
  "v_mfma_f32_16x16x32_bf16 v[64:67], a[0:3], a[216:219], v[64:67]\n\t" \
  "buffer_load_dwordx4 v201, %[sw], s96 offen offset:1024 lds\n\t" \
  "v_mfma_f32_16x16x32_bf16 v[96:99], a[0:3], a[220:223], v[96:99]\n\t" \
  "ds_read_b64_tr_b16 a[134:135], v185 offset:17408\n\t" \
  "v_mfma_f32_16x16x32_bf16 v[96:99], a[160:163], a[204:207], v[96:99]\n\t" \
  "ds_read_b64_tr_b16 a[136:137], v186 offset:16384\n\t" \
  "v_mfma_f32_16x16x32_bf16 v[4:7], a[164:167], a[192:195], v[4:7]\n\t" \
  "ds_read_b64_tr_b16 a[138:139], v186 offset:17408\n\t" \
  "v_mfma_f32_16x16x32_bf16 v[4:7], a[4:7], a[208:211], v[4:7]\n\t" \
  "ds_read_b64_tr_b16 a[140:141], v187 offset:16384\n\t" \
  "v_mfma_f32_16x16x32_bf16 v[36:39], a[4:7], a[212:215], v[36:39]\n\t" \
  "buffer_load_dwordx4 v202, %[sw], s96 offen offset:2048 lds\n\t" \
  "v_mfma_f32_16x16x32_bf16 v[36:39], a[164:167], a[196:199], v[36:39]\n\t" \
  "ds_read_b64_tr_b16 a[142:143], v187 offset:17408\n\t" \
  "v_mfma_f32_16x16x32_bf16 v[68:71], a[164:167], a[200:203], v[68:71]\n\t" \
  "ds_read_b64_tr_b16 a[144:145], v188 offset:16384\n\t" \
  "v_mfma_f32_16x16x32_bf16 v[68:71], a[4:7], a[216:219], v[68:71]\n\t" \
  "v_mfma_f32_16x16x32_bf16 v[100:103], a[4:7], a[220:223], v[100:103]\n\t" \
  "ds_read_b64_tr_b16 a[146:147], v188 offset:17408\n\t" \
  "v_mfma_f32_16x16x32_bf16 v[100:103], a[164:167], a[204:207], v[100:103]\n\t" \
  "buffer_load_dwordx4 v203, %[sw], s96 offen offset:3072 lds\n\t" \
  "v_mfma_f32_16x16x32_bf16 v[8:11], a[168:171], a[192:195], v[8:11]\n\t" \
  "s_add_u32 s97, %[rot19], 6815744\n\t" \
  "ds_read_b64_tr_b16 a[148:149], v189 offset:16384\n\t" \
  "v_mfma_f32_16x16x32_bf16 v[8:11], a[8:11], a[208:211], v[8:11]\n\t" \
  "s_bfe_u32 s97, s97, 0x140003\n\t" \
  "ds_read_b64_tr_b16 a[150:151], v189 offset:17408\n\t" \
  "v_mfma_f32_16x16x32_bf16 v[40:43], a[8:11], a[212:215], v[40:43]\n\t" \
  "s_add_u32 s97, s97, %[sox]\n\t" \
  "ds_read_b64_tr_b16 a[152:153], v190 offset:16384\n\t" \
  "v_mfma_f32_16x16x32_bf16 v[40:43], a[168:171], a[196:199], v[40:43]\n\t" \
  "v_mfma_f32_16x16x32_bf16 v[72:75], a[168:171], a[200:203], v[72:75]\n\t" \
  "buffer_load_dwordx4 a[96:99], v248, %[ws], s97 offen sc1\n\t" \
  "v_mfma_f32_16x16x32_bf16 v[72:75], a[8:11], a[216:219], v[72:75]\n\t" \
  "ds_read_b64_tr_b16 a[154:155], v190 offset:17408\n\t" \
  "v_mfma_f32_16x16x32_bf16 v[104:107], a[8:11], a[220:223], v[104:107]\n\t" \
  "ds_read_b64_tr_b16 a[156:157], v191 offset:16384\n\t" \
  "v_mfma_f32_16x16x32_bf16 v[104:107], a[168:171], a[204:207], v[104:107]\n\t" \
  "ds_read_b64_tr_b16 a[158:159], v191 offset:17408\n\t" \
  "v_mfma_f32_16x16x32_bf16 v[12:15], a[172:175], a[192:195], v[12:15]\n\t" \
  "ds_read_b64_tr_b16 a[64:65], v184 offset:24576\n\t" \
  "v_mfma_f32_16x16x32_bf16 v[12:15], a[12:15], a[208:211], v[12:15]\n\t" \
  "buffer_load_dwordx4 a[100:103], v249, %[ws], s97 offen sc1\n\t" \
  "v_mfma_f32_16x16x32_bf16 v[44:47], a[12:15], a[212:215], v[44:47]\n\t" \
  "ds_read_b64_tr_b16 a[66:67], v184 offset:25600\n\t" \
  "v_mfma_f32_16x16x32_bf16 v[44:47], a[172:175], a[196:199], v[44:47]\n\t" \
  "ds_read_b64_tr_b16 a[68:69], v185 offset:24576\n\t" \
  "v_mfma_f32_16x16x32_bf16 v[76:79], a[172:175], a[200:203], v[76:79]\n\t" \
  "v_mfma_f32_16x16x32_bf16 v[76:79], a[12:15], a[216:219], v[76:79]\n\t" \
  "ds_read_b64_tr_b16 a[70:71], v185 offset:25600\n\t" \
  "v_mfma_f32_16x16x32_bf16 v[108:111], a[12:15], a[220:223], v[108:111]\n\t" \
  "buffer_load_dwordx4 a[104:107], v250, %[ws], s97 offen sc1\n\t" \
  "v_mfma_f32_16x16x32_bf16 v[108:111], a[172:175], a[204:207], v[108:111]\n\t" \
  "ds_read_b64_tr_b16 a[72:73], v186 offset:24576\n\t" \
  "v_mfma_f32_16x16x32_bf16 v[16:19], a[176:179], a[192:195], v[16:19]\n\t" \
  "ds_read_b64_tr_b16 a[74:75], v186 offset:25600\n\t" \
  "v_mfma_f32_16x16x32_bf16 v[16:19], a[16:19], a[208:211], v[16:19]\n\t" \
  "ds_read_b64_tr_b16 a[76:77], v187 offset:24576\n\t" \
  "v_mfma_f32_16x16x32_bf16 v[48:51], a[16:19], a[212:215], v[48:51]\n\t" \
  "v_mfma_f32_16x16x32_bf16 v[48:51], a[176:179], a[196:199], v[48:51]\n\t" \
  "buffer_load_dwordx4 a[108:111], v251, %[ws], s97 offen sc1\n\t" \
  "v_mfma_f32_16x16x32_bf16 v[80:83], a[176:179], a[200:203], v[80:83]\n\t" \
  "s_add_u32 s97, s97, %[dk]\n\t" \
  "ds_read_b64_tr_b16 a[78:79], v187 offset:25600\n\t" \
  "v_mfma_f32_16x16x32_bf16 v[80:83], a[16:19], a[216:219], v[80:83]\n\t" \
  "ds_read_b64_tr_b16 a[80:81], v188 offset:24576\n\t" \
  "v_mfma_f32_16x16x32_bf16 v[112:115], a[16:19], a[220:223], v[112:115]\n\t" \
  "ds_read_b64_tr_b16 a[82:83], v188 offset:25600\n\t" \
  "v_mfma_f32_16x16x32_bf16 v[112:115], a[176:179], a[204:207], v[112:115]\n\t" \
  "ds_read_b64_tr_b16 a[84:85], v189 offset:24576\n\t" \
  "v_mfma_f32_16x16x32_bf16 v[20:23], a[180:183], a[192:195], v[20:23]\n\t" \
  "buffer_load_dwordx4 a[112:115], v248, %[ws], s97 offen sc1\n\t" \
  "v_mfma_f32_16x16x32_bf16 v[20:23], a[20:23], a[208:211], v[20:23]\n\t" \
  "ds_read_b64_tr_b16 a[86:87], v189 offset:25600\n\t" \
  "v_mfma_f32_16x16x32_bf16 v[52:55], a[20:23], a[212:215], v[52:55]\n\t" \
  "ds_read_b64_tr_b16 a[88:89], v190 offset:24576\n\t" \
  "v_mfma_f32_16x16x32_bf16 v[52:55], a[180:183], a[196:199], v[52:55]\n\t" \
  "v_mfma_f32_16x16x32_bf16 v[84:87], a[180:183], a[200:203], v[84:87]\n\t" \
  "ds_read_b64_tr_b16 a[90:91], v190 offset:25600\n\t" \
  "v_mfma_f32_16x16x32_bf16 v[84:87], a[20:23], a[216:219], v[84:87]\n\t" \
  "buffer_load_dwordx4 a[116:119], v249, %[ws], s97 offen sc1\n\t" \
  "v_mfma_f32_16x16x32_bf16 v[116:119], a[20:23], a[220:223], v[116:119]\n\t" \
  "ds_read_b64_tr_b16 a[92:93], v191 offset:24576\n\t" \
  "v_mfma_f32_16x16x32_bf16 v[116:119], a[180:183], a[204:207], v[116:119]\n\t" \
  "ds_read_b64_tr_b16 a[94:95], v191 offset:25600\n\t" \
  "v_mfma_f32_16x16x32_bf16 v[24:27], a[184:187], a[192:195], v[24:27]\n\t" \
  "v_mfma_f32_16x16x32_bf16 v[24:27], a[24:27], a[208:211], v[24:27]\n\t" \
  "v_mfma_f32_16x16x32_bf16 v[56:59], a[24:27], a[212:215], v[56:59]\n\t" \
  "buffer_load_dwordx4 a[120:123], v250, %[ws], s97 offen sc1\n\t" \
  "v_mfma_f32_16x16x32_bf16 v[56:59], a[184:187], a[196:199], v[56:59]\n\t" \
  "v_mfma_f32_16x16x32_bf16 v[88:91], a[184:187], a[200:203], v[88:91]\n\t" \
  "v_mfma_f32_16x16x32_bf16 v[88:91], a[24:27], a[216:219], v[88:91]\n\t" \
  "v_mfma_f32_16x16x32_bf16 v[120:123], a[24:27], a[220:223], v[120:123]\n\t" \
  "v_mfma_f32_16x16x32_bf16 v[120:123], a[184:187], a[204:207], v[120:123]\n\t" \
  "buffer_load_dwordx4 a[124:127], v251, %[ws], s97 offen sc1\n\t" \
  "v_mfma_f32_16x16x32_bf16 v[28:31], a[188:191], a[192:195], v[28:31]\n\t" \
  "v_mfma_f32_16x16x32_bf16 v[28:31], a[28:31], a[208:211], v[28:31]\n\t" \
  "v_mfma_f32_16x16x32_bf16 v[60:63], a[28:31], a[212:215], v[60:63]\n\t" \
  "v_mfma_f32_16x16x32_bf16 v[60:63], a[188:191], a[196:199], v[60:63]\n\t" \
  "v_mfma_f32_16x16x32_bf16 v[92:95], a[188:191], a[200:203], v[92:95]\n\t" \
  "v_mfma_f32_16x16x32_bf16 v[92:95], a[28:31], a[216:219], v[92:95]\n\t" \
  "v_mfma_f32_16x16x32_bf16 v[124:127], a[28:31], a[220:223], v[124:127]\n\t" \
  "v_mfma_f32_16x16x32_bf16 v[124:127], a[188:191], a[204:207], v[124:127]\n\t" \
  "s_waitcnt vmcnt(25) lgkmcnt(0)\n\t" \
  "s_barrier\n\t" \
  "v_mfma_f32_16x16x32_bf16 v[0:3], a[128:131], a[224:227], v[0:3]\n\t" \
  "s_add_u32 s96, %[rot19], 7864320\n\t" \
  "ds_read_b64_tr_b16 a[160:161], v184 offset:32768\n\t" \
  "v_mfma_f32_16x16x32_bf16 v[0:3], a[64:67], a[240:243], v[0:3]\n\t" \
  "s_and_b32 s96, s96, 0x780000\n\t" \
  "v_mfma_f32_16x16x32_bf16 v[32:35], a[64:67], a[244:247], v[32:35]\n\t" \
  "s_add_u32 m0, %[mw], 98304\n\t" \
  "ds_read_b64_tr_b16 a[162:163], v184 offset:33792\n\t" \
  "v_mfma_f32_16x16x32_bf16 v[32:35], a[128:131], a[228:231], v[32:35]\n\t" \
  "buffer_load_dwordx4 v200, %[sw], s96 offen offset:0 lds\n\t" \
  "v_mfma_f32_16x16x32_bf16 v[64:67], a[128:131], a[232:235], v[64:67]\n\t" \
  "buffer_load_dwordx4 v201, %[sw], s96 offen offset:1024 lds\n\t" \
  "v_mfma_f32_16x16x32_bf16 v[64:67], a[64:67], a[248:251], v[64:67]\n\t" \
  "ds_read_b64_tr_b16 a[164:165], v185 offset:32768\n\t" \
  "v_mfma_f32_16x16x32_bf16 v[96:99], a[64:67], a[252:255], v[96:99]\n\t" \
  "ds_read_b64_tr_b16 a[166:167], v185 offset:33792\n\t" \
  "v_mfma_f32_16x16x32_bf16 v[96:99], a[128:131], a[236:239], v[96:99]\n\t" \
  "ds_read_b64_tr_b16 a[168:169], v186 offset:32768\n\t" \
  "v_mfma_f32_16x16x32_bf16 v[4:7], a[132:135], a[224:227], v[4:7]\n\t" \
  "ds_read_b64_tr_b16 a[170:171], v186 offset:33792\n\t" \
  "v_mfma_f32_16x16x32_bf16 v[4:7], a[68:71], a[240:243], v[4:7]\n\t" \
  "buffer_load_dwordx4 v202, %[sw], s96 offen offset:2048 lds\n\t" \
  "v_mfma_f32_16x16x32_bf16 v[36:39], a[68:71], a[244:247], v[36:39]\n\t" \
  "ds_read_b64_tr_b16 a[172:173], v187 offset:32768\n\t" \
  "v_mfma_f32_16x16x32_bf16 v[36:39], a[132:135], a[228:231], v[36:39]\n\t" \
  "ds_read_b64_tr_b16 a[174:175], v187 offset:33792\n\t" \
  "v_mfma_f32_16x16x32_bf16 v[68:71], a[132:135], a[232:235], v[68:71]\n\t" \
  "ds_read_b64_tr_b16 a[176:177], v188 offset:32768\n\t" \
  "v_mfma_f32_16x16x32_bf16 v[68:71], a[68:71], a[248:251], v[68:71]\n\t" \
  "buffer_load_dwordx4 v203, %[sw], s96 offen offset:3072 lds\n\t" \
  "v_mfma_f32_16x16x32_bf16 v[100:103], a[68:71], a[252:255], v[100:103]\n\t" \
  "s_add_u32 s97, %[rot19], 7340032\n\t" \
  "ds_read_b64_tr_b16 a[178:179], v188 offset:33792\n\t" \
  "v_mfma_f32_16x16x32_bf16 v[100:103], a[132:135], a[236:239], v[100:103]\n\t" \
  "s_bfe_u32 s97, s97, 0x140003\n\t" \
  "ds_read_b64_tr_b16 a[180:181], v189 offset:32768\n\t" \
  "v_mfma_f32_16x16x32_bf16 v[8:11], a[136:139], a[224:227], v[8:11]\n\t" \
  "s_add_u32 s97, s97, %[sox]\n\t" \
  "v_mfma_f32_16x16x32_bf16 v[8:11], a[72:75], a[240:243], v[8:11]\n\t" \
  "ds_read_b64_tr_b16 a[182:183], v189 offset:33792\n\t" \
  "v_mfma_f32_16x16x32_bf16 v[40:43], a[72:75], a[244:247], v[40:43]\n\t" \
  "buffer_load_dwordx4 a[192:195], v248, %[ws], s97 offen sc1\n\t" \
  "v_mfma_f32_16x16x32_bf16 v[40:43], a[136:139], a[228:231], v[40:43]\n\t" \
  "ds_read_b64_tr_b16 a[184:185], v190 offset:32768\n\t" \
  "v_mfma_f32_16x16x32_bf16 v[72:75], a[136:139], a[232:235], v[72:75]\n\t" \
  "ds_read_b64_tr_b16 a[186:187], v190 offset:33792\n\t" \
  "v_mfma_f32_16x16x32_bf16 v[72:75], a[72:75], a[248:251], v[72:75]\n\t" \
  "ds_read_b64_tr_b16 a[188:189], v191 offset:32768\n\t" \
  "v_mfma_f32_16x16x32_bf16 v[104:107], a[72:75], a[252:255], v[104:107]\n\t" \
  "v_mfma_f32_16x16x32_bf16 v[104:107], a[136:139], a[236:239], v[104:107]\n\t" \
  "buffer_load_dwordx4 a[196:199], v249, %[ws], s97 offen sc1\n\t" \
  "v_mfma_f32_16x16x32_bf16 v[12:15], a[140:143], a[224:227], v[12:15]\n\t" \
  "ds_read_b64_tr_b16 a[190:191], v191 offset:33792\n\t" \
  "v_mfma_f32_16x16x32_bf16 v[12:15], a[76:79], a[240:243], v[12:15]\n\t" \
  "ds_read_b64_tr_b16 a[0:1], v184 offset:40960\n\t" \
  "v_mfma_f32_16x16x32_bf16 v[44:47], a[76:79], a[244:247], v[44:47]\n\t" \
  "ds_read_b64_tr_b16 a[2:3], v184 offset:41984\n\t" \
  "v_mfma_f32_16x16x32_bf16 v[44:47], a[140:143], a[228:231], v[44:47]\n\t" \
  "buffer_load_dwordx4 a[200:203], v250, %[ws], s97 offen sc1\n\t" \
  "v_mfma_f32_16x16x32_bf16 v[76:79], a[140:143], a[232:235], v[76:79]\n\t" \
  "ds_read_b64_tr_b16 a[4:5], v185 offset:40960\n\t" \
  "v_mfma_f32_16x16x32_bf16 v[76:79], a[76:79], a[248:251], v[76:79]\n\t" \
  "ds_read_b64_tr_b16 a[6:7], v185 offset:41984\n\t" \
  "v_mfma_f32_16x16x32_bf16 v[108:111], a[76:79], a[252:255], v[108:111]\n\t" \
  "ds_read_b64_tr_b16 a[8:9], v186 offset:40960\n\t" \
  "v_mfma_f32_16x16x32_bf16 v[108:111], a[140:143], a[236:239], v[108:111]\n\t" \
  "v_mfma_f32_16x16x32_bf16 v[16:19], a[144:147], a[224:227], v[16:19]\n\t" \
  "buffer_load_dwordx4 a[204:207], v251, %[ws], s97 offen sc1\n\t" \
  "v_mfma_f32_16x16x32_bf16 v[16:19], a[80:83], a[240:243], v[16:19]\n\t" \
  "s_add_u32 s99, %[pf1], 6144\n\t" \
  "ds_read_b64_tr_b16 a[10:11], v186 offset:41984\n\t" \
  "v_mfma_f32_16x16x32_bf16 v[48:51], a[80:83], a[244:247], v[48:51]\n\t" \
  "ds_read_b64_tr_b16 a[12:13], v187 offset:40960\n\t" \
  ".Lhs4:\n\ts_cbranch_execz .Lhs4n\n\t" \
  "v_mfma_f32_16x16x32_bf16 v[48:51], a[144:147], a[228:231], v[48:51]\n\t" \
  "ds_read_b64_tr_b16 a[14:15], v187 offset:41984\n\t" \
  "v_mfma_f32_16x16x32_bf16 v[80:83], a[144:147], a[232:235], v[80:83]\n\t" \
  "buffer_load_dword %[pf], v217, %[wo], s99 offen\n\t" \
  "v_mfma_f32_16x16x32_bf16 v[80:83], a[80:83], a[248:251], v[80:83]\n\t" \
  "s_add_u32 s97, s97, %[dk]\n\t" \
  "ds_read_b64_tr_b16 a[16:17], v188 offset:40960\n\t" \
  "v_mfma_f32_16x16x32_bf16 v[112:115], a[80:83], a[252:255], v[112:115]\n\t" \
  "ds_read_b64_tr_b16 a[18:19], v188 offset:41984\n\t" \
  "v_mfma_f32_16x16x32_bf16 v[112:115], a[144:147], a[236:239], v[112:115]\n\t" \
  "ds_read_b64_tr_b16 a[20:21], v189 offset:40960\n\t" \
  "v_mfma_f32_16x16x32_bf16 v[20:23], a[148:151], a[224:227], v[20:23]\n\t" \
  "v_mfma_f32_16x16x32_bf16 v[20:23], a[84:87], a[240:243], v[20:23]\n\t" \
  "buffer_load_dwordx4 a[208:211], v248, %[ws], s97 offen sc1\n\t" \
  "v_mfma_f32_16x16x32_bf16 v[52:55], a[84:87], a[244:247], v[52:55]\n\t" \
  "ds_read_b64_tr_b16 a[22:23], v189 offset:41984\n\t" \
  "v_mfma_f32_16x16x32_bf16 v[52:55], a[148:151], a[228:231], v[52:55]\n\t" \
  "ds_read_b64_tr_b16 a[24:25], v190 offset:40960\n\t" \
  "v_mfma_f32_16x16x32_bf16 v[84:87], a[148:151], a[232:235], v[84:87]\n\t" \
  "ds_read_b64_tr_b16 a[26:27], v190 offset:41984\n\t" \
  "v_mfma_f32_16x16x32_bf16 v[84:87], a[84:87], a[248:251], v[84:87]\n\t" \
  "ds_read_b64_tr_b16 a[28:29], v191 offset:40960\n\t" \
  "v_mfma_f32_16x16x32_bf16 v[116:119], a[84:87], a[252:255], v[116:119]\n\t" \
  "buffer_load_dwordx4 a[212:215], v249, %[ws], s97 offen sc1\n\t" \
  "v_mfma_f32_16x16x32_bf16 v[116:119], a[148:151], a[236:239], v[116:119]\n\t" \
  "ds_read_b64_tr_b16 a[30:31], v191 offset:41984\n\t" \
  "v_mfma_f32_16x16x32_bf16 v[24:27], a[152:155], a[224:227], v[24:27]\n\t" \
  "v_mfma_f32_16x16x32_bf16 v[24:27], a[88:91], a[240:243], v[24:27]\n\t" \
  "v_mfma_f32_16x16x32_bf16 v[56:59], a[88:91], a[244:247], v[56:59]\n\t" \
  "buffer_load_dwordx4 a[216:219], v250, %[ws], s97 offen sc1\n\t" \
  "v_mfma_f32_16x16x32_bf16 v[56:59], a[152:155], a[228:231], v[56:59]\n\t" \
  "v_mfma_f32_16x16x32_bf16 v[88:91], a[152:155], a[232:235], v[88:91]\n\t" \
  "v_mfma_f32_16x16x32_bf16 v[88:91], a[88:91], a[248:251], v[88:91]\n\t" \
  "v_mfma_f32_16x16x32_bf16 v[120:123], a[88:91], a[252:255], v[120:123]\n\t" \
  "v_mfma_f32_16x16x32_bf16 v[120:123], a[152:155], a[236:239], v[120:123]\n\t" \
  "buffer_load_dwordx4 a[220:223], v251, %[ws], s97 offen sc1\n\t" \
  "v_mfma_f32_16x16x32_bf16 v[28:31], a[156:159], a[224:227], v[28:31]\n\t" \
  "v_mfma_f32_16x16x32_bf16 v[28:31], a[92:95], a[240:243], v[28:31]\n\t" \
  "v_mfma_f32_16x16x32_bf16 v[60:63], a[92:95], a[244:247], v[60:63]\n\t" \
  "v_mfma_f32_16x16x32_bf16 v[60:63], a[156:159], a[228:231], v[60:63]\n\t" \
  "v_mfma_f32_16x16x32_bf16 v[92:95], a[156:159], a[232:235], v[92:95]\n\t" \
  "v_mfma_f32_16x16x32_bf16 v[92:95], a[92:95], a[248:251], v[92:95]\n\t" \
  "v_mfma_f32_16x16x32_bf16 v[124:127], a[92:95], a[252:255], v[124:127]\n\t" \
  "v_mfma_f32_16x16x32_bf16 v[124:127], a[156:159], a[236:239], v[124:127]\n\t" \
  "s_waitcnt vmcnt(26) lgkmcnt(0)\n\t" \
  "s_barrier\n\t" \
  "v_mfma_f32_16x16x32_bf16 v[0:3], a[160:163], a[32:35], v[0:3]\n\t" \
  "s_add_u32 s97, %[rot19], 7864320\n\t" \
  "ds_read_b64_tr_b16 a[128:129], v184 offset:49152\n\t" \
  "v_mfma_f32_16x16x32_bf16 v[0:3], a[0:3], a[48:51], v[0:3]\n\t" \
  "s_bfe_u32 s97, s97, 0x140003\n\t" \
  "v_mfma_f32_16x16x32_bf16 v[32:35], a[0:3], a[52:55], v[32:35]\n\t" \
  "s_add_u32 s97, s97, %[sox]\n\t" \
  "ds_read_b64_tr_b16 a[130:131], v184 offset:50176\n\t" \
  "v_mfma_f32_16x16x32_bf16 v[32:35], a[160:163], a[36:39], v[32:35]\n\t" \
  "buffer_load_dwordx4 a[224:227], v248, %[ws], s97 offen sc1\n\t" \
  "v_mfma_f32_16x16x32_bf16 v[64:67], a[160:163], a[40:43], v[64:67]\n\t" \
  "ds_read_b64_tr_b16 a[132:133], v185 offset:49152\n\t" \
  "v_mfma_f32_16x16x32_bf16 v[64:67], a[0:3], a[56:59], v[64:67]\n\t" \
  "ds_read_b64_tr_b16 a[134:135], v185 offset:50176\n\t" \
  "v_mfma_f32_16x16x32_bf16 v[96:99], a[0:3], a[60:63], v[96:99]\n\t" \
  "buffer_load_dwordx4 a[228:231], v249, %[ws], s97 offen sc1\n\t" \
  "v_mfma_f32_16x16x32_bf16 v[96:99], a[160:163], a[44:47], v[96:99]\n\t" \
  "ds_read_b64_tr_b16 a[136:137], v186 offset:49152\n\t" \
  "v_mfma_f32_16x16x32_bf16 v[4:7], a[164:167], a[32:35], v[4:7]\n\t" \
  "ds_read_b64_tr_b16 a[138:139], v186 offset:50176\n\t" \
  "v_mfma_f32_16x16x32_bf16 v[4:7], a[4:7], a[48:51], v[4:7]\n\t" \
  "ds_read_b64_tr_b16 a[140:141], v187 offset:49152\n\t" \
  "v_mfma_f32_16x16x32_bf16 v[36:39], a[4:7], a[52:55], v[36:39]\n\t" \
  "v_mfma_f32_16x16x32_bf16 v[36:39], a[164:167], a[36:39], v[36:39]\n\t" \
  "ds_read_b64_tr_b16 a[142:143], v187 offset:50176\n\t" \
  "v_mfma_f32_16x16x32_bf16 v[68:71], a[164:167], a[40:43], v[68:71]\n\t" \
  "buffer_load_dwordx4 a[232:235], v250, %[ws], s97 offen sc1\n\t" \
  "v_mfma_f32_16x16x32_bf16 v[68:71], a[4:7], a[56:59], v[68:71]\n\t" \
  "ds_read_b64_tr_b16 a[144:145], v188 offset:49152\n\t" \
  "v_mfma_f32_16x16x32_bf16 v[100:103], a[4:7], a[60:63], v[100:103]\n\t" \
  "ds_read_b64_tr_b16 a[146:147], v188 offset:50176\n\t" \
  "v_mfma_f32_16x16x32_bf16 v[100:103], a[164:167], a[44:47], v[100:103]\n\t" \
  "ds_read_b64_tr_b16 a[148:149], v189 offset:49152\n\t" \
  "v_mfma_f32_16x16x32_bf16 v[8:11], a[168:171], a[32:35], v[8:11]\n\t" \
  "v_mfma_f32_16x16x32_bf16 v[8:11], a[8:11], a[48:51], v[8:11]\n\t" \
  "ds_read_b64_tr_b16 a[150:151], v189 offset:50176\n\t" \
  "v_mfma_f32_16x16x32_bf16 v[40:43], a[8:11], a[52:55], v[40:43]\n\t" \
  "buffer_load_dwordx4 a[236:239], v251, %[ws], s97 offen sc1\n\t" \
  "v_mfma_f32_16x16x32_bf16 v[40:43], a[168:171], a[36:39], v[40:43]\n\t" \
  "ds_read_b64_tr_b16 a[152:153], v190 offset:49152\n\t" \
  "v_mfma_f32_16x16x32_bf16 v[72:75], a[168:171], a[40:43], v[72:75]\n\t" \
  "s_add_u32 s97, s97, %[dk]\n\t" \
  "ds_read_b64_tr_b16 a[154:155], v190 offset:50176\n\t" \
  "v_mfma_f32_16x16x32_bf16 v[72:75], a[8:11], a[56:59], v[72:75]\n\t" \
  "ds_read_b64_tr_b16 a[156:157], v191 offset:49152\n\t" \
  "v_mfma_f32_16x16x32_bf16 v[104:107], a[8:11], a[60:63], v[104:107]\n\t" \
  "v_mfma_f32_16x16x32_bf16 v[104:107], a[168:171], a[44:47], v[104:107]\n\t" \
  "ds_read_b64_tr_b16 a[158:159], v191 offset:50176\n\t" \
  "v_mfma_f32_16x16x32_bf16 v[12:15], a[172:175], a[32:35], v[12:15]\n\t" \
  "buffer_load_dwordx4 a[240:243], v248, %[ws], s97 offen sc1\n\t" \
  "v_mfma_f32_16x16x32_bf16 v[12:15], a[12:15], a[48:51], v[12:15]\n\t" \
  "ds_read_b64_tr_b16 a[64:65], v184 offset:57344\n\t" \
  "v_mfma_f32_16x16x32_bf16 v[44:47], a[12:15], a[52:55], v[44:47]\n\t" \
  "ds_read_b64_tr_b16 a[66:67], v184 offset:58368\n\t" \
  "v_mfma_f32_16x16x32_bf16 v[44:47], a[172:175], a[36:39], v[44:47]\n\t" \
  "ds_read_b64_tr_b16 a[68:69], v185 offset:57344\n\t" \
  "v_mfma_f32_16x16x32_bf16 v[76:79], a[172:175], a[40:43], v[76:79]\n\t" \
  "v_mfma_f32_16x16x32_bf16 v[76:79], a[12:15], a[56:59], v[76:79]\n\t" \
  "ds_read_b64_tr_b16 a[70:71], v185 offset:58368\n\t" \
  "v_mfma_f32_16x16x32_bf16 v[108:111], a[12:15], a[60:63], v[108:111]\n\t" \
  "buffer_load_dwordx4 a[244:247], v249, %[ws], s97 offen sc1\n\t" \
  "v_mfma_f32_16x16x32_bf16 v[108:111], a[172:175], a[44:47], v[108:111]\n\t" \
  "ds_read_b64_tr_b16 a[72:73], v186 offset:57344\n\t" \
  "v_mfma_f32_16x16x32_bf16 v[16:19], a[176:179], a[32:35], v[16:19]\n\t" \
  "ds_read_b64_tr_b16 a[74:75], v186 offset:58368\n\t" \
  "v_mfma_f32_16x16x32_bf16 v[16:19], a[16:19], a[48:51], v[16:19]\n\t" \
  "ds_read_b64_tr_b16 a[76:77], v187 offset:57344\n\t" \
  "v_mfma_f32_16x16x32_bf16 v[48:51], a[16:19], a[52:55], v[48:51]\n\t" \
  "v_mfma_f32_16x16x32_bf16 v[48:51], a[176:179], a[36:39], v[48:51]\n\t" \
  "ds_read_b64_tr_b16 a[78:79], v187 offset:58368\n\t" \
  "v_mfma_f32_16x16x32_bf16 v[80:83], a[176:179], a[40:43], v[80:83]\n\t" \
  "buffer_load_dwordx4 a[248:251], v250, %[ws], s97 offen sc1\n\t" \
  "v_mfma_f32_16x16x32_bf16 v[80:83], a[16:19], a[56:59], v[80:83]\n\t" \
  "ds_read_b64_tr_b16 a[80:81], v188 offset:57344\n\t" \
  "v_mfma_f32_16x16x32_bf16 v[112:115], a[16:19], a[60:63], v[112:115]\n\t" \
  "ds_read_b64_tr_b16 a[82:83], v188 offset:58368\n\t" \
  "v_mfma_f32_16x16x32_bf16 v[112:115], a[176:179], a[44:47], v[112:115]\n\t" \
  "ds_read_b64_tr_b16 a[84:85], v189 offset:57344\n\t" \
  "v_mfma_f32_16x16x32_bf16 v[20:23], a[180:183], a[32:35], v[20:23]\n\t" \
  "v_mfma_f32_16x16x32_bf16 v[20:23], a[20:23], a[48:51], v[20:23]\n\t" \
  "ds_read_b64_tr_b16 a[86:87], v189 offset:58368\n\t" \
  "v_mfma_f32_16x16x32_bf16 v[52:55], a[20:23], a[52:55], v[52:55]\n\t" \
  "buffer_load_dwordx4 a[252:255], v251, %[ws], s97 offen sc1\n\t" \
  "v_mfma_f32_16x16x32_bf16 v[52:55], a[180:183], a[36:39], v[52:55]\n\t" \
  "ds_read_b64_tr_b16 a[88:89], v190 offset:57344\n\t" \
  "v_mfma_f32_16x16x32_bf16 v[84:87], a[180:183], a[40:43], v[84:87]\n\t" \
  "ds_read_b64_tr_b16 a[90:91], v190 offset:58368\n\t" \
  "v_mfma_f32_16x16x32_bf16 v[84:87], a[20:23], a[56:59], v[84:87]\n\t" \
  "ds_read_b64_tr_b16 a[92:93], v191 offset:57344\n\t" \
  "v_mfma_f32_16x16x32_bf16 v[116:119], a[20:23], a[60:63], v[116:119]\n\t" \
  "v_mfma_f32_16x16x32_bf16 v[116:119], a[180:183], a[44:47], v[116:119]\n\t" \
  "ds_read_b64_tr_b16 a[94:95], v191 offset:58368\n\t" \
  "v_mfma_f32_16x16x32_bf16 v[24:27], a[184:187], a[32:35], v[24:27]\n\t" \
  "buffer_load_dword %[pf], v217, %[ws], %[pto] offen sc1\n\t" \
  "v_mfma_f32_16x16x32_bf16 v[24:27], a[24:27], a[48:51], v[24:27]\n\t" \
  "v_mfma_f32_16x16x32_bf16 v[56:59], a[24:27], a[52:55], v[56:59]\n\t" \
  "s_mul_i32 s99, %[sofk], 5\n\t" \
  "v_mfma_f32_16x16x32_bf16 v[56:59], a[184:187], a[36:39], v[56:59]\n\t" \
  "s_xor_b32 s99, s99, 0\n\t" \
  "v_mfma_f32_16x16x32_bf16 v[88:91], a[184:187], a[40:43], v[88:91]\n\t" \
  "s_lshr_b32 s99, s99, 1\n\t" \
  "v_mfma_f32_16x16x32_bf16 v[88:91], a[24:27], a[56:59], v[88:91]\n\t" \
  "v_mfma_f32_16x16x32_bf16 v[120:123], a[24:27], a[60:63], v[120:123]\n\t" \
  "buffer_load_dword %[pf], v217, %[sD], s99 offen\n\t" \
  "v_mfma_f32_16x16x32_bf16 v[120:123], a[184:187], a[44:47], v[120:123]\n\t" \
  "v_mfma_f32_16x16x32_bf16 v[28:31], a[188:191], a[32:35], v[28:31]\n\t" \
  "v_mfma_f32_16x16x32_bf16 v[28:31], a[28:31], a[48:51], v[28:31]\n\t" \
  "v_mfma_f32_16x16x32_bf16 v[60:63], a[28:31], a[52:55], v[60:63]\n\t" \
  "v_mfma_f32_16x16x32_bf16 v[60:63], a[188:191], a[36:39], v[60:63]\n\t" \
  "v_mfma_f32_16x16x32_bf16 v[92:95], a[188:191], a[40:43], v[92:95]\n\t" \
  "v_mfma_f32_16x16x32_bf16 v[92:95], a[28:31], a[56:59], v[92:95]\n\t" \
  "v_mfma_f32_16x16x32_bf16 v[124:127], a[28:31], a[60:63], v[124:127]\n\t" \
  "v_mfma_f32_16x16x32_bf16 v[124:127], a[188:191], a[44:47], v[124:127]\n\t" \
  "s_waitcnt vmcnt(23) lgkmcnt(0)\n\t" \
  "s_barrier\n\t" \
  "v_mfma_f32_16x16x32_bf16 v[0:3], a[128:131], a[96:99], v[0:3]\n\t" \
  "s_mul_i32 s99, %[sofk], 5\n\t" \
  "ds_read_b64_tr_b16 a[160:161], v184 offset:0\n\t" \
  "v_mfma_f32_16x16x32_bf16 v[0:3], a[64:67], a[112:115], v[0:3]\n\t" \
  "s_xor_b32 s99, s99, 4096\n\t" \
  "v_mfma_f32_16x16x32_bf16 v[32:35], a[64:67], a[116:119], v[32:35]\n\t" \
  "s_lshr_b32 s99, s99, 1\n\t" \
  "ds_read_b64_tr_b16 a[162:163], v184 offset:1024\n\t" \
  "v_mfma_f32_16x16x32_bf16 v[32:35], a[128:131], a[100:103], v[32:35]\n\t" \
  "buffer_load_dword %[pf], v217, %[sD], s99 offen\n\t" \
  "v_mfma_f32_16x16x32_bf16 v[64:67], a[128:131], a[104:107], v[64:67]\n\t" \
  "s_add_u32 m0, %[mw], 16384\n\t" \
  "ds_read_b64_tr_b16 a[164:165], v185 offset:0\n\t" \
  "v_mfma_f32_16x16x32_bf16 v[64:67], a[64:67], a[120:123], v[64:67]\n\t" \
  "s_add_u32 s96, %[sofk], 0\n\t" \
  "ds_read_b64_tr_b16 a[166:167], v185 offset:1024\n\t" \
  "v_mfma_f32_16x16x32_bf16 v[96:99], a[64:67], a[124:127], v[96:99]\n\t" \
  "ds_read_b64_tr_b16 a[168:169], v186 offset:0\n\t" \
  "v_mfma_f32_16x16x32_bf16 v[96:99], a[128:131], a[108:111], v[96:99]\n\t" \
  "v_mfma_f32_16x16x32_bf16 v[4:7], a[132:135], a[96:99], v[4:7]\n\t" \
  "buffer_load_dwordx4 v224, %[sK], s96 offen offset:0 lds\n\t" \
  "v_mfma_f32_16x16x32_bf16 v[4:7], a[68:71], a[112:115], v[4:7]\n\t" \
  "ds_read_b64_tr_b16 a[170:171], v186 offset:1024\n\t" \
  "v_mfma_f32_16x16x32_bf16 v[36:39], a[68:71], a[116:119], v[36:39]\n\t" \
  "ds_read_b64_tr_b16 a[172:173], v187 offset:0\n\t" \
  "v_mfma_f32_16x16x32_bf16 v[36:39], a[132:135], a[100:103], v[36:39]\n\t" \
  "ds_read_b64_tr_b16 a[174:175], v187 offset:1024\n\t" \
  "v_mfma_f32_16x16x32_bf16 v[68:71], a[132:135], a[104:107], v[68:71]\n\t" \
  "ds_read_b64_tr_b16 a[176:177], v188 offset:0\n\t" \
  "v_mfma_f32_16x16x32_bf16 v[68:71], a[68:71], a[120:123], v[68:71]\n\t" \
  "v_mfma_f32_16x16x32_bf16 v[100:103], a[68:71], a[124:127], v[100:103]\n\t" \
  "ds_read_b64_tr_b16 a[178:179], v188 offset:1024\n\t" \
  "v_mfma_f32_16x16x32_bf16 v[100:103], a[132:135], a[108:111], v[100:103]\n\t" \
  "ds_read_b64_tr_b16 a[180:181], v189 offset:0\n\t" \
  "v_mfma_f32_16x16x32_bf16 v[8:11], a[136:139], a[96:99], v[8:11]\n\t" \
  "v_mfma_f32_16x16x32_bf16 v[8:11], a[72:75], a[112:115], v[8:11]\n\t" \
  "buffer_load_dwordx4 v225, %[sK], s96 offen offset:1024 lds\n\t" \
  "v_mfma_f32_16x16x32_bf16 v[40:43], a[72:75], a[116:119], v[40:43]\n\t" \
  "ds_read_b64_tr_b16 a[182:183], v189 offset:1024\n\t" \
  "v_mfma_f32_16x16x32_bf16 v[40:43], a[136:139], a[100:103], v[40:43]\n\t" \
  "ds_read_b64_tr_b16 a[184:185], v190 offset:0\n\t" \
  "v_mfma_f32_16x16x32_bf16 v[72:75], a[136:139], a[104:107], v[72:75]\n\t" \
  "ds_read_b64_tr_b16 a[186:187], v190 offset:1024\n\t" \
  "v_mfma_f32_16x16x32_bf16 v[72:75], a[72:75], a[120:123], v[72:75]\n\t" \
  "ds_read_b64_tr_b16 a[188:189], v191 offset:0\n\t" \
  "v_mfma_f32_16x16x32_bf16 v[104:107], a[72:75], a[124:127], v[104:107]\n\t" \
  "v_mfma_f32_16x16x32_bf16 v[104:107], a[136:139], a[108:111], v[104:107]\n\t" \
  "ds_read_b64_tr_b16 a[190:191], v191 offset:1024\n\t" \
  "v_mfma_f32_16x16x32_bf16 v[12:15], a[140:143], a[96:99], v[12:15]\n\t" \
  "ds_read_b64_tr_b16 a[0:1], v184 offset:8192\n\t" \
  "v_mfma_f32_16x16x32_bf16 v[12:15], a[76:79], a[112:115], v[12:15]\n\t" \
  "buffer_load_dwordx4 v226, %[sK], s96 offen offset:2048 lds\n\t" \
  "v_mfma_f32_16x16x32_bf16 v[44:47], a[76:79], a[116:119], v[44:47]\n\t" \
  "ds_read_b64_tr_b16 a[2:3], v184 offset:9216\n\t" \
  "v_mfma_f32_16x16x32_bf16 v[44:47], a[140:143], a[100:103], v[44:47]\n\t" \
  "ds_read_b64_tr_b16 a[4:5], v185 offset:8192\n\t" \
  "v_mfma_f32_16x16x32_bf16 v[76:79], a[140:143], a[104:107], v[76:79]\n\t" \
  "v_mfma_f32_16x16x32_bf16 v[76:79], a[76:79], a[120:123], v[76:79]\n\t" \
  "ds_read_b64_tr_b16 a[6:7], v185 offset:9216\n\t" \
  "v_mfma_f32_16x16x32_bf16 v[108:111], a[76:79], a[124:127], v[108:111]\n\t" \
  "ds_read_b64_tr_b16 a[8:9], v186 offset:8192\n\t" \
  "v_mfma_f32_16x16x32_bf16 v[108:111], a[140:143], a[108:111], v[108:111]\n\t" \
  "v_mfma_f32_16x16x32_bf16 v[16:19], a[144:147], a[96:99], v[16:19]\n\t" \
  "ds_read_b64_tr_b16 a[10:11], v186 offset:9216\n\t" \
  "v_mfma_f32_16x16x32_bf16 v[16:19], a[80:83], a[112:115], v[16:19]\n\t" \
  "ds_read_b64_tr_b16 a[12:13], v187 offset:8192\n\t" \
  "v_mfma_f32_16x16x32_bf16 v[48:51], a[80:83], a[116:119], v[48:51]\n\t" \
  "buffer_load_dwordx4 v227, %[sK], s96 offen offset:3072 lds\n\t" \
  "v_mfma_f32_16x16x32_bf16 v[48:51], a[144:147], a[100:103], v[48:51]\n\t" \
  "ds_read_b64_tr_b16 a[14:15], v187 offset:9216\n\t" \
  "v_mfma_f32_16x16x32_bf16 v[80:83], a[144:147], a[104:107], v[80:83]\n\t" \
  "ds_read_b64_tr_b16 a[16:17], v188 offset:8192\n\t" \
  "v_mfma_f32_16x16x32_bf16 v[80:83], a[80:83], a[120:123], v[80:83]\n\t" \
  "v_mfma_f32_16x16x32_bf16 v[112:115], a[80:83], a[124:127], v[112:115]\n\t" \
  "s_add_u32 m0, %[mw], 49152\n\t" \
  "ds_read_b64_tr_b16 a[18:19], v188 offset:9216\n\t" \
  "v_mfma_f32_16x16x32_bf16 v[112:115], a[144:147], a[108:111], v[112:115]\n\t" \
  "s_add_u32 s96, %[sofk], 0\n\t" \
  "ds_read_b64_tr_b16 a[20:21], v189 offset:8192\n\t" \
  "v_mfma_f32_16x16x32_bf16 v[20:23], a[148:151], a[96:99], v[20:23]\n\t" \
  "v_mfma_f32_16x16x32_bf16 v[20:23], a[84:87], a[112:115], v[20:23]\n\t" \
  "ds_read_b64_tr_b16 a[22:23], v189 offset:9216\n\t" \
  "v_mfma_f32_16x16x32_bf16 v[52:55], a[84:87], a[116:119], v[52:55]\n\t" \
  "buffer_load_dwordx4 v230, %[sV], s96 offen offset:0 lds\n\t" \
  "v_mfma_f32_16x16x32_bf16 v[52:55], a[148:151], a[100:103], v[52:55]\n\t" \
  "ds_read_b64_tr_b16 a[24:25], v190 offset:8192\n\t" \
  "v_mfma_f32_16x16x32_bf16 v[84:87], a[148:151], a[104:107], v[84:87]\n\t" \
  "ds_read_b64_tr_b16 a[26:27], v190 offset:9216\n\t" \
  "v_mfma_f32_16x16x32_bf16 v[84:87], a[84:87], a[120:123], v[84:87]\n\t" \
  "ds_read_b64_tr_b16 a[28:29], v191 offset:8192\n\t" \
  "v_mfma_f32_16x16x32_bf16 v[116:119], a[84:87], a[124:127], v[116:119]\n\t" \
  "v_mfma_f32_16x16x32_bf16 v[116:119], a[148:151], a[108:111], v[116:119]\n\t" \
  "ds_read_b64_tr_b16 a[30:31], v191 offset:9216\n\t" \
  "v_mfma_f32_16x16x32_bf16 v[24:27], a[152:155], a[96:99], v[24:27]\n\t" \
  "v_mfma_f32_16x16x32_bf16 v[24:27], a[88:91], a[112:115], v[24:27]\n\t" \
  "v_mfma_f32_16x16x32_bf16 v[56:59], a[88:91], a[116:119], v[56:59]\n\t" \
  "v_mfma_f32_16x16x32_bf16 v[56:59], a[152:155], a[100:103], v[56:59]\n\t" \
  "buffer_load_dwordx4 v231, %[sV], s96 offen offset:1024 lds\n\t" \
  "v_mfma_f32_16x16x32_bf16 v[88:91], a[152:155], a[104:107], v[88:91]\n\t" \
  "v_mfma_f32_16x16x32_bf16 v[88:91], a[88:91], a[120:123], v[88:91]\n\t" \
  "v_mfma_f32_16x16x32_bf16 v[120:123], a[88:91], a[124:127], v[120:123]\n\t" \
  "v_mfma_f32_16x16x32_bf16 v[120:123], a[152:155], a[108:111], v[120:123]\n\t" \
  "v_mfma_f32_16x16x32_bf16 v[28:31], a[156:159], a[96:99], v[28:31]\n\t" \
  "v_mfma_f32_16x16x32_bf16 v[28:31], a[92:95], a[112:115], v[28:31]\n\t" \
  "v_mfma_f32_16x16x32_bf16 v[60:63], a[92:95], a[116:119], v[60:63]\n\t" \
  "v_mfma_f32_16x16x32_bf16 v[60:63], a[156:159], a[100:103], v[60:63]\n\t" \
  "v_mfma_f32_16x16x32_bf16 v[92:95], a[156:159], a[104:107], v[92:95]\n\t" \
  "v_mfma_f32_16x16x32_bf16 v[92:95], a[92:95], a[120:123], v[92:95]\n\t" \
  "v_mfma_f32_16x16x32_bf16 v[124:127], a[92:95], a[124:127], v[124:127]\n\t" \
  "v_mfma_f32_16x16x32_bf16 v[124:127], a[156:159], a[108:111], v[124:127]\n\t" \
  "s_waitcnt vmcnt(17) lgkmcnt(0)\n\t" \
  "s_barrier\n\t" \
  "v_mfma_f32_16x16x32_bf16 v[0:3], a[160:163], a[192:195], v[0:3]\n\t" \
  "buffer_load_dwordx4 v230, %[sV], s96 offen offset:2048 lds\n\t" \
  "v_mfma_f32_16x16x32_bf16 v[0:3], a[0:3], a[208:211], v[0:3]\n\t" \
  "ds_read_b64_tr_b16 a[128:129], v192 offset:0\n\t" \
  "v_mfma_f32_16x16x32_bf16 v[32:35], a[0:3], a[212:215], v[32:35]\n\t" \
  "ds_read_b64_tr_b16 a[130:131], v192 offset:1024\n\t" \
  "v_mfma_f32_16x16x32_bf16 v[32:35], a[160:163], a[196:199], v[32:35]\n\t" \
  "ds_read_b64_tr_b16 a[132:133], v193 offset:0\n\t" \
  "v_mfma_f32_16x16x32_bf16 v[64:67], a[160:163], a[200:203], v[64:67]\n\t" \
  "v_mfma_f32_16x16x32_bf16 v[64:67], a[0:3], a[216:219], v[64:67]\n\t" \
  "ds_read_b64_tr_b16 a[134:135], v193 offset:1024\n\t" \
  "v_mfma_f32_16x16x32_bf16 v[96:99], a[0:3], a[220:223], v[96:99]\n\t" \
  "ds_read_b64_tr_b16 a[136:137], v194 offset:0\n\t" \
  "v_mfma_f32_16x16x32_bf16 v[96:99], a[160:163], a[204:207], v[96:99]\n\t" \
  "buffer_load_dwordx4 v231, %[sV], s96 offen offset:3072 lds\n\t" \
  "v_mfma_f32_16x16x32_bf16 v[4:7], a[164:167], a[192:195], v[4:7]\n\t" \
  "ds_read_b64_tr_b16 a[138:139], v194 offset:1024\n\t" \
  "v_mfma_f32_16x16x32_bf16 v[4:7], a[4:7], a[208:211], v[4:7]\n\t" \
  "ds_read_b64_tr_b16 a[140:141], v195 offset:0\n\t" \
  "v_mfma_f32_16x16x32_bf16 v[36:39], a[4:7], a[212:215], v[36:39]\n\t" \
  "v_mfma_f32_16x16x32_bf16 v[36:39], a[164:167], a[196:199], v[36:39]\n\t" \
  "s_add_u32 m0, %[mw], 32768\n\t" \
  "ds_read_b64_tr_b16 a[142:143], v195 offset:1024\n\t" \
  "v_mfma_f32_16x16x32_bf16 v[68:71], a[164:167], a[200:203], v[68:71]\n\t" \
  "s_add_u32 s96, %[sofk], 16384\n\t" \
  "ds_read_b64_tr_b16 a[144:145], v196 offset:0\n\t" \
  "v_mfma_f32_16x16x32_bf16 v[68:71], a[4:7], a[216:219], v[68:71]\n\t" \
  "v_mfma_f32_16x16x32_bf16 v[100:103], a[4:7], a[220:223], v[100:103]\n\t" \
  "ds_read_b64_tr_b16 a[146:147], v196 offset:1024\n\t" \
  "v_mfma_f32_16x16x32_bf16 v[100:103], a[164:167], a[204:207], v[100:103]\n\t" \
  "buffer_load_dwordx4 v224, %[sK], s96 offen offset:0 lds\n\t" \
  "v_mfma_f32_16x16x32_bf16 v[8:11], a[168:171], a[192:195], v[8:11]\n\t" \
  "ds_read_b64_tr_b16 a[148:149], v197 offset:0\n\t" \
  "v_mfma_f32_16x16x32_bf16 v[8:11], a[8:11], a[208:211], v[8:11]\n\t" \
  "ds_read_b64_tr_b16 a[150:151], v197 offset:1024\n\t" \
  "v_mfma_f32_16x16x32_bf16 v[40:43], a[8:11], a[212:215], v[40:43]\n\t" \
  "ds_read_b64_tr_b16 a[152:153], v198 offset:0\n\t" \
  "v_mfma_f32_16x16x32_bf16 v[40:43], a[168:171], a[196:199], v[40:43]\n\t" \
  "v_mfma_f32_16x16x32_bf16 v[72:75], a[168:171], a[200:203], v[72:75]\n\t" \
  "ds_read_b64_tr_b16 a[154:155], v198 offset:1024\n\t" \
  "v_mfma_f32_16x16x32_bf16 v[72:75], a[8:11], a[216:219], v[72:75]\n\t" \
  "ds_read_b64_tr_b16 a[156:157], v199 offset:0\n\t" \
  "v_mfma_f32_16x16x32_bf16 v[104:107], a[8:11], a[220:223], v[104:107]\n\t" \
  "buffer_load_dwordx4 v225, %[sK], s96 offen offset:1024 lds\n\t" \
  "v_mfma_f32_16x16x32_bf16 v[104:107], a[168:171], a[204:207], v[104:107]\n\t" \
  "ds_read_b64_tr_b16 a[158:159], v199 offset:1024\n\t" \
  "v_mfma_f32_16x16x32_bf16 v[12:15], a[172:175], a[192:195], v[12:15]\n\t" \
  "ds_read_b64_tr_b16 a[64:65], v192 offset:8192\n\t" \
  "v_mfma_f32_16x16x32_bf16 v[12:15], a[12:15], a[208:211], v[12:15]\n\t" \
  "v_mfma_f32_16x16x32_bf16 v[44:47], a[12:15], a[212:215], v[44:47]\n\t" \
  "ds_read_b64_tr_b16 a[66:67], v192 offset:9216\n\t" \
  "v_mfma_f32_16x16x32_bf16 v[44:47], a[172:175], a[196:199], v[44:47]\n\t" \
  "ds_read_b64_tr_b16 a[68:69], v193 offset:8192\n\t" \
  "v_mfma_f32_16x16x32_bf16 v[76:79], a[172:175], a[200:203], v[76:79]\n\t" \
  "v_mfma_f32_16x16x32_bf16 v[76:79], a[12:15], a[216:219], v[76:79]\n\t" \
  "ds_read_b64_tr_b16 a[70:71], v193 offset:9216\n\t" \
  "v_mfma_f32_16x16x32_bf16 v[108:111], a[12:15], a[220:223], v[108:111]\n\t" \
  "buffer_load_dwordx4 v226, %[sK], s96 offen offset:2048 lds\n\t" \
  "v_mfma_f32_16x16x32_bf16 v[108:111], a[172:175], a[204:207], v[108:111]\n\t" \
  "ds_read_b64_tr_b16 a[72:73], v194 offset:8192\n\t" \
  "v_mfma_f32_16x16x32_bf16 v[16:19], a[176:179], a[192:195], v[16:19]\n\t" \
  "ds_read_b64_tr_b16 a[74:75], v194 offset:9216\n\t" \
  "v_mfma_f32_16x16x32_bf16 v[16:19], a[16:19], a[208:211], v[16:19]\n\t" \
  "ds_read_b64_tr_b16 a[76:77], v195 offset:8192\n\t" \
  "v_mfma_f32_16x16x32_bf16 v[48:51], a[16:19], a[212:215], v[48:51]\n\t" \
  "v_mfma_f32_16x16x32_bf16 v[48:51], a[176:179], a[196:199], v[48:51]\n\t" \
  "ds_read_b64_tr_b16 a[78:79], v195 offset:9216\n\t" \
  "v_mfma_f32_16x16x32_bf16 v[80:83], a[176:179], a[200:203], v[80:83]\n\t" \
  "ds_read_b64_tr_b16 a[80:81], v196 offset:8192\n\t" \
  "v_mfma_f32_16x16x32_bf16 v[80:83], a[16:19], a[216:219], v[80:83]\n\t" \
  "buffer_load_dwordx4 v227, %[sK], s96 offen offset:3072 lds\n\t" \
  "v_mfma_f32_16x16x32_bf16 v[112:115], a[16:19], a[220:223], v[112:115]\n\t" \
  "ds_read_b64_tr_b16 a[82:83], v196 offset:9216\n\t" \
  "v_mfma_f32_16x16x32_bf16 v[112:115], a[176:179], a[204:207], v[112:115]\n\t" \
  "ds_read_b64_tr_b16 a[84:85], v197 offset:8192\n\t" \
  "v_mfma_f32_16x16x32_bf16 v[20:23], a[180:183], a[192:195], v[20:23]\n\t" \
  "v_mfma_f32_16x16x32_bf16 v[20:23], a[20:23], a[208:211], v[20:23]\n\t" \
  "s_add_u32 s99, %[pf4], 0\n\t" \
  "ds_read_b64_tr_b16 a[86:87], v197 offset:9216\n\t" \
  "v_mfma_f32_16x16x32_bf16 v[52:55], a[20:23], a[212:215], v[52:55]\n\t" \
  "ds_read_b64_tr_b16 a[88:89], v198 offset:8192\n\t" \
  "v_mfma_f32_16x16x32_bf16 v[52:55], a[180:183], a[196:199], v[52:55]\n\t" \
  "v_mfma_f32_16x16x32_bf16 v[84:87], a[180:183], a[200:203], v[84:87]\n\t" \
  "ds_read_b64_tr_b16 a[90:91], v198 offset:9216\n\t" \
  "v_mfma_f32_16x16x32_bf16 v[84:87], a[20:23], a[216:219], v[84:87]\n\t" \
  "buffer_load_dword %[pf], v245, %[sU], s99 offen\n\t" \
  "v_mfma_f32_16x16x32_bf16 v[116:119], a[20:23], a[220:223], v[116:119]\n\t" \
  "ds_read_b64_tr_b16 a[92:93], v199 offset:8192\n\t" \
  "v_mfma_f32_16x16x32_bf16 v[116:119], a[180:183], a[204:207], v[116:119]\n\t" \
  "ds_read_b64_tr_b16 a[94:95], v199 offset:9216\n\t" \
  "v_mfma_f32_16x16x32_bf16 v[24:27], a[184:187], a[192:195], v[24:27]\n\t" \
  "s_add_u32 s99, %[pf4], 14336\n\t" \
  "v_mfma_f32_16x16x32_bf16 v[24:27], a[24:27], a[208:211], v[24:27]\n\t" \
  "v_mfma_f32_16x16x32_bf16 v[56:59], a[24:27], a[212:215], v[56:59]\n\t" \
  "v_mfma_f32_16x16x32_bf16 v[56:59], a[184:187], a[196:199], v[56:59]\n\t" \
  "v_mfma_f32_16x16x32_bf16 v[88:91], a[184:187], a[200:203], v[88:91]\n\t" \
  "buffer_load_dword %[pf], v245, %[sU], s99 offen\n\t" \
  "v_mfma_f32_16x16x32_bf16 v[88:91], a[24:27], a[216:219], v[88:91]\n\t" \
  "v_mfma_f32_16x16x32_bf16 v[120:123], a[24:27], a[220:223], v[120:123]\n\t" \
  "v_mfma_f32_16x16x32_bf16 v[120:123], a[184:187], a[204:207], v[120:123]\n\t" \
  "v_mfma_f32_16x16x32_bf16 v[28:31], a[188:191], a[192:195], v[28:31]\n\t" \
  "v_mfma_f32_16x16x32_bf16 v[28:31], a[28:31], a[208:211], v[28:31]\n\t" \
  "v_mfma_f32_16x16x32_bf16 v[60:63], a[28:31], a[212:215], v[60:63]\n\t" \
  "v_mfma_f32_16x16x32_bf16 v[60:63], a[188:191], a[196:199], v[60:63]\n\t" \
  "v_mfma_f32_16x16x32_bf16 v[92:95], a[188:191], a[200:203], v[92:95]\n\t" \
  "v_mfma_f32_16x16x32_bf16 v[92:95], a[28:31], a[216:219], v[92:95]\n\t" \
  "v_mfma_f32_16x16x32_bf16 v[124:127], a[28:31], a[220:223], v[124:127]\n\t" \
  "v_mfma_f32_16x16x32_bf16 v[124:127], a[188:191], a[204:207], v[124:127]\n\t" \
  "s_waitcnt vmcnt(17)\n\t" \
  "s_waitcnt lgkmcnt(0)\n\t" \
  FSTP("s_waitcnt lgkmcnt(0)\n\t") \
  FSTP("v_writelane_b32 %[stv], s94, 52\n\t") \
  FSTP("v_writelane_b32 %[stv], s92, 53\n\t") \
  ".subsection 1\n\t" \
  ".Lfq_s:\n\t" \
  "s_mov_b32 s97, 0\n\t" \
  ".Lfq_r:\n\t" \
  "buffer_load_dword %[fl], v210, %[ws], %[fo] offen sc1\n\t" \
  "s_waitcnt vmcnt(0)\n\t" \
  "v_cmp_ne_u32 vcc, v211, %[fl]\n\t" \
  "s_cbranch_vccz .Lfq_k\n\t" \
  "s_add_u32 s97, s97, 1\n\t" \
  "s_cmp_lt_u32 s97, " FQ_CAP "\n\t" \
  "s_cbranch_scc1 .Lfq_r\n\t" \
  "buffer_store_dword v211, off, %[ws], 0 offset:256 sc1\n\t" \
  "s_branch .Lfq_k\n\t" \
  ".subsection 0\n\t"
#define FQ_CL "memory", "scc", "vcc", "s96", "s97", "s98", "s99", "v128", "v129", "v130", "v131", "v132", "v133", "v134", "v135", "v136", "v137", "v138", "v139", "v140", "v141", "v142", "v143", "v144", "v145", "v146", "v147", "v148", "v149", "v150", "v151", "v152", "v153", "v154", "v155", "v156", "v157", "v158", "v159", "v176", "v177", "v178", "v179", "v180", "v181", "v182", "v183", "v249", "v250", "v251" FQ_STC
#define FQ_X2E 0
#define FQ_W1L 1
#define FQ_W2E 0
static_assert(W_VOTE == 8192 && W_FLAGR == 32768 && W_FLAGO == 131072 && W_MODB == 2228224 && W_SIL == 2359296 && W_OT == 8388608 && W_XCH == 25165824);
#define FQ_EV 0
#define FQ_TE 0
#define FQ_SD [sU] "s"(c.sU), [sD] "s"(c.sD),
#define FQ_EB 0
#define FQ_CTO
#define FQ_XD 1
#define STS(x)
#define AT_STV
#define STA(x)
#define AT_ASM \
  "s_cmp_eq_u32 %[pm], 0\n\t" \
  "s_cbranch_scc1 .Lsf_go\n\t" \
  "v_mfma_f32_16x16x32_bf16 v[28:31], a[156:159], a[224:227], v[28:31]\n\t" \
  "v_mov_b32 v216, 0x3f803f80\n\t" \
  "v_lshlrev_b32 v204, 7, v233\n\t" \
  "v_mfma_f32_16x16x32_bf16 v[28:31], a[92:95], a[240:243], v[28:31]\n\t" \
  "v_cmp_gt_u32 vcc, 16, v233\n\t" \
  "v_mov_b32 v217, 0x3f803f80\n\t" \
  "v_mfma_f32_16x16x32_bf16 v[24:27], a[152:155], a[224:227], v[24:27]\n\t" \
  "v_lshrrev_b32 v202, 3, v233\n\t" \
  "v_mov_b32 v218, 0x3f803f80\n\t" \
  "v_mfma_f32_16x16x32_bf16 v[24:27], a[88:91], a[240:243], v[24:27]\n\t" \
  "v_mul_u32_u24 v202, 11264, v202\n\t" \
  "v_mov_b32 v219, 0x3f803f80\n\t" \
  "v_mfma_f32_16x16x32_bf16 v[20:23], a[148:151], a[224:227], v[20:23]\n\t" \
  "v_cvt_pk_bf16_f32 v31, v30, v31\n\t" \
  "v_add_u32 v202, v202, v204\n\t" \
  "v_mfma_f32_16x16x32_bf16 v[20:23], a[84:87], a[240:243], v[20:23]\n\t" \
  "v_cvt_pk_bf16_f32 v30, v28, v29\n\t" \
  "v_lshlrev_b32 v242, 4, v233\n\t" \
  "v_mfma_f32_16x16x32_bf16 v[16:19], a[144:147], a[224:227], v[16:19]\n\t" \
  "s_mov_b32 s99, 0\n\t" \
  "v_bfrev_b32 v195, 1\n\t" \
  "v_cvt_pk_bf16_f32 v28, v24, v25\n\t" \
  "v_mfma_f32_16x16x32_bf16 v[16:19], a[80:83], a[240:243], v[16:19]\n\t" \
  "v_cvt_pk_bf16_f32 v29, v26, v27\n\t" \
  "v_add_u32 v237, 65536, v228\n\t" \
  "v_mfma_f32_16x16x32_bf16 v[12:15], a[140:143], a[224:227], v[12:15]\n\t" \
  "v_cndmask_b32 v204, v195, v204, vcc\n\t" \
  "v_cndmask_b32 v202, v195, v202, vcc\n\t" \
  "v_mfma_f32_16x16x32_bf16 v[12:15], a[76:79], a[240:243], v[12:15]\n\t" \
  "v_cvt_pk_bf16_f32 v27, v22, v23\n\t" \
  "v_add_u32 v238, 65536, v229\n\t" \
  "v_mfma_f32_16x16x32_bf16 v[8:11], a[136:139], a[224:227], v[8:11]\n\t" \
  "s_mov_b32 %[na], 0\n\t" \
  "v_permlane16_swap_b32 v28, v30\n\t" \
  "v_mfma_f32_16x16x32_bf16 v[8:11], a[72:75], a[240:243], v[8:11]\n\t" \
  "v_cvt_pk_bf16_f32 v26, v20, v21\n\t" \
  "v_cvt_pk_bf16_f32 v24, v16, v17\n\t" \
  "v_mfma_f32_16x16x32_bf16 v[4:7], a[132:135], a[224:227], v[4:7]\n\t" \
  "v_permlane16_swap_b32 v29, v31\n\t" \
  "v_mfma_f32_16x16x32_bf16 v[4:7], a[68:71], a[240:243], v[4:7]\n\t" \
  "v_cvt_pk_bf16_f32 v25, v18, v19\n\t" \
  "v_cvt_pk_bf16_f32 v23, v14, v15\n\t" \
  "v_mfma_f32_16x16x32_bf16 v[0:3], a[128:131], a[224:227], v[0:3]\n\t" \
  "v_cvt_pk_bf16_f32 v22, v12, v13\n\t" \
  "v_cvt_pk_bf16_f32 v20, v8, v9\n\t" \
  "v_mfma_f32_16x16x32_bf16 v[0:3], a[64:67], a[240:243], v[0:3]\n\t" \
  "s_waitcnt vmcnt(2)\n\t" \
  "s_barrier\n\t" \
  "v_mfma_f32_16x16x32_bf16 v[60:63], a[156:159], a[228:231], v[60:63]\n\t" \
  "ds_read_b128 a[0:3], v220 offset:0\n\t" \
  "v_cvt_pk_bf16_f32 v21, v10, v11\n\t" \
  "v_mfma_f32_16x16x32_bf16 v[60:63], a[92:95], a[244:247], v[60:63]\n\t" \
  "ds_read_b128 a[4:7], v221 offset:0\n\t" \
  "v_cvt_pk_bf16_f32 v19, v6, v7\n\t" \
  "v_mfma_f32_16x16x32_bf16 v[56:59], a[152:155], a[228:231], v[56:59]\n\t" \
  "s_add_u32 m0, %[m], 0\n\t" \
  "ds_read_b128 a[8:11], v222 offset:0\n\t" \
  "v_cvt_pk_bf16_f32 v18, v4, v5\n\t" \
  "v_mfma_f32_16x16x32_bf16 v[56:59], a[88:91], a[244:247], v[56:59]\n\t" \
  "s_xor_b32 s97, %[wob], 0\n\t" \
  "ds_read_b128 a[12:15], v223 offset:0\n\t" \
  "v_cvt_pk_bf16_f32 v16, v0, v1\n\t" \
  "v_mfma_f32_16x16x32_bf16 v[52:55], a[148:151], a[228:231], v[52:55]\n\t" \
  "ds_read_b128 a[16:19], v220 offset:4096\n\t" \
  "v_cvt_pk_bf16_f32 v17, v2, v3\n\t" \
  "v_mfma_f32_16x16x32_bf16 v[52:55], a[84:87], a[244:247], v[52:55]\n\t" \
  "ds_read_b128 a[20:23], v221 offset:4096\n\t" \
  "v_cvt_pk_bf16_f32 v63, v62, v63\n\t" \
  "v_mfma_f32_16x16x32_bf16 v[48:51], a[144:147], a[228:231], v[48:51]\n\t" \
  "ds_read_b128 a[24:27], v222 offset:4096\n\t" \
  "v_cvt_pk_bf16_f32 v62, v60, v61\n\t" \
  "v_mfma_f32_16x16x32_bf16 v[48:51], a[80:83], a[244:247], v[48:51]\n\t" \
  "ds_read_b128 a[28:31], v223 offset:4096\n\t" \
  "v_cvt_pk_bf16_f32 v60, v56, v57\n\t" \
  "v_mfma_f32_16x16x32_bf16 v[44:47], a[140:143], a[228:231], v[44:47]\n\t" \
  "ds_read_b128 a[32:35], v220 offset:8192\n\t" \
  "v_cvt_pk_bf16_f32 v61, v58, v59\n\t" \
  "v_mfma_f32_16x16x32_bf16 v[44:47], a[76:79], a[244:247], v[44:47]\n\t" \
  "buffer_load_dwordx4 v240, %[wo], s97 offen offset:0 lds\n\t" \
  "v_mfma_f32_16x16x32_bf16 v[40:43], a[136:139], a[228:231], v[40:43]\n\t" \
  "s_add_u32 s97, s97, 7168\n\t" \
  "ds_read_b128 a[36:39], v221 offset:8192\n\t" \
  "v_cvt_pk_bf16_f32 v59, v54, v55\n\t" \
  "v_mfma_f32_16x16x32_bf16 v[40:43], a[72:75], a[244:247], v[40:43]\n\t" \
  "ds_read_b128 a[40:43], v222 offset:8192\n\t" \
  "v_cvt_pk_bf16_f32 v58, v52, v53\n\t" \
  "v_mfma_f32_16x16x32_bf16 v[36:39], a[132:135], a[228:231], v[36:39]\n\t" \
  "ds_read_b128 a[44:47], v223 offset:8192\n\t" \
  "v_cvt_pk_bf16_f32 v56, v48, v49\n\t" \
  "v_mfma_f32_16x16x32_bf16 v[36:39], a[68:71], a[244:247], v[36:39]\n\t" \
  "buffer_load_dwordx4 v241, %[wo], s97 offen offset:1024 lds\n\t" \
  "v_mfma_f32_16x16x32_bf16 v[32:35], a[128:131], a[228:231], v[32:35]\n\t" \
  "s_add_u32 s97, s97, 7168\n\t" \
  "ds_read_b128 a[48:51], v220 offset:12288\n\t" \
  "v_cvt_pk_bf16_f32 v57, v50, v51\n\t" \
  "v_mfma_f32_16x16x32_bf16 v[32:35], a[64:67], a[244:247], v[32:35]\n\t" \
  "ds_read_b128 a[52:55], v221 offset:12288\n\t" \
  "v_cvt_pk_bf16_f32 v55, v46, v47\n\t" \
  "v_mfma_f32_16x16x32_bf16 v[92:95], a[156:159], a[232:235], v[92:95]\n\t" \
  "buffer_load_dwordx4 v240, %[wo], s97 offen offset:2048 lds\n\t" \
  "v_mfma_f32_16x16x32_bf16 v[92:95], a[92:95], a[248:251], v[92:95]\n\t" \
  "s_add_u32 s97, s97, 7168\n\t" \
  "ds_read_b128 a[56:59], v222 offset:12288\n\t" \
  "v_cvt_pk_bf16_f32 v54, v44, v45\n\t" \
  "v_mfma_f32_16x16x32_bf16 v[88:91], a[152:155], a[232:235], v[88:91]\n\t" \
  "ds_read_b128 a[60:63], v223 offset:12288\n\t" \
  "v_cvt_pk_bf16_f32 v52, v40, v41\n\t" \
  "v_mfma_f32_16x16x32_bf16 v[88:91], a[88:91], a[248:251], v[88:91]\n\t" \
  "v_permlane16_swap_b32 v24, v26\n\t" \
  "v_mfma_f32_16x16x32_bf16 v[84:87], a[148:151], a[232:235], v[84:87]\n\t" \
  "buffer_load_dwordx4 v241, %[wo], s97 offen offset:3072 lds\n\t" \
  "v_mfma_f32_16x16x32_bf16 v[84:87], a[84:87], a[248:251], v[84:87]\n\t" \
  "s_add_u32 m0, %[m], 16384\n\t" \
  "v_cvt_pk_bf16_f32 v53, v42, v43\n\t" \
  "v_cvt_pk_bf16_f32 v51, v38, v39\n\t" \
  "v_mfma_f32_16x16x32_bf16 v[80:83], a[144:147], a[232:235], v[80:83]\n\t" \
  "s_xor_b32 s97, %[wob], 131072\n\t" \
  "v_permlane16_swap_b32 v25, v27\n\t" \
  "v_mfma_f32_16x16x32_bf16 v[80:83], a[80:83], a[248:251], v[80:83]\n\t" \
  "v_cvt_pk_bf16_f32 v50, v36, v37\n\t" \
  "v_cvt_pk_bf16_f32 v48, v32, v33\n\t" \
  "v_mfma_f32_16x16x32_bf16 v[76:79], a[140:143], a[232:235], v[76:79]\n\t" \
  "v_permlane16_swap_b32 v20, v22\n\t" \
  "v_mfma_f32_16x16x32_bf16 v[76:79], a[76:79], a[248:251], v[76:79]\n\t" \
  "v_cvt_pk_bf16_f32 v49, v34, v35\n\t" \
  "v_cvt_pk_bf16_f32 v95, v94, v95\n\t" \
  "v_mfma_f32_16x16x32_bf16 v[72:75], a[136:139], a[232:235], v[72:75]\n\t" \
  "v_permlane16_swap_b32 v21, v23\n\t" \
  "v_mfma_f32_16x16x32_bf16 v[72:75], a[72:75], a[248:251], v[72:75]\n\t" \
  "v_cvt_pk_bf16_f32 v94, v92, v93\n\t" \
  "v_cvt_pk_bf16_f32 v92, v88, v89\n\t" \
  "v_mfma_f32_16x16x32_bf16 v[68:71], a[132:135], a[232:235], v[68:71]\n\t" \
  "v_permlane16_swap_b32 v16, v18\n\t" \
  "v_mfma_f32_16x16x32_bf16 v[68:71], a[68:71], a[248:251], v[68:71]\n\t" \
  "v_cvt_pk_bf16_f32 v93, v90, v91\n\t" \
  "v_cvt_pk_bf16_f32 v91, v86, v87\n\t" \
  "v_mfma_f32_16x16x32_bf16 v[64:67], a[128:131], a[232:235], v[64:67]\n\t" \
  "buffer_load_dwordx4 v240, %[wo], s97 offen offset:0 lds\n\t" \
  "v_mfma_f32_16x16x32_bf16 v[64:67], a[64:67], a[248:251], v[64:67]\n\t" \
  "s_add_u32 s97, s97, 7168\n\t" \
  "v_permlane16_swap_b32 v17, v19\n\t" \
  "v_mfma_f32_16x16x32_bf16 v[124:127], a[156:159], a[236:239], v[124:127]\n\t" \
  "v_cvt_pk_bf16_f32 v90, v84, v85\n\t" \
  "v_cvt_pk_bf16_f32 v88, v80, v81\n\t" \
  "v_mfma_f32_16x16x32_bf16 v[124:127], a[92:95], a[252:255], v[124:127]\n\t" \
  "v_permlane16_swap_b32 v60, v62\n\t" \
  "v_mfma_f32_16x16x32_bf16 v[120:123], a[152:155], a[236:239], v[120:123]\n\t" \
  "buffer_load_dwordx4 v241, %[wo], s97 offen offset:1024 lds\n\t" \
  ".Lhs5:\n\ts_cbranch_execz .Lhs5n\n\t" \
  "v_mfma_f32_16x16x32_bf16 v[120:123], a[88:91], a[252:255], v[120:123]\n\t" \
  "s_add_u32 s97, s97, 7168\n\t" \
  "v_cvt_pk_bf16_f32 v89, v82, v83\n\t" \
  "v_cvt_pk_bf16_f32 v87, v78, v79\n\t" \
  "v_mfma_f32_16x16x32_bf16 v[116:119], a[148:151], a[236:239], v[116:119]\n\t" \
  "v_permlane16_swap_b32 v61, v63\n\t" \
  "v_mfma_f32_16x16x32_bf16 v[116:119], a[84:87], a[252:255], v[116:119]\n\t" \
  "buffer_load_dwordx4 v240, %[wo], s97 offen offset:2048 lds\n\t" \
  "v_mfma_f32_16x16x32_bf16 v[112:115], a[144:147], a[236:239], v[112:115]\n\t" \
  "s_add_u32 s97, s97, 7168\n\t" \
  "v_cvt_pk_bf16_f32 v86, v76, v77\n\t" \
  "v_cvt_pk_bf16_f32 v84, v72, v73\n\t" \
  "v_mfma_f32_16x16x32_bf16 v[112:115], a[80:83], a[252:255], v[112:115]\n\t" \
  "v_permlane16_swap_b32 v56, v58\n\t" \
  "v_mfma_f32_16x16x32_bf16 v[108:111], a[140:143], a[236:239], v[108:111]\n\t" \
  "v_cvt_pk_bf16_f32 v85, v74, v75\n\t" \
  "v_cvt_pk_bf16_f32 v83, v70, v71\n\t" \
  "v_mfma_f32_16x16x32_bf16 v[108:111], a[76:79], a[252:255], v[108:111]\n\t" \
  "buffer_load_dwordx4 v241, %[wo], s97 offen offset:3072 lds\n\t" \
  "v_mfma_f32_16x16x32_bf16 v[104:107], a[136:139], a[236:239], v[104:107]\n\t" \
  "v_permlane16_swap_b32 v57, v59\n\t" \
  "v_mfma_f32_16x16x32_bf16 v[104:107], a[72:75], a[252:255], v[104:107]\n\t" \
  "v_cvt_pk_bf16_f32 v82, v68, v69\n\t" \
  "v_cvt_pk_bf16_f32 v80, v64, v65\n\t" \
  "v_mfma_f32_16x16x32_bf16 v[100:103], a[132:135], a[236:239], v[100:103]\n\t" \
  "v_permlane16_swap_b32 v52, v54\n\t" \
  "v_mfma_f32_16x16x32_bf16 v[100:103], a[68:71], a[252:255], v[100:103]\n\t" \
  "buffer_load_dword %[pfd], v247, %[ws], 0 offen sc1\n\t" \
  "v_mfma_f32_16x16x32_bf16 v[96:99], a[128:131], a[236:239], v[96:99]\n\t" \
  "s_add_u32 s98, %[pf4], 28672\n\t" \
  "v_cvt_pk_bf16_f32 v81, v66, v67\n\t" \
  "v_cvt_pk_bf16_f32 v127, v126, v127\n\t" \
  "v_mfma_f32_16x16x32_bf16 v[96:99], a[64:67], a[252:255], v[96:99]\n\t" \
  "s_waitcnt lgkmcnt(0)\n\t" \
  "s_barrier\n\t" \
  STS("s_memrealtime s[94:95]\n\t") \
  STS("s_memtime s[92:93]\n\t") \
  "v_mfma_f32_16x16x32_bf16 v[0:3], a[0:3], v[16:19], 0\n\t" \
  "s_add_u32 m0, %[m], 49152\n\t" \
  "buffer_load_dword %[pfd], v245, %[sU], s98 offen\n\t" \
  "v_mfma_f32_16x16x32_bf16 v[0:3], a[4:7], v[20:23], v[0:3]\n\t" \
  "s_add_u32 s96, %[sofk], 32768\n\t" \
  "ds_read_b128 a[192:195], v220 offset:16384\n\t" \
  "v_cvt_pk_bf16_f32 v126, v124, v125\n\t" \
  "v_mfma_f32_16x16x32_bf16 v[0:3], a[8:11], v[24:27], v[0:3]\n\t" \
  "ds_read_b128 a[196:199], v221 offset:16384\n\t" \
  "v_cvt_pk_bf16_f32 v124, v120, v121\n\t" \
  "v_mfma_f32_16x16x32_bf16 v[0:3], a[12:15], v[28:31], v[0:3]\n\t" \
  "ds_read_b128 a[200:203], v222 offset:16384\n\t" \
  "v_cvt_pk_bf16_f32 v125, v122, v123\n\t" \
  "v_permlane16_swap_b32 v53, v55\n\t" \
  "v_permlane16_swap_b32 v48, v50\n\t" \
  "v_permlane16_swap_b32 v49, v51\n\t" \
  "s_nop 1\n\t" \
  "v_mfma_f32_16x16x32_bf16 v[32:35], a[0:3], v[48:51], 0\n\t" \
  "ds_read_b128 a[204:207], v223 offset:16384\n\t" \
  "v_max3_f32 v194, v0, v1, v2\n\t" \
  "v_mfma_f32_16x16x32_bf16 v[32:35], a[4:7], v[52:55], v[32:35]\n\t" \
  "ds_read_b128 a[208:211], v220 offset:20480\n\t" \
  "v_max_f32 v194, v194, v3\n\t" \
  "v_mfma_f32_16x16x32_bf16 v[32:35], a[8:11], v[56:59], v[32:35]\n\t" \
  "ds_read_b128 a[212:215], v221 offset:20480\n\t" \
  "v_mov_b32 v195, v194\n\t" \
  "v_mfma_f32_16x16x32_bf16 v[32:35], a[12:15], v[60:63], v[32:35]\n\t" \
  "ds_read_b128 a[216:219], v222 offset:20480\n\t" \
  "v_cvt_pk_bf16_f32 v123, v118, v119\n\t" \
  "v_permlane32_swap_b32 v194, v195\n\t" \
  "v_max_f32 v194, v194, v195\n\t" \
  "v_mov_b32 v195, v194\n\t" \
  "s_nop 1\n\t" \
  "v_permlane16_swap_b32 v194, v195\n\t" \
  "v_max_f32 v194, v194, v195\n\t" \
  "v_sub_f32 v176, 0xc2c80000, v194\n\t" \
  "v_sub_f32 v177, 0xc2c80000, v194\n\t" \
  "v_sub_f32 v178, 0xc2c80000, v194\n\t" \
  "v_sub_f32 v179, 0xc2c80000, v194\n\t" \
  "s_nop 1\n\t" \
  "v_mfma_f32_16x16x32_bf16 v[4:7], a[16:19], v[16:19], v[176:179]\n\t" \
  "ds_read_b128 a[220:223], v223 offset:20480\n\t" \
  "v_add_f32 v0, v0, v176\n\t" \
  "v_mfma_f32_16x16x32_bf16 v[4:7], a[20:23], v[20:23], v[4:7]\n\t" \
  "ds_read_b128 a[224:227], v220 offset:24576\n\t" \
  "v_add_f32 v1, v1, v176\n\t" \
  "v_mfma_f32_16x16x32_bf16 v[4:7], a[24:27], v[24:27], v[4:7]\n\t" \
  "ds_read_b128 a[228:231], v221 offset:24576\n\t" \
  "v_add_f32 v2, v2, v176\n\t" \
  "v_mfma_f32_16x16x32_bf16 v[4:7], a[28:31], v[28:31], v[4:7]\n\t" \
  "v_add_f32 v3, v3, v176\n\t" \
  "v_max3_f32 v194, v32, v33, v34\n\t" \
  "v_mfma_f32_16x16x32_bf16 v[8:11], a[32:35], v[16:19], v[176:179]\n\t" \
  STS("v_writelane_b32 %[stv], s94, 18\n\t") \
  STS("v_writelane_b32 %[stv], s92, 19\n\t") \
  "ds_read_b128 a[232:235], v222 offset:24576\n\t" \
  "v_max_f32 v194, v194, v35\n\t" \
  "v_mfma_f32_16x16x32_bf16 v[8:11], a[36:39], v[20:23], v[8:11]\n\t" \
  "v_mov_b32 v195, v194\n\t" \
  "v_cvt_pk_bf16_f32 v122, v116, v117\n\t" \
  "v_mfma_f32_16x16x32_bf16 v[8:11], a[40:43], v[24:27], v[8:11]\n\t" \
  "v_permlane32_swap_b32 v194, v195\n\t" \
  "v_mfma_f32_16x16x32_bf16 v[8:11], a[44:47], v[28:31], v[8:11]\n\t" \
  "ds_read_b128 a[236:239], v223 offset:24576\n\t" \
  "v_max_f32 v194, v194, v195\n\t" \
  "v_mfma_f32_16x16x32_bf16 v[12:15], a[48:51], v[16:19], v[176:179]\n\t" \
  "ds_read_b128 a[240:243], v220 offset:28672\n\t" \
  "v_mov_b32 v195, v194\n\t" \
  "v_mfma_f32_16x16x32_bf16 v[12:15], a[52:55], v[20:23], v[12:15]\n\t" \
  "buffer_load_dwordx4 v224, %[sK], s96 offen offset:0 lds\n\t" \
  "v_mfma_f32_16x16x32_bf16 v[12:15], a[56:59], v[24:27], v[12:15]\n\t" \
  "v_permlane16_swap_b32 v194, v195\n\t" \
  "v_mfma_f32_16x16x32_bf16 v[12:15], a[60:63], v[28:31], v[12:15]\n\t" \
  "ds_read_b128 a[244:247], v221 offset:28672\n\t" \
  "v_max_f32 v194, v194, v195\n\t" \
  "v_permlane16_swap_b32 v92, v94\n\t" \
  "v_permlane16_swap_b32 v93, v95\n\t" \
  "v_permlane16_swap_b32 v88, v90\n\t" \
  "v_permlane16_swap_b32 v89, v91\n\t" \
  "v_permlane16_swap_b32 v84, v86\n\t" \
  "v_permlane16_swap_b32 v85, v87\n\t" \
  "v_permlane16_swap_b32 v80, v82\n\t" \
  "v_permlane16_swap_b32 v81, v83\n\t" \
  "s_nop 1\n\t" \
  "v_mfma_f32_16x16x32_bf16 v[64:67], a[0:3], v[80:83], 0\n\t" \
  "ds_read_b128 a[248:251], v222 offset:28672\n\t" \
  "v_sub_f32 v180, 0xc2c80000, v194\n\t" \
  "v_mfma_f32_16x16x32_bf16 v[64:67], a[4:7], v[84:87], v[64:67]\n\t" \
  "v_sub_f32 v181, 0xc2c80000, v194\n\t" \
  "v_sub_f32 v182, 0xc2c80000, v194\n\t" \
  "v_mfma_f32_16x16x32_bf16 v[64:67], a[8:11], v[88:91], v[64:67]\n\t" \
  "ds_read_b128 a[252:255], v223 offset:28672\n\t" \
  "v_sub_f32 v183, 0xc2c80000, v194\n\t" \
  "v_mfma_f32_16x16x32_bf16 v[64:67], a[12:15], v[92:95], v[64:67]\n\t" \
  "v_add_f32 v32, v32, v180\n\t" \
  "v_add_f32 v33, v33, v180\n\t" \
  "v_mfma_f32_16x16x32_bf16 v[36:39], a[16:19], v[48:51], v[180:183]\n\t" \
  "v_add_f32 v34, v34, v180\n\t" \
  "v_add_f32 v35, v35, v180\n\t" \
  "v_mfma_f32_16x16x32_bf16 v[36:39], a[20:23], v[52:55], v[36:39]\n\t" \
  "buffer_load_dwordx4 v225, %[sK], s96 offen offset:1024 lds\n\t" \
  "v_mfma_f32_16x16x32_bf16 v[36:39], a[24:27], v[56:59], v[36:39]\n\t" \
  "v_max3_f32 v194, v64, v65, v66\n\t" \
  "v_max_f32 v194, v194, v67\n\t" \
  "v_mfma_f32_16x16x32_bf16 v[36:39], a[28:31], v[60:63], v[36:39]\n\t" \
  "v_mov_b32 v195, v194\n\t" \
  "v_cvt_pk_bf16_f32 v120, v112, v113\n\t" \
  "v_mfma_f32_16x16x32_bf16 v[40:43], a[32:35], v[48:51], v[180:183]\n\t" \
  "v_permlane32_swap_b32 v194, v195\n\t" \
  "v_mfma_f32_16x16x32_bf16 v[40:43], a[36:39], v[52:55], v[40:43]\n\t" \
  "v_max_f32 v194, v194, v195\n\t" \
  "v_mov_b32 v195, v194\n\t" \
  "v_mfma_f32_16x16x32_bf16 v[40:43], a[40:43], v[56:59], v[40:43]\n\t" \
  "buffer_load_dwordx4 v226, %[sK], s96 offen offset:2048 lds\n\t" \
  "v_mfma_f32_16x16x32_bf16 v[40:43], a[44:47], v[60:63], v[40:43]\n\t" \
  "v_permlane16_swap_b32 v194, v195\n\t" \
  "v_mfma_f32_16x16x32_bf16 v[44:47], a[48:51], v[48:51], v[180:183]\n\t" \
  "v_max_f32 v194, v194, v195\n\t" \
  "v_sub_f32 v184, 0xc2c80000, v194\n\t" \
  "v_mfma_f32_16x16x32_bf16 v[44:47], a[52:55], v[52:55], v[44:47]\n\t" \
  "v_sub_f32 v185, 0xc2c80000, v194\n\t" \
  "v_sub_f32 v186, 0xc2c80000, v194\n\t" \
  "v_mfma_f32_16x16x32_bf16 v[44:47], a[56:59], v[56:59], v[44:47]\n\t" \
  "v_sub_f32 v187, 0xc2c80000, v194\n\t" \
  "v_add_f32 v64, v64, v184\n\t" \
  "v_mfma_f32_16x16x32_bf16 v[44:47], a[60:63], v[60:63], v[44:47]\n\t" \
  "ds_read_b64_tr_b16 a[128:129], v228 offset:0\n\t" \
  "v_add_f32 v65, v65, v184\n\t" \
  "v_cvt_pk_bf16_f32 v121, v114, v115\n\t" \
  "v_cvt_pk_bf16_f32 v119, v110, v111\n\t" \
  "v_cvt_pk_bf16_f32 v118, v108, v109\n\t" \
  "v_cvt_pk_bf16_f32 v116, v104, v105\n\t" \
  "v_cvt_pk_bf16_f32 v117, v106, v107\n\t" \
  "v_cvt_pk_bf16_f32 v115, v102, v103\n\t" \
  "v_cvt_pk_bf16_f32 v114, v100, v101\n\t" \
  "v_cvt_pk_bf16_f32 v112, v96, v97\n\t" \
  "v_cvt_pk_bf16_f32 v113, v98, v99\n\t" \
  "v_permlane16_swap_b32 v124, v126\n\t" \
  "v_permlane16_swap_b32 v125, v127\n\t" \
  "v_permlane16_swap_b32 v120, v122\n\t" \
  "v_permlane16_swap_b32 v121, v123\n\t" \
  "v_permlane16_swap_b32 v116, v118\n\t" \
  "v_permlane16_swap_b32 v117, v119\n\t" \
  "v_permlane16_swap_b32 v112, v114\n\t" \
  "v_permlane16_swap_b32 v113, v115\n\t" \
  "s_nop 1\n\t" \
  "v_mfma_f32_16x16x32_bf16 v[96:99], a[0:3], v[112:115], 0\n\t" \
  "ds_read_b64_tr_b16 a[130:131], v228 offset:4096\n\t" \
  "v_add_f32 v66, v66, v184\n\t" \
  "v_mfma_f32_16x16x32_bf16 v[96:99], a[4:7], v[116:119], v[96:99]\n\t" \
  "ds_read_b64_tr_b16 a[132:133], v228 offset:8192\n\t" \
  "v_add_f32 v67, v67, v184\n\t" \
  "v_mfma_f32_16x16x32_bf16 v[96:99], a[8:11], v[120:123], v[96:99]\n\t" \
  "buffer_load_dwordx4 v227, %[sK], s96 offen offset:3072 lds\n\t" \
  "v_mfma_f32_16x16x32_bf16 v[96:99], a[12:15], v[124:127], v[96:99]\n\t" \
  "s_add_u32 m0, %[m], 147456\n\t" \
  "v_exp_f32 v0, v0\n\t" \
  "v_mfma_f32_16x16x32_bf16 v[68:71], a[16:19], v[80:83], v[184:187]\n\t" \
  "s_add_u32 s96, %[sofk], 16384\n\t" \
  "v_exp_f32 v1, v1\n\t" \
  "v_mfma_f32_16x16x32_bf16 v[68:71], a[20:23], v[84:87], v[68:71]\n\t" \
  "v_exp_f32 v2, v2\n\t" \
  "v_mfma_f32_16x16x32_bf16 v[68:71], a[24:27], v[88:91], v[68:71]\n\t" \
  "ds_read_b64_tr_b16 a[134:135], v228 offset:12288\n\t" \
  "v_max3_f32 v194, v96, v97, v98\n\t" \
  "v_mfma_f32_16x16x32_bf16 v[68:71], a[28:31], v[92:95], v[68:71]\n\t" \
  "ds_read_b64_tr_b16 a[136:137], v229 offset:0\n\t" \
  "v_max_f32 v194, v194, v99\n\t" \
  "v_mfma_f32_16x16x32_bf16 v[72:75], a[32:35], v[80:83], v[184:187]\n\t" \
  "ds_read_b64_tr_b16 a[138:139], v229 offset:4096\n\t" \
  "v_mov_b32 v195, v194\n\t" \
  "v_mfma_f32_16x16x32_bf16 v[72:75], a[36:39], v[84:87], v[72:75]\n\t" \
  "v_exp_f32 v3, v3\n\t" \
  "v_mfma_f32_16x16x32_bf16 v[72:75], a[40:43], v[88:91], v[72:75]\n\t" \
  "v_permlane32_swap_b32 v194, v195\n\t" \
  "v_mfma_f32_16x16x32_bf16 v[72:75], a[44:47], v[92:95], v[72:75]\n\t" \
  "ds_read_b64_tr_b16 a[140:141], v229 offset:8192\n\t" \
  "v_max_f32 v194, v194, v195\n\t" \
  "v_mfma_f32_16x16x32_bf16 v[76:79], a[48:51], v[80:83], v[184:187]\n\t" \
  "ds_read_b64_tr_b16 a[142:143], v229 offset:12288\n\t" \
  "v_mov_b32 v195, v194\n\t" \
  "v_mfma_f32_16x16x32_bf16 v[76:79], a[52:55], v[84:87], v[76:79]\n\t" \
  "buffer_load_dwordx4 v230, %[sV], s96 offen offset:0 lds\n\t" \
  "v_mfma_f32_16x16x32_bf16 v[76:79], a[56:59], v[88:91], v[76:79]\n\t" \
  "v_permlane16_swap_b32 v194, v195\n\t" \
  "v_mfma_f32_16x16x32_bf16 v[76:79], a[60:63], v[92:95], v[76:79]\n\t" \
  "ds_read_b64_tr_b16 a[144:145], v228 offset:256\n\t" \
  "v_max_f32 v194, v194, v195\n\t" \
  "v_sub_f32 v188, 0xc2c80000, v194\n\t" \
  "v_sub_f32 v189, 0xc2c80000, v194\n\t" \
  "v_sub_f32 v190, 0xc2c80000, v194\n\t" \
  "v_sub_f32 v191, 0xc2c80000, v194\n\t" \
  "s_nop 1\n\t" \
  "v_mfma_f32_16x16x32_bf16 v[100:103], a[16:19], v[112:115], v[188:191]\n\t" \
  "ds_read_b64_tr_b16 a[146:147], v228 offset:4352\n\t" \
  "v_add_f32 v96, v96, v188\n\t" \
  "v_mfma_f32_16x16x32_bf16 v[100:103], a[20:23], v[116:119], v[100:103]\n\t" \
  "ds_read_b64_tr_b16 a[148:149], v228 offset:8448\n\t" \
  "v_add_f32 v97, v97, v188\n\t" \
  "v_mfma_f32_16x16x32_bf16 v[100:103], a[24:27], v[120:123], v[100:103]\n\t" \
  "v_add_f32 v98, v98, v188\n\t" \
  "v_add_f32 v99, v99, v188\n\t" \
  "v_mfma_f32_16x16x32_bf16 v[100:103], a[28:31], v[124:127], v[100:103]\n\t" \
  "buffer_load_dwordx4 v231, %[sV], s96 offen offset:1024 lds\n\t" \
  "v_mfma_f32_16x16x32_bf16 v[104:107], a[32:35], v[112:115], v[188:191]\n\t" \
  "ds_read_b64_tr_b16 a[150:151], v228 offset:12544\n\t" \
  "v_cvt_pk_bf16_f32 v128, v0, v1\n\t" \
  "v_mfma_f32_16x16x32_bf16 v[104:107], a[36:39], v[116:119], v[104:107]\n\t" \
  "v_exp_f32 v4, v4\n\t" \
  "v_mfma_f32_16x16x32_bf16 v[104:107], a[40:43], v[120:123], v[104:107]\n\t" \
  "v_exp_f32 v5, v5\n\t" \
  "v_mfma_f32_16x16x32_bf16 v[104:107], a[44:47], v[124:127], v[104:107]\n\t" \
  "v_exp_f32 v6, v6\n\t" \
  "v_mfma_f32_16x16x32_bf16 v[108:111], a[48:51], v[112:115], v[188:191]\n\t" \
  "buffer_load_dwordx4 v230, %[sV], s96 offen offset:2048 lds\n\t" \
  "v_mfma_f32_16x16x32_bf16 v[108:111], a[52:55], v[116:119], v[108:111]\n\t" \
  "v_exp_f32 v7, v7\n\t" \
  "v_mfma_f32_16x16x32_bf16 v[108:111], a[56:59], v[120:123], v[108:111]\n\t" \
  "ds_read_b64_tr_b16 a[152:153], v229 offset:256\n\t" \
  "v_cvt_pk_bf16_f32 v129, v2, v3\n\t" \
  "v_mfma_f32_16x16x32_bf16 v[108:111], a[60:63], v[124:127], v[108:111]\n\t" \
  "s_waitcnt lgkmcnt(13)\n\t" \
  "v_exp_f32 v8, v8\n\t" \
  STA("s_memrealtime s[94:95]\n\t") \
  STA("s_memtime s[92:93]\n\t") \
  "v_mfma_f32_16x16x32_bf16 v[0:3], a[192:195], v[16:19], v[176:179]\n\t" \
  "v_exp_f32 v9, v9\n\t" \
  "v_mfma_f32_16x16x32_bf16 v[0:3], a[196:199], v[20:23], v[0:3]\n\t" \
  "ds_read_b64_tr_b16 a[154:155], v229 offset:4352\n\t" \
  "v_cvt_pk_bf16_f32 v130, v4, v5\n\t" \
  "v_mfma_f32_16x16x32_bf16 v[0:3], a[200:203], v[24:27], v[0:3]\n\t" \
  "buffer_load_dwordx4 v231, %[sV], s96 offen offset:3072 lds\n\t" \
  "v_mfma_f32_16x16x32_bf16 v[0:3], a[204:207], v[28:31], v[0:3]\n\t" \
  "ds_read_b64_tr_b16 a[156:157], v229 offset:8448\n\t" \
  "v_cvt_pk_bf16_f32 v131, v6, v7\n\t" \
  "v_mfma_f32_16x16x32_bf16 v[4:7], a[220:223], v[28:31], v[176:179]\n\t" \
  "v_exp_f32 v10, v10\n\t" \
  "v_mfma_f32_16x16x32_bf16 v[4:7], a[216:219], v[24:27], v[4:7]\n\t" \
  "s_add_u32 s98, %[pf4], 43008\n\t" \
  "v_exp_f32 v11, v11\n\t" \
  "v_mfma_f32_16x16x32_bf16 v[4:7], a[212:215], v[20:23], v[4:7]\n\t" \
  "v_exp_f32 v12, v12\n\t" \
  "v_mfma_f32_16x16x32_bf16 v[4:7], a[208:211], v[16:19], v[4:7]\n\t" \
  "v_exp_f32 v13, v13\n\t" \
  "v_cvt_pk_bf16_f32 v132, v8, v9\n\t" \
  "v_cvt_pk_bf16_f32 v133, v10, v11\n\t" \
  "v_mfma_f32_16x16x32_bf16 v[8:11], a[224:227], v[16:19], v[176:179]\n\t" \
  "v_exp_f32 v14, v14\n\t" \
  "v_mfma_f32_16x16x32_bf16 v[8:11], a[228:231], v[20:23], v[8:11]\n\t" \
  "v_exp_f32 v15, v15\n\t" \
  "v_mfma_f32_16x16x32_bf16 v[8:11], a[232:235], v[24:27], v[8:11]\n\t" \
  "buffer_load_dword %[pfd], v245, %[sU], s98 offen\n\t" \
  "v_mfma_f32_16x16x32_bf16 v[8:11], a[236:239], v[28:31], v[8:11]\n\t" \
  "v_exp_f32 v32, v32\n\t" \
  "v_cvt_pk_bf16_f32 v134, v12, v13\n\t" \
  "v_cvt_pk_bf16_f32 v135, v14, v15\n\t" \
  "v_mfma_f32_16x16x32_bf16 v[12:15], a[252:255], v[28:31], v[176:179]\n\t" \
  STA("v_writelane_b32 %[stv], s94, 26\n\t") \
  STA("v_writelane_b32 %[stv], s92, 27\n\t") \
  "s_add_u32 s98, %[pf4], 0\n\t" \
  "v_exp_f32 v33, v33\n\t" \
  "v_mfma_f32_16x16x32_bf16 v[12:15], a[248:251], v[24:27], v[12:15]\n\t" \
  "v_exp_f32 v34, v34\n\t" \
  "v_mfma_f32_16x16x32_bf16 v[12:15], a[244:247], v[20:23], v[12:15]\n\t" \
  "v_exp_f32 v35, v35\n\t" \
  "v_mfma_f32_16x16x32_bf16 v[12:15], a[240:243], v[16:19], v[12:15]\n\t" \
  "buffer_load_dword %[pfd], v245, %[sG], s98 offen\n\t" \
  "v_cvt_pk_bf16_f32 v136, v32, v33\n\t" \
  "v_cvt_pk_bf16_f32 v137, v34, v35\n\t" \
  "v_mfma_f32_16x16x32_bf16 v[32:35], a[192:195], v[48:51], v[180:183]\n\t" \
  "ds_read_b64_tr_b16 a[158:159], v229 offset:12544\n\t" \
  "ds_read_b64_tr_b16 a[160:161], v228 offset:512\n\t" \
  "v_exp_f32 v36, v36\n\t" \
  "v_mfma_f32_16x16x32_bf16 v[32:35], a[196:199], v[52:55], v[32:35]\n\t" \
  "v_exp_f32 v37, v37\n\t" \
  "v_mfma_f32_16x16x32_bf16 v[32:35], a[200:203], v[56:59], v[32:35]\n\t" \
  "v_exp_f32 v38, v38\n\t" \
  "v_mfma_f32_16x16x32_bf16 v[32:35], a[204:207], v[60:63], v[32:35]\n\t" \
  "s_add_u32 s98, %[pf4], 14336\n\t" \
  "v_exp_f32 v39, v39\n\t" \
  "v_cvt_pk_bf16_f32 v138, v36, v37\n\t" \
  "v_cvt_pk_bf16_f32 v139, v38, v39\n\t" \
  "v_lshl_add_u32 v207, v233, 14, %[cto]\n\t" \
  "v_min_u32 v207, .Lfz_z-.Lat_t0-4, v207\n\t" \
  "v_mfma_f32_16x16x32_bf16 v[36:39], a[220:223], v[60:63], v[180:183]\n\t" \
  "v_exp_f32 v40, v40\n\t" \
  "v_mfma_f32_16x16x32_bf16 v[36:39], a[216:219], v[56:59], v[36:39]\n\t" \
  "buffer_load_dword %[pfd], v245, %[sG], s98 offen\n\t" \
  "v_exp_f32 v41, v41\n\t" \
  "s_getpc_b64 s[96:97]\n\t" \
  ".Lat_t0:\n\t" \
  "global_load_dword %[pfd], v207, s[96:97]\n\t" \
  "v_mfma_f32_16x16x32_bf16 v[36:39], a[212:215], v[52:55], v[36:39]\n\t" \
  "ds_read_b64_tr_b16 a[162:163], v228 offset:4608\n\t" \
  "v_cvt_pk_bf16_f32 v140, v40, v41\n\t" \
  "v_exp_f32 v42, v42\n\t" \
  "v_mfma_f32_16x16x32_bf16 v[36:39], a[208:211], v[48:51], v[36:39]\n\t" \
  "ds_read_b64_tr_b16 a[164:165], v228 offset:8704\n\t" \
  "ds_read_b64_tr_b16 a[166:167], v228 offset:12800\n\t" \
  "v_exp_f32 v43, v43\n\t" \
  "s_nop 0\n\t" \
  "v_cvt_pk_bf16_f32 v141, v42, v43\n\t" \
  "v_mfma_f32_16x16x32_bf16 v[40:43], a[224:227], v[48:51], v[180:183]\n\t" \
  "v_exp_f32 v44, v44\n\t" \
  "v_mfma_f32_16x16x32_bf16 v[40:43], a[228:231], v[52:55], v[40:43]\n\t" \
  "v_exp_f32 v45, v45\n\t" \
  "v_mfma_f32_16x16x32_bf16 v[40:43], a[232:235], v[56:59], v[40:43]\n\t" \
  "v_exp_f32 v46, v46\n\t" \
  "v_mfma_f32_16x16x32_bf16 v[40:43], a[236:239], v[60:63], v[40:43]\n\t" \
  "v_exp_f32 v47, v47\n\t" \
  "v_cvt_pk_bf16_f32 v142, v44, v45\n\t" \
  "v_cvt_pk_bf16_f32 v143, v46, v47\n\t" \
  "v_mfma_f32_16x16x32_bf16 v[44:47], a[252:255], v[60:63], v[180:183]\n\t" \
  "v_exp_f32 v64, v64\n\t" \
  "v_mfma_f32_16x16x32_bf16 v[44:47], a[248:251], v[56:59], v[44:47]\n\t" \
  "ds_read_b64_tr_b16 a[168:169], v229 offset:512\n\t" \
  "ds_read_b64_tr_b16 a[170:171], v229 offset:4608\n\t" \
  "v_exp_f32 v65, v65\n\t" \
  "v_mfma_f32_16x16x32_bf16 v[44:47], a[244:247], v[52:55], v[44:47]\n\t" \
  "v_exp_f32 v66, v66\n\t" \
  "v_mfma_f32_16x16x32_bf16 v[44:47], a[240:243], v[48:51], v[44:47]\n\t" \
  "v_exp_f32 v67, v67\n\t" \
  "v_cvt_pk_bf16_f32 v144, v64, v65\n\t" \
  "v_cvt_pk_bf16_f32 v145, v66, v67\n\t" \
  "v_mfma_f32_16x16x32_bf16 v[64:67], a[192:195], v[80:83], v[184:187]\n\t" \
  "v_exp_f32 v68, v68\n\t" \
  "v_mfma_f32_16x16x32_bf16 v[64:67], a[196:199], v[84:87], v[64:67]\n\t" \
  "v_exp_f32 v69, v69\n\t" \
  "v_mfma_f32_16x16x32_bf16 v[64:67], a[200:203], v[88:91], v[64:67]\n\t" \
  "ds_read_b64_tr_b16 a[172:173], v229 offset:8704\n\t" \
  "v_cvt_pk_bf16_f32 v146, v68, v69\n\t" \
  "v_exp_f32 v70, v70\n\t" \
  "v_mfma_f32_16x16x32_bf16 v[64:67], a[204:207], v[92:95], v[64:67]\n\t" \
  "v_exp_f32 v71, v71\n\t" \
  "s_nop 0\n\t" \
  "v_cvt_pk_bf16_f32 v147, v70, v71\n\t" \
  "v_mfma_f32_16x16x32_bf16 v[68:71], a[220:223], v[92:95], v[184:187]\n\t" \
  "v_exp_f32 v72, v72\n\t" \
  "v_mfma_f32_16x16x32_bf16 v[68:71], a[216:219], v[88:91], v[68:71]\n\t" \
  "ds_read_b64_tr_b16 a[174:175], v229 offset:12800\n\t" \
  "ds_read_b64_tr_b16 a[176:177], v228 offset:768\n\t" \
  "v_exp_f32 v73, v73\n\t" \
  "v_mfma_f32_16x16x32_bf16 v[68:71], a[212:215], v[84:87], v[68:71]\n\t" \
  "v_exp_f32 v74, v74\n\t" \
  "v_mfma_f32_16x16x32_bf16 v[68:71], a[208:211], v[80:83], v[68:71]\n\t" \
  "v_exp_f32 v75, v75\n\t" \
  "v_cvt_pk_bf16_f32 v148, v72, v73\n\t" \
  "v_cvt_pk_bf16_f32 v149, v74, v75\n\t" \
  "v_mfma_f32_16x16x32_bf16 v[72:75], a[224:227], v[80:83], v[184:187]\n\t" \
  "v_exp_f32 v76, v76\n\t" \
  "v_mfma_f32_16x16x32_bf16 v[72:75], a[228:231], v[84:87], v[72:75]\n\t" \
  "v_exp_f32 v77, v77\n\t" \
  "v_mfma_f32_16x16x32_bf16 v[72:75], a[232:235], v[88:91], v[72:75]\n\t" \
  "ds_read_b64_tr_b16 a[178:179], v228 offset:4864\n\t" \
  "v_cvt_pk_bf16_f32 v150, v76, v77\n\t" \
  "v_exp_f32 v78, v78\n\t" \
  "v_mfma_f32_16x16x32_bf16 v[72:75], a[236:239], v[92:95], v[72:75]\n\t" \
  "v_exp_f32 v79, v79\n\t" \
  "s_nop 0\n\t" \
  "v_cvt_pk_bf16_f32 v151, v78, v79\n\t" \
  "v_mfma_f32_16x16x32_bf16 v[76:79], a[252:255], v[92:95], v[184:187]\n\t" \
  "v_exp_f32 v96, v96\n\t" \
  "v_mfma_f32_16x16x32_bf16 v[76:79], a[248:251], v[88:91], v[76:79]\n\t" \
  "ds_read_b64_tr_b16 a[180:181], v228 offset:8960\n\t" \
  "ds_read_b64_tr_b16 a[182:183], v228 offset:13056\n\t" \
  "v_exp_f32 v97, v97\n\t" \
  "v_mfma_f32_16x16x32_bf16 v[76:79], a[244:247], v[84:87], v[76:79]\n\t" \
  "v_exp_f32 v98, v98\n\t" \
  "v_mfma_f32_16x16x32_bf16 v[76:79], a[240:243], v[80:83], v[76:79]\n\t" \
  "v_exp_f32 v99, v99\n\t" \
  "v_cvt_pk_bf16_f32 v152, v96, v97\n\t" \
  "v_cvt_pk_bf16_f32 v153, v98, v99\n\t" \
  "v_mfma_f32_16x16x32_bf16 v[96:99], a[192:195], v[112:115], v[188:191]\n\t" \
  "v_exp_f32 v100, v100\n\t" \
  "v_mfma_f32_16x16x32_bf16 v[96:99], a[196:199], v[116:119], v[96:99]\n\t" \
  "v_exp_f32 v101, v101\n\t" \
  "v_mfma_f32_16x16x32_bf16 v[96:99], a[200:203], v[120:123], v[96:99]\n\t" \
  "ds_read_b64_tr_b16 a[184:185], v229 offset:768\n\t" \
  "v_cvt_pk_bf16_f32 v154, v100, v101\n\t" \
  "v_exp_f32 v102, v102\n\t" \
  "v_mfma_f32_16x16x32_bf16 v[96:99], a[204:207], v[124:127], v[96:99]\n\t" \
  "v_exp_f32 v103, v103\n\t" \
  "s_nop 0\n\t" \
  "v_cvt_pk_bf16_f32 v155, v102, v103\n\t" \
  "v_mfma_f32_16x16x32_bf16 v[100:103], a[220:223], v[124:127], v[188:191]\n\t" \
  "v_exp_f32 v104, v104\n\t" \
  "v_mfma_f32_16x16x32_bf16 v[100:103], a[216:219], v[120:123], v[100:103]\n\t" \
  "ds_read_b64_tr_b16 a[186:187], v229 offset:4864\n\t" \
  "ds_read_b64_tr_b16 a[188:189], v229 offset:8960\n\t" \
  "v_exp_f32 v105, v105\n\t" \
  "v_mfma_f32_16x16x32_bf16 v[100:103], a[212:215], v[116:119], v[100:103]\n\t" \
  "v_exp_f32 v106, v106\n\t" \
  "v_mfma_f32_16x16x32_bf16 v[100:103], a[208:211], v[112:115], v[100:103]\n\t" \
  "v_exp_f32 v107, v107\n\t" \
  "v_cvt_pk_bf16_f32 v156, v104, v105\n\t" \
  "v_cvt_pk_bf16_f32 v157, v106, v107\n\t" \
  "v_mfma_f32_16x16x32_bf16 v[104:107], a[224:227], v[112:115], v[188:191]\n\t" \
  "v_exp_f32 v108, v108\n\t" \
  "v_mfma_f32_16x16x32_bf16 v[104:107], a[228:231], v[116:119], v[104:107]\n\t" \
  "v_exp_f32 v109, v109\n\t" \
  "v_mfma_f32_16x16x32_bf16 v[104:107], a[232:235], v[120:123], v[104:107]\n\t" \
  "ds_read_b64_tr_b16 a[190:191], v229 offset:13056\n\t" \
  "v_cvt_pk_bf16_f32 v158, v108, v109\n\t" \
  "v_exp_f32 v110, v110\n\t" \
  "v_mfma_f32_16x16x32_bf16 v[104:107], a[236:239], v[124:127], v[104:107]\n\t" \
  "v_exp_f32 v111, v111\n\t" \
  "s_nop 0\n\t" \
  "v_cvt_pk_bf16_f32 v159, v110, v111\n\t" \
  "v_mfma_f32_16x16x32_bf16 v[108:111], a[252:255], v[124:127], v[188:191]\n\t" \
  "v_exp_f32 v0, v0\n\t" \
  "v_mfma_f32_16x16x32_bf16 v[108:111], a[248:251], v[120:123], v[108:111]\n\t" \
  "v_exp_f32 v1, v1\n\t" \
  "v_mfma_f32_16x16x32_bf16 v[108:111], a[244:247], v[116:119], v[108:111]\n\t" \
  "v_exp_f32 v2, v2\n\t" \
  "v_mfma_f32_16x16x32_bf16 v[108:111], a[240:243], v[112:115], v[108:111]\n\t" \
  "s_waitcnt vmcnt(4) lgkmcnt(0)\n\t" \
  "s_barrier\n\t" \
  STA("s_memrealtime s[94:95]\n\t") \
  STA("s_memtime s[92:93]\n\t") \
  "v_mfma_f32_16x16x32_bf16 a[0:3], a[128:131], v[128:131], 0\n\t" \
  "v_exp_f32 v3, v3\n\t" \
  "v_mfma_f32_16x16x32_bf16 a[0:3], a[132:135], v[132:135], a[0:3]\n\t" \
  "v_exp_f32 v4, v4\n\t" \
  "v_mfma_f32_16x16x32_bf16 a[16:19], a[136:139], v[128:131], 0\n\t" \
  "ds_read_b128 a[192:195], v220 offset:0\n\t" \
  "ds_read_b128 a[196:199], v221 offset:0\n\t" \
  "v_mfma_f32_16x16x32_bf16 a[16:19], a[140:143], v[132:135], a[16:19]\n\t" \
  "v_exp_f32 v5, v5\n\t" \
  "v_mfma_f32_16x16x32_bf16 a[32:35], a[144:147], v[128:131], 0\n\t" \
  "v_exp_f32 v6, v6\n\t" \
  "v_mfma_f32_16x16x32_bf16 a[32:35], a[148:151], v[132:135], a[32:35]\n\t" \
  "v_exp_f32 v7, v7\n\t" \
  "v_mfma_f32_16x16x32_bf16 a[48:51], a[152:155], v[128:131], 0\n\t" \
  "v_exp_f32 v8, v8\n\t" \
  "v_mfma_f32_16x16x32_bf16 a[48:51], a[156:159], v[132:135], a[48:51]\n\t" \
  "v_exp_f32 v9, v9\n\t" \
  "v_mfma_f32_16x16x32_bf16 a[64:67], a[160:163], v[128:131], 0\n\t" \
  "v_exp_f32 v10, v10\n\t" \
  "v_mfma_f32_16x16x32_bf16 a[64:67], a[164:167], v[132:135], a[64:67]\n\t" \
  "v_exp_f32 v11, v11\n\t" \
  "v_mfma_f32_16x16x32_bf16 a[80:83], a[168:171], v[128:131], 0\n\t" \
  "ds_read_b128 a[200:203], v222 offset:0\n\t" \
  "ds_read_b128 a[204:207], v223 offset:0\n\t" \
  "v_mfma_f32_16x16x32_bf16 a[80:83], a[172:175], v[132:135], a[80:83]\n\t" \
  "v_exp_f32 v12, v12\n\t" \
  "v_mfma_f32_16x16x32_bf16 a[96:99], a[176:179], v[128:131], 0\n\t" \
  STA("v_writelane_b32 %[stv], s94, 28\n\t") \
  STA("v_writelane_b32 %[stv], s92, 29\n\t") \
  "v_exp_f32 v13, v13\n\t" \
  "v_mfma_f32_16x16x32_bf16 a[96:99], a[180:183], v[132:135], a[96:99]\n\t" \
  "v_exp_f32 v14, v14\n\t" \
  "v_mfma_f32_16x16x32_bf16 a[112:115], a[184:187], v[128:131], 0\n\t" \
  "v_exp_f32 v15, v15\n\t" \
  "v_mfma_f32_16x16x32_bf16 a[112:115], a[188:191], v[132:135], a[112:115]\n\t" \
  "v_exp_f32 v32, v32\n\t" \
  "v_mfma_f32_16x16x32_bf16 v[160:163], v[216:219], v[128:131], 0\n\t" \
  "v_exp_f32 v33, v33\n\t" \
  "v_mfma_f32_16x16x32_bf16 v[160:163], v[216:219], v[132:135], v[160:163]\n\t" \
  "ds_read_b128 a[208:211], v220 offset:4096\n\t" \
  "v_cvt_pk_bf16_f32 v128, v0, v1\n\t" \
  "v_mfma_f32_16x16x32_bf16 a[4:7], a[128:131], v[136:139], 0\n\t" \
  "v_exp_f32 v34, v34\n\t" \
  "v_mfma_f32_16x16x32_bf16 a[4:7], a[132:135], v[140:143], a[4:7]\n\t" \
  "ds_read_b128 a[212:215], v221 offset:4096\n\t" \
  "v_cvt_pk_bf16_f32 v129, v2, v3\n\t" \
  "v_mfma_f32_16x16x32_bf16 a[20:23], a[136:139], v[136:139], 0\n\t" \
  "s_add_u32 m0, %[m], 65536\n\t" \
  "v_exp_f32 v35, v35\n\t" \
  "v_mfma_f32_16x16x32_bf16 a[20:23], a[140:143], v[140:143], a[20:23]\n\t" \
  "s_add_u32 s96, %[sofk], 49152\n\t" \
  "v_exp_f32 v36, v36\n\t" \
  "v_mfma_f32_16x16x32_bf16 a[36:39], a[144:147], v[136:139], 0\n\t" \
  "v_exp_f32 v37, v37\n\t" \
  "v_mfma_f32_16x16x32_bf16 a[36:39], a[148:151], v[140:143], a[36:39]\n\t" \
  "ds_read_b128 a[216:219], v222 offset:4096\n\t" \
  "v_cvt_pk_bf16_f32 v130, v4, v5\n\t" \
  "v_mfma_f32_16x16x32_bf16 a[52:55], a[152:155], v[136:139], 0\n\t" \
  "v_exp_f32 v38, v38\n\t" \
  "v_mfma_f32_16x16x32_bf16 a[52:55], a[156:159], v[140:143], a[52:55]\n\t" \
  "v_exp_f32 v39, v39\n\t" \
  "v_mfma_f32_16x16x32_bf16 a[68:71], a[160:163], v[136:139], 0\n\t" \
  "v_exp_f32 v40, v40\n\t" \
  "v_mfma_f32_16x16x32_bf16 a[68:71], a[164:167], v[140:143], a[68:71]\n\t" \
  "buffer_load_dwordx4 v224, %[sK], s96 offen offset:0 lds\n\t" \
  "v_mfma_f32_16x16x32_bf16 a[84:87], a[168:171], v[136:139], 0\n\t" \
  "ds_read_b128 a[220:223], v223 offset:4096\n\t" \
  "v_cvt_pk_bf16_f32 v131, v6, v7\n\t" \
  "v_mfma_f32_16x16x32_bf16 a[84:87], a[172:175], v[140:143], a[84:87]\n\t" \
  "v_exp_f32 v41, v41\n\t" \
  "v_mfma_f32_16x16x32_bf16 a[100:103], a[176:179], v[136:139], 0\n\t" \
  "ds_read_b128 a[224:227], v220 offset:8192\n\t" \
  "v_cvt_pk_bf16_f32 v132, v8, v9\n\t" \
  "v_mfma_f32_16x16x32_bf16 a[100:103], a[180:183], v[140:143], a[100:103]\n\t" \
  "buffer_load_dwordx4 v225, %[sK], s96 offen offset:1024 lds\n\t" \
  "v_mfma_f32_16x16x32_bf16 a[116:119], a[184:187], v[136:139], 0\n\t" \
  "v_exp_f32 v42, v42\n\t" \
  "v_mfma_f32_16x16x32_bf16 a[116:119], a[188:191], v[140:143], a[116:119]\n\t" \
  "v_exp_f32 v43, v43\n\t" \
  "v_mfma_f32_16x16x32_bf16 v[164:167], v[216:219], v[136:139], 0\n\t" \
  "buffer_load_dwordx4 v226, %[sK], s96 offen offset:2048 lds\n\t" \
  "v_mfma_f32_16x16x32_bf16 v[164:167], v[216:219], v[140:143], v[164:167]\n\t" \
  "ds_read_b128 a[228:231], v221 offset:8192\n\t" \
  "v_cvt_pk_bf16_f32 v133, v10, v11\n\t" \
  "v_mfma_f32_16x16x32_bf16 a[8:11], a[128:131], v[144:147], 0\n\t" \
  "v_exp_f32 v44, v44\n\t" \
  "v_mfma_f32_16x16x32_bf16 a[8:11], a[132:135], v[148:151], a[8:11]\n\t" \
  "v_exp_f32 v45, v45\n\t" \
  "v_mfma_f32_16x16x32_bf16 a[24:27], a[136:139], v[144:147], 0\n\t" \
  "buffer_load_dwordx4 v227, %[sK], s96 offen offset:3072 lds\n\t" \
  "v_mfma_f32_16x16x32_bf16 a[24:27], a[140:143], v[148:151], a[24:27]\n\t" \
  "ds_read_b128 a[232:235], v222 offset:8192\n\t" \
  "v_cvt_pk_bf16_f32 v134, v12, v13\n\t" \
  "v_mfma_f32_16x16x32_bf16 a[40:43], a[144:147], v[144:147], 0\n\t" \
  "v_exp_f32 v46, v46\n\t" \
  "v_mfma_f32_16x16x32_bf16 a[40:43], a[148:151], v[148:151], a[40:43]\n\t" \
  "s_add_u32 m0, %[m], 114688\n\t" \
  "s_mov_b32 s96, 0\n\t" \
  "ds_read_b128 a[236:239], v223 offset:8192\n\t" \
  "v_mfma_f32_16x16x32_bf16 a[56:59], a[152:155], v[144:147], 0\n\t" \
  "buffer_load_dwordx4 v232, %[sU], s96 offen offset:0 lds\n\t" \
  "v_mfma_f32_16x16x32_bf16 a[56:59], a[156:159], v[148:151], a[56:59]\n\t" \
  "s_add_u32 m0, %[m], 81920\n\t" \
  "v_exp_f32 v47, v47\n\t" \
  "v_mfma_f32_16x16x32_bf16 a[72:75], a[160:163], v[144:147], 0\n\t" \
  "s_add_u32 s96, %[sofk], 32768\n\t" \
  "v_exp_f32 v64, v64\n\t" \
  "v_mfma_f32_16x16x32_bf16 a[72:75], a[164:167], v[148:151], a[72:75]\n\t" \
  "ds_read_b128 a[240:243], v220 offset:12288\n\t" \
  "v_cvt_pk_bf16_f32 v135, v14, v15\n\t" \
  "v_mfma_f32_16x16x32_bf16 a[88:91], a[168:171], v[144:147], 0\n\t" \
  "v_exp_f32 v65, v65\n\t" \
  "v_mfma_f32_16x16x32_bf16 a[88:91], a[172:175], v[148:151], a[88:91]\n\t" \
  "v_exp_f32 v66, v66\n\t" \
  "v_mfma_f32_16x16x32_bf16 a[104:107], a[176:179], v[144:147], 0\n\t" \
  "v_exp_f32 v67, v67\n\t" \
  "v_mfma_f32_16x16x32_bf16 a[104:107], a[180:183], v[148:151], a[104:107]\n\t" \
  "ds_read_b128 a[244:247], v221 offset:12288\n\t" \
  "v_cvt_pk_bf16_f32 v136, v32, v33\n\t" \
  "v_mfma_f32_16x16x32_bf16 a[120:123], a[184:187], v[144:147], 0\n\t" \
  "v_exp_f32 v68, v68\n\t" \
  "v_mfma_f32_16x16x32_bf16 a[120:123], a[188:191], v[148:151], a[120:123]\n\t" \
  "buffer_load_dwordx4 v230, %[sV], s96 offen offset:0 lds\n\t" \
  "v_mfma_f32_16x16x32_bf16 v[168:171], v[216:219], v[144:147], 0\n\t" \
  "v_exp_f32 v69, v69\n\t" \
  "v_mfma_f32_16x16x32_bf16 v[168:171], v[216:219], v[148:151], v[168:171]\n\t" \
  "ds_read_b128 a[248:251], v222 offset:12288\n\t" \
  "v_cvt_pk_bf16_f32 v137, v34, v35\n\t" \
  "v_mfma_f32_16x16x32_bf16 a[12:15], a[128:131], v[152:155], 0\n\t" \
  "v_exp_f32 v70, v70\n\t" \
  "v_mfma_f32_16x16x32_bf16 a[12:15], a[132:135], v[156:159], a[12:15]\n\t" \
  "buffer_load_dwordx4 v231, %[sV], s96 offen offset:1024 lds\n\t" \
  "v_mfma_f32_16x16x32_bf16 a[28:31], a[136:139], v[152:155], 0\n\t" \
  "v_exp_f32 v71, v71\n\t" \
  "v_mfma_f32_16x16x32_bf16 a[28:31], a[140:143], v[156:159], a[28:31]\n\t" \
  "ds_read_b128 a[252:255], v223 offset:12288\n\t" \
  "v_cvt_pk_bf16_f32 v138, v36, v37\n\t" \
  "v_mfma_f32_16x16x32_bf16 a[44:47], a[144:147], v[152:155], 0\n\t" \
  "buffer_load_dwordx4 v230, %[sV], s96 offen offset:2048 lds\n\t" \
  "v_mfma_f32_16x16x32_bf16 a[44:47], a[148:151], v[156:159], a[44:47]\n\t" \
  "v_exp_f32 v72, v72\n\t" \
  "v_mfma_f32_16x16x32_bf16 a[60:63], a[152:155], v[152:155], 0\n\t" \
  "v_exp_f32 v73, v73\n\t" \
  "v_mfma_f32_16x16x32_bf16 a[60:63], a[156:159], v[156:159], a[60:63]\n\t" \
  "v_exp_f32 v74, v74\n\t" \
  "v_mfma_f32_16x16x32_bf16 a[76:79], a[160:163], v[152:155], 0\n\t" \
  "buffer_load_dwordx4 v231, %[sV], s96 offen offset:3072 lds\n\t" \
  "v_mfma_f32_16x16x32_bf16 a[76:79], a[164:167], v[156:159], a[76:79]\n\t" \
  "v_exp_f32 v75, v75\n\t" \
  "v_mfma_f32_16x16x32_bf16 a[92:95], a[168:171], v[152:155], 0\n\t" \
  "v_exp_f32 v76, v76\n\t" \
  "v_mfma_f32_16x16x32_bf16 a[92:95], a[172:175], v[156:159], a[92:95]\n\t" \
  "v_cvt_pk_bf16_f32 v139, v38, v39\n\t" \
  "v_cvt_pk_bf16_f32 v140, v40, v41\n\t" \
  "v_mfma_f32_16x16x32_bf16 a[108:111], a[176:179], v[152:155], 0\n\t" \
  "s_add_u32 m0, %[m], 114688\n\t" \
  "s_mov_b32 s96, 6144\n\t" \
  "buffer_load_dwordx4 v232, %[sU], s96 offen offset:1024 lds\n\t" \
  "v_mfma_f32_16x16x32_bf16 a[108:111], a[180:183], v[156:159], a[108:111]\n\t" \
  "v_exp_f32 v77, v77\n\t" \
  "v_mfma_f32_16x16x32_bf16 a[124:127], a[184:187], v[152:155], 0\n\t" \
  "v_exp_f32 v78, v78\n\t" \
  "v_mfma_f32_16x16x32_bf16 a[124:127], a[188:191], v[156:159], a[124:127]\n\t" \
  "v_exp_f32 v79, v79\n\t" \
  "v_mfma_f32_16x16x32_bf16 v[172:175], v[216:219], v[152:155], 0\n\t" \
  "v_exp_f32 v96, v96\n\t" \
  "v_mfma_f32_16x16x32_bf16 v[172:175], v[216:219], v[156:159], v[172:175]\n\t" \
  "s_waitcnt lgkmcnt(0)\n\t" \
  "v_exp_f32 v97, v97\n\t" \
  STA("s_memrealtime s[94:95]\n\t") \
  STA("s_memtime s[92:93]\n\t") \
  ".Lhs6:\n\ts_cbranch_execz .Lhs6n\n\t" \
  "v_mfma_f32_16x16x32_bf16 v[0:3], a[192:195], v[16:19], v[176:179]\n\t" \
  "ds_read_b64_tr_b16 a[128:129], v237 offset:0\n\t" \
  "v_cvt_pk_bf16_f32 v141, v42, v43\n\t" \
  "v_mfma_f32_16x16x32_bf16 v[0:3], a[196:199], v[20:23], v[0:3]\n\t" \
  "v_exp_f32 v98, v98\n\t" \
  "v_mfma_f32_16x16x32_bf16 v[0:3], a[200:203], v[24:27], v[0:3]\n\t" \
  "ds_read_b64_tr_b16 a[130:131], v237 offset:4096\n\t" \
  "v_cvt_pk_bf16_f32 v142, v44, v45\n\t" \
  "v_mfma_f32_16x16x32_bf16 v[0:3], a[204:207], v[28:31], v[0:3]\n\t" \
  "v_exp_f32 v99, v99\n\t" \
  "v_mfma_f32_16x16x32_bf16 v[4:7], a[220:223], v[28:31], v[176:179]\n\t" \
  "ds_read_b64_tr_b16 a[132:133], v237 offset:8192\n\t" \
  "v_cvt_pk_bf16_f32 v143, v46, v47\n\t" \
  "v_mfma_f32_16x16x32_bf16 v[4:7], a[216:219], v[24:27], v[4:7]\n\t" \
  "v_exp_f32 v100, v100\n\t" \
  "v_mfma_f32_16x16x32_bf16 v[4:7], a[212:215], v[20:23], v[4:7]\n\t" \
  "ds_read_b64_tr_b16 a[134:135], v237 offset:12288\n\t" \
  "v_cvt_pk_bf16_f32 v144, v64, v65\n\t" \
  "v_mfma_f32_16x16x32_bf16 v[4:7], a[208:211], v[16:19], v[4:7]\n\t" \
  "ds_read_b64_tr_b16 a[136:137], v238 offset:0\n\t" \
  "v_cvt_pk_bf16_f32 v145, v66, v67\n\t" \
  "v_mfma_f32_16x16x32_bf16 v[8:11], a[224:227], v[16:19], v[176:179]\n\t" \
  "s_add_u32 m0, %[m], 114688\n\t" \
  "s_mov_b32 s96, 12288\n\t" \
  "ds_read_b64_tr_b16 a[138:139], v238 offset:4096\n\t" \
  "v_mfma_f32_16x16x32_bf16 v[8:11], a[228:231], v[20:23], v[8:11]\n\t" \
  "buffer_load_dwordx4 v232, %[sU], s96 offen offset:2048 lds\n\t" \
  "v_mfma_f32_16x16x32_bf16 v[8:11], a[232:235], v[24:27], v[8:11]\n\t" \
  "s_mov_b32 s96, 18432\n\t" \
  "v_exp_f32 v101, v101\n\t" \
  "v_mfma_f32_16x16x32_bf16 v[8:11], a[236:239], v[28:31], v[8:11]\n\t" \
  "ds_read_b64_tr_b16 a[140:141], v238 offset:8192\n\t" \
  "v_cvt_pk_bf16_f32 v146, v68, v69\n\t" \
  "v_mfma_f32_16x16x32_bf16 v[12:15], a[252:255], v[28:31], v[176:179]\n\t" \
  STA("v_writelane_b32 %[stv], s94, 30\n\t") \
  STA("v_writelane_b32 %[stv], s92, 31\n\t") \
  "v_exp_f32 v102, v102\n\t" \
  "v_mfma_f32_16x16x32_bf16 v[12:15], a[248:251], v[24:27], v[12:15]\n\t" \
  "ds_read_b64_tr_b16 a[142:143], v238 offset:12288\n\t" \
  "v_cvt_pk_bf16_f32 v147, v70, v71\n\t" \
  "v_mfma_f32_16x16x32_bf16 v[12:15], a[244:247], v[20:23], v[12:15]\n\t" \
  "ds_read_b64_tr_b16 a[144:145], v237 offset:256\n\t" \
  "v_cvt_pk_bf16_f32 v148, v72, v73\n\t" \
  "v_mfma_f32_16x16x32_bf16 v[12:15], a[240:243], v[16:19], v[12:15]\n\t" \
  "v_exp_f32 v103, v103\n\t" \
  "v_mfma_f32_16x16x32_bf16 v[32:35], a[192:195], v[48:51], v[180:183]\n\t" \
  "ds_read_b64_tr_b16 a[146:147], v237 offset:4352\n\t" \
  "v_cvt_pk_bf16_f32 v149, v74, v75\n\t" \
  "v_mfma_f32_16x16x32_bf16 v[32:35], a[196:199], v[52:55], v[32:35]\n\t" \
  "v_exp_f32 v104, v104\n\t" \
  "v_mfma_f32_16x16x32_bf16 v[32:35], a[200:203], v[56:59], v[32:35]\n\t" \
  "ds_read_b64_tr_b16 a[148:149], v237 offset:8448\n\t" \
  "v_cvt_pk_bf16_f32 v150, v76, v77\n\t" \
  "v_mfma_f32_16x16x32_bf16 v[32:35], a[204:207], v[60:63], v[32:35]\n\t" \
  "v_exp_f32 v105, v105\n\t" \
  "v_mfma_f32_16x16x32_bf16 v[36:39], a[220:223], v[60:63], v[180:183]\n\t" \
  "ds_read_b64_tr_b16 a[150:151], v237 offset:12544\n\t" \
  "v_cvt_pk_bf16_f32 v151, v78, v79\n\t" \
  "v_mfma_f32_16x16x32_bf16 v[36:39], a[216:219], v[56:59], v[36:39]\n\t" \
  "buffer_load_dwordx4 v232, %[sU], s96 offen offset:3072 lds\n\t" \
  "v_mfma_f32_16x16x32_bf16 v[36:39], a[212:215], v[52:55], v[36:39]\n\t" \
  "ds_read_b64_tr_b16 a[152:153], v238 offset:256\n\t" \
  "v_cvt_pk_bf16_f32 v152, v96, v97\n\t" \
  "v_mfma_f32_16x16x32_bf16 v[36:39], a[208:211], v[48:51], v[36:39]\n\t" \
  "ds_read_b64_tr_b16 a[154:155], v238 offset:4352\n\t" \
  "v_cvt_pk_bf16_f32 v153, v98, v99\n\t" \
  "v_mfma_f32_16x16x32_bf16 v[40:43], a[224:227], v[48:51], v[180:183]\n\t" \
  "v_exp_f32 v106, v106\n\t" \
  "v_mfma_f32_16x16x32_bf16 v[40:43], a[228:231], v[52:55], v[40:43]\n\t" \
  "ds_read_b64_tr_b16 a[156:157], v238 offset:8448\n\t" \
  "v_cvt_pk_bf16_f32 v154, v100, v101\n\t" \
  "v_mfma_f32_16x16x32_bf16 v[40:43], a[232:235], v[56:59], v[40:43]\n\t" \
  "v_exp_f32 v107, v107\n\t" \
  "v_mfma_f32_16x16x32_bf16 v[40:43], a[236:239], v[60:63], v[40:43]\n\t" \
  "ds_read_b64_tr_b16 a[158:159], v238 offset:12544\n\t" \
  "v_cvt_pk_bf16_f32 v155, v102, v103\n\t" \
  "v_mfma_f32_16x16x32_bf16 v[44:47], a[252:255], v[60:63], v[180:183]\n\t" \
  "ds_read_b64_tr_b16 a[160:161], v237 offset:512\n\t" \
  "v_cvt_pk_bf16_f32 v156, v104, v105\n\t" \
  "v_mfma_f32_16x16x32_bf16 v[44:47], a[248:251], v[56:59], v[44:47]\n\t" \
  "v_exp_f32 v108, v108\n\t" \
  "v_mfma_f32_16x16x32_bf16 v[44:47], a[244:247], v[52:55], v[44:47]\n\t" \
  "ds_read_b64_tr_b16 a[162:163], v237 offset:4608\n\t" \
  "v_cvt_pk_bf16_f32 v157, v106, v107\n\t" \
  "v_mfma_f32_16x16x32_bf16 v[44:47], a[240:243], v[48:51], v[44:47]\n\t" \
  "v_exp_f32 v109, v109\n\t" \
  "v_mfma_f32_16x16x32_bf16 v[64:67], a[192:195], v[80:83], v[184:187]\n\t" \
  "ds_read_b64_tr_b16 a[164:165], v237 offset:8704\n\t" \
  "v_cvt_pk_bf16_f32 v158, v108, v109\n\t" \
  "v_mfma_f32_16x16x32_bf16 v[64:67], a[196:199], v[84:87], v[64:67]\n\t" \
  "v_exp_f32 v110, v110\n\t" \
  "v_mfma_f32_16x16x32_bf16 v[64:67], a[200:203], v[88:91], v[64:67]\n\t" \
  "ds_read_b64_tr_b16 a[166:167], v237 offset:12800\n\t" \
  "ds_read_b64_tr_b16 a[168:169], v238 offset:512\n\t" \
  "v_mfma_f32_16x16x32_bf16 v[64:67], a[204:207], v[92:95], v[64:67]\n\t" \
  "s_add_u32 m0, %[m], 131072\n\t" \
  "s_mov_b32 s96, 28672\n\t" \
  "buffer_load_dwordx4 v232, %[sU], s96 offen offset:0 lds\n\t" \
  "v_mfma_f32_16x16x32_bf16 v[68:71], a[220:223], v[92:95], v[184:187]\n\t" \
  "s_mov_b32 s96, 34816\n\t" \
  "v_exp_f32 v111, v111\n\t" \
  "v_mfma_f32_16x16x32_bf16 v[68:71], a[216:219], v[88:91], v[68:71]\n\t" \
  "ds_read_b64_tr_b16 a[170:171], v238 offset:4608\n\t" \
  "v_cvt_pk_bf16_f32 v159, v110, v111\n\t" \
  "v_mfma_f32_16x16x32_bf16 v[68:71], a[212:215], v[84:87], v[68:71]\n\t" \
  "v_exp_f32 v0, v0\n\t" \
  "v_mfma_f32_16x16x32_bf16 v[68:71], a[208:211], v[80:83], v[68:71]\n\t" \
  "v_exp_f32 v1, v1\n\t" \
  "v_mfma_f32_16x16x32_bf16 v[72:75], a[224:227], v[80:83], v[184:187]\n\t" \
  "v_exp_f32 v2, v2\n\t" \
  "v_mfma_f32_16x16x32_bf16 v[72:75], a[228:231], v[84:87], v[72:75]\n\t" \
  "ds_read_b64_tr_b16 a[172:173], v238 offset:8704\n\t" \
  "ds_read_b64_tr_b16 a[174:175], v238 offset:12800\n\t" \
  "v_mfma_f32_16x16x32_bf16 v[72:75], a[232:235], v[88:91], v[72:75]\n\t" \
  "v_exp_f32 v3, v3\n\t" \
  "v_mfma_f32_16x16x32_bf16 v[72:75], a[236:239], v[92:95], v[72:75]\n\t" \
  "v_exp_f32 v4, v4\n\t" \
  "v_mfma_f32_16x16x32_bf16 v[76:79], a[252:255], v[92:95], v[184:187]\n\t" \
  "ds_read_b64_tr_b16 a[176:177], v237 offset:768\n\t" \
  "ds_read_b64_tr_b16 a[178:179], v237 offset:4864\n\t" \
  "v_mfma_f32_16x16x32_bf16 v[76:79], a[248:251], v[88:91], v[76:79]\n\t" \
  "v_exp_f32 v5, v5\n\t" \
  "v_mfma_f32_16x16x32_bf16 v[76:79], a[244:247], v[84:87], v[76:79]\n\t" \
  "v_exp_f32 v6, v6\n\t" \
  "v_mfma_f32_16x16x32_bf16 v[76:79], a[240:243], v[80:83], v[76:79]\n\t" \
  "buffer_load_dwordx4 v232, %[sU], s96 offen offset:1024 lds\n\t" \
  "v_mfma_f32_16x16x32_bf16 v[96:99], a[192:195], v[112:115], v[188:191]\n\t" \
  "ds_read_b64_tr_b16 a[180:181], v237 offset:8960\n\t" \
  "ds_read_b64_tr_b16 a[182:183], v237 offset:13056\n\t" \
  "v_mfma_f32_16x16x32_bf16 v[96:99], a[196:199], v[116:119], v[96:99]\n\t" \
  "v_exp_f32 v7, v7\n\t" \
  "v_mfma_f32_16x16x32_bf16 v[96:99], a[200:203], v[120:123], v[96:99]\n\t" \
  "v_exp_f32 v8, v8\n\t" \
  "v_mfma_f32_16x16x32_bf16 v[96:99], a[204:207], v[124:127], v[96:99]\n\t" \
  "ds_read_b64_tr_b16 a[184:185], v238 offset:768\n\t" \
  "ds_read_b64_tr_b16 a[186:187], v238 offset:4864\n\t" \
  "v_mfma_f32_16x16x32_bf16 v[100:103], a[220:223], v[124:127], v[188:191]\n\t" \
  "v_exp_f32 v9, v9\n\t" \
  "v_mfma_f32_16x16x32_bf16 v[100:103], a[216:219], v[120:123], v[100:103]\n\t" \
  "v_exp_f32 v10, v10\n\t" \
  "v_mfma_f32_16x16x32_bf16 v[100:103], a[212:215], v[116:119], v[100:103]\n\t" \
  "v_exp_f32 v11, v11\n\t" \
  "v_mfma_f32_16x16x32_bf16 v[100:103], a[208:211], v[112:115], v[100:103]\n\t" \
  "ds_read_b64_tr_b16 a[188:189], v238 offset:8960\n\t" \
  "ds_read_b64_tr_b16 a[190:191], v238 offset:13056\n\t" \
  "v_mfma_f32_16x16x32_bf16 v[104:107], a[224:227], v[112:115], v[188:191]\n\t" \
  "v_exp_f32 v12, v12\n\t" \
  "v_mfma_f32_16x16x32_bf16 v[104:107], a[228:231], v[116:119], v[104:107]\n\t" \
  "v_exp_f32 v13, v13\n\t" \
  "v_mfma_f32_16x16x32_bf16 v[104:107], a[232:235], v[120:123], v[104:107]\n\t" \
  "v_exp_f32 v14, v14\n\t" \
  "v_mfma_f32_16x16x32_bf16 v[104:107], a[236:239], v[124:127], v[104:107]\n\t" \
  "v_exp_f32 v15, v15\n\t" \
  "v_mfma_f32_16x16x32_bf16 v[108:111], a[252:255], v[124:127], v[188:191]\n\t" \
  "v_exp_f32 v32, v32\n\t" \
  "v_mfma_f32_16x16x32_bf16 v[108:111], a[248:251], v[120:123], v[108:111]\n\t" \
  "v_exp_f32 v33, v33\n\t" \
  "v_mfma_f32_16x16x32_bf16 v[108:111], a[244:247], v[116:119], v[108:111]\n\t" \
  "v_exp_f32 v34, v34\n\t" \
  "v_mfma_f32_16x16x32_bf16 v[108:111], a[240:243], v[112:115], v[108:111]\n\t" \
  "s_waitcnt vmcnt(5) lgkmcnt(0)\n\t" \
  "s_barrier\n\t" \
  STA("s_memrealtime s[94:95]\n\t") \
  STA("s_memtime s[92:93]\n\t") \
  "v_mfma_f32_16x16x32_bf16 a[0:3], a[128:131], v[128:131], a[0:3]\n\t" \
  "v_exp_f32 v35, v35\n\t" \
  "v_mfma_f32_16x16x32_bf16 a[0:3], a[132:135], v[132:135], a[0:3]\n\t" \
  "v_exp_f32 v36, v36\n\t" \
  "v_mfma_f32_16x16x32_bf16 a[16:19], a[136:139], v[128:131], a[16:19]\n\t" \
  "ds_read_b128 a[192:195], v220 offset:16384\n\t" \
  "ds_read_b128 a[196:199], v221 offset:16384\n\t" \
  "v_mfma_f32_16x16x32_bf16 a[16:19], a[140:143], v[132:135], a[16:19]\n\t" \
  "v_exp_f32 v37, v37\n\t" \
  "v_mfma_f32_16x16x32_bf16 a[32:35], a[144:147], v[128:131], a[32:35]\n\t" \
  "v_exp_f32 v38, v38\n\t" \
  "v_mfma_f32_16x16x32_bf16 a[32:35], a[148:151], v[132:135], a[32:35]\n\t" \
  "v_exp_f32 v39, v39\n\t" \
  "v_mfma_f32_16x16x32_bf16 a[48:51], a[152:155], v[128:131], a[48:51]\n\t" \
  "v_exp_f32 v40, v40\n\t" \
  "v_mfma_f32_16x16x32_bf16 a[48:51], a[156:159], v[132:135], a[48:51]\n\t" \
  "v_exp_f32 v41, v41\n\t" \
  "v_mfma_f32_16x16x32_bf16 a[64:67], a[160:163], v[128:131], a[64:67]\n\t" \
  "v_exp_f32 v42, v42\n\t" \
  "v_mfma_f32_16x16x32_bf16 a[64:67], a[164:167], v[132:135], a[64:67]\n\t" \
  "v_exp_f32 v43, v43\n\t" \
  "v_mfma_f32_16x16x32_bf16 a[80:83], a[168:171], v[128:131], a[80:83]\n\t" \
  "ds_read_b128 a[200:203], v222 offset:16384\n\t" \
  "ds_read_b128 a[204:207], v223 offset:16384\n\t" \
  "v_mfma_f32_16x16x32_bf16 a[80:83], a[172:175], v[132:135], a[80:83]\n\t" \
  "v_exp_f32 v44, v44\n\t" \
  "v_mfma_f32_16x16x32_bf16 a[96:99], a[176:179], v[128:131], a[96:99]\n\t" \
  STA("v_writelane_b32 %[stv], s94, 32\n\t") \
  STA("v_writelane_b32 %[stv], s92, 33\n\t") \
  "buffer_store_dword v233, v247, %[ws], 0 offen sc1\n\t" \
  "v_mfma_f32_16x16x32_bf16 a[96:99], a[180:183], v[132:135], a[96:99]\n\t" \
  "v_exp_f32 v45, v45\n\t" \
  "v_mfma_f32_16x16x32_bf16 a[112:115], a[184:187], v[128:131], a[112:115]\n\t" \
  "v_exp_f32 v46, v46\n\t" \
  "v_mfma_f32_16x16x32_bf16 a[112:115], a[188:191], v[132:135], a[112:115]\n\t" \
  "v_exp_f32 v47, v47\n\t" \
  "v_mfma_f32_16x16x32_bf16 v[160:163], v[216:219], v[128:131], v[160:163]\n\t" \
  "v_exp_f32 v64, v64\n\t" \
  "v_mfma_f32_16x16x32_bf16 v[160:163], v[216:219], v[132:135], v[160:163]\n\t" \
  "ds_read_b128 a[208:211], v220 offset:20480\n\t" \
  "v_cvt_pk_bf16_f32 v128, v0, v1\n\t" \
  "v_mfma_f32_16x16x32_bf16 a[4:7], a[128:131], v[136:139], a[4:7]\n\t" \
  "v_exp_f32 v65, v65\n\t" \
  "v_mfma_f32_16x16x32_bf16 a[4:7], a[132:135], v[140:143], a[4:7]\n\t" \
  "ds_read_b128 a[212:215], v221 offset:20480\n\t" \
  "v_cvt_pk_bf16_f32 v129, v2, v3\n\t" \
  "v_mfma_f32_16x16x32_bf16 a[20:23], a[136:139], v[136:139], a[20:23]\n\t" \
  "s_add_u32 m0, %[m], 49152\n\t" \
  "v_exp_f32 v66, v66\n\t" \
  "v_mfma_f32_16x16x32_bf16 a[20:23], a[140:143], v[140:143], a[20:23]\n\t" \
  "s_add_u32 s96, %[sofk], 65536\n\t" \
  "v_exp_f32 v67, v67\n\t" \
  "v_mfma_f32_16x16x32_bf16 a[36:39], a[144:147], v[136:139], a[36:39]\n\t" \
  "v_exp_f32 v68, v68\n\t" \
  "v_mfma_f32_16x16x32_bf16 a[36:39], a[148:151], v[140:143], a[36:39]\n\t" \
  "ds_read_b128 a[216:219], v222 offset:20480\n\t" \
  "v_cvt_pk_bf16_f32 v130, v4, v5\n\t" \
  "v_mfma_f32_16x16x32_bf16 a[52:55], a[152:155], v[136:139], a[52:55]\n\t" \
  "v_exp_f32 v69, v69\n\t" \
  "v_mfma_f32_16x16x32_bf16 a[52:55], a[156:159], v[140:143], a[52:55]\n\t" \
  "v_exp_f32 v70, v70\n\t" \
  "v_mfma_f32_16x16x32_bf16 a[68:71], a[160:163], v[136:139], a[68:71]\n\t" \
  "v_exp_f32 v71, v71\n\t" \
  "v_mfma_f32_16x16x32_bf16 a[68:71], a[164:167], v[140:143], a[68:71]\n\t" \
  "buffer_load_dwordx4 v224, %[sK], s96 offen offset:0 lds\n\t" \
  "v_mfma_f32_16x16x32_bf16 a[84:87], a[168:171], v[136:139], a[84:87]\n\t" \
  "ds_read_b128 a[220:223], v223 offset:20480\n\t" \
  "v_cvt_pk_bf16_f32 v131, v6, v7\n\t" \
  "v_mfma_f32_16x16x32_bf16 a[84:87], a[172:175], v[140:143], a[84:87]\n\t" \
  "v_exp_f32 v72, v72\n\t" \
  "v_mfma_f32_16x16x32_bf16 a[100:103], a[176:179], v[136:139], a[100:103]\n\t" \
  "ds_read_b128 a[224:227], v220 offset:24576\n\t" \
  "v_cvt_pk_bf16_f32 v132, v8, v9\n\t" \
  "v_mfma_f32_16x16x32_bf16 a[100:103], a[180:183], v[140:143], a[100:103]\n\t" \
  "buffer_load_dwordx4 v225, %[sK], s96 offen offset:1024 lds\n\t" \
  "v_mfma_f32_16x16x32_bf16 a[116:119], a[184:187], v[136:139], a[116:119]\n\t" \
  "v_exp_f32 v73, v73\n\t" \
  "v_mfma_f32_16x16x32_bf16 a[116:119], a[188:191], v[140:143], a[116:119]\n\t" \
  "v_exp_f32 v74, v74\n\t" \
  "v_mfma_f32_16x16x32_bf16 v[164:167], v[216:219], v[136:139], v[164:167]\n\t" \
  "buffer_load_dwordx4 v226, %[sK], s96 offen offset:2048 lds\n\t" \
  "v_mfma_f32_16x16x32_bf16 v[164:167], v[216:219], v[140:143], v[164:167]\n\t" \
  "ds_read_b128 a[228:231], v221 offset:24576\n\t" \
  "v_cvt_pk_bf16_f32 v133, v10, v11\n\t" \
  "v_mfma_f32_16x16x32_bf16 a[8:11], a[128:131], v[144:147], a[8:11]\n\t" \
  "v_exp_f32 v75, v75\n\t" \
  "v_mfma_f32_16x16x32_bf16 a[8:11], a[132:135], v[148:151], a[8:11]\n\t" \
  "v_exp_f32 v76, v76\n\t" \
  "v_mfma_f32_16x16x32_bf16 a[24:27], a[136:139], v[144:147], a[24:27]\n\t" \
  "buffer_load_dwordx4 v227, %[sK], s96 offen offset:3072 lds\n\t" \
  "v_mfma_f32_16x16x32_bf16 a[24:27], a[140:143], v[148:151], a[24:27]\n\t" \
  "ds_read_b128 a[232:235], v222 offset:24576\n\t" \
  "v_cvt_pk_bf16_f32 v134, v12, v13\n\t" \
  "v_mfma_f32_16x16x32_bf16 a[40:43], a[144:147], v[144:147], a[40:43]\n\t" \
  "v_exp_f32 v77, v77\n\t" \
  "v_mfma_f32_16x16x32_bf16 a[40:43], a[148:151], v[148:151], a[40:43]\n\t" \
  "s_add_u32 m0, %[m], 131072\n\t" \
  "s_mov_b32 s96, 40960\n\t" \
  "ds_read_b128 a[236:239], v223 offset:24576\n\t" \
  "v_mfma_f32_16x16x32_bf16 a[56:59], a[152:155], v[144:147], a[56:59]\n\t" \
  "buffer_load_dwordx4 v232, %[sU], s96 offen offset:2048 lds\n\t" \
  "v_mfma_f32_16x16x32_bf16 a[56:59], a[156:159], v[148:151], a[56:59]\n\t" \
  "s_add_u32 m0, %[m], 147456\n\t" \
  "v_exp_f32 v78, v78\n\t" \
  "v_mfma_f32_16x16x32_bf16 a[72:75], a[160:163], v[144:147], a[72:75]\n\t" \
  "s_add_u32 s96, %[sofk], 49152\n\t" \
  "v_exp_f32 v79, v79\n\t" \
  "v_mfma_f32_16x16x32_bf16 a[72:75], a[164:167], v[148:151], a[72:75]\n\t" \
  "ds_read_b128 a[240:243], v220 offset:28672\n\t" \
  "v_cvt_pk_bf16_f32 v135, v14, v15\n\t" \
  "v_mfma_f32_16x16x32_bf16 a[88:91], a[168:171], v[144:147], a[88:91]\n\t" \
  "v_exp_f32 v96, v96\n\t" \
  "v_mfma_f32_16x16x32_bf16 a[88:91], a[172:175], v[148:151], a[88:91]\n\t" \
  "v_exp_f32 v97, v97\n\t" \
  "v_mfma_f32_16x16x32_bf16 a[104:107], a[176:179], v[144:147], a[104:107]\n\t" \
  "v_exp_f32 v98, v98\n\t" \
  "v_mfma_f32_16x16x32_bf16 a[104:107], a[180:183], v[148:151], a[104:107]\n\t" \
  "ds_read_b128 a[244:247], v221 offset:28672\n\t" \
  "v_cvt_pk_bf16_f32 v136, v32, v33\n\t" \
  "v_mfma_f32_16x16x32_bf16 a[120:123], a[184:187], v[144:147], a[120:123]\n\t" \
  "v_exp_f32 v99, v99\n\t" \
  "v_mfma_f32_16x16x32_bf16 a[120:123], a[188:191], v[148:151], a[120:123]\n\t" \
  "buffer_load_dwordx4 v230, %[sV], s96 offen offset:0 lds\n\t" \
  "v_mfma_f32_16x16x32_bf16 v[168:171], v[216:219], v[144:147], v[168:171]\n\t" \
  "v_exp_f32 v100, v100\n\t" \
  "v_mfma_f32_16x16x32_bf16 v[168:171], v[216:219], v[148:151], v[168:171]\n\t" \
  "ds_read_b128 a[248:251], v222 offset:28672\n\t" \
  "v_cvt_pk_bf16_f32 v137, v34, v35\n\t" \
  "v_mfma_f32_16x16x32_bf16 a[12:15], a[128:131], v[152:155], a[12:15]\n\t" \
  "v_exp_f32 v101, v101\n\t" \
  "v_mfma_f32_16x16x32_bf16 a[12:15], a[132:135], v[156:159], a[12:15]\n\t" \
  "buffer_load_dwordx4 v231, %[sV], s96 offen offset:1024 lds\n\t" \
  "v_mfma_f32_16x16x32_bf16 a[28:31], a[136:139], v[152:155], a[28:31]\n\t" \
  "v_exp_f32 v102, v102\n\t" \
  "v_mfma_f32_16x16x32_bf16 a[28:31], a[140:143], v[156:159], a[28:31]\n\t" \
  "ds_read_b128 a[252:255], v223 offset:28672\n\t" \
  "v_cvt_pk_bf16_f32 v138, v36, v37\n\t" \
  "v_mfma_f32_16x16x32_bf16 a[44:47], a[144:147], v[152:155], a[44:47]\n\t" \
  "buffer_load_dwordx4 v230, %[sV], s96 offen offset:2048 lds\n\t" \
  "v_mfma_f32_16x16x32_bf16 a[44:47], a[148:151], v[156:159], a[44:47]\n\t" \
  "v_exp_f32 v103, v103\n\t" \
  "v_mfma_f32_16x16x32_bf16 a[60:63], a[152:155], v[152:155], a[60:63]\n\t" \
  "v_exp_f32 v104, v104\n\t" \
  "v_mfma_f32_16x16x32_bf16 a[60:63], a[156:159], v[156:159], a[60:63]\n\t" \
  "v_exp_f32 v105, v105\n\t" \
  "v_mfma_f32_16x16x32_bf16 a[76:79], a[160:163], v[152:155], a[76:79]\n\t" \
  "buffer_load_dwordx4 v231, %[sV], s96 offen offset:3072 lds\n\t" \
  "v_mfma_f32_16x16x32_bf16 a[76:79], a[164:167], v[156:159], a[76:79]\n\t" \
  "v_exp_f32 v106, v106\n\t" \
  "v_mfma_f32_16x16x32_bf16 a[92:95], a[168:171], v[152:155], a[92:95]\n\t" \
  "v_exp_f32 v107, v107\n\t" \
  "v_mfma_f32_16x16x32_bf16 a[92:95], a[172:175], v[156:159], a[92:95]\n\t" \
  "v_cvt_pk_bf16_f32 v139, v38, v39\n\t" \
  "v_cvt_pk_bf16_f32 v140, v40, v41\n\t" \
  "v_mfma_f32_16x16x32_bf16 a[108:111], a[176:179], v[152:155], a[108:111]\n\t" \
  "s_add_u32 m0, %[m], 131072\n\t" \
  "s_mov_b32 s96, 47104\n\t" \
  "buffer_load_dwordx4 v232, %[sU], s96 offen offset:3072 lds\n\t" \
  "v_mfma_f32_16x16x32_bf16 a[108:111], a[180:183], v[156:159], a[108:111]\n\t" \
  "v_exp_f32 v108, v108\n\t" \
  "v_mfma_f32_16x16x32_bf16 a[124:127], a[184:187], v[152:155], a[124:127]\n\t" \
  "v_exp_f32 v109, v109\n\t" \
  "v_mfma_f32_16x16x32_bf16 a[124:127], a[188:191], v[156:159], a[124:127]\n\t" \
  "v_exp_f32 v110, v110\n\t" \
  "v_mfma_f32_16x16x32_bf16 v[172:175], v[216:219], v[152:155], v[172:175]\n\t" \
  "v_cvt_pk_bf16_f32 v141, v42, v43\n\t" \
  "v_cvt_pk_bf16_f32 v142, v44, v45\n\t" \
  "v_mfma_f32_16x16x32_bf16 v[172:175], v[216:219], v[156:159], v[172:175]\n\t" \
  "s_waitcnt lgkmcnt(0)\n\t" \
  "s_mul_i32 s98, %[sofk], 5\n\t" \
  "s_xor_b32 s98, s98, 8192\n\t" \
  "s_lshr_b32 s98, s98, 1\n\t" \
  "v_cvt_pk_bf16_f32 v143, v46, v47\n\t" \
  STA("s_memrealtime s[94:95]\n\t") \
  STA("s_memtime s[92:93]\n\t") \
  "v_mfma_f32_16x16x32_bf16 v[0:3], a[192:195], v[16:19], v[176:179]\n\t" \
  "buffer_load_dword %[pfd], v204, %[sD], s98 offen\n\t" \
  "v_mfma_f32_16x16x32_bf16 v[0:3], a[196:199], v[20:23], v[0:3]\n\t" \
  "ds_read_b64_tr_b16 a[128:129], v228 offset:0\n\t" \
  "v_cvt_pk_bf16_f32 v144, v64, v65\n\t" \
  "v_mfma_f32_16x16x32_bf16 v[0:3], a[200:203], v[24:27], v[0:3]\n\t" \
  "ds_read_b64_tr_b16 a[130:131], v228 offset:4096\n\t" \
  "v_cvt_pk_bf16_f32 v145, v66, v67\n\t" \
  "v_mfma_f32_16x16x32_bf16 v[0:3], a[204:207], v[28:31], v[0:3]\n\t" \
  "v_exp_f32 v111, v111\n\t" \
  "v_mfma_f32_16x16x32_bf16 v[4:7], a[220:223], v[28:31], v[176:179]\n\t" \
  "ds_read_b64_tr_b16 a[132:133], v228 offset:8192\n\t" \
  "v_cvt_pk_bf16_f32 v146, v68, v69\n\t" \
  "v_mfma_f32_16x16x32_bf16 v[4:7], a[216:219], v[24:27], v[4:7]\n\t" \
  "ds_read_b64_tr_b16 a[134:135], v228 offset:12288\n\t" \
  "v_cvt_pk_bf16_f32 v147, v70, v71\n\t" \
  "v_mfma_f32_16x16x32_bf16 v[4:7], a[212:215], v[20:23], v[4:7]\n\t" \
  "v_exp_f32 v0, v0\n\t" \
  "v_mfma_f32_16x16x32_bf16 v[4:7], a[208:211], v[16:19], v[4:7]\n\t" \
  "ds_read_b64_tr_b16 a[136:137], v229 offset:0\n\t" \
  "v_cvt_pk_bf16_f32 v148, v72, v73\n\t" \
  "v_mfma_f32_16x16x32_bf16 v[8:11], a[224:227], v[16:19], v[176:179]\n\t" \
  "v_exp_f32 v1, v1\n\t" \
  "v_mfma_f32_16x16x32_bf16 v[8:11], a[228:231], v[20:23], v[8:11]\n\t" \
  "ds_read_b64_tr_b16 a[138:139], v229 offset:4096\n\t" \
  "v_cvt_pk_bf16_f32 v149, v74, v75\n\t" \
  "v_mfma_f32_16x16x32_bf16 v[8:11], a[232:235], v[24:27], v[8:11]\n\t" \
  "v_exp_f32 v2, v2\n\t" \
  "v_mfma_f32_16x16x32_bf16 v[8:11], a[236:239], v[28:31], v[8:11]\n\t" \
  "ds_read_b64_tr_b16 a[140:141], v229 offset:8192\n\t" \
  "v_cvt_pk_bf16_f32 v150, v76, v77\n\t" \
  "v_mfma_f32_16x16x32_bf16 v[12:15], a[252:255], v[28:31], v[176:179]\n\t" \
  STA("v_writelane_b32 %[stv], s94, 34\n\t") \
  STA("v_writelane_b32 %[stv], s92, 35\n\t") \
  "s_mul_i32 s98, %[sofk], 5\n\t" \
  "s_xor_b32 s98, s98, 12288\n\t" \
  "s_lshr_b32 s98, s98, 1\n\t" \
  "v_cvt_pk_bf16_f32 v151, v78, v79\n\t" \
  "v_mfma_f32_16x16x32_bf16 v[12:15], a[248:251], v[24:27], v[12:15]\n\t" \
  "buffer_load_dword %[pfd], v204, %[sD], s98 offen\n\t" \
  "v_mfma_f32_16x16x32_bf16 v[12:15], a[244:247], v[20:23], v[12:15]\n\t" \
  "ds_read_b64_tr_b16 a[142:143], v229 offset:12288\n\t" \
  "v_cvt_pk_bf16_f32 v152, v96, v97\n\t" \
  "v_mfma_f32_16x16x32_bf16 v[12:15], a[240:243], v[16:19], v[12:15]\n\t" \
  "ds_read_b64_tr_b16 a[144:145], v228 offset:256\n\t" \
  "v_cvt_pk_bf16_f32 v153, v98, v99\n\t" \
  "v_mfma_f32_16x16x32_bf16 v[32:35], a[192:195], v[48:51], v[180:183]\n\t" \
  "ds_read_b64_tr_b16 a[146:147], v228 offset:4352\n\t" \
  "v_cvt_pk_bf16_f32 v154, v100, v101\n\t" \
  "v_mfma_f32_16x16x32_bf16 v[32:35], a[196:199], v[52:55], v[32:35]\n\t" \
  "v_exp_f32 v3, v3\n\t" \
  "v_mfma_f32_16x16x32_bf16 v[32:35], a[200:203], v[56:59], v[32:35]\n\t" \
  "ds_read_b64_tr_b16 a[148:149], v228 offset:8448\n\t" \
  "v_cvt_pk_bf16_f32 v155, v102, v103\n\t" \
  "v_mfma_f32_16x16x32_bf16 v[32:35], a[204:207], v[60:63], v[32:35]\n\t" \
  "v_exp_f32 v4, v4\n\t" \
  "v_mfma_f32_16x16x32_bf16 v[36:39], a[220:223], v[60:63], v[180:183]\n\t" \
  "ds_read_b64_tr_b16 a[150:151], v228 offset:12544\n\t" \
  "v_cvt_pk_bf16_f32 v156, v104, v105\n\t" \
  "v_mfma_f32_16x16x32_bf16 v[36:39], a[216:219], v[56:59], v[36:39]\n\t" \
  "ds_read_b64_tr_b16 a[152:153], v229 offset:256\n\t" \
  "v_cvt_pk_bf16_f32 v157, v106, v107\n\t" \
  "v_mfma_f32_16x16x32_bf16 v[36:39], a[212:215], v[52:55], v[36:39]\n\t" \
  "v_exp_f32 v5, v5\n\t" \
  "v_mfma_f32_16x16x32_bf16 v[36:39], a[208:211], v[48:51], v[36:39]\n\t" \
  "ds_read_b64_tr_b16 a[154:155], v229 offset:4352\n\t" \
  "v_cvt_pk_bf16_f32 v158, v108, v109\n\t" \
  "v_mfma_f32_16x16x32_bf16 v[40:43], a[224:227], v[48:51], v[180:183]\n\t" \
  "v_exp_f32 v6, v6\n\t" \
  "v_mfma_f32_16x16x32_bf16 v[40:43], a[228:231], v[52:55], v[40:43]\n\t" \
  "ds_read_b64_tr_b16 a[156:157], v229 offset:8448\n\t" \
  "v_cvt_pk_bf16_f32 v159, v110, v111\n\t" \
  "v_mfma_f32_16x16x32_bf16 v[40:43], a[232:235], v[56:59], v[40:43]\n\t" \
  "v_exp_f32 v7, v7\n\t" \
  "v_mfma_f32_16x16x32_bf16 v[40:43], a[236:239], v[60:63], v[40:43]\n\t" \
  "v_exp_f32 v8, v8\n\t" \
  "v_mfma_f32_16x16x32_bf16 v[44:47], a[252:255], v[60:63], v[180:183]\n\t" \
  "v_exp_f32 v9, v9\n\t" \
  "v_mfma_f32_16x16x32_bf16 v[44:47], a[248:251], v[56:59], v[44:47]\n\t" \
  "ds_read_b64_tr_b16 a[158:159], v229 offset:12544\n\t" \
  "ds_read_b64_tr_b16 a[160:161], v228 offset:512\n\t" \
  "v_mfma_f32_16x16x32_bf16 v[44:47], a[244:247], v[52:55], v[44:47]\n\t" \
  "v_exp_f32 v10, v10\n\t" \
  "v_mfma_f32_16x16x32_bf16 v[44:47], a[240:243], v[48:51], v[44:47]\n\t" \
  "v_exp_f32 v11, v11\n\t" \
  "v_mfma_f32_16x16x32_bf16 v[64:67], a[192:195], v[80:83], v[184:187]\n\t" \
  "buffer_load_dword %[pfd], v247, %[ws], 0 offen sc1\n\t" \
  "v_mfma_f32_16x16x32_bf16 v[64:67], a[196:199], v[84:87], v[64:67]\n\t" \
  "ds_read_b64_tr_b16 a[162:163], v228 offset:4608\n\t" \
  "ds_read_b64_tr_b16 a[164:165], v228 offset:8704\n\t" \
  "v_mfma_f32_16x16x32_bf16 v[64:67], a[200:203], v[88:91], v[64:67]\n\t" \
  "v_exp_f32 v12, v12\n\t" \
  "v_mfma_f32_16x16x32_bf16 v[64:67], a[204:207], v[92:95], v[64:67]\n\t" \
  "v_exp_f32 v13, v13\n\t" \
  "v_mfma_f32_16x16x32_bf16 v[68:71], a[220:223], v[92:95], v[184:187]\n\t" \
  "ds_read_b64_tr_b16 a[166:167], v228 offset:12800\n\t" \
  "ds_read_b64_tr_b16 a[168:169], v229 offset:512\n\t" \
  "v_mfma_f32_16x16x32_bf16 v[68:71], a[216:219], v[88:91], v[68:71]\n\t" \
  "v_exp_f32 v14, v14\n\t" \
  "v_mfma_f32_16x16x32_bf16 v[68:71], a[212:215], v[84:87], v[68:71]\n\t" \
  "v_exp_f32 v15, v15\n\t" \
  "v_mfma_f32_16x16x32_bf16 v[68:71], a[208:211], v[80:83], v[68:71]\n\t" \
  "ds_read_b64_tr_b16 a[170:171], v229 offset:4608\n\t" \
  "ds_read_b64_tr_b16 a[172:173], v229 offset:8704\n\t" \
  "v_mfma_f32_16x16x32_bf16 v[72:75], a[224:227], v[80:83], v[184:187]\n\t" \
  "v_exp_f32 v32, v32\n\t" \
  "v_mfma_f32_16x16x32_bf16 v[72:75], a[228:231], v[84:87], v[72:75]\n\t" \
  "v_exp_f32 v33, v33\n\t" \
  "v_mfma_f32_16x16x32_bf16 v[72:75], a[232:235], v[88:91], v[72:75]\n\t" \
  "v_exp_f32 v34, v34\n\t" \
  "v_mfma_f32_16x16x32_bf16 v[72:75], a[236:239], v[92:95], v[72:75]\n\t" \
  "ds_read_b64_tr_b16 a[174:175], v229 offset:12800\n\t" \
  "ds_read_b64_tr_b16 a[176:177], v228 offset:768\n\t" \
  "v_mfma_f32_16x16x32_bf16 v[76:79], a[252:255], v[92:95], v[184:187]\n\t" \
  "v_exp_f32 v35, v35\n\t" \
  "v_mfma_f32_16x16x32_bf16 v[76:79], a[248:251], v[88:91], v[76:79]\n\t" \
  "v_exp_f32 v36, v36\n\t" \
  "v_mfma_f32_16x16x32_bf16 v[76:79], a[244:247], v[84:87], v[76:79]\n\t" \
  "ds_read_b64_tr_b16 a[178:179], v228 offset:4864\n\t" \
  "ds_read_b64_tr_b16 a[180:181], v228 offset:8960\n\t" \
  "v_mfma_f32_16x16x32_bf16 v[76:79], a[240:243], v[80:83], v[76:79]\n\t" \
  "v_exp_f32 v37, v37\n\t" \
  "v_mfma_f32_16x16x32_bf16 v[96:99], a[192:195], v[112:115], v[188:191]\n\t" \
  "v_exp_f32 v38, v38\n\t" \
  "v_mfma_f32_16x16x32_bf16 v[96:99], a[196:199], v[116:119], v[96:99]\n\t" \
  "v_exp_f32 v39, v39\n\t" \
  "v_mfma_f32_16x16x32_bf16 v[96:99], a[200:203], v[120:123], v[96:99]\n\t" \
  "ds_read_b64_tr_b16 a[182:183], v228 offset:13056\n\t" \
  "ds_read_b64_tr_b16 a[184:185], v229 offset:768\n\t" \
  "v_mfma_f32_16x16x32_bf16 v[96:99], a[204:207], v[124:127], v[96:99]\n\t" \
  "v_exp_f32 v40, v40\n\t" \
  "v_mfma_f32_16x16x32_bf16 v[100:103], a[220:223], v[124:127], v[188:191]\n\t" \
  "v_exp_f32 v41, v41\n\t" \
  "v_mfma_f32_16x16x32_bf16 v[100:103], a[216:219], v[120:123], v[100:103]\n\t" \
  "ds_read_b64_tr_b16 a[186:187], v229 offset:4864\n\t" \
  "ds_read_b64_tr_b16 a[188:189], v229 offset:8960\n\t" \
  "v_mfma_f32_16x16x32_bf16 v[100:103], a[212:215], v[116:119], v[100:103]\n\t" \
  "v_exp_f32 v42, v42\n\t" \
  "v_mfma_f32_16x16x32_bf16 v[100:103], a[208:211], v[112:115], v[100:103]\n\t" \
  "v_exp_f32 v43, v43\n\t" \
  "v_mfma_f32_16x16x32_bf16 v[104:107], a[224:227], v[112:115], v[188:191]\n\t" \
  "v_exp_f32 v44, v44\n\t" \
  "v_mfma_f32_16x16x32_bf16 v[104:107], a[228:231], v[116:119], v[104:107]\n\t" \
  "ds_read_b64_tr_b16 a[190:191], v229 offset:13056\n\t" \
  "v_mfma_f32_16x16x32_bf16 v[104:107], a[232:235], v[120:123], v[104:107]\n\t" \
  "s_getreg_b32 s98, hwreg(HW_REG_IB_STS, 0, 4)\n\t" \
  "s_cmp_lg_u32 s98, 0\n\t" \
  "s_addc_u32 %[na], %[na], 0\n\t" \
  "v_mfma_f32_16x16x32_bf16 v[104:107], a[236:239], v[124:127], v[104:107]\n\t" \
  "v_exp_f32 v45, v45\n\t" \
  "v_mfma_f32_16x16x32_bf16 v[108:111], a[252:255], v[124:127], v[188:191]\n\t" \
  "v_exp_f32 v46, v46\n\t" \
  "v_mfma_f32_16x16x32_bf16 v[108:111], a[248:251], v[120:123], v[108:111]\n\t" \
  "s_getreg_b32 s98, hwreg(HW_REG_IB_STS, 0, 4)\n\t" \
  "s_cmp_lg_u32 s98, 0\n\t" \
  "s_addc_u32 %[na], %[na], 0\n\t" \
  "v_mfma_f32_16x16x32_bf16 v[108:111], a[244:247], v[116:119], v[108:111]\n\t" \
  "v_exp_f32 v47, v47\n\t" \
  "v_mfma_f32_16x16x32_bf16 v[108:111], a[240:243], v[112:115], v[108:111]\n\t" \
  "s_waitcnt vmcnt(4) lgkmcnt(0)\n\t" \
  "s_barrier\n\t" \
  STA("s_memrealtime s[94:95]\n\t") \
  STA("s_memtime s[92:93]\n\t") \
  "v_mfma_f32_16x16x32_bf16 a[0:3], a[128:131], v[128:131], a[0:3]\n\t" \
  "v_exp_f32 v64, v64\n\t" \
  "v_mfma_f32_16x16x32_bf16 a[0:3], a[132:135], v[132:135], a[0:3]\n\t" \
  "v_exp_f32 v65, v65\n\t" \
  "v_mfma_f32_16x16x32_bf16 a[16:19], a[136:139], v[128:131], a[16:19]\n\t" \
  "ds_read_b128 a[192:195], v220 offset:0\n\t" \
  "ds_read_b128 a[196:199], v221 offset:0\n\t" \
  "v_mfma_f32_16x16x32_bf16 a[16:19], a[140:143], v[132:135], a[16:19]\n\t" \
  "v_exp_f32 v66, v66\n\t" \
  "v_mfma_f32_16x16x32_bf16 a[32:35], a[144:147], v[128:131], a[32:35]\n\t" \
  "v_exp_f32 v67, v67\n\t" \
  "v_mfma_f32_16x16x32_bf16 a[32:35], a[148:151], v[132:135], a[32:35]\n\t" \
  "v_exp_f32 v68, v68\n\t" \
  "v_mfma_f32_16x16x32_bf16 a[48:51], a[152:155], v[128:131], a[48:51]\n\t" \
  STS("v_writelane_b32 %[stv], %[na], 58\n\t") \
  "s_cmp_ge_u32 %[na], 1\n\t" \
  "s_cselect_b32 s98, 0x4000, 0\n\t" \
  "s_or_b32 s98, s98, %[tag]\n\t" \
  "s_cmp_ge_u32 %[na], 2\n\t" \
  "s_cselect_b32 %[na], 2.0, 0\n\t" \
  "s_or_b32 s98, s98, %[na]\n\t" \
  "v_mov_b32 v209, s98\n\t" \
  "v_mfma_f32_16x16x32_bf16 a[48:51], a[156:159], v[132:135], a[48:51]\n\t" \
  "v_exp_f32 v69, v69\n\t" \
  "v_mfma_f32_16x16x32_bf16 a[64:67], a[160:163], v[128:131], a[64:67]\n\t" \
  "ds_read_b128 a[200:203], v222 offset:0\n\t" \
  "ds_read_b128 a[204:207], v223 offset:0\n\t" \
  "v_mfma_f32_16x16x32_bf16 a[64:67], a[164:167], v[132:135], a[64:67]\n\t" \
  "v_exp_f32 v70, v70\n\t" \
  "v_mfma_f32_16x16x32_bf16 a[80:83], a[168:171], v[128:131], a[80:83]\n\t" \
  "ds_read_b128 a[208:211], v220 offset:4096\n\t" \
  "ds_read_b128 a[212:215], v221 offset:4096\n\t" \
  "v_mfma_f32_16x16x32_bf16 a[80:83], a[172:175], v[132:135], a[80:83]\n\t" \
  "v_bfrev_b32 v205, 1\n\t" \
  "v_writelane_b32 v205, %[vo], 0\n\t" \
  "v_mfma_f32_16x16x32_bf16 a[96:99], a[176:179], v[128:131], a[96:99]\n\t" \
  STA("v_writelane_b32 %[stv], s94, 36\n\t") \
  STA("v_writelane_b32 %[stv], s92, 37\n\t") \
  "buffer_store_dword v209, v205, %[ws], 0 offen sc1\n\t" \
  "v_mfma_f32_16x16x32_bf16 a[96:99], a[180:183], v[132:135], a[96:99]\n\t" \
  "v_exp_f32 v71, v71\n\t" \
  "v_mfma_f32_16x16x32_bf16 a[112:115], a[184:187], v[128:131], a[112:115]\n\t" \
  "v_exp_f32 v72, v72\n\t" \
  "v_mfma_f32_16x16x32_bf16 a[112:115], a[188:191], v[132:135], a[112:115]\n\t" \
  "v_exp_f32 v73, v73\n\t" \
  "v_mfma_f32_16x16x32_bf16 v[160:163], v[216:219], v[128:131], v[160:163]\n\t" \
  "v_exp_f32 v74, v74\n\t" \
  "v_mfma_f32_16x16x32_bf16 v[160:163], v[216:219], v[132:135], v[160:163]\n\t" \
  "v_exp_f32 v75, v75\n\t" \
  "v_mfma_f32_16x16x32_bf16 a[4:7], a[128:131], v[136:139], a[4:7]\n\t" \
  "v_exp_f32 v76, v76\n\t" \
  "v_mfma_f32_16x16x32_bf16 a[4:7], a[132:135], v[140:143], a[4:7]\n\t" \
  "v_exp_f32 v77, v77\n\t" \
  "v_mfma_f32_16x16x32_bf16 a[20:23], a[136:139], v[136:139], a[20:23]\n\t" \
  "s_add_u32 m0, %[m], 65536\n\t" \
  "v_exp_f32 v78, v78\n\t" \
  "v_mfma_f32_16x16x32_bf16 a[20:23], a[140:143], v[140:143], a[20:23]\n\t" \
  "s_add_u32 s96, %[sofk], 81920\n\t" \
  "v_exp_f32 v79, v79\n\t" \
  "v_mfma_f32_16x16x32_bf16 a[36:39], a[144:147], v[136:139], a[36:39]\n\t" \
  "v_exp_f32 v96, v96\n\t" \
  "v_mfma_f32_16x16x32_bf16 a[36:39], a[148:151], v[140:143], a[36:39]\n\t" \
  "ds_read_b128 a[216:219], v222 offset:4096\n\t" \
  "v_cvt_pk_bf16_f32 v128, v0, v1\n\t" \
  "v_mfma_f32_16x16x32_bf16 a[52:55], a[152:155], v[136:139], a[52:55]\n\t" \
  "v_exp_f32 v97, v97\n\t" \
  "v_mfma_f32_16x16x32_bf16 a[52:55], a[156:159], v[140:143], a[52:55]\n\t" \
  "v_exp_f32 v98, v98\n\t" \
  "v_mfma_f32_16x16x32_bf16 a[68:71], a[160:163], v[136:139], a[68:71]\n\t" \
  "v_exp_f32 v99, v99\n\t" \
  "v_mfma_f32_16x16x32_bf16 a[68:71], a[164:167], v[140:143], a[68:71]\n\t" \
  "buffer_load_dwordx4 v224, %[sK], s96 offen offset:0 lds\n\t" \
  "v_mfma_f32_16x16x32_bf16 a[84:87], a[168:171], v[136:139], a[84:87]\n\t" \
  "ds_read_b128 a[220:223], v223 offset:4096\n\t" \
  "v_cvt_pk_bf16_f32 v129, v2, v3\n\t" \
  "v_mfma_f32_16x16x32_bf16 a[84:87], a[172:175], v[140:143], a[84:87]\n\t" \
  "v_exp_f32 v100, v100\n\t" \
  "v_mfma_f32_16x16x32_bf16 a[100:103], a[176:179], v[136:139], a[100:103]\n\t" \
  "ds_read_b128 a[224:227], v220 offset:8192\n\t" \
  "v_cvt_pk_bf16_f32 v130, v4, v5\n\t" \
  "v_mfma_f32_16x16x32_bf16 a[100:103], a[180:183], v[140:143], a[100:103]\n\t" \
  "buffer_load_dwordx4 v225, %[sK], s96 offen offset:1024 lds\n\t" \
  "v_mfma_f32_16x16x32_bf16 a[116:119], a[184:187], v[136:139], a[116:119]\n\t" \
  "v_exp_f32 v101, v101\n\t" \
  "v_mfma_f32_16x16x32_bf16 a[116:119], a[188:191], v[140:143], a[116:119]\n\t" \
  "v_exp_f32 v102, v102\n\t" \
  "v_mfma_f32_16x16x32_bf16 v[164:167], v[216:219], v[136:139], v[164:167]\n\t" \
  "buffer_load_dwordx4 v226, %[sK], s96 offen offset:2048 lds\n\t" \
  "v_mfma_f32_16x16x32_bf16 v[164:167], v[216:219], v[140:143], v[164:167]\n\t" \
  "ds_read_b128 a[228:231], v221 offset:8192\n\t" \
  "v_cvt_pk_bf16_f32 v131, v6, v7\n\t" \
  "v_mfma_f32_16x16x32_bf16 a[8:11], a[128:131], v[144:147], a[8:11]\n\t" \
  "v_exp_f32 v103, v103\n\t" \
  "v_mfma_f32_16x16x32_bf16 a[8:11], a[132:135], v[148:151], a[8:11]\n\t" \
  "v_exp_f32 v104, v104\n\t" \
  "v_mfma_f32_16x16x32_bf16 a[24:27], a[136:139], v[144:147], a[24:27]\n\t" \
  "buffer_load_dwordx4 v227, %[sK], s96 offen offset:3072 lds\n\t" \
  "v_mfma_f32_16x16x32_bf16 a[24:27], a[140:143], v[148:151], a[24:27]\n\t" \
  "ds_read_b128 a[232:235], v222 offset:8192\n\t" \
  "v_cvt_pk_bf16_f32 v132, v8, v9\n\t" \
  "v_mfma_f32_16x16x32_bf16 a[40:43], a[144:147], v[144:147], a[40:43]\n\t" \
  "v_exp_f32 v105, v105\n\t" \
  "v_mfma_f32_16x16x32_bf16 a[40:43], a[148:151], v[148:151], a[40:43]\n\t" \
  "s_add_u32 m0, %[m], 32768\n\t" \
  ".Lhs7:\n\ts_cbranch_execz .Lhs7n\n\t" \
  "s_mov_b32 s96, 0\n\t" \
  "ds_read_b128 a[236:239], v223 offset:8192\n\t" \
  "v_mfma_f32_16x16x32_bf16 a[56:59], a[152:155], v[144:147], a[56:59]\n\t" \
  "buffer_load_dwordx4 v232, %[sG], s96 offen offset:0 lds\n\t" \
  "v_mfma_f32_16x16x32_bf16 a[56:59], a[156:159], v[148:151], a[56:59]\n\t" \
  "s_add_u32 m0, %[m], 81920\n\t" \
  "v_exp_f32 v106, v106\n\t" \
  "v_mfma_f32_16x16x32_bf16 a[72:75], a[160:163], v[144:147], a[72:75]\n\t" \
  "s_add_u32 s96, %[sofk], 65536\n\t" \
  "v_exp_f32 v107, v107\n\t" \
  "v_mfma_f32_16x16x32_bf16 a[72:75], a[164:167], v[148:151], a[72:75]\n\t" \
  "ds_read_b128 a[240:243], v220 offset:12288\n\t" \
  "v_cvt_pk_bf16_f32 v133, v10, v11\n\t" \
  "v_mfma_f32_16x16x32_bf16 a[88:91], a[168:171], v[144:147], a[88:91]\n\t" \
  "v_exp_f32 v108, v108\n\t" \
  "v_mfma_f32_16x16x32_bf16 a[88:91], a[172:175], v[148:151], a[88:91]\n\t" \
  "v_exp_f32 v109, v109\n\t" \
  "v_mfma_f32_16x16x32_bf16 a[104:107], a[176:179], v[144:147], a[104:107]\n\t" \
  "v_exp_f32 v110, v110\n\t" \
  "v_mfma_f32_16x16x32_bf16 a[104:107], a[180:183], v[148:151], a[104:107]\n\t" \
  "ds_read_b128 a[244:247], v221 offset:12288\n\t" \
  "v_cvt_pk_bf16_f32 v134, v12, v13\n\t" \
  "v_mfma_f32_16x16x32_bf16 a[120:123], a[184:187], v[144:147], a[120:123]\n\t" \
  "v_exp_f32 v111, v111\n\t" \
  "v_mfma_f32_16x16x32_bf16 a[120:123], a[188:191], v[148:151], a[120:123]\n\t" \
  "buffer_load_dwordx4 v230, %[sV], s96 offen offset:0 lds\n\t" \
  "v_mfma_f32_16x16x32_bf16 v[168:171], v[216:219], v[144:147], v[168:171]\n\t" \
  "ds_read_b128 a[248:251], v222 offset:12288\n\t" \
  "v_cvt_pk_bf16_f32 v135, v14, v15\n\t" \
  "v_mfma_f32_16x16x32_bf16 v[168:171], v[216:219], v[148:151], v[168:171]\n\t" \
  "ds_read_b128 a[252:255], v223 offset:12288\n\t" \
  "v_cvt_pk_bf16_f32 v136, v32, v33\n\t" \
  "v_mfma_f32_16x16x32_bf16 a[12:15], a[128:131], v[152:155], a[12:15]\n\t" \
  "v_cvt_pk_bf16_f32 v137, v34, v35\n\t" \
  "v_cvt_pk_bf16_f32 v138, v36, v37\n\t" \
  "v_mfma_f32_16x16x32_bf16 a[12:15], a[132:135], v[156:159], a[12:15]\n\t" \
  "buffer_load_dwordx4 v231, %[sV], s96 offen offset:1024 lds\n\t" \
  "v_mfma_f32_16x16x32_bf16 a[28:31], a[136:139], v[152:155], a[28:31]\n\t" \
  "v_cvt_pk_bf16_f32 v139, v38, v39\n\t" \
  "v_cvt_pk_bf16_f32 v140, v40, v41\n\t" \
  "v_mfma_f32_16x16x32_bf16 a[28:31], a[140:143], v[156:159], a[28:31]\n\t" \
  "v_cvt_pk_bf16_f32 v141, v42, v43\n\t" \
  "v_cvt_pk_bf16_f32 v142, v44, v45\n\t" \
  "v_mfma_f32_16x16x32_bf16 a[44:47], a[144:147], v[152:155], a[44:47]\n\t" \
  "buffer_load_dwordx4 v230, %[sV], s96 offen offset:2048 lds\n\t" \
  "v_mfma_f32_16x16x32_bf16 a[44:47], a[148:151], v[156:159], a[44:47]\n\t" \
  "v_cvt_pk_bf16_f32 v143, v46, v47\n\t" \
  "v_cvt_pk_bf16_f32 v144, v64, v65\n\t" \
  "v_mfma_f32_16x16x32_bf16 a[60:63], a[152:155], v[152:155], a[60:63]\n\t" \
  "v_cvt_pk_bf16_f32 v145, v66, v67\n\t" \
  "v_cvt_pk_bf16_f32 v146, v68, v69\n\t" \
  "v_mfma_f32_16x16x32_bf16 a[60:63], a[156:159], v[156:159], a[60:63]\n\t" \
  "v_cvt_pk_bf16_f32 v147, v70, v71\n\t" \
  "v_cvt_pk_bf16_f32 v148, v72, v73\n\t" \
  "v_mfma_f32_16x16x32_bf16 a[76:79], a[160:163], v[152:155], a[76:79]\n\t" \
  "buffer_load_dwordx4 v231, %[sV], s96 offen offset:3072 lds\n\t" \
  "v_mfma_f32_16x16x32_bf16 a[76:79], a[164:167], v[156:159], a[76:79]\n\t" \
  "v_cvt_pk_bf16_f32 v149, v74, v75\n\t" \
  "v_cvt_pk_bf16_f32 v150, v76, v77\n\t" \
  "v_mfma_f32_16x16x32_bf16 a[92:95], a[168:171], v[152:155], a[92:95]\n\t" \
  "s_add_u32 m0, %[m], 32768\n\t" \
  "s_mov_b32 s96, 6144\n\t" \
  "v_cvt_pk_bf16_f32 v151, v78, v79\n\t" \
  "v_mfma_f32_16x16x32_bf16 a[92:95], a[172:175], v[156:159], a[92:95]\n\t" \
  "v_mfma_f32_16x16x32_bf16 a[108:111], a[176:179], v[152:155], a[108:111]\n\t" \
  "buffer_load_dwordx4 v232, %[sG], s96 offen offset:1024 lds\n\t" \
  "v_mfma_f32_16x16x32_bf16 a[108:111], a[180:183], v[156:159], a[108:111]\n\t" \
  "v_mfma_f32_16x16x32_bf16 a[124:127], a[184:187], v[152:155], a[124:127]\n\t" \
  "v_mfma_f32_16x16x32_bf16 a[124:127], a[188:191], v[156:159], a[124:127]\n\t" \
  "s_add_u32 s98, %[pf4], 28672\n\t" \
  "v_mfma_f32_16x16x32_bf16 v[172:175], v[216:219], v[152:155], v[172:175]\n\t" \
  "buffer_load_dword %[pfd], v245, %[sG], s98 offen\n\t" \
  "v_mfma_f32_16x16x32_bf16 v[172:175], v[216:219], v[156:159], v[172:175]\n\t" \
  "s_waitcnt lgkmcnt(0)\n\t" \
  "v_cvt_pk_bf16_f32 v152, v96, v97\n\t" \
  "v_cvt_pk_bf16_f32 v153, v98, v99\n\t" \
  STA("s_memrealtime s[94:95]\n\t") \
  STA("s_memtime s[92:93]\n\t") \
  "v_mfma_f32_16x16x32_bf16 v[0:3], a[192:195], v[16:19], v[176:179]\n\t" \
  "ds_read_b64_tr_b16 a[128:129], v237 offset:0\n\t" \
  "v_cvt_pk_bf16_f32 v154, v100, v101\n\t" \
  "v_mfma_f32_16x16x32_bf16 v[0:3], a[196:199], v[20:23], v[0:3]\n\t" \
  "ds_read_b64_tr_b16 a[130:131], v237 offset:4096\n\t" \
  "v_cvt_pk_bf16_f32 v155, v102, v103\n\t" \
  "v_mfma_f32_16x16x32_bf16 v[0:3], a[200:203], v[24:27], v[0:3]\n\t" \
  "ds_read_b64_tr_b16 a[132:133], v237 offset:8192\n\t" \
  "v_cvt_pk_bf16_f32 v156, v104, v105\n\t" \
  "v_mfma_f32_16x16x32_bf16 v[0:3], a[204:207], v[28:31], v[0:3]\n\t" \
  "ds_read_b64_tr_b16 a[134:135], v237 offset:12288\n\t" \
  "v_cvt_pk_bf16_f32 v157, v106, v107\n\t" \
  "v_mfma_f32_16x16x32_bf16 v[4:7], a[220:223], v[28:31], v[176:179]\n\t" \
  "ds_read_b64_tr_b16 a[136:137], v238 offset:0\n\t" \
  "v_cvt_pk_bf16_f32 v158, v108, v109\n\t" \
  "v_mfma_f32_16x16x32_bf16 v[4:7], a[216:219], v[24:27], v[4:7]\n\t" \
  "ds_read_b64_tr_b16 a[138:139], v238 offset:4096\n\t" \
  "v_cvt_pk_bf16_f32 v159, v110, v111\n\t" \
  "v_mfma_f32_16x16x32_bf16 v[4:7], a[212:215], v[20:23], v[4:7]\n\t" \
  "v_exp_f32 v0, v0\n\t" \
  "v_mfma_f32_16x16x32_bf16 v[4:7], a[208:211], v[16:19], v[4:7]\n\t" \
  "v_exp_f32 v1, v1\n\t" \
  "v_mfma_f32_16x16x32_bf16 v[8:11], a[224:227], v[16:19], v[176:179]\n\t" \
  "v_exp_f32 v2, v2\n\t" \
  "v_mfma_f32_16x16x32_bf16 v[8:11], a[228:231], v[20:23], v[8:11]\n\t" \
  "v_exp_f32 v3, v3\n\t" \
  "v_mfma_f32_16x16x32_bf16 v[8:11], a[232:235], v[24:27], v[8:11]\n\t" \
  "v_mfma_f32_16x16x32_bf16 v[8:11], a[236:239], v[28:31], v[8:11]\n\t" \
  "s_add_u32 s98, %[pf2], 0\n\t" \
  "v_exp_f32 v4, v4\n\t" \
  "v_mfma_f32_16x16x32_bf16 v[12:15], a[252:255], v[28:31], v[176:179]\n\t" \
  STA("v_writelane_b32 %[stv], s94, 38\n\t") \
  STA("v_writelane_b32 %[stv], s92, 39\n\t") \
  "buffer_load_dword %[pfd], v202, %[sM], s98 offen\n\t" \
  "v_mfma_f32_16x16x32_bf16 v[12:15], a[248:251], v[24:27], v[12:15]\n\t" \
  "ds_read_b64_tr_b16 a[140:141], v238 offset:8192\n\t" \
  "ds_read_b64_tr_b16 a[142:143], v238 offset:12288\n\t" \
  "v_mfma_f32_16x16x32_bf16 v[12:15], a[244:247], v[20:23], v[12:15]\n\t" \
  "v_exp_f32 v5, v5\n\t" \
  "v_mfma_f32_16x16x32_bf16 v[12:15], a[240:243], v[16:19], v[12:15]\n\t" \
  "v_exp_f32 v6, v6\n\t" \
  "v_mfma_f32_16x16x32_bf16 v[32:35], a[192:195], v[48:51], v[180:183]\n\t" \
  "ds_read_b64_tr_b16 a[144:145], v237 offset:256\n\t" \
  "ds_read_b64_tr_b16 a[146:147], v237 offset:4352\n\t" \
  "v_mfma_f32_16x16x32_bf16 v[32:35], a[196:199], v[52:55], v[32:35]\n\t" \
  "v_exp_f32 v7, v7\n\t" \
  "v_mfma_f32_16x16x32_bf16 v[32:35], a[200:203], v[56:59], v[32:35]\n\t" \
  "v_exp_f32 v8, v8\n\t" \
  "v_mfma_f32_16x16x32_bf16 v[32:35], a[204:207], v[60:63], v[32:35]\n\t" \
  "v_exp_f32 v9, v9\n\t" \
  "v_mfma_f32_16x16x32_bf16 v[36:39], a[220:223], v[60:63], v[180:183]\n\t" \
  "ds_read_b64_tr_b16 a[148:149], v237 offset:8448\n\t" \
  "ds_read_b64_tr_b16 a[150:151], v237 offset:12544\n\t" \
  "v_mfma_f32_16x16x32_bf16 v[36:39], a[216:219], v[56:59], v[36:39]\n\t" \
  "v_exp_f32 v10, v10\n\t" \
  "v_mfma_f32_16x16x32_bf16 v[36:39], a[212:215], v[52:55], v[36:39]\n\t" \
  "v_exp_f32 v11, v11\n\t" \
  "v_mfma_f32_16x16x32_bf16 v[36:39], a[208:211], v[48:51], v[36:39]\n\t" \
  "ds_read_b64_tr_b16 a[152:153], v238 offset:256\n\t" \
  "ds_read_b64_tr_b16 a[154:155], v238 offset:4352\n\t" \
  "v_mfma_f32_16x16x32_bf16 v[40:43], a[224:227], v[48:51], v[180:183]\n\t" \
  "v_exp_f32 v12, v12\n\t" \
  "v_mfma_f32_16x16x32_bf16 v[40:43], a[228:231], v[52:55], v[40:43]\n\t" \
  "v_exp_f32 v13, v13\n\t" \
  "v_mfma_f32_16x16x32_bf16 v[40:43], a[232:235], v[56:59], v[40:43]\n\t" \
  "v_exp_f32 v14, v14\n\t" \
  "v_mfma_f32_16x16x32_bf16 v[40:43], a[236:239], v[60:63], v[40:43]\n\t" \
  "ds_read_b64_tr_b16 a[156:157], v238 offset:8448\n\t" \
  "ds_read_b64_tr_b16 a[158:159], v238 offset:12544\n\t" \
  "v_mfma_f32_16x16x32_bf16 v[44:47], a[252:255], v[60:63], v[180:183]\n\t" \
  "v_exp_f32 v15, v15\n\t" \
  "v_mfma_f32_16x16x32_bf16 v[44:47], a[248:251], v[56:59], v[44:47]\n\t" \
  "v_exp_f32 v32, v32\n\t" \
  "v_mfma_f32_16x16x32_bf16 v[44:47], a[244:247], v[52:55], v[44:47]\n\t" \
  "ds_read_b64_tr_b16 a[160:161], v237 offset:512\n\t" \
  "ds_read_b64_tr_b16 a[162:163], v237 offset:4608\n\t" \
  "v_mfma_f32_16x16x32_bf16 v[44:47], a[240:243], v[48:51], v[44:47]\n\t" \
  "v_exp_f32 v33, v33\n\t" \
  "v_mfma_f32_16x16x32_bf16 v[64:67], a[192:195], v[80:83], v[184:187]\n\t" \
  "v_exp_f32 v34, v34\n\t" \
  "v_mfma_f32_16x16x32_bf16 v[64:67], a[196:199], v[84:87], v[64:67]\n\t" \
  "v_exp_f32 v35, v35\n\t" \
  "v_mfma_f32_16x16x32_bf16 v[64:67], a[200:203], v[88:91], v[64:67]\n\t" \
  "ds_read_b64_tr_b16 a[164:165], v237 offset:8704\n\t" \
  "ds_read_b64_tr_b16 a[166:167], v237 offset:12800\n\t" \
  "v_mfma_f32_16x16x32_bf16 v[64:67], a[204:207], v[92:95], v[64:67]\n\t" \
  "v_exp_f32 v36, v36\n\t" \
  "v_mfma_f32_16x16x32_bf16 v[68:71], a[220:223], v[92:95], v[184:187]\n\t" \
  "v_exp_f32 v37, v37\n\t" \
  "v_mfma_f32_16x16x32_bf16 v[68:71], a[216:219], v[88:91], v[68:71]\n\t" \
  "ds_read_b64_tr_b16 a[168:169], v238 offset:512\n\t" \
  "ds_read_b64_tr_b16 a[170:171], v238 offset:4608\n\t" \
  "v_mfma_f32_16x16x32_bf16 v[68:71], a[212:215], v[84:87], v[68:71]\n\t" \
  "v_exp_f32 v38, v38\n\t" \
  "v_mfma_f32_16x16x32_bf16 v[68:71], a[208:211], v[80:83], v[68:71]\n\t" \
  "v_exp_f32 v39, v39\n\t" \
  "v_mfma_f32_16x16x32_bf16 v[72:75], a[224:227], v[80:83], v[184:187]\n\t" \
  "v_exp_f32 v40, v40\n\t" \
  "v_mfma_f32_16x16x32_bf16 v[72:75], a[228:231], v[84:87], v[72:75]\n\t" \
  "ds_read_b64_tr_b16 a[172:173], v238 offset:8704\n\t" \
  "ds_read_b64_tr_b16 a[174:175], v238 offset:12800\n\t" \
  "v_mfma_f32_16x16x32_bf16 v[72:75], a[232:235], v[88:91], v[72:75]\n\t" \
  "v_exp_f32 v41, v41\n\t" \
  "v_mfma_f32_16x16x32_bf16 v[72:75], a[236:239], v[92:95], v[72:75]\n\t" \
  "v_exp_f32 v42, v42\n\t" \
  "v_mfma_f32_16x16x32_bf16 v[76:79], a[252:255], v[92:95], v[184:187]\n\t" \
  "ds_read_b64_tr_b16 a[176:177], v237 offset:768\n\t" \
  "ds_read_b64_tr_b16 a[178:179], v237 offset:4864\n\t" \
  "v_mfma_f32_16x16x32_bf16 v[76:79], a[248:251], v[88:91], v[76:79]\n\t" \
  "v_exp_f32 v43, v43\n\t" \
  "v_mfma_f32_16x16x32_bf16 v[76:79], a[244:247], v[84:87], v[76:79]\n\t" \
  "v_exp_f32 v44, v44\n\t" \
  "v_mfma_f32_16x16x32_bf16 v[76:79], a[240:243], v[80:83], v[76:79]\n\t" \
  "v_exp_f32 v45, v45\n\t" \
  "v_mfma_f32_16x16x32_bf16 v[96:99], a[192:195], v[112:115], v[188:191]\n\t" \
  "ds_read_b64_tr_b16 a[180:181], v237 offset:8960\n\t" \
  "ds_read_b64_tr_b16 a[182:183], v237 offset:13056\n\t" \
  "v_mfma_f32_16x16x32_bf16 v[96:99], a[196:199], v[116:119], v[96:99]\n\t" \
  "v_exp_f32 v46, v46\n\t" \
  "v_mfma_f32_16x16x32_bf16 v[96:99], a[200:203], v[120:123], v[96:99]\n\t" \
  "v_exp_f32 v47, v47\n\t" \
  "v_mfma_f32_16x16x32_bf16 v[96:99], a[204:207], v[124:127], v[96:99]\n\t" \
  "ds_read_b64_tr_b16 a[184:185], v238 offset:768\n\t" \
  "ds_read_b64_tr_b16 a[186:187], v238 offset:4864\n\t" \
  "v_mfma_f32_16x16x32_bf16 v[100:103], a[220:223], v[124:127], v[188:191]\n\t" \
  "v_exp_f32 v64, v64\n\t" \
  "v_mfma_f32_16x16x32_bf16 v[100:103], a[216:219], v[120:123], v[100:103]\n\t" \
  "v_exp_f32 v65, v65\n\t" \
  "v_mfma_f32_16x16x32_bf16 v[100:103], a[212:215], v[116:119], v[100:103]\n\t" \
  "v_exp_f32 v66, v66\n\t" \
  "v_mfma_f32_16x16x32_bf16 v[100:103], a[208:211], v[112:115], v[100:103]\n\t" \
  "ds_read_b64_tr_b16 a[188:189], v238 offset:8960\n\t" \
  "ds_read_b64_tr_b16 a[190:191], v238 offset:13056\n\t" \
  "v_mfma_f32_16x16x32_bf16 v[104:107], a[224:227], v[112:115], v[188:191]\n\t" \
  "v_exp_f32 v67, v67\n\t" \
  "v_mfma_f32_16x16x32_bf16 v[104:107], a[228:231], v[116:119], v[104:107]\n\t" \
  "v_exp_f32 v68, v68\n\t" \
  "v_mfma_f32_16x16x32_bf16 v[104:107], a[232:235], v[120:123], v[104:107]\n\t" \
  "v_exp_f32 v69, v69\n\t" \
  "v_mfma_f32_16x16x32_bf16 v[104:107], a[236:239], v[124:127], v[104:107]\n\t" \
  "v_exp_f32 v70, v70\n\t" \
  "v_mfma_f32_16x16x32_bf16 v[108:111], a[252:255], v[124:127], v[188:191]\n\t" \
  "v_exp_f32 v71, v71\n\t" \
  "v_mfma_f32_16x16x32_bf16 v[108:111], a[248:251], v[120:123], v[108:111]\n\t" \
  "v_exp_f32 v72, v72\n\t" \
  "v_mfma_f32_16x16x32_bf16 v[108:111], a[244:247], v[116:119], v[108:111]\n\t" \
  "v_exp_f32 v73, v73\n\t" \
  "v_mfma_f32_16x16x32_bf16 v[108:111], a[240:243], v[112:115], v[108:111]\n\t" \
  "s_waitcnt vmcnt(3) lgkmcnt(0)\n\t" \
  "s_barrier\n\t" \
  STA("s_memrealtime s[94:95]\n\t") \
  STA("s_memtime s[92:93]\n\t") \
  "v_mfma_f32_16x16x32_bf16 a[0:3], a[128:131], v[128:131], a[0:3]\n\t" \
  "v_exp_f32 v74, v74\n\t" \
  "v_mfma_f32_16x16x32_bf16 a[0:3], a[132:135], v[132:135], a[0:3]\n\t" \
  "v_exp_f32 v75, v75\n\t" \
  "v_mfma_f32_16x16x32_bf16 a[16:19], a[136:139], v[128:131], a[16:19]\n\t" \
  "ds_read_b128 a[192:195], v220 offset:16384\n\t" \
  "ds_read_b128 a[196:199], v221 offset:16384\n\t" \
  "v_mfma_f32_16x16x32_bf16 a[16:19], a[140:143], v[132:135], a[16:19]\n\t" \
  "v_exp_f32 v76, v76\n\t" \
  "v_mfma_f32_16x16x32_bf16 a[32:35], a[144:147], v[128:131], a[32:35]\n\t" \
  "s_lshr_b32 s98, %[sofk], 6\n\t" \
  "v_xor_b32 v234, s98, v244\n\t" \
  "v_add_u32 v213, %[m], v242\n\t" \
  "v_mfma_f32_16x16x32_bf16 a[32:35], a[148:151], v[132:135], a[32:35]\n\t" \
  "v_exp_f32 v77, v77\n\t" \
  "v_mfma_f32_16x16x32_bf16 a[48:51], a[152:155], v[128:131], a[48:51]\n\t" \
  "v_exp_f32 v78, v78\n\t" \
  "v_mfma_f32_16x16x32_bf16 a[48:51], a[156:159], v[132:135], a[48:51]\n\t" \
  "v_exp_f32 v79, v79\n\t" \
  "v_mfma_f32_16x16x32_bf16 a[64:67], a[160:163], v[128:131], a[64:67]\n\t" \
  "ds_read_b128 a[200:203], v222 offset:16384\n\t" \
  "v_add_u32 v215, 98304, v213\n\t" \
  "v_mfma_f32_16x16x32_bf16 a[64:67], a[164:167], v[132:135], a[64:67]\n\t" \
  "v_exp_f32 v96, v96\n\t" \
  "v_mfma_f32_16x16x32_bf16 a[80:83], a[168:171], v[128:131], a[80:83]\n\t" \
  "v_exp_f32 v97, v97\n\t" \
  "v_mfma_f32_16x16x32_bf16 a[80:83], a[172:175], v[132:135], a[80:83]\n\t" \
  "v_exp_f32 v98, v98\n\t" \
  "v_mfma_f32_16x16x32_bf16 a[96:99], a[176:179], v[128:131], a[96:99]\n\t" \
  STA("v_writelane_b32 %[stv], s94, 40\n\t") \
  STA("v_writelane_b32 %[stv], s92, 41\n\t") \
  "ds_read_b128 a[204:207], v223 offset:16384\n\t" \
  "v_mov_b32 v243, %[tag]\n\t" \
  "v_mfma_f32_16x16x32_bf16 a[96:99], a[180:183], v[132:135], a[96:99]\n\t" \
  "v_exp_f32 v99, v99\n\t" \
  "v_mfma_f32_16x16x32_bf16 a[112:115], a[184:187], v[128:131], a[112:115]\n\t" \
  "v_exp_f32 v100, v100\n\t" \
  "v_mfma_f32_16x16x32_bf16 a[112:115], a[188:191], v[132:135], a[112:115]\n\t" \
  "ds_read_b128 a[208:211], v220 offset:20480\n\t" \
  "v_lshrrev_b32 v254, 3, v233\n\t" \
  "v_mfma_f32_16x16x32_bf16 v[160:163], v[216:219], v[128:131], v[160:163]\n\t" \
  "ds_read_b128 a[212:215], v221 offset:20480\n\t" \
  "v_min_u32 v254, 1, v254\n\t" \
  "v_mfma_f32_16x16x32_bf16 v[160:163], v[216:219], v[132:135], v[160:163]\n\t" \
  "ds_read_b128 a[216:219], v222 offset:20480\n\t" \
  "v_lshlrev_b32 v254, 31, v254\n\t" \
  "v_mfma_f32_16x16x32_bf16 a[4:7], a[128:131], v[136:139], a[4:7]\n\t" \
  "ds_read_b128 a[220:223], v223 offset:20480\n\t" \
  "v_lshl_or_b32 v254, v233, 9, v254\n\t" \
  "v_mfma_f32_16x16x32_bf16 a[4:7], a[132:135], v[140:143], a[4:7]\n\t" \
  "s_lshr_b32 m0, %[hl18], 18\n\t" \
  "s_brev_b32 s98, 1\n\t" \
  "v_writelane_b32 v254, s98, m0\n\t" \
  "v_mfma_f32_16x16x32_bf16 a[20:23], a[136:139], v[136:139], a[20:23]\n\t" \
  "s_add_u32 m0, %[m], 49152\n\t" \
  "v_exp_f32 v101, v101\n\t" \
  "v_mfma_f32_16x16x32_bf16 a[20:23], a[140:143], v[140:143], a[20:23]\n\t" \
  "s_add_u32 s96, %[sofk], 98304\n\t" \
  "v_exp_f32 v102, v102\n\t" \
  "v_mfma_f32_16x16x32_bf16 a[36:39], a[144:147], v[136:139], a[36:39]\n\t" \
  "v_exp_f32 v103, v103\n\t" \
  "v_mfma_f32_16x16x32_bf16 a[36:39], a[148:151], v[140:143], a[36:39]\n\t" \
  "v_exp_f32 v104, v104\n\t" \
  "v_mfma_f32_16x16x32_bf16 a[52:55], a[152:155], v[136:139], a[52:55]\n\t" \
  "v_exp_f32 v105, v105\n\t" \
  "v_mfma_f32_16x16x32_bf16 a[52:55], a[156:159], v[140:143], a[52:55]\n\t" \
  "v_exp_f32 v106, v106\n\t" \
  "v_mfma_f32_16x16x32_bf16 a[68:71], a[160:163], v[136:139], a[68:71]\n\t" \
  "v_exp_f32 v107, v107\n\t" \
  "v_mfma_f32_16x16x32_bf16 a[68:71], a[164:167], v[140:143], a[68:71]\n\t" \
  "buffer_load_dwordx4 v224, %[sK], s96 offen offset:0 lds\n\t" \
  "v_mfma_f32_16x16x32_bf16 a[84:87], a[168:171], v[136:139], a[84:87]\n\t" \
  "ds_read_b128 a[224:227], v220 offset:24576\n\t" \
  "v_cvt_pk_bf16_f32 v128, v0, v1\n\t" \
  "v_mfma_f32_16x16x32_bf16 a[84:87], a[172:175], v[140:143], a[84:87]\n\t" \
  "v_exp_f32 v108, v108\n\t" \
  "v_mfma_f32_16x16x32_bf16 a[100:103], a[176:179], v[136:139], a[100:103]\n\t" \
  "v_exp_f32 v109, v109\n\t" \
  "v_mfma_f32_16x16x32_bf16 a[100:103], a[180:183], v[140:143], a[100:103]\n\t" \
  "buffer_load_dwordx4 v225, %[sK], s96 offen offset:1024 lds\n\t" \
  "v_mfma_f32_16x16x32_bf16 a[116:119], a[184:187], v[136:139], a[116:119]\n\t" \
  "v_exp_f32 v110, v110\n\t" \
  "v_mfma_f32_16x16x32_bf16 a[116:119], a[188:191], v[140:143], a[116:119]\n\t" \
  "v_exp_f32 v111, v111\n\t" \
  "v_mfma_f32_16x16x32_bf16 v[164:167], v[216:219], v[136:139], v[164:167]\n\t" \
  "buffer_load_dwordx4 v226, %[sK], s96 offen offset:2048 lds\n\t" \
  "v_mfma_f32_16x16x32_bf16 v[164:167], v[216:219], v[140:143], v[164:167]\n\t" \
  "ds_read_b128 a[228:231], v221 offset:24576\n\t" \
  "v_cvt_pk_bf16_f32 v129, v2, v3\n\t" \
  "v_mfma_f32_16x16x32_bf16 a[8:11], a[128:131], v[144:147], a[8:11]\n\t" \
  "ds_read_b128 a[232:235], v222 offset:24576\n\t" \
  "v_cvt_pk_bf16_f32 v130, v4, v5\n\t" \
  "v_mfma_f32_16x16x32_bf16 a[8:11], a[132:135], v[148:151], a[8:11]\n\t" \
  "ds_read_b128 a[236:239], v223 offset:24576\n\t" \
  "v_cvt_pk_bf16_f32 v131, v6, v7\n\t" \
  "v_mfma_f32_16x16x32_bf16 a[24:27], a[136:139], v[144:147], a[24:27]\n\t" \
  "buffer_load_dwordx4 v227, %[sK], s96 offen offset:3072 lds\n\t" \
  "v_mfma_f32_16x16x32_bf16 a[24:27], a[140:143], v[148:151], a[24:27]\n\t" \
  "ds_read_b128 a[240:243], v220 offset:28672\n\t" \
  "v_cvt_pk_bf16_f32 v132, v8, v9\n\t" \
  "v_mfma_f32_16x16x32_bf16 a[40:43], a[144:147], v[144:147], a[40:43]\n\t" \
  "ds_read_b128 a[244:247], v221 offset:28672\n\t" \
  "v_cvt_pk_bf16_f32 v133, v10, v11\n\t" \
  "v_mfma_f32_16x16x32_bf16 a[40:43], a[148:151], v[148:151], a[40:43]\n\t" \
  "s_add_u32 m0, %[m], 32768\n\t" \
  "s_mov_b32 s96, 12288\n\t" \
  "v_cvt_pk_bf16_f32 v134, v12, v13\n\t" \
  "v_mfma_f32_16x16x32_bf16 a[56:59], a[152:155], v[144:147], a[56:59]\n\t" \
  "buffer_load_dwordx4 v232, %[sG], s96 offen offset:2048 lds\n\t" \
  "v_mfma_f32_16x16x32_bf16 a[56:59], a[156:159], v[148:151], a[56:59]\n\t" \
  "s_add_u32 m0, %[m], 147456\n\t" \
  "ds_read_b128 a[248:251], v222 offset:28672\n\t" \
  "v_cvt_pk_bf16_f32 v135, v14, v15\n\t" \
  "v_mfma_f32_16x16x32_bf16 a[72:75], a[160:163], v[144:147], a[72:75]\n\t" \
  "s_add_u32 s96, %[sofk], 81920\n\t" \
  "v_cvt_pk_bf16_f32 v136, v32, v33\n\t" \
  "v_cvt_pk_bf16_f32 v137, v34, v35\n\t" \
  "v_mfma_f32_16x16x32_bf16 a[72:75], a[164:167], v[148:151], a[72:75]\n\t" \
  "v_cvt_pk_bf16_f32 v138, v36, v37\n\t" \
  "v_cvt_pk_bf16_f32 v139, v38, v39\n\t" \
  "v_mfma_f32_16x16x32_bf16 a[88:91], a[168:171], v[144:147], a[88:91]\n\t" \
  "v_cvt_pk_bf16_f32 v140, v40, v41\n\t" \
  "v_cvt_pk_bf16_f32 v141, v42, v43\n\t" \
  "v_mfma_f32_16x16x32_bf16 a[88:91], a[172:175], v[148:151], a[88:91]\n\t" \
  "ds_read_b128 a[252:255], v223 offset:28672\n\t" \
  "v_cvt_pk_bf16_f32 v142, v44, v45\n\t" \
  "v_mfma_f32_16x16x32_bf16 a[104:107], a[176:179], v[144:147], a[104:107]\n\t" \
  "v_cvt_pk_bf16_f32 v143, v46, v47\n\t" \
  "v_mfma_f32_16x16x32_bf16 a[104:107], a[180:183], v[148:151], a[104:107]\n\t" \
  "v_mfma_f32_16x16x32_bf16 a[120:123], a[184:187], v[144:147], a[120:123]\n\t" \
  "v_mfma_f32_16x16x32_bf16 a[120:123], a[188:191], v[148:151], a[120:123]\n\t" \
  "buffer_load_dwordx4 v230, %[sV], s96 offen offset:0 lds\n\t" \
  "v_mfma_f32_16x16x32_bf16 v[168:171], v[216:219], v[144:147], v[168:171]\n\t" \
  "v_mfma_f32_16x16x32_bf16 v[168:171], v[216:219], v[148:151], v[168:171]\n\t" \
  "v_cvt_pk_bf16_f32 v144, v64, v65\n\t" \
  "v_cvt_pk_bf16_f32 v145, v66, v67\n\t" \
  "v_mfma_f32_16x16x32_bf16 a[12:15], a[128:131], v[152:155], a[12:15]\n\t" \
  "v_cvt_pk_bf16_f32 v146, v68, v69\n\t" \
  "v_cvt_pk_bf16_f32 v147, v70, v71\n\t" \
  "v_mfma_f32_16x16x32_bf16 a[12:15], a[132:135], v[156:159], a[12:15]\n\t" \
  "buffer_load_dwordx4 v231, %[sV], s96 offen offset:1024 lds\n\t" \
  "v_mfma_f32_16x16x32_bf16 a[28:31], a[136:139], v[152:155], a[28:31]\n\t" \
  "v_cvt_pk_bf16_f32 v148, v72, v73\n\t" \
  "v_cvt_pk_bf16_f32 v149, v74, v75\n\t" \
  "v_mfma_f32_16x16x32_bf16 a[28:31], a[140:143], v[156:159], a[28:31]\n\t" \
  "v_cvt_pk_bf16_f32 v150, v76, v77\n\t" \
  "v_cvt_pk_bf16_f32 v151, v78, v79\n\t" \
  "v_mfma_f32_16x16x32_bf16 a[44:47], a[144:147], v[152:155], a[44:47]\n\t" \
  "buffer_load_dwordx4 v230, %[sV], s96 offen offset:2048 lds\n\t" \
  "v_mfma_f32_16x16x32_bf16 a[44:47], a[148:151], v[156:159], a[44:47]\n\t" \
  "v_mfma_f32_16x16x32_bf16 a[60:63], a[152:155], v[152:155], a[60:63]\n\t" \
  "v_mfma_f32_16x16x32_bf16 a[60:63], a[156:159], v[156:159], a[60:63]\n\t" \
  "v_mfma_f32_16x16x32_bf16 a[76:79], a[160:163], v[152:155], a[76:79]\n\t" \
  "buffer_load_dwordx4 v231, %[sV], s96 offen offset:3072 lds\n\t" \
  "v_mfma_f32_16x16x32_bf16 a[76:79], a[164:167], v[156:159], a[76:79]\n\t" \
  "s_add_u32 m0, %[m], 32768\n\t" \
  "s_mov_b32 s96, 18432\n\t" \
  "v_mfma_f32_16x16x32_bf16 a[92:95], a[168:171], v[152:155], a[92:95]\n\t" \
  "v_mfma_f32_16x16x32_bf16 a[92:95], a[172:175], v[156:159], a[92:95]\n\t" \
  "v_mfma_f32_16x16x32_bf16 a[108:111], a[176:179], v[152:155], a[108:111]\n\t" \
  "buffer_load_dwordx4 v232, %[sG], s96 offen offset:3072 lds\n\t" \
  "v_mfma_f32_16x16x32_bf16 a[108:111], a[180:183], v[156:159], a[108:111]\n\t" \
  "v_mfma_f32_16x16x32_bf16 a[124:127], a[184:187], v[152:155], a[124:127]\n\t" \
  "v_mfma_f32_16x16x32_bf16 a[124:127], a[188:191], v[156:159], a[124:127]\n\t" \
  "s_add_u32 s98, %[pf4], 43008\n\t" \
  "v_mfma_f32_16x16x32_bf16 v[172:175], v[216:219], v[152:155], v[172:175]\n\t" \
  "buffer_load_dword %[pfd], v245, %[sG], s98 offen\n\t" \
  "v_mfma_f32_16x16x32_bf16 v[172:175], v[216:219], v[156:159], v[172:175]\n\t" \
  "s_waitcnt lgkmcnt(0)\n\t" \
  "v_cvt_pk_bf16_f32 v152, v96, v97\n\t" \
  "v_cvt_pk_bf16_f32 v153, v98, v99\n\t" \
  STA("s_memrealtime s[94:95]\n\t") \
  STA("s_memtime s[92:93]\n\t") \
  "v_mfma_f32_16x16x32_bf16 v[0:3], a[192:195], v[16:19], v[176:179]\n\t" \
  "ds_read_b64_tr_b16 a[128:129], v228 offset:0\n\t" \
  "v_cvt_pk_bf16_f32 v154, v100, v101\n\t" \
  "v_mfma_f32_16x16x32_bf16 v[0:3], a[196:199], v[20:23], v[0:3]\n\t" \
  "ds_read_b64_tr_b16 a[130:131], v228 offset:4096\n\t" \
  "v_cvt_pk_bf16_f32 v155, v102, v103\n\t" \
  "v_mfma_f32_16x16x32_bf16 v[0:3], a[200:203], v[24:27], v[0:3]\n\t" \
  "ds_read_b64_tr_b16 a[132:133], v228 offset:8192\n\t" \
  "v_lshrrev_b32 v252, 3, v233\n\t" \
  "v_mfma_f32_16x16x32_bf16 v[0:3], a[204:207], v[28:31], v[0:3]\n\t" \
  "ds_read_b64_tr_b16 a[134:135], v228 offset:12288\n\t" \
  "v_cvt_pk_bf16_f32 v156, v104, v105\n\t" \
  "v_mfma_f32_16x16x32_bf16 v[4:7], a[220:223], v[28:31], v[176:179]\n\t" \
  "ds_read_b64_tr_b16 a[136:137], v229 offset:0\n\t" \
  "v_cvt_pk_bf16_f32 v157, v106, v107\n\t" \
  "v_mfma_f32_16x16x32_bf16 v[4:7], a[216:219], v[24:27], v[4:7]\n\t" \
  "ds_read_b64_tr_b16 a[138:139], v229 offset:4096\n\t" \
  "v_cvt_pk_bf16_f32 v158, v108, v109\n\t" \
  "v_mfma_f32_16x16x32_bf16 v[4:7], a[212:215], v[20:23], v[4:7]\n\t" \
  "v_exp_f32 v0, v0\n\t" \
  "v_mfma_f32_16x16x32_bf16 v[4:7], a[208:211], v[16:19], v[4:7]\n\t" \
  "v_exp_f32 v1, v1\n\t" \
  "v_mfma_f32_16x16x32_bf16 v[8:11], a[224:227], v[16:19], v[176:179]\n\t" \
  "v_exp_f32 v2, v2\n\t" \
  "v_mfma_f32_16x16x32_bf16 v[8:11], a[228:231], v[20:23], v[8:11]\n\t" \
  "v_exp_f32 v3, v3\n\t" \
  "v_mfma_f32_16x16x32_bf16 v[8:11], a[232:235], v[24:27], v[8:11]\n\t" \
  "ds_read_b64_tr_b16 a[140:141], v229 offset:8192\n\t" \
  "v_mul_u32_u24 v247, 98176, v252\n\t" \
  "v_mfma_f32_16x16x32_bf16 v[8:11], a[236:239], v[28:31], v[8:11]\n\t" \
  "s_add_u32 s98, %[pf2], 24576\n\t" \
  "v_exp_f32 v4, v4\n\t" \
  "v_mfma_f32_16x16x32_bf16 v[12:15], a[252:255], v[28:31], v[176:179]\n\t" \
  STA("v_writelane_b32 %[stv], s94, 42\n\t") \
  STA("v_writelane_b32 %[stv], s92, 43\n\t") \
  "buffer_load_dword %[pfd], v202, %[sM], s98 offen\n\t" \
  "v_mfma_f32_16x16x32_bf16 v[12:15], a[248:251], v[24:27], v[12:15]\n\t" \
  "ds_read_b64_tr_b16 a[142:143], v229 offset:12288\n\t" \
  "v_cvt_pk_bf16_f32 v159, v110, v111\n\t" \
  "v_mfma_f32_16x16x32_bf16 v[12:15], a[244:247], v[20:23], v[12:15]\n\t" \
  "ds_read_b64_tr_b16 a[144:145], v228 offset:256\n\t" \
  "v_lshl_add_u32 v247, v233, 4, v247\n\t" \
  "v_mfma_f32_16x16x32_bf16 v[12:15], a[240:243], v[16:19], v[12:15]\n\t" \
  "v_exp_f32 v5, v5\n\t" \
  "v_mfma_f32_16x16x32_bf16 v[32:35], a[192:195], v[48:51], v[180:183]\n\t" \
  "ds_read_b64_tr_b16 a[146:147], v228 offset:4352\n\t" \
  "v_lshlrev_b32 v252, 4, v252\n\t" \
  "v_mfma_f32_16x16x32_bf16 v[32:35], a[196:199], v[52:55], v[32:35]\n\t" \
  "v_exp_f32 v6, v6\n\t" \
  "v_mfma_f32_16x16x32_bf16 v[32:35], a[200:203], v[56:59], v[32:35]\n\t" \
  "v_exp_f32 v7, v7\n\t" \
  "v_mfma_f32_16x16x32_bf16 v[32:35], a[204:207], v[60:63], v[32:35]\n\t" \
  "v_exp_f32 v8, v8\n\t" \
  "v_mfma_f32_16x16x32_bf16 v[36:39], a[220:223], v[60:63], v[180:183]\n\t" \
  "ds_read_b64_tr_b16 a[148:149], v228 offset:8448\n\t" \
  "ds_read_b64_tr_b16 a[150:151], v228 offset:12544\n\t" \
  "v_mfma_f32_16x16x32_bf16 v[36:39], a[216:219], v[56:59], v[36:39]\n\t" \
  "v_exp_f32 v9, v9\n\t" \
  "v_mfma_f32_16x16x32_bf16 v[36:39], a[212:215], v[52:55], v[36:39]\n\t" \
  "v_exp_f32 v10, v10\n\t" \
  "v_mfma_f32_16x16x32_bf16 v[36:39], a[208:211], v[48:51], v[36:39]\n\t" \
  "ds_read_b64_tr_b16 a[152:153], v229 offset:256\n\t" \
  "ds_read_b64_tr_b16 a[154:155], v229 offset:4352\n\t" \
  "v_mfma_f32_16x16x32_bf16 v[40:43], a[224:227], v[48:51], v[180:183]\n\t" \
  "v_exp_f32 v11, v11\n\t" \
  "v_mfma_f32_16x16x32_bf16 v[40:43], a[228:231], v[52:55], v[40:43]\n\t" \
  "v_exp_f32 v12, v12\n\t" \
  "v_mfma_f32_16x16x32_bf16 v[40:43], a[232:235], v[56:59], v[40:43]\n\t" \
  "v_exp_f32 v13, v13\n\t" \
  "v_mfma_f32_16x16x32_bf16 v[40:43], a[236:239], v[60:63], v[40:43]\n\t" \
  "ds_read_b64_tr_b16 a[156:157], v229 offset:8448\n\t" \
  "ds_read_b64_tr_b16 a[158:159], v229 offset:12544\n\t" \
  "v_mfma_f32_16x16x32_bf16 v[44:47], a[252:255], v[60:63], v[180:183]\n\t" \
  "v_exp_f32 v14, v14\n\t" \
  "v_mfma_f32_16x16x32_bf16 v[44:47], a[248:251], v[56:59], v[44:47]\n\t" \
  "v_exp_f32 v15, v15\n\t" \
  "v_mfma_f32_16x16x32_bf16 v[44:47], a[244:247], v[52:55], v[44:47]\n\t" \
  "ds_read_b64_tr_b16 a[160:161], v228 offset:512\n\t" \
  "ds_read_b64_tr_b16 a[162:163], v228 offset:4608\n\t" \
  "v_mfma_f32_16x16x32_bf16 v[44:47], a[240:243], v[48:51], v[44:47]\n\t" \
  "v_exp_f32 v32, v32\n\t" \
  "v_mfma_f32_16x16x32_bf16 v[64:67], a[192:195], v[80:83], v[184:187]\n\t" \
  "v_exp_f32 v33, v33\n\t" \
  "v_mfma_f32_16x16x32_bf16 v[64:67], a[196:199], v[84:87], v[64:67]\n\t" \
  "v_exp_f32 v34, v34\n\t" \
  "v_mfma_f32_16x16x32_bf16 v[64:67], a[200:203], v[88:91], v[64:67]\n\t" \
  "ds_read_b64_tr_b16 a[164:165], v228 offset:8704\n\t" \
  "ds_read_b64_tr_b16 a[166:167], v228 offset:12800\n\t" \
  "v_mfma_f32_16x16x32_bf16 v[64:67], a[204:207], v[92:95], v[64:67]\n\t" \
  "v_exp_f32 v35, v35\n\t" \
  "v_mfma_f32_16x16x32_bf16 v[68:71], a[220:223], v[92:95], v[184:187]\n\t" \
  "v_exp_f32 v36, v36\n\t" \
  "v_mfma_f32_16x16x32_bf16 v[68:71], a[216:219], v[88:91], v[68:71]\n\t" \
  "ds_read_b64_tr_b16 a[168:169], v229 offset:512\n\t" \
  "ds_read_b64_tr_b16 a[170:171], v229 offset:4608\n\t" \
  "v_mfma_f32_16x16x32_bf16 v[68:71], a[212:215], v[84:87], v[68:71]\n\t" \
  "v_exp_f32 v37, v37\n\t" \
  "v_mfma_f32_16x16x32_bf16 v[68:71], a[208:211], v[80:83], v[68:71]\n\t" \
  "v_exp_f32 v38, v38\n\t" \
  "v_mfma_f32_16x16x32_bf16 v[72:75], a[224:227], v[80:83], v[184:187]\n\t" \
  "v_exp_f32 v39, v39\n\t" \
  "v_mfma_f32_16x16x32_bf16 v[72:75], a[228:231], v[84:87], v[72:75]\n\t" \
  "ds_read_b64_tr_b16 a[172:173], v229 offset:8704\n\t" \
  "ds_read_b64_tr_b16 a[174:175], v229 offset:12800\n\t" \
  "v_mfma_f32_16x16x32_bf16 v[72:75], a[232:235], v[88:91], v[72:75]\n\t" \
  "v_exp_f32 v40, v40\n\t" \
  "v_mfma_f32_16x16x32_bf16 v[72:75], a[236:239], v[92:95], v[72:75]\n\t" \
  "v_exp_f32 v41, v41\n\t" \
  "v_mfma_f32_16x16x32_bf16 v[76:79], a[252:255], v[92:95], v[184:187]\n\t" \
  "ds_read_b64_tr_b16 a[176:177], v228 offset:768\n\t" \
  "ds_read_b64_tr_b16 a[178:179], v228 offset:4864\n\t" \
  "v_mfma_f32_16x16x32_bf16 v[76:79], a[248:251], v[88:91], v[76:79]\n\t" \
  "v_exp_f32 v42, v42\n\t" \
  "v_mfma_f32_16x16x32_bf16 v[76:79], a[244:247], v[84:87], v[76:79]\n\t" \
  "v_exp_f32 v43, v43\n\t" \
  "v_mfma_f32_16x16x32_bf16 v[76:79], a[240:243], v[80:83], v[76:79]\n\t" \
  "v_exp_f32 v44, v44\n\t" \
  "v_mfma_f32_16x16x32_bf16 v[96:99], a[192:195], v[112:115], v[188:191]\n\t" \
  "ds_read_b64_tr_b16 a[180:181], v228 offset:8960\n\t" \
  "ds_read_b64_tr_b16 a[182:183], v228 offset:13056\n\t" \
  "v_mfma_f32_16x16x32_bf16 v[96:99], a[196:199], v[116:119], v[96:99]\n\t" \
  "v_exp_f32 v45, v45\n\t" \
  "v_mfma_f32_16x16x32_bf16 v[96:99], a[200:203], v[120:123], v[96:99]\n\t" \
  "v_exp_f32 v46, v46\n\t" \
  "v_mfma_f32_16x16x32_bf16 v[96:99], a[204:207], v[124:127], v[96:99]\n\t" \
  "ds_read_b64_tr_b16 a[184:185], v229 offset:768\n\t" \
  "ds_read_b64_tr_b16 a[186:187], v229 offset:4864\n\t" \
  "v_mfma_f32_16x16x32_bf16 v[100:103], a[220:223], v[124:127], v[188:191]\n\t" \
  "v_exp_f32 v47, v47\n\t" \
  "v_mfma_f32_16x16x32_bf16 v[100:103], a[216:219], v[120:123], v[100:103]\n\t" \
  "v_exp_f32 v64, v64\n\t" \
  "v_mfma_f32_16x16x32_bf16 v[100:103], a[212:215], v[116:119], v[100:103]\n\t" \
  "v_exp_f32 v65, v65\n\t" \
  "v_mfma_f32_16x16x32_bf16 v[100:103], a[208:211], v[112:115], v[100:103]\n\t" \
  "ds_read_b64_tr_b16 a[188:189], v229 offset:8960\n\t" \
  "ds_read_b64_tr_b16 a[190:191], v229 offset:13056\n\t" \
  "v_mfma_f32_16x16x32_bf16 v[104:107], a[224:227], v[112:115], v[188:191]\n\t" \
  "v_exp_f32 v66, v66\n\t" \
  "v_mfma_f32_16x16x32_bf16 v[104:107], a[228:231], v[116:119], v[104:107]\n\t" \
  "v_exp_f32 v67, v67\n\t" \
  "v_mfma_f32_16x16x32_bf16 v[104:107], a[232:235], v[120:123], v[104:107]\n\t" \
  "v_exp_f32 v68, v68\n\t" \
  "v_mfma_f32_16x16x32_bf16 v[104:107], a[236:239], v[124:127], v[104:107]\n\t" \
  "v_exp_f32 v69, v69\n\t" \
  "v_mfma_f32_16x16x32_bf16 v[108:111], a[252:255], v[124:127], v[188:191]\n\t" \
  "v_exp_f32 v70, v70\n\t" \
  "v_mfma_f32_16x16x32_bf16 v[108:111], a[248:251], v[120:123], v[108:111]\n\t" \
  "v_exp_f32 v71, v71\n\t" \
  "v_mfma_f32_16x16x32_bf16 v[108:111], a[244:247], v[116:119], v[108:111]\n\t" \
  "v_exp_f32 v72, v72\n\t" \
  "v_mfma_f32_16x16x32_bf16 v[108:111], a[240:243], v[112:115], v[108:111]\n\t" \
  "s_waitcnt vmcnt(3) lgkmcnt(0)\n\t" \
  "s_barrier\n\t" \
  STA("s_memrealtime s[94:95]\n\t") \
  STA("s_memtime s[92:93]\n\t") \
  "v_mfma_f32_16x16x32_bf16 a[0:3], a[128:131], v[128:131], a[0:3]\n\t" \
  "v_exp_f32 v73, v73\n\t" \
  "v_mfma_f32_16x16x32_bf16 a[0:3], a[132:135], v[132:135], a[0:3]\n\t" \
  "v_exp_f32 v74, v74\n\t" \
  "v_mfma_f32_16x16x32_bf16 a[16:19], a[136:139], v[128:131], a[16:19]\n\t" \
  "ds_read_b128 a[192:195], v220 offset:0\n\t" \
  "ds_read_b128 a[196:199], v221 offset:0\n\t" \
  "v_mfma_f32_16x16x32_bf16 a[16:19], a[140:143], v[132:135], a[16:19]\n\t" \
  "v_exp_f32 v75, v75\n\t" \
  "v_mfma_f32_16x16x32_bf16 a[32:35], a[144:147], v[128:131], a[32:35]\n\t" \
  "v_exp_f32 v76, v76\n\t" \
  "v_mfma_f32_16x16x32_bf16 a[32:35], a[148:151], v[132:135], a[32:35]\n\t" \
  "v_exp_f32 v77, v77\n\t" \
  "v_mfma_f32_16x16x32_bf16 a[48:51], a[152:155], v[128:131], a[48:51]\n\t" \
  "v_exp_f32 v78, v78\n\t" \
  "v_mfma_f32_16x16x32_bf16 a[48:51], a[156:159], v[132:135], a[48:51]\n\t" \
  "v_exp_f32 v79, v79\n\t" \
  "v_mfma_f32_16x16x32_bf16 a[64:67], a[160:163], v[128:131], a[64:67]\n\t" \
  "v_exp_f32 v96, v96\n\t" \
  "v_mfma_f32_16x16x32_bf16 a[64:67], a[164:167], v[132:135], a[64:67]\n\t" \
  "v_exp_f32 v97, v97\n\t" \
  "v_mfma_f32_16x16x32_bf16 a[80:83], a[168:171], v[128:131], a[80:83]\n\t" \
  "ds_read_b128 a[200:203], v222 offset:0\n\t" \
  "ds_read_b128 a[204:207], v223 offset:0\n\t" \
  "v_mfma_f32_16x16x32_bf16 a[80:83], a[172:175], v[132:135], a[80:83]\n\t" \
  "v_exp_f32 v98, v98\n\t" \
  "v_mfma_f32_16x16x32_bf16 a[96:99], a[176:179], v[128:131], a[96:99]\n\t" \
  STA("v_writelane_b32 %[stv], s94, 44\n\t") \
  STA("v_writelane_b32 %[stv], s92, 45\n\t") \
  "v_exp_f32 v99, v99\n\t" \
  "v_mfma_f32_16x16x32_bf16 a[96:99], a[180:183], v[132:135], a[96:99]\n\t" \
  "v_exp_f32 v100, v100\n\t" \
  "v_mfma_f32_16x16x32_bf16 a[112:115], a[184:187], v[128:131], a[112:115]\n\t" \
  "v_exp_f32 v101, v101\n\t" \
  "v_mfma_f32_16x16x32_bf16 a[112:115], a[188:191], v[132:135], a[112:115]\n\t" \
  "v_exp_f32 v102, v102\n\t" \
  "v_mfma_f32_16x16x32_bf16 v[160:163], v[216:219], v[128:131], v[160:163]\n\t" \
  "v_exp_f32 v103, v103\n\t" \
  "v_mfma_f32_16x16x32_bf16 v[160:163], v[216:219], v[132:135], v[160:163]\n\t" \
  "ds_read_b128 a[208:211], v220 offset:4096\n\t" \
  "v_cvt_pk_bf16_f32 v128, v0, v1\n\t" \
  "v_mfma_f32_16x16x32_bf16 a[4:7], a[128:131], v[136:139], a[4:7]\n\t" \
  ".Lhs8:\n\ts_cbranch_execz .Lhs8n\n\t" \
  "v_exp_f32 v104, v104\n\t" \
  "v_mfma_f32_16x16x32_bf16 a[4:7], a[132:135], v[140:143], a[4:7]\n\t" \
  "ds_read_b128 a[212:215], v221 offset:4096\n\t" \
  "v_cvt_pk_bf16_f32 v129, v2, v3\n\t" \
  "v_mfma_f32_16x16x32_bf16 a[20:23], a[136:139], v[136:139], a[20:23]\n\t" \
  "s_add_u32 m0, %[m], 65536\n\t" \
  "v_exp_f32 v105, v105\n\t" \
  "v_mfma_f32_16x16x32_bf16 a[20:23], a[140:143], v[140:143], a[20:23]\n\t" \
  "s_add_u32 s96, %[sofk], 114688\n\t" \
  "v_exp_f32 v106, v106\n\t" \
  "v_mfma_f32_16x16x32_bf16 a[36:39], a[144:147], v[136:139], a[36:39]\n\t" \
  "v_exp_f32 v107, v107\n\t" \
  "v_mfma_f32_16x16x32_bf16 a[36:39], a[148:151], v[140:143], a[36:39]\n\t" \
  "ds_read_b128 a[216:219], v222 offset:4096\n\t" \
  "v_cvt_pk_bf16_f32 v130, v4, v5\n\t" \
  "v_mfma_f32_16x16x32_bf16 a[52:55], a[152:155], v[136:139], a[52:55]\n\t" \
  "v_exp_f32 v108, v108\n\t" \
  "v_mfma_f32_16x16x32_bf16 a[52:55], a[156:159], v[140:143], a[52:55]\n\t" \
  "v_exp_f32 v109, v109\n\t" \
  "v_mfma_f32_16x16x32_bf16 a[68:71], a[160:163], v[136:139], a[68:71]\n\t" \
  "v_exp_f32 v110, v110\n\t" \
  "v_mfma_f32_16x16x32_bf16 a[68:71], a[164:167], v[140:143], a[68:71]\n\t" \
  "buffer_load_dwordx4 v224, %[sK], s96 offen offset:0 lds\n\t" \
  "v_mfma_f32_16x16x32_bf16 a[84:87], a[168:171], v[136:139], a[84:87]\n\t" \
  "ds_read_b128 a[220:223], v223 offset:4096\n\t" \
  "v_cvt_pk_bf16_f32 v131, v6, v7\n\t" \
  "v_mfma_f32_16x16x32_bf16 a[84:87], a[172:175], v[140:143], a[84:87]\n\t" \
  "v_exp_f32 v111, v111\n\t" \
  "v_mfma_f32_16x16x32_bf16 a[100:103], a[176:179], v[136:139], a[100:103]\n\t" \
  "ds_read_b128 a[224:227], v220 offset:8192\n\t" \
  "v_cvt_pk_bf16_f32 v132, v8, v9\n\t" \
  "v_mfma_f32_16x16x32_bf16 a[100:103], a[180:183], v[140:143], a[100:103]\n\t" \
  "buffer_load_dwordx4 v225, %[sK], s96 offen offset:1024 lds\n\t" \
  "v_mfma_f32_16x16x32_bf16 a[116:119], a[184:187], v[136:139], a[116:119]\n\t" \
  "ds_read_b128 a[228:231], v221 offset:8192\n\t" \
  "v_cvt_pk_bf16_f32 v133, v10, v11\n\t" \
  "v_mfma_f32_16x16x32_bf16 a[116:119], a[188:191], v[140:143], a[116:119]\n\t" \
  "ds_read_b128 a[232:235], v222 offset:8192\n\t" \
  "v_cvt_pk_bf16_f32 v134, v12, v13\n\t" \
  "v_mfma_f32_16x16x32_bf16 v[164:167], v[216:219], v[136:139], v[164:167]\n\t" \
  "buffer_load_dwordx4 v226, %[sK], s96 offen offset:2048 lds\n\t" \
  "v_mfma_f32_16x16x32_bf16 v[164:167], v[216:219], v[140:143], v[164:167]\n\t" \
  "ds_read_b128 a[236:239], v223 offset:8192\n\t" \
  "v_cvt_pk_bf16_f32 v135, v14, v15\n\t" \
  "v_mfma_f32_16x16x32_bf16 a[8:11], a[128:131], v[144:147], a[8:11]\n\t" \
  "ds_read_b128 a[240:243], v220 offset:12288\n\t" \
  "v_cvt_pk_bf16_f32 v136, v32, v33\n\t" \
  "v_mfma_f32_16x16x32_bf16 a[8:11], a[132:135], v[148:151], a[8:11]\n\t" \
  "v_cvt_pk_bf16_f32 v137, v34, v35\n\t" \
  "v_cvt_pk_bf16_f32 v138, v36, v37\n\t" \
  "v_mfma_f32_16x16x32_bf16 a[24:27], a[136:139], v[144:147], a[24:27]\n\t" \
  "buffer_load_dwordx4 v227, %[sK], s96 offen offset:3072 lds\n\t" \
  "v_mfma_f32_16x16x32_bf16 a[24:27], a[140:143], v[148:151], a[24:27]\n\t" \
  "ds_read_b128 a[244:247], v221 offset:12288\n\t" \
  "v_cvt_pk_bf16_f32 v139, v38, v39\n\t" \
  "v_mfma_f32_16x16x32_bf16 a[40:43], a[144:147], v[144:147], a[40:43]\n\t" \
  "s_add_u32 m0, %[m], 98304\n\t" \
  "s_mov_b32 s96, 28672\n\t" \
  "v_cvt_pk_bf16_f32 v140, v40, v41\n\t" \
  "v_mfma_f32_16x16x32_bf16 a[40:43], a[148:151], v[148:151], a[40:43]\n\t" \
  "v_cvt_pk_bf16_f32 v141, v42, v43\n\t" \
  "v_cvt_pk_bf16_f32 v142, v44, v45\n\t" \
  "v_mfma_f32_16x16x32_bf16 a[56:59], a[152:155], v[144:147], a[56:59]\n\t" \
  "buffer_load_dwordx4 v232, %[sG], s96 offen offset:0 lds\n\t" \
  "v_mfma_f32_16x16x32_bf16 a[56:59], a[156:159], v[148:151], a[56:59]\n\t" \
  "s_add_u32 m0, %[m], 81920\n\t" \
  "ds_read_b128 a[248:251], v222 offset:12288\n\t" \
  "v_cvt_pk_bf16_f32 v143, v46, v47\n\t" \
  "v_mfma_f32_16x16x32_bf16 a[72:75], a[160:163], v[144:147], a[72:75]\n\t" \
  "s_add_u32 s96, %[sofk], 98304\n\t" \
  "v_mfma_f32_16x16x32_bf16 a[72:75], a[164:167], v[148:151], a[72:75]\n\t" \
  "v_mfma_f32_16x16x32_bf16 a[88:91], a[168:171], v[144:147], a[88:91]\n\t" \
  "v_mfma_f32_16x16x32_bf16 a[88:91], a[172:175], v[148:151], a[88:91]\n\t" \
  "v_mfma_f32_16x16x32_bf16 a[104:107], a[176:179], v[144:147], a[104:107]\n\t" \
  "v_mfma_f32_16x16x32_bf16 a[104:107], a[180:183], v[148:151], a[104:107]\n\t" \
  "v_mfma_f32_16x16x32_bf16 a[120:123], a[184:187], v[144:147], a[120:123]\n\t" \
  "v_mfma_f32_16x16x32_bf16 a[120:123], a[188:191], v[148:151], a[120:123]\n\t" \
  "buffer_load_dwordx4 v230, %[sV], s96 offen offset:0 lds\n\t" \
  "v_mfma_f32_16x16x32_bf16 v[168:171], v[216:219], v[144:147], v[168:171]\n\t" \
  "v_mfma_f32_16x16x32_bf16 v[168:171], v[216:219], v[148:151], v[168:171]\n\t" \
  "ds_read_b128 a[252:255], v223 offset:12288\n\t" \
  "v_cvt_pk_bf16_f32 v144, v64, v65\n\t" \
  "v_mfma_f32_16x16x32_bf16 a[12:15], a[128:131], v[152:155], a[12:15]\n\t" \
  "v_cvt_pk_bf16_f32 v145, v66, v67\n\t" \
  "v_cvt_pk_bf16_f32 v146, v68, v69\n\t" \
  "v_mfma_f32_16x16x32_bf16 a[12:15], a[132:135], v[156:159], a[12:15]\n\t" \
  "buffer_load_dwordx4 v231, %[sV], s96 offen offset:1024 lds\n\t" \
  "v_mfma_f32_16x16x32_bf16 a[28:31], a[136:139], v[152:155], a[28:31]\n\t" \
  "v_cvt_pk_bf16_f32 v147, v70, v71\n\t" \
  "v_cvt_pk_bf16_f32 v148, v72, v73\n\t" \
  "v_mfma_f32_16x16x32_bf16 a[28:31], a[140:143], v[156:159], a[28:31]\n\t" \
  "v_cvt_pk_bf16_f32 v149, v74, v75\n\t" \
  "v_cvt_pk_bf16_f32 v150, v76, v77\n\t" \
  "v_mfma_f32_16x16x32_bf16 a[44:47], a[144:147], v[152:155], a[44:47]\n\t" \
  "buffer_load_dwordx4 v230, %[sV], s96 offen offset:2048 lds\n\t" \
  "v_mfma_f32_16x16x32_bf16 a[44:47], a[148:151], v[156:159], a[44:47]\n\t" \
  "v_cvt_pk_bf16_f32 v151, v78, v79\n\t" \
  "v_mfma_f32_16x16x32_bf16 a[60:63], a[152:155], v[152:155], a[60:63]\n\t" \
  "v_mfma_f32_16x16x32_bf16 a[60:63], a[156:159], v[156:159], a[60:63]\n\t" \
  "v_mfma_f32_16x16x32_bf16 a[76:79], a[160:163], v[152:155], a[76:79]\n\t" \
  "buffer_load_dwordx4 v231, %[sV], s96 offen offset:3072 lds\n\t" \
  "v_mfma_f32_16x16x32_bf16 a[76:79], a[164:167], v[156:159], a[76:79]\n\t" \
  "v_lshrrev_b32 v205, 1, v233\n\t" \
  "v_lshlrev_b32 v205, 2, v205\n\t" \
  "v_mfma_f32_16x16x32_bf16 a[92:95], a[168:171], v[152:155], a[92:95]\n\t" \
  "v_add_u32 v205, 8192, v205\n\t" \
  "v_mfma_f32_16x16x32_bf16 a[92:95], a[172:175], v[156:159], a[92:95]\n\t" \
  "buffer_load_dword v209, v205, %[ws], 0 offen sc1\n\t" \
  "v_mfma_f32_16x16x32_bf16 a[108:111], a[176:179], v[152:155], a[108:111]\n\t" \
  "s_add_u32 m0, %[m], 98304\n\t" \
  "s_mov_b32 s96, 34816\n\t" \
  "v_mfma_f32_16x16x32_bf16 a[108:111], a[180:183], v[156:159], a[108:111]\n\t" \
  "buffer_load_dwordx4 v232, %[sG], s96 offen offset:1024 lds\n\t" \
  "v_mfma_f32_16x16x32_bf16 a[124:127], a[184:187], v[152:155], a[124:127]\n\t" \
  "v_mfma_f32_16x16x32_bf16 a[124:127], a[188:191], v[156:159], a[124:127]\n\t" \
  "s_add_u32 s98, %[pf2], 49152\n\t" \
  "v_mfma_f32_16x16x32_bf16 v[172:175], v[216:219], v[152:155], v[172:175]\n\t" \
  "buffer_load_dword %[pfd], v202, %[sM], s98 offen\n\t" \
  "v_mfma_f32_16x16x32_bf16 v[172:175], v[216:219], v[156:159], v[172:175]\n\t" \
  "s_waitcnt lgkmcnt(0)\n\t" \
  "v_cvt_pk_bf16_f32 v152, v96, v97\n\t" \
  "v_cvt_pk_bf16_f32 v153, v98, v99\n\t" \
  STA("s_memrealtime s[94:95]\n\t") \
  STA("s_memtime s[92:93]\n\t") \
  "v_mfma_f32_16x16x32_bf16 v[0:3], a[192:195], v[16:19], v[176:179]\n\t" \
  "ds_read_b64_tr_b16 a[128:129], v237 offset:0\n\t" \
  "v_cvt_pk_bf16_f32 v154, v100, v101\n\t" \
  "v_mfma_f32_16x16x32_bf16 v[0:3], a[196:199], v[20:23], v[0:3]\n\t" \
  "ds_read_b64_tr_b16 a[130:131], v237 offset:4096\n\t" \
  "v_cvt_pk_bf16_f32 v155, v102, v103\n\t" \
  "v_mfma_f32_16x16x32_bf16 v[0:3], a[200:203], v[24:27], v[0:3]\n\t" \
  "ds_read_b64_tr_b16 a[132:133], v237 offset:8192\n\t" \
  "v_cvt_pk_bf16_f32 v156, v104, v105\n\t" \
  "v_mfma_f32_16x16x32_bf16 v[0:3], a[204:207], v[28:31], v[0:3]\n\t" \
  "ds_read_b64_tr_b16 a[134:135], v237 offset:12288\n\t" \
  "v_cvt_pk_bf16_f32 v157, v106, v107\n\t" \
  "v_mfma_f32_16x16x32_bf16 v[4:7], a[220:223], v[28:31], v[176:179]\n\t" \
  "ds_read_b64_tr_b16 a[136:137], v238 offset:0\n\t" \
  "v_cvt_pk_bf16_f32 v158, v108, v109\n\t" \
  "v_mfma_f32_16x16x32_bf16 v[4:7], a[216:219], v[24:27], v[4:7]\n\t" \
  "ds_read_b64_tr_b16 a[138:139], v238 offset:4096\n\t" \
  "v_cvt_pk_bf16_f32 v159, v110, v111\n\t" \
  "v_mfma_f32_16x16x32_bf16 v[4:7], a[212:215], v[20:23], v[4:7]\n\t" \
  "v_exp_f32 v0, v0\n\t" \
  "v_mfma_f32_16x16x32_bf16 v[4:7], a[208:211], v[16:19], v[4:7]\n\t" \
  "v_exp_f32 v1, v1\n\t" \
  "v_mfma_f32_16x16x32_bf16 v[8:11], a[224:227], v[16:19], v[176:179]\n\t" \
  "v_exp_f32 v2, v2\n\t" \
  "v_mfma_f32_16x16x32_bf16 v[8:11], a[228:231], v[20:23], v[8:11]\n\t" \
  "v_exp_f32 v3, v3\n\t" \
  "v_mfma_f32_16x16x32_bf16 v[8:11], a[232:235], v[24:27], v[8:11]\n\t" \
  "ds_read_b64_tr_b16 a[140:141], v238 offset:8192\n\t" \
  "ds_read_b64_tr_b16 a[142:143], v238 offset:12288\n\t" \
  "v_mfma_f32_16x16x32_bf16 v[8:11], a[236:239], v[28:31], v[8:11]\n\t" \
  "s_add_u32 s98, %[pf2], 73728\n\t" \
  "v_exp_f32 v4, v4\n\t" \
  "v_mfma_f32_16x16x32_bf16 v[12:15], a[252:255], v[28:31], v[176:179]\n\t" \
  STA("v_writelane_b32 %[stv], s94, 46\n\t") \
  STA("v_writelane_b32 %[stv], s92, 47\n\t") \
  "buffer_load_dword %[pfd], v202, %[sM], s98 offen\n\t" \
  "v_mfma_f32_16x16x32_bf16 v[12:15], a[248:251], v[24:27], v[12:15]\n\t" \
  "ds_read_b64_tr_b16 a[144:145], v237 offset:256\n\t" \
  "ds_read_b64_tr_b16 a[146:147], v237 offset:4352\n\t" \
  "v_lshl_add_u32 v207, v233, 14, %[cto]\n\t" \
  "v_min_u32 v207, .Lfz_z-.Lat_t1-4, v207\n\t" \
  "v_mfma_f32_16x16x32_bf16 v[12:15], a[244:247], v[20:23], v[12:15]\n\t" \
  "s_getpc_b64 s[96:97]\n\t" \
  ".Lat_t1:\n\t" \
  "global_load_dword %[pfd], v207, s[96:97]\n\t" \
  "v_mfma_f32_16x16x32_bf16 v[12:15], a[240:243], v[16:19], v[12:15]\n\t" \
  "v_exp_f32 v5, v5\n\t" \
  "v_mfma_f32_16x16x32_bf16 v[32:35], a[192:195], v[48:51], v[180:183]\n\t" \
  "v_exp_f32 v6, v6\n\t" \
  "v_mfma_f32_16x16x32_bf16 v[32:35], a[196:199], v[52:55], v[32:35]\n\t" \
  "v_exp_f32 v7, v7\n\t" \
  "v_mfma_f32_16x16x32_bf16 v[32:35], a[200:203], v[56:59], v[32:35]\n\t" \
  "v_exp_f32 v8, v8\n\t" \
  "v_mfma_f32_16x16x32_bf16 v[32:35], a[204:207], v[60:63], v[32:35]\n\t" \
  "v_exp_f32 v9, v9\n\t" \
  "v_mfma_f32_16x16x32_bf16 v[36:39], a[220:223], v[60:63], v[180:183]\n\t" \
  "ds_read_b64_tr_b16 a[148:149], v237 offset:8448\n\t" \
  "ds_read_b64_tr_b16 a[150:151], v237 offset:12544\n\t" \
  "v_mfma_f32_16x16x32_bf16 v[36:39], a[216:219], v[56:59], v[36:39]\n\t" \
  "v_exp_f32 v10, v10\n\t" \
  "v_mfma_f32_16x16x32_bf16 v[36:39], a[212:215], v[52:55], v[36:39]\n\t" \
  "v_exp_f32 v11, v11\n\t" \
  "v_mfma_f32_16x16x32_bf16 v[36:39], a[208:211], v[48:51], v[36:39]\n\t" \
  "ds_read_b64_tr_b16 a[152:153], v238 offset:256\n\t" \
  "ds_read_b64_tr_b16 a[154:155], v238 offset:4352\n\t" \
  "v_mfma_f32_16x16x32_bf16 v[40:43], a[224:227], v[48:51], v[180:183]\n\t" \
  "v_exp_f32 v12, v12\n\t" \
  "v_mfma_f32_16x16x32_bf16 v[40:43], a[228:231], v[52:55], v[40:43]\n\t" \
  "v_exp_f32 v13, v13\n\t" \
  "v_mfma_f32_16x16x32_bf16 v[40:43], a[232:235], v[56:59], v[40:43]\n\t" \
  "v_exp_f32 v14, v14\n\t" \
  "v_mfma_f32_16x16x32_bf16 v[40:43], a[236:239], v[60:63], v[40:43]\n\t" \
  "ds_read_b64_tr_b16 a[156:157], v238 offset:8448\n\t" \
  "ds_read_b64_tr_b16 a[158:159], v238 offset:12544\n\t" \
  "v_mfma_f32_16x16x32_bf16 v[44:47], a[252:255], v[60:63], v[180:183]\n\t" \
  "v_exp_f32 v15, v15\n\t" \
  "v_mfma_f32_16x16x32_bf16 v[44:47], a[248:251], v[56:59], v[44:47]\n\t" \
  "v_exp_f32 v32, v32\n\t" \
  "v_mfma_f32_16x16x32_bf16 v[44:47], a[244:247], v[52:55], v[44:47]\n\t" \
  "ds_read_b64_tr_b16 a[160:161], v237 offset:512\n\t" \
  "ds_read_b64_tr_b16 a[162:163], v237 offset:4608\n\t" \
  "v_mfma_f32_16x16x32_bf16 v[44:47], a[240:243], v[48:51], v[44:47]\n\t" \
  "v_exp_f32 v33, v33\n\t" \
  "v_mfma_f32_16x16x32_bf16 v[64:67], a[192:195], v[80:83], v[184:187]\n\t" \
  "v_exp_f32 v34, v34\n\t" \
  "v_mfma_f32_16x16x32_bf16 v[64:67], a[196:199], v[84:87], v[64:67]\n\t" \
  "v_exp_f32 v35, v35\n\t" \
  "v_mfma_f32_16x16x32_bf16 v[64:67], a[200:203], v[88:91], v[64:67]\n\t" \
  "ds_read_b64_tr_b16 a[164:165], v237 offset:8704\n\t" \
  "ds_read_b64_tr_b16 a[166:167], v237 offset:12800\n\t" \
  "v_mfma_f32_16x16x32_bf16 v[64:67], a[204:207], v[92:95], v[64:67]\n\t" \
  "v_exp_f32 v36, v36\n\t" \
  "v_mfma_f32_16x16x32_bf16 v[68:71], a[220:223], v[92:95], v[184:187]\n\t" \
  "v_exp_f32 v37, v37\n\t" \
  "v_mfma_f32_16x16x32_bf16 v[68:71], a[216:219], v[88:91], v[68:71]\n\t" \
  "ds_read_b64_tr_b16 a[168:169], v238 offset:512\n\t" \
  "ds_read_b64_tr_b16 a[170:171], v238 offset:4608\n\t" \
  "v_mfma_f32_16x16x32_bf16 v[68:71], a[212:215], v[84:87], v[68:71]\n\t" \
  "v_exp_f32 v38, v38\n\t" \
  "v_mfma_f32_16x16x32_bf16 v[68:71], a[208:211], v[80:83], v[68:71]\n\t" \
  "v_exp_f32 v39, v39\n\t" \
  "v_mfma_f32_16x16x32_bf16 v[72:75], a[224:227], v[80:83], v[184:187]\n\t" \
  "v_exp_f32 v40, v40\n\t" \
  "v_mfma_f32_16x16x32_bf16 v[72:75], a[228:231], v[84:87], v[72:75]\n\t" \
  "ds_read_b64_tr_b16 a[172:173], v238 offset:8704\n\t" \
  "ds_read_b64_tr_b16 a[174:175], v238 offset:12800\n\t" \
  "v_mfma_f32_16x16x32_bf16 v[72:75], a[232:235], v[88:91], v[72:75]\n\t" \
  "ds_read_b64_tr_b16 a[176:177], v237 offset:768\n\t" \
  "ds_read_b64_tr_b16 a[178:179], v237 offset:4864\n\t" \
  "v_mfma_f32_16x16x32_bf16 v[72:75], a[236:239], v[92:95], v[72:75]\n\t" \
  "s_lshr_b32 s98, %[hl18], 18\n\t" \
  "v_xor_b32 v245, s98, v233\n\t" \
  "v_and_b32 v245, 31, v245\n\t" \
  "v_mfma_f32_16x16x32_bf16 v[76:79], a[252:255], v[92:95], v[184:187]\n\t" \
  "s_waitcnt vmcnt(4)\n\t" \
  "v_and_b32 v206, 0xbfffbfff, v209\n\t" \
  "v_cmp_ne_u32_e64 s[96:97], %[tag], v206\n\t" \
  "s_cmp_lg_u64 s[96:97], 0\n\t" \
  "s_cbranch_scc1 .Lat_v\n\t" \
  ".Lat_w:\n\t" \
  "v_mfma_f32_16x16x32_bf16 v[76:79], a[248:251], v[88:91], v[76:79]\n\t" \
  "v_exp_f32 v41, v41\n\t" \
  "v_mfma_f32_16x16x32_bf16 v[76:79], a[244:247], v[84:87], v[76:79]\n\t" \
  "ds_read_b64_tr_b16 a[180:181], v237 offset:8960\n\t" \
  "v_and_b32 v206, 1, v233\n\t" \
  "v_mfma_f32_16x16x32_bf16 v[76:79], a[240:243], v[80:83], v[76:79]\n\t" \
  "v_exp_f32 v42, v42\n\t" \
  "v_mfma_f32_16x16x32_bf16 v[96:99], a[192:195], v[112:115], v[188:191]\n\t" \
  "ds_read_b64_tr_b16 a[182:183], v237 offset:13056\n\t" \
  "v_lshlrev_b32 v206, 4, v206\n\t" \
  "v_mfma_f32_16x16x32_bf16 v[96:99], a[196:199], v[116:119], v[96:99]\n\t" \
  "ds_read_b64_tr_b16 a[184:185], v238 offset:768\n\t" \
  "v_lshrrev_b32 v206, v206, v209\n\t" \
  "v_mfma_f32_16x16x32_bf16 v[96:99], a[200:203], v[120:123], v[96:99]\n\t" \
  "v_exp_f32 v43, v43\n\t" \
  "v_mfma_f32_16x16x32_bf16 v[96:99], a[204:207], v[124:127], v[96:99]\n\t" \
  "ds_read_b64_tr_b16 a[186:187], v238 offset:4864\n\t" \
  "v_and_b32 v206, 0x4000, v206\n\t" \
  "v_mfma_f32_16x16x32_bf16 v[100:103], a[220:223], v[124:127], v[188:191]\n\t" \
  "v_exp_f32 v44, v44\n\t" \
  "v_mfma_f32_16x16x32_bf16 v[100:103], a[216:219], v[120:123], v[100:103]\n\t" \
  "ds_read_b64_tr_b16 a[188:189], v238 offset:8960\n\t" \
  "v_cmp_ne_u32_e64 s[96:97], 0, v206\n\t" \
  STS("s_nop 4\n\t") \
  STS("v_writelane_b32 %[stv], s96, 60\n\t") \
  STS("v_writelane_b32 %[stv], s97, 61\n\t") \
  "v_mfma_f32_16x16x32_bf16 v[100:103], a[212:215], v[116:119], v[100:103]\n\t" \
  "s_bcnt1_i32_b32 %[nb], s96\n\t" \
  "s_bcnt1_i32_b32 %[na], s97\n\t" \
  "s_lshr_b32 s96, s96, 16\n\t" \
  "s_lshr_b32 s98, %[hl18], 18\n\t" \
  "s_mul_i32 s98, s98, 127\n\t" \
  "v_add_lshl_u32 v245, v245, s98, 2\n\t" \
  "v_mfma_f32_16x16x32_bf16 v[100:103], a[208:211], v[112:115], v[100:103]\n\t" \
  "s_lshr_b32 s97, s97, 16\n\t" \
  "ds_read_b64_tr_b16 a[190:191], v238 offset:13056\n\t" \
  "s_bcnt1_i32_b32 s96, s96\n\t" \
  "v_mfma_f32_16x16x32_bf16 v[104:107], a[224:227], v[112:115], v[188:191]\n\t" \
  "s_bcnt1_i32_b32 s97, s97\n\t" \
  "s_sub_u32 s96, s97, s96\n\t" \
  "s_sub_u32 %[nb], %[na], %[nb]\n\t" \
  "s_ashr_i32 %[nb], %[nb], 31\n\t" \
  "v_mfma_f32_16x16x32_bf16 v[104:107], a[228:231], v[116:119], v[104:107]\n\t" \
  "s_xor_b32 s96, s96, %[nb]\n\t" \
  "v_exp_f32 v45, v45\n\t" \
  "v_mfma_f32_16x16x32_bf16 v[104:107], a[232:235], v[120:123], v[104:107]\n\t" \
  "s_sub_u32 s96, s96, %[nb]\n\t" \
  "v_exp_f32 v46, v46\n\t" \
  "v_mfma_f32_16x16x32_bf16 v[104:107], a[236:239], v[124:127], v[104:107]\n\t" \
  "s_sub_u32 %[na], 6, s96\n\t" \
  "v_exp_f32 v47, v47\n\t" \
  "v_mfma_f32_16x16x32_bf16 v[108:111], a[252:255], v[124:127], v[188:191]\n\t" \
  "s_ashr_i32 %[na], %[na], 31\n\t" \
  "v_exp_f32 v64, v64\n\t" \
  "v_mfma_f32_16x16x32_bf16 v[108:111], a[248:251], v[120:123], v[108:111]\n\t" \
  "s_and_b32 %[nb], %[nb], 0x2000\n\t" \
  "v_exp_f32 v65, v65\n\t" \
  "v_mfma_f32_16x16x32_bf16 v[108:111], a[244:247], v[116:119], v[108:111]\n\t" \
  "s_xor_b32 s96, %[po], %[nb]\n\t" \
  "v_exp_f32 v66, v66\n\t" \
  "v_mfma_f32_16x16x32_bf16 v[108:111], a[240:243], v[112:115], v[108:111]\n\t" \
  "s_waitcnt lgkmcnt(0)\n\t" \
  "s_barrier\n\t" \
  STA("s_memrealtime s[94:95]\n\t") \
  STA("s_memtime s[92:93]\n\t") \
  "v_mfma_f32_16x16x32_bf16 a[0:3], a[128:131], v[128:131], a[0:3]\n\t" \
  "s_and_b32 s96, s96, %[na]\n\t" \
  "v_exp_f32 v67, v67\n\t" \
  "v_mfma_f32_16x16x32_bf16 a[0:3], a[132:135], v[132:135], a[0:3]\n\t" \
  "s_xor_b32 %[eso], %[eso], s96\n\t" \
  STS("v_writelane_b32 %[stv], %[na], 57\n\t") \
  STS("v_writelane_b32 %[stv], %[nb], 59\n\t") \
  "v_exp_f32 v68, v68\n\t" \
  "v_mfma_f32_16x16x32_bf16 a[16:19], a[136:139], v[128:131], a[16:19]\n\t" \
  "s_xor_b32 s96, %[px], %[nb]\n\t" \
  "ds_read_b128 a[192:195], v220 offset:16384\n\t" \
  "s_and_b32 s96, s96, %[na]\n\t" \
  "v_mfma_f32_16x16x32_bf16 a[16:19], a[140:143], v[132:135], a[16:19]\n\t" \
  "s_xor_b32 %[xcb], %[xcb], s96\n\t" \
  "v_exp_f32 v69, v69\n\t" \
  "v_mfma_f32_16x16x32_bf16 a[32:35], a[144:147], v[128:131], a[32:35]\n\t" \
  "s_xor_b32 %[frb], %[frb], %[nb]\n\t" \
  "v_exp_f32 v70, v70\n\t" \
  "v_mfma_f32_16x16x32_bf16 a[32:35], a[148:151], v[132:135], a[32:35]\n\t" \
  "s_xor_b32 %[fw2], %[fw2], %[nb]\n\t" \
  "v_exp_f32 v71, v71\n\t" \
  "v_mfma_f32_16x16x32_bf16 a[48:51], a[152:155], v[128:131], a[48:51]\n\t" \
  "ds_read_b128 a[196:199], v221 offset:16384\n\t" \
  "ds_read_b128 a[200:203], v222 offset:16384\n\t" \
  "v_mfma_f32_16x16x32_bf16 a[48:51], a[156:159], v[132:135], a[48:51]\n\t" \
  "v_exp_f32 v72, v72\n\t" \
  "v_mfma_f32_16x16x32_bf16 a[64:67], a[160:163], v[128:131], a[64:67]\n\t" \
  "v_exp_f32 v73, v73\n\t" \
  "v_mfma_f32_16x16x32_bf16 a[64:67], a[164:167], v[132:135], a[64:67]\n\t" \
  "v_exp_f32 v74, v74\n\t" \
  "v_mfma_f32_16x16x32_bf16 a[80:83], a[168:171], v[128:131], a[80:83]\n\t" \
  "v_exp_f32 v75, v75\n\t" \
  "v_mfma_f32_16x16x32_bf16 a[80:83], a[172:175], v[132:135], a[80:83]\n\t" \
  "v_exp_f32 v76, v76\n\t" \
  "v_mfma_f32_16x16x32_bf16 a[96:99], a[176:179], v[128:131], a[96:99]\n\t" \
  STA("v_writelane_b32 %[stv], s94, 48\n\t") \
  STA("v_writelane_b32 %[stv], s92, 49\n\t") \
  "v_exp_f32 v77, v77\n\t" \
  "v_mfma_f32_16x16x32_bf16 a[96:99], a[180:183], v[132:135], a[96:99]\n\t" \
  "v_exp_f32 v78, v78\n\t" \
  "v_mfma_f32_16x16x32_bf16 a[112:115], a[184:187], v[128:131], a[112:115]\n\t" \
  "ds_read_b128 a[204:207], v223 offset:16384\n\t" \
  "ds_read_b128 a[208:211], v220 offset:20480\n\t" \
  "v_mfma_f32_16x16x32_bf16 a[112:115], a[188:191], v[132:135], a[112:115]\n\t" \
  "v_exp_f32 v79, v79\n\t" \
  "v_mfma_f32_16x16x32_bf16 v[160:163], v[216:219], v[128:131], v[160:163]\n\t" \
  "v_exp_f32 v96, v96\n\t" \
  "v_mfma_f32_16x16x32_bf16 v[160:163], v[216:219], v[132:135], v[160:163]\n\t" \
  "v_exp_f32 v97, v97\n\t" \
  "v_mfma_f32_16x16x32_bf16 a[4:7], a[128:131], v[136:139], a[4:7]\n\t" \
  "v_exp_f32 v98, v98\n\t" \
  "v_mfma_f32_16x16x32_bf16 a[4:7], a[132:135], v[140:143], a[4:7]\n\t" \
  "ds_read_b128 a[212:215], v221 offset:20480\n\t" \
  "v_cvt_pk_bf16_f32 v128, v0, v1\n\t" \
  "v_mfma_f32_16x16x32_bf16 a[20:23], a[136:139], v[136:139], a[20:23]\n\t" \
  "s_add_u32 m0, %[m], 147456\n\t" \
  "v_exp_f32 v99, v99\n\t" \
  "v_mfma_f32_16x16x32_bf16 a[20:23], a[140:143], v[140:143], a[20:23]\n\t" \
  "s_add_u32 s96, %[sofk], 114688\n\t" \
  "v_exp_f32 v100, v100\n\t" \
  "v_mfma_f32_16x16x32_bf16 a[36:39], a[144:147], v[136:139], a[36:39]\n\t" \
  "v_exp_f32 v101, v101\n\t" \
  "v_mfma_f32_16x16x32_bf16 a[36:39], a[148:151], v[140:143], a[36:39]\n\t" \
  "ds_read_b128 a[216:219], v222 offset:20480\n\t" \
  "v_cvt_pk_bf16_f32 v129, v2, v3\n\t" \
  "v_mfma_f32_16x16x32_bf16 a[52:55], a[152:155], v[136:139], a[52:55]\n\t" \
  "v_exp_f32 v102, v102\n\t" \
  "v_mfma_f32_16x16x32_bf16 a[52:55], a[156:159], v[140:143], a[52:55]\n\t" \
  "v_exp_f32 v103, v103\n\t" \
  "v_mfma_f32_16x16x32_bf16 a[68:71], a[160:163], v[136:139], a[68:71]\n\t" \
  "v_exp_f32 v104, v104\n\t" \
  "v_mfma_f32_16x16x32_bf16 a[68:71], a[164:167], v[140:143], a[68:71]\n\t" \
  "buffer_load_dwordx4 v230, %[sV], s96 offen offset:0 lds\n\t" \
  "v_mfma_f32_16x16x32_bf16 a[84:87], a[168:171], v[136:139], a[84:87]\n\t" \
  "ds_read_b128 a[220:223], v223 offset:20480\n\t" \
  "v_cvt_pk_bf16_f32 v130, v4, v5\n\t" \
  "v_mfma_f32_16x16x32_bf16 a[84:87], a[172:175], v[140:143], a[84:87]\n\t" \
  "v_exp_f32 v105, v105\n\t" \
  "v_mfma_f32_16x16x32_bf16 a[100:103], a[176:179], v[136:139], a[100:103]\n\t" \
  "ds_read_b128 a[224:227], v220 offset:24576\n\t" \
  "v_cvt_pk_bf16_f32 v131, v6, v7\n\t" \
  "v_mfma_f32_16x16x32_bf16 a[100:103], a[180:183], v[140:143], a[100:103]\n\t" \
  "buffer_load_dwordx4 v231, %[sV], s96 offen offset:1024 lds\n\t" \
  "v_mfma_f32_16x16x32_bf16 a[116:119], a[184:187], v[136:139], a[116:119]\n\t" \
  "v_exp_f32 v106, v106\n\t" \
  "v_mfma_f32_16x16x32_bf16 a[116:119], a[188:191], v[140:143], a[116:119]\n\t" \
  "v_exp_f32 v107, v107\n\t" \
  "v_mfma_f32_16x16x32_bf16 v[164:167], v[216:219], v[136:139], v[164:167]\n\t" \
  "buffer_load_dwordx4 v230, %[sV], s96 offen offset:2048 lds\n\t" \
  "v_mfma_f32_16x16x32_bf16 v[164:167], v[216:219], v[140:143], v[164:167]\n\t" \
  "ds_read_b128 a[228:231], v221 offset:24576\n\t" \
  "v_cvt_pk_bf16_f32 v132, v8, v9\n\t" \
  "v_mfma_f32_16x16x32_bf16 a[8:11], a[128:131], v[144:147], a[8:11]\n\t" \
  "v_exp_f32 v108, v108\n\t" \
  "v_mfma_f32_16x16x32_bf16 a[8:11], a[132:135], v[148:151], a[8:11]\n\t" \
  "v_exp_f32 v109, v109\n\t" \
  "v_mfma_f32_16x16x32_bf16 a[24:27], a[136:139], v[144:147], a[24:27]\n\t" \
  "buffer_load_dwordx4 v231, %[sV], s96 offen offset:3072 lds\n\t" \
  "v_mfma_f32_16x16x32_bf16 a[24:27], a[140:143], v[148:151], a[24:27]\n\t" \
  "s_add_u32 m0, %[m], 49152\n\t" \
  "ds_read_b128 a[232:235], v222 offset:24576\n\t" \
  "v_cvt_pk_bf16_f32 v133, v10, v11\n\t" \
  "v_mfma_f32_16x16x32_bf16 a[40:43], a[144:147], v[144:147], a[40:43]\n\t" \
  "s_xor_b32 s97, %[wob], 262144\n\t" \
  "v_exp_f32 v110, v110\n\t" \
  "v_mfma_f32_16x16x32_bf16 a[40:43], a[148:151], v[148:151], a[40:43]\n\t" \
  "v_exp_f32 v111, v111\n\t" \
  "v_mfma_f32_16x16x32_bf16 a[56:59], a[152:155], v[144:147], a[56:59]\n\t" \
  "ds_read_b128 a[236:239], v223 offset:24576\n\t" \
  "v_cvt_pk_bf16_f32 v134, v12, v13\n\t" \
  "v_mfma_f32_16x16x32_bf16 a[56:59], a[156:159], v[148:151], a[56:59]\n\t" \
  "ds_read_b128 a[240:243], v220 offset:28672\n\t" \
  "v_cvt_pk_bf16_f32 v135, v14, v15\n\t" \
  "v_mfma_f32_16x16x32_bf16 a[72:75], a[160:163], v[144:147], a[72:75]\n\t" \
  "ds_read_b128 a[244:247], v221 offset:28672\n\t" \
  "v_cvt_pk_bf16_f32 v136, v32, v33\n\t" \
  "v_mfma_f32_16x16x32_bf16 a[72:75], a[164:167], v[148:151], a[72:75]\n\t" \
  "ds_read_b128 a[248:251], v222 offset:28672\n\t" \
  "v_cvt_pk_bf16_f32 v137, v34, v35\n\t" \
  "v_mfma_f32_16x16x32_bf16 a[88:91], a[168:171], v[144:147], a[88:91]\n\t" \
  "v_cvt_pk_bf16_f32 v138, v36, v37\n\t" \
  "v_cvt_pk_bf16_f32 v139, v38, v39\n\t" \
  "v_mfma_f32_16x16x32_bf16 a[88:91], a[172:175], v[148:151], a[88:91]\n\t" \
  "ds_read_b128 a[252:255], v223 offset:28672\n\t" \
  "v_cvt_pk_bf16_f32 v140, v40, v41\n\t" \
  "v_mfma_f32_16x16x32_bf16 a[104:107], a[176:179], v[144:147], a[104:107]\n\t" \
  "buffer_load_dwordx4 v240, %[wo], s97 offen offset:0 lds\n\t" \
  "v_mfma_f32_16x16x32_bf16 a[104:107], a[180:183], v[148:151], a[104:107]\n\t" \
  "s_add_u32 s97, s97, 7168\n\t" \
  "v_cvt_pk_bf16_f32 v141, v42, v43\n\t" \
  "v_cvt_pk_bf16_f32 v142, v44, v45\n\t" \
  "v_mfma_f32_16x16x32_bf16 a[120:123], a[184:187], v[144:147], a[120:123]\n\t" \
  "v_cvt_pk_bf16_f32 v143, v46, v47\n\t" \
  "v_mfma_f32_16x16x32_bf16 a[120:123], a[188:191], v[148:151], a[120:123]\n\t" \
  "buffer_load_dwordx4 v241, %[wo], s97 offen offset:1024 lds\n\t" \
  "v_mfma_f32_16x16x32_bf16 v[168:171], v[216:219], v[144:147], v[168:171]\n\t" \
  "s_add_u32 s97, s97, 7168\n\t" \
  "v_mfma_f32_16x16x32_bf16 v[168:171], v[216:219], v[148:151], v[168:171]\n\t" \
  "v_cvt_pk_bf16_f32 v144, v64, v65\n\t" \
  "v_cvt_pk_bf16_f32 v145, v66, v67\n\t" \
  "v_mfma_f32_16x16x32_bf16 a[12:15], a[128:131], v[152:155], a[12:15]\n\t" \
  "v_cvt_pk_bf16_f32 v146, v68, v69\n\t" \
  "v_cvt_pk_bf16_f32 v147, v70, v71\n\t" \
  "v_mfma_f32_16x16x32_bf16 a[12:15], a[132:135], v[156:159], a[12:15]\n\t" \
  "buffer_load_dwordx4 v240, %[wo], s97 offen offset:2048 lds\n\t" \
  "v_mfma_f32_16x16x32_bf16 a[28:31], a[136:139], v[152:155], a[28:31]\n\t" \
  "s_add_u32 s97, s97, 7168\n\t" \
  "v_cvt_pk_bf16_f32 v148, v72, v73\n\t" \
  "v_cvt_pk_bf16_f32 v149, v74, v75\n\t" \
  "v_mfma_f32_16x16x32_bf16 a[28:31], a[140:143], v[156:159], a[28:31]\n\t" \
  "v_cvt_pk_bf16_f32 v150, v76, v77\n\t" \
  "v_cvt_pk_bf16_f32 v151, v78, v79\n\t" \
  "v_mfma_f32_16x16x32_bf16 a[44:47], a[144:147], v[152:155], a[44:47]\n\t" \
  "buffer_load_dwordx4 v241, %[wo], s97 offen offset:3072 lds\n\t" \
  "v_mfma_f32_16x16x32_bf16 a[44:47], a[148:151], v[156:159], a[44:47]\n\t" \
  "v_mfma_f32_16x16x32_bf16 a[60:63], a[152:155], v[152:155], a[60:63]\n\t" \
  "s_add_u32 m0, %[m], 98304\n\t" \
  "s_mov_b32 s96, 40960\n\t" \
  "v_mfma_f32_16x16x32_bf16 a[60:63], a[156:159], v[156:159], a[60:63]\n\t" \
  "v_mfma_f32_16x16x32_bf16 a[76:79], a[160:163], v[152:155], a[76:79]\n\t" \
  "v_mfma_f32_16x16x32_bf16 a[76:79], a[164:167], v[156:159], a[76:79]\n\t" \
  "buffer_load_dwordx4 v232, %[sG], s96 offen offset:2048 lds\n\t" \
  "v_mfma_f32_16x16x32_bf16 a[92:95], a[168:171], v[152:155], a[92:95]\n\t" \
  "s_mov_b32 s96, 47104\n\t" \
  "v_mfma_f32_16x16x32_bf16 a[92:95], a[172:175], v[156:159], a[92:95]\n\t" \
  "v_mfma_f32_16x16x32_bf16 a[108:111], a[176:179], v[152:155], a[108:111]\n\t" \
  "v_mfma_f32_16x16x32_bf16 a[108:111], a[180:183], v[156:159], a[108:111]\n\t" \
  "buffer_load_dwordx4 v232, %[sG], s96 offen offset:3072 lds\n\t" \
  "v_mfma_f32_16x16x32_bf16 a[124:127], a[184:187], v[152:155], a[124:127]\n\t" \
  "v_mfma_f32_16x16x32_bf16 a[124:127], a[188:191], v[156:159], a[124:127]\n\t" \
  "v_mfma_f32_16x16x32_bf16 v[172:175], v[216:219], v[152:155], v[172:175]\n\t" \
  "v_mfma_f32_16x16x32_bf16 v[172:175], v[216:219], v[156:159], v[172:175]\n\t" \
  "s_waitcnt lgkmcnt(0)\n\t" \
  "s_barrier\n\t" \
  STA("s_memrealtime s[94:95]\n\t") \
  STA("s_memtime s[92:93]\n\t") \
  "v_mfma_f32_16x16x32_bf16 v[0:3], a[192:195], v[16:19], v[176:179]\n\t" \
  "ds_read_b64_tr_b16 a[128:129], v228 offset:0\n\t" \
  "v_cvt_pk_bf16_f32 v152, v96, v97\n\t" \
  "v_mfma_f32_16x16x32_bf16 v[0:3], a[196:199], v[20:23], v[0:3]\n\t" \
  "ds_read_b64_tr_b16 a[130:131], v228 offset:4096\n\t" \
  "v_cvt_pk_bf16_f32 v153, v98, v99\n\t" \
  "v_mfma_f32_16x16x32_bf16 v[0:3], a[200:203], v[24:27], v[0:3]\n\t" \
  "ds_read_b64_tr_b16 a[132:133], v228 offset:8192\n\t" \
  "v_cvt_pk_bf16_f32 v154, v100, v101\n\t" \
  "v_mfma_f32_16x16x32_bf16 v[0:3], a[204:207], v[28:31], v[0:3]\n\t" \
  "ds_read_b64_tr_b16 a[134:135], v228 offset:12288\n\t" \
  "v_cvt_pk_bf16_f32 v155, v102, v103\n\t" \
  "v_mfma_f32_16x16x32_bf16 v[4:7], a[220:223], v[28:31], v[176:179]\n\t" \
  "s_add_u32 m0, %[m], 65536\n\t" \
  "ds_read_b64_tr_b16 a[136:137], v229 offset:0\n\t" \
  "v_cvt_pk_bf16_f32 v156, v104, v105\n\t" \
  "v_mfma_f32_16x16x32_bf16 v[4:7], a[216:219], v[24:27], v[4:7]\n\t" \
  "s_xor_b32 s97, %[wob], 393216\n\t" \
  "v_exp_f32 v0, v0\n\t" \
  "v_mfma_f32_16x16x32_bf16 v[4:7], a[212:215], v[20:23], v[4:7]\n\t" \
  "v_exp_f32 v1, v1\n\t" \
  "v_mfma_f32_16x16x32_bf16 v[4:7], a[208:211], v[16:19], v[4:7]\n\t" \
  "v_exp_f32 v2, v2\n\t" \
  "v_mfma_f32_16x16x32_bf16 v[8:11], a[224:227], v[16:19], v[176:179]\n\t" \
  "v_exp_f32 v3, v3\n\t" \
  "v_mfma_f32_16x16x32_bf16 v[8:11], a[228:231], v[20:23], v[8:11]\n\t" \
  "ds_read_b64_tr_b16 a[138:139], v229 offset:4096\n\t" \
  "v_cvt_pk_bf16_f32 v157, v106, v107\n\t" \
  "v_mfma_f32_16x16x32_bf16 v[8:11], a[232:235], v[24:27], v[8:11]\n\t" \
  "ds_read_b64_tr_b16 a[140:141], v229 offset:8192\n\t" \
  "v_cvt_pk_bf16_f32 v158, v108, v109\n\t" \
  "v_mfma_f32_16x16x32_bf16 v[8:11], a[236:239], v[28:31], v[8:11]\n\t" \
  "v_exp_f32 v4, v4\n\t" \
  "v_mfma_f32_16x16x32_bf16 v[12:15], a[252:255], v[28:31], v[176:179]\n\t" \
  STA("v_writelane_b32 %[stv], s94, 50\n\t") \
  STA("v_writelane_b32 %[stv], s92, 51\n\t") \
  "buffer_load_dwordx4 v240, %[wo], s97 offen offset:0 lds\n\t" \
  "v_mfma_f32_16x16x32_bf16 v[12:15], a[248:251], v[24:27], v[12:15]\n\t" \
  "s_add_u32 s97, s97, 7168\n\t" \
  "ds_read_b64_tr_b16 a[142:143], v229 offset:12288\n\t" \
  "v_cvt_pk_bf16_f32 v159, v110, v111\n\t" \
  "v_mfma_f32_16x16x32_bf16 v[12:15], a[244:247], v[20:23], v[12:15]\n\t" \
  "v_exp_f32 v5, v5\n\t" \
  "v_mfma_f32_16x16x32_bf16 v[12:15], a[240:243], v[16:19], v[12:15]\n\t" \
  "v_exp_f32 v6, v6\n\t" \
  "v_mfma_f32_16x16x32_bf16 v[32:35], a[192:195], v[48:51], v[180:183]\n\t" \
  "buffer_load_dwordx4 v241, %[wo], s97 offen offset:1024 lds\n\t" \
  "v_mfma_f32_16x16x32_bf16 v[32:35], a[196:199], v[52:55], v[32:35]\n\t" \
  "s_add_u32 s97, s97, 7168\n\t" \
  "ds_read_b64_tr_b16 a[144:145], v228 offset:256\n\t" \
  "ds_read_b64_tr_b16 a[146:147], v228 offset:4352\n\t" \
  "v_mfma_f32_16x16x32_bf16 v[32:35], a[200:203], v[56:59], v[32:35]\n\t" \
  "v_exp_f32 v7, v7\n\t" \
  "v_mfma_f32_16x16x32_bf16 v[32:35], a[204:207], v[60:63], v[32:35]\n\t" \
  "v_exp_f32 v8, v8\n\t" \
  "v_mfma_f32_16x16x32_bf16 v[36:39], a[220:223], v[60:63], v[180:183]\n\t" \
  "buffer_load_dwordx4 v240, %[wo], s97 offen offset:2048 lds\n\t" \
  "v_mfma_f32_16x16x32_bf16 v[36:39], a[216:219], v[56:59], v[36:39]\n\t" \
  "s_add_u32 s97, s97, 7168\n\t" \
  "ds_read_b64_tr_b16 a[148:149], v228 offset:8448\n\t" \
  "ds_read_b64_tr_b16 a[150:151], v228 offset:12544\n\t" \
  "v_mfma_f32_16x16x32_bf16 v[36:39], a[212:215], v[52:55], v[36:39]\n\t" \
  "v_exp_f32 v9, v9\n\t" \
  "v_mfma_f32_16x16x32_bf16 v[36:39], a[208:211], v[48:51], v[36:39]\n\t" \
  "ds_read_b64_tr_b16 a[152:153], v229 offset:256\n\t" \
  "ds_read_b64_tr_b16 a[154:155], v229 offset:4352\n\t" \
  "v_mfma_f32_16x16x32_bf16 v[40:43], a[224:227], v[48:51], v[180:183]\n\t" \
  "buffer_load_dwordx4 v241, %[wo], s97 offen offset:3072 lds\n\t" \
  "v_mfma_f32_16x16x32_bf16 v[40:43], a[228:231], v[52:55], v[40:43]\n\t" \
  "v_exp_f32 v10, v10\n\t" \
  "v_mfma_f32_16x16x32_bf16 v[40:43], a[232:235], v[56:59], v[40:43]\n\t" \
  "v_exp_f32 v11, v11\n\t" \
  "v_mfma_f32_16x16x32_bf16 v[40:43], a[236:239], v[60:63], v[40:43]\n\t" \
  "ds_read_b64_tr_b16 a[156:157], v229 offset:8448\n\t" \
  "ds_read_b64_tr_b16 a[158:159], v229 offset:12544\n\t" \
  "v_mfma_f32_16x16x32_bf16 v[44:47], a[252:255], v[60:63], v[180:183]\n\t" \
  "v_exp_f32 v12, v12\n\t" \
  "v_mfma_f32_16x16x32_bf16 v[44:47], a[248:251], v[56:59], v[44:47]\n\t" \
  "v_exp_f32 v13, v13\n\t" \
  "v_mfma_f32_16x16x32_bf16 v[44:47], a[244:247], v[52:55], v[44:47]\n\t" \
  "ds_read_b64_tr_b16 a[160:161], v228 offset:512\n\t" \
  "ds_read_b64_tr_b16 a[162:163], v228 offset:4608\n\t" \
  "v_mfma_f32_16x16x32_bf16 v[44:47], a[240:243], v[48:51], v[44:47]\n\t" \
  "v_exp_f32 v14, v14\n\t" \
  "v_mfma_f32_16x16x32_bf16 v[64:67], a[192:195], v[80:83], v[184:187]\n\t" \
  "v_exp_f32 v15, v15\n\t" \
  "v_mfma_f32_16x16x32_bf16 v[64:67], a[196:199], v[84:87], v[64:67]\n\t" \
  "v_exp_f32 v32, v32\n\t" \
  "v_mfma_f32_16x16x32_bf16 v[64:67], a[200:203], v[88:91], v[64:67]\n\t" \
  "ds_read_b64_tr_b16 a[164:165], v228 offset:8704\n\t" \
  "ds_read_b64_tr_b16 a[166:167], v228 offset:12800\n\t" \
  "v_mfma_f32_16x16x32_bf16 v[64:67], a[204:207], v[92:95], v[64:67]\n\t" \
  "v_exp_f32 v33, v33\n\t" \
  "v_mfma_f32_16x16x32_bf16 v[68:71], a[220:223], v[92:95], v[184:187]\n\t" \
  "v_exp_f32 v34, v34\n\t" \
  "v_mfma_f32_16x16x32_bf16 v[68:71], a[216:219], v[88:91], v[68:71]\n\t" \
  "ds_read_b64_tr_b16 a[168:169], v229 offset:512\n\t" \
  "ds_read_b64_tr_b16 a[170:171], v229 offset:4608\n\t" \
  "v_mfma_f32_16x16x32_bf16 v[68:71], a[212:215], v[84:87], v[68:71]\n\t" \
  "v_exp_f32 v35, v35\n\t" \
  "v_mfma_f32_16x16x32_bf16 v[68:71], a[208:211], v[80:83], v[68:71]\n\t" \
  "v_exp_f32 v36, v36\n\t" \
  "v_mfma_f32_16x16x32_bf16 v[72:75], a[224:227], v[80:83], v[184:187]\n\t" \
  "v_exp_f32 v37, v37\n\t" \
  "v_mfma_f32_16x16x32_bf16 v[72:75], a[228:231], v[84:87], v[72:75]\n\t" \
  "ds_read_b64_tr_b16 a[172:173], v229 offset:8704\n\t" \
  "ds_read_b64_tr_b16 a[174:175], v229 offset:12800\n\t" \
  "v_mfma_f32_16x16x32_bf16 v[72:75], a[232:235], v[88:91], v[72:75]\n\t" \
  "v_exp_f32 v38, v38\n\t" \
  "v_mfma_f32_16x16x32_bf16 v[72:75], a[236:239], v[92:95], v[72:75]\n\t" \
  "v_exp_f32 v39, v39\n\t" \
  "v_mfma_f32_16x16x32_bf16 v[76:79], a[252:255], v[92:95], v[184:187]\n\t" \
  "ds_read_b64_tr_b16 a[176:177], v228 offset:768\n\t" \
  ".Lhs9:\n\ts_cbranch_execz .Lhs9n\n\t" \
  "ds_read_b64_tr_b16 a[178:179], v228 offset:4864\n\t" \
  "v_mfma_f32_16x16x32_bf16 v[76:79], a[248:251], v[88:91], v[76:79]\n\t" \
  "v_exp_f32 v40, v40\n\t" \
  "v_mfma_f32_16x16x32_bf16 v[76:79], a[244:247], v[84:87], v[76:79]\n\t" \
  "v_exp_f32 v41, v41\n\t" \
  "v_mfma_f32_16x16x32_bf16 v[76:79], a[240:243], v[80:83], v[76:79]\n\t" \
  "v_exp_f32 v42, v42\n\t" \
  "v_mfma_f32_16x16x32_bf16 v[96:99], a[192:195], v[112:115], v[188:191]\n\t" \
  "ds_read_b64_tr_b16 a[180:181], v228 offset:8960\n\t" \
  "ds_read_b64_tr_b16 a[182:183], v228 offset:13056\n\t" \
  "v_mfma_f32_16x16x32_bf16 v[96:99], a[196:199], v[116:119], v[96:99]\n\t" \
  "v_exp_f32 v43, v43\n\t" \
  "v_mfma_f32_16x16x32_bf16 v[96:99], a[200:203], v[120:123], v[96:99]\n\t" \
  "v_exp_f32 v44, v44\n\t" \
  "v_mfma_f32_16x16x32_bf16 v[96:99], a[204:207], v[124:127], v[96:99]\n\t" \
  "ds_read_b64_tr_b16 a[184:185], v229 offset:768\n\t" \
  "ds_read_b64_tr_b16 a[186:187], v229 offset:4864\n\t" \
  "v_mfma_f32_16x16x32_bf16 v[100:103], a[220:223], v[124:127], v[188:191]\n\t" \
  "v_exp_f32 v45, v45\n\t" \
  "v_mfma_f32_16x16x32_bf16 v[100:103], a[216:219], v[120:123], v[100:103]\n\t" \
  "v_exp_f32 v46, v46\n\t" \
  "v_mfma_f32_16x16x32_bf16 v[100:103], a[212:215], v[116:119], v[100:103]\n\t" \
  "v_exp_f32 v47, v47\n\t" \
  "v_mfma_f32_16x16x32_bf16 v[100:103], a[208:211], v[112:115], v[100:103]\n\t" \
  "ds_read_b64_tr_b16 a[188:189], v229 offset:8960\n\t" \
  "ds_read_b64_tr_b16 a[190:191], v229 offset:13056\n\t" \
  "v_mfma_f32_16x16x32_bf16 v[104:107], a[224:227], v[112:115], v[188:191]\n\t" \
  "v_exp_f32 v64, v64\n\t" \
  "v_mfma_f32_16x16x32_bf16 v[104:107], a[228:231], v[116:119], v[104:107]\n\t" \
  "v_exp_f32 v65, v65\n\t" \
  "v_mfma_f32_16x16x32_bf16 v[104:107], a[232:235], v[120:123], v[104:107]\n\t" \
  "v_exp_f32 v66, v66\n\t" \
  "v_mfma_f32_16x16x32_bf16 v[104:107], a[236:239], v[124:127], v[104:107]\n\t" \
  "v_exp_f32 v67, v67\n\t" \
  "v_mfma_f32_16x16x32_bf16 v[108:111], a[252:255], v[124:127], v[188:191]\n\t" \
  "v_exp_f32 v68, v68\n\t" \
  "v_mfma_f32_16x16x32_bf16 v[108:111], a[248:251], v[120:123], v[108:111]\n\t" \
  "v_exp_f32 v69, v69\n\t" \
  "v_mfma_f32_16x16x32_bf16 v[108:111], a[244:247], v[116:119], v[108:111]\n\t" \
  "v_exp_f32 v70, v70\n\t" \
  "v_mfma_f32_16x16x32_bf16 v[108:111], a[240:243], v[112:115], v[108:111]\n\t" \
  "s_waitcnt vmcnt(6) lgkmcnt(0)\n\t" \
  "s_barrier\n\t" \
  STA("s_memrealtime s[94:95]\n\t") \
  STA("s_memtime s[92:93]\n\t") \
  "v_mfma_f32_16x16x32_bf16 a[0:3], a[128:131], v[128:131], a[0:3]\n\t" \
  "s_add_u32 m0, %[m], 81920\n\t" \
  "v_exp_f32 v71, v71\n\t" \
  "v_mfma_f32_16x16x32_bf16 a[0:3], a[132:135], v[132:135], a[0:3]\n\t" \
  "s_xor_b32 s97, %[wob], 524288\n\t" \
  "v_exp_f32 v72, v72\n\t" \
  "v_mfma_f32_16x16x32_bf16 a[16:19], a[136:139], v[128:131], a[16:19]\n\t" \
  "ds_read_b64_tr_b16 a[192:193], v237 offset:0\n\t" \
  "ds_read_b64_tr_b16 a[194:195], v237 offset:4096\n\t" \
  "v_mfma_f32_16x16x32_bf16 a[16:19], a[140:143], v[132:135], a[16:19]\n\t" \
  "v_exp_f32 v73, v73\n\t" \
  "v_mfma_f32_16x16x32_bf16 a[32:35], a[144:147], v[128:131], a[32:35]\n\t" \
  "ds_read_b128 v[176:179], v215 offset:16384\n\t" \
  "ds_read_b64_tr_b16 a[196:197], v237 offset:8192\n\t" \
  "v_mfma_f32_16x16x32_bf16 a[32:35], a[148:151], v[132:135], a[32:35]\n\t" \
  "v_exp_f32 v74, v74\n\t" \
  "v_mfma_f32_16x16x32_bf16 a[48:51], a[152:155], v[128:131], a[48:51]\n\t" \
  "buffer_load_dwordx4 v240, %[wo], s97 offen offset:0 lds\n\t" \
  "v_mfma_f32_16x16x32_bf16 a[48:51], a[156:159], v[132:135], a[48:51]\n\t" \
  "s_add_u32 s97, s97, 7168\n\t" \
  "ds_read_b128 v[180:183], v215 offset:17408\n\t" \
  "ds_read_b64_tr_b16 a[198:199], v237 offset:12288\n\t" \
  "v_mfma_f32_16x16x32_bf16 a[64:67], a[160:163], v[128:131], a[64:67]\n\t" \
  "buffer_load_dwordx4 v241, %[wo], s97 offen offset:1024 lds\n\t" \
  "v_mfma_f32_16x16x32_bf16 a[64:67], a[164:167], v[132:135], a[64:67]\n\t" \
  "s_add_u32 s97, s97, 7168\n\t" \
  "ds_read_b64_tr_b16 a[200:201], v238 offset:0\n\t" \
  "ds_read_b128 v[184:187], v215 offset:18432\n\t" \
  "v_mfma_f32_16x16x32_bf16 a[80:83], a[168:171], v[128:131], a[80:83]\n\t" \
  "ds_read_b64_tr_b16 a[202:203], v238 offset:4096\n\t" \
  "ds_read_b128 v[188:191], v215 offset:19456\n\t" \
  "v_mfma_f32_16x16x32_bf16 a[80:83], a[172:175], v[132:135], a[80:83]\n\t" \
  "ds_read_b64_tr_b16 a[204:205], v238 offset:8192\n\t" \
  "ds_read_b128 v[192:195], v215 offset:32768\n\t" \
  "v_mfma_f32_16x16x32_bf16 a[96:99], a[176:179], v[128:131], a[96:99]\n\t" \
  STA("v_writelane_b32 %[stv], s94, 52\n\t") \
  STA("v_writelane_b32 %[stv], s92, 53\n\t") \
  "buffer_load_dwordx4 v240, %[wo], s97 offen offset:2048 lds\n\t" \
  "v_mfma_f32_16x16x32_bf16 a[96:99], a[180:183], v[132:135], a[96:99]\n\t" \
  "s_add_u32 s97, s97, 7168\n\t" \
  "ds_read_b64_tr_b16 a[206:207], v238 offset:12288\n\t" \
  "ds_read_b128 v[196:199], v215 offset:33792\n\t" \
  "v_mfma_f32_16x16x32_bf16 a[112:115], a[184:187], v[128:131], a[112:115]\n\t" \
  "ds_read_b64_tr_b16 a[208:209], v237 offset:256\n\t" \
  "ds_read_b128 v[200:203], v215 offset:34816\n\t" \
  "v_mfma_f32_16x16x32_bf16 a[112:115], a[188:191], v[132:135], a[112:115]\n\t" \
  "ds_read_b64_tr_b16 a[210:211], v237 offset:4352\n\t" \
  "ds_read_b64_tr_b16 a[212:213], v237 offset:8448\n\t" \
  "v_mfma_f32_16x16x32_bf16 v[160:163], v[216:219], v[128:131], v[160:163]\n\t" \
  "buffer_load_dwordx4 v241, %[wo], s97 offen offset:3072 lds\n\t" \
  "v_mfma_f32_16x16x32_bf16 v[160:163], v[216:219], v[132:135], v[160:163]\n\t" \
  "ds_read_b128 v[204:207], v215 offset:35840\n\t" \
  "v_cvt_pk_bf16_f32 v128, v0, v1\n\t" \
  "v_mfma_f32_16x16x32_bf16 a[4:7], a[128:131], v[136:139], a[4:7]\n\t" \
  "ds_read_b64_tr_b16 a[214:215], v237 offset:12544\n\t" \
  "v_cvt_pk_bf16_f32 v129, v2, v3\n\t" \
  "v_mfma_f32_16x16x32_bf16 a[4:7], a[132:135], v[140:143], a[4:7]\n\t" \
  "ds_read_b64_tr_b16 a[216:217], v238 offset:256\n\t" \
  "v_cvt_pk_bf16_f32 v130, v4, v5\n\t" \
  "v_mfma_f32_16x16x32_bf16 a[20:23], a[136:139], v[136:139], a[20:23]\n\t" \
  "s_add_u32 m0, %[m], 114688\n\t" \
  "ds_read_b64_tr_b16 a[218:219], v238 offset:4352\n\t" \
  "v_cvt_pk_bf16_f32 v131, v6, v7\n\t" \
  "v_mfma_f32_16x16x32_bf16 a[20:23], a[140:143], v[140:143], a[20:23]\n\t" \
  "s_xor_b32 s97, %[wob], 786432\n\t" \
  "ds_read_b64_tr_b16 a[220:221], v238 offset:8448\n\t" \
  "v_cvt_pk_bf16_f32 v132, v8, v9\n\t" \
  "v_mfma_f32_16x16x32_bf16 a[36:39], a[144:147], v[136:139], a[36:39]\n\t" \
  "s_waitcnt lgkmcnt(13)\n\t" \
  "ds_read_b64_tr_b16 a[222:223], v238 offset:12544\n\t" \
  "v_cvt_pk_bf16_f32 v133, v10, v11\n\t" \
  "v_mfma_f32_16x16x32_bf16 a[36:39], a[148:151], v[140:143], a[36:39]\n\t" \
  "ds_read_b64_tr_b16 a[224:225], v237 offset:512\n\t" \
  "v_cvt_pk_bf16_f32 v134, v12, v13\n\t" \
  "v_mfma_f32_16x16x32_bf16 a[52:55], a[152:155], v[136:139], a[52:55]\n\t" \
  "ds_read_b64_tr_b16 a[226:227], v237 offset:4608\n\t" \
  "v_cvt_pk_bf16_f32 v135, v14, v15\n\t" \
  "v_mfma_f32_16x16x32_bf16 a[52:55], a[156:159], v[140:143], a[52:55]\n\t" \
  "ds_read_b64_tr_b16 a[228:229], v237 offset:8704\n\t" \
  "ds_read_b64_tr_b16 a[230:231], v237 offset:12800\n\t" \
  "v_mfma_f32_16x16x32_bf16 a[68:71], a[160:163], v[136:139], a[68:71]\n\t" \
  "v_exp_f32 v75, v75\n\t" \
  "v_mfma_f32_16x16x32_bf16 a[68:71], a[164:167], v[140:143], a[68:71]\n\t" \
  "ds_read_b64_tr_b16 a[232:233], v238 offset:512\n\t" \
  "ds_read_b64_tr_b16 a[234:235], v238 offset:4608\n\t" \
  "v_mfma_f32_16x16x32_bf16 a[84:87], a[168:171], v[136:139], a[84:87]\n\t" \
  "buffer_load_dwordx4 v240, %[wo], s97 offen offset:0 lds\n\t" \
  "v_mfma_f32_16x16x32_bf16 a[84:87], a[172:175], v[140:143], a[84:87]\n\t" \
  "s_add_u32 s97, s97, 7168\n\t" \
  "v_exp_f32 v76, v76\n\t" \
  "v_mfma_f32_16x16x32_bf16 a[100:103], a[176:179], v[136:139], a[100:103]\n\t" \
  "ds_read_b64_tr_b16 a[236:237], v238 offset:8704\n\t" \
  "ds_read_b64_tr_b16 a[238:239], v238 offset:12800\n\t" \
  "v_mfma_f32_16x16x32_bf16 a[100:103], a[180:183], v[140:143], a[100:103]\n\t" \
  "buffer_load_dwordx4 v241, %[wo], s97 offen offset:1024 lds\n\t" \
  "v_mfma_f32_16x16x32_bf16 a[116:119], a[184:187], v[136:139], a[116:119]\n\t" \
  "s_add_u32 s97, s97, 7168\n\t" \
  "ds_read_b64_tr_b16 a[240:241], v237 offset:768\n\t" \
  "ds_read_b64_tr_b16 a[242:243], v237 offset:4864\n\t" \
  "v_mfma_f32_16x16x32_bf16 a[116:119], a[188:191], v[140:143], a[116:119]\n\t" \
  "v_exp_f32 v77, v77\n\t" \
  "v_mfma_f32_16x16x32_bf16 v[164:167], v[216:219], v[136:139], v[164:167]\n\t" \
  "buffer_load_dwordx4 v240, %[wo], s97 offen offset:2048 lds\n\t" \
  "v_mfma_f32_16x16x32_bf16 v[164:167], v[216:219], v[140:143], v[164:167]\n\t" \
  "s_add_u32 s97, s97, 7168\n\t" \
  "ds_read_b64_tr_b16 a[244:245], v237 offset:8960\n\t" \
  "v_cvt_pk_bf16_f32 v136, v32, v33\n\t" \
  "v_mfma_f32_16x16x32_bf16 a[0:3], a[192:195], v[128:131], a[0:3]\n\t" \
  "ds_read_b64_tr_b16 a[246:247], v237 offset:13056\n\t" \
  "v_cvt_pk_bf16_f32 v137, v34, v35\n\t" \
  "v_mfma_f32_16x16x32_bf16 a[0:3], a[196:199], v[132:135], a[0:3]\n\t" \
  "ds_read_b64_tr_b16 a[248:249], v238 offset:768\n\t" \
  "v_cvt_pk_bf16_f32 v138, v36, v37\n\t" \
  "v_mfma_f32_16x16x32_bf16 v[160:163], v[216:219], v[128:131], v[160:163]\n\t" \
  "buffer_load_dwordx4 v241, %[wo], s97 offen offset:3072 lds\n\t" \
  "v_mfma_f32_16x16x32_bf16 v[160:163], v[216:219], v[132:135], v[160:163]\n\t" \
  "s_add_u32 m0, %[m], 131072\n\t" \
  "ds_read_b64_tr_b16 a[250:251], v238 offset:4864\n\t" \
  "v_cvt_pk_bf16_f32 v139, v38, v39\n\t" \
  "v_mfma_f32_16x16x32_bf16 a[16:19], a[200:203], v[128:131], a[16:19]\n\t" \
  "s_waitcnt lgkmcnt(15)\n\t" \
  "ds_read_b64_tr_b16 a[252:253], v238 offset:8960\n\t" \
  "v_cvt_pk_bf16_f32 v140, v40, v41\n\t" \
  "v_mfma_f32_16x16x32_bf16 a[16:19], a[204:207], v[132:135], a[16:19]\n\t" \
  "s_xor_b32 s97, %[wob], 917504\n\t" \
  "ds_read_b64_tr_b16 a[254:255], v238 offset:13056\n\t" \
  "v_cvt_pk_bf16_f32 v141, v42, v43\n\t" \
  "v_xor_b32 v235, 64, v234\n\t" \
  "v_mfma_f32_16x16x32_bf16 a[32:35], a[208:211], v[128:131], a[32:35]\n\t" \
  "v_exp_f32 v78, v78\n\t" \
  "v_cvt_pk_bf16_f32 v142, v44, v45\n\t" \
  "v_mfma_f32_16x16x32_bf16 a[32:35], a[212:215], v[132:135], a[32:35]\n\t" \
  "ds_read_b64_tr_b16 v[48:49], v235 offset:0\n\t" \
  "v_cvt_pk_bf16_f32 v143, v46, v47\n\t" \
  "v_mfma_f32_16x16x32_bf16 a[48:51], a[216:219], v[128:131], a[48:51]\n\t" \
  "s_waitcnt lgkmcnt(15)\n\t" \
  "v_exp_f32 v79, v79\n\t" \
  "v_mfma_f32_16x16x32_bf16 a[48:51], a[220:223], v[132:135], a[48:51]\n\t" \
  "ds_read_b64_tr_b16 v[50:51], v235 offset:4096\n\t" \
  "ds_read_b64_tr_b16 v[52:53], v235 offset:8192\n\t" \
  "v_mfma_f32_16x16x32_bf16 a[64:67], a[224:227], v[128:131], a[64:67]\n\t" \
  "s_waitcnt lgkmcnt(15)\n\t" \
  "buffer_load_dwordx4 v240, %[wo], s97 offen offset:0 lds\n\t" \
  "v_mfma_f32_16x16x32_bf16 a[64:67], a[228:231], v[132:135], a[64:67]\n\t" \
  "s_waitcnt lgkmcnt(13)\n\t" \
  "v_rcp_f32 v208, v160\n\t" \
  "v_mfma_f32_16x16x32_bf16 a[80:83], a[232:235], v[128:131], a[80:83]\n\t" \
  "s_waitcnt lgkmcnt(11)\n\t" \
  "v_cmp_lt_f32 vcc, 0x5f800000, v160\n\t" \
  "s_cbranch_vccnz .Lat_x0\n\t" \
  ".Lat_y0:\n\t" \
  "v_accvgpr_read_b32 v16, a0\n\t" \
  "v_mfma_f32_16x16x32_bf16 a[80:83], a[236:239], v[132:135], a[80:83]\n\t" \
  "s_waitcnt lgkmcnt(9)\n\t" \
  "ds_read_b64_tr_b16 v[54:55], v235 offset:12288\n\t" \
  "v_accvgpr_read_b32 v17, a1\n\t" \
  "v_accvgpr_read_b32 v18, a2\n\t" \
  "v_mfma_f32_16x16x32_bf16 a[96:99], a[240:243], v[128:131], a[96:99]\n\t" \
  "s_waitcnt lgkmcnt(8)\n\t" \
  "v_accvgpr_read_b32 v19, a3\n\t" \
  "v_accvgpr_read_b32 v20, a16\n\t" \
  "v_accvgpr_read_b32 v21, a17\n\t" \
  "v_accvgpr_read_b32 v22, a18\n\t" \
  "v_accvgpr_read_b32 v23, a19\n\t" \
  "v_pk_mul_f32 v[16:17], v[16:17], v[208:209] op_sel_hi:[1,0]\n\t" \
  "v_pk_mul_f32 v[18:19], v[18:19], v[208:209] op_sel_hi:[1,0]\n\t" \
  "v_pk_mul_f32 v[20:21], v[20:21], v[208:209] op_sel_hi:[1,0]\n\t" \
  "v_pk_mul_f32 v[22:23], v[22:23], v[208:209] op_sel_hi:[1,0]\n\t" \
  "v_cvt_pk_bf16_f32 v0, v16, v17\n\t" \
  "v_mfma_f32_16x16x32_bf16 a[96:99], a[244:247], v[132:135], a[96:99]\n\t" \
  "s_waitcnt lgkmcnt(6)\n\t" \
  "ds_read_b64_tr_b16 v[56:57], v235 offset:16384\n\t" \
  "v_cvt_pk_bf16_f32 v1, v18, v19\n\t" \
  "v_cvt_pk_bf16_f32 v2, v20, v21\n\t" \
  "v_cvt_pk_bf16_f32 v3, v22, v23\n\t" \
  "v_mfma_f32_16x16x32_bf16 a[112:115], a[248:251], v[128:131], a[112:115]\n\t" \
  "s_waitcnt lgkmcnt(5)\n\t" \
  "ds_read_b64_tr_b16 v[58:59], v235 offset:20480\n\t" \
  "v_max3_f32 v209, |v0|, |v1|, |v2|\n\t" \
  "v_mfma_f32_16x16x32_bf16 a[112:115], a[252:255], v[132:135], a[112:115]\n\t" \
  "s_waitcnt vmcnt(9)\n\t" \
  "s_barrier\n\t" \
  "v_mfma_f32_16x16x32_bf16 a[8:11], a[128:131], v[144:147], a[8:11]\n\t" \
  "s_add_u32 s97, s97, 7168\n\t" \
  "buffer_load_dwordx4 v241, %[wo], s97 offen offset:1024 lds\n\t" \
  "v_mfma_f32_16x16x32_bf16 a[8:11], a[132:135], v[148:151], a[8:11]\n\t" \
  "s_add_u32 s97, s97, 7168\n\t" \
  "v_cmp_lt_f32 vcc, 0x33800000, v209\n\t" \
  "s_cbranch_vccz .Lat_x4\n\t" \
  ".Lat_y4:\n\t" \
  "v_accvgpr_read_b32 v24, a32\n\t" \
  "v_mfma_f32_16x16x32_bf16 a[24:27], a[136:139], v[144:147], a[24:27]\n\t" \
  "buffer_load_dwordx4 v240, %[wo], s97 offen offset:2048 lds\n\t" \
  "v_accvgpr_read_b32 v25, a33\n\t" \
  "v_accvgpr_read_b32 v26, a34\n\t" \
  "v_mfma_f32_16x16x32_bf16 a[24:27], a[140:143], v[148:151], a[24:27]\n\t" \
  "s_add_u32 s97, s97, 7168\n\t" \
  "v_accvgpr_read_b32 v27, a35\n\t" \
  "v_accvgpr_read_b32 v28, a48\n\t" \
  "v_accvgpr_read_b32 v29, a49\n\t" \
  "v_accvgpr_read_b32 v30, a50\n\t" \
  "v_accvgpr_read_b32 v31, a51\n\t" \
  "v_pk_mul_f32 v[24:25], v[24:25], v[208:209] op_sel_hi:[1,0]\n\t" \
  "v_pk_mul_f32 v[26:27], v[26:27], v[208:209] op_sel_hi:[1,0]\n\t" \
  "v_pk_mul_f32 v[28:29], v[28:29], v[208:209] op_sel_hi:[1,0]\n\t" \
  "v_pk_mul_f32 v[30:31], v[30:31], v[208:209] op_sel_hi:[1,0]\n\t" \
  "v_cvt_pk_bf16_f32 v4, v24, v25\n\t" \
  "v_mfma_f32_16x16x32_bf16 a[40:43], a[144:147], v[144:147], a[40:43]\n\t" \
  "s_xor_b32 s98, %[eso], 0\n\t" \
  "ds_read_b64_tr_b16 v[60:61], v235 offset:24576\n\t" \
  "v_cvt_pk_bf16_f32 v5, v26, v27\n\t" \
  "v_cvt_pk_bf16_f32 v6, v28, v29\n\t" \
  "v_cvt_pk_bf16_f32 v7, v30, v31\n\t" \
  "v_mfma_f32_16x16x32_bf16 a[40:43], a[148:151], v[148:151], a[40:43]\n\t" \
  "buffer_load_dwordx4 v241, %[wo], s97 offen offset:3072 lds\n\t" \
  "v_accvgpr_read_b32 v16, a64\n\t" \
  "v_accvgpr_read_b32 v17, a65\n\t" \
  "v_accvgpr_read_b32 v18, a66\n\t" \
  "v_mfma_f32_16x16x32_bf16 a[56:59], a[152:155], v[144:147], a[56:59]\n\t" \
  "v_exp_f32 v96, v96\n\t" \
  "v_mfma_f32_16x16x32_bf16 a[56:59], a[156:159], v[148:151], a[56:59]\n\t" \
  "buffer_store_dwordx4 v[0:3], v242, %[ws], s98 offen offset:0 sc1\n\t" \
  "v_mfma_f32_16x16x32_bf16 a[72:75], a[160:163], v[144:147], a[72:75]\n\t" \
  "v_accvgpr_read_b32 v19, a67\n\t" \
  "v_accvgpr_read_b32 v20, a80\n\t" \
  "v_accvgpr_read_b32 v21, a81\n\t" \
  "v_accvgpr_read_b32 v22, a82\n\t" \
  "v_accvgpr_read_b32 v23, a83\n\t" \
  "v_pk_mul_f32 v[16:17], v[16:17], v[208:209] op_sel_hi:[1,0]\n\t" \
  "v_pk_mul_f32 v[18:19], v[18:19], v[208:209] op_sel_hi:[1,0]\n\t" \
  "v_pk_mul_f32 v[20:21], v[20:21], v[208:209] op_sel_hi:[1,0]\n\t" \
  "v_pk_mul_f32 v[22:23], v[22:23], v[208:209] op_sel_hi:[1,0]\n\t" \
  "v_cvt_pk_bf16_f32 v8, v16, v17\n\t" \
  "v_mfma_f32_16x16x32_bf16 a[72:75], a[164:167], v[148:151], a[72:75]\n\t" \
  "s_xor_b32 s98, %[eso], 0\n\t" \
  "ds_read_b64_tr_b16 v[62:63], v235 offset:28672\n\t" \
  "v_xor_b32 v236, 96, v234\n\t" \
  "v_mfma_f32_16x16x32_bf16 a[88:91], a[168:171], v[144:147], a[88:91]\n\t" \
  "buffer_store_dwordx4 v[4:7], v242, %[ws], s98 offen offset:1024 sc1\n\t" \
  "v_mfma_f32_16x16x32_bf16 a[88:91], a[172:175], v[148:151], a[88:91]\n\t" \
  "ds_read_b64_tr_b16 v[80:81], v236 offset:0\n\t" \
  "v_cvt_pk_bf16_f32 v9, v18, v19\n\t" \
  "v_cvt_pk_bf16_f32 v10, v20, v21\n\t" \
  "v_cvt_pk_bf16_f32 v11, v22, v23\n\t" \
  "v_mfma_f32_16x16x32_bf16 a[104:107], a[176:179], v[144:147], a[104:107]\n\t" \
  "ds_read_b64_tr_b16 v[82:83], v236 offset:4096\n\t" \
  "v_accvgpr_read_b32 v24, a96\n\t" \
  "v_mfma_f32_16x16x32_bf16 a[104:107], a[180:183], v[148:151], a[104:107]\n\t" \
  "ds_read_b64_tr_b16 v[84:85], v236 offset:8192\n\t" \
  "v_accvgpr_read_b32 v25, a97\n\t" \
  "v_accvgpr_read_b32 v26, a98\n\t" \
  "v_mfma_f32_16x16x32_bf16 a[120:123], a[184:187], v[144:147], a[120:123]\n\t" \
  "v_exp_f32 v97, v97\n\t" \
  "v_mfma_f32_16x16x32_bf16 a[120:123], a[188:191], v[148:151], a[120:123]\n\t" \
  "v_accvgpr_read_b32 v27, a99\n\t" \
  "v_accvgpr_read_b32 v28, a112\n\t" \
  "v_accvgpr_read_b32 v29, a113\n\t" \
  "v_accvgpr_read_b32 v30, a114\n\t" \
  "v_accvgpr_read_b32 v31, a115\n\t" \
  "v_pk_mul_f32 v[24:25], v[24:25], v[208:209] op_sel_hi:[1,0]\n\t" \
  "v_pk_mul_f32 v[26:27], v[26:27], v[208:209] op_sel_hi:[1,0]\n\t" \
  "v_pk_mul_f32 v[28:29], v[28:29], v[208:209] op_sel_hi:[1,0]\n\t" \
  "v_pk_mul_f32 v[30:31], v[30:31], v[208:209] op_sel_hi:[1,0]\n\t" \
  "v_cvt_pk_bf16_f32 v12, v24, v25\n\t" \
  "v_mfma_f32_16x16x32_bf16 v[168:171], v[216:219], v[144:147], v[168:171]\n\t" \
  "v_exp_f32 v98, v98\n\t" \
  "v_mfma_f32_16x16x32_bf16 v[168:171], v[216:219], v[148:151], v[168:171]\n\t" \
  "s_xor_b32 s98, %[eso], 0\n\t" \
  "v_exp_f32 v99, v99\n\t" \
  STS("s_memrealtime s[94:95]\n\t") \
  STS("s_memtime s[92:93]\n\t") \
  "v_mfma_f32_16x16x32_bf16 a[4:7], a[192:195], v[136:139], a[4:7]\n\t" \
  "buffer_store_dwordx4 v[8:11], v242, %[ws], s98 offen offset:2048 sc1\n\t" \
  "v_cvt_pk_bf16_f32 v144, v64, v65\n\t" \
  "v_mfma_f32_16x16x32_bf16 a[4:7], a[196:199], v[140:143], a[4:7]\n\t" \
  "ds_read_b64_tr_b16 v[86:87], v236 offset:12288\n\t" \
  "v_cvt_pk_bf16_f32 v13, v26, v27\n\t" \
  "v_cvt_pk_bf16_f32 v14, v28, v29\n\t" \
  "v_cvt_pk_bf16_f32 v15, v30, v31\n\t" \
  "v_cvt_pk_bf16_f32 v145, v66, v67\n\t" \
  "v_mfma_f32_16x16x32_bf16 v[164:167], v[216:219], v[136:139], v[164:167]\n\t" \
  "v_exp_f32 v100, v100\n\t" \
  "v_cvt_pk_bf16_f32 v146, v68, v69\n\t" \
  "v_mfma_f32_16x16x32_bf16 v[164:167], v[216:219], v[140:143], v[164:167]\n\t" \
  "ds_read_b64_tr_b16 v[88:89], v236 offset:16384\n\t" \
  "v_cvt_pk_bf16_f32 v147, v70, v71\n\t" \
  "v_mfma_f32_16x16x32_bf16 a[20:23], a[200:203], v[136:139], a[20:23]\n\t" \
  "v_exp_f32 v101, v101\n\t" \
  "v_cvt_pk_bf16_f32 v148, v72, v73\n\t" \
  "v_mfma_f32_16x16x32_bf16 a[20:23], a[204:207], v[140:143], a[20:23]\n\t" \
  "v_exp_f32 v102, v102\n\t" \
  "v_cvt_pk_bf16_f32 v149, v74, v75\n\t" \
  "v_mfma_f32_16x16x32_bf16 a[36:39], a[208:211], v[136:139], a[36:39]\n\t" \
  "ds_read_b64_tr_b16 v[90:91], v236 offset:20480\n\t" \
  "v_cvt_pk_bf16_f32 v150, v76, v77\n\t" \
  "v_mfma_f32_16x16x32_bf16 a[36:39], a[212:215], v[140:143], a[36:39]\n\t" \
  "s_xor_b32 s98, %[eso], 0\n\t" \
  "v_exp_f32 v103, v103\n\t" \
  "v_cvt_pk_bf16_f32 v151, v78, v79\n\t" \
  "v_mfma_f32_16x16x32_bf16 a[52:55], a[216:219], v[136:139], a[52:55]\n\t" \
  "buffer_store_dwordx4 v[12:15], v242, %[ws], s98 offen offset:3072 sc1\n\t" \
  "v_mfma_f32_16x16x32_bf16 a[52:55], a[220:223], v[140:143], a[52:55]\n\t" \
  "ds_read_b64_tr_b16 v[92:93], v236 offset:24576\n\t" \
  "ds_read_b64_tr_b16 v[94:95], v236 offset:28672\n\t" \
  "v_mfma_f32_16x16x32_bf16 a[68:71], a[224:227], v[136:139], a[68:71]\n\t" \
  "v_rcp_f32 v210, v164\n\t" \
  "v_mfma_f32_16x16x32_bf16 a[68:71], a[228:231], v[140:143], a[68:71]\n\t" \
  "v_cmp_lt_f32 vcc, 0x5f800000, v164\n\t" \
  "s_cbranch_vccnz .Lat_x1\n\t" \
  ".Lat_y1:\n\t" \
  "v_accvgpr_read_b32 v16, a4\n\t" \
  "v_mfma_f32_16x16x32_bf16 a[84:87], a[232:235], v[136:139], a[84:87]\n\t" \
  STS("v_writelane_b32 %[stv], s94, 20\n\t") \
  STS("v_writelane_b32 %[stv], s92, 21\n\t") \
  "v_accvgpr_read_b32 v17, a5\n\t" \
  "v_accvgpr_read_b32 v18, a6\n\t" \
  "v_mfma_f32_16x16x32_bf16 a[84:87], a[236:239], v[140:143], a[84:87]\n\t" \
  "v_accvgpr_read_b32 v19, a7\n\t" \
  "v_accvgpr_read_b32 v20, a20\n\t" \
  "v_accvgpr_read_b32 v21, a21\n\t" \
  "v_accvgpr_read_b32 v22, a22\n\t" \
  "v_accvgpr_read_b32 v23, a23\n\t" \
  "v_pk_mul_f32 v[16:17], v[16:17], v[210:211] op_sel_hi:[1,0]\n\t" \
  "v_pk_mul_f32 v[18:19], v[18:19], v[210:211] op_sel_hi:[1,0]\n\t" \
  "v_pk_mul_f32 v[20:21], v[20:21], v[210:211] op_sel_hi:[1,0]\n\t" \
  "v_pk_mul_f32 v[22:23], v[22:23], v[210:211] op_sel_hi:[1,0]\n\t" \
  "v_cvt_pk_bf16_f32 v32, v16, v17\n\t" \
  "v_mfma_f32_16x16x32_bf16 a[100:103], a[240:243], v[136:139], a[100:103]\n\t" \
  "v_xor_b32 v235, 128, v234\n\t" \
  "v_cvt_pk_bf16_f32 v33, v18, v19\n\t" \
  "v_cvt_pk_bf16_f32 v34, v20, v21\n\t" \
  "v_cvt_pk_bf16_f32 v35, v22, v23\n\t" \
  "v_mfma_f32_16x16x32_bf16 a[100:103], a[244:247], v[140:143], a[100:103]\n\t" \
  "s_xor_b32 s98, %[eso], 32768\n\t" \
  "ds_read_b64_tr_b16 v[112:113], v235 offset:0\n\t" \
  "v_accvgpr_read_b32 v24, a36\n\t" \
  "v_mfma_f32_16x16x32_bf16 a[116:119], a[248:251], v[136:139], a[116:119]\n\t" \
  "buffer_store_dwordx4 v[32:35], v242, %[ws], s98 offen offset:0 sc1\n\t" \
  "v_accvgpr_read_b32 v25, a37\n\t" \
  "v_accvgpr_read_b32 v26, a38\n\t" \
  "v_mfma_f32_16x16x32_bf16 a[116:119], a[252:255], v[140:143], a[116:119]\n\t" \
  "v_exp_f32 v104, v104\n\t" \
  "v_mfma_f32_16x16x32_bf16 a[12:15], a[128:131], v[152:155], a[12:15]\n\t" \
  "v_accvgpr_read_b32 v27, a39\n\t" \
  "v_accvgpr_read_b32 v28, a52\n\t" \
  "v_accvgpr_read_b32 v29, a53\n\t" \
  "v_accvgpr_read_b32 v30, a54\n\t" \
  "v_accvgpr_read_b32 v31, a55\n\t" \
  "v_pk_mul_f32 v[24:25], v[24:25], v[210:211] op_sel_hi:[1,0]\n\t" \
  "v_pk_mul_f32 v[26:27], v[26:27], v[210:211] op_sel_hi:[1,0]\n\t" \
  "v_pk_mul_f32 v[28:29], v[28:29], v[210:211] op_sel_hi:[1,0]\n\t" \
  "v_pk_mul_f32 v[30:31], v[30:31], v[210:211] op_sel_hi:[1,0]\n\t" \
  "v_cvt_pk_bf16_f32 v36, v24, v25\n\t" \
  "v_mfma_f32_16x16x32_bf16 a[12:15], a[132:135], v[156:159], a[12:15]\n\t" \
  "ds_read_b64_tr_b16 v[114:115], v235 offset:4096\n\t" \
  "v_cvt_pk_bf16_f32 v37, v26, v27\n\t" \
  "v_mfma_f32_16x16x32_bf16 a[28:31], a[136:139], v[152:155], a[28:31]\n\t" \
  "v_exp_f32 v105, v105\n\t" \
  "v_mfma_f32_16x16x32_bf16 a[28:31], a[140:143], v[156:159], a[28:31]\n\t" \
  "ds_read_b64_tr_b16 v[116:117], v235 offset:8192\n\t" \
  "v_cvt_pk_bf16_f32 v38, v28, v29\n\t" \
  "v_mfma_f32_16x16x32_bf16 a[44:47], a[144:147], v[152:155], a[44:47]\n\t" \
  "ds_read_b64_tr_b16 v[118:119], v235 offset:12288\n\t" \
  "v_cvt_pk_bf16_f32 v39, v30, v31\n\t" \
  "v_mfma_f32_16x16x32_bf16 a[44:47], a[148:151], v[156:159], a[44:47]\n\t" \
  "s_xor_b32 s98, %[eso], 32768\n\t" \
  "ds_read_b64_tr_b16 v[120:121], v235 offset:16384\n\t" \
  "v_accvgpr_read_b32 v16, a68\n\t" \
  "v_accvgpr_read_b32 v17, a69\n\t" \
  "v_accvgpr_read_b32 v18, a70\n\t" \
  "v_mfma_f32_16x16x32_bf16 a[60:63], a[152:155], v[152:155], a[60:63]\n\t" \
  "buffer_store_dwordx4 v[36:39], v242, %[ws], s98 offen offset:1024 sc1\n\t" \
  "v_mfma_f32_16x16x32_bf16 a[60:63], a[156:159], v[156:159], a[60:63]\n\t" \
  "v_accvgpr_read_b32 v19, a71\n\t" \
  "v_accvgpr_read_b32 v20, a84\n\t" \
  "v_accvgpr_read_b32 v21, a85\n\t" \
  "v_accvgpr_read_b32 v22, a86\n\t" \
  "v_accvgpr_read_b32 v23, a87\n\t" \
  "v_pk_mul_f32 v[16:17], v[16:17], v[210:211] op_sel_hi:[1,0]\n\t" \
  "v_pk_mul_f32 v[18:19], v[18:19], v[210:211] op_sel_hi:[1,0]\n\t" \
  "v_pk_mul_f32 v[20:21], v[20:21], v[210:211] op_sel_hi:[1,0]\n\t" \
  "v_pk_mul_f32 v[22:23], v[22:23], v[210:211] op_sel_hi:[1,0]\n\t" \
  "v_cvt_pk_bf16_f32 v40, v16, v17\n\t" \
  "v_mfma_f32_16x16x32_bf16 a[76:79], a[160:163], v[152:155], a[76:79]\n\t" \
  "v_exp_f32 v106, v106\n\t" \
  "v_mfma_f32_16x16x32_bf16 a[76:79], a[164:167], v[156:159], a[76:79]\n\t" \
  "v_exp_f32 v107, v107\n\t" \
  "v_mfma_f32_16x16x32_bf16 a[92:95], a[168:171], v[152:155], a[92:95]\n\t" \
  "ds_read_b64_tr_b16 v[122:123], v235 offset:20480\n\t" \
  "v_cvt_pk_bf16_f32 v41, v18, v19\n\t" \
  "v_mfma_f32_16x16x32_bf16 a[92:95], a[172:175], v[156:159], a[92:95]\n\t" \
  "ds_read_b64_tr_b16 v[124:125], v235 offset:24576\n\t" \
  "v_cvt_pk_bf16_f32 v42, v20, v21\n\t" \
  "v_cvt_pk_bf16_f32 v43, v22, v23\n\t" \
  "v_mfma_f32_16x16x32_bf16 a[108:111], a[176:179], v[152:155], a[108:111]\n\t" \
  "ds_read_b64_tr_b16 v[126:127], v235 offset:28672\n\t" \
  "v_accvgpr_read_b32 v24, a100\n\t" \
  "v_mfma_f32_16x16x32_bf16 a[108:111], a[180:183], v[156:159], a[108:111]\n\t" \
  "s_xor_b32 s98, %[eso], 32768\n\t" \
  "v_accvgpr_read_b32 v25, a101\n\t" \
  "v_accvgpr_read_b32 v26, a102\n\t" \
  "v_mfma_f32_16x16x32_bf16 a[124:127], a[184:187], v[152:155], a[124:127]\n\t" \
  "buffer_store_dwordx4 v[40:43], v242, %[ws], s98 offen offset:2048 sc1\n\t" \
  "v_mfma_f32_16x16x32_bf16 a[124:127], a[188:191], v[156:159], a[124:127]\n\t" \
  "v_accvgpr_read_b32 v27, a103\n\t" \
  "v_accvgpr_read_b32 v28, a116\n\t" \
  "v_accvgpr_read_b32 v29, a117\n\t" \
  "v_accvgpr_read_b32 v30, a118\n\t" \
  "v_accvgpr_read_b32 v31, a119\n\t" \
  "v_pk_mul_f32 v[24:25], v[24:25], v[210:211] op_sel_hi:[1,0]\n\t" \
  "v_pk_mul_f32 v[26:27], v[26:27], v[210:211] op_sel_hi:[1,0]\n\t" \
  "v_pk_mul_f32 v[28:29], v[28:29], v[210:211] op_sel_hi:[1,0]\n\t" \
  "v_pk_mul_f32 v[30:31], v[30:31], v[210:211] op_sel_hi:[1,0]\n\t" \
  "v_cvt_pk_bf16_f32 v44, v24, v25\n\t" \
  "v_mfma_f32_16x16x32_bf16 v[172:175], v[216:219], v[152:155], v[172:175]\n\t" \
  "v_exp_f32 v108, v108\n\t" \
  "v_mfma_f32_16x16x32_bf16 v[172:175], v[216:219], v[156:159], v[172:175]\n\t" \
  "ds_read_b64_tr_b16 a[128:129], v234 offset:0\n\t" \
  "v_cvt_pk_bf16_f32 v152, v96, v97\n\t" \
  "v_mfma_f32_16x16x32_bf16 a[8:11], a[192:195], v[144:147], a[8:11]\n\t" \
  "ds_read_b64_tr_b16 a[130:131], v234 offset:4096\n\t" \
  "v_cvt_pk_bf16_f32 v153, v98, v99\n\t" \
  "v_mfma_f32_16x16x32_bf16 a[8:11], a[196:199], v[148:151], a[8:11]\n\t" \
  "ds_read_b64_tr_b16 a[132:133], v234 offset:8192\n\t" \
  "v_cvt_pk_bf16_f32 v45, v26, v27\n\t" \
  "v_cvt_pk_bf16_f32 v46, v28, v29\n\t" \
  "v_cvt_pk_bf16_f32 v47, v30, v31\n\t" \
  "v_cvt_pk_bf16_f32 v154, v100, v101\n\t" \
  "v_mfma_f32_16x16x32_bf16 v[168:171], v[216:219], v[144:147], v[168:171]\n\t" \
  "v_exp_f32 v109, v109\n\t" \
  "v_cvt_pk_bf16_f32 v155, v102, v103\n\t" \
  "v_mfma_f32_16x16x32_bf16 v[168:171], v[216:219], v[148:151], v[168:171]\n\t" \
  "s_xor_b32 s98, %[eso], 32768\n\t" \
  "ds_read_b64_tr_b16 a[134:135], v234 offset:12288\n\t" \
  "v_cvt_pk_bf16_f32 v156, v104, v105\n\t" \
  "v_mfma_f32_16x16x32_bf16 a[24:27], a[200:203], v[144:147], a[24:27]\n\t" \
  "buffer_store_dwordx4 v[44:47], v242, %[ws], s98 offen offset:3072 sc1\n\t" \
  "v_cvt_pk_bf16_f32 v157, v106, v107\n\t" \
  "v_mfma_f32_16x16x32_bf16 a[24:27], a[204:207], v[148:151], a[24:27]\n\t" \
  "ds_read_b64_tr_b16 a[136:137], v234 offset:16384\n\t" \
  "v_cvt_pk_bf16_f32 v158, v108, v109\n\t" \
  "v_mfma_f32_16x16x32_bf16 a[40:43], a[208:211], v[144:147], a[40:43]\n\t" \
  "v_exp_f32 v110, v110\n\t" \
  "v_mfma_f32_16x16x32_bf16 a[40:43], a[212:215], v[148:151], a[40:43]\n\t" \
  "ds_read_b64_tr_b16 a[138:139], v234 offset:20480\n\t" \
  "ds_read_b64_tr_b16 a[140:141], v234 offset:24576\n\t" \
  "v_mfma_f32_16x16x32_bf16 a[56:59], a[216:219], v[144:147], a[56:59]\n\t" \
  "v_exp_f32 v111, v111\n\t" \
  "v_mfma_f32_16x16x32_bf16 a[56:59], a[220:223], v[148:151], a[56:59]\n\t" \
  "ds_read_b64_tr_b16 a[142:143], v234 offset:28672\n\t" \
  "v_xor_b32 v236, 32, v234\n\t" \
  "v_mfma_f32_16x16x32_bf16 a[72:75], a[224:227], v[144:147], a[72:75]\n\t" \
  "ds_read_b64_tr_b16 a[144:145], v236 offset:0\n\t" \
  "v_cvt_pk_bf16_f32 v159, v110, v111\n\t" \
  "v_mfma_f32_16x16x32_bf16 a[72:75], a[228:231], v[148:151], a[72:75]\n\t" \
  "ds_read_b64_tr_b16 a[146:147], v236 offset:4096\n\t" \
  "ds_read_b64_tr_b16 a[148:149], v236 offset:8192\n\t" \
  "v_mfma_f32_16x16x32_bf16 a[88:91], a[232:235], v[144:147], a[88:91]\n\t" \
  "v_rcp_f32 v212, v168\n\t" \
  "v_mfma_f32_16x16x32_bf16 a[88:91], a[236:239], v[148:151], a[88:91]\n\t" \
  "ds_read_b64_tr_b16 a[150:151], v236 offset:12288\n\t" \
  "ds_read_b64_tr_b16 a[152:153], v236 offset:16384\n\t" \
  "v_mfma_f32_16x16x32_bf16 a[104:107], a[240:243], v[144:147], a[104:107]\n\t" \
  "v_cmp_lt_f32 vcc, 0x5f800000, v168\n\t" \
  "s_cbranch_vccnz .Lat_x2\n\t" \
  ".Lat_y2:\n\t" \
  "v_accvgpr_read_b32 v16, a8\n\t" \
  "v_mfma_f32_16x16x32_bf16 a[104:107], a[244:247], v[148:151], a[104:107]\n\t" \
  "ds_read_b64_tr_b16 a[154:155], v236 offset:20480\n\t" \
  "v_accvgpr_read_b32 v17, a9\n\t" \
  "v_accvgpr_read_b32 v18, a10\n\t" \
  "v_mfma_f32_16x16x32_bf16 a[120:123], a[248:251], v[144:147], a[120:123]\n\t" \
  "v_accvgpr_read_b32 v19, a11\n\t" \
  "v_accvgpr_read_b32 v20, a24\n\t" \
  "v_accvgpr_read_b32 v21, a25\n\t" \
  "v_accvgpr_read_b32 v22, a26\n\t" \
  "v_accvgpr_read_b32 v23, a27\n\t" \
  "v_pk_mul_f32 v[16:17], v[16:17], v[212:213] op_sel_hi:[1,0]\n\t" \
  "v_pk_mul_f32 v[18:19], v[18:19], v[212:213] op_sel_hi:[1,0]\n\t" \
  "v_pk_mul_f32 v[20:21], v[20:21], v[212:213] op_sel_hi:[1,0]\n\t" \
  "v_pk_mul_f32 v[22:23], v[22:23], v[212:213] op_sel_hi:[1,0]\n\t" \
  "v_cvt_pk_bf16_f32 v64, v16, v17\n\t" \
  "v_mfma_f32_16x16x32_bf16 a[120:123], a[252:255], v[148:151], a[120:123]\n\t" \
  "ds_read_b64_tr_b16 a[156:157], v236 offset:24576\n\t" \
  "v_cvt_pk_bf16_f32 v65, v18, v19\n\t" \
  "v_cvt_pk_bf16_f32 v66, v20, v21\n\t" \
  "v_cvt_pk_bf16_f32 v67, v22, v23\n\t" \
  "v_accvgpr_read_b32 v24, a40\n\t" \
  "v_mfma_f32_16x16x32_bf16 a[12:15], a[192:195], v[152:155], a[12:15]\n\t" \
  "s_xor_b32 s98, %[eso], 65536\n\t" \
  "ds_read_b64_tr_b16 a[158:159], v236 offset:28672\n\t" \
  "v_accvgpr_read_b32 v25, a41\n\t" \
  "v_accvgpr_read_b32 v26, a42\n\t" \
  "v_mfma_f32_16x16x32_bf16 a[12:15], a[196:199], v[156:159], a[12:15]\n\t" \
  "buffer_store_dwordx4 v[64:67], v242, %[ws], s98 offen offset:0 sc1\n\t" \
  "v_mfma_f32_16x16x32_bf16 v[172:175], v[216:219], v[152:155], v[172:175]\n\t" \
  "v_accvgpr_read_b32 v27, a43\n\t" \
  "v_accvgpr_read_b32 v28, a56\n\t" \
  "v_accvgpr_read_b32 v29, a57\n\t" \
  "v_accvgpr_read_b32 v30, a58\n\t" \
  "v_accvgpr_read_b32 v31, a59\n\t" \
  "v_pk_mul_f32 v[24:25], v[24:25], v[212:213] op_sel_hi:[1,0]\n\t" \
  "v_pk_mul_f32 v[26:27], v[26:27], v[212:213] op_sel_hi:[1,0]\n\t" \
  "v_pk_mul_f32 v[28:29], v[28:29], v[212:213] op_sel_hi:[1,0]\n\t" \
  "v_pk_mul_f32 v[30:31], v[30:31], v[212:213] op_sel_hi:[1,0]\n\t" \
  "v_cvt_pk_bf16_f32 v68, v24, v25\n\t" \
  "v_mfma_f32_16x16x32_bf16 v[172:175], v[216:219], v[156:159], v[172:175]\n\t" \
  "v_xor_b32 v235, 192, v234\n\t" \
  "v_cvt_pk_bf16_f32 v69, v26, v27\n\t" \
  "v_mfma_f32_16x16x32_bf16 a[28:31], a[200:203], v[152:155], a[28:31]\n\t" \
  "ds_read_b64_tr_b16 a[160:161], v235 offset:0\n\t" \
  "v_cvt_pk_bf16_f32 v70, v28, v29\n\t" \
  ".Lhs10:\n\ts_cbranch_execz .Lhs10n\n\t" \
  "v_cvt_pk_bf16_f32 v71, v30, v31\n\t" \
  "v_mfma_f32_16x16x32_bf16 a[28:31], a[204:207], v[156:159], a[28:31]\n\t" \
  "s_xor_b32 s98, %[eso], 65536\n\t" \
  "ds_read_b64_tr_b16 a[162:163], v235 offset:4096\n\t" \
  "v_accvgpr_read_b32 v16, a72\n\t" \
  "v_accvgpr_read_b32 v17, a73\n\t" \
  "v_accvgpr_read_b32 v18, a74\n\t" \
  "v_mfma_f32_16x16x32_bf16 a[44:47], a[208:211], v[152:155], a[44:47]\n\t" \
  "buffer_store_dwordx4 v[68:71], v242, %[ws], s98 offen offset:1024 sc1\n\t" \
  "v_mfma_f32_16x16x32_bf16 a[44:47], a[212:215], v[156:159], a[44:47]\n\t" \
  "v_accvgpr_read_b32 v19, a75\n\t" \
  "v_accvgpr_read_b32 v20, a88\n\t" \
  "v_accvgpr_read_b32 v21, a89\n\t" \
  "v_accvgpr_read_b32 v22, a90\n\t" \
  "v_accvgpr_read_b32 v23, a91\n\t" \
  "v_pk_mul_f32 v[16:17], v[16:17], v[212:213] op_sel_hi:[1,0]\n\t" \
  "v_pk_mul_f32 v[18:19], v[18:19], v[212:213] op_sel_hi:[1,0]\n\t" \
  "v_pk_mul_f32 v[20:21], v[20:21], v[212:213] op_sel_hi:[1,0]\n\t" \
  "v_pk_mul_f32 v[22:23], v[22:23], v[212:213] op_sel_hi:[1,0]\n\t" \
  "v_cvt_pk_bf16_f32 v72, v16, v17\n\t" \
  "v_mfma_f32_16x16x32_bf16 a[60:63], a[216:219], v[152:155], a[60:63]\n\t" \
  "ds_read_b64_tr_b16 a[164:165], v235 offset:8192\n\t" \
  "v_cvt_pk_bf16_f32 v73, v18, v19\n\t" \
  "v_mfma_f32_16x16x32_bf16 a[60:63], a[220:223], v[156:159], a[60:63]\n\t" \
  "ds_read_b64_tr_b16 a[166:167], v235 offset:12288\n\t" \
  "v_cvt_pk_bf16_f32 v74, v20, v21\n\t" \
  "v_mfma_f32_16x16x32_bf16 a[76:79], a[224:227], v[152:155], a[76:79]\n\t" \
  "ds_read_b64_tr_b16 a[168:169], v235 offset:16384\n\t" \
  "v_cvt_pk_bf16_f32 v75, v22, v23\n\t" \
  "v_mfma_f32_16x16x32_bf16 a[76:79], a[228:231], v[156:159], a[76:79]\n\t" \
  "s_xor_b32 s98, %[eso], 65536\n\t" \
  "ds_read_b64_tr_b16 a[170:171], v235 offset:20480\n\t" \
  "v_accvgpr_read_b32 v24, a104\n\t" \
  "v_mfma_f32_16x16x32_bf16 a[92:95], a[232:235], v[152:155], a[92:95]\n\t" \
  "buffer_store_dwordx4 v[72:75], v242, %[ws], s98 offen offset:2048 sc1\n\t" \
  "v_mfma_f32_16x16x32_bf16 a[92:95], a[236:239], v[156:159], a[92:95]\n\t" \
  "ds_read_b64_tr_b16 a[172:173], v235 offset:24576\n\t" \
  "v_accvgpr_read_b32 v25, a105\n\t" \
  "v_accvgpr_read_b32 v26, a106\n\t" \
  "v_mfma_f32_16x16x32_bf16 a[108:111], a[240:243], v[152:155], a[108:111]\n\t" \
  "ds_read_b64_tr_b16 a[174:175], v235 offset:28672\n\t" \
  "v_xor_b32 v236, 224, v234\n\t" \
  "v_mfma_f32_16x16x32_bf16 a[108:111], a[244:247], v[156:159], a[108:111]\n\t" \
  "v_accvgpr_read_b32 v27, a107\n\t" \
  "v_accvgpr_read_b32 v28, a120\n\t" \
  "v_accvgpr_read_b32 v29, a121\n\t" \
  "v_accvgpr_read_b32 v30, a122\n\t" \
  "v_accvgpr_read_b32 v31, a123\n\t" \
  "v_pk_mul_f32 v[24:25], v[24:25], v[212:213] op_sel_hi:[1,0]\n\t" \
  "v_pk_mul_f32 v[26:27], v[26:27], v[212:213] op_sel_hi:[1,0]\n\t" \
  "v_pk_mul_f32 v[28:29], v[28:29], v[212:213] op_sel_hi:[1,0]\n\t" \
  "v_pk_mul_f32 v[30:31], v[30:31], v[212:213] op_sel_hi:[1,0]\n\t" \
  "v_cvt_pk_bf16_f32 v76, v24, v25\n\t" \
  "v_mfma_f32_16x16x32_bf16 a[124:127], a[248:251], v[152:155], a[124:127]\n\t" \
  "ds_read_b64_tr_b16 a[176:177], v236 offset:0\n\t" \
  "v_cvt_pk_bf16_f32 v77, v26, v27\n\t" \
  "v_mfma_f32_16x16x32_bf16 a[124:127], a[252:255], v[156:159], a[124:127]\n\t" \
  "s_waitcnt lgkmcnt(15)\n\t" \
  "ds_read_b64_tr_b16 a[178:179], v236 offset:4096\n\t" \
  "v_cvt_pk_bf16_f32 v78, v28, v29\n\t" \
  "v_mfma_f32_16x16x32_bf16 a[32:35], v[48:51], v[0:3], 0\n\t" \
  "ds_read_b64_tr_b16 a[180:181], v236 offset:8192\n\t" \
  "v_cvt_pk_bf16_f32 v79, v30, v31\n\t" \
  "v_mfma_f32_16x16x32_bf16 a[32:35], v[52:55], v[4:7], a[32:35]\n\t" \
  "s_xor_b32 s98, %[eso], 65536\n\t" \
  "ds_read_b64_tr_b16 a[182:183], v236 offset:12288\n\t" \
  "ds_read_b64_tr_b16 a[184:185], v236 offset:16384\n\t" \
  "v_mfma_f32_16x16x32_bf16 a[32:35], v[56:59], v[8:11], a[32:35]\n\t" \
  "buffer_store_dwordx4 v[76:79], v242, %[ws], s98 offen offset:3072 sc1\n\t" \
  "v_mfma_f32_16x16x32_bf16 a[32:35], v[60:63], v[12:15], a[32:35]\n\t" \
  "ds_read_b64_tr_b16 a[186:187], v236 offset:20480\n\t" \
  "ds_read_b64_tr_b16 a[188:189], v236 offset:24576\n\t" \
  "v_mfma_f32_16x16x32_bf16 a[48:51], v[92:95], v[12:15], 0\n\t" \
  "ds_read_b64_tr_b16 a[190:191], v236 offset:28672\n\t" \
  "v_xor_b32 v236, 160, v234\n\t" \
  "v_mfma_f32_16x16x32_bf16 a[48:51], v[88:91], v[8:11], a[48:51]\n\t" \
  "ds_read_b64_tr_b16 v[96:97], v236 offset:0\n\t" \
  "ds_read_b64_tr_b16 v[98:99], v236 offset:4096\n\t" \
  "v_mfma_f32_16x16x32_bf16 a[48:51], v[84:87], v[4:7], a[48:51]\n\t" \
  "v_rcp_f32 v214, v172\n\t" \
  "v_mfma_f32_16x16x32_bf16 a[48:51], v[80:83], v[0:3], a[48:51]\n\t" \
  "v_cmp_lt_f32 vcc, 0x5f800000, v172\n\t" \
  "s_cbranch_vccnz .Lat_x3\n\t" \
  ".Lat_y3:\n\t" \
  "v_mfma_f32_16x16x32_bf16 a[64:67], v[112:115], v[0:3], 0\n\t" \
  "ds_read_b64_tr_b16 v[100:101], v236 offset:8192\n\t" \
  "v_accvgpr_read_b32 v16, a12\n\t" \
  "v_mfma_f32_16x16x32_bf16 a[64:67], v[116:119], v[4:7], a[64:67]\n\t" \
  "ds_read_b64_tr_b16 v[102:103], v236 offset:12288\n\t" \
  "v_accvgpr_read_b32 v17, a13\n\t" \
  "v_mfma_f32_16x16x32_bf16 a[64:67], v[120:123], v[8:11], a[64:67]\n\t" \
  "ds_read_b64_tr_b16 v[104:105], v236 offset:16384\n\t" \
  "v_accvgpr_read_b32 v18, a14\n\t" \
  "v_mfma_f32_16x16x32_bf16 a[64:67], v[124:127], v[12:15], a[64:67]\n\t" \
  "ds_read_b64_tr_b16 v[106:107], v236 offset:20480\n\t" \
  "v_accvgpr_read_b32 v19, a15\n\t" \
  "v_mfma_f32_16x16x32_bf16 a[0:3], a[140:143], v[12:15], 0\n\t" \
  "ds_read_b64_tr_b16 v[108:109], v236 offset:24576\n\t" \
  "v_accvgpr_read_b32 v20, a28\n\t" \
  "v_mfma_f32_16x16x32_bf16 a[0:3], a[136:139], v[8:11], a[0:3]\n\t" \
  "ds_read_b64_tr_b16 v[110:111], v236 offset:28672\n\t" \
  "v_accvgpr_read_b32 v21, a29\n\t" \
  "v_mfma_f32_16x16x32_bf16 a[0:3], a[132:135], v[4:7], a[0:3]\n\t" \
  "ds_read_b128 a[224:227], v213 offset:32768\n\t" \
  "v_accvgpr_read_b32 v22, a30\n\t" \
  "v_mfma_f32_16x16x32_bf16 a[0:3], a[128:131], v[0:3], a[0:3]\n\t" \
  "ds_read_b128 a[228:231], v213 offset:33792\n\t" \
  "v_accvgpr_read_b32 v23, a31\n\t" \
  "v_mfma_f32_16x16x32_bf16 a[16:19], a[144:147], v[0:3], 0\n\t" \
  "s_waitcnt lgkmcnt(15)\n\t" \
  "ds_read_b128 a[232:235], v213 offset:34816\n\t" \
  "v_mul_f32 v16, v16, v214\n\t" \
  "v_mfma_f32_16x16x32_bf16 a[16:19], a[148:151], v[4:7], a[16:19]\n\t" \
  "ds_read_b128 a[236:239], v213 offset:35840\n\t" \
  "v_mul_f32 v17, v17, v214\n\t" \
  "v_mfma_f32_16x16x32_bf16 a[16:19], a[152:155], v[8:11], a[16:19]\n\t" \
  "ds_read_b128 a[240:243], v215 offset:0\n\t" \
  "v_mul_f32 v18, v18, v214\n\t" \
  "v_mfma_f32_16x16x32_bf16 a[16:19], a[156:159], v[12:15], a[16:19]\n\t" \
  "ds_read_b128 a[244:247], v215 offset:1024\n\t" \
  "v_mul_f32 v19, v19, v214\n\t" \
  "v_mfma_f32_16x16x32_bf16 a[96:99], a[172:175], v[12:15], 0\n\t" \
  "buffer_load_dwordx4 v[224:227], v252, %[ws], %[ss] offen sc1\n\t" \
  "v_mfma_f32_16x16x32_bf16 a[96:99], a[168:171], v[8:11], a[96:99]\n\t" \
  "ds_read_b128 a[248:251], v215 offset:2048\n\t" \
  "v_mul_f32 v20, v20, v214\n\t" \
  "v_mfma_f32_16x16x32_bf16 a[96:99], a[164:167], v[4:7], a[96:99]\n\t" \
  "ds_read_b128 a[252:255], v215 offset:3072\n\t" \
  "v_mul_f32 v21, v21, v214\n\t" \
  "v_mfma_f32_16x16x32_bf16 a[96:99], a[160:163], v[0:3], a[96:99]\n\t" \
  "v_mul_f32 v22, v22, v214\n\t" \
  "v_mul_f32 v23, v23, v214\n\t" \
  "v_mfma_f32_16x16x32_bf16 a[112:115], a[176:179], v[0:3], 0\n\t" \
  "s_waitcnt lgkmcnt(15)\n\t" \
  "buffer_load_dwordx4 v[128:131], v247, %[sM], %[gvo] offen\n\t" \
  "v_mfma_f32_16x16x32_bf16 a[112:115], a[180:183], v[4:7], a[112:115]\n\t" \
  "s_add_u32 s98, %[gvo], 12288\n\t" \
  "v_cvt_pk_bf16_f32 v160, v16, v17\n\t" \
  "v_cvt_pk_bf16_f32 v161, v18, v19\n\t" \
  "v_mfma_f32_16x16x32_bf16 a[112:115], a[184:187], v[8:11], a[112:115]\n\t" \
  "v_cvt_pk_bf16_f32 v162, v20, v21\n\t" \
  "v_cvt_pk_bf16_f32 v163, v22, v23\n\t" \
  "v_mfma_f32_16x16x32_bf16 a[112:115], a[188:191], v[12:15], a[112:115]\n\t" \
  "s_waitcnt lgkmcnt(8)\n\t" \
  "s_waitcnt lgkmcnt(4)\n\t" \
  "ds_write_b128 v213, v[160:163] offset:32768\n\t" \
  "v_mfma_f32_16x16x32_bf16 a[80:83], v[108:111], v[12:15], 0\n\t" \
  "buffer_load_dwordx4 v[132:135], v247, %[sM], s98 offen\n\t" \
  "v_mfma_f32_16x16x32_bf16 a[80:83], v[104:107], v[8:11], a[80:83]\n\t" \
  "s_add_u32 s98, %[gvo], 24576\n\t" \
  "v_accvgpr_read_b32 v24, a44\n\t" \
  "v_accvgpr_read_b32 v25, a45\n\t" \
  "v_mfma_f32_16x16x32_bf16 a[80:83], v[100:103], v[4:7], a[80:83]\n\t" \
  "v_accvgpr_read_b32 v26, a46\n\t" \
  "v_accvgpr_read_b32 v27, a47\n\t" \
  "v_mfma_f32_16x16x32_bf16 a[80:83], v[96:99], v[0:3], a[80:83]\n\t" \
  "v_accvgpr_read_b32 v28, a60\n\t" \
  "v_accvgpr_read_b32 v29, a61\n\t" \
  "v_mfma_f32_16x16x32_bf16 a[36:39], v[48:51], v[32:35], 0\n\t" \
  "buffer_load_dwordx4 v[136:139], v247, %[sM], s98 offen\n\t" \
  "v_mfma_f32_16x16x32_bf16 a[36:39], v[52:55], v[36:39], a[36:39]\n\t" \
  "s_add_u32 s98, %[gvo], 36864\n\t" \
  "v_accvgpr_read_b32 v30, a62\n\t" \
  "v_accvgpr_read_b32 v31, a63\n\t" \
  "v_mfma_f32_16x16x32_bf16 a[36:39], v[56:59], v[40:43], a[36:39]\n\t" \
  "v_mul_f32 v24, v24, v214\n\t" \
  "v_mul_f32 v25, v25, v214\n\t" \
  "v_mfma_f32_16x16x32_bf16 a[36:39], v[60:63], v[44:47], a[36:39]\n\t" \
  "v_mul_f32 v26, v26, v214\n\t" \
  "v_mul_f32 v27, v27, v214\n\t" \
  "s_waitcnt vmcnt(8)\n\t" \
  "buffer_store_dword v243, v254, %[ws], %[fw2] offen offset:0 sc1\n\t" \
  "v_mfma_f32_16x16x32_bf16 a[52:55], v[92:95], v[44:47], 0\n\t" \
  "buffer_load_dwordx4 v[140:143], v247, %[sM], s98 offen\n\t" \
  "v_mfma_f32_16x16x32_bf16 a[52:55], v[88:91], v[40:43], a[52:55]\n\t" \
  "s_add_u32 s98, %[gvo], 49152\n\t" \
  "v_mul_f32 v28, v28, v214\n\t" \
  "v_mul_f32 v29, v29, v214\n\t" \
  "v_mfma_f32_16x16x32_bf16 a[52:55], v[84:87], v[36:39], a[52:55]\n\t" \
  "v_mul_f32 v30, v30, v214\n\t" \
  "v_mul_f32 v31, v31, v214\n\t" \
  "v_mfma_f32_16x16x32_bf16 a[52:55], v[80:83], v[32:35], a[52:55]\n\t" \
  "v_cvt_pk_bf16_f32 v164, v24, v25\n\t" \
  "v_cvt_pk_bf16_f32 v165, v26, v27\n\t" \
  "v_mfma_f32_16x16x32_bf16 a[68:71], v[112:115], v[32:35], 0\n\t" \
  "buffer_load_dwordx4 v[144:147], v247, %[sM], s98 offen\n\t" \
  "v_mfma_f32_16x16x32_bf16 a[68:71], v[116:119], v[36:39], a[68:71]\n\t" \
  "s_add_u32 s98, %[gvo], 61440\n\t" \
  "v_cvt_pk_bf16_f32 v166, v28, v29\n\t" \
  "v_cvt_pk_bf16_f32 v167, v30, v31\n\t" \
  "v_mfma_f32_16x16x32_bf16 a[68:71], v[120:123], v[40:43], a[68:71]\n\t" \
  "ds_write_b128 v213, v[164:167] offset:33792\n\t" \
  "v_mfma_f32_16x16x32_bf16 a[68:71], v[124:127], v[44:47], a[68:71]\n\t" \
  "v_accvgpr_read_b32 v16, a76\n\t" \
  "v_accvgpr_read_b32 v17, a77\n\t" \
  "v_mfma_f32_16x16x32_bf16 a[4:7], a[140:143], v[44:47], 0\n\t" \
  "buffer_load_dwordx4 v[148:151], v247, %[sM], s98 offen\n\t" \
  "v_mfma_f32_16x16x32_bf16 a[4:7], a[136:139], v[40:43], a[4:7]\n\t" \
  "s_add_u32 s98, %[gvo], 73728\n\t" \
  "v_accvgpr_read_b32 v18, a78\n\t" \
  "v_accvgpr_read_b32 v19, a79\n\t" \
  "v_mfma_f32_16x16x32_bf16 a[4:7], a[132:135], v[36:39], a[4:7]\n\t" \
  "v_accvgpr_read_b32 v20, a92\n\t" \
  "v_accvgpr_read_b32 v21, a93\n\t" \
  "v_mfma_f32_16x16x32_bf16 a[4:7], a[128:131], v[32:35], a[4:7]\n\t" \
  "v_accvgpr_read_b32 v22, a94\n\t" \
  "v_accvgpr_read_b32 v23, a95\n\t" \
  "v_mfma_f32_16x16x32_bf16 a[20:23], a[144:147], v[32:35], 0\n\t" \
  "buffer_load_dwordx4 v[152:155], v247, %[sM], s98 offen\n\t" \
  "v_mfma_f32_16x16x32_bf16 a[20:23], a[148:151], v[36:39], a[20:23]\n\t" \
  "s_add_u32 s98, %[gvo], 86016\n\t" \
  "v_mul_f32 v16, v16, v214\n\t" \
  "v_mul_f32 v17, v17, v214\n\t" \
  "v_mfma_f32_16x16x32_bf16 a[20:23], a[152:155], v[40:43], a[20:23]\n\t" \
  "v_mul_f32 v18, v18, v214\n\t" \
  "v_mul_f32 v19, v19, v214\n\t" \
  "v_mfma_f32_16x16x32_bf16 a[20:23], a[156:159], v[44:47], a[20:23]\n\t" \
  "v_mul_f32 v20, v20, v214\n\t" \
  "v_mul_f32 v21, v21, v214\n\t" \
  "v_mfma_f32_16x16x32_bf16 a[100:103], a[172:175], v[44:47], 0\n\t" \
  "buffer_load_dwordx4 v[156:159], v247, %[sM], s98 offen\n\t" \
  "v_mfma_f32_16x16x32_bf16 a[100:103], a[168:171], v[40:43], a[100:103]\n\t" \
  "v_mul_f32 v22, v22, v214\n\t" \
  "v_mul_f32 v23, v23, v214\n\t" \
  "v_mfma_f32_16x16x32_bf16 a[100:103], a[164:167], v[36:39], a[100:103]\n\t" \
  "v_cvt_pk_bf16_f32 v168, v16, v17\n\t" \
  "v_cvt_pk_bf16_f32 v169, v18, v19\n\t" \
  "v_mfma_f32_16x16x32_bf16 a[100:103], a[160:163], v[32:35], a[100:103]\n\t" \
  "v_cvt_pk_bf16_f32 v170, v20, v21\n\t" \
  "v_cvt_pk_bf16_f32 v171, v22, v23\n\t" \
  "v_mfma_f32_16x16x32_bf16 a[116:119], a[176:179], v[32:35], 0\n\t" \
  "ds_write_b128 v213, v[168:171] offset:34816\n\t" \
  "v_mfma_f32_16x16x32_bf16 a[116:119], a[180:183], v[36:39], a[116:119]\n\t" \
  "v_accvgpr_read_b32 v24, a108\n\t" \
  "v_accvgpr_read_b32 v25, a109\n\t" \
  "v_mfma_f32_16x16x32_bf16 a[116:119], a[184:187], v[40:43], a[116:119]\n\t" \
  "v_accvgpr_read_b32 v26, a110\n\t" \
  "v_accvgpr_read_b32 v27, a111\n\t" \
  "v_mfma_f32_16x16x32_bf16 a[116:119], a[188:191], v[44:47], a[116:119]\n\t" \
  "v_accvgpr_read_b32 v28, a124\n\t" \
  "v_accvgpr_read_b32 v29, a125\n\t" \
  "v_mfma_f32_16x16x32_bf16 a[84:87], v[108:111], v[44:47], 0\n\t" \
  "s_add_u32 m0, %[m], 147456\n\t" \
  "v_accvgpr_read_b32 v30, a126\n\t" \
  "v_accvgpr_read_b32 v31, a127\n\t" \
  "v_mfma_f32_16x16x32_bf16 a[84:87], v[104:107], v[40:43], a[84:87]\n\t" \
  "s_xor_b32 s97, %[wob], 655360\n\t" \
  "v_mul_f32 v24, v24, v214\n\t" \
  "v_mul_f32 v25, v25, v214\n\t" \
  "v_mfma_f32_16x16x32_bf16 a[84:87], v[100:103], v[36:39], a[84:87]\n\t" \
  "v_mul_f32 v26, v26, v214\n\t" \
  "v_mul_f32 v27, v27, v214\n\t" \
  "v_mfma_f32_16x16x32_bf16 a[84:87], v[96:99], v[32:35], a[84:87]\n\t" \
  "v_mul_f32 v28, v28, v214\n\t" \
  "v_mul_f32 v29, v29, v214\n\t" \
  "v_mfma_f32_16x16x32_bf16 a[40:43], v[48:51], v[64:67], 0\n\t" \
  "v_mul_f32 v30, v30, v214\n\t" \
  "v_mul_f32 v31, v31, v214\n\t" \
  "v_mfma_f32_16x16x32_bf16 a[40:43], v[52:55], v[68:71], a[40:43]\n\t" \
  "v_cvt_pk_bf16_f32 v172, v24, v25\n\t" \
  "v_cvt_pk_bf16_f32 v173, v26, v27\n\t" \
  "v_mfma_f32_16x16x32_bf16 a[40:43], v[56:59], v[72:75], a[40:43]\n\t" \
  "buffer_load_dwordx4 v240, %[wo], s97 offen offset:0 lds\n\t" \
  "v_mfma_f32_16x16x32_bf16 a[40:43], v[60:63], v[76:79], a[40:43]\n\t" \
  "s_add_u32 s97, s97, 7168\n\t" \
  "v_cvt_pk_bf16_f32 v174, v28, v29\n\t" \
  "v_cvt_pk_bf16_f32 v175, v30, v31\n\t" \
  "v_mfma_f32_16x16x32_bf16 a[56:59], v[92:95], v[76:79], 0\n\t" \
  "ds_write_b128 v213, v[172:175] offset:35840\n\t" \
  "v_mfma_f32_16x16x32_bf16 a[56:59], v[88:91], v[72:75], a[56:59]\n\t" \
  "buffer_load_dwordx4 v241, %[wo], s97 offen offset:1024 lds\n\t" \
  "v_mfma_f32_16x16x32_bf16 a[56:59], v[84:87], v[68:71], a[56:59]\n\t" \
  "s_add_u32 s97, s97, 7168\n\t" \
  "v_mfma_f32_16x16x32_bf16 a[56:59], v[80:83], v[64:67], a[56:59]\n\t" \
  "v_mfma_f32_16x16x32_bf16 a[72:75], v[112:115], v[64:67], 0\n\t" \
  "buffer_load_dwordx4 v240, %[wo], s97 offen offset:2048 lds\n\t" \
  "v_mfma_f32_16x16x32_bf16 a[72:75], v[116:119], v[68:71], a[72:75]\n\t" \
  "s_add_u32 s97, s97, 7168\n\t" \
  "v_mfma_f32_16x16x32_bf16 a[72:75], v[120:123], v[72:75], a[72:75]\n\t" \
  "v_mfma_f32_16x16x32_bf16 a[72:75], v[124:127], v[76:79], a[72:75]\n\t" \
  "buffer_load_dwordx4 v241, %[wo], s97 offen offset:3072 lds\n\t" \
  STS("s_memrealtime s[94:95]\n\t") \
  STS("s_memtime s[92:93]\n\t") \
  STS("s_waitcnt lgkmcnt(0)\n\t") \
  STS("v_writelane_b32 %[stv], s94, 22\n\t") \
  STS("v_writelane_b32 %[stv], s92, 23\n\t") \
  ".subsection 1\n\t" \
  "s_endpgm\n\t" \
  ".Lat_v:\n\t" \
  "s_mov_b32 s98, 0\n\t" \
  ".Lat_v1:\n\t" \
  "s_sleep 4\n\t" \
  "buffer_load_dword v209, v205, %[ws], 0 offen sc1\n\t" \
  "s_waitcnt vmcnt(0)\n\t" \
  "v_and_b32 v206, 0xbfffbfff, v209\n\t" \
  "v_cmp_ne_u32_e64 s[96:97], %[tag], v206\n\t" \
  "s_cmp_lg_u64 s[96:97], 0\n\t" \
  "s_cbranch_scc0 .Lat_w\n\t" \
  "s_add_u32 s98, s98, 1\n\t" \
  "s_cmp_lt_u32 s98, %[cap]\n\t" \
  "s_cbranch_scc1 .Lat_v1\n\t" \
  "v_mov_b32 v206, %[tag]\n\t" \
  "buffer_store_dword v206, off, %[ws], 0 offset:256 sc1\n\t" \
  "s_branch .Lat_w\n\t" \
  ".Lat_x0:\n\t" \
  "v_lshlrev_b32 v209, 12, v233\n\t" \
  "v_bfrev_b32 v254, 1\n\t" \
  "s_mov_b64 exec, 15\n\t" \
  "ds_write_b32 v209, v243 offset:49152\n\t" \
  "s_mov_b64 exec, -1\n\t" \
  "s_waitcnt lgkmcnt(0)\n\t" \
  "s_branch .Lat_y0\n\t" \
  ".Lat_x4:\n\t" \
  "v_lshlrev_b32 v209, 12, v233\n\t" \
  "v_bfrev_b32 v254, 1\n\t" \
  "s_mov_b64 exec, 15\n\t" \
  "ds_write_b32 v209, v243 offset:49152\n\t" \
  "s_mov_b64 exec, -1\n\t" \
  "s_waitcnt lgkmcnt(0)\n\t" \
  "s_branch .Lat_y4\n\t" \
  ".Lat_x1:\n\t" \
  "v_lshlrev_b32 v209, 12, v233\n\t" \
  "v_bfrev_b32 v254, 1\n\t" \
  "s_mov_b64 exec, 15\n\t" \
  "ds_write_b32 v209, v243 offset:49152\n\t" \
  "s_mov_b64 exec, -1\n\t" \
  "s_waitcnt lgkmcnt(0)\n\t" \
  "s_branch .Lat_y1\n\t" \
  ".Lat_x2:\n\t" \
  "v_lshlrev_b32 v209, 12, v233\n\t" \
  "v_bfrev_b32 v254, 1\n\t" \
  "s_mov_b64 exec, 15\n\t" \
  "ds_write_b32 v209, v243 offset:49152\n\t" \
  "s_mov_b64 exec, -1\n\t" \
  "s_waitcnt lgkmcnt(0)\n\t" \
  "s_branch .Lat_y2\n\t" \
  ".Lat_x3:\n\t" \
  "v_lshlrev_b32 v209, 12, v233\n\t" \
  "v_bfrev_b32 v254, 1\n\t" \
  "s_mov_b64 exec, 15\n\t" \
  "ds_write_b32 v209, v243 offset:49152\n\t" \
  "s_mov_b64 exec, -1\n\t" \
  "s_waitcnt lgkmcnt(0)\n\t" \
  "s_branch .Lat_y3\n\t" \
  ".subsection 2\n\t" \
  ".Lsf_go:\n\t" \
  "v_mfma_f32_16x16x32_bf16 v[28:31], a[156:159], a[224:227], v[28:31]\n\t" \
  "v_mov_b32 v216, 0x3f803f80\n\t" \
  "v_lshlrev_b32 v204, 7, v233\n\t" \
  "v_mfma_f32_16x16x32_bf16 v[28:31], a[92:95], a[240:243], v[28:31]\n\t" \
  "v_cmp_gt_u32 vcc, 16, v233\n\t" \
  "v_lshrrev_b32 v202, 3, v233\n\t" \
  "v_mfma_f32_16x16x32_bf16 v[24:27], a[152:155], a[224:227], v[24:27]\n\t" \
  "v_mul_u32_u24 v202, 11264, v202\n\t" \
  "v_add_u32 v202, v202, v204\n\t" \
  "v_mfma_f32_16x16x32_bf16 v[24:27], a[88:91], a[240:243], v[24:27]\n\t" \
  "v_bfrev_b32 v195, 1\n\t" \
  "v_mov_b32 v217, 0x3f803f80\n\t" \
  "v_mfma_f32_16x16x32_bf16 v[20:23], a[148:151], a[224:227], v[20:23]\n\t" \
  "v_cvt_pk_bf16_f32 v31, v30, v31\n\t" \
  "v_cvt_pk_bf16_f32 v30, v28, v29\n\t" \
  "v_mfma_f32_16x16x32_bf16 v[20:23], a[84:87], a[240:243], v[20:23]\n\t" \
  "v_cndmask_b32 v204, v195, v204, vcc\n\t" \
  "v_cndmask_b32 v202, v195, v202, vcc\n\t" \
  "v_mfma_f32_16x16x32_bf16 v[16:19], a[144:147], a[224:227], v[16:19]\n\t" \
  "v_cvt_pk_bf16_f32 v28, v24, v25\n\t" \
  "v_cvt_pk_bf16_f32 v29, v26, v27\n\t" \
  "v_mfma_f32_16x16x32_bf16 v[16:19], a[80:83], a[240:243], v[16:19]\n\t" \
  "v_permlane16_swap_b32 v28, v30\n\t" \
  "v_permlane16_swap_b32 v29, v31\n\t" \
  "v_mfma_f32_16x16x32_bf16 v[12:15], a[140:143], a[224:227], v[12:15]\n\t" \
  "v_cvt_pk_bf16_f32 v27, v22, v23\n\t" \
  "v_cvt_pk_bf16_f32 v26, v20, v21\n\t" \
  "v_mfma_f32_16x16x32_bf16 v[12:15], a[76:79], a[240:243], v[12:15]\n\t" \
  "v_mov_b32 v218, 0x3f803f80\n\t" \
  "v_mov_b32 v219, 0x3f803f80\n\t" \
  "v_mfma_f32_16x16x32_bf16 v[8:11], a[136:139], a[224:227], v[8:11]\n\t" \
  "v_cvt_pk_bf16_f32 v24, v16, v17\n\t" \
  "v_cvt_pk_bf16_f32 v25, v18, v19\n\t" \
  "v_mfma_f32_16x16x32_bf16 v[8:11], a[72:75], a[240:243], v[8:11]\n\t" \
  "v_permlane16_swap_b32 v24, v26\n\t" \
  "v_permlane16_swap_b32 v25, v27\n\t" \
  "v_mfma_f32_16x16x32_bf16 v[4:7], a[132:135], a[224:227], v[4:7]\n\t" \
  "v_cvt_pk_bf16_f32 v23, v14, v15\n\t" \
  "v_cvt_pk_bf16_f32 v22, v12, v13\n\t" \
  "v_mfma_f32_16x16x32_bf16 v[4:7], a[68:71], a[240:243], v[4:7]\n\t" \
  "v_mov_b32 v196, 1.0\n\t" \
  "v_mov_b32 v197, 1.0\n\t" \
  "v_mfma_f32_16x16x32_bf16 v[0:3], a[128:131], a[224:227], v[0:3]\n\t" \
  "v_cvt_pk_bf16_f32 v20, v8, v9\n\t" \
  "v_cvt_pk_bf16_f32 v21, v10, v11\n\t" \
  "v_mfma_f32_16x16x32_bf16 v[0:3], a[64:67], a[240:243], v[0:3]\n\t" \
  "v_permlane16_swap_b32 v20, v22\n\t" \
  "v_permlane16_swap_b32 v21, v23\n\t" \
  "s_waitcnt vmcnt(2)\n\t" \
  "s_barrier\n\t" \
  "v_mfma_f32_16x16x32_bf16 v[60:63], a[156:159], a[228:231], v[60:63]\n\t" \
  "v_cvt_pk_bf16_f32 v19, v6, v7\n\t" \
  "v_cvt_pk_bf16_f32 v18, v4, v5\n\t" \
  "v_mfma_f32_16x16x32_bf16 v[60:63], a[92:95], a[244:247], v[60:63]\n\t" \
  "v_cvt_pk_bf16_f32 v16, v0, v1\n\t" \
  "v_cvt_pk_bf16_f32 v17, v2, v3\n\t" \
  "v_mfma_f32_16x16x32_bf16 v[56:59], a[152:155], a[228:231], v[56:59]\n\t" \
  "v_permlane16_swap_b32 v16, v18\n\t" \
  "v_permlane16_swap_b32 v17, v19\n\t" \
  "v_mfma_f32_16x16x32_bf16 v[56:59], a[88:91], a[244:247], v[56:59]\n\t" \
  "v_mov_b32 v198, 1.0\n\t" \
  "v_mov_b32 v199, 1.0\n\t" \
  "v_mfma_f32_16x16x32_bf16 v[52:55], a[148:151], a[228:231], v[52:55]\n\t" \
  "v_cvt_pk_bf16_f32 v63, v62, v63\n\t" \
  "v_cvt_pk_bf16_f32 v62, v60, v61\n\t" \
  "v_mfma_f32_16x16x32_bf16 v[52:55], a[84:87], a[244:247], v[52:55]\n\t" \
  "v_lshlrev_b32 v242, 4, v233\n\t" \
  "s_mov_b32 s99, 0\n\t" \
  "v_mfma_f32_16x16x32_bf16 v[48:51], a[144:147], a[228:231], v[48:51]\n\t" \
  "v_cvt_pk_bf16_f32 v60, v56, v57\n\t" \
  "v_cvt_pk_bf16_f32 v61, v58, v59\n\t" \
  "v_mfma_f32_16x16x32_bf16 v[48:51], a[80:83], a[244:247], v[48:51]\n\t" \
  "v_permlane16_swap_b32 v60, v62\n\t" \
  "v_permlane16_swap_b32 v61, v63\n\t" \
  "v_mfma_f32_16x16x32_bf16 v[44:47], a[140:143], a[228:231], v[44:47]\n\t" \
  "v_cvt_pk_bf16_f32 v59, v54, v55\n\t" \
  "v_cvt_pk_bf16_f32 v58, v52, v53\n\t" \
  "v_mfma_f32_16x16x32_bf16 v[44:47], a[76:79], a[244:247], v[44:47]\n\t" \
  "v_add_u32 v237, 65536, v228\n\t" \
  "v_add_u32 v238, 65536, v229\n\t" \
  "v_mfma_f32_16x16x32_bf16 v[40:43], a[136:139], a[228:231], v[40:43]\n\t" \
  "v_cvt_pk_bf16_f32 v56, v48, v49\n\t" \
  "v_cvt_pk_bf16_f32 v57, v50, v51\n\t" \
  "v_mfma_f32_16x16x32_bf16 v[40:43], a[72:75], a[244:247], v[40:43]\n\t" \
  "v_permlane16_swap_b32 v56, v58\n\t" \
  "v_permlane16_swap_b32 v57, v59\n\t" \
  "v_mfma_f32_16x16x32_bf16 v[36:39], a[132:135], a[228:231], v[36:39]\n\t" \
  "v_cvt_pk_bf16_f32 v55, v46, v47\n\t" \
  "v_cvt_pk_bf16_f32 v54, v44, v45\n\t" \
  "v_mfma_f32_16x16x32_bf16 v[36:39], a[68:71], a[244:247], v[36:39]\n\t" \
  "ds_read_b128 a[0:3], v220 offset:0\n\t" \
  "ds_read_b128 a[4:7], v221 offset:0\n\t" \
  "v_mfma_f32_16x16x32_bf16 v[32:35], a[128:131], a[228:231], v[32:35]\n\t" \
  "v_cvt_pk_bf16_f32 v52, v40, v41\n\t" \
  "v_cvt_pk_bf16_f32 v53, v42, v43\n\t" \
  "v_mfma_f32_16x16x32_bf16 v[32:35], a[64:67], a[244:247], v[32:35]\n\t" \
  "v_permlane16_swap_b32 v52, v54\n\t" \
  "v_permlane16_swap_b32 v53, v55\n\t" \
  "v_mfma_f32_16x16x32_bf16 v[92:95], a[156:159], a[232:235], v[92:95]\n\t" \
  "v_cvt_pk_bf16_f32 v51, v38, v39\n\t" \
  "v_cvt_pk_bf16_f32 v50, v36, v37\n\t" \
  "v_mfma_f32_16x16x32_bf16 v[92:95], a[92:95], a[248:251], v[92:95]\n\t" \
  "s_add_u32 m0, %[m], 0\n\t" \
  "ds_read_b128 a[8:11], v222 offset:0\n\t" \
  "v_mfma_f32_16x16x32_bf16 v[88:91], a[152:155], a[232:235], v[88:91]\n\t" \
  "v_cvt_pk_bf16_f32 v48, v32, v33\n\t" \
  "v_cvt_pk_bf16_f32 v49, v34, v35\n\t" \
  "v_mfma_f32_16x16x32_bf16 v[88:91], a[88:91], a[248:251], v[88:91]\n\t" \
  "v_permlane16_swap_b32 v48, v50\n\t" \
  "v_permlane16_swap_b32 v49, v51\n\t" \
  "v_mfma_f32_16x16x32_bf16 v[84:87], a[148:151], a[232:235], v[84:87]\n\t" \
  "v_cvt_pk_bf16_f32 v95, v94, v95\n\t" \
  "v_cvt_pk_bf16_f32 v94, v92, v93\n\t" \
  "v_mfma_f32_16x16x32_bf16 v[84:87], a[84:87], a[248:251], v[84:87]\n\t" \
  "ds_read_b128 a[12:15], v223 offset:0\n\t" \
  "s_xor_b32 s97, %[wob], 0\n\t" \
  "v_mfma_f32_16x16x32_bf16 v[80:83], a[144:147], a[232:235], v[80:83]\n\t" \
  "v_cvt_pk_bf16_f32 v92, v88, v89\n\t" \
  "v_cvt_pk_bf16_f32 v93, v90, v91\n\t" \
  "v_mfma_f32_16x16x32_bf16 v[80:83], a[80:83], a[248:251], v[80:83]\n\t" \
  "v_permlane16_swap_b32 v92, v94\n\t" \
  "v_permlane16_swap_b32 v93, v95\n\t" \
  "v_mfma_f32_16x16x32_bf16 v[76:79], a[140:143], a[232:235], v[76:79]\n\t" \
  "v_cvt_pk_bf16_f32 v91, v86, v87\n\t" \
  "v_cvt_pk_bf16_f32 v90, v84, v85\n\t" \
  "v_mfma_f32_16x16x32_bf16 v[76:79], a[76:79], a[248:251], v[76:79]\n\t" \
  "ds_read_b128 a[16:19], v220 offset:4096\n\t" \
  "ds_read_b128 a[20:23], v221 offset:4096\n\t" \
  "v_mfma_f32_16x16x32_bf16 v[72:75], a[136:139], a[232:235], v[72:75]\n\t" \
  "v_cvt_pk_bf16_f32 v88, v80, v81\n\t" \
  "v_cvt_pk_bf16_f32 v89, v82, v83\n\t" \
  "v_mfma_f32_16x16x32_bf16 v[72:75], a[72:75], a[248:251], v[72:75]\n\t" \
  "v_permlane16_swap_b32 v88, v90\n\t" \
  "v_permlane16_swap_b32 v89, v91\n\t" \
  "v_mfma_f32_16x16x32_bf16 v[68:71], a[132:135], a[232:235], v[68:71]\n\t" \
  "v_cvt_pk_bf16_f32 v87, v78, v79\n\t" \
  "v_cvt_pk_bf16_f32 v86, v76, v77\n\t" \
  "v_mfma_f32_16x16x32_bf16 v[68:71], a[68:71], a[248:251], v[68:71]\n\t" \
  "ds_read_b128 a[24:27], v222 offset:4096\n\t" \
  "buffer_load_dwordx4 v240, %[wo], s97 offen offset:0 lds\n\t" \
  "v_mfma_f32_16x16x32_bf16 v[64:67], a[128:131], a[232:235], v[64:67]\n\t" \
  "v_cvt_pk_bf16_f32 v84, v72, v73\n\t" \
  "v_cvt_pk_bf16_f32 v85, v74, v75\n\t" \
  "v_mfma_f32_16x16x32_bf16 v[64:67], a[64:67], a[248:251], v[64:67]\n\t" \
  "v_permlane16_swap_b32 v84, v86\n\t" \
  "v_permlane16_swap_b32 v85, v87\n\t" \
  "v_mfma_f32_16x16x32_bf16 v[124:127], a[156:159], a[236:239], v[124:127]\n\t" \
  "v_cvt_pk_bf16_f32 v83, v70, v71\n\t" \
  "v_cvt_pk_bf16_f32 v82, v68, v69\n\t" \
  "v_mfma_f32_16x16x32_bf16 v[124:127], a[92:95], a[252:255], v[124:127]\n\t" \
  "ds_read_b128 a[28:31], v223 offset:4096\n\t" \
  "ds_read_b128 a[32:35], v220 offset:8192\n\t" \
  "v_mfma_f32_16x16x32_bf16 v[120:123], a[152:155], a[236:239], v[120:123]\n\t" \
  "v_cvt_pk_bf16_f32 v80, v64, v65\n\t" \
  "v_cvt_pk_bf16_f32 v81, v66, v67\n\t" \
  "v_mfma_f32_16x16x32_bf16 v[120:123], a[88:91], a[252:255], v[120:123]\n\t" \
  "v_permlane16_swap_b32 v80, v82\n\t" \
  "v_permlane16_swap_b32 v81, v83\n\t" \
  "v_mfma_f32_16x16x32_bf16 v[116:119], a[148:151], a[236:239], v[116:119]\n\t" \
  "v_cvt_pk_bf16_f32 v127, v126, v127\n\t" \
  "v_cvt_pk_bf16_f32 v126, v124, v125\n\t" \
  "v_mfma_f32_16x16x32_bf16 v[116:119], a[84:87], a[252:255], v[116:119]\n\t" \
  "s_add_u32 s97, s97, 7168\n\t" \
  "buffer_load_dwordx4 v241, %[wo], s97 offen offset:1024 lds\n\t" \
  "v_mfma_f32_16x16x32_bf16 v[112:115], a[144:147], a[236:239], v[112:115]\n\t" \
  "v_cvt_pk_bf16_f32 v124, v120, v121\n\t" \
  "v_cvt_pk_bf16_f32 v125, v122, v123\n\t" \
  "v_mfma_f32_16x16x32_bf16 v[112:115], a[80:83], a[252:255], v[112:115]\n\t" \
  "v_permlane16_swap_b32 v124, v126\n\t" \
  "v_permlane16_swap_b32 v125, v127\n\t" \
  "ds_read_b128 a[36:39], v221 offset:8192\n\t" \
  "ds_read_b128 a[40:43], v222 offset:8192\n\t" \
  "ds_read_b128 a[44:47], v223 offset:8192\n\t" \
  "ds_read_b128 a[48:51], v220 offset:12288\n\t" \
  "ds_read_b128 a[52:55], v221 offset:12288\n\t" \
  "ds_read_b128 a[56:59], v222 offset:12288\n\t" \
  "ds_read_b128 a[60:63], v223 offset:12288\n\t" \
  "v_mfma_f32_16x16x32_bf16 v[108:111], a[140:143], a[236:239], v[108:111]\n\t" \
  "v_cvt_pk_bf16_f32 v123, v118, v119\n\t" \
  "v_cvt_pk_bf16_f32 v122, v116, v117\n\t" \
  "v_mfma_f32_16x16x32_bf16 v[108:111], a[76:79], a[252:255], v[108:111]\n\t" \
  "v_cvt_pk_bf16_f32 v120, v112, v113\n\t" \
  "v_cvt_pk_bf16_f32 v121, v114, v115\n\t" \
  "v_mfma_f32_16x16x32_bf16 v[104:107], a[136:139], a[236:239], v[104:107]\n\t" \
  "v_permlane16_swap_b32 v120, v122\n\t" \
  "v_permlane16_swap_b32 v121, v123\n\t" \
  "v_mfma_f32_16x16x32_bf16 v[104:107], a[72:75], a[252:255], v[104:107]\n\t" \
  "s_add_u32 s97, s97, 7168\n\t" \
  "buffer_load_dwordx4 v240, %[wo], s97 offen offset:2048 lds\n\t" \
  "v_mfma_f32_16x16x32_bf16 v[100:103], a[132:135], a[236:239], v[100:103]\n\t" \
  "v_cvt_pk_bf16_f32 v119, v110, v111\n\t" \
  "v_cvt_pk_bf16_f32 v118, v108, v109\n\t" \
  "v_mfma_f32_16x16x32_bf16 v[100:103], a[68:71], a[252:255], v[100:103]\n\t" \
  "s_add_u32 s97, s97, 7168\n\t" \
  "buffer_load_dwordx4 v241, %[wo], s97 offen offset:3072 lds\n\t" \
  "v_mfma_f32_16x16x32_bf16 v[96:99], a[128:131], a[236:239], v[96:99]\n\t" \
  "v_cvt_pk_bf16_f32 v116, v104, v105\n\t" \
  "v_cvt_pk_bf16_f32 v117, v106, v107\n\t" \
  "v_mfma_f32_16x16x32_bf16 v[96:99], a[64:67], a[252:255], v[96:99]\n\t" \
  "v_permlane16_swap_b32 v116, v118\n\t" \
  "v_permlane16_swap_b32 v117, v119\n\t" \
  "s_add_u32 m0, %[m], 16384\n\t" \
  "s_xor_b32 s97, %[wob], 131072\n\t" \
  "buffer_load_dwordx4 v240, %[wo], s97 offen offset:0 lds\n\t" \
  "s_add_u32 s97, s97, 7168\n\t" \
  "buffer_load_dwordx4 v241, %[wo], s97 offen offset:1024 lds\n\t" \
  "s_add_u32 s97, s97, 7168\n\t" \
  "buffer_load_dwordx4 v240, %[wo], s97 offen offset:2048 lds\n\t" \
  "s_add_u32 s97, s97, 7168\n\t" \
  "buffer_load_dwordx4 v241, %[wo], s97 offen offset:3072 lds\n\t" \
  "s_waitcnt lgkmcnt(0)\n\t" \
  "s_barrier\n\t" \
  "v_mfma_f32_16x16x32_bf16 v[0:3], a[0:3], v[16:19], 0\n\t" \
  "v_cvt_pk_bf16_f32 v115, v102, v103\n\t" \
  "v_cvt_pk_bf16_f32 v114, v100, v101\n\t" \
  "v_mfma_f32_16x16x32_bf16 v[0:3], a[4:7], v[20:23], v[0:3]\n\t" \
  "v_cvt_pk_bf16_f32 v112, v96, v97\n\t" \
  "v_cvt_pk_bf16_f32 v113, v98, v99\n\t" \
  "v_mfma_f32_16x16x32_bf16 v[0:3], a[8:11], v[24:27], v[0:3]\n\t" \
  "v_permlane16_swap_b32 v112, v114\n\t" \
  "v_permlane16_swap_b32 v113, v115\n\t" \
  "v_mfma_f32_16x16x32_bf16 v[0:3], a[12:15], v[28:31], v[0:3]\n\t" \
  "s_add_u32 s98, %[pf4], 28672\n\t" \
  "buffer_load_dword %[pfd], v245, %[sU], s98 offen\n\t" \
  "v_mfma_f32_16x16x32_bf16 v[32:35], a[0:3], v[48:51], 0\n\t" \
  "ds_read_b128 a[192:195], v220 offset:16384\n\t" \
  "s_add_u32 m0, %[m], 49152\n\t" \
  "v_mfma_f32_16x16x32_bf16 v[32:35], a[4:7], v[52:55], v[32:35]\n\t" \
  "ds_read_b128 a[196:199], v221 offset:16384\n\t" \
  "ds_read_b128 a[200:203], v222 offset:16384\n\t" \
  "v_mfma_f32_16x16x32_bf16 v[32:35], a[8:11], v[56:59], v[32:35]\n\t" \
  "v_max3_f32 v194, v0, v1, v2\n\t" \
  "v_max_f32 v194, v194, v3\n\t" \
  "v_mfma_f32_16x16x32_bf16 v[32:35], a[12:15], v[60:63], v[32:35]\n\t" \
  "v_mov_b32 v195, v194\n\t" \
  "s_add_u32 s96, %[sofk], 32768\n\t" \
  "s_nop 0\n\t" \
  "v_permlane32_swap_b32 v194, v195\n\t" \
  "v_max_f32 v194, v194, v195\n\t" \
  "v_mov_b32 v195, v194\n\t" \
  "s_nop 1\n\t" \
  "v_permlane16_swap_b32 v194, v195\n\t" \
  "v_max_f32 v194, v194, v195\n\t" \
  "v_sub_f32 v176, 0xc2000000, v194\n\t" \
  "v_sub_f32 v177, 0xc2000000, v194\n\t" \
  "v_sub_f32 v178, 0xc2000000, v194\n\t" \
  "v_sub_f32 v179, 0xc2000000, v194\n\t" \
  "s_nop 1\n\t" \
  "v_mfma_f32_16x16x32_bf16 v[4:7], a[16:19], v[16:19], v[176:179]\n\t" \
  "v_add_f32 v0, v0, v176\n\t" \
  "v_add_f32 v1, v1, v176\n\t" \
  "v_mfma_f32_16x16x32_bf16 v[4:7], a[20:23], v[20:23], v[4:7]\n\t" \
  "v_add_f32 v2, v2, v176\n\t" \
  "v_add_f32 v3, v3, v176\n\t" \
  "v_mfma_f32_16x16x32_bf16 v[4:7], a[24:27], v[24:27], v[4:7]\n\t" \
  "ds_read_b128 a[204:207], v223 offset:16384\n\t" \
  "v_max3_f32 v194, v32, v33, v34\n\t" \
  "v_mfma_f32_16x16x32_bf16 v[4:7], a[28:31], v[28:31], v[4:7]\n\t" \
  "v_max_f32 v194, v194, v35\n\t" \
  "v_mov_b32 v195, v194\n\t" \
  "v_mfma_f32_16x16x32_bf16 v[8:11], a[32:35], v[16:19], v[176:179]\n\t" \
  "ds_read_b128 a[208:211], v220 offset:20480\n\t" \
  "v_permlane32_swap_b32 v194, v195\n\t" \
  "v_mfma_f32_16x16x32_bf16 v[8:11], a[36:39], v[20:23], v[8:11]\n\t" \
  "v_max_f32 v194, v194, v195\n\t" \
  "v_mov_b32 v195, v194\n\t" \
  "v_mfma_f32_16x16x32_bf16 v[8:11], a[40:43], v[24:27], v[8:11]\n\t" \
  "ds_read_b128 a[212:215], v221 offset:20480\n\t" \
  "v_permlane16_swap_b32 v194, v195\n\t" \
  "v_mfma_f32_16x16x32_bf16 v[8:11], a[44:47], v[28:31], v[8:11]\n\t" \
  "v_max_f32 v194, v194, v195\n\t" \
  "v_sub_f32 v180, 0xc2000000, v194\n\t" \
  "v_mfma_f32_16x16x32_bf16 v[12:15], a[48:51], v[16:19], v[176:179]\n\t" \
  "v_sub_f32 v181, 0xc2000000, v194\n\t" \
  "v_sub_f32 v182, 0xc2000000, v194\n\t" \
  "v_mfma_f32_16x16x32_bf16 v[12:15], a[52:55], v[20:23], v[12:15]\n\t" \
  "v_sub_f32 v183, 0xc2000000, v194\n\t" \
  "v_add_f32 v32, v32, v180\n\t" \
  "v_mfma_f32_16x16x32_bf16 v[12:15], a[56:59], v[24:27], v[12:15]\n\t" \
  "v_add_f32 v33, v33, v180\n\t" \
  "v_add_f32 v34, v34, v180\n\t" \
  "v_mfma_f32_16x16x32_bf16 v[12:15], a[60:63], v[28:31], v[12:15]\n\t" \
  "v_add_f32 v35, v35, v180\n\t" \
  "v_max3_f32 v192, v4, v5, v6\n\t" \
  "v_mfma_f32_16x16x32_bf16 v[64:67], a[0:3], v[80:83], 0\n\t" \
  "buffer_load_dwordx4 v224, %[sK], s96 offen offset:0 lds\n\t" \
  "ds_read_b128 a[216:219], v222 offset:20480\n\t" \
  "v_mfma_f32_16x16x32_bf16 v[64:67], a[4:7], v[84:87], v[64:67]\n\t" \
  "ds_read_b128 a[220:223], v223 offset:20480\n\t" \
  "v_max3_f32 v203, v7, v8, v9\n\t" \
  "v_mfma_f32_16x16x32_bf16 v[64:67], a[8:11], v[88:91], v[64:67]\n\t" \
  "v_max3_f32 v192, v192, v10, v11\n\t" \
  "ds_read_b128 a[224:227], v220 offset:24576\n\t" \
  "v_mfma_f32_16x16x32_bf16 v[64:67], a[12:15], v[92:95], v[64:67]\n\t" \
  "buffer_load_dwordx4 v225, %[sK], s96 offen offset:1024 lds\n\t" \
  "ds_read_b128 a[228:231], v221 offset:24576\n\t" \
  "v_mfma_f32_16x16x32_bf16 v[36:39], a[16:19], v[48:51], v[180:183]\n\t" \
  "v_max3_f32 v203, v203, v12, v13\n\t" \
  "v_max3_f32 v192, v192, v14, v15\n\t" \
  "v_mfma_f32_16x16x32_bf16 v[36:39], a[20:23], v[52:55], v[36:39]\n\t" \
  "v_max_f32 v192, v192, v203\n\t" \
  "ds_read_b128 a[232:235], v222 offset:24576\n\t" \
  "v_mfma_f32_16x16x32_bf16 v[36:39], a[24:27], v[56:59], v[36:39]\n\t" \
  "v_cmp_lt_f32 vcc, 0x42800000, v192\n\t" \
  "v_max3_f32 v194, v64, v65, v66\n\t" \
  "v_mfma_f32_16x16x32_bf16 v[36:39], a[28:31], v[60:63], v[36:39]\n\t" \
  "s_cbranch_vccnz .Lsf_c00\n\t" \
  ".Lsf_r00:\n\t" \
  "v_max_f32 v194, v194, v67\n\t" \
  "v_mfma_f32_16x16x32_bf16 v[40:43], a[32:35], v[48:51], v[180:183]\n\t" \
  "v_mov_b32 v195, v194\n\t" \
  "ds_read_b128 a[236:239], v223 offset:24576\n\t" \
  "v_mfma_f32_16x16x32_bf16 v[40:43], a[36:39], v[52:55], v[40:43]\n\t" \
  "v_permlane32_swap_b32 v194, v195\n\t" \
  "v_max_f32 v194, v194, v195\n\t" \
  "v_mfma_f32_16x16x32_bf16 v[40:43], a[40:43], v[56:59], v[40:43]\n\t" \
  "v_mov_b32 v195, v194\n\t" \
  "buffer_load_dwordx4 v226, %[sK], s96 offen offset:2048 lds\n\t" \
  "v_mfma_f32_16x16x32_bf16 v[40:43], a[44:47], v[60:63], v[40:43]\n\t" \
  "v_permlane16_swap_b32 v194, v195\n\t" \
  "v_max_f32 v194, v194, v195\n\t" \
  "v_mfma_f32_16x16x32_bf16 v[44:47], a[48:51], v[48:51], v[180:183]\n\t" \
  "v_sub_f32 v184, 0xc2000000, v194\n\t" \
  "v_sub_f32 v185, 0xc2000000, v194\n\t" \
  "v_mfma_f32_16x16x32_bf16 v[44:47], a[52:55], v[52:55], v[44:47]\n\t" \
  "v_sub_f32 v186, 0xc2000000, v194\n\t" \
  "v_sub_f32 v187, 0xc2000000, v194\n\t" \
  "v_mfma_f32_16x16x32_bf16 v[44:47], a[56:59], v[56:59], v[44:47]\n\t" \
  "v_add_f32 v64, v64, v184\n\t" \
  "v_add_f32 v65, v65, v184\n\t" \
  "v_mfma_f32_16x16x32_bf16 v[44:47], a[60:63], v[60:63], v[44:47]\n\t" \
  "v_add_f32 v66, v66, v184\n\t" \
  "v_add_f32 v67, v67, v184\n\t" \
  "v_mfma_f32_16x16x32_bf16 v[96:99], a[0:3], v[112:115], 0\n\t" \
  "ds_read_b128 a[240:243], v220 offset:28672\n\t" \
  "ds_read_b128 a[244:247], v221 offset:28672\n\t" \
  "v_mfma_f32_16x16x32_bf16 v[96:99], a[4:7], v[116:119], v[96:99]\n\t" \
  "v_max3_f32 v192, v36, v37, v38\n\t" \
  "ds_read_b128 a[248:251], v222 offset:28672\n\t" \
  "v_mfma_f32_16x16x32_bf16 v[96:99], a[8:11], v[120:123], v[96:99]\n\t" \
  "buffer_load_dwordx4 v227, %[sK], s96 offen offset:3072 lds\n\t" \
  "ds_read_b128 a[252:255], v223 offset:28672\n\t" \
  "v_mfma_f32_16x16x32_bf16 v[96:99], a[12:15], v[124:127], v[96:99]\n\t" \
  "v_max3_f32 v203, v39, v40, v41\n\t" \
  "v_max3_f32 v192, v192, v42, v43\n\t" \
  "v_mfma_f32_16x16x32_bf16 v[68:71], a[16:19], v[80:83], v[184:187]\n\t" \
  "ds_read_b64_tr_b16 a[128:129], v228 offset:0\n\t" \
  "v_max3_f32 v203, v203, v44, v45\n\t" \
  "v_mfma_f32_16x16x32_bf16 v[68:71], a[20:23], v[84:87], v[68:71]\n\t" \
  "v_max3_f32 v192, v192, v46, v47\n\t" \
  "v_max_f32 v192, v192, v203\n\t" \
  "v_mfma_f32_16x16x32_bf16 v[68:71], a[24:27], v[88:91], v[68:71]\n\t" \
  "v_cmp_lt_f32 vcc, 0x42800000, v192\n\t" \
  "s_add_u32 m0, %[m], 147456\n\t" \
  "v_mfma_f32_16x16x32_bf16 v[68:71], a[28:31], v[92:95], v[68:71]\n\t" \
  "s_cbranch_vccnz .Lsf_c01\n\t" \
  ".Lsf_r01:\n\t" \
  "ds_read_b64_tr_b16 a[130:131], v228 offset:4096\n\t" \
  "v_mfma_f32_16x16x32_bf16 v[72:75], a[32:35], v[80:83], v[184:187]\n\t" \
  "v_max3_f32 v194, v96, v97, v98\n\t" \
  "v_max_f32 v194, v194, v99\n\t" \
  "v_mfma_f32_16x16x32_bf16 v[72:75], a[36:39], v[84:87], v[72:75]\n\t" \
  "v_mov_b32 v195, v194\n\t" \
  "ds_read_b64_tr_b16 a[132:133], v228 offset:8192\n\t" \
  "v_mfma_f32_16x16x32_bf16 v[72:75], a[40:43], v[88:91], v[72:75]\n\t" \
  "v_permlane32_swap_b32 v194, v195\n\t" \
  "v_max_f32 v194, v194, v195\n\t" \
  "v_mfma_f32_16x16x32_bf16 v[72:75], a[44:47], v[92:95], v[72:75]\n\t" \
  "v_mov_b32 v195, v194\n\t" \
  "s_add_u32 s96, %[sofk], 16384\n\t" \
  "v_mfma_f32_16x16x32_bf16 v[76:79], a[48:51], v[80:83], v[184:187]\n\t" \
  "v_permlane16_swap_b32 v194, v195\n\t" \
  "v_max_f32 v194, v194, v195\n\t" \
  "v_mfma_f32_16x16x32_bf16 v[76:79], a[52:55], v[84:87], v[76:79]\n\t" \
  "v_sub_f32 v188, 0xc2000000, v194\n\t" \
  "v_sub_f32 v189, 0xc2000000, v194\n\t" \
  "v_mfma_f32_16x16x32_bf16 v[76:79], a[56:59], v[88:91], v[76:79]\n\t" \
  "v_sub_f32 v190, 0xc2000000, v194\n\t" \
  "v_sub_f32 v191, 0xc2000000, v194\n\t" \
  "v_mfma_f32_16x16x32_bf16 v[76:79], a[60:63], v[92:95], v[76:79]\n\t" \
  "v_add_f32 v96, v96, v188\n\t" \
  "v_add_f32 v97, v97, v188\n\t" \
  "v_mfma_f32_16x16x32_bf16 v[100:103], a[16:19], v[112:115], v[188:191]\n\t" \
  "v_add_f32 v98, v98, v188\n\t" \
  "v_add_f32 v99, v99, v188\n\t" \
  "v_mfma_f32_16x16x32_bf16 v[100:103], a[20:23], v[116:119], v[100:103]\n\t" \
  "ds_read_b64_tr_b16 a[134:135], v228 offset:12288\n\t" \
  "v_max3_f32 v192, v68, v69, v70\n\t" \
  "v_mfma_f32_16x16x32_bf16 v[100:103], a[24:27], v[120:123], v[100:103]\n\t" \
  "ds_read_b64_tr_b16 a[136:137], v229 offset:0\n\t" \
  "v_max3_f32 v203, v71, v72, v73\n\t" \
  "v_mfma_f32_16x16x32_bf16 v[100:103], a[28:31], v[124:127], v[100:103]\n\t" \
  "v_max3_f32 v192, v192, v74, v75\n\t" \
  "buffer_load_dwordx4 v230, %[sV], s96 offen offset:0 lds\n\t" \
  "v_mfma_f32_16x16x32_bf16 v[104:107], a[32:35], v[112:115], v[188:191]\n\t" \
  "ds_read_b64_tr_b16 a[138:139], v229 offset:4096\n\t" \
  "ds_read_b64_tr_b16 a[140:141], v229 offset:8192\n\t" \
  "v_mfma_f32_16x16x32_bf16 v[104:107], a[36:39], v[116:119], v[104:107]\n\t" \
  "v_max3_f32 v203, v203, v76, v77\n\t" \
  "v_max3_f32 v192, v192, v78, v79\n\t" \
  "v_mfma_f32_16x16x32_bf16 v[104:107], a[40:43], v[120:123], v[104:107]\n\t" \
  "v_max_f32 v192, v192, v203\n\t" \
  "ds_read_b64_tr_b16 a[142:143], v229 offset:12288\n\t" \
  "v_mfma_f32_16x16x32_bf16 v[104:107], a[44:47], v[124:127], v[104:107]\n\t" \
  "v_cmp_lt_f32 vcc, 0x42800000, v192\n\t" \
  "buffer_load_dwordx4 v231, %[sV], s96 offen offset:1024 lds\n\t" \
  "v_mfma_f32_16x16x32_bf16 v[108:111], a[48:51], v[112:115], v[188:191]\n\t" \
  "s_cbranch_vccnz .Lsf_c02\n\t" \
  ".Lsf_r02:\n\t" \
  "v_max3_f32 v192, v100, v101, v102\n\t" \
  "v_mfma_f32_16x16x32_bf16 v[108:111], a[52:55], v[116:119], v[108:111]\n\t" \
  "ds_read_b64_tr_b16 a[144:145], v228 offset:256\n\t" \
  "ds_read_b64_tr_b16 a[146:147], v228 offset:4352\n\t" \
  "v_mfma_f32_16x16x32_bf16 v[108:111], a[56:59], v[120:123], v[108:111]\n\t" \
  "v_max3_f32 v203, v103, v104, v105\n\t" \
  "v_max3_f32 v192, v192, v106, v107\n\t" \
  "v_mfma_f32_16x16x32_bf16 v[108:111], a[60:63], v[124:127], v[108:111]\n\t" \
  "buffer_load_dwordx4 v230, %[sV], s96 offen offset:2048 lds\n\t" \
  "ds_read_b64_tr_b16 a[148:149], v228 offset:8448\n\t" \
  "v_exp_f32 v0, v0\n\t" \
  "v_exp_f32 v1, v1\n\t" \
  "v_exp_f32 v2, v2\n\t" \
  "v_exp_f32 v3, v3\n\t" \
  "v_cvt_pk_bf16_f32 v128, v0, v1\n\t" \
  "v_cvt_pk_bf16_f32 v129, v2, v3\n\t" \
  "s_waitcnt lgkmcnt(11)\n\t" \
  "v_mfma_f32_16x16x32_bf16 v[0:3], a[192:195], v[16:19], v[176:179]\n\t" \
  "v_exp_f32 v4, v4\n\t" \
  "v_mfma_f32_16x16x32_bf16 v[0:3], a[196:199], v[20:23], v[0:3]\n\t" \
  "v_max3_f32 v203, v203, v108, v109\n\t" \
  "v_max3_f32 v192, v192, v110, v111\n\t" \
  "v_mfma_f32_16x16x32_bf16 v[0:3], a[200:203], v[24:27], v[0:3]\n\t" \
  "v_max_f32 v192, v192, v203\n\t" \
  "ds_read_b64_tr_b16 a[150:151], v228 offset:12544\n\t" \
  "v_mfma_f32_16x16x32_bf16 v[0:3], a[204:207], v[28:31], v[0:3]\n\t" \
  "v_cmp_lt_f32 vcc, 0x42800000, v192\n\t" \
  "buffer_load_dwordx4 v231, %[sV], s96 offen offset:3072 lds\n\t" \
  "v_exp_f32 v5, v5\n\t" \
  "v_exp_f32 v6, v6\n\t" \
  "v_exp_f32 v7, v7\n\t" \
  "v_cvt_pk_bf16_f32 v130, v4, v5\n\t" \
  "v_cvt_pk_bf16_f32 v131, v6, v7\n\t" \
  "v_mfma_f32_16x16x32_bf16 v[4:7], a[220:223], v[28:31], v[176:179]\n\t" \
  "s_cbranch_vccnz .Lsf_c03\n\t" \
  ".Lsf_r03:\n\t" \
  "ds_read_b64_tr_b16 a[152:153], v229 offset:256\n\t" \
  "v_mfma_f32_16x16x32_bf16 v[4:7], a[216:219], v[24:27], v[4:7]\n\t" \
  "s_cmp_lg_u32 s99, 0\n\t" \
  "s_cbranch_scc1 .Lsf_p0\n\t" \
  ".Lsf_q0:\n\t" \
  "v_mfma_f32_16x16x32_bf16 v[4:7], a[212:215], v[20:23], v[4:7]\n\t" \
  "v_max3_f32 v192, v0, v1, v2\n\t" \
  "ds_read_b64_tr_b16 a[154:155], v229 offset:4352\n\t" \
  "v_mfma_f32_16x16x32_bf16 v[4:7], a[208:211], v[16:19], v[4:7]\n\t" \
  "v_exp_f32 v8, v8\n\t" \
  "v_exp_f32 v9, v9\n\t" \
  "v_exp_f32 v10, v10\n\t" \
  "v_exp_f32 v11, v11\n\t" \
  "v_cvt_pk_bf16_f32 v132, v8, v9\n\t" \
  "v_cvt_pk_bf16_f32 v133, v10, v11\n\t" \
  "v_mfma_f32_16x16x32_bf16 v[8:11], a[224:227], v[16:19], v[176:179]\n\t" \
  "s_add_u32 s98, %[pf4], 43008\n\t" \
  "buffer_load_dword %[pfd], v245, %[sU], s98 offen\n\t" \
  "v_mfma_f32_16x16x32_bf16 v[8:11], a[228:231], v[20:23], v[8:11]\n\t" \
  "v_max3_f32 v203, v3, v4, v5\n\t" \
  "v_max3_f32 v192, v192, v6, v7\n\t" \
  "v_mfma_f32_16x16x32_bf16 v[8:11], a[232:235], v[24:27], v[8:11]\n\t" \
  "v_exp_f32 v12, v12\n\t" \
  "v_mfma_f32_16x16x32_bf16 v[8:11], a[236:239], v[28:31], v[8:11]\n\t" \
  "ds_read_b64_tr_b16 a[156:157], v229 offset:8448\n\t" \
  "ds_read_b64_tr_b16 a[158:159], v229 offset:12544\n\t" \
  "v_exp_f32 v13, v13\n\t" \
  "v_exp_f32 v14, v14\n\t" \
  "v_exp_f32 v15, v15\n\t" \
  "v_cvt_pk_bf16_f32 v134, v12, v13\n\t" \
  "v_cvt_pk_bf16_f32 v135, v14, v15\n\t" \
  "v_mfma_f32_16x16x32_bf16 v[12:15], a[252:255], v[28:31], v[176:179]\n\t" \
  "v_exp_f32 v32, v32\n\t" \
  "v_mfma_f32_16x16x32_bf16 v[12:15], a[248:251], v[24:27], v[12:15]\n\t" \
  "v_max3_f32 v203, v203, v8, v9\n\t" \
  "v_max3_f32 v192, v192, v10, v11\n\t" \
  "v_mfma_f32_16x16x32_bf16 v[12:15], a[244:247], v[20:23], v[12:15]\n\t" \
  "s_add_u32 s98, %[pf4], 0\n\t" \
  "buffer_load_dword %[pfd], v245, %[sG], s98 offen\n\t" \
  "v_mfma_f32_16x16x32_bf16 v[12:15], a[240:243], v[16:19], v[12:15]\n\t" \
  "v_exp_f32 v33, v33\n\t" \
  "v_exp_f32 v34, v34\n\t" \
  "v_exp_f32 v35, v35\n\t" \
  "v_cvt_pk_bf16_f32 v136, v32, v33\n\t" \
  "v_cvt_pk_bf16_f32 v137, v34, v35\n\t" \
  "v_mfma_f32_16x16x32_bf16 v[32:35], a[192:195], v[48:51], v[180:183]\n\t" \
  "ds_read_b64_tr_b16 a[160:161], v228 offset:512\n\t" \
  "ds_read_b64_tr_b16 a[162:163], v228 offset:4608\n\t" \
  "v_mfma_f32_16x16x32_bf16 v[32:35], a[196:199], v[52:55], v[32:35]\n\t" \
  "v_exp_f32 v36, v36\n\t" \
  "v_mfma_f32_16x16x32_bf16 v[32:35], a[200:203], v[56:59], v[32:35]\n\t" \
  "v_max3_f32 v203, v203, v12, v13\n\t" \
  "v_max3_f32 v192, v192, v14, v15\n\t" \
  "v_mfma_f32_16x16x32_bf16 v[32:35], a[204:207], v[60:63], v[32:35]\n\t" \
  "v_max_f32 v192, v192, v203\n\t" \
  "ds_read_b64_tr_b16 a[164:165], v228 offset:8704\n\t" \
  "v_exp_f32 v37, v37\n\t" \
  "v_exp_f32 v38, v38\n\t" \
  "v_exp_f32 v39, v39\n\t" \
  "v_cvt_pk_bf16_f32 v138, v36, v37\n\t" \
  "v_cvt_pk_bf16_f32 v139, v38, v39\n\t" \
  "v_mfma_f32_16x16x32_bf16 v[36:39], a[220:223], v[60:63], v[180:183]\n\t" \
  "v_cmp_lt_f32 vcc, 0x42800000, v192\n\t" \
  "ds_read_b64_tr_b16 a[166:167], v228 offset:12800\n\t" \
  "v_mfma_f32_16x16x32_bf16 v[36:39], a[216:219], v[56:59], v[36:39]\n\t" \
  "s_cbranch_vccnz .Lsf_c10\n\t" \
  ".Lsf_r10:\n\t" \
  "v_max3_f32 v192, v32, v33, v34\n\t" \
  "v_mfma_f32_16x16x32_bf16 v[36:39], a[212:215], v[52:55], v[36:39]\n\t" \
  "s_add_u32 s98, %[pf4], 14336\n\t" \
  "buffer_load_dword %[pfd], v245, %[sG], s98 offen\n\t" \
  "v_mfma_f32_16x16x32_bf16 v[36:39], a[208:211], v[48:51], v[36:39]\n\t" \
  "v_exp_f32 v40, v40\n\t" \
  "v_exp_f32 v41, v41\n\t" \
  "v_exp_f32 v42, v42\n\t" \
  "v_exp_f32 v43, v43\n\t" \
  "v_cvt_pk_bf16_f32 v140, v40, v41\n\t" \
  "v_cvt_pk_bf16_f32 v141, v42, v43\n\t" \
  "v_mfma_f32_16x16x32_bf16 v[40:43], a[224:227], v[48:51], v[180:183]\n\t" \
  "v_exp_f32 v44, v44\n\t" \
  "v_mfma_f32_16x16x32_bf16 v[40:43], a[228:231], v[52:55], v[40:43]\n\t" \
  "v_max3_f32 v203, v35, v36, v37\n\t" \
  "v_max3_f32 v192, v192, v38, v39\n\t" \
  "v_mfma_f32_16x16x32_bf16 v[40:43], a[232:235], v[56:59], v[40:43]\n\t" \
  "v_exp_f32 v45, v45\n\t" \
  "v_mfma_f32_16x16x32_bf16 v[40:43], a[236:239], v[60:63], v[40:43]\n\t" \
  "v_cvt_pk_bf16_f32 v142, v44, v45\n\t" \
  "ds_read_b64_tr_b16 a[168:169], v229 offset:512\n\t" \
  "v_exp_f32 v46, v46\n\t" \
  "v_exp_f32 v47, v47\n\t" \
  "s_nop 0\n\t" \
  "v_cvt_pk_bf16_f32 v143, v46, v47\n\t" \
  "v_mfma_f32_16x16x32_bf16 v[44:47], a[252:255], v[60:63], v[180:183]\n\t" \
  "v_exp_f32 v64, v64\n\t" \
  "v_mfma_f32_16x16x32_bf16 v[44:47], a[248:251], v[56:59], v[44:47]\n\t" \
  "ds_read_b64_tr_b16 a[170:171], v229 offset:4608\n\t" \
  "v_max3_f32 v203, v203, v40, v41\n\t" \
  "v_mfma_f32_16x16x32_bf16 v[44:47], a[244:247], v[52:55], v[44:47]\n\t" \
  "v_max3_f32 v192, v192, v42, v43\n\t" \
  "ds_read_b64_tr_b16 a[172:173], v229 offset:8704\n\t" \
  "v_mfma_f32_16x16x32_bf16 v[44:47], a[240:243], v[48:51], v[44:47]\n\t" \
  "v_exp_f32 v65, v65\n\t" \
  "v_exp_f32 v66, v66\n\t" \
  "v_exp_f32 v67, v67\n\t" \
  "v_cvt_pk_bf16_f32 v144, v64, v65\n\t" \
  "v_cvt_pk_bf16_f32 v145, v66, v67\n\t" \
  "v_mfma_f32_16x16x32_bf16 v[64:67], a[192:195], v[80:83], v[184:187]\n\t" \
  "v_exp_f32 v68, v68\n\t" \
  "v_mfma_f32_16x16x32_bf16 v[64:67], a[196:199], v[84:87], v[64:67]\n\t" \
  "v_max3_f32 v203, v203, v44, v45\n\t" \
  "v_max3_f32 v192, v192, v46, v47\n\t" \
  "v_mfma_f32_16x16x32_bf16 v[64:67], a[200:203], v[88:91], v[64:67]\n\t" \
  "v_max_f32 v192, v192, v203\n\t" \
  "ds_read_b64_tr_b16 a[174:175], v229 offset:12800\n\t" \
  "v_mfma_f32_16x16x32_bf16 v[64:67], a[204:207], v[92:95], v[64:67]\n\t" \
  "v_cmp_lt_f32 vcc, 0x42800000, v192\n\t" \
  "ds_read_b64_tr_b16 a[176:177], v228 offset:768\n\t" \
  "v_exp_f32 v69, v69\n\t" \
  "v_exp_f32 v70, v70\n\t" \
  "v_exp_f32 v71, v71\n\t" \
  "v_cvt_pk_bf16_f32 v146, v68, v69\n\t" \
  "v_cvt_pk_bf16_f32 v147, v70, v71\n\t" \
  "v_mfma_f32_16x16x32_bf16 v[68:71], a[220:223], v[92:95], v[184:187]\n\t" \
  "s_cbranch_vccnz .Lsf_c11\n\t" \
  ".Lsf_r11:\n\t" \
  "v_max3_f32 v192, v64, v65, v66\n\t" \
  "v_mfma_f32_16x16x32_bf16 v[68:71], a[216:219], v[88:91], v[68:71]\n\t" \
  "v_exp_f32 v72, v72\n\t" \
  "v_mfma_f32_16x16x32_bf16 v[68:71], a[212:215], v[84:87], v[68:71]\n\t" \
  "v_exp_f32 v73, v73\n\t" \
  "v_mfma_f32_16x16x32_bf16 v[68:71], a[208:211], v[80:83], v[68:71]\n\t" \
  "v_cvt_pk_bf16_f32 v148, v72, v73\n\t" \
  "ds_read_b64_tr_b16 a[178:179], v228 offset:4864\n\t" \
  "v_exp_f32 v74, v74\n\t" \
  "v_exp_f32 v75, v75\n\t" \
  "s_nop 0\n\t" \
  "v_cvt_pk_bf16_f32 v149, v74, v75\n\t" \
  "v_mfma_f32_16x16x32_bf16 v[72:75], a[224:227], v[80:83], v[184:187]\n\t" \
  "v_exp_f32 v76, v76\n\t" \
  "v_mfma_f32_16x16x32_bf16 v[72:75], a[228:231], v[84:87], v[72:75]\n\t" \
  "v_max3_f32 v203, v67, v68, v69\n\t" \
  "v_max3_f32 v192, v192, v70, v71\n\t" \
  "v_mfma_f32_16x16x32_bf16 v[72:75], a[232:235], v[88:91], v[72:75]\n\t" \
  "v_exp_f32 v77, v77\n\t" \
  "v_mfma_f32_16x16x32_bf16 v[72:75], a[236:239], v[92:95], v[72:75]\n\t" \
  "v_cvt_pk_bf16_f32 v150, v76, v77\n\t" \
  "ds_read_b64_tr_b16 a[180:181], v228 offset:8960\n\t" \
  "v_exp_f32 v78, v78\n\t" \
  "v_exp_f32 v79, v79\n\t" \
  "s_nop 0\n\t" \
  "v_cvt_pk_bf16_f32 v151, v78, v79\n\t" \
  "v_mfma_f32_16x16x32_bf16 v[76:79], a[252:255], v[92:95], v[184:187]\n\t" \
  "v_exp_f32 v96, v96\n\t" \
  "v_mfma_f32_16x16x32_bf16 v[76:79], a[248:251], v[88:91], v[76:79]\n\t" \
  "ds_read_b64_tr_b16 a[182:183], v228 offset:13056\n\t" \
  "v_max3_f32 v203, v203, v72, v73\n\t" \
  "v_mfma_f32_16x16x32_bf16 v[76:79], a[244:247], v[84:87], v[76:79]\n\t" \
  "v_max3_f32 v192, v192, v74, v75\n\t" \
  "ds_read_b64_tr_b16 a[184:185], v229 offset:768\n\t" \
  "v_mfma_f32_16x16x32_bf16 v[76:79], a[240:243], v[80:83], v[76:79]\n\t" \
  "v_exp_f32 v97, v97\n\t" \
  "v_exp_f32 v98, v98\n\t" \
  "v_exp_f32 v99, v99\n\t" \
  "v_cvt_pk_bf16_f32 v152, v96, v97\n\t" \
  "v_cvt_pk_bf16_f32 v153, v98, v99\n\t" \
  "v_mfma_f32_16x16x32_bf16 v[96:99], a[192:195], v[112:115], v[188:191]\n\t" \
  "v_exp_f32 v100, v100\n\t" \
  "v_mfma_f32_16x16x32_bf16 v[96:99], a[196:199], v[116:119], v[96:99]\n\t" \
  "v_max3_f32 v203, v203, v76, v77\n\t" \
  "v_max3_f32 v192, v192, v78, v79\n\t" \
  "v_mfma_f32_16x16x32_bf16 v[96:99], a[200:203], v[120:123], v[96:99]\n\t" \
  "v_max_f32 v192, v192, v203\n\t" \
  "ds_read_b64_tr_b16 a[186:187], v229 offset:4864\n\t" \
  "v_mfma_f32_16x16x32_bf16 v[96:99], a[204:207], v[124:127], v[96:99]\n\t" \
  "v_cmp_lt_f32 vcc, 0x42800000, v192\n\t" \
  "ds_read_b64_tr_b16 a[188:189], v229 offset:8960\n\t" \
  "v_exp_f32 v101, v101\n\t" \
  "v_exp_f32 v102, v102\n\t" \
  "v_exp_f32 v103, v103\n\t" \
  "v_cvt_pk_bf16_f32 v154, v100, v101\n\t" \
  "v_cvt_pk_bf16_f32 v155, v102, v103\n\t" \
  "v_mfma_f32_16x16x32_bf16 v[100:103], a[220:223], v[124:127], v[188:191]\n\t" \
  "s_cbranch_vccnz .Lsf_c12\n\t" \
  ".Lsf_r12:\n\t" \
  "v_max3_f32 v192, v96, v97, v98\n\t" \
  "v_mfma_f32_16x16x32_bf16 v[100:103], a[216:219], v[120:123], v[100:103]\n\t" \
  "v_exp_f32 v104, v104\n\t" \
  "v_mfma_f32_16x16x32_bf16 v[100:103], a[212:215], v[116:119], v[100:103]\n\t" \
  "v_exp_f32 v105, v105\n\t" \
  "v_mfma_f32_16x16x32_bf16 v[100:103], a[208:211], v[112:115], v[100:103]\n\t" \
  "v_cvt_pk_bf16_f32 v156, v104, v105\n\t" \
  "ds_read_b64_tr_b16 a[190:191], v229 offset:13056\n\t" \
  "v_exp_f32 v106, v106\n\t" \
  "v_exp_f32 v107, v107\n\t" \
  "s_nop 0\n\t" \
  "v_cvt_pk_bf16_f32 v157, v106, v107\n\t" \
  "v_mfma_f32_16x16x32_bf16 v[104:107], a[224:227], v[112:115], v[188:191]\n\t" \
  "v_exp_f32 v108, v108\n\t" \
  "v_mfma_f32_16x16x32_bf16 v[104:107], a[228:231], v[116:119], v[104:107]\n\t" \
  "v_exp_f32 v109, v109\n\t" \
  "v_mfma_f32_16x16x32_bf16 v[104:107], a[232:235], v[120:123], v[104:107]\n\t" \
  "v_cvt_pk_bf16_f32 v158, v108, v109\n\t" \
  "v_max3_f32 v203, v99, v100, v101\n\t" \
  "v_mfma_f32_16x16x32_bf16 v[104:107], a[236:239], v[124:127], v[104:107]\n\t" \
  "v_max3_f32 v192, v192, v102, v103\n\t" \
  "v_exp_f32 v110, v110\n\t" \
  "v_exp_f32 v111, v111\n\t" \
  "s_nop 0\n\t" \
  "v_cvt_pk_bf16_f32 v159, v110, v111\n\t" \
  "v_mfma_f32_16x16x32_bf16 v[108:111], a[252:255], v[124:127], v[188:191]\n\t" \
  "v_exp_f32 v0, v0\n\t" \
  "v_mfma_f32_16x16x32_bf16 v[108:111], a[248:251], v[120:123], v[108:111]\n\t" \
  "v_exp_f32 v1, v1\n\t" \
  "v_mfma_f32_16x16x32_bf16 v[108:111], a[244:247], v[116:119], v[108:111]\n\t" \
  "v_max3_f32 v203, v203, v104, v105\n\t" \
  "v_max3_f32 v192, v192, v106, v107\n\t" \
  "v_mfma_f32_16x16x32_bf16 v[108:111], a[240:243], v[112:115], v[108:111]\n\t" \
  "v_exp_f32 v2, v2\n\t" \
  "s_waitcnt vmcnt(3) lgkmcnt(0)\n\t" \
  "s_barrier\n\t" \
  "v_mfma_f32_16x16x32_bf16 a[0:3], a[128:131], v[128:131], 0\n\t" \
  "v_exp_f32 v3, v3\n\t" \
  "v_mfma_f32_16x16x32_bf16 a[0:3], a[132:135], v[132:135], a[0:3]\n\t" \
  "v_exp_f32 v4, v4\n\t" \
  "v_mfma_f32_16x16x32_bf16 a[16:19], a[136:139], v[128:131], 0\n\t" \
  "ds_read_b128 a[192:195], v220 offset:0\n\t" \
  "v_max3_f32 v203, v203, v108, v109\n\t" \
  "v_mfma_f32_16x16x32_bf16 a[16:19], a[140:143], v[132:135], a[16:19]\n\t" \
  "v_max3_f32 v192, v192, v110, v111\n\t" \
  "v_max_f32 v192, v192, v203\n\t" \
  "v_mfma_f32_16x16x32_bf16 a[32:35], a[144:147], v[128:131], 0\n\t" \
  "v_cmp_lt_f32 vcc, 0x42800000, v192\n\t" \
  "ds_read_b128 a[196:199], v221 offset:0\n\t" \
  "v_mfma_f32_16x16x32_bf16 a[32:35], a[148:151], v[132:135], a[32:35]\n\t" \
  "s_cbranch_vccnz .Lsf_c13\n\t" \
  ".Lsf_r13:\n\t" \
  "ds_read_b128 a[200:203], v222 offset:0\n\t" \
  "v_mfma_f32_16x16x32_bf16 a[48:51], a[152:155], v[128:131], 0\n\t" \
  "v_exp_f32 v5, v5\n\t" \
  "v_mfma_f32_16x16x32_bf16 a[48:51], a[156:159], v[132:135], a[48:51]\n\t" \
  "v_exp_f32 v6, v6\n\t" \
  "v_mfma_f32_16x16x32_bf16 a[64:67], a[160:163], v[128:131], 0\n\t" \
  "v_exp_f32 v7, v7\n\t" \
  "v_mfma_f32_16x16x32_bf16 a[64:67], a[164:167], v[132:135], a[64:67]\n\t" \
  "v_exp_f32 v8, v8\n\t" \
  "v_mfma_f32_16x16x32_bf16 a[80:83], a[168:171], v[128:131], 0\n\t" \
  "v_exp_f32 v9, v9\n\t" \
  "v_mfma_f32_16x16x32_bf16 a[80:83], a[172:175], v[132:135], a[80:83]\n\t" \
  "v_exp_f32 v10, v10\n\t" \
  "v_mfma_f32_16x16x32_bf16 a[96:99], a[176:179], v[128:131], 0\n\t" \
  "v_exp_f32 v11, v11\n\t" \
  "v_mfma_f32_16x16x32_bf16 a[96:99], a[180:183], v[132:135], a[96:99]\n\t" \
  "v_exp_f32 v12, v12\n\t" \
  "v_mfma_f32_16x16x32_bf16 a[112:115], a[184:187], v[128:131], 0\n\t" \
  "ds_read_b128 a[204:207], v223 offset:0\n\t" \
  "ds_read_b128 a[208:211], v220 offset:4096\n\t" \
  "v_mfma_f32_16x16x32_bf16 a[112:115], a[188:191], v[132:135], a[112:115]\n\t" \
  "v_exp_f32 v13, v13\n\t" \
  "v_mfma_f32_16x16x32_bf16 v[160:163], v[216:219], v[128:131], 0\n\t" \
  "v_exp_f32 v14, v14\n\t" \
  "v_mfma_f32_16x16x32_bf16 v[160:163], v[216:219], v[132:135], v[160:163]\n\t" \
  "v_cvt_pk_bf16_f32 v128, v0, v1\n\t" \
  "v_cvt_pk_bf16_f32 v129, v2, v3\n\t" \
  "v_mfma_f32_16x16x32_bf16 a[4:7], a[128:131], v[136:139], 0\n\t" \
  "v_cvt_pk_bf16_f32 v130, v4, v5\n\t" \
  "v_cvt_pk_bf16_f32 v131, v6, v7\n\t" \
  "v_mfma_f32_16x16x32_bf16 a[4:7], a[132:135], v[140:143], a[4:7]\n\t" \
  "v_cvt_pk_bf16_f32 v132, v8, v9\n\t" \
  "v_cvt_pk_bf16_f32 v133, v10, v11\n\t" \
  "v_mfma_f32_16x16x32_bf16 a[20:23], a[136:139], v[136:139], 0\n\t" \
  "v_cvt_pk_bf16_f32 v134, v12, v13\n\t" \
  "ds_read_b128 a[212:215], v221 offset:4096\n\t" \
  "v_mfma_f32_16x16x32_bf16 a[20:23], a[140:143], v[140:143], a[20:23]\n\t" \
  "v_exp_f32 v15, v15\n\t" \
  "v_mfma_f32_16x16x32_bf16 a[36:39], a[144:147], v[136:139], 0\n\t" \
  "v_cvt_pk_bf16_f32 v135, v14, v15\n\t" \
  "s_add_u32 m0, %[m], 65536\n\t" \
  "v_mfma_f32_16x16x32_bf16 a[36:39], a[148:151], v[140:143], a[36:39]\n\t" \
  "v_exp_f32 v32, v32\n\t" \
  "v_mfma_f32_16x16x32_bf16 a[52:55], a[152:155], v[136:139], 0\n\t" \
  "ds_read_b128 a[216:219], v222 offset:4096\n\t" \
  "s_add_u32 s96, %[sofk], 49152\n\t" \
  "v_mfma_f32_16x16x32_bf16 a[52:55], a[156:159], v[140:143], a[52:55]\n\t" \
  "v_exp_f32 v33, v33\n\t" \
  "v_mfma_f32_16x16x32_bf16 a[68:71], a[160:163], v[136:139], 0\n\t" \
  "v_exp_f32 v34, v34\n\t" \
  "v_mfma_f32_16x16x32_bf16 a[68:71], a[164:167], v[140:143], a[68:71]\n\t" \
  "ds_read_b128 a[220:223], v223 offset:4096\n\t" \
  "buffer_load_dwordx4 v224, %[sK], s96 offen offset:0 lds\n\t" \
  "v_mfma_f32_16x16x32_bf16 a[84:87], a[168:171], v[136:139], 0\n\t" \
  "v_exp_f32 v35, v35\n\t" \
  "v_mfma_f32_16x16x32_bf16 a[84:87], a[172:175], v[140:143], a[84:87]\n\t" \
  "v_exp_f32 v36, v36\n\t" \
  "v_mfma_f32_16x16x32_bf16 a[100:103], a[176:179], v[136:139], 0\n\t" \
  "v_exp_f32 v37, v37\n\t" \
  "v_mfma_f32_16x16x32_bf16 a[100:103], a[180:183], v[140:143], a[100:103]\n\t" \
  "ds_read_b128 a[224:227], v220 offset:8192\n\t" \
  "buffer_load_dwordx4 v225, %[sK], s96 offen offset:1024 lds\n\t" \
  "v_mfma_f32_16x16x32_bf16 a[116:119], a[184:187], v[136:139], 0\n\t" \
  "v_exp_f32 v38, v38\n\t" \
  "v_mfma_f32_16x16x32_bf16 a[116:119], a[188:191], v[140:143], a[116:119]\n\t" \
  "v_exp_f32 v39, v39\n\t" \
  "v_mfma_f32_16x16x32_bf16 v[164:167], v[216:219], v[136:139], 0\n\t" \
  "buffer_load_dwordx4 v226, %[sK], s96 offen offset:2048 lds\n\t" \
  "ds_read_b128 a[228:231], v221 offset:8192\n\t" \
  "v_mfma_f32_16x16x32_bf16 v[164:167], v[216:219], v[140:143], v[164:167]\n\t" \
  "v_cvt_pk_bf16_f32 v136, v32, v33\n\t" \
  "v_cvt_pk_bf16_f32 v137, v34, v35\n\t" \
  "v_mfma_f32_16x16x32_bf16 a[8:11], a[128:131], v[144:147], 0\n\t" \
  "v_cvt_pk_bf16_f32 v138, v36, v37\n\t" \
  "v_cvt_pk_bf16_f32 v139, v38, v39\n\t" \
  "v_mfma_f32_16x16x32_bf16 a[8:11], a[132:135], v[148:151], a[8:11]\n\t" \
  "v_exp_f32 v40, v40\n\t" \
  "v_mfma_f32_16x16x32_bf16 a[24:27], a[136:139], v[144:147], 0\n\t" \
  "buffer_load_dwordx4 v227, %[sK], s96 offen offset:3072 lds\n\t" \
  "ds_read_b128 a[232:235], v222 offset:8192\n\t" \
  "v_mfma_f32_16x16x32_bf16 a[24:27], a[140:143], v[148:151], a[24:27]\n\t" \
  "v_exp_f32 v41, v41\n\t" \
  "v_mfma_f32_16x16x32_bf16 a[40:43], a[144:147], v[144:147], 0\n\t" \
  "v_cvt_pk_bf16_f32 v140, v40, v41\n\t" \
  "ds_read_b128 a[236:239], v223 offset:8192\n\t" \
  "v_mfma_f32_16x16x32_bf16 a[40:43], a[148:151], v[148:151], a[40:43]\n\t" \
  "v_exp_f32 v42, v42\n\t" \
  "v_mfma_f32_16x16x32_bf16 a[56:59], a[152:155], v[144:147], 0\n\t" \
  "v_exp_f32 v43, v43\n\t" \
  "v_mfma_f32_16x16x32_bf16 a[56:59], a[156:159], v[148:151], a[56:59]\n\t" \
  "v_cvt_pk_bf16_f32 v141, v42, v43\n\t" \
  "ds_read_b128 a[240:243], v220 offset:12288\n\t" \
  "v_mfma_f32_16x16x32_bf16 a[72:75], a[160:163], v[144:147], 0\n\t" \
  "s_add_u32 m0, %[m], 114688\n\t" \
  "s_mov_b32 s96, 0\n\t" \
  "buffer_load_dwordx4 v232, %[sU], s96 offen offset:0 lds\n\t" \
  "v_mfma_f32_16x16x32_bf16 a[72:75], a[164:167], v[148:151], a[72:75]\n\t" \
  "v_exp_f32 v44, v44\n\t" \
  "v_mfma_f32_16x16x32_bf16 a[88:91], a[168:171], v[144:147], 0\n\t" \
  "s_add_u32 m0, %[m], 81920\n\t" \
  "s_add_u32 s96, %[sofk], 32768\n\t" \
  "v_mfma_f32_16x16x32_bf16 a[88:91], a[172:175], v[148:151], a[88:91]\n\t" \
  "v_exp_f32 v45, v45\n\t" \
  "v_mfma_f32_16x16x32_bf16 a[104:107], a[176:179], v[144:147], 0\n\t" \
  "v_cvt_pk_bf16_f32 v142, v44, v45\n\t" \
  "ds_read_b128 a[244:247], v221 offset:12288\n\t" \
  "v_mfma_f32_16x16x32_bf16 a[104:107], a[180:183], v[148:151], a[104:107]\n\t" \
  "v_exp_f32 v46, v46\n\t" \
  "v_mfma_f32_16x16x32_bf16 a[120:123], a[184:187], v[144:147], 0\n\t" \
  "v_exp_f32 v47, v47\n\t" \
  "v_mfma_f32_16x16x32_bf16 a[120:123], a[188:191], v[148:151], a[120:123]\n\t" \
  "v_cvt_pk_bf16_f32 v143, v46, v47\n\t" \
  "buffer_load_dwordx4 v230, %[sV], s96 offen offset:0 lds\n\t" \
  "v_mfma_f32_16x16x32_bf16 v[168:171], v[216:219], v[144:147], 0\n\t" \
  "v_exp_f32 v64, v64\n\t" \
  "v_mfma_f32_16x16x32_bf16 v[168:171], v[216:219], v[148:151], v[168:171]\n\t" \
  "v_exp_f32 v65, v65\n\t" \
  "v_mfma_f32_16x16x32_bf16 a[12:15], a[128:131], v[152:155], 0\n\t" \
  "v_cvt_pk_bf16_f32 v144, v64, v65\n\t" \
  "ds_read_b128 a[248:251], v222 offset:12288\n\t" \
  "v_mfma_f32_16x16x32_bf16 a[12:15], a[132:135], v[156:159], a[12:15]\n\t" \
  "v_exp_f32 v66, v66\n\t" \
  "v_mfma_f32_16x16x32_bf16 a[28:31], a[136:139], v[152:155], 0\n\t" \
  "v_exp_f32 v67, v67\n\t" \
  "v_mfma_f32_16x16x32_bf16 a[28:31], a[140:143], v[156:159], a[28:31]\n\t" \
  "v_cvt_pk_bf16_f32 v145, v66, v67\n\t" \
  "buffer_load_dwordx4 v231, %[sV], s96 offen offset:1024 lds\n\t" \
  "v_mfma_f32_16x16x32_bf16 a[44:47], a[144:147], v[152:155], 0\n\t" \
  "ds_read_b128 a[252:255], v223 offset:12288\n\t" \
  "buffer_load_dwordx4 v230, %[sV], s96 offen offset:2048 lds\n\t" \
  "v_mfma_f32_16x16x32_bf16 a[44:47], a[148:151], v[156:159], a[44:47]\n\t" \
  "v_exp_f32 v68, v68\n\t" \
  "v_mfma_f32_16x16x32_bf16 a[60:63], a[152:155], v[152:155], 0\n\t" \
  "v_exp_f32 v69, v69\n\t" \
  "v_mfma_f32_16x16x32_bf16 a[60:63], a[156:159], v[156:159], a[60:63]\n\t" \
  "v_cvt_pk_bf16_f32 v146, v68, v69\n\t" \
  "buffer_load_dwordx4 v231, %[sV], s96 offen offset:3072 lds\n\t" \
  "v_mfma_f32_16x16x32_bf16 a[76:79], a[160:163], v[152:155], 0\n\t" \
  "v_exp_f32 v70, v70\n\t" \
  "v_mfma_f32_16x16x32_bf16 a[76:79], a[164:167], v[156:159], a[76:79]\n\t" \
  "v_exp_f32 v71, v71\n\t" \
  "v_mfma_f32_16x16x32_bf16 a[92:95], a[168:171], v[152:155], 0\n\t" \
  "v_cvt_pk_bf16_f32 v147, v70, v71\n\t" \
  "v_mfma_f32_16x16x32_bf16 a[92:95], a[172:175], v[156:159], a[92:95]\n\t" \
  "v_exp_f32 v72, v72\n\t" \
  "v_mfma_f32_16x16x32_bf16 a[108:111], a[176:179], v[152:155], 0\n\t" \
  "v_exp_f32 v73, v73\n\t" \
  "v_mfma_f32_16x16x32_bf16 a[108:111], a[180:183], v[156:159], a[108:111]\n\t" \
  "v_cvt_pk_bf16_f32 v148, v72, v73\n\t" \
  "v_mfma_f32_16x16x32_bf16 a[124:127], a[184:187], v[152:155], 0\n\t" \
  "s_add_u32 m0, %[m], 114688\n\t" \
  "s_mov_b32 s96, 6144\n\t" \
  "buffer_load_dwordx4 v232, %[sU], s96 offen offset:1024 lds\n\t" \
  "v_mfma_f32_16x16x32_bf16 a[124:127], a[188:191], v[156:159], a[124:127]\n\t" \
  "v_exp_f32 v74, v74\n\t" \
  "v_mfma_f32_16x16x32_bf16 v[172:175], v[216:219], v[152:155], 0\n\t" \
  "v_exp_f32 v75, v75\n\t" \
  "v_mfma_f32_16x16x32_bf16 v[172:175], v[216:219], v[156:159], v[172:175]\n\t" \
  "v_cvt_pk_bf16_f32 v149, v74, v75\n\t" \
  "s_cmp_lg_u32 s99, 0\n\t" \
  "s_cbranch_scc1 .Lsf_p1\n\t" \
  ".Lsf_q1:\n\t" \
  "s_waitcnt lgkmcnt(0)\n\t" \
  "v_mfma_f32_16x16x32_bf16 v[0:3], a[192:195], v[16:19], v[176:179]\n\t" \
  "v_exp_f32 v76, v76\n\t" \
  "v_mfma_f32_16x16x32_bf16 v[0:3], a[196:199], v[20:23], v[0:3]\n\t" \
  "v_exp_f32 v77, v77\n\t" \
  "v_mfma_f32_16x16x32_bf16 v[0:3], a[200:203], v[24:27], v[0:3]\n\t" \
  "v_cvt_pk_bf16_f32 v150, v76, v77\n\t" \
  "ds_read_b64_tr_b16 a[128:129], v237 offset:0\n\t" \
  "v_mfma_f32_16x16x32_bf16 v[0:3], a[204:207], v[28:31], v[0:3]\n\t" \
  "v_exp_f32 v78, v78\n\t" \
  "v_mfma_f32_16x16x32_bf16 v[4:7], a[220:223], v[28:31], v[176:179]\n\t" \
  "ds_read_b64_tr_b16 a[130:131], v237 offset:4096\n\t" \
  "ds_read_b64_tr_b16 a[132:133], v237 offset:8192\n\t" \
  "v_mfma_f32_16x16x32_bf16 v[4:7], a[216:219], v[24:27], v[4:7]\n\t" \
  "v_exp_f32 v79, v79\n\t" \
  "v_mfma_f32_16x16x32_bf16 v[4:7], a[212:215], v[20:23], v[4:7]\n\t" \
  "v_cvt_pk_bf16_f32 v151, v78, v79\n\t" \
  "v_max3_f32 v192, v0, v1, v2\n\t" \
  "v_mfma_f32_16x16x32_bf16 v[4:7], a[208:211], v[16:19], v[4:7]\n\t" \
  "v_exp_f32 v96, v96\n\t" \
  "v_mfma_f32_16x16x32_bf16 v[8:11], a[224:227], v[16:19], v[176:179]\n\t" \
  "ds_read_b64_tr_b16 a[134:135], v237 offset:12288\n\t" \
  "ds_read_b64_tr_b16 a[136:137], v238 offset:0\n\t" \
  "v_mfma_f32_16x16x32_bf16 v[8:11], a[228:231], v[20:23], v[8:11]\n\t" \
  "s_add_u32 m0, %[m], 114688\n\t" \
  "s_mov_b32 s96, 12288\n\t" \
  "buffer_load_dwordx4 v232, %[sU], s96 offen offset:2048 lds\n\t" \
  "v_mfma_f32_16x16x32_bf16 v[8:11], a[232:235], v[24:27], v[8:11]\n\t" \
  "v_max3_f32 v203, v3, v4, v5\n\t" \
  "v_max3_f32 v192, v192, v6, v7\n\t" \
  "v_mfma_f32_16x16x32_bf16 v[8:11], a[236:239], v[28:31], v[8:11]\n\t" \
  "ds_read_b64_tr_b16 a[138:139], v238 offset:4096\n\t" \
  "ds_read_b64_tr_b16 a[140:141], v238 offset:8192\n\t" \
  "v_mfma_f32_16x16x32_bf16 v[12:15], a[252:255], v[28:31], v[176:179]\n\t" \
  "v_exp_f32 v97, v97\n\t" \
  "v_mfma_f32_16x16x32_bf16 v[12:15], a[248:251], v[24:27], v[12:15]\n\t" \
  "v_cvt_pk_bf16_f32 v152, v96, v97\n\t" \
  "ds_read_b64_tr_b16 a[142:143], v238 offset:12288\n\t" \
  "v_mfma_f32_16x16x32_bf16 v[12:15], a[244:247], v[20:23], v[12:15]\n\t" \
  "v_max3_f32 v203, v203, v8, v9\n\t" \
  "v_max3_f32 v192, v192, v10, v11\n\t" \
  "v_mfma_f32_16x16x32_bf16 v[12:15], a[240:243], v[16:19], v[12:15]\n\t" \
  "v_exp_f32 v98, v98\n\t" \
  "v_mfma_f32_16x16x32_bf16 v[32:35], a[192:195], v[48:51], v[180:183]\n\t" \
  "ds_read_b64_tr_b16 a[144:145], v237 offset:256\n\t" \
  "ds_read_b64_tr_b16 a[146:147], v237 offset:4352\n\t" \
  "v_mfma_f32_16x16x32_bf16 v[32:35], a[196:199], v[52:55], v[32:35]\n\t" \
  "v_exp_f32 v99, v99\n\t" \
  "v_mfma_f32_16x16x32_bf16 v[32:35], a[200:203], v[56:59], v[32:35]\n\t" \
  "v_cvt_pk_bf16_f32 v153, v98, v99\n\t" \
  "v_max3_f32 v203, v203, v12, v13\n\t" \
  "v_mfma_f32_16x16x32_bf16 v[32:35], a[204:207], v[60:63], v[32:35]\n\t" \
  "v_max3_f32 v192, v192, v14, v15\n\t" \
  "v_max_f32 v192, v192, v203\n\t" \
  "v_mfma_f32_16x16x32_bf16 v[36:39], a[220:223], v[60:63], v[180:183]\n\t" \
  "v_cmp_lt_f32 vcc, 0x42800000, v192\n\t" \
  "ds_read_b64_tr_b16 a[148:149], v237 offset:8448\n\t" \
  "v_mfma_f32_16x16x32_bf16 v[36:39], a[216:219], v[56:59], v[36:39]\n\t" \
  "s_cbranch_vccnz .Lsf_c20\n\t" \
  ".Lsf_r20:\n\t" \
  "ds_read_b64_tr_b16 a[150:151], v237 offset:12544\n\t" \
  "v_mfma_f32_16x16x32_bf16 v[36:39], a[212:215], v[52:55], v[36:39]\n\t" \
  "v_max3_f32 v192, v32, v33, v34\n\t" \
  "ds_read_b64_tr_b16 a[152:153], v238 offset:256\n\t" \
  "v_mfma_f32_16x16x32_bf16 v[36:39], a[208:211], v[48:51], v[36:39]\n\t" \
  "s_mov_b32 s96, 18432\n\t" \
  "buffer_load_dwordx4 v232, %[sU], s96 offen offset:3072 lds\n\t" \
  "v_mfma_f32_16x16x32_bf16 v[40:43], a[224:227], v[48:51], v[180:183]\n\t" \
  "v_exp_f32 v100, v100\n\t" \
  "v_mfma_f32_16x16x32_bf16 v[40:43], a[228:231], v[52:55], v[40:43]\n\t" \
  "ds_read_b64_tr_b16 a[154:155], v238 offset:4352\n\t" \
  "ds_read_b64_tr_b16 a[156:157], v238 offset:8448\n\t" \
  "v_mfma_f32_16x16x32_bf16 v[40:43], a[232:235], v[56:59], v[40:43]\n\t" \
  "v_max3_f32 v203, v35, v36, v37\n\t" \
  "v_max3_f32 v192, v192, v38, v39\n\t" \
  "v_mfma_f32_16x16x32_bf16 v[40:43], a[236:239], v[60:63], v[40:43]\n\t" \
  "v_exp_f32 v101, v101\n\t" \
  "v_mfma_f32_16x16x32_bf16 v[44:47], a[252:255], v[60:63], v[180:183]\n\t" \
  "v_cvt_pk_bf16_f32 v154, v100, v101\n\t" \
  "ds_read_b64_tr_b16 a[158:159], v238 offset:12544\n\t" \
  "v_mfma_f32_16x16x32_bf16 v[44:47], a[248:251], v[56:59], v[44:47]\n\t" \
  "v_exp_f32 v102, v102\n\t" \
  "v_mfma_f32_16x16x32_bf16 v[44:47], a[244:247], v[52:55], v[44:47]\n\t" \
  "ds_read_b64_tr_b16 a[160:161], v237 offset:512\n\t" \
  "v_max3_f32 v203, v203, v40, v41\n\t" \
  "v_mfma_f32_16x16x32_bf16 v[44:47], a[240:243], v[48:51], v[44:47]\n\t" \
  "v_max3_f32 v192, v192, v42, v43\n\t" \
  "ds_read_b64_tr_b16 a[162:163], v237 offset:4608\n\t" \
  "v_mfma_f32_16x16x32_bf16 v[64:67], a[192:195], v[80:83], v[184:187]\n\t" \
  "v_exp_f32 v103, v103\n\t" \
  "v_mfma_f32_16x16x32_bf16 v[64:67], a[196:199], v[84:87], v[64:67]\n\t" \
  "v_cvt_pk_bf16_f32 v155, v102, v103\n\t" \
  "ds_read_b64_tr_b16 a[164:165], v237 offset:8704\n\t" \
  "v_mfma_f32_16x16x32_bf16 v[64:67], a[200:203], v[88:91], v[64:67]\n\t" \
  "v_max3_f32 v203, v203, v44, v45\n\t" \
  "v_max3_f32 v192, v192, v46, v47\n\t" \
  "v_mfma_f32_16x16x32_bf16 v[64:67], a[204:207], v[92:95], v[64:67]\n\t" \
  "v_max_f32 v192, v192, v203\n\t" \
  "ds_read_b64_tr_b16 a[166:167], v237 offset:12800\n\t" \
  "v_mfma_f32_16x16x32_bf16 v[68:71], a[220:223], v[92:95], v[184:187]\n\t" \
  "v_cmp_lt_f32 vcc, 0x42800000, v192\n\t" \
  "ds_read_b64_tr_b16 a[168:169], v238 offset:512\n\t" \
  "v_mfma_f32_16x16x32_bf16 v[68:71], a[216:219], v[88:91], v[68:71]\n\t" \
  "s_cbranch_vccnz .Lsf_c21\n\t" \
  ".Lsf_r21:\n\t" \
  "ds_read_b64_tr_b16 a[170:171], v238 offset:4608\n\t" \
  "v_mfma_f32_16x16x32_bf16 v[68:71], a[212:215], v[84:87], v[68:71]\n\t" \
  "s_add_u32 m0, %[m], 131072\n\t" \
  "s_mov_b32 s96, 28672\n\t" \
  "buffer_load_dwordx4 v232, %[sU], s96 offen offset:0 lds\n\t" \
  "v_mfma_f32_16x16x32_bf16 v[68:71], a[208:211], v[80:83], v[68:71]\n\t" \
  "v_max3_f32 v192, v64, v65, v66\n\t" \
  "ds_read_b64_tr_b16 a[172:173], v238 offset:8704\n\t" \
  "v_mfma_f32_16x16x32_bf16 v[72:75], a[224:227], v[80:83], v[184:187]\n\t" \
  "v_exp_f32 v104, v104\n\t" \
  "v_mfma_f32_16x16x32_bf16 v[72:75], a[228:231], v[84:87], v[72:75]\n\t" \
  "v_exp_f32 v105, v105\n\t" \
  "v_mfma_f32_16x16x32_bf16 v[72:75], a[232:235], v[88:91], v[72:75]\n\t" \
  "v_cvt_pk_bf16_f32 v156, v104, v105\n\t" \
  "v_max3_f32 v203, v67, v68, v69\n\t" \
  "v_mfma_f32_16x16x32_bf16 v[72:75], a[236:239], v[92:95], v[72:75]\n\t" \
  "v_max3_f32 v192, v192, v70, v71\n\t" \
  "ds_read_b64_tr_b16 a[174:175], v238 offset:12800\n\t" \
  "v_mfma_f32_16x16x32_bf16 v[76:79], a[252:255], v[92:95], v[184:187]\n\t" \
  "ds_read_b64_tr_b16 a[176:177], v237 offset:768\n\t" \
  "ds_read_b64_tr_b16 a[178:179], v237 offset:4864\n\t" \
  "v_mfma_f32_16x16x32_bf16 v[76:79], a[248:251], v[88:91], v[76:79]\n\t" \
  "v_exp_f32 v106, v106\n\t" \
  "v_mfma_f32_16x16x32_bf16 v[76:79], a[244:247], v[84:87], v[76:79]\n\t" \
  "v_max3_f32 v203, v203, v72, v73\n\t" \
  "v_max3_f32 v192, v192, v74, v75\n\t" \
  "v_mfma_f32_16x16x32_bf16 v[76:79], a[240:243], v[80:83], v[76:79]\n\t" \
  "ds_read_b64_tr_b16 a[180:181], v237 offset:8960\n\t" \
  "ds_read_b64_tr_b16 a[182:183], v237 offset:13056\n\t" \
  "v_mfma_f32_16x16x32_bf16 v[96:99], a[192:195], v[112:115], v[188:191]\n\t" \
  "v_exp_f32 v107, v107\n\t" \
  "v_mfma_f32_16x16x32_bf16 v[96:99], a[196:199], v[116:119], v[96:99]\n\t" \
  "v_cvt_pk_bf16_f32 v157, v106, v107\n\t" \
  "ds_read_b64_tr_b16 a[184:185], v238 offset:768\n\t" \
  "v_mfma_f32_16x16x32_bf16 v[96:99], a[200:203], v[120:123], v[96:99]\n\t" \
  "s_mov_b32 s96, 34816\n\t" \
  "buffer_load_dwordx4 v232, %[sU], s96 offen offset:1024 lds\n\t" \
  "v_mfma_f32_16x16x32_bf16 v[96:99], a[204:207], v[124:127], v[96:99]\n\t" \
  "v_max3_f32 v203, v203, v76, v77\n\t" \
  "v_max3_f32 v192, v192, v78, v79\n\t" \
  "v_mfma_f32_16x16x32_bf16 v[100:103], a[220:223], v[124:127], v[188:191]\n\t" \
  "v_max_f32 v192, v192, v203\n\t" \
  "ds_read_b64_tr_b16 a[186:187], v238 offset:4864\n\t" \
  "v_mfma_f32_16x16x32_bf16 v[100:103], a[216:219], v[120:123], v[100:103]\n\t" \
  "v_cmp_lt_f32 vcc, 0x42800000, v192\n\t" \
  "ds_read_b64_tr_b16 a[188:189], v238 offset:8960\n\t" \
  "v_mfma_f32_16x16x32_bf16 v[100:103], a[212:215], v[116:119], v[100:103]\n\t" \
  "s_cbranch_vccnz .Lsf_c22\n\t" \
  ".Lsf_r22:\n\t" \
  "v_max3_f32 v192, v96, v97, v98\n\t" \
  "v_mfma_f32_16x16x32_bf16 v[100:103], a[208:211], v[112:115], v[100:103]\n\t" \
  "v_exp_f32 v108, v108\n\t" \
  "v_mfma_f32_16x16x32_bf16 v[104:107], a[224:227], v[112:115], v[188:191]\n\t" \
  "v_exp_f32 v109, v109\n\t" \
  "v_mfma_f32_16x16x32_bf16 v[104:107], a[228:231], v[116:119], v[104:107]\n\t" \
  "v_cvt_pk_bf16_f32 v158, v108, v109\n\t" \
  "ds_read_b64_tr_b16 a[190:191], v238 offset:13056\n\t" \
  "v_mfma_f32_16x16x32_bf16 v[104:107], a[232:235], v[120:123], v[104:107]\n\t" \
  "v_exp_f32 v110, v110\n\t" \
  "v_mfma_f32_16x16x32_bf16 v[104:107], a[236:239], v[124:127], v[104:107]\n\t" \
  "v_max3_f32 v203, v99, v100, v101\n\t" \
  "v_max3_f32 v192, v192, v102, v103\n\t" \
  "v_exp_f32 v111, v111\n\t" \
  "s_nop 0\n\t" \
  "v_cvt_pk_bf16_f32 v159, v110, v111\n\t" \
  "v_mfma_f32_16x16x32_bf16 v[108:111], a[252:255], v[124:127], v[188:191]\n\t" \
  "v_exp_f32 v0, v0\n\t" \
  "v_mfma_f32_16x16x32_bf16 v[108:111], a[248:251], v[120:123], v[108:111]\n\t" \
  "v_exp_f32 v1, v1\n\t" \
  "v_mfma_f32_16x16x32_bf16 v[108:111], a[244:247], v[116:119], v[108:111]\n\t" \
  "v_max3_f32 v203, v203, v104, v105\n\t" \
  "v_max3_f32 v192, v192, v106, v107\n\t" \
  "v_mfma_f32_16x16x32_bf16 v[108:111], a[240:243], v[112:115], v[108:111]\n\t" \
  "v_exp_f32 v2, v2\n\t" \
  "s_waitcnt vmcnt(5) lgkmcnt(0)\n\t" \
  "s_barrier\n\t" \
  "v_mfma_f32_16x16x32_bf16 a[0:3], a[128:131], v[128:131], a[0:3]\n\t" \
  "v_exp_f32 v3, v3\n\t" \
  "v_mfma_f32_16x16x32_bf16 a[0:3], a[132:135], v[132:135], a[0:3]\n\t" \
  "v_exp_f32 v4, v4\n\t" \
  "v_mfma_f32_16x16x32_bf16 a[16:19], a[136:139], v[128:131], a[16:19]\n\t" \
  "ds_read_b128 a[192:195], v220 offset:16384\n\t" \
  "v_max3_f32 v203, v203, v108, v109\n\t" \
  "v_mfma_f32_16x16x32_bf16 a[16:19], a[140:143], v[132:135], a[16:19]\n\t" \
  "v_max3_f32 v192, v192, v110, v111\n\t" \
  "v_max_f32 v192, v192, v203\n\t" \
  "v_mfma_f32_16x16x32_bf16 a[32:35], a[144:147], v[128:131], a[32:35]\n\t" \
  "v_cmp_lt_f32 vcc, 0x42800000, v192\n\t" \
  "ds_read_b128 a[196:199], v221 offset:16384\n\t" \
  "v_mfma_f32_16x16x32_bf16 a[32:35], a[148:151], v[132:135], a[32:35]\n\t" \
  "s_cbranch_vccnz .Lsf_c23\n\t" \
  ".Lsf_r23:\n\t" \
  "ds_read_b128 a[200:203], v222 offset:16384\n\t" \
  "v_mfma_f32_16x16x32_bf16 a[48:51], a[152:155], v[128:131], a[48:51]\n\t" \
  "v_exp_f32 v5, v5\n\t" \
  "v_mfma_f32_16x16x32_bf16 a[48:51], a[156:159], v[132:135], a[48:51]\n\t" \
  "v_exp_f32 v6, v6\n\t" \
  "v_mfma_f32_16x16x32_bf16 a[64:67], a[160:163], v[128:131], a[64:67]\n\t" \
  "v_exp_f32 v7, v7\n\t" \
  "v_mfma_f32_16x16x32_bf16 a[64:67], a[164:167], v[132:135], a[64:67]\n\t" \
  "v_exp_f32 v8, v8\n\t" \
  "v_mfma_f32_16x16x32_bf16 a[80:83], a[168:171], v[128:131], a[80:83]\n\t" \
  "v_exp_f32 v9, v9\n\t" \
  "v_mfma_f32_16x16x32_bf16 a[80:83], a[172:175], v[132:135], a[80:83]\n\t" \
  "v_exp_f32 v10, v10\n\t" \
  "v_mfma_f32_16x16x32_bf16 a[96:99], a[176:179], v[128:131], a[96:99]\n\t" \
  "v_exp_f32 v11, v11\n\t" \
  "v_mfma_f32_16x16x32_bf16 a[96:99], a[180:183], v[132:135], a[96:99]\n\t" \
  "v_exp_f32 v12, v12\n\t" \
  "v_mfma_f32_16x16x32_bf16 a[112:115], a[184:187], v[128:131], a[112:115]\n\t" \
  "ds_read_b128 a[204:207], v223 offset:16384\n\t" \
  "ds_read_b128 a[208:211], v220 offset:20480\n\t" \
  "v_mfma_f32_16x16x32_bf16 a[112:115], a[188:191], v[132:135], a[112:115]\n\t" \
  "v_exp_f32 v13, v13\n\t" \
  "v_mfma_f32_16x16x32_bf16 v[160:163], v[216:219], v[128:131], v[160:163]\n\t" \
  "v_exp_f32 v14, v14\n\t" \
  "v_mfma_f32_16x16x32_bf16 v[160:163], v[216:219], v[132:135], v[160:163]\n\t" \
  "v_cvt_pk_bf16_f32 v128, v0, v1\n\t" \
  "v_cvt_pk_bf16_f32 v129, v2, v3\n\t" \
  "v_mfma_f32_16x16x32_bf16 a[4:7], a[128:131], v[136:139], a[4:7]\n\t" \
  "v_cvt_pk_bf16_f32 v130, v4, v5\n\t" \
  "v_cvt_pk_bf16_f32 v131, v6, v7\n\t" \
  "v_mfma_f32_16x16x32_bf16 a[4:7], a[132:135], v[140:143], a[4:7]\n\t" \
  "v_cvt_pk_bf16_f32 v132, v8, v9\n\t" \
  "v_cvt_pk_bf16_f32 v133, v10, v11\n\t" \
  "v_mfma_f32_16x16x32_bf16 a[20:23], a[136:139], v[136:139], a[20:23]\n\t" \
  "v_cvt_pk_bf16_f32 v134, v12, v13\n\t" \
  "ds_read_b128 a[212:215], v221 offset:20480\n\t" \
  "v_mfma_f32_16x16x32_bf16 a[20:23], a[140:143], v[140:143], a[20:23]\n\t" \
  "v_exp_f32 v15, v15\n\t" \
  "v_mfma_f32_16x16x32_bf16 a[36:39], a[144:147], v[136:139], a[36:39]\n\t" \
  "v_cvt_pk_bf16_f32 v135, v14, v15\n\t" \
  "s_add_u32 m0, %[m], 49152\n\t" \
  "v_mfma_f32_16x16x32_bf16 a[36:39], a[148:151], v[140:143], a[36:39]\n\t" \
  "v_exp_f32 v32, v32\n\t" \
  "v_mfma_f32_16x16x32_bf16 a[52:55], a[152:155], v[136:139], a[52:55]\n\t" \
  "ds_read_b128 a[216:219], v222 offset:20480\n\t" \
  "s_add_u32 s96, %[sofk], 65536\n\t" \
  "v_mfma_f32_16x16x32_bf16 a[52:55], a[156:159], v[140:143], a[52:55]\n\t" \
  "v_exp_f32 v33, v33\n\t" \
  "v_mfma_f32_16x16x32_bf16 a[68:71], a[160:163], v[136:139], a[68:71]\n\t" \
  "v_exp_f32 v34, v34\n\t" \
  "v_mfma_f32_16x16x32_bf16 a[68:71], a[164:167], v[140:143], a[68:71]\n\t" \
  "ds_read_b128 a[220:223], v223 offset:20480\n\t" \
  "buffer_load_dwordx4 v224, %[sK], s96 offen offset:0 lds\n\t" \
  "v_mfma_f32_16x16x32_bf16 a[84:87], a[168:171], v[136:139], a[84:87]\n\t" \
  "v_exp_f32 v35, v35\n\t" \
  "v_mfma_f32_16x16x32_bf16 a[84:87], a[172:175], v[140:143], a[84:87]\n\t" \
  "v_exp_f32 v36, v36\n\t" \
  "v_mfma_f32_16x16x32_bf16 a[100:103], a[176:179], v[136:139], a[100:103]\n\t" \
  "v_exp_f32 v37, v37\n\t" \
  "v_mfma_f32_16x16x32_bf16 a[100:103], a[180:183], v[140:143], a[100:103]\n\t" \
  "ds_read_b128 a[224:227], v220 offset:24576\n\t" \
  "buffer_load_dwordx4 v225, %[sK], s96 offen offset:1024 lds\n\t" \
  "v_mfma_f32_16x16x32_bf16 a[116:119], a[184:187], v[136:139], a[116:119]\n\t" \
  "v_exp_f32 v38, v38\n\t" \
  "v_mfma_f32_16x16x32_bf16 a[116:119], a[188:191], v[140:143], a[116:119]\n\t" \
  "v_exp_f32 v39, v39\n\t" \
  "v_mfma_f32_16x16x32_bf16 v[164:167], v[216:219], v[136:139], v[164:167]\n\t" \
  "buffer_load_dwordx4 v226, %[sK], s96 offen offset:2048 lds\n\t" \
  "ds_read_b128 a[228:231], v221 offset:24576\n\t" \
  "v_mfma_f32_16x16x32_bf16 v[164:167], v[216:219], v[140:143], v[164:167]\n\t" \
  "v_cvt_pk_bf16_f32 v136, v32, v33\n\t" \
  "v_cvt_pk_bf16_f32 v137, v34, v35\n\t" \
  "v_mfma_f32_16x16x32_bf16 a[8:11], a[128:131], v[144:147], a[8:11]\n\t" \
  "v_cvt_pk_bf16_f32 v138, v36, v37\n\t" \
  "v_cvt_pk_bf16_f32 v139, v38, v39\n\t" \
  "v_mfma_f32_16x16x32_bf16 a[8:11], a[132:135], v[148:151], a[8:11]\n\t" \
  "v_exp_f32 v40, v40\n\t" \
  "v_mfma_f32_16x16x32_bf16 a[24:27], a[136:139], v[144:147], a[24:27]\n\t" \
  "buffer_load_dwordx4 v227, %[sK], s96 offen offset:3072 lds\n\t" \
  "ds_read_b128 a[232:235], v222 offset:24576\n\t" \
  "v_mfma_f32_16x16x32_bf16 a[24:27], a[140:143], v[148:151], a[24:27]\n\t" \
  "v_exp_f32 v41, v41\n\t" \
  "v_mfma_f32_16x16x32_bf16 a[40:43], a[144:147], v[144:147], a[40:43]\n\t" \
  "v_cvt_pk_bf16_f32 v140, v40, v41\n\t" \
  "ds_read_b128 a[236:239], v223 offset:24576\n\t" \
  "v_mfma_f32_16x16x32_bf16 a[40:43], a[148:151], v[148:151], a[40:43]\n\t" \
  "v_exp_f32 v42, v42\n\t" \
  "v_mfma_f32_16x16x32_bf16 a[56:59], a[152:155], v[144:147], a[56:59]\n\t" \
  "v_exp_f32 v43, v43\n\t" \
  "v_mfma_f32_16x16x32_bf16 a[56:59], a[156:159], v[148:151], a[56:59]\n\t" \
  "v_cvt_pk_bf16_f32 v141, v42, v43\n\t" \
  "ds_read_b128 a[240:243], v220 offset:28672\n\t" \
  "v_mfma_f32_16x16x32_bf16 a[72:75], a[160:163], v[144:147], a[72:75]\n\t" \
  "s_add_u32 m0, %[m], 131072\n\t" \
  "s_mov_b32 s96, 40960\n\t" \
  "buffer_load_dwordx4 v232, %[sU], s96 offen offset:2048 lds\n\t" \
  "v_mfma_f32_16x16x32_bf16 a[72:75], a[164:167], v[148:151], a[72:75]\n\t" \
  "v_exp_f32 v44, v44\n\t" \
  "v_mfma_f32_16x16x32_bf16 a[88:91], a[168:171], v[144:147], a[88:91]\n\t" \
  "s_add_u32 m0, %[m], 147456\n\t" \
  "s_add_u32 s96, %[sofk], 49152\n\t" \
  "v_mfma_f32_16x16x32_bf16 a[88:91], a[172:175], v[148:151], a[88:91]\n\t" \
  "v_exp_f32 v45, v45\n\t" \
  "v_mfma_f32_16x16x32_bf16 a[104:107], a[176:179], v[144:147], a[104:107]\n\t" \
  "v_cvt_pk_bf16_f32 v142, v44, v45\n\t" \
  "ds_read_b128 a[244:247], v221 offset:28672\n\t" \
  "v_mfma_f32_16x16x32_bf16 a[104:107], a[180:183], v[148:151], a[104:107]\n\t" \
  "v_exp_f32 v46, v46\n\t" \
  "v_mfma_f32_16x16x32_bf16 a[120:123], a[184:187], v[144:147], a[120:123]\n\t" \
  "v_exp_f32 v47, v47\n\t" \
  "v_mfma_f32_16x16x32_bf16 a[120:123], a[188:191], v[148:151], a[120:123]\n\t" \
  "v_cvt_pk_bf16_f32 v143, v46, v47\n\t" \
  "buffer_load_dwordx4 v230, %[sV], s96 offen offset:0 lds\n\t" \
  "v_mfma_f32_16x16x32_bf16 v[168:171], v[216:219], v[144:147], v[168:171]\n\t" \
  "v_exp_f32 v64, v64\n\t" \
  "v_mfma_f32_16x16x32_bf16 v[168:171], v[216:219], v[148:151], v[168:171]\n\t" \
  "v_exp_f32 v65, v65\n\t" \
  "v_mfma_f32_16x16x32_bf16 a[12:15], a[128:131], v[152:155], a[12:15]\n\t" \
  "v_cvt_pk_bf16_f32 v144, v64, v65\n\t" \
  "ds_read_b128 a[248:251], v222 offset:28672\n\t" \
  "v_mfma_f32_16x16x32_bf16 a[12:15], a[132:135], v[156:159], a[12:15]\n\t" \
  "v_exp_f32 v66, v66\n\t" \
  "v_mfma_f32_16x16x32_bf16 a[28:31], a[136:139], v[152:155], a[28:31]\n\t" \
  "v_exp_f32 v67, v67\n\t" \
  "v_mfma_f32_16x16x32_bf16 a[28:31], a[140:143], v[156:159], a[28:31]\n\t" \
  "v_cvt_pk_bf16_f32 v145, v66, v67\n\t" \
  "buffer_load_dwordx4 v231, %[sV], s96 offen offset:1024 lds\n\t" \
  "v_mfma_f32_16x16x32_bf16 a[44:47], a[144:147], v[152:155], a[44:47]\n\t" \
  "ds_read_b128 a[252:255], v223 offset:28672\n\t" \
  "buffer_load_dwordx4 v230, %[sV], s96 offen offset:2048 lds\n\t" \
  "v_mfma_f32_16x16x32_bf16 a[44:47], a[148:151], v[156:159], a[44:47]\n\t" \
  "v_exp_f32 v68, v68\n\t" \
  "v_mfma_f32_16x16x32_bf16 a[60:63], a[152:155], v[152:155], a[60:63]\n\t" \
  "v_exp_f32 v69, v69\n\t" \
  "v_mfma_f32_16x16x32_bf16 a[60:63], a[156:159], v[156:159], a[60:63]\n\t" \
  "v_cvt_pk_bf16_f32 v146, v68, v69\n\t" \
  "buffer_load_dwordx4 v231, %[sV], s96 offen offset:3072 lds\n\t" \
  "v_mfma_f32_16x16x32_bf16 a[76:79], a[160:163], v[152:155], a[76:79]\n\t" \
  "v_exp_f32 v70, v70\n\t" \
  "v_mfma_f32_16x16x32_bf16 a[76:79], a[164:167], v[156:159], a[76:79]\n\t" \
  "v_exp_f32 v71, v71\n\t" \
  "v_mfma_f32_16x16x32_bf16 a[92:95], a[168:171], v[152:155], a[92:95]\n\t" \
  "v_cvt_pk_bf16_f32 v147, v70, v71\n\t" \
  "v_mfma_f32_16x16x32_bf16 a[92:95], a[172:175], v[156:159], a[92:95]\n\t" \
  "v_exp_f32 v72, v72\n\t" \
  "v_mfma_f32_16x16x32_bf16 a[108:111], a[176:179], v[152:155], a[108:111]\n\t" \
  "v_exp_f32 v73, v73\n\t" \
  "v_mfma_f32_16x16x32_bf16 a[108:111], a[180:183], v[156:159], a[108:111]\n\t" \
  "v_cvt_pk_bf16_f32 v148, v72, v73\n\t" \
  "v_mfma_f32_16x16x32_bf16 a[124:127], a[184:187], v[152:155], a[124:127]\n\t" \
  "s_add_u32 m0, %[m], 131072\n\t" \
  "s_mov_b32 s96, 47104\n\t" \
  "buffer_load_dwordx4 v232, %[sU], s96 offen offset:3072 lds\n\t" \
  "v_mfma_f32_16x16x32_bf16 a[124:127], a[188:191], v[156:159], a[124:127]\n\t" \
  "v_exp_f32 v74, v74\n\t" \
  "v_mfma_f32_16x16x32_bf16 v[172:175], v[216:219], v[152:155], v[172:175]\n\t" \
  "v_exp_f32 v75, v75\n\t" \
  "v_mfma_f32_16x16x32_bf16 v[172:175], v[216:219], v[156:159], v[172:175]\n\t" \
  "v_cvt_pk_bf16_f32 v149, v74, v75\n\t" \
  "s_cmp_lg_u32 s99, 0\n\t" \
  "s_cbranch_scc1 .Lsf_p2\n\t" \
  ".Lsf_q2:\n\t" \
  "s_waitcnt lgkmcnt(0)\n\t" \
  "v_mfma_f32_16x16x32_bf16 v[0:3], a[192:195], v[16:19], v[176:179]\n\t" \
  "s_mul_i32 s98, %[sofk], 5\n\t" \
  "s_xor_b32 s98, s98, 8192\n\t" \
  "s_lshr_b32 s98, s98, 1\n\t" \
  "buffer_load_dword %[pfd], v204, %[sD], s98 offen\n\t" \
  "v_mfma_f32_16x16x32_bf16 v[0:3], a[196:199], v[20:23], v[0:3]\n\t" \
  "v_exp_f32 v76, v76\n\t" \
  "v_mfma_f32_16x16x32_bf16 v[0:3], a[200:203], v[24:27], v[0:3]\n\t" \
  "ds_read_b64_tr_b16 a[128:129], v228 offset:0\n\t" \
  "ds_read_b64_tr_b16 a[130:131], v228 offset:4096\n\t" \
  "v_mfma_f32_16x16x32_bf16 v[0:3], a[204:207], v[28:31], v[0:3]\n\t" \
  "v_exp_f32 v77, v77\n\t" \
  "v_mfma_f32_16x16x32_bf16 v[4:7], a[220:223], v[28:31], v[176:179]\n\t" \
  "v_cvt_pk_bf16_f32 v150, v76, v77\n\t" \
  "ds_read_b64_tr_b16 a[132:133], v228 offset:8192\n\t" \
  "v_mfma_f32_16x16x32_bf16 v[4:7], a[216:219], v[24:27], v[4:7]\n\t" \
  "v_exp_f32 v78, v78\n\t" \
  "v_mfma_f32_16x16x32_bf16 v[4:7], a[212:215], v[20:23], v[4:7]\n\t" \
  "v_exp_f32 v79, v79\n\t" \
  "v_mfma_f32_16x16x32_bf16 v[4:7], a[208:211], v[16:19], v[4:7]\n\t" \
  "v_cvt_pk_bf16_f32 v151, v78, v79\n\t" \
  "v_max3_f32 v192, v0, v1, v2\n\t" \
  "v_mfma_f32_16x16x32_bf16 v[8:11], a[224:227], v[16:19], v[176:179]\n\t" \
  "ds_read_b64_tr_b16 a[134:135], v228 offset:12288\n\t" \
  "ds_read_b64_tr_b16 a[136:137], v229 offset:0\n\t" \
  "v_mfma_f32_16x16x32_bf16 v[8:11], a[228:231], v[20:23], v[8:11]\n\t" \
  "v_exp_f32 v96, v96\n\t" \
  "v_mfma_f32_16x16x32_bf16 v[8:11], a[232:235], v[24:27], v[8:11]\n\t" \
  "v_max3_f32 v203, v3, v4, v5\n\t" \
  "v_max3_f32 v192, v192, v6, v7\n\t" \
  "v_mfma_f32_16x16x32_bf16 v[8:11], a[236:239], v[28:31], v[8:11]\n\t" \
  "ds_read_b64_tr_b16 a[138:139], v229 offset:4096\n\t" \
  "ds_read_b64_tr_b16 a[140:141], v229 offset:8192\n\t" \
  "v_mfma_f32_16x16x32_bf16 v[12:15], a[252:255], v[28:31], v[176:179]\n\t" \
  "v_exp_f32 v97, v97\n\t" \
  "v_mfma_f32_16x16x32_bf16 v[12:15], a[248:251], v[24:27], v[12:15]\n\t" \
  "v_cvt_pk_bf16_f32 v152, v96, v97\n\t" \
  "ds_read_b64_tr_b16 a[142:143], v229 offset:12288\n\t" \
  "v_mfma_f32_16x16x32_bf16 v[12:15], a[244:247], v[20:23], v[12:15]\n\t" \
  "v_max3_f32 v203, v203, v8, v9\n\t" \
  "v_max3_f32 v192, v192, v10, v11\n\t" \
  "v_mfma_f32_16x16x32_bf16 v[12:15], a[240:243], v[16:19], v[12:15]\n\t" \
  "s_mul_i32 s98, %[sofk], 5\n\t" \
  "s_xor_b32 s98, s98, 12288\n\t" \
  "s_lshr_b32 s98, s98, 1\n\t" \
  "buffer_load_dword %[pfd], v204, %[sD], s98 offen\n\t" \
  "v_mfma_f32_16x16x32_bf16 v[32:35], a[192:195], v[48:51], v[180:183]\n\t" \
  "ds_read_b64_tr_b16 a[144:145], v228 offset:256\n\t" \
  "ds_read_b64_tr_b16 a[146:147], v228 offset:4352\n\t" \
  "v_mfma_f32_16x16x32_bf16 v[32:35], a[196:199], v[52:55], v[32:35]\n\t" \
  "v_exp_f32 v98, v98\n\t" \
  "v_mfma_f32_16x16x32_bf16 v[32:35], a[200:203], v[56:59], v[32:35]\n\t" \
  "v_max3_f32 v203, v203, v12, v13\n\t" \
  "v_max3_f32 v192, v192, v14, v15\n\t" \
  "v_mfma_f32_16x16x32_bf16 v[32:35], a[204:207], v[60:63], v[32:35]\n\t" \
  "v_max_f32 v192, v192, v203\n\t" \
  "ds_read_b64_tr_b16 a[148:149], v228 offset:8448\n\t" \
  "v_mfma_f32_16x16x32_bf16 v[36:39], a[220:223], v[60:63], v[180:183]\n\t" \
  "v_cmp_lt_f32 vcc, 0x42800000, v192\n\t" \
  "ds_read_b64_tr_b16 a[150:151], v228 offset:12544\n\t" \
  "v_mfma_f32_16x16x32_bf16 v[36:39], a[216:219], v[56:59], v[36:39]\n\t" \
  "s_cbranch_vccnz .Lsf_c30\n\t" \
  ".Lsf_r30:\n\t" \
  "ds_read_b64_tr_b16 a[152:153], v229 offset:256\n\t" \
  "v_mfma_f32_16x16x32_bf16 v[36:39], a[212:215], v[52:55], v[36:39]\n\t" \
  "v_max3_f32 v192, v32, v33, v34\n\t" \
  "ds_read_b64_tr_b16 a[154:155], v229 offset:4352\n\t" \
  "v_mfma_f32_16x16x32_bf16 v[36:39], a[208:211], v[48:51], v[36:39]\n\t" \
  "v_exp_f32 v99, v99\n\t" \
  "v_mfma_f32_16x16x32_bf16 v[40:43], a[224:227], v[48:51], v[180:183]\n\t" \
  "v_cvt_pk_bf16_f32 v153, v98, v99\n\t" \
  "ds_read_b64_tr_b16 a[156:157], v229 offset:8448\n\t" \
  "v_mfma_f32_16x16x32_bf16 v[40:43], a[228:231], v[52:55], v[40:43]\n\t" \
  "v_exp_f32 v100, v100\n\t" \
  "v_mfma_f32_16x16x32_bf16 v[40:43], a[232:235], v[56:59], v[40:43]\n\t" \
  "v_exp_f32 v101, v101\n\t" \
  "v_mfma_f32_16x16x32_bf16 v[40:43], a[236:239], v[60:63], v[40:43]\n\t" \
  "v_cvt_pk_bf16_f32 v154, v100, v101\n\t" \
  "v_max3_f32 v203, v35, v36, v37\n\t" \
  "v_mfma_f32_16x16x32_bf16 v[44:47], a[252:255], v[60:63], v[180:183]\n\t" \
  "v_max3_f32 v192, v192, v38, v39\n\t" \
  "ds_read_b64_tr_b16 a[158:159], v229 offset:12544\n\t" \
  "v_mfma_f32_16x16x32_bf16 v[44:47], a[248:251], v[56:59], v[44:47]\n\t" \
  "v_exp_f32 v102, v102\n\t" \
  "v_mfma_f32_16x16x32_bf16 v[44:47], a[244:247], v[52:55], v[44:47]\n\t" \
  "ds_read_b64_tr_b16 a[160:161], v228 offset:512\n\t" \
  "v_max3_f32 v203, v203, v40, v41\n\t" \
  "v_mfma_f32_16x16x32_bf16 v[44:47], a[240:243], v[48:51], v[44:47]\n\t" \
  "v_max3_f32 v192, v192, v42, v43\n\t" \
  "ds_read_b64_tr_b16 a[162:163], v228 offset:4608\n\t" \
  "v_mfma_f32_16x16x32_bf16 v[64:67], a[192:195], v[80:83], v[184:187]\n\t" \
  "v_exp_f32 v103, v103\n\t" \
  "v_mfma_f32_16x16x32_bf16 v[64:67], a[196:199], v[84:87], v[64:67]\n\t" \
  "v_cvt_pk_bf16_f32 v155, v102, v103\n\t" \
  "ds_read_b64_tr_b16 a[164:165], v228 offset:8704\n\t" \
  "v_mfma_f32_16x16x32_bf16 v[64:67], a[200:203], v[88:91], v[64:67]\n\t" \
  "v_max3_f32 v203, v203, v44, v45\n\t" \
  "v_max3_f32 v192, v192, v46, v47\n\t" \
  "v_mfma_f32_16x16x32_bf16 v[64:67], a[204:207], v[92:95], v[64:67]\n\t" \
  "v_max_f32 v192, v192, v203\n\t" \
  "ds_read_b64_tr_b16 a[166:167], v228 offset:12800\n\t" \
  "v_mfma_f32_16x16x32_bf16 v[68:71], a[220:223], v[92:95], v[184:187]\n\t" \
  "v_cmp_lt_f32 vcc, 0x42800000, v192\n\t" \
  "ds_read_b64_tr_b16 a[168:169], v229 offset:512\n\t" \
  "v_mfma_f32_16x16x32_bf16 v[68:71], a[216:219], v[88:91], v[68:71]\n\t" \
  "s_cbranch_vccnz .Lsf_c31\n\t" \
  ".Lsf_r31:\n\t" \
  "ds_read_b64_tr_b16 a[170:171], v229 offset:4608\n\t" \
  "v_mfma_f32_16x16x32_bf16 v[68:71], a[212:215], v[84:87], v[68:71]\n\t" \
  "v_max3_f32 v192, v64, v65, v66\n\t" \
  "ds_read_b64_tr_b16 a[172:173], v229 offset:8704\n\t" \
  "v_mfma_f32_16x16x32_bf16 v[68:71], a[208:211], v[80:83], v[68:71]\n\t" \
  "v_exp_f32 v104, v104\n\t" \
  "v_mfma_f32_16x16x32_bf16 v[72:75], a[224:227], v[80:83], v[184:187]\n\t" \
  "v_exp_f32 v105, v105\n\t" \
  "v_mfma_f32_16x16x32_bf16 v[72:75], a[228:231], v[84:87], v[72:75]\n\t" \
  "v_cvt_pk_bf16_f32 v156, v104, v105\n\t" \
  "ds_read_b64_tr_b16 a[174:175], v229 offset:12800\n\t" \
  "v_mfma_f32_16x16x32_bf16 v[72:75], a[232:235], v[88:91], v[72:75]\n\t" \
  "v_exp_f32 v106, v106\n\t" \
  "v_mfma_f32_16x16x32_bf16 v[72:75], a[236:239], v[92:95], v[72:75]\n\t" \
  "v_max3_f32 v203, v67, v68, v69\n\t" \
  "v_max3_f32 v192, v192, v70, v71\n\t" \
  "v_mfma_f32_16x16x32_bf16 v[76:79], a[252:255], v[92:95], v[184:187]\n\t" \
  "ds_read_b64_tr_b16 a[176:177], v228 offset:768\n\t" \
  "ds_read_b64_tr_b16 a[178:179], v228 offset:4864\n\t" \
  "v_mfma_f32_16x16x32_bf16 v[76:79], a[248:251], v[88:91], v[76:79]\n\t" \
  "v_exp_f32 v107, v107\n\t" \
  "v_mfma_f32_16x16x32_bf16 v[76:79], a[244:247], v[84:87], v[76:79]\n\t" \
  "v_cvt_pk_bf16_f32 v157, v106, v107\n\t" \
  "v_max3_f32 v203, v203, v72, v73\n\t" \
  "v_mfma_f32_16x16x32_bf16 v[76:79], a[240:243], v[80:83], v[76:79]\n\t" \
  "v_max3_f32 v192, v192, v74, v75\n\t" \
  "ds_read_b64_tr_b16 a[180:181], v228 offset:8960\n\t" \
  "v_mfma_f32_16x16x32_bf16 v[96:99], a[192:195], v[112:115], v[188:191]\n\t" \
  "v_exp_f32 v108, v108\n\t" \
  "v_mfma_f32_16x16x32_bf16 v[96:99], a[196:199], v[116:119], v[96:99]\n\t" \
  "v_exp_f32 v109, v109\n\t" \
  "v_mfma_f32_16x16x32_bf16 v[96:99], a[200:203], v[120:123], v[96:99]\n\t" \
  "v_cvt_pk_bf16_f32 v158, v108, v109\n\t" \
  "ds_read_b64_tr_b16 a[182:183], v228 offset:13056\n\t" \
  "v_mfma_f32_16x16x32_bf16 v[96:99], a[204:207], v[124:127], v[96:99]\n\t" \
  "v_max3_f32 v203, v203, v76, v77\n\t" \
  "v_max3_f32 v192, v192, v78, v79\n\t" \
  "v_mfma_f32_16x16x32_bf16 v[100:103], a[220:223], v[124:127], v[188:191]\n\t" \
  "v_max_f32 v192, v192, v203\n\t" \
  "ds_read_b64_tr_b16 a[184:185], v229 offset:768\n\t" \
  "v_mfma_f32_16x16x32_bf16 v[100:103], a[216:219], v[120:123], v[100:103]\n\t" \
  "v_cmp_lt_f32 vcc, 0x42800000, v192\n\t" \
  "ds_read_b64_tr_b16 a[186:187], v229 offset:4864\n\t" \
  "v_mfma_f32_16x16x32_bf16 v[100:103], a[212:215], v[116:119], v[100:103]\n\t" \
  "s_cbranch_vccnz .Lsf_c32\n\t" \
  ".Lsf_r32:\n\t" \
  "v_max3_f32 v192, v96, v97, v98\n\t" \
  "v_mfma_f32_16x16x32_bf16 v[100:103], a[208:211], v[112:115], v[100:103]\n\t" \
  "ds_read_b64_tr_b16 a[188:189], v229 offset:8960\n\t" \
  "ds_read_b64_tr_b16 a[190:191], v229 offset:13056\n\t" \
  "v_mfma_f32_16x16x32_bf16 v[104:107], a[224:227], v[112:115], v[188:191]\n\t" \
  "v_exp_f32 v110, v110\n\t" \
  "v_mfma_f32_16x16x32_bf16 v[104:107], a[228:231], v[116:119], v[104:107]\n\t" \
  "v_exp_f32 v111, v111\n\t" \
  "v_mfma_f32_16x16x32_bf16 v[104:107], a[232:235], v[120:123], v[104:107]\n\t" \
  "v_cvt_pk_bf16_f32 v159, v110, v111\n\t" \
  "v_max3_f32 v203, v99, v100, v101\n\t" \
  "v_mfma_f32_16x16x32_bf16 v[104:107], a[236:239], v[124:127], v[104:107]\n\t" \
  "v_max3_f32 v192, v192, v102, v103\n\t" \
  "v_mfma_f32_16x16x32_bf16 v[108:111], a[252:255], v[124:127], v[188:191]\n\t" \
  "v_exp_f32 v0, v0\n\t" \
  "v_mfma_f32_16x16x32_bf16 v[108:111], a[248:251], v[120:123], v[108:111]\n\t" \
  "v_exp_f32 v1, v1\n\t" \
  "v_mfma_f32_16x16x32_bf16 v[108:111], a[244:247], v[116:119], v[108:111]\n\t" \
  "v_exp_f32 v2, v2\n\t" \
  "v_mfma_f32_16x16x32_bf16 v[108:111], a[240:243], v[112:115], v[108:111]\n\t" \
  "v_max3_f32 v203, v203, v104, v105\n\t" \
  "v_max3_f32 v192, v192, v106, v107\n\t" \
  "s_waitcnt vmcnt(3) lgkmcnt(0)\n\t" \
  "s_barrier\n\t" \
  "v_mfma_f32_16x16x32_bf16 a[0:3], a[128:131], v[128:131], a[0:3]\n\t" \
  "v_exp_f32 v3, v3\n\t" \
  "v_mfma_f32_16x16x32_bf16 a[0:3], a[132:135], v[132:135], a[0:3]\n\t" \
  "v_exp_f32 v4, v4\n\t" \
  "v_mfma_f32_16x16x32_bf16 a[16:19], a[136:139], v[128:131], a[16:19]\n\t" \
  "ds_read_b128 a[192:195], v220 offset:0\n\t" \
  "v_max3_f32 v203, v203, v108, v109\n\t" \
  "v_mfma_f32_16x16x32_bf16 a[16:19], a[140:143], v[132:135], a[16:19]\n\t" \
  "v_max3_f32 v192, v192, v110, v111\n\t" \
  "v_max_f32 v192, v192, v203\n\t" \
  "v_mfma_f32_16x16x32_bf16 a[32:35], a[144:147], v[128:131], a[32:35]\n\t" \
  "v_cmp_lt_f32 vcc, 0x42800000, v192\n\t" \
  "ds_read_b128 a[196:199], v221 offset:0\n\t" \
  "v_mfma_f32_16x16x32_bf16 a[32:35], a[148:151], v[132:135], a[32:35]\n\t" \
  "s_cbranch_vccnz .Lsf_c33\n\t" \
  ".Lsf_r33:\n\t" \
  "ds_read_b128 a[200:203], v222 offset:0\n\t" \
  "v_mfma_f32_16x16x32_bf16 a[48:51], a[152:155], v[128:131], a[48:51]\n\t" \
  "v_exp_f32 v5, v5\n\t" \
  "v_mfma_f32_16x16x32_bf16 a[48:51], a[156:159], v[132:135], a[48:51]\n\t" \
  "v_exp_f32 v6, v6\n\t" \
  "v_mfma_f32_16x16x32_bf16 a[64:67], a[160:163], v[128:131], a[64:67]\n\t" \
  "v_exp_f32 v7, v7\n\t" \
  "v_mfma_f32_16x16x32_bf16 a[64:67], a[164:167], v[132:135], a[64:67]\n\t" \
  "v_exp_f32 v8, v8\n\t" \
  "v_mfma_f32_16x16x32_bf16 a[80:83], a[168:171], v[128:131], a[80:83]\n\t" \
  "v_exp_f32 v9, v9\n\t" \
  "v_mfma_f32_16x16x32_bf16 a[80:83], a[172:175], v[132:135], a[80:83]\n\t" \
  "v_exp_f32 v10, v10\n\t" \
  "v_mfma_f32_16x16x32_bf16 a[96:99], a[176:179], v[128:131], a[96:99]\n\t" \
  "v_exp_f32 v11, v11\n\t" \
  "v_mfma_f32_16x16x32_bf16 a[96:99], a[180:183], v[132:135], a[96:99]\n\t" \
  "v_exp_f32 v12, v12\n\t" \
  "v_mfma_f32_16x16x32_bf16 a[112:115], a[184:187], v[128:131], a[112:115]\n\t" \
  "ds_read_b128 a[204:207], v223 offset:0\n\t" \
  "ds_read_b128 a[208:211], v220 offset:4096\n\t" \
  "v_mfma_f32_16x16x32_bf16 a[112:115], a[188:191], v[132:135], a[112:115]\n\t" \
  "v_exp_f32 v13, v13\n\t" \
  "v_mfma_f32_16x16x32_bf16 v[160:163], v[216:219], v[128:131], v[160:163]\n\t" \
  "v_exp_f32 v14, v14\n\t" \
  "v_mfma_f32_16x16x32_bf16 v[160:163], v[216:219], v[132:135], v[160:163]\n\t" \
  "v_cvt_pk_bf16_f32 v128, v0, v1\n\t" \
  "v_cvt_pk_bf16_f32 v129, v2, v3\n\t" \
  "v_mfma_f32_16x16x32_bf16 a[4:7], a[128:131], v[136:139], a[4:7]\n\t" \
  "v_cvt_pk_bf16_f32 v130, v4, v5\n\t" \
  "v_cvt_pk_bf16_f32 v131, v6, v7\n\t" \
  "v_mfma_f32_16x16x32_bf16 a[4:7], a[132:135], v[140:143], a[4:7]\n\t" \
  "v_cvt_pk_bf16_f32 v132, v8, v9\n\t" \
  "v_cvt_pk_bf16_f32 v133, v10, v11\n\t" \
  "v_mfma_f32_16x16x32_bf16 a[20:23], a[136:139], v[136:139], a[20:23]\n\t" \
  "v_cvt_pk_bf16_f32 v134, v12, v13\n\t" \
  "ds_read_b128 a[212:215], v221 offset:4096\n\t" \
  "v_mfma_f32_16x16x32_bf16 a[20:23], a[140:143], v[140:143], a[20:23]\n\t" \
  "v_exp_f32 v15, v15\n\t" \
  "v_mfma_f32_16x16x32_bf16 a[36:39], a[144:147], v[136:139], a[36:39]\n\t" \
  "v_cvt_pk_bf16_f32 v135, v14, v15\n\t" \
  "s_add_u32 m0, %[m], 65536\n\t" \
  "v_mfma_f32_16x16x32_bf16 a[36:39], a[148:151], v[140:143], a[36:39]\n\t" \
  "v_exp_f32 v32, v32\n\t" \
  "v_mfma_f32_16x16x32_bf16 a[52:55], a[152:155], v[136:139], a[52:55]\n\t" \
  "ds_read_b128 a[216:219], v222 offset:4096\n\t" \
  "s_add_u32 s96, %[sofk], 81920\n\t" \
  "v_mfma_f32_16x16x32_bf16 a[52:55], a[156:159], v[140:143], a[52:55]\n\t" \
  "v_exp_f32 v33, v33\n\t" \
  "v_mfma_f32_16x16x32_bf16 a[68:71], a[160:163], v[136:139], a[68:71]\n\t" \
  "v_exp_f32 v34, v34\n\t" \
  "v_mfma_f32_16x16x32_bf16 a[68:71], a[164:167], v[140:143], a[68:71]\n\t" \
  "ds_read_b128 a[220:223], v223 offset:4096\n\t" \
  "buffer_load_dwordx4 v224, %[sK], s96 offen offset:0 lds\n\t" \
  "v_mfma_f32_16x16x32_bf16 a[84:87], a[168:171], v[136:139], a[84:87]\n\t" \
  "v_exp_f32 v35, v35\n\t" \
  "v_mfma_f32_16x16x32_bf16 a[84:87], a[172:175], v[140:143], a[84:87]\n\t" \
  "v_exp_f32 v36, v36\n\t" \
  "v_mfma_f32_16x16x32_bf16 a[100:103], a[176:179], v[136:139], a[100:103]\n\t" \
  "v_exp_f32 v37, v37\n\t" \
  "v_mfma_f32_16x16x32_bf16 a[100:103], a[180:183], v[140:143], a[100:103]\n\t" \
  "ds_read_b128 a[224:227], v220 offset:8192\n\t" \
  "buffer_load_dwordx4 v225, %[sK], s96 offen offset:1024 lds\n\t" \
  "v_mfma_f32_16x16x32_bf16 a[116:119], a[184:187], v[136:139], a[116:119]\n\t" \
  "v_exp_f32 v38, v38\n\t" \
  "v_mfma_f32_16x16x32_bf16 a[116:119], a[188:191], v[140:143], a[116:119]\n\t" \
  "v_exp_f32 v39, v39\n\t" \
  "v_mfma_f32_16x16x32_bf16 v[164:167], v[216:219], v[136:139], v[164:167]\n\t" \
  "buffer_load_dwordx4 v226, %[sK], s96 offen offset:2048 lds\n\t" \
  "ds_read_b128 a[228:231], v221 offset:8192\n\t" \
  "v_mfma_f32_16x16x32_bf16 v[164:167], v[216:219], v[140:143], v[164:167]\n\t" \
  "v_cvt_pk_bf16_f32 v136, v32, v33\n\t" \
  "v_cvt_pk_bf16_f32 v137, v34, v35\n\t" \
  "v_mfma_f32_16x16x32_bf16 a[8:11], a[128:131], v[144:147], a[8:11]\n\t" \
  "v_cvt_pk_bf16_f32 v138, v36, v37\n\t" \
  "v_cvt_pk_bf16_f32 v139, v38, v39\n\t" \
  "v_mfma_f32_16x16x32_bf16 a[8:11], a[132:135], v[148:151], a[8:11]\n\t" \
  "v_exp_f32 v40, v40\n\t" \
  "v_mfma_f32_16x16x32_bf16 a[24:27], a[136:139], v[144:147], a[24:27]\n\t" \
  "buffer_load_dwordx4 v227, %[sK], s96 offen offset:3072 lds\n\t" \
  "ds_read_b128 a[232:235], v222 offset:8192\n\t" \
  "v_mfma_f32_16x16x32_bf16 a[24:27], a[140:143], v[148:151], a[24:27]\n\t" \
  "v_exp_f32 v41, v41\n\t" \
  "v_mfma_f32_16x16x32_bf16 a[40:43], a[144:147], v[144:147], a[40:43]\n\t" \
  "v_cvt_pk_bf16_f32 v140, v40, v41\n\t" \
  "ds_read_b128 a[236:239], v223 offset:8192\n\t" \
  "v_mfma_f32_16x16x32_bf16 a[40:43], a[148:151], v[148:151], a[40:43]\n\t" \
  "v_exp_f32 v42, v42\n\t" \
  "v_mfma_f32_16x16x32_bf16 a[56:59], a[152:155], v[144:147], a[56:59]\n\t" \
  "v_exp_f32 v43, v43\n\t" \
  "v_mfma_f32_16x16x32_bf16 a[56:59], a[156:159], v[148:151], a[56:59]\n\t" \
  "v_cvt_pk_bf16_f32 v141, v42, v43\n\t" \
  "ds_read_b128 a[240:243], v220 offset:12288\n\t" \
  "v_mfma_f32_16x16x32_bf16 a[72:75], a[160:163], v[144:147], a[72:75]\n\t" \
  "s_add_u32 m0, %[m], 32768\n\t" \
  "s_mov_b32 s96, 0\n\t" \
  "buffer_load_dwordx4 v232, %[sG], s96 offen offset:0 lds\n\t" \
  "v_mfma_f32_16x16x32_bf16 a[72:75], a[164:167], v[148:151], a[72:75]\n\t" \
  "v_exp_f32 v44, v44\n\t" \
  "v_mfma_f32_16x16x32_bf16 a[88:91], a[168:171], v[144:147], a[88:91]\n\t" \
  "s_add_u32 m0, %[m], 81920\n\t" \
  "s_add_u32 s96, %[sofk], 65536\n\t" \
  "v_mfma_f32_16x16x32_bf16 a[88:91], a[172:175], v[148:151], a[88:91]\n\t" \
  "v_exp_f32 v45, v45\n\t" \
  "v_mfma_f32_16x16x32_bf16 a[104:107], a[176:179], v[144:147], a[104:107]\n\t" \
  "v_cvt_pk_bf16_f32 v142, v44, v45\n\t" \
  "ds_read_b128 a[244:247], v221 offset:12288\n\t" \
  "v_mfma_f32_16x16x32_bf16 a[104:107], a[180:183], v[148:151], a[104:107]\n\t" \
  "v_exp_f32 v46, v46\n\t" \
  "v_mfma_f32_16x16x32_bf16 a[120:123], a[184:187], v[144:147], a[120:123]\n\t" \
  "v_exp_f32 v47, v47\n\t" \
  "v_mfma_f32_16x16x32_bf16 a[120:123], a[188:191], v[148:151], a[120:123]\n\t" \
  "v_cvt_pk_bf16_f32 v143, v46, v47\n\t" \
  "buffer_load_dwordx4 v230, %[sV], s96 offen offset:0 lds\n\t" \
  "v_mfma_f32_16x16x32_bf16 v[168:171], v[216:219], v[144:147], v[168:171]\n\t" \
  "v_exp_f32 v64, v64\n\t" \
  "v_mfma_f32_16x16x32_bf16 v[168:171], v[216:219], v[148:151], v[168:171]\n\t" \
  "v_exp_f32 v65, v65\n\t" \
  "v_mfma_f32_16x16x32_bf16 a[12:15], a[128:131], v[152:155], a[12:15]\n\t" \
  "v_cvt_pk_bf16_f32 v144, v64, v65\n\t" \
  "ds_read_b128 a[248:251], v222 offset:12288\n\t" \
  "v_mfma_f32_16x16x32_bf16 a[12:15], a[132:135], v[156:159], a[12:15]\n\t" \
  "v_exp_f32 v66, v66\n\t" \
  "v_mfma_f32_16x16x32_bf16 a[28:31], a[136:139], v[152:155], a[28:31]\n\t" \
  "v_exp_f32 v67, v67\n\t" \
  "v_mfma_f32_16x16x32_bf16 a[28:31], a[140:143], v[156:159], a[28:31]\n\t" \
  "v_cvt_pk_bf16_f32 v145, v66, v67\n\t" \
  "buffer_load_dwordx4 v231, %[sV], s96 offen offset:1024 lds\n\t" \
  "v_mfma_f32_16x16x32_bf16 a[44:47], a[144:147], v[152:155], a[44:47]\n\t" \
  "ds_read_b128 a[252:255], v223 offset:12288\n\t" \
  "buffer_load_dwordx4 v230, %[sV], s96 offen offset:2048 lds\n\t" \
  "v_mfma_f32_16x16x32_bf16 a[44:47], a[148:151], v[156:159], a[44:47]\n\t" \
  "v_exp_f32 v68, v68\n\t" \
  "v_mfma_f32_16x16x32_bf16 a[60:63], a[152:155], v[152:155], a[60:63]\n\t" \
  "v_exp_f32 v69, v69\n\t" \
  "v_mfma_f32_16x16x32_bf16 a[60:63], a[156:159], v[156:159], a[60:63]\n\t" \
  "v_cvt_pk_bf16_f32 v146, v68, v69\n\t" \
  "buffer_load_dwordx4 v231, %[sV], s96 offen offset:3072 lds\n\t" \
  "v_mfma_f32_16x16x32_bf16 a[76:79], a[160:163], v[152:155], a[76:79]\n\t" \
  "v_exp_f32 v70, v70\n\t" \
  "v_mfma_f32_16x16x32_bf16 a[76:79], a[164:167], v[156:159], a[76:79]\n\t" \
  "v_exp_f32 v71, v71\n\t" \
  "v_mfma_f32_16x16x32_bf16 a[92:95], a[168:171], v[152:155], a[92:95]\n\t" \
  "v_cvt_pk_bf16_f32 v147, v70, v71\n\t" \
  "v_mfma_f32_16x16x32_bf16 a[92:95], a[172:175], v[156:159], a[92:95]\n\t" \
  "v_exp_f32 v72, v72\n\t" \
  "v_mfma_f32_16x16x32_bf16 a[108:111], a[176:179], v[152:155], a[108:111]\n\t" \
  "v_exp_f32 v73, v73\n\t" \
  "v_mfma_f32_16x16x32_bf16 a[108:111], a[180:183], v[156:159], a[108:111]\n\t" \
  "v_cvt_pk_bf16_f32 v148, v72, v73\n\t" \
  "v_mfma_f32_16x16x32_bf16 a[124:127], a[184:187], v[152:155], a[124:127]\n\t" \
  "s_add_u32 m0, %[m], 32768\n\t" \
  "s_mov_b32 s96, 6144\n\t" \
  "buffer_load_dwordx4 v232, %[sG], s96 offen offset:1024 lds\n\t" \
  "v_mfma_f32_16x16x32_bf16 a[124:127], a[188:191], v[156:159], a[124:127]\n\t" \
  "v_exp_f32 v74, v74\n\t" \
  "v_mfma_f32_16x16x32_bf16 v[172:175], v[216:219], v[152:155], v[172:175]\n\t" \
  "v_exp_f32 v75, v75\n\t" \
  "v_mfma_f32_16x16x32_bf16 v[172:175], v[216:219], v[156:159], v[172:175]\n\t" \
  "v_cvt_pk_bf16_f32 v149, v74, v75\n\t" \
  "s_cmp_lg_u32 s99, 0\n\t" \
  "s_cbranch_scc1 .Lsf_p3\n\t" \
  ".Lsf_q3:\n\t" \
  "s_waitcnt lgkmcnt(0)\n\t" \
  "v_mfma_f32_16x16x32_bf16 v[0:3], a[192:195], v[16:19], v[176:179]\n\t" \
  "s_add_u32 s98, %[pf4], 28672\n\t" \
  "buffer_load_dword %[pfd], v245, %[sG], s98 offen\n\t" \
  "v_mfma_f32_16x16x32_bf16 v[0:3], a[196:199], v[20:23], v[0:3]\n\t" \
  "v_exp_f32 v76, v76\n\t" \
  "v_mfma_f32_16x16x32_bf16 v[0:3], a[200:203], v[24:27], v[0:3]\n\t" \
  "ds_read_b64_tr_b16 a[128:129], v237 offset:0\n\t" \
  "ds_read_b64_tr_b16 a[130:131], v237 offset:4096\n\t" \
  "v_mfma_f32_16x16x32_bf16 v[0:3], a[204:207], v[28:31], v[0:3]\n\t" \
  "v_exp_f32 v77, v77\n\t" \
  "v_mfma_f32_16x16x32_bf16 v[4:7], a[220:223], v[28:31], v[176:179]\n\t" \
  "v_cvt_pk_bf16_f32 v150, v76, v77\n\t" \
  "ds_read_b64_tr_b16 a[132:133], v237 offset:8192\n\t" \
  "v_mfma_f32_16x16x32_bf16 v[4:7], a[216:219], v[24:27], v[4:7]\n\t" \
  "v_exp_f32 v78, v78\n\t" \
  "v_mfma_f32_16x16x32_bf16 v[4:7], a[212:215], v[20:23], v[4:7]\n\t" \
  "v_exp_f32 v79, v79\n\t" \
  "v_mfma_f32_16x16x32_bf16 v[4:7], a[208:211], v[16:19], v[4:7]\n\t" \
  "v_cvt_pk_bf16_f32 v151, v78, v79\n\t" \
  "v_max3_f32 v192, v0, v1, v2\n\t" \
  "v_mfma_f32_16x16x32_bf16 v[8:11], a[224:227], v[16:19], v[176:179]\n\t" \
  "ds_read_b64_tr_b16 a[134:135], v237 offset:12288\n\t" \
  "ds_read_b64_tr_b16 a[136:137], v238 offset:0\n\t" \
  "v_mfma_f32_16x16x32_bf16 v[8:11], a[228:231], v[20:23], v[8:11]\n\t" \
  "v_exp_f32 v96, v96\n\t" \
  "v_mfma_f32_16x16x32_bf16 v[8:11], a[232:235], v[24:27], v[8:11]\n\t" \
  "v_max3_f32 v203, v3, v4, v5\n\t" \
  "v_max3_f32 v192, v192, v6, v7\n\t" \
  "v_mfma_f32_16x16x32_bf16 v[8:11], a[236:239], v[28:31], v[8:11]\n\t" \
  "ds_read_b64_tr_b16 a[138:139], v238 offset:4096\n\t" \
  "ds_read_b64_tr_b16 a[140:141], v238 offset:8192\n\t" \
  "v_mfma_f32_16x16x32_bf16 v[12:15], a[252:255], v[28:31], v[176:179]\n\t" \
  "v_exp_f32 v97, v97\n\t" \
  "v_mfma_f32_16x16x32_bf16 v[12:15], a[248:251], v[24:27], v[12:15]\n\t" \
  "v_cvt_pk_bf16_f32 v152, v96, v97\n\t" \
  "ds_read_b64_tr_b16 a[142:143], v238 offset:12288\n\t" \
  "v_mfma_f32_16x16x32_bf16 v[12:15], a[244:247], v[20:23], v[12:15]\n\t" \
  "v_max3_f32 v203, v203, v8, v9\n\t" \
  "v_max3_f32 v192, v192, v10, v11\n\t" \
  "v_mfma_f32_16x16x32_bf16 v[12:15], a[240:243], v[16:19], v[12:15]\n\t" \
  "s_add_u32 s98, %[pf2], 0\n\t" \
  "buffer_load_dword %[pfd], v202, %[sM], s98 offen\n\t" \
  "v_mfma_f32_16x16x32_bf16 v[32:35], a[192:195], v[48:51], v[180:183]\n\t" \
  "ds_read_b64_tr_b16 a[144:145], v237 offset:256\n\t" \
  "ds_read_b64_tr_b16 a[146:147], v237 offset:4352\n\t" \
  "v_mfma_f32_16x16x32_bf16 v[32:35], a[196:199], v[52:55], v[32:35]\n\t" \
  "v_exp_f32 v98, v98\n\t" \
  "v_mfma_f32_16x16x32_bf16 v[32:35], a[200:203], v[56:59], v[32:35]\n\t" \
  "v_max3_f32 v203, v203, v12, v13\n\t" \
  "v_max3_f32 v192, v192, v14, v15\n\t" \
  "v_mfma_f32_16x16x32_bf16 v[32:35], a[204:207], v[60:63], v[32:35]\n\t" \
  "v_max_f32 v192, v192, v203\n\t" \
  "ds_read_b64_tr_b16 a[148:149], v237 offset:8448\n\t" \
  "v_mfma_f32_16x16x32_bf16 v[36:39], a[220:223], v[60:63], v[180:183]\n\t" \
  "v_cmp_lt_f32 vcc, 0x42800000, v192\n\t" \
  "ds_read_b64_tr_b16 a[150:151], v237 offset:12544\n\t" \
  "v_mfma_f32_16x16x32_bf16 v[36:39], a[216:219], v[56:59], v[36:39]\n\t" \
  "s_cbranch_vccnz .Lsf_c40\n\t" \
  ".Lsf_r40:\n\t" \
  "ds_read_b64_tr_b16 a[152:153], v238 offset:256\n\t" \
  "v_mfma_f32_16x16x32_bf16 v[36:39], a[212:215], v[52:55], v[36:39]\n\t" \
  "v_max3_f32 v192, v32, v33, v34\n\t" \
  "ds_read_b64_tr_b16 a[154:155], v238 offset:4352\n\t" \
  "v_mfma_f32_16x16x32_bf16 v[36:39], a[208:211], v[48:51], v[36:39]\n\t" \
  "v_exp_f32 v99, v99\n\t" \
  "v_mfma_f32_16x16x32_bf16 v[40:43], a[224:227], v[48:51], v[180:183]\n\t" \
  "v_cvt_pk_bf16_f32 v153, v98, v99\n\t" \
  "ds_read_b64_tr_b16 a[156:157], v238 offset:8448\n\t" \
  "v_mfma_f32_16x16x32_bf16 v[40:43], a[228:231], v[52:55], v[40:43]\n\t" \
  "v_exp_f32 v100, v100\n\t" \
  "v_mfma_f32_16x16x32_bf16 v[40:43], a[232:235], v[56:59], v[40:43]\n\t" \
  "v_exp_f32 v101, v101\n\t" \
  "v_mfma_f32_16x16x32_bf16 v[40:43], a[236:239], v[60:63], v[40:43]\n\t" \
  "v_cvt_pk_bf16_f32 v154, v100, v101\n\t" \
  "v_max3_f32 v203, v35, v36, v37\n\t" \
  "v_mfma_f32_16x16x32_bf16 v[44:47], a[252:255], v[60:63], v[180:183]\n\t" \
  "v_max3_f32 v192, v192, v38, v39\n\t" \
  "ds_read_b64_tr_b16 a[158:159], v238 offset:12544\n\t" \
  "v_mfma_f32_16x16x32_bf16 v[44:47], a[248:251], v[56:59], v[44:47]\n\t" \
  "v_exp_f32 v102, v102\n\t" \
  "v_mfma_f32_16x16x32_bf16 v[44:47], a[244:247], v[52:55], v[44:47]\n\t" \
  "ds_read_b64_tr_b16 a[160:161], v237 offset:512\n\t" \
  "v_max3_f32 v203, v203, v40, v41\n\t" \
  "v_mfma_f32_16x16x32_bf16 v[44:47], a[240:243], v[48:51], v[44:47]\n\t" \
  "v_max3_f32 v192, v192, v42, v43\n\t" \
  "ds_read_b64_tr_b16 a[162:163], v237 offset:4608\n\t" \
  "v_mfma_f32_16x16x32_bf16 v[64:67], a[192:195], v[80:83], v[184:187]\n\t" \
  "v_exp_f32 v103, v103\n\t" \
  "v_mfma_f32_16x16x32_bf16 v[64:67], a[196:199], v[84:87], v[64:67]\n\t" \
  "v_cvt_pk_bf16_f32 v155, v102, v103\n\t" \
  "ds_read_b64_tr_b16 a[164:165], v237 offset:8704\n\t" \
  "v_mfma_f32_16x16x32_bf16 v[64:67], a[200:203], v[88:91], v[64:67]\n\t" \
  "v_max3_f32 v203, v203, v44, v45\n\t" \
  "v_max3_f32 v192, v192, v46, v47\n\t" \
  "v_mfma_f32_16x16x32_bf16 v[64:67], a[204:207], v[92:95], v[64:67]\n\t" \
  "v_max_f32 v192, v192, v203\n\t" \
  "ds_read_b64_tr_b16 a[166:167], v237 offset:12800\n\t" \
  "v_mfma_f32_16x16x32_bf16 v[68:71], a[220:223], v[92:95], v[184:187]\n\t" \
  "v_cmp_lt_f32 vcc, 0x42800000, v192\n\t" \
  "ds_read_b64_tr_b16 a[168:169], v238 offset:512\n\t" \
  "v_mfma_f32_16x16x32_bf16 v[68:71], a[216:219], v[88:91], v[68:71]\n\t" \
  "s_cbranch_vccnz .Lsf_c41\n\t" \
  ".Lsf_r41:\n\t" \
  "ds_read_b64_tr_b16 a[170:171], v238 offset:4608\n\t" \
  "v_mfma_f32_16x16x32_bf16 v[68:71], a[212:215], v[84:87], v[68:71]\n\t" \
  "v_max3_f32 v192, v64, v65, v66\n\t" \
  "ds_read_b64_tr_b16 a[172:173], v238 offset:8704\n\t" \
  "v_mfma_f32_16x16x32_bf16 v[68:71], a[208:211], v[80:83], v[68:71]\n\t" \
  "v_exp_f32 v104, v104\n\t" \
  "v_mfma_f32_16x16x32_bf16 v[72:75], a[224:227], v[80:83], v[184:187]\n\t" \
  "v_exp_f32 v105, v105\n\t" \
  "v_mfma_f32_16x16x32_bf16 v[72:75], a[228:231], v[84:87], v[72:75]\n\t" \
  "v_cvt_pk_bf16_f32 v156, v104, v105\n\t" \
  "ds_read_b64_tr_b16 a[174:175], v238 offset:12800\n\t" \
  "v_mfma_f32_16x16x32_bf16 v[72:75], a[232:235], v[88:91], v[72:75]\n\t" \
  "v_exp_f32 v106, v106\n\t" \
  "v_mfma_f32_16x16x32_bf16 v[72:75], a[236:239], v[92:95], v[72:75]\n\t" \
  "v_max3_f32 v203, v67, v68, v69\n\t" \
  "v_max3_f32 v192, v192, v70, v71\n\t" \
  "v_mfma_f32_16x16x32_bf16 v[76:79], a[252:255], v[92:95], v[184:187]\n\t" \
  "ds_read_b64_tr_b16 a[176:177], v237 offset:768\n\t" \
  "ds_read_b64_tr_b16 a[178:179], v237 offset:4864\n\t" \
  "v_mfma_f32_16x16x32_bf16 v[76:79], a[248:251], v[88:91], v[76:79]\n\t" \
  "v_exp_f32 v107, v107\n\t" \
  "v_mfma_f32_16x16x32_bf16 v[76:79], a[244:247], v[84:87], v[76:79]\n\t" \
  "v_cvt_pk_bf16_f32 v157, v106, v107\n\t" \
  "v_max3_f32 v203, v203, v72, v73\n\t" \
  "v_mfma_f32_16x16x32_bf16 v[76:79], a[240:243], v[80:83], v[76:79]\n\t" \
  "v_max3_f32 v192, v192, v74, v75\n\t" \
  "ds_read_b64_tr_b16 a[180:181], v237 offset:8960\n\t" \
  "v_mfma_f32_16x16x32_bf16 v[96:99], a[192:195], v[112:115], v[188:191]\n\t" \
  "v_exp_f32 v108, v108\n\t" \
  "v_mfma_f32_16x16x32_bf16 v[96:99], a[196:199], v[116:119], v[96:99]\n\t" \
  "v_exp_f32 v109, v109\n\t" \
  "v_mfma_f32_16x16x32_bf16 v[96:99], a[200:203], v[120:123], v[96:99]\n\t" \
  "v_cvt_pk_bf16_f32 v158, v108, v109\n\t" \
  "ds_read_b64_tr_b16 a[182:183], v237 offset:13056\n\t" \
  "v_mfma_f32_16x16x32_bf16 v[96:99], a[204:207], v[124:127], v[96:99]\n\t" \
  "v_max3_f32 v203, v203, v76, v77\n\t" \
  "v_max3_f32 v192, v192, v78, v79\n\t" \
  "v_mfma_f32_16x16x32_bf16 v[100:103], a[220:223], v[124:127], v[188:191]\n\t" \
  "v_max_f32 v192, v192, v203\n\t" \
  "ds_read_b64_tr_b16 a[184:185], v238 offset:768\n\t" \
  "v_mfma_f32_16x16x32_bf16 v[100:103], a[216:219], v[120:123], v[100:103]\n\t" \
  "v_cmp_lt_f32 vcc, 0x42800000, v192\n\t" \
  "ds_read_b64_tr_b16 a[186:187], v238 offset:4864\n\t" \
  "v_mfma_f32_16x16x32_bf16 v[100:103], a[212:215], v[116:119], v[100:103]\n\t" \
  "s_cbranch_vccnz .Lsf_c42\n\t" \
  ".Lsf_r42:\n\t" \
  "v_max3_f32 v192, v96, v97, v98\n\t" \
  "v_mfma_f32_16x16x32_bf16 v[100:103], a[208:211], v[112:115], v[100:103]\n\t" \
  "ds_read_b64_tr_b16 a[188:189], v238 offset:8960\n\t" \
  "ds_read_b64_tr_b16 a[190:191], v238 offset:13056\n\t" \
  "v_mfma_f32_16x16x32_bf16 v[104:107], a[224:227], v[112:115], v[188:191]\n\t" \
  "v_exp_f32 v110, v110\n\t" \
  "v_mfma_f32_16x16x32_bf16 v[104:107], a[228:231], v[116:119], v[104:107]\n\t" \
  "v_exp_f32 v111, v111\n\t" \
  "v_mfma_f32_16x16x32_bf16 v[104:107], a[232:235], v[120:123], v[104:107]\n\t" \
  "v_cvt_pk_bf16_f32 v159, v110, v111\n\t" \
  "v_max3_f32 v203, v99, v100, v101\n\t" \
  "v_mfma_f32_16x16x32_bf16 v[104:107], a[236:239], v[124:127], v[104:107]\n\t" \
  "v_max3_f32 v192, v192, v102, v103\n\t" \
  "v_mfma_f32_16x16x32_bf16 v[108:111], a[252:255], v[124:127], v[188:191]\n\t" \
  "v_exp_f32 v0, v0\n\t" \
  "v_mfma_f32_16x16x32_bf16 v[108:111], a[248:251], v[120:123], v[108:111]\n\t" \
  "v_exp_f32 v1, v1\n\t" \
  "v_mfma_f32_16x16x32_bf16 v[108:111], a[244:247], v[116:119], v[108:111]\n\t" \
  "v_exp_f32 v2, v2\n\t" \
  "v_mfma_f32_16x16x32_bf16 v[108:111], a[240:243], v[112:115], v[108:111]\n\t" \
  "v_max3_f32 v203, v203, v104, v105\n\t" \
  "v_max3_f32 v192, v192, v106, v107\n\t" \
  "s_waitcnt vmcnt(3) lgkmcnt(0)\n\t" \
  "s_barrier\n\t" \
  "v_mfma_f32_16x16x32_bf16 a[0:3], a[128:131], v[128:131], a[0:3]\n\t" \
  "v_exp_f32 v3, v3\n\t" \
  "v_mfma_f32_16x16x32_bf16 a[0:3], a[132:135], v[132:135], a[0:3]\n\t" \
  "v_exp_f32 v4, v4\n\t" \
  "v_mfma_f32_16x16x32_bf16 a[16:19], a[136:139], v[128:131], a[16:19]\n\t" \
  "ds_read_b128 a[192:195], v220 offset:16384\n\t" \
  "v_max3_f32 v203, v203, v108, v109\n\t" \
  "v_mfma_f32_16x16x32_bf16 a[16:19], a[140:143], v[132:135], a[16:19]\n\t" \
  "v_max3_f32 v192, v192, v110, v111\n\t" \
  "v_max_f32 v192, v192, v203\n\t" \
  "v_mfma_f32_16x16x32_bf16 a[32:35], a[144:147], v[128:131], a[32:35]\n\t" \
  "v_cmp_lt_f32 vcc, 0x42800000, v192\n\t" \
  "ds_read_b128 a[196:199], v221 offset:16384\n\t" \
  "v_mfma_f32_16x16x32_bf16 a[32:35], a[148:151], v[132:135], a[32:35]\n\t" \
  "s_cbranch_vccnz .Lsf_c43\n\t" \
  ".Lsf_r43:\n\t" \
  "ds_read_b128 a[200:203], v222 offset:16384\n\t" \
  "v_mfma_f32_16x16x32_bf16 a[48:51], a[152:155], v[128:131], a[48:51]\n\t" \
  "s_lshr_b32 s98, %[sofk], 6\n\t" \
  "v_xor_b32 v234, s98, v244\n\t" \
  "v_mfma_f32_16x16x32_bf16 a[48:51], a[156:159], v[132:135], a[48:51]\n\t" \
  "v_add_u32 v213, %[m], v242\n\t" \
  "v_add_u32 v215, 98304, v213\n\t" \
  "v_mfma_f32_16x16x32_bf16 a[64:67], a[160:163], v[128:131], a[64:67]\n\t" \
  "v_exp_f32 v5, v5\n\t" \
  "v_mfma_f32_16x16x32_bf16 a[64:67], a[164:167], v[132:135], a[64:67]\n\t" \
  "v_exp_f32 v6, v6\n\t" \
  "v_mfma_f32_16x16x32_bf16 a[80:83], a[168:171], v[128:131], a[80:83]\n\t" \
  "v_mov_b32 v243, %[tag]\n\t" \
  "v_lshrrev_b32 v254, 3, v233\n\t" \
  "v_mfma_f32_16x16x32_bf16 a[80:83], a[172:175], v[132:135], a[80:83]\n\t" \
  "v_exp_f32 v7, v7\n\t" \
  "v_mfma_f32_16x16x32_bf16 a[96:99], a[176:179], v[128:131], a[96:99]\n\t" \
  "v_min_u32 v254, 1, v254\n\t" \
  "ds_read_b128 a[204:207], v223 offset:16384\n\t" \
  "v_mfma_f32_16x16x32_bf16 a[96:99], a[180:183], v[132:135], a[96:99]\n\t" \
  "v_exp_f32 v8, v8\n\t" \
  "v_mfma_f32_16x16x32_bf16 a[112:115], a[184:187], v[128:131], a[112:115]\n\t" \
  "v_exp_f32 v9, v9\n\t" \
  "v_mfma_f32_16x16x32_bf16 a[112:115], a[188:191], v[132:135], a[112:115]\n\t" \
  "v_lshlrev_b32 v254, 31, v254\n\t" \
  "v_lshl_or_b32 v254, v233, 9, v254\n\t" \
  "v_mfma_f32_16x16x32_bf16 v[160:163], v[216:219], v[128:131], v[160:163]\n\t" \
  "v_exp_f32 v10, v10\n\t" \
  "v_mfma_f32_16x16x32_bf16 v[160:163], v[216:219], v[132:135], v[160:163]\n\t" \
  "v_cvt_pk_bf16_f32 v128, v0, v1\n\t" \
  "v_cvt_pk_bf16_f32 v129, v2, v3\n\t" \
  "v_mfma_f32_16x16x32_bf16 a[4:7], a[128:131], v[136:139], a[4:7]\n\t" \
  "v_cvt_pk_bf16_f32 v130, v4, v5\n\t" \
  "v_cvt_pk_bf16_f32 v131, v6, v7\n\t" \
  "v_mfma_f32_16x16x32_bf16 a[4:7], a[132:135], v[140:143], a[4:7]\n\t" \
  "v_cvt_pk_bf16_f32 v132, v8, v9\n\t" \
  "ds_read_b128 a[208:211], v220 offset:20480\n\t" \
  "s_lshr_b32 m0, %[hl18], 18\n\t" \
  "s_brev_b32 s98, 1\n\t" \
  "v_writelane_b32 v254, s98, m0\n\t" \
  "v_mfma_f32_16x16x32_bf16 a[20:23], a[136:139], v[136:139], a[20:23]\n\t" \
  "ds_read_b128 a[212:215], v221 offset:20480\n\t" \
  "s_add_u32 m0, %[m], 49152\n\t" \
  "v_mfma_f32_16x16x32_bf16 a[20:23], a[140:143], v[140:143], a[20:23]\n\t" \
  "v_exp_f32 v11, v11\n\t" \
  "v_mfma_f32_16x16x32_bf16 a[36:39], a[144:147], v[136:139], a[36:39]\n\t" \
  "v_cvt_pk_bf16_f32 v133, v10, v11\n\t" \
  "ds_read_b128 a[216:219], v222 offset:20480\n\t" \
  "v_mfma_f32_16x16x32_bf16 a[36:39], a[148:151], v[140:143], a[36:39]\n\t" \
  "v_exp_f32 v12, v12\n\t" \
  "v_mfma_f32_16x16x32_bf16 a[52:55], a[152:155], v[136:139], a[52:55]\n\t" \
  "v_exp_f32 v13, v13\n\t" \
  "v_mfma_f32_16x16x32_bf16 a[52:55], a[156:159], v[140:143], a[52:55]\n\t" \
  "v_cvt_pk_bf16_f32 v134, v12, v13\n\t" \
  "s_add_u32 s96, %[sofk], 98304\n\t" \
  "v_mfma_f32_16x16x32_bf16 a[68:71], a[160:163], v[136:139], a[68:71]\n\t" \
  "v_exp_f32 v14, v14\n\t" \
  "v_mfma_f32_16x16x32_bf16 a[68:71], a[164:167], v[140:143], a[68:71]\n\t" \
  "ds_read_b128 a[220:223], v223 offset:20480\n\t" \
  "buffer_load_dwordx4 v224, %[sK], s96 offen offset:0 lds\n\t" \
  "v_mfma_f32_16x16x32_bf16 a[84:87], a[168:171], v[136:139], a[84:87]\n\t" \
  "v_exp_f32 v15, v15\n\t" \
  "v_mfma_f32_16x16x32_bf16 a[84:87], a[172:175], v[140:143], a[84:87]\n\t" \
  "v_cvt_pk_bf16_f32 v135, v14, v15\n\t" \
  "ds_read_b128 a[224:227], v220 offset:24576\n\t" \
  "v_mfma_f32_16x16x32_bf16 a[100:103], a[176:179], v[136:139], a[100:103]\n\t" \
  "v_exp_f32 v32, v32\n\t" \
  "v_mfma_f32_16x16x32_bf16 a[100:103], a[180:183], v[140:143], a[100:103]\n\t" \
  "v_exp_f32 v33, v33\n\t" \
  "v_mfma_f32_16x16x32_bf16 a[116:119], a[184:187], v[136:139], a[116:119]\n\t" \
  "v_exp_f32 v34, v34\n\t" \
  "v_mfma_f32_16x16x32_bf16 a[116:119], a[188:191], v[140:143], a[116:119]\n\t" \
  "buffer_load_dwordx4 v225, %[sK], s96 offen offset:1024 lds\n\t" \
  "buffer_load_dwordx4 v226, %[sK], s96 offen offset:2048 lds\n\t" \
  "v_mfma_f32_16x16x32_bf16 v[164:167], v[216:219], v[136:139], v[164:167]\n\t" \
  "v_exp_f32 v35, v35\n\t" \
  "v_mfma_f32_16x16x32_bf16 v[164:167], v[216:219], v[140:143], v[164:167]\n\t" \
  "v_cvt_pk_bf16_f32 v136, v32, v33\n\t" \
  "v_cvt_pk_bf16_f32 v137, v34, v35\n\t" \
  "v_mfma_f32_16x16x32_bf16 a[8:11], a[128:131], v[144:147], a[8:11]\n\t" \
  "ds_read_b128 a[228:231], v221 offset:24576\n\t" \
  "buffer_load_dwordx4 v227, %[sK], s96 offen offset:3072 lds\n\t" \
  "v_mfma_f32_16x16x32_bf16 a[8:11], a[132:135], v[148:151], a[8:11]\n\t" \
  "v_exp_f32 v36, v36\n\t" \
  "v_mfma_f32_16x16x32_bf16 a[24:27], a[136:139], v[144:147], a[24:27]\n\t" \
  "v_exp_f32 v37, v37\n\t" \
  "v_mfma_f32_16x16x32_bf16 a[24:27], a[140:143], v[148:151], a[24:27]\n\t" \
  "v_cvt_pk_bf16_f32 v138, v36, v37\n\t" \
  "ds_read_b128 a[232:235], v222 offset:24576\n\t" \
  "v_mfma_f32_16x16x32_bf16 a[40:43], a[144:147], v[144:147], a[40:43]\n\t" \
  "v_exp_f32 v38, v38\n\t" \
  "v_mfma_f32_16x16x32_bf16 a[40:43], a[148:151], v[148:151], a[40:43]\n\t" \
  "v_exp_f32 v39, v39\n\t" \
  "v_mfma_f32_16x16x32_bf16 a[56:59], a[152:155], v[144:147], a[56:59]\n\t" \
  "v_cvt_pk_bf16_f32 v139, v38, v39\n\t" \
  "ds_read_b128 a[236:239], v223 offset:24576\n\t" \
  "v_mfma_f32_16x16x32_bf16 a[56:59], a[156:159], v[148:151], a[56:59]\n\t" \
  "s_add_u32 m0, %[m], 32768\n\t" \
  "s_mov_b32 s96, 12288\n\t" \
  "buffer_load_dwordx4 v232, %[sG], s96 offen offset:2048 lds\n\t" \
  "v_mfma_f32_16x16x32_bf16 a[72:75], a[160:163], v[144:147], a[72:75]\n\t" \
  "v_exp_f32 v40, v40\n\t" \
  "v_mfma_f32_16x16x32_bf16 a[72:75], a[164:167], v[148:151], a[72:75]\n\t" \
  "s_add_u32 m0, %[m], 147456\n\t" \
  "ds_read_b128 a[240:243], v220 offset:28672\n\t" \
  "v_mfma_f32_16x16x32_bf16 a[88:91], a[168:171], v[144:147], a[88:91]\n\t" \
  "v_exp_f32 v41, v41\n\t" \
  "v_mfma_f32_16x16x32_bf16 a[88:91], a[172:175], v[148:151], a[88:91]\n\t" \
  "v_cvt_pk_bf16_f32 v140, v40, v41\n\t" \
  "s_add_u32 s96, %[sofk], 81920\n\t" \
  "v_mfma_f32_16x16x32_bf16 a[104:107], a[176:179], v[144:147], a[104:107]\n\t" \
  "v_exp_f32 v42, v42\n\t" \
  "v_mfma_f32_16x16x32_bf16 a[104:107], a[180:183], v[148:151], a[104:107]\n\t" \
  "v_exp_f32 v43, v43\n\t" \
  "v_mfma_f32_16x16x32_bf16 a[120:123], a[184:187], v[144:147], a[120:123]\n\t" \
  "v_cvt_pk_bf16_f32 v141, v42, v43\n\t" \
  "ds_read_b128 a[244:247], v221 offset:28672\n\t" \
  "v_mfma_f32_16x16x32_bf16 a[120:123], a[188:191], v[148:151], a[120:123]\n\t" \
  "v_exp_f32 v44, v44\n\t" \
  "v_mfma_f32_16x16x32_bf16 v[168:171], v[216:219], v[144:147], v[168:171]\n\t" \
  "v_exp_f32 v45, v45\n\t" \
  "v_mfma_f32_16x16x32_bf16 v[168:171], v[216:219], v[148:151], v[168:171]\n\t" \
  "v_cvt_pk_bf16_f32 v142, v44, v45\n\t" \
  "buffer_load_dwordx4 v230, %[sV], s96 offen offset:0 lds\n\t" \
  "v_mfma_f32_16x16x32_bf16 a[12:15], a[128:131], v[152:155], a[12:15]\n\t" \
  "v_exp_f32 v46, v46\n\t" \
  "v_mfma_f32_16x16x32_bf16 a[12:15], a[132:135], v[156:159], a[12:15]\n\t" \
  "ds_read_b128 a[248:251], v222 offset:28672\n\t" \
  "buffer_load_dwordx4 v231, %[sV], s96 offen offset:1024 lds\n\t" \
  "v_mfma_f32_16x16x32_bf16 a[28:31], a[136:139], v[152:155], a[28:31]\n\t" \
  "v_exp_f32 v47, v47\n\t" \
  "v_mfma_f32_16x16x32_bf16 a[28:31], a[140:143], v[156:159], a[28:31]\n\t" \
  "v_cvt_pk_bf16_f32 v143, v46, v47\n\t" \
  "ds_read_b128 a[252:255], v223 offset:28672\n\t" \
  "v_mfma_f32_16x16x32_bf16 a[44:47], a[144:147], v[152:155], a[44:47]\n\t" \
  "v_exp_f32 v64, v64\n\t" \
  "v_mfma_f32_16x16x32_bf16 a[44:47], a[148:151], v[156:159], a[44:47]\n\t" \
  "v_exp_f32 v65, v65\n\t" \
  "v_mfma_f32_16x16x32_bf16 a[60:63], a[152:155], v[152:155], a[60:63]\n\t" \
  "v_cvt_pk_bf16_f32 v144, v64, v65\n\t" \
  "buffer_load_dwordx4 v230, %[sV], s96 offen offset:2048 lds\n\t" \
  "v_mfma_f32_16x16x32_bf16 a[60:63], a[156:159], v[156:159], a[60:63]\n\t" \
  "v_exp_f32 v66, v66\n\t" \
  "v_mfma_f32_16x16x32_bf16 a[76:79], a[160:163], v[152:155], a[76:79]\n\t" \
  "v_exp_f32 v67, v67\n\t" \
  "v_mfma_f32_16x16x32_bf16 a[76:79], a[164:167], v[156:159], a[76:79]\n\t" \
  "v_cvt_pk_bf16_f32 v145, v66, v67\n\t" \
  "buffer_load_dwordx4 v231, %[sV], s96 offen offset:3072 lds\n\t" \
  "v_mfma_f32_16x16x32_bf16 a[92:95], a[168:171], v[152:155], a[92:95]\n\t" \
  "v_exp_f32 v68, v68\n\t" \
  "v_mfma_f32_16x16x32_bf16 a[92:95], a[172:175], v[156:159], a[92:95]\n\t" \
  "v_exp_f32 v69, v69\n\t" \
  "v_mfma_f32_16x16x32_bf16 a[108:111], a[176:179], v[152:155], a[108:111]\n\t" \
  "v_cvt_pk_bf16_f32 v146, v68, v69\n\t" \
  "v_mfma_f32_16x16x32_bf16 a[108:111], a[180:183], v[156:159], a[108:111]\n\t" \
  "s_add_u32 m0, %[m], 32768\n\t" \
  "s_mov_b32 s96, 18432\n\t" \
  "buffer_load_dwordx4 v232, %[sG], s96 offen offset:3072 lds\n\t" \
  "v_mfma_f32_16x16x32_bf16 a[124:127], a[184:187], v[152:155], a[124:127]\n\t" \
  "v_exp_f32 v70, v70\n\t" \
  "v_mfma_f32_16x16x32_bf16 a[124:127], a[188:191], v[156:159], a[124:127]\n\t" \
  "v_exp_f32 v71, v71\n\t" \
  "v_mfma_f32_16x16x32_bf16 v[172:175], v[216:219], v[152:155], v[172:175]\n\t" \
  "v_cvt_pk_bf16_f32 v147, v70, v71\n\t" \
  "v_mfma_f32_16x16x32_bf16 v[172:175], v[216:219], v[156:159], v[172:175]\n\t" \
  "v_exp_f32 v72, v72\n\t" \
  "s_cmp_lg_u32 s99, 0\n\t" \
  "s_cbranch_scc1 .Lsf_p4\n\t" \
  ".Lsf_q4:\n\t" \
  "s_waitcnt lgkmcnt(0)\n\t" \
  "v_mfma_f32_16x16x32_bf16 v[0:3], a[192:195], v[16:19], v[176:179]\n\t" \
  "s_add_u32 s98, %[pf4], 43008\n\t" \
  "buffer_load_dword %[pfd], v245, %[sG], s98 offen\n\t" \
  "v_mfma_f32_16x16x32_bf16 v[0:3], a[196:199], v[20:23], v[0:3]\n\t" \
  "ds_read_b64_tr_b16 a[128:129], v228 offset:0\n\t" \
  "v_lshrrev_b32 v252, 3, v233\n\t" \
  "v_mfma_f32_16x16x32_bf16 v[0:3], a[200:203], v[24:27], v[0:3]\n\t" \
  "v_exp_f32 v73, v73\n\t" \
  "v_mfma_f32_16x16x32_bf16 v[0:3], a[204:207], v[28:31], v[0:3]\n\t" \
  "v_cvt_pk_bf16_f32 v148, v72, v73\n\t" \
  "ds_read_b64_tr_b16 a[130:131], v228 offset:4096\n\t" \
  "v_mfma_f32_16x16x32_bf16 v[4:7], a[220:223], v[28:31], v[176:179]\n\t" \
  "ds_read_b64_tr_b16 a[132:133], v228 offset:8192\n\t" \
  "v_mul_u32_u24 v247, 98176, v252\n\t" \
  "v_mfma_f32_16x16x32_bf16 v[4:7], a[216:219], v[24:27], v[4:7]\n\t" \
  "v_exp_f32 v74, v74\n\t" \
  "v_mfma_f32_16x16x32_bf16 v[4:7], a[212:215], v[20:23], v[4:7]\n\t" \
  "v_max3_f32 v192, v0, v1, v2\n\t" \
  "ds_read_b64_tr_b16 a[134:135], v228 offset:12288\n\t" \
  "v_mfma_f32_16x16x32_bf16 v[4:7], a[208:211], v[16:19], v[4:7]\n\t" \
  "v_exp_f32 v75, v75\n\t" \
  "v_mfma_f32_16x16x32_bf16 v[8:11], a[224:227], v[16:19], v[176:179]\n\t" \
  "v_cvt_pk_bf16_f32 v149, v74, v75\n\t" \
  "ds_read_b64_tr_b16 a[136:137], v229 offset:0\n\t" \
  "v_mfma_f32_16x16x32_bf16 v[8:11], a[228:231], v[20:23], v[8:11]\n\t" \
  "v_lshl_add_u32 v247, v233, 4, v247\n\t" \
  "ds_read_b64_tr_b16 a[138:139], v229 offset:4096\n\t" \
  "v_mfma_f32_16x16x32_bf16 v[8:11], a[232:235], v[24:27], v[8:11]\n\t" \
  "v_max3_f32 v203, v3, v4, v5\n\t" \
  "v_max3_f32 v192, v192, v6, v7\n\t" \
  "v_mfma_f32_16x16x32_bf16 v[8:11], a[236:239], v[28:31], v[8:11]\n\t" \
  "v_exp_f32 v76, v76\n\t" \
  "v_mfma_f32_16x16x32_bf16 v[12:15], a[252:255], v[28:31], v[176:179]\n\t" \
  "ds_read_b64_tr_b16 a[140:141], v229 offset:8192\n\t" \
  "v_lshlrev_b32 v252, 4, v252\n\t" \
  "v_mfma_f32_16x16x32_bf16 v[12:15], a[248:251], v[24:27], v[12:15]\n\t" \
  "s_add_u32 s98, %[pf2], 24576\n\t" \
  "buffer_load_dword %[pfd], v202, %[sM], s98 offen\n\t" \
  "v_mfma_f32_16x16x32_bf16 v[12:15], a[244:247], v[20:23], v[12:15]\n\t" \
  "v_max3_f32 v203, v203, v8, v9\n\t" \
  "v_max3_f32 v192, v192, v10, v11\n\t" \
  "v_mfma_f32_16x16x32_bf16 v[12:15], a[240:243], v[16:19], v[12:15]\n\t" \
  "ds_read_b64_tr_b16 a[142:143], v229 offset:12288\n\t" \
  "ds_read_b64_tr_b16 a[144:145], v228 offset:256\n\t" \
  "v_mfma_f32_16x16x32_bf16 v[32:35], a[192:195], v[48:51], v[180:183]\n\t" \
  "v_exp_f32 v77, v77\n\t" \
  "v_mfma_f32_16x16x32_bf16 v[32:35], a[196:199], v[52:55], v[32:35]\n\t" \
  "v_cvt_pk_bf16_f32 v150, v76, v77\n\t" \
  "ds_read_b64_tr_b16 a[146:147], v228 offset:4352\n\t" \
  "v_mfma_f32_16x16x32_bf16 v[32:35], a[200:203], v[56:59], v[32:35]\n\t" \
  "v_max3_f32 v203, v203, v12, v13\n\t" \
  "v_max3_f32 v192, v192, v14, v15\n\t" \
  "v_mfma_f32_16x16x32_bf16 v[32:35], a[204:207], v[60:63], v[32:35]\n\t" \
  "v_max_f32 v192, v192, v203\n\t" \
  "ds_read_b64_tr_b16 a[148:149], v228 offset:8448\n\t" \
  "v_mfma_f32_16x16x32_bf16 v[36:39], a[220:223], v[60:63], v[180:183]\n\t" \
  "v_cmp_lt_f32 vcc, 0x42800000, v192\n\t" \
  "ds_read_b64_tr_b16 a[150:151], v228 offset:12544\n\t" \
  "v_mfma_f32_16x16x32_bf16 v[36:39], a[216:219], v[56:59], v[36:39]\n\t" \
  "s_cbranch_vccnz .Lsf_c50\n\t" \
  ".Lsf_r50:\n\t" \
  "ds_read_b64_tr_b16 a[152:153], v229 offset:256\n\t" \
  "v_mfma_f32_16x16x32_bf16 v[36:39], a[212:215], v[52:55], v[36:39]\n\t" \
  "v_max3_f32 v192, v32, v33, v34\n\t" \
  "ds_read_b64_tr_b16 a[154:155], v229 offset:4352\n\t" \
  "v_mfma_f32_16x16x32_bf16 v[36:39], a[208:211], v[48:51], v[36:39]\n\t" \
  "v_exp_f32 v78, v78\n\t" \
  "v_mfma_f32_16x16x32_bf16 v[40:43], a[224:227], v[48:51], v[180:183]\n\t" \
  "v_exp_f32 v79, v79\n\t" \
  "v_mfma_f32_16x16x32_bf16 v[40:43], a[228:231], v[52:55], v[40:43]\n\t" \
  "v_cvt_pk_bf16_f32 v151, v78, v79\n\t" \
  "ds_read_b64_tr_b16 a[156:157], v229 offset:8448\n\t" \
  "v_mfma_f32_16x16x32_bf16 v[40:43], a[232:235], v[56:59], v[40:43]\n\t" \
  "v_exp_f32 v96, v96\n\t" \
  "v_mfma_f32_16x16x32_bf16 v[40:43], a[236:239], v[60:63], v[40:43]\n\t" \
  "v_max3_f32 v203, v35, v36, v37\n\t" \
  "v_max3_f32 v192, v192, v38, v39\n\t" \
  "v_mfma_f32_16x16x32_bf16 v[44:47], a[252:255], v[60:63], v[180:183]\n\t" \
  "v_exp_f32 v97, v97\n\t" \
  "v_mfma_f32_16x16x32_bf16 v[44:47], a[248:251], v[56:59], v[44:47]\n\t" \
  "v_cvt_pk_bf16_f32 v152, v96, v97\n\t" \
  "ds_read_b64_tr_b16 a[158:159], v229 offset:12544\n\t" \
  "v_mfma_f32_16x16x32_bf16 v[44:47], a[244:247], v[52:55], v[44:47]\n\t" \
  "ds_read_b64_tr_b16 a[160:161], v228 offset:512\n\t" \
  "v_max3_f32 v203, v203, v40, v41\n\t" \
  "v_mfma_f32_16x16x32_bf16 v[44:47], a[240:243], v[48:51], v[44:47]\n\t" \
  "v_max3_f32 v192, v192, v42, v43\n\t" \
  "ds_read_b64_tr_b16 a[162:163], v228 offset:4608\n\t" \
  "v_mfma_f32_16x16x32_bf16 v[64:67], a[192:195], v[80:83], v[184:187]\n\t" \
  "v_exp_f32 v98, v98\n\t" \
  "v_mfma_f32_16x16x32_bf16 v[64:67], a[196:199], v[84:87], v[64:67]\n\t" \
  "v_exp_f32 v99, v99\n\t" \
  "v_mfma_f32_16x16x32_bf16 v[64:67], a[200:203], v[88:91], v[64:67]\n\t" \
  "v_cvt_pk_bf16_f32 v153, v98, v99\n\t" \
  "ds_read_b64_tr_b16 a[164:165], v228 offset:8704\n\t" \
  "v_mfma_f32_16x16x32_bf16 v[64:67], a[204:207], v[92:95], v[64:67]\n\t" \
  "v_max3_f32 v203, v203, v44, v45\n\t" \
  "v_max3_f32 v192, v192, v46, v47\n\t" \
  "v_mfma_f32_16x16x32_bf16 v[68:71], a[220:223], v[92:95], v[184:187]\n\t" \
  "v_max_f32 v192, v192, v203\n\t" \
  "ds_read_b64_tr_b16 a[166:167], v228 offset:12800\n\t" \
  "v_mfma_f32_16x16x32_bf16 v[68:71], a[216:219], v[88:91], v[68:71]\n\t" \
  "v_cmp_lt_f32 vcc, 0x42800000, v192\n\t" \
  "ds_read_b64_tr_b16 a[168:169], v229 offset:512\n\t" \
  "v_mfma_f32_16x16x32_bf16 v[68:71], a[212:215], v[84:87], v[68:71]\n\t" \
  "s_cbranch_vccnz .Lsf_c51\n\t" \
  ".Lsf_r51:\n\t" \
  "v_max3_f32 v192, v64, v65, v66\n\t" \
  "v_mfma_f32_16x16x32_bf16 v[68:71], a[208:211], v[80:83], v[68:71]\n\t" \
  "ds_read_b64_tr_b16 a[170:171], v229 offset:4608\n\t" \
  "ds_read_b64_tr_b16 a[172:173], v229 offset:8704\n\t" \
  "v_mfma_f32_16x16x32_bf16 v[72:75], a[224:227], v[80:83], v[184:187]\n\t" \
  "v_exp_f32 v100, v100\n\t" \
  "v_mfma_f32_16x16x32_bf16 v[72:75], a[228:231], v[84:87], v[72:75]\n\t" \
  "v_exp_f32 v101, v101\n\t" \
  "v_mfma_f32_16x16x32_bf16 v[72:75], a[232:235], v[88:91], v[72:75]\n\t" \
  "v_cvt_pk_bf16_f32 v154, v100, v101\n\t" \
  "v_max3_f32 v203, v67, v68, v69\n\t" \
  "v_mfma_f32_16x16x32_bf16 v[72:75], a[236:239], v[92:95], v[72:75]\n\t" \
  "v_max3_f32 v192, v192, v70, v71\n\t" \
  "ds_read_b64_tr_b16 a[174:175], v229 offset:12800\n\t" \
  "v_mfma_f32_16x16x32_bf16 v[76:79], a[252:255], v[92:95], v[184:187]\n\t" \
  "ds_read_b64_tr_b16 a[176:177], v228 offset:768\n\t" \
  "ds_read_b64_tr_b16 a[178:179], v228 offset:4864\n\t" \
  "v_mfma_f32_16x16x32_bf16 v[76:79], a[248:251], v[88:91], v[76:79]\n\t" \
  "v_exp_f32 v102, v102\n\t" \
  "v_mfma_f32_16x16x32_bf16 v[76:79], a[244:247], v[84:87], v[76:79]\n\t" \
  "v_max3_f32 v203, v203, v72, v73\n\t" \
  "v_max3_f32 v192, v192, v74, v75\n\t" \
  "v_mfma_f32_16x16x32_bf16 v[76:79], a[240:243], v[80:83], v[76:79]\n\t" \
  "v_exp_f32 v103, v103\n\t" \
  "v_mfma_f32_16x16x32_bf16 v[96:99], a[192:195], v[112:115], v[188:191]\n\t" \
  "v_cvt_pk_bf16_f32 v155, v102, v103\n\t" \
  "ds_read_b64_tr_b16 a[180:181], v228 offset:8960\n\t" \
  "v_mfma_f32_16x16x32_bf16 v[96:99], a[196:199], v[116:119], v[96:99]\n\t" \
  "v_exp_f32 v104, v104\n\t" \
  "v_mfma_f32_16x16x32_bf16 v[96:99], a[200:203], v[120:123], v[96:99]\n\t" \
  "ds_read_b64_tr_b16 a[182:183], v228 offset:13056\n\t" \
  "v_max3_f32 v203, v203, v76, v77\n\t" \
  "v_mfma_f32_16x16x32_bf16 v[96:99], a[204:207], v[124:127], v[96:99]\n\t" \
  "v_max3_f32 v192, v192, v78, v79\n\t" \
  "v_max_f32 v192, v192, v203\n\t" \
  "v_mfma_f32_16x16x32_bf16 v[100:103], a[220:223], v[124:127], v[188:191]\n\t" \
  "v_cmp_lt_f32 vcc, 0x42800000, v192\n\t" \
  "ds_read_b64_tr_b16 a[184:185], v229 offset:768\n\t" \
  "v_mfma_f32_16x16x32_bf16 v[100:103], a[216:219], v[120:123], v[100:103]\n\t" \
  "s_cbranch_vccnz .Lsf_c52\n\t" \
  ".Lsf_r52:\n\t" \
  "ds_read_b64_tr_b16 a[186:187], v229 offset:4864\n\t" \
  "v_mfma_f32_16x16x32_bf16 v[100:103], a[212:215], v[116:119], v[100:103]\n\t" \
  "v_max3_f32 v192, v96, v97, v98\n\t" \
  "ds_read_b64_tr_b16 a[188:189], v229 offset:8960\n\t" \
  "v_mfma_f32_16x16x32_bf16 v[100:103], a[208:211], v[112:115], v[100:103]\n\t" \
  "v_exp_f32 v105, v105\n\t" \
  "v_exp_f32 v106, v106\n\t" \
  "v_exp_f32 v107, v107\n\t" \
  "v_cvt_pk_bf16_f32 v156, v104, v105\n\t" \
  "v_cvt_pk_bf16_f32 v157, v106, v107\n\t" \
  "v_mfma_f32_16x16x32_bf16 v[104:107], a[224:227], v[112:115], v[188:191]\n\t" \
  "v_exp_f32 v108, v108\n\t" \
  "v_mfma_f32_16x16x32_bf16 v[104:107], a[228:231], v[116:119], v[104:107]\n\t" \
  "ds_read_b64_tr_b16 a[190:191], v229 offset:13056\n\t" \
  "v_max3_f32 v203, v99, v100, v101\n\t" \
  "v_mfma_f32_16x16x32_bf16 v[104:107], a[232:235], v[120:123], v[104:107]\n\t" \
  "v_max3_f32 v192, v192, v102, v103\n\t" \
  "v_mfma_f32_16x16x32_bf16 v[104:107], a[236:239], v[124:127], v[104:107]\n\t" \
  "v_exp_f32 v109, v109\n\t" \
  "v_exp_f32 v110, v110\n\t" \
  "v_exp_f32 v111, v111\n\t" \
  "v_cvt_pk_bf16_f32 v158, v108, v109\n\t" \
  "v_cvt_pk_bf16_f32 v159, v110, v111\n\t" \
  "v_mfma_f32_16x16x32_bf16 v[108:111], a[252:255], v[124:127], v[188:191]\n\t" \
  "v_exp_f32 v0, v0\n\t" \
  "v_mfma_f32_16x16x32_bf16 v[108:111], a[248:251], v[120:123], v[108:111]\n\t" \
  "v_exp_f32 v1, v1\n\t" \
  "v_mfma_f32_16x16x32_bf16 v[108:111], a[244:247], v[116:119], v[108:111]\n\t" \
  "v_max3_f32 v203, v203, v104, v105\n\t" \
  "v_max3_f32 v192, v192, v106, v107\n\t" \
  "v_mfma_f32_16x16x32_bf16 v[108:111], a[240:243], v[112:115], v[108:111]\n\t" \
  "v_exp_f32 v2, v2\n\t" \
  "s_waitcnt vmcnt(3) lgkmcnt(0)\n\t" \
  "s_barrier\n\t" \
  "v_mfma_f32_16x16x32_bf16 a[0:3], a[128:131], v[128:131], a[0:3]\n\t" \
  "v_exp_f32 v3, v3\n\t" \
  "v_mfma_f32_16x16x32_bf16 a[0:3], a[132:135], v[132:135], a[0:3]\n\t" \
  "v_exp_f32 v4, v4\n\t" \
  "v_mfma_f32_16x16x32_bf16 a[16:19], a[136:139], v[128:131], a[16:19]\n\t" \
  "ds_read_b128 a[192:195], v220 offset:0\n\t" \
  "v_max3_f32 v203, v203, v108, v109\n\t" \
  "v_mfma_f32_16x16x32_bf16 a[16:19], a[140:143], v[132:135], a[16:19]\n\t" \
  "v_max3_f32 v192, v192, v110, v111\n\t" \
  "v_max_f32 v192, v192, v203\n\t" \
  "v_mfma_f32_16x16x32_bf16 a[32:35], a[144:147], v[128:131], a[32:35]\n\t" \
  "v_cmp_lt_f32 vcc, 0x42800000, v192\n\t" \
  "ds_read_b128 a[196:199], v221 offset:0\n\t" \
  "v_mfma_f32_16x16x32_bf16 a[32:35], a[148:151], v[132:135], a[32:35]\n\t" \
  "s_cbranch_vccnz .Lsf_c53\n\t" \
  ".Lsf_r53:\n\t" \
  "ds_read_b128 a[200:203], v222 offset:0\n\t" \
  "v_mfma_f32_16x16x32_bf16 a[48:51], a[152:155], v[128:131], a[48:51]\n\t" \
  "v_exp_f32 v5, v5\n\t" \
  "v_mfma_f32_16x16x32_bf16 a[48:51], a[156:159], v[132:135], a[48:51]\n\t" \
  "v_exp_f32 v6, v6\n\t" \
  "v_mfma_f32_16x16x32_bf16 a[64:67], a[160:163], v[128:131], a[64:67]\n\t" \
  "v_exp_f32 v7, v7\n\t" \
  "v_mfma_f32_16x16x32_bf16 a[64:67], a[164:167], v[132:135], a[64:67]\n\t" \
  "v_exp_f32 v8, v8\n\t" \
  "v_mfma_f32_16x16x32_bf16 a[80:83], a[168:171], v[128:131], a[80:83]\n\t" \
  "v_exp_f32 v9, v9\n\t" \
  "v_mfma_f32_16x16x32_bf16 a[80:83], a[172:175], v[132:135], a[80:83]\n\t" \
  "v_exp_f32 v10, v10\n\t" \
  "v_mfma_f32_16x16x32_bf16 a[96:99], a[176:179], v[128:131], a[96:99]\n\t" \
  "v_exp_f32 v11, v11\n\t" \
  "v_mfma_f32_16x16x32_bf16 a[96:99], a[180:183], v[132:135], a[96:99]\n\t" \
  "v_exp_f32 v12, v12\n\t" \
  "v_mfma_f32_16x16x32_bf16 a[112:115], a[184:187], v[128:131], a[112:115]\n\t" \
  "ds_read_b128 a[204:207], v223 offset:0\n\t" \
  "ds_read_b128 a[208:211], v220 offset:4096\n\t" \
  "v_mfma_f32_16x16x32_bf16 a[112:115], a[188:191], v[132:135], a[112:115]\n\t" \
  "v_exp_f32 v13, v13\n\t" \
  "v_mfma_f32_16x16x32_bf16 v[160:163], v[216:219], v[128:131], v[160:163]\n\t" \
  "v_exp_f32 v14, v14\n\t" \
  "v_mfma_f32_16x16x32_bf16 v[160:163], v[216:219], v[132:135], v[160:163]\n\t" \
  "v_cvt_pk_bf16_f32 v128, v0, v1\n\t" \
  "v_cvt_pk_bf16_f32 v129, v2, v3\n\t" \
  "v_mfma_f32_16x16x32_bf16 a[4:7], a[128:131], v[136:139], a[4:7]\n\t" \
  "v_cvt_pk_bf16_f32 v130, v4, v5\n\t" \
  "v_cvt_pk_bf16_f32 v131, v6, v7\n\t" \
  "v_mfma_f32_16x16x32_bf16 a[4:7], a[132:135], v[140:143], a[4:7]\n\t" \
  "v_cvt_pk_bf16_f32 v132, v8, v9\n\t" \
  "v_cvt_pk_bf16_f32 v133, v10, v11\n\t" \
  "v_mfma_f32_16x16x32_bf16 a[20:23], a[136:139], v[136:139], a[20:23]\n\t" \
  "v_cvt_pk_bf16_f32 v134, v12, v13\n\t" \
  "ds_read_b128 a[212:215], v221 offset:4096\n\t" \
  "v_mfma_f32_16x16x32_bf16 a[20:23], a[140:143], v[140:143], a[20:23]\n\t" \
  "v_exp_f32 v15, v15\n\t" \
  "v_mfma_f32_16x16x32_bf16 a[36:39], a[144:147], v[136:139], a[36:39]\n\t" \
  "v_cvt_pk_bf16_f32 v135, v14, v15\n\t" \
  "s_add_u32 m0, %[m], 65536\n\t" \
  "v_mfma_f32_16x16x32_bf16 a[36:39], a[148:151], v[140:143], a[36:39]\n\t" \
  "v_exp_f32 v32, v32\n\t" \
  "v_mfma_f32_16x16x32_bf16 a[52:55], a[152:155], v[136:139], a[52:55]\n\t" \
  "ds_read_b128 a[216:219], v222 offset:4096\n\t" \
  "s_add_u32 s96, %[sofk], 114688\n\t" \
  "v_mfma_f32_16x16x32_bf16 a[52:55], a[156:159], v[140:143], a[52:55]\n\t" \
  "v_exp_f32 v33, v33\n\t" \
  "v_mfma_f32_16x16x32_bf16 a[68:71], a[160:163], v[136:139], a[68:71]\n\t" \
  "v_exp_f32 v34, v34\n\t" \
  "v_mfma_f32_16x16x32_bf16 a[68:71], a[164:167], v[140:143], a[68:71]\n\t" \
  "ds_read_b128 a[220:223], v223 offset:4096\n\t" \
  "buffer_load_dwordx4 v224, %[sK], s96 offen offset:0 lds\n\t" \
  "v_mfma_f32_16x16x32_bf16 a[84:87], a[168:171], v[136:139], a[84:87]\n\t" \
  "v_exp_f32 v35, v35\n\t" \
  "v_mfma_f32_16x16x32_bf16 a[84:87], a[172:175], v[140:143], a[84:87]\n\t" \
  "v_exp_f32 v36, v36\n\t" \
  "v_mfma_f32_16x16x32_bf16 a[100:103], a[176:179], v[136:139], a[100:103]\n\t" \
  "v_exp_f32 v37, v37\n\t" \
  "v_mfma_f32_16x16x32_bf16 a[100:103], a[180:183], v[140:143], a[100:103]\n\t" \
  "ds_read_b128 a[224:227], v220 offset:8192\n\t" \
  "buffer_load_dwordx4 v225, %[sK], s96 offen offset:1024 lds\n\t" \
  "v_mfma_f32_16x16x32_bf16 a[116:119], a[184:187], v[136:139], a[116:119]\n\t" \
  "v_exp_f32 v38, v38\n\t" \
  "v_mfma_f32_16x16x32_bf16 a[116:119], a[188:191], v[140:143], a[116:119]\n\t" \
  "v_exp_f32 v39, v39\n\t" \
  "v_mfma_f32_16x16x32_bf16 v[164:167], v[216:219], v[136:139], v[164:167]\n\t" \
  "buffer_load_dwordx4 v226, %[sK], s96 offen offset:2048 lds\n\t" \
  "ds_read_b128 a[228:231], v221 offset:8192\n\t" \
  "v_mfma_f32_16x16x32_bf16 v[164:167], v[216:219], v[140:143], v[164:167]\n\t" \
  "v_cvt_pk_bf16_f32 v136, v32, v33\n\t" \
  "v_cvt_pk_bf16_f32 v137, v34, v35\n\t" \
  "v_mfma_f32_16x16x32_bf16 a[8:11], a[128:131], v[144:147], a[8:11]\n\t" \
  "v_cvt_pk_bf16_f32 v138, v36, v37\n\t" \
  "v_cvt_pk_bf16_f32 v139, v38, v39\n\t" \
  "v_mfma_f32_16x16x32_bf16 a[8:11], a[132:135], v[148:151], a[8:11]\n\t" \
  "v_exp_f32 v40, v40\n\t" \
  "v_mfma_f32_16x16x32_bf16 a[24:27], a[136:139], v[144:147], a[24:27]\n\t" \
  "buffer_load_dwordx4 v227, %[sK], s96 offen offset:3072 lds\n\t" \
  "ds_read_b128 a[232:235], v222 offset:8192\n\t" \
  "v_mfma_f32_16x16x32_bf16 a[24:27], a[140:143], v[148:151], a[24:27]\n\t" \
  "v_exp_f32 v41, v41\n\t" \
  "v_mfma_f32_16x16x32_bf16 a[40:43], a[144:147], v[144:147], a[40:43]\n\t" \
  "v_cvt_pk_bf16_f32 v140, v40, v41\n\t" \
  "ds_read_b128 a[236:239], v223 offset:8192\n\t" \
  "v_mfma_f32_16x16x32_bf16 a[40:43], a[148:151], v[148:151], a[40:43]\n\t" \
  "v_exp_f32 v42, v42\n\t" \
  "v_mfma_f32_16x16x32_bf16 a[56:59], a[152:155], v[144:147], a[56:59]\n\t" \
  "v_exp_f32 v43, v43\n\t" \
  "v_mfma_f32_16x16x32_bf16 a[56:59], a[156:159], v[148:151], a[56:59]\n\t" \
  "v_cvt_pk_bf16_f32 v141, v42, v43\n\t" \
  "ds_read_b128 a[240:243], v220 offset:12288\n\t" \
  "v_mfma_f32_16x16x32_bf16 a[72:75], a[160:163], v[144:147], a[72:75]\n\t" \
  "s_add_u32 m0, %[m], 98304\n\t" \
  "s_mov_b32 s96, 28672\n\t" \
  "buffer_load_dwordx4 v232, %[sG], s96 offen offset:0 lds\n\t" \
  "v_mfma_f32_16x16x32_bf16 a[72:75], a[164:167], v[148:151], a[72:75]\n\t" \
  "v_exp_f32 v44, v44\n\t" \
  "v_mfma_f32_16x16x32_bf16 a[88:91], a[168:171], v[144:147], a[88:91]\n\t" \
  "s_add_u32 m0, %[m], 81920\n\t" \
  "s_add_u32 s96, %[sofk], 98304\n\t" \
  "v_mfma_f32_16x16x32_bf16 a[88:91], a[172:175], v[148:151], a[88:91]\n\t" \
  "v_exp_f32 v45, v45\n\t" \
  "v_mfma_f32_16x16x32_bf16 a[104:107], a[176:179], v[144:147], a[104:107]\n\t" \
  "v_cvt_pk_bf16_f32 v142, v44, v45\n\t" \
  "ds_read_b128 a[244:247], v221 offset:12288\n\t" \
  "v_mfma_f32_16x16x32_bf16 a[104:107], a[180:183], v[148:151], a[104:107]\n\t" \
  "v_exp_f32 v46, v46\n\t" \
  "v_mfma_f32_16x16x32_bf16 a[120:123], a[184:187], v[144:147], a[120:123]\n\t" \
  "v_exp_f32 v47, v47\n\t" \
  "v_mfma_f32_16x16x32_bf16 a[120:123], a[188:191], v[148:151], a[120:123]\n\t" \
  "v_cvt_pk_bf16_f32 v143, v46, v47\n\t" \
  "buffer_load_dwordx4 v230, %[sV], s96 offen offset:0 lds\n\t" \
  "v_mfma_f32_16x16x32_bf16 v[168:171], v[216:219], v[144:147], v[168:171]\n\t" \
  "v_exp_f32 v64, v64\n\t" \
  "v_mfma_f32_16x16x32_bf16 v[168:171], v[216:219], v[148:151], v[168:171]\n\t" \
  "v_exp_f32 v65, v65\n\t" \
  "v_mfma_f32_16x16x32_bf16 a[12:15], a[128:131], v[152:155], a[12:15]\n\t" \
  "v_cvt_pk_bf16_f32 v144, v64, v65\n\t" \
  "ds_read_b128 a[248:251], v222 offset:12288\n\t" \
  "v_mfma_f32_16x16x32_bf16 a[12:15], a[132:135], v[156:159], a[12:15]\n\t" \
  "v_exp_f32 v66, v66\n\t" \
  "v_mfma_f32_16x16x32_bf16 a[28:31], a[136:139], v[152:155], a[28:31]\n\t" \
  "v_exp_f32 v67, v67\n\t" \
  "v_mfma_f32_16x16x32_bf16 a[28:31], a[140:143], v[156:159], a[28:31]\n\t" \
  "v_cvt_pk_bf16_f32 v145, v66, v67\n\t" \
  "buffer_load_dwordx4 v231, %[sV], s96 offen offset:1024 lds\n\t" \
  "v_mfma_f32_16x16x32_bf16 a[44:47], a[144:147], v[152:155], a[44:47]\n\t" \
  "ds_read_b128 a[252:255], v223 offset:12288\n\t" \
  "buffer_load_dwordx4 v230, %[sV], s96 offen offset:2048 lds\n\t" \
  "v_mfma_f32_16x16x32_bf16 a[44:47], a[148:151], v[156:159], a[44:47]\n\t" \
  "v_exp_f32 v68, v68\n\t" \
  "v_mfma_f32_16x16x32_bf16 a[60:63], a[152:155], v[152:155], a[60:63]\n\t" \
  "v_exp_f32 v69, v69\n\t" \
  "v_mfma_f32_16x16x32_bf16 a[60:63], a[156:159], v[156:159], a[60:63]\n\t" \
  "v_cvt_pk_bf16_f32 v146, v68, v69\n\t" \
  "buffer_load_dwordx4 v231, %[sV], s96 offen offset:3072 lds\n\t" \
  "v_mfma_f32_16x16x32_bf16 a[76:79], a[160:163], v[152:155], a[76:79]\n\t" \
  "v_exp_f32 v70, v70\n\t" \
  "v_mfma_f32_16x16x32_bf16 a[76:79], a[164:167], v[156:159], a[76:79]\n\t" \
  "v_exp_f32 v71, v71\n\t" \
  "v_mfma_f32_16x16x32_bf16 a[92:95], a[168:171], v[152:155], a[92:95]\n\t" \
  "v_cvt_pk_bf16_f32 v147, v70, v71\n\t" \
  "v_mfma_f32_16x16x32_bf16 a[92:95], a[172:175], v[156:159], a[92:95]\n\t" \
  "v_lshrrev_b32 v205, 1, v233\n\t" \
  "v_lshlrev_b32 v205, 2, v205\n\t" \
  "v_mfma_f32_16x16x32_bf16 a[108:111], a[176:179], v[152:155], a[108:111]\n\t" \
  "v_add_u32 v205, 8192, v205\n\t" \
  "buffer_load_dword v209, v205, %[ws], 0 offen sc1\n\t" \
  "v_mfma_f32_16x16x32_bf16 a[108:111], a[180:183], v[156:159], a[108:111]\n\t" \
  "s_add_u32 m0, %[m], 98304\n\t" \
  "s_mov_b32 s96, 34816\n\t" \
  "buffer_load_dwordx4 v232, %[sG], s96 offen offset:1024 lds\n\t" \
  "v_mfma_f32_16x16x32_bf16 a[124:127], a[184:187], v[152:155], a[124:127]\n\t" \
  "v_exp_f32 v72, v72\n\t" \
  "v_mfma_f32_16x16x32_bf16 a[124:127], a[188:191], v[156:159], a[124:127]\n\t" \
  "v_exp_f32 v73, v73\n\t" \
  "v_mfma_f32_16x16x32_bf16 v[172:175], v[216:219], v[152:155], v[172:175]\n\t" \
  "v_cvt_pk_bf16_f32 v148, v72, v73\n\t" \
  "v_mfma_f32_16x16x32_bf16 v[172:175], v[216:219], v[156:159], v[172:175]\n\t" \
  "v_exp_f32 v74, v74\n\t" \
  "s_cmp_lg_u32 s99, 0\n\t" \
  "s_cbranch_scc1 .Lsf_p5\n\t" \
  ".Lsf_q5:\n\t" \
  "s_waitcnt lgkmcnt(0)\n\t" \
  "v_mfma_f32_16x16x32_bf16 v[0:3], a[192:195], v[16:19], v[176:179]\n\t" \
  "s_add_u32 s98, %[pf2], 49152\n\t" \
  "buffer_load_dword %[pfd], v202, %[sM], s98 offen\n\t" \
  "v_mfma_f32_16x16x32_bf16 v[0:3], a[196:199], v[20:23], v[0:3]\n\t" \
  "v_exp_f32 v75, v75\n\t" \
  "v_mfma_f32_16x16x32_bf16 v[0:3], a[200:203], v[24:27], v[0:3]\n\t" \
  "v_cvt_pk_bf16_f32 v149, v74, v75\n\t" \
  "ds_read_b64_tr_b16 a[128:129], v237 offset:0\n\t" \
  "v_mfma_f32_16x16x32_bf16 v[0:3], a[204:207], v[28:31], v[0:3]\n\t" \
  "v_exp_f32 v76, v76\n\t" \
  "v_mfma_f32_16x16x32_bf16 v[4:7], a[220:223], v[28:31], v[176:179]\n\t" \
  "ds_read_b64_tr_b16 a[130:131], v237 offset:4096\n\t" \
  "ds_read_b64_tr_b16 a[132:133], v237 offset:8192\n\t" \
  "v_mfma_f32_16x16x32_bf16 v[4:7], a[216:219], v[24:27], v[4:7]\n\t" \
  "v_exp_f32 v77, v77\n\t" \
  "v_mfma_f32_16x16x32_bf16 v[4:7], a[212:215], v[20:23], v[4:7]\n\t" \
  "v_cvt_pk_bf16_f32 v150, v76, v77\n\t" \
  "v_max3_f32 v192, v0, v1, v2\n\t" \
  "v_mfma_f32_16x16x32_bf16 v[4:7], a[208:211], v[16:19], v[4:7]\n\t" \
  "v_exp_f32 v78, v78\n\t" \
  "v_mfma_f32_16x16x32_bf16 v[8:11], a[224:227], v[16:19], v[176:179]\n\t" \
  "ds_read_b64_tr_b16 a[134:135], v237 offset:12288\n\t" \
  "ds_read_b64_tr_b16 a[136:137], v238 offset:0\n\t" \
  "v_mfma_f32_16x16x32_bf16 v[8:11], a[228:231], v[20:23], v[8:11]\n\t" \
  "v_exp_f32 v79, v79\n\t" \
  "v_mfma_f32_16x16x32_bf16 v[8:11], a[232:235], v[24:27], v[8:11]\n\t" \
  "v_cvt_pk_bf16_f32 v151, v78, v79\n\t" \
  "v_max3_f32 v203, v3, v4, v5\n\t" \
  "v_mfma_f32_16x16x32_bf16 v[8:11], a[236:239], v[28:31], v[8:11]\n\t" \
  "v_max3_f32 v192, v192, v6, v7\n\t" \
  "ds_read_b64_tr_b16 a[138:139], v238 offset:4096\n\t" \
  "v_mfma_f32_16x16x32_bf16 v[12:15], a[252:255], v[28:31], v[176:179]\n\t" \
  "ds_read_b64_tr_b16 a[140:141], v238 offset:8192\n\t" \
  "ds_read_b64_tr_b16 a[142:143], v238 offset:12288\n\t" \
  "v_mfma_f32_16x16x32_bf16 v[12:15], a[248:251], v[24:27], v[12:15]\n\t" \
  "v_exp_f32 v96, v96\n\t" \
  "v_mfma_f32_16x16x32_bf16 v[12:15], a[244:247], v[20:23], v[12:15]\n\t" \
  "v_max3_f32 v203, v203, v8, v9\n\t" \
  "v_max3_f32 v192, v192, v10, v11\n\t" \
  "v_mfma_f32_16x16x32_bf16 v[12:15], a[240:243], v[16:19], v[12:15]\n\t" \
  "s_add_u32 s98, %[pf2], 73728\n\t" \
  "buffer_load_dword %[pfd], v202, %[sM], s98 offen\n\t" \
  "v_mfma_f32_16x16x32_bf16 v[32:35], a[192:195], v[48:51], v[180:183]\n\t" \
  "ds_read_b64_tr_b16 a[144:145], v237 offset:256\n\t" \
  "ds_read_b64_tr_b16 a[146:147], v237 offset:4352\n\t" \
  "v_mfma_f32_16x16x32_bf16 v[32:35], a[196:199], v[52:55], v[32:35]\n\t" \
  "v_exp_f32 v97, v97\n\t" \
  "v_mfma_f32_16x16x32_bf16 v[32:35], a[200:203], v[56:59], v[32:35]\n\t" \
  "v_cvt_pk_bf16_f32 v152, v96, v97\n\t" \
  "v_max3_f32 v203, v203, v12, v13\n\t" \
  "v_mfma_f32_16x16x32_bf16 v[32:35], a[204:207], v[60:63], v[32:35]\n\t" \
  "v_max3_f32 v192, v192, v14, v15\n\t" \
  "v_max_f32 v192, v192, v203\n\t" \
  "v_mfma_f32_16x16x32_bf16 v[36:39], a[220:223], v[60:63], v[180:183]\n\t" \
  "v_cmp_lt_f32 vcc, 0x42800000, v192\n\t" \
  "ds_read_b64_tr_b16 a[148:149], v237 offset:8448\n\t" \
  "v_mfma_f32_16x16x32_bf16 v[36:39], a[216:219], v[56:59], v[36:39]\n\t" \
  "s_cbranch_vccnz .Lsf_c60\n\t" \
  ".Lsf_r60:\n\t" \
  "ds_read_b64_tr_b16 a[150:151], v237 offset:12544\n\t" \
  "v_mfma_f32_16x16x32_bf16 v[36:39], a[212:215], v[52:55], v[36:39]\n\t" \
  "v_max3_f32 v192, v32, v33, v34\n\t" \
  "ds_read_b64_tr_b16 a[152:153], v238 offset:256\n\t" \
  "v_mfma_f32_16x16x32_bf16 v[36:39], a[208:211], v[48:51], v[36:39]\n\t" \
  "v_exp_f32 v98, v98\n\t" \
  "v_mfma_f32_16x16x32_bf16 v[40:43], a[224:227], v[48:51], v[180:183]\n\t" \
  "v_exp_f32 v99, v99\n\t" \
  "v_mfma_f32_16x16x32_bf16 v[40:43], a[228:231], v[52:55], v[40:43]\n\t" \
  "v_cvt_pk_bf16_f32 v153, v98, v99\n\t" \
  "ds_read_b64_tr_b16 a[154:155], v238 offset:4352\n\t" \
  "v_mfma_f32_16x16x32_bf16 v[40:43], a[232:235], v[56:59], v[40:43]\n\t" \
  "v_exp_f32 v100, v100\n\t" \
  "v_mfma_f32_16x16x32_bf16 v[40:43], a[236:239], v[60:63], v[40:43]\n\t" \
  "v_max3_f32 v203, v35, v36, v37\n\t" \
  "v_max3_f32 v192, v192, v38, v39\n\t" \
  "v_mfma_f32_16x16x32_bf16 v[44:47], a[252:255], v[60:63], v[180:183]\n\t" \
  "ds_read_b64_tr_b16 a[156:157], v238 offset:8448\n\t" \
  "ds_read_b64_tr_b16 a[158:159], v238 offset:12544\n\t" \
  "v_mfma_f32_16x16x32_bf16 v[44:47], a[248:251], v[56:59], v[44:47]\n\t" \
  "v_exp_f32 v101, v101\n\t" \
  "v_mfma_f32_16x16x32_bf16 v[44:47], a[244:247], v[52:55], v[44:47]\n\t" \
  "v_cvt_pk_bf16_f32 v154, v100, v101\n\t" \
  "ds_read_b64_tr_b16 a[160:161], v237 offset:512\n\t" \
  "v_mfma_f32_16x16x32_bf16 v[44:47], a[240:243], v[48:51], v[44:47]\n\t" \
  "v_max3_f32 v203, v203, v40, v41\n\t" \
  "v_max3_f32 v192, v192, v42, v43\n\t" \
  "v_mfma_f32_16x16x32_bf16 v[64:67], a[192:195], v[80:83], v[184:187]\n\t" \
  "ds_read_b64_tr_b16 a[162:163], v237 offset:4608\n\t" \
  "ds_read_b64_tr_b16 a[164:165], v237 offset:8704\n\t" \
  "v_mfma_f32_16x16x32_bf16 v[64:67], a[196:199], v[84:87], v[64:67]\n\t" \
  "v_exp_f32 v102, v102\n\t" \
  "v_mfma_f32_16x16x32_bf16 v[64:67], a[200:203], v[88:91], v[64:67]\n\t" \
  "v_max3_f32 v203, v203, v44, v45\n\t" \
  "v_max3_f32 v192, v192, v46, v47\n\t" \
  "v_mfma_f32_16x16x32_bf16 v[64:67], a[204:207], v[92:95], v[64:67]\n\t" \
  "v_max_f32 v192, v192, v203\n\t" \
  "ds_read_b64_tr_b16 a[166:167], v237 offset:12800\n\t" \
  "v_mfma_f32_16x16x32_bf16 v[68:71], a[220:223], v[92:95], v[184:187]\n\t" \
  "v_cmp_lt_f32 vcc, 0x42800000, v192\n\t" \
  "ds_read_b64_tr_b16 a[168:169], v238 offset:512\n\t" \
  "v_mfma_f32_16x16x32_bf16 v[68:71], a[216:219], v[88:91], v[68:71]\n\t" \
  "s_cbranch_vccnz .Lsf_c61\n\t" \
  ".Lsf_r61:\n\t" \
  "ds_read_b64_tr_b16 a[170:171], v238 offset:4608\n\t" \
  "v_mfma_f32_16x16x32_bf16 v[68:71], a[212:215], v[84:87], v[68:71]\n\t" \
  "v_max3_f32 v192, v64, v65, v66\n\t" \
  "ds_read_b64_tr_b16 a[172:173], v238 offset:8704\n\t" \
  "v_mfma_f32_16x16x32_bf16 v[68:71], a[208:211], v[80:83], v[68:71]\n\t" \
  "v_exp_f32 v103, v103\n\t" \
  "v_mfma_f32_16x16x32_bf16 v[72:75], a[224:227], v[80:83], v[184:187]\n\t" \
  "v_cvt_pk_bf16_f32 v155, v102, v103\n\t" \
  "ds_read_b64_tr_b16 a[174:175], v238 offset:12800\n\t" \
  "v_mfma_f32_16x16x32_bf16 v[72:75], a[228:231], v[84:87], v[72:75]\n\t" \
  "v_exp_f32 v104, v104\n\t" \
  "v_mfma_f32_16x16x32_bf16 v[72:75], a[232:235], v[88:91], v[72:75]\n\t" \
  "s_lshr_b32 s98, %[hl18], 18\n\t" \
  "v_xor_b32 v245, s98, v233\n\t" \
  "v_mfma_f32_16x16x32_bf16 v[72:75], a[236:239], v[92:95], v[72:75]\n\t" \
  "v_max3_f32 v203, v67, v68, v69\n\t" \
  "v_max3_f32 v192, v192, v70, v71\n\t" \
  "v_mfma_f32_16x16x32_bf16 v[76:79], a[252:255], v[92:95], v[184:187]\n\t" \
  "ds_read_b64_tr_b16 a[176:177], v237 offset:768\n\t" \
  "ds_read_b64_tr_b16 a[178:179], v237 offset:4864\n\t" \
  "v_mfma_f32_16x16x32_bf16 v[76:79], a[248:251], v[88:91], v[76:79]\n\t" \
  "s_waitcnt vmcnt(3)\n\t" \
  "v_and_b32 v206, 0xbfffbfff, v209\n\t" \
  "v_cmp_ne_u32_e64 s[96:97], %[tag], v206\n\t" \
  "s_cmp_lg_u64 s[96:97], 0\n\t" \
  "s_cbranch_scc1 .Lsf_v\n\t" \
  ".Lsf_w:\n\t" \
  "v_mfma_f32_16x16x32_bf16 v[76:79], a[244:247], v[84:87], v[76:79]\n\t" \
  "v_and_b32 v206, 1, v233\n\t" \
  "v_max3_f32 v203, v203, v72, v73\n\t" \
  "v_mfma_f32_16x16x32_bf16 v[76:79], a[240:243], v[80:83], v[76:79]\n\t" \
  "v_max3_f32 v192, v192, v74, v75\n\t" \
  "v_lshlrev_b32 v206, 4, v206\n\t" \
  "v_mfma_f32_16x16x32_bf16 v[96:99], a[192:195], v[112:115], v[188:191]\n\t" \
  "v_lshrrev_b32 v206, v206, v209\n\t" \
  "v_and_b32 v245, 31, v245\n\t" \
  "v_mfma_f32_16x16x32_bf16 v[96:99], a[196:199], v[116:119], v[96:99]\n\t" \
  "ds_read_b64_tr_b16 a[180:181], v237 offset:8960\n\t" \
  "v_and_b32 v206, 0x4000, v206\n\t" \
  "v_mfma_f32_16x16x32_bf16 v[96:99], a[200:203], v[120:123], v[96:99]\n\t" \
  "v_cmp_ne_u32_e64 s[96:97], 0, v206\n\t" \
  "v_mfma_f32_16x16x32_bf16 v[96:99], a[204:207], v[124:127], v[96:99]\n\t" \
  "ds_read_b64_tr_b16 a[182:183], v237 offset:13056\n\t" \
  "v_max3_f32 v203, v203, v76, v77\n\t" \
  "v_mfma_f32_16x16x32_bf16 v[100:103], a[220:223], v[124:127], v[188:191]\n\t" \
  "v_max3_f32 v192, v192, v78, v79\n\t" \
  "v_max_f32 v192, v192, v203\n\t" \
  "v_mfma_f32_16x16x32_bf16 v[100:103], a[216:219], v[120:123], v[100:103]\n\t" \
  "v_cmp_lt_f32 vcc, 0x42800000, v192\n\t" \
  "s_bcnt1_i32_b32 %[nb], s96\n\t" \
  "v_mfma_f32_16x16x32_bf16 v[100:103], a[212:215], v[116:119], v[100:103]\n\t" \
  "s_cbranch_vccnz .Lsf_c62\n\t" \
  ".Lsf_r62:\n\t" \
  "ds_read_b64_tr_b16 a[184:185], v238 offset:768\n\t" \
  "v_mfma_f32_16x16x32_bf16 v[100:103], a[208:211], v[112:115], v[100:103]\n\t" \
  "s_bcnt1_i32_b32 %[na], s97\n\t" \
  "s_lshr_b32 s96, s96, 16\n\t" \
  "v_exp_f32 v105, v105\n\t" \
  "v_exp_f32 v106, v106\n\t" \
  "v_exp_f32 v107, v107\n\t" \
  "v_cvt_pk_bf16_f32 v156, v104, v105\n\t" \
  "v_cvt_pk_bf16_f32 v157, v106, v107\n\t" \
  "v_mfma_f32_16x16x32_bf16 v[104:107], a[224:227], v[112:115], v[188:191]\n\t" \
  "s_lshr_b32 s98, %[hl18], 18\n\t" \
  "s_mul_i32 s98, s98, 127\n\t" \
  "v_add_lshl_u32 v245, v245, s98, 2\n\t" \
  "v_mfma_f32_16x16x32_bf16 v[104:107], a[228:231], v[116:119], v[104:107]\n\t" \
  "ds_read_b64_tr_b16 a[186:187], v238 offset:4864\n\t" \
  "s_lshr_b32 s97, s97, 16\n\t" \
  "v_mfma_f32_16x16x32_bf16 v[104:107], a[232:235], v[120:123], v[104:107]\n\t" \
  "v_max3_f32 v192, v96, v97, v98\n\t" \
  "s_bcnt1_i32_b32 s96, s96\n\t" \
  "v_mfma_f32_16x16x32_bf16 v[104:107], a[236:239], v[124:127], v[104:107]\n\t" \
  "ds_read_b64_tr_b16 a[188:189], v238 offset:8960\n\t" \
  "s_bcnt1_i32_b32 s97, s97\n\t" \
  "v_exp_f32 v108, v108\n\t" \
  "v_exp_f32 v109, v109\n\t" \
  "v_exp_f32 v110, v110\n\t" \
  "v_exp_f32 v111, v111\n\t" \
  "v_cvt_pk_bf16_f32 v158, v108, v109\n\t" \
  "v_cvt_pk_bf16_f32 v159, v110, v111\n\t" \
  "v_mfma_f32_16x16x32_bf16 v[108:111], a[252:255], v[124:127], v[188:191]\n\t" \
  "s_sub_u32 s96, s97, s96\n\t" \
  "ds_read_b64_tr_b16 a[190:191], v238 offset:13056\n\t" \
  "v_mfma_f32_16x16x32_bf16 v[108:111], a[248:251], v[120:123], v[108:111]\n\t" \
  "s_sub_u32 %[nb], %[na], %[nb]\n\t" \
  "v_max3_f32 v203, v99, v100, v101\n\t" \
  "v_mfma_f32_16x16x32_bf16 v[108:111], a[244:247], v[116:119], v[108:111]\n\t" \
  "v_max3_f32 v192, v192, v102, v103\n\t" \
  "s_ashr_i32 %[nb], %[nb], 31\n\t" \
  "v_mfma_f32_16x16x32_bf16 v[108:111], a[240:243], v[112:115], v[108:111]\n\t" \
  "s_xor_b32 s96, s96, %[nb]\n\t" \
  "s_sub_u32 s96, s96, %[nb]\n\t" \
  "s_waitcnt lgkmcnt(0)\n\t" \
  "s_barrier\n\t" \
  "v_mfma_f32_16x16x32_bf16 a[0:3], a[128:131], v[128:131], a[0:3]\n\t" \
  "s_sub_u32 %[na], 6, s96\n\t" \
  "v_max3_f32 v203, v203, v104, v105\n\t" \
  "v_mfma_f32_16x16x32_bf16 a[0:3], a[132:135], v[132:135], a[0:3]\n\t" \
  "v_max3_f32 v192, v192, v106, v107\n\t" \
  "s_ashr_i32 %[na], %[na], 31\n\t" \
  "v_mfma_f32_16x16x32_bf16 a[16:19], a[136:139], v[128:131], a[16:19]\n\t" \
  "s_and_b32 %[nb], %[nb], 0x2000\n\t" \
  "s_xor_b32 s96, %[po], %[nb]\n\t" \
  "v_mfma_f32_16x16x32_bf16 a[16:19], a[140:143], v[132:135], a[16:19]\n\t" \
  "s_and_b32 s96, s96, %[na]\n\t" \
  "ds_read_b128 a[192:195], v220 offset:16384\n\t" \
  "v_mfma_f32_16x16x32_bf16 a[32:35], a[144:147], v[128:131], a[32:35]\n\t" \
  "v_max3_f32 v203, v203, v108, v109\n\t" \
  "v_max3_f32 v192, v192, v110, v111\n\t" \
  "v_mfma_f32_16x16x32_bf16 a[32:35], a[148:151], v[132:135], a[32:35]\n\t" \
  "v_max_f32 v192, v192, v203\n\t" \
  "ds_read_b128 a[196:199], v221 offset:16384\n\t" \
  "v_mfma_f32_16x16x32_bf16 a[48:51], a[152:155], v[128:131], a[48:51]\n\t" \
  "v_cmp_lt_f32 vcc, 0x42800000, v192\n\t" \
  "ds_read_b128 a[200:203], v222 offset:16384\n\t" \
  "v_mfma_f32_16x16x32_bf16 a[48:51], a[156:159], v[132:135], a[48:51]\n\t" \
  "s_cbranch_vccnz .Lsf_c63\n\t" \
  ".Lsf_r63:\n\t" \
  "ds_read_b128 a[204:207], v223 offset:16384\n\t" \
  "v_mfma_f32_16x16x32_bf16 a[64:67], a[160:163], v[128:131], a[64:67]\n\t" \
  "s_xor_b32 %[eso], %[eso], s96\n\t" \
  "v_mfma_f32_16x16x32_bf16 a[64:67], a[164:167], v[132:135], a[64:67]\n\t" \
  "s_xor_b32 s96, %[px], %[nb]\n\t" \
  "s_and_b32 s96, s96, %[na]\n\t" \
  "v_mfma_f32_16x16x32_bf16 a[80:83], a[168:171], v[128:131], a[80:83]\n\t" \
  "s_xor_b32 %[xcb], %[xcb], s96\n\t" \
  "s_xor_b32 %[frb], %[frb], %[nb]\n\t" \
  "v_mfma_f32_16x16x32_bf16 a[80:83], a[172:175], v[132:135], a[80:83]\n\t" \
  "s_xor_b32 %[fw2], %[fw2], %[nb]\n\t" \
  "ds_read_b128 a[208:211], v220 offset:20480\n\t" \
  "v_mfma_f32_16x16x32_bf16 a[96:99], a[176:179], v[128:131], a[96:99]\n\t" \
  "v_exp_f32 v0, v0\n\t" \
  "v_mfma_f32_16x16x32_bf16 a[96:99], a[180:183], v[132:135], a[96:99]\n\t" \
  "v_exp_f32 v1, v1\n\t" \
  "v_mfma_f32_16x16x32_bf16 a[112:115], a[184:187], v[128:131], a[112:115]\n\t" \
  "v_exp_f32 v2, v2\n\t" \
  "v_mfma_f32_16x16x32_bf16 a[112:115], a[188:191], v[132:135], a[112:115]\n\t" \
  "v_exp_f32 v3, v3\n\t" \
  "v_mfma_f32_16x16x32_bf16 v[160:163], v[216:219], v[128:131], v[160:163]\n\t" \
  "v_exp_f32 v4, v4\n\t" \
  "v_mfma_f32_16x16x32_bf16 v[160:163], v[216:219], v[132:135], v[160:163]\n\t" \
  "v_cvt_pk_bf16_f32 v128, v0, v1\n\t" \
  "v_cvt_pk_bf16_f32 v129, v2, v3\n\t" \
  "v_mfma_f32_16x16x32_bf16 a[4:7], a[128:131], v[136:139], a[4:7]\n\t" \
  "v_exp_f32 v5, v5\n\t" \
  "v_mfma_f32_16x16x32_bf16 a[4:7], a[132:135], v[140:143], a[4:7]\n\t" \
  "v_cvt_pk_bf16_f32 v130, v4, v5\n\t" \
  "ds_read_b128 a[212:215], v221 offset:20480\n\t" \
  "v_mfma_f32_16x16x32_bf16 a[20:23], a[136:139], v[136:139], a[20:23]\n\t" \
  "v_exp_f32 v6, v6\n\t" \
  "v_mfma_f32_16x16x32_bf16 a[20:23], a[140:143], v[140:143], a[20:23]\n\t" \
  "v_exp_f32 v7, v7\n\t" \
  "v_mfma_f32_16x16x32_bf16 a[36:39], a[144:147], v[136:139], a[36:39]\n\t" \
  "v_cvt_pk_bf16_f32 v131, v6, v7\n\t" \
  "s_add_u32 m0, %[m], 147456\n\t" \
  "v_mfma_f32_16x16x32_bf16 a[36:39], a[148:151], v[140:143], a[36:39]\n\t" \
  "v_exp_f32 v8, v8\n\t" \
  "v_mfma_f32_16x16x32_bf16 a[52:55], a[152:155], v[136:139], a[52:55]\n\t" \
  "ds_read_b128 a[216:219], v222 offset:20480\n\t" \
  "s_add_u32 s96, %[sofk], 114688\n\t" \
  "v_mfma_f32_16x16x32_bf16 a[52:55], a[156:159], v[140:143], a[52:55]\n\t" \
  "v_exp_f32 v9, v9\n\t" \
  "v_mfma_f32_16x16x32_bf16 a[68:71], a[160:163], v[136:139], a[68:71]\n\t" \
  "v_cvt_pk_bf16_f32 v132, v8, v9\n\t" \
  "ds_read_b128 a[220:223], v223 offset:20480\n\t" \
  "v_mfma_f32_16x16x32_bf16 a[68:71], a[164:167], v[140:143], a[68:71]\n\t" \
  "v_exp_f32 v10, v10\n\t" \
  "v_mfma_f32_16x16x32_bf16 a[84:87], a[168:171], v[136:139], a[84:87]\n\t" \
  "v_exp_f32 v11, v11\n\t" \
  "v_mfma_f32_16x16x32_bf16 a[84:87], a[172:175], v[140:143], a[84:87]\n\t" \
  "v_cvt_pk_bf16_f32 v133, v10, v11\n\t" \
  "buffer_load_dwordx4 v230, %[sV], s96 offen offset:0 lds\n\t" \
  "v_mfma_f32_16x16x32_bf16 a[100:103], a[176:179], v[136:139], a[100:103]\n\t" \
  "v_exp_f32 v12, v12\n\t" \
  "v_mfma_f32_16x16x32_bf16 a[100:103], a[180:183], v[140:143], a[100:103]\n\t" \
  "ds_read_b128 a[224:227], v220 offset:24576\n\t" \
  "buffer_load_dwordx4 v231, %[sV], s96 offen offset:1024 lds\n\t" \
  "v_mfma_f32_16x16x32_bf16 a[116:119], a[184:187], v[136:139], a[116:119]\n\t" \
  "v_exp_f32 v13, v13\n\t" \
  "v_mfma_f32_16x16x32_bf16 a[116:119], a[188:191], v[140:143], a[116:119]\n\t" \
  "v_cvt_pk_bf16_f32 v134, v12, v13\n\t" \
  "buffer_load_dwordx4 v230, %[sV], s96 offen offset:2048 lds\n\t" \
  "v_mfma_f32_16x16x32_bf16 v[164:167], v[216:219], v[136:139], v[164:167]\n\t" \
  "v_exp_f32 v14, v14\n\t" \
  "v_mfma_f32_16x16x32_bf16 v[164:167], v[216:219], v[140:143], v[164:167]\n\t" \
  "v_exp_f32 v15, v15\n\t" \
  "v_mfma_f32_16x16x32_bf16 a[8:11], a[128:131], v[144:147], a[8:11]\n\t" \
  "v_cvt_pk_bf16_f32 v135, v14, v15\n\t" \
  "ds_read_b128 a[228:231], v221 offset:24576\n\t" \
  "v_mfma_f32_16x16x32_bf16 a[8:11], a[132:135], v[148:151], a[8:11]\n\t" \
  "v_exp_f32 v32, v32\n\t" \
  "v_mfma_f32_16x16x32_bf16 a[24:27], a[136:139], v[144:147], a[24:27]\n\t" \
  "buffer_load_dwordx4 v231, %[sV], s96 offen offset:3072 lds\n\t" \
  "ds_read_b128 a[232:235], v222 offset:24576\n\t" \
  "v_mfma_f32_16x16x32_bf16 a[24:27], a[140:143], v[148:151], a[24:27]\n\t" \
  "v_exp_f32 v33, v33\n\t" \
  "v_mfma_f32_16x16x32_bf16 a[40:43], a[144:147], v[144:147], a[40:43]\n\t" \
  "v_cvt_pk_bf16_f32 v136, v32, v33\n\t" \
  "s_add_u32 m0, %[m], 49152\n\t" \
  "v_mfma_f32_16x16x32_bf16 a[40:43], a[148:151], v[148:151], a[40:43]\n\t" \
  "v_exp_f32 v34, v34\n\t" \
  "v_mfma_f32_16x16x32_bf16 a[56:59], a[152:155], v[144:147], a[56:59]\n\t" \
  "v_exp_f32 v35, v35\n\t" \
  "v_mfma_f32_16x16x32_bf16 a[56:59], a[156:159], v[148:151], a[56:59]\n\t" \
  "v_cvt_pk_bf16_f32 v137, v34, v35\n\t" \
  "ds_read_b128 a[236:239], v223 offset:24576\n\t" \
  "v_mfma_f32_16x16x32_bf16 a[72:75], a[160:163], v[144:147], a[72:75]\n\t" \
  "v_exp_f32 v36, v36\n\t" \
  "v_mfma_f32_16x16x32_bf16 a[72:75], a[164:167], v[148:151], a[72:75]\n\t" \
  "s_xor_b32 s97, %[wob], 262144\n\t" \
  "ds_read_b128 a[240:243], v220 offset:28672\n\t" \
  "v_mfma_f32_16x16x32_bf16 a[88:91], a[168:171], v[144:147], a[88:91]\n\t" \
  "v_exp_f32 v37, v37\n\t" \
  "v_mfma_f32_16x16x32_bf16 a[88:91], a[172:175], v[148:151], a[88:91]\n\t" \
  "v_cvt_pk_bf16_f32 v138, v36, v37\n\t" \
  "buffer_load_dwordx4 v240, %[wo], s97 offen offset:0 lds\n\t" \
  "v_mfma_f32_16x16x32_bf16 a[104:107], a[176:179], v[144:147], a[104:107]\n\t" \
  "v_exp_f32 v38, v38\n\t" \
  "v_mfma_f32_16x16x32_bf16 a[104:107], a[180:183], v[148:151], a[104:107]\n\t" \
  "v_exp_f32 v39, v39\n\t" \
  "v_mfma_f32_16x16x32_bf16 a[120:123], a[184:187], v[144:147], a[120:123]\n\t" \
  "v_cvt_pk_bf16_f32 v139, v38, v39\n\t" \
  "ds_read_b128 a[244:247], v221 offset:28672\n\t" \
  "v_mfma_f32_16x16x32_bf16 a[120:123], a[188:191], v[148:151], a[120:123]\n\t" \
  "v_exp_f32 v40, v40\n\t" \
  "v_mfma_f32_16x16x32_bf16 v[168:171], v[216:219], v[144:147], v[168:171]\n\t" \
  "v_exp_f32 v41, v41\n\t" \
  "v_mfma_f32_16x16x32_bf16 v[168:171], v[216:219], v[148:151], v[168:171]\n\t" \
  "v_cvt_pk_bf16_f32 v140, v40, v41\n\t" \
  "ds_read_b128 a[248:251], v222 offset:28672\n\t" \
  "v_mfma_f32_16x16x32_bf16 a[12:15], a[128:131], v[152:155], a[12:15]\n\t" \
  "s_add_u32 s97, s97, 7168\n\t" \
  "buffer_load_dwordx4 v241, %[wo], s97 offen offset:1024 lds\n\t" \
  "v_mfma_f32_16x16x32_bf16 a[12:15], a[132:135], v[156:159], a[12:15]\n\t" \
  "v_exp_f32 v42, v42\n\t" \
  "v_mfma_f32_16x16x32_bf16 a[28:31], a[136:139], v[152:155], a[28:31]\n\t" \
  "v_exp_f32 v43, v43\n\t" \
  "v_mfma_f32_16x16x32_bf16 a[28:31], a[140:143], v[156:159], a[28:31]\n\t" \
  "v_cvt_pk_bf16_f32 v141, v42, v43\n\t" \
  "ds_read_b128 a[252:255], v223 offset:28672\n\t" \
  "v_mfma_f32_16x16x32_bf16 a[44:47], a[144:147], v[152:155], a[44:47]\n\t" \
  "s_add_u32 s97, s97, 7168\n\t" \
  "buffer_load_dwordx4 v240, %[wo], s97 offen offset:2048 lds\n\t" \
  "v_mfma_f32_16x16x32_bf16 a[44:47], a[148:151], v[156:159], a[44:47]\n\t" \
  "v_exp_f32 v44, v44\n\t" \
  "v_mfma_f32_16x16x32_bf16 a[60:63], a[152:155], v[152:155], a[60:63]\n\t" \
  "s_add_u32 s97, s97, 7168\n\t" \
  "buffer_load_dwordx4 v241, %[wo], s97 offen offset:3072 lds\n\t" \
  "v_mfma_f32_16x16x32_bf16 a[60:63], a[156:159], v[156:159], a[60:63]\n\t" \
  "v_exp_f32 v45, v45\n\t" \
  "v_mfma_f32_16x16x32_bf16 a[76:79], a[160:163], v[152:155], a[76:79]\n\t" \
  "v_cvt_pk_bf16_f32 v142, v44, v45\n\t" \
  "v_mfma_f32_16x16x32_bf16 a[76:79], a[164:167], v[156:159], a[76:79]\n\t" \
  "v_exp_f32 v46, v46\n\t" \
  "v_mfma_f32_16x16x32_bf16 a[92:95], a[168:171], v[152:155], a[92:95]\n\t" \
  "s_add_u32 m0, %[m], 98304\n\t" \
  "s_mov_b32 s96, 40960\n\t" \
  "buffer_load_dwordx4 v232, %[sG], s96 offen offset:2048 lds\n\t" \
  "v_mfma_f32_16x16x32_bf16 a[92:95], a[172:175], v[156:159], a[92:95]\n\t" \
  "v_exp_f32 v47, v47\n\t" \
  "v_mfma_f32_16x16x32_bf16 a[108:111], a[176:179], v[152:155], a[108:111]\n\t" \
  "v_cvt_pk_bf16_f32 v143, v46, v47\n\t" \
  "v_mfma_f32_16x16x32_bf16 a[108:111], a[180:183], v[156:159], a[108:111]\n\t" \
  "s_mov_b32 s96, 47104\n\t" \
  "buffer_load_dwordx4 v232, %[sG], s96 offen offset:3072 lds\n\t" \
  "v_mfma_f32_16x16x32_bf16 a[124:127], a[184:187], v[152:155], a[124:127]\n\t" \
  "v_exp_f32 v64, v64\n\t" \
  "v_mfma_f32_16x16x32_bf16 a[124:127], a[188:191], v[156:159], a[124:127]\n\t" \
  "v_exp_f32 v65, v65\n\t" \
  "v_mfma_f32_16x16x32_bf16 v[172:175], v[216:219], v[152:155], v[172:175]\n\t" \
  "v_cvt_pk_bf16_f32 v144, v64, v65\n\t" \
  "v_mfma_f32_16x16x32_bf16 v[172:175], v[216:219], v[156:159], v[172:175]\n\t" \
  "v_exp_f32 v66, v66\n\t" \
  "s_cmp_lg_u32 s99, 0\n\t" \
  "s_cbranch_scc1 .Lsf_p6\n\t" \
  ".Lsf_q6:\n\t" \
  "s_waitcnt lgkmcnt(0)\n\t" \
  "s_barrier\n\t" \
  "v_mfma_f32_16x16x32_bf16 v[0:3], a[192:195], v[16:19], v[176:179]\n\t" \
  "v_exp_f32 v67, v67\n\t" \
  "v_mfma_f32_16x16x32_bf16 v[0:3], a[196:199], v[20:23], v[0:3]\n\t" \
  "v_cvt_pk_bf16_f32 v145, v66, v67\n\t" \
  "ds_read_b64_tr_b16 a[128:129], v228 offset:0\n\t" \
  "v_mfma_f32_16x16x32_bf16 v[0:3], a[200:203], v[24:27], v[0:3]\n\t" \
  "v_exp_f32 v68, v68\n\t" \
  "v_mfma_f32_16x16x32_bf16 v[0:3], a[204:207], v[28:31], v[0:3]\n\t" \
  "v_exp_f32 v69, v69\n\t" \
  "v_mfma_f32_16x16x32_bf16 v[4:7], a[220:223], v[28:31], v[176:179]\n\t" \
  "v_cvt_pk_bf16_f32 v146, v68, v69\n\t" \
  "ds_read_b64_tr_b16 a[130:131], v228 offset:4096\n\t" \
  "v_mfma_f32_16x16x32_bf16 v[4:7], a[216:219], v[24:27], v[4:7]\n\t" \
  "ds_read_b64_tr_b16 a[132:133], v228 offset:8192\n\t" \
  "s_add_u32 m0, %[m], 65536\n\t" \
  "v_mfma_f32_16x16x32_bf16 v[4:7], a[212:215], v[20:23], v[4:7]\n\t" \
  "v_max3_f32 v192, v0, v1, v2\n\t" \
  "ds_read_b64_tr_b16 a[134:135], v228 offset:12288\n\t" \
  "v_mfma_f32_16x16x32_bf16 v[4:7], a[208:211], v[16:19], v[4:7]\n\t" \
  "v_exp_f32 v70, v70\n\t" \
  "v_mfma_f32_16x16x32_bf16 v[8:11], a[224:227], v[16:19], v[176:179]\n\t" \
  "ds_read_b64_tr_b16 a[136:137], v229 offset:0\n\t" \
  "s_xor_b32 s97, %[wob], 393216\n\t" \
  "v_mfma_f32_16x16x32_bf16 v[8:11], a[228:231], v[20:23], v[8:11]\n\t" \
  "v_exp_f32 v71, v71\n\t" \
  "v_mfma_f32_16x16x32_bf16 v[8:11], a[232:235], v[24:27], v[8:11]\n\t" \
  "v_cvt_pk_bf16_f32 v147, v70, v71\n\t" \
  "v_max3_f32 v203, v3, v4, v5\n\t" \
  "v_mfma_f32_16x16x32_bf16 v[8:11], a[236:239], v[28:31], v[8:11]\n\t" \
  "v_max3_f32 v192, v192, v6, v7\n\t" \
  "ds_read_b64_tr_b16 a[138:139], v229 offset:4096\n\t" \
  "v_mfma_f32_16x16x32_bf16 v[12:15], a[252:255], v[28:31], v[176:179]\n\t" \
  "ds_read_b64_tr_b16 a[140:141], v229 offset:8192\n\t" \
  "buffer_load_dwordx4 v240, %[wo], s97 offen offset:0 lds\n\t" \
  "v_mfma_f32_16x16x32_bf16 v[12:15], a[248:251], v[24:27], v[12:15]\n\t" \
  "v_exp_f32 v72, v72\n\t" \
  "v_mfma_f32_16x16x32_bf16 v[12:15], a[244:247], v[20:23], v[12:15]\n\t" \
  "v_max3_f32 v203, v203, v8, v9\n\t" \
  "v_max3_f32 v192, v192, v10, v11\n\t" \
  "v_mfma_f32_16x16x32_bf16 v[12:15], a[240:243], v[16:19], v[12:15]\n\t" \
  "ds_read_b64_tr_b16 a[142:143], v229 offset:12288\n\t" \
  "ds_read_b64_tr_b16 a[144:145], v228 offset:256\n\t" \
  "v_mfma_f32_16x16x32_bf16 v[32:35], a[192:195], v[48:51], v[180:183]\n\t" \
  "ds_read_b64_tr_b16 a[146:147], v228 offset:4352\n\t" \
  "ds_read_b64_tr_b16 a[148:149], v228 offset:8448\n\t" \
  "v_mfma_f32_16x16x32_bf16 v[32:35], a[196:199], v[52:55], v[32:35]\n\t" \
  "v_exp_f32 v73, v73\n\t" \
  "v_mfma_f32_16x16x32_bf16 v[32:35], a[200:203], v[56:59], v[32:35]\n\t" \
  "v_cvt_pk_bf16_f32 v148, v72, v73\n\t" \
  "v_max3_f32 v203, v203, v12, v13\n\t" \
  "v_mfma_f32_16x16x32_bf16 v[32:35], a[204:207], v[60:63], v[32:35]\n\t" \
  "v_max3_f32 v192, v192, v14, v15\n\t" \
  "v_max_f32 v192, v192, v203\n\t" \
  "v_mfma_f32_16x16x32_bf16 v[36:39], a[220:223], v[60:63], v[180:183]\n\t" \
  "v_cmp_lt_f32 vcc, 0x42800000, v192\n\t" \
  "ds_read_b64_tr_b16 a[150:151], v228 offset:12544\n\t" \
  "v_mfma_f32_16x16x32_bf16 v[36:39], a[216:219], v[56:59], v[36:39]\n\t" \
  "s_cbranch_vccnz .Lsf_c70\n\t" \
  ".Lsf_r70:\n\t" \
  "ds_read_b64_tr_b16 a[152:153], v229 offset:256\n\t" \
  "v_mfma_f32_16x16x32_bf16 v[36:39], a[212:215], v[52:55], v[36:39]\n\t" \
  "s_add_u32 s97, s97, 7168\n\t" \
  "buffer_load_dwordx4 v241, %[wo], s97 offen offset:1024 lds\n\t" \
  "v_mfma_f32_16x16x32_bf16 v[36:39], a[208:211], v[48:51], v[36:39]\n\t" \
  "v_max3_f32 v192, v32, v33, v34\n\t" \
  "ds_read_b64_tr_b16 a[154:155], v229 offset:4352\n\t" \
  "v_mfma_f32_16x16x32_bf16 v[40:43], a[224:227], v[48:51], v[180:183]\n\t" \
  "s_add_u32 s97, s97, 7168\n\t" \
  "buffer_load_dwordx4 v240, %[wo], s97 offen offset:2048 lds\n\t" \
  "v_mfma_f32_16x16x32_bf16 v[40:43], a[228:231], v[52:55], v[40:43]\n\t" \
  "s_add_u32 s97, s97, 7168\n\t" \
  "buffer_load_dwordx4 v241, %[wo], s97 offen offset:3072 lds\n\t" \
  "v_mfma_f32_16x16x32_bf16 v[40:43], a[232:235], v[56:59], v[40:43]\n\t" \
  "v_max3_f32 v203, v35, v36, v37\n\t" \
  "v_max3_f32 v192, v192, v38, v39\n\t" \
  "v_mfma_f32_16x16x32_bf16 v[40:43], a[236:239], v[60:63], v[40:43]\n\t" \
  "ds_read_b64_tr_b16 a[156:157], v229 offset:8448\n\t" \
  "ds_read_b64_tr_b16 a[158:159], v229 offset:12544\n\t" \
  "v_mfma_f32_16x16x32_bf16 v[44:47], a[252:255], v[60:63], v[180:183]\n\t" \
  "v_exp_f32 v74, v74\n\t" \
  "v_mfma_f32_16x16x32_bf16 v[44:47], a[248:251], v[56:59], v[44:47]\n\t" \
  "v_exp_f32 v75, v75\n\t" \
  "v_mfma_f32_16x16x32_bf16 v[44:47], a[244:247], v[52:55], v[44:47]\n\t" \
  "v_cvt_pk_bf16_f32 v149, v74, v75\n\t" \
  "ds_read_b64_tr_b16 a[160:161], v228 offset:512\n\t" \
  "v_mfma_f32_16x16x32_bf16 v[44:47], a[240:243], v[48:51], v[44:47]\n\t" \
  "v_max3_f32 v203, v203, v40, v41\n\t" \
  "v_max3_f32 v192, v192, v42, v43\n\t" \
  "v_mfma_f32_16x16x32_bf16 v[64:67], a[192:195], v[80:83], v[184:187]\n\t" \
  "ds_read_b64_tr_b16 a[162:163], v228 offset:4608\n\t" \
  "ds_read_b64_tr_b16 a[164:165], v228 offset:8704\n\t" \
  "v_mfma_f32_16x16x32_bf16 v[64:67], a[196:199], v[84:87], v[64:67]\n\t" \
  "v_exp_f32 v76, v76\n\t" \
  "v_mfma_f32_16x16x32_bf16 v[64:67], a[200:203], v[88:91], v[64:67]\n\t" \
  "v_max3_f32 v203, v203, v44, v45\n\t" \
  "v_max3_f32 v192, v192, v46, v47\n\t" \
  "v_mfma_f32_16x16x32_bf16 v[64:67], a[204:207], v[92:95], v[64:67]\n\t" \
  "v_max_f32 v192, v192, v203\n\t" \
  "ds_read_b64_tr_b16 a[166:167], v228 offset:12800\n\t" \
  "v_mfma_f32_16x16x32_bf16 v[68:71], a[220:223], v[92:95], v[184:187]\n\t" \
  "v_cmp_lt_f32 vcc, 0x42800000, v192\n\t" \
  "ds_read_b64_tr_b16 a[168:169], v229 offset:512\n\t" \
  "v_mfma_f32_16x16x32_bf16 v[68:71], a[216:219], v[88:91], v[68:71]\n\t" \
  "s_cbranch_vccnz .Lsf_c71\n\t" \
  ".Lsf_r71:\n\t" \
  "ds_read_b64_tr_b16 a[170:171], v229 offset:4608\n\t" \
  "v_mfma_f32_16x16x32_bf16 v[68:71], a[212:215], v[84:87], v[68:71]\n\t" \
  "v_max3_f32 v192, v64, v65, v66\n\t" \
  "ds_read_b64_tr_b16 a[172:173], v229 offset:8704\n\t" \
  "v_mfma_f32_16x16x32_bf16 v[68:71], a[208:211], v[80:83], v[68:71]\n\t" \
  "v_exp_f32 v77, v77\n\t" \
  "v_mfma_f32_16x16x32_bf16 v[72:75], a[224:227], v[80:83], v[184:187]\n\t" \
  "v_cvt_pk_bf16_f32 v150, v76, v77\n\t" \
  "ds_read_b64_tr_b16 a[174:175], v229 offset:12800\n\t" \
  "v_mfma_f32_16x16x32_bf16 v[72:75], a[228:231], v[84:87], v[72:75]\n\t" \
  "v_exp_f32 v78, v78\n\t" \
  "v_mfma_f32_16x16x32_bf16 v[72:75], a[232:235], v[88:91], v[72:75]\n\t" \
  "v_exp_f32 v79, v79\n\t" \
  "v_mfma_f32_16x16x32_bf16 v[72:75], a[236:239], v[92:95], v[72:75]\n\t" \
  "v_cvt_pk_bf16_f32 v151, v78, v79\n\t" \
  "v_max3_f32 v203, v67, v68, v69\n\t" \
  "v_mfma_f32_16x16x32_bf16 v[76:79], a[252:255], v[92:95], v[184:187]\n\t" \
  "v_max3_f32 v192, v192, v70, v71\n\t" \
  "ds_read_b64_tr_b16 a[176:177], v228 offset:768\n\t" \
  "v_mfma_f32_16x16x32_bf16 v[76:79], a[248:251], v[88:91], v[76:79]\n\t" \
  "v_exp_f32 v96, v96\n\t" \
  "v_mfma_f32_16x16x32_bf16 v[76:79], a[244:247], v[84:87], v[76:79]\n\t" \
  "ds_read_b64_tr_b16 a[178:179], v228 offset:4864\n\t" \
  "v_max3_f32 v203, v203, v72, v73\n\t" \
  "v_mfma_f32_16x16x32_bf16 v[76:79], a[240:243], v[80:83], v[76:79]\n\t" \
  "v_max3_f32 v192, v192, v74, v75\n\t" \
  "ds_read_b64_tr_b16 a[180:181], v228 offset:8960\n\t" \
  "v_exp_f32 v97, v97\n\t" \
  "v_exp_f32 v98, v98\n\t" \
  "v_exp_f32 v99, v99\n\t" \
  "v_cvt_pk_bf16_f32 v152, v96, v97\n\t" \
  "v_cvt_pk_bf16_f32 v153, v98, v99\n\t" \
  "v_mfma_f32_16x16x32_bf16 v[96:99], a[192:195], v[112:115], v[188:191]\n\t" \
  "ds_read_b64_tr_b16 a[182:183], v228 offset:13056\n\t" \
  "v_max3_f32 v203, v203, v76, v77\n\t" \
  "v_mfma_f32_16x16x32_bf16 v[96:99], a[196:199], v[116:119], v[96:99]\n\t" \
  "v_max3_f32 v192, v192, v78, v79\n\t" \
  "v_max_f32 v192, v192, v203\n\t" \
  "v_mfma_f32_16x16x32_bf16 v[96:99], a[200:203], v[120:123], v[96:99]\n\t" \
  "v_cmp_lt_f32 vcc, 0x42800000, v192\n\t" \
  "ds_read_b64_tr_b16 a[184:185], v229 offset:768\n\t" \
  "v_mfma_f32_16x16x32_bf16 v[96:99], a[204:207], v[124:127], v[96:99]\n\t" \
  "s_cbranch_vccnz .Lsf_c72\n\t" \
  ".Lsf_r72:\n\t" \
  "ds_read_b64_tr_b16 a[186:187], v229 offset:4864\n\t" \
  "v_exp_f32 v100, v100\n\t" \
  "v_exp_f32 v101, v101\n\t" \
  "v_exp_f32 v102, v102\n\t" \
  "v_exp_f32 v103, v103\n\t" \
  "v_cvt_pk_bf16_f32 v154, v100, v101\n\t" \
  "v_cvt_pk_bf16_f32 v155, v102, v103\n\t" \
  "v_mfma_f32_16x16x32_bf16 v[100:103], a[220:223], v[124:127], v[188:191]\n\t" \
  "v_exp_f32 v104, v104\n\t" \
  "v_mfma_f32_16x16x32_bf16 v[100:103], a[216:219], v[120:123], v[100:103]\n\t" \
  "v_max3_f32 v192, v96, v97, v98\n\t" \
  "ds_read_b64_tr_b16 a[188:189], v229 offset:8960\n\t" \
  "v_mfma_f32_16x16x32_bf16 v[100:103], a[212:215], v[116:119], v[100:103]\n\t" \
  "v_exp_f32 v105, v105\n\t" \
  "v_mfma_f32_16x16x32_bf16 v[100:103], a[208:211], v[112:115], v[100:103]\n\t" \
  "v_cvt_pk_bf16_f32 v156, v104, v105\n\t" \
  "ds_read_b64_tr_b16 a[190:191], v229 offset:13056\n\t" \
  "v_exp_f32 v106, v106\n\t" \
  "v_exp_f32 v107, v107\n\t" \
  "s_nop 0\n\t" \
  "v_cvt_pk_bf16_f32 v157, v106, v107\n\t" \
  "v_mfma_f32_16x16x32_bf16 v[104:107], a[224:227], v[112:115], v[188:191]\n\t" \
  "v_exp_f32 v108, v108\n\t" \
  "v_mfma_f32_16x16x32_bf16 v[104:107], a[228:231], v[116:119], v[104:107]\n\t" \
  "v_exp_f32 v109, v109\n\t" \
  "v_mfma_f32_16x16x32_bf16 v[104:107], a[232:235], v[120:123], v[104:107]\n\t" \
  "v_cvt_pk_bf16_f32 v158, v108, v109\n\t" \
  "v_max3_f32 v203, v99, v100, v101\n\t" \
  "v_mfma_f32_16x16x32_bf16 v[104:107], a[236:239], v[124:127], v[104:107]\n\t" \
  "v_max3_f32 v192, v192, v102, v103\n\t" \
  "v_exp_f32 v110, v110\n\t" \
  "v_exp_f32 v111, v111\n\t" \
  "s_nop 0\n\t" \
  "v_cvt_pk_bf16_f32 v159, v110, v111\n\t" \
  "v_mfma_f32_16x16x32_bf16 v[108:111], a[252:255], v[124:127], v[188:191]\n\t" \
  "v_exp_f32 v0, v0\n\t" \
  "v_mfma_f32_16x16x32_bf16 v[108:111], a[248:251], v[120:123], v[108:111]\n\t" \
  "v_exp_f32 v1, v1\n\t" \
  "v_mfma_f32_16x16x32_bf16 v[108:111], a[244:247], v[116:119], v[108:111]\n\t" \
  "v_max3_f32 v203, v203, v104, v105\n\t" \
  "v_max3_f32 v192, v192, v106, v107\n\t" \
  "v_mfma_f32_16x16x32_bf16 v[108:111], a[240:243], v[112:115], v[108:111]\n\t" \
  "v_exp_f32 v2, v2\n\t" \
  "s_waitcnt vmcnt(6) lgkmcnt(0)\n\t" \
  "s_barrier\n\t" \
  "v_mfma_f32_16x16x32_bf16 a[0:3], a[128:131], v[128:131], a[0:3]\n\t" \
  "ds_read_b64_tr_b16 a[192:193], v237 offset:0\n\t" \
  "s_add_u32 m0, %[m], 81920\n\t" \
  "v_mfma_f32_16x16x32_bf16 a[0:3], a[132:135], v[132:135], a[0:3]\n\t" \
  "v_exp_f32 v3, v3\n\t" \
  "v_mfma_f32_16x16x32_bf16 a[16:19], a[136:139], v[128:131], a[16:19]\n\t" \
  "v_max3_f32 v203, v203, v108, v109\n\t" \
  "v_max3_f32 v192, v192, v110, v111\n\t" \
  "v_mfma_f32_16x16x32_bf16 a[16:19], a[140:143], v[132:135], a[16:19]\n\t" \
  "v_max_f32 v192, v192, v203\n\t" \
  "ds_read_b64_tr_b16 a[194:195], v237 offset:4096\n\t" \
  "v_mfma_f32_16x16x32_bf16 a[32:35], a[144:147], v[128:131], a[32:35]\n\t" \
  "v_cmp_lt_f32 vcc, 0x42800000, v192\n\t" \
  "ds_read_b64_tr_b16 a[196:197], v237 offset:8192\n\t" \
  "v_mfma_f32_16x16x32_bf16 a[32:35], a[148:151], v[132:135], a[32:35]\n\t" \
  "s_cbranch_vccnz .Lsf_c73\n\t" \
  ".Lsf_r73:\n\t" \
  "s_xor_b32 s97, %[wob], 524288\n\t" \
  "v_mfma_f32_16x16x32_bf16 a[48:51], a[152:155], v[128:131], a[48:51]\n\t" \
  "ds_read_b64_tr_b16 a[198:199], v237 offset:12288\n\t" \
  "ds_read_b64_tr_b16 a[200:201], v238 offset:0\n\t" \
  "v_mfma_f32_16x16x32_bf16 a[48:51], a[156:159], v[132:135], a[48:51]\n\t" \
  "buffer_load_dwordx4 v240, %[wo], s97 offen offset:0 lds\n\t" \
  "ds_read_b64_tr_b16 a[202:203], v238 offset:4096\n\t" \
  "v_mfma_f32_16x16x32_bf16 a[64:67], a[160:163], v[128:131], a[64:67]\n\t" \
  "ds_read_b64_tr_b16 a[204:205], v238 offset:8192\n\t" \
  "ds_read_b64_tr_b16 a[206:207], v238 offset:12288\n\t" \
  "v_mfma_f32_16x16x32_bf16 a[64:67], a[164:167], v[132:135], a[64:67]\n\t" \
  "v_exp_f32 v4, v4\n\t" \
  "v_mfma_f32_16x16x32_bf16 a[80:83], a[168:171], v[128:131], a[80:83]\n\t" \
  "s_add_u32 s97, s97, 7168\n\t" \
  "buffer_load_dwordx4 v241, %[wo], s97 offen offset:1024 lds\n\t" \
  "v_mfma_f32_16x16x32_bf16 a[80:83], a[172:175], v[132:135], a[80:83]\n\t" \
  "ds_read_b64_tr_b16 a[208:209], v237 offset:256\n\t" \
  "ds_read_b64_tr_b16 a[210:211], v237 offset:4352\n\t" \
  "v_mfma_f32_16x16x32_bf16 a[96:99], a[176:179], v[128:131], a[96:99]\n\t" \
  "v_exp_f32 v5, v5\n\t" \
  "v_mfma_f32_16x16x32_bf16 a[96:99], a[180:183], v[132:135], a[96:99]\n\t" \
  "s_add_u32 s97, s97, 7168\n\t" \
  "buffer_load_dwordx4 v240, %[wo], s97 offen offset:2048 lds\n\t" \
  "v_mfma_f32_16x16x32_bf16 a[112:115], a[184:187], v[128:131], a[112:115]\n\t" \
  "ds_read_b64_tr_b16 a[212:213], v237 offset:8448\n\t" \
  "ds_read_b64_tr_b16 a[214:215], v237 offset:12544\n\t" \
  "v_mfma_f32_16x16x32_bf16 a[112:115], a[188:191], v[132:135], a[112:115]\n\t" \
  "s_add_u32 s97, s97, 7168\n\t" \
  "buffer_load_dwordx4 v241, %[wo], s97 offen offset:3072 lds\n\t" \
  "v_mfma_f32_16x16x32_bf16 v[160:163], v[216:219], v[128:131], v[160:163]\n\t" \
  "v_exp_f32 v6, v6\n\t" \
  "v_mfma_f32_16x16x32_bf16 v[160:163], v[216:219], v[132:135], v[160:163]\n\t" \
  "v_cvt_pk_bf16_f32 v128, v0, v1\n\t" \
  "v_cvt_pk_bf16_f32 v129, v2, v3\n\t" \
  "v_mfma_f32_16x16x32_bf16 a[4:7], a[128:131], v[136:139], a[4:7]\n\t" \
  "v_cvt_pk_bf16_f32 v130, v4, v5\n\t" \
  "ds_read_b64_tr_b16 a[216:217], v238 offset:256\n\t" \
  "v_mfma_f32_16x16x32_bf16 a[4:7], a[132:135], v[140:143], a[4:7]\n\t" \
  "ds_read_b64_tr_b16 a[218:219], v238 offset:4352\n\t" \
  "ds_read_b64_tr_b16 a[220:221], v238 offset:8448\n\t" \
  "v_mfma_f32_16x16x32_bf16 a[20:23], a[136:139], v[136:139], a[20:23]\n\t" \
  "ds_read_b64_tr_b16 a[222:223], v238 offset:12544\n\t" \
  "ds_read_b64_tr_b16 a[224:225], v237 offset:512\n\t" \
  "v_mfma_f32_16x16x32_bf16 a[20:23], a[140:143], v[140:143], a[20:23]\n\t" \
  "v_exp_f32 v7, v7\n\t" \
  "v_mfma_f32_16x16x32_bf16 a[36:39], a[144:147], v[136:139], a[36:39]\n\t" \
  "v_cvt_pk_bf16_f32 v131, v6, v7\n\t" \
  "s_add_u32 m0, %[m], 114688\n\t" \
  "v_mfma_f32_16x16x32_bf16 a[36:39], a[148:151], v[140:143], a[36:39]\n\t" \
  "ds_read_b64_tr_b16 a[226:227], v237 offset:4608\n\t" \
  "ds_read_b64_tr_b16 a[228:229], v237 offset:8704\n\t" \
  "v_mfma_f32_16x16x32_bf16 a[52:55], a[152:155], v[136:139], a[52:55]\n\t" \
  "s_xor_b32 s97, %[wob], 786432\n\t" \
  "ds_read_b64_tr_b16 a[230:231], v237 offset:12800\n\t" \
  "v_mfma_f32_16x16x32_bf16 a[52:55], a[156:159], v[140:143], a[52:55]\n\t" \
  "v_exp_f32 v8, v8\n\t" \
  "v_mfma_f32_16x16x32_bf16 a[68:71], a[160:163], v[136:139], a[68:71]\n\t" \
  "v_exp_f32 v9, v9\n\t" \
  "v_mfma_f32_16x16x32_bf16 a[68:71], a[164:167], v[140:143], a[68:71]\n\t" \
  "v_cvt_pk_bf16_f32 v132, v8, v9\n\t" \
  "ds_read_b64_tr_b16 a[232:233], v238 offset:512\n\t" \
  "v_mfma_f32_16x16x32_bf16 a[84:87], a[168:171], v[136:139], a[84:87]\n\t" \
  "v_exp_f32 v10, v10\n\t" \
  "v_mfma_f32_16x16x32_bf16 a[84:87], a[172:175], v[140:143], a[84:87]\n\t" \
  "ds_read_b64_tr_b16 a[234:235], v238 offset:4608\n\t" \
  "ds_read_b64_tr_b16 a[236:237], v238 offset:8704\n\t" \
  "v_mfma_f32_16x16x32_bf16 a[100:103], a[176:179], v[136:139], a[100:103]\n\t" \
  "v_exp_f32 v11, v11\n\t" \
  "v_mfma_f32_16x16x32_bf16 a[100:103], a[180:183], v[140:143], a[100:103]\n\t" \
  "v_cvt_pk_bf16_f32 v133, v10, v11\n\t" \
  "ds_read_b64_tr_b16 a[238:239], v238 offset:12800\n\t" \
  "v_mfma_f32_16x16x32_bf16 a[116:119], a[184:187], v[136:139], a[116:119]\n\t" \
  "ds_read_b64_tr_b16 a[240:241], v237 offset:768\n\t" \
  "ds_read_b64_tr_b16 a[242:243], v237 offset:4864\n\t" \
  "v_mfma_f32_16x16x32_bf16 a[116:119], a[188:191], v[140:143], a[116:119]\n\t" \
  "v_exp_f32 v12, v12\n\t" \
  "v_mfma_f32_16x16x32_bf16 v[164:167], v[216:219], v[136:139], v[164:167]\n\t" \
  "v_exp_f32 v13, v13\n\t" \
  "v_mfma_f32_16x16x32_bf16 v[164:167], v[216:219], v[140:143], v[164:167]\n\t" \
  "v_cvt_pk_bf16_f32 v134, v12, v13\n\t" \
  "ds_read_b64_tr_b16 a[244:245], v237 offset:8960\n\t" \
  "v_mfma_f32_16x16x32_bf16 a[8:11], a[128:131], v[144:147], a[8:11]\n\t" \
  "ds_read_b64_tr_b16 a[246:247], v237 offset:13056\n\t" \
  "ds_read_b64_tr_b16 a[248:249], v238 offset:768\n\t" \
  "v_mfma_f32_16x16x32_bf16 a[8:11], a[132:135], v[148:151], a[8:11]\n\t" \
  "v_exp_f32 v14, v14\n\t" \
  "v_mfma_f32_16x16x32_bf16 a[24:27], a[136:139], v[144:147], a[24:27]\n\t" \
  "v_exp_f32 v15, v15\n\t" \
  "v_mfma_f32_16x16x32_bf16 a[24:27], a[140:143], v[148:151], a[24:27]\n\t" \
  "v_cvt_pk_bf16_f32 v135, v14, v15\n\t" \
  "ds_read_b64_tr_b16 a[250:251], v238 offset:4864\n\t" \
  "v_mfma_f32_16x16x32_bf16 a[40:43], a[144:147], v[144:147], a[40:43]\n\t" \
  "ds_read_b64_tr_b16 a[252:253], v238 offset:8960\n\t" \
  "ds_read_b64_tr_b16 a[254:255], v238 offset:13056\n\t" \
  "v_mfma_f32_16x16x32_bf16 a[40:43], a[148:151], v[148:151], a[40:43]\n\t" \
  "v_exp_f32 v32, v32\n\t" \
  "v_mfma_f32_16x16x32_bf16 a[56:59], a[152:155], v[144:147], a[56:59]\n\t" \
  "v_xor_b32 v235, 64, v234\n\t" \
  "ds_read_b64_tr_b16 v[48:49], v235 offset:0\n\t" \
  "v_mfma_f32_16x16x32_bf16 a[56:59], a[156:159], v[148:151], a[56:59]\n\t" \
  "v_exp_f32 v33, v33\n\t" \
  "v_mfma_f32_16x16x32_bf16 a[72:75], a[160:163], v[144:147], a[72:75]\n\t" \
  "v_cvt_pk_bf16_f32 v136, v32, v33\n\t" \
  "ds_read_b64_tr_b16 v[50:51], v235 offset:4096\n\t" \
  "v_mfma_f32_16x16x32_bf16 a[72:75], a[164:167], v[148:151], a[72:75]\n\t" \
  "v_exp_f32 v34, v34\n\t" \
  "v_mfma_f32_16x16x32_bf16 a[88:91], a[168:171], v[144:147], a[88:91]\n\t" \
  "v_exp_f32 v35, v35\n\t" \
  "v_mfma_f32_16x16x32_bf16 a[88:91], a[172:175], v[148:151], a[88:91]\n\t" \
  "v_cvt_pk_bf16_f32 v137, v34, v35\n\t" \
  "ds_read_b64_tr_b16 v[52:53], v235 offset:8192\n\t" \
  "v_mfma_f32_16x16x32_bf16 a[104:107], a[176:179], v[144:147], a[104:107]\n\t" \
  "v_exp_f32 v36, v36\n\t" \
  "v_mfma_f32_16x16x32_bf16 a[104:107], a[180:183], v[148:151], a[104:107]\n\t" \
  "v_exp_f32 v37, v37\n\t" \
  "v_mfma_f32_16x16x32_bf16 a[120:123], a[184:187], v[144:147], a[120:123]\n\t" \
  "v_cvt_pk_bf16_f32 v138, v36, v37\n\t" \
  "ds_read_b64_tr_b16 v[54:55], v235 offset:12288\n\t" \
  "v_mfma_f32_16x16x32_bf16 a[120:123], a[188:191], v[148:151], a[120:123]\n\t" \
  "v_exp_f32 v38, v38\n\t" \
  "v_mfma_f32_16x16x32_bf16 v[168:171], v[216:219], v[144:147], v[168:171]\n\t" \
  "v_exp_f32 v39, v39\n\t" \
  "v_mfma_f32_16x16x32_bf16 v[168:171], v[216:219], v[148:151], v[168:171]\n\t" \
  "v_cvt_pk_bf16_f32 v139, v38, v39\n\t" \
  "ds_read_b64_tr_b16 v[56:57], v235 offset:16384\n\t" \
  "v_mfma_f32_16x16x32_bf16 a[12:15], a[128:131], v[152:155], a[12:15]\n\t" \
  "v_exp_f32 v40, v40\n\t" \
  "v_mfma_f32_16x16x32_bf16 a[12:15], a[132:135], v[156:159], a[12:15]\n\t" \
  "v_exp_f32 v41, v41\n\t" \
  "v_mfma_f32_16x16x32_bf16 a[28:31], a[136:139], v[152:155], a[28:31]\n\t" \
  "v_cvt_pk_bf16_f32 v140, v40, v41\n\t" \
  "ds_read_b64_tr_b16 v[58:59], v235 offset:20480\n\t" \
  "v_mfma_f32_16x16x32_bf16 a[28:31], a[140:143], v[156:159], a[28:31]\n\t" \
  "v_exp_f32 v42, v42\n\t" \
  "v_mfma_f32_16x16x32_bf16 a[44:47], a[144:147], v[152:155], a[44:47]\n\t" \
  "ds_read_b64_tr_b16 v[60:61], v235 offset:24576\n\t" \
  "ds_read_b64_tr_b16 v[62:63], v235 offset:28672\n\t" \
  "v_mfma_f32_16x16x32_bf16 a[44:47], a[148:151], v[156:159], a[44:47]\n\t" \
  "v_exp_f32 v43, v43\n\t" \
  "v_mfma_f32_16x16x32_bf16 a[60:63], a[152:155], v[152:155], a[60:63]\n\t" \
  "v_cvt_pk_bf16_f32 v141, v42, v43\n\t" \
  "v_xor_b32 v236, 96, v234\n\t" \
  "v_mfma_f32_16x16x32_bf16 a[60:63], a[156:159], v[156:159], a[60:63]\n\t" \
  "v_exp_f32 v44, v44\n\t" \
  "v_mfma_f32_16x16x32_bf16 a[76:79], a[160:163], v[152:155], a[76:79]\n\t" \
  "v_exp_f32 v45, v45\n\t" \
  "v_mfma_f32_16x16x32_bf16 a[76:79], a[164:167], v[156:159], a[76:79]\n\t" \
  "v_cvt_pk_bf16_f32 v142, v44, v45\n\t" \
  "ds_read_b64_tr_b16 v[80:81], v236 offset:0\n\t" \
  "v_mfma_f32_16x16x32_bf16 a[92:95], a[168:171], v[152:155], a[92:95]\n\t" \
  "v_exp_f32 v46, v46\n\t" \
  "v_mfma_f32_16x16x32_bf16 a[92:95], a[172:175], v[156:159], a[92:95]\n\t" \
  "v_exp_f32 v47, v47\n\t" \
  "v_mfma_f32_16x16x32_bf16 a[108:111], a[176:179], v[152:155], a[108:111]\n\t" \
  "v_cvt_pk_bf16_f32 v143, v46, v47\n\t" \
  "ds_read_b64_tr_b16 v[82:83], v236 offset:4096\n\t" \
  "v_mfma_f32_16x16x32_bf16 a[108:111], a[180:183], v[156:159], a[108:111]\n\t" \
  "v_exp_f32 v64, v64\n\t" \
  "v_mfma_f32_16x16x32_bf16 a[124:127], a[184:187], v[152:155], a[124:127]\n\t" \
  "v_exp_f32 v65, v65\n\t" \
  "v_mfma_f32_16x16x32_bf16 a[124:127], a[188:191], v[156:159], a[124:127]\n\t" \
  "v_cvt_pk_bf16_f32 v144, v64, v65\n\t" \
  "ds_read_b64_tr_b16 v[84:85], v236 offset:8192\n\t" \
  "v_mfma_f32_16x16x32_bf16 v[172:175], v[216:219], v[152:155], v[172:175]\n\t" \
  "v_exp_f32 v66, v66\n\t" \
  "v_mfma_f32_16x16x32_bf16 v[172:175], v[216:219], v[156:159], v[172:175]\n\t" \
  "ds_read_b64_tr_b16 v[86:87], v236 offset:12288\n\t" \
  "ds_read_b64_tr_b16 v[88:89], v236 offset:16384\n\t" \
  "s_cmp_lg_u32 s99, 0\n\t" \
  "s_cbranch_scc1 .Lsf_p7\n\t" \
  ".Lsf_q7:\n\t" \
  "s_waitcnt lgkmcnt(15)\n\t" \
  "v_mfma_f32_16x16x32_bf16 a[0:3], a[192:195], v[128:131], a[0:3]\n\t" \
  "v_exp_f32 v67, v67\n\t" \
  "v_mfma_f32_16x16x32_bf16 a[0:3], a[196:199], v[132:135], a[0:3]\n\t" \
  "v_cvt_pk_bf16_f32 v145, v66, v67\n\t" \
  "ds_read_b128 v[176:179], v215 offset:16384\n\t" \
  "v_mfma_f32_16x16x32_bf16 v[160:163], v[216:219], v[128:131], v[160:163]\n\t" \
  "v_exp_f32 v68, v68\n\t" \
  "v_mfma_f32_16x16x32_bf16 v[160:163], v[216:219], v[132:135], v[160:163]\n\t" \
  "ds_read_b64_tr_b16 v[90:91], v236 offset:20480\n\t" \
  "ds_read_b128 v[180:183], v215 offset:17408\n\t" \
  "v_mfma_f32_16x16x32_bf16 a[16:19], a[200:203], v[128:131], a[16:19]\n\t" \
  "ds_read_b64_tr_b16 v[92:93], v236 offset:24576\n\t" \
  "ds_read_b128 v[184:187], v215 offset:18432\n\t" \
  "v_mfma_f32_16x16x32_bf16 a[16:19], a[204:207], v[132:135], a[16:19]\n\t" \
  "v_exp_f32 v69, v69\n\t" \
  "v_mfma_f32_16x16x32_bf16 a[32:35], a[208:211], v[128:131], a[32:35]\n\t" \
  "v_cvt_pk_bf16_f32 v146, v68, v69\n\t" \
  "ds_read_b128 v[188:191], v215 offset:19456\n\t" \
  "v_mfma_f32_16x16x32_bf16 a[32:35], a[212:215], v[132:135], a[32:35]\n\t" \
  "s_waitcnt lgkmcnt(0)\n\t" \
  "buffer_load_dwordx4 v240, %[wo], s97 offen offset:0 lds\n\t" \
  "v_mfma_f32_16x16x32_bf16 a[48:51], a[216:219], v[128:131], a[48:51]\n\t" \
  "s_add_u32 s97, s97, 7168\n\t" \
  "buffer_load_dwordx4 v241, %[wo], s97 offen offset:1024 lds\n\t" \
  "v_mfma_f32_16x16x32_bf16 a[48:51], a[220:223], v[132:135], a[48:51]\n\t" \
  "s_add_u32 s97, s97, 7168\n\t" \
  "buffer_load_dwordx4 v240, %[wo], s97 offen offset:2048 lds\n\t" \
  "v_mfma_f32_16x16x32_bf16 a[64:67], a[224:227], v[128:131], a[64:67]\n\t" \
  "s_add_u32 s97, s97, 7168\n\t" \
  "buffer_load_dwordx4 v241, %[wo], s97 offen offset:3072 lds\n\t" \
  "v_mfma_f32_16x16x32_bf16 a[64:67], a[228:231], v[132:135], a[64:67]\n\t" \
  "s_add_u32 m0, %[m], 131072\n\t" \
  "s_xor_b32 s97, %[wob], 917504\n\t" \
  "v_mfma_f32_16x16x32_bf16 a[80:83], a[232:235], v[128:131], a[80:83]\n\t" \
  "ds_read_b64_tr_b16 v[94:95], v236 offset:28672\n\t" \
  "ds_read_b128 v[192:195], v215 offset:32768\n\t" \
  "v_mfma_f32_16x16x32_bf16 a[80:83], a[236:239], v[132:135], a[80:83]\n\t" \
  "v_rcp_f32 v208, v160\n\t" \
  "v_mfma_f32_16x16x32_bf16 a[96:99], a[240:243], v[128:131], a[96:99]\n\t" \
  "v_accvgpr_read_b32 v16, a0\n\t" \
  "v_accvgpr_read_b32 v17, a1\n\t" \
  "v_mfma_f32_16x16x32_bf16 a[96:99], a[244:247], v[132:135], a[96:99]\n\t" \
  "v_accvgpr_read_b32 v18, a2\n\t" \
  "v_accvgpr_read_b32 v19, a3\n\t" \
  "v_mfma_f32_16x16x32_bf16 a[112:115], a[248:251], v[128:131], a[112:115]\n\t" \
  "v_accvgpr_read_b32 v20, a16\n\t" \
  "v_accvgpr_read_b32 v21, a17\n\t" \
  "v_mfma_f32_16x16x32_bf16 a[112:115], a[252:255], v[132:135], a[112:115]\n\t" \
  "v_accvgpr_read_b32 v22, a18\n\t" \
  "v_accvgpr_read_b32 v23, a19\n\t" \
  "s_waitcnt vmcnt(8)\n\t" \
  "s_barrier\n\t" \
  "v_mfma_f32_16x16x32_bf16 a[4:7], a[192:195], v[136:139], a[4:7]\n\t" \
  "v_xor_b32 v235, 128, v234\n\t" \
  "ds_read_b128 v[196:199], v215 offset:33792\n\t" \
  "v_mfma_f32_16x16x32_bf16 a[4:7], a[196:199], v[140:143], a[4:7]\n\t" \
  "v_mul_f32 v16, v16, v208\n\t" \
  "v_mul_f32 v17, v17, v208\n\t" \
  "v_mfma_f32_16x16x32_bf16 v[164:167], v[216:219], v[136:139], v[164:167]\n\t" \
  "v_mul_f32 v18, v18, v208\n\t" \
  "v_mul_f32 v19, v19, v208\n\t" \
  "v_mfma_f32_16x16x32_bf16 v[164:167], v[216:219], v[140:143], v[164:167]\n\t" \
  "v_mul_f32 v20, v20, v208\n\t" \
  "v_mul_f32 v21, v21, v208\n\t" \
  "v_mfma_f32_16x16x32_bf16 a[20:23], a[200:203], v[136:139], a[20:23]\n\t" \
  "v_mul_f32 v22, v22, v208\n\t" \
  "v_mul_f32 v23, v23, v208\n\t" \
  "v_mfma_f32_16x16x32_bf16 a[20:23], a[204:207], v[140:143], a[20:23]\n\t" \
  "ds_read_b128 v[200:203], v215 offset:34816\n\t" \
  "ds_read_b64_tr_b16 v[112:113], v235 offset:0\n\t" \
  "v_mfma_f32_16x16x32_bf16 a[36:39], a[208:211], v[136:139], a[36:39]\n\t" \
  "ds_read_b128 v[204:207], v215 offset:35840\n\t" \
  "s_waitcnt lgkmcnt(0)\n\t" \
  "v_mfma_f32_16x16x32_bf16 a[36:39], a[212:215], v[140:143], a[36:39]\n\t" \
  "buffer_load_dwordx4 v240, %[wo], s97 offen offset:0 lds\n\t" \
  "ds_read_b64_tr_b16 v[114:115], v235 offset:4096\n\t" \
  "v_mfma_f32_16x16x32_bf16 a[52:55], a[216:219], v[136:139], a[52:55]\n\t" \
  "s_add_u32 s97, s97, 7168\n\t" \
  "buffer_load_dwordx4 v241, %[wo], s97 offen offset:1024 lds\n\t" \
  "v_mfma_f32_16x16x32_bf16 a[52:55], a[220:223], v[140:143], a[52:55]\n\t" \
  "s_add_u32 s97, s97, 7168\n\t" \
  "buffer_load_dwordx4 v240, %[wo], s97 offen offset:2048 lds\n\t" \
  "v_mfma_f32_16x16x32_bf16 a[68:71], a[224:227], v[136:139], a[68:71]\n\t" \
  "s_add_u32 s97, s97, 7168\n\t" \
  "buffer_load_dwordx4 v241, %[wo], s97 offen offset:3072 lds\n\t" \
  "v_mfma_f32_16x16x32_bf16 a[68:71], a[228:231], v[140:143], a[68:71]\n\t" \
  "v_cvt_pk_bf16_f32 v0, v16, v17\n\t" \
  "v_cvt_pk_bf16_f32 v1, v18, v19\n\t" \
  "v_mfma_f32_16x16x32_bf16 a[84:87], a[232:235], v[136:139], a[84:87]\n\t" \
  "v_cvt_pk_bf16_f32 v2, v20, v21\n\t" \
  "v_cvt_pk_bf16_f32 v3, v22, v23\n\t" \
  "v_mfma_f32_16x16x32_bf16 a[84:87], a[236:239], v[140:143], a[84:87]\n\t" \
  "s_xor_b32 s98, %[eso], 0\n\t" \
  "buffer_store_dwordx4 v[0:3], v242, %[ws], s98 offen offset:0 sc1\n\t" \
  "v_mfma_f32_16x16x32_bf16 a[100:103], a[240:243], v[136:139], a[100:103]\n\t" \
  "v_accvgpr_read_b32 v24, a32\n\t" \
  "v_accvgpr_read_b32 v25, a33\n\t" \
  "v_mfma_f32_16x16x32_bf16 a[100:103], a[244:247], v[140:143], a[100:103]\n\t" \
  "v_accvgpr_read_b32 v26, a34\n\t" \
  "v_accvgpr_read_b32 v27, a35\n\t" \
  "v_mfma_f32_16x16x32_bf16 a[116:119], a[248:251], v[136:139], a[116:119]\n\t" \
  "v_accvgpr_read_b32 v28, a48\n\t" \
  "v_accvgpr_read_b32 v29, a49\n\t" \
  "v_mfma_f32_16x16x32_bf16 a[116:119], a[252:255], v[140:143], a[116:119]\n\t" \
  "v_accvgpr_read_b32 v30, a50\n\t" \
  "v_accvgpr_read_b32 v31, a51\n\t" \
  "v_exp_f32 v70, v70\n\t" \
  "v_exp_f32 v71, v71\n\t" \
  "s_nop 0\n\t" \
  "v_cvt_pk_bf16_f32 v147, v70, v71\n\t" \
  "s_nop 1\n\t" \
  "v_mfma_f32_16x16x32_bf16 a[8:11], a[192:195], v[144:147], a[8:11]\n\t" \
  "ds_read_b64_tr_b16 v[116:117], v235 offset:8192\n\t" \
  "ds_read_b64_tr_b16 v[118:119], v235 offset:12288\n\t" \
  "v_exp_f32 v72, v72\n\t" \
  "v_exp_f32 v73, v73\n\t" \
  "v_exp_f32 v74, v74\n\t" \
  "v_exp_f32 v75, v75\n\t" \
  "v_exp_f32 v76, v76\n\t" \
  "v_exp_f32 v77, v77\n\t" \
  "v_exp_f32 v78, v78\n\t" \
  "v_exp_f32 v79, v79\n\t" \
  "v_cvt_pk_bf16_f32 v148, v72, v73\n\t" \
  "v_cvt_pk_bf16_f32 v149, v74, v75\n\t" \
  "v_cvt_pk_bf16_f32 v150, v76, v77\n\t" \
  "v_cvt_pk_bf16_f32 v151, v78, v79\n\t" \
  "s_nop 1\n\t" \
  "v_mfma_f32_16x16x32_bf16 a[8:11], a[196:199], v[148:151], a[8:11]\n\t" \
  "v_mul_f32 v24, v24, v208\n\t" \
  "v_mul_f32 v25, v25, v208\n\t" \
  "v_mfma_f32_16x16x32_bf16 v[168:171], v[216:219], v[144:147], v[168:171]\n\t" \
  "v_mul_f32 v26, v26, v208\n\t" \
  "v_mul_f32 v27, v27, v208\n\t" \
  "v_mfma_f32_16x16x32_bf16 v[168:171], v[216:219], v[148:151], v[168:171]\n\t" \
  "v_mul_f32 v28, v28, v208\n\t" \
  "v_mul_f32 v29, v29, v208\n\t" \
  "v_mfma_f32_16x16x32_bf16 a[24:27], a[200:203], v[144:147], a[24:27]\n\t" \
  "v_mul_f32 v30, v30, v208\n\t" \
  "v_mul_f32 v31, v31, v208\n\t" \
  "v_mfma_f32_16x16x32_bf16 a[24:27], a[204:207], v[148:151], a[24:27]\n\t" \
  "ds_read_b64_tr_b16 v[120:121], v235 offset:16384\n\t" \
  "v_cvt_pk_bf16_f32 v4, v24, v25\n\t" \
  "v_mfma_f32_16x16x32_bf16 a[40:43], a[208:211], v[144:147], a[40:43]\n\t" \
  "v_cvt_pk_bf16_f32 v5, v26, v27\n\t" \
  "v_cvt_pk_bf16_f32 v6, v28, v29\n\t" \
  "v_mfma_f32_16x16x32_bf16 a[40:43], a[212:215], v[148:151], a[40:43]\n\t" \
  "v_cvt_pk_bf16_f32 v7, v30, v31\n\t" \
  "ds_read_b64_tr_b16 v[122:123], v235 offset:20480\n\t" \
  "v_mfma_f32_16x16x32_bf16 a[56:59], a[216:219], v[144:147], a[56:59]\n\t" \
  "s_xor_b32 s98, %[eso], 0\n\t" \
  "buffer_store_dwordx4 v[4:7], v242, %[ws], s98 offen offset:1024 sc1\n\t" \
  "v_mfma_f32_16x16x32_bf16 a[56:59], a[220:223], v[148:151], a[56:59]\n\t" \
  "v_accvgpr_read_b32 v16, a64\n\t" \
  "v_accvgpr_read_b32 v17, a65\n\t" \
  "v_mfma_f32_16x16x32_bf16 a[72:75], a[224:227], v[144:147], a[72:75]\n\t" \
  "v_accvgpr_read_b32 v18, a66\n\t" \
  "v_accvgpr_read_b32 v19, a67\n\t" \
  "v_mfma_f32_16x16x32_bf16 a[72:75], a[228:231], v[148:151], a[72:75]\n\t" \
  "v_accvgpr_read_b32 v20, a80\n\t" \
  "v_accvgpr_read_b32 v21, a81\n\t" \
  "v_mfma_f32_16x16x32_bf16 a[88:91], a[232:235], v[144:147], a[88:91]\n\t" \
  "v_accvgpr_read_b32 v22, a82\n\t" \
  "v_accvgpr_read_b32 v23, a83\n\t" \
  "v_mfma_f32_16x16x32_bf16 a[88:91], a[236:239], v[148:151], a[88:91]\n\t" \
  "ds_read_b64_tr_b16 v[124:125], v235 offset:24576\n\t" \
  "v_mul_f32 v16, v16, v208\n\t" \
  "v_mfma_f32_16x16x32_bf16 a[104:107], a[240:243], v[144:147], a[104:107]\n\t" \
  "v_mul_f32 v17, v17, v208\n\t" \
  "v_mul_f32 v18, v18, v208\n\t" \
  "v_mfma_f32_16x16x32_bf16 a[104:107], a[244:247], v[148:151], a[104:107]\n\t" \
  "v_mul_f32 v19, v19, v208\n\t" \
  "v_mul_f32 v20, v20, v208\n\t" \
  "v_mfma_f32_16x16x32_bf16 a[120:123], a[248:251], v[144:147], a[120:123]\n\t" \
  "v_mul_f32 v21, v21, v208\n\t" \
  "v_mul_f32 v22, v22, v208\n\t" \
  "v_mfma_f32_16x16x32_bf16 a[120:123], a[252:255], v[148:151], a[120:123]\n\t" \
  "v_mul_f32 v23, v23, v208\n\t" \
  "ds_read_b64_tr_b16 v[126:127], v235 offset:28672\n\t" \
  "v_exp_f32 v96, v96\n\t" \
  "v_exp_f32 v97, v97\n\t" \
  "v_exp_f32 v98, v98\n\t" \
  "v_exp_f32 v99, v99\n\t" \
  "v_exp_f32 v100, v100\n\t" \
  "v_exp_f32 v101, v101\n\t" \
  "v_exp_f32 v102, v102\n\t" \
  "v_exp_f32 v103, v103\n\t" \
  "v_cvt_pk_bf16_f32 v152, v96, v97\n\t" \
  "v_cvt_pk_bf16_f32 v153, v98, v99\n\t" \
  "v_cvt_pk_bf16_f32 v154, v100, v101\n\t" \
  "v_cvt_pk_bf16_f32 v155, v102, v103\n\t" \
  "s_nop 1\n\t" \
  "v_mfma_f32_16x16x32_bf16 a[12:15], a[192:195], v[152:155], a[12:15]\n\t" \
  "v_cvt_pk_bf16_f32 v8, v16, v17\n\t" \
  "v_cvt_pk_bf16_f32 v9, v18, v19\n\t" \
  "v_exp_f32 v104, v104\n\t" \
  "v_exp_f32 v105, v105\n\t" \
  "v_exp_f32 v106, v106\n\t" \
  "v_exp_f32 v107, v107\n\t" \
  "v_exp_f32 v108, v108\n\t" \
  "v_exp_f32 v109, v109\n\t" \
  "v_exp_f32 v110, v110\n\t" \
  "v_exp_f32 v111, v111\n\t" \
  "v_cvt_pk_bf16_f32 v156, v104, v105\n\t" \
  "v_cvt_pk_bf16_f32 v157, v106, v107\n\t" \
  "v_cvt_pk_bf16_f32 v158, v108, v109\n\t" \
  "v_cvt_pk_bf16_f32 v159, v110, v111\n\t" \
  "s_nop 1\n\t" \
  "v_mfma_f32_16x16x32_bf16 a[12:15], a[196:199], v[156:159], a[12:15]\n\t" \
  "v_cvt_pk_bf16_f32 v10, v20, v21\n\t" \
  "v_cvt_pk_bf16_f32 v11, v22, v23\n\t" \
  "v_mfma_f32_16x16x32_bf16 v[172:175], v[216:219], v[152:155], v[172:175]\n\t" \
  "s_xor_b32 s98, %[eso], 0\n\t" \
  "buffer_store_dwordx4 v[8:11], v242, %[ws], s98 offen offset:2048 sc1\n\t" \
  "v_mfma_f32_16x16x32_bf16 v[172:175], v[216:219], v[156:159], v[172:175]\n\t" \
  "ds_read_b64_tr_b16 a[128:129], v234 offset:0\n\t" \
  "ds_read_b64_tr_b16 a[130:131], v234 offset:4096\n\t" \
  "v_mfma_f32_16x16x32_bf16 a[28:31], a[200:203], v[152:155], a[28:31]\n\t" \
  "v_accvgpr_read_b32 v24, a96\n\t" \
  "v_accvgpr_read_b32 v25, a97\n\t" \
  "v_mfma_f32_16x16x32_bf16 a[28:31], a[204:207], v[156:159], a[28:31]\n\t" \
  "v_accvgpr_read_b32 v26, a98\n\t" \
  "v_accvgpr_read_b32 v27, a99\n\t" \
  "v_mfma_f32_16x16x32_bf16 a[44:47], a[208:211], v[152:155], a[44:47]\n\t" \
  "v_accvgpr_read_b32 v28, a112\n\t" \
  "v_accvgpr_read_b32 v29, a113\n\t" \
  "v_mfma_f32_16x16x32_bf16 a[44:47], a[212:215], v[156:159], a[44:47]\n\t" \
  "v_accvgpr_read_b32 v30, a114\n\t" \
  "v_accvgpr_read_b32 v31, a115\n\t" \
  "v_mfma_f32_16x16x32_bf16 a[60:63], a[216:219], v[152:155], a[60:63]\n\t" \
  "ds_read_b64_tr_b16 a[132:133], v234 offset:8192\n\t" \
  "ds_read_b64_tr_b16 a[134:135], v234 offset:12288\n\t" \
  "v_mfma_f32_16x16x32_bf16 a[60:63], a[220:223], v[156:159], a[60:63]\n\t" \
  "v_mul_f32 v24, v24, v208\n\t" \
  "v_mul_f32 v25, v25, v208\n\t" \
  "v_mfma_f32_16x16x32_bf16 a[76:79], a[224:227], v[152:155], a[76:79]\n\t" \
  "v_mul_f32 v26, v26, v208\n\t" \
  "v_mul_f32 v27, v27, v208\n\t" \
  "v_mfma_f32_16x16x32_bf16 a[76:79], a[228:231], v[156:159], a[76:79]\n\t" \
  "v_mul_f32 v28, v28, v208\n\t" \
  "v_mul_f32 v29, v29, v208\n\t" \
  "v_mfma_f32_16x16x32_bf16 a[92:95], a[232:235], v[152:155], a[92:95]\n\t" \
  "v_mul_f32 v30, v30, v208\n\t" \
  "v_mul_f32 v31, v31, v208\n\t" \
  "v_mfma_f32_16x16x32_bf16 a[92:95], a[236:239], v[156:159], a[92:95]\n\t" \
  "ds_read_b64_tr_b16 a[136:137], v234 offset:16384\n\t" \
  "ds_read_b64_tr_b16 a[138:139], v234 offset:20480\n\t" \
  "v_mfma_f32_16x16x32_bf16 a[108:111], a[240:243], v[152:155], a[108:111]\n\t" \
  "v_cvt_pk_bf16_f32 v12, v24, v25\n\t" \
  "v_cvt_pk_bf16_f32 v13, v26, v27\n\t" \
  "v_mfma_f32_16x16x32_bf16 a[108:111], a[244:247], v[156:159], a[108:111]\n\t" \
  "v_cvt_pk_bf16_f32 v14, v28, v29\n\t" \
  "v_cvt_pk_bf16_f32 v15, v30, v31\n\t" \
  "v_mfma_f32_16x16x32_bf16 a[124:127], a[248:251], v[152:155], a[124:127]\n\t" \
  "s_xor_b32 s98, %[eso], 0\n\t" \
  "buffer_store_dwordx4 v[12:15], v242, %[ws], s98 offen offset:3072 sc1\n\t" \
  "v_mfma_f32_16x16x32_bf16 a[124:127], a[252:255], v[156:159], a[124:127]\n\t" \
  "ds_read_b64_tr_b16 a[140:141], v234 offset:24576\n\t" \
  "ds_read_b64_tr_b16 a[142:143], v234 offset:28672\n\t" \
  "v_mfma_f32_16x16x32_bf16 a[32:35], v[48:51], v[0:3], 0\n\t" \
  "v_rcp_f32 v210, v164\n\t" \
  "v_mfma_f32_16x16x32_bf16 a[32:35], v[52:55], v[4:7], a[32:35]\n\t" \
  "v_accvgpr_read_b32 v16, a4\n\t" \
  "v_accvgpr_read_b32 v17, a5\n\t" \
  "v_mfma_f32_16x16x32_bf16 a[32:35], v[56:59], v[8:11], a[32:35]\n\t" \
  "v_accvgpr_read_b32 v18, a6\n\t" \
  "v_accvgpr_read_b32 v19, a7\n\t" \
  "v_mfma_f32_16x16x32_bf16 a[32:35], v[60:63], v[12:15], a[32:35]\n\t" \
  "v_accvgpr_read_b32 v20, a20\n\t" \
  "v_accvgpr_read_b32 v21, a21\n\t" \
  "v_mfma_f32_16x16x32_bf16 a[48:51], v[92:95], v[12:15], 0\n\t" \
  "v_accvgpr_read_b32 v22, a22\n\t" \
  "v_accvgpr_read_b32 v23, a23\n\t" \
  "v_mfma_f32_16x16x32_bf16 a[48:51], v[88:91], v[8:11], a[48:51]\n\t" \
  "v_xor_b32 v236, 32, v234\n\t" \
  "ds_read_b64_tr_b16 a[144:145], v236 offset:0\n\t" \
  "v_mfma_f32_16x16x32_bf16 a[48:51], v[84:87], v[4:7], a[48:51]\n\t" \
  "v_mul_f32 v16, v16, v210\n\t" \
  "v_mul_f32 v17, v17, v210\n\t" \
  "v_mfma_f32_16x16x32_bf16 a[48:51], v[80:83], v[0:3], a[48:51]\n\t" \
  "v_mul_f32 v18, v18, v210\n\t" \
  "v_mul_f32 v19, v19, v210\n\t" \
  "s_waitcnt lgkmcnt(15)\n\t" \
  "v_mfma_f32_16x16x32_bf16 a[64:67], v[112:115], v[0:3], 0\n\t" \
  "v_mul_f32 v20, v20, v210\n\t" \
  "v_mul_f32 v21, v21, v210\n\t" \
  "s_waitcnt lgkmcnt(13)\n\t" \
  "v_mfma_f32_16x16x32_bf16 a[64:67], v[116:119], v[4:7], a[64:67]\n\t" \
  "v_mul_f32 v22, v22, v210\n\t" \
  "v_mul_f32 v23, v23, v210\n\t" \
  "s_waitcnt lgkmcnt(11)\n\t" \
  "v_mfma_f32_16x16x32_bf16 a[64:67], v[120:123], v[8:11], a[64:67]\n\t" \
  "ds_read_b64_tr_b16 a[146:147], v236 offset:4096\n\t" \
  "ds_read_b64_tr_b16 a[148:149], v236 offset:8192\n\t" \
  "s_waitcnt lgkmcnt(11)\n\t" \
  "v_mfma_f32_16x16x32_bf16 a[64:67], v[124:127], v[12:15], a[64:67]\n\t" \
  "v_cvt_pk_bf16_f32 v32, v16, v17\n\t" \
  "v_cvt_pk_bf16_f32 v33, v18, v19\n\t" \
  "ds_read_b64_tr_b16 a[150:151], v236 offset:12288\n\t" \
  "ds_read_b64_tr_b16 a[152:153], v236 offset:16384\n\t" \
  "ds_read_b64_tr_b16 a[154:155], v236 offset:20480\n\t" \
  "ds_read_b64_tr_b16 a[156:157], v236 offset:24576\n\t" \
  "ds_read_b64_tr_b16 a[158:159], v236 offset:28672\n\t" \
  "s_waitcnt lgkmcnt(8)\n\t" \
  "v_mfma_f32_16x16x32_bf16 a[0:3], a[140:143], v[12:15], 0\n\t" \
  "v_cvt_pk_bf16_f32 v34, v20, v21\n\t" \
  "v_cvt_pk_bf16_f32 v35, v22, v23\n\t" \
  "v_mfma_f32_16x16x32_bf16 a[0:3], a[136:139], v[8:11], a[0:3]\n\t" \
  "s_xor_b32 s98, %[eso], 32768\n\t" \
  "buffer_store_dwordx4 v[32:35], v242, %[ws], s98 offen offset:0 sc1\n\t" \
  "v_mfma_f32_16x16x32_bf16 a[0:3], a[132:135], v[4:7], a[0:3]\n\t" \
  "v_accvgpr_read_b32 v24, a36\n\t" \
  "v_accvgpr_read_b32 v25, a37\n\t" \
  "v_mfma_f32_16x16x32_bf16 a[0:3], a[128:131], v[0:3], a[0:3]\n\t" \
  "v_accvgpr_read_b32 v26, a38\n\t" \
  "v_accvgpr_read_b32 v27, a39\n\t" \
  "v_xor_b32 v235, 192, v234\n\t" \
  "ds_read_b64_tr_b16 a[160:161], v235 offset:0\n\t" \
  "ds_read_b64_tr_b16 a[162:163], v235 offset:4096\n\t" \
  "ds_read_b64_tr_b16 a[164:165], v235 offset:8192\n\t" \
  "ds_read_b64_tr_b16 a[166:167], v235 offset:12288\n\t" \
  "ds_read_b64_tr_b16 a[168:169], v235 offset:16384\n\t" \
  "ds_read_b64_tr_b16 a[170:171], v235 offset:20480\n\t" \
  "ds_read_b64_tr_b16 a[172:173], v235 offset:24576\n\t" \
  "ds_read_b64_tr_b16 a[174:175], v235 offset:28672\n\t" \
  "s_waitcnt lgkmcnt(14)\n\t" \
  "v_mfma_f32_16x16x32_bf16 a[16:19], a[144:147], v[0:3], 0\n\t" \
  "v_accvgpr_read_b32 v28, a52\n\t" \
  "v_accvgpr_read_b32 v29, a53\n\t" \
  "s_waitcnt lgkmcnt(12)\n\t" \
  "v_mfma_f32_16x16x32_bf16 a[16:19], a[148:151], v[4:7], a[16:19]\n\t" \
  "v_accvgpr_read_b32 v30, a54\n\t" \
  "v_accvgpr_read_b32 v31, a55\n\t" \
  "s_waitcnt lgkmcnt(10)\n\t" \
  "v_mfma_f32_16x16x32_bf16 a[16:19], a[152:155], v[8:11], a[16:19]\n\t" \
  "v_mul_f32 v24, v24, v210\n\t" \
  "v_mul_f32 v25, v25, v210\n\t" \
  "s_waitcnt lgkmcnt(8)\n\t" \
  "v_mfma_f32_16x16x32_bf16 a[16:19], a[156:159], v[12:15], a[16:19]\n\t" \
  "v_mul_f32 v26, v26, v210\n\t" \
  "v_mul_f32 v27, v27, v210\n\t" \
  "v_xor_b32 v236, 224, v234\n\t" \
  "ds_read_b64_tr_b16 a[176:177], v236 offset:0\n\t" \
  "ds_read_b64_tr_b16 a[178:179], v236 offset:4096\n\t" \
  "ds_read_b64_tr_b16 a[180:181], v236 offset:8192\n\t" \
  "ds_read_b64_tr_b16 a[182:183], v236 offset:12288\n\t" \
  "ds_read_b64_tr_b16 a[184:185], v236 offset:16384\n\t" \
  "ds_read_b64_tr_b16 a[186:187], v236 offset:20480\n\t" \
  "ds_read_b64_tr_b16 a[188:189], v236 offset:24576\n\t" \
  "ds_read_b64_tr_b16 a[190:191], v236 offset:28672\n\t" \
  "s_waitcnt lgkmcnt(8)\n\t" \
  "v_mfma_f32_16x16x32_bf16 a[96:99], a[172:175], v[12:15], 0\n\t" \
  "v_mul_f32 v28, v28, v210\n\t" \
  "v_mul_f32 v29, v29, v210\n\t" \
  "v_mfma_f32_16x16x32_bf16 a[96:99], a[168:171], v[8:11], a[96:99]\n\t" \
  "v_mul_f32 v30, v30, v210\n\t" \
  "v_mul_f32 v31, v31, v210\n\t" \
  "v_mfma_f32_16x16x32_bf16 a[96:99], a[164:167], v[4:7], a[96:99]\n\t" \
  "v_cvt_pk_bf16_f32 v36, v24, v25\n\t" \
  "v_cvt_pk_bf16_f32 v37, v26, v27\n\t" \
  "v_mfma_f32_16x16x32_bf16 a[96:99], a[160:163], v[0:3], a[96:99]\n\t" \
  "v_cvt_pk_bf16_f32 v38, v28, v29\n\t" \
  "v_cvt_pk_bf16_f32 v39, v30, v31\n\t" \
  "v_xor_b32 v236, 160, v234\n\t" \
  "ds_read_b64_tr_b16 v[96:97], v236 offset:0\n\t" \
  "ds_read_b64_tr_b16 v[98:99], v236 offset:4096\n\t" \
  "ds_read_b64_tr_b16 v[100:101], v236 offset:8192\n\t" \
  "ds_read_b64_tr_b16 v[102:103], v236 offset:12288\n\t" \
  "ds_read_b64_tr_b16 v[104:105], v236 offset:16384\n\t" \
  "ds_read_b64_tr_b16 v[106:107], v236 offset:20480\n\t" \
  "ds_read_b64_tr_b16 v[108:109], v236 offset:24576\n\t" \
  "ds_read_b64_tr_b16 v[110:111], v236 offset:28672\n\t" \
  "s_waitcnt lgkmcnt(14)\n\t" \
  "v_mfma_f32_16x16x32_bf16 a[112:115], a[176:179], v[0:3], 0\n\t" \
  "s_xor_b32 s98, %[eso], 32768\n\t" \
  "buffer_store_dwordx4 v[36:39], v242, %[ws], s98 offen offset:1024 sc1\n\t" \
  "s_waitcnt lgkmcnt(12)\n\t" \
  "v_mfma_f32_16x16x32_bf16 a[112:115], a[180:183], v[4:7], a[112:115]\n\t" \
  "v_accvgpr_read_b32 v16, a68\n\t" \
  "v_accvgpr_read_b32 v17, a69\n\t" \
  "s_waitcnt lgkmcnt(10)\n\t" \
  "v_mfma_f32_16x16x32_bf16 a[112:115], a[184:187], v[8:11], a[112:115]\n\t" \
  "v_accvgpr_read_b32 v18, a70\n\t" \
  "v_accvgpr_read_b32 v19, a71\n\t" \
  "s_waitcnt lgkmcnt(8)\n\t" \
  "v_mfma_f32_16x16x32_bf16 a[112:115], a[188:191], v[12:15], a[112:115]\n\t" \
  "v_accvgpr_read_b32 v20, a84\n\t" \
  "v_accvgpr_read_b32 v21, a85\n\t" \
  "s_waitcnt lgkmcnt(0)\n\t" \
  "v_mfma_f32_16x16x32_bf16 a[80:83], v[108:111], v[12:15], 0\n\t" \
  "v_accvgpr_read_b32 v22, a86\n\t" \
  "v_accvgpr_read_b32 v23, a87\n\t" \
  "v_mfma_f32_16x16x32_bf16 a[80:83], v[104:107], v[8:11], a[80:83]\n\t" \
  "v_mul_f32 v16, v16, v210\n\t" \
  "v_mul_f32 v17, v17, v210\n\t" \
  "v_mfma_f32_16x16x32_bf16 a[80:83], v[100:103], v[4:7], a[80:83]\n\t" \
  "v_mul_f32 v18, v18, v210\n\t" \
  "v_mul_f32 v19, v19, v210\n\t" \
  "v_mfma_f32_16x16x32_bf16 a[80:83], v[96:99], v[0:3], a[80:83]\n\t" \
  "v_mul_f32 v20, v20, v210\n\t" \
  "v_mul_f32 v21, v21, v210\n\t" \
  "v_mul_f32 v22, v22, v210\n\t" \
  "v_mul_f32 v23, v23, v210\n\t" \
  "v_cvt_pk_bf16_f32 v40, v16, v17\n\t" \
  "v_cvt_pk_bf16_f32 v41, v18, v19\n\t" \
  "v_cvt_pk_bf16_f32 v42, v20, v21\n\t" \
  "v_cvt_pk_bf16_f32 v43, v22, v23\n\t" \
  "v_accvgpr_read_b32 v24, a100\n\t" \
  "v_accvgpr_read_b32 v25, a101\n\t" \
  "v_accvgpr_read_b32 v26, a102\n\t" \
  "v_accvgpr_read_b32 v27, a103\n\t" \
  "v_accvgpr_read_b32 v28, a116\n\t" \
  "v_accvgpr_read_b32 v29, a117\n\t" \
  "v_accvgpr_read_b32 v30, a118\n\t" \
  "v_accvgpr_read_b32 v31, a119\n\t" \
  "v_mul_f32 v24, v24, v210\n\t" \
  "v_mul_f32 v25, v25, v210\n\t" \
  "v_mul_f32 v26, v26, v210\n\t" \
  "v_mul_f32 v27, v27, v210\n\t" \
  "v_mul_f32 v28, v28, v210\n\t" \
  "v_mul_f32 v29, v29, v210\n\t" \
  "v_mul_f32 v30, v30, v210\n\t" \
  "v_mul_f32 v31, v31, v210\n\t" \
  "v_cvt_pk_bf16_f32 v44, v24, v25\n\t" \
  "v_cvt_pk_bf16_f32 v45, v26, v27\n\t" \
  "v_cvt_pk_bf16_f32 v46, v28, v29\n\t" \
  "v_cvt_pk_bf16_f32 v47, v30, v31\n\t" \
  "v_mfma_f32_16x16x32_bf16 a[36:39], v[48:51], v[32:35], 0\n\t" \
  "s_xor_b32 s98, %[eso], 32768\n\t" \
  "buffer_store_dwordx4 v[40:43], v242, %[ws], s98 offen offset:2048 sc1\n\t" \
  "v_mfma_f32_16x16x32_bf16 a[36:39], v[52:55], v[36:39], a[36:39]\n\t" \
  "s_xor_b32 s98, %[eso], 32768\n\t" \
  "buffer_store_dwordx4 v[44:47], v242, %[ws], s98 offen offset:3072 sc1\n\t" \
  "v_mfma_f32_16x16x32_bf16 a[36:39], v[56:59], v[40:43], a[36:39]\n\t" \
  "v_rcp_f32 v212, v168\n\t" \
  "v_mfma_f32_16x16x32_bf16 a[36:39], v[60:63], v[44:47], a[36:39]\n\t" \
  "v_accvgpr_read_b32 v16, a8\n\t" \
  "v_accvgpr_read_b32 v17, a9\n\t" \
  "s_waitcnt vmcnt(0)\n\t" \
  "buffer_store_dword v243, v254, %[ws], %[fw2] offen offset:0 sc1\n\t" \
  "v_mfma_f32_16x16x32_bf16 a[52:55], v[92:95], v[44:47], 0\n\t" \
  "v_accvgpr_read_b32 v18, a10\n\t" \
  "v_accvgpr_read_b32 v19, a11\n\t" \
  "v_mfma_f32_16x16x32_bf16 a[52:55], v[88:91], v[40:43], a[52:55]\n\t" \
  "v_accvgpr_read_b32 v20, a24\n\t" \
  "v_accvgpr_read_b32 v21, a25\n\t" \
  "v_mfma_f32_16x16x32_bf16 a[52:55], v[84:87], v[36:39], a[52:55]\n\t" \
  "v_accvgpr_read_b32 v22, a26\n\t" \
  "v_accvgpr_read_b32 v23, a27\n\t" \
  "v_mfma_f32_16x16x32_bf16 a[52:55], v[80:83], v[32:35], a[52:55]\n\t" \
  "v_mul_f32 v16, v16, v212\n\t" \
  "v_mul_f32 v17, v17, v212\n\t" \
  "ds_read_b128 a[224:227], v213 offset:32768\n\t" \
  "ds_read_b128 a[228:231], v213 offset:33792\n\t" \
  "ds_read_b128 a[232:235], v213 offset:34816\n\t" \
  "ds_read_b128 a[236:239], v213 offset:35840\n\t" \
  "ds_read_b128 a[240:243], v215 offset:0\n\t" \
  "ds_read_b128 a[244:247], v215 offset:1024\n\t" \
  "ds_read_b128 a[248:251], v215 offset:2048\n\t" \
  "ds_read_b128 a[252:255], v215 offset:3072\n\t" \
  "v_mfma_f32_16x16x32_bf16 a[68:71], v[112:115], v[32:35], 0\n\t" \
  "v_mul_f32 v18, v18, v212\n\t" \
  "v_mul_f32 v19, v19, v212\n\t" \
  "v_mfma_f32_16x16x32_bf16 a[68:71], v[116:119], v[36:39], a[68:71]\n\t" \
  "v_mul_f32 v20, v20, v212\n\t" \
  "v_mul_f32 v21, v21, v212\n\t" \
  "v_mfma_f32_16x16x32_bf16 a[68:71], v[120:123], v[40:43], a[68:71]\n\t" \
  "v_mul_f32 v22, v22, v212\n\t" \
  "v_mul_f32 v23, v23, v212\n\t" \
  "v_mfma_f32_16x16x32_bf16 a[68:71], v[124:127], v[44:47], a[68:71]\n\t" \
  "v_cvt_pk_bf16_f32 v64, v16, v17\n\t" \
  "v_cvt_pk_bf16_f32 v65, v18, v19\n\t" \
  "v_mfma_f32_16x16x32_bf16 a[4:7], a[140:143], v[44:47], 0\n\t" \
  "v_cvt_pk_bf16_f32 v66, v20, v21\n\t" \
  "v_cvt_pk_bf16_f32 v67, v22, v23\n\t" \
  "v_mfma_f32_16x16x32_bf16 a[4:7], a[136:139], v[40:43], a[4:7]\n\t" \
  "s_xor_b32 s98, %[eso], 65536\n\t" \
  "buffer_store_dwordx4 v[64:67], v242, %[ws], s98 offen offset:0 sc1\n\t" \
  "v_mfma_f32_16x16x32_bf16 a[4:7], a[132:135], v[36:39], a[4:7]\n\t" \
  "v_accvgpr_read_b32 v24, a40\n\t" \
  "v_accvgpr_read_b32 v25, a41\n\t" \
  "v_mfma_f32_16x16x32_bf16 a[4:7], a[128:131], v[32:35], a[4:7]\n\t" \
  "v_accvgpr_read_b32 v26, a42\n\t" \
  "v_accvgpr_read_b32 v27, a43\n\t" \
  "v_mfma_f32_16x16x32_bf16 a[20:23], a[144:147], v[32:35], 0\n\t" \
  "v_accvgpr_read_b32 v28, a56\n\t" \
  "v_accvgpr_read_b32 v29, a57\n\t" \
  "v_mfma_f32_16x16x32_bf16 a[20:23], a[148:151], v[36:39], a[20:23]\n\t" \
  "v_accvgpr_read_b32 v30, a58\n\t" \
  "v_accvgpr_read_b32 v31, a59\n\t" \
  "v_mfma_f32_16x16x32_bf16 a[20:23], a[152:155], v[40:43], a[20:23]\n\t" \
  "v_mul_f32 v24, v24, v212\n\t" \
  "v_mul_f32 v25, v25, v212\n\t" \
  "v_mfma_f32_16x16x32_bf16 a[20:23], a[156:159], v[44:47], a[20:23]\n\t" \
  "v_mul_f32 v26, v26, v212\n\t" \
  "v_mul_f32 v27, v27, v212\n\t" \
  "v_mfma_f32_16x16x32_bf16 a[100:103], a[172:175], v[44:47], 0\n\t" \
  "v_mul_f32 v28, v28, v212\n\t" \
  "v_mul_f32 v29, v29, v212\n\t" \
  "v_mfma_f32_16x16x32_bf16 a[100:103], a[168:171], v[40:43], a[100:103]\n\t" \
  "v_mul_f32 v30, v30, v212\n\t" \
  "v_mul_f32 v31, v31, v212\n\t" \
  "v_mfma_f32_16x16x32_bf16 a[100:103], a[164:167], v[36:39], a[100:103]\n\t" \
  "v_cvt_pk_bf16_f32 v68, v24, v25\n\t" \
  "v_cvt_pk_bf16_f32 v69, v26, v27\n\t" \
  "v_mfma_f32_16x16x32_bf16 a[100:103], a[160:163], v[32:35], a[100:103]\n\t" \
  "v_cvt_pk_bf16_f32 v70, v28, v29\n\t" \
  "v_cvt_pk_bf16_f32 v71, v30, v31\n\t" \
  "v_mfma_f32_16x16x32_bf16 a[116:119], a[176:179], v[32:35], 0\n\t" \
  "s_xor_b32 s98, %[eso], 65536\n\t" \
  "buffer_store_dwordx4 v[68:71], v242, %[ws], s98 offen offset:1024 sc1\n\t" \
  "v_mfma_f32_16x16x32_bf16 a[116:119], a[180:183], v[36:39], a[116:119]\n\t" \
  "v_accvgpr_read_b32 v16, a72\n\t" \
  "v_accvgpr_read_b32 v17, a73\n\t" \
  "v_mfma_f32_16x16x32_bf16 a[116:119], a[184:187], v[40:43], a[116:119]\n\t" \
  "v_accvgpr_read_b32 v18, a74\n\t" \
  "v_accvgpr_read_b32 v19, a75\n\t" \
  "v_mfma_f32_16x16x32_bf16 a[116:119], a[188:191], v[44:47], a[116:119]\n\t" \
  "v_accvgpr_read_b32 v20, a88\n\t" \
  "v_accvgpr_read_b32 v21, a89\n\t" \
  "v_mfma_f32_16x16x32_bf16 a[84:87], v[108:111], v[44:47], 0\n\t" \
  "v_accvgpr_read_b32 v22, a90\n\t" \
  "v_accvgpr_read_b32 v23, a91\n\t" \
  "v_mfma_f32_16x16x32_bf16 a[84:87], v[104:107], v[40:43], a[84:87]\n\t" \
  "v_mul_f32 v16, v16, v212\n\t" \
  "v_mul_f32 v17, v17, v212\n\t" \
  "v_mfma_f32_16x16x32_bf16 a[84:87], v[100:103], v[36:39], a[84:87]\n\t" \
  "v_mul_f32 v18, v18, v212\n\t" \
  "v_mul_f32 v19, v19, v212\n\t" \
  "v_mfma_f32_16x16x32_bf16 a[84:87], v[96:99], v[32:35], a[84:87]\n\t" \
  "v_mul_f32 v20, v20, v212\n\t" \
  "v_mul_f32 v21, v21, v212\n\t" \
  "v_mul_f32 v22, v22, v212\n\t" \
  "v_mul_f32 v23, v23, v212\n\t" \
  "v_cvt_pk_bf16_f32 v72, v16, v17\n\t" \
  "v_cvt_pk_bf16_f32 v73, v18, v19\n\t" \
  "v_cvt_pk_bf16_f32 v74, v20, v21\n\t" \
  "v_cvt_pk_bf16_f32 v75, v22, v23\n\t" \
  "v_accvgpr_read_b32 v24, a104\n\t" \
  "v_accvgpr_read_b32 v25, a105\n\t" \
  "v_accvgpr_read_b32 v26, a106\n\t" \
  "v_accvgpr_read_b32 v27, a107\n\t" \
  "v_accvgpr_read_b32 v28, a120\n\t" \
  "v_accvgpr_read_b32 v29, a121\n\t" \
  "v_accvgpr_read_b32 v30, a122\n\t" \
  "v_accvgpr_read_b32 v31, a123\n\t" \
  "v_mul_f32 v24, v24, v212\n\t" \
  "v_mul_f32 v25, v25, v212\n\t" \
  "v_mul_f32 v26, v26, v212\n\t" \
  "v_mul_f32 v27, v27, v212\n\t" \
  "v_mul_f32 v28, v28, v212\n\t" \
  "v_mul_f32 v29, v29, v212\n\t" \
  "v_mul_f32 v30, v30, v212\n\t" \
  "v_mul_f32 v31, v31, v212\n\t" \
  "v_cvt_pk_bf16_f32 v76, v24, v25\n\t" \
  "v_cvt_pk_bf16_f32 v77, v26, v27\n\t" \
  "v_cvt_pk_bf16_f32 v78, v28, v29\n\t" \
  "v_cvt_pk_bf16_f32 v79, v30, v31\n\t" \
  "v_mfma_f32_16x16x32_bf16 a[40:43], v[48:51], v[64:67], 0\n\t" \
  "s_xor_b32 s98, %[eso], 65536\n\t" \
  "buffer_store_dwordx4 v[72:75], v242, %[ws], s98 offen offset:2048 sc1\n\t" \
  "v_mfma_f32_16x16x32_bf16 a[40:43], v[52:55], v[68:71], a[40:43]\n\t" \
  "s_xor_b32 s98, %[eso], 65536\n\t" \
  "buffer_store_dwordx4 v[76:79], v242, %[ws], s98 offen offset:3072 sc1\n\t" \
  "v_mfma_f32_16x16x32_bf16 a[40:43], v[56:59], v[72:75], a[40:43]\n\t" \
  "buffer_load_dwordx4 v[224:227], v252, %[ws], %[ss] offen sc1\n\t" \
  "buffer_load_dwordx4 v[128:131], v247, %[sM], %[gvo] offen\n\t" \
  "v_mfma_f32_16x16x32_bf16 a[40:43], v[60:63], v[76:79], a[40:43]\n\t" \
  "v_rcp_f32 v214, v172\n\t" \
  "v_mfma_f32_16x16x32_bf16 a[56:59], v[92:95], v[76:79], 0\n\t" \
  "v_accvgpr_read_b32 v16, a12\n\t" \
  "v_accvgpr_read_b32 v17, a13\n\t" \
  "v_mfma_f32_16x16x32_bf16 a[56:59], v[88:91], v[72:75], a[56:59]\n\t" \
  "v_accvgpr_read_b32 v18, a14\n\t" \
  "v_accvgpr_read_b32 v19, a15\n\t" \
  "v_mfma_f32_16x16x32_bf16 a[56:59], v[84:87], v[68:71], a[56:59]\n\t" \
  "v_accvgpr_read_b32 v20, a28\n\t" \
  "v_accvgpr_read_b32 v21, a29\n\t" \
  "v_mfma_f32_16x16x32_bf16 a[56:59], v[80:83], v[64:67], a[56:59]\n\t" \
  "v_accvgpr_read_b32 v22, a30\n\t" \
  "v_accvgpr_read_b32 v23, a31\n\t" \
  "v_mfma_f32_16x16x32_bf16 a[72:75], v[112:115], v[64:67], 0\n\t" \
  "v_mul_f32 v16, v16, v214\n\t" \
  "v_mul_f32 v17, v17, v214\n\t" \
  "v_mul_f32 v18, v18, v214\n\t" \
  "v_mul_f32 v19, v19, v214\n\t" \
  "v_mul_f32 v20, v20, v214\n\t" \
  "v_mul_f32 v21, v21, v214\n\t" \
  "v_mul_f32 v22, v22, v214\n\t" \
  "v_mul_f32 v23, v23, v214\n\t" \
  "v_cvt_pk_bf16_f32 v160, v16, v17\n\t" \
  "v_cvt_pk_bf16_f32 v161, v18, v19\n\t" \
  "v_cvt_pk_bf16_f32 v162, v20, v21\n\t" \
  "v_cvt_pk_bf16_f32 v163, v22, v23\n\t" \
  "s_waitcnt lgkmcnt(4)\n\t" \
  "ds_write_b128 v213, v[160:163] offset:32768\n\t" \
  "v_accvgpr_read_b32 v24, a44\n\t" \
  "v_accvgpr_read_b32 v25, a45\n\t" \
  "v_accvgpr_read_b32 v26, a46\n\t" \
  "v_accvgpr_read_b32 v27, a47\n\t" \
  "v_accvgpr_read_b32 v28, a60\n\t" \
  "v_accvgpr_read_b32 v29, a61\n\t" \
  "v_accvgpr_read_b32 v30, a62\n\t" \
  "v_accvgpr_read_b32 v31, a63\n\t" \
  "v_mul_f32 v24, v24, v214\n\t" \
  "v_mul_f32 v25, v25, v214\n\t" \
  "v_mul_f32 v26, v26, v214\n\t" \
  "v_mul_f32 v27, v27, v214\n\t" \
  "v_mul_f32 v28, v28, v214\n\t" \
  "v_mul_f32 v29, v29, v214\n\t" \
  "v_mul_f32 v30, v30, v214\n\t" \
  "v_mul_f32 v31, v31, v214\n\t" \
  "v_cvt_pk_bf16_f32 v164, v24, v25\n\t" \
  "v_cvt_pk_bf16_f32 v165, v26, v27\n\t" \
  "v_cvt_pk_bf16_f32 v166, v28, v29\n\t" \
  "v_cvt_pk_bf16_f32 v167, v30, v31\n\t" \
  "ds_write_b128 v213, v[164:167] offset:33792\n\t" \
  "v_accvgpr_read_b32 v16, a76\n\t" \
  "v_accvgpr_read_b32 v17, a77\n\t" \
  "v_accvgpr_read_b32 v18, a78\n\t" \
  "v_accvgpr_read_b32 v19, a79\n\t" \
  "v_accvgpr_read_b32 v20, a92\n\t" \
  "v_accvgpr_read_b32 v21, a93\n\t" \
  "v_accvgpr_read_b32 v22, a94\n\t" \
  "v_accvgpr_read_b32 v23, a95\n\t" \
  "v_mul_f32 v16, v16, v214\n\t" \
  "v_mul_f32 v17, v17, v214\n\t" \
  "v_mul_f32 v18, v18, v214\n\t" \
  "v_mul_f32 v19, v19, v214\n\t" \
  "v_mul_f32 v20, v20, v214\n\t" \
  "v_mul_f32 v21, v21, v214\n\t" \
  "v_mul_f32 v22, v22, v214\n\t" \
  "v_mul_f32 v23, v23, v214\n\t" \
  "v_cvt_pk_bf16_f32 v168, v16, v17\n\t" \
  "v_cvt_pk_bf16_f32 v169, v18, v19\n\t" \
  "v_cvt_pk_bf16_f32 v170, v20, v21\n\t" \
  "v_cvt_pk_bf16_f32 v171, v22, v23\n\t" \
  "ds_write_b128 v213, v[168:171] offset:34816\n\t" \
  "v_accvgpr_read_b32 v24, a108\n\t" \
  "v_accvgpr_read_b32 v25, a109\n\t" \
  "v_accvgpr_read_b32 v26, a110\n\t" \
  "v_accvgpr_read_b32 v27, a111\n\t" \
  "v_accvgpr_read_b32 v28, a124\n\t" \
  "v_accvgpr_read_b32 v29, a125\n\t" \
  "v_accvgpr_read_b32 v30, a126\n\t" \
  "v_accvgpr_read_b32 v31, a127\n\t" \
  "v_mul_f32 v24, v24, v214\n\t" \
  "v_mul_f32 v25, v25, v214\n\t" \
  "v_mul_f32 v26, v26, v214\n\t" \
  "v_mul_f32 v27, v27, v214\n\t" \
  "v_mul_f32 v28, v28, v214\n\t" \
  "v_mul_f32 v29, v29, v214\n\t" \
  "v_mul_f32 v30, v30, v214\n\t" \
  "v_mul_f32 v31, v31, v214\n\t" \
  "v_cvt_pk_bf16_f32 v172, v24, v25\n\t" \
  "v_cvt_pk_bf16_f32 v173, v26, v27\n\t" \
  "v_cvt_pk_bf16_f32 v174, v28, v29\n\t" \
  "v_cvt_pk_bf16_f32 v175, v30, v31\n\t" \
  "ds_write_b128 v213, v[172:175] offset:35840\n\t" \
  "v_mfma_f32_16x16x32_bf16 a[72:75], v[116:119], v[68:71], a[72:75]\n\t" \
  "s_add_u32 s98, %[gvo], 12288\n\t" \
  "buffer_load_dwordx4 v[132:135], v247, %[sM], s98 offen\n\t" \
  "v_mfma_f32_16x16x32_bf16 a[72:75], v[120:123], v[72:75], a[72:75]\n\t" \
  "s_add_u32 s98, %[gvo], 24576\n\t" \
  "buffer_load_dwordx4 v[136:139], v247, %[sM], s98 offen\n\t" \
  "v_mfma_f32_16x16x32_bf16 a[72:75], v[124:127], v[76:79], a[72:75]\n\t" \
  "s_add_u32 s98, %[gvo], 36864\n\t" \
  "buffer_load_dwordx4 v[140:143], v247, %[sM], s98 offen\n\t" \
  "s_add_u32 m0, %[m], 147456\n\t" \
  "s_xor_b32 s97, %[wob], 655360\n\t" \
  "buffer_load_dwordx4 v240, %[wo], s97 offen offset:0 lds\n\t" \
  "s_add_u32 s97, s97, 7168\n\t" \
  "buffer_load_dwordx4 v241, %[wo], s97 offen offset:1024 lds\n\t" \
  "s_add_u32 s97, s97, 7168\n\t" \
  "buffer_load_dwordx4 v240, %[wo], s97 offen offset:2048 lds\n\t" \
  "s_add_u32 s97, s97, 7168\n\t" \
  "buffer_load_dwordx4 v241, %[wo], s97 offen offset:3072 lds\n\t" \
  "s_add_u32 s98, %[gvo], 49152\n\t" \
  "buffer_load_dwordx4 v[144:147], v247, %[sM], s98 offen\n\t" \
  "s_add_u32 s98, %[gvo], 61440\n\t" \
  "buffer_load_dwordx4 v[148:151], v247, %[sM], s98 offen\n\t" \
  "s_add_u32 s98, %[gvo], 73728\n\t" \
  "buffer_load_dwordx4 v[152:155], v247, %[sM], s98 offen\n\t" \
  "s_add_u32 s98, %[gvo], 86016\n\t" \
  "buffer_load_dwordx4 v[156:159], v247, %[sM], s98 offen\n\t" \
  "s_waitcnt vmcnt(0)\n\t" \
  "s_branch .Lat_end\n\t" \
  ".Lsf_c00:\n\t" \
  "s_mov_b32 vcc_lo, 0\n\t" \
  "s_branch .Lsf_s0\n\t" \
  ".Lsf_c10:\n\t" \
  "s_mov_b32 vcc_lo, 1\n\t" \
  "s_branch .Lsf_s0\n\t" \
  ".Lsf_c20:\n\t" \
  "s_mov_b32 vcc_lo, 2\n\t" \
  "s_branch .Lsf_s0\n\t" \
  ".Lsf_c30:\n\t" \
  "s_mov_b32 vcc_lo, 3\n\t" \
  "s_branch .Lsf_s0\n\t" \
  ".Lsf_c40:\n\t" \
  "s_mov_b32 vcc_lo, 4\n\t" \
  "s_branch .Lsf_s0\n\t" \
  ".Lsf_c50:\n\t" \
  "s_mov_b32 vcc_lo, 5\n\t" \
  "s_branch .Lsf_s0\n\t" \
  ".Lsf_c60:\n\t" \
  "s_mov_b32 vcc_lo, 6\n\t" \
  "s_branch .Lsf_s0\n\t" \
  ".Lsf_c70:\n\t" \
  "s_mov_b32 vcc_lo, 7\n\t" \
  "s_branch .Lsf_s0\n\t" \
  ".Lsf_s0:\n\t" \
  "s_nop 7\n\t" \
  "s_nop 1\n\t" \
  "v_mov_b32 v193, v192\n\t" \
  "s_nop 1\n\t" \
  "v_permlane32_swap_b32 v192, v193\n\t" \
  "s_nop 1\n\t" \
  "v_max_f32 v192, v192, v193\n\t" \
  "s_nop 1\n\t" \
  "v_mov_b32 v193, v192\n\t" \
  "s_nop 1\n\t" \
  "v_permlane16_swap_b32 v192, v193\n\t" \
  "s_nop 1\n\t" \
  "v_max_f32 v192, v192, v193\n\t" \
  "v_max_f32 v192, 0, v192\n\t" \
  "v_sub_f32 v0, v0, v192\n\t" \
  "v_sub_f32 v1, v1, v192\n\t" \
  "v_sub_f32 v2, v2, v192\n\t" \
  "v_sub_f32 v3, v3, v192\n\t" \
  "v_sub_f32 v4, v4, v192\n\t" \
  "v_sub_f32 v5, v5, v192\n\t" \
  "v_sub_f32 v6, v6, v192\n\t" \
  "v_sub_f32 v7, v7, v192\n\t" \
  "v_sub_f32 v8, v8, v192\n\t" \
  "v_sub_f32 v9, v9, v192\n\t" \
  "v_sub_f32 v10, v10, v192\n\t" \
  "v_sub_f32 v11, v11, v192\n\t" \
  "v_sub_f32 v12, v12, v192\n\t" \
  "v_sub_f32 v13, v13, v192\n\t" \
  "v_sub_f32 v14, v14, v192\n\t" \
  "v_sub_f32 v15, v15, v192\n\t" \
  "v_sub_f32 v176, v176, v192\n\t" \
  "v_sub_f32 v177, v177, v192\n\t" \
  "v_sub_f32 v178, v178, v192\n\t" \
  "v_sub_f32 v179, v179, v192\n\t" \
  "v_exp_f32 v196, -v192\n\t" \
  "s_mov_b32 s99, 1\n\t" \
  "s_nop 1\n\t" \
  "s_cselect_b32 vcc_hi, 1, 0\n\t" \
  "s_cmp_eq_u32 vcc_lo, 0\n\t" \
  "s_cbranch_scc1 .Lsf_b00\n\t" \
  "s_cmp_eq_u32 vcc_lo, 1\n\t" \
  "s_cbranch_scc1 .Lsf_b10\n\t" \
  "s_cmp_eq_u32 vcc_lo, 2\n\t" \
  "s_cbranch_scc1 .Lsf_b20\n\t" \
  "s_cmp_eq_u32 vcc_lo, 3\n\t" \
  "s_cbranch_scc1 .Lsf_b30\n\t" \
  "s_cmp_eq_u32 vcc_lo, 4\n\t" \
  "s_cbranch_scc1 .Lsf_b40\n\t" \
  "s_cmp_eq_u32 vcc_lo, 5\n\t" \
  "s_cbranch_scc1 .Lsf_b50\n\t" \
  "s_cmp_eq_u32 vcc_lo, 6\n\t" \
  "s_cbranch_scc1 .Lsf_b60\n\t" \
  "s_cmp_eq_u32 vcc_lo, 7\n\t" \
  "s_cbranch_scc1 .Lsf_b70\n\t" \
  ".Lsf_b00:\n\t" \
  "s_cmp_lg_u32 vcc_hi, 0\n\t" \
  "s_branch .Lsf_r00\n\t" \
  ".Lsf_b10:\n\t" \
  "s_cmp_lg_u32 vcc_hi, 0\n\t" \
  "s_branch .Lsf_r10\n\t" \
  ".Lsf_b20:\n\t" \
  "s_cmp_lg_u32 vcc_hi, 0\n\t" \
  "s_branch .Lsf_r20\n\t" \
  ".Lsf_b30:\n\t" \
  "s_cmp_lg_u32 vcc_hi, 0\n\t" \
  "s_branch .Lsf_r30\n\t" \
  ".Lsf_b40:\n\t" \
  "s_cmp_lg_u32 vcc_hi, 0\n\t" \
  "s_branch .Lsf_r40\n\t" \
  ".Lsf_b50:\n\t" \
  "s_cmp_lg_u32 vcc_hi, 0\n\t" \
  "s_branch .Lsf_r50\n\t" \
  ".Lsf_b60:\n\t" \
  "s_cmp_lg_u32 vcc_hi, 0\n\t" \
  "s_branch .Lsf_r60\n\t" \
  ".Lsf_b70:\n\t" \
  "s_cmp_lg_u32 vcc_hi, 0\n\t" \
  "s_branch .Lsf_r70\n\t" \
  ".Lsf_c01:\n\t" \
  "s_mov_b32 vcc_lo, 0\n\t" \
  "s_branch .Lsf_s1\n\t" \
  ".Lsf_c11:\n\t" \
  "s_mov_b32 vcc_lo, 1\n\t" \
  "s_branch .Lsf_s1\n\t" \
  ".Lsf_c21:\n\t" \
  "s_mov_b32 vcc_lo, 2\n\t" \
  "s_branch .Lsf_s1\n\t" \
  ".Lsf_c31:\n\t" \
  "s_mov_b32 vcc_lo, 3\n\t" \
  "s_branch .Lsf_s1\n\t" \
  ".Lsf_c41:\n\t" \
  "s_mov_b32 vcc_lo, 4\n\t" \
  "s_branch .Lsf_s1\n\t" \
  ".Lsf_c51:\n\t" \
  "s_mov_b32 vcc_lo, 5\n\t" \
  "s_branch .Lsf_s1\n\t" \
  ".Lsf_c61:\n\t" \
  "s_mov_b32 vcc_lo, 6\n\t" \
  "s_branch .Lsf_s1\n\t" \
  ".Lsf_c71:\n\t" \
  "s_mov_b32 vcc_lo, 7\n\t" \
  "s_branch .Lsf_s1\n\t" \
  ".Lsf_s1:\n\t" \
  "s_nop 7\n\t" \
  "s_nop 1\n\t" \
  "v_mov_b32 v193, v192\n\t" \
  "s_nop 1\n\t" \
  "v_permlane32_swap_b32 v192, v193\n\t" \
  "s_nop 1\n\t" \
  "v_max_f32 v192, v192, v193\n\t" \
  "s_nop 1\n\t" \
  "v_mov_b32 v193, v192\n\t" \
  "s_nop 1\n\t" \
  "v_permlane16_swap_b32 v192, v193\n\t" \
  "s_nop 1\n\t" \
  "v_max_f32 v192, v192, v193\n\t" \
  "v_max_f32 v192, 0, v192\n\t" \
  "v_sub_f32 v32, v32, v192\n\t" \
  "v_sub_f32 v33, v33, v192\n\t" \
  "v_sub_f32 v34, v34, v192\n\t" \
  "v_sub_f32 v35, v35, v192\n\t" \
  "v_sub_f32 v36, v36, v192\n\t" \
  "v_sub_f32 v37, v37, v192\n\t" \
  "v_sub_f32 v38, v38, v192\n\t" \
  "v_sub_f32 v39, v39, v192\n\t" \
  "v_sub_f32 v40, v40, v192\n\t" \
  "v_sub_f32 v41, v41, v192\n\t" \
  "v_sub_f32 v42, v42, v192\n\t" \
  "v_sub_f32 v43, v43, v192\n\t" \
  "v_sub_f32 v44, v44, v192\n\t" \
  "v_sub_f32 v45, v45, v192\n\t" \
  "v_sub_f32 v46, v46, v192\n\t" \
  "v_sub_f32 v47, v47, v192\n\t" \
  "v_sub_f32 v180, v180, v192\n\t" \
  "v_sub_f32 v181, v181, v192\n\t" \
  "v_sub_f32 v182, v182, v192\n\t" \
  "v_sub_f32 v183, v183, v192\n\t" \
  "v_exp_f32 v197, -v192\n\t" \
  "s_mov_b32 s99, 1\n\t" \
  "s_nop 1\n\t" \
  "s_cselect_b32 vcc_hi, 1, 0\n\t" \
  "s_cmp_eq_u32 vcc_lo, 0\n\t" \
  "s_cbranch_scc1 .Lsf_b01\n\t" \
  "s_cmp_eq_u32 vcc_lo, 1\n\t" \
  "s_cbranch_scc1 .Lsf_b11\n\t" \
  "s_cmp_eq_u32 vcc_lo, 2\n\t" \
  "s_cbranch_scc1 .Lsf_b21\n\t" \
  "s_cmp_eq_u32 vcc_lo, 3\n\t" \
  "s_cbranch_scc1 .Lsf_b31\n\t" \
  "s_cmp_eq_u32 vcc_lo, 4\n\t" \
  "s_cbranch_scc1 .Lsf_b41\n\t" \
  "s_cmp_eq_u32 vcc_lo, 5\n\t" \
  "s_cbranch_scc1 .Lsf_b51\n\t" \
  "s_cmp_eq_u32 vcc_lo, 6\n\t" \
  "s_cbranch_scc1 .Lsf_b61\n\t" \
  "s_cmp_eq_u32 vcc_lo, 7\n\t" \
  "s_cbranch_scc1 .Lsf_b71\n\t" \
  ".Lsf_b01:\n\t" \
  "s_cmp_lg_u32 vcc_hi, 0\n\t" \
  "s_branch .Lsf_r01\n\t" \
  ".Lsf_b11:\n\t" \
  "s_cmp_lg_u32 vcc_hi, 0\n\t" \
  "s_branch .Lsf_r11\n\t" \
  ".Lsf_b21:\n\t" \
  "s_cmp_lg_u32 vcc_hi, 0\n\t" \
  "s_branch .Lsf_r21\n\t" \
  ".Lsf_b31:\n\t" \
  "s_cmp_lg_u32 vcc_hi, 0\n\t" \
  "s_branch .Lsf_r31\n\t" \
  ".Lsf_b41:\n\t" \
  "s_cmp_lg_u32 vcc_hi, 0\n\t" \
  "s_branch .Lsf_r41\n\t" \
  ".Lsf_b51:\n\t" \
  "s_cmp_lg_u32 vcc_hi, 0\n\t" \
  "s_branch .Lsf_r51\n\t" \
  ".Lsf_b61:\n\t" \
  "s_cmp_lg_u32 vcc_hi, 0\n\t" \
  "s_branch .Lsf_r61\n\t" \
  ".Lsf_b71:\n\t" \
  "s_cmp_lg_u32 vcc_hi, 0\n\t" \
  "s_branch .Lsf_r71\n\t" \
  ".Lsf_c02:\n\t" \
  "s_mov_b32 vcc_lo, 0\n\t" \
  "s_branch .Lsf_s2\n\t" \
  ".Lsf_c12:\n\t" \
  "s_mov_b32 vcc_lo, 1\n\t" \
  "s_branch .Lsf_s2\n\t" \
  ".Lsf_c22:\n\t" \
  "s_mov_b32 vcc_lo, 2\n\t" \
  "s_branch .Lsf_s2\n\t" \
  ".Lsf_c32:\n\t" \
  "s_mov_b32 vcc_lo, 3\n\t" \
  "s_branch .Lsf_s2\n\t" \
  ".Lsf_c42:\n\t" \
  "s_mov_b32 vcc_lo, 4\n\t" \
  "s_branch .Lsf_s2\n\t" \
  ".Lsf_c52:\n\t" \
  "s_mov_b32 vcc_lo, 5\n\t" \
  "s_branch .Lsf_s2\n\t" \
  ".Lsf_c62:\n\t" \
  "s_mov_b32 vcc_lo, 6\n\t" \
  "s_branch .Lsf_s2\n\t" \
  ".Lsf_c72:\n\t" \
  "s_mov_b32 vcc_lo, 7\n\t" \
  "s_branch .Lsf_s2\n\t" \
  ".Lsf_s2:\n\t" \
  "s_nop 7\n\t" \
  "s_nop 1\n\t" \
  "v_mov_b32 v193, v192\n\t" \
  "s_nop 1\n\t" \
  "v_permlane32_swap_b32 v192, v193\n\t" \
  "s_nop 1\n\t" \
  "v_max_f32 v192, v192, v193\n\t" \
  "s_nop 1\n\t" \
  "v_mov_b32 v193, v192\n\t" \
  "s_nop 1\n\t" \
  "v_permlane16_swap_b32 v192, v193\n\t" \
  "s_nop 1\n\t" \
  "v_max_f32 v192, v192, v193\n\t" \
  "v_max_f32 v192, 0, v192\n\t" \
  "v_sub_f32 v64, v64, v192\n\t" \
  "v_sub_f32 v65, v65, v192\n\t" \
  "v_sub_f32 v66, v66, v192\n\t" \
  "v_sub_f32 v67, v67, v192\n\t" \
  "v_sub_f32 v68, v68, v192\n\t" \
  "v_sub_f32 v69, v69, v192\n\t" \
  "v_sub_f32 v70, v70, v192\n\t" \
  "v_sub_f32 v71, v71, v192\n\t" \
  "v_sub_f32 v72, v72, v192\n\t" \
  "v_sub_f32 v73, v73, v192\n\t" \
  "v_sub_f32 v74, v74, v192\n\t" \
  "v_sub_f32 v75, v75, v192\n\t" \
  "v_sub_f32 v76, v76, v192\n\t" \
  "v_sub_f32 v77, v77, v192\n\t" \
  "v_sub_f32 v78, v78, v192\n\t" \
  "v_sub_f32 v79, v79, v192\n\t" \
  "v_sub_f32 v184, v184, v192\n\t" \
  "v_sub_f32 v185, v185, v192\n\t" \
  "v_sub_f32 v186, v186, v192\n\t" \
  "v_sub_f32 v187, v187, v192\n\t" \
  "v_exp_f32 v198, -v192\n\t" \
  "s_mov_b32 s99, 1\n\t" \
  "s_nop 1\n\t" \
  "s_cselect_b32 vcc_hi, 1, 0\n\t" \
  "s_cmp_eq_u32 vcc_lo, 0\n\t" \
  "s_cbranch_scc1 .Lsf_b02\n\t" \
  "s_cmp_eq_u32 vcc_lo, 1\n\t" \
  "s_cbranch_scc1 .Lsf_b12\n\t" \
  "s_cmp_eq_u32 vcc_lo, 2\n\t" \
  "s_cbranch_scc1 .Lsf_b22\n\t" \
  "s_cmp_eq_u32 vcc_lo, 3\n\t" \
  "s_cbranch_scc1 .Lsf_b32\n\t" \
  "s_cmp_eq_u32 vcc_lo, 4\n\t" \
  "s_cbranch_scc1 .Lsf_b42\n\t" \
  "s_cmp_eq_u32 vcc_lo, 5\n\t" \
  "s_cbranch_scc1 .Lsf_b52\n\t" \
  "s_cmp_eq_u32 vcc_lo, 6\n\t" \
  "s_cbranch_scc1 .Lsf_b62\n\t" \
  "s_cmp_eq_u32 vcc_lo, 7\n\t" \
  "s_cbranch_scc1 .Lsf_b72\n\t" \
  ".Lsf_b02:\n\t" \
  "s_cmp_lg_u32 vcc_hi, 0\n\t" \
  "s_branch .Lsf_r02\n\t" \
  ".Lsf_b12:\n\t" \
  "s_cmp_lg_u32 vcc_hi, 0\n\t" \
  "s_branch .Lsf_r12\n\t" \
  ".Lsf_b22:\n\t" \
  "s_cmp_lg_u32 vcc_hi, 0\n\t" \
  "s_branch .Lsf_r22\n\t" \
  ".Lsf_b32:\n\t" \
  "s_cmp_lg_u32 vcc_hi, 0\n\t" \
  "s_branch .Lsf_r32\n\t" \
  ".Lsf_b42:\n\t" \
  "s_cmp_lg_u32 vcc_hi, 0\n\t" \
  "s_branch .Lsf_r42\n\t" \
  ".Lsf_b52:\n\t" \
  "s_cmp_lg_u32 vcc_hi, 0\n\t" \
  "s_branch .Lsf_r52\n\t" \
  ".Lsf_b62:\n\t" \
  "s_cmp_lg_u32 vcc_hi, 0\n\t" \
  "s_branch .Lsf_r62\n\t" \
  ".Lsf_b72:\n\t" \
  "s_cmp_lg_u32 vcc_hi, 0\n\t" \
  "s_branch .Lsf_r72\n\t" \
  ".Lsf_c03:\n\t" \
  "s_mov_b32 vcc_lo, 0\n\t" \
  "s_branch .Lsf_s3\n\t" \
  ".Lsf_c13:\n\t" \
  "s_mov_b32 vcc_lo, 1\n\t" \
  "s_branch .Lsf_s3\n\t" \
  ".Lsf_c23:\n\t" \
  "s_mov_b32 vcc_lo, 2\n\t" \
  "s_branch .Lsf_s3\n\t" \
  ".Lsf_c33:\n\t" \
  "s_mov_b32 vcc_lo, 3\n\t" \
  "s_branch .Lsf_s3\n\t" \
  ".Lsf_c43:\n\t" \
  "s_mov_b32 vcc_lo, 4\n\t" \
  "s_branch .Lsf_s3\n\t" \
  ".Lsf_c53:\n\t" \
  "s_mov_b32 vcc_lo, 5\n\t" \
  "s_branch .Lsf_s3\n\t" \
  ".Lsf_c63:\n\t" \
  "s_mov_b32 vcc_lo, 6\n\t" \
  "s_branch .Lsf_s3\n\t" \
  ".Lsf_c73:\n\t" \
  "s_mov_b32 vcc_lo, 7\n\t" \
  "s_branch .Lsf_s3\n\t" \
  ".Lsf_s3:\n\t" \
  "s_nop 7\n\t" \
  "s_nop 1\n\t" \
  "v_mov_b32 v193, v192\n\t" \
  "s_nop 1\n\t" \
  "v_permlane32_swap_b32 v192, v193\n\t" \
  "s_nop 1\n\t" \
  "v_max_f32 v192, v192, v193\n\t" \
  "s_nop 1\n\t" \
  "v_mov_b32 v193, v192\n\t" \
  "s_nop 1\n\t" \
  "v_permlane16_swap_b32 v192, v193\n\t" \
  "s_nop 1\n\t" \
  "v_max_f32 v192, v192, v193\n\t" \
  "v_max_f32 v192, 0, v192\n\t" \
  "v_sub_f32 v96, v96, v192\n\t" \
  "v_sub_f32 v97, v97, v192\n\t" \
  "v_sub_f32 v98, v98, v192\n\t" \
  "v_sub_f32 v99, v99, v192\n\t" \
  "v_sub_f32 v100, v100, v192\n\t" \
  "v_sub_f32 v101, v101, v192\n\t" \
  "v_sub_f32 v102, v102, v192\n\t" \
  "v_sub_f32 v103, v103, v192\n\t" \
  "v_sub_f32 v104, v104, v192\n\t" \
  "v_sub_f32 v105, v105, v192\n\t" \
  "v_sub_f32 v106, v106, v192\n\t" \
  "v_sub_f32 v107, v107, v192\n\t" \
  "v_sub_f32 v108, v108, v192\n\t" \
  "v_sub_f32 v109, v109, v192\n\t" \
  "v_sub_f32 v110, v110, v192\n\t" \
  "v_sub_f32 v111, v111, v192\n\t" \
  "v_sub_f32 v188, v188, v192\n\t" \
  "v_sub_f32 v189, v189, v192\n\t" \
  "v_sub_f32 v190, v190, v192\n\t" \
  "v_sub_f32 v191, v191, v192\n\t" \
  "v_exp_f32 v199, -v192\n\t" \
  "s_mov_b32 s99, 1\n\t" \
  "s_nop 1\n\t" \
  "s_cselect_b32 vcc_hi, 1, 0\n\t" \
  "s_cmp_eq_u32 vcc_lo, 0\n\t" \
  "s_cbranch_scc1 .Lsf_b03\n\t" \
  "s_cmp_eq_u32 vcc_lo, 1\n\t" \
  "s_cbranch_scc1 .Lsf_b13\n\t" \
  "s_cmp_eq_u32 vcc_lo, 2\n\t" \
  "s_cbranch_scc1 .Lsf_b23\n\t" \
  "s_cmp_eq_u32 vcc_lo, 3\n\t" \
  "s_cbranch_scc1 .Lsf_b33\n\t" \
  "s_cmp_eq_u32 vcc_lo, 4\n\t" \
  "s_cbranch_scc1 .Lsf_b43\n\t" \
  "s_cmp_eq_u32 vcc_lo, 5\n\t" \
  "s_cbranch_scc1 .Lsf_b53\n\t" \
  "s_cmp_eq_u32 vcc_lo, 6\n\t" \
  "s_cbranch_scc1 .Lsf_b63\n\t" \
  "s_cmp_eq_u32 vcc_lo, 7\n\t" \
  "s_cbranch_scc1 .Lsf_b73\n\t" \
  ".Lsf_b03:\n\t" \
  "s_cmp_lg_u32 vcc_hi, 0\n\t" \
  "s_branch .Lsf_r03\n\t" \
  ".Lsf_b13:\n\t" \
  "s_cmp_lg_u32 vcc_hi, 0\n\t" \
  "s_branch .Lsf_r13\n\t" \
  ".Lsf_b23:\n\t" \
  "s_cmp_lg_u32 vcc_hi, 0\n\t" \
  "s_branch .Lsf_r23\n\t" \
  ".Lsf_b33:\n\t" \
  "s_cmp_lg_u32 vcc_hi, 0\n\t" \
  "s_branch .Lsf_r33\n\t" \
  ".Lsf_b43:\n\t" \
  "s_cmp_lg_u32 vcc_hi, 0\n\t" \
  "s_branch .Lsf_r43\n\t" \
  ".Lsf_b53:\n\t" \
  "s_cmp_lg_u32 vcc_hi, 0\n\t" \
  "s_branch .Lsf_r53\n\t" \
  ".Lsf_b63:\n\t" \
  "s_cmp_lg_u32 vcc_hi, 0\n\t" \
  "s_branch .Lsf_r63\n\t" \
  ".Lsf_b73:\n\t" \
  "s_cmp_lg_u32 vcc_hi, 0\n\t" \
  "s_branch .Lsf_r73\n\t" \
  ".Lsf_p0:\n\t" \
  "s_mov_b32 vcc_lo, 0\n\t" \
  "s_branch .Lsf_rs\n\t" \
  ".Lsf_p1:\n\t" \
  "s_mov_b32 vcc_lo, 1\n\t" \
  "s_branch .Lsf_rs\n\t" \
  ".Lsf_p2:\n\t" \
  "s_mov_b32 vcc_lo, 2\n\t" \
  "s_branch .Lsf_rs\n\t" \
  ".Lsf_p3:\n\t" \
  "s_mov_b32 vcc_lo, 3\n\t" \
  "s_branch .Lsf_rs\n\t" \
  ".Lsf_p4:\n\t" \
  "s_mov_b32 vcc_lo, 4\n\t" \
  "s_branch .Lsf_rs\n\t" \
  ".Lsf_p5:\n\t" \
  "s_mov_b32 vcc_lo, 5\n\t" \
  "s_branch .Lsf_rs\n\t" \
  ".Lsf_p6:\n\t" \
  "s_mov_b32 vcc_lo, 6\n\t" \
  "s_branch .Lsf_rs\n\t" \
  ".Lsf_p7:\n\t" \
  "s_mov_b32 vcc_lo, 7\n\t" \
  "s_branch .Lsf_rs\n\t" \
  ".Lsf_rs:\n\t" \
  "s_nop 7\n\t" \
  "s_nop 7\n\t" \
  "v_accvgpr_read_b32 v193, a0\n\t" \
  "v_mul_f32 v193, v196, v193\n\t" \
  "v_accvgpr_write_b32 a0, v193\n\t" \
  "v_accvgpr_read_b32 v193, a1\n\t" \
  "v_mul_f32 v193, v196, v193\n\t" \
  "v_accvgpr_write_b32 a1, v193\n\t" \
  "v_accvgpr_read_b32 v193, a2\n\t" \
  "v_mul_f32 v193, v196, v193\n\t" \
  "v_accvgpr_write_b32 a2, v193\n\t" \
  "v_accvgpr_read_b32 v193, a3\n\t" \
  "v_mul_f32 v193, v196, v193\n\t" \
  "v_accvgpr_write_b32 a3, v193\n\t" \
  "v_accvgpr_read_b32 v193, a4\n\t" \
  "v_mul_f32 v193, v197, v193\n\t" \
  "v_accvgpr_write_b32 a4, v193\n\t" \
  "v_accvgpr_read_b32 v193, a5\n\t" \
  "v_mul_f32 v193, v197, v193\n\t" \
  "v_accvgpr_write_b32 a5, v193\n\t" \
  "v_accvgpr_read_b32 v193, a6\n\t" \
  "v_mul_f32 v193, v197, v193\n\t" \
  "v_accvgpr_write_b32 a6, v193\n\t" \
  "v_accvgpr_read_b32 v193, a7\n\t" \
  "v_mul_f32 v193, v197, v193\n\t" \
  "v_accvgpr_write_b32 a7, v193\n\t" \
  "v_accvgpr_read_b32 v193, a8\n\t" \
  "v_mul_f32 v193, v198, v193\n\t" \
  "v_accvgpr_write_b32 a8, v193\n\t" \
  "v_accvgpr_read_b32 v193, a9\n\t" \
  "v_mul_f32 v193, v198, v193\n\t" \
  "v_accvgpr_write_b32 a9, v193\n\t" \
  "v_accvgpr_read_b32 v193, a10\n\t" \
  "v_mul_f32 v193, v198, v193\n\t" \
  "v_accvgpr_write_b32 a10, v193\n\t" \
  "v_accvgpr_read_b32 v193, a11\n\t" \
  "v_mul_f32 v193, v198, v193\n\t" \
  "v_accvgpr_write_b32 a11, v193\n\t" \
  "v_accvgpr_read_b32 v193, a12\n\t" \
  "v_mul_f32 v193, v199, v193\n\t" \
  "v_accvgpr_write_b32 a12, v193\n\t" \
  "v_accvgpr_read_b32 v193, a13\n\t" \
  "v_mul_f32 v193, v199, v193\n\t" \
  "v_accvgpr_write_b32 a13, v193\n\t" \
  "v_accvgpr_read_b32 v193, a14\n\t" \
  "v_mul_f32 v193, v199, v193\n\t" \
  "v_accvgpr_write_b32 a14, v193\n\t" \
  "v_accvgpr_read_b32 v193, a15\n\t" \
  "v_mul_f32 v193, v199, v193\n\t" \
  "v_accvgpr_write_b32 a15, v193\n\t" \
  "v_accvgpr_read_b32 v193, a16\n\t" \
  "v_mul_f32 v193, v196, v193\n\t" \
  "v_accvgpr_write_b32 a16, v193\n\t" \
  "v_accvgpr_read_b32 v193, a17\n\t" \
  "v_mul_f32 v193, v196, v193\n\t" \
  "v_accvgpr_write_b32 a17, v193\n\t" \
  "v_accvgpr_read_b32 v193, a18\n\t" \
  "v_mul_f32 v193, v196, v193\n\t" \
  "v_accvgpr_write_b32 a18, v193\n\t" \
  "v_accvgpr_read_b32 v193, a19\n\t" \
  "v_mul_f32 v193, v196, v193\n\t" \
  "v_accvgpr_write_b32 a19, v193\n\t" \
  "v_accvgpr_read_b32 v193, a20\n\t" \
  "v_mul_f32 v193, v197, v193\n\t" \
  "v_accvgpr_write_b32 a20, v193\n\t" \
  "v_accvgpr_read_b32 v193, a21\n\t" \
  "v_mul_f32 v193, v197, v193\n\t" \
  "v_accvgpr_write_b32 a21, v193\n\t" \
  "v_accvgpr_read_b32 v193, a22\n\t" \
  "v_mul_f32 v193, v197, v193\n\t" \
  "v_accvgpr_write_b32 a22, v193\n\t" \
  "v_accvgpr_read_b32 v193, a23\n\t" \
  "v_mul_f32 v193, v197, v193\n\t" \
  "v_accvgpr_write_b32 a23, v193\n\t" \
  "v_accvgpr_read_b32 v193, a24\n\t" \
  "v_mul_f32 v193, v198, v193\n\t" \
  "v_accvgpr_write_b32 a24, v193\n\t" \
  "v_accvgpr_read_b32 v193, a25\n\t" \
  "v_mul_f32 v193, v198, v193\n\t" \
  "v_accvgpr_write_b32 a25, v193\n\t" \
  "v_accvgpr_read_b32 v193, a26\n\t" \
  "v_mul_f32 v193, v198, v193\n\t" \
  "v_accvgpr_write_b32 a26, v193\n\t" \
  "v_accvgpr_read_b32 v193, a27\n\t" \
  "v_mul_f32 v193, v198, v193\n\t" \
  "v_accvgpr_write_b32 a27, v193\n\t" \
  "v_accvgpr_read_b32 v193, a28\n\t" \
  "v_mul_f32 v193, v199, v193\n\t" \
  "v_accvgpr_write_b32 a28, v193\n\t" \
  "v_accvgpr_read_b32 v193, a29\n\t" \
  "v_mul_f32 v193, v199, v193\n\t" \
  "v_accvgpr_write_b32 a29, v193\n\t" \
  "v_accvgpr_read_b32 v193, a30\n\t" \
  "v_mul_f32 v193, v199, v193\n\t" \
  "v_accvgpr_write_b32 a30, v193\n\t" \
  "v_accvgpr_read_b32 v193, a31\n\t" \
  "v_mul_f32 v193, v199, v193\n\t" \
  "v_accvgpr_write_b32 a31, v193\n\t" \
  "v_accvgpr_read_b32 v193, a32\n\t" \
  "v_mul_f32 v193, v196, v193\n\t" \
  "v_accvgpr_write_b32 a32, v193\n\t" \
  "v_accvgpr_read_b32 v193, a33\n\t" \
  "v_mul_f32 v193, v196, v193\n\t" \
  "v_accvgpr_write_b32 a33, v193\n\t" \
  "v_accvgpr_read_b32 v193, a34\n\t" \
  "v_mul_f32 v193, v196, v193\n\t" \
  "v_accvgpr_write_b32 a34, v193\n\t" \
  "v_accvgpr_read_b32 v193, a35\n\t" \
  "v_mul_f32 v193, v196, v193\n\t" \
  "v_accvgpr_write_b32 a35, v193\n\t" \
  "v_accvgpr_read_b32 v193, a36\n\t" \
  "v_mul_f32 v193, v197, v193\n\t" \
  "v_accvgpr_write_b32 a36, v193\n\t" \
  "v_accvgpr_read_b32 v193, a37\n\t" \
  "v_mul_f32 v193, v197, v193\n\t" \
  "v_accvgpr_write_b32 a37, v193\n\t" \
  "v_accvgpr_read_b32 v193, a38\n\t" \
  "v_mul_f32 v193, v197, v193\n\t" \
  "v_accvgpr_write_b32 a38, v193\n\t" \
  "v_accvgpr_read_b32 v193, a39\n\t" \
  "v_mul_f32 v193, v197, v193\n\t" \
  "v_accvgpr_write_b32 a39, v193\n\t" \
  "v_accvgpr_read_b32 v193, a40\n\t" \
  "v_mul_f32 v193, v198, v193\n\t" \
  "v_accvgpr_write_b32 a40, v193\n\t" \
  "v_accvgpr_read_b32 v193, a41\n\t" \
  "v_mul_f32 v193, v198, v193\n\t" \
  "v_accvgpr_write_b32 a41, v193\n\t" \
  "v_accvgpr_read_b32 v193, a42\n\t" \
  "v_mul_f32 v193, v198, v193\n\t" \
  "v_accvgpr_write_b32 a42, v193\n\t" \
  "v_accvgpr_read_b32 v193, a43\n\t" \
  "v_mul_f32 v193, v198, v193\n\t" \
  "v_accvgpr_write_b32 a43, v193\n\t" \
  "v_accvgpr_read_b32 v193, a44\n\t" \
  "v_mul_f32 v193, v199, v193\n\t" \
  "v_accvgpr_write_b32 a44, v193\n\t" \
  "v_accvgpr_read_b32 v193, a45\n\t" \
  "v_mul_f32 v193, v199, v193\n\t" \
  "v_accvgpr_write_b32 a45, v193\n\t" \
  "v_accvgpr_read_b32 v193, a46\n\t" \
  "v_mul_f32 v193, v199, v193\n\t" \
  "v_accvgpr_write_b32 a46, v193\n\t" \
  "v_accvgpr_read_b32 v193, a47\n\t" \
  "v_mul_f32 v193, v199, v193\n\t" \
  "v_accvgpr_write_b32 a47, v193\n\t" \
  "v_accvgpr_read_b32 v193, a48\n\t" \
  "v_mul_f32 v193, v196, v193\n\t" \
  "v_accvgpr_write_b32 a48, v193\n\t" \
  "v_accvgpr_read_b32 v193, a49\n\t" \
  "v_mul_f32 v193, v196, v193\n\t" \
  "v_accvgpr_write_b32 a49, v193\n\t" \
  "v_accvgpr_read_b32 v193, a50\n\t" \
  "v_mul_f32 v193, v196, v193\n\t" \
  "v_accvgpr_write_b32 a50, v193\n\t" \
  "v_accvgpr_read_b32 v193, a51\n\t" \
  "v_mul_f32 v193, v196, v193\n\t" \
  "v_accvgpr_write_b32 a51, v193\n\t" \
  "v_accvgpr_read_b32 v193, a52\n\t" \
  "v_mul_f32 v193, v197, v193\n\t" \
  "v_accvgpr_write_b32 a52, v193\n\t" \
  "v_accvgpr_read_b32 v193, a53\n\t" \
  "v_mul_f32 v193, v197, v193\n\t" \
  "v_accvgpr_write_b32 a53, v193\n\t" \
  "v_accvgpr_read_b32 v193, a54\n\t" \
  "v_mul_f32 v193, v197, v193\n\t" \
  "v_accvgpr_write_b32 a54, v193\n\t" \
  "v_accvgpr_read_b32 v193, a55\n\t" \
  "v_mul_f32 v193, v197, v193\n\t" \
  "v_accvgpr_write_b32 a55, v193\n\t" \
  "v_accvgpr_read_b32 v193, a56\n\t" \
  "v_mul_f32 v193, v198, v193\n\t" \
  "v_accvgpr_write_b32 a56, v193\n\t" \
  "v_accvgpr_read_b32 v193, a57\n\t" \
  "v_mul_f32 v193, v198, v193\n\t" \
  "v_accvgpr_write_b32 a57, v193\n\t" \
  "v_accvgpr_read_b32 v193, a58\n\t" \
  "v_mul_f32 v193, v198, v193\n\t" \
  "v_accvgpr_write_b32 a58, v193\n\t" \
  "v_accvgpr_read_b32 v193, a59\n\t" \
  "v_mul_f32 v193, v198, v193\n\t" \
  "v_accvgpr_write_b32 a59, v193\n\t" \
  "v_accvgpr_read_b32 v193, a60\n\t" \
  "v_mul_f32 v193, v199, v193\n\t" \
  "v_accvgpr_write_b32 a60, v193\n\t" \
  "v_accvgpr_read_b32 v193, a61\n\t" \
  "v_mul_f32 v193, v199, v193\n\t" \
  "v_accvgpr_write_b32 a61, v193\n\t" \
  "v_accvgpr_read_b32 v193, a62\n\t" \
  "v_mul_f32 v193, v199, v193\n\t" \
  "v_accvgpr_write_b32 a62, v193\n\t" \
  "v_accvgpr_read_b32 v193, a63\n\t" \
  "v_mul_f32 v193, v199, v193\n\t" \
  "v_accvgpr_write_b32 a63, v193\n\t" \
  "v_accvgpr_read_b32 v193, a64\n\t" \
  "v_mul_f32 v193, v196, v193\n\t" \
  "v_accvgpr_write_b32 a64, v193\n\t" \
  "v_accvgpr_read_b32 v193, a65\n\t" \
  "v_mul_f32 v193, v196, v193\n\t" \
  "v_accvgpr_write_b32 a65, v193\n\t" \
  "v_accvgpr_read_b32 v193, a66\n\t" \
  "v_mul_f32 v193, v196, v193\n\t" \
  "v_accvgpr_write_b32 a66, v193\n\t" \
  "v_accvgpr_read_b32 v193, a67\n\t" \
  "v_mul_f32 v193, v196, v193\n\t" \
  "v_accvgpr_write_b32 a67, v193\n\t" \
  "v_accvgpr_read_b32 v193, a68\n\t" \
  "v_mul_f32 v193, v197, v193\n\t" \
  "v_accvgpr_write_b32 a68, v193\n\t" \
  "v_accvgpr_read_b32 v193, a69\n\t" \
  "v_mul_f32 v193, v197, v193\n\t" \
  "v_accvgpr_write_b32 a69, v193\n\t" \
  "v_accvgpr_read_b32 v193, a70\n\t" \
  "v_mul_f32 v193, v197, v193\n\t" \
  "v_accvgpr_write_b32 a70, v193\n\t" \
  "v_accvgpr_read_b32 v193, a71\n\t" \
  "v_mul_f32 v193, v197, v193\n\t" \
  "v_accvgpr_write_b32 a71, v193\n\t" \
  "v_accvgpr_read_b32 v193, a72\n\t" \
  "v_mul_f32 v193, v198, v193\n\t" \
  "v_accvgpr_write_b32 a72, v193\n\t" \
  "v_accvgpr_read_b32 v193, a73\n\t" \
  "v_mul_f32 v193, v198, v193\n\t" \
  "v_accvgpr_write_b32 a73, v193\n\t" \
  "v_accvgpr_read_b32 v193, a74\n\t" \
  "v_mul_f32 v193, v198, v193\n\t" \
  "v_accvgpr_write_b32 a74, v193\n\t" \
  "v_accvgpr_read_b32 v193, a75\n\t" \
  "v_mul_f32 v193, v198, v193\n\t" \
  "v_accvgpr_write_b32 a75, v193\n\t" \
  "v_accvgpr_read_b32 v193, a76\n\t" \
  "v_mul_f32 v193, v199, v193\n\t" \
  "v_accvgpr_write_b32 a76, v193\n\t" \
  "v_accvgpr_read_b32 v193, a77\n\t" \
  "v_mul_f32 v193, v199, v193\n\t" \
  "v_accvgpr_write_b32 a77, v193\n\t" \
  "v_accvgpr_read_b32 v193, a78\n\t" \
  "v_mul_f32 v193, v199, v193\n\t" \
  "v_accvgpr_write_b32 a78, v193\n\t" \
  "v_accvgpr_read_b32 v193, a79\n\t" \
  "v_mul_f32 v193, v199, v193\n\t" \
  "v_accvgpr_write_b32 a79, v193\n\t" \
  "v_accvgpr_read_b32 v193, a80\n\t" \
  "v_mul_f32 v193, v196, v193\n\t" \
  "v_accvgpr_write_b32 a80, v193\n\t" \
  "v_accvgpr_read_b32 v193, a81\n\t" \
  "v_mul_f32 v193, v196, v193\n\t" \
  "v_accvgpr_write_b32 a81, v193\n\t" \
  "v_accvgpr_read_b32 v193, a82\n\t" \
  "v_mul_f32 v193, v196, v193\n\t" \
  "v_accvgpr_write_b32 a82, v193\n\t" \
  "v_accvgpr_read_b32 v193, a83\n\t" \
  "v_mul_f32 v193, v196, v193\n\t" \
  "v_accvgpr_write_b32 a83, v193\n\t" \
  "v_accvgpr_read_b32 v193, a84\n\t" \
  "v_mul_f32 v193, v197, v193\n\t" \
  "v_accvgpr_write_b32 a84, v193\n\t" \
  "v_accvgpr_read_b32 v193, a85\n\t" \
  "v_mul_f32 v193, v197, v193\n\t" \
  "v_accvgpr_write_b32 a85, v193\n\t" \
  "v_accvgpr_read_b32 v193, a86\n\t" \
  "v_mul_f32 v193, v197, v193\n\t" \
  "v_accvgpr_write_b32 a86, v193\n\t" \
  "v_accvgpr_read_b32 v193, a87\n\t" \
  "v_mul_f32 v193, v197, v193\n\t" \
  "v_accvgpr_write_b32 a87, v193\n\t" \
  "v_accvgpr_read_b32 v193, a88\n\t" \
  "v_mul_f32 v193, v198, v193\n\t" \
  "v_accvgpr_write_b32 a88, v193\n\t" \
  "v_accvgpr_read_b32 v193, a89\n\t" \
  "v_mul_f32 v193, v198, v193\n\t" \
  "v_accvgpr_write_b32 a89, v193\n\t" \
  "v_accvgpr_read_b32 v193, a90\n\t" \
  "v_mul_f32 v193, v198, v193\n\t" \
  "v_accvgpr_write_b32 a90, v193\n\t" \
  "v_accvgpr_read_b32 v193, a91\n\t" \
  "v_mul_f32 v193, v198, v193\n\t" \
  "v_accvgpr_write_b32 a91, v193\n\t" \
  "v_accvgpr_read_b32 v193, a92\n\t" \
  "v_mul_f32 v193, v199, v193\n\t" \
  "v_accvgpr_write_b32 a92, v193\n\t" \
  "v_accvgpr_read_b32 v193, a93\n\t" \
  "v_mul_f32 v193, v199, v193\n\t" \
  "v_accvgpr_write_b32 a93, v193\n\t" \
  "v_accvgpr_read_b32 v193, a94\n\t" \
  "v_mul_f32 v193, v199, v193\n\t" \
  "v_accvgpr_write_b32 a94, v193\n\t" \
  "v_accvgpr_read_b32 v193, a95\n\t" \
  "v_mul_f32 v193, v199, v193\n\t" \
  "v_accvgpr_write_b32 a95, v193\n\t" \
  "v_accvgpr_read_b32 v193, a96\n\t" \
  "v_mul_f32 v193, v196, v193\n\t" \
  "v_accvgpr_write_b32 a96, v193\n\t" \
  "v_accvgpr_read_b32 v193, a97\n\t" \
  "v_mul_f32 v193, v196, v193\n\t" \
  "v_accvgpr_write_b32 a97, v193\n\t" \
  "v_accvgpr_read_b32 v193, a98\n\t" \
  "v_mul_f32 v193, v196, v193\n\t" \
  "v_accvgpr_write_b32 a98, v193\n\t" \
  "v_accvgpr_read_b32 v193, a99\n\t" \
  "v_mul_f32 v193, v196, v193\n\t" \
  "v_accvgpr_write_b32 a99, v193\n\t" \
  "v_accvgpr_read_b32 v193, a100\n\t" \
  "v_mul_f32 v193, v197, v193\n\t" \
  "v_accvgpr_write_b32 a100, v193\n\t" \
  "v_accvgpr_read_b32 v193, a101\n\t" \
  "v_mul_f32 v193, v197, v193\n\t" \
  "v_accvgpr_write_b32 a101, v193\n\t" \
  "v_accvgpr_read_b32 v193, a102\n\t" \
  "v_mul_f32 v193, v197, v193\n\t" \
  "v_accvgpr_write_b32 a102, v193\n\t" \
  "v_accvgpr_read_b32 v193, a103\n\t" \
  "v_mul_f32 v193, v197, v193\n\t" \
  "v_accvgpr_write_b32 a103, v193\n\t" \
  "v_accvgpr_read_b32 v193, a104\n\t" \
  "v_mul_f32 v193, v198, v193\n\t" \
  "v_accvgpr_write_b32 a104, v193\n\t" \
  "v_accvgpr_read_b32 v193, a105\n\t" \
  "v_mul_f32 v193, v198, v193\n\t" \
  "v_accvgpr_write_b32 a105, v193\n\t" \
  "v_accvgpr_read_b32 v193, a106\n\t" \
  "v_mul_f32 v193, v198, v193\n\t" \
  "v_accvgpr_write_b32 a106, v193\n\t" \
  "v_accvgpr_read_b32 v193, a107\n\t" \
  "v_mul_f32 v193, v198, v193\n\t" \
  "v_accvgpr_write_b32 a107, v193\n\t" \
  "v_accvgpr_read_b32 v193, a108\n\t" \
  "v_mul_f32 v193, v199, v193\n\t" \
  "v_accvgpr_write_b32 a108, v193\n\t" \
  "v_accvgpr_read_b32 v193, a109\n\t" \
  "v_mul_f32 v193, v199, v193\n\t" \
  "v_accvgpr_write_b32 a109, v193\n\t" \
  "v_accvgpr_read_b32 v193, a110\n\t" \
  "v_mul_f32 v193, v199, v193\n\t" \
  "v_accvgpr_write_b32 a110, v193\n\t" \
  "v_accvgpr_read_b32 v193, a111\n\t" \
  "v_mul_f32 v193, v199, v193\n\t" \
  "v_accvgpr_write_b32 a111, v193\n\t" \
  "v_accvgpr_read_b32 v193, a112\n\t" \
  "v_mul_f32 v193, v196, v193\n\t" \
  "v_accvgpr_write_b32 a112, v193\n\t" \
  "v_accvgpr_read_b32 v193, a113\n\t" \
  "v_mul_f32 v193, v196, v193\n\t" \
  "v_accvgpr_write_b32 a113, v193\n\t" \
  "v_accvgpr_read_b32 v193, a114\n\t" \
  "v_mul_f32 v193, v196, v193\n\t" \
  "v_accvgpr_write_b32 a114, v193\n\t" \
  "v_accvgpr_read_b32 v193, a115\n\t" \
  "v_mul_f32 v193, v196, v193\n\t" \
  "v_accvgpr_write_b32 a115, v193\n\t" \
  "v_accvgpr_read_b32 v193, a116\n\t" \
  "v_mul_f32 v193, v197, v193\n\t" \
  "v_accvgpr_write_b32 a116, v193\n\t" \
  "v_accvgpr_read_b32 v193, a117\n\t" \
  "v_mul_f32 v193, v197, v193\n\t" \
  "v_accvgpr_write_b32 a117, v193\n\t" \
  "v_accvgpr_read_b32 v193, a118\n\t" \
  "v_mul_f32 v193, v197, v193\n\t" \
  "v_accvgpr_write_b32 a118, v193\n\t" \
  "v_accvgpr_read_b32 v193, a119\n\t" \
  "v_mul_f32 v193, v197, v193\n\t" \
  "v_accvgpr_write_b32 a119, v193\n\t" \
  "v_accvgpr_read_b32 v193, a120\n\t" \
  "v_mul_f32 v193, v198, v193\n\t" \
  "v_accvgpr_write_b32 a120, v193\n\t" \
  "v_accvgpr_read_b32 v193, a121\n\t" \
  "v_mul_f32 v193, v198, v193\n\t" \
  "v_accvgpr_write_b32 a121, v193\n\t" \
  "v_accvgpr_read_b32 v193, a122\n\t" \
  "v_mul_f32 v193, v198, v193\n\t" \
  "v_accvgpr_write_b32 a122, v193\n\t" \
  "v_accvgpr_read_b32 v193, a123\n\t" \
  "v_mul_f32 v193, v198, v193\n\t" \
  "v_accvgpr_write_b32 a123, v193\n\t" \
  "v_accvgpr_read_b32 v193, a124\n\t" \
  "v_mul_f32 v193, v199, v193\n\t" \
  "v_accvgpr_write_b32 a124, v193\n\t" \
  "v_accvgpr_read_b32 v193, a125\n\t" \
  "v_mul_f32 v193, v199, v193\n\t" \
  "v_accvgpr_write_b32 a125, v193\n\t" \
  "v_accvgpr_read_b32 v193, a126\n\t" \
  "v_mul_f32 v193, v199, v193\n\t" \
  "v_accvgpr_write_b32 a126, v193\n\t" \
  "v_accvgpr_read_b32 v193, a127\n\t" \
  "v_mul_f32 v193, v199, v193\n\t" \
  "v_accvgpr_write_b32 a127, v193\n\t" \
  "v_mul_f32 v160, v196, v160\n\t" \
  "v_mul_f32 v161, v196, v161\n\t" \
  "v_mul_f32 v162, v196, v162\n\t" \
  "v_mul_f32 v163, v196, v163\n\t" \
  "v_mov_b32 v196, 1.0\n\t" \
  "v_mul_f32 v164, v197, v164\n\t" \
  "v_mul_f32 v165, v197, v165\n\t" \
  "v_mul_f32 v166, v197, v166\n\t" \
  "v_mul_f32 v167, v197, v167\n\t" \
  "v_mov_b32 v197, 1.0\n\t" \
  "v_mul_f32 v168, v198, v168\n\t" \
  "v_mul_f32 v169, v198, v169\n\t" \
  "v_mul_f32 v170, v198, v170\n\t" \
  "v_mul_f32 v171, v198, v171\n\t" \
  "v_mov_b32 v198, 1.0\n\t" \
  "v_mul_f32 v172, v199, v172\n\t" \
  "v_mul_f32 v173, v199, v173\n\t" \
  "v_mul_f32 v174, v199, v174\n\t" \
  "v_mul_f32 v175, v199, v175\n\t" \
  "v_mov_b32 v199, 1.0\n\t" \
  "s_mov_b32 s99, 0\n\t" \
  "s_nop 1\n\t" \
  "s_cmp_eq_u32 vcc_lo, 0\n\t" \
  "s_cbranch_scc1 .Lsf_q0\n\t" \
  "s_cmp_eq_u32 vcc_lo, 1\n\t" \
  "s_cbranch_scc1 .Lsf_q1\n\t" \
  "s_cmp_eq_u32 vcc_lo, 2\n\t" \
  "s_cbranch_scc1 .Lsf_q2\n\t" \
  "s_cmp_eq_u32 vcc_lo, 3\n\t" \
  "s_cbranch_scc1 .Lsf_q3\n\t" \
  "s_cmp_eq_u32 vcc_lo, 4\n\t" \
  "s_cbranch_scc1 .Lsf_q4\n\t" \
  "s_cmp_eq_u32 vcc_lo, 5\n\t" \
  "s_cbranch_scc1 .Lsf_q5\n\t" \
  "s_cmp_eq_u32 vcc_lo, 6\n\t" \
  "s_cbranch_scc1 .Lsf_q6\n\t" \
  "s_cmp_eq_u32 vcc_lo, 7\n\t" \
  "s_cbranch_scc1 .Lsf_q7\n\t" \
  "s_endpgm\n\t" \
  ".Lsf_v:\n\t" \
  "s_mov_b32 s98, 0\n\t" \
  ".Lsf_v1:\n\t" \
  "s_sleep 4\n\t" \
  "buffer_load_dword v209, v205, %[ws], 0 offen sc1\n\t" \
  "s_waitcnt vmcnt(0)\n\t" \
  "v_and_b32 v206, 0xbfffbfff, v209\n\t" \
  "v_cmp_ne_u32_e64 s[96:97], %[tag], v206\n\t" \
  "s_cmp_lg_u64 s[96:97], 0\n\t" \
  "s_cbranch_scc0 .Lsf_w\n\t" \
  "s_add_u32 s98, s98, 1\n\t" \
  "s_cmp_lt_u32 s98, %[cap]\n\t" \
  "s_cbranch_scc1 .Lsf_v1\n\t" \
  "v_mov_b32 v206, %[tag]\n\t" \
  "buffer_store_dword v206, off, %[ws], 0 offset:256 sc1\n\t" \
  "s_branch .Lsf_w\n\t" \
  ".subsection 0\n\t" \
  ".Lat_end:\n\t" \
  ""
#define AT_CLOB "v208", "v209", "v210", "v211", "v212", "v214", "v215", "v216", "v217", "v218", "v219", "v235", "v236", "v237", "v238", "v239", "s92", "s93", "s94", "s95", "s96", "s97", "s98", "s99", "vcc", "scc", "memory"
#define AT_PF1
#define AT_NU 1
#define AT_BN 0
#define AT_XF "+{v[0:15]}"
#define AT_WF "+{v[16:47]}"
#define TL_ASM \
  "v_pk_fma_f32 v[0:1], v[128:129], v[96:97], v[0:1]\n\t" \
  "v_pk_fma_f32 v[2:3], v[130:131], v[98:99], v[2:3]\n\t" \
  "v_pk_fma_f32 v[16:17], v[132:133], v[112:113], v[16:17]\n\t" \
  "v_pk_fma_f32 v[18:19], v[134:135], v[114:115], v[18:19]\n\t" \
  "v_pk_fma_f32 v[4:5], v[128:129], v[100:101], v[4:5]\n\t" \
  "v_pk_fma_f32 v[6:7], v[130:131], v[102:103], v[6:7]\n\t" \
  "v_pk_fma_f32 v[20:21], v[132:133], v[116:117], v[20:21]\n\t" \
  "v_pk_fma_f32 v[22:23], v[134:135], v[118:119], v[22:23]\n\t" \
  "v_cvt_pk_bf16_f32 v160, v0, v1\n\t" \
  "v_cvt_pk_bf16_f32 v161, v2, v3\n\t" \
  "v_cvt_pk_bf16_f32 v162, v16, v17\n\t" \
  "v_cvt_pk_bf16_f32 v163, v18, v19\n\t" \
  "v_pk_fma_f32 v[8:9], v[128:129], v[104:105], v[8:9]\n\t" \
  "v_pk_fma_f32 v[10:11], v[130:131], v[106:107], v[10:11]\n\t" \
  "v_pk_fma_f32 v[24:25], v[132:133], v[120:121], v[24:25]\n\t" \
  "v_pk_fma_f32 v[26:27], v[134:135], v[122:123], v[26:27]\n\t" \
  "v_cvt_pk_bf16_f32 v164, v4, v5\n\t" \
  "v_cvt_pk_bf16_f32 v165, v6, v7\n\t" \
  "v_cvt_pk_bf16_f32 v166, v20, v21\n\t" \
  "v_cvt_pk_bf16_f32 v167, v22, v23\n\t" \
  "v_pk_fma_f32 v[12:13], v[128:129], v[108:109], v[12:13]\n\t" \
  "v_pk_fma_f32 v[14:15], v[130:131], v[110:111], v[14:15]\n\t" \
  "v_pk_fma_f32 v[28:29], v[132:133], v[124:125], v[28:29]\n\t" \
  "v_pk_fma_f32 v[30:31], v[134:135], v[126:127], v[30:31]\n\t" \
  "v_cvt_pk_bf16_f32 v168, v8, v9\n\t" \
  "v_cvt_pk_bf16_f32 v169, v10, v11\n\t" \
  "v_cvt_pk_bf16_f32 v170, v24, v25\n\t" \
  "v_cvt_pk_bf16_f32 v171, v26, v27\n\t" \
  "v_cvt_pk_bf16_f32 v172, v12, v13\n\t" \
  "v_cvt_pk_bf16_f32 v173, v14, v15\n\t" \
  "v_cvt_pk_bf16_f32 v174, v28, v29\n\t" \
  "v_cvt_pk_bf16_f32 v175, v30, v31\n\t" \
  "v_dot2_f32_bf16 v152, v160, v160, 0\n\t" \
  "v_dot2_f32_bf16 v153, v164, v164, 0\n\t" \
  "v_dot2_f32_bf16 v154, v168, v168, 0\n\t" \
  "v_dot2_f32_bf16 v155, v172, v172, 0\n\t" \
  "v_dot2_f32_bf16 v152, v161, v161, v152\n\t" \
  "v_dot2_f32_bf16 v153, v165, v165, v153\n\t" \
  "v_dot2_f32_bf16 v154, v169, v169, v154\n\t" \
  "v_dot2_f32_bf16 v155, v173, v173, v155\n\t" \
  "v_dot2_f32_bf16 v152, v162, v162, v152\n\t" \
  "v_dot2_f32_bf16 v153, v166, v166, v153\n\t" \
  "v_dot2_f32_bf16 v154, v170, v170, v154\n\t" \
  "v_dot2_f32_bf16 v155, v174, v174, v155\n\t" \
  "v_dot2_f32_bf16 v152, v163, v163, v152\n\t" \
  "v_dot2_f32_bf16 v153, v167, v167, v153\n\t" \
  "v_dot2_f32_bf16 v154, v171, v171, v154\n\t" \
  "v_dot2_f32_bf16 v155, v175, v175, v155\n\t" \
  "v_mfma_f32_16x16x32_bf16 v[156:159], a[208:211], v[160:163], 0\n\t" \
  "s_lshl_b32 s64, %[wid], 10\n\t" \
  "v_add_u32 v234, s64, v242\n\t" \
  "v_mov_b32 v250, s87\n\t" \
  "v_mfma_f32_16x16x32_bf16 v[156:159], a[212:215], v[164:167], v[156:159]\n\t" \
  "s_lshl_b32 s61, %[bid], 11\n\t" \
  "v_permlane32_swap_b32 v152, v154\n\t" \
  "v_mfma_f32_16x16x32_bf16 v[156:159], a[216:219], v[168:171], v[156:159]\n\t" \
  "s_and_b32 s61, s61, 0x2000\n\t" \
  "v_permlane32_swap_b32 v153, v155\n\t" \
  "v_mfma_f32_16x16x32_bf16 v[156:159], a[220:223], v[172:175], v[156:159]\n\t" \
  "s_xor_b32 s61, s61, %[nb]\n\t" \
  "v_add_f32 v152, v152, v154\n\t" \
  "v_add_f32 v153, v153, v155\n\t" \
  "v_writelane_b32 v250, s88, 1\n\t" \
  "s_and_b32 s80, %[bid], 3\n\t" \
  "v_permlane16_swap_b32 v152, v153\n\t" \
  "s_lshr_b32 s60, %[bid], 6\n\t" \
  "s_lshl2_add_u32 s60, s60, s80\n\t" \
  "v_add_f32 v158, v157, v158\n\t" \
  "v_add_f32 v157, v152, v153\n\t" \
  "v_mov_b32 v159, v250\n\t" \
  "ds_write_b128 v234, v[156:159]\n\t" \
  "s_lshl_b32 s62, s60, 15\n\t" \
  "s_add_u32 s62, s62, s61\n\t" \
  "s_add_u32 s62, s62, 524288\n\t" \
  "s_and_b32 s63, %[bid], 0x20\n\t" \
  "s_lshl_b32 s63, s63, 9\n\t" \
  "s_add_u32 s77, s62, s63\n\t" \
  "s_and_b32 s63, %[bid], 0x18\n\t" \
  "s_lshl_b32 s63, s63, 8\n\t" \
  "s_add_u32 s77, s77, s63\n\t" \
  "s_add_u32 s77, s77, s64\n\t" \
  "v_lshl_add_u32 v248, %[wid], 3, v242\n\t" \
  "v_mov_b32 v217, v243\n\t" \
  "v_mov_b32 v219, v243\n\t" \
  "s_cmp_gt_u32 %[wid], 1\n\t" \
  "s_waitcnt lgkmcnt(0)\n\t" \
  "s_barrier\n\t" \
  "s_cbranch_scc1 .Ltl_np\n\t" \
  "ds_read2st64_b64 v[208:211], v248 offset1:2\n\t" \
  "ds_read2st64_b64 v[212:215], v248 offset0:4 offset1:6\n\t" \
  "s_waitcnt lgkmcnt(1)\n\t" \
  "v_pk_add_f32 v[208:209], v[208:209], v[210:211]\n\t" \
  "s_waitcnt lgkmcnt(0)\n\t" \
  "v_pk_add_f32 v[212:213], v[212:213], v[214:215]\n\t" \
  "s_nop 0\n\t" \
  "v_add_f32 v216, v208, v212\n\t" \
  "v_add_f32 v218, v209, v213\n\t" \
  "buffer_store_dwordx4 v[216:219], v242, %[ws], s77 offen sc1\n\t" \
  ".Ltl_np:\n\t" \
  STP("s_memrealtime s[90:91]\n\t") \
  STP("s_memtime s[82:83]\n\t") \
  STP("s_waitcnt lgkmcnt(0)\n\t") \
  STP("s_or_b32 s90, s90, 1\n\t") \
  STP("v_writelane_b32 %[stv], s90, 40\n\t") \
  STP("v_writelane_b32 %[stv], s82, 41\n\t") \
  "s_and_b32 s63, %[wid], 2\n\t" \
  "s_lshl_b32 s63, s63, 13\n\t" \
  "s_add_u32 s62, s62, s63\n\t" \
  "s_and_b32 s63, %[wid], 1\n\t" \
  "s_lshl_b32 s63, s63, 12\n\t" \
  "s_add_u32 s62, s62, s63\n\t" \
  "ds_read_b128 v[128:131], v237 offset:2688\n\t" \
  "ds_read_b128 v[132:135], v237 offset:2752\n\t" \
  "ds_read_b128 v[136:139], v237 offset:2816\n\t" \
  "ds_read_b128 v[140:143], v237 offset:2880\n\t" \
  "ds_read_b128 v[144:147], v237 offset:2944\n\t" \
  "ds_read_b128 v[148:151], v237 offset:3008\n\t" \
  "s_mov_b32 s75, 0x05040100\n\t" \
  "s_mov_b32 s76, 0x07060302\n\t" \
  "s_mov_b32 s72, 0\n\t" \
  "s_mov_b32 s81, 0xffff0000\n\t" \
  "s_bfe_u32 s79, %[bid], 0x30003\n\t" \
  "s_and_b32 s63, %[bid], 0x4\n\t" \
  "s_lshl_b32 s63, s63, 2\n\t" \
  "s_add_u32 s60, s60, s63\n\t" \
  "v_lshlrev_b32 v0, 16, v160\n\t" \
  "v_and_b32 v1, 0xffff0000, v160\n\t" \
  "v_lshlrev_b32 v2, 16, v161\n\t" \
  "v_and_b32 v3, 0xffff0000, v161\n\t" \
  "v_lshlrev_b32 v16, 16, v162\n\t" \
  "v_and_b32 v17, 0xffff0000, v162\n\t" \
  "v_lshlrev_b32 v18, 16, v163\n\t" \
  "v_and_b32 v19, 0xffff0000, v163\n\t" \
  "v_lshlrev_b32 v4, 16, v164\n\t" \
  "v_and_b32 v5, 0xffff0000, v164\n\t" \
  "v_lshlrev_b32 v6, 16, v165\n\t" \
  "v_and_b32 v7, 0xffff0000, v165\n\t" \
  "v_lshlrev_b32 v20, 16, v166\n\t" \
  "v_and_b32 v21, 0xffff0000, v166\n\t" \
  "v_lshlrev_b32 v22, 16, v167\n\t" \
  "v_and_b32 v23, 0xffff0000, v167\n\t" \
  "v_lshlrev_b32 v8, 16, v168\n\t" \
  "v_and_b32 v9, 0xffff0000, v168\n\t" \
  "v_lshlrev_b32 v10, 16, v169\n\t" \
  "v_and_b32 v11, 0xffff0000, v169\n\t" \
  "v_lshlrev_b32 v24, 16, v170\n\t" \
  "v_and_b32 v25, 0xffff0000, v170\n\t" \
  "v_lshlrev_b32 v26, 16, v171\n\t" \
  "v_and_b32 v27, 0xffff0000, v171\n\t" \
  "v_lshlrev_b32 v12, 16, v172\n\t" \
  "v_and_b32 v13, 0xffff0000, v172\n\t" \
  "buffer_load_dwordx4 v[208:211], v242, %[ws], s62 offen offset:0 sc1\n\t" \
  "buffer_load_dwordx4 v[212:215], v242, %[ws], s62 offen offset:1024 sc1\n\t" \
  "buffer_load_dwordx4 v[216:219], v242, %[ws], s62 offen offset:2048 sc1\n\t" \
  "buffer_load_dwordx4 v[220:223], v242, %[ws], s62 offen offset:3072 sc1\n\t" \
  "s_branch .Lht0\n\t" \
  ".p2align 12\n\t" \
  ".Lht0:\n\t" \
  "s_cbranch_execz .Lht0n\n\t" \
  "v_lshlrev_b32 v14, 16, v173\n\t" \
  "v_and_b32 v15, 0xffff0000, v173\n\t" \
  "v_lshlrev_b32 v28, 16, v174\n\t" \
  "v_and_b32 v29, 0xffff0000, v174\n\t" \
  "v_lshlrev_b32 v30, 16, v175\n\t" \
  "v_and_b32 v31, 0xffff0000, v175\n\t" \
  "s_lshl_b32 s63, s60, 6\n\t" \
  "s_sub_u32 s63, 0x7ff, s63\n\t" \
  "v_sub_u32 v248, s63, v244\n\t" \
  "v_and_b32 v246, 31, v244\n\t" \
  "s_and_b32 s63, %[bid], 0x18\n\t" \
  "s_lshl_b32 s63, s63, 7\n\t" \
  "s_add_u32 s65, s61, s63\n\t" \
  "s_and_b32 s63, %[bid], 0x3\n\t" \
  "s_lshl_b32 s63, s63, 8\n\t" \
  "s_add_u32 s65, s65, s63\n\t" \
  "s_add_u32 s65, s65, 65536\n\t" \
  "v_lshl_add_u32 v235, v246, 3, s65\n\t" \
  "s_lshl_b32 s68, 1, s60\n\t" \
  "s_lshl2_add_u32 s65, s79, %[wid]\n\t" \
  "s_lshl_b32 s63, s65, 13\n\t" \
  "s_and_b32 s63, s63, 0x2000\n\t" \
  "s_xor_b32 s63, s63, %[nb]\n\t" \
  "s_lshr_b32 s65, s65, 1\n\t" \
  "s_lshl_b32 s65, s65, 8\n\t" \
  "s_add_u32 s65, s65, s63\n\t" \
  "s_lshl3_add_u32 s65, s60, s65\n\t" \
  "s_add_u32 s65, s65, 65536\n\t" \
  "v_bfrev_b32 v249, 1\n\t" \
  "s_nop 0\n\t" \
  "v_writelane_b32 v249, 0, 63\n\t" \
  "v_mov_b32 v159, v243\n\t" \
  "s_add_u32 s63, s61, 262144\n\t" \
  "v_and_b32 v236, -32, v234\n\t" \
  "v_add_u32 v236, s63, v236\n\t" \
  "v_mov_b32 v113, v243\n\t" \
  "v_mov_b32 v115, v243\n\t" \
  "v_mov_b32 v117, v243\n\t" \
  "v_mov_b32 v119, v243\n\t" \
  "v_mov_b32 v121, v243\n\t" \
  "v_mov_b32 v123, v243\n\t" \
  "v_mov_b32 v125, v243\n\t" \
  "v_mov_b32 v127, v243\n\t" \
  "v_and_b32 v239, 48, v244\n\t" \
  "v_lshl_add_u32 v239, %[wid], 7, v239\n\t" \
  "v_and_b32 v157, 15, v244\n\t" \
  "v_bfrev_b32 v156, 1\n\t" \
  "s_lshl_b32 s66, s60, 13\n\t" \
  "s_and_b32 s66, s66, 0x3c000\n\t" \
  "s_and_b32 s63, %[bid], 0x1\n\t" \
  "s_lshl_b32 s63, s63, 12\n\t" \
  "s_add_u32 s66, s66, s63\n\t" \
  "s_and_b32 s63, %[bid], 0x38\n\t" \
  "s_lshl_b32 s63, s63, 6\n\t" \
  "s_add_u32 s66, s66, s63\n\t" \
  "s_add_u32 s66, s66, 262144\n\t" \
  "s_xor_b32 s77, s66, 0x2000\n\t" \
  "s_lshl_b32 s67, s60, 12\n\t" \
  "s_and_b32 s67, s67, 0x1c000\n\t" \
  "s_and_b32 s63, %[bid], 0x20\n\t" \
  "s_lshl_b32 s63, s63, 8\n\t" \
  "s_add_u32 s67, s67, s63\n\t" \
  "s_xor_b32 s67, s67, %[nb]\n\t" \
  "s_and_b32 s63, %[bid], 0x3\n\t" \
  "s_lshl_b32 s63, s63, 11\n\t" \
  "s_add_u32 s67, s67, s63\n\t" \
  "s_and_b32 s63, %[bid], 0x18\n\t" \
  "s_lshl_b32 s63, s63, 6\n\t" \
  "s_add_u32 s67, s67, s63\n\t" \
  "s_add_u32 s67, s67, 1966080\n\t" \
  "v_perm_b32 v240, v34, v32, s75\n\t" \
  "v_perm_b32 v34, v34, v32, s76\n\t" \
  "v_perm_b32 v32, v35, v33, s75\n\t" \
  "v_perm_b32 v35, v35, v33, s76\n\t" \
  "v_perm_b32 v33, v38, v36, s75\n\t" \
  "v_perm_b32 v38, v38, v36, s76\n\t" \
  "v_perm_b32 v36, v39, v37, s75\n\t" \
  "v_perm_b32 v39, v39, v37, s76\n\t" \
  "v_perm_b32 v37, v42, v40, s75\n\t" \
  "v_perm_b32 v42, v42, v40, s76\n\t" \
  "v_perm_b32 v40, v43, v41, s75\n\t" \
  "v_perm_b32 v43, v43, v41, s76\n\t" \
  "v_perm_b32 v41, v46, v44, s75\n\t" \
  "v_perm_b32 v46, v46, v44, s76\n\t" \
  "v_perm_b32 v44, v47, v45, s75\n\t" \
  "v_perm_b32 v47, v47, v45, s76\n\t" \
  "v_perm_b32 v45, v50, v48, s75\n\t" \
  "v_perm_b32 v50, v50, v48, s76\n\t" \
  "v_perm_b32 v48, v51, v49, s75\n\t" \
  "v_perm_b32 v51, v51, v49, s76\n\t" \
  "v_perm_b32 v49, v54, v52, s75\n\t" \
  "v_perm_b32 v54, v54, v52, s76\n\t" \
  "v_perm_b32 v52, v55, v53, s75\n\t" \
  "v_perm_b32 v55, v55, v53, s76\n\t" \
  "v_perm_b32 v53, v58, v56, s75\n\t" \
  "v_perm_b32 v58, v58, v56, s76\n\t" \
  "v_perm_b32 v56, v59, v57, s75\n\t" \
  "v_perm_b32 v59, v59, v57, s76\n\t" \
  "v_perm_b32 v57, v62, v60, s75\n\t" \
  "v_perm_b32 v62, v62, v60, s76\n\t" \
  "v_perm_b32 v60, v63, v61, s75\n\t" \
  "v_perm_b32 v63, v63, v61, s76\n\t" \
  "s_waitcnt vmcnt(0)\n\t" \
  ".Ltl_c0:\n\t" \
  "v_xor_b32 v152, %[tag], v209\n\t" \
  "v_bitop3_b32 v152, v152, v211, %[tag] bitop3:0xf6\n\t" \
  "v_bitop3_b32 v152, v152, v213, %[tag] bitop3:0xf6\n\t" \
  "v_bitop3_b32 v152, v152, v215, %[tag] bitop3:0xf6\n\t" \
  "v_bitop3_b32 v152, v152, v217, %[tag] bitop3:0xf6\n\t" \
  "v_bitop3_b32 v152, v152, v219, %[tag] bitop3:0xf6\n\t" \
  "v_bitop3_b32 v152, v152, v221, %[tag] bitop3:0xf6\n\t" \
  "v_bitop3_b32 v152, v152, v223, %[tag] bitop3:0xf6\n\t" \
  "v_cmp_ne_u32 vcc, 0, v152\n\t" \
  "v_add_f32 v152, v208, v216\n\t" \
  "v_add_f32 v153, v210, v218\n\t" \
  "v_add_f32 v154, v212, v220\n\t" \
  "v_add_f32 v155, v214, v222\n\t" \
  "s_cbranch_vccnz .Ltl_s0\n\t" \
  ".Ltl_k0:\n\t" \
  "ds_write_b128 v234, v[152:155] offset:4096\n\t" \
  "buffer_load_dwordx2 v[216:217], v235, %[ws], 0 offen sc1\n\t" \
  "s_waitcnt lgkmcnt(0)\n\t" \
  "s_barrier\n\t" \
  STP("s_memrealtime s[90:91]\n\t") \
  STP("s_memtime s[82:83]\n\t") \
  STP("s_waitcnt lgkmcnt(0)\n\t") \
  STP("s_or_b32 s90, s90, 1\n\t") \
  STP("v_writelane_b32 %[stv], s90, 42\n\t") \
  STP("v_writelane_b32 %[stv], s82, 43\n\t") \
  "ds_read_b128 v[96:99], v242 offset:4096\n\t" \
  "ds_read_b128 v[100:103], v242 offset:5120\n\t" \
  "ds_read_b128 v[104:107], v242 offset:6144\n\t" \
  "ds_read_b128 v[108:111], v242 offset:7168\n\t" \
  "s_waitcnt lgkmcnt(2)\n\t" \
  "v_pk_add_f32 v[96:97], v[96:97], v[100:101]\n\t" \
  "v_pk_add_f32 v[98:99], v[98:99], v[102:103]\n\t" \
  "s_waitcnt lgkmcnt(0)\n\t" \
  "v_pk_add_f32 v[104:105], v[104:105], v[108:109]\n\t" \
  "v_pk_add_f32 v[106:107], v[106:107], v[110:111]\n\t" \
  "v_pk_add_f32 v[96:97], v[96:97], v[104:105]\n\t" \
  "v_pk_add_f32 v[98:99], v[98:99], v[106:107]\n\t" \
  "v_mul_f32 v96, 0x3a800000, v96\n\t" \
  "v_mul_f32 v97, 0x3a800000, v97\n\t" \
  "v_readlane_b32 s87, v99, 0\n\t" \
  "v_fma_f32 v97, -v96, v96, v97\n\t" \
  "v_readlane_b32 s88, v99, 1\n\t" \
  "v_max_f32 v97, 0, v97\n\t" \
  "v_add_f32 v97, 0x3727c5ac, v97\n\t" \
  "v_rsq_f32 v208, v97\n\t" \
  "v_fma_f32 v98, -v96, s87, v98\n\t" \
  "v_fma_f32 v98, v208, v98, s88\n\t" \
  "v_cvt_pk_bf16_f32 v98, v98, v98\n\t" \
  "v_ashrrev_i32 v99, 31, v98\n\t" \
  "v_or_b32 v99, 0x80000000, v99\n\t" \
  "v_xor_b32 v98, v98, v99\n\t" \
  "v_and_or_b32 v158, v98, s81, v248\n\t" \
  "v_mul_f32 v209, v208, -v96\n\t" \
  "v_mov_b32 v212, v208\n\t" \
  "v_max_u32_dpp v158, v158, v158 quad_perm:[1,0,3,2] row_mask:0xf bank_mask:0xf\n\t" \
  "v_mov_b32 v213, v209\n\t" \
  "v_permlane32_swap_b32 v208, v212\n\t" \
  "v_max_u32_dpp v158, v158, v158 quad_perm:[2,3,0,1] row_mask:0xf bank_mask:0xf\n\t" \
  "v_permlane32_swap_b32 v209, v213\n\t" \
  "v_mov_b32 v210, v208\n\t" \
  "v_max_u32_dpp v158, v158, v158 row_half_mirror row_mask:0xf bank_mask:0xf\n\t" \
  "v_mov_b32 v211, v209\n\t" \
  "v_mov_b32 v214, v212\n\t" \
  "v_max_u32_dpp v158, v158, v158 row_mirror row_mask:0xf bank_mask:0xf\n\t" \
  "v_mov_b32 v215, v213\n\t" \
  "v_permlane16_swap_b32 v208, v210\n\t" \
  "v_max_u32_dpp v158, v158, v158 row_bcast:15 row_mask:0xa bank_mask:0xf\n\t" \
  "v_permlane16_swap_b32 v209, v211\n\t" \
  "v_permlane16_swap_b32 v212, v214\n\t" \
  "v_max_u32_dpp v158, v158, v158 row_bcast:31 row_mask:0xc bank_mask:0xf\n\t" \
  "buffer_store_dwordx2 v[158:159], v249, %[ws], s65 offen sc1\n\t" \
  "v_readlane_b32 s69, v158, 63\n\t" \
  STP("s_memrealtime s[90:91]\n\t") \
  STP("s_memtime s[82:83]\n\t") \
  STP("s_waitcnt lgkmcnt(0)\n\t") \
  STP("s_or_b32 s90, s90, 1\n\t") \
  STP("v_writelane_b32 %[stv], s90, 44\n\t") \
  STP("v_writelane_b32 %[stv], s82, 45\n\t") \
  "v_permlane16_swap_b32 v213, v215\n\t" \
  "s_andn2_b32 s73, 63, s69\n\t" \
  "s_waitcnt vmcnt(1)\n\t" \
  "v_cmp_eq_u32 vcc, %[tag], v217\n\t" \
  "s_mov_b32 s74, 0x100\n\t" \
  "s_nop 0\n\t" \
  "v_cndmask_b32 v152, 0, v216, vcc\n\t" \
  "s_bcnt1_i32_b32 s78, vcc_lo\n\t" \
  "v_cmp_lt_u32 vcc, s69, v152\n\t" \
  "s_cbranch_vccz .Ltl_cd\n\t" \
  ".Ltl_ce:\n\t" \
  "v_max_u32_dpp v152, v152, v152 quad_perm:[1,0,3,2] row_mask:0xf bank_mask:0xf\n\t" \
  "s_lshl_b32 s63, s60, 17\n\t" \
  "s_lshl_b32 s64, s79, 8\n\t" \
  "v_max_u32_dpp v152, v152, v152 quad_perm:[2,3,0,1] row_mask:0xf bank_mask:0xf\n\t" \
  "s_add_u32 s63, s63, s64\n\t" \
  "s_lshl_b32 s64, %[wid], 6\n\t" \
  "v_max_u32_dpp v152, v152, v152 row_half_mirror row_mask:0xf bank_mask:0xf\n\t" \
  "s_add_u32 s63, s63, s64\n\t" \
  "v_lshl_add_u32 v238, v157, 11, s63\n\t" \
  "v_max_u32_dpp v152, v152, v152 row_mirror row_mask:0xf bank_mask:0xf\n\t" \
  "s_nop 1\n\t" \
  "v_max_u32_dpp v152, v152, v152 row_bcast:15 row_mask:0xa bank_mask:0xf\n\t" \
  "s_nop 0\n\t" \
  "v_readlane_b32 s70, v152, 31\n\t" \
  "s_max_u32 s70, s70, s69\n\t" \
  "s_andn2_b32 s63, 0x780, s70\n\t" \
  "s_lshl_b32 s63, s63, 7\n\t" \
  "s_andn2_b32 s71, 64, s70\n\t" \
  "s_lshl_b32 s71, s71, 6\n\t" \
  "s_or_b32 s71, s71, s63\n\t" \
  "buffer_load_dwordx2 v[216:217], v235, %[ws], 0 offen sc1\n\t" \
  "buffer_load_dwordx4 v[218:221], v236, %[ws], s71 offen sc1\n\t" \
  "buffer_load_dwordx4 v[222:225], v236, %[ws], s71 offen offset:16 sc1\n\t" \
  "v_bfe_u32 v153, v244, 4, 1\n\t" \
  "v_lshl_add_u32 v238, v153, 5, v238\n\t" \
  "v_lshrrev_b32 v154, 5, v244\n\t" \
  "v_lshl_add_u32 v238, v154, 4, v238\n\t" \
  "s_mov_b64 s[96:97], %[out]\n\t" \
  "s_and_b32 s97, s97, 0xffff\n\t" \
  "s_mov_b32 s98, 0x400000\n\t" \
  "s_mov_b32 s99, 0x20000\n\t" \
  "s_lshr_b32 s64, %[bid], 3\n\t" \
  "s_and_b32 s63, s64, 27\n\t" \
  "s_cmp_lt_u32 s64, 24\n\t" \
  "s_cselect_b32 s64, s64, s63\n\t" \
  "s_mul_i32 s63, s80, 56\n\t" \
  "s_add_u32 s64, s64, s63\n\t" \
  "s_and_b32 s63, %[bid], 4\n\t" \
  "s_mul_i32 s63, s63, 7\n\t" \
  "s_add_u32 s64, s64, s63\n\t" \
  "s_lshl_b32 s63, %[wid], 12\n\t" \
  "s_and_b32 s63, s63, 0x2000\n\t" \
  "s_xor_b32 s85, s63, %[nb]\n\t" \
  "s_lshr_b32 s63, s64, 2\n\t" \
  "s_lshl_b32 s63, s63, 14\n\t" \
  "s_add_u32 s85, s85, s63\n\t" \
  "s_and_b32 s63, s64, 3\n\t" \
  "s_lshl_b32 s63, s63, 11\n\t" \
  "s_add_u32 s85, s85, s63\n\t" \
  "s_add_u32 s85, s85, 1048576\n\t" \
  "v_and_b32 v247, 0x7f0, v234\n\t" \
  "v_lshlrev_b32 v245, 6, v154\n\t" \
  "v_lshl_add_u32 v245, v153, 4, v245\n\t" \
  "v_and_b32 v153, 1, v244\n\t" \
  "v_lshl_add_u32 v245, v153, 5, v245\n\t" \
  "v_lshl_add_u32 v245, %[wid], 7, v245\n\t" \
  "v_and_b32 v153, 14, v244\n\t" \
  "v_lshl_add_u32 v245, v153, 8, v245\n\t" \
  "v_lshlrev_b32 v246, 2, v246\n\t" \
  "v_add_u32 v248, 0x80, v246\n\t" \
  "s_mov_b32 s72, 0\n\t" \
  "s_lshr_b32 s62, %[bid], 5\n\t" \
  "s_bfe_u32 s63, %[bid], 0x10002\n\t" \
  "s_sub_u32 s62, s62, s63\n\t" \
  "s_cmp_ge_u32 s78, 0\n\t" \
  "s_cselect_b32 s84, 1, 0\n\t" \
  "s_cmp_eq_u32 s62, 6\n\t" \
  "s_cselect_b32 s84, 0, s84\n\t" \
  "s_cmp_lt_u32 s78, 33\n\t" \
  "s_cselect_b32 s78, 0, 0x80000000\n\t" \
  "s_cmp_lg_u32 s84, 0\n\t" \
  "s_cbranch_scc1 .Lht1\n\t" \
  ".Ltl_h2:\n\t" \
  "v_pk_fma_f32 v[112:113], v[0:1], v[208:209], v[208:209] op_sel:[0,0,1] op_sel_hi:[1,0,1]\n\t" \
  "v_pk_fma_f32 v[114:115], v[2:3], v[208:209], v[208:209] op_sel:[0,0,1] op_sel_hi:[1,0,1]\n\t" \
  "v_pk_fma_f32 v[116:117], v[16:17], v[208:209], v[208:209] op_sel:[0,0,1] op_sel_hi:[1,0,1]\n\t" \
  "v_pk_fma_f32 v[118:119], v[18:19], v[208:209], v[208:209] op_sel:[0,0,1] op_sel_hi:[1,0,1]\n\t" \
  "v_pk_fma_f32 v[112:113], v[112:113], v[136:137], v[128:129]\n\t" \
  "v_pk_fma_f32 v[114:115], v[114:115], v[138:139], v[130:131]\n\t" \
  "v_pk_fma_f32 v[116:117], v[116:117], v[140:141], v[132:133]\n\t" \
  "v_pk_fma_f32 v[118:119], v[118:119], v[142:143], v[134:135]\n\t" \
  "v_cvt_pk_bf16_f32 v96, v112, v113\n\t" \
  "v_cvt_pk_bf16_f32 v97, v114, v115\n\t" \
  "v_cvt_pk_bf16_f32 v98, v116, v117\n\t" \
  "v_cvt_pk_bf16_f32 v99, v118, v119\n\t" \
  "v_pk_fma_f32 v[120:121], v[4:5], v[210:211], v[210:211] op_sel:[0,0,1] op_sel_hi:[1,0,1]\n\t" \
  "v_pk_fma_f32 v[122:123], v[6:7], v[210:211], v[210:211] op_sel:[0,0,1] op_sel_hi:[1,0,1]\n\t" \
  "v_pk_fma_f32 v[124:125], v[20:21], v[210:211], v[210:211] op_sel:[0,0,1] op_sel_hi:[1,0,1]\n\t" \
  "v_pk_fma_f32 v[126:127], v[22:23], v[210:211], v[210:211] op_sel:[0,0,1] op_sel_hi:[1,0,1]\n\t" \
  "v_pk_fma_f32 v[120:121], v[120:121], v[136:137], v[128:129]\n\t" \
  "v_pk_fma_f32 v[122:123], v[122:123], v[138:139], v[130:131]\n\t" \
  "v_pk_fma_f32 v[124:125], v[124:125], v[140:141], v[132:133]\n\t" \
  "v_pk_fma_f32 v[126:127], v[126:127], v[142:143], v[134:135]\n\t" \
  "v_cvt_pk_bf16_f32 v100, v120, v121\n\t" \
  "v_cvt_pk_bf16_f32 v101, v122, v123\n\t" \
  "v_cvt_pk_bf16_f32 v102, v124, v125\n\t" \
  "v_cvt_pk_bf16_f32 v103, v126, v127\n\t" \
  "v_pk_fma_f32 v[112:113], v[8:9], v[212:213], v[212:213] op_sel:[0,0,1] op_sel_hi:[1,0,1]\n\t" \
  "v_pk_fma_f32 v[114:115], v[10:11], v[212:213], v[212:213] op_sel:[0,0,1] op_sel_hi:[1,0,1]\n\t" \
  "v_pk_fma_f32 v[116:117], v[24:25], v[212:213], v[212:213] op_sel:[0,0,1] op_sel_hi:[1,0,1]\n\t" \
  "v_pk_fma_f32 v[118:119], v[26:27], v[212:213], v[212:213] op_sel:[0,0,1] op_sel_hi:[1,0,1]\n\t" \
  "v_pk_fma_f32 v[112:113], v[112:113], v[136:137], v[128:129]\n\t" \
  "v_pk_fma_f32 v[114:115], v[114:115], v[138:139], v[130:131]\n\t" \
  "v_pk_fma_f32 v[116:117], v[116:117], v[140:141], v[132:133]\n\t" \
  "v_pk_fma_f32 v[118:119], v[118:119], v[142:143], v[134:135]\n\t" \
  "v_cvt_pk_bf16_f32 v104, v112, v113\n\t" \
  "v_cvt_pk_bf16_f32 v105, v114, v115\n\t" \
  "v_cvt_pk_bf16_f32 v106, v116, v117\n\t" \
  "v_cvt_pk_bf16_f32 v107, v118, v119\n\t" \
  "v_pk_fma_f32 v[120:121], v[12:13], v[214:215], v[214:215] op_sel:[0,0,1] op_sel_hi:[1,0,1]\n\t" \
  "v_pk_fma_f32 v[122:123], v[14:15], v[214:215], v[214:215] op_sel:[0,0,1] op_sel_hi:[1,0,1]\n\t" \
  "v_pk_fma_f32 v[124:125], v[28:29], v[214:215], v[214:215] op_sel:[0,0,1] op_sel_hi:[1,0,1]\n\t" \
  "v_pk_fma_f32 v[126:127], v[30:31], v[214:215], v[214:215] op_sel:[0,0,1] op_sel_hi:[1,0,1]\n\t" \
  "v_pk_fma_f32 v[120:121], v[120:121], v[136:137], v[128:129]\n\t" \
  "v_pk_fma_f32 v[122:123], v[122:123], v[138:139], v[130:131]\n\t" \
  "v_pk_fma_f32 v[124:125], v[124:125], v[140:141], v[132:133]\n\t" \
  "v_pk_fma_f32 v[126:127], v[126:127], v[142:143], v[134:135]\n\t" \
  "v_cvt_pk_bf16_f32 v108, v120, v121\n\t" \
  "v_cvt_pk_bf16_f32 v109, v122, v123\n\t" \
  "v_cvt_pk_bf16_f32 v110, v124, v125\n\t" \
  "v_cvt_pk_bf16_f32 v111, v126, v127\n\t" \
  "v_sub_u32 v152, s74, v157\n\t" \
  "v_bfrev_b32 v155, 1\n\t" \
  "v_cmp_eq_u32 vcc, 0, v152\n\t" \
  "v_lshlrev_b32 v160, 16, v96\n\t" \
  "v_and_b32 v161, 0xffff0000, v96\n\t" \
  "v_lshlrev_b32 v162, 16, v97\n\t" \
  "v_and_b32 v163, 0xffff0000, v97\n\t" \
  "v_lshlrev_b32 v164, 16, v98\n\t" \
  "v_and_b32 v165, 0xffff0000, v98\n\t" \
  "v_lshlrev_b32 v166, 16, v99\n\t" \
  "v_and_b32 v167, 0xffff0000, v99\n\t" \
  "v_pk_fma_f32 v[160:161], v[144:145], v[160:161], v[0:1]\n\t" \
  "v_pk_fma_f32 v[162:163], v[146:147], v[162:163], v[2:3]\n\t" \
  "v_pk_fma_f32 v[164:165], v[148:149], v[164:165], v[16:17]\n\t" \
  "v_pk_fma_f32 v[166:167], v[150:151], v[166:167], v[18:19]\n\t" \
  "v_cndmask_b32 v228, v238, v155, vcc\n\t" \
  "v_cvt_pk_bf16_f32 v112, v160, v161\n\t" \
  "v_cvt_pk_bf16_f32 v114, v164, v165\n\t" \
  "v_cvt_pk_bf16_f32 v113, v162, v163\n\t" \
  "v_cvt_pk_bf16_f32 v115, v166, v167\n\t" \
  "v_or_b32 v232, s78, v228\n\t" \
  "s_mov_b32 s64, 0\n\t" \
  "v_permlane16_swap_b32 v112, v114\n\t" \
  "v_permlane16_swap_b32 v113, v115\n\t" \
  "buffer_store_dwordx4 v[112:115], v232, s[96:99], s64 offen sc1\n\t" \
  "v_cmp_eq_u32 vcc, 16, v152\n\t" \
  "v_lshlrev_b32 v160, 16, v100\n\t" \
  "v_and_b32 v161, 0xffff0000, v100\n\t" \
  "v_lshlrev_b32 v162, 16, v101\n\t" \
  "v_and_b32 v163, 0xffff0000, v101\n\t" \
  "v_lshlrev_b32 v164, 16, v102\n\t" \
  "v_and_b32 v165, 0xffff0000, v102\n\t" \
  "v_lshlrev_b32 v166, 16, v103\n\t" \
  "v_and_b32 v167, 0xffff0000, v103\n\t" \
  "v_pk_fma_f32 v[160:161], v[144:145], v[160:161], v[4:5]\n\t" \
  "v_pk_fma_f32 v[162:163], v[146:147], v[162:163], v[6:7]\n\t" \
  "v_pk_fma_f32 v[164:165], v[148:149], v[164:165], v[20:21]\n\t" \
  "v_pk_fma_f32 v[166:167], v[150:151], v[166:167], v[22:23]\n\t" \
  "v_cndmask_b32 v229, v238, v155, vcc\n\t" \
  "v_cvt_pk_bf16_f32 v116, v160, v161\n\t" \
  "v_cvt_pk_bf16_f32 v118, v164, v165\n\t" \
  "v_cvt_pk_bf16_f32 v117, v162, v163\n\t" \
  "v_cvt_pk_bf16_f32 v119, v166, v167\n\t" \
  "v_or_b32 v232, s78, v229\n\t" \
  "s_mov_b32 s64, 32768\n\t" \
  "v_permlane16_swap_b32 v116, v118\n\t" \
  "v_permlane16_swap_b32 v117, v119\n\t" \
  "buffer_store_dwordx4 v[116:119], v232, s[96:99], s64 offen sc1\n\t" \
  "v_cmp_eq_u32 vcc, 32, v152\n\t" \
  "v_lshlrev_b32 v160, 16, v104\n\t" \
  "v_and_b32 v161, 0xffff0000, v104\n\t" \
  "v_lshlrev_b32 v162, 16, v105\n\t" \
  "v_and_b32 v163, 0xffff0000, v105\n\t" \
  "v_lshlrev_b32 v164, 16, v106\n\t" \
  "v_and_b32 v165, 0xffff0000, v106\n\t" \
  "v_lshlrev_b32 v166, 16, v107\n\t" \
  "v_and_b32 v167, 0xffff0000, v107\n\t" \
  "v_pk_fma_f32 v[160:161], v[144:145], v[160:161], v[8:9]\n\t" \
  "v_pk_fma_f32 v[162:163], v[146:147], v[162:163], v[10:11]\n\t" \
  "v_pk_fma_f32 v[164:165], v[148:149], v[164:165], v[24:25]\n\t" \
  "v_pk_fma_f32 v[166:167], v[150:151], v[166:167], v[26:27]\n\t" \
  "v_cndmask_b32 v230, v238, v155, vcc\n\t" \
  "v_cvt_pk_bf16_f32 v120, v160, v161\n\t" \
  "v_cvt_pk_bf16_f32 v122, v164, v165\n\t" \
  "v_cvt_pk_bf16_f32 v121, v162, v163\n\t" \
  "v_cvt_pk_bf16_f32 v123, v166, v167\n\t" \
  "v_or_b32 v232, s78, v230\n\t" \
  "s_mov_b32 s64, 65536\n\t" \
  "v_permlane16_swap_b32 v120, v122\n\t" \
  "v_permlane16_swap_b32 v121, v123\n\t" \
  "buffer_store_dwordx4 v[120:123], v232, s[96:99], s64 offen sc1\n\t" \
  "v_cmp_eq_u32 vcc, 48, v152\n\t" \
  "v_lshlrev_b32 v160, 16, v108\n\t" \
  "v_and_b32 v161, 0xffff0000, v108\n\t" \
  "v_lshlrev_b32 v162, 16, v109\n\t" \
  "v_and_b32 v163, 0xffff0000, v109\n\t" \
  "v_lshlrev_b32 v164, 16, v110\n\t" \
  "v_and_b32 v165, 0xffff0000, v110\n\t" \
  "v_lshlrev_b32 v166, 16, v111\n\t" \
  "v_and_b32 v167, 0xffff0000, v111\n\t" \
  "v_pk_fma_f32 v[160:161], v[144:145], v[160:161], v[12:13]\n\t" \
  "v_pk_fma_f32 v[162:163], v[146:147], v[162:163], v[14:15]\n\t" \
  "v_pk_fma_f32 v[164:165], v[148:149], v[164:165], v[28:29]\n\t" \
  "v_pk_fma_f32 v[166:167], v[150:151], v[166:167], v[30:31]\n\t" \
  "v_cndmask_b32 v231, v238, v155, vcc\n\t" \
  "v_cvt_pk_bf16_f32 v124, v160, v161\n\t" \
  "v_cvt_pk_bf16_f32 v126, v164, v165\n\t" \
  "v_cvt_pk_bf16_f32 v125, v162, v163\n\t" \
  "v_cvt_pk_bf16_f32 v127, v166, v167\n\t" \
  "v_or_b32 v232, s78, v231\n\t" \
  "s_mov_b32 s64, 98304\n\t" \
  "v_permlane16_swap_b32 v124, v126\n\t" \
  "v_permlane16_swap_b32 v125, v127\n\t" \
  "buffer_store_dwordx4 v[124:127], v232, s[96:99], s64 offen sc1\n\t" \
  STP("s_memrealtime s[90:91]\n\t") \
  STP("s_memtime s[82:83]\n\t") \
  STP("s_waitcnt lgkmcnt(0)\n\t") \
  STP("s_or_b32 s90, s90, 1\n\t") \
  STP("v_writelane_b32 %[stv], s90, 46\n\t") \
  STP("v_writelane_b32 %[stv], s82, 47\n\t") \
  "s_cmp_lg_u32 s84, 0\n\t" \
  "s_cbranch_scc1 .Ltl_oe\n\t" \
  "s_waitcnt vmcnt(4)\n\t" \
  "s_branch .Ltl_c1\n\t" \
  ".Ltl_s0:\n\t" \
  "s_sleep 1\n\t" \
  "buffer_load_dwordx4 v[208:211], v242, %[ws], s62 offen offset:0 sc1\n\t" \
  "buffer_load_dwordx4 v[212:215], v242, %[ws], s62 offen offset:1024 sc1\n\t" \
  "buffer_load_dwordx4 v[216:219], v242, %[ws], s62 offen offset:2048 sc1\n\t" \
  "buffer_load_dwordx4 v[220:223], v242, %[ws], s62 offen offset:3072 sc1\n\t" \
  "s_add_u32 s72, s72, 1\n\t" \
  "s_cmp_lt_u32 s72, %[cap]\n\t" \
  "s_waitcnt vmcnt(0)\n\t" \
  "s_cbranch_scc1 .Ltl_c0\n\t" \
  "buffer_store_dword v243, off, %[ws], 0 offset:256 sc1\n\t" \
  "s_branch .Ltl_k0\n\t" \
  ".Ltl_cd:\n\t" \
  "s_mov_b32 s74, s73\n\t" \
  "s_and_b32 s63, s73, 15\n\t" \
  "v_cmp_eq_u32 vcc, s63, v157\n\t" \
  "s_cmp_lt_u32 s73, 32\n\t" \
  "s_cbranch_scc1 .Ltl_d0\n\t" \
  "s_cmp_lt_u32 s73, 48\n\t" \
  "s_cbranch_scc1 .Ltl_e2\n\t" \
  ".Ltl_e3:\n\t" \
  "v_pk_fma_f32 v[96:97], v[12:13], v[214:215], v[214:215] op_sel:[0,0,1] op_sel_hi:[1,0,1]\n\t" \
  "v_pk_fma_f32 v[98:99], v[14:15], v[214:215], v[214:215] op_sel:[0,0,1] op_sel_hi:[1,0,1]\n\t" \
  "v_pk_fma_f32 v[100:101], v[28:29], v[214:215], v[214:215] op_sel:[0,0,1] op_sel_hi:[1,0,1]\n\t" \
  "v_pk_fma_f32 v[102:103], v[30:31], v[214:215], v[214:215] op_sel:[0,0,1] op_sel_hi:[1,0,1]\n\t" \
  "v_mov_b32 v120, v172\n\t" \
  "v_mov_b32 v122, v173\n\t" \
  "v_mov_b32 v124, v174\n\t" \
  "v_mov_b32 v126, v175\n\t" \
  "s_branch .Ltl_cs\n\t" \
  ".Ltl_e2:\n\t" \
  "v_pk_fma_f32 v[96:97], v[8:9], v[212:213], v[212:213] op_sel:[0,0,1] op_sel_hi:[1,0,1]\n\t" \
  "v_pk_fma_f32 v[98:99], v[10:11], v[212:213], v[212:213] op_sel:[0,0,1] op_sel_hi:[1,0,1]\n\t" \
  "v_pk_fma_f32 v[100:101], v[24:25], v[212:213], v[212:213] op_sel:[0,0,1] op_sel_hi:[1,0,1]\n\t" \
  "v_pk_fma_f32 v[102:103], v[26:27], v[212:213], v[212:213] op_sel:[0,0,1] op_sel_hi:[1,0,1]\n\t" \
  "v_mov_b32 v120, v168\n\t" \
  "v_mov_b32 v122, v169\n\t" \
  "v_mov_b32 v124, v170\n\t" \
  "v_mov_b32 v126, v171\n\t" \
  "s_branch .Ltl_cs\n\t" \
  ".Ltl_d0:\n\t" \
  "s_cmp_lt_u32 s73, 16\n\t" \
  "s_cbranch_scc1 .Ltl_e0\n\t" \
  ".Ltl_e1:\n\t" \
  "v_pk_fma_f32 v[96:97], v[4:5], v[210:211], v[210:211] op_sel:[0,0,1] op_sel_hi:[1,0,1]\n\t" \
  "v_pk_fma_f32 v[98:99], v[6:7], v[210:211], v[210:211] op_sel:[0,0,1] op_sel_hi:[1,0,1]\n\t" \
  "v_pk_fma_f32 v[100:101], v[20:21], v[210:211], v[210:211] op_sel:[0,0,1] op_sel_hi:[1,0,1]\n\t" \
  "v_pk_fma_f32 v[102:103], v[22:23], v[210:211], v[210:211] op_sel:[0,0,1] op_sel_hi:[1,0,1]\n\t" \
  "v_mov_b32 v120, v164\n\t" \
  "v_mov_b32 v122, v165\n\t" \
  "v_mov_b32 v124, v166\n\t" \
  "v_mov_b32 v126, v167\n\t" \
  "s_branch .Ltl_cs\n\t" \
  ".Ltl_e0:\n\t" \
  "v_pk_fma_f32 v[96:97], v[0:1], v[208:209], v[208:209] op_sel:[0,0,1] op_sel_hi:[1,0,1]\n\t" \
  "v_pk_fma_f32 v[98:99], v[2:3], v[208:209], v[208:209] op_sel:[0,0,1] op_sel_hi:[1,0,1]\n\t" \
  "v_pk_fma_f32 v[100:101], v[16:17], v[208:209], v[208:209] op_sel:[0,0,1] op_sel_hi:[1,0,1]\n\t" \
  "v_pk_fma_f32 v[102:103], v[18:19], v[208:209], v[208:209] op_sel:[0,0,1] op_sel_hi:[1,0,1]\n\t" \
  "v_mov_b32 v120, v160\n\t" \
  "v_mov_b32 v122, v161\n\t" \
  "v_mov_b32 v124, v162\n\t" \
  "v_mov_b32 v126, v163\n\t" \
  ".Ltl_cs:\n\t" \
  "v_pk_fma_f32 v[96:97], v[96:97], v[136:137], v[128:129]\n\t" \
  "v_pk_fma_f32 v[98:99], v[98:99], v[138:139], v[130:131]\n\t" \
  "v_pk_fma_f32 v[100:101], v[100:101], v[140:141], v[132:133]\n\t" \
  "v_pk_fma_f32 v[102:103], v[102:103], v[142:143], v[134:135]\n\t" \
  "v_cndmask_b32 v239, v156, v239, vcc\n\t" \
  "v_cvt_pk_bf16_f32 v112, v96, v97\n\t" \
  "v_cvt_pk_bf16_f32 v114, v98, v99\n\t" \
  "v_cvt_pk_bf16_f32 v116, v100, v101\n\t" \
  "v_cvt_pk_bf16_f32 v118, v102, v103\n\t" \
  "buffer_store_dwordx4 v[112:115], v239, %[ws], s66 offen offset:0 sc1\n\t" \
  "buffer_store_dwordx4 v[116:119], v239, %[ws], s66 offen offset:64 sc1\n\t" \
  "buffer_store_dwordx4 v[112:115], v239, %[ws], s77 offen offset:0 sc1\n\t" \
  "buffer_store_dwordx4 v[116:119], v239, %[ws], s77 offen offset:64 sc1\n\t" \
  "buffer_store_dwordx4 v[120:123], v239, %[ws], s67 offen offset:0 sc1\n\t" \
  "buffer_store_dwordx4 v[124:127], v239, %[ws], s67 offen offset:64 sc1\n\t" \
  "s_branch .Ltl_ce\n\t" \
  ".p2align 12\n\t" \
  ".Lht1:\n\t" \
  "s_cbranch_execz .Lht1n\n\t" \
  "s_waitcnt vmcnt(0)\n\t" \
  ".Ltl_c1:\n\t" \
  "v_cmp_eq_u32 vcc, %[tag], v217\n\t" \
  "v_xor_b32 v153, %[tag], v219\n\t" \
  "v_bitop3_b32 v153, v153, v221, %[tag] bitop3:0xf6\n\t" \
  "v_cndmask_b32 v152, 0, v216, vcc\n\t" \
  "v_bitop3_b32 v153, v153, v223, %[tag] bitop3:0xf6\n\t" \
  "v_bitop3_b32 v153, v153, v225, %[tag] bitop3:0xf6\n\t" \
  "v_max_u32_dpp v152, v152, v152 quad_perm:[1,0,3,2] row_mask:0xf bank_mask:0xf\n\t" \
  "s_or_b32 s77, vcc_lo, s68\n\t" \
  "v_cmp_ne_u32 vcc, 0, v153\n\t" \
  "v_max_u32_dpp v152, v152, v152 quad_perm:[2,3,0,1] row_mask:0xf bank_mask:0xf\n\t" \
  "s_nop 1\n\t" \
  "v_max_u32_dpp v152, v152, v152 row_half_mirror row_mask:0xf bank_mask:0xf\n\t" \
  "s_nop 1\n\t" \
  "v_max_u32_dpp v152, v152, v152 row_mirror row_mask:0xf bank_mask:0xf\n\t" \
  "s_nop 1\n\t" \
  "v_max_u32_dpp v152, v152, v152 row_bcast:15 row_mask:0xa bank_mask:0xf\n\t" \
  "s_nop 0\n\t" \
  "v_readlane_b32 s70, v152, 31\n\t" \
  "s_max_u32 s70, s70, s69\n\t" \
  "s_andn2_b32 s63, 0x780, s70\n\t" \
  "s_lshl_b32 s63, s63, 7\n\t" \
  "s_andn2_b32 s64, 64, s70\n\t" \
  "s_lshl_b32 s64, s64, 6\n\t" \
  "s_or_b32 s64, s64, s63\n\t" \
  "s_xor_b32 s63, s64, s71\n\t" \
  "s_mov_b32 s71, s64\n\t" \
  "s_or_b32 vcc_lo, vcc_lo, vcc_hi\n\t" \
  "s_or_b32 s65, s63, vcc_lo\n\t" \
  "s_not_b32 s93, s77\n\t" \
  "s_or_b32 s63, s65, s93\n\t" \
  "s_cbranch_scc1 .Ltl_s1\n\t" \
  ".Ltl_k1:\n\t" \
  STP("s_memrealtime s[90:91]\n\t") \
  STP("s_memtime s[82:83]\n\t") \
  STP("s_waitcnt lgkmcnt(0)\n\t") \
  STP("s_or_b32 s90, s90, 1\n\t") \
  STP("v_writelane_b32 %[stv], s90, 48\n\t") \
  STP("v_writelane_b32 %[stv], s82, 49\n\t") \
  "s_cmp_eq_u32 s62, 6\n\t" \
  "s_cbranch_scc1 .Ltl_nf\n\t" \
  "v_dot2_f32_bf16 v96, v64, v218, 0\n\t" \
  "v_dot2_f32_bf16 v104, v254, v218, 0\n\t" \
  "v_dot2_f32_bf16 v97, v68, v218, 0\n\t" \
  "v_dot2_f32_bf16 v105, v180, v218, 0\n\t" \
  "v_dot2_f32_bf16 v98, v65, v218, 0\n\t" \
  "v_dot2_f32_bf16 v106, v176, v218, 0\n\t" \
  "v_dot2_f32_bf16 v99, v69, v218, 0\n\t" \
  "v_dot2_f32_bf16 v107, v181, v218, 0\n\t" \
  "v_dot2_f32_bf16 v100, v66, v218, 0\n\t" \
  "v_dot2_f32_bf16 v108, v177, v218, 0\n\t" \
  "v_dot2_f32_bf16 v101, v70, v218, 0\n\t" \
  "v_dot2_f32_bf16 v109, v182, v218, 0\n\t" \
  "v_dot2_f32_bf16 v102, v67, v218, 0\n\t" \
  "v_dot2_f32_bf16 v110, v178, v218, 0\n\t" \
  "v_dot2_f32_bf16 v103, v71, v218, 0\n\t" \
  "v_dot2_f32_bf16 v111, v183, v218, 0\n\t" \
  "v_dot2_f32_bf16 v96, v72, v220, v96\n\t" \
  "v_dot2_f32_bf16 v104, v179, v220, v104\n\t" \
  "v_dot2_f32_bf16 v97, v76, v220, v97\n\t" \
  "v_dot2_f32_bf16 v105, v188, v220, v105\n\t" \
  "v_dot2_f32_bf16 v98, v73, v220, v98\n\t" \
  "v_dot2_f32_bf16 v106, v184, v220, v106\n\t" \
  "v_dot2_f32_bf16 v99, v77, v220, v99\n\t" \
  "v_dot2_f32_bf16 v107, v189, v220, v107\n\t" \
  "v_dot2_f32_bf16 v100, v74, v220, v100\n\t" \
  "v_dot2_f32_bf16 v108, v185, v220, v108\n\t" \
  "v_dot2_f32_bf16 v101, v78, v220, v101\n\t" \
  "v_dot2_f32_bf16 v109, v190, v220, v109\n\t" \
  "v_dot2_f32_bf16 v102, v75, v220, v102\n\t" \
  "v_dot2_f32_bf16 v110, v186, v220, v110\n\t" \
  "v_dot2_f32_bf16 v103, v79, v220, v103\n\t" \
  "v_dot2_f32_bf16 v111, v191, v220, v111\n\t" \
  "v_dot2_f32_bf16 v96, v80, v222, v96\n\t" \
  "v_dot2_f32_bf16 v104, v187, v222, v104\n\t" \
  "v_dot2_f32_bf16 v97, v84, v222, v97\n\t" \
  "v_dot2_f32_bf16 v105, v196, v222, v105\n\t" \
  "v_dot2_f32_bf16 v98, v81, v222, v98\n\t" \
  "v_dot2_f32_bf16 v106, v192, v222, v106\n\t" \
  "v_dot2_f32_bf16 v99, v85, v222, v99\n\t" \
  "v_dot2_f32_bf16 v107, v197, v222, v107\n\t" \
  "v_dot2_f32_bf16 v100, v82, v222, v100\n\t" \
  "v_dot2_f32_bf16 v108, v193, v222, v108\n\t" \
  "v_dot2_f32_bf16 v101, v86, v222, v101\n\t" \
  "v_dot2_f32_bf16 v109, v198, v222, v109\n\t" \
  "v_dot2_f32_bf16 v102, v83, v222, v102\n\t" \
  "v_dot2_f32_bf16 v110, v194, v222, v110\n\t" \
  "v_dot2_f32_bf16 v103, v87, v222, v103\n\t" \
  "v_dot2_f32_bf16 v111, v199, v222, v111\n\t" \
  "v_dot2_f32_bf16 v96, v88, v224, v96\n\t" \
  "v_dot2_f32_bf16 v104, v195, v224, v104\n\t" \
  "v_dot2_f32_bf16 v97, v92, v224, v97\n\t" \
  "v_dot2_f32_bf16 v105, v204, v224, v105\n\t" \
  "v_dot2_f32_bf16 v98, v89, v224, v98\n\t" \
  "v_dot2_f32_bf16 v106, v200, v224, v106\n\t" \
  "v_dot2_f32_bf16 v99, v93, v224, v99\n\t" \
  "v_dot2_f32_bf16 v107, v205, v224, v107\n\t" \
  "v_dot2_f32_bf16 v100, v90, v224, v100\n\t" \
  "v_dot2_f32_bf16 v108, v201, v224, v108\n\t" \
  "v_dot2_f32_bf16 v101, v94, v224, v101\n\t" \
  "v_dot2_f32_bf16 v109, v206, v224, v109\n\t" \
  "v_dot2_f32_bf16 v102, v91, v224, v102\n\t" \
  "v_dot2_f32_bf16 v110, v202, v224, v110\n\t" \
  "v_dot2_f32_bf16 v103, v95, v224, v103\n\t" \
  "v_dot2_f32_bf16 v111, v207, v224, v111\n\t" \
  "v_permlane32_swap_b32 v96, v104\n\t" \
  "v_permlane32_swap_b32 v97, v105\n\t" \
  "v_permlane32_swap_b32 v98, v106\n\t" \
  "v_permlane32_swap_b32 v99, v107\n\t" \
  "v_permlane32_swap_b32 v100, v108\n\t" \
  "v_permlane32_swap_b32 v101, v109\n\t" \
  "v_permlane32_swap_b32 v102, v110\n\t" \
  "v_permlane32_swap_b32 v103, v111\n\t" \
  "v_pk_add_f32 v[96:97], v[96:97], v[104:105]\n\t" \
  "v_pk_add_f32 v[100:101], v[100:101], v[108:109]\n\t" \
  "v_pk_add_f32 v[98:99], v[98:99], v[106:107]\n\t" \
  "v_pk_add_f32 v[102:103], v[102:103], v[110:111]\n\t" \
  "v_permlane16_swap_b32 v96, v100\n\t" \
  "v_permlane16_swap_b32 v97, v101\n\t" \
  "v_permlane16_swap_b32 v98, v102\n\t" \
  "v_permlane16_swap_b32 v99, v103\n\t" \
  "v_add_f32 v96, v96, v100\n\t" \
  "v_add_f32 v97, v97, v101\n\t" \
  "v_add_f32 v98, v98, v102\n\t" \
  "v_add_f32 v99, v99, v103\n\t" \
  "v_add_f32_dpp v96, v96, v96 row_ror:8 row_mask:0xf bank_mask:0xf\n\t" \
  "v_add_f32_dpp v97, v97, v97 row_ror:8 row_mask:0xf bank_mask:0xf\n\t" \
  "v_add_f32_dpp v98, v98, v98 row_ror:8 row_mask:0xf bank_mask:0xf\n\t" \
  "v_add_f32_dpp v99, v99, v99 row_ror:8 row_mask:0xf bank_mask:0xf\n\t" \
  "v_add_f32_dpp v96, v96, v96 row_ror:4 row_mask:0xf bank_mask:0xf\n\t" \
  "v_add_f32_dpp v97, v97, v97 row_ror:4 row_mask:0xf bank_mask:0xf\n\t" \
  "v_add_f32_dpp v98, v98, v98 row_ror:4 row_mask:0xf bank_mask:0xf\n\t" \
  "v_add_f32_dpp v99, v99, v99 row_ror:4 row_mask:0xf bank_mask:0xf\n\t" \
  "v_add_f32_dpp v96, v96, v96 row_ror:2 row_mask:0xf bank_mask:0xf\n\t" \
  "v_add_f32_dpp v97, v97, v97 row_ror:2 row_mask:0xf bank_mask:0xf\n\t" \
  "v_add_f32_dpp v98, v98, v98 row_ror:2 row_mask:0xf bank_mask:0xf\n\t" \
  "v_add_f32_dpp v99, v99, v99 row_ror:2 row_mask:0xf bank_mask:0xf\n\t" \
  "ds_write_b128 v245, v[96:99] offset:8192\n\t" \
  "v_mov_b32 v169, v243\n\t" \
  "v_mov_b32 v171, v243\n\t" \
  "s_waitcnt lgkmcnt(0)\n\t" \
  "s_barrier\n\t" \
  "ds_read2st64_b32 v[96:97], v246 offset0:32 offset1:33\n\t" \
  "ds_read2st64_b32 v[98:99], v248 offset0:32 offset1:33\n\t" \
  "s_waitcnt lgkmcnt(0)\n\t" \
  "v_pk_add_f32 v[96:97], v[96:97], v[98:99]\n\t" \
  "s_nop 0\n\t" \
  "v_add_f32 v96, v96, v97\n\t" \
  "v_cvt_pk_bf16_f32 v96, v96, v96\n\t" \
  "v_lshlrev_b32 v97, 16, v96\n\t" \
  "v_lshlrev_b32 v96, 16, v96\n\t" \
  "s_nop 1\n\t" \
  "v_permlane16_swap_b32 v96, v97\n\t" \
  "v_mul_f32 v98, 0xbfb8aa3b, v96\n\t" \
  "v_exp_f32 v98, v98\n\t" \
  "s_nop 0\n\t" \
  "v_add_f32 v98, 1.0, v98\n\t" \
  "v_rcp_f32 v98, v98\n\t" \
  "s_nop 0\n\t" \
  "v_mul_f32 v96, v98, v96\n\t" \
  "v_cvt_pk_bf16_f32 v96, v96, v96\n\t" \
  "v_lshlrev_b32 v96, 16, v96\n\t" \
  "v_mul_f32 v96, v97, v96\n\t" \
  "s_nop 1\n\t" \
  "v_mov_b32_dpp v97, v96 quad_perm:[1,0,3,2] row_mask:0xf bank_mask:0xf\n\t" \
  "v_cvt_pk_bf16_f32 v96, v96, v97\n\t" \
  "s_nop 0\n\t" \
  "v_readlane_b32 s75, v96, 0\n\t" \
  "v_readlane_b32 s76, v96, 2\n\t" \
  "v_readlane_b32 s63, v96, 4\n\t" \
  "v_readlane_b32 s64, v96, 6\n\t" \
  "v_readlane_b32 s65, v96, 8\n\t" \
  "v_readlane_b32 s77, v96, 10\n\t" \
  "v_readlane_b32 s87, v96, 12\n\t" \
  "v_readlane_b32 s88, v96, 14\n\t" \
  "v_dot2_f32_bf16 v100, v240, s75, 0\n\t" \
  "v_dot2_f32_bf16 v101, v34, s75, 0\n\t" \
  "v_dot2_f32_bf16 v102, v32, s75, 0\n\t" \
  "v_dot2_f32_bf16 v103, v35, s75, 0\n\t" \
  "v_dot2_f32_bf16 v100, v33, s76, v100\n\t" \
  "v_dot2_f32_bf16 v101, v38, s76, v101\n\t" \
  "v_dot2_f32_bf16 v102, v36, s76, v102\n\t" \
  "v_dot2_f32_bf16 v103, v39, s76, v103\n\t" \
  "v_dot2_f32_bf16 v100, v37, s63, v100\n\t" \
  "v_dot2_f32_bf16 v101, v42, s63, v101\n\t" \
  "v_dot2_f32_bf16 v102, v40, s63, v102\n\t" \
  "v_dot2_f32_bf16 v103, v43, s63, v103\n\t" \
  "v_dot2_f32_bf16 v100, v41, s64, v100\n\t" \
  "v_dot2_f32_bf16 v101, v46, s64, v101\n\t" \
  "v_dot2_f32_bf16 v102, v44, s64, v102\n\t" \
  "v_dot2_f32_bf16 v103, v47, s64, v103\n\t" \
  "v_dot2_f32_bf16 v100, v45, s65, v100\n\t" \
  "v_dot2_f32_bf16 v101, v50, s65, v101\n\t" \
  "v_dot2_f32_bf16 v102, v48, s65, v102\n\t" \
  "v_dot2_f32_bf16 v103, v51, s65, v103\n\t" \
  "v_dot2_f32_bf16 v100, v49, s77, v100\n\t" \
  "v_dot2_f32_bf16 v101, v54, s77, v101\n\t" \
  "v_dot2_f32_bf16 v102, v52, s77, v102\n\t" \
  "v_dot2_f32_bf16 v103, v55, s77, v103\n\t" \
  "v_dot2_f32_bf16 v100, v53, s87, v100\n\t" \
  "v_dot2_f32_bf16 v101, v58, s87, v101\n\t" \
  "v_dot2_f32_bf16 v102, v56, s87, v102\n\t" \
  "v_dot2_f32_bf16 v103, v59, s87, v103\n\t" \
  "v_dot2_f32_bf16 v100, v57, s88, v100\n\t" \
  "v_dot2_f32_bf16 v101, v62, s88, v101\n\t" \
  "v_dot2_f32_bf16 v102, v60, s88, v102\n\t" \
  "v_dot2_f32_bf16 v103, v63, s88, v103\n\t" \
  "s_nop 0\n\t" \
  "v_cvt_pk_bf16_f32 v168, v100, v101\n\t" \
  "s_nop 0\n\t" \
  "v_cvt_pk_bf16_f32 v170, v102, v103\n\t" \
  "buffer_store_dwordx4 v[168:171], v247, %[ws], s85 offen sc1\n\t" \
  ".Ltl_nf:\n\t" \
  STP("s_memrealtime s[90:91]\n\t") \
  STP("s_memtime s[82:83]\n\t") \
  STP("s_waitcnt lgkmcnt(0)\n\t") \
  STP("s_or_b32 s90, s90, 1\n\t") \
  STP("v_writelane_b32 %[stv], s90, 50\n\t") \
  STP("v_writelane_b32 %[stv], s82, 51\n\t") \
  "s_cmp_lg_u32 s84, 0\n\t" \
  "s_cbranch_scc1 .Ltl_df\n\t" \
  "s_cmp_lg_u32 s78, 0\n\t" \
  "s_cbranch_scc1 .Ltl_o1\n\t" \
  "s_cmp_lt_u32 s74, 64\n\t" \
  "s_cbranch_scc1 .Ltl_o1\n\t" \
  ".Ltl_oe:\n\t" \
  "s_or_b32 s63, %[bid], %[wid]\n\t" \
  "s_cbranch_scc0 .Ltl_ep\n\t" \
  ".Ltl_ee:\n\t" \
  "s_cmp_eq_u32 s62, 6\n\t" \
  "s_cbranch_scc0 .Ltl_end\n\t" \
  "s_lshl_b32 s77, s79, 9\n\t" \
  "s_lshl_b32 s63, s80, 7\n\t" \
  "s_add_u32 s77, s77, s63\n\t" \
  "v_lshl_add_u32 v37, v157, 3, s77\n\t" \
  "s_add_u32 s63, s71, s61\n\t" \
  "s_add_u32 s63, s63, 262144\n\t" \
  "v_add_u32 v33, s63, v37\n\t" \
  "s_andn2_b32 s63, 0x700, s70\n\t" \
  "s_lshl_b32 s63, s63, 6\n\t" \
  "s_andn2_b32 s64, 0xc0, s70\n\t" \
  "s_lshl_b32 s64, s64, 5\n\t" \
  "s_add_u32 s63, s63, s64\n\t" \
  "s_add_u32 s63, s63, s61\n\t" \
  "s_add_u32 s63, s63, 1966080\n\t" \
  "v_and_b32 v34, 0x7ff, v37\n\t" \
  "v_add_u32 v34, s63, v34\n\t" \
  "s_and_b32 s77, s77, 0x7ff\n\t" \
  "s_add_u32 s77, s77, s61\n\t" \
  "s_lshl_b32 s63, %[wid], 15\n\t" \
  "s_add_u32 s77, s77, s63\n\t" \
  "v_and_b32 v36, 0x70, v242\n\t" \
  "v_lshl_add_u32 v32, v154, 14, v36\n\t" \
  "v_and_b32 v35, 24, v244\n\t" \
  "v_lshl_add_u32 v32, v35, 8, v32\n\t" \
  "s_add_u32 s60, s77, 1048576\n\t" \
  "s_add_u32 s65, s77, 1179648\n\t" \
  "s_add_u32 s66, s77, 1310720\n\t" \
  "s_add_u32 s67, s77, 1441792\n\t" \
  "s_add_u32 s68, s77, 1572864\n\t" \
  "s_add_u32 s73, s77, 1703936\n\t" \
  "s_add_u32 s74, s77, 1835008\n\t" \
  "s_lshl_b32 s63, s80, 12\n\t" \
  "s_add_u32 s63, s63, 147456\n\t" \
  "v_lshl_add_u32 v40, v157, 3, s63\n\t" \
  "ds_read_b64 v[40:41], v40 offset:2944\n\t" \
  "s_ashr_i32 s63, s70, 31\n\t" \
  "s_orn2_b32 s63, 0x80000000, s63\n\t" \
  "s_xor_b32 s63, s63, s70\n\t" \
  "s_and_b32 s63, s63, 0xffff0000\n\t" \
  "v_mov_b32 v38, 0xbfb8aa3b\n\t" \
  "v_mul_f32 v38, s63, v38\n\t" \
  "v_exp_f32 v38, v38\n\t" \
  "v_lshrrev_b32 v37, 1, v37\n\t" \
  "v_add_f32 v38, 1.0, v38\n\t" \
  "v_rcp_f32 v38, v38\n\t" \
  "s_andn2_b32 s64, 0x7ff, s70\n\t" \
  "s_lshl_b32 s64, s64, 11\n\t" \
  "v_cvt_pk_bf16_f32 v38, v38, v38\n\t" \
  "v_lshlrev_b32 v38, 16, v38\n\t" \
  "v_lshrrev_b32 v153, 4, v244\n\t" \
  "v_lshl_add_u32 v36, v153, 2, v36\n\t" \
  "v_lshl_add_u32 v36, %[wid], 7, v36\n\t" \
  "v_and_b32 v35, 8, v244\n\t" \
  "v_lshl_add_u32 v36, v35, 7, v36\n\t" \
  "v_lshlrev_b32 v35, 3, v157\n\t" \
  "v_add_u32 v35, 0x3000, v35\n\t" \
  "v_min_u32 v153, 1, v153\n\t" \
  "v_lshl_or_b32 v37, v153, 31, v37\n\t" \
  "s_mov_b32 s75, 0x3f80\n\t" \
  "s_mov_b32 s76, 0x3f800000\n\t" \
  "s_mov_b32 s72, 0\n\t" \
  "buffer_load_dwordx2 v[42:43], v33, %[ws], 0 offen sc1\n\t" \
  "buffer_load_dwordx2 v[44:45], v34, %[ws], 0 offen sc1\n\t" \
  "buffer_load_dwordx4 v[0:3], v32, %[ws], s60 offen sc1\n\t" \
  "buffer_load_dwordx4 v[4:7], v32, %[ws], s65 offen sc1\n\t" \
  "buffer_load_dwordx4 v[8:11], v32, %[ws], s66 offen sc1\n\t" \
  "buffer_load_dwordx4 v[12:15], v32, %[ws], s67 offen sc1\n\t" \
  "buffer_load_dwordx4 v[16:19], v32, %[ws], s68 offen sc1\n\t" \
  "buffer_load_dwordx4 v[20:23], v32, %[ws], s73 offen sc1\n\t" \
  "buffer_load_dwordx4 v[24:27], v32, %[ws], s74 offen sc1\n\t" \
  "s_waitcnt vmcnt(0)\n\t" \
  ".Ltl_c2:\n\t" \
  "v_xor_b32 v152, %[tag], v43\n\t" \
  "v_bitop3_b32 v152, v152, v45, %[tag] bitop3:0xf6\n\t" \
  "v_bitop3_b32 v152, v152, v1, %[tag] bitop3:0xf6\n\t" \
  "v_bitop3_b32 v152, v152, v3, %[tag] bitop3:0xf6\n\t" \
  "v_bitop3_b32 v152, v152, v5, %[tag] bitop3:0xf6\n\t" \
  "v_bitop3_b32 v152, v152, v7, %[tag] bitop3:0xf6\n\t" \
  "v_bitop3_b32 v152, v152, v9, %[tag] bitop3:0xf6\n\t" \
  "v_bitop3_b32 v152, v152, v11, %[tag] bitop3:0xf6\n\t" \
  "v_bitop3_b32 v152, v152, v13, %[tag] bitop3:0xf6\n\t" \
  "v_bitop3_b32 v152, v152, v15, %[tag] bitop3:0xf6\n\t" \
  "v_bitop3_b32 v152, v152, v17, %[tag] bitop3:0xf6\n\t" \
  "v_bitop3_b32 v152, v152, v19, %[tag] bitop3:0xf6\n\t" \
  "v_bitop3_b32 v152, v152, v21, %[tag] bitop3:0xf6\n\t" \
  "v_bitop3_b32 v152, v152, v23, %[tag] bitop3:0xf6\n\t" \
  "v_bitop3_b32 v152, v152, v25, %[tag] bitop3:0xf6\n\t" \
  "v_bitop3_b32 v152, v152, v27, %[tag] bitop3:0xf6\n\t" \
  "v_cmp_ne_u32 vcc, 0, v152\n\t" \
  "s_cbranch_vccnz .Ltl_s2\n\t" \
  ".Ltl_k2:\n\t" \
  "v_dot2_f32_bf16 v48, v0, s75, 0\n\t" \
  "v_dot2_f32_bf16 v49, v0, s76, 0\n\t" \
  "v_dot2_f32_bf16 v50, v2, s75, 0\n\t" \
  "v_dot2_f32_bf16 v51, v2, s76, 0\n\t" \
  "v_dot2_f32_bf16 v48, v4, s75, v48\n\t" \
  "v_dot2_f32_bf16 v49, v4, s76, v49\n\t" \
  "v_dot2_f32_bf16 v50, v6, s75, v50\n\t" \
  "v_dot2_f32_bf16 v51, v6, s76, v51\n\t" \
  "v_dot2_f32_bf16 v48, v8, s75, v48\n\t" \
  "v_dot2_f32_bf16 v49, v8, s76, v49\n\t" \
  "v_dot2_f32_bf16 v50, v10, s75, v50\n\t" \
  "v_dot2_f32_bf16 v51, v10, s76, v51\n\t" \
  "v_dot2_f32_bf16 v48, v12, s75, v48\n\t" \
  "v_dot2_f32_bf16 v49, v12, s76, v49\n\t" \
  "v_dot2_f32_bf16 v50, v14, s75, v50\n\t" \
  "v_dot2_f32_bf16 v51, v14, s76, v51\n\t" \
  "v_dot2_f32_bf16 v48, v16, s75, v48\n\t" \
  "v_dot2_f32_bf16 v49, v16, s76, v49\n\t" \
  "v_dot2_f32_bf16 v50, v18, s75, v50\n\t" \
  "v_dot2_f32_bf16 v51, v18, s76, v51\n\t" \
  "v_dot2_f32_bf16 v48, v20, s75, v48\n\t" \
  "v_dot2_f32_bf16 v49, v20, s76, v49\n\t" \
  "v_dot2_f32_bf16 v50, v22, s75, v50\n\t" \
  "v_dot2_f32_bf16 v51, v22, s76, v51\n\t" \
  "v_dot2_f32_bf16 v48, v24, s75, v48\n\t" \
  "v_dot2_f32_bf16 v49, v24, s76, v49\n\t" \
  "v_dot2_f32_bf16 v50, v26, s75, v50\n\t" \
  "v_dot2_f32_bf16 v51, v26, s76, v51\n\t" \
  "s_nop 1\n\t" \
  "v_permlane32_swap_b32 v48, v50\n\t" \
  "v_permlane32_swap_b32 v49, v51\n\t" \
  "v_add_f32 v48, v48, v50\n\t" \
  "v_add_f32 v49, v49, v51\n\t" \
  "v_and_b32 v43, 0xffff0000, v42\n\t" \
  "v_lshlrev_b32 v42, 16, v42\n\t" \
  "v_permlane16_swap_b32 v48, v49\n\t" \
  "v_add_f32 v48, v48, v49\n\t" \
  "v_and_b32 v45, 0xffff0000, v44\n\t" \
  "v_lshlrev_b32 v44, 16, v44\n\t" \
  "v_add_f32_dpp v48, v48, v48 row_ror:8 row_mask:0xf bank_mask:0xf\n\t" \
  "ds_write_b32 v36, v48 offset:12288\n\t" \
  "s_cmp_lg_u32 %[wid], 0\n\t" \
  "s_waitcnt lgkmcnt(0)\n\t" \
  "s_barrier\n\t" \
  "s_cbranch_scc1 .Ltl_end\n\t" \
  "ds_read2_b64 v[0:3], v35 offset1:16\n\t" \
  "ds_read2_b64 v[4:7], v35 offset0:32 offset1:48\n\t" \
  "s_waitcnt lgkmcnt(1)\n\t" \
  "v_pk_add_f32 v[0:1], v[0:1], v[2:3]\n\t" \
  "s_waitcnt lgkmcnt(0)\n\t" \
  "v_pk_add_f32 v[4:5], v[4:5], v[6:7]\n\t" \
  "s_nop 0\n\t" \
  "v_pk_add_f32 v[0:1], v[0:1], v[4:5]\n\t" \
  "s_nop 0\n\t" \
  "v_cvt_pk_bf16_f32 v0, v0, v1\n\t" \
  "v_and_b32 v1, 0xffff0000, v0\n\t" \
  "v_lshlrev_b32 v0, 16, v0\n\t" \
  "v_mul_f32 v0, v38, v0\n\t" \
  "v_mul_f32 v1, v38, v1\n\t" \
  "v_cvt_pk_bf16_f32 v0, v0, v1\n\t" \
  "v_and_b32 v1, 0xffff0000, v0\n\t" \
  "v_lshlrev_b32 v0, 16, v0\n\t" \
  "v_add_f32 v0, v0, v42\n\t" \
  "v_add_f32 v1, v1, v43\n\t" \
  "v_cvt_pk_bf16_f32 v0, v0, v1\n\t" \
  "v_and_b32 v1, 0xffff0000, v0\n\t" \
  "v_lshlrev_b32 v0, 16, v0\n\t" \
  "v_mul_f32 v0, v0, v40\n\t" \
  "v_mul_f32 v1, v1, v41\n\t" \
  "v_cvt_pk_bf16_f32 v0, v0, v1\n\t" \
  "v_and_b32 v1, 0xffff0000, v0\n\t" \
  "v_lshlrev_b32 v0, 16, v0\n\t" \
  "v_add_f32 v0, v0, v44\n\t" \
  "v_add_f32 v1, v1, v45\n\t" \
  "v_cvt_pk_bf16_f32 v0, v0, v1\n\t" \
  "buffer_store_dword v0, v37, s[96:99], s64 offen\n\t" \
  "s_branch .Ltl_end\n\t" \
  ".Ltl_s1:\n\t" \
  "s_sleep 1\n\t" \
  "buffer_load_dwordx2 v[216:217], v235, %[ws], 0 offen sc1\n\t" \
  "buffer_load_dwordx4 v[218:221], v236, %[ws], s71 offen sc1\n\t" \
  "buffer_load_dwordx4 v[222:225], v236, %[ws], s71 offen offset:16 sc1\n\t" \
  "s_add_u32 s72, s72, 1\n\t" \
  "s_cmp_lt_u32 s72, %[cap]\n\t" \
  "s_waitcnt vmcnt(0)\n\t" \
  "s_cbranch_scc1 .Ltl_c1\n\t" \
  "buffer_store_dword v243, off, %[ws], 0 offset:256 sc1\n\t" \
  "s_branch .Ltl_k1\n\t" \
  ".Ltl_o1:\n\t" \
  "s_cmp_eq_u32 s70, s69\n\t" \
  "s_cselect_b32 s63, 0x80000000, 0\n\t" \
  "s_xor_b32 s77, s78, 0x80000000\n\t" \
  "v_or_b32 v152, s63, v238\n\t" \
  "v_or_b32 v153, s77, v238\n\t" \
  "v_cmp_gt_i32 vcc, 0, v228\n\t" \
  "s_mov_b32 s64, 0\n\t" \
  "s_nop 0\n\t" \
  "v_cndmask_b32 v228, v153, v152, vcc\n\t" \
  "buffer_store_dwordx4 v[112:115], v228, s[96:99], s64 offen sc1\n\t" \
  "v_cmp_gt_i32 vcc, 0, v229\n\t" \
  "s_mov_b32 s64, 32768\n\t" \
  "s_nop 0\n\t" \
  "v_cndmask_b32 v229, v153, v152, vcc\n\t" \
  "buffer_store_dwordx4 v[116:119], v229, s[96:99], s64 offen sc1\n\t" \
  "v_cmp_gt_i32 vcc, 0, v230\n\t" \
  "s_mov_b32 s64, 65536\n\t" \
  "s_nop 0\n\t" \
  "v_cndmask_b32 v230, v153, v152, vcc\n\t" \
  "buffer_store_dwordx4 v[120:123], v230, s[96:99], s64 offen sc1\n\t" \
  "v_cmp_gt_i32 vcc, 0, v231\n\t" \
  "s_mov_b32 s64, 98304\n\t" \
  "s_nop 0\n\t" \
  "v_cndmask_b32 v231, v153, v152, vcc\n\t" \
  "buffer_store_dwordx4 v[124:127], v231, s[96:99], s64 offen sc1\n\t" \
  "s_branch .Ltl_oe\n\t" \
  ".Ltl_df:\n\t" \
  "s_mov_b32 s78, 0\n\t" \
  "s_cmp_eq_u32 s70, s69\n\t" \
  "s_cselect_b32 s74, s74, 0x100\n\t" \
  "s_branch .Ltl_h2\n\t" \
  ".Ltl_ep:\n\t" \
  "s_mov_b32 s63, 2490368\n\t" \
  "buffer_store_dword v243, v249, %[ws], s63 offen sc1\n\t" \
  "s_branch .Ltl_ee\n\t" \
  ".Ltl_s2:\n\t" \
  "s_sleep 1\n\t" \
  "buffer_load_dwordx2 v[42:43], v33, %[ws], 0 offen sc1\n\t" \
  "buffer_load_dwordx2 v[44:45], v34, %[ws], 0 offen sc1\n\t" \
  "buffer_load_dwordx4 v[0:3], v32, %[ws], s60 offen sc1\n\t" \
  "buffer_load_dwordx4 v[4:7], v32, %[ws], s65 offen sc1\n\t" \
  "buffer_load_dwordx4 v[8:11], v32, %[ws], s66 offen sc1\n\t" \
  "buffer_load_dwordx4 v[12:15], v32, %[ws], s67 offen sc1\n\t" \
  "buffer_load_dwordx4 v[16:19], v32, %[ws], s68 offen sc1\n\t" \
  "buffer_load_dwordx4 v[20:23], v32, %[ws], s73 offen sc1\n\t" \
  "buffer_load_dwordx4 v[24:27], v32, %[ws], s74 offen sc1\n\t" \
  "s_add_u32 s72, s72, 1\n\t" \
  "s_cmp_lt_u32 s72, %[cap]\n\t" \
  "s_waitcnt vmcnt(0)\n\t" \
  "s_cbranch_scc1 .Ltl_c2\n\t" \
  "buffer_store_dword v243, off, %[ws], 0 offset:256 sc1\n\t" \
  "s_branch .Ltl_k2\n\t" \
  ".Ltl_end:\n\t" \
  ""
#define TL_CLOB "v208", "v209", "v210", "v211", "v212", "v213", "v214", "v215", "v216", "v217", "v218", "v219", "v220", "v221", "v222", "v223", "v224", "v225", "v226", "v227", "v228", "v229", "v230", "v231", "v232", "v233", "v234", "v235", "v236", "v238", "v239", "v245", "v246", "v247", "v248", "v249", "v250", "s60", "s61", "s62", "s63", "s64", "s65", "s66", "s67", "s68", "s69", "s70", "s71", "s72", "s73", "s74", "s75", "s76", "s77", "s78", "s79", "s80", "s81", "s82", "s83", "s84", "s85", "s86", "s90", "s91", "s92", "s93", "s94", "s96", "s97", "s98", "s99", "vcc", "scc", "memory"
static_assert(W_EPOCH == 2490368 && W_GKEY == 65536 && W_CANDH == 262144 && W_STAT == 524288 && W_YG == 1048576 && W_CANDX == 1966080);
struct WoP { srd_t srd; unsigned wv[2], m, hl, base, wa; };
struct W4C {
  KVP kv;
  unsigned pf1, pf2, pf4, cto, po, px, vo, eso, hl, hl18, frb, xcb, xso, ss, mbp, gvo, l16, gvm, vs, c3;
  WoP wo; u16* out;
  srd_t sG, sU, sD, sX, sR, sM;
  unsigned gv, wa0, fw2, tagv, fbv, tlb, pre, otb, na, gra, fl; u32x32 q[4], up; f32x32 wm; u32x16 o3;
};
struct Ptrs { const u16 *wo, *w_r, *wg, *wu, *wd; u16* out; };
DI void attn_setup(const Ctx& k, W4C& c, const u16* kc, const u16* vc, const u16* pm, srd_t srdx, const Ptrs& a) {
  const unsigned wid = k.wid, fb = k.xcd * 28 + ffn_j(k);
  c.kv.sofk = KRS * 16u * wid;
  const size_t kvoff = ((size_t)k.kvh * SKV + (SKV - WIN)) * DH;
  c.kv.srdK = mk_srd(kc + kvoff, WIN * DH * 2); c.kv.srdV = mk_srd(vc + kvoff, WIN * DH * 2);
  const unsigned go = wid * 256 * FF + fb * 16, nG = (DM * FF - go) * 2;
  c.sG = mk_srd(a.wg + go, nG); c.sU = mk_srd(a.wu + go, nG);
  c.sM = mk_srd(pm + 2048 + k.xcd * 512, DM * 6 * DM * 2 - 4096 - k.xcd * 1024);
  c.wo.srd = mk_srd(a.wo, QD * DM * 2); c.sX = srdx;
  c.sD = mk_srd(a.wd + (size_t)fb * 16 * DM, ffn_idle(k) ? 0u : 32768u); c.sR = mk_srd(a.w_r, DM * 2);
  c.out = a.out;
  asm volatile("" : "+s"(c.out), "+s"(c.kv.srdK), "+s"(c.kv.srdV), "+s"(c.sG), "+s"(c.sU), "+s"(c.sM), "+s"(c.wo.srd), "+s"(c.sD), "+s"(c.sR));
  c.wo.m = wid * 4096; c.wo.base = (k.kvh * 1024 + k.hl * 128 + wid * 16) * (DM * 2) + k.hl * 256;
  c.pf1 = (k.kvh * 1024 + k.j * 32 + wid * 8) * (DM * 2) + (k.xcd & 1) * 8192;
  c.pf4 = k.j * (8 * FF * 2) - ffn_j(k) * 32;
}
DI void attention(Ctx& k, H4& g, W4C& c STAMP_ARG) {
  unsigned nb;
  asm volatile(AT_ASM
               : "+{v[0:31]}"(g.q[0]), "+{v[32:63]}"(g.q[1]), "+{v[64:95]}"(g.q[2]), "+{v[96:127]}"(g.q[3]), [pfd] "+{v246}"(g.pfd), "=&{v[128:159]}"(c.wm), "=&{v[160:175]}"(c.o3), "+{v224}"(c.kv.vksrc[0]), "+{v225}"(c.kv.vksrc[1]), "+{v226}"(c.kv.vksrc[2]), "+{v227}"(c.kv.vksrc[3]), "=&{v[176:207]}"(c.up),
                 "=&{v234}"(c.wo.wa), "=&{v213}"(c.gra), "+{v245}"(g.fvo), "=&{v243}"(c.tagv), "=&{v254}"(c.fbv),
                 "=&{v242}"(c.l16), "+{v247}"(c.tlb), "=&{v252}"(c.vs), [na] "=&s"(c.na), [nb] "=&s"(nb), [eso] "+s"(c.eso), [xcb] "+s"(c.xcb), [frb] "+s"(c.frb), [fw2] "+s"(c.fw2) AT_STV
               : "{v220}"(c.kv.vkb[0]), "{v221}"(c.kv.vkb[1]), "{v222}"(c.kv.vkb[2]), "{v223}"(c.kv.vkb[3]),
                 "{v228}"(c.kv.vvb[0]), "{v229}"(c.kv.vvb[1]), "{v230}"(c.kv.vvsrc[0]), "{v231}"(c.kv.vvsrc[1]), "{v232}"(c.gv), "{v233}"(c.gvm), "{v240}"(c.wo.wv[0]), "{v241}"(c.wo.wv[1]), "{v244}"(c.wa0), "{v248}"(c.c3),
                 [ws] "s"(k.ws), [sK] "s"(c.kv.srdK), [sV] "s"(c.kv.srdV), [sG] "s"(c.sG), [sU] "s"(c.sU), [wo] "s"(c.wo.srd), [sM] "s"(c.sM), [sD] "s"(c.sD), [sR] "s"(c.sR),
                 [m] "s"(c.wo.m), [sofk] "s"(c.wo.m), [hl18] "s"(c.hl18), [wob] "s"(c.wo.base), [pf2] "s"(c.pf2), AT_PF1 [pf4] "s"(c.pf4),
                 [tag] "s"(k.tag), [cap] "i"(kSpinCap), [cto] "s"(c.cto), [po] "s"(c.po), [px] "s"(c.px), [vo] "s"(c.vo), [pm] "s"(k.pm), [ss] "s"(c.ss), [gvo] "s"(c.gvo)
               : AT_CLOB, ACLOB_LO, ACLOB_HI);
  k.nb = nb; c.fl = g.fvo;
#pragma unroll
  for (int i = 0; i < 4; ++i) c.q[i] = g.q[i];
  GAP();
}
constexpr unsigned SCR = 9 * UNIT;
#define STAMPF(I) do { } while (0)
#define FQ_STR2(x) #x
#define FQ_STR(x) FQ_STR2(x)
#define FQ_CAP FQ_STR(SPINCAP)
DI bool tags2(const u32x4& a, unsigned tag) { return (a[1] == tag) & (a[3] == tag); }
DI void bcast4(float v, float (&o)[4]) {
  auto a = __builtin_amdgcn_permlane32_swap(__float_as_uint(v), __float_as_uint(v), false, false);
  auto b = __builtin_amdgcn_permlane16_swap(a[0], a[0], false, false);
  auto c = __builtin_amdgcn_permlane16_swap(a[1], a[1], false, false);
  o[0] = __uint_as_float(b[0]); o[1] = __uint_as_float(b[1]); o[2] = __uint_as_float(c[0]); o[3] = __uint_as_float(c[1]);
}
DI void x_raw2(unsigned m0, srd_t srd, unsigned v0, unsigned v1, unsigned so) {
  asm volatile("s_mov_b32 m0, %0\n\ts_nop 0\n\tbuffer_load_dwordx4 %1, %3, %4 offen lds\n\tbuffer_load_dwordx4 %2, %3, %4 offen offset:1024 lds"
               :: "s"(m0), "v"(v0), "v"(v1), "s"(srd), "s"(so) : "memory");
}
DI void w_touch2(H4& g, const u16* wq, unsigned base, unsigned t) {
  touch(g.pfd, wq, base + ((g.rot + t) & 15u) * kWT);
}
#define TB0 6u
#define TBN 6u
#define SE 0
#define F1 0
#define FT 0
#define MK 0
#define PGA 0
#define WT 0
#define LT 0
#define EL 0
#define PT0 0
#define XH 0
#define S3 0
#define X1L 0
#define PBK 1
#define IL 1
#define GE 0
#define PPOS 2
DI void front(Ctx& k, H4& g, W4C& c STAMP_ARG) {
  argp_t ap = args();
  const u16 *px = ap->x, *kc = ap->kc, *vc = ap->vc, *wqp = ap->wq, *pc_ = ap->c, *pm = ap->w_mod; char* pw = ap->ws;
  GAP();
  const unsigned lane = lane_id(), wid = k.wid, tid = wid * 64 + lane;
  g.pfd = 0;
  {
    unsigned long long pc; unsigned len;
    asm volatile("s_getpc_b64 %0\n.Lfz_a:\n\ts_mov_b32 %1, .Lfz_z-.Lfz_a-4" : "=s"(pc), "=s"(len));
    touch(g.pfd, (const void*)pc, min(((lane * 4 + wid) * 32 + k.j) * 128, len));
  }
  GAP();
  k.ws = mk_srd((char*)(((uintptr_t)pw + 0x1fffff) & ~(uintptr_t)0x1fffff), W_END);
  const srd_t srdx = mk_srd(px, S * DM * 2), srdm = mk_srd(pm, DM * 6 * DM * 2), srdc = mk_srd(pc_, DM * 2);
  asm volatile("" : "+s"(kc), "+s"(vc));
  const unsigned head = k.kvh * 8 + k.hl, row64 = k.rblk * 256 + k.kvh * 64, rot = k.kvh * 4, u0 = k.hl >> 1, kt = rot + ((u0 + wid) & 3), k0 = k.hl & 1, cq = k.kvh ^ 3;
  g.srdW = mk_srd(wqp + head * DH, (DM * QD - head * DH) * 2); g.rot = rot;
  u32x32 xa;
  const unsigned st1 = W_ST1 + (k.rblk << 12);
  u32x4 xs[4];
  const unsigned cbk = (k.xcd & 1) * 32 + k.j, kp = k.kvh;
  const unsigned s3 = S3 && (k.j & 14) == 6, od = s3 & k.j, rg = tid >> (2 + s3), cm = 3 + 4 * s3;
  u32x4 wm[4];
  u32x2 cv = ald8<0>(srdc, rg * 8, kp * 512 + od * 256);
  { const unsigned vo = rg * (4 * 6 * DM * 2) + (tid & cm) * 16, so = kp * (256 * 6 * DM * 2) + cbk * 64 + od * (128 * 6 * DM * 2 - 64);
#pragma unroll
    for (int i = 0; i < 4; ++i) wm[i] = ald16<0>(srdm, vo, so + i * (6 * DM * 2)); }
  unsigned E = ald4<0, true>(k.ws, EL == 0 || lane == 0 ? W_EPOCH : lane == 1 ? W_HT + ((k.rblk & 3) << 20) : lane == 2 ? st1 + (k.kvh * 8 + k.hl) * 128 : W_FLAGH + (k.rblk * 4 + wid) * 128, 0u);
  { const unsigned so = (row64 + k.hl * 8 + 2 * wid) * (DM * 2);
    xs[0] = ald16<0>(srdx, lane * 16, so); xs[1] = ald16<1024>(srdx, lane * 16, so); xs[2] = ald16<2048>(srdx, lane * 16, so); xs[3] = ald16<3072>(srdx, lane * 16, so); }
  unsigned vx = (lane >> 3) * (DM * 2) + (((lane & 7) ^ (lane >> 4)) << 4), vx1 = (vx ^ 64) + 15360, sl[4];
  g.mX = k.ldsb + wid * 8192;
#pragma unroll
  for (int u = 0; u < 4; ++u) sl[u] = (k.rblk * 256 + wid * 16) * (DM * 2) + (rot + ((u0 + u) & 3)) * 128;
#define XRAW(u, rb) x_raw2(g.mX + (u) * GSLOT + (rb) * 2048, srdx, vx, vx1, sl[u] + (((rb) ^ cq) << 17))
  const unsigned hx = XH ? 2 * k0 : 0;
  if (!(LT & 2)) { XRAW(0, hx); XRAW(0, hx + 1); }
  const unsigned tw2 = (k.j * 2 + (wid >> 1)) * (QD * 2) + k.kvh * 2048 + (lane & 7) * 256 + (wid & 1) * 128;
  if (!(LT & 1)) w_touch2(g, wqp, tw2, (lane >> 3) & 3);
  constexpr int NL = (LT & 1) + 2 * (LT & 2);
  GAP();
  g.mW = k.ldsb + 32768 + wid * 4096;
#define X1() do { XRAW(1, 0); XRAW(1, 1); XRAW(1, 2); XRAW(1, 3); } while (0)
#define X01() do { XRAW(0, 2 - hx); XRAW(0, 3 - hx); if (!X1L) X1(); } while (0)
#define X2() do { XRAW(2, 0); XRAW(2, 1); XRAW(2, 2); XRAW(2, 3); } while (0)
  const unsigned tb = k.ldsb + SCR + 1024;
  u32x16 wbs; u32x8 vw, ma; u32x4 mb; const u16* kvt; unsigned pso, fw, w2, w3, gso, gpo;
  auto setup_w = [&]() {
    w_voffs(g.voffW, QD, wid, lane, k0);
    gso = st1 + (wid * 16 + (lane & 15)) * 16; gpo = W_MODA + ((lane >> 5) * 1024 + kt * 64 + (lane & 31) * 2) * 8;
    asm volatile("" : W4(g.voffW), "+v"(gso), "+v"(gpo));
    GAP();
  };
  auto setup_b = [&]() {
    g.voffX = wid * 8192 + ((lane >> 4) ^ cq) * 256 + (lane & 15) * 16; g.soX = W_HT + ((k.rblk & 3) << 20) + (((k.xcd ^ wid) & 1) << 13) + wid * 8192; g.fo = W_FLAGH + (k.rblk * 4 + wid) * 128;
    h4_init_addr(g, k.ldsb, wid, lane, k0);
    const unsigned xr = g.mX + (lane & 15) * 128 + (((4 * k0 + (lane >> 4)) ^ ((lane >> 1) & 7)) << 4);
    vw = u32x8{g.voffW[0], g.voffW[1], g.voffW[2], g.voffW[3], xr, xr ^ 64, xr + 2 * GSLOT, (xr ^ 64) + 2 * GSLOT};
#pragma unroll
    for (int i = 0; i < 8; ++i) { wbs[i] = g.wb[0][i]; wbs[8 + i] = g.wb[1][i]; }
    ma = u32x8{tb + (lane >> 4) * 32, g.voffX, (lane & 31) * 4, 0u, vx, vx1, g.xb1,
               WT == 1 ? tw2 + ((rot + 4 + (k.xcd & 1) + 2 * min(lane >> 3, 5u)) & 15u) * kWT : WT == 2 ? tw2 + ((rot + 4 + (k.xcd & 1) + 2 * min(lane >> 3, 2u)) & 15u) * kWT
                       : tw2 - (lane & 7) * 256 + (1 + 4 * (lane & 1)) * 256 + ((rot + 4 + min(lane >> 1, 11u)) & 15u) * kWT};
    mb = u32x4{WT == 2 ? tw2 + ((rot + 10 + (k.xcd & 1) + 2 * min(lane >> 3, 2u)) & 15u) * kWT : (tw2 & ~(((lane & 3) == 1) ? 256u : 0u)) + ((rot + TB0 + min(lane >> 3, TBN - 1u)) & 15u) * kWT, ((k.j & 15) * 64 + wid * 16 + (lane & 15)) * 128, 0u, W_SIL + k.bid * 512 + tid * 2};
    kvt = ((k.j & 16) ? vc : kc) + ((size_t)k.kvh * SKV + (SKV - WIN)) * DH;
    pso = g.soX + (rot + u0) * kXT + cq * 256 - GSLOT - k.ldsb; fw = g.fo + head * 4; w2 = (rot + ((u0 + 2) & 3)) * kWT; w3 = (rot + ((u0 + 3) & 3)) * kWT;
    asm volatile("" : "+{v[184:199]}"(wbs), "+{v[200:207]}"(vw), "+{v[208:215]}"(ma), "+{v[216:219]}"(mb), "+s"(kvt), "+s"(pso), "+s"(fw), "+s"(w2), "+s"(w3));
    GAP();
  };
  if constexpr (SE & 2) { setup_w(); setup_b(); }
  unsigned tag;
  HOP_GO(2, wid);
  unsigned la = k.ldsb + SCR + wid * 256 + (4 * (lane & 7) + 2 * (lane >> 5) + ((lane >> 4) & 1)) * 8 + (lane & 8) * 512, lr = k.ldsb + SCR + lane * 8;
  unsigned pa_ = (W_MODA + od * (W_MODC - W_MODA - 256) + (kp * 2048 + cbk * 32 + lane * 2) * 8) | (lane < (16u << s3) ? 0u : 0x80000000u);
  unsigned ps_ = (st1 + (k.kvh * 64 + k.hl * 8 + 2 * wid + (lane >> 5)) * 16) | ((lane & 31) == 0 ? 0u : 0x80000000u);
  asm volatile("" : "+v"(la), "+v"(lr), "+v"(pa_), "+v"(ps_));
  GAP();
  asm volatile("s_waitcnt vmcnt(%1)" : "+v"(cv) : "i"(14 + PT0 - NL) : "memory");
  unsigned sc[2];
#pragma unroll
  for (int i = 0; i < 2; ++i) { const float a = lo16f(cv[i]), b = hi16f(cv[i]); sc[i] = cvtpk(a * __builtin_amdgcn_rcpf(1.f + __expf(-a)), b * __builtin_amdgcn_rcpf(1.f + __expf(-b))); }
  asm volatile("" : "+v"(sc[0]), "+v"(sc[1]));
  GAP();
  STAMPF(27);
  asm volatile("s_waitcnt vmcnt(%4)" : W4(wm) : "i"(10 + PT0 - NL) : "memory");
  STAMP(1);
  if (LT & 2) { XRAW(0, 0); XRAW(0, 1); }
  if (LT & 1) w_touch2(g, wqp, tw2, (lane >> 3) & 3);
  GAP();
  {
    float a[8];
#pragma unroll
    for (int d = 0; d < 4; ++d) {
      a[2 * d] = dot2(plo(wm[2][d], wm[3][d]), sc[1], dot2z(plo(wm[0][d], wm[1][d]), sc[0]));
      a[2 * d + 1] = dot2(phi(wm[2][d], wm[3][d]), sc[1], dot2z(phi(wm[0][d], wm[1][d]), sc[0]));
    }
    float c0 = fold16(fold32(a[0], a[4]), fold32(a[2], a[6])), c1 = fold16(fold32(a[1], a[5]), fold32(a[3], a[7]));
    c0 += dpp_f<0x128>(c0); c1 += dpp_f<0x128>(c1);
    if (S3) { const float kf = s3 ? 0.f : 1.f; c0 = fmaf(dpp_f<0x124>(c0), kf, c0); c1 = fmaf(dpp_f<0x124>(c1), kf, c1); }
    else    { c0 += dpp_f<0x124>(c0); c1 += dpp_f<0x124>(c1); }
    lds_st(la, f32x2{c0, c1});
    asm volatile("s_waitcnt vmcnt(%1)" : "+v"(E) : "i"(9 + PT0) : "memory");
    tag = ((rfl(E) | TAGM) + TAGC) & ~TAGM;
    k.tag = tag;
    unsigned tv = tag;
    asm volatile("s_waitcnt lgkmcnt(0)\n\ts_barrier" : "+v"(tv) :: "memory");
    if (wid == 0) {
      const f32x2 s = (lds_ld<f32x2>(lr) + lds_ld<f32x2>(lr + 256)) + (lds_ld<f32x2>(lr + 512) + lds_ld<f32x2>(lr + 768));
      st16(k.ws, pa_, u32x4{__float_as_uint(s[0]), tv, __float_as_uint(s[1]), tv});
    }
  }
  GAP();
  asm volatile("s_waitcnt vmcnt(%5)\n\ts_cmp_eq_u32 %4, 0\n\ts_cbranch_scc1 .Lxs%=\n\ts_waitcnt vmcnt(%6)\n.Lxs%=:" : W4(xs) : "s"(wid), "i"(6 + PT0), "i"(5 + PT0) : "memory", "scc");
  STAMP(2);
  {
    float sm[2], sq[2];
#pragma unroll
    for (int r = 0; r < 2; ++r) { sm[r] = dot2z(xs[2 * r][0], 0x3f803f80u); sq[r] = dot2z(xs[2 * r][0], xs[2 * r][0]); }
#pragma unroll
    for (int e = 1; e < 8; ++e)
#pragma unroll
      for (int r = 0; r < 2; ++r) { const unsigned w = xs[2 * r + (e >> 2)][e & 3]; sm[r] = dot2(w, 0x3f803f80u, sm[r]); sq[r] = dot2(w, w, sq[r]); }
    const float z = sum_row16(fold16(fold32(sm[0], sm[1]), fold32(sq[0], sq[1]))) * (1.f / DM);
    auto zz = __builtin_amdgcn_permlane16_swap(__float_as_uint(z), __float_as_uint(z), false, false);
    const float mean = __uint_as_float(zz[0]);
    const float rstd = __builtin_amdgcn_rsqf(fmaxf(fmaf(-mean, mean, __uint_as_float(zz[1])), 0.f) + 1e-5f);
    st16(k.ws, ps_, u32x4{__float_as_uint(rstd), tag, __float_as_uint(-mean * rstd), tag});
  }
  GAP();
  STAMP(3);
  Ptrs pa;
  { argp_t aq = args(); pa = Ptrs{aq->wo, aq->w_r, aq->wg, aq->wu, aq->wd, aq->out}; }
  if (IL) { XRAW(0, 2 - hx); XRAW(0, 3 - hx); if (IL & 2) XRAW(1, 0); GAP(); } else X01();
  unsigned cb;
  asm volatile("buffer_load_ushort %0, %1, %2, %3 offen" : "=v"(cb) : "v"(tid * 2), "s"(srdc), "s"((k.j >> 3) * 512) : "memory");
  if constexpr (!(SE & 2)) setup_w();
  if (IL) { if (!(IL & 2)) XRAW(1, 0); XRAW(1, 1); if (IL & 2) XRAW(1, 2); } else w_dma4(g.mW, g.srdW, g.voffW, (rot + u0) * kWT);
#define W1() w_dma4(g.mW + GSLOT, g.srdW, g.voffW, (rot + ((u0 + 1) & 3)) * kWT)
#define W2() w_dma4(g.mW + 2 * GSLOT, g.srdW, g.voffW, (rot + ((u0 + 2) & 3)) * kWT)
  if constexpr (FQ_W1L == 0) W1();
  if constexpr (FQ_X2E == 2) X2();
  if constexpr (FQ_W2E == 2) W2();
  u32x4 st[4], p[4], p2[4];
  const bool k3 = S3 && (kt & 7) == 3;
  const unsigned g2 = gpo | (k3 ? 0u : 0x80000000u);
#define POLL() do { \
    asm volatile("buffer_load_dwordx4 %0, %4, %5, %6 offen sc1\n\tbuffer_load_dwordx4 %1, %4, %5, %7 offen sc1\n\tbuffer_load_dwordx4 %2, %4, %5, %8 offen sc1\n\tbuffer_load_dwordx4 %3, %4, %5, %9 offen sc1" \
                 : "=&{v[160:163]}"(st[0]), "=&{v[164:167]}"(st[1]), "=&{v[168:171]}"(st[2]), "=&{v[172:175]}"(st[3]) : "v"(gso), "s"(k.ws), "s"(cq << 10), "s"((cq ^ 1) << 10), "s"((cq ^ 2) << 10), "s"((cq ^ 3) << 10) : "memory"); \
    asm volatile("buffer_load_dwordx4 %0, %4, %5, 0 offen sc1\n\tbuffer_load_dwordx4 %1, %4, %5, %6 offen sc1\n\tbuffer_load_dwordx4 %2, %4, %5, %7 offen sc1\n\tbuffer_load_dwordx4 %3, %4, %5, %8 offen sc1" \
                 : "=&{v[128:131]}"(p[0]), "=&{v[132:135]}"(p[1]), "=&{v[136:139]}"(p[2]), "=&{v[140:143]}"(p[3]) : "v"(gpo), "s"(k.ws), "s"(16384u), "s"(32768u), "s"(49152u) : "memory"); \
    if (S3) asm volatile("buffer_load_dwordx4 %0, %4, %5, %6 offen sc1\n\tbuffer_load_dwordx4 %1, %4, %5, %7 offen sc1\n\tbuffer_load_dwordx4 %2, %4, %5, %8 offen sc1\n\tbuffer_load_dwordx4 %3, %4, %5, %9 offen sc1" \
                 : "=&{v[144:147]}"(p2[0]), "=&{v[148:151]}"(p2[1]), "=&{v[152:155]}"(p2[2]), "=&{v[156:159]}"(p2[3]) : "v"(g2), "s"(k.ws), "s"(W_MODC - W_MODA), "s"(W_MODC - W_MODA + 16384u), "s"(W_MODC - W_MODA + 32768u), "s"(W_MODC - W_MODA + 49152u) : "memory"); \
    GAP(); } while (0)
#define POLL_WAIT(N) asm volatile("s_waitcnt vmcnt(%12)" : "+{v[160:163]}"(st[0]), "+{v[164:167]}"(st[1]), "+{v[168:171]}"(st[2]), "+{v[172:175]}"(st[3]), "+{v[128:131]}"(p[0]), "+{v[132:135]}"(p[1]), "+{v[136:139]}"(p[2]), \
                                  "+{v[140:143]}"(p[3]), "+{v[144:147]}"(p2[0]), "+{v[148:151]}"(p2[1]), "+{v[152:155]}"(p2[2]), "+{v[156:159]}"(p2[3]) : "i"(N) : "memory")
  GAP();
  if (PBK) { HOP_FRONT(); }
  if constexpr (PPOS == 0) POLL();
  if constexpr (!(SE & 2)) setup_b();
  if constexpr (PPOS == 2 && !IL) POLL();
  if (X1L) X1();
  if (IL) { if (!(IL & 2)) XRAW(1, 2); XRAW(1, 3); if (IL & 2) w_dma4(g.mW, g.srdW, g.voffW, (rot + u0) * kWT); GAP(); }
  attn_setup(k, c, kc, vc, pm, srdx, pa);
  GAP();
  if (IL && !(IL & 2)) { w_dma4(g.mW, g.srdW, g.voffW, (rot + u0) * kWT); GAP(); }
  if (IL & 4) POLL();
  ma[3] = tag;
  static_assert(W_XCH == W_OT + (16u << 20));
  const unsigned vp = k.xcd * 2 + ((k.j >> 1) & 1);
  unsigned o_rot = rot << 19, o_sox = g.soX - GSLOT - k.ldsb, o_dk = 4096u - k0 * 8192u, o_c17 = cq << 17, o_w1 = (rot + ((u0 + 1) & 3)) * kWT, o_idm = ffn_idle(k) ? 0x80000000u : 0u;
  unsigned o_prb = ((k.j & 1) ? W_OT + (vp << 21) : W_PROBE + ((vp & 1) << 13) + vp * 128) | (ffn_idle(k) && wid == 0 ? 0u : 0x80000000u), o_pto = (blockIdx.x * 4 + wid) * 2048;
  unsigned ta = tb + wid * 512 + (((lane ^ 32) * 8) ^ (k0 * 128)); float one = lane >= 32 ? 1.f : 0.f;
  asm volatile("" : "+{v[208:215]}"(ma), "+s"(o_rot), "+s"(o_sox), "+s"(o_dk), "+s"(o_c17), "+s"(o_w1), "+s"(o_idm), "+s"(o_prb), "+s"(o_pto), "+s"(sl[2]), "+s"(sl[3]), "+s"(c.pf1), "+s"(c.pf4), "+s"(c.wo.base),
                    "+s"(k.pm), "+v"(ta), "+v"(one));
  GAP();
  if (IL && !(IL & 4)) POLL();
  if constexpr (FQ_W1L == 2) W1();
  if constexpr (FQ_X2E == 1) X2();
  if constexpr (FQ_W2E == 1) W2();
  GAP();
  STAMPF(5);
  HOP_GO(0, wid);
  if (!PBK) { HOP_FRONT(); }
  STAMPF(6);
  {
    POLL_WAIT((X1L ? 8 : 0) + (FQ_X2E == 1 ? 8 : 0) + (FQ_W1L == 2 ? 4 : 0) + (FQ_W2E == 1 ? 4 : 0));
    asm volatile("ds_read_b128 v[0:3], %3\n\tds_read_b128 v[4:7], %3 offset:2048\n\tds_read_b128 v[8:11], %3 offset:4096\n\tds_read_b128 v[12:15], %3 offset:6144\n\t"
                 "ds_read_b128 v[16:19], %4\n\tds_read_b128 v[20:23], %4 offset:2048\n\tds_read_b128 v[24:27], %4 offset:4096\n\tds_read_b128 v[28:31], %4 offset:6144"
                 : "=&{v[0:31]}"(xa), "+v"(g.pfd), "+v"(cb) : "v"(vw[4]), "v"(vw[5]) : "memory");
    STAMPF(7);
    static_assert(W_MODC - W_MODA == 0x60000);
#define FR_C2
#define FR_L3
    unsigned bad, sp = 0;
    asm volatile(".Lfr_c:\n\t" "v_xor_b32 %[b], %[t], v161\n\t" "v_bitop3_b32 %[b], %[b], v163, %[t] bitop3:0xf6\n\t" "v_bitop3_b32 %[b], %[b], v165, %[t] bitop3:0xf6\n\t" "v_bitop3_b32 %[b], %[b], v167, %[t] bitop3:0xf6\n\t" "v_bitop3_b32 %[b], %[b], v169, %[t] bitop3:0xf6\n\t" "v_bitop3_b32 %[b], %[b], v171, %[t] bitop3:0xf6\n\t" "v_bitop3_b32 %[b], %[b], v173, %[t] bitop3:0xf6\n\t" "v_bitop3_b32 %[b], %[b], v175, %[t] bitop3:0xf6\n\t" "v_bitop3_b32 %[b], %[b], v129, %[t] bitop3:0xf6\n\t" "v_bitop3_b32 %[b], %[b], v131, %[t] bitop3:0xf6\n\t" "v_bitop3_b32 %[b], %[b], v133, %[t] bitop3:0xf6\n\t" "v_bitop3_b32 %[b], %[b], v135, %[t] bitop3:0xf6\n\t" "v_bitop3_b32 %[b], %[b], v137, %[t] bitop3:0xf6\n\t" "v_bitop3_b32 %[b], %[b], v139, %[t] bitop3:0xf6\n\t" "v_bitop3_b32 %[b], %[b], v141, %[t] bitop3:0xf6\n\t" "v_bitop3_b32 %[b], %[b], v143, %[t] bitop3:0xf6\n\t" FR_C2 "v_cmp_ne_u32 vcc, 0, %[b]\n\ts_cbranch_vccnz .Lfr_s\n.Lfr_k:\n\t.subsection 1\n.Lfr_s:\n\ts_sleep 1\n\t" "buffer_load_dwordx4 v[160:163], %[gs], %[ws], %[q0] offen sc1\n\t" "buffer_load_dwordx4 v[164:167], %[gs], %[ws], %[q1] offen sc1\n\t" "buffer_load_dwordx4 v[168:171], %[gs], %[ws], %[q2] offen sc1\n\t" "buffer_load_dwordx4 v[172:175], %[gs], %[ws], %[q3] offen sc1\n\t" "s_mov_b32 vcc_lo, 0\n\t" "buffer_load_dwordx4 v[128:131], %[gp], %[ws], vcc_lo offen sc1\n\t" "s_mov_b32 vcc_lo, 16384\n\t" "buffer_load_dwordx4 v[132:135], %[gp], %[ws], vcc_lo offen sc1\n\t" "s_mov_b32 vcc_lo, 32768\n\t" "buffer_load_dwordx4 v[136:139], %[gp], %[ws], vcc_lo offen sc1\n\t" "s_mov_b32 vcc_lo, 49152\n\t" "buffer_load_dwordx4 v[140:143], %[gp], %[ws], vcc_lo offen sc1\n\t" FR_L3
                 "s_add_u32 %[sp], %[sp], 1\n\ts_cmp_lt_u32 %[sp], %[cap]\n\ts_waitcnt vmcnt(0)\n\ts_cbranch_scc1 .Lfr_c\n\tbuffer_store_dword v211, off, %[ws], 0 offset:256 sc1\n\ts_branch .Lfr_k\n\t.subsection 0"
                 : "+{v[160:163]}"(st[0]), "+{v[164:167]}"(st[1]), "+{v[168:171]}"(st[2]), "+{v[172:175]}"(st[3]), "+{v[128:131]}"(p[0]), "+{v[132:135]}"(p[1]), "+{v[136:139]}"(p[2]), "+{v[140:143]}"(p[3]),
                   "+{v[144:147]}"(p2[0]), "+{v[148:151]}"(p2[1]), "+{v[152:155]}"(p2[2]), "+{v[156:159]}"(p2[3]), [b] "=&v"(bad), [sp] "+s"(sp)
                 : [t] "s"(tag), [t2] "s"(k3 ? tag : 0u), [gs] "v"(gso), [gp] "v"(gpo), [g2] "v"(g2), [ws] "s"(k.ws), [q0] "s"(cq << 10), [q1] "s"((cq ^ 1) << 10), [q2] "s"((cq ^ 2) << 10), [q3] "s"((cq ^ 3) << 10),
                   [cap] "i"(kSpinCap), "{v[208:215]}"(ma) : "vcc", "scc", "memory");
    STAMP(4);
    if (FQ_TE & 1) touch(g.pfd, wqp, ma[7]);
    if (FQ_TE & 2) touch(g.pfd, wqp, mb[0]);
    if (FQ_TE & 4) touch(g.pfd, kvt, mb[1]);
    GAP();
    f32x2 m[4];
#pragma unroll
    for (int i = 0; i < 4; ++i) { m[i] = f32x2{__uint_as_float(p[i][0]), __uint_as_float(p[i][2])}; if (S3) m[i] += f32x2{__uint_as_float(p2[i][0]), __uint_as_float(p2[i][2])}; }
    f32x2 s = (m[0] + m[1]) + (m[2] + m[3]);
    s = up2(cvtpk(s[0], s[1])) + one;
    lds_st(ta, up2(cvtpk(s[0], s[1])) * QSCALE);
    GAP();
    mb[2] = __float_as_uint(silu_bf(lo16f(cb))) >> 16;
    asm volatile("" : "+{v[216:219]}"(mb));
    STAMPF(12);
    if constexpr (FQ_EB) asm volatile("s_waitcnt lgkmcnt(1)" ::: "memory"); else BARRIER_LGKM();
    STAMPF(13);
  }
  g.q[0] = xa;
  asm volatile(FQ_ASM : "+{v[0:31]}"(g.q[0]), "=&{v[32:63]}"(g.q[1]), "=&{v[64:95]}"(g.q[2]), "=&{v[96:127]}"(g.q[3]), [fl] "=&v"(g.fl), [pf] "+{v246}"(g.pfd), "+{v[200:207]}"(vw), "+{v[208:215]}"(ma), "+{v[216:219]}"(mb),
                 "=&{v220}"(c.kv.vkb[0]), "=&{v221}"(c.kv.vkb[1]), "=&{v222}"(c.kv.vkb[2]), "=&{v223}"(c.kv.vkb[3]), "=&{v224}"(c.kv.vksrc[0]), "=&{v225}"(c.kv.vksrc[1]), "=&{v226}"(c.kv.vksrc[2]), "=&{v227}"(c.kv.vksrc[3]),
                 "=&{v228}"(c.kv.vvb[0]), "=&{v229}"(c.kv.vvb[1]), "=&{v230}"(c.kv.vvsrc[0]), "=&{v231}"(c.kv.vvsrc[1]), "=&{v232}"(c.gv), "=&{v240}"(c.wo.wv[0]), "=&{v241}"(c.wo.wv[1]), "=&{v244}"(c.wa0),
                 "=&{v245}"(g.fvo), "=&{v247}"(c.tlb), "=&{v233}"(c.gvm), "=&{v248}"(c.c3),
                 [hl] "=&s"(c.hl), [hl18] "=&s"(c.hl18), [cto] "=&s"(c.cto), [pf2] "=&s"(c.pf2), [po] "=&s"(c.po), [px] "=&s"(c.px), [vo] "=&s"(c.vo), [eso] "=&s"(c.eso), [otb] "=&s"(c.otb),
                 [xcb] "=&s"(c.xcb), [frb] "=&s"(c.frb), [fw2] "=&s"(c.fw2), [xso] "=&s"(c.xso), [ss] "=&s"(c.ss), [mbp] "=&s"(c.mbp), [gvo] "=&s"(c.gvo) FQ_STV
               : "{v[160:163]}"(st[0]), "{v[164:167]}"(st[1]), "{v[168:171]}"(st[2]), "{v[172:175]}"(st[3]), "{v[184:199]}"(wbs),
                 [ws] "s"(k.ws), [sw] "s"(g.srdW), [sx] "s"(srdx), [sK] "s"(c.kv.srdK), [sV] "s"(c.kv.srdV), FQ_SD [mw] "s"(g.mW), [mx] "s"(g.mX),
                 [rot19] "s"(o_rot), [w2] "s"(w2), [w3] "s"(w3), [sox] "s"(o_sox), [dk] "s"(o_dk), [fo] "s"(g.fo), [fw] "s"(fw), [pso] "s"(pso), [c17] "s"(o_c17), [sl2] "s"(sl[2]), [w1] "s"(o_w1),
                 [sl3] "s"(sl[3]), [wq] "s"(wqp), [kv] "s"(kvt), [sofk] "s"(c.wo.m), [pf4] "s"(c.pf4), [pf1] "s"(c.pf1), [wo] "s"(c.wo.srd),
                 [bid] "s"(k.bid), [wid] "s"(wid), [idm] "s"(o_idm), [prb] "s"(o_prb), [pto] "s"(o_pto)
               : FQ_CL, ACLOB_LO, ACLOB_HI);
  GAP();
}
DI void oproj(Ctx& k, W4C& c STAMP_ARG) {
  const unsigned wid = k.wid, tag = k.tag; unsigned gad, ln;
  u32x32 UP = c.up, DN = c.q[1]; f32x32 A3 = __builtin_bit_cast(f32x32, c.q[3]), XT = __builtin_bit_cast(f32x32, c.q[0]); float cww, bbw;
  asm volatile(OP_ASM
               : "+{v[176:207]}"(UP), "+{v[160:175]}"(c.o3), "+{v[32:63]}"(DN), "+{v[96:127]}"(A3), "+{v[0:31]}"(XT), "+{v[64:95]}"(c.q[2]), "+{v[128:159]}"(c.wm), "+{v224}"(c.kv.vksrc[0]), "+{v225}"(c.kv.vksrc[1]), "+{v226}"(c.kv.vksrc[2]), "+{v227}"(c.kv.vksrc[3]), "=&{s87}"(cww), "=&{s88}"(bbw),
                 "=&{v237}"(gad), "=&{v244}"(ln), "+{v213}"(c.gra), "+{v240}"(c.wo.wv[0]), "+{v241}"(c.wo.wv[1]), "+{v247}"(c.tlb), "+{v252}"(c.vs), "+{v254}"(c.fbv), [pm] "+s"(k.pm)
               : "{v234}"(c.wo.wa), "{v242}"(c.l16), "{v243}"(c.tagv), "{v245}"(c.fl),
                 [ws] "s"(k.ws), [wo] "s"(c.wo.srd), [sd] "s"(c.sD), [sx] "s"(c.sX), [sr] "s"(c.sR),
                 [tag] "s"(tag), [wid] "s"(wid), [hl] "s"(c.hl), [eso] "s"(c.eso), [na] "s"(c.na), [frb] "s"(c.frb), [xcb] "s"(c.xcb), [xso] "s"(c.xso),
                 [m] "s"(c.wo.m), [wob] "s"(c.wo.base), [ss] "s"(c.ss), [mbp] "s"(c.mbp), [cap] "i"(kSpinCap), [fw2] "s"(c.fw2)
               : OP_CLOB, ACLOB_LO, ACLOB_HI);
  if (__builtin_expect(k.pm == 2, 0)) {
    asm volatile("s_getpc_b64 vcc\n.Lrs_a:\n\ts_sub_u32 vcc_lo, vcc_lo, .Lrs_a-.Lfz_a\n\ts_subb_u32 vcc_hi, vcc_hi, 0\n\ts_and_b32 vcc_lo, vcc_lo, 0xfffff000\n\ts_setpc_b64 vcc"
                 :: "{s[0:1]}"(__builtin_amdgcn_kernarg_segment_ptr()), "{s2}"(blockIdx.x), "{v0}"((wid * 64 + lane_id()) | 0x10000u) : "vcc", "scc", "memory");
    __builtin_unreachable();
  }
  asm volatile(TL_ASM
               : "+{v[176:207]}"(UP), "+{v[32:63]}"(DN), "+{v[96:127]}"(A3), "+{v[0:31]}"(XT), "+{v[64:95]}"(c.q[2]), "+{v[128:159]}"(c.wm), "+{v[160:175]}"(c.o3), "+{s87}"(cww), "+{s88}"(bbw), "+{v240}"(c.wo.wv[0])
               : "{v237}"(gad), "{v254}"(c.fbv), "{v242}"(c.l16), "{v243}"(c.tagv), "{v244}"(ln),
                 [ws] "s"(k.ws), [tag] "s"(tag), [wid] "s"(wid), [bid] "s"(k.bid), [nb] "s"(k.nb), [out] "s"(c.out), [cap] "i"(kSpinCap)
               : TL_CLOB);
  STAMPO(26);
}
#define PERM 0
__global__ __launch_bounds__(256, 1) __attribute__((aligned(4096))) void k_fused(Args a) {
  Ctx k;
  const unsigned bid = PERM ? ((blockIdx.x << 3) | (blockIdx.x >> 5)) & 255u : blockIdx.x;
  unsigned t0;
  asm volatile("v_readfirstlane_b32 %0, %1" : "=s"(t0) : "v"(threadIdx.x));
  const unsigned wid = (t0 >> 6) & 3;
  k.pm = 1 - (t0 >> 16);
  k.wid = wid; k.bid = bid; k.xcd = ((bid & 3) << 1) | ((bid >> 2) & 1); k.j = bid >> 3; k.kvh = k.xcd >> 1; k.hl = k.j & 7; k.rblk = (k.xcd & 1) * 4 + (k.j >> 3);
  k.cs = (k.xcd & 1) << 13; k.nb = 0;
  STAMP_DECL;
  STAMP(0);
#define SPINTO(i) do { } while (0)
  W4C c;
  SPINTO(0);
  {
    H4 g;
    front(k, g, c STAMP_PASS);
    STAMP(8);
    SPINTO(1);
    attention(k, g, c STAMP_PASS);
  }
  SPINTO(2);
  oproj(k, c STAMP_PASS);
  SPINTO(3);
  STAMP_FLUSH();
  asm volatile(HOP_DEFS);
}

extern "C" void kernel_launch(void* const* d_in, const int* in_sizes, int n_in, void* d_out, int out_size, void* d_ws, size_t ws_size,
                              hipStream_t stream) {
  Args a;
  a.x = (const u16*)d_in[0]; a.kc = (const u16*)d_in[1]; a.vc = (const u16*)d_in[2]; a.wq = (const u16*)d_in[3]; a.wo = (const u16*)d_in[4];
  a.w_r = (const u16*)d_in[6]; a.wg = (const u16*)d_in[7]; a.wu = (const u16*)d_in[8]; a.wd = (const u16*)d_in[9]; a.c = (const u16*)d_in[10];
  a.w_mod = (const u16*)d_in[11]; a.out = (u16*)d_out; a.ws = (char*)d_ws;
  (void)hipFuncSetAttribute((const void*)k_fused, hipFuncAttributeMaxDynamicSharedMemorySize, 163840);
  k_fused<<<256, 256, 163840, stream>>>(a);
}
```

```cpp
#include <hip/hip_runtime.h>
#include <hip/hip_bf16.h>
#include <stdint.h>
#include <utility>

typedef unsigned short u16;
using bf16x8 = __attribute__((ext_vector_type(8))) short;
using s16x4  = __attribute__((ext_vector_type(4))) short;
using f32x16 = __attribute__((ext_vector_type(16))) float;
using u32x16 = __attribute__((ext_vector_type(16))) unsigned;
using f32x4  = __attribute__((ext_vector_type(4))) float;
using f32x2  = __attribute__((ext_vector_type(2))) float;
using u32x4  = __attribute__((ext_vector_type(4))) unsigned;
using u32x2  = __attribute__((ext_vector_type(2))) unsigned;
using u32x8 = __attribute__((ext_vector_type(8))) unsigned;
using f32x8 = __attribute__((ext_vector_type(8))) float;
using f32x32 = __attribute__((ext_vector_type(32))) float;
using u32x32 = __attribute__((ext_vector_type(32))) unsigned;
typedef __bf16 bf16x2_t __attribute__((ext_vector_type(2)));
#define DI __device__ __forceinline__
#define NTMASK 0
#define NT_SEL(bit) ((NTMASK >> (bit)) & 1)
#if NT_SEL(0)
#define NTX " nt"
#else
#define NTX ""
#endif
#if NT_SEL(1)
#define NTM " nt"
#else
#define NTM ""
#endif
#if NT_SEL(2)
#define NTQ " nt"
#else
#define NTQ ""
#endif
#if NT_SEL(3)
#define NTO " nt"
#else
#define NTO ""
#endif
#if NT_SEL(4)
#define NTG " nt"
#else
#define NTG ""
#endif
#if NT_SEL(5)
#define NTD " nt"
#else
#define NTD ""
#endif
#if NT_SEL(6)
#define NTT " nt"
#else
#define NTT ""
#endif
#if NT_SEL(7)
#define NTF " nt"
#else
#define NTF ""
#endif

constexpr int S = 2048, DM = 1024, HQ = 32, HKV = 4, DH = 128, SKV = 2048, WIN = 512, FF = 3584;
constexpr int QD = HQ * DH;
constexpr float QSCALE = 0.12751743082f;

DI float bf2f(u16 v) { return __uint_as_float(((unsigned)v) << 16); }
DI u16 f2bf(float x) { unsigned u = __float_as_uint(x); u += 0x7fffu + ((u >> 16) & 1u); return (u16)(u >> 16); }
DI unsigned cvtpk(float lo, float hi) { f32x2 v = {lo, hi}; bf16x2_t b = __builtin_convertvector(v, bf16x2_t); return __builtin_bit_cast(unsigned, b); }
DI float lo16f(unsigned w) { return __uint_as_float(w << 16); }
DI float hi16f(unsigned w) { return __uint_as_float(w & 0xffff0000u); }
DI float rbf(float x) { return lo16f(cvtpk(x, x)); }
DI f32x2 up2(unsigned w) { return f32x2{lo16f(w), hi16f(w)}; }
DI float dot2(unsigned a, unsigned b, float c) { return __builtin_amdgcn_fdot2_f32_bf16(__builtin_bit_cast(bf16x2_t, a), __builtin_bit_cast(bf16x2_t, b), c, false); }
DI float dot2z(unsigned a, unsigned b) { return __builtin_amdgcn_fdot2_f32_bf16(__builtin_bit_cast(bf16x2_t, a), __builtin_bit_cast(bf16x2_t, b), 0.f, false); }
DI unsigned plo(unsigned a, unsigned b) { return __builtin_amdgcn_perm(b, a, 0x05040100u); }
DI unsigned phi(unsigned a, unsigned b) { return __builtin_amdgcn_perm(b, a, 0x07060302u); }
DI unsigned rfl(unsigned x) { return __builtin_amdgcn_readfirstlane(x); }
template <int CTRL> DI float dpp_f(float v) { return __int_as_float(__builtin_amdgcn_update_dpp(0, __float_as_int(v), CTRL, 0xf, 0xf, true)); }
template <int CTRL> DI unsigned dpp_u(unsigned v) { return (unsigned)__builtin_amdgcn_update_dpp(0, (int)v, CTRL, 0xf, 0xf, true); }
DI float fold16(float a, float b) { auto r = __builtin_amdgcn_permlane16_swap(__float_as_uint(a), __float_as_uint(b), false, false); return __uint_as_float(r[0]) + __uint_as_float(r[1]); }
DI float fold32(float a, float b) { auto r = __builtin_amdgcn_permlane32_swap(__float_as_uint(a), __float_as_uint(b), false, false); return __uint_as_float(r[0]) + __uint_as_float(r[1]); }
DI float sum16(float v) { return fold16(v, v); }
DI float sum32(float v) { return fold32(v, v); }
DI float sum_row16(float v) { v += dpp_f<0xB1>(v); v += dpp_f<0x4E>(v); v += dpp_f<0x141>(v); v += dpp_f<0x140>(v); return v; }
DI float wave_sum(float v) { return sum32(sum16(sum_row16(v))); }
DI unsigned wave_maxu(unsigned v) {
  v = max(v, dpp_u<0xB1>(v)); v = max(v, dpp_u<0x4E>(v)); v = max(v, dpp_u<0x141>(v)); v = max(v, dpp_u<0x140>(v));
  { auto r = __builtin_amdgcn_permlane16_swap(v, v, false, false); v = max(r[0], r[1]); }
  { auto r = __builtin_amdgcn_permlane32_swap(v, v, false, false); v = max(r[0], r[1]); }
  return v;
}
#define GAP() __builtin_amdgcn_sched_barrier(0)
template <int N> using iseq = std::make_integer_sequence<int, N>;
template <int... Is> using ilist = std::integer_sequence<int, Is...>;
typedef __amdgpu_buffer_rsrc_t srd_t;
#define SRD3 0x00020000
DI srd_t mk_srd(const void* p, unsigned bytes) { return __builtin_amdgcn_make_buffer_rsrc((void*)p, (short)0, (int)bytes, SRD3); }
DI unsigned lane_id() { unsigned l; asm volatile("v_mbcnt_lo_u32_b32 %0, -1, 0\n\tv_mbcnt_hi_u32_b32 %0, -1, %0" : "=v"(l)); return l; }
struct Args { const u16 *x, *kc, *vc, *wq, *wo, *w_r, *wg, *wu, *wd, *c, *w_mod; u16* out; char* ws; };
typedef const __attribute__((address_space(4))) Args* argp_t;
DI argp_t args() { argp_t p = (argp_t)__builtin_amdgcn_kernarg_segment_ptr(); asm volatile("" : "+s"(p)); return p; }
#define BARRIER_LGKM() do { asm volatile("s_waitcnt lgkmcnt(0)\n\ts_barrier" ::: "memory"); } while (0)

constexpr unsigned W_EPOCH = 0x260000, W_TMO = 256;
constexpr unsigned W_FLAGH = 0x261000;
constexpr unsigned W_VOTE = 0x2000, W_PROBE = 0x4000;
constexpr unsigned W_GKEY = 0x10000;
constexpr unsigned W_FLAGR = 0x8000;
constexpr unsigned W_FLAGO = 0x20000;
constexpr unsigned W_CANDH = 0x40000;
constexpr unsigned W_STAT = 0x80000;
constexpr unsigned W_YG = 0x100000;
constexpr unsigned W_CANDX = 0x1E0000;
constexpr unsigned W_ST1 = 0x200000;
constexpr unsigned W_MODA = 0x210000;
constexpr unsigned W_MODC = 0x270000;
constexpr unsigned W_MODB = 0x220000;
constexpr unsigned W_SIL = 0x240000;
constexpr unsigned W_HT = 4u << 20;
constexpr unsigned W_OT = 8u << 20;
constexpr unsigned W_XCH = 24u << 20;
constexpr unsigned W_END = 40u << 20;
constexpr unsigned TAGM = 0x40004000u, TAGC = 0x9E3739B9u;
#define SPINCAP 262144
constexpr unsigned kSpinCap = SPINCAP;
#define SL0 1
#define SL1 1
#define SL2 1
#define SL3 1
template <int SL = 1> DI bool spin_fail(unsigned& spins, srd_t ws, unsigned tag) {
  __builtin_amdgcn_s_sleep(SL);
  if (++spins > kSpinCap) { __builtin_amdgcn_raw_buffer_store_b32(tag, ws, W_TMO, 0, 16); return true; }
  return (spins & 255u) == 0u && rfl(__builtin_amdgcn_raw_buffer_load_b32(ws, W_TMO, 0, 16)) == tag;
}
DI u32x4 ld16(srd_t ws, unsigned off) { return __builtin_amdgcn_raw_buffer_load_b128(ws, off, 0, 16); }
DI u32x2 ld8(srd_t ws, unsigned off) { return __builtin_amdgcn_raw_buffer_load_b64(ws, off, 0, 16); }
DI void st16(srd_t ws, unsigned off, u32x4 v) { __builtin_amdgcn_raw_buffer_store_b128(v, ws, off, 0, 16); }
DI void st8(srd_t ws, unsigned off, u32x2 v) { __builtin_amdgcn_raw_buffer_store_b64(v, ws, off, 0, 16); }
DI void st4(srd_t ws, unsigned off, unsigned v) { __builtin_amdgcn_raw_buffer_store_b32(v, ws, off, 0, 16); }
DI void wait_flags(srd_t ws, unsigned off, unsigned tag) {
  const unsigned o = off + (lane_id() & 31u) * 4u;
  for (unsigned spins = 0;;) {
    const unsigned f = __builtin_amdgcn_raw_buffer_load_b32(ws, o, 0, 16);
    if (__all(f == tag)) break;
    if (spin_fail(spins, ws, tag)) break;
  }
  asm volatile("" ::: "memory");
}
template <int OFF, bool DEV = false> DI u32x4 ald16(srd_t s, unsigned vo, unsigned so) {
  u32x4 r;
  if constexpr (DEV) asm volatile("buffer_load_dwordx4 %0, %1, %2, %3 offen offset:%4 sc1" : "=v"(r) : "v"(vo), "s"(s), "s"(so), "i"(OFF) : "memory");
  else               asm volatile("buffer_load_dwordx4 %0, %1, %2, %3 offen offset:%4" NTF : "=v"(r) : "v"(vo), "s"(s), "s"(so), "i"(OFF) : "memory");
  return r;
}
template <int OFF> DI u32x2 ald8(srd_t s, unsigned vo, unsigned so) {
  u32x2 r; asm volatile("buffer_load_dwordx2 %0, %1, %2, %3 offen offset:%4" : "=v"(r) : "v"(vo), "s"(s), "s"(so), "i"(OFF) : "memory"); return r;
}
DI u32x2 ald8d(srd_t s, unsigned vo) { u32x2 r; asm volatile("buffer_load_dwordx2 %0, %1, %2, 0 offen sc1" : "=v"(r) : "v"(vo), "s"(s) : "memory"); return r; }
template <int OFF, bool DEV = false> DI unsigned ald4(srd_t s, unsigned vo, unsigned so) {
  unsigned r;
  if constexpr (DEV) asm volatile("buffer_load_dword %0, %1, %2, %3 offen offset:%4 sc1" : "=v"(r) : "v"(vo), "s"(s), "s"(so), "i"(OFF) : "memory");
  else               asm volatile("buffer_load_dword %0, %1, %2, %3 offen offset:%4" : "=v"(r) : "v"(vo), "s"(s), "s"(so), "i"(OFF) : "memory");
  return r;
}
DI void touch(unsigned& d, const void* base, unsigned vo) { asm volatile("global_load_dword %0, %1, %2" : "+v"(d) : "v"(vo), "s"(base) : "memory"); }
DI void touchq(unsigned& d, const void* base, unsigned vo) { asm volatile("global_load_dword %0, %1, %2" NTQ : "+v"(d) : "v"(vo), "s"(base) : "memory"); }
template <class T> DI T lds_ld(unsigned a) { return *reinterpret_cast<const __attribute__((address_space(3))) T*>(a); }
template <class T> DI void lds_st(unsigned a, T v) { *reinterpret_cast<__attribute__((address_space(3))) T*>(a) = v; }
DI float silu_bf(float v) { return rbf(v * __builtin_amdgcn_rcpf(1.f + __expf(-v))); }
struct Ctx { static constexpr unsigned ldsb = 0; unsigned pm, bid, wid, xcd, j, kvh, hl, rblk, tag, cs, nb; srd_t ws; };
#define TAIL2 1
DI unsigned ffn_j(const Ctx& k) { return TAIL2 ? (k.j < 24 ? k.j : 24 + (k.j & 3)) : min(k.j, 27u); }
DI bool ffn_idle(const Ctx& k) { return TAIL2 ? (k.j >> 2) == 6 + (k.xcd & 1) : k.j >= 28; }
constexpr unsigned UNIT = 16384;
#define STAMPA(I) do { } while (0)
#define STAMPO(I) do { } while (0)
#define STAMP_DECL
#define STAMP(I) do { } while (0)
#define STAMP_FLUSH() do { } while (0)
#define STAMP_ARG
#define STAMP_PASS
#define ACLOB_LO "a0","a1","a2","a3","a4","a5","a6","a7","a8","a9","a10","a11","a12","a13","a14","a15","a16","a17","a18","a19","a20","a21","a22","a23","a24","a25","a26","a27","a28","a29","a30","a31","a32","a33","a34","a35","a36","a37","a38","a39","a40","a41","a42","a43","a44","a45","a46","a47","a48","a49","a50","a51","a52","a53","a54","a55","a56","a57","a58","a59","a60","a61","a62","a63","a64","a65","a66","a67","a68","a69","a70","a71","a72","a73","a74","a75","a76","a77","a78","a79","a80","a81","a82","a83","a84","a85","a86","a87","a88","a89","a90","a91","a92","a93","a94","a95","a96","a97","a98","a99","a100","a101","a102","a103","a104","a105","a106","a107","a108","a109","a110","a111","a112","a113","a114","a115","a116","a117","a118","a119","a120","a121","a122","a123","a124","a125","a126","a127"
#define ACLOB_HI "a128","a129","a130","a131","a132","a133","a134","a135","a136","a137","a138","a139","a140","a141","a142","a143","a144","a145","a146","a147","a148","a149","a150","a151","a152","a153","a154","a155","a156","a157","a158","a159","a160","a161","a162","a163","a164","a165","a166","a167","a168","a169","a170","a171","a172","a173","a174","a175","a176","a177","a178","a179","a180","a181","a182","a183","a184","a185","a186","a187","a188","a189","a190","a191","a192","a193","a194","a195","a196","a197","a198","a199","a200","a201","a202","a203","a204","a205","a206","a207","a208","a209","a210","a211","a212","a213","a214","a215","a216","a217","a218","a219","a220","a221","a222","a223","a224","a225","a226","a227","a228","a229","a230","a231","a232","a233","a234","a235","a236","a237","a238","a239","a240","a241","a242","a243","a244","a245","a246","a247","a248","a249","a250","a251","a252","a253","a254","a255"
#define HOP_DEFS ".set .Lhc00, .Lhs0\n\t.set .Lhs0n, .Lhs4\n\t.set .Lhs4n, .Lhs8\n\t.set .Lhs8n, .Lhr0\n\t.set .Lhc01, .Lhs1\n\t.set .Lhs1n, .Lhs5\n\t.set .Lhs5n, .Lhs9\n\t.set .Lhs9n, .Lhr0\n\t.set .Lhc02, .Lhs2\n\t.set .Lhs2n, .Lhs6\n\t.set .Lhs6n, .Lhs10\n\t.set .Lhs10n, .Lhr0\n\t.set .Lhc03, .Lhs3\n\t.set .Lhs3n, .Lhs7\n\t.set .Lhs7n, .Lhr0\n\t.set .Lhc10, .Lhs11\n\t.set .Lhs11n, .Lhs15\n\t.set .Lhs15n, .Lhr1\n\t.set .Lhc11, .Lhs12\n\t.set .Lhs12n, .Lht0\n\t.set .Lht0n, .Lhr1\n\t.set .Lhc12, .Lhs13\n\t.set .Lhs13n, .Lht1\n\t.set .Lht1n, .Lhr1\n\t.set .Lhc13, .Lhs14\n\t.set .Lhs14n, .Lhr1\n\t.set .Lhc20, .Lhf\n\t.set .Lhfn, .Lhr2\n\t.set .Lhc21, .Lhr2\n\t.set .Lhc22, .Lhr2\n\t.set .Lhc23, .Lhr2"
#define HOP_PAGE(n) asm volatile("s_branch .Lht" #n "\n\t.p2align 12\n.Lht" #n ":\n\ts_cbranch_execz .Lht" #n "n")
#define PAGE_ALIGN() asm volatile("s_branch .Lpa%=\n\t.p2align 12\n.Lpa%=:" ::: "memory")
#define HOP_FRONT() asm volatile("s_branch .Lhf\n\t.p2align 12\n.Lhf:\n\ts_cbranch_execz .Lhfn")
#define HOP_GO(g, w) asm volatile("s_mov_b64 exec, 0\n\ts_cmp_eq_u32 %0, 0\n\ts_cbranch_scc1 .Lhc" #g "0\n\ts_cmp_eq_u32 %0, 1\n\ts_cbranch_scc1 .Lhc" #g "1\n\ts_cmp_eq_u32 %0, 2\n\ts_cbranch_scc1 .Lhc" #g "2\n\ts_branch .Lhc" #g "3\n.Lhr" #g ":\n\ts_mov_b64 exec, -1" :: "s"(w) : "scc", "memory")
#define STP(x)
#define OP_ASM \
  "s_waitcnt lgkmcnt(0)\n\t" \
  "s_barrier\n\t" \
  STP("s_memrealtime s[94:95]\n\t") \
  STP("s_memtime s[92:93]\n\t") \
  "ds_read_b32 v228, v213 offset:49152\n\t" \
  "v_mfma_f32_16x16x32_bf16 a[8:11], a[140:143], v[76:79], 0\n\t" \
  "v_add_u32 v238, 14336, v240\n\t" \
  "v_add_u32 v239, 21504, v241\n\t" \
  "v_mfma_f32_16x16x32_bf16 a[8:11], a[136:139], v[72:75], a[8:11]\n\t" \
  "ds_read_b128 a[12:15], v242 offset:32768\n\t" \
  "v_add_u32 v241, 7168, v241\n\t" \
  "v_mfma_f32_16x16x32_bf16 a[8:11], a[132:135], v[68:71], a[8:11]\n\t" \
  "s_mov_b32 s79, 0x05040100\n\t" \
  "ds_read_b128 a[28:31], v242 offset:33792\n\t" \
  "s_mov_b32 s80, 0x07060302\n\t" \
  "v_mfma_f32_16x16x32_bf16 a[8:11], a[128:131], v[64:67], a[8:11]\n\t" \
  "ds_read_b128 a[44:47], v242 offset:34816\n\t" \
  "v_mfma_f32_16x16x32_bf16 a[24:27], a[144:147], v[64:67], 0\n\t" \
  "ds_read_b128 a[60:63], v242 offset:35840\n\t" \
  "v_mfma_f32_16x16x32_bf16 a[24:27], a[148:151], v[68:71], a[24:27]\n\t" \
  "v_mfma_f32_16x16x32_bf16 a[24:27], a[152:155], v[72:75], a[24:27]\n\t" \
  "s_waitcnt vmcnt(14)\n\t" \
  "buffer_store_dword v243, v254, %[ws], %[fw2] offen offset:32 sc1\n\t" \
  "v_mfma_f32_16x16x32_bf16 a[24:27], a[156:159], v[76:79], a[24:27]\n\t" \
  "ds_read_b128 a[76:79], v242 offset:36864\n\t" \
  "v_mfma_f32_16x16x32_bf16 a[104:107], a[172:175], v[76:79], 0\n\t" \
  "ds_read_b128 a[92:95], v242 offset:37888\n\t" \
  "v_mfma_f32_16x16x32_bf16 a[104:107], a[168:171], v[72:75], a[104:107]\n\t" \
  "ds_read_b128 a[108:111], v242 offset:38912\n\t" \
  "s_waitcnt lgkmcnt(7)\n\t" \
  "v_cmp_eq_u32 vcc, %[tag], v228\n\t" \
  "s_and_b32 vcc_lo, vcc_lo, %[pm]\n\t" \
  "s_cbranch_scc1 .Lop_rd\n\t" \
  "v_mfma_f32_16x16x32_bf16 a[104:107], a[164:167], v[68:71], a[104:107]\n\t" \
  "ds_read_b128 a[124:127], v242 offset:39936\n\t" \
  "v_mfma_f32_16x16x32_bf16 a[104:107], a[160:163], v[64:67], a[104:107]\n\t" \
  "ds_read_b128 a[208:211], v242 offset:40960\n\t" \
  "v_mfma_f32_16x16x32_bf16 a[120:123], a[176:179], v[64:67], 0\n\t" \
  "buffer_load_dword v246, v245, %[ws], %[fw2] offen sc1\n\t" \
  "v_mfma_f32_16x16x32_bf16 a[120:123], a[180:183], v[68:71], a[120:123]\n\t" \
  "ds_read_b128 a[212:215], v242 offset:41984\n\t" \
  "v_mfma_f32_16x16x32_bf16 a[120:123], a[184:187], v[72:75], a[120:123]\n\t" \
  "ds_read_b128 a[216:219], v242 offset:43008\n\t" \
  "v_mfma_f32_16x16x32_bf16 a[120:123], a[188:191], v[76:79], a[120:123]\n\t" \
  "s_xor_b32 s73, %[eso], 131072\n\t" \
  "ds_read_b128 a[220:223], v242 offset:44032\n\t" \
  "v_mfma_f32_16x16x32_bf16 a[88:91], v[108:111], v[76:79], 0\n\t" \
  "s_add_u32 m0, %[m], 0\n\t" \
  "ds_read_b128 a[192:195], v242 offset:45056\n\t" \
  "v_mfma_f32_16x16x32_bf16 a[88:91], v[104:107], v[72:75], a[88:91]\n\t" \
  "ds_read_b128 a[196:199], v242 offset:46080\n\t" \
  "v_mfma_f32_16x16x32_bf16 a[88:91], v[100:103], v[68:71], a[88:91]\n\t" \
  "ds_read_b128 a[200:203], v242 offset:47104\n\t" \
  "ds_read_b128 a[204:207], v242 offset:48128\n\t" \
  "v_mfma_f32_16x16x32_bf16 a[88:91], v[96:99], v[64:67], a[88:91]\n\t" \
  "s_waitcnt lgkmcnt(12)\n\t" \
  "s_mov_b64 exec, 0\n\t" \
  "s_cmp_eq_u32 %[wid], 0\n\t" \
  "s_cbranch_scc1 .Lhc10\n\t" \
  "s_cmp_eq_u32 %[wid], 1\n\t" \
  "s_cbranch_scc1 .Lhc11\n\t" \
  "s_cmp_eq_u32 %[wid], 2\n\t" \
  "s_cbranch_scc1 .Lhc12\n\t" \
  "s_branch .Lhc13\n\t" \
  ".Lhr1:\n\t" \
  "s_mov_b64 exec, -1\n\t" \
  "s_waitcnt vmcnt(6)\n\t" \
  "s_waitcnt vmcnt(0)\n\t" \
  "v_cmp_ne_u32 vcc, %[tag], v246\n\t" \
  "s_and_b32 vcc_lo, vcc_lo, 0x6\n\t" \
  "s_cbranch_scc1 .Lop_s0\n\t" \
  ".Lop_k0:\n\t" \
  "buffer_load_dwordx4 v[16:19], v242, %[ws], s73 offen offset:0 sc1\n\t" \
  "v_perm_b32 v248, v132, v128, s79\n\t" \
  "v_perm_b32 v128, v132, v128, s80\n\t" \
  "v_dot2_f32_bf16 v132, v248, v224, 0\n\t" \
  "v_dot2_f32_bf16 v128, v128, v224, 0\n\t" \
  "v_perm_b32 v249, v133, v129, s79\n\t" \
  "v_perm_b32 v129, v133, v129, s80\n\t" \
  "v_dot2_f32_bf16 v133, v249, v224, 0\n\t" \
  "v_dot2_f32_bf16 v129, v129, v224, 0\n\t" \
  "buffer_load_dwordx4 v[20:23], v242, %[ws], s73 offen offset:1024 sc1\n\t" \
  "v_perm_b32 v248, v134, v130, s79\n\t" \
  "v_perm_b32 v130, v134, v130, s80\n\t" \
  "v_dot2_f32_bf16 v134, v248, v224, 0\n\t" \
  "v_dot2_f32_bf16 v130, v130, v224, 0\n\t" \
  "v_perm_b32 v249, v135, v131, s79\n\t" \
  "v_perm_b32 v131, v135, v131, s80\n\t" \
  "v_dot2_f32_bf16 v135, v249, v224, 0\n\t" \
  "v_dot2_f32_bf16 v131, v131, v224, 0\n\t" \
  "buffer_load_dwordx4 v[24:27], v242, %[ws], s73 offen offset:2048 sc1\n\t" \
  "v_perm_b32 v248, v140, v136, s79\n\t" \
  "v_perm_b32 v136, v140, v136, s80\n\t" \
  "v_dot2_f32_bf16 v132, v248, v225, v132\n\t" \
  "v_dot2_f32_bf16 v128, v136, v225, v128\n\t" \
  "v_perm_b32 v249, v141, v137, s79\n\t" \
  "v_perm_b32 v137, v141, v137, s80\n\t" \
  "v_dot2_f32_bf16 v133, v249, v225, v133\n\t" \
  "v_dot2_f32_bf16 v129, v137, v225, v129\n\t" \
  "buffer_load_dwordx4 v[28:31], v242, %[ws], s73 offen offset:3072 sc1\n\t" \
  "v_perm_b32 v248, v142, v138, s79\n\t" \
  "v_perm_b32 v138, v142, v138, s80\n\t" \
  "v_dot2_f32_bf16 v134, v248, v225, v134\n\t" \
  "v_dot2_f32_bf16 v130, v138, v225, v130\n\t" \
  "v_perm_b32 v249, v143, v139, s79\n\t" \
  "v_perm_b32 v139, v143, v139, s80\n\t" \
  "v_dot2_f32_bf16 v135, v249, v225, v135\n\t" \
  "v_dot2_f32_bf16 v131, v139, v225, v131\n\t" \
  "s_xor_b32 s73, %[eso], 163840\n\t" \
  "buffer_load_dwordx4 v[0:3], v242, %[ws], s73 offen offset:0 sc1\n\t" \
  "v_perm_b32 v248, v148, v144, s79\n\t" \
  "v_perm_b32 v144, v148, v144, s80\n\t" \
  "v_dot2_f32_bf16 v132, v248, v226, v132\n\t" \
  "v_dot2_f32_bf16 v128, v144, v226, v128\n\t" \
  "v_perm_b32 v249, v149, v145, s79\n\t" \
  "v_perm_b32 v145, v149, v145, s80\n\t" \
  "v_dot2_f32_bf16 v133, v249, v226, v133\n\t" \
  "v_dot2_f32_bf16 v129, v145, v226, v129\n\t" \
  "buffer_load_dwordx4 v[4:7], v242, %[ws], s73 offen offset:1024 sc1\n\t" \
  "v_perm_b32 v248, v150, v146, s79\n\t" \
  "v_perm_b32 v146, v150, v146, s80\n\t" \
  "v_dot2_f32_bf16 v134, v248, v226, v134\n\t" \
  "v_dot2_f32_bf16 v130, v146, v226, v130\n\t" \
  "v_perm_b32 v249, v151, v147, s79\n\t" \
  "v_perm_b32 v147, v151, v147, s80\n\t" \
  "v_dot2_f32_bf16 v135, v249, v226, v135\n\t" \
  "v_dot2_f32_bf16 v131, v147, v226, v131\n\t" \
  "buffer_load_dwordx4 v[8:11], v242, %[ws], s73 offen offset:2048 sc1\n\t" \
  "v_perm_b32 v248, v156, v152, s79\n\t" \
  "v_perm_b32 v152, v156, v152, s80\n\t" \
  "v_dot2_f32_bf16 v132, v248, v227, v132\n\t" \
  "v_dot2_f32_bf16 v128, v152, v227, v128\n\t" \
  "v_perm_b32 v249, v157, v153, s79\n\t" \
  "v_perm_b32 v153, v157, v153, s80\n\t" \
  "v_dot2_f32_bf16 v133, v249, v227, v133\n\t" \
  "v_dot2_f32_bf16 v129, v153, v227, v129\n\t" \
  "buffer_load_dwordx4 v[12:15], v242, %[ws], s73 offen offset:3072 sc1\n\t" \
  "v_perm_b32 v248, v158, v154, s79\n\t" \
  "v_perm_b32 v154, v158, v154, s80\n\t" \
  "v_dot2_f32_bf16 v134, v248, v227, v134\n\t" \
  "v_dot2_f32_bf16 v130, v154, v227, v130\n\t" \
  "v_perm_b32 v249, v159, v155, s79\n\t" \
  "v_perm_b32 v155, v159, v155, s80\n\t" \
  "v_dot2_f32_bf16 v135, v249, v227, v135\n\t" \
  "v_dot2_f32_bf16 v131, v155, v227, v131\n\t" \
  "v_mfma_f32_16x16x32_bf16 v[96:99], a[128:131], a[12:15], 0\n\t" \
  "s_xor_b32 s73, %[eso], 262144\n\t" \
  "s_xor_b32 s71, %[wob], 1048576\n\t" \
  "v_mfma_f32_16x16x32_bf16 v[96:99], a[132:135], a[28:31], v[96:99]\n\t" \
  "v_mfma_f32_16x16x32_bf16 v[96:99], a[136:139], a[44:47], v[96:99]\n\t" \
  "v_mfma_f32_16x16x32_bf16 v[96:99], a[140:143], a[60:63], v[96:99]\n\t" \
  "v_mfma_f32_16x16x32_bf16 v[112:115], a[156:159], a[60:63], 0\n\t" \
  "buffer_load_dwordx4 v[48:51], v242, %[ws], s73 offen offset:0 sc1\n\t" \
  "v_mfma_f32_16x16x32_bf16 v[112:115], a[152:155], a[44:47], v[112:115]\n\t" \
  "v_mfma_f32_16x16x32_bf16 v[112:115], a[148:151], a[28:31], v[112:115]\n\t" \
  "v_mfma_f32_16x16x32_bf16 v[112:115], a[144:147], a[12:15], v[112:115]\n\t" \
  "s_waitcnt lgkmcnt(8)\n\t" \
  "v_mfma_f32_16x16x32_bf16 v[100:103], a[128:131], a[76:79], 0\n\t" \
  "buffer_load_dwordx4 v[52:55], v242, %[ws], s73 offen offset:1024 sc1\n\t" \
  "v_mfma_f32_16x16x32_bf16 v[100:103], a[132:135], a[92:95], v[100:103]\n\t" \
  "v_mfma_f32_16x16x32_bf16 v[100:103], a[136:139], a[108:111], v[100:103]\n\t" \
  "v_mfma_f32_16x16x32_bf16 v[100:103], a[140:143], a[124:127], v[100:103]\n\t" \
  "v_mfma_f32_16x16x32_bf16 v[116:119], a[156:159], a[124:127], 0\n\t" \
  "v_mfma_f32_16x16x32_bf16 v[116:119], a[152:155], a[108:111], v[116:119]\n\t" \
  "buffer_load_dwordx4 v[56:59], v242, %[ws], s73 offen offset:2048 sc1\n\t" \
  "v_mfma_f32_16x16x32_bf16 v[116:119], a[148:151], a[92:95], v[116:119]\n\t" \
  "v_mfma_f32_16x16x32_bf16 v[116:119], a[144:147], a[76:79], v[116:119]\n\t" \
  "s_waitcnt lgkmcnt(4)\n\t" \
  "v_mfma_f32_16x16x32_bf16 v[104:107], a[128:131], a[208:211], 0\n\t" \
  "v_xor_b32 v236, 0xc000, v234\n\t" \
  "v_mfma_f32_16x16x32_bf16 v[104:107], a[132:135], a[212:215], v[104:107]\n\t" \
  "buffer_load_dwordx4 v[60:63], v242, %[ws], s73 offen offset:3072 sc1\n\t" \
  "s_xor_b32 s73, %[eso], 294912\n\t" \
  "v_mfma_f32_16x16x32_bf16 v[104:107], a[136:139], a[216:219], v[104:107]\n\t" \
  "ds_read_b64_tr_b16 a[160:161], v236 offset:0\n\t" \
  "v_mfma_f32_16x16x32_bf16 v[104:107], a[140:143], a[220:223], v[104:107]\n\t" \
  "ds_read_b64_tr_b16 a[162:163], v236 offset:4096\n\t" \
  "v_mfma_f32_16x16x32_bf16 v[120:123], a[156:159], a[220:223], 0\n\t" \
  "ds_read_b64_tr_b16 a[164:165], v236 offset:8192\n\t" \
  "v_mfma_f32_16x16x32_bf16 v[120:123], a[152:155], a[216:219], v[120:123]\n\t" \
  "ds_read_b64_tr_b16 a[166:167], v236 offset:12288\n\t" \
  "v_mfma_f32_16x16x32_bf16 v[120:123], a[148:151], a[212:215], v[120:123]\n\t" \
  "buffer_load_dwordx4 v[80:83], v242, %[ws], s73 offen offset:0 sc1\n\t" \
  "v_mfma_f32_16x16x32_bf16 v[120:123], a[144:147], a[208:211], v[120:123]\n\t" \
  "s_waitcnt lgkmcnt(4)\n\t" \
  "ds_read_b64_tr_b16 a[168:169], v236 offset:16384\n\t" \
  "v_mfma_f32_16x16x32_bf16 v[108:111], a[128:131], a[192:195], 0\n\t" \
  "ds_read_b64_tr_b16 a[170:171], v236 offset:20480\n\t" \
  "v_mfma_f32_16x16x32_bf16 v[108:111], a[132:135], a[196:199], v[108:111]\n\t" \
  "ds_read_b64_tr_b16 a[172:173], v236 offset:24576\n\t" \
  "v_mfma_f32_16x16x32_bf16 v[108:111], a[136:139], a[200:203], v[108:111]\n\t" \
  "buffer_load_dwordx4 v[84:87], v242, %[ws], s73 offen offset:1024 sc1\n\t" \
  "v_mfma_f32_16x16x32_bf16 v[108:111], a[140:143], a[204:207], v[108:111]\n\t" \
  "ds_read_b64_tr_b16 a[174:175], v236 offset:28672\n\t" \
  "v_xor_b32 v237, 0xc020, v234\n\t" \
  "v_mfma_f32_16x16x32_bf16 v[124:127], a[156:159], a[204:207], 0\n\t" \
  "ds_read_b64_tr_b16 a[176:177], v237 offset:0\n\t" \
  "v_mfma_f32_16x16x32_bf16 v[124:127], a[152:155], a[200:203], v[124:127]\n\t" \
  "ds_read_b64_tr_b16 a[178:179], v237 offset:4096\n\t" \
  "v_mfma_f32_16x16x32_bf16 v[124:127], a[148:151], a[196:199], v[124:127]\n\t" \
  "buffer_load_dwordx4 v[88:91], v242, %[ws], s73 offen offset:2048 sc1\n\t" \
  "v_mfma_f32_16x16x32_bf16 v[124:127], a[144:147], a[192:195], v[124:127]\n\t" \
  "s_waitcnt vmcnt(11) lgkmcnt(6)\n\t" \
  "ds_read_b64_tr_b16 a[180:181], v237 offset:8192\n\t" \
  "ds_read_b64_tr_b16 a[182:183], v237 offset:12288\n\t" \
  "buffer_load_dword v246, v245, %[ws], %[fw2] offen sc1\n\t" \
  STP("v_writelane_b32 %[stv], s94, 24\n\t") \
  STP("v_writelane_b32 %[stv], s92, 25\n\t") \
  STP("s_memrealtime s[94:95]\n\t") \
  STP("s_memtime s[92:93]\n\t") \
  "v_mfma_f32_16x16x32_bf16 a[0:3], a[160:163], v[16:19], a[0:3]\n\t" \
  "ds_read_b64_tr_b16 a[184:185], v237 offset:16384\n\t" \
  "v_mfma_f32_16x16x32_bf16 a[0:3], a[164:167], v[20:23], a[0:3]\n\t" \
  "s_waitcnt lgkmcnt(5)\n\t" \
  "ds_read_b64_tr_b16 a[186:187], v237 offset:20480\n\t" \
  "v_mfma_f32_16x16x32_bf16 a[0:3], a[168:171], v[24:27], a[0:3]\n\t" \
  "ds_read_b64_tr_b16 a[188:189], v237 offset:24576\n\t" \
  "v_mfma_f32_16x16x32_bf16 a[0:3], a[172:175], v[28:31], a[0:3]\n\t" \
  "s_waitcnt vmcnt(8)\n\t" \
  "ds_read_b64_tr_b16 a[190:191], v237 offset:28672\n\t" \
  "v_xor_b32 v235, 0xc040, v234\n\t" \
  "v_mfma_f32_16x16x32_bf16 a[4:7], a[172:175], v[12:15], a[4:7]\n\t" \
  "buffer_load_dwordx4 v[92:95], v242, %[ws], s73 offen offset:3072 sc1\n\t" \
  "v_mfma_f32_16x16x32_bf16 a[4:7], a[168:171], v[8:11], a[4:7]\n\t" \
  "ds_read_b64_tr_b16 a[128:129], v235 offset:0\n\t" \
  "ds_read_b64_tr_b16 a[130:131], v235 offset:4096\n\t" \
  "v_mfma_f32_16x16x32_bf16 a[4:7], a[164:167], v[4:7], a[4:7]\n\t" \
  "ds_read_b64_tr_b16 a[132:133], v235 offset:8192\n\t" \
  "v_mfma_f32_16x16x32_bf16 a[4:7], a[160:163], v[0:3], a[4:7]\n\t" \
  "s_waitcnt lgkmcnt(7)\n\t" \
  "ds_read_b64_tr_b16 a[134:135], v235 offset:12288\n\t" \
  "v_mfma_f32_16x16x32_bf16 a[16:19], a[176:179], v[16:19], a[16:19]\n\t" \
  "buffer_load_dwordx4 v240, %[wo], s71 offen offset:0 lds\n\t" \
  "v_mfma_f32_16x16x32_bf16 a[16:19], a[180:183], v[20:23], a[16:19]\n\t" \
  "s_waitcnt lgkmcnt(4)\n\t" \
  "ds_read_b64_tr_b16 a[136:137], v235 offset:16384\n\t" \
  "ds_read_b64_tr_b16 a[138:139], v235 offset:20480\n\t" \
  "v_mfma_f32_16x16x32_bf16 a[16:19], a[184:187], v[24:27], a[16:19]\n\t" \
  "ds_read_b64_tr_b16 a[140:141], v235 offset:24576\n\t" \
  "v_mfma_f32_16x16x32_bf16 a[16:19], a[188:191], v[28:31], a[16:19]\n\t" \
  "ds_read_b64_tr_b16 a[142:143], v235 offset:28672\n\t" \
  "v_xor_b32 v236, 0xc060, v234\n\t" \
  "v_mfma_f32_16x16x32_bf16 a[20:23], a[188:191], v[12:15], a[20:23]\n\t" \
  "ds_read_b64_tr_b16 a[160:161], v236 offset:0\n\t" \
  "v_mfma_f32_16x16x32_bf16 a[20:23], a[184:187], v[8:11], a[20:23]\n\t" \
  "buffer_load_dwordx4 v241, %[wo], s71 offen offset:1024 lds\n\t" \
  "v_mfma_f32_16x16x32_bf16 a[20:23], a[180:183], v[4:7], a[20:23]\n\t" \
  "ds_read_b64_tr_b16 a[162:163], v236 offset:4096\n\t" \
  "ds_read_b64_tr_b16 a[164:165], v236 offset:8192\n\t" \
  "v_mfma_f32_16x16x32_bf16 a[20:23], a[176:179], v[0:3], a[20:23]\n\t" \
  "s_waitcnt lgkmcnt(7)\n\t" \
  "ds_read_b64_tr_b16 a[166:167], v236 offset:12288\n\t" \
  "v_mfma_f32_16x16x32_bf16 a[32:35], a[128:131], v[16:19], a[32:35]\n\t" \
  "s_xor_b32 s73, %[eso], 327680\n\t" \
  "ds_read_b64_tr_b16 a[168:169], v236 offset:16384\n\t" \
  "v_mfma_f32_16x16x32_bf16 a[32:35], a[132:135], v[20:23], a[32:35]\n\t" \
  "s_waitcnt lgkmcnt(5)\n\t" \
  "buffer_load_dwordx4 v238, %[wo], s71 offen offset:2048 lds\n\t" \
  "v_mfma_f32_16x16x32_bf16 a[32:35], a[136:139], v[24:27], a[32:35]\n\t" \
  "ds_read_b64_tr_b16 a[170:171], v236 offset:20480\n\t" \
  "ds_read_b64_tr_b16 a[172:173], v236 offset:24576\n\t" \
  "v_mfma_f32_16x16x32_bf16 a[32:35], a[140:143], v[28:31], a[32:35]\n\t" \
  "ds_read_b64_tr_b16 a[174:175], v236 offset:28672\n\t" \
  "v_xor_b32 v237, 0xc080, v234\n\t" \
  "v_mfma_f32_16x16x32_bf16 a[36:39], a[140:143], v[12:15], a[36:39]\n\t" \
  "ds_read_b64_tr_b16 a[176:177], v237 offset:0\n\t" \
  "v_mfma_f32_16x16x32_bf16 a[36:39], a[136:139], v[8:11], a[36:39]\n\t" \
  "ds_read_b64_tr_b16 a[178:179], v237 offset:4096\n\t" \
  "v_mfma_f32_16x16x32_bf16 a[36:39], a[132:135], v[4:7], a[36:39]\n\t" \
  "buffer_load_dwordx4 v239, %[wo], s71 offen offset:3072 lds\n\t" \
  "s_add_u32 m0, %[m], 16384\n\t" \
  "v_mfma_f32_16x16x32_bf16 a[36:39], a[128:131], v[0:3], a[36:39]\n\t" \
  "s_waitcnt lgkmcnt(6)\n\t" \
  "s_xor_b32 s71, %[wob], 1179648\n\t" \
  "ds_read_b64_tr_b16 a[180:181], v237 offset:8192\n\t" \
  "v_mfma_f32_16x16x32_bf16 a[48:51], a[160:163], v[16:19], a[48:51]\n\t" \
  "ds_read_b64_tr_b16 a[182:183], v237 offset:12288\n\t" \
  "ds_read_b64_tr_b16 a[184:185], v237 offset:16384\n\t" \
  "v_mfma_f32_16x16x32_bf16 a[48:51], a[164:167], v[20:23], a[48:51]\n\t" \
  "s_waitcnt lgkmcnt(5)\n\t" \
  "ds_read_b64_tr_b16 a[186:187], v237 offset:20480\n\t" \
  "s_waitcnt vmcnt(5)\n\t" \
  "v_cmp_ne_u32 vcc, %[tag], v246\n\t" \
  "s_and_b32 vcc_lo, vcc_lo, 0xe00\n\t" \
  "s_cbranch_scc1 .Lop_s1\n\t" \
  ".Lop_k1:\n\t" \
  "v_mfma_f32_16x16x32_bf16 a[48:51], a[168:171], v[24:27], a[48:51]\n\t" \
  "buffer_load_dwordx4 v[64:67], v242, %[ws], s73 offen offset:0 sc1\n\t" \
  "v_mfma_f32_16x16x32_bf16 a[48:51], a[172:175], v[28:31], a[48:51]\n\t" \
  "ds_read_b64_tr_b16 a[188:189], v237 offset:24576\n\t" \
  "ds_read_b64_tr_b16 a[190:191], v237 offset:28672\n\t" \
  "v_mfma_f32_16x16x32_bf16 a[52:55], a[172:175], v[12:15], a[52:55]\n\t" \
  "v_xor_b32 v235, 0xc0a0, v234\n\t" \
  "v_mfma_f32_16x16x32_bf16 a[52:55], a[168:171], v[8:11], a[52:55]\n\t" \
  "ds_read_b64_tr_b16 a[144:145], v235 offset:0\n\t" \
  "ds_read_b64_tr_b16 a[146:147], v235 offset:4096\n\t" \
  "v_mfma_f32_16x16x32_bf16 a[52:55], a[164:167], v[4:7], a[52:55]\n\t" \
  ".Lhs11:\n\ts_cbranch_execz .Lhs11n\n\t" \
  "buffer_load_dwordx4 v[68:71], v242, %[ws], s73 offen offset:1024 sc1\n\t" \
  "v_mfma_f32_16x16x32_bf16 a[52:55], a[160:163], v[0:3], a[52:55]\n\t" \
  "s_waitcnt lgkmcnt(6)\n\t" \
  "ds_read_b64_tr_b16 a[148:149], v235 offset:8192\n\t" \
  "ds_read_b64_tr_b16 a[150:151], v235 offset:12288\n\t" \
  "v_mfma_f32_16x16x32_bf16 a[64:67], a[176:179], v[16:19], a[64:67]\n\t" \
  "ds_read_b64_tr_b16 a[152:153], v235 offset:16384\n\t" \
  "v_mfma_f32_16x16x32_bf16 a[64:67], a[180:183], v[20:23], a[64:67]\n\t" \
  "s_waitcnt lgkmcnt(5)\n\t" \
  "ds_read_b64_tr_b16 a[154:155], v235 offset:20480\n\t" \
  "v_mfma_f32_16x16x32_bf16 a[64:67], a[184:187], v[24:27], a[64:67]\n\t" \
  "ds_read_b64_tr_b16 a[156:157], v235 offset:24576\n\t" \
  "v_mfma_f32_16x16x32_bf16 a[64:67], a[188:191], v[28:31], a[64:67]\n\t" \
  "buffer_load_dwordx4 v[72:75], v242, %[ws], s73 offen offset:2048 sc1\n\t" \
  "v_mfma_f32_16x16x32_bf16 a[68:71], a[188:191], v[12:15], a[68:71]\n\t" \
  "ds_read_b64_tr_b16 a[158:159], v235 offset:28672\n\t" \
  "v_xor_b32 v236, 0xc0c0, v234\n\t" \
  "v_mfma_f32_16x16x32_bf16 a[68:71], a[184:187], v[8:11], a[68:71]\n\t" \
  "ds_read_b64_tr_b16 a[128:129], v236 offset:0\n\t" \
  "ds_read_b64_tr_b16 a[130:131], v236 offset:4096\n\t" \
  "v_mfma_f32_16x16x32_bf16 a[68:71], a[180:183], v[4:7], a[68:71]\n\t" \
  "ds_read_b64_tr_b16 a[132:133], v236 offset:8192\n\t" \
  "v_mfma_f32_16x16x32_bf16 a[68:71], a[176:179], v[0:3], a[68:71]\n\t" \
  "s_waitcnt lgkmcnt(7)\n\t" \
  "s_barrier\n\t" \
  "v_mfma_f32_16x16x32_bf16 a[80:83], a[144:147], v[16:19], a[80:83]\n\t" \
  "buffer_load_dwordx4 v[76:79], v242, %[ws], s73 offen offset:3072 sc1\n\t" \
  "v_mfma_f32_16x16x32_bf16 a[80:83], a[148:151], v[20:23], a[80:83]\n\t" \
  "s_waitcnt lgkmcnt(3)\n\t" \
  "ds_read_b64_tr_b16 a[134:135], v236 offset:12288\n\t" \
  "ds_read_b64_tr_b16 a[136:137], v236 offset:16384\n\t" \
  "v_mfma_f32_16x16x32_bf16 a[80:83], a[152:155], v[24:27], a[80:83]\n\t" \
  "ds_read_b64_tr_b16 a[138:139], v236 offset:20480\n\t" \
  "ds_read_b64_tr_b16 a[140:141], v236 offset:24576\n\t" \
  "v_mfma_f32_16x16x32_bf16 a[80:83], a[156:159], v[28:31], a[80:83]\n\t" \
  "ds_read_b64_tr_b16 a[142:143], v236 offset:28672\n\t" \
  "v_xor_b32 v237, 0xc0e0, v234\n\t" \
  "v_mfma_f32_16x16x32_bf16 a[84:87], a[156:159], v[12:15], a[84:87]\n\t" \
  "buffer_load_dwordx4 v240, %[wo], s71 offen offset:0 lds\n\t" \
  "v_mfma_f32_16x16x32_bf16 a[84:87], a[152:155], v[8:11], a[84:87]\n\t" \
  "ds_read_b64_tr_b16 a[12:13], v237 offset:0\n\t" \
  "ds_read_b64_tr_b16 a[14:15], v237 offset:4096\n\t" \
  "v_mfma_f32_16x16x32_bf16 a[84:87], a[148:151], v[4:7], a[84:87]\n\t" \
  "ds_read_b64_tr_b16 a[28:29], v237 offset:8192\n\t" \
  "v_mfma_f32_16x16x32_bf16 a[84:87], a[144:147], v[0:3], a[84:87]\n\t" \
  "s_waitcnt lgkmcnt(7)\n\t" \
  "ds_read_b64_tr_b16 a[30:31], v237 offset:12288\n\t" \
  "v_mfma_f32_16x16x32_bf16 a[96:99], a[128:131], v[16:19], a[96:99]\n\t" \
  "buffer_load_dwordx4 v241, %[wo], s71 offen offset:1024 lds\n\t" \
  "v_mfma_f32_16x16x32_bf16 a[96:99], a[132:135], v[20:23], a[96:99]\n\t" \
  "s_waitcnt lgkmcnt(4)\n\t" \
  "ds_read_b64_tr_b16 a[44:45], v237 offset:16384\n\t" \
  "ds_read_b64_tr_b16 a[46:47], v237 offset:20480\n\t" \
  "v_mfma_f32_16x16x32_bf16 a[96:99], a[136:139], v[24:27], a[96:99]\n\t" \
  "ds_read_b64_tr_b16 a[60:61], v237 offset:24576\n\t" \
  "v_lshrrev_b32 v244, 4, v242\n\t" \
  "v_mfma_f32_16x16x32_bf16 a[96:99], a[140:143], v[28:31], a[96:99]\n\t" \
  "s_lshl_b32 s77, %[hl], 2\n\t" \
  "ds_read_b64_tr_b16 a[62:63], v237 offset:28672\n\t" \
  "v_xor_b32 v235, 0x14000, v234\n\t" \
  "v_mfma_f32_16x16x32_bf16 a[100:103], a[140:143], v[12:15], a[100:103]\n\t" \
  "buffer_load_dwordx4 v238, %[wo], s71 offen offset:2048 lds\n\t" \
  "v_mfma_f32_16x16x32_bf16 a[100:103], a[136:139], v[8:11], a[100:103]\n\t" \
  "s_add_u32 s77, s77, %[wid]\n\t" \
  "ds_read_b64_tr_b16 a[160:161], v235 offset:0\n\t" \
  "ds_read_b64_tr_b16 a[162:163], v235 offset:4096\n\t" \
  "v_mfma_f32_16x16x32_bf16 a[100:103], a[132:135], v[4:7], a[100:103]\n\t" \
  "s_lshl_b32 s77, s77, 6\n\t" \
  "ds_read_b64_tr_b16 a[164:165], v235 offset:8192\n\t" \
  "v_add_u32 v230, 0x10000, v242\n\t" \
  "v_mfma_f32_16x16x32_bf16 a[100:103], a[128:131], v[0:3], a[100:103]\n\t" \
  "s_waitcnt lgkmcnt(7)\n\t" \
  "ds_read_b64_tr_b16 a[166:167], v235 offset:12288\n\t" \
  "v_xor_b32 v236, 0x24000, v234\n\t" \
  "v_mfma_f32_16x16x32_bf16 a[112:115], a[12:15], v[16:19], a[112:115]\n\t" \
  "ds_read_b64_tr_b16 a[168:169], v236 offset:0\n\t" \
  "v_add_u32 v232, 0x20000, v242\n\t" \
  "v_mfma_f32_16x16x32_bf16 a[112:115], a[28:31], v[20:23], a[112:115]\n\t" \
  "s_waitcnt lgkmcnt(5)\n\t" \
  "buffer_load_dwordx4 v239, %[wo], s71 offen offset:3072 lds\n\t" \
  "s_add_u32 m0, %[m], 98304\n\t" \
  "v_mfma_f32_16x16x32_bf16 a[112:115], a[44:47], v[24:27], a[112:115]\n\t" \
  "s_xor_b32 s71, %[wob], 1310720\n\t" \
  "ds_read_b64_tr_b16 a[170:171], v236 offset:4096\n\t" \
  "ds_read_b64_tr_b16 a[172:173], v236 offset:8192\n\t" \
  "v_mfma_f32_16x16x32_bf16 a[112:115], a[60:63], v[28:31], a[112:115]\n\t" \
  "s_xor_b32 s73, %[eso], 393216\n\t" \
  "ds_read_b64_tr_b16 a[174:175], v236 offset:12288\n\t" \
  "s_add_u32 s70, %[m], 147456\n\t" \
  "v_mfma_f32_16x16x32_bf16 a[116:119], a[60:63], v[12:15], a[116:119]\n\t" \
  "s_lshl_b32 s69, s77, 2\n\t" \
  "v_xor_b32 v237, 0x14020, v234\n\t" \
  "s_add_u32 s69, s69, 2228224\n\t" \
  "v_mfma_f32_16x16x32_bf16 a[116:119], a[44:47], v[8:11], a[116:119]\n\t" \
  "buffer_load_dwordx4 v240, %[wo], s71 offen offset:0 lds\n\t" \
  "v_mfma_f32_16x16x32_bf16 a[116:119], a[28:31], v[4:7], a[116:119]\n\t" \
  "s_bfe_u32 s68, %[eso], 0x2000f\n\t" \
  "ds_read_b64_tr_b16 a[176:177], v237 offset:0\n\t" \
  "v_mfma_f32_16x16x32_bf16 a[116:119], a[12:15], v[0:3], a[116:119]\n\t" \
  "s_waitcnt lgkmcnt(5)\n\t" \
  "ds_read_b64_tr_b16 a[178:179], v237 offset:4096\n\t" \
  "s_lshl_b32 s60, s68, 17\n\t" \
  STP("v_writelane_b32 %[stv], s94, 26\n\t") \
  STP("v_writelane_b32 %[stv], s92, 27\n\t") \
  STP("s_memrealtime s[94:95]\n\t") \
  STP("s_memtime s[92:93]\n\t") \
  "v_mfma_f32_16x16x32_bf16 a[0:3], a[160:163], v[48:51], a[0:3]\n\t" \
  "ds_read_b64_tr_b16 a[180:181], v237 offset:8192\n\t" \
  "v_mfma_f32_16x16x32_bf16 a[0:3], a[164:167], v[52:55], a[0:3]\n\t" \
  "s_waitcnt lgkmcnt(3)\n\t" \
  "ds_read_b64_tr_b16 a[182:183], v237 offset:12288\n\t" \
  "v_xor_b32 v235, 0x24020, v234\n\t" \
  "v_mfma_f32_16x16x32_bf16 a[0:3], a[168:171], v[56:59], a[0:3]\n\t" \
  "buffer_load_dwordx4 v[16:19], v242, %[ws], s73 offen offset:0 sc1\n\t" \
  "v_mfma_f32_16x16x32_bf16 a[0:3], a[172:175], v[60:63], a[0:3]\n\t" \
  "s_waitcnt vmcnt(14)\n\t" \
  "ds_read_b64_tr_b16 a[184:185], v235 offset:0\n\t" \
  "s_add_u32 s60, s60, %[xcb]\n\t" \
  "v_mfma_f32_16x16x32_bf16 a[4:7], a[172:175], v[92:95], a[4:7]\n\t" \
  "s_lshl_b32 s63, %[wid], 10\n\t" \
  "ds_read_b64_tr_b16 a[186:187], v235 offset:4096\n\t" \
  "v_and_b32 v250, 7, v244\n\t" \
  "v_mfma_f32_16x16x32_bf16 a[4:7], a[168:171], v[88:91], a[4:7]\n\t" \
  "s_add_u32 s60, s60, s63\n\t" \
  "ds_read_b64_tr_b16 a[188:189], v235 offset:8192\n\t" \
  "v_lshlrev_b32 v250, 5, v250\n\t" \
  "v_mfma_f32_16x16x32_bf16 a[4:7], a[164:167], v[84:87], a[4:7]\n\t" \
  "buffer_load_dwordx4 v[20:23], v242, %[ws], s73 offen offset:1024 sc1\n\t" \
  "v_mfma_f32_16x16x32_bf16 a[4:7], a[160:163], v[80:83], a[4:7]\n\t" \
  "s_waitcnt vmcnt(7)\n\t" \
  "ds_read_b64_tr_b16 a[190:191], v235 offset:12288\n\t" \
  "v_lshrrev_b32 v251, 4, v244\n\t" \
  "v_mfma_f32_16x16x32_bf16 a[8:11], a[160:163], v[64:67], a[8:11]\n\t" \
  "s_xor_b32 s61, s60, 135168\n\t" \
  "v_xor_b32 v236, 0x14040, v234\n\t" \
  "v_lshl_add_u32 v250, v251, 3, v250\n\t" \
  "v_mfma_f32_16x16x32_bf16 a[8:11], a[164:167], v[68:71], a[8:11]\n\t" \
  "ds_read_b64_tr_b16 a[144:145], v236 offset:0\n\t" \
  "v_mfma_f32_16x16x32_bf16 a[8:11], a[168:171], v[72:75], a[8:11]\n\t" \
  "buffer_load_dwordx4 v[24:27], v242, %[ws], s73 offen offset:2048 sc1\n\t" \
  "v_mfma_f32_16x16x32_bf16 a[8:11], a[172:175], v[76:79], a[8:11]\n\t" \
  "s_waitcnt lgkmcnt(5)\n\t" \
  "ds_read_b64_tr_b16 a[146:147], v236 offset:4096\n\t" \
  "s_xor_b32 s62, s60, 278528\n\t" \
  "v_mfma_f32_16x16x32_bf16 a[16:19], a[176:179], v[48:51], a[16:19]\n\t" \
  "s_lshl_b32 s65, s68, 7\n\t" \
  "ds_read_b64_tr_b16 a[148:149], v236 offset:8192\n\t" \
  "v_mfma_f32_16x16x32_bf16 a[16:19], a[180:183], v[52:55], a[16:19]\n\t" \
  "s_waitcnt lgkmcnt(3)\n\t" \
  "ds_read_b64_tr_b16 a[150:151], v236 offset:12288\n\t" \
  "v_xor_b32 v237, 0x24040, v234\n\t" \
  "v_mfma_f32_16x16x32_bf16 a[16:19], a[184:187], v[56:59], a[16:19]\n\t" \
  "s_add_u32 s65, s65, %[frb]\n\t" \
  "ds_read_b64_tr_b16 a[152:153], v237 offset:0\n\t" \
  "v_mfma_f32_16x16x32_bf16 a[16:19], a[188:191], v[60:63], a[16:19]\n\t" \
  "buffer_load_dwordx4 v[28:31], v242, %[ws], s73 offen offset:3072 sc1\n\t" \
  "s_xor_b32 s73, %[eso], 425984\n\t" \
  "v_mfma_f32_16x16x32_bf16 a[20:23], a[188:191], v[92:95], a[20:23]\n\t" \
  "s_xor_b32 s66, s65, 144\n\t" \
  "ds_read_b64_tr_b16 a[154:155], v237 offset:4096\n\t" \
  "v_mfma_f32_16x16x32_bf16 a[20:23], a[184:187], v[88:91], a[20:23]\n\t" \
  "ds_read_b64_tr_b16 a[156:157], v237 offset:8192\n\t" \
  "v_mfma_f32_16x16x32_bf16 a[20:23], a[180:183], v[84:87], a[20:23]\n\t" \
  "ds_read_b64_tr_b16 a[158:159], v237 offset:12288\n\t" \
  "v_mfma_f32_16x16x32_bf16 a[20:23], a[176:179], v[80:83], a[20:23]\n\t" \
  "buffer_load_dwordx4 v[0:3], v242, %[ws], s73 offen offset:0 sc1\n\t" \
  "v_mfma_f32_16x16x32_bf16 a[24:27], a[176:179], v[64:67], a[24:27]\n\t" \
  "s_xor_b32 s67, s65, 288\n\t" \
  "v_xor_b32 v235, 0x14060, v234\n\t" \
  "v_mfma_f32_16x16x32_bf16 a[24:27], a[180:183], v[68:71], a[24:27]\n\t" \
  "s_xor_b32 s68, s65, 384\n\t" \
  "ds_read_b64_tr_b16 a[128:129], v235 offset:0\n\t" \
  "v_mfma_f32_16x16x32_bf16 a[24:27], a[184:187], v[72:75], a[24:27]\n\t" \
  "s_lshr_b32 s75, %[m], 4\n\t" \
  "s_bitset1_b32 s75, 17\n\t" \
  "v_add_u32 v250, s75, v250\n\t" \
  "v_mfma_f32_16x16x32_bf16 a[24:27], a[188:191], v[76:79], a[24:27]\n\t" \
  "s_waitcnt lgkmcnt(5)\n\t" \
  "ds_read_b64_tr_b16 a[130:131], v235 offset:4096\n\t" \
  "s_andn2_b32 s68, s68, 12\n\t" \
  "v_mfma_f32_16x16x32_bf16 a[32:35], a[144:147], v[48:51], a[32:35]\n\t" \
  "buffer_load_dwordx4 v[4:7], v242, %[ws], s73 offen offset:1024 sc1\n\t" \
  "v_mfma_f32_16x16x32_bf16 a[32:35], a[148:151], v[52:55], a[32:35]\n\t" \
  "s_waitcnt lgkmcnt(2)\n\t" \
  "ds_read_b64_tr_b16 a[132:133], v235 offset:8192\n\t" \
  "s_andn2_b32 s64, 0x2000, %[na]\n\t" \
  "v_mfma_f32_16x16x32_bf16 a[32:35], a[152:155], v[56:59], a[32:35]\n\t" \
  "v_permlane32_swap_b32 v132, v134\n\t" \
  "v_mfma_f32_16x16x32_bf16 a[32:35], a[156:159], v[60:63], a[32:35]\n\t" \
  "ds_read_b64_tr_b16 a[134:135], v235 offset:12288\n\t" \
  "v_xor_b32 v236, 0x24060, v234\n\t" \
  "v_mfma_f32_16x16x32_bf16 a[36:39], a[156:159], v[92:95], a[36:39]\n\t" \
  "buffer_load_dwordx4 v[8:11], v242, %[ws], s73 offen offset:2048 sc1\n\t" \
  "v_mfma_f32_16x16x32_bf16 a[36:39], a[152:155], v[88:91], a[36:39]\n\t" \
  "s_xor_b32 s63, s60, 393216\n\t" \
  "ds_read_b64_tr_b16 a[136:137], v236 offset:0\n\t" \
  "ds_read_b64_tr_b16 a[138:139], v236 offset:4096\n\t" \
  "v_mfma_f32_16x16x32_bf16 a[36:39], a[148:151], v[84:87], a[36:39]\n\t" \
  "s_andn2_b32 s63, s63, 98304\n\t" \
  "v_permlane32_swap_b32 v128, v130\n\t" \
  "v_mfma_f32_16x16x32_bf16 a[36:39], a[144:147], v[80:83], a[36:39]\n\t" \
  "ds_read_b64_tr_b16 a[140:141], v236 offset:8192\n\t" \
  "ds_read_b64_tr_b16 a[142:143], v236 offset:12288\n\t" \
  "v_mfma_f32_16x16x32_bf16 a[40:43], a[144:147], v[64:67], a[40:43]\n\t" \
  "buffer_load_dwordx4 v[12:15], v242, %[ws], s73 offen offset:3072 sc1\n\t" \
  "s_xor_b32 s73, %[eso], 458752\n\t" \
  "v_mfma_f32_16x16x32_bf16 a[40:43], a[148:151], v[68:71], a[40:43]\n\t" \
  "v_permlane32_swap_b32 v133, v135\n\t" \
  "v_mfma_f32_16x16x32_bf16 a[40:43], a[152:155], v[72:75], a[40:43]\n\t" \
  "v_permlane32_swap_b32 v129, v131\n\t" \
  "v_mfma_f32_16x16x32_bf16 a[40:43], a[156:159], v[76:79], a[40:43]\n\t" \
  "s_waitcnt lgkmcnt(4)\n\t" \
  "v_xor_b32 v237, 0x14080, v234\n\t" \
  "v_add_f32 v132, v132, v134\n\t" \
  "v_mfma_f32_16x16x32_bf16 a[48:51], a[128:131], v[48:51], a[48:51]\n\t" \
  "ds_read_b64_tr_b16 a[76:77], v237 offset:0\n\t" \
  "v_add_f32 v128, v128, v130\n\t" \
  "v_mfma_f32_16x16x32_bf16 a[48:51], a[132:135], v[52:55], a[48:51]\n\t" \
  "s_waitcnt lgkmcnt(1)\n\t" \
  "buffer_load_dwordx4 v[32:35], v242, %[ws], s73 offen offset:0 sc1\n\t" \
  "v_mfma_f32_16x16x32_bf16 a[48:51], a[136:139], v[56:59], a[48:51]\n\t" \
  "ds_read_b64_tr_b16 a[78:79], v237 offset:4096\n\t" \
  "ds_read_b64_tr_b16 a[92:93], v237 offset:8192\n\t" \
  "v_mfma_f32_16x16x32_bf16 a[48:51], a[140:143], v[60:63], a[48:51]\n\t" \
  "ds_read_b64_tr_b16 a[94:95], v237 offset:12288\n\t" \
  "v_xor_b32 v235, 0x24080, v234\n\t" \
  "v_mfma_f32_16x16x32_bf16 a[52:55], a[140:143], v[92:95], a[52:55]\n\t" \
  "ds_read_b64_tr_b16 a[108:109], v235 offset:0\n\t" \
  "v_add_f32 v133, v133, v135\n\t" \
  "v_mfma_f32_16x16x32_bf16 a[52:55], a[136:139], v[88:91], a[52:55]\n\t" \
  "buffer_load_dwordx4 v[36:39], v242, %[ws], s73 offen offset:1024 sc1\n\t" \
  "buffer_load_dword v246, v245, %[ws], %[fw2] offen sc1\n\t" \
  "v_mfma_f32_16x16x32_bf16 a[52:55], a[132:135], v[84:87], a[52:55]\n\t" \
  "ds_read_b64_tr_b16 a[110:111], v235 offset:4096\n\t" \
  "ds_read_b64_tr_b16 a[124:125], v235 offset:8192\n\t" \
  "v_mfma_f32_16x16x32_bf16 a[52:55], a[128:131], v[80:83], a[52:55]\n\t" \
  "ds_read_b64_tr_b16 a[126:127], v235 offset:12288\n\t" \
  "v_add_f32 v129, v129, v131\n\t" \
  "v_mfma_f32_16x16x32_bf16 a[56:59], a[128:131], v[64:67], a[56:59]\n\t" \
  "s_bitcmp1_b32 %[wid], 0\n\t" \
  "s_cselect_b32 s72, s64, 0\n\t" \
  "s_xor_b32 s63, s63, s72\n\t" \
  "v_mfma_f32_16x16x32_bf16 a[56:59], a[132:135], v[68:71], a[56:59]\n\t" \
  "s_xor_b32 s64, s64, s63\n\t" \
  "v_xor_b32 v236, 0x140a0, v234\n\t" \
  "v_mfma_f32_16x16x32_bf16 a[56:59], a[136:139], v[72:75], a[56:59]\n\t" \
  "ds_read_b64_tr_b16 a[144:145], v236 offset:0\n\t" \
  "v_mfma_f32_16x16x32_bf16 a[56:59], a[140:143], v[76:79], a[56:59]\n\t" \
  "s_waitcnt lgkmcnt(5)\n\t" \
  "buffer_load_dwordx4 v[40:43], v242, %[ws], s73 offen offset:2048 sc1\n\t" \
  "v_mfma_f32_16x16x32_bf16 a[64:67], a[76:79], v[48:51], a[64:67]\n\t" \
  "ds_read_b64_tr_b16 a[146:147], v236 offset:4096\n\t" \
  "v_permlane16_swap_b32 v132, v133\n\t" \
  "v_mfma_f32_16x16x32_bf16 a[64:67], a[92:95], v[52:55], a[64:67]\n\t" \
  "s_waitcnt lgkmcnt(2)\n\t" \
  "ds_read_b64_tr_b16 a[148:149], v236 offset:8192\n\t" \
  "v_mfma_f32_16x16x32_bf16 a[64:67], a[108:111], v[56:59], a[64:67]\n\t" \
  "ds_read_b64_tr_b16 a[150:151], v236 offset:12288\n\t" \
  "v_xor_b32 v237, 0x240a0, v234\n\t" \
  "v_mfma_f32_16x16x32_bf16 a[64:67], a[124:127], v[60:63], a[64:67]\n\t" \
  "buffer_load_dwordx4 v[44:47], v242, %[ws], s73 offen offset:3072 sc1\n\t" \
  "v_mfma_f32_16x16x32_bf16 a[68:71], a[124:127], v[92:95], a[68:71]\n\t" \
  "ds_read_b64_tr_b16 a[152:153], v237 offset:0\n\t" \
  "v_permlane16_swap_b32 v128, v129\n\t" \
  "v_mfma_f32_16x16x32_bf16 a[68:71], a[108:111], v[88:91], a[68:71]\n\t" \
  "ds_read_b64_tr_b16 a[154:155], v237 offset:4096\n\t" \
  "v_add_f32 v132, v132, v133\n\t" \
  "v_mfma_f32_16x16x32_bf16 a[68:71], a[92:95], v[84:87], a[68:71]\n\t" \
  "ds_read_b64_tr_b16 a[156:157], v237 offset:8192\n\t" \
  "v_add_f32 v128, v128, v129\n\t" \
  "v_mfma_f32_16x16x32_bf16 a[68:71], a[76:79], v[80:83], a[68:71]\n\t" \
  "ds_read_b64_tr_b16 a[158:159], v237 offset:12288\n\t" \
  "v_add_f32_dpp v248, v132, v132 row_ror:8 row_mask:0xf bank_mask:0xf\n\t" \
  "v_mfma_f32_16x16x32_bf16 a[72:75], a[76:79], v[64:67], a[72:75]\n\t" \
  "buffer_load_dwordx4 v241, %[wo], s71 offen offset:1024 lds\n\t" \
  "v_mfma_f32_16x16x32_bf16 a[72:75], a[92:95], v[68:71], a[72:75]\n\t" \
  "v_xor_b32 v235, 0x140c0, v234\n\t" \
  "v_add_f32_dpp v249, v128, v128 row_ror:8 row_mask:0xf bank_mask:0xf\n\t" \
  "v_mfma_f32_16x16x32_bf16 a[72:75], a[108:111], v[72:75], a[72:75]\n\t" \
  "ds_read_b64_tr_b16 a[12:13], v235 offset:0\n\t" \
  "v_mfma_f32_16x16x32_bf16 a[72:75], a[124:127], v[76:79], a[72:75]\n\t" \
  "s_waitcnt lgkmcnt(5)\n\t" \
  "s_barrier\n\t" \
  "v_mfma_f32_16x16x32_bf16 a[80:83], a[144:147], v[48:51], a[80:83]\n\t" \
  "buffer_load_dwordx4 v238, %[wo], s71 offen offset:2048 lds\n\t" \
  "v_mfma_f32_16x16x32_bf16 a[80:83], a[148:151], v[52:55], a[80:83]\n\t" \
  "s_waitcnt lgkmcnt(1)\n\t" \
  "ds_read_b64_tr_b16 a[14:15], v235 offset:4096\n\t" \
  "v_mfma_f32_16x16x32_bf16 a[80:83], a[152:155], v[56:59], a[80:83]\n\t" \
  "ds_read_b64_tr_b16 a[28:29], v235 offset:8192\n\t" \
  "v_mfma_f32_16x16x32_bf16 a[80:83], a[156:159], v[60:63], a[80:83]\n\t" \
  "ds_read_b64_tr_b16 a[30:31], v235 offset:12288\n\t" \
  "v_xor_b32 v236, 0x240c0, v234\n\t" \
  "v_mfma_f32_16x16x32_bf16 a[84:87], a[156:159], v[92:95], a[84:87]\n\t" \
  "ds_read_b64_tr_b16 a[44:45], v236 offset:0\n\t" \
  "v_mfma_f32_16x16x32_bf16 a[84:87], a[152:155], v[88:91], a[84:87]\n\t" \
  "buffer_load_dwordx4 v239, %[wo], s71 offen offset:3072 lds\n\t" \
  "s_add_u32 m0, %[m], 32768\n\t" \
  "v_mfma_f32_16x16x32_bf16 a[84:87], a[148:151], v[84:87], a[84:87]\n\t" \
  "s_xor_b32 s71, %[wob], 1441792\n\t" \
  "ds_read_b64_tr_b16 a[46:47], v236 offset:4096\n\t" \
  "ds_read_b64_tr_b16 a[60:61], v236 offset:8192\n\t" \
  "v_mfma_f32_16x16x32_bf16 a[84:87], a[144:147], v[80:83], a[84:87]\n\t" \
  "ds_read_b64_tr_b16 a[62:63], v236 offset:12288\n\t" \
  "v_mfma_f32_16x16x32_bf16 a[88:91], a[144:147], v[64:67], a[88:91]\n\t" \
  "v_xor_b32 v237, 0x140e0, v234\n\t" \
  "v_mfma_f32_16x16x32_bf16 a[88:91], a[148:151], v[68:71], a[88:91]\n\t" \
  "buffer_load_dwordx4 v240, %[wo], s71 offen offset:0 lds\n\t" \
  "v_mfma_f32_16x16x32_bf16 a[88:91], a[152:155], v[72:75], a[88:91]\n\t" \
  "ds_read_b64_tr_b16 a[76:77], v237 offset:0\n\t" \
  "v_mfma_f32_16x16x32_bf16 a[88:91], a[156:159], v[76:79], a[88:91]\n\t" \
  "s_waitcnt lgkmcnt(5)\n\t" \
  "ds_read_b64_tr_b16 a[78:79], v237 offset:4096\n\t" \
  "v_mfma_f32_16x16x32_bf16 a[96:99], a[12:15], v[48:51], a[96:99]\n\t" \
  "ds_read_b64_tr_b16 a[92:93], v237 offset:8192\n\t" \
  "v_mfma_f32_16x16x32_bf16 a[96:99], a[28:31], v[52:55], a[96:99]\n\t" \
  "s_waitcnt lgkmcnt(3)\n\t" \
  "buffer_load_dwordx4 v241, %[wo], s71 offen offset:1024 lds\n\t" \
  "v_mfma_f32_16x16x32_bf16 a[96:99], a[44:47], v[56:59], a[96:99]\n\t" \
  "ds_read_b64_tr_b16 a[94:95], v237 offset:12288\n\t" \
  "v_xor_b32 v235, 0x240e0, v234\n\t" \
  "v_mfma_f32_16x16x32_bf16 a[96:99], a[60:63], v[60:63], a[96:99]\n\t" \
  "ds_read_b64_tr_b16 a[108:109], v235 offset:0\n\t" \
  "v_mfma_f32_16x16x32_bf16 a[100:103], a[60:63], v[92:95], a[100:103]\n\t" \
  "ds_read_b64_tr_b16 a[110:111], v235 offset:4096\n\t" \
  "v_mfma_f32_16x16x32_bf16 a[100:103], a[44:47], v[88:91], a[100:103]\n\t" \
  "ds_read_b64_tr_b16 a[124:125], v235 offset:8192\n\t" \
  "v_mfma_f32_16x16x32_bf16 a[100:103], a[28:31], v[84:87], a[100:103]\n\t" \
  "buffer_load_dwordx4 v238, %[wo], s71 offen offset:2048 lds\n\t" \
  "v_mfma_f32_16x16x32_bf16 a[100:103], a[12:15], v[80:83], a[100:103]\n\t" \
  "ds_read_b64_tr_b16 a[126:127], v235 offset:12288\n\t" \
  "s_waitcnt vmcnt(8)\n\t" \
  "v_cmp_ne_u32 vcc, %[tag], v246\n\t" \
  "s_and_b32 vcc_lo, vcc_lo, 0xfe00\n\t" \
  "s_cbranch_scc1 .Lop_s2\n\t" \
  ".Lop_k2:\n\t" \
  "v_mfma_f32_16x16x32_bf16 a[104:107], a[12:15], v[64:67], a[104:107]\n\t" \
  "v_xor_b32 v236, 0x1c000, v234\n\t" \
  "v_mfma_f32_16x16x32_bf16 a[104:107], a[28:31], v[68:71], a[104:107]\n\t" \
  "ds_read_b64_tr_b16 a[128:129], v236 offset:0\n\t" \
  "v_mfma_f32_16x16x32_bf16 a[104:107], a[44:47], v[72:75], a[104:107]\n\t" \
  "buffer_load_dwordx4 v239, %[wo], s71 offen offset:3072 lds\n\t" \
  "v_mfma_f32_16x16x32_bf16 a[104:107], a[60:63], v[76:79], a[104:107]\n\t" \
  "s_waitcnt lgkmcnt(5)\n\t" \
  "ds_read_b64_tr_b16 a[130:131], v236 offset:4096\n\t" \
  "v_mfma_f32_16x16x32_bf16 a[112:115], a[76:79], v[48:51], a[112:115]\n\t" \
  "ds_read_b64_tr_b16 a[132:133], v236 offset:8192\n\t" \
  "v_mfma_f32_16x16x32_bf16 a[112:115], a[92:95], v[52:55], a[112:115]\n\t" \
  "s_waitcnt lgkmcnt(3)\n\t" \
  "ds_read_b64_tr_b16 a[134:135], v236 offset:12288\n\t" \
  "v_mfma_f32_16x16x32_bf16 a[112:115], a[108:111], v[56:59], a[112:115]\n\t" \
  "ds_read_b64_tr_b16 a[136:137], v236 offset:16384\n\t" \
  "v_mfma_f32_16x16x32_bf16 a[112:115], a[124:127], v[60:63], a[112:115]\n\t" \
  "ds_read_b64_tr_b16 a[138:139], v236 offset:20480\n\t" \
  "v_mfma_f32_16x16x32_bf16 a[116:119], a[124:127], v[92:95], a[116:119]\n\t" \
  "ds_read_b64_tr_b16 a[140:141], v236 offset:24576\n\t" \
  "v_mfma_f32_16x16x32_bf16 a[116:119], a[108:111], v[88:91], a[116:119]\n\t" \
  "ds_read_b64_tr_b16 a[142:143], v236 offset:28672\n\t" \
  "v_mfma_f32_16x16x32_bf16 a[116:119], a[92:95], v[84:87], a[116:119]\n\t" \
  "v_mfma_f32_16x16x32_bf16 a[116:119], a[76:79], v[80:83], a[116:119]\n\t" \
  "s_xor_b32 s73, %[eso], 524288\n\t" \
  "v_mfma_f32_16x16x32_bf16 a[120:123], a[76:79], v[64:67], a[120:123]\n\t" \
  "v_xor_b32 v237, 0x1c020, v234\n\t" \
  "v_mfma_f32_16x16x32_bf16 a[120:123], a[92:95], v[68:71], a[120:123]\n\t" \
  "ds_read_b64_tr_b16 a[144:145], v237 offset:0\n\t" \
  "v_mfma_f32_16x16x32_bf16 a[120:123], a[108:111], v[72:75], a[120:123]\n\t" \
  "ds_read_b64_tr_b16 a[146:147], v237 offset:4096\n\t" \
  "v_mfma_f32_16x16x32_bf16 a[120:123], a[124:127], v[76:79], a[120:123]\n\t" \
  "s_waitcnt lgkmcnt(6)\n\t" \
  "buffer_load_dwordx4 v[48:51], v242, %[ws], s73 offen offset:0 sc1\n\t" \
  STP("v_writelane_b32 %[stv], s94, 28\n\t") \
  STP("v_writelane_b32 %[stv], s92, 29\n\t") \
  STP("s_memrealtime s[94:95]\n\t") \
  STP("s_memtime s[92:93]\n\t") \
  "v_mfma_f32_16x16x32_bf16 a[0:3], a[128:131], v[16:19], a[0:3]\n\t" \
  "ds_read_b64_tr_b16 a[148:149], v237 offset:8192\n\t" \
  "v_mfma_f32_16x16x32_bf16 a[0:3], a[132:135], v[20:23], a[0:3]\n\t" \
  "s_waitcnt lgkmcnt(3)\n\t" \
  "buffer_load_dwordx4 v[52:55], v242, %[ws], s73 offen offset:1024 sc1\n\t" \
  "v_mfma_f32_16x16x32_bf16 a[0:3], a[136:139], v[24:27], a[0:3]\n\t" \
  "ds_read_b64_tr_b16 a[150:151], v237 offset:12288\n\t" \
  "v_mfma_f32_16x16x32_bf16 a[0:3], a[140:143], v[28:31], a[0:3]\n\t" \
  "ds_read_b64_tr_b16 a[152:153], v237 offset:16384\n\t" \
  "v_mfma_f32_16x16x32_bf16 a[4:7], a[140:143], v[12:15], a[4:7]\n\t" \
  "ds_read_b64_tr_b16 a[154:155], v237 offset:20480\n\t" \
  "v_mfma_f32_16x16x32_bf16 a[4:7], a[136:139], v[8:11], a[4:7]\n\t" \
  "buffer_load_dwordx4 v[56:59], v242, %[ws], s73 offen offset:2048 sc1\n\t" \
  "v_mfma_f32_16x16x32_bf16 a[4:7], a[132:135], v[4:7], a[4:7]\n\t" \
  "ds_read_b64_tr_b16 a[156:157], v237 offset:24576\n\t" \
  "v_mfma_f32_16x16x32_bf16 a[4:7], a[128:131], v[0:3], a[4:7]\n\t" \
  "s_waitcnt vmcnt(10)\n\t" \
  "ds_read_b64_tr_b16 a[158:159], v237 offset:28672\n\t" \
  "v_mfma_f32_16x16x32_bf16 a[8:11], a[128:131], v[32:35], a[8:11]\n\t" \
  "v_xor_b32 v235, 0x1c040, v234\n\t" \
  "v_mfma_f32_16x16x32_bf16 a[8:11], a[132:135], v[36:39], a[8:11]\n\t" \
  "buffer_load_dwordx4 v[60:63], v242, %[ws], s73 offen offset:3072 sc1\n\t" \
  "s_xor_b32 s73, %[eso], 557056\n\t" \
  "v_mfma_f32_16x16x32_bf16 a[8:11], a[136:139], v[40:43], a[8:11]\n\t" \
  "ds_read_b64_tr_b16 a[12:13], v235 offset:0\n\t" \
  "v_mfma_f32_16x16x32_bf16 a[8:11], a[140:143], v[44:47], a[8:11]\n\t" \
  "s_waitcnt lgkmcnt(5)\n\t" \
  "ds_read_b64_tr_b16 a[14:15], v235 offset:4096\n\t" \
  "v_mfma_f32_16x16x32_bf16 a[16:19], a[144:147], v[16:19], a[16:19]\n\t" \
  "ds_read_b64_tr_b16 a[28:29], v235 offset:8192\n\t" \
  "v_mfma_f32_16x16x32_bf16 a[16:19], a[148:151], v[20:23], a[16:19]\n\t" \
  "s_waitcnt lgkmcnt(3)\n\t" \
  "ds_read_b64_tr_b16 a[30:31], v235 offset:12288\n\t" \
  "v_mfma_f32_16x16x32_bf16 a[16:19], a[152:155], v[24:27], a[16:19]\n\t" \
  "buffer_load_dwordx4 v[80:83], v242, %[ws], s73 offen offset:0 sc1\n\t" \
  "v_mfma_f32_16x16x32_bf16 a[16:19], a[156:159], v[28:31], a[16:19]\n\t" \
  "ds_read_b64_tr_b16 a[44:45], v235 offset:16384\n\t" \
  "v_mfma_f32_16x16x32_bf16 a[20:23], a[156:159], v[12:15], a[20:23]\n\t" \
  "ds_read_b64_tr_b16 a[46:47], v235 offset:20480\n\t" \
  "v_mfma_f32_16x16x32_bf16 a[20:23], a[152:155], v[8:11], a[20:23]\n\t" \
  "ds_read_b64_tr_b16 a[60:61], v235 offset:24576\n\t" \
  "v_mfma_f32_16x16x32_bf16 a[20:23], a[148:151], v[4:7], a[20:23]\n\t" \
  "buffer_load_dwordx4 v[84:87], v242, %[ws], s73 offen offset:1024 sc1\n\t" \
  "v_mfma_f32_16x16x32_bf16 a[20:23], a[144:147], v[0:3], a[20:23]\n\t" \
  "ds_read_b64_tr_b16 a[62:63], v235 offset:28672\n\t" \
  "v_mfma_f32_16x16x32_bf16 a[24:27], a[144:147], v[32:35], a[24:27]\n\t" \
  "v_xor_b32 v236, 0x1c060, v234\n\t" \
  "v_mfma_f32_16x16x32_bf16 a[24:27], a[148:151], v[36:39], a[24:27]\n\t" \
  "ds_read_b64_tr_b16 a[76:77], v236 offset:0\n\t" \
  "v_mfma_f32_16x16x32_bf16 a[24:27], a[152:155], v[40:43], a[24:27]\n\t" \
  "buffer_load_dwordx4 v[88:91], v242, %[ws], s73 offen offset:2048 sc1\n\t" \
  "v_mfma_f32_16x16x32_bf16 a[24:27], a[156:159], v[44:47], a[24:27]\n\t" \
  "s_waitcnt lgkmcnt(5)\n\t" \
  "ds_read_b64_tr_b16 a[78:79], v236 offset:4096\n\t" \
  "v_mfma_f32_16x16x32_bf16 a[32:35], a[12:15], v[16:19], a[32:35]\n\t" \
  "ds_read_b64_tr_b16 a[92:93], v236 offset:8192\n\t" \
  "v_mfma_f32_16x16x32_bf16 a[32:35], a[28:31], v[20:23], a[32:35]\n\t" \
  "s_waitcnt lgkmcnt(3)\n\t" \
  "ds_read_b64_tr_b16 a[94:95], v236 offset:12288\n\t" \
  "v_mfma_f32_16x16x32_bf16 a[32:35], a[44:47], v[24:27], a[32:35]\n\t" \
  "ds_read_b64_tr_b16 a[108:109], v236 offset:16384\n\t" \
  "v_mfma_f32_16x16x32_bf16 a[32:35], a[60:63], v[28:31], a[32:35]\n\t" \
  "buffer_load_dwordx4 v[92:95], v242, %[ws], s73 offen offset:3072 sc1\n\t" \
  "s_xor_b32 s73, %[eso], 589824\n\t" \
  "v_mfma_f32_16x16x32_bf16 a[36:39], a[60:63], v[12:15], a[36:39]\n\t" \
  "ds_read_b64_tr_b16 a[110:111], v236 offset:20480\n\t" \
  "v_mfma_f32_16x16x32_bf16 a[36:39], a[44:47], v[8:11], a[36:39]\n\t" \
  "ds_read_b64_tr_b16 a[124:125], v236 offset:24576\n\t" \
  "v_mfma_f32_16x16x32_bf16 a[36:39], a[28:31], v[4:7], a[36:39]\n\t" \
  "ds_read_b64_tr_b16 a[126:127], v236 offset:28672\n\t" \
  "v_mfma_f32_16x16x32_bf16 a[36:39], a[12:15], v[0:3], a[36:39]\n\t" \
  "buffer_load_dwordx4 v[64:67], v242, %[ws], s73 offen offset:0 sc1\n\t" \
  "v_mfma_f32_16x16x32_bf16 a[40:43], a[12:15], v[32:35], a[40:43]\n\t" \
  "v_xor_b32 v237, 0x1c080, v234\n\t" \
  "v_mfma_f32_16x16x32_bf16 a[40:43], a[28:31], v[36:39], a[40:43]\n\t" \
  "ds_read_b64_tr_b16 v[128:129], v237 offset:0\n\t" \
  "v_mfma_f32_16x16x32_bf16 a[40:43], a[44:47], v[40:43], a[40:43]\n\t" \
  "ds_read_b64_tr_b16 v[130:131], v237 offset:4096\n\t" \
  "v_mfma_f32_16x16x32_bf16 a[40:43], a[60:63], v[44:47], a[40:43]\n\t" \
  "s_waitcnt lgkmcnt(6)\n\t" \
  "ds_read_b64_tr_b16 v[132:133], v237 offset:8192\n\t" \
  "v_mfma_f32_16x16x32_bf16 a[48:51], a[76:79], v[16:19], a[48:51]\n\t" \
  "buffer_load_dwordx4 v[68:71], v242, %[ws], s73 offen offset:1024 sc1\n\t" \
  "v_mfma_f32_16x16x32_bf16 a[48:51], a[92:95], v[20:23], a[48:51]\n\t" \
  "s_waitcnt lgkmcnt(3)\n\t" \
  "ds_read_b64_tr_b16 v[134:135], v237 offset:12288\n\t" \
  "v_mfma_f32_16x16x32_bf16 a[48:51], a[108:111], v[24:27], a[48:51]\n\t" \
  "ds_read_b64_tr_b16 v[136:137], v237 offset:16384\n\t" \
  "v_mfma_f32_16x16x32_bf16 a[48:51], a[124:127], v[28:31], a[48:51]\n\t" \
  "ds_read_b64_tr_b16 v[138:139], v237 offset:20480\n\t" \
  "v_mfma_f32_16x16x32_bf16 a[52:55], a[124:127], v[12:15], a[52:55]\n\t" \
  "buffer_load_dwordx4 v[72:75], v242, %[ws], s73 offen offset:2048 sc1\n\t" \
  "v_mfma_f32_16x16x32_bf16 a[52:55], a[108:111], v[8:11], a[52:55]\n\t" \
  "ds_read_b64_tr_b16 v[140:141], v237 offset:24576\n\t" \
  "v_mfma_f32_16x16x32_bf16 a[52:55], a[92:95], v[4:7], a[52:55]\n\t" \
  "ds_read_b64_tr_b16 v[142:143], v237 offset:28672\n\t" \
  "v_mfma_f32_16x16x32_bf16 a[52:55], a[76:79], v[0:3], a[52:55]\n\t" \
  "v_mfma_f32_16x16x32_bf16 a[56:59], a[76:79], v[32:35], a[56:59]\n\t" \
  "buffer_load_dwordx4 v[76:79], v242, %[ws], s73 offen offset:3072 sc1\n\t" \
  "v_mfma_f32_16x16x32_bf16 a[56:59], a[92:95], v[36:39], a[56:59]\n\t" \
  "s_xor_b32 s74, %[eso], 98304\n\t" \
  "v_xor_b32 v235, 0x1c0a0, v234\n\t" \
  "v_mfma_f32_16x16x32_bf16 a[56:59], a[108:111], v[40:43], a[56:59]\n\t" \
  "ds_read_b64_tr_b16 a[12:13], v235 offset:0\n\t" \
  "v_mfma_f32_16x16x32_bf16 a[56:59], a[124:127], v[44:47], a[56:59]\n\t" \
  "s_waitcnt lgkmcnt(5)\n\t" \
  "ds_read_b64_tr_b16 a[14:15], v235 offset:4096\n\t" \
  "v_mfma_f32_16x16x32_bf16 a[64:67], v[128:131], v[16:19], a[64:67]\n\t" \
  "buffer_store_dwordx4 v[160:163], v242, %[ws], s74 offen offset:0 sc1\n\t" \
  "v_mfma_f32_16x16x32_bf16 a[64:67], v[132:135], v[20:23], a[64:67]\n\t" \
  "s_waitcnt lgkmcnt(2)\n\t" \
  "ds_read_b64_tr_b16 a[28:29], v235 offset:8192\n\t" \
  "v_mfma_f32_16x16x32_bf16 a[64:67], v[136:139], v[24:27], a[64:67]\n\t" \
  "s_xor_b32 s74, %[eso], 98304\n\t" \
  "ds_read_b64_tr_b16 a[30:31], v235 offset:12288\n\t" \
  "v_mfma_f32_16x16x32_bf16 a[64:67], v[140:143], v[28:31], a[64:67]\n\t" \
  "ds_read_b64_tr_b16 a[44:45], v235 offset:16384\n\t" \
  "v_mfma_f32_16x16x32_bf16 a[68:71], v[140:143], v[12:15], a[68:71]\n\t" \
  "ds_read_b64_tr_b16 a[46:47], v235 offset:20480\n\t" \
  "v_mfma_f32_16x16x32_bf16 a[68:71], v[136:139], v[8:11], a[68:71]\n\t" \
  "ds_read_b64_tr_b16 a[60:61], v235 offset:24576\n\t" \
  "v_mfma_f32_16x16x32_bf16 a[68:71], v[132:135], v[4:7], a[68:71]\n\t" \
  "buffer_store_dwordx4 v[164:167], v242, %[ws], s74 offen offset:1024 sc1\n\t" \
  "v_mfma_f32_16x16x32_bf16 a[68:71], v[128:131], v[0:3], a[68:71]\n\t" \
  "ds_read_b64_tr_b16 a[62:63], v235 offset:28672\n\t" \
  "v_mfma_f32_16x16x32_bf16 a[72:75], v[128:131], v[32:35], a[72:75]\n\t" \
  "s_add_u32 m0, %[m], 81920\n\t" \
  "v_xor_b32 v236, 0x1c0c0, v234\n\t" \
  "s_xor_b32 s74, %[eso], 98304\n\t" \
  "v_mfma_f32_16x16x32_bf16 a[72:75], v[132:135], v[36:39], a[72:75]\n\t" \
  "ds_read_b64_tr_b16 v[144:145], v236 offset:0\n\t" \
  "v_mfma_f32_16x16x32_bf16 a[72:75], v[136:139], v[40:43], a[72:75]\n\t" \
  "ds_read_b64_tr_b16 v[146:147], v236 offset:4096\n\t" \
  "v_mfma_f32_16x16x32_bf16 a[72:75], v[140:143], v[44:47], a[72:75]\n\t" \
  "s_waitcnt lgkmcnt(6)\n\t" \
  "s_barrier\n\t" \
  "v_mfma_f32_16x16x32_bf16 a[80:83], a[12:15], v[16:19], a[80:83]\n\t" \
  "s_xor_b32 s71, %[wob], 1572864\n\t" \
  "ds_write_b64 v250, v[248:249] offset:19456\n\t" \
  "v_mfma_f32_16x16x32_bf16 a[80:83], a[28:31], v[20:23], a[80:83]\n\t" \
  "s_waitcnt lgkmcnt(3)\n\t" \
  "buffer_store_dwordx4 v[168:171], v242, %[ws], s74 offen offset:2048 sc1\n\t" \
  "v_mfma_f32_16x16x32_bf16 a[80:83], a[44:47], v[24:27], a[80:83]\n\t" \
  "s_xor_b32 s74, %[eso], 98304\n\t" \
  "ds_read_b64_tr_b16 v[148:149], v236 offset:8192\n\t" \
  "v_mfma_f32_16x16x32_bf16 a[80:83], a[60:63], v[28:31], a[80:83]\n\t" \
  "buffer_load_dwordx4 v240, %[wo], s71 offen offset:0 lds\n\t" \
  "v_mfma_f32_16x16x32_bf16 a[84:87], a[60:63], v[12:15], a[84:87]\n\t" \
  "ds_read_b64_tr_b16 v[150:151], v236 offset:12288\n\t" \
  "ds_read_b64_tr_b16 v[152:153], v236 offset:16384\n\t" \
  "v_mfma_f32_16x16x32_bf16 a[84:87], a[44:47], v[8:11], a[84:87]\n\t" \
  "buffer_load_dwordx4 v241, %[wo], s71 offen offset:1024 lds\n\t" \
  "v_mfma_f32_16x16x32_bf16 a[84:87], a[28:31], v[4:7], a[84:87]\n\t" \
  "ds_read_b64_tr_b16 v[154:155], v236 offset:20480\n\t" \
  "ds_read_b64_tr_b16 v[156:157], v236 offset:24576\n\t" \
  "v_mfma_f32_16x16x32_bf16 a[84:87], a[12:15], v[0:3], a[84:87]\n\t" \
  "buffer_store_dwordx4 v[172:175], v242, %[ws], s74 offen offset:3072 sc1\n\t" \
  "v_mfma_f32_16x16x32_bf16 a[88:91], a[12:15], v[32:35], a[88:91]\n\t" \
  "ds_read_b64_tr_b16 v[158:159], v236 offset:28672\n\t" \
  "v_xor_b32 v237, 0x1c0e0, v234\n\t" \
  "v_mfma_f32_16x16x32_bf16 a[88:91], a[28:31], v[36:39], a[88:91]\n\t" \
  "ds_read_b64_tr_b16 v[128:129], v237 offset:0\n\t" \
  "v_mfma_f32_16x16x32_bf16 a[88:91], a[44:47], v[40:43], a[88:91]\n\t" \
  "ds_read_b64_tr_b16 v[130:131], v237 offset:4096\n\t" \
  "v_mfma_f32_16x16x32_bf16 a[88:91], a[60:63], v[44:47], a[88:91]\n\t" \
  "s_waitcnt lgkmcnt(6)\n\t" \
  "buffer_load_dwordx4 v238, %[wo], s71 offen offset:2048 lds\n\t" \
  "v_mfma_f32_16x16x32_bf16 a[96:99], v[144:147], v[16:19], a[96:99]\n\t" \
  "ds_read_b64_tr_b16 v[132:133], v237 offset:8192\n\t" \
  "v_mfma_f32_16x16x32_bf16 a[96:99], v[148:151], v[20:23], a[96:99]\n\t" \
  "s_waitcnt lgkmcnt(3)\n\t" \
  "ds_read_b64_tr_b16 v[134:135], v237 offset:12288\n\t" \
  "v_mfma_f32_16x16x32_bf16 a[96:99], v[152:155], v[24:27], a[96:99]\n\t" \
  "ds_read_b64_tr_b16 v[136:137], v237 offset:16384\n\t" \
  "v_mfma_f32_16x16x32_bf16 a[96:99], v[156:159], v[28:31], a[96:99]\n\t" \
  "buffer_load_dwordx4 v239, %[wo], s71 offen offset:3072 lds\n\t" \
  "v_mfma_f32_16x16x32_bf16 a[100:103], v[156:159], v[12:15], a[100:103]\n\t" \
  "ds_read_b64_tr_b16 v[138:139], v237 offset:20480\n\t" \
  "v_mfma_f32_16x16x32_bf16 a[100:103], v[152:155], v[8:11], a[100:103]\n\t" \
  "ds_read_b64_tr_b16 v[140:141], v237 offset:24576\n\t" \
  "v_mfma_f32_16x16x32_bf16 a[100:103], v[148:151], v[4:7], a[100:103]\n\t" \
  "ds_read_b64_tr_b16 v[142:143], v237 offset:28672\n\t" \
  "v_mfma_f32_16x16x32_bf16 a[100:103], v[144:147], v[0:3], a[100:103]\n\t" \
  "v_mfma_f32_16x16x32_bf16 a[104:107], v[144:147], v[32:35], a[104:107]\n\t" \
  "v_xor_b32 v235, 0x0, v234\n\t" \
  "v_mfma_f32_16x16x32_bf16 a[104:107], v[148:151], v[36:39], a[104:107]\n\t" \
  "ds_read_b64_tr_b16 a[12:13], v235 offset:0\n\t" \
  "v_mfma_f32_16x16x32_bf16 a[104:107], v[152:155], v[40:43], a[104:107]\n\t" \
  ".Lhs12:\n\ts_cbranch_execz .Lhs12n\n\t" \
  "ds_read_b64_tr_b16 a[14:15], v235 offset:4096\n\t" \
  "v_mfma_f32_16x16x32_bf16 a[104:107], v[156:159], v[44:47], a[104:107]\n\t" \
  "s_waitcnt lgkmcnt(6)\n\t" \
  "s_barrier\n\t" \
  "v_mfma_f32_16x16x32_bf16 a[112:115], v[128:131], v[16:19], a[112:115]\n\t" \
  "ds_read_b64_tr_b16 a[28:29], v235 offset:8192\n\t" \
  "v_and_b32 v250, 3, v244\n\t" \
  "v_mfma_f32_16x16x32_bf16 a[112:115], v[132:135], v[20:23], a[112:115]\n\t" \
  "s_waitcnt lgkmcnt(3)\n\t" \
  "ds_read_b64_tr_b16 a[30:31], v235 offset:12288\n\t" \
  "v_bfe_u32 v251, v244, 4, 1\n\t" \
  "v_mfma_f32_16x16x32_bf16 a[112:115], v[136:139], v[24:27], a[112:115]\n\t" \
  "ds_read_b64_tr_b16 a[44:45], v235 offset:16384\n\t" \
  "v_lshl_add_u32 v250, v251, 2, v250\n\t" \
  "v_mfma_f32_16x16x32_bf16 a[112:115], v[140:143], v[28:31], a[112:115]\n\t" \
  "ds_read_b64_tr_b16 a[46:47], v235 offset:20480\n\t" \
  "v_bfe_u32 v251, v244, 2, 2\n\t" \
  "v_mfma_f32_16x16x32_bf16 a[116:119], v[140:143], v[12:15], a[116:119]\n\t" \
  "ds_read_b64_tr_b16 a[60:61], v235 offset:24576\n\t" \
  "v_lshlrev_b32 v251, 8, v251\n\t" \
  "v_mfma_f32_16x16x32_bf16 a[116:119], v[136:139], v[8:11], a[116:119]\n\t" \
  "ds_read_b64_tr_b16 a[62:63], v235 offset:28672\n\t" \
  "v_lshl_add_u32 v251, v250, 3, v251\n\t" \
  "v_mfma_f32_16x16x32_bf16 a[116:119], v[132:135], v[4:7], a[116:119]\n\t" \
  "s_lshr_b32 s75, %[m], 6\n\t" \
  "s_bitset1_b32 s75, 17\n\t" \
  "v_add_u32 v251, s75, v251\n\t" \
  "v_mfma_f32_16x16x32_bf16 a[116:119], v[128:131], v[0:3], a[116:119]\n\t" \
  "s_xor_b32 s73, %[eso], 655360\n\t" \
  "ds_read_b64 v[224:225], v251 offset:19456\n\t" \
  "v_lshlrev_b32 v228, 4, v250\n\t" \
  "v_mfma_f32_16x16x32_bf16 a[120:123], v[128:131], v[32:35], a[120:123]\n\t" \
  "v_xor_b32 v236, 0x20, v234\n\t" \
  "v_mov_b32 v249, %[tag]\n\t" \
  "v_mfma_f32_16x16x32_bf16 a[120:123], v[132:135], v[36:39], a[120:123]\n\t" \
  "ds_read_b64_tr_b16 a[76:77], v236 offset:0\n\t" \
  "v_mov_b32 v251, %[tag]\n\t" \
  "v_mfma_f32_16x16x32_bf16 a[120:123], v[136:139], v[40:43], a[120:123]\n\t" \
  "s_mov_b32 s82, 0x000f000f\n\t" \
  "ds_read_b64_tr_b16 a[78:79], v236 offset:4096\n\t" \
  "s_mov_b32 s83, 0\n\t" \
  "v_mfma_f32_16x16x32_bf16 a[120:123], v[140:143], v[44:47], a[120:123]\n\t" \
  "s_waitcnt vmcnt(16) lgkmcnt(7)\n\t" \
  "buffer_load_dwordx4 v[16:19], v242, %[ws], s73 offen offset:0 sc1\n\t" \
  "v_mfma_f32_16x16x32_bf16 a[0:3], a[12:15], v[48:51], a[0:3]\n\t" \
  "ds_read_b64_tr_b16 a[92:93], v236 offset:8192\n\t" \
  "v_mfma_f32_16x16x32_bf16 a[0:3], a[28:31], v[52:55], a[0:3]\n\t" \
  "s_waitcnt lgkmcnt(4)\n\t" \
  "buffer_load_dwordx4 v[20:23], v242, %[ws], s73 offen offset:1024 sc1\n\t" \
  "v_mfma_f32_16x16x32_bf16 a[0:3], a[44:47], v[56:59], a[0:3]\n\t" \
  "ds_read_b64_tr_b16 a[94:95], v236 offset:12288\n\t" \
  "v_mfma_f32_16x16x32_bf16 a[0:3], a[60:63], v[60:63], a[0:3]\n\t" \
  "s_waitcnt vmcnt(14)\n\t" \
  "ds_read_b64_tr_b16 a[108:109], v236 offset:16384\n\t" \
  "v_mfma_f32_16x16x32_bf16 a[4:7], a[60:63], v[92:95], a[4:7]\n\t" \
  "ds_read_b64_tr_b16 a[110:111], v236 offset:20480\n\t" \
  "v_mfma_f32_16x16x32_bf16 a[4:7], a[44:47], v[88:91], a[4:7]\n\t" \
  "buffer_load_dwordx4 v[24:27], v242, %[ws], s73 offen offset:2048 sc1\n\t" \
  "v_mfma_f32_16x16x32_bf16 a[4:7], a[28:31], v[84:87], a[4:7]\n\t" \
  "ds_read_b64_tr_b16 a[124:125], v236 offset:24576\n\t" \
  "v_mfma_f32_16x16x32_bf16 a[4:7], a[12:15], v[80:83], a[4:7]\n\t" \
  "s_waitcnt vmcnt(11)\n\t" \
  "ds_read_b64_tr_b16 a[126:127], v236 offset:28672\n\t" \
  "v_mfma_f32_16x16x32_bf16 a[8:11], a[12:15], v[64:67], a[8:11]\n\t" \
  "v_xor_b32 v237, 0x40, v234\n\t" \
  "v_mfma_f32_16x16x32_bf16 a[8:11], a[28:31], v[68:71], a[8:11]\n\t" \
  "buffer_load_dwordx4 v[28:31], v242, %[ws], s73 offen offset:3072 sc1\n\t" \
  "s_xor_b32 s73, %[eso], 688128\n\t" \
  "v_mfma_f32_16x16x32_bf16 a[8:11], a[44:47], v[72:75], a[8:11]\n\t" \
  "ds_read_b64_tr_b16 v[144:145], v237 offset:0\n\t" \
  "v_mfma_f32_16x16x32_bf16 a[8:11], a[60:63], v[76:79], a[8:11]\n\t" \
  "s_waitcnt lgkmcnt(5)\n\t" \
  "ds_read_b64_tr_b16 v[146:147], v237 offset:4096\n\t" \
  "v_add_f32_dpp v226, v224, v224 row_ror:4 row_mask:0xf bank_mask:0xf\n\t" \
  "v_mfma_f32_16x16x32_bf16 a[16:19], a[76:79], v[48:51], a[16:19]\n\t" \
  "ds_read_b64_tr_b16 v[148:149], v237 offset:8192\n\t" \
  "v_add_f32_dpp v227, v225, v225 row_ror:4 row_mask:0xf bank_mask:0xf\n\t" \
  "v_mfma_f32_16x16x32_bf16 a[16:19], a[92:95], v[52:55], a[16:19]\n\t" \
  "s_waitcnt lgkmcnt(3)\n\t" \
  "ds_read_b64_tr_b16 v[150:151], v237 offset:12288\n\t" \
  "v_add_f32_dpp v248, v226, v226 row_ror:8 row_mask:0xf bank_mask:0xf\n\t" \
  "v_mfma_f32_16x16x32_bf16 a[16:19], a[108:111], v[56:59], a[16:19]\n\t" \
  "buffer_load_dwordx4 v[0:3], v242, %[ws], s73 offen offset:0 sc1\n\t" \
  "v_mfma_f32_16x16x32_bf16 a[16:19], a[124:127], v[60:63], a[16:19]\n\t" \
  "ds_read_b64_tr_b16 v[152:153], v237 offset:16384\n\t" \
  "v_add_f32_dpp v250, v227, v227 row_ror:8 row_mask:0xf bank_mask:0xf\n\t" \
  "v_mfma_f32_16x16x32_bf16 a[20:23], a[124:127], v[92:95], a[20:23]\n\t" \
  "ds_read_b64_tr_b16 v[154:155], v237 offset:20480\n\t" \
  "v_mfma_f32_16x16x32_bf16 a[20:23], a[108:111], v[88:91], a[20:23]\n\t" \
  "ds_read_b64_tr_b16 v[156:157], v237 offset:24576\n\t" \
  "v_mfma_f32_16x16x32_bf16 a[20:23], a[92:95], v[84:87], a[20:23]\n\t" \
  "buffer_load_dwordx4 v[4:7], v242, %[ws], s73 offen offset:1024 sc1\n\t" \
  "v_mfma_f32_16x16x32_bf16 a[20:23], a[76:79], v[80:83], a[20:23]\n\t" \
  "ds_read_b64_tr_b16 v[158:159], v237 offset:28672\n\t" \
  "v_mfma_f32_16x16x32_bf16 a[24:27], a[76:79], v[64:67], a[24:27]\n\t" \
  "v_xor_b32 v235, 0x60, v234\n\t" \
  "v_mfma_f32_16x16x32_bf16 a[24:27], a[92:95], v[68:71], a[24:27]\n\t" \
  "ds_read_b64_tr_b16 v[128:129], v235 offset:0\n\t" \
  "v_mfma_f32_16x16x32_bf16 a[24:27], a[108:111], v[72:75], a[24:27]\n\t" \
  "buffer_load_dwordx4 v[8:11], v242, %[ws], s73 offen offset:2048 sc1\n\t" \
  "v_mfma_f32_16x16x32_bf16 a[24:27], a[124:127], v[76:79], a[24:27]\n\t" \
  "s_waitcnt lgkmcnt(5)\n\t" \
  "ds_read_b64_tr_b16 v[130:131], v235 offset:4096\n\t" \
  "v_mfma_f32_16x16x32_bf16 a[32:35], v[144:147], v[48:51], a[32:35]\n\t" \
  "ds_read_b64_tr_b16 v[132:133], v235 offset:8192\n\t" \
  "v_mfma_f32_16x16x32_bf16 a[32:35], v[148:151], v[52:55], a[32:35]\n\t" \
  "s_waitcnt lgkmcnt(3)\n\t" \
  "ds_read_b64_tr_b16 v[134:135], v235 offset:12288\n\t" \
  "v_mfma_f32_16x16x32_bf16 a[32:35], v[152:155], v[56:59], a[32:35]\n\t" \
  "ds_read_b64_tr_b16 v[136:137], v235 offset:16384\n\t" \
  "v_mfma_f32_16x16x32_bf16 a[32:35], v[156:159], v[60:63], a[32:35]\n\t" \
  "buffer_load_dwordx4 v[12:15], v242, %[ws], s73 offen offset:3072 sc1\n\t" \
  "s_xor_b32 s73, %[eso], 720896\n\t" \
  "s_mov_b64 exec, s[82:83]\n\t" \
  "buffer_store_dwordx4 v[248:251], v228, %[ws], %[mbp] offen sc1\n\t" \
  "s_mov_b64 exec, -1\n\t" \
  "v_mfma_f32_16x16x32_bf16 a[36:39], v[156:159], v[92:95], a[36:39]\n\t" \
  "ds_read_b64_tr_b16 v[138:139], v235 offset:20480\n\t" \
  "v_mov_b32 v160, 0\n\t" \
  "v_mov_b32 v161, 0\n\t" \
  "v_mfma_f32_16x16x32_bf16 a[36:39], v[152:155], v[88:91], a[36:39]\n\t" \
  "ds_read_b64_tr_b16 v[140:141], v235 offset:24576\n\t" \
  "v_mov_b32 v162, 0\n\t" \
  "v_mov_b32 v163, 0\n\t" \
  "v_mfma_f32_16x16x32_bf16 a[36:39], v[148:151], v[84:87], a[36:39]\n\t" \
  "ds_read_b64_tr_b16 v[142:143], v235 offset:28672\n\t" \
  "v_mov_b32 v164, 0x3f803f80\n\t" \
  "v_mfma_f32_16x16x32_bf16 a[36:39], v[144:147], v[80:83], a[36:39]\n\t" \
  "v_add_u32 v165, s70, v242\n\t" \
  "v_mfma_f32_16x16x32_bf16 a[40:43], v[144:147], v[64:67], a[40:43]\n\t" \
  "v_xor_b32 v236, 0x80, v234\n\t" \
  "v_mfma_f32_16x16x32_bf16 a[40:43], v[148:151], v[68:71], a[40:43]\n\t" \
  "buffer_load_dwordx4 v[32:35], v242, %[ws], s73 offen offset:0 sc1\n\t" \
  "v_mfma_f32_16x16x32_bf16 a[40:43], v[152:155], v[72:75], a[40:43]\n\t" \
  "ds_read_b64_tr_b16 a[208:209], v236 offset:0\n\t" \
  "v_mfma_f32_16x16x32_bf16 a[40:43], v[156:159], v[76:79], a[40:43]\n\t" \
  "s_waitcnt lgkmcnt(5)\n\t" \
  "ds_write_b128 v165, v[160:163]\n\t" \
  "v_mfma_f32_16x16x32_bf16 a[48:51], v[128:131], v[48:51], a[48:51]\n\t" \
  "ds_read_b64_tr_b16 a[210:211], v236 offset:4096\n\t" \
  "v_and_b32 v171, 15, v244\n\t" \
  "v_mfma_f32_16x16x32_bf16 a[48:51], v[132:135], v[52:55], a[48:51]\n\t" \
  "s_waitcnt lgkmcnt(3)\n\t" \
  "ds_read_b64_tr_b16 a[212:213], v236 offset:8192\n\t" \
  "v_lshrrev_b32 v172, 4, v244\n\t" \
  "v_mfma_f32_16x16x32_bf16 a[48:51], v[136:139], v[56:59], a[48:51]\n\t" \
  "buffer_load_dwordx4 v[36:39], v242, %[ws], s73 offen offset:1024 sc1\n\t" \
  "v_mfma_f32_16x16x32_bf16 a[48:51], v[140:143], v[60:63], a[48:51]\n\t" \
  "ds_read_b64_tr_b16 a[214:215], v236 offset:12288\n\t" \
  "v_lshlrev_b32 v170, 13, v172\n\t" \
  "v_mfma_f32_16x16x32_bf16 a[52:55], v[140:143], v[92:95], a[52:55]\n\t" \
  "ds_read_b64_tr_b16 a[216:217], v236 offset:16384\n\t" \
  "v_lshl_add_u32 v170, v171, 4, v170\n\t" \
  "v_mfma_f32_16x16x32_bf16 a[52:55], v[136:139], v[88:91], a[52:55]\n\t" \
  "ds_read_b64_tr_b16 a[218:219], v236 offset:20480\n\t" \
  "v_lshlrev_b32 v174, 7, v172\n\t" \
  "v_mfma_f32_16x16x32_bf16 a[52:55], v[132:135], v[84:87], a[52:55]\n\t" \
  "buffer_load_dwordx4 v[40:43], v242, %[ws], s73 offen offset:2048 sc1\n\t" \
  "v_mfma_f32_16x16x32_bf16 a[52:55], v[128:131], v[80:83], a[52:55]\n\t" \
  "ds_read_b64_tr_b16 a[220:221], v236 offset:24576\n\t" \
  "ds_read_b64_tr_b16 a[222:223], v236 offset:28672\n\t" \
  "v_mfma_f32_16x16x32_bf16 a[56:59], v[128:131], v[64:67], a[56:59]\n\t" \
  "v_xor_b32 v237, 0xa0, v234\n\t" \
  "v_lshl_add_u32 v174, v171, 3, v174\n\t" \
  "v_mfma_f32_16x16x32_bf16 a[56:59], v[132:135], v[68:71], a[56:59]\n\t" \
  "ds_read_b64_tr_b16 v[144:145], v237 offset:0\n\t" \
  "v_add_u32 v174, s70, v174\n\t" \
  "v_mfma_f32_16x16x32_bf16 a[56:59], v[136:139], v[72:75], a[56:59]\n\t" \
  "buffer_load_dwordx4 v[44:47], v242, %[ws], s73 offen offset:3072 sc1\n\t" \
  "v_mfma_f32_16x16x32_bf16 a[56:59], v[140:143], v[76:79], a[56:59]\n\t" \
  "s_waitcnt lgkmcnt(5)\n\t" \
  "ds_read_b64_tr_b16 v[146:147], v237 offset:4096\n\t" \
  "v_mfma_f32_16x16x32_bf16 a[64:67], a[208:211], v[48:51], a[64:67]\n\t" \
  "s_waitcnt vmcnt(15)\n\t" \
  "buffer_store_dword v243, v254, %[ws], %[fw2] offen offset:64 sc1\n\t" \
  "v_mfma_f32_16x16x32_bf16 a[64:67], a[212:215], v[52:55], a[64:67]\n\t" \
  "s_waitcnt lgkmcnt(2)\n\t" \
  "ds_read_b64_tr_b16 v[148:149], v237 offset:8192\n\t" \
  "v_mfma_f32_16x16x32_bf16 a[64:67], a[216:219], v[56:59], a[64:67]\n\t" \
  "ds_read_b64_tr_b16 v[150:151], v237 offset:12288\n\t" \
  "ds_write_b128 v165, v[160:163] offset:1024\n\t" \
  "v_mfma_f32_16x16x32_bf16 a[64:67], a[220:223], v[60:63], a[64:67]\n\t" \
  "ds_read_b64_tr_b16 v[152:153], v237 offset:16384\n\t" \
  "v_lshlrev_b32 v171, 2, v171\n\t" \
  "v_mfma_f32_16x16x32_bf16 a[68:71], a[220:223], v[92:95], a[68:71]\n\t" \
  "ds_read_b64_tr_b16 v[154:155], v237 offset:20480\n\t" \
  "v_mfma_f32_16x16x32_bf16 a[68:71], a[216:219], v[88:91], a[68:71]\n\t" \
  "ds_read_b64_tr_b16 v[156:157], v237 offset:24576\n\t" \
  "v_mfma_f32_16x16x32_bf16 a[68:71], a[212:215], v[84:87], a[68:71]\n\t" \
  "ds_read_b64_tr_b16 v[158:159], v237 offset:28672\n\t" \
  "v_mfma_f32_16x16x32_bf16 a[68:71], a[208:211], v[80:83], a[68:71]\n\t" \
  "v_cmp_eq_u32 vcc, 2, v172\n\t" \
  "s_nop 1\n\t" \
  "v_cndmask_b32 v172, 0, 1.0, vcc\n\t" \
  "v_mfma_f32_16x16x32_bf16 a[72:75], a[208:211], v[64:67], a[72:75]\n\t" \
  "s_add_u32 m0, %[m], 114688\n\t" \
  "ds_write_b128 v165, v[160:163] offset:1536\n\t" \
  "v_mfma_f32_16x16x32_bf16 a[72:75], a[212:215], v[68:71], a[72:75]\n\t" \
  "v_xor_b32 v235, 0xc0, v234\n\t" \
  "v_mfma_f32_16x16x32_bf16 a[72:75], a[216:219], v[72:75], a[72:75]\n\t" \
  "s_waitcnt lgkmcnt(0)\n\t" \
  "v_bfe_u32 v160, v244, 3, 1\n\t" \
  "v_bfe_u32 v173, v244, 1, 2\n\t" \
  "v_mfma_f32_16x16x32_bf16 a[72:75], a[220:223], v[76:79], a[72:75]\n\t" \
  "s_barrier\n\t" \
  "v_mfma_f32_16x16x32_bf16 a[80:83], v[144:147], v[48:51], a[80:83]\n\t" \
  "s_xor_b32 s71, %[wob], 1703936\n\t" \
  "ds_read_b64_tr_b16 a[192:193], v235 offset:0\n\t" \
  "v_lshlrev_b32 v173, 4, v173\n\t" \
  "v_mfma_f32_16x16x32_bf16 a[80:83], v[148:151], v[52:55], a[80:83]\n\t" \
  "ds_read_b64_tr_b16 a[194:195], v235 offset:4096\n\t" \
  "v_lshl_add_u32 v173, v160, 3, v173\n\t" \
  "v_mfma_f32_16x16x32_bf16 a[80:83], v[152:155], v[56:59], a[80:83]\n\t" \
  "buffer_load_dwordx4 v240, %[wo], s71 offen offset:0 lds\n\t" \
  "v_mfma_f32_16x16x32_bf16 a[80:83], v[156:159], v[60:63], a[80:83]\n\t" \
  "ds_read_b64_tr_b16 a[196:197], v235 offset:8192\n\t" \
  "ds_read_b64_tr_b16 a[198:199], v235 offset:12288\n\t" \
  "v_mfma_f32_16x16x32_bf16 a[84:87], v[156:159], v[92:95], a[84:87]\n\t" \
  "buffer_load_dwordx4 v241, %[wo], s71 offen offset:1024 lds\n\t" \
  "v_mfma_f32_16x16x32_bf16 a[84:87], v[152:155], v[88:91], a[84:87]\n\t" \
  "ds_read_b64_tr_b16 a[200:201], v235 offset:16384\n\t" \
  "ds_read_b64_tr_b16 a[202:203], v235 offset:20480\n\t" \
  "v_mfma_f32_16x16x32_bf16 a[84:87], v[148:151], v[84:87], a[84:87]\n\t" \
  "ds_read_b64_tr_b16 a[204:205], v235 offset:24576\n\t" \
  "v_and_b32 v160, 1, v244\n\t" \
  "v_mfma_f32_16x16x32_bf16 a[84:87], v[144:147], v[80:83], a[84:87]\n\t" \
  "ds_read_b64_tr_b16 a[206:207], v235 offset:28672\n\t" \
  "v_lshl_add_u32 v173, v160, 2, v173\n\t" \
  "v_mfma_f32_16x16x32_bf16 a[88:91], v[144:147], v[64:67], a[88:91]\n\t" \
  "buffer_load_dwordx4 v238, %[wo], s71 offen offset:2048 lds\n\t" \
  "v_mfma_f32_16x16x32_bf16 a[88:91], v[148:151], v[68:71], a[88:91]\n\t" \
  "v_xor_b32 v236, 0xe0, v234\n\t" \
  "v_add_u32 v173, s70, v173\n\t" \
  "v_mfma_f32_16x16x32_bf16 a[88:91], v[152:155], v[72:75], a[88:91]\n\t" \
  "ds_read_b64_tr_b16 a[208:209], v236 offset:0\n\t" \
  "v_mfma_f32_16x16x32_bf16 a[88:91], v[156:159], v[76:79], a[88:91]\n\t" \
  "s_waitcnt lgkmcnt(5)\n\t" \
  "ds_read_b64_tr_b16 a[210:211], v236 offset:4096\n\t" \
  "v_mfma_f32_16x16x32_bf16 a[96:99], a[192:195], v[48:51], a[96:99]\n\t" \
  "buffer_load_dwordx4 v239, %[wo], s71 offen offset:3072 lds\n\t" \
  "s_add_u32 m0, %[m], 131072\n\t" \
  "v_mfma_f32_16x16x32_bf16 a[96:99], a[196:199], v[52:55], a[96:99]\n\t" \
  "s_waitcnt lgkmcnt(2)\n\t" \
  "s_mov_b64 exec, 0xffff\n\t" \
  "ds_write_b32 v173, v164 offset:960\n\t" \
  "s_mov_b64 exec, -1\n\t" \
  "v_mfma_f32_16x16x32_bf16 a[96:99], a[200:203], v[56:59], a[96:99]\n\t" \
  "s_xor_b32 s71, %[wob], 1835008\n\t" \
  "ds_read_b64_tr_b16 a[212:213], v236 offset:8192\n\t" \
  "v_lshrrev_b32 v229, 4, v244\n\t" \
  "v_mfma_f32_16x16x32_bf16 a[96:99], a[204:207], v[60:63], a[96:99]\n\t" \
  "ds_read_b64_tr_b16 a[214:215], v236 offset:12288\n\t" \
  "v_and_b32 v231, 15, v244\n\t" \
  "v_mfma_f32_16x16x32_bf16 a[100:103], a[204:207], v[92:95], a[100:103]\n\t" \
  "ds_read_b64_tr_b16 a[216:217], v236 offset:16384\n\t" \
  "v_xor_b32 v248, v231, v229\n\t" \
  "v_mfma_f32_16x16x32_bf16 a[100:103], a[200:203], v[88:91], a[100:103]\n\t" \
  "buffer_load_dwordx4 v240, %[wo], s71 offen offset:0 lds\n\t" \
  "v_lshlrev_b32 v248, 4, v248\n\t" \
  "v_mfma_f32_16x16x32_bf16 a[100:103], a[196:199], v[84:87], a[100:103]\n\t" \
  "ds_read_b64_tr_b16 a[218:219], v236 offset:20480\n\t" \
  "ds_read_b64_tr_b16 a[220:221], v236 offset:24576\n\t" \
  "v_perm_b32 v254, v180, v176, s79\n\t" \
  "v_mfma_f32_16x16x32_bf16 a[100:103], a[192:195], v[80:83], a[100:103]\n\t" \
  "ds_read_b64_tr_b16 a[222:223], v236 offset:28672\n\t" \
  "v_lshl_add_u32 v248, v229, 11, v248\n\t" \
  "v_mfma_f32_16x16x32_bf16 a[104:107], a[192:195], v[64:67], a[104:107]\n\t" \
  "v_xor_b32 v249, 64, v248\n\t" \
  "v_xor_b32 v250, 0x80, v248\n\t" \
  "v_mfma_f32_16x16x32_bf16 a[104:107], a[196:199], v[68:71], a[104:107]\n\t" \
  "buffer_load_dwordx4 v241, %[wo], s71 offen offset:1024 lds\n\t" \
  "v_perm_b32 v180, v180, v176, s80\n\t" \
  "v_mfma_f32_16x16x32_bf16 a[104:107], a[200:203], v[72:75], a[104:107]\n\t" \
  "v_xor_b32 v237, 0x18000, v234\n\t" \
  "v_xor_b32 v251, 0xc0, v248\n\t" \
  "v_mfma_f32_16x16x32_bf16 a[104:107], a[204:207], v[76:79], a[104:107]\n\t" \
  "s_waitcnt lgkmcnt(4)\n\t" \
  "ds_read_b64_tr_b16 v[128:129], v237 offset:0\n\t" \
  "v_lshrrev_b32 v227, 5, v244\n\t" \
  "s_lshl_b32 s75, %[wid], 2\n\t" \
  "v_mfma_f32_16x16x32_bf16 a[112:115], a[208:211], v[48:51], a[112:115]\n\t" \
  "ds_read_b64_tr_b16 v[130:131], v237 offset:4096\n\t" \
  "v_perm_b32 v176, v181, v177, s79\n\t" \
  "v_mfma_f32_16x16x32_bf16 a[112:115], a[212:215], v[52:55], a[112:115]\n\t" \
  "s_waitcnt lgkmcnt(2)\n\t" \
  "buffer_load_dwordx4 v238, %[wo], s71 offen offset:2048 lds\n\t" \
  "v_mfma_f32_16x16x32_bf16 a[112:115], a[216:219], v[56:59], a[112:115]\n\t" \
  "ds_read_b64_tr_b16 v[132:133], v237 offset:8192\n\t" \
  "v_add_u32 v227, s75, v227\n\t" \
  "v_mfma_f32_16x16x32_bf16 a[112:115], a[220:223], v[60:63], a[112:115]\n\t" \
  "ds_read_b64_tr_b16 v[134:135], v237 offset:12288\n\t" \
  "v_xor_b32 v235, 0x8000, v234\n\t" \
  "v_mfma_f32_16x16x32_bf16 a[116:119], a[220:223], v[92:95], a[116:119]\n\t" \
  "ds_read_b64_tr_b16 v[136:137], v235 offset:0\n\t" \
  "v_perm_b32 v181, v181, v177, s80\n\t" \
  "v_mfma_f32_16x16x32_bf16 a[116:119], a[216:219], v[88:91], a[116:119]\n\t" \
  "ds_read_b64_tr_b16 v[138:139], v235 offset:4096\n\t" \
  "v_xor_b32 v227, v227, v231\n\t" \
  "v_mfma_f32_16x16x32_bf16 a[116:119], a[212:215], v[84:87], a[116:119]\n\t" \
  "buffer_load_dwordx4 v239, %[wo], s71 offen offset:3072 lds\n\t" \
  "v_mfma_f32_16x16x32_bf16 a[116:119], a[208:211], v[80:83], a[116:119]\n\t" \
  "s_xor_b32 s73, %[eso], 786432\n\t" \
  "ds_read_b64_tr_b16 v[140:141], v235 offset:8192\n\t" \
  "ds_read_b64_tr_b16 v[142:143], v235 offset:12288\n\t" \
  "v_mfma_f32_16x16x32_bf16 a[120:123], a[208:211], v[64:67], a[120:123]\n\t" \
  "v_lshlrev_b32 v227, 4, v227\n\t" \
  "v_perm_b32 v177, v182, v178, s79\n\t" \
  "v_mfma_f32_16x16x32_bf16 a[120:123], a[212:215], v[68:71], a[120:123]\n\t" \
  "v_xor_b32 v236, 0x18020, v234\n\t" \
  "v_lshl_add_u32 v227, v231, 8, v227\n\t" \
  "v_mfma_f32_16x16x32_bf16 a[120:123], a[216:219], v[72:75], a[120:123]\n\t" \
  "ds_read_b64_tr_b16 v[144:145], v236 offset:0\n\t" \
  "v_perm_b32 v182, v182, v178, s80\n\t" \
  "v_mfma_f32_16x16x32_bf16 a[120:123], a[220:223], v[76:79], a[120:123]\n\t" \
  "s_waitcnt vmcnt(18) lgkmcnt(5)\n\t" \
  "buffer_load_dwordx4 v[48:51], v242, %[ws], s73 offen offset:0 sc1\n\t" \
  "v_mfma_f32_16x16x32_bf16 a[0:3], v[128:131], v[16:19], a[0:3]\n\t" \
  "ds_read_b64_tr_b16 v[146:147], v236 offset:4096\n\t" \
  "v_and_b32 v229, 1, v229\n\t" \
  "v_mfma_f32_16x16x32_bf16 a[0:3], v[132:135], v[20:23], a[0:3]\n\t" \
  "s_waitcnt lgkmcnt(2)\n\t" \
  "ds_read_b64_tr_b16 v[148:149], v236 offset:8192\n\t" \
  "v_perm_b32 v178, v183, v179, s79\n\t" \
  "v_mfma_f32_16x16x32_bf16 a[0:3], v[136:139], v[24:27], a[0:3]\n\t" \
  "ds_read_b64_tr_b16 v[150:151], v236 offset:12288\n\t" \
  "v_xor_b32 v237, 0x8020, v234\n\t" \
  "v_mfma_f32_16x16x32_bf16 a[0:3], v[140:143], v[28:31], a[0:3]\n\t" \
  "s_waitcnt vmcnt(15)\n\t" \
  "buffer_load_dwordx4 v[52:55], v242, %[ws], s73 offen offset:1024 sc1\n\t" \
  "v_mfma_f32_16x16x32_bf16 a[4:7], v[140:143], v[12:15], a[4:7]\n\t" \
  "ds_read_b64_tr_b16 v[152:153], v237 offset:0\n\t" \
  "v_lshl_add_u32 v227, v229, 3, v227\n\t" \
  "v_mfma_f32_16x16x32_bf16 a[4:7], v[136:139], v[8:11], a[4:7]\n\t" \
  "ds_read_b64_tr_b16 v[154:155], v237 offset:4096\n\t" \
  "v_perm_b32 v183, v183, v179, s80\n\t" \
  "v_mfma_f32_16x16x32_bf16 a[4:7], v[132:135], v[4:7], a[4:7]\n\t" \
  "ds_read_b64_tr_b16 v[156:157], v237 offset:8192\n\t" \
  "v_add_u32 v231, 131072, v227\n\t" \
  "v_mfma_f32_16x16x32_bf16 a[4:7], v[128:131], v[0:3], a[4:7]\n\t" \
  "s_waitcnt vmcnt(11)\n\t" \
  "buffer_load_dwordx4 v[56:59], v242, %[ws], s73 offen offset:2048 sc1\n\t" \
  "v_mfma_f32_16x16x32_bf16 a[8:11], v[128:131], v[32:35], a[8:11]\n\t" \
  "ds_read_b64_tr_b16 v[158:159], v237 offset:12288\n\t" \
  "v_xor_b32 v235, 0x18040, v234\n\t" \
  "v_mfma_f32_16x16x32_bf16 a[8:11], v[132:135], v[36:39], a[8:11]\n\t" \
  "ds_read_b64_tr_b16 a[208:209], v235 offset:0\n\t" \
  "v_xor_b32 v233, 32, v231\n\t" \
  "v_mfma_f32_16x16x32_bf16 a[8:11], v[136:139], v[40:43], a[8:11]\n\t" \
  "s_lshl_b32 s75, %[wid], 9\n\t" \
  "ds_read_b64_tr_b16 a[210:211], v235 offset:4096\n\t" \
  "v_perm_b32 v179, v188, v184, s79\n\t" \
  "v_mfma_f32_16x16x32_bf16 a[8:11], v[140:143], v[44:47], a[8:11]\n\t" \
  "s_waitcnt lgkmcnt(6)\n\t" \
  "buffer_load_dwordx4 v[60:63], v242, %[ws], s73 offen offset:3072 sc1\n\t" \
  "s_xor_b32 s73, %[eso], 819200\n\t" \
  "v_mfma_f32_16x16x32_bf16 a[16:19], v[144:147], v[16:19], a[16:19]\n\t" \
  "ds_read_b64_tr_b16 a[212:213], v235 offset:8192\n\t" \
  "v_lshl_add_u32 v252, v244, 3, s75\n\t" \
  "v_mfma_f32_16x16x32_bf16 a[16:19], v[148:151], v[20:23], a[16:19]\n\t" \
  "s_waitcnt lgkmcnt(3)\n\t" \
  "ds_read_b64_tr_b16 a[214:215], v235 offset:12288\n\t" \
  "v_xor_b32 v236, 0x8040, v234\n\t" \
  "v_mfma_f32_16x16x32_bf16 a[16:19], v[152:155], v[24:27], a[16:19]\n\t" \
  "ds_read_b64_tr_b16 a[216:217], v236 offset:0\n\t" \
  "v_perm_b32 v188, v188, v184, s80\n\t" \
  "v_mfma_f32_16x16x32_bf16 a[16:19], v[156:159], v[28:31], a[16:19]\n\t" \
  "ds_read_b64_tr_b16 a[218:219], v236 offset:4096\n\t" \
  "v_perm_b32 v184, v189, v185, s79\n\t" \
  "v_mfma_f32_16x16x32_bf16 a[20:23], v[156:159], v[12:15], a[20:23]\n\t" \
  "buffer_load_dwordx4 v[80:83], v242, %[ws], s73 offen offset:0 sc1\n\t" \
  "v_mfma_f32_16x16x32_bf16 a[20:23], v[152:155], v[8:11], a[20:23]\n\t" \
  "ds_read_b64_tr_b16 a[220:221], v236 offset:8192\n\t" \
  "v_perm_b32 v189, v189, v185, s80\n\t" \
  "v_mfma_f32_16x16x32_bf16 a[20:23], v[148:151], v[4:7], a[20:23]\n\t" \
  "ds_read_b64_tr_b16 a[222:223], v236 offset:12288\n\t" \
  "v_perm_b32 v185, v190, v186, s79\n\t" \
  "v_mfma_f32_16x16x32_bf16 a[20:23], v[144:147], v[0:3], a[20:23]\n\t" \
  "v_perm_b32 v190, v190, v186, s80\n\t" \
  "v_mfma_f32_16x16x32_bf16 a[24:27], v[144:147], v[32:35], a[24:27]\n\t" \
  "buffer_load_dwordx4 v[84:87], v242, %[ws], s73 offen offset:1024 sc1\n\t" \
  "v_mfma_f32_16x16x32_bf16 a[24:27], v[148:151], v[36:39], a[24:27]\n\t" \
  "v_xor_b32 v237, 0x18060, v234\n\t" \
  "v_perm_b32 v186, v191, v187, s79\n\t" \
  "v_mfma_f32_16x16x32_bf16 a[24:27], v[152:155], v[40:43], a[24:27]\n\t" \
  "ds_read_b64_tr_b16 a[192:193], v237 offset:0\n\t" \
  "v_mfma_f32_16x16x32_bf16 a[24:27], v[156:159], v[44:47], a[24:27]\n\t" \
  "s_waitcnt lgkmcnt(5)\n\t" \
  "ds_read_b64_tr_b16 a[194:195], v237 offset:4096\n\t" \
  "v_mfma_f32_16x16x32_bf16 a[32:35], a[208:211], v[16:19], a[32:35]\n\t" \
  "ds_read_b64_tr_b16 a[196:197], v237 offset:8192\n\t" \
  "v_mfma_f32_16x16x32_bf16 a[32:35], a[212:215], v[20:23], a[32:35]\n\t" \
  "s_waitcnt lgkmcnt(3)\n\t" \
  "buffer_load_dwordx4 v[88:91], v242, %[ws], s73 offen offset:2048 sc1\n\t" \
  "v_mfma_f32_16x16x32_bf16 a[32:35], a[216:219], v[24:27], a[32:35]\n\t" \
  "ds_read_b64_tr_b16 a[198:199], v237 offset:12288\n\t" \
  "v_xor_b32 v235, 0x8060, v234\n\t" \
  "v_mfma_f32_16x16x32_bf16 a[32:35], a[220:223], v[28:31], a[32:35]\n\t" \
  "ds_read_b64_tr_b16 a[200:201], v235 offset:0\n\t" \
  "v_perm_b32 v191, v191, v187, s80\n\t" \
  "v_mfma_f32_16x16x32_bf16 a[36:39], a[220:223], v[12:15], a[36:39]\n\t" \
  "ds_read_b64_tr_b16 a[202:203], v235 offset:4096\n\t" \
  "v_perm_b32 v187, v196, v192, s79\n\t" \
  "v_mfma_f32_16x16x32_bf16 a[36:39], a[216:219], v[8:11], a[36:39]\n\t" \
  "buffer_load_dwordx4 v[92:95], v242, %[ws], s73 offen offset:3072 sc1\n\t" \
  "s_xor_b32 s73, %[eso], 851968\n\t" \
  "v_mfma_f32_16x16x32_bf16 a[36:39], a[212:215], v[4:7], a[36:39]\n\t" \
  "ds_read_b64_tr_b16 a[204:205], v235 offset:8192\n\t" \
  "v_mfma_f32_16x16x32_bf16 a[36:39], a[208:211], v[0:3], a[36:39]\n\t" \
  "ds_read_b64_tr_b16 a[206:207], v235 offset:12288\n\t" \
  "v_mfma_f32_16x16x32_bf16 a[40:43], a[208:211], v[32:35], a[40:43]\n\t" \
  "v_perm_b32 v196, v196, v192, s80\n\t" \
  "v_mfma_f32_16x16x32_bf16 a[40:43], a[212:215], v[36:39], a[40:43]\n\t" \
  "buffer_load_dwordx4 v[64:67], v242, %[ws], s73 offen offset:0 sc1\n\t" \
  "buffer_load_dword v246, v245, %[ws], %[fw2] offen sc1\n\t" \
  "v_mfma_f32_16x16x32_bf16 a[40:43], a[216:219], v[40:43], a[40:43]\n\t" \
  "v_mfma_f32_16x16x32_bf16 a[40:43], a[220:223], v[44:47], a[40:43]\n\t" \
  "s_waitcnt lgkmcnt(4)\n\t" \
  "v_xor_b32 v236, 0x18080, v234\n\t" \
  "v_mfma_f32_16x16x32_bf16 a[48:51], a[192:195], v[16:19], a[48:51]\n\t" \
  "ds_read_b64_tr_b16 a[208:209], v236 offset:0\n\t" \
  "v_perm_b32 v192, v197, v193, s79\n\t" \
  "v_mfma_f32_16x16x32_bf16 a[48:51], a[196:199], v[20:23], a[48:51]\n\t" \
  "s_waitcnt lgkmcnt(1)\n\t" \
  "ds_read_b64_tr_b16 a[210:211], v236 offset:4096\n\t" \
  "v_mfma_f32_16x16x32_bf16 a[48:51], a[200:203], v[24:27], a[48:51]\n\t" \
  "ds_read_b64_tr_b16 a[212:213], v236 offset:8192\n\t" \
  "v_mfma_f32_16x16x32_bf16 a[48:51], a[204:207], v[28:31], a[48:51]\n\t" \
  "buffer_load_dwordx4 v[68:71], v242, %[ws], s73 offen offset:1024 sc1\n\t" \
  "v_mfma_f32_16x16x32_bf16 a[52:55], a[204:207], v[12:15], a[52:55]\n\t" \
  "ds_read_b64_tr_b16 a[214:215], v236 offset:12288\n\t" \
  "v_xor_b32 v237, 0x8080, v234\n\t" \
  "v_mfma_f32_16x16x32_bf16 a[52:55], a[200:203], v[8:11], a[52:55]\n\t" \
  "ds_read_b64_tr_b16 a[216:217], v237 offset:0\n\t" \
  "v_perm_b32 v197, v197, v193, s80\n\t" \
  "v_mfma_f32_16x16x32_bf16 a[52:55], a[196:199], v[4:7], a[52:55]\n\t" \
  "ds_read_b64_tr_b16 a[218:219], v237 offset:4096\n\t" \
  "v_mfma_f32_16x16x32_bf16 a[52:55], a[192:195], v[0:3], a[52:55]\n\t" \
  "buffer_load_dwordx4 v[72:75], v242, %[ws], s73 offen offset:2048 sc1\n\t" \
  "v_mfma_f32_16x16x32_bf16 a[56:59], a[192:195], v[32:35], a[56:59]\n\t" \
  "ds_read_b64_tr_b16 a[220:221], v237 offset:8192\n\t" \
  "v_perm_b32 v193, v198, v194, s79\n\t" \
  "v_mfma_f32_16x16x32_bf16 a[56:59], a[196:199], v[36:39], a[56:59]\n\t" \
  "ds_read_b64_tr_b16 a[222:223], v237 offset:12288\n\t" \
  "v_mfma_f32_16x16x32_bf16 a[56:59], a[200:203], v[40:43], a[56:59]\n\t" \
  "v_mfma_f32_16x16x32_bf16 a[56:59], a[204:207], v[44:47], a[56:59]\n\t" \
  "s_waitcnt lgkmcnt(4)\n\t" \
  "v_xor_b32 v235, 0x180a0, v234\n\t" \
  "v_perm_b32 v198, v198, v194, s80\n\t" \
  "v_mfma_f32_16x16x32_bf16 a[64:67], a[208:211], v[16:19], a[64:67]\n\t" \
  "buffer_load_dwordx4 v[76:79], v242, %[ws], s73 offen offset:3072 sc1\n\t" \
  "v_mfma_f32_16x16x32_bf16 a[64:67], a[212:215], v[20:23], a[64:67]\n\t" \
  "s_waitcnt lgkmcnt(0)\n\t" \
  "ds_read_b64_tr_b16 a[192:193], v235 offset:0\n\t" \
  "s_add_u32 s75, s69, 0\n\t" \
  "v_mfma_f32_16x16x32_bf16 a[64:67], a[216:219], v[24:27], a[64:67]\n\t" \
  "ds_read_b64_tr_b16 a[194:195], v235 offset:4096\n\t" \
  "v_mfma_f32_16x16x32_bf16 a[64:67], a[220:223], v[28:31], a[64:67]\n\t" \
  "ds_read_b64_tr_b16 a[196:197], v235 offset:8192\n\t" \
  "v_perm_b32 v194, v199, v195, s79\n\t" \
  "v_mfma_f32_16x16x32_bf16 a[68:71], a[220:223], v[12:15], a[68:71]\n\t" \
  "buffer_load_dwordx4 v[208:211], v170, %[ws], s75 offen sc1\n\t" \
  "v_mfma_f32_16x16x32_bf16 a[68:71], a[216:219], v[8:11], a[68:71]\n\t" \
  "s_add_u32 s75, s69, 32768\n\t" \
  "ds_read_b64_tr_b16 a[198:199], v235 offset:12288\n\t" \
  "v_xor_b32 v236, 0x80a0, v234\n\t" \
  "v_mfma_f32_16x16x32_bf16 a[68:71], a[212:215], v[4:7], a[68:71]\n\t" \
  "ds_read_b64_tr_b16 a[200:201], v236 offset:0\n\t" \
  "v_mfma_f32_16x16x32_bf16 a[68:71], a[208:211], v[0:3], a[68:71]\n\t" \
  "ds_read_b64_tr_b16 a[202:203], v236 offset:4096\n\t" \
  "v_perm_b32 v199, v199, v195, s80\n\t" \
  "v_mfma_f32_16x16x32_bf16 a[72:75], a[208:211], v[32:35], a[72:75]\n\t" \
  "s_add_u32 m0, %[m], 0\n\t" \
  "ds_read_b64_tr_b16 a[204:205], v236 offset:8192\n\t" \
  "v_mfma_f32_16x16x32_bf16 a[72:75], a[212:215], v[36:39], a[72:75]\n\t" \
  "buffer_load_dwordx4 v[212:215], v170, %[ws], s75 offen sc1\n\t" \
  "v_mfma_f32_16x16x32_bf16 a[72:75], a[216:219], v[40:43], a[72:75]\n\t" \
  "s_add_u32 s75, s69, 65536\n\t" \
  "ds_read_b64_tr_b16 a[206:207], v236 offset:12288\n\t" \
  "v_perm_b32 v195, v204, v200, s79\n\t" \
  "v_mfma_f32_16x16x32_bf16 a[72:75], a[220:223], v[44:47], a[72:75]\n\t" \
  "s_waitcnt vmcnt(19) lgkmcnt(4)\n\t" \
  "s_barrier\n\t" \
  "v_mfma_f32_16x16x32_bf16 a[80:83], a[192:195], v[16:19], a[80:83]\n\t" \
  "s_xor_b32 s71, %[wob], 1966080\n\t" \
  "v_xor_b32 v237, 0x180c0, v234\n\t" \
  "v_mfma_f32_16x16x32_bf16 a[80:83], a[196:199], v[20:23], a[80:83]\n\t" \
  "s_waitcnt lgkmcnt(0)\n\t" \
  "buffer_load_dwordx4 v[216:219], v170, %[ws], s75 offen sc1\n\t" \
  "v_mfma_f32_16x16x32_bf16 a[80:83], a[200:203], v[24:27], a[80:83]\n\t" \
  "s_add_u32 s75, s69, 98304\n\t" \
  "ds_read_b64_tr_b16 a[208:209], v237 offset:0\n\t" \
  "v_perm_b32 v204, v204, v200, s80\n\t" \
  "v_mfma_f32_16x16x32_bf16 a[80:83], a[204:207], v[28:31], a[80:83]\n\t" \
  "ds_read_b64_tr_b16 a[210:211], v237 offset:4096\n\t" \
  "v_mfma_f32_16x16x32_bf16 a[84:87], a[204:207], v[12:15], a[84:87]\n\t" \
  "ds_read_b64_tr_b16 a[212:213], v237 offset:8192\n\t" \
  "v_mfma_f32_16x16x32_bf16 a[84:87], a[200:203], v[8:11], a[84:87]\n\t" \
  "buffer_load_dwordx4 v[220:223], v170, %[ws], s75 offen sc1\n\t" \
  "v_mfma_f32_16x16x32_bf16 a[84:87], a[196:199], v[4:7], a[84:87]\n\t" \
  "ds_read_b64_tr_b16 a[214:215], v237 offset:12288\n\t" \
  "v_xor_b32 v235, 0x80c0, v234\n\t" \
  "v_mfma_f32_16x16x32_bf16 a[84:87], a[192:195], v[0:3], a[84:87]\n\t" \
  "ds_read_b64_tr_b16 a[216:217], v235 offset:0\n\t" \
  "v_perm_b32 v200, v205, v201, s79\n\t" \
  "v_mfma_f32_16x16x32_bf16 a[88:91], a[192:195], v[32:35], a[88:91]\n\t" \
  "buffer_load_dword v224, v171, %[sr], s77 offen\n\t" \
  "v_mfma_f32_16x16x32_bf16 a[88:91], a[196:199], v[36:39], a[88:91]\n\t" \
  "ds_read_b64_tr_b16 a[218:219], v235 offset:4096\n\t" \
  "ds_read_b64_tr_b16 a[220:221], v235 offset:8192\n\t" \
  "v_mfma_f32_16x16x32_bf16 a[88:91], a[200:203], v[40:43], a[88:91]\n\t" \
  "ds_read_b64_tr_b16 a[222:223], v235 offset:12288\n\t" \
  "v_perm_b32 v205, v205, v201, s80\n\t" \
  "v_mfma_f32_16x16x32_bf16 a[88:91], a[204:207], v[44:47], a[88:91]\n\t" \
  "s_waitcnt lgkmcnt(4)\n\t" \
  "buffer_load_dwordx4 v240, %[wo], s71 offen offset:0 lds\n\t" \
  "s_waitcnt vmcnt(9)\n\t" \
  "v_cmp_ne_u32 vcc, %[tag], v246\n\t" \
  "s_and_b32 vcc_lo, vcc_lo, 0xfe0000\n\t" \
  "s_cbranch_scc1 .Lop_s3\n\t" \
  ".Lop_k3:\n\t" \
  "v_mfma_f32_16x16x32_bf16 a[96:99], a[208:211], v[16:19], a[96:99]\n\t" \
  "v_xor_b32 v236, 0x180e0, v234\n\t" \
  "v_mfma_f32_16x16x32_bf16 a[96:99], a[212:215], v[20:23], a[96:99]\n\t" \
  "s_waitcnt lgkmcnt(0)\n\t" \
  "ds_read_b64_tr_b16 a[192:193], v236 offset:0\n\t" \
  "v_perm_b32 v201, v206, v202, s79\n\t" \
  "v_mfma_f32_16x16x32_bf16 a[96:99], a[216:219], v[24:27], a[96:99]\n\t" \
  "ds_read_b64_tr_b16 a[194:195], v236 offset:4096\n\t" \
  "v_mfma_f32_16x16x32_bf16 a[96:99], a[220:223], v[28:31], a[96:99]\n\t" \
  "ds_read_b64_tr_b16 a[196:197], v236 offset:8192\n\t" \
  "v_mfma_f32_16x16x32_bf16 a[100:103], a[220:223], v[12:15], a[100:103]\n\t" \
  "buffer_load_dwordx4 v241, %[wo], s71 offen offset:1024 lds\n\t" \
  "v_mfma_f32_16x16x32_bf16 a[100:103], a[216:219], v[8:11], a[100:103]\n\t" \
  "ds_read_b64_tr_b16 a[198:199], v236 offset:12288\n\t" \
  "v_xor_b32 v237, 0x80e0, v234\n\t" \
  "v_mfma_f32_16x16x32_bf16 a[100:103], a[212:215], v[4:7], a[100:103]\n\t" \
  "ds_read_b64_tr_b16 a[200:201], v237 offset:0\n\t" \
  "v_perm_b32 v206, v206, v202, s80\n\t" \
  "v_mfma_f32_16x16x32_bf16 a[100:103], a[208:211], v[0:3], a[100:103]\n\t" \
  "ds_read_b64_tr_b16 a[202:203], v237 offset:4096\n\t" \
  "v_mfma_f32_16x16x32_bf16 a[104:107], a[208:211], v[32:35], a[104:107]\n\t" \
  "buffer_load_dwordx4 v238, %[wo], s71 offen offset:2048 lds\n\t" \
  "v_mfma_f32_16x16x32_bf16 a[104:107], a[212:215], v[36:39], a[104:107]\n\t" \
  "ds_read_b64_tr_b16 a[204:205], v237 offset:8192\n\t" \
  "v_perm_b32 v202, v207, v203, s79\n\t" \
  "v_mfma_f32_16x16x32_bf16 a[104:107], a[216:219], v[40:43], a[104:107]\n\t" \
  "ds_read_b64_tr_b16 a[206:207], v237 offset:12288\n\t" \
  "v_mfma_f32_16x16x32_bf16 a[104:107], a[220:223], v[44:47], a[104:107]\n\t" \
  "s_waitcnt lgkmcnt(4)\n\t" \
  "v_xor_b32 v235, 0x14000, v234\n\t" \
  "v_mfma_f32_16x16x32_bf16 a[112:115], a[192:195], v[16:19], a[112:115]\n\t" \
  "buffer_load_dwordx4 v239, %[wo], s71 offen offset:3072 lds\n\t" \
  "s_add_u32 m0, %[m], 16384\n\t" \
  "v_mfma_f32_16x16x32_bf16 a[112:115], a[196:199], v[20:23], a[112:115]\n\t" \
  "s_waitcnt lgkmcnt(0)\n\t" \
  "s_xor_b32 s71, %[eso], 1015808\n\t" \
  "ds_read_b64_tr_b16 a[208:209], v235 offset:0\n\t" \
  ".Lhs13:\n\ts_cbranch_execz .Lhs13n\n\t" \
  "v_mfma_f32_16x16x32_bf16 a[112:115], a[200:203], v[24:27], a[112:115]\n\t" \
  "ds_read_b64_tr_b16 a[210:211], v235 offset:4096\n\t" \
  "v_perm_b32 v207, v207, v203, s80\n\t" \
  "v_mfma_f32_16x16x32_bf16 a[112:115], a[204:207], v[28:31], a[112:115]\n\t" \
  "ds_read_b64_tr_b16 a[212:213], v235 offset:8192\n\t" \
  "v_mfma_f32_16x16x32_bf16 a[116:119], a[204:207], v[12:15], a[116:119]\n\t" \
  "ds_read_b64_tr_b16 a[214:215], v235 offset:12288\n\t" \
  "v_mfma_f32_16x16x32_bf16 a[116:119], a[200:203], v[8:11], a[116:119]\n\t" \
  "buffer_load_dwordx4 v242, %[ws], s71 offen offset:0 sc1 lds\n\t" \
  "v_mfma_f32_16x16x32_bf16 a[116:119], a[196:199], v[4:7], a[116:119]\n\t" \
  "ds_read_b64_tr_b16 a[216:217], v235 offset:32768\n\t" \
  "v_mfma_f32_16x16x32_bf16 a[116:119], a[192:195], v[0:3], a[116:119]\n\t" \
  "s_xor_b32 s73, %[eso], 917504\n\t" \
  "ds_read_b64_tr_b16 a[218:219], v235 offset:36864\n\t" \
  "v_mfma_f32_16x16x32_bf16 a[120:123], a[192:195], v[32:35], a[120:123]\n\t" \
  "ds_read_b64_tr_b16 a[220:221], v235 offset:40960\n\t" \
  "v_mfma_f32_16x16x32_bf16 a[120:123], a[196:199], v[36:39], a[120:123]\n\t" \
  "buffer_load_dwordx4 v242, %[ws], s71 offen offset:1024 sc1 lds\n\t" \
  "v_mfma_f32_16x16x32_bf16 a[120:123], a[200:203], v[40:43], a[120:123]\n\t" \
  "ds_read_b64_tr_b16 a[222:223], v235 offset:45056\n\t" \
  "v_mfma_f32_16x16x32_bf16 a[120:123], a[204:207], v[44:47], a[120:123]\n\t" \
  "s_waitcnt lgkmcnt(4)\n\t" \
  "v_xor_b32 v236, 0x14020, v234\n\t" \
  STP("v_writelane_b32 %[stv], s94, 30\n\t") \
  STP("v_writelane_b32 %[stv], s92, 31\n\t") \
  STP("s_memrealtime s[94:95]\n\t") \
  STP("s_memtime s[92:93]\n\t") \
  "v_mfma_f32_16x16x32_bf16 a[0:3], a[208:211], v[48:51], a[0:3]\n\t" \
  "ds_read_b64_tr_b16 a[192:193], v236 offset:0\n\t" \
  "v_mfma_f32_16x16x32_bf16 a[0:3], a[212:215], v[52:55], a[0:3]\n\t" \
  "s_waitcnt lgkmcnt(1)\n\t" \
  "ds_read_b64_tr_b16 a[194:195], v236 offset:4096\n\t" \
  "v_mfma_f32_16x16x32_bf16 a[0:3], a[216:219], v[56:59], a[0:3]\n\t" \
  "buffer_load_dwordx4 v[16:19], v242, %[ws], s73 offen offset:0 sc1\n\t" \
  "v_mfma_f32_16x16x32_bf16 a[0:3], a[220:223], v[60:63], a[0:3]\n\t" \
  "ds_read_b64_tr_b16 a[196:197], v236 offset:8192\n\t" \
  "v_mfma_f32_16x16x32_bf16 a[4:7], a[220:223], v[92:95], a[4:7]\n\t" \
  "ds_read_b64_tr_b16 a[198:199], v236 offset:12288\n\t" \
  "v_mfma_f32_16x16x32_bf16 a[4:7], a[216:219], v[88:91], a[4:7]\n\t" \
  "ds_read_b64_tr_b16 a[200:201], v236 offset:32768\n\t" \
  "v_mfma_f32_16x16x32_bf16 a[4:7], a[212:215], v[84:87], a[4:7]\n\t" \
  "buffer_load_dwordx4 v[20:23], v242, %[ws], s73 offen offset:1024 sc1\n\t" \
  "v_mfma_f32_16x16x32_bf16 a[4:7], a[208:211], v[80:83], a[4:7]\n\t" \
  "s_waitcnt vmcnt(13)\n\t" \
  "ds_read_b64_tr_b16 a[202:203], v236 offset:36864\n\t" \
  "v_mfma_f32_16x16x32_bf16 a[8:11], a[208:211], v[64:67], a[8:11]\n\t" \
  "ds_read_b64_tr_b16 a[204:205], v236 offset:40960\n\t" \
  "v_mfma_f32_16x16x32_bf16 a[8:11], a[212:215], v[68:71], a[8:11]\n\t" \
  "ds_read_b64_tr_b16 a[206:207], v236 offset:45056\n\t" \
  "v_mfma_f32_16x16x32_bf16 a[8:11], a[216:219], v[72:75], a[8:11]\n\t" \
  "buffer_load_dwordx4 v[24:27], v242, %[ws], s73 offen offset:2048 sc1\n\t" \
  "v_mfma_f32_16x16x32_bf16 a[8:11], a[220:223], v[76:79], a[8:11]\n\t" \
  "s_waitcnt lgkmcnt(4)\n\t" \
  "v_xor_b32 v237, 0x14040, v234\n\t" \
  "v_mfma_f32_16x16x32_bf16 a[16:19], a[192:195], v[48:51], a[16:19]\n\t" \
  "ds_read_b64_tr_b16 a[208:209], v237 offset:0\n\t" \
  "v_mfma_f32_16x16x32_bf16 a[16:19], a[196:199], v[52:55], a[16:19]\n\t" \
  "s_waitcnt lgkmcnt(1)\n\t" \
  "ds_read_b64_tr_b16 a[210:211], v237 offset:4096\n\t" \
  "v_mfma_f32_16x16x32_bf16 a[16:19], a[200:203], v[56:59], a[16:19]\n\t" \
  "ds_read_b64_tr_b16 a[212:213], v237 offset:8192\n\t" \
  "v_mfma_f32_16x16x32_bf16 a[16:19], a[204:207], v[60:63], a[16:19]\n\t" \
  "buffer_load_dwordx4 v[28:31], v242, %[ws], s73 offen offset:3072 sc1\n\t" \
  "s_xor_b32 s73, %[eso], 950272\n\t" \
  "v_mfma_f32_16x16x32_bf16 a[20:23], a[204:207], v[92:95], a[20:23]\n\t" \
  "ds_read_b64_tr_b16 a[214:215], v237 offset:12288\n\t" \
  "v_bfrev_b32 v225, 1\n\t" \
  "v_mfma_f32_16x16x32_bf16 a[20:23], a[200:203], v[88:91], a[20:23]\n\t" \
  "ds_read_b64_tr_b16 a[216:217], v237 offset:32768\n\t" \
  "v_bfrev_b32 v226, 1\n\t" \
  "v_mfma_f32_16x16x32_bf16 a[20:23], a[196:199], v[84:87], a[20:23]\n\t" \
  "ds_read_b64_tr_b16 a[218:219], v237 offset:36864\n\t" \
  "v_writelane_b32 v225, s65, 0\n\t" \
  "v_mfma_f32_16x16x32_bf16 a[20:23], a[192:195], v[80:83], a[20:23]\n\t" \
  "buffer_load_dwordx4 v[0:3], v242, %[ws], s73 offen offset:0 sc1\n\t" \
  "v_mfma_f32_16x16x32_bf16 a[24:27], a[192:195], v[64:67], a[24:27]\n\t" \
  "ds_read_b64_tr_b16 a[220:221], v237 offset:40960\n\t" \
  "v_writelane_b32 v225, s66, 1\n\t" \
  "v_mfma_f32_16x16x32_bf16 a[24:27], a[196:199], v[68:71], a[24:27]\n\t" \
  "ds_read_b64_tr_b16 a[222:223], v237 offset:45056\n\t" \
  "s_waitcnt vmcnt(11)\n\t" \
  "v_xor_b32 v246, v243, v209\n\t" \
  "v_mfma_f32_16x16x32_bf16 a[24:27], a[200:203], v[72:75], a[24:27]\n\t" \
  "v_bitop3_b32 v246, v246, v211, v243 bitop3:0xf6\n\t" \
  "v_bitop3_b32 v246, v246, v213, v243 bitop3:0xf6\n\t" \
  "v_mfma_f32_16x16x32_bf16 a[24:27], a[204:207], v[76:79], a[24:27]\n\t" \
  "s_waitcnt lgkmcnt(4)\n\t" \
  "v_xor_b32 v235, 0x14060, v234\n\t" \
  "v_bitop3_b32 v246, v246, v215, v243 bitop3:0xf6\n\t" \
  "v_mfma_f32_16x16x32_bf16 a[32:35], a[208:211], v[48:51], a[32:35]\n\t" \
  "buffer_load_dwordx4 v[4:7], v242, %[ws], s73 offen offset:1024 sc1\n\t" \
  "v_mfma_f32_16x16x32_bf16 a[32:35], a[212:215], v[52:55], a[32:35]\n\t" \
  "s_waitcnt lgkmcnt(0)\n\t" \
  "ds_read_b64_tr_b16 a[192:193], v235 offset:0\n\t" \
  "v_bitop3_b32 v246, v246, v217, v243 bitop3:0xf6\n\t" \
  "v_mfma_f32_16x16x32_bf16 a[32:35], a[216:219], v[56:59], a[32:35]\n\t" \
  "ds_read_b64_tr_b16 a[194:195], v235 offset:4096\n\t" \
  "v_bitop3_b32 v246, v246, v219, v243 bitop3:0xf6\n\t" \
  "v_mfma_f32_16x16x32_bf16 a[32:35], a[220:223], v[60:63], a[32:35]\n\t" \
  "ds_read_b64_tr_b16 a[196:197], v235 offset:8192\n\t" \
  "v_bitop3_b32 v246, v246, v221, v243 bitop3:0xf6\n\t" \
  "v_mfma_f32_16x16x32_bf16 a[36:39], a[220:223], v[92:95], a[36:39]\n\t" \
  "buffer_load_dwordx4 v[8:11], v242, %[ws], s73 offen offset:2048 sc1\n\t" \
  "v_mfma_f32_16x16x32_bf16 a[36:39], a[216:219], v[88:91], a[36:39]\n\t" \
  "ds_read_b64_tr_b16 a[198:199], v235 offset:12288\n\t" \
  "v_bitop3_b32 v246, v246, v223, v243 bitop3:0xf6\n\t" \
  "v_mfma_f32_16x16x32_bf16 a[36:39], a[212:215], v[84:87], a[36:39]\n\t" \
  "ds_read_b64_tr_b16 a[200:201], v235 offset:32768\n\t" \
  "v_writelane_b32 v226, s67, 2\n\t" \
  "v_mfma_f32_16x16x32_bf16 a[36:39], a[208:211], v[80:83], a[36:39]\n\t" \
  "s_mov_b32 s81, 0\n\t" \
  "ds_read_b64_tr_b16 a[202:203], v235 offset:36864\n\t" \
  "v_mfma_f32_16x16x32_bf16 a[40:43], a[208:211], v[64:67], a[40:43]\n\t" \
  "buffer_load_dwordx4 v[12:15], v242, %[ws], s73 offen offset:3072 sc1\n\t" \
  "s_xor_b32 s73, %[eso], 983040\n\t" \
  "v_mfma_f32_16x16x32_bf16 a[40:43], a[212:215], v[68:71], a[40:43]\n\t" \
  "ds_read_b64_tr_b16 a[204:205], v235 offset:40960\n\t" \
  "v_mfma_f32_16x16x32_bf16 a[40:43], a[216:219], v[72:75], a[40:43]\n\t" \
  "ds_read_b64_tr_b16 a[206:207], v235 offset:45056\n\t" \
  "v_mfma_f32_16x16x32_bf16 a[40:43], a[220:223], v[76:79], a[40:43]\n\t" \
  "s_waitcnt lgkmcnt(4)\n\t" \
  "v_cmp_ne_u32 vcc, 0, v246\n\t" \
  "s_cbranch_vccnz .Lop_s4\n\t" \
  ".Lop_k4:\n\t" \
  "v_mfma_f32_16x16x32_bf16 a[48:51], a[192:195], v[48:51], a[48:51]\n\t" \
  "v_add_f32 v208, v208, v212\n\t" \
  "v_add_f32 v216, v216, v220\n\t" \
  "v_add_f32 v210, v210, v214\n\t" \
  "v_mfma_f32_16x16x32_bf16 a[48:51], a[196:199], v[52:55], a[48:51]\n\t" \
  "s_waitcnt lgkmcnt(0)\n\t" \
  "buffer_load_dwordx4 v[32:35], v242, %[ws], s73 offen offset:0 sc1\n\t" \
  "v_add_f32 v218, v218, v222\n\t" \
  "v_mfma_f32_16x16x32_bf16 a[48:51], a[200:203], v[56:59], a[48:51]\n\t" \
  "v_xor_b32 v236, 0x14080, v234\n\t" \
  "v_add_f32 v208, v208, v216\n\t" \
  "v_mfma_f32_16x16x32_bf16 a[48:51], a[204:207], v[60:63], a[48:51]\n\t" \
  "ds_read_b64_tr_b16 a[208:209], v236 offset:0\n\t" \
  "v_add_f32 v210, v210, v218\n\t" \
  "v_mfma_f32_16x16x32_bf16 a[52:55], a[204:207], v[92:95], a[52:55]\n\t" \
  "ds_read_b64_tr_b16 a[210:211], v236 offset:4096\n\t" \
  "v_lshlrev_b32 v163, 16, v224\n\t" \
  "v_mfma_f32_16x16x32_bf16 a[52:55], a[200:203], v[88:91], a[52:55]\n\t" \
  "buffer_load_dwordx4 v[36:39], v242, %[ws], s73 offen offset:1024 sc1\n\t" \
  "v_mfma_f32_16x16x32_bf16 a[52:55], a[196:199], v[84:87], a[52:55]\n\t" \
  "ds_read_b64_tr_b16 a[212:213], v236 offset:8192\n\t" \
  "ds_read_b64_tr_b16 a[214:215], v236 offset:12288\n\t" \
  "v_and_b32 v164, 0xffff0000, v224\n\t" \
  "v_mfma_f32_16x16x32_bf16 a[52:55], a[192:195], v[80:83], a[52:55]\n\t" \
  "ds_read_b64_tr_b16 a[216:217], v236 offset:32768\n\t" \
  "v_cvt_pk_bf16_f32 v162, v208, v210\n\t" \
  "v_mfma_f32_16x16x32_bf16 a[56:59], a[192:195], v[64:67], a[56:59]\n\t" \
  "ds_read_b64_tr_b16 a[218:219], v236 offset:36864\n\t" \
  "v_lshlrev_b32 v160, 16, v162\n\t" \
  "v_mfma_f32_16x16x32_bf16 a[56:59], a[196:199], v[68:71], a[56:59]\n\t" \
  "ds_read_b64_tr_b16 a[220:221], v236 offset:40960\n\t" \
  "v_and_b32 v161, 0xffff0000, v162\n\t" \
  "v_mfma_f32_16x16x32_bf16 a[56:59], a[200:203], v[72:75], a[56:59]\n\t" \
  "buffer_load_dwordx4 v[40:43], v242, %[ws], s73 offen offset:2048 sc1\n\t" \
  "ds_read_b64_tr_b16 a[222:223], v236 offset:45056\n\t" \
  "v_mfma_f32_16x16x32_bf16 a[56:59], a[204:207], v[76:79], a[56:59]\n\t" \
  "s_waitcnt lgkmcnt(4)\n\t" \
  "v_add_f32 v160, v160, v172\n\t" \
  "v_add_f32 v161, v161, v172\n\t" \
  "v_mfma_f32_16x16x32_bf16 a[64:67], a[208:211], v[48:51], a[64:67]\n\t" \
  "v_xor_b32 v237, 0x140a0, v234\n\t" \
  "v_cvt_pk_bf16_f32 v162, v160, v161\n\t" \
  "v_mfma_f32_16x16x32_bf16 a[64:67], a[212:215], v[52:55], a[64:67]\n\t" \
  "s_waitcnt lgkmcnt(0)\n\t" \
  "ds_read_b64_tr_b16 a[192:193], v237 offset:0\n\t" \
  "v_lshlrev_b32 v160, 16, v162\n\t" \
  "v_mfma_f32_16x16x32_bf16 a[64:67], a[216:219], v[56:59], a[64:67]\n\t" \
  "buffer_load_dwordx4 v[44:47], v242, %[ws], s73 offen offset:3072 sc1\n\t" \
  "v_mfma_f32_16x16x32_bf16 a[64:67], a[220:223], v[60:63], a[64:67]\n\t" \
  "ds_read_b64_tr_b16 a[194:195], v237 offset:4096\n\t" \
  "v_and_b32 v161, 0xffff0000, v162\n\t" \
  "v_mfma_f32_16x16x32_bf16 a[68:71], a[220:223], v[92:95], a[68:71]\n\t" \
  "ds_read_b64_tr_b16 a[196:197], v237 offset:8192\n\t" \
  "v_mfma_f32_16x16x32_bf16 a[68:71], a[216:219], v[88:91], a[68:71]\n\t" \
  "ds_read_b64_tr_b16 a[198:199], v237 offset:12288\n\t" \
  "ds_write_b64 v174, v[160:161] offset:2560\n\t" \
  "v_mfma_f32_16x16x32_bf16 a[68:71], a[212:215], v[84:87], a[68:71]\n\t" \
  "buffer_load_dwordx4 v242, %[ws], s71 offen offset:2048 sc1 lds\n\t" \
  "v_mfma_f32_16x16x32_bf16 a[68:71], a[208:211], v[80:83], a[68:71]\n\t" \
  "ds_read_b64_tr_b16 a[200:201], v237 offset:32768\n\t" \
  "v_mul_f32 v166, v160, v163\n\t" \
  "v_mfma_f32_16x16x32_bf16 a[72:75], a[208:211], v[64:67], a[72:75]\n\t" \
  "ds_read_b64_tr_b16 a[202:203], v237 offset:36864\n\t" \
  "v_mul_f32 v167, v161, v164\n\t" \
  "v_mfma_f32_16x16x32_bf16 a[72:75], a[212:215], v[68:71], a[72:75]\n\t" \
  "ds_read_b64_tr_b16 a[204:205], v237 offset:40960\n\t" \
  "v_cvt_pk_bf16_f32 v168, v166, v167\n\t" \
  "v_mfma_f32_16x16x32_bf16 a[72:75], a[216:219], v[72:75], a[72:75]\n\t" \
  "ds_read_b64_tr_b16 a[206:207], v237 offset:45056\n\t" \
  "v_add_f32 v162, v166, v167\n\t" \
  "v_mfma_f32_16x16x32_bf16 a[72:75], a[220:223], v[76:79], a[72:75]\n\t" \
  "s_waitcnt vmcnt(15) lgkmcnt(5)\n\t" \
  "s_barrier\n\t" \
  "v_mfma_f32_16x16x32_bf16 a[80:83], a[192:195], v[48:51], a[80:83]\n\t" \
  "buffer_load_dwordx4 v242, %[ws], s71 offen offset:3072 sc1 lds\n\t" \
  "v_lshlrev_b32 v163, 16, v168\n\t" \
  "v_mfma_f32_16x16x32_bf16 a[80:83], a[196:199], v[52:55], a[80:83]\n\t" \
  "s_waitcnt lgkmcnt(0)\n\t" \
  "v_xor_b32 v235, 0x140c0, v234\n\t" \
  "v_and_b32 v164, 0xffff0000, v168\n\t" \
  "v_mfma_f32_16x16x32_bf16 a[80:83], a[200:203], v[56:59], a[80:83]\n\t" \
  "s_add_u32 m0, %[m], 32768\n\t" \
  "ds_read_b64_tr_b16 a[208:209], v235 offset:0\n\t" \
  "v_add_f32_dpp v162, v162, v162 row_ror:8 row_mask:0xf bank_mask:0xf\n\t" \
  "v_mfma_f32_16x16x32_bf16 a[80:83], a[204:207], v[60:63], a[80:83]\n\t" \
  "s_xor_b32 s71, %[eso], 360448\n\t" \
  "ds_read_b64_tr_b16 a[210:211], v235 offset:4096\n\t" \
  "v_sub_f32 v166, v166, v163\n\t" \
  "v_mfma_f32_16x16x32_bf16 a[84:87], a[204:207], v[92:95], a[84:87]\n\t" \
  "buffer_load_dwordx4 v242, %[ws], s71 offen offset:0 sc1 lds\n\t" \
  "v_mfma_f32_16x16x32_bf16 a[84:87], a[200:203], v[88:91], a[84:87]\n\t" \
  "ds_read_b64_tr_b16 a[212:213], v235 offset:8192\n\t" \
  "v_sub_f32 v167, v167, v164\n\t" \
  "v_mfma_f32_16x16x32_bf16 a[84:87], a[196:199], v[84:87], a[84:87]\n\t" \
  "ds_read_b64_tr_b16 a[214:215], v235 offset:12288\n\t" \
  "v_add_f32_dpp v162, v162, v162 row_ror:4 row_mask:0xf bank_mask:0xf\n\t" \
  "v_mfma_f32_16x16x32_bf16 a[84:87], a[192:195], v[80:83], a[84:87]\n\t" \
  "ds_read_b64_tr_b16 a[216:217], v235 offset:32768\n\t" \
  "v_cvt_pk_bf16_f32 v169, v166, v167\n\t" \
  "v_mfma_f32_16x16x32_bf16 a[88:91], a[192:195], v[64:67], a[88:91]\n\t" \
  "buffer_load_dwordx4 v242, %[ws], s71 offen offset:1024 sc1 lds\n\t" \
  "v_mfma_f32_16x16x32_bf16 a[88:91], a[196:199], v[68:71], a[88:91]\n\t" \
  "s_mov_b32 s82, 0\n\t" \
  "ds_read_b64_tr_b16 a[218:219], v235 offset:36864\n\t" \
  "ds_read_b64_tr_b16 a[220:221], v235 offset:40960\n\t" \
  "v_mfma_f32_16x16x32_bf16 a[88:91], a[200:203], v[72:75], a[88:91]\n\t" \
  "ds_read_b64_tr_b16 a[222:223], v235 offset:45056\n\t" \
  "v_add_f32_dpp v162, v162, v162 row_ror:2 row_mask:0xf bank_mask:0xf\n\t" \
  "v_mfma_f32_16x16x32_bf16 a[88:91], a[204:207], v[76:79], a[88:91]\n\t" \
  "s_waitcnt lgkmcnt(4)\n\t" \
  "s_mov_b32 s83, 0xffff\n\t" \
  "v_add_f32_dpp v162, v162, v162 row_ror:1 row_mask:0xf bank_mask:0xf\n\t" \
  "v_mfma_f32_16x16x32_bf16 a[96:99], a[208:211], v[48:51], a[96:99]\n\t" \
  "buffer_load_dwordx4 v242, %[ws], s71 offen offset:2048 sc1 lds\n\t" \
  "v_mfma_f32_16x16x32_bf16 a[96:99], a[212:215], v[52:55], a[96:99]\n\t" \
  "s_waitcnt lgkmcnt(0)\n\t" \
  "v_xor_b32 v236, 0x140e0, v234\n\t" \
  "v_mfma_f32_16x16x32_bf16 a[96:99], a[216:219], v[56:59], a[96:99]\n\t" \
  "ds_read_b64_tr_b16 a[192:193], v236 offset:0\n\t" \
  "v_mfma_f32_16x16x32_bf16 a[96:99], a[220:223], v[60:63], a[96:99]\n\t" \
  "s_mov_b64 exec, s[82:83]\n\t" \
  "ds_write_b32 v173, v168 offset:1040\n\t" \
  "ds_write_b32 v173, v169 offset:1120\n\t" \
  "s_mov_b64 exec, -1\n\t" \
  "v_mfma_f32_16x16x32_bf16 a[100:103], a[220:223], v[92:95], a[100:103]\n\t" \
  "buffer_load_dwordx4 v242, %[ws], s71 offen offset:3072 sc1 lds\n\t" \
  "s_add_u32 m0, %[m], 98304\n\t" \
  "v_mfma_f32_16x16x32_bf16 a[100:103], a[216:219], v[88:91], a[100:103]\n\t" \
  "s_xor_b32 s71, %[eso], 491520\n\t" \
  "ds_read_b64_tr_b16 a[194:195], v236 offset:4096\n\t" \
  "v_readlane_b32 s87, v162, 32\n\t" \
  "v_readlane_b32 s88, v162, 16\n\t" \
  "v_mfma_f32_16x16x32_bf16 a[100:103], a[212:215], v[84:87], a[100:103]\n\t" \
  "s_xor_b32 s73, %[eso], 196608\n\t" \
  "ds_read_b64_tr_b16 a[196:197], v236 offset:8192\n\t" \
  "ds_read_b64_tr_b16 a[198:199], v236 offset:12288\n\t" \
  "v_mfma_f32_16x16x32_bf16 a[100:103], a[208:211], v[80:83], a[100:103]\n\t" \
  "ds_read_b64_tr_b16 a[200:201], v236 offset:32768\n\t" \
  "v_mfma_f32_16x16x32_bf16 a[104:107], a[208:211], v[64:67], a[104:107]\n\t" \
  "ds_read_b64_tr_b16 a[202:203], v236 offset:36864\n\t" \
  "v_mfma_f32_16x16x32_bf16 a[104:107], a[212:215], v[68:71], a[104:107]\n\t" \
  "buffer_load_dwordx4 v242, %[ws], s71 offen offset:0 sc1 lds\n\t" \
  "v_mfma_f32_16x16x32_bf16 a[104:107], a[216:219], v[72:75], a[104:107]\n\t" \
  "ds_read_b64_tr_b16 a[204:205], v236 offset:40960\n\t" \
  "ds_read_b64_tr_b16 a[206:207], v236 offset:45056\n\t" \
  "v_mfma_f32_16x16x32_bf16 a[104:107], a[220:223], v[76:79], a[104:107]\n\t" \
  "s_waitcnt lgkmcnt(4)\n\t" \
  "v_xor_b32 v237, 0x20040, v234\n\t" \
  "v_mfma_f32_16x16x32_bf16 a[112:115], a[192:195], v[48:51], a[112:115]\n\t" \
  "ds_read_b64_tr_b16 a[208:209], v237 offset:0\n\t" \
  "v_mfma_f32_16x16x32_bf16 a[112:115], a[196:199], v[52:55], a[112:115]\n\t" \
  "s_waitcnt lgkmcnt(1)\n\t" \
  "buffer_load_dwordx4 v[208:211], v242, %[ws], s73 offen offset:0 sc1\n\t" \
  "v_mfma_f32_16x16x32_bf16 a[112:115], a[200:203], v[56:59], a[112:115]\n\t" \
  "ds_read_b64_tr_b16 a[210:211], v237 offset:4096\n\t" \
  "v_mfma_f32_16x16x32_bf16 a[112:115], a[204:207], v[60:63], a[112:115]\n\t" \
  "ds_read_b64_tr_b16 a[212:213], v237 offset:8192\n\t" \
  "v_mfma_f32_16x16x32_bf16 a[116:119], a[204:207], v[92:95], a[116:119]\n\t" \
  "ds_read_b64_tr_b16 a[214:215], v237 offset:12288\n\t" \
  "v_xor_b32 v235, 0x40, v234\n\t" \
  "v_mfma_f32_16x16x32_bf16 a[116:119], a[200:203], v[88:91], a[116:119]\n\t" \
  "ds_read_b64_tr_b16 a[216:217], v235 offset:0\n\t" \
  "v_mfma_f32_16x16x32_bf16 a[116:119], a[196:199], v[84:87], a[116:119]\n\t" \
  "buffer_load_dwordx4 v[212:215], v242, %[ws], s73 offen offset:1024 sc1\n\t" \
  "v_mfma_f32_16x16x32_bf16 a[116:119], a[192:195], v[80:83], a[116:119]\n\t" \
  "ds_read_b64_tr_b16 a[218:219], v235 offset:4096\n\t" \
  "v_mfma_f32_16x16x32_bf16 a[120:123], a[192:195], v[64:67], a[120:123]\n\t" \
  "s_mov_b32 s74, 16384\n\t" \
  "ds_read_b64_tr_b16 a[220:221], v235 offset:8192\n\t" \
  "v_mfma_f32_16x16x32_bf16 a[120:123], a[196:199], v[68:71], a[120:123]\n\t" \
  "ds_read_b64_tr_b16 a[222:223], v235 offset:12288\n\t" \
  "v_mfma_f32_16x16x32_bf16 a[120:123], a[200:203], v[72:75], a[120:123]\n\t" \
  "buffer_load_dwordx4 v[216:219], v242, %[ws], s73 offen offset:2048 sc1\n\t" \
  "v_mfma_f32_16x16x32_bf16 a[120:123], a[204:207], v[76:79], a[120:123]\n\t" \
  "s_waitcnt vmcnt(18) lgkmcnt(4)\n\t" \
  "v_xor_b32 v236, 0x20060, v234\n\t" \
  STP("v_writelane_b32 %[stv], s94, 32\n\t") \
  STP("v_writelane_b32 %[stv], s92, 33\n\t") \
  STP("s_memrealtime s[94:95]\n\t") \
  STP("s_memtime s[92:93]\n\t") \
  "v_mfma_f32_16x16x32_bf16 a[32:35], a[208:211], v[16:19], a[32:35]\n\t" \
  "ds_read_b64_tr_b16 a[192:193], v236 offset:0\n\t" \
  "v_mfma_f32_16x16x32_bf16 a[32:35], a[212:215], v[20:23], a[32:35]\n\t" \
  "s_waitcnt lgkmcnt(1)\n\t" \
  "ds_read_b64_tr_b16 a[194:195], v236 offset:4096\n\t" \
  "v_mfma_f32_16x16x32_bf16 a[32:35], a[216:219], v[24:27], a[32:35]\n\t" \
  "buffer_load_dwordx4 v[220:223], v242, %[ws], s73 offen offset:3072 sc1\n\t" \
  "v_mfma_f32_16x16x32_bf16 a[32:35], a[220:223], v[28:31], a[32:35]\n\t" \
  "s_waitcnt vmcnt(15)\n\t" \
  "ds_read_b64_tr_b16 a[196:197], v236 offset:8192\n\t" \
  "v_mfma_f32_16x16x32_bf16 a[36:39], a[220:223], v[12:15], a[36:39]\n\t" \
  "ds_read_b64_tr_b16 a[198:199], v236 offset:12288\n\t" \
  "v_xor_b32 v237, 0x60, v234\n\t" \
  "v_mfma_f32_16x16x32_bf16 a[36:39], a[216:219], v[8:11], a[36:39]\n\t" \
  "ds_read_b64_tr_b16 a[200:201], v237 offset:0\n\t" \
  "v_mfma_f32_16x16x32_bf16 a[36:39], a[212:215], v[4:7], a[36:39]\n\t" \
  "ds_read_b64_tr_b16 a[202:203], v237 offset:4096\n\t" \
  "v_mfma_f32_16x16x32_bf16 a[36:39], a[208:211], v[0:3], a[36:39]\n\t" \
  "s_waitcnt vmcnt(11)\n\t" \
  "buffer_load_dwordx4 v242, %[ws], s71 offen offset:1024 sc1 lds\n\t" \
  "v_mfma_f32_16x16x32_bf16 a[40:43], a[208:211], v[32:35], a[40:43]\n\t" \
  "ds_read_b64_tr_b16 a[204:205], v237 offset:8192\n\t" \
  "v_mfma_f32_16x16x32_bf16 a[40:43], a[212:215], v[36:39], a[40:43]\n\t" \
  "ds_read_b64_tr_b16 a[206:207], v237 offset:12288\n\t" \
  "v_xor_b32 v235, 0x20000, v234\n\t" \
  "v_mfma_f32_16x16x32_bf16 a[40:43], a[216:219], v[40:43], a[40:43]\n\t" \
  "ds_read_b64_tr_b16 v[64:65], v235 offset:0\n\t" \
  "v_mfma_f32_16x16x32_bf16 a[40:43], a[220:223], v[44:47], a[40:43]\n\t" \
  "s_waitcnt lgkmcnt(5)\n\t" \
  "buffer_load_dwordx4 v242, %[ws], s71 offen offset:2048 sc1 lds\n\t" \
  "v_mfma_f32_16x16x32_bf16 a[48:51], a[192:195], v[16:19], a[48:51]\n\t" \
  "ds_read_b64_tr_b16 v[66:67], v235 offset:4096\n\t" \
  "v_mfma_f32_16x16x32_bf16 a[48:51], a[196:199], v[20:23], a[48:51]\n\t" \
  "s_waitcnt lgkmcnt(2)\n\t" \
  "ds_read_b64_tr_b16 v[68:69], v235 offset:8192\n\t" \
  "v_mfma_f32_16x16x32_bf16 a[48:51], a[200:203], v[24:27], a[48:51]\n\t" \
  "ds_read_b64_tr_b16 v[70:71], v235 offset:12288\n\t" \
  "v_xor_b32 v236, 0x0, v234\n\t" \
  "v_mfma_f32_16x16x32_bf16 a[48:51], a[204:207], v[28:31], a[48:51]\n\t" \
  "ds_read_b64_tr_b16 v[72:73], v236 offset:0\n\t" \
  "v_mfma_f32_16x16x32_bf16 a[52:55], a[204:207], v[12:15], a[52:55]\n\t" \
  "buffer_load_dwordx4 v242, %[ws], s71 offen offset:3072 sc1 lds\n\t" \
  "v_mfma_f32_16x16x32_bf16 a[52:55], a[200:203], v[8:11], a[52:55]\n\t" \
  "ds_read_b64_tr_b16 v[74:75], v236 offset:4096\n\t" \
  "v_accvgpr_read_b32 v160, a32\n\t" \
  "v_mfma_f32_16x16x32_bf16 a[52:55], a[196:199], v[4:7], a[52:55]\n\t" \
  "ds_read_b64_tr_b16 v[76:77], v236 offset:8192\n\t" \
  "v_accvgpr_read_b32 v161, a33\n\t" \
  "v_mfma_f32_16x16x32_bf16 a[52:55], a[192:195], v[0:3], a[52:55]\n\t" \
  "ds_read_b64_tr_b16 v[78:79], v236 offset:12288\n\t" \
  "v_accvgpr_read_b32 v162, a34\n\t" \
  "v_mfma_f32_16x16x32_bf16 a[56:59], a[192:195], v[32:35], a[56:59]\n\t" \
  "buffer_load_dwordx2 v[48:49], v252, %[sd], s74 offen offset:0\n\t" \
  "v_mfma_f32_16x16x32_bf16 a[56:59], a[196:199], v[36:39], a[56:59]\n\t" \
  "v_xor_b32 v237, 0x20020, v234\n\t" \
  "v_accvgpr_read_b32 v163, a35\n\t" \
  "v_mfma_f32_16x16x32_bf16 a[56:59], a[200:203], v[40:43], a[56:59]\n\t" \
  "ds_read_b64_tr_b16 v[80:81], v237 offset:0\n\t" \
  "v_accvgpr_read_b32 v164, a48\n\t" \
  "v_mfma_f32_16x16x32_bf16 a[56:59], a[204:207], v[44:47], a[56:59]\n\t" \
  "s_waitcnt lgkmcnt(5)\n\t" \
  "buffer_load_dwordx2 v[50:51], v252, %[sd], s74 offen offset:2048\n\t" \
  "s_mov_b32 s74, 20480\n\t" \
  "v_mfma_f32_16x16x32_bf16 a[0:3], v[64:67], v[16:19], a[0:3]\n\t" \
  "ds_read_b64_tr_b16 v[82:83], v237 offset:4096\n\t" \
  "v_accvgpr_read_b32 v165, a49\n\t" \
  "v_mfma_f32_16x16x32_bf16 a[0:3], v[68:71], v[20:23], a[0:3]\n\t" \
  "s_waitcnt lgkmcnt(2)\n\t" \
  "ds_read_b64_tr_b16 v[84:85], v237 offset:8192\n\t" \
  "v_accvgpr_read_b32 v166, a50\n\t" \
  "v_accvgpr_read_b32 v167, a51\n\t" \
  "v_mfma_f32_16x16x32_bf16 a[0:3], v[72:75], v[24:27], a[0:3]\n\t" \
  "ds_read_b64_tr_b16 v[86:87], v237 offset:12288\n\t" \
  "v_xor_b32 v235, 0x20, v234\n\t" \
  "v_cvt_pk_bf16_f32 v168, v160, v161\n\t" \
  "v_mfma_f32_16x16x32_bf16 a[0:3], v[76:79], v[28:31], a[0:3]\n\t" \
  "buffer_load_dwordx2 v[52:53], v252, %[sd], s74 offen offset:0\n\t" \
  "v_cvt_pk_bf16_f32 v169, v162, v163\n\t" \
  "v_mfma_f32_16x16x32_bf16 a[4:7], v[76:79], v[12:15], a[4:7]\n\t" \
  "ds_read_b64_tr_b16 v[88:89], v235 offset:0\n\t" \
  "v_cvt_pk_bf16_f32 v170, v164, v165\n\t" \
  "v_cvt_pk_bf16_f32 v171, v166, v167\n\t" \
  "v_mfma_f32_16x16x32_bf16 a[4:7], v[72:75], v[8:11], a[4:7]\n\t" \
  "s_xor_b32 s76, s60, 1024\n\t" \
  "ds_read_b64_tr_b16 v[90:91], v235 offset:4096\n\t" \
  "buffer_store_dwordx4 v[168:171], v242, %[ws], s76 offen sc1\n\t" \
  "v_mfma_f32_16x16x32_bf16 a[4:7], v[68:71], v[4:7], a[4:7]\n\t" \
  "ds_read_b64_tr_b16 v[92:93], v235 offset:8192\n\t" \
  "v_accvgpr_read_b32 v160, a36\n\t" \
  "v_accvgpr_read_b32 v161, a37\n\t" \
  "v_mfma_f32_16x16x32_bf16 a[4:7], v[64:67], v[0:3], a[4:7]\n\t" \
  "ds_read_b64_tr_b16 v[94:95], v235 offset:12288\n\t" \
  "v_accvgpr_read_b32 v162, a38\n\t" \
  "v_accvgpr_read_b32 v163, a39\n\t" \
  "v_mfma_f32_16x16x32_bf16 a[8:11], v[64:67], v[32:35], a[8:11]\n\t" \
  "v_accvgpr_read_b32 v164, a52\n\t" \
  "v_accvgpr_read_b32 v165, a53\n\t" \
  "v_accvgpr_read_b32 v166, a54\n\t" \
  "v_mfma_f32_16x16x32_bf16 a[8:11], v[68:71], v[36:39], a[8:11]\n\t" \
  "buffer_load_dwordx2 v[54:55], v252, %[sd], s74 offen offset:2048\n\t" \
  "s_mov_b32 s74, 24576\n\t" \
  "v_mfma_f32_16x16x32_bf16 a[8:11], v[72:75], v[40:43], a[8:11]\n\t" \
  "v_accvgpr_read_b32 v167, a55\n\t" \
  "v_cvt_pk_bf16_f32 v174, v160, v161\n\t" \
  "v_cvt_pk_bf16_f32 v175, v162, v163\n\t" \
  "v_mfma_f32_16x16x32_bf16 a[8:11], v[76:79], v[44:47], a[8:11]\n\t" \
  "s_waitcnt lgkmcnt(4)\n\t" \
  "v_xor_b32 v236, 0x20080, v234\n\t" \
  "v_cvt_pk_bf16_f32 v172, v164, v165\n\t" \
  "v_cvt_pk_bf16_f32 v173, v166, v167\n\t" \
  "v_mfma_f32_16x16x32_bf16 a[16:19], v[80:83], v[16:19], a[16:19]\n\t" \
  "s_xor_b32 s76, s61, 1024\n\t" \
  "ds_read_b64_tr_b16 a[208:209], v236 offset:0\n\t" \
  "buffer_store_dwordx4 v[172:175], v242, %[ws], s76 offen sc1\n\t" \
  "v_mfma_f32_16x16x32_bf16 a[16:19], v[84:87], v[20:23], a[16:19]\n\t" \
  "s_waitcnt lgkmcnt(1)\n\t" \
  "ds_read_b64_tr_b16 a[210:211], v236 offset:4096\n\t" \
  "v_mfma_f32_16x16x32_bf16 a[16:19], v[88:91], v[24:27], a[16:19]\n\t" \
  "ds_read_b64_tr_b16 a[212:213], v236 offset:8192\n\t" \
  "v_mfma_f32_16x16x32_bf16 a[16:19], v[92:95], v[28:31], a[16:19]\n\t" \
  "buffer_load_dwordx2 v[56:57], v252, %[sd], s74 offen offset:0\n\t" \
  "v_mfma_f32_16x16x32_bf16 a[20:23], v[92:95], v[12:15], a[20:23]\n\t" \
  "ds_read_b64_tr_b16 a[214:215], v236 offset:12288\n\t" \
  "v_xor_b32 v237, 0x80, v234\n\t" \
  "v_mfma_f32_16x16x32_bf16 a[20:23], v[88:91], v[8:11], a[20:23]\n\t" \
  "ds_read_b64_tr_b16 a[216:217], v237 offset:0\n\t" \
  "ds_read_b64_tr_b16 a[218:219], v237 offset:4096\n\t" \
  "v_mfma_f32_16x16x32_bf16 a[20:23], v[84:87], v[4:7], a[20:23]\n\t" \
  "ds_read_b64_tr_b16 a[220:221], v237 offset:8192\n\t" \
  "v_accvgpr_read_b32 v160, a0\n\t" \
  "v_mfma_f32_16x16x32_bf16 a[20:23], v[80:83], v[0:3], a[20:23]\n\t" \
  "buffer_load_dwordx2 v[58:59], v252, %[sd], s74 offen offset:2048\n\t" \
  "s_mov_b32 s74, 28672\n\t" \
  "v_mfma_f32_16x16x32_bf16 a[24:27], v[80:83], v[32:35], a[24:27]\n\t" \
  "ds_read_b64_tr_b16 a[222:223], v237 offset:12288\n\t" \
  "v_xor_b32 v235, 0x200a0, v234\n\t" \
  "v_mfma_f32_16x16x32_bf16 a[24:27], v[84:87], v[36:39], a[24:27]\n\t" \
  "ds_read_b64_tr_b16 a[192:193], v235 offset:0\n\t" \
  "v_accvgpr_read_b32 v161, a1\n\t" \
  "v_mfma_f32_16x16x32_bf16 a[24:27], v[88:91], v[40:43], a[24:27]\n\t" \
  "ds_read_b64_tr_b16 a[194:195], v235 offset:4096\n\t" \
  "v_accvgpr_read_b32 v162, a2\n\t" \
  "v_mfma_f32_16x16x32_bf16 a[24:27], v[92:95], v[44:47], a[24:27]\n\t" \
  "s_waitcnt lgkmcnt(6)\n\t" \
  "buffer_load_dwordx2 v[60:61], v252, %[sd], s74 offen offset:0\n\t" \
  "v_accvgpr_read_b32 v163, a3\n\t" \
  "v_mfma_f32_16x16x32_bf16 a[64:67], a[208:211], v[16:19], a[64:67]\n\t" \
  "ds_read_b64_tr_b16 a[196:197], v235 offset:8192\n\t" \
  "v_accvgpr_read_b32 v164, a16\n\t" \
  "v_accvgpr_read_b32 v165, a17\n\t" \
  "v_mfma_f32_16x16x32_bf16 a[64:67], a[212:215], v[20:23], a[64:67]\n\t" \
  "s_waitcnt lgkmcnt(3)\n\t" \
  "ds_read_b64_tr_b16 a[198:199], v235 offset:12288\n\t" \
  "v_accvgpr_read_b32 v166, a18\n\t" \
  "v_accvgpr_read_b32 v167, a19\n\t" \
  "v_mfma_f32_16x16x32_bf16 a[64:67], a[216:219], v[24:27], a[64:67]\n\t" \
  "v_xor_b32 v236, 0xa0, v234\n\t" \
  "v_cvt_pk_bf16_f32 v168, v160, v161\n\t" \
  "v_cvt_pk_bf16_f32 v169, v162, v163\n\t" \
  "v_mfma_f32_16x16x32_bf16 a[64:67], a[220:223], v[28:31], a[64:67]\n\t" \
  "buffer_load_dwordx2 v[62:63], v252, %[sd], s74 offen offset:2048\n\t" \
  "v_cvt_pk_bf16_f32 v170, v164, v165\n\t" \
  "v_mfma_f32_16x16x32_bf16 a[68:71], a[220:223], v[12:15], a[68:71]\n\t" \
  "ds_read_b64_tr_b16 a[200:201], v236 offset:0\n\t" \
  "v_cvt_pk_bf16_f32 v171, v166, v167\n\t" \
  "s_xor_b32 s76, s60, 0\n\t" \
  "v_mfma_f32_16x16x32_bf16 a[68:71], a[216:219], v[8:11], a[68:71]\n\t" \
  "buffer_store_dwordx4 v[168:171], v242, %[ws], s76 offen sc1\n\t" \
  "v_accvgpr_read_b32 v160, a4\n\t" \
  "v_mfma_f32_16x16x32_bf16 a[68:71], a[212:215], v[4:7], a[68:71]\n\t" \
  "ds_read_b64_tr_b16 a[202:203], v236 offset:4096\n\t" \
  "ds_read_b64_tr_b16 a[204:205], v236 offset:8192\n\t" \
  "v_accvgpr_read_b32 v161, a5\n\t" \
  "v_mfma_f32_16x16x32_bf16 a[68:71], a[208:211], v[0:3], a[68:71]\n\t" \
  "ds_read_b64_tr_b16 a[206:207], v236 offset:12288\n\t" \
  "v_accvgpr_read_b32 v162, a6\n\t" \
  "v_accvgpr_read_b32 v163, a7\n\t" \
  "v_mfma_f32_16x16x32_bf16 a[72:75], a[208:211], v[32:35], a[72:75]\n\t" \
  "v_accvgpr_read_b32 v164, a20\n\t" \
  "v_accvgpr_read_b32 v165, a21\n\t" \
  "v_accvgpr_read_b32 v166, a22\n\t" \
  "v_mfma_f32_16x16x32_bf16 a[72:75], a[212:215], v[36:39], a[72:75]\n\t" \
  "v_accvgpr_read_b32 v167, a23\n\t" \
  "v_cvt_pk_bf16_f32 v174, v160, v161\n\t" \
  "v_cvt_pk_bf16_f32 v175, v162, v163\n\t" \
  "v_mfma_f32_16x16x32_bf16 a[72:75], a[216:219], v[40:43], a[72:75]\n\t" \
  "v_cvt_pk_bf16_f32 v172, v164, v165\n\t" \
  "v_cvt_pk_bf16_f32 v173, v166, v167\n\t" \
  "s_xor_b32 s76, s61, 0\n\t" \
  "v_mfma_f32_16x16x32_bf16 a[72:75], a[220:223], v[44:47], a[72:75]\n\t" \
  "s_waitcnt lgkmcnt(4)\n\t" \
  "s_barrier\n\t" \
  "v_mfma_f32_16x16x32_bf16 a[80:83], a[192:195], v[16:19], a[80:83]\n\t" \
  "buffer_store_dwordx4 v[172:175], v242, %[ws], s76 offen sc1\n\t" \
  "v_mfma_f32_16x16x32_bf16 a[80:83], a[196:199], v[20:23], a[80:83]\n\t" \
  "s_waitcnt lgkmcnt(0)\n\t" \
  "v_xor_b32 v237, 0x200c0, v234\n\t" \
  "v_mfma_f32_16x16x32_bf16 a[80:83], a[200:203], v[24:27], a[80:83]\n\t" \
  "ds_read_b64_tr_b16 a[208:209], v237 offset:0\n\t" \
  "v_mfma_f32_16x16x32_bf16 a[80:83], a[204:207], v[28:31], a[80:83]\n\t" \
  "ds_read_b64_tr_b16 a[210:211], v237 offset:4096\n\t" \
  "v_mfma_f32_16x16x32_bf16 a[84:87], a[204:207], v[12:15], a[84:87]\n\t" \
  "ds_read_b64_tr_b16 a[212:213], v237 offset:8192\n\t" \
  "v_accvgpr_read_b32 v160, a64\n\t" \
  "v_mfma_f32_16x16x32_bf16 a[84:87], a[200:203], v[8:11], a[84:87]\n\t" \
  "ds_read_b64_tr_b16 a[214:215], v237 offset:12288\n\t" \
  "v_xor_b32 v235, 0xc0, v234\n\t" \
  "v_mfma_f32_16x16x32_bf16 a[84:87], a[196:199], v[4:7], a[84:87]\n\t" \
  "ds_read_b64_tr_b16 a[216:217], v235 offset:0\n\t" \
  "v_accvgpr_read_b32 v161, a65\n\t" \
  "v_mfma_f32_16x16x32_bf16 a[84:87], a[192:195], v[0:3], a[84:87]\n\t" \
  "ds_read_b64_tr_b16 a[218:219], v235 offset:4096\n\t" \
  "v_accvgpr_read_b32 v162, a66\n\t" \
  "v_mfma_f32_16x16x32_bf16 a[88:91], a[192:195], v[32:35], a[88:91]\n\t" \
  "ds_read_b64_tr_b16 a[220:221], v235 offset:8192\n\t" \
  "v_accvgpr_read_b32 v163, a67\n\t" \
  "v_mfma_f32_16x16x32_bf16 a[88:91], a[196:199], v[36:39], a[88:91]\n\t" \
  "ds_read_b64_tr_b16 a[222:223], v235 offset:12288\n\t" \
  "v_accvgpr_read_b32 v164, a80\n\t" \
  "v_mfma_f32_16x16x32_bf16 a[88:91], a[200:203], v[40:43], a[88:91]\n\t" \
  "v_accvgpr_read_b32 v165, a81\n\t" \
  "v_accvgpr_read_b32 v166, a82\n\t" \
  "v_mfma_f32_16x16x32_bf16 a[88:91], a[204:207], v[44:47], a[88:91]\n\t" \
  "s_waitcnt lgkmcnt(4)\n\t" \
  "v_xor_b32 v236, 0x200e0, v234\n\t" \
  "v_accvgpr_read_b32 v167, a83\n\t" \
  "v_mfma_f32_16x16x32_bf16 a[96:99], a[208:211], v[16:19], a[96:99]\n\t" \
  "ds_read_b64_tr_b16 a[192:193], v236 offset:0\n\t" \
  "v_cvt_pk_bf16_f32 v168, v160, v161\n\t" \
  "v_mfma_f32_16x16x32_bf16 a[96:99], a[212:215], v[20:23], a[96:99]\n\t" \
  "s_waitcnt lgkmcnt(1)\n\t" \
  "ds_read_b64_tr_b16 a[194:195], v236 offset:4096\n\t" \
  "v_cvt_pk_bf16_f32 v169, v162, v163\n\t" \
  "v_mfma_f32_16x16x32_bf16 a[96:99], a[216:219], v[24:27], a[96:99]\n\t" \
  "ds_read_b64_tr_b16 a[196:197], v236 offset:8192\n\t" \
  "v_cvt_pk_bf16_f32 v170, v164, v165\n\t" \
  "v_mfma_f32_16x16x32_bf16 a[96:99], a[220:223], v[28:31], a[96:99]\n\t" \
  "ds_read_b64_tr_b16 a[198:199], v236 offset:12288\n\t" \
  "v_cvt_pk_bf16_f32 v171, v166, v167\n\t" \
  "s_xor_b32 s76, s60, 2048\n\t" \
  "v_mfma_f32_16x16x32_bf16 a[100:103], a[220:223], v[12:15], a[100:103]\n\t" \
  "buffer_store_dwordx4 v[168:171], v242, %[ws], s76 offen sc1\n\t" \
  "v_accvgpr_read_b32 v160, a68\n\t" \
  "v_mfma_f32_16x16x32_bf16 a[100:103], a[216:219], v[8:11], a[100:103]\n\t" \
  "v_xor_b32 v237, 0xe0, v234\n\t" \
  "v_accvgpr_read_b32 v161, a69\n\t" \
  "v_mfma_f32_16x16x32_bf16 a[100:103], a[212:215], v[4:7], a[100:103]\n\t" \
  "ds_read_b64_tr_b16 a[200:201], v237 offset:0\n\t" \
  "v_accvgpr_read_b32 v162, a70\n\t" \
  "v_accvgpr_read_b32 v163, a71\n\t" \
  ".Lhs14:\n\ts_cbranch_execz .Lhs14n\n\t" \
  "v_mfma_f32_16x16x32_bf16 a[100:103], a[208:211], v[0:3], a[100:103]\n\t" \
  "ds_read_b64_tr_b16 a[202:203], v237 offset:4096\n\t" \
  "v_accvgpr_read_b32 v164, a84\n\t" \
  "v_accvgpr_read_b32 v165, a85\n\t" \
  "v_mfma_f32_16x16x32_bf16 a[104:107], a[208:211], v[32:35], a[104:107]\n\t" \
  "ds_read_b64_tr_b16 a[204:205], v237 offset:8192\n\t" \
  "v_accvgpr_read_b32 v166, a86\n\t" \
  "v_accvgpr_read_b32 v167, a87\n\t" \
  "v_mfma_f32_16x16x32_bf16 a[104:107], a[212:215], v[36:39], a[104:107]\n\t" \
  "ds_read_b64_tr_b16 a[206:207], v237 offset:12288\n\t" \
  "v_xor_b32 v235, 0xc000, v234\n\t" \
  "v_cvt_pk_bf16_f32 v174, v160, v161\n\t" \
  "v_mfma_f32_16x16x32_bf16 a[104:107], a[216:219], v[40:43], a[104:107]\n\t" \
  "ds_read_b64_tr_b16 a[32:33], v235 offset:0\n\t" \
  "ds_read_b64_tr_b16 a[34:35], v235 offset:4096\n\t" \
  "ds_read_b64_tr_b16 a[48:49], v235 offset:8192\n\t" \
  "v_mfma_f32_16x16x32_bf16 a[104:107], a[220:223], v[44:47], a[104:107]\n\t" \
  "s_waitcnt lgkmcnt(7)\n\t" \
  "ds_read_b64_tr_b16 a[50:51], v235 offset:12288\n\t" \
  "ds_read_b64_tr_b16 a[36:37], v235 offset:16384\n\t" \
  "ds_read_b64_tr_b16 a[38:39], v235 offset:20480\n\t" \
  "v_mfma_f32_16x16x32_bf16 a[112:115], a[192:195], v[16:19], a[112:115]\n\t" \
  "ds_read_b64_tr_b16 a[52:53], v235 offset:24576\n\t" \
  "ds_read_b64_tr_b16 a[54:55], v235 offset:28672\n\t" \
  "v_xor_b32 v236, 0xc020, v234\n\t" \
  "v_mfma_f32_16x16x32_bf16 a[112:115], a[196:199], v[20:23], a[112:115]\n\t" \
  "s_waitcnt lgkmcnt(8)\n\t" \
  "ds_read_b64_tr_b16 a[0:1], v236 offset:0\n\t" \
  "ds_read_b64_tr_b16 a[2:3], v236 offset:4096\n\t" \
  "ds_read_b64_tr_b16 a[16:17], v236 offset:8192\n\t" \
  "v_mfma_f32_16x16x32_bf16 a[112:115], a[200:203], v[24:27], a[112:115]\n\t" \
  "ds_read_b64_tr_b16 a[18:19], v236 offset:12288\n\t" \
  "ds_read_b64_tr_b16 a[4:5], v236 offset:16384\n\t" \
  "ds_read_b64_tr_b16 a[6:7], v236 offset:20480\n\t" \
  "v_mfma_f32_16x16x32_bf16 a[112:115], a[204:207], v[28:31], a[112:115]\n\t" \
  "ds_read_b64_tr_b16 a[20:21], v236 offset:24576\n\t" \
  "ds_read_b64_tr_b16 a[22:23], v236 offset:28672\n\t" \
  "v_xor_b32 v237, 0xc040, v234\n\t" \
  "v_mfma_f32_16x16x32_bf16 a[116:119], a[204:207], v[12:15], a[116:119]\n\t" \
  "ds_read_b64_tr_b16 a[64:65], v237 offset:0\n\t" \
  "ds_read_b64_tr_b16 a[66:67], v237 offset:4096\n\t" \
  "v_cvt_pk_bf16_f32 v175, v162, v163\n\t" \
  "v_mfma_f32_16x16x32_bf16 a[116:119], a[200:203], v[8:11], a[116:119]\n\t" \
  "ds_read_b64_tr_b16 a[80:81], v237 offset:8192\n\t" \
  "v_cvt_pk_bf16_f32 v172, v164, v165\n\t" \
  "v_cvt_pk_bf16_f32 v173, v166, v167\n\t" \
  "v_mfma_f32_16x16x32_bf16 a[116:119], a[196:199], v[4:7], a[116:119]\n\t" \
  "s_xor_b32 s76, s61, 2048\n\t" \
  "ds_read_b64_tr_b16 a[82:83], v237 offset:12288\n\t" \
  "ds_read_b64_tr_b16 a[68:69], v237 offset:16384\n\t" \
  "ds_read_b64_tr_b16 a[70:71], v237 offset:20480\n\t" \
  "v_mfma_f32_16x16x32_bf16 a[116:119], a[192:195], v[0:3], a[116:119]\n\t" \
  "buffer_store_dwordx4 v[172:175], v242, %[ws], s76 offen sc1\n\t" \
  "ds_read_b64_tr_b16 a[84:85], v237 offset:24576\n\t" \
  "v_mfma_f32_16x16x32_bf16 a[120:123], a[192:195], v[32:35], a[120:123]\n\t" \
  "ds_read_b64_tr_b16 a[86:87], v237 offset:28672\n\t" \
  "v_xor_b32 v235, 0xc060, v234\n\t" \
  "v_mfma_f32_16x16x32_bf16 a[120:123], a[196:199], v[36:39], a[120:123]\n\t" \
  "ds_read_b64_tr_b16 a[208:209], v235 offset:0\n\t" \
  "ds_read_b64_tr_b16 a[210:211], v235 offset:4096\n\t" \
  "ds_read_b64_tr_b16 a[212:213], v235 offset:8192\n\t" \
  "v_mfma_f32_16x16x32_bf16 a[120:123], a[200:203], v[40:43], a[120:123]\n\t" \
  "ds_read_b64_tr_b16 a[214:215], v235 offset:12288\n\t" \
  "ds_read_b64_tr_b16 a[216:217], v235 offset:16384\n\t" \
  "ds_read_b64_tr_b16 a[218:219], v235 offset:20480\n\t" \
  "v_mfma_f32_16x16x32_bf16 a[120:123], a[204:207], v[44:47], a[120:123]\n\t" \
  "s_waitcnt vmcnt(17) lgkmcnt(15)\n\t" \
  "ds_read_b64_tr_b16 a[220:221], v235 offset:24576\n\t" \
  "ds_read_b64_tr_b16 a[222:223], v235 offset:28672\n\t" \
  "v_accvgpr_read_b32 v160, a96\n\t" \
  "v_mfma_f32_16x16x32_bf16 a[8:11], a[32:35], v[208:211], a[8:11]\n\t" \
  "v_accvgpr_read_b32 v161, a97\n\t" \
  "v_accvgpr_read_b32 v162, a98\n\t" \
  "v_accvgpr_read_b32 v163, a99\n\t" \
  "v_mfma_f32_16x16x32_bf16 a[8:11], a[48:51], v[212:215], a[8:11]\n\t" \
  "v_accvgpr_read_b32 v164, a112\n\t" \
  "v_accvgpr_read_b32 v165, a113\n\t" \
  "v_accvgpr_read_b32 v166, a114\n\t" \
  "v_mfma_f32_16x16x32_bf16 a[8:11], a[36:39], v[216:219], a[8:11]\n\t" \
  "v_xor_b32 v236, 0xc080, v234\n\t" \
  "v_accvgpr_read_b32 v167, a115\n\t" \
  "v_cvt_pk_bf16_f32 v168, v160, v161\n\t" \
  "v_mfma_f32_16x16x32_bf16 a[8:11], a[52:55], v[220:223], a[8:11]\n\t" \
  "ds_read_b64_tr_b16 v[32:33], v236 offset:0\n\t" \
  "v_cvt_pk_bf16_f32 v169, v162, v163\n\t" \
  "v_cvt_pk_bf16_f32 v170, v164, v165\n\t" \
  "v_mfma_f32_16x16x32_bf16 a[24:27], a[0:3], v[208:211], a[24:27]\n\t" \
  "ds_read_b64_tr_b16 v[34:35], v236 offset:4096\n\t" \
  "v_cvt_pk_bf16_f32 v171, v166, v167\n\t" \
  "s_xor_b32 s76, s60, 3072\n\t" \
  "v_mfma_f32_16x16x32_bf16 a[24:27], a[16:19], v[212:215], a[24:27]\n\t" \
  "s_waitcnt lgkmcnt(15)\n\t" \
  "buffer_store_dwordx4 v[168:171], v242, %[ws], s76 offen sc1\n\t" \
  "v_accvgpr_read_b32 v160, a100\n\t" \
  "v_mfma_f32_16x16x32_bf16 a[24:27], a[4:7], v[216:219], a[24:27]\n\t" \
  "ds_read_b64_tr_b16 v[36:37], v236 offset:8192\n\t" \
  "ds_read_b64_tr_b16 v[38:39], v236 offset:12288\n\t" \
  "v_accvgpr_read_b32 v161, a101\n\t" \
  "v_mfma_f32_16x16x32_bf16 a[24:27], a[20:23], v[220:223], a[24:27]\n\t" \
  "s_waitcnt lgkmcnt(15)\n\t" \
  "ds_read_b64_tr_b16 v[40:41], v236 offset:16384\n\t" \
  "v_accvgpr_read_b32 v162, a102\n\t" \
  "v_accvgpr_read_b32 v163, a103\n\t" \
  "v_mfma_f32_16x16x32_bf16 a[40:43], a[64:67], v[208:211], a[40:43]\n\t" \
  "ds_read_b64_tr_b16 v[42:43], v236 offset:20480\n\t" \
  "v_accvgpr_read_b32 v164, a116\n\t" \
  "v_accvgpr_read_b32 v165, a117\n\t" \
  "v_mfma_f32_16x16x32_bf16 a[40:43], a[80:83], v[212:215], a[40:43]\n\t" \
  "s_waitcnt lgkmcnt(14)\n\t" \
  "ds_read_b64_tr_b16 v[44:45], v236 offset:24576\n\t" \
  "v_accvgpr_read_b32 v166, a118\n\t" \
  "v_accvgpr_read_b32 v167, a119\n\t" \
  "v_mfma_f32_16x16x32_bf16 a[40:43], a[68:71], v[216:219], a[40:43]\n\t" \
  "ds_read_b64_tr_b16 v[46:47], v236 offset:28672\n\t" \
  "v_xor_b32 v237, 0xc0a0, v234\n\t" \
  "v_cvt_pk_bf16_f32 v174, v160, v161\n\t" \
  "v_mfma_f32_16x16x32_bf16 a[40:43], a[84:87], v[220:223], a[40:43]\n\t" \
  "s_waitcnt lgkmcnt(12)\n\t" \
  "ds_read_b64_tr_b16 a[192:193], v237 offset:0\n\t" \
  "ds_read_b64_tr_b16 a[194:195], v237 offset:4096\n\t" \
  "ds_read_b64_tr_b16 a[196:197], v237 offset:8192\n\t" \
  "v_mfma_f32_16x16x32_bf16 a[56:59], a[208:211], v[208:211], a[56:59]\n\t" \
  "s_add_u32 m0, %[m], 0\n\t" \
  "ds_read_b64_tr_b16 a[198:199], v237 offset:12288\n\t" \
  "ds_read_b64_tr_b16 a[200:201], v237 offset:16384\n\t" \
  "ds_read_b64_tr_b16 a[202:203], v237 offset:20480\n\t" \
  "v_mfma_f32_16x16x32_bf16 a[56:59], a[212:215], v[212:215], a[56:59]\n\t" \
  "s_waitcnt lgkmcnt(14)\n\t" \
  "ds_read_b64_tr_b16 a[204:205], v237 offset:24576\n\t" \
  "ds_read_b64_tr_b16 a[206:207], v237 offset:28672\n\t" \
  "v_xor_b32 v235, 0xc0c0, v234\n\t" \
  "v_mfma_f32_16x16x32_bf16 a[56:59], a[216:219], v[216:219], a[56:59]\n\t" \
  "ds_read_b64_tr_b16 v[0:1], v235 offset:0\n\t" \
  "ds_read_b64_tr_b16 v[2:3], v235 offset:4096\n\t" \
  "ds_read_b64_tr_b16 v[4:5], v235 offset:8192\n\t" \
  "v_mfma_f32_16x16x32_bf16 a[56:59], a[220:223], v[220:223], a[56:59]\n\t" \
  "s_waitcnt lgkmcnt(15)\n\t" \
  "s_barrier\n\t" \
  "v_mfma_f32_16x16x32_bf16 a[72:75], v[32:35], v[208:211], a[72:75]\n\t" \
  "s_xor_b32 s71, %[eso], 753664\n\t" \
  "ds_read_b64_tr_b16 v[6:7], v235 offset:12288\n\t" \
  "v_cvt_pk_bf16_f32 v175, v162, v163\n\t" \
  "v_cvt_pk_bf16_f32 v172, v164, v165\n\t" \
  "v_mfma_f32_16x16x32_bf16 a[72:75], v[36:39], v[212:215], a[72:75]\n\t" \
  "s_waitcnt lgkmcnt(12)\n\t" \
  "ds_read_b64_tr_b16 v[8:9], v235 offset:16384\n\t" \
  "v_cvt_pk_bf16_f32 v173, v166, v167\n\t" \
  "s_xor_b32 s76, s61, 3072\n\t" \
  "v_mfma_f32_16x16x32_bf16 a[72:75], v[40:43], v[216:219], a[72:75]\n\t" \
  "ds_read_b64_tr_b16 v[10:11], v235 offset:20480\n\t" \
  "ds_read_b64_tr_b16 v[12:13], v235 offset:24576\n\t" \
  "ds_read_b64_tr_b16 v[14:15], v235 offset:28672\n\t" \
  "v_mfma_f32_16x16x32_bf16 a[72:75], v[44:47], v[220:223], a[72:75]\n\t" \
  "s_waitcnt lgkmcnt(12)\n\t" \
  "v_xor_b32 v236, 0xc0e0, v234\n\t" \
  "v_mfma_f32_16x16x32_bf16 a[88:91], a[192:195], v[208:211], a[88:91]\n\t" \
  "ds_read_b64_tr_b16 v[16:17], v236 offset:0\n\t" \
  "ds_read_b64_tr_b16 v[18:19], v236 offset:4096\n\t" \
  "ds_read_b64_tr_b16 v[20:21], v236 offset:8192\n\t" \
  "v_mfma_f32_16x16x32_bf16 a[88:91], a[196:199], v[212:215], a[88:91]\n\t" \
  "s_waitcnt lgkmcnt(11)\n\t" \
  "buffer_store_dwordx4 v[172:175], v242, %[ws], s76 offen sc1\n\t" \
  "v_accvgpr_read_b32 v160, a8\n\t" \
  "v_mfma_f32_16x16x32_bf16 a[88:91], a[200:203], v[216:219], a[88:91]\n\t" \
  "ds_read_b64_tr_b16 v[22:23], v236 offset:12288\n\t" \
  "v_accvgpr_read_b32 v161, a9\n\t" \
  "v_accvgpr_read_b32 v162, a10\n\t" \
  "v_mfma_f32_16x16x32_bf16 a[88:91], a[204:207], v[220:223], a[88:91]\n\t" \
  "s_waitcnt lgkmcnt(8)\n\t" \
  "ds_read_b64_tr_b16 v[24:25], v236 offset:16384\n\t" \
  "v_accvgpr_read_b32 v163, a11\n\t" \
  "v_accvgpr_read_b32 v164, a24\n\t" \
  "v_mfma_f32_16x16x32_bf16 a[104:107], v[0:3], v[208:211], a[104:107]\n\t" \
  "ds_read_b64_tr_b16 v[26:27], v236 offset:20480\n\t" \
  "v_accvgpr_read_b32 v165, a25\n\t" \
  "v_accvgpr_read_b32 v166, a26\n\t" \
  "v_mfma_f32_16x16x32_bf16 a[104:107], v[4:7], v[212:215], a[104:107]\n\t" \
  "s_waitcnt lgkmcnt(6)\n\t" \
  "ds_read_b64_tr_b16 v[28:29], v236 offset:24576\n\t" \
  "v_accvgpr_read_b32 v167, a27\n\t" \
  "v_cvt_pk_bf16_f32 v168, v160, v161\n\t" \
  "v_mfma_f32_16x16x32_bf16 a[104:107], v[8:11], v[216:219], a[104:107]\n\t" \
  "ds_read_b64_tr_b16 v[30:31], v236 offset:28672\n\t" \
  "ds_read_b128 a[208:211], v242 offset:16384\n\t" \
  "v_cvt_pk_bf16_f32 v169, v162, v163\n\t" \
  "v_mfma_f32_16x16x32_bf16 a[104:107], v[12:15], v[220:223], a[104:107]\n\t" \
  "s_waitcnt lgkmcnt(5)\n\t" \
  "ds_read_b128 a[212:215], v242 offset:17408\n\t" \
  "v_cvt_pk_bf16_f32 v170, v164, v165\n\t" \
  "v_cvt_pk_bf16_f32 v171, v166, v167\n\t" \
  "v_mfma_f32_16x16x32_bf16 a[120:123], v[16:19], v[208:211], a[120:123]\n\t" \
  "s_xor_b32 s76, s62, 0\n\t" \
  "ds_read_b128 a[216:219], v242 offset:18432\n\t" \
  "ds_read_b128 a[220:223], v242 offset:19456\n\t" \
  "v_mfma_f32_16x16x32_bf16 a[120:123], v[20:23], v[212:215], a[120:123]\n\t" \
  "s_waitcnt lgkmcnt(4)\n\t" \
  "buffer_store_dwordx4 v[168:171], v242, %[ws], s76 offen sc1\n\t" \
  "v_accvgpr_read_b32 v160, a40\n\t" \
  "v_mfma_f32_16x16x32_bf16 a[120:123], v[24:27], v[216:219], a[120:123]\n\t" \
  "ds_read_b128 v[32:35], v242 offset:20480\n\t" \
  "v_accvgpr_read_b32 v161, a41\n\t" \
  "v_accvgpr_read_b32 v162, a42\n\t" \
  "v_mfma_f32_16x16x32_bf16 a[120:123], v[28:31], v[220:223], a[120:123]\n\t" \
  "s_waitcnt lgkmcnt(1)\n\t" \
  "ds_read_b128 v[36:39], v242 offset:21504\n\t" \
  "v_accvgpr_read_b32 v163, a43\n\t" \
  "v_accvgpr_read_b32 v164, a56\n\t" \
  STP("v_writelane_b32 %[stv], s94, 34\n\t") \
  STP("v_writelane_b32 %[stv], s92, 35\n\t") \
  STP("s_memrealtime s[94:95]\n\t") \
  STP("s_memtime s[92:93]\n\t") \
  "v_mfma_f32_16x16x32_bf16 v[96:99], v[64:67], a[208:211], v[96:99]\n\t" \
  "ds_read_b128 v[40:43], v242 offset:22528\n\t" \
  "v_accvgpr_read_b32 v165, a57\n\t" \
  "v_accvgpr_read_b32 v166, a58\n\t" \
  "v_mfma_f32_16x16x32_bf16 v[96:99], v[68:71], a[212:215], v[96:99]\n\t" \
  "ds_read_b128 v[44:47], v242 offset:23552\n\t" \
  "v_accvgpr_read_b32 v167, a59\n\t" \
  "v_cvt_pk_bf16_f32 v172, v160, v161\n\t" \
  "v_mfma_f32_16x16x32_bf16 v[96:99], v[72:75], a[216:219], v[96:99]\n\t" \
  "ds_read_b128 a[192:195], v242 offset:24576\n\t" \
  "v_cvt_pk_bf16_f32 v173, v162, v163\n\t" \
  "v_cvt_pk_bf16_f32 v174, v164, v165\n\t" \
  "v_mfma_f32_16x16x32_bf16 v[96:99], v[76:79], a[220:223], v[96:99]\n\t" \
  "ds_read_b128 a[196:199], v242 offset:25600\n\t" \
  "v_cvt_pk_bf16_f32 v175, v166, v167\n\t" \
  "s_xor_b32 s76, s62, 1024\n\t" \
  "v_mfma_f32_16x16x32_bf16 v[112:115], v[92:95], a[220:223], v[112:115]\n\t" \
  "buffer_store_dwordx4 v[172:175], v242, %[ws], s76 offen sc1\n\t" \
  "v_accvgpr_read_b32 v160, a72\n\t" \
  "v_mfma_f32_16x16x32_bf16 v[112:115], v[88:91], a[216:219], v[112:115]\n\t" \
  "ds_read_b128 a[200:203], v242 offset:26624\n\t" \
  "v_accvgpr_read_b32 v161, a73\n\t" \
  "v_accvgpr_read_b32 v162, a74\n\t" \
  "v_mfma_f32_16x16x32_bf16 v[112:115], v[84:87], a[212:215], v[112:115]\n\t" \
  "ds_read_b128 a[204:207], v242 offset:27648\n\t" \
  "v_accvgpr_read_b32 v163, a75\n\t" \
  "v_accvgpr_read_b32 v164, a88\n\t" \
  "v_mfma_f32_16x16x32_bf16 v[112:115], v[80:83], a[208:211], v[112:115]\n\t" \
  "s_waitcnt lgkmcnt(4)\n\t" \
  "v_accvgpr_read_b32 v165, a89\n\t" \
  "v_accvgpr_read_b32 v166, a90\n\t" \
  "v_accvgpr_read_b32 v167, a91\n\t" \
  "v_mfma_f32_16x16x32_bf16 v[100:103], v[64:67], v[32:35], v[100:103]\n\t" \
  "buffer_load_dwordx4 v242, %[ws], s71 offen offset:0 sc1 lds\n\t" \
  "v_cvt_pk_bf16_f32 v168, v160, v161\n\t" \
  "v_mfma_f32_16x16x32_bf16 v[100:103], v[68:71], v[36:39], v[100:103]\n\t" \
  "ds_read_b128 a[208:211], v242 offset:28672\n\t" \
  "v_cvt_pk_bf16_f32 v169, v162, v163\n\t" \
  "v_cvt_pk_bf16_f32 v170, v164, v165\n\t" \
  "v_mfma_f32_16x16x32_bf16 v[100:103], v[72:75], v[40:43], v[100:103]\n\t" \
  "ds_read_b128 a[212:215], v242 offset:29696\n\t" \
  "v_cvt_pk_bf16_f32 v171, v166, v167\n\t" \
  "s_xor_b32 s76, s62, 2048\n\t" \
  "v_mfma_f32_16x16x32_bf16 v[100:103], v[76:79], v[44:47], v[100:103]\n\t" \
  "buffer_store_dwordx4 v[168:171], v242, %[ws], s76 offen sc1\n\t" \
  "v_accvgpr_read_b32 v160, a104\n\t" \
  "v_mfma_f32_16x16x32_bf16 v[116:119], v[92:95], v[44:47], v[116:119]\n\t" \
  "s_mov_b32 s74, 0\n\t" \
  "ds_read_b128 a[216:219], v242 offset:30720\n\t" \
  "v_accvgpr_read_b32 v161, a105\n\t" \
  "v_accvgpr_read_b32 v162, a106\n\t" \
  "v_mfma_f32_16x16x32_bf16 v[116:119], v[88:91], v[40:43], v[116:119]\n\t" \
  "buffer_load_dwordx4 v242, %[ws], s71 offen offset:1024 sc1 lds\n\t" \
  "v_accvgpr_read_b32 v163, a107\n\t" \
  "v_mfma_f32_16x16x32_bf16 v[116:119], v[84:87], v[36:39], v[116:119]\n\t" \
  "ds_read_b128 a[220:223], v242 offset:31744\n\t" \
  "v_accvgpr_read_b32 v164, a120\n\t" \
  "v_accvgpr_read_b32 v165, a121\n\t" \
  "v_mfma_f32_16x16x32_bf16 v[116:119], v[80:83], v[32:35], v[116:119]\n\t" \
  "s_waitcnt lgkmcnt(4)\n\t" \
  "s_barrier\n\t" \
  "v_mfma_f32_16x16x32_bf16 v[104:107], v[64:67], a[192:195], v[104:107]\n\t" \
  "ds_read_b128 v[208:211], v242 offset:32768\n\t" \
  "v_accvgpr_read_b32 v166, a122\n\t" \
  "v_accvgpr_read_b32 v167, a123\n\t" \
  "v_mfma_f32_16x16x32_bf16 v[104:107], v[68:71], a[196:199], v[104:107]\n\t" \
  "ds_read_b128 v[212:215], v242 offset:33792\n\t" \
  "v_cvt_pk_bf16_f32 v172, v160, v161\n\t" \
  "v_cvt_pk_bf16_f32 v173, v162, v163\n\t" \
  "v_mfma_f32_16x16x32_bf16 v[104:107], v[72:75], a[200:203], v[104:107]\n\t" \
  "ds_read_b128 v[216:219], v242 offset:34816\n\t" \
  "v_cvt_pk_bf16_f32 v174, v164, v165\n\t" \
  "v_cvt_pk_bf16_f32 v175, v166, v167\n\t" \
  "v_mfma_f32_16x16x32_bf16 v[104:107], v[76:79], a[204:207], v[104:107]\n\t" \
  "s_xor_b32 s76, s62, 3072\n\t" \
  "ds_read_b128 v[220:223], v242 offset:35840\n\t" \
  "buffer_store_dwordx4 v[172:175], v242, %[ws], s76 offen sc1\n\t" \
  "v_mfma_f32_16x16x32_bf16 v[120:123], v[92:95], a[204:207], v[120:123]\n\t" \
  "v_mfma_f32_16x16x32_bf16 v[120:123], v[88:91], a[200:203], v[120:123]\n\t" \
  "buffer_load_dwordx4 v242, %[ws], s71 offen offset:2048 sc1 lds\n\t" \
  "v_mfma_f32_16x16x32_bf16 v[120:123], v[84:87], a[196:199], v[120:123]\n\t" \
  "v_mfma_f32_16x16x32_bf16 v[120:123], v[80:83], a[192:195], v[120:123]\n\t" \
  "s_waitcnt lgkmcnt(4)\n\t" \
  "buffer_load_dwordx4 v242, %[ws], s71 offen offset:3072 sc1 lds\n\t" \
  "s_add_u32 m0, %[m], 131072\n\t" \
  "v_mfma_f32_16x16x32_bf16 v[108:111], v[64:67], a[208:211], v[108:111]\n\t" \
  "s_add_u32 s71, %[xso], 0\n\t" \
  "ds_read_b128 a[192:195], v242 offset:36864\n\t" \
  "v_mfma_f32_16x16x32_bf16 v[108:111], v[68:71], a[212:215], v[108:111]\n\t" \
  "ds_read_b128 a[196:199], v242 offset:37888\n\t" \
  "v_mfma_f32_16x16x32_bf16 v[108:111], v[72:75], a[216:219], v[108:111]\n\t" \
  "ds_read_b128 a[200:203], v242 offset:38912\n\t" \
  "v_mfma_f32_16x16x32_bf16 v[108:111], v[76:79], a[220:223], v[108:111]\n\t" \
  "buffer_load_dwordx4 v248, %[sx], s71 offen offset:0 lds\n\t" \
  "s_add_u32 s71, %[xso], 7168\n\t" \
  "v_mfma_f32_16x16x32_bf16 v[124:127], v[92:95], a[220:223], v[124:127]\n\t" \
  "ds_read_b128 a[204:207], v242 offset:39936\n\t" \
  "v_mfma_f32_16x16x32_bf16 v[124:127], v[88:91], a[216:219], v[124:127]\n\t" \
  "v_mfma_f32_16x16x32_bf16 v[124:127], v[84:87], a[212:215], v[124:127]\n\t" \
  "v_xor_b32 v237, 0x14000, v234\n\t" \
  "v_mfma_f32_16x16x32_bf16 v[124:127], v[80:83], a[208:211], v[124:127]\n\t" \
  "s_waitcnt lgkmcnt(4)\n\t" \
  "buffer_load_dwordx4 v249, %[sx], s71 offen offset:1024 lds\n\t" \
  "s_add_u32 s71, %[xso], 14336\n\t" \
  "v_mfma_f32_16x16x32_bf16 v[96:99], a[160:163], v[208:211], v[96:99]\n\t" \
  "ds_read_b64_tr_b16 a[96:97], v237 offset:0\n\t" \
  "v_mfma_f32_16x16x32_bf16 v[96:99], a[164:167], v[212:215], v[96:99]\n\t" \
  "ds_read_b64_tr_b16 a[98:99], v237 offset:4096\n\t" \
  "v_mfma_f32_16x16x32_bf16 v[96:99], a[168:171], v[216:219], v[96:99]\n\t" \
  "s_waitcnt vmcnt(10)\n\t" \
  "buffer_store_dword v243, v225, %[ws], 0 offen sc1\n\t" \
  "v_mfma_f32_16x16x32_bf16 v[96:99], a[172:175], v[220:223], v[96:99]\n\t" \
  "ds_read_b64_tr_b16 a[112:113], v237 offset:8192\n\t" \
  "v_mfma_f32_16x16x32_bf16 v[112:115], a[188:191], v[220:223], v[112:115]\n\t" \
  "buffer_load_dwordx4 v250, %[sx], s71 offen offset:2048 lds\n\t" \
  "s_add_u32 s71, %[xso], 21504\n\t" \
  "v_mfma_f32_16x16x32_bf16 v[112:115], a[184:187], v[216:219], v[112:115]\n\t" \
  "ds_read_b64_tr_b16 a[114:115], v237 offset:12288\n\t" \
  "v_mfma_f32_16x16x32_bf16 v[112:115], a[180:183], v[212:215], v[112:115]\n\t" \
  "ds_read_b64_tr_b16 a[100:101], v237 offset:32768\n\t" \
  "v_mfma_f32_16x16x32_bf16 v[112:115], a[176:179], v[208:211], v[112:115]\n\t" \
  "s_waitcnt lgkmcnt(5)\n\t" \
  "ds_read_b64_tr_b16 a[102:103], v237 offset:36864\n\t" \
  "v_mfma_f32_16x16x32_bf16 v[100:103], a[160:163], a[192:195], v[100:103]\n\t" \
  "ds_read_b64_tr_b16 a[116:117], v237 offset:40960\n\t" \
  "v_mfma_f32_16x16x32_bf16 v[100:103], a[164:167], a[196:199], v[100:103]\n\t" \
  "buffer_load_dwordx4 v251, %[sx], s71 offen offset:3072 lds\n\t" \
  "s_add_u32 m0, %[m], 49152\n\t" \
  "v_mfma_f32_16x16x32_bf16 v[100:103], a[168:171], a[200:203], v[100:103]\n\t" \
  "s_xor_b32 s71, %[eso], 622592\n\t" \
  "ds_read_b64_tr_b16 a[118:119], v237 offset:45056\n\t" \
  "ds_read_b128 a[208:211], v242 offset:40960\n\t" \
  "ds_read_b128 a[212:215], v242 offset:41984\n\t" \
  "v_mfma_f32_16x16x32_bf16 v[100:103], a[172:175], a[204:207], v[100:103]\n\t" \
  "ds_read_b128 a[216:219], v242 offset:43008\n\t" \
  "ds_read_b128 a[220:223], v242 offset:44032\n\t" \
  "v_mfma_f32_16x16x32_bf16 v[116:119], a[188:191], a[204:207], v[116:119]\n\t" \
  "ds_read_b128 v[208:211], v242 offset:45056\n\t" \
  "v_mfma_f32_16x16x32_bf16 v[116:119], a[184:187], a[200:203], v[116:119]\n\t" \
  "buffer_load_dwordx4 v242, %[ws], s71 offen offset:0 sc1 lds\n\t" \
  "v_mfma_f32_16x16x32_bf16 v[116:119], a[180:183], a[196:199], v[116:119]\n\t" \
  "ds_read_b128 v[212:215], v242 offset:46080\n\t" \
  "ds_read_b128 v[216:219], v242 offset:47104\n\t" \
  "v_mfma_f32_16x16x32_bf16 v[116:119], a[176:179], a[192:195], v[116:119]\n\t" \
  "s_waitcnt lgkmcnt(3)\n\t" \
  "s_barrier\n\t" \
  "v_mfma_f32_16x16x32_bf16 v[104:107], a[160:163], a[208:211], v[104:107]\n\t" \
  "ds_read_b128 v[220:223], v242 offset:48128\n\t" \
  "v_xor_b32 v235, 0x14020, v234\n\t" \
  "v_mfma_f32_16x16x32_bf16 v[104:107], a[164:167], a[212:215], v[104:107]\n\t" \
  "buffer_load_dwordx4 v242, %[ws], s71 offen offset:1024 sc1 lds\n\t" \
  "ds_read_b64_tr_b16 a[64:65], v235 offset:0\n\t" \
  "v_mfma_f32_16x16x32_bf16 v[104:107], a[168:171], a[216:219], v[104:107]\n\t" \
  "ds_read_b64_tr_b16 a[66:67], v235 offset:4096\n\t" \
  "ds_read_b64_tr_b16 a[68:69], v235 offset:8192\n\t" \
  "ds_read_b64_tr_b16 a[70:71], v235 offset:12288\n\t" \
  "v_mfma_f32_16x16x32_bf16 v[104:107], a[172:175], a[220:223], v[104:107]\n\t" \
  "ds_read_b64_tr_b16 a[80:81], v235 offset:32768\n\t" \
  "ds_read_b64_tr_b16 a[82:83], v235 offset:36864\n\t" \
  "v_mfma_f32_16x16x32_bf16 v[120:123], a[188:191], a[220:223], v[120:123]\n\t" \
  "ds_read_b64_tr_b16 a[84:85], v235 offset:40960\n\t" \
  "ds_read_b64_tr_b16 a[86:87], v235 offset:45056\n\t" \
  "v_mfma_f32_16x16x32_bf16 v[120:123], a[184:187], a[216:219], v[120:123]\n\t" \
  "ds_read_b128 a[192:195], v230 offset:32768\n\t" \
  "ds_read_b128 a[196:199], v230 offset:33792\n\t" \
  "v_and_b32 v235, 16, v242\n\t" \
  "v_mfma_f32_16x16x32_bf16 v[120:123], a[180:183], a[212:215], v[120:123]\n\t" \
  "buffer_load_dwordx4 v242, %[ws], s71 offen offset:2048 sc1 lds\n\t" \
  "v_accvgpr_read_b32 v224, a224\n\t" \
  "v_mfma_f32_16x16x32_bf16 v[120:123], a[176:179], a[208:211], v[120:123]\n\t" \
  "s_waitcnt lgkmcnt(10)\n\t" \
  "ds_read_b128 a[200:203], v230 offset:34816\n\t" \
  "ds_read_b128 a[204:207], v230 offset:35840\n\t" \
  "v_mov_b32 v229, 0x3f80\n\t" \
  "v_mfma_f32_16x16x32_bf16 v[108:111], a[160:163], v[208:211], v[108:111]\n\t" \
  "ds_read_b128 v[0:3], v230 offset:36864\n\t" \
  "v_accvgpr_read_b32 v227, a228\n\t" \
  "v_lshlrev_b32 v229, v235, v229\n\t" \
  "v_mfma_f32_16x16x32_bf16 v[108:111], a[164:167], v[212:215], v[108:111]\n\t" \
  "s_mov_b32 s82, 0x300003\n\t" \
  "ds_read_b128 v[4:7], v230 offset:37888\n\t" \
  "v_perm_b32 v64, v227, v224, s79\n\t" \
  "v_mfma_f32_16x16x32_bf16 v[108:111], a[168:171], v[216:219], v[108:111]\n\t" \
  "buffer_load_dwordx4 v242, %[ws], s71 offen offset:3072 sc1 lds\n\t" \
  "s_add_u32 m0, %[m], 65536\n\t" \
  "v_mfma_f32_16x16x32_bf16 v[108:111], a[172:175], v[220:223], v[108:111]\n\t" \
  "s_xor_b32 s71, %[eso], 884736\n\t" \
  "s_mov_b32 s83, 0x30000300\n\t" \
  "ds_read_b128 v[8:11], v230 offset:38912\n\t" \
  "v_mfma_f32_16x16x32_bf16 v[124:127], a[188:191], v[220:223], v[124:127]\n\t" \
  "s_mov_b32 s84, 0xc0000c\n\t" \
  "ds_read_b128 v[12:15], v230 offset:39936\n\t" \
  "v_perm_b32 v68, v227, v224, s80\n\t" \
  "v_mfma_f32_16x16x32_bf16 v[124:127], a[184:187], v[216:219], v[124:127]\n\t" \
  "s_mov_b32 s85, 0xc0000c00\n\t" \
  "v_accvgpr_read_b32 v224, a225\n\t" \
  "v_cndmask_b32 v238, 0, v229, s[82:83]\n\t" \
  "v_mfma_f32_16x16x32_bf16 v[124:127], a[180:183], v[212:215], v[124:127]\n\t" \
  "v_cndmask_b32 v239, 0, v229, s[84:85]\n\t" \
  "v_accvgpr_read_b32 v227, a229\n\t" \
  "v_mfma_f32_16x16x32_bf16 v[124:127], a[176:179], v[208:211], v[124:127]\n\t" \
  "s_waitcnt lgkmcnt(4)\n\t" \
  "buffer_load_dwordx4 v242, %[ws], s71 offen offset:0 sc1 lds\n\t" \
  "v_mfma_f32_16x16x32_bf16 v[96:99], a[128:131], a[192:195], v[96:99]\n\t" \
  "ds_read_b128 v[208:211], v230 offset:40960\n\t" \
  "v_and_b32 v236, 15, v244\n\t" \
  "v_mfma_f32_16x16x32_bf16 v[96:99], a[132:135], a[196:199], v[96:99]\n\t" \
  "ds_read_b128 v[212:215], v230 offset:41984\n\t" \
  "v_lshrrev_b32 v229, 4, v244\n\t" \
  "v_mfma_f32_16x16x32_bf16 v[96:99], a[136:139], a[200:203], v[96:99]\n\t" \
  "ds_read_b128 v[216:219], v230 offset:43008\n\t" \
  "v_perm_b32 v65, v227, v224, s79\n\t" \
  "v_mfma_f32_16x16x32_bf16 v[96:99], a[140:143], a[204:207], v[96:99]\n\t" \
  "s_waitcnt vmcnt(12)\n\t" \
  "buffer_store_dword v243, v226, %[ws], 0 offen sc1\n\t" \
  "v_mfma_f32_16x16x32_bf16 v[112:115], a[156:159], a[204:207], v[112:115]\n\t" \
  "ds_read_b128 v[220:223], v230 offset:44032\n\t" \
  "v_lshlrev_b32 v235, 2, v236\n\t" \
  "v_mfma_f32_16x16x32_bf16 v[112:115], a[152:155], a[200:203], v[112:115]\n\t" \
  "buffer_load_dwordx4 v242, %[ws], s71 offen offset:1024 sc1 lds\n\t" \
  "v_mfma_f32_16x16x32_bf16 v[112:115], a[148:151], a[196:199], v[112:115]\n\t" \
  "v_perm_b32 v69, v227, v224, s80\n\t" \
  "v_mul_u32_u24 v236, 80, v236\n\t" \
  "v_mfma_f32_16x16x32_bf16 v[112:115], a[144:147], a[192:195], v[112:115]\n\t" \
  "s_waitcnt lgkmcnt(4)\n\t" \
  "v_lshlrev_b32 v237, 4, v229\n\t" \
  "v_accvgpr_read_b32 v224, a226\n\t" \
  "v_mfma_f32_16x16x32_bf16 v[100:103], a[128:131], v[0:3], v[100:103]\n\t" \
  "buffer_load_dwordx4 v242, %[ws], s71 offen offset:2048 sc1 lds\n\t" \
  "v_mfma_f32_16x16x32_bf16 v[100:103], a[132:135], v[4:7], v[100:103]\n\t" \
  "ds_read_b128 a[192:195], v230 offset:45056\n\t" \
  "v_add_u32 v237, s70, v237\n\t" \
  "v_mfma_f32_16x16x32_bf16 v[100:103], a[136:139], v[8:11], v[100:103]\n\t" \
  "ds_read_b128 a[196:199], v230 offset:46080\n\t" \
  "v_add_u32 v236, v237, v236\n\t" \
  "v_mfma_f32_16x16x32_bf16 v[100:103], a[140:143], v[12:15], v[100:103]\n\t" \
  "ds_read_b128 a[200:203], v230 offset:47104\n\t" \
  "v_accvgpr_read_b32 v227, a230\n\t" \
  "v_mfma_f32_16x16x32_bf16 v[116:119], a[156:159], v[12:15], v[116:119]\n\t" \
  "buffer_load_dwordx4 v242, %[ws], s71 offen offset:3072 sc1 lds\n\t" \
  "s_add_u32 m0, %[m], 16384\n\t" \
  "v_mfma_f32_16x16x32_bf16 v[116:119], a[152:155], v[8:11], v[116:119]\n\t" \
  "s_xor_b32 s71, %[eso], 229376\n\t" \
  "ds_read_b128 a[204:207], v230 offset:48128\n\t" \
  "ds_read_b128 a[208:211], v236 offset:960\n\t" \
  "v_mfma_f32_16x16x32_bf16 v[116:119], a[148:151], v[4:7], v[116:119]\n\t" \
  "ds_read_b128 a[212:215], v236 offset:640\n\t" \
  "v_perm_b32 v66, v227, v224, s79\n\t" \
  "v_mfma_f32_16x16x32_bf16 v[116:119], a[144:147], v[0:3], v[116:119]\n\t" \
  "s_waitcnt vmcnt(14) lgkmcnt(6)\n\t" \
  "s_barrier\n\t" \
  "v_mfma_f32_16x16x32_bf16 v[104:107], a[128:131], v[208:211], v[104:107]\n\t" \
  "buffer_load_dword v246, v235, %[ws], s68 offen sc1\n\t" \
  "v_mfma_f32_16x16x32_bf16 v[104:107], a[132:135], v[212:215], v[104:107]\n\t" \
  "ds_read_b128 a[160:163], v242 offset:0\n\t" \
  "v_perm_b32 v70, v227, v224, s80\n\t" \
  "v_mfma_f32_16x16x32_bf16 v[104:107], a[136:139], v[216:219], v[104:107]\n\t" \
  "buffer_load_dwordx2 v[32:33], v252, %[sd], s74 offen offset:0\n\t" \
  "v_mfma_f32_16x16x32_bf16 v[104:107], a[140:143], v[220:223], v[104:107]\n\t" \
  "ds_read_b128 a[164:167], v242 offset:1024\n\t" \
  "v_accvgpr_read_b32 v224, a227\n\t" \
  "v_mfma_f32_16x16x32_bf16 v[120:123], a[156:159], v[220:223], v[120:123]\n\t" \
  "ds_read_b128 a[168:171], v242 offset:2048\n\t" \
  "v_accvgpr_read_b32 v227, a231\n\t" \
  "v_mfma_f32_16x16x32_bf16 v[120:123], a[152:155], v[216:219], v[120:123]\n\t" \
  "ds_read_b128 a[172:175], v242 offset:3072\n\t" \
  "v_perm_b32 v67, v227, v224, s79\n\t" \
  "v_mfma_f32_16x16x32_bf16 v[120:123], a[148:151], v[212:215], v[120:123]\n\t" \
  "buffer_load_dwordx2 v[34:35], v252, %[sd], s74 offen offset:2048\n\t" \
  "s_mov_b32 s74, 4096\n\t" \
  "v_mfma_f32_16x16x32_bf16 v[120:123], a[144:147], v[208:211], v[120:123]\n\t" \
  "s_waitcnt lgkmcnt(6)\n\t" \
  "ds_read_b128 a[216:219], v236 offset:320\n\t" \
  "v_perm_b32 v71, v227, v224, s80\n\t" \
  "v_mfma_f32_16x16x32_bf16 v[108:111], a[128:131], a[192:195], v[108:111]\n\t" \
  "ds_read_b128 v[208:211], v242 offset:4096\n\t" \
  "v_accvgpr_read_b32 v224, a232\n\t" \
  "v_mfma_f32_16x16x32_bf16 v[108:111], a[132:135], a[196:199], v[108:111]\n\t" \
  "ds_read_b128 v[212:215], v242 offset:5120\n\t" \
  "v_accvgpr_read_b32 v227, a236\n\t" \
  "v_mfma_f32_16x16x32_bf16 v[108:111], a[136:139], a[200:203], v[108:111]\n\t" \
  "buffer_load_dwordx2 v[36:37], v252, %[sd], s74 offen offset:0\n\t" \
  "v_mfma_f32_16x16x32_bf16 v[108:111], a[140:143], a[204:207], v[108:111]\n\t" \
  "ds_read_b128 v[216:219], v242 offset:6144\n\t" \
  "v_perm_b32 v72, v227, v224, s79\n\t" \
  "v_mfma_f32_16x16x32_bf16 v[124:127], a[156:159], a[204:207], v[124:127]\n\t" \
  "ds_read_b128 v[220:223], v242 offset:7168\n\t" \
  "v_perm_b32 v76, v227, v224, s80\n\t" \
  "v_mfma_f32_16x16x32_bf16 v[124:127], a[152:155], a[200:203], v[124:127]\n\t" \
  "buffer_load_dwordx2 v[38:39], v252, %[sd], s74 offen offset:2048\n\t" \
  "s_mov_b32 s74, 8192\n\t" \
  "v_mfma_f32_16x16x32_bf16 v[124:127], a[148:151], a[196:199], v[124:127]\n\t" \
  "ds_read_b128 a[220:223], v236 offset:0\n\t" \
  "v_accvgpr_read_b32 v224, a233\n\t" \
  "v_mfma_f32_16x16x32_bf16 v[124:127], a[144:147], a[192:195], v[124:127]\n\t" \
  "s_waitcnt lgkmcnt(6)\n\t" \
  "v_accvgpr_read_b32 v227, a237\n\t" \
  "v_perm_b32 v73, v227, v224, s79\n\t" \
  "v_mfma_f32_16x16x32_bf16 v[96:99], v[128:131], a[160:163], v[96:99]\n\t" \
  "ds_read_b128 a[128:131], v242 offset:8192\n\t" \
  "v_mfma_f32_16x16x32_bf16 v[96:99], v[132:135], a[164:167], v[96:99]\n\t" \
  "buffer_load_dwordx2 v[40:41], v252, %[sd], s74 offen offset:0\n\t" \
  "v_mfma_f32_16x16x32_bf16 v[96:99], v[136:139], a[168:171], v[96:99]\n\t" \
  "ds_read_b128 a[132:135], v242 offset:9216\n\t" \
  "v_perm_b32 v77, v227, v224, s80\n\t" \
  "v_mfma_f32_16x16x32_bf16 v[96:99], v[140:143], a[172:175], v[96:99]\n\t" \
  "ds_read_b128 a[136:139], v242 offset:10240\n\t" \
  "v_mfma_f32_16x16x32_bf16 v[112:115], v[156:159], a[172:175], v[112:115]\n\t" \
  "ds_read_b128 a[140:143], v242 offset:11264\n\t" \
  "v_accvgpr_read_b32 v224, a234\n\t" \
  "v_mfma_f32_16x16x32_bf16 v[112:115], v[152:155], a[168:171], v[112:115]\n\t" \
  "buffer_load_dwordx2 v[42:43], v252, %[sd], s74 offen offset:2048\n\t" \
  "s_mov_b32 s74, 12288\n\t" \
  "v_mfma_f32_16x16x32_bf16 v[112:115], v[148:151], a[164:167], v[112:115]\n\t" \
  "v_mfma_f32_16x16x32_bf16 v[112:115], v[144:147], a[160:163], v[112:115]\n\t" \
  "s_waitcnt lgkmcnt(5)\n\t" \
  "v_accvgpr_read_b32 v227, a238\n\t" \
  "v_mfma_f32_16x16x32_bf16 v[100:103], v[128:131], v[208:211], v[100:103]\n\t" \
  "ds_read_b128 a[144:147], v242 offset:12288\n\t" \
  "v_accvgpr_write_b32 a192, 0\n\t" \
  "v_mfma_f32_16x16x32_bf16 v[100:103], v[132:135], v[212:215], v[100:103]\n\t" \
  "buffer_load_dwordx2 v[44:45], v252, %[sd], s74 offen offset:0\n\t" \
  "v_mfma_f32_16x16x32_bf16 v[100:103], v[136:139], v[216:219], v[100:103]\n\t" \
  "ds_read_b128 a[148:151], v242 offset:13312\n\t" \
  "v_accvgpr_write_b32 a193, 0\n\t" \
  "v_mfma_f32_16x16x32_bf16 v[100:103], v[140:143], v[220:223], v[100:103]\n\t" \
  "ds_read_b128 a[152:155], v242 offset:14336\n\t" \
  "v_accvgpr_write_b32 a194, v238\n\t" \
  ".Lhs15:\n\ts_cbranch_execz .Lhs15n\n\t" \
  "v_mfma_f32_16x16x32_bf16 v[116:119], v[156:159], v[220:223], v[116:119]\n\t" \
  "ds_read_b128 a[156:159], v242 offset:15360\n\t" \
  "v_accvgpr_write_b32 a195, v239\n\t" \
  "v_mfma_f32_16x16x32_bf16 v[116:119], v[152:155], v[216:219], v[116:119]\n\t" \
  "buffer_load_dwordx2 v[46:47], v252, %[sd], s74 offen offset:2048\n\t" \
  "v_mfma_f32_16x16x32_bf16 v[116:119], v[148:151], v[212:215], v[116:119]\n\t" \
  "v_accvgpr_write_b32 a196, v238\n\t" \
  "v_accvgpr_write_b32 a197, v239\n\t" \
  "v_mfma_f32_16x16x32_bf16 v[116:119], v[144:147], v[208:211], v[116:119]\n\t" \
  "s_waitcnt vmcnt(14) lgkmcnt(4)\n\t" \
  "s_barrier\n\t" \
  "v_mfma_f32_16x16x32_bf16 v[104:107], v[128:131], a[128:131], v[104:107]\n\t" \
  "ds_read_b128 v[16:19], v242 offset:49152\n\t" \
  "v_accvgpr_write_b32 a198, 0\n\t" \
  "v_mfma_f32_16x16x32_bf16 v[104:107], v[132:135], a[132:135], v[104:107]\n\t" \
  "ds_read_b128 v[20:23], v242 offset:50176\n\t" \
  "v_accvgpr_write_b32 a199, 0\n\t" \
  "v_mfma_f32_16x16x32_bf16 v[104:107], v[136:139], a[136:139], v[104:107]\n\t" \
  "buffer_load_dwordx4 v242, %[ws], s71 offen offset:0 sc1 lds\n\t" \
  "v_mfma_f32_16x16x32_bf16 v[104:107], v[140:143], a[140:143], v[104:107]\n\t" \
  "ds_read_b128 v[24:27], v242 offset:51200\n\t" \
  "ds_read_b64 v[208:209], v231 offset:0\n\t" \
  "v_mfma_f32_16x16x32_bf16 v[120:123], v[156:159], a[140:143], v[120:123]\n\t" \
  "ds_read_b128 v[28:31], v242 offset:52224\n\t" \
  "v_perm_b32 v74, v227, v224, s79\n\t" \
  "v_mfma_f32_16x16x32_bf16 v[120:123], v[152:155], a[136:139], v[120:123]\n\t" \
  "ds_read_b64 v[210:211], v231 offset:4096\n\t" \
  "v_perm_b32 v78, v227, v224, s80\n\t" \
  "v_mfma_f32_16x16x32_bf16 v[120:123], v[148:151], a[132:135], v[120:123]\n\t" \
  "buffer_load_dwordx4 v242, %[ws], s71 offen offset:1024 sc1 lds\n\t" \
  "v_mfma_f32_16x16x32_bf16 v[120:123], v[144:147], a[128:131], v[120:123]\n\t" \
  "s_waitcnt lgkmcnt(6)\n\t" \
  "ds_read_b64 v[212:213], v231 offset:8192\n\t" \
  "v_accvgpr_read_b32 v224, a235\n\t" \
  "v_mfma_f32_16x16x32_bf16 v[108:111], v[128:131], a[144:147], v[108:111]\n\t" \
  "ds_read_b128 a[8:11], v242 offset:53248\n\t" \
  "ds_read_b64 v[214:215], v231 offset:12288\n\t" \
  "v_mfma_f32_16x16x32_bf16 v[108:111], v[132:135], a[148:151], v[108:111]\n\t" \
  "ds_read_b128 a[24:27], v242 offset:54272\n\t" \
  "ds_read_b64 v[216:217], v233 offset:0\n\t" \
  "v_mfma_f32_16x16x32_bf16 v[108:111], v[136:139], a[152:155], v[108:111]\n\t" \
  "ds_read_b128 a[40:43], v242 offset:55296\n\t" \
  "v_accvgpr_read_b32 v227, a239\n\t" \
  "v_mfma_f32_16x16x32_bf16 v[108:111], v[140:143], a[156:159], v[108:111]\n\t" \
  "buffer_load_dwordx4 v242, %[ws], s71 offen offset:2048 sc1 lds\n\t" \
  "v_mfma_f32_16x16x32_bf16 v[124:127], v[156:159], a[156:159], v[124:127]\n\t" \
  "ds_read_b64 v[218:219], v233 offset:4096\n\t" \
  "ds_read_b64 v[220:221], v233 offset:8192\n\t" \
  "v_mfma_f32_16x16x32_bf16 v[124:127], v[152:155], a[152:155], v[124:127]\n\t" \
  "ds_read_b128 a[56:59], v242 offset:56320\n\t" \
  "v_perm_b32 v75, v227, v224, s79\n\t" \
  "ds_read_b64 v[222:223], v233 offset:12288\n\t" \
  "buffer_load_dword v228, v235, %[ws], s68 offen sc1\n\t" \
  "v_mfma_f32_16x16x32_bf16 v[124:127], v[148:151], a[148:151], v[124:127]\n\t" \
  "v_perm_b32 v79, v227, v224, s80\n\t" \
  "v_accvgpr_read_b32 v224, a240\n\t" \
  "v_mfma_f32_16x16x32_bf16 v[124:127], v[144:147], a[144:147], v[124:127]\n\t" \
  "s_waitcnt lgkmcnt(11)\n\t" \
  "v_accvgpr_read_b32 v227, a244\n\t" \
  "v_perm_b32 v80, v227, v224, s79\n\t" \
  "v_mfma_f32_16x16x32_bf16 v[96:99], a[12:15], v[16:19], v[96:99]\n\t" \
  "buffer_load_dwordx4 v242, %[ws], s71 offen offset:3072 sc1 lds\n\t" \
  "v_mfma_f32_16x16x32_bf16 v[96:99], a[28:31], v[20:23], v[96:99]\n\t" \
  "ds_read_b128 v[128:131], v242 offset:57344\n\t" \
  "v_perm_b32 v84, v227, v224, s80\n\t" \
  "v_mfma_f32_16x16x32_bf16 v[96:99], a[44:47], v[24:27], v[96:99]\n\t" \
  "ds_read_b128 v[132:135], v242 offset:58368\n\t" \
  "v_accvgpr_read_b32 v224, a241\n\t" \
  "v_mfma_f32_16x16x32_bf16 v[96:99], a[60:63], v[28:31], v[96:99]\n\t" \
  "ds_read_b128 a[72:75], v242 offset:59392\n\t" \
  "s_waitcnt vmcnt(13)\n\t" \
  "v_cmp_ne_u32 vcc, %[tag], v246\n\t" \
  "s_and_b32 vcc_lo, vcc_lo, 0xff\n\t" \
  "s_or_b32 s81, s81, vcc_lo\n\t" \
  "v_mfma_f32_16x16x32_bf16 v[112:115], a[124:127], v[28:31], v[112:115]\n\t" \
  "ds_read_b128 a[88:91], v242 offset:60416\n\t" \
  "v_mfma_f32_16x16x32_bf16 v[112:115], a[108:111], v[24:27], v[112:115]\n\t" \
  "s_and_b32 s72, s81, 15\n\t" \
  "s_cselect_b32 s72, -1, 0\n\t" \
  "v_lshl_or_b32 v247, s72, 31, v242\n\t" \
  "s_add_u32 s72, s63, 0\n\t" \
  "v_mfma_f32_16x16x32_bf16 v[112:115], a[92:95], v[20:23], v[112:115]\n\t" \
  "buffer_load_dwordx4 a[128:131], v247, %[ws], s72 offen sc1\n\t" \
  "v_mfma_f32_16x16x32_bf16 v[112:115], a[76:79], v[16:19], v[112:115]\n\t" \
  "s_waitcnt lgkmcnt(5)\n\t" \
  "s_add_u32 s72, s64, 32768\n\t" \
  "v_accvgpr_read_b32 v227, a245\n\t" \
  "v_mfma_f32_16x16x32_bf16 v[100:103], a[12:15], a[8:11], v[100:103]\n\t" \
  "ds_read_b128 a[104:107], v242 offset:61440\n\t" \
  "v_perm_b32 v81, v227, v224, s79\n\t" \
  "v_mfma_f32_16x16x32_bf16 v[100:103], a[28:31], a[24:27], v[100:103]\n\t" \
  "buffer_load_dwordx4 a[140:143], v247, %[ws], s72 offen sc1\n\t" \
  "v_mfma_f32_16x16x32_bf16 v[100:103], a[44:47], a[40:43], v[100:103]\n\t" \
  "s_add_u32 s72, s63, 65536\n\t" \
  "ds_read_b128 a[120:123], v242 offset:62464\n\t" \
  "v_perm_b32 v85, v227, v224, s80\n\t" \
  "v_mfma_f32_16x16x32_bf16 v[100:103], a[60:63], a[56:59], v[100:103]\n\t" \
  "ds_read_b128 a[176:179], v242 offset:63488\n\t" \
  "v_mfma_f32_16x16x32_bf16 v[116:119], a[124:127], a[56:59], v[116:119]\n\t" \
  "ds_read_b128 a[180:183], v242 offset:64512\n\t" \
  "v_accvgpr_read_b32 v224, a242\n\t" \
  "v_mfma_f32_16x16x32_bf16 v[116:119], a[108:111], a[40:43], v[116:119]\n\t" \
  "buffer_load_dwordx4 a[152:155], v247, %[ws], s72 offen sc1\n\t" \
  "s_add_u32 s72, s64, 98304\n\t" \
  "v_mfma_f32_16x16x32_bf16 v[116:119], a[92:95], a[24:27], v[116:119]\n\t" \
  "s_waitcnt lgkmcnt(8)\n\t" \
  "v_lshlrev_b32 v0, 16, v208\n\t" \
  "v_and_b32 v1, 0xffff0000, v208\n\t" \
  "v_mfma_f32_16x16x32_bf16 v[116:119], a[76:79], a[8:11], v[116:119]\n\t" \
  "s_waitcnt lgkmcnt(4)\n\t" \
  "s_barrier\n\t" \
  "v_mfma_f32_16x16x32_bf16 v[104:107], a[12:15], v[128:131], v[104:107]\n\t" \
  "ds_read_b128 a[184:187], v230 offset:0\n\t" \
  "v_lshlrev_b32 v2, 16, v209\n\t" \
  "v_mfma_f32_16x16x32_bf16 v[104:107], a[28:31], v[132:135], v[104:107]\n\t" \
  "ds_read_b128 a[188:191], v230 offset:1024\n\t" \
  "v_and_b32 v3, 0xffff0000, v209\n\t" \
  "v_mfma_f32_16x16x32_bf16 v[104:107], a[44:47], a[72:75], v[104:107]\n\t" \
  "buffer_load_dwordx4 a[164:167], v247, %[ws], s72 offen sc1\n\t" \
  "v_mfma_f32_16x16x32_bf16 v[104:107], a[60:63], a[88:91], v[104:107]\n\t" \
  "s_and_b32 s72, s81, 240\n\t" \
  "s_cselect_b32 s72, -1, 0\n\t" \
  "v_lshl_or_b32 v247, s72, 31, v242\n\t" \
  "s_add_u32 s72, s63, 4096\n\t" \
  "v_mfma_f32_16x16x32_bf16 v[120:123], a[124:127], a[88:91], v[120:123]\n\t" \
  "ds_read_b128 v[136:139], v230 offset:2048\n\t" \
  "v_lshlrev_b32 v4, 16, v210\n\t" \
  "v_mfma_f32_16x16x32_bf16 v[120:123], a[108:111], a[72:75], v[120:123]\n\t" \
  "ds_read_b128 v[140:143], v230 offset:3072\n\t" \
  "v_and_b32 v5, 0xffff0000, v210\n\t" \
  "v_mfma_f32_16x16x32_bf16 v[120:123], a[92:95], v[132:135], v[120:123]\n\t" \
  "buffer_load_dwordx4 a[132:135], v247, %[ws], s72 offen sc1\n\t" \
  "s_add_u32 s72, s64, 36864\n\t" \
  "v_mfma_f32_16x16x32_bf16 v[120:123], a[76:79], v[128:131], v[120:123]\n\t" \
  "s_waitcnt lgkmcnt(4)\n\t" \
  "v_lshlrev_b32 v6, 16, v211\n\t" \
  "v_and_b32 v7, 0xffff0000, v211\n\t" \
  "v_mfma_f32_16x16x32_bf16 v[108:111], a[12:15], a[104:107], v[108:111]\n\t" \
  "ds_read_b128 v[128:131], v230 offset:4096\n\t" \
  "v_accvgpr_read_b32 v227, a246\n\t" \
  "v_mfma_f32_16x16x32_bf16 v[108:111], a[28:31], a[120:123], v[108:111]\n\t" \
  "ds_read_b128 v[132:135], v230 offset:5120\n\t" \
  "v_lshlrev_b32 v8, 16, v212\n\t" \
  "v_mfma_f32_16x16x32_bf16 v[108:111], a[44:47], a[176:179], v[108:111]\n\t" \
  "ds_read_b128 v[144:147], v230 offset:6144\n\t" \
  "v_perm_b32 v82, v227, v224, s79\n\t" \
  "v_mfma_f32_16x16x32_bf16 v[108:111], a[60:63], a[180:183], v[108:111]\n\t" \
  "buffer_load_dwordx4 a[144:147], v247, %[ws], s72 offen sc1\n\t" \
  "s_add_u32 s72, s63, 69632\n\t" \
  "v_mfma_f32_16x16x32_bf16 v[124:127], a[124:127], a[180:183], v[124:127]\n\t" \
  "ds_read_b128 v[148:151], v230 offset:7168\n\t" \
  "v_and_b32 v9, 0xffff0000, v212\n\t" \
  "v_mfma_f32_16x16x32_bf16 v[124:127], a[108:111], a[176:179], v[124:127]\n\t" \
  "v_perm_b32 v86, v227, v224, s80\n\t" \
  "v_lshlrev_b32 v10, 16, v213\n\t" \
  "v_mfma_f32_16x16x32_bf16 v[124:127], a[92:95], a[120:123], v[124:127]\n\t" \
  "v_accvgpr_read_b32 v224, a243\n\t" \
  "v_and_b32 v11, 0xffff0000, v213\n\t" \
  "v_mfma_f32_16x16x32_bf16 v[124:127], a[76:79], a[104:107], v[124:127]\n\t" \
  "s_waitcnt lgkmcnt(4)\n\t" \
  "buffer_load_dwordx4 a[156:159], v247, %[ws], s72 offen sc1\n\t" \
  "s_add_u32 s72, s64, 102400\n\t" \
  "v_mfma_f32_16x16x32_bf16 v[96:99], a[96:99], a[184:187], v[96:99]\n\t" \
  "ds_read_b128 v[152:155], v230 offset:8192\n\t" \
  "v_lshlrev_b32 v12, 16, v214\n\t" \
  "v_mfma_f32_16x16x32_bf16 v[96:99], a[112:115], a[188:191], v[96:99]\n\t" \
  "ds_read_b128 v[156:159], v230 offset:9216\n\t" \
  "v_accvgpr_read_b32 v227, a247\n\t" \
  "v_mfma_f32_16x16x32_bf16 v[96:99], a[100:103], v[136:139], v[96:99]\n\t" \
  "ds_read_b128 a[8:11], v230 offset:10240\n\t" \
  "v_and_b32 v13, 0xffff0000, v214\n\t" \
  "v_mfma_f32_16x16x32_bf16 v[96:99], a[116:119], v[140:143], v[96:99]\n\t" \
  "buffer_load_dwordx4 a[168:171], v247, %[ws], s72 offen sc1\n\t" \
  "v_mfma_f32_16x16x32_bf16 v[112:115], a[84:87], v[140:143], v[112:115]\n\t" \
  "ds_read_b128 a[24:27], v230 offset:11264\n\t" \
  "v_perm_b32 v83, v227, v224, s79\n\t" \
  "v_mfma_f32_16x16x32_bf16 v[112:115], a[80:83], v[136:139], v[112:115]\n\t" \
  "v_lshlrev_b32 v14, 16, v215\n\t" \
  "v_perm_b32 v87, v227, v224, s80\n\t" \
  "v_mfma_f32_16x16x32_bf16 v[112:115], a[68:71], a[188:191], v[112:115]\n\t" \
  "v_and_b32 v15, 0xffff0000, v215\n\t" \
  "v_accvgpr_read_b32 v224, a248\n\t" \
  "v_mfma_f32_16x16x32_bf16 v[112:115], a[64:67], a[184:187], v[112:115]\n\t" \
  "s_waitcnt lgkmcnt(4)\n\t" \
  "v_lshlrev_b32 v16, 16, v216\n\t" \
  "v_and_b32 v17, 0xffff0000, v216\n\t" \
  "v_mfma_f32_16x16x32_bf16 v[100:103], a[96:99], v[128:131], v[100:103]\n\t" \
  "ds_read_b128 a[40:43], v230 offset:12288\n\t" \
  "v_accvgpr_read_b32 v227, a252\n\t" \
  "v_mfma_f32_16x16x32_bf16 v[100:103], a[112:115], v[132:135], v[100:103]\n\t" \
  "ds_read_b128 a[56:59], v230 offset:13312\n\t" \
  "v_lshlrev_b32 v18, 16, v217\n\t" \
  "v_mfma_f32_16x16x32_bf16 v[100:103], a[100:103], v[144:147], v[100:103]\n\t" \
  "ds_read_b128 a[72:75], v230 offset:14336\n\t" \
  "v_perm_b32 v88, v227, v224, s79\n\t" \
  "v_mfma_f32_16x16x32_bf16 v[100:103], a[116:119], v[148:151], v[100:103]\n\t" \
  "ds_read_b128 a[88:91], v230 offset:15360\n\t" \
  "v_and_b32 v19, 0xffff0000, v217\n\t" \
  "s_waitcnt vmcnt(9)\n\t" \
  "v_cmp_ne_u32 vcc, %[tag], v228\n\t" \
  "s_and_b32 vcc_lo, vcc_lo, 0xf00\n\t" \
  "s_or_b32 s81, s81, vcc_lo\n\t" \
  "v_mfma_f32_16x16x32_bf16 v[116:119], a[84:87], v[148:151], v[116:119]\n\t" \
  "s_and_b32 s72, s81, 3840\n\t" \
  "s_cselect_b32 s72, -1, 0\n\t" \
  "v_lshl_or_b32 v247, s72, 31, v242\n\t" \
  "s_add_u32 s72, s63, 16384\n\t" \
  "v_mfma_f32_16x16x32_bf16 v[116:119], a[80:83], v[144:147], v[116:119]\n\t" \
  "buffer_load_dwordx4 a[136:139], v247, %[ws], s72 offen sc1\n\t" \
  "v_mfma_f32_16x16x32_bf16 v[116:119], a[68:71], v[132:135], v[116:119]\n\t" \
  "s_add_u32 s72, s64, 49152\n\t" \
  "v_perm_b32 v92, v227, v224, s80\n\t" \
  "v_lshlrev_b32 v20, 16, v218\n\t" \
  "v_mfma_f32_16x16x32_bf16 v[116:119], a[64:67], v[128:131], v[116:119]\n\t" \
  "s_waitcnt vmcnt(9) lgkmcnt(4)\n\t" \
  "s_barrier\n\t" \
  "v_mfma_f32_16x16x32_bf16 v[104:107], a[96:99], v[152:155], v[104:107]\n\t" \
  "buffer_load_dwordx4 a[148:151], v247, %[ws], s72 offen sc1\n\t" \
  "v_mfma_f32_16x16x32_bf16 v[104:107], a[112:115], v[156:159], v[104:107]\n\t" \
  "s_add_u32 s72, s63, 81920\n\t" \
  "ds_read_b128 v[128:131], v242 offset:16384\n\t" \
  "v_accvgpr_read_b32 v224, a249\n\t" \
  "v_mfma_f32_16x16x32_bf16 v[104:107], a[100:103], a[8:11], v[104:107]\n\t" \
  "ds_read_b128 v[132:135], v242 offset:17408\n\t" \
  "v_and_b32 v21, 0xffff0000, v218\n\t" \
  "v_mfma_f32_16x16x32_bf16 v[104:107], a[116:119], a[24:27], v[104:107]\n\t" \
  "buffer_load_dwordx4 a[160:163], v247, %[ws], s72 offen sc1\n\t" \
  "v_mfma_f32_16x16x32_bf16 v[120:123], a[84:87], a[24:27], v[120:123]\n\t" \
  "s_add_u32 s72, s64, 114688\n\t" \
  "ds_read_b128 a[12:15], v242 offset:18432\n\t" \
  "v_lshlrev_b32 v22, 16, v219\n\t" \
  "v_mfma_f32_16x16x32_bf16 v[120:123], a[80:83], a[8:11], v[120:123]\n\t" \
  "ds_read_b128 a[28:31], v242 offset:19456\n\t" \
  "v_accvgpr_read_b32 v227, a253\n\t" \
  "v_mfma_f32_16x16x32_bf16 v[120:123], a[68:71], v[156:159], v[120:123]\n\t" \
  "v_and_b32 v23, 0xffff0000, v219\n\t" \
  "v_perm_b32 v89, v227, v224, s79\n\t" \
  "v_mfma_f32_16x16x32_bf16 v[120:123], a[64:67], v[152:155], v[120:123]\n\t" \
  "s_waitcnt lgkmcnt(4)\n\t" \
  "buffer_load_dwordx4 a[172:175], v247, %[ws], s72 offen sc1\n\t" \
  "v_mfma_f32_16x16x32_bf16 v[108:111], a[96:99], a[40:43], v[108:111]\n\t" \
  "ds_read_b128 a[44:47], v242 offset:20480\n\t" \
  "v_lshlrev_b32 v24, 16, v220\n\t" \
  "v_mfma_f32_16x16x32_bf16 v[108:111], a[112:115], a[56:59], v[108:111]\n\t" \
  "ds_read_b128 a[60:63], v242 offset:21504\n\t" \
  "v_perm_b32 v93, v227, v224, s80\n\t" \
  "v_mfma_f32_16x16x32_bf16 v[108:111], a[100:103], a[72:75], v[108:111]\n\t" \
  "ds_read_b128 a[76:79], v242 offset:22528\n\t" \
  "v_and_b32 v25, 0xffff0000, v220\n\t" \
  "v_mfma_f32_16x16x32_bf16 v[108:111], a[116:119], a[88:91], v[108:111]\n\t" \
  "ds_read_b128 a[92:95], v242 offset:23552\n\t" \
  "v_accvgpr_read_b32 v224, a250\n\t" \
  "v_mfma_f32_16x16x32_bf16 v[124:127], a[84:87], a[88:91], v[124:127]\n\t" \
  "v_lshlrev_b32 v26, 16, v221\n\t" \
  "v_and_b32 v27, 0xffff0000, v221\n\t" \
  "v_mfma_f32_16x16x32_bf16 v[124:127], a[80:83], a[72:75], v[124:127]\n\t" \
  "v_accvgpr_read_b32 v227, a254\n\t" \
  "v_lshlrev_b32 v28, 16, v222\n\t" \
  "v_mfma_f32_16x16x32_bf16 v[124:127], a[68:71], a[56:59], v[124:127]\n\t" \
  "v_perm_b32 v90, v227, v224, s79\n\t" \
  "v_and_b32 v29, 0xffff0000, v222\n\t" \
  "v_mfma_f32_16x16x32_bf16 v[124:127], a[64:67], a[40:43], v[124:127]\n\t" \
  "s_waitcnt lgkmcnt(4)\n\t" \
  "v_perm_b32 v94, v227, v224, s80\n\t" \
  "v_lshlrev_b32 v30, 16, v223\n\t" \
  "v_mfma_f32_16x16x32_bf16 v[96:99], a[32:35], v[128:131], v[96:99]\n\t" \
  "ds_read_b128 a[104:107], v242 offset:24576\n\t" \
  "v_accvgpr_read_b32 v224, a251\n\t" \
  "v_mfma_f32_16x16x32_bf16 v[96:99], a[48:51], v[132:135], v[96:99]\n\t" \
  "ds_read_b128 a[108:111], v242 offset:25600\n\t" \
  "v_and_b32 v31, 0xffff0000, v223\n\t" \
  "v_mfma_f32_16x16x32_bf16 v[96:99], a[36:39], a[12:15], v[96:99]\n\t" \
  "buffer_load_dword v246, v235, %[ws], s68 offen sc1\n\t" \
  "v_mfma_f32_16x16x32_bf16 v[96:99], a[52:55], a[28:31], v[96:99]\n\t" \
  "ds_read_b128 a[120:123], v242 offset:26624\n\t" \
  "v_accvgpr_read_b32 v227, a255\n\t" \
  "v_mfma_f32_16x16x32_bf16 v[112:115], a[20:23], a[28:31], v[112:115]\n\t" \
  "ds_read_b128 a[124:127], v242 offset:27648\n\t" \
  "v_perm_b32 v91, v227, v224, s79\n\t" \
  "v_mfma_f32_16x16x32_bf16 v[112:115], a[4:7], a[12:15], v[112:115]\n\t" \
  "v_mfma_f32_16x16x32_bf16 v[112:115], a[16:19], v[132:135], v[112:115]\n\t" \
  "v_perm_b32 v95, v227, v224, s80\n\t" \
  "v_mfma_f32_16x16x32_bf16 v[112:115], a[0:3], v[128:131], v[112:115]\n\t" \
  "s_waitcnt lgkmcnt(4)\n\t" \
  "v_mfma_f32_16x16x32_bf16 v[100:103], a[32:35], a[44:47], v[100:103]\n\t" \
  "ds_read_b128 a[176:179], v242 offset:28672\n\t" \
  "v_mfma_f32_16x16x32_bf16 v[100:103], a[48:51], a[60:63], v[100:103]\n\t" \
  "ds_read_b128 a[180:183], v242 offset:29696\n\t" \
  "v_mfma_f32_16x16x32_bf16 v[100:103], a[36:39], a[76:79], v[100:103]\n\t" \
  "ds_read_b128 a[184:187], v242 offset:30720\n\t" \
  "v_mfma_f32_16x16x32_bf16 v[100:103], a[52:55], a[92:95], v[100:103]\n\t" \
  "ds_read_b128 a[188:191], v242 offset:31744\n\t" \
  "v_mfma_f32_16x16x32_bf16 v[116:119], a[20:23], a[92:95], v[116:119]\n\t" \
  "v_mfma_f32_16x16x32_bf16 v[116:119], a[4:7], a[76:79], v[116:119]\n\t" \
  "v_mfma_f32_16x16x32_bf16 v[116:119], a[16:19], a[60:63], v[116:119]\n\t" \
  "v_mfma_f32_16x16x32_bf16 v[116:119], a[0:3], a[44:47], v[116:119]\n\t" \
  "s_waitcnt lgkmcnt(4)\n\t" \
  "v_mfma_f32_16x16x32_bf16 v[104:107], a[32:35], a[104:107], v[104:107]\n\t" \
  "ds_read_b128 v[128:131], v237 offset:2560\n\t" \
  "v_mfma_f32_16x16x32_bf16 v[104:107], a[48:51], a[108:111], v[104:107]\n\t" \
  "ds_read_b128 v[132:135], v237 offset:2624\n\t" \
  "v_mfma_f32_16x16x32_bf16 v[104:107], a[36:39], a[120:123], v[104:107]\n\t" \
  "v_mfma_f32_16x16x32_bf16 v[104:107], a[52:55], a[124:127], v[104:107]\n\t" \
  "v_mfma_f32_16x16x32_bf16 v[120:123], a[20:23], a[124:127], v[120:123]\n\t" \
  "v_mfma_f32_16x16x32_bf16 v[120:123], a[4:7], a[120:123], v[120:123]\n\t" \
  "v_mfma_f32_16x16x32_bf16 v[120:123], a[16:19], a[108:111], v[120:123]\n\t" \
  "v_mfma_f32_16x16x32_bf16 v[120:123], a[0:3], a[104:107], v[120:123]\n\t" \
  "s_waitcnt lgkmcnt(2)\n\t" \
  "v_mfma_f32_16x16x32_bf16 v[108:111], a[32:35], a[176:179], v[108:111]\n\t" \
  "v_mfma_f32_16x16x32_bf16 v[108:111], a[48:51], a[180:183], v[108:111]\n\t" \
  "v_mfma_f32_16x16x32_bf16 v[108:111], a[36:39], a[184:187], v[108:111]\n\t" \
  "v_mfma_f32_16x16x32_bf16 v[108:111], a[52:55], a[188:191], v[108:111]\n\t" \
  "v_mfma_f32_16x16x32_bf16 v[124:127], a[20:23], a[188:191], v[124:127]\n\t" \
  "v_mfma_f32_16x16x32_bf16 v[124:127], a[4:7], a[184:187], v[124:127]\n\t" \
  "v_mfma_f32_16x16x32_bf16 v[124:127], a[16:19], a[180:183], v[124:127]\n\t" \
  "v_mfma_f32_16x16x32_bf16 v[124:127], a[0:3], a[176:179], v[124:127]\n\t" \
  STP("v_writelane_b32 %[stv], s94, 36\n\t") \
  STP("v_writelane_b32 %[stv], s92, 37\n\t") \
  STP("s_memrealtime s[94:95]\n\t") \
  STP("s_memtime s[92:93]\n\t") \
  "s_cmp_lg_u32 s81, 0\n\t" \
  "s_cbranch_scc1 .Lop_s99\n\t" \
  ".Lop_d2:\n\t" \
  "s_waitcnt vmcnt(9)\n\t" \
  "v_mfma_f32_16x16x32_bf16 v[96:99], a[196:199], a[128:131], v[96:99]\n\t" \
  "v_mfma_f32_16x16x32_bf16 v[100:103], a[196:199], a[140:143], v[100:103]\n\t" \
  "v_mfma_f32_16x16x32_bf16 v[104:107], a[196:199], a[152:155], v[104:107]\n\t" \
  "v_mfma_f32_16x16x32_bf16 v[108:111], a[196:199], a[164:167], v[108:111]\n\t" \
  "s_waitcnt vmcnt(5)\n\t" \
  "v_mfma_f32_16x16x32_bf16 v[112:115], a[194:197], a[130:133], v[112:115]\n\t" \
  "v_mfma_f32_16x16x32_bf16 v[116:119], a[194:197], a[142:145], v[116:119]\n\t" \
  "v_mfma_f32_16x16x32_bf16 v[120:123], a[194:197], a[154:157], v[120:123]\n\t" \
  "v_mfma_f32_16x16x32_bf16 v[124:127], a[194:197], a[166:169], v[124:127]\n\t" \
  "s_waitcnt vmcnt(1)\n\t" \
  "v_mfma_f32_16x16x32_bf16 v[96:99], a[194:197], a[134:137], v[96:99]\n\t" \
  "v_mfma_f32_16x16x32_bf16 v[100:103], a[194:197], a[146:149], v[100:103]\n\t" \
  "v_mfma_f32_16x16x32_bf16 v[104:107], a[194:197], a[158:161], v[104:107]\n\t" \
  "v_mfma_f32_16x16x32_bf16 v[108:111], a[194:197], a[170:173], v[108:111]\n\t" \
  "v_mfma_f32_16x16x32_bf16 v[112:115], a[192:195], a[136:139], v[112:115]\n\t" \
  "v_mfma_f32_16x16x32_bf16 v[116:119], a[192:195], a[148:151], v[116:119]\n\t" \
  "v_mfma_f32_16x16x32_bf16 v[120:123], a[192:195], a[160:163], v[120:123]\n\t" \
  "v_mfma_f32_16x16x32_bf16 v[124:127], a[192:195], a[172:175], v[124:127]\n\t" \
  "s_waitcnt vmcnt(0) lgkmcnt(0)\n\t" \
  STP("v_writelane_b32 %[stv], s94, 38\n\t") \
  STP("v_writelane_b32 %[stv], s92, 39\n\t") \
  "s_nop 7\n\t" \
  ".subsection 1\n\t" \
  ".Lop_s0:\n\t" \
  "s_mov_b32 s78, 0\n\t" \
  ".Lop_r0:\n\t" \
  "s_sleep 2\n\t" \
  "buffer_load_dword v246, v245, %[ws], %[fw2] offen sc1\n\t" \
  "s_waitcnt vmcnt(0)\n\t" \
  "v_cmp_ne_u32 vcc, %[tag], v246\n\t" \
  "s_and_b32 vcc_lo, vcc_lo, 0x6\n\t" \
  "s_cbranch_scc0 .Lop_k0\n\t" \
  "s_add_u32 s78, s78, 1\n\t" \
  "s_cmp_lt_u32 s78, %[cap]\n\t" \
  "s_cbranch_scc1 .Lop_r0\n\t" \
  "buffer_store_dword v243, off, %[ws], 0 offset:256 sc1\n\t" \
  "s_branch .Lop_k0\n\t" \
  ".Lop_s1:\n\t" \
  "s_mov_b32 s78, 0\n\t" \
  ".Lop_r1:\n\t" \
  "s_sleep 2\n\t" \
  "buffer_load_dword v246, v245, %[ws], %[fw2] offen sc1\n\t" \
  "s_waitcnt vmcnt(0)\n\t" \
  "v_cmp_ne_u32 vcc, %[tag], v246\n\t" \
  "s_and_b32 vcc_lo, vcc_lo, 0xe00\n\t" \
  "s_cbranch_scc0 .Lop_k1\n\t" \
  "s_add_u32 s78, s78, 1\n\t" \
  "s_cmp_lt_u32 s78, %[cap]\n\t" \
  "s_cbranch_scc1 .Lop_r1\n\t" \
  "buffer_store_dword v243, off, %[ws], 0 offset:256 sc1\n\t" \
  "s_branch .Lop_k1\n\t" \
  ".Lop_s2:\n\t" \
  "s_mov_b32 s78, 0\n\t" \
  ".Lop_r2:\n\t" \
  "s_sleep 2\n\t" \
  "buffer_load_dword v246, v245, %[ws], %[fw2] offen sc1\n\t" \
  "s_waitcnt vmcnt(0)\n\t" \
  "v_cmp_ne_u32 vcc, %[tag], v246\n\t" \
  "s_and_b32 vcc_lo, vcc_lo, 0xfe00\n\t" \
  "s_cbranch_scc0 .Lop_k2\n\t" \
  "s_add_u32 s78, s78, 1\n\t" \
  "s_cmp_lt_u32 s78, %[cap]\n\t" \
  "s_cbranch_scc1 .Lop_r2\n\t" \
  "buffer_store_dword v243, off, %[ws], 0 offset:256 sc1\n\t" \
  "s_branch .Lop_k2\n\t" \
  ".Lop_s3:\n\t" \
  "s_mov_b32 s78, 0\n\t" \
  ".Lop_r3:\n\t" \
  "s_sleep 2\n\t" \
  "buffer_load_dword v246, v245, %[ws], %[fw2] offen sc1\n\t" \
  "s_waitcnt vmcnt(0)\n\t" \
  "v_cmp_ne_u32 vcc, %[tag], v246\n\t" \
  "s_and_b32 vcc_lo, vcc_lo, 0xfe0000\n\t" \
  "s_cbranch_scc0 .Lop_k3\n\t" \
  "s_add_u32 s78, s78, 1\n\t" \
  "s_cmp_lt_u32 s78, %[cap]\n\t" \
  "s_cbranch_scc1 .Lop_r3\n\t" \
  "buffer_store_dword v243, off, %[ws], 0 offset:256 sc1\n\t" \
  "s_branch .Lop_k3\n\t" \
  ".Lop_s4:\n\t" \
  "s_mov_b32 s78, 0\n\t" \
  ".Lop_r4:\n\t" \
  "s_sleep 4\n\t" \
  "s_add_u32 s75, s69, 0\n\t" \
  "buffer_load_dwordx4 v[208:211], v170, %[ws], s75 offen sc1\n\t" \
  "s_add_u32 s75, s69, 32768\n\t" \
  "buffer_load_dwordx4 v[212:215], v170, %[ws], s75 offen sc1\n\t" \
  "s_add_u32 s75, s69, 65536\n\t" \
  "buffer_load_dwordx4 v[216:219], v170, %[ws], s75 offen sc1\n\t" \
  "s_add_u32 s75, s69, 98304\n\t" \
  "buffer_load_dwordx4 v[220:223], v170, %[ws], s75 offen sc1\n\t" \
  "s_waitcnt vmcnt(0)\n\t" \
  "v_xor_b32 v246, v243, v209\n\t" \
  "v_bitop3_b32 v246, v246, v211, v243 bitop3:0xf6\n\t" \
  "v_bitop3_b32 v246, v246, v213, v243 bitop3:0xf6\n\t" \
  "v_bitop3_b32 v246, v246, v215, v243 bitop3:0xf6\n\t" \
  "v_bitop3_b32 v246, v246, v217, v243 bitop3:0xf6\n\t" \
  "v_bitop3_b32 v246, v246, v219, v243 bitop3:0xf6\n\t" \
  "v_bitop3_b32 v246, v246, v221, v243 bitop3:0xf6\n\t" \
  "v_bitop3_b32 v246, v246, v223, v243 bitop3:0xf6\n\t" \
  "v_cmp_ne_u32 vcc, 0, v246\n\t" \
  "s_cbranch_vccz .Lop_k4\n\t" \
  "s_add_u32 s78, s78, 1\n\t" \
  "s_cmp_lt_u32 s78, %[cap]\n\t" \
  "s_cbranch_scc1 .Lop_r4\n\t" \
  "buffer_store_dword v243, off, %[ws], 0 offset:256 sc1\n\t" \
  "s_branch .Lop_k4\n\t" \
  ".Lop_rd:\n\t" \
  "s_waitcnt vmcnt(0) lgkmcnt(0)\n\t" \
  "s_barrier\n\t" \
  "s_mov_b32 %[pm], 2\n\t" \
  "s_branch .Lop_end\n\t" \
  ".Lop_s99:\n\t" \
  "s_waitcnt vmcnt(0)\n\t" \
  "v_cmp_ne_u32 vcc, %[tag], v246\n\t" \
  "s_and_b32 vcc_lo, vcc_lo, 0xfff\n\t" \
  "s_cbranch_scc0 .Lop_k99\n\t" \
  "s_mov_b32 s78, 0\n\t" \
  ".Lop_r99:\n\t" \
  "buffer_load_dword v246, v235, %[ws], s68 offen sc1\n\t" \
  "s_waitcnt vmcnt(0)\n\t" \
  "v_cmp_ne_u32 vcc, %[tag], v246\n\t" \
  "s_and_b32 vcc_lo, vcc_lo, 0xfff\n\t" \
  "s_cbranch_scc0 .Lop_k99\n\t" \
  "s_add_u32 s78, s78, 1\n\t" \
  "s_cmp_lt_u32 s78, %[cap]\n\t" \
  "s_cbranch_scc1 .Lop_r99\n\t" \
  "buffer_store_dword v243, off, %[ws], 0 offset:256 sc1\n\t" \
  "s_branch .Lop_k99\n\t" \
  ".Lop_k99:\n\t" \
  "s_and_b32 s72, s81, 15\n\t" \
  "s_cbranch_scc0 .Lop_m0\n\t" \
  "s_add_u32 s72, s63, 0\n\t" \
  "buffer_load_dwordx4 a[128:131], v242, %[ws], s72 offen sc1\n\t" \
  "s_add_u32 s72, s64, 32768\n\t" \
  "buffer_load_dwordx4 a[140:143], v242, %[ws], s72 offen sc1\n\t" \
  "s_add_u32 s72, s63, 65536\n\t" \
  "buffer_load_dwordx4 a[152:155], v242, %[ws], s72 offen sc1\n\t" \
  "s_add_u32 s72, s64, 98304\n\t" \
  "buffer_load_dwordx4 a[164:167], v242, %[ws], s72 offen sc1\n\t" \
  ".Lop_m0:\n\t" \
  "s_and_b32 s72, s81, 240\n\t" \
  "s_cbranch_scc0 .Lop_m1\n\t" \
  "s_add_u32 s72, s63, 4096\n\t" \
  "buffer_load_dwordx4 a[132:135], v242, %[ws], s72 offen sc1\n\t" \
  "s_add_u32 s72, s64, 36864\n\t" \
  "buffer_load_dwordx4 a[144:147], v242, %[ws], s72 offen sc1\n\t" \
  "s_add_u32 s72, s63, 69632\n\t" \
  "buffer_load_dwordx4 a[156:159], v242, %[ws], s72 offen sc1\n\t" \
  "s_add_u32 s72, s64, 102400\n\t" \
  "buffer_load_dwordx4 a[168:171], v242, %[ws], s72 offen sc1\n\t" \
  ".Lop_m1:\n\t" \
  "s_and_b32 s72, s81, 3840\n\t" \
  "s_cbranch_scc0 .Lop_m2\n\t" \
  "s_add_u32 s72, s63, 16384\n\t" \
  "buffer_load_dwordx4 a[136:139], v242, %[ws], s72 offen sc1\n\t" \
  "s_add_u32 s72, s64, 49152\n\t" \
  "buffer_load_dwordx4 a[148:151], v242, %[ws], s72 offen sc1\n\t" \
  "s_add_u32 s72, s63, 81920\n\t" \
  "buffer_load_dwordx4 a[160:163], v242, %[ws], s72 offen sc1\n\t" \
  "s_add_u32 s72, s64, 114688\n\t" \
  "buffer_load_dwordx4 a[172:175], v242, %[ws], s72 offen sc1\n\t" \
  ".Lop_m2:\n\t" \
  "s_waitcnt vmcnt(0)\n\t" \
  "s_branch .Lop_d2\n\t" \
  ".Lfz_z:\n\t" \
  ".fill 128, 4, 0xBF800000\n\t" \
  ".subsection 0\n\t" \
  ".Lop_end:\n\t" \
  ""
#define OP_CLOB "v208", "v209", "v210", "v211", "v212", "v214", "v215", "v216", "v217", "v218", "v219", "v220", "v221", "v222", "v223", "v228", "v229", "v230", "v231", "v232", "v233", "v235", "v236", "v238", "v239", "v246", "v248", "v249", "v250", "v251", "s60", "s61", "s62", "s63", "s64", "s65", "s66", "s67", "s68", "s69", "s70", "s71", "s72", "s73", "s74", "s75", "s76", "s77", "s78", "s79", "s80", "s81", "s82", "s83", "s84", "s85", "s92", "s93", "s94", "s95", "vcc", "scc", "memory"
#define ST_ASM "s_nop 1\n\t" "v_mfma_f32_16x16x32_bf16 v[80:83], a[208:211], v[160:163], 0\n\t" "v_mfma_f32_16x16x32_bf16 v[80:83], a[212:215], v[164:167], v[80:83]\n\t" "v_mfma_f32_16x16x32_bf16 v[80:83], a[216:219], v[168:171], v[80:83]\n\t" "v_mfma_f32_16x16x32_bf16 v[80:83], a[220:223], v[172:175], v[80:83]\n\t" "v_dot2_f32_bf16 v64, v160, v160, 0\n\t" "v_dot2_f32_bf16 v65, v164, v164, 0\n\t" "v_dot2_f32_bf16 v66, v168, v168, 0\n\t" "v_dot2_f32_bf16 v67, v172, v172, 0\n\t" "v_dot2_f32_bf16 v64, v161, v161, v64\n\t" "v_dot2_f32_bf16 v65, v165, v165, v65\n\t" "v_dot2_f32_bf16 v66, v169, v169, v66\n\t" "v_dot2_f32_bf16 v67, v173, v173, v67\n\t" "v_dot2_f32_bf16 v64, v162, v162, v64\n\t" "v_dot2_f32_bf16 v65, v166, v166, v65\n\t" "v_dot2_f32_bf16 v66, v170, v170, v66\n\t" "v_dot2_f32_bf16 v67, v174, v174, v67\n\t" "v_dot2_f32_bf16 v64, v163, v163, v64\n\t" "v_dot2_f32_bf16 v65, v167, v167, v65\n\t" "v_dot2_f32_bf16 v66, v171, v171, v66\n\t" "v_dot2_f32_bf16 v67, v175, v175, v67\n\t" "s_nop 2\n\t"
static_assert(W_MODB == 2228224);
constexpr int GSLOT = 49152;
constexpr unsigned KRS = 256;
struct KVP { srd_t srdK, srdV; unsigned vkb[4], vvb[2], vksrc[4], vvsrc[2], sofk; };
struct H4 {
  srd_t srdW;
  unsigned voffX, voffW[4];
  unsigned xb1, wb[2][8];
  unsigned mX, mW, soX, rot;
  unsigned pfd, fo, fl, fvo;
  u32x32 q[4];
};
#define W4(a) "+v"(a[0]), "+v"(a[1]), "+v"(a[2]), "+v"(a[3])
#define W8(a) W4(a), "+v"(a[4]), "+v"(a[5]), "+v"(a[6]), "+v"(a[7])
constexpr unsigned kXT = 65536, kWT = 64 * QD * 2;
DI void w_dma4(unsigned m0, srd_t srd, const unsigned (&vo)[4], unsigned so) {
  asm volatile("s_mov_b32 m0, %0\n\ts_nop 0\n\tbuffer_load_dwordx4 %1, %5, %6 offen lds\n\tbuffer_load_dwordx4 %2, %5, %6 offen offset:1024 lds\n\t"
               "buffer_load_dwordx4 %3, %5, %6 offen offset:2048 lds\n\tbuffer_load_dwordx4 %4, %5, %6 offen offset:3072 lds"
               :: "s"(m0), "v"(vo[0]), "v"(vo[1]), "v"(vo[2]), "v"(vo[3]), "s"(srd), "s"(so) : "memory");
}
DI void w_voffs(unsigned (&vo)[4], int ldw, unsigned wid, unsigned lane, unsigned k0) {
#pragma unroll
  for (int j = 0; j < 4; ++j) {
    const unsigned k = (4 * wid + j) * 4 + (lane >> 4), c16 = lane & 15, sw = (k & 3) | (((k >> 3) & 1) << 2);
    vo[j] = (k ^ (32 * k0)) * ldw * 2 + ((((c16 >> 1) ^ sw) << 1) + (c16 & 1)) * 16 - 1024 * j;
  }
}
DI void h4_init_addr(H4& c, unsigned ldsbase, unsigned wid, unsigned lane, unsigned k0) {
  const unsigned r16 = lane & 15, g = lane >> 4, q = (lane >> 2) & 3, p = lane & 3;
  c.xb1 = ldsbase + GSLOT + wid * 8192 + k0 * 4096 + g * 1024 + r16 * 16;
#pragma unroll
  for (int sl = 0; sl < 2; ++sl)
#pragma unroll
    for (int nb = 0; nb < 8; ++nb) { c.wb[sl][nb] = ldsbase + 2 * sl * GSLOT + 32768 + (8 * g + q) * 256 + ((nb ^ (q | ((g & 1) << 2))) << 5) + p * 8; asm volatile("" : "+v"(c.wb[sl][nb])); }
}
#define FSTP(x)
#define FSTS(x)
#define FQ_STV
#define FQ_STC
#define FQ_ASM \
  "s_mov_b32 s98, 0xffff0000\n\t" \
  ".p2align 3\n\t" \
  "ds_read_b128 v[128:131], v208 offset:0\n\t" \
  "ds_read_b128 v[132:135], v208 offset:16\n\t" \
  "ds_read_b128 v[136:139], v208 offset:256\n\t" \
  "ds_read_b128 v[140:143], v208 offset:272\n\t" \
  "ds_read_b128 v[32:35], v204 offset:49152\n\t" \
  "ds_read_b128 v[36:39], v204 offset:51200\n\t" \
  "ds_read_b128 v[40:43], v204 offset:53248\n\t" \
  "ds_read_b128 v[44:47], v204 offset:55296\n\t" \
  "ds_read_b128 v[144:147], v208 offset:128\n\t" \
  "ds_read_b128 v[148:151], v208 offset:144\n\t" \
  "ds_read_b128 v[152:155], v208 offset:384\n\t" \
  "ds_read_b128 v[156:159], v208 offset:400\n\t" \
  "ds_read_b128 v[48:51], v205 offset:49152\n\t" \
  "ds_read_b128 v[52:55], v205 offset:51200\n\t" \
  "ds_read_b128 v[56:59], v205 offset:53248\n\t" \
  "ds_read_b128 v[60:63], v205 offset:55296\n\t" \
  "v_lshlrev_b32 v176, 16, v0\n\t" \
  "v_and_b32 v177, s98, v0\n\t" \
  "v_lshlrev_b32 v178, 16, v1\n\t" \
  "v_and_b32 v179, s98, v1\n\t" \
  "v_lshlrev_b32 v180, 16, v2\n\t" \
  "v_and_b32 v181, s98, v2\n\t" \
  "v_lshlrev_b32 v182, 16, v3\n\t" \
  "v_and_b32 v183, s98, v3\n\t" \
  "v_pk_fma_f32 v[176:177], v[176:177], v[160:161], v[162:163] op_sel_hi:[1,0,0]\n\t" \
  "v_pk_fma_f32 v[178:179], v[178:179], v[160:161], v[162:163] op_sel_hi:[1,0,0]\n\t" \
  "v_pk_fma_f32 v[180:181], v[180:181], v[160:161], v[162:163] op_sel_hi:[1,0,0]\n\t" \
  "v_pk_fma_f32 v[182:183], v[182:183], v[160:161], v[162:163] op_sel_hi:[1,0,0]\n\t" \
  "s_waitcnt lgkmcnt(12)\n\t" \
  "s_nop 0\n\t" \
  "s_xor_b32 s97, %[pso], 0\n\t" \
  ".p2align 3\n\t" \
  "v_pk_fma_f32 v[176:177], v[176:177], v[128:129], v[136:137]\n\t" \
  "v_pk_fma_f32 v[178:179], v[178:179], v[130:131], v[138:139]\n\t" \
  "v_pk_fma_f32 v[180:181], v[180:181], v[132:133], v[140:141]\n\t" \
  "v_pk_fma_f32 v[182:183], v[182:183], v[134:135], v[142:143]\n\t" \
  "v_cvt_pk_bf16_f32 v0, v176, v177\n\t" \
  "v_cvt_pk_bf16_f32 v1, v178, v179\n\t" \
  "v_cvt_pk_bf16_f32 v2, v180, v181\n\t" \
  "v_cvt_pk_bf16_f32 v3, v182, v183\n\t" \
  "buffer_store_dwordx4 v[0:3], v214, %[ws], s97 offen sc1\n\t" \
  "v_lshlrev_b32 v176, 16, v4\n\t" \
  "v_and_b32 v177, s98, v4\n\t" \
  "v_lshlrev_b32 v178, 16, v5\n\t" \
  "v_and_b32 v179, s98, v5\n\t" \
  "v_lshlrev_b32 v180, 16, v6\n\t" \
  "v_and_b32 v181, s98, v6\n\t" \
  "v_lshlrev_b32 v182, 16, v7\n\t" \
  "v_and_b32 v183, s98, v7\n\t" \
  "v_pk_fma_f32 v[176:177], v[176:177], v[164:165], v[166:167] op_sel_hi:[1,0,0]\n\t" \
  "v_pk_fma_f32 v[178:179], v[178:179], v[164:165], v[166:167] op_sel_hi:[1,0,0]\n\t" \
  "v_pk_fma_f32 v[180:181], v[180:181], v[164:165], v[166:167] op_sel_hi:[1,0,0]\n\t" \
  "v_pk_fma_f32 v[182:183], v[182:183], v[164:165], v[166:167] op_sel_hi:[1,0,0]\n\t" \
  "s_xor_b32 s97, %[pso], 256\n\t" \
  ".p2align 3\n\t" \
  "v_pk_fma_f32 v[176:177], v[176:177], v[128:129], v[136:137]\n\t" \
  "v_pk_fma_f32 v[178:179], v[178:179], v[130:131], v[138:139]\n\t" \
  "v_pk_fma_f32 v[180:181], v[180:181], v[132:133], v[140:141]\n\t" \
  "v_pk_fma_f32 v[182:183], v[182:183], v[134:135], v[142:143]\n\t" \
  "v_cvt_pk_bf16_f32 v4, v176, v177\n\t" \
  "v_cvt_pk_bf16_f32 v5, v178, v179\n\t" \
  "v_cvt_pk_bf16_f32 v6, v180, v181\n\t" \
  "v_cvt_pk_bf16_f32 v7, v182, v183\n\t" \
  "buffer_store_dwordx4 v[4:7], v214, %[ws], s97 offen sc1\n\t" \
  "v_lshlrev_b32 v176, 16, v8\n\t" \
  "v_and_b32 v177, s98, v8\n\t" \
  "v_lshlrev_b32 v178, 16, v9\n\t" \
  "v_and_b32 v179, s98, v9\n\t" \
  "v_lshlrev_b32 v180, 16, v10\n\t" \
  "v_and_b32 v181, s98, v10\n\t" \
  "v_lshlrev_b32 v182, 16, v11\n\t" \
  "v_and_b32 v183, s98, v11\n\t" \
  "v_pk_fma_f32 v[176:177], v[176:177], v[168:169], v[170:171] op_sel_hi:[1,0,0]\n\t" \
  "v_pk_fma_f32 v[178:179], v[178:179], v[168:169], v[170:171] op_sel_hi:[1,0,0]\n\t" \
  "v_pk_fma_f32 v[180:181], v[180:181], v[168:169], v[170:171] op_sel_hi:[1,0,0]\n\t" \
  "v_pk_fma_f32 v[182:183], v[182:183], v[168:169], v[170:171] op_sel_hi:[1,0,0]\n\t" \
  "s_xor_b32 s97, %[pso], 512\n\t" \
  ".p2align 3\n\t" \
  "v_pk_fma_f32 v[176:177], v[176:177], v[128:129], v[136:137]\n\t" \
  "v_pk_fma_f32 v[178:179], v[178:179], v[130:131], v[138:139]\n\t" \
  "v_pk_fma_f32 v[180:181], v[180:181], v[132:133], v[140:141]\n\t" \
  "v_pk_fma_f32 v[182:183], v[182:183], v[134:135], v[142:143]\n\t" \
  "v_cvt_pk_bf16_f32 v8, v176, v177\n\t" \
  "v_cvt_pk_bf16_f32 v9, v178, v179\n\t" \
  "v_cvt_pk_bf16_f32 v10, v180, v181\n\t" \
  "v_cvt_pk_bf16_f32 v11, v182, v183\n\t" \
  "buffer_store_dwordx4 v[8:11], v214, %[ws], s97 offen sc1\n\t" \
  "v_lshlrev_b32 v176, 16, v12\n\t" \
  "v_and_b32 v177, s98, v12\n\t" \
  "v_lshlrev_b32 v178, 16, v13\n\t" \
  "v_and_b32 v179, s98, v13\n\t" \
  "v_lshlrev_b32 v180, 16, v14\n\t" \
  "v_and_b32 v181, s98, v14\n\t" \
  "v_lshlrev_b32 v182, 16, v15\n\t" \
  "v_and_b32 v183, s98, v15\n\t" \
  "v_pk_fma_f32 v[176:177], v[176:177], v[172:173], v[174:175] op_sel_hi:[1,0,0]\n\t" \
  "v_pk_fma_f32 v[178:179], v[178:179], v[172:173], v[174:175] op_sel_hi:[1,0,0]\n\t" \
  "v_pk_fma_f32 v[180:181], v[180:181], v[172:173], v[174:175] op_sel_hi:[1,0,0]\n\t" \
  "v_pk_fma_f32 v[182:183], v[182:183], v[172:173], v[174:175] op_sel_hi:[1,0,0]\n\t" \
  "s_xor_b32 s97, %[pso], 768\n\t" \
  ".p2align 3\n\t" \
  "v_pk_fma_f32 v[176:177], v[176:177], v[128:129], v[136:137]\n\t" \
  "v_pk_fma_f32 v[178:179], v[178:179], v[130:131], v[138:139]\n\t" \
  "v_pk_fma_f32 v[180:181], v[180:181], v[132:133], v[140:141]\n\t" \
  "v_pk_fma_f32 v[182:183], v[182:183], v[134:135], v[142:143]\n\t" \
  "v_cvt_pk_bf16_f32 v12, v176, v177\n\t" \
  "v_cvt_pk_bf16_f32 v13, v178, v179\n\t" \
  "v_cvt_pk_bf16_f32 v14, v180, v181\n\t" \
  "v_cvt_pk_bf16_f32 v15, v182, v183\n\t" \
  "buffer_store_dwordx4 v[12:15], v214, %[ws], s97 offen sc1\n\t" \
  "ds_read_b128 v[128:131], v208 offset:512\n\t" \
  "ds_read_b128 v[132:135], v208 offset:528\n\t" \
  "ds_read_b128 v[136:139], v208 offset:768\n\t" \
  "ds_read_b128 v[140:143], v208 offset:784\n\t" \
  FSTP("s_memrealtime s[94:95]\n\t") \
  FSTP("s_memtime s[92:93]\n\t") \
  "v_lshlrev_b32 v176, 16, v16\n\t" \
  "v_and_b32 v177, s98, v16\n\t" \
  "v_lshlrev_b32 v178, 16, v17\n\t" \
  "v_and_b32 v179, s98, v17\n\t" \
  "v_lshlrev_b32 v180, 16, v18\n\t" \
  "v_and_b32 v181, s98, v18\n\t" \
  "v_lshlrev_b32 v182, 16, v19\n\t" \
  "v_and_b32 v183, s98, v19\n\t" \
  "v_pk_fma_f32 v[176:177], v[176:177], v[160:161], v[162:163] op_sel_hi:[1,0,0]\n\t" \
  "v_pk_fma_f32 v[178:179], v[178:179], v[160:161], v[162:163] op_sel_hi:[1,0,0]\n\t" \
  "v_pk_fma_f32 v[180:181], v[180:181], v[160:161], v[162:163] op_sel_hi:[1,0,0]\n\t" \
  "v_pk_fma_f32 v[182:183], v[182:183], v[160:161], v[162:163] op_sel_hi:[1,0,0]\n\t" \
  "s_waitcnt lgkmcnt(8)\n\t" \
  "s_nop 0\n\t" \
  "v_pk_fma_f32 v[176:177], v[176:177], v[144:145], v[152:153]\n\t" \
  "v_pk_fma_f32 v[178:179], v[178:179], v[146:147], v[154:155]\n\t" \
  "v_pk_fma_f32 v[180:181], v[180:181], v[148:149], v[156:157]\n\t" \
  "v_pk_fma_f32 v[182:183], v[182:183], v[150:151], v[158:159]\n\t" \
  "v_cvt_pk_bf16_f32 v16, v176, v177\n\t" \
  "v_cvt_pk_bf16_f32 v17, v178, v179\n\t" \
  "v_cvt_pk_bf16_f32 v18, v180, v181\n\t" \
  "v_cvt_pk_bf16_f32 v19, v182, v183\n\t" \
  "s_add_u32 m0, %[mx], 98304\n\t" \
  "s_xor_b32 s96, %[c17], 0\n\t" \
  ".p2align 3\n\t" \
  "s_add_u32 s96, s96, %[sl2]\n\t" \
  "buffer_load_dwordx4 v212, %[sx], s96 offen lds\n\t" \
  ".p2align 3\n\t" \
  "v_lshlrev_b32 v176, 16, v20\n\t" \
  "v_and_b32 v177, s98, v20\n\t" \
  "v_lshlrev_b32 v178, 16, v21\n\t" \
  "v_and_b32 v179, s98, v21\n\t" \
  "v_lshlrev_b32 v180, 16, v22\n\t" \
  "v_and_b32 v181, s98, v22\n\t" \
  "v_lshlrev_b32 v182, 16, v23\n\t" \
  "v_and_b32 v183, s98, v23\n\t" \
  "v_pk_fma_f32 v[176:177], v[176:177], v[164:165], v[166:167] op_sel_hi:[1,0,0]\n\t" \
  "v_pk_fma_f32 v[178:179], v[178:179], v[164:165], v[166:167] op_sel_hi:[1,0,0]\n\t" \
  "v_pk_fma_f32 v[180:181], v[180:181], v[164:165], v[166:167] op_sel_hi:[1,0,0]\n\t" \
  "v_pk_fma_f32 v[182:183], v[182:183], v[164:165], v[166:167] op_sel_hi:[1,0,0]\n\t" \
  "v_pk_fma_f32 v[176:177], v[176:177], v[144:145], v[152:153]\n\t" \
  "v_pk_fma_f32 v[178:179], v[178:179], v[146:147], v[154:155]\n\t" \
  "v_pk_fma_f32 v[180:181], v[180:181], v[148:149], v[156:157]\n\t" \
  "v_pk_fma_f32 v[182:183], v[182:183], v[150:151], v[158:159]\n\t" \
  "v_cvt_pk_bf16_f32 v20, v176, v177\n\t" \
  "v_cvt_pk_bf16_f32 v21, v178, v179\n\t" \
  "v_cvt_pk_bf16_f32 v22, v180, v181\n\t" \
  "v_cvt_pk_bf16_f32 v23, v182, v183\n\t" \
  "buffer_load_dwordx4 v213, %[sx], s96 offen offset:1024 lds\n\t" \
  ".p2align 3\n\t" \
  "v_lshlrev_b32 v176, 16, v24\n\t" \
  "v_and_b32 v177, s98, v24\n\t" \
  "v_lshlrev_b32 v178, 16, v25\n\t" \
  "v_and_b32 v179, s98, v25\n\t" \
  "v_lshlrev_b32 v180, 16, v26\n\t" \
  "v_and_b32 v181, s98, v26\n\t" \
  "v_lshlrev_b32 v182, 16, v27\n\t" \
  "v_and_b32 v183, s98, v27\n\t" \
  "v_pk_fma_f32 v[176:177], v[176:177], v[168:169], v[170:171] op_sel_hi:[1,0,0]\n\t" \
  "v_pk_fma_f32 v[178:179], v[178:179], v[168:169], v[170:171] op_sel_hi:[1,0,0]\n\t" \
  "v_pk_fma_f32 v[180:181], v[180:181], v[168:169], v[170:171] op_sel_hi:[1,0,0]\n\t" \
  "v_pk_fma_f32 v[182:183], v[182:183], v[168:169], v[170:171] op_sel_hi:[1,0,0]\n\t" \
  "v_pk_fma_f32 v[176:177], v[176:177], v[144:145], v[152:153]\n\t" \
  "v_pk_fma_f32 v[178:179], v[178:179], v[146:147], v[154:155]\n\t" \
  "v_pk_fma_f32 v[180:181], v[180:181], v[148:149], v[156:157]\n\t" \
  "v_pk_fma_f32 v[182:183], v[182:183], v[150:151], v[158:159]\n\t" \
  "v_cvt_pk_bf16_f32 v24, v176, v177\n\t" \
  "v_cvt_pk_bf16_f32 v25, v178, v179\n\t" \
  "v_cvt_pk_bf16_f32 v26, v180, v181\n\t" \
  "v_cvt_pk_bf16_f32 v27, v182, v183\n\t" \
  "s_add_u32 m0, %[mx], 100352\n\t" \
  "s_xor_b32 s96, %[c17], 131072\n\t" \
  ".p2align 3\n\t" \
  "s_add_u32 s96, s96, %[sl2]\n\t" \
  "buffer_load_dwordx4 v212, %[sx], s96 offen lds\n\t" \
  ".p2align 3\n\t" \
  "v_lshlrev_b32 v176, 16, v28\n\t" \
  "v_and_b32 v177, s98, v28\n\t" \
  "v_lshlrev_b32 v178, 16, v29\n\t" \
  "v_and_b32 v179, s98, v29\n\t" \
  "v_lshlrev_b32 v180, 16, v30\n\t" \
  "v_and_b32 v181, s98, v30\n\t" \
  "v_lshlrev_b32 v182, 16, v31\n\t" \
  "v_and_b32 v183, s98, v31\n\t" \
  "v_pk_fma_f32 v[176:177], v[176:177], v[172:173], v[174:175] op_sel_hi:[1,0,0]\n\t" \
  "v_pk_fma_f32 v[178:179], v[178:179], v[172:173], v[174:175] op_sel_hi:[1,0,0]\n\t" \
  "v_pk_fma_f32 v[180:181], v[180:181], v[172:173], v[174:175] op_sel_hi:[1,0,0]\n\t" \
  "v_pk_fma_f32 v[182:183], v[182:183], v[172:173], v[174:175] op_sel_hi:[1,0,0]\n\t" \
  "v_pk_fma_f32 v[176:177], v[176:177], v[144:145], v[152:153]\n\t" \
  "v_pk_fma_f32 v[178:179], v[178:179], v[146:147], v[154:155]\n\t" \
  "v_pk_fma_f32 v[180:181], v[180:181], v[148:149], v[156:157]\n\t" \
  "v_pk_fma_f32 v[182:183], v[182:183], v[150:151], v[158:159]\n\t" \
  "v_cvt_pk_bf16_f32 v28, v176, v177\n\t" \
  "v_cvt_pk_bf16_f32 v29, v178, v179\n\t" \
  "v_cvt_pk_bf16_f32 v30, v180, v181\n\t" \
  "v_cvt_pk_bf16_f32 v31, v182, v183\n\t" \
  "buffer_load_dwordx4 v213, %[sx], s96 offen offset:1024 lds\n\t" \
  ".p2align 3\n\t" \
  "ds_read_b128 v[144:147], v208 offset:640\n\t" \
  "ds_read_b128 v[148:151], v208 offset:656\n\t" \
  "ds_read_b128 v[152:155], v208 offset:896\n\t" \
  "ds_read_b128 v[156:159], v208 offset:912\n\t" \
  FSTP("v_writelane_b32 %[stv], s94, 28\n\t") \
  FSTP("v_writelane_b32 %[stv], s92, 29\n\t") \
  FSTP("s_memrealtime s[94:95]\n\t") \
  FSTP("s_memtime s[92:93]\n\t") \
  "v_lshlrev_b32 v176, 16, v32\n\t" \
  "v_and_b32 v177, s98, v32\n\t" \
  "v_lshlrev_b32 v178, 16, v33\n\t" \
  "v_and_b32 v179, s98, v33\n\t" \
  "v_lshlrev_b32 v180, 16, v34\n\t" \
  "v_and_b32 v181, s98, v34\n\t" \
  "v_lshlrev_b32 v182, 16, v35\n\t" \
  "v_and_b32 v183, s98, v35\n\t" \
  "v_pk_fma_f32 v[176:177], v[176:177], v[160:161], v[162:163] op_sel_hi:[1,0,0]\n\t" \
  "v_pk_fma_f32 v[178:179], v[178:179], v[160:161], v[162:163] op_sel_hi:[1,0,0]\n\t" \
  "v_pk_fma_f32 v[180:181], v[180:181], v[160:161], v[162:163] op_sel_hi:[1,0,0]\n\t" \
  "v_pk_fma_f32 v[182:183], v[182:183], v[160:161], v[162:163] op_sel_hi:[1,0,0]\n\t" \
  "s_waitcnt lgkmcnt(4)\n\t" \
  "s_nop 0\n\t" \
  "v_pk_fma_f32 v[176:177], v[176:177], v[128:129], v[136:137]\n\t" \
  "v_pk_fma_f32 v[178:179], v[178:179], v[130:131], v[138:139]\n\t" \
  "v_pk_fma_f32 v[180:181], v[180:181], v[132:133], v[140:141]\n\t" \
  "v_pk_fma_f32 v[182:183], v[182:183], v[134:135], v[142:143]\n\t" \
  "v_cvt_pk_bf16_f32 v32, v176, v177\n\t" \
  "v_cvt_pk_bf16_f32 v33, v178, v179\n\t" \
  "v_cvt_pk_bf16_f32 v34, v180, v181\n\t" \
  "v_cvt_pk_bf16_f32 v35, v182, v183\n\t" \
  "s_add_u32 m0, %[mx], 102400\n\t" \
  "s_xor_b32 s96, %[c17], 262144\n\t" \
  ".p2align 3\n\t" \
  "s_add_u32 s96, s96, %[sl2]\n\t" \
  "buffer_load_dwordx4 v212, %[sx], s96 offen lds\n\t" \
  ".p2align 3\n\t" \
  "v_lshlrev_b32 v176, 16, v36\n\t" \
  "v_and_b32 v177, s98, v36\n\t" \
  "v_lshlrev_b32 v178, 16, v37\n\t" \
  "v_and_b32 v179, s98, v37\n\t" \
  "v_lshlrev_b32 v180, 16, v38\n\t" \
  "v_and_b32 v181, s98, v38\n\t" \
  "v_lshlrev_b32 v182, 16, v39\n\t" \
  "v_and_b32 v183, s98, v39\n\t" \
  "v_pk_fma_f32 v[176:177], v[176:177], v[164:165], v[166:167] op_sel_hi:[1,0,0]\n\t" \
  "v_pk_fma_f32 v[178:179], v[178:179], v[164:165], v[166:167] op_sel_hi:[1,0,0]\n\t" \
  "v_pk_fma_f32 v[180:181], v[180:181], v[164:165], v[166:167] op_sel_hi:[1,0,0]\n\t" \
  "v_pk_fma_f32 v[182:183], v[182:183], v[164:165], v[166:167] op_sel_hi:[1,0,0]\n\t" \
  "v_pk_fma_f32 v[176:177], v[176:177], v[128:129], v[136:137]\n\t" \
  "v_pk_fma_f32 v[178:179], v[178:179], v[130:131], v[138:139]\n\t" \
  "v_pk_fma_f32 v[180:181], v[180:181], v[132:133], v[140:141]\n\t" \
  "v_pk_fma_f32 v[182:183], v[182:183], v[134:135], v[142:143]\n\t" \
  "v_cvt_pk_bf16_f32 v36, v176, v177\n\t" \
  "v_cvt_pk_bf16_f32 v37, v178, v179\n\t" \
  "v_cvt_pk_bf16_f32 v38, v180, v181\n\t" \
  "v_cvt_pk_bf16_f32 v39, v182, v183\n\t" \
  "buffer_load_dwordx4 v213, %[sx], s96 offen offset:1024 lds\n\t" \
  ".p2align 3\n\t" \
  "v_lshlrev_b32 v176, 16, v40\n\t" \
  "v_and_b32 v177, s98, v40\n\t" \
  "v_lshlrev_b32 v178, 16, v41\n\t" \
  "v_and_b32 v179, s98, v41\n\t" \
  "v_lshlrev_b32 v180, 16, v42\n\t" \
  "v_and_b32 v181, s98, v42\n\t" \
  "v_lshlrev_b32 v182, 16, v43\n\t" \
  "v_and_b32 v183, s98, v43\n\t" \
  "v_pk_fma_f32 v[176:177], v[176:177], v[168:169], v[170:171] op_sel_hi:[1,0,0]\n\t" \
  "v_pk_fma_f32 v[178:179], v[178:179], v[168:169], v[170:171] op_sel_hi:[1,0,0]\n\t" \
  "v_pk_fma_f32 v[180:181], v[180:181], v[168:169], v[170:171] op_sel_hi:[1,0,0]\n\t" \
  "v_pk_fma_f32 v[182:183], v[182:183], v[168:169], v[170:171] op_sel_hi:[1,0,0]\n\t" \
  "v_pk_fma_f32 v[176:177], v[176:177], v[128:129], v[136:137]\n\t" \
  "v_pk_fma_f32 v[178:179], v[178:179], v[130:131], v[138:139]\n\t" \
  "v_pk_fma_f32 v[180:181], v[180:181], v[132:133], v[140:141]\n\t" \
  "v_pk_fma_f32 v[182:183], v[182:183], v[134:135], v[142:143]\n\t" \
  "v_cvt_pk_bf16_f32 v40, v176, v177\n\t" \
  "v_cvt_pk_bf16_f32 v41, v178, v179\n\t" \
  "v_cvt_pk_bf16_f32 v42, v180, v181\n\t" \
  "v_cvt_pk_bf16_f32 v43, v182, v183\n\t" \
  "s_add_u32 m0, %[mx], 104448\n\t" \
  "s_xor_b32 s96, %[c17], 393216\n\t" \
  ".p2align 3\n\t" \
  "s_add_u32 s96, s96, %[sl2]\n\t" \
  "buffer_load_dwordx4 v212, %[sx], s96 offen lds\n\t" \
  ".p2align 3\n\t" \
  "v_lshlrev_b32 v176, 16, v44\n\t" \
  "v_and_b32 v177, s98, v44\n\t" \
  "v_lshlrev_b32 v178, 16, v45\n\t" \
  "v_and_b32 v179, s98, v45\n\t" \
  "v_lshlrev_b32 v180, 16, v46\n\t" \
  "v_and_b32 v181, s98, v46\n\t" \
  "v_lshlrev_b32 v182, 16, v47\n\t" \
  "v_and_b32 v183, s98, v47\n\t" \
  "v_pk_fma_f32 v[176:177], v[176:177], v[172:173], v[174:175] op_sel_hi:[1,0,0]\n\t" \
  "v_pk_fma_f32 v[178:179], v[178:179], v[172:173], v[174:175] op_sel_hi:[1,0,0]\n\t" \
  "v_pk_fma_f32 v[180:181], v[180:181], v[172:173], v[174:175] op_sel_hi:[1,0,0]\n\t" \
  "v_pk_fma_f32 v[182:183], v[182:183], v[172:173], v[174:175] op_sel_hi:[1,0,0]\n\t" \
  "v_pk_fma_f32 v[176:177], v[176:177], v[128:129], v[136:137]\n\t" \
  "v_pk_fma_f32 v[178:179], v[178:179], v[130:131], v[138:139]\n\t" \
  "v_pk_fma_f32 v[180:181], v[180:181], v[132:133], v[140:141]\n\t" \
  "v_pk_fma_f32 v[182:183], v[182:183], v[134:135], v[142:143]\n\t" \
  "v_cvt_pk_bf16_f32 v44, v176, v177\n\t" \
  "v_cvt_pk_bf16_f32 v45, v178, v179\n\t" \
  "v_cvt_pk_bf16_f32 v46, v180, v181\n\t" \
  "v_cvt_pk_bf16_f32 v47, v182, v183\n\t" \
  "buffer_load_dwordx4 v213, %[sx], s96 offen offset:1024 lds\n\t" \
  ".p2align 3\n\t" \
  "ds_read_b128 v[128:131], v208 offset:1024\n\t" \
  "ds_read_b128 v[132:135], v208 offset:1040\n\t" \
  "ds_read_b128 v[136:139], v208 offset:1280\n\t" \
  "ds_read_b128 v[140:143], v208 offset:1296\n\t" \
  FSTP("v_writelane_b32 %[stv], s94, 30\n\t") \
  FSTP("v_writelane_b32 %[stv], s92, 31\n\t") \
  FSTP("s_memrealtime s[94:95]\n\t") \
  FSTP("s_memtime s[92:93]\n\t") \
  "v_lshlrev_b32 v176, 16, v48\n\t" \
  "v_and_b32 v177, s98, v48\n\t" \
  "v_lshlrev_b32 v178, 16, v49\n\t" \
  "v_and_b32 v179, s98, v49\n\t" \
  "v_lshlrev_b32 v180, 16, v50\n\t" \
  "v_and_b32 v181, s98, v50\n\t" \
  "v_lshlrev_b32 v182, 16, v51\n\t" \
  "v_and_b32 v183, s98, v51\n\t" \
  "v_pk_fma_f32 v[176:177], v[176:177], v[160:161], v[162:163] op_sel_hi:[1,0,0]\n\t" \
  "v_pk_fma_f32 v[178:179], v[178:179], v[160:161], v[162:163] op_sel_hi:[1,0,0]\n\t" \
  "v_pk_fma_f32 v[180:181], v[180:181], v[160:161], v[162:163] op_sel_hi:[1,0,0]\n\t" \
  "v_pk_fma_f32 v[182:183], v[182:183], v[160:161], v[162:163] op_sel_hi:[1,0,0]\n\t" \
  "s_waitcnt lgkmcnt(4)\n\t" \
  "s_nop 0\n\t" \
  "v_pk_fma_f32 v[176:177], v[176:177], v[144:145], v[152:153]\n\t" \
  "v_pk_fma_f32 v[178:179], v[178:179], v[146:147], v[154:155]\n\t" \
  "v_pk_fma_f32 v[180:181], v[180:181], v[148:149], v[156:157]\n\t" \
  "v_pk_fma_f32 v[182:183], v[182:183], v[150:151], v[158:159]\n\t" \
  "v_cvt_pk_bf16_f32 v48, v176, v177\n\t" \
  "v_cvt_pk_bf16_f32 v49, v178, v179\n\t" \
  "v_cvt_pk_bf16_f32 v50, v180, v181\n\t" \
  "v_cvt_pk_bf16_f32 v51, v182, v183\n\t" \
  "s_add_u32 m0, %[mw], 49152\n\t" \
  "s_mov_b32 s96, %[w1]\n\t" \
  ".p2align 3\n\t" \
  "buffer_load_dwordx4 v200, %[sw], s96 offen offset:0 lds\n\t" \
  ".p2align 3\n\t" \
  "ds_read_b64_tr_b16 a[128:129], v184 offset:0\n\t" \
  "ds_read_b64_tr_b16 a[130:131], v184 offset:1024\n\t" \
  "ds_read_b64_tr_b16 a[132:133], v185 offset:0\n\t" \
  "ds_read_b64_tr_b16 a[134:135], v185 offset:1024\n\t" \
  "v_lshlrev_b32 v176, 16, v52\n\t" \
  "v_and_b32 v177, s98, v52\n\t" \
  "v_lshlrev_b32 v178, 16, v53\n\t" \
  "v_and_b32 v179, s98, v53\n\t" \
  "v_lshlrev_b32 v180, 16, v54\n\t" \
  "v_and_b32 v181, s98, v54\n\t" \
  "v_lshlrev_b32 v182, 16, v55\n\t" \
  "v_and_b32 v183, s98, v55\n\t" \
  "v_pk_fma_f32 v[176:177], v[176:177], v[164:165], v[166:167] op_sel_hi:[1,0,0]\n\t" \
  "v_pk_fma_f32 v[178:179], v[178:179], v[164:165], v[166:167] op_sel_hi:[1,0,0]\n\t" \
  "v_pk_fma_f32 v[180:181], v[180:181], v[164:165], v[166:167] op_sel_hi:[1,0,0]\n\t" \
  "v_pk_fma_f32 v[182:183], v[182:183], v[164:165], v[166:167] op_sel_hi:[1,0,0]\n\t" \
  "v_pk_fma_f32 v[176:177], v[176:177], v[144:145], v[152:153]\n\t" \
  "v_pk_fma_f32 v[178:179], v[178:179], v[146:147], v[154:155]\n\t" \
  "v_pk_fma_f32 v[180:181], v[180:181], v[148:149], v[156:157]\n\t" \
  "v_pk_fma_f32 v[182:183], v[182:183], v[150:151], v[158:159]\n\t" \
  "v_cvt_pk_bf16_f32 v52, v176, v177\n\t" \
  "v_cvt_pk_bf16_f32 v53, v178, v179\n\t" \
  "v_cvt_pk_bf16_f32 v54, v180, v181\n\t" \
  "v_cvt_pk_bf16_f32 v55, v182, v183\n\t" \
  "buffer_load_dwordx4 v201, %[sw], s96 offen offset:1024 lds\n\t" \
  ".p2align 3\n\t" \
  "ds_read_b64_tr_b16 a[136:137], v186 offset:0\n\t" \
  "ds_read_b64_tr_b16 a[138:139], v186 offset:1024\n\t" \
  "ds_read_b64_tr_b16 a[140:141], v187 offset:0\n\t" \
  "ds_read_b64_tr_b16 a[142:143], v187 offset:1024\n\t" \
  "v_lshlrev_b32 v176, 16, v56\n\t" \
  "v_and_b32 v177, s98, v56\n\t" \
  "v_lshlrev_b32 v178, 16, v57\n\t" \
  "v_and_b32 v179, s98, v57\n\t" \
  "v_lshlrev_b32 v180, 16, v58\n\t" \
  "v_and_b32 v181, s98, v58\n\t" \
  "v_lshlrev_b32 v182, 16, v59\n\t" \
  "v_and_b32 v183, s98, v59\n\t" \
  "v_pk_fma_f32 v[176:177], v[176:177], v[168:169], v[170:171] op_sel_hi:[1,0,0]\n\t" \
  "v_pk_fma_f32 v[178:179], v[178:179], v[168:169], v[170:171] op_sel_hi:[1,0,0]\n\t" \
  "v_pk_fma_f32 v[180:181], v[180:181], v[168:169], v[170:171] op_sel_hi:[1,0,0]\n\t" \
  "v_pk_fma_f32 v[182:183], v[182:183], v[168:169], v[170:171] op_sel_hi:[1,0,0]\n\t" \
  "v_pk_fma_f32 v[176:177], v[176:177], v[144:145], v[152:153]\n\t" \
  "v_pk_fma_f32 v[178:179], v[178:179], v[146:147], v[154:155]\n\t" \
  "v_pk_fma_f32 v[180:181], v[180:181], v[148:149], v[156:157]\n\t" \
  "v_pk_fma_f32 v[182:183], v[182:183], v[150:151], v[158:159]\n\t" \
  "v_cvt_pk_bf16_f32 v56, v176, v177\n\t" \
  "v_cvt_pk_bf16_f32 v57, v178, v179\n\t" \
  "v_cvt_pk_bf16_f32 v58, v180, v181\n\t" \
  "v_cvt_pk_bf16_f32 v59, v182, v183\n\t" \
  "buffer_load_dwordx4 v202, %[sw], s96 offen offset:2048 lds\n\t" \
  ".p2align 3\n\t" \
  "ds_read_b64_tr_b16 a[144:145], v188 offset:0\n\t" \
  "ds_read_b64_tr_b16 a[146:147], v188 offset:1024\n\t" \
  "ds_read_b64_tr_b16 a[148:149], v189 offset:0\n\t" \
  "ds_read_b64_tr_b16 a[150:151], v189 offset:1024\n\t" \
  "v_lshlrev_b32 v176, 16, v60\n\t" \
  "v_and_b32 v177, s98, v60\n\t" \
  "v_lshlrev_b32 v178, 16, v61\n\t" \
  "v_and_b32 v179, s98, v61\n\t" \
  "v_lshlrev_b32 v180, 16, v62\n\t" \
  "v_and_b32 v181, s98, v62\n\t" \
  "v_lshlrev_b32 v182, 16, v63\n\t" \
  "v_and_b32 v183, s98, v63\n\t" \
  "v_pk_fma_f32 v[176:177], v[176:177], v[172:173], v[174:175] op_sel_hi:[1,0,0]\n\t" \
  "v_pk_fma_f32 v[178:179], v[178:179], v[172:173], v[174:175] op_sel_hi:[1,0,0]\n\t" \
  "v_pk_fma_f32 v[180:181], v[180:181], v[172:173], v[174:175] op_sel_hi:[1,0,0]\n\t" \
  "v_pk_fma_f32 v[182:183], v[182:183], v[172:173], v[174:175] op_sel_hi:[1,0,0]\n\t" \
  "v_pk_fma_f32 v[176:177], v[176:177], v[144:145], v[152:153]\n\t" \
  "v_pk_fma_f32 v[178:179], v[178:179], v[146:147], v[154:155]\n\t" \
  "v_pk_fma_f32 v[180:181], v[180:181], v[148:149], v[156:157]\n\t" \
  "v_pk_fma_f32 v[182:183], v[182:183], v[150:151], v[158:159]\n\t" \
  "v_cvt_pk_bf16_f32 v60, v176, v177\n\t" \
  "v_cvt_pk_bf16_f32 v61, v178, v179\n\t" \
  "v_cvt_pk_bf16_f32 v62, v180, v181\n\t" \
  "v_cvt_pk_bf16_f32 v63, v182, v183\n\t" \
  "buffer_load_dwordx4 v203, %[sw], s96 offen offset:3072 lds\n\t" \
  ".p2align 3\n\t" \
  "ds_read_b64_tr_b16 a[152:153], v190 offset:0\n\t" \
  "ds_read_b64_tr_b16 a[154:155], v190 offset:1024\n\t" \
  "ds_read_b64_tr_b16 a[156:157], v191 offset:0\n\t" \
  "ds_read_b64_tr_b16 a[158:159], v191 offset:1024\n\t" \
  "ds_read_b128 v[144:147], v208 offset:1152\n\t" \
  "ds_read_b128 v[148:151], v208 offset:1168\n\t" \
  "ds_read_b128 v[152:155], v208 offset:1408\n\t" \
  "ds_read_b128 v[156:159], v208 offset:1424\n\t" \
  FSTP("v_writelane_b32 %[stv], s94, 32\n\t") \
  FSTP("v_writelane_b32 %[stv], s92, 33\n\t") \
  FSTP("s_memrealtime s[94:95]\n\t") \
  FSTP("s_memtime s[92:93]\n\t") \
  "s_waitcnt vmcnt(12)\n\t" \
  "s_nop 0\n\t" \
  "s_mov_b64 exec, 1\n\t" \
  "buffer_store_dword v211, off, %[ws], %[fw] sc1\n\t" \
  "s_mov_b64 exec, -1\n\t" \
  FSTP("v_writelane_b32 %[stv], s94, 34\n\t") \
  FSTP("v_writelane_b32 %[stv], s92, 35\n\t") \
  FSTP("s_memrealtime s[94:95]\n\t") \
  FSTP("s_memtime s[92:93]\n\t") \
  "s_waitcnt lgkmcnt(4)\n\t" \
  "v_mfma_f32_16x16x32_bf16 a[0:3], a[128:131], v[0:3], 0\n\t" \
  ".Lhs0:\n\ts_cbranch_execz .Lhs0n\n\t" \
  "s_add_u32 m0, %[mw], 98304\n\t" \
  "ds_read_b64_tr_b16 a[160:161], v184 offset:8192\n\t" \
  "v_mfma_f32_16x16x32_bf16 a[4:7], a[128:131], v[4:7], 0\n\t" \
  "s_mov_b32 s96, %[w2]\n\t" \
  "ds_read_b64_tr_b16 a[162:163], v184 offset:9216\n\t" \
  "v_mfma_f32_16x16x32_bf16 a[8:11], a[128:131], v[8:11], 0\n\t" \
  "buffer_load_dwordx4 v200, %[sw], s96 offen offset:0 lds\n\t" \
  "v_mfma_f32_16x16x32_bf16 a[12:15], a[128:131], v[12:15], 0\n\t" \
  "buffer_load_dwordx4 v201, %[sw], s96 offen offset:1024 lds\n\t" \
  "v_mfma_f32_16x16x32_bf16 a[28:31], a[132:135], v[12:15], 0\n\t" \
  "ds_read_b64_tr_b16 a[164:165], v185 offset:8192\n\t" \
  "v_mfma_f32_16x16x32_bf16 a[24:27], a[132:135], v[8:11], 0\n\t" \
  "ds_read_b64_tr_b16 a[166:167], v185 offset:9216\n\t" \
  "v_mfma_f32_16x16x32_bf16 a[20:23], a[132:135], v[4:7], 0\n\t" \
  "buffer_load_dwordx4 v202, %[sw], s96 offen offset:2048 lds\n\t" \
  "v_mfma_f32_16x16x32_bf16 a[16:19], a[132:135], v[0:3], 0\n\t" \
  "ds_read_b64_tr_b16 a[168:169], v186 offset:8192\n\t" \
  "v_mfma_f32_16x16x32_bf16 a[32:35], a[136:139], v[0:3], 0\n\t" \
  "s_waitcnt vmcnt(8)\n\t" \
  "ds_read_b64_tr_b16 a[170:171], v186 offset:9216\n\t" \
  "ds_read_b128 v[64:67], v206 offset:0\n\t" \
  "v_mfma_f32_16x16x32_bf16 a[36:39], a[136:139], v[4:7], 0\n\t" \
  "buffer_load_dwordx4 v203, %[sw], s96 offen offset:3072 lds\n\t" \
  "v_mfma_f32_16x16x32_bf16 a[40:43], a[136:139], v[8:11], 0\n\t" \
  "s_add_u32 m0, %[mx], 0\n\t" \
  "ds_read_b64_tr_b16 a[172:173], v187 offset:8192\n\t" \
  "ds_read_b128 v[68:71], v206 offset:2048\n\t" \
  "v_mfma_f32_16x16x32_bf16 a[44:47], a[136:139], v[12:15], 0\n\t" \
  "s_xor_b32 s96, %[c17], 0\n\t" \
  "ds_read_b64_tr_b16 a[174:175], v187 offset:9216\n\t" \
  "ds_read_b128 v[72:75], v206 offset:4096\n\t" \
  "v_mfma_f32_16x16x32_bf16 a[60:63], a[140:143], v[12:15], 0\n\t" \
  "s_add_u32 s96, s96, %[sl3]\n\t" \
  "ds_read_b64_tr_b16 a[176:177], v188 offset:8192\n\t" \
  "ds_read_b128 v[76:79], v206 offset:6144\n\t" \
  "s_waitcnt lgkmcnt(0)\n\t" \
  "v_lshlrev_b32 v176, 16, v64\n\t" \
  "v_and_b32 v177, s98, v64\n\t" \
  "v_mfma_f32_16x16x32_bf16 a[56:59], a[140:143], v[8:11], 0\n\t" \
  "buffer_load_dwordx4 v212, %[sx], s96 offen lds\n\t" \
  "v_lshlrev_b32 v178, 16, v65\n\t" \
  "v_and_b32 v179, s98, v65\n\t" \
  "v_lshlrev_b32 v180, 16, v66\n\t" \
  "v_mfma_f32_16x16x32_bf16 a[52:55], a[140:143], v[4:7], 0\n\t" \
  "ds_read_b64_tr_b16 a[178:179], v188 offset:9216\n\t" \
  "v_and_b32 v181, s98, v66\n\t" \
  "v_lshlrev_b32 v182, 16, v67\n\t" \
  "v_and_b32 v183, s98, v67\n\t" \
  "v_fma_f32 v176, v176, v160, v162\n\t" \
  "v_mfma_f32_16x16x32_bf16 a[48:51], a[140:143], v[0:3], 0\n\t" \
  "buffer_load_dwordx4 v213, %[sx], s96 offen offset:1024 lds\n\t" \
  "v_fma_f32 v177, v177, v160, v162\n\t" \
  "v_fma_f32 v178, v178, v160, v162\n\t" \
  "v_fma_f32 v179, v179, v160, v162\n\t" \
  "v_mfma_f32_16x16x32_bf16 a[64:67], a[144:147], v[0:3], 0\n\t" \
  "s_add_u32 m0, %[mx], 2048\n\t" \
  "ds_read_b64_tr_b16 a[180:181], v189 offset:8192\n\t" \
  "v_fma_f32 v180, v180, v160, v162\n\t" \
  "v_fma_f32 v181, v181, v160, v162\n\t" \
  "v_fma_f32 v182, v182, v160, v162\n\t" \
  "v_fma_f32 v183, v183, v160, v162\n\t" \
  "v_mfma_f32_16x16x32_bf16 a[68:71], a[144:147], v[4:7], 0\n\t" \
  "s_xor_b32 s96, %[c17], 131072\n\t" \
  "ds_read_b64_tr_b16 a[182:183], v189 offset:9216\n\t" \
  "v_fma_f32 v176, v176, v128, v136\n\t" \
  "v_fma_f32 v177, v177, v129, v137\n\t" \
  "v_fma_f32 v178, v178, v130, v138\n\t" \
  "v_fma_f32 v179, v179, v131, v139\n\t" \
  "v_mfma_f32_16x16x32_bf16 a[72:75], a[144:147], v[8:11], 0\n\t" \
  "s_add_u32 s96, s96, %[sl3]\n\t" \
  "buffer_load_dwordx4 v212, %[sx], s96 offen lds\n\t" \
  "v_fma_f32 v180, v180, v132, v140\n\t" \
  "v_fma_f32 v181, v181, v133, v141\n\t" \
  "v_mfma_f32_16x16x32_bf16 a[76:79], a[144:147], v[12:15], 0\n\t" \
  "ds_read_b64_tr_b16 a[184:185], v190 offset:8192\n\t" \
  "v_fma_f32 v182, v182, v134, v142\n\t" \
  "v_fma_f32 v183, v183, v135, v143\n\t" \
  "v_cvt_pk_bf16_f32 v64, v176, v177\n\t" \
  "v_cvt_pk_bf16_f32 v65, v178, v179\n\t" \
  "v_mfma_f32_16x16x32_bf16 a[92:95], a[148:151], v[12:15], 0\n\t" \
  "buffer_load_dwordx4 v213, %[sx], s96 offen offset:1024 lds\n\t" \
  "v_cvt_pk_bf16_f32 v66, v180, v181\n\t" \
  "v_cvt_pk_bf16_f32 v67, v182, v183\n\t" \
  "v_lshlrev_b32 v176, 16, v68\n\t" \
  "v_mfma_f32_16x16x32_bf16 a[88:91], a[148:151], v[8:11], 0\n\t" \
  "s_add_u32 m0, %[mx], 4096\n\t" \
  "ds_read_b64_tr_b16 a[186:187], v190 offset:9216\n\t" \
  "v_and_b32 v177, s98, v68\n\t" \
  "v_lshlrev_b32 v178, 16, v69\n\t" \
  "v_and_b32 v179, s98, v69\n\t" \
  "v_lshlrev_b32 v180, 16, v70\n\t" \
  "v_mfma_f32_16x16x32_bf16 a[84:87], a[148:151], v[4:7], 0\n\t" \
  "s_xor_b32 s96, %[c17], 262144\n\t" \
  "s_add_u32 s96, s96, %[sl3]\n\t" \
  "buffer_load_dwordx4 v212, %[sx], s96 offen lds\n\t" \
  "v_and_b32 v181, s98, v70\n\t" \
  "v_mfma_f32_16x16x32_bf16 a[80:83], a[148:151], v[0:3], 0\n\t" \
  "ds_read_b64_tr_b16 a[188:189], v191 offset:8192\n\t" \
  "v_lshlrev_b32 v182, 16, v71\n\t" \
  "v_and_b32 v183, s98, v71\n\t" \
  "v_fma_f32 v176, v176, v164, v166\n\t" \
  "v_fma_f32 v177, v177, v164, v166\n\t" \
  "v_mfma_f32_16x16x32_bf16 a[96:99], a[152:155], v[0:3], 0\n\t" \
  "buffer_load_dwordx4 v213, %[sx], s96 offen offset:1024 lds\n\t" \
  "v_fma_f32 v178, v178, v164, v166\n\t" \
  "v_fma_f32 v179, v179, v164, v166\n\t" \
  "v_fma_f32 v180, v180, v164, v166\n\t" \
  "v_mfma_f32_16x16x32_bf16 a[100:103], a[152:155], v[4:7], 0\n\t" \
  "s_add_u32 m0, %[mx], 6144\n\t" \
  "ds_read_b64_tr_b16 a[190:191], v191 offset:9216\n\t" \
  "v_fma_f32 v181, v181, v164, v166\n\t" \
  "v_fma_f32 v182, v182, v164, v166\n\t" \
  "v_fma_f32 v183, v183, v164, v166\n\t" \
  "v_fma_f32 v176, v176, v128, v136\n\t" \
  "v_mfma_f32_16x16x32_bf16 a[104:107], a[152:155], v[8:11], 0\n\t" \
  "s_xor_b32 s96, %[c17], 393216\n\t" \
  "s_add_u32 s96, s96, %[sl3]\n\t" \
  "buffer_load_dwordx4 v212, %[sx], s96 offen lds\n\t" \
  "v_fma_f32 v177, v177, v129, v137\n\t" \
  "v_mfma_f32_16x16x32_bf16 a[108:111], a[152:155], v[12:15], 0\n\t" \
  "v_fma_f32 v178, v178, v130, v138\n\t" \
  "v_fma_f32 v179, v179, v131, v139\n\t" \
  "v_fma_f32 v180, v180, v132, v140\n\t" \
  "v_fma_f32 v181, v181, v133, v141\n\t" \
  "v_fma_f32 v182, v182, v134, v142\n\t" \
  "v_mfma_f32_16x16x32_bf16 a[124:127], a[156:159], v[12:15], 0\n\t" \
  "buffer_load_dwordx4 v213, %[sx], s96 offen offset:1024 lds\n\t" \
  "v_fma_f32 v183, v183, v135, v143\n\t" \
  "v_cvt_pk_bf16_f32 v68, v176, v177\n\t" \
  "v_cvt_pk_bf16_f32 v69, v178, v179\n\t" \
  "v_mfma_f32_16x16x32_bf16 a[120:123], a[156:159], v[8:11], 0\n\t" \
  "v_cvt_pk_bf16_f32 v70, v180, v181\n\t" \
  "v_cvt_pk_bf16_f32 v71, v182, v183\n\t" \
  "v_lshlrev_b32 v176, 16, v72\n\t" \
  "v_and_b32 v177, s98, v72\n\t" \
  "v_mfma_f32_16x16x32_bf16 a[116:119], a[156:159], v[4:7], 0\n\t" \
  "v_lshlrev_b32 v178, 16, v73\n\t" \
  "v_and_b32 v179, s98, v73\n\t" \
  "v_lshlrev_b32 v180, 16, v74\n\t" \
  "v_and_b32 v181, s98, v74\n\t" \
  "v_lshlrev_b32 v182, 16, v75\n\t" \
  "v_and_b32 v183, s98, v75\n\t" \
  "v_mfma_f32_16x16x32_bf16 a[112:115], a[156:159], v[0:3], 0\n\t" \
  FSTP("v_writelane_b32 %[stv], s94, 36\n\t") \
  FSTP("v_writelane_b32 %[stv], s92, 37\n\t") \
  FSTP("s_memrealtime s[94:95]\n\t") \
  FSTP("s_memtime s[92:93]\n\t") \
  "s_waitcnt vmcnt(13) lgkmcnt(0)\n\t" \
  "s_barrier\n\t" \
  "v_mfma_f32_16x16x32_bf16 a[0:3], a[160:163], v[16:19], a[0:3]\n\t" \
  "s_add_u32 m0, %[mw], 0\n\t" \
  "ds_read_b64_tr_b16 a[128:129], v184 offset:49152\n\t" \
  "v_fma_f32 v176, v176, v168, v170\n\t" \
  "v_fma_f32 v177, v177, v168, v170\n\t" \
  "v_mfma_f32_16x16x32_bf16 a[4:7], a[160:163], v[20:23], a[4:7]\n\t" \
  "s_mov_b32 s96, %[w3]\n\t" \
  "ds_read_b64_tr_b16 a[130:131], v184 offset:50176\n\t" \
  "v_fma_f32 v178, v178, v168, v170\n\t" \
  "v_fma_f32 v179, v179, v168, v170\n\t" \
  "v_mfma_f32_16x16x32_bf16 a[8:11], a[160:163], v[24:27], a[8:11]\n\t" \
  "ds_read_b64_tr_b16 a[132:133], v185 offset:49152\n\t" \
  "v_fma_f32 v180, v180, v168, v170\n\t" \
  "v_fma_f32 v181, v181, v168, v170\n\t" \
  "v_mfma_f32_16x16x32_bf16 a[12:15], a[160:163], v[28:31], a[12:15]\n\t" \
  "ds_read_b64_tr_b16 a[134:135], v185 offset:50176\n\t" \
  "v_fma_f32 v182, v182, v168, v170\n\t" \
  "v_fma_f32 v183, v183, v168, v170\n\t" \
  "v_mfma_f32_16x16x32_bf16 a[28:31], a[164:167], v[28:31], a[28:31]\n\t" \
  "buffer_load_dwordx4 v200, %[sw], s96 offen offset:0 lds\n\t" \
  "v_fma_f32 v176, v176, v128, v136\n\t" \
  "v_mfma_f32_16x16x32_bf16 a[24:27], a[164:167], v[24:27], a[24:27]\n\t" \
  "ds_read_b64_tr_b16 a[136:137], v186 offset:49152\n\t" \
  "v_fma_f32 v177, v177, v129, v137\n\t" \
  "v_fma_f32 v178, v178, v130, v138\n\t" \
  "v_mfma_f32_16x16x32_bf16 a[20:23], a[164:167], v[20:23], a[20:23]\n\t" \
  "ds_read_b64_tr_b16 a[138:139], v186 offset:50176\n\t" \
  "v_fma_f32 v179, v179, v131, v139\n\t" \
  "v_fma_f32 v180, v180, v132, v140\n\t" \
  "v_mfma_f32_16x16x32_bf16 a[16:19], a[164:167], v[16:19], a[16:19]\n\t" \
  "buffer_load_dwordx4 v201, %[sw], s96 offen offset:1024 lds\n\t" \
  "v_fma_f32 v181, v181, v133, v141\n\t" \
  "v_mfma_f32_16x16x32_bf16 a[32:35], a[168:171], v[16:19], a[32:35]\n\t" \
  "ds_read_b64_tr_b16 a[140:141], v187 offset:49152\n\t" \
  "v_fma_f32 v182, v182, v134, v142\n\t" \
  "v_fma_f32 v183, v183, v135, v143\n\t" \
  "v_mfma_f32_16x16x32_bf16 a[36:39], a[168:171], v[20:23], a[36:39]\n\t" \
  "ds_read_b64_tr_b16 a[142:143], v187 offset:50176\n\t" \
  "v_cvt_pk_bf16_f32 v72, v176, v177\n\t" \
  "v_cvt_pk_bf16_f32 v73, v178, v179\n\t" \
  "v_mfma_f32_16x16x32_bf16 a[40:43], a[168:171], v[24:27], a[40:43]\n\t" \
  "ds_read_b64_tr_b16 a[144:145], v188 offset:49152\n\t" \
  "v_cvt_pk_bf16_f32 v74, v180, v181\n\t" \
  "v_cvt_pk_bf16_f32 v75, v182, v183\n\t" \
  "v_mfma_f32_16x16x32_bf16 a[44:47], a[168:171], v[28:31], a[44:47]\n\t" \
  "buffer_load_dwordx4 v202, %[sw], s96 offen offset:2048 lds\n\t" \
  "v_mfma_f32_16x16x32_bf16 a[60:63], a[172:175], v[28:31], a[60:63]\n\t" \
  "ds_read_b64_tr_b16 a[146:147], v188 offset:50176\n\t" \
  "ds_read_b128 v[80:83], v207 offset:0\n\t" \
  "v_mfma_f32_16x16x32_bf16 a[56:59], a[172:175], v[24:27], a[56:59]\n\t" \
  "ds_read_b64_tr_b16 a[148:149], v189 offset:49152\n\t" \
  "ds_read_b128 v[84:87], v207 offset:2048\n\t" \
  "v_mfma_f32_16x16x32_bf16 a[52:55], a[172:175], v[20:23], a[52:55]\n\t" \
  "ds_read_b64_tr_b16 a[150:151], v189 offset:50176\n\t" \
  "ds_read_b128 v[88:91], v207 offset:4096\n\t" \
  "v_mfma_f32_16x16x32_bf16 a[48:51], a[172:175], v[16:19], a[48:51]\n\t" \
  "ds_read_b64_tr_b16 a[152:153], v190 offset:49152\n\t" \
  "ds_read_b128 v[92:95], v207 offset:6144\n\t" \
  "v_lshlrev_b32 v176, 16, v76\n\t" \
  "v_and_b32 v177, s98, v76\n\t" \
  "v_mfma_f32_16x16x32_bf16 a[64:67], a[176:179], v[16:19], a[64:67]\n\t" \
  "buffer_load_dwordx4 v203, %[sw], s96 offen offset:3072 lds\n\t" \
  "v_lshlrev_b32 v178, 16, v77\n\t" \
  "v_and_b32 v179, s98, v77\n\t" \
  "v_mfma_f32_16x16x32_bf16 a[68:71], a[176:179], v[20:23], a[68:71]\n\t" \
  "ds_read_b64_tr_b16 a[154:155], v190 offset:50176\n\t" \
  "v_lshlrev_b32 v180, 16, v78\n\t" \
  "v_and_b32 v181, s98, v78\n\t" \
  "v_lshlrev_b32 v182, 16, v79\n\t" \
  "v_mfma_f32_16x16x32_bf16 a[72:75], a[176:179], v[24:27], a[72:75]\n\t" \
  "ds_read_b64_tr_b16 a[156:157], v191 offset:49152\n\t" \
  "v_and_b32 v183, s98, v79\n\t" \
  "v_fma_f32 v176, v176, v172, v174\n\t" \
  "v_fma_f32 v177, v177, v172, v174\n\t" \
  "v_mfma_f32_16x16x32_bf16 a[76:79], a[176:179], v[28:31], a[76:79]\n\t" \
  "ds_read_b64_tr_b16 a[158:159], v191 offset:50176\n\t" \
  "v_fma_f32 v178, v178, v172, v174\n\t" \
  "v_fma_f32 v179, v179, v172, v174\n\t" \
  "v_mfma_f32_16x16x32_bf16 a[92:95], a[180:183], v[28:31], a[92:95]\n\t" \
  "v_fma_f32 v180, v180, v172, v174\n\t" \
  "v_fma_f32 v181, v181, v172, v174\n\t" \
  "v_fma_f32 v182, v182, v172, v174\n\t" \
  "v_mfma_f32_16x16x32_bf16 a[88:91], a[180:183], v[24:27], a[88:91]\n\t" \
  "v_fma_f32 v183, v183, v172, v174\n\t" \
  "v_fma_f32 v176, v176, v128, v136\n\t" \
  "v_fma_f32 v177, v177, v129, v137\n\t" \
  "v_mfma_f32_16x16x32_bf16 a[84:87], a[180:183], v[20:23], a[84:87]\n\t" \
  "global_load_dword %[pf], v215, %[wq]\n\t" \
  "v_fma_f32 v178, v178, v130, v138\n\t" \
  "v_mfma_f32_16x16x32_bf16 a[80:83], a[180:183], v[16:19], a[80:83]\n\t" \
  "v_fma_f32 v179, v179, v131, v139\n\t" \
  "v_fma_f32 v180, v180, v132, v140\n\t" \
  "v_fma_f32 v181, v181, v133, v141\n\t" \
  "v_mfma_f32_16x16x32_bf16 a[96:99], a[184:187], v[16:19], a[96:99]\n\t" \
  "v_fma_f32 v182, v182, v134, v142\n\t" \
  "v_fma_f32 v183, v183, v135, v143\n\t" \
  "v_cvt_pk_bf16_f32 v76, v176, v177\n\t" \
  "v_mfma_f32_16x16x32_bf16 a[100:103], a[184:187], v[20:23], a[100:103]\n\t" \
  "v_cvt_pk_bf16_f32 v77, v178, v179\n\t" \
  "v_cvt_pk_bf16_f32 v78, v180, v181\n\t" \
  "v_cvt_pk_bf16_f32 v79, v182, v183\n\t" \
  "v_mfma_f32_16x16x32_bf16 a[104:107], a[184:187], v[24:27], a[104:107]\n\t" \
  "ds_read_b128 v[128:131], v208 offset:1536\n\t" \
  "ds_read_b128 v[132:135], v208 offset:1552\n\t" \
  "v_mfma_f32_16x16x32_bf16 a[108:111], a[184:187], v[28:31], a[108:111]\n\t" \
  "ds_read_b128 v[136:139], v208 offset:1792\n\t" \
  "ds_read_b128 v[140:143], v208 offset:1808\n\t" \
  "s_waitcnt lgkmcnt(7)\n\t" \
  "v_lshlrev_b32 v176, 16, v80\n\t" \
  "v_mfma_f32_16x16x32_bf16 a[124:127], a[188:191], v[28:31], a[124:127]\n\t" \
  "v_and_b32 v177, s98, v80\n\t" \
  "v_lshlrev_b32 v178, 16, v81\n\t" \
  "v_and_b32 v179, s98, v81\n\t" \
  "v_lshlrev_b32 v180, 16, v82\n\t" \
  "v_mfma_f32_16x16x32_bf16 a[120:123], a[188:191], v[24:27], a[120:123]\n\t" \
  "v_and_b32 v181, s98, v82\n\t" \
  "v_lshlrev_b32 v182, 16, v83\n\t" \
  "v_and_b32 v183, s98, v83\n\t" \
  "v_mfma_f32_16x16x32_bf16 a[116:119], a[188:191], v[20:23], a[116:119]\n\t" \
  "v_fma_f32 v176, v176, v160, v162\n\t" \
  "v_fma_f32 v177, v177, v160, v162\n\t" \
  "v_fma_f32 v178, v178, v160, v162\n\t" \
  "v_fma_f32 v179, v179, v160, v162\n\t" \
  "v_mfma_f32_16x16x32_bf16 a[112:115], a[188:191], v[16:19], a[112:115]\n\t" \
  FSTP("v_writelane_b32 %[stv], s94, 38\n\t") \
  FSTP("v_writelane_b32 %[stv], s92, 39\n\t") \
  FSTP("s_memrealtime s[94:95]\n\t") \
  FSTP("s_memtime s[92:93]\n\t") \
  "s_waitcnt lgkmcnt(4)\n\t" \
  "v_mfma_f32_16x16x32_bf16 a[0:3], a[128:131], v[32:35], a[0:3]\n\t" \
  "global_load_dword %[pf], v217, %[kv]\n\t" \
  "v_fma_f32 v180, v180, v160, v162\n\t" \
  "v_mfma_f32_16x16x32_bf16 a[4:7], a[128:131], v[36:39], a[4:7]\n\t" \
  "ds_read_b64_tr_b16 a[160:161], v184 offset:57344\n\t" \
  "v_fma_f32 v181, v181, v160, v162\n\t" \
  "v_fma_f32 v182, v182, v160, v162\n\t" \
  "v_mfma_f32_16x16x32_bf16 a[8:11], a[128:131], v[40:43], a[8:11]\n\t" \
  "ds_read_b64_tr_b16 a[162:163], v184 offset:58368\n\t" \
  "v_fma_f32 v183, v183, v160, v162\n\t" \
  "v_fma_f32 v176, v176, v144, v152\n\t" \
  "v_mfma_f32_16x16x32_bf16 a[12:15], a[128:131], v[44:47], a[12:15]\n\t" \
  "ds_read_b64_tr_b16 a[164:165], v185 offset:57344\n\t" \
  "v_fma_f32 v177, v177, v145, v153\n\t" \
  "v_fma_f32 v178, v178, v146, v154\n\t" \
  "v_mfma_f32_16x16x32_bf16 a[28:31], a[132:135], v[44:47], a[28:31]\n\t" \
  "ds_read_b64_tr_b16 a[166:167], v185 offset:58368\n\t" \
  "v_fma_f32 v179, v179, v147, v155\n\t" \
  "v_fma_f32 v180, v180, v148, v156\n\t" \
  "v_mfma_f32_16x16x32_bf16 a[24:27], a[132:135], v[40:43], a[24:27]\n\t" \
  "ds_read_b64_tr_b16 a[168:169], v186 offset:57344\n\t" \
  "v_fma_f32 v181, v181, v149, v157\n\t" \
  "v_fma_f32 v182, v182, v150, v158\n\t" \
  "v_mfma_f32_16x16x32_bf16 a[20:23], a[132:135], v[36:39], a[20:23]\n\t" \
  "ds_read_b64_tr_b16 a[170:171], v186 offset:58368\n\t" \
  "v_fma_f32 v183, v183, v151, v159\n\t" \
  "v_cvt_pk_bf16_f32 v80, v176, v177\n\t" \
  "v_mfma_f32_16x16x32_bf16 a[16:19], a[132:135], v[32:35], a[16:19]\n\t" \
  "ds_read_b64_tr_b16 a[172:173], v187 offset:57344\n\t" \
  "v_cvt_pk_bf16_f32 v81, v178, v179\n\t" \
  "v_cvt_pk_bf16_f32 v82, v180, v181\n\t" \
  "v_mfma_f32_16x16x32_bf16 a[32:35], a[136:139], v[32:35], a[32:35]\n\t" \
  "ds_read_b64_tr_b16 a[174:175], v187 offset:58368\n\t" \
  "v_cvt_pk_bf16_f32 v83, v182, v183\n\t" \
  "v_lshlrev_b32 v176, 16, v84\n\t" \
  "v_mfma_f32_16x16x32_bf16 a[36:39], a[136:139], v[36:39], a[36:39]\n\t" \
  "ds_read_b64_tr_b16 a[176:177], v188 offset:57344\n\t" \
  "v_and_b32 v177, s98, v84\n\t" \
  "v_lshlrev_b32 v178, 16, v85\n\t" \
  "v_mfma_f32_16x16x32_bf16 a[40:43], a[136:139], v[40:43], a[40:43]\n\t" \
  "ds_read_b64_tr_b16 a[178:179], v188 offset:58368\n\t" \
  "v_and_b32 v179, s98, v85\n\t" \
  "v_lshlrev_b32 v180, 16, v86\n\t" \
  "v_mfma_f32_16x16x32_bf16 a[44:47], a[136:139], v[44:47], a[44:47]\n\t" \
  "ds_read_b64_tr_b16 a[180:181], v189 offset:57344\n\t" \
  "v_and_b32 v181, s98, v86\n\t" \
  "v_lshlrev_b32 v182, 16, v87\n\t" \
  "v_mfma_f32_16x16x32_bf16 a[60:63], a[140:143], v[44:47], a[60:63]\n\t" \
  "ds_read_b64_tr_b16 a[182:183], v189 offset:58368\n\t" \
  "v_and_b32 v183, s98, v87\n\t" \
  "v_fma_f32 v176, v176, v164, v166\n\t" \
  "v_mfma_f32_16x16x32_bf16 a[56:59], a[140:143], v[40:43], a[56:59]\n\t" \
  "ds_read_b64_tr_b16 a[184:185], v190 offset:57344\n\t" \
  "v_fma_f32 v177, v177, v164, v166\n\t" \
  "v_fma_f32 v178, v178, v164, v166\n\t" \
  "v_mfma_f32_16x16x32_bf16 a[52:55], a[140:143], v[36:39], a[52:55]\n\t" \
  "buffer_store_short v218, v219, %[ws], 0 offen sc1\n\t" \
  "v_fma_f32 v179, v179, v164, v166\n\t" \
  "v_mfma_f32_16x16x32_bf16 a[48:51], a[140:143], v[32:35], a[48:51]\n\t" \
  "ds_read_b64_tr_b16 a[186:187], v190 offset:58368\n\t" \
  "v_fma_f32 v180, v180, v164, v166\n\t" \
  "v_fma_f32 v181, v181, v164, v166\n\t" \
  "v_mfma_f32_16x16x32_bf16 a[64:67], a[144:147], v[32:35], a[64:67]\n\t" \
  "ds_read_b64_tr_b16 a[188:189], v191 offset:57344\n\t" \
  "v_fma_f32 v182, v182, v164, v166\n\t" \
  "v_fma_f32 v183, v183, v164, v166\n\t" \
  "v_mfma_f32_16x16x32_bf16 a[68:71], a[144:147], v[36:39], a[68:71]\n\t" \
  "ds_read_b64_tr_b16 a[190:191], v191 offset:58368\n\t" \
  "v_fma_f32 v176, v176, v144, v152\n\t" \
  "v_fma_f32 v177, v177, v145, v153\n\t" \
  "v_mfma_f32_16x16x32_bf16 a[72:75], a[144:147], v[40:43], a[72:75]\n\t" \
  "v_fma_f32 v178, v178, v146, v154\n\t" \
  "v_fma_f32 v179, v179, v147, v155\n\t" \
  "v_fma_f32 v180, v180, v148, v156\n\t" \
  "v_mfma_f32_16x16x32_bf16 a[76:79], a[144:147], v[44:47], a[76:79]\n\t" \
  "v_fma_f32 v181, v181, v149, v157\n\t" \
  "v_fma_f32 v182, v182, v150, v158\n\t" \
  "v_fma_f32 v183, v183, v151, v159\n\t" \
  "v_mfma_f32_16x16x32_bf16 a[92:95], a[148:151], v[44:47], a[92:95]\n\t" \
  "v_cvt_pk_bf16_f32 v84, v176, v177\n\t" \
  "v_cvt_pk_bf16_f32 v85, v178, v179\n\t" \
  "v_cvt_pk_bf16_f32 v86, v180, v181\n\t" \
  "v_mfma_f32_16x16x32_bf16 a[88:91], a[148:151], v[40:43], a[88:91]\n\t" \
  "v_cvt_pk_bf16_f32 v87, v182, v183\n\t" \
  "v_lshlrev_b32 v176, 16, v88\n\t" \
  "v_and_b32 v177, s98, v88\n\t" \
  "v_mfma_f32_16x16x32_bf16 a[84:87], a[148:151], v[36:39], a[84:87]\n\t" \
  "v_lshlrev_b32 v178, 16, v89\n\t" \
  "v_and_b32 v179, s98, v89\n\t" \
  "v_lshlrev_b32 v180, 16, v90\n\t" \
  "v_mfma_f32_16x16x32_bf16 a[80:83], a[148:151], v[32:35], a[80:83]\n\t" \
  "v_and_b32 v181, s98, v90\n\t" \
  "v_lshlrev_b32 v182, 16, v91\n\t" \
  "v_and_b32 v183, s98, v91\n\t" \
  "v_mfma_f32_16x16x32_bf16 a[96:99], a[152:155], v[32:35], a[96:99]\n\t" \
  "v_fma_f32 v176, v176, v168, v170\n\t" \
  "v_fma_f32 v177, v177, v168, v170\n\t" \
  "v_fma_f32 v178, v178, v168, v170\n\t" \
  "v_mfma_f32_16x16x32_bf16 a[100:103], a[152:155], v[36:39], a[100:103]\n\t" \
  "v_fma_f32 v179, v179, v168, v170\n\t" \
  "v_fma_f32 v180, v180, v168, v170\n\t" \
  "v_fma_f32 v181, v181, v168, v170\n\t" \
  "v_mfma_f32_16x16x32_bf16 a[104:107], a[152:155], v[40:43], a[104:107]\n\t" \
  "v_fma_f32 v182, v182, v168, v170\n\t" \
  "v_fma_f32 v183, v183, v168, v170\n\t" \
  "v_mfma_f32_16x16x32_bf16 a[108:111], a[152:155], v[44:47], a[108:111]\n\t" \
  "v_fma_f32 v176, v176, v144, v152\n\t" \
  "v_fma_f32 v177, v177, v145, v153\n\t" \
  "v_mfma_f32_16x16x32_bf16 a[124:127], a[156:159], v[44:47], a[124:127]\n\t" \
  "v_fma_f32 v178, v178, v146, v154\n\t" \
  "v_fma_f32 v179, v179, v147, v155\n\t" \
  "v_mfma_f32_16x16x32_bf16 a[120:123], a[156:159], v[40:43], a[120:123]\n\t" \
  "v_fma_f32 v180, v180, v148, v156\n\t" \
  "v_fma_f32 v181, v181, v149, v157\n\t" \
  "v_mfma_f32_16x16x32_bf16 a[116:119], a[156:159], v[36:39], a[116:119]\n\t" \
  "v_fma_f32 v182, v182, v150, v158\n\t" \
  "v_fma_f32 v183, v183, v151, v159\n\t" \
  "v_mfma_f32_16x16x32_bf16 a[112:115], a[156:159], v[32:35], a[112:115]\n\t" \
  FSTP("v_writelane_b32 %[stv], s94, 40\n\t") \
  FSTP("v_writelane_b32 %[stv], s92, 41\n\t") \
  FSTP("s_memrealtime s[94:95]\n\t") \
  FSTP("s_memtime s[92:93]\n\t") \
  "s_waitcnt vmcnt(15) lgkmcnt(0)\n\t" \
  "s_barrier\n\t" \
  "v_mfma_f32_16x16x32_bf16 a[0:3], a[160:163], v[48:51], a[0:3]\n\t" \
  "buffer_load_dword %[fl], v210, %[ws], %[fo] offen sc1\n\t" \
  "v_cvt_pk_bf16_f32 v88, v176, v177\n\t" \
  "v_cvt_pk_bf16_f32 v89, v178, v179\n\t" \
  "v_mfma_f32_16x16x32_bf16 a[4:7], a[160:163], v[52:55], a[4:7]\n\t" \
  "s_add_u32 s96, %[rot19], 2097152\n\t" \
  "ds_read_b64_tr_b16 a[128:129], v192 offset:0\n\t" \
  "v_cvt_pk_bf16_f32 v90, v180, v181\n\t" \
  "v_cvt_pk_bf16_f32 v91, v182, v183\n\t" \
  "s_waitcnt vmcnt(8)\n\t" \
  "v_mfma_f32_16x16x32_bf16 a[8:11], a[160:163], v[56:59], a[8:11]\n\t" \
  "s_and_b32 s96, s96, 0x780000\n\t" \
  "ds_read_b64_tr_b16 a[130:131], v192 offset:1024\n\t" \
  "ds_read_b128 v[96:99], v204 offset:0\n\t" \
  "v_mfma_f32_16x16x32_bf16 a[12:15], a[160:163], v[60:63], a[12:15]\n\t" \
  "s_add_u32 m0, %[mw], 49152\n\t" \
  "ds_read_b64_tr_b16 a[132:133], v193 offset:0\n\t" \
  "ds_read_b128 v[100:103], v204 offset:2048\n\t" \
  "v_mfma_f32_16x16x32_bf16 a[28:31], a[164:167], v[60:63], a[28:31]\n\t" \
  "buffer_load_dwordx4 v200, %[sw], s96 offen offset:0 lds\n\t" \
  "v_mfma_f32_16x16x32_bf16 a[24:27], a[164:167], v[56:59], a[24:27]\n\t" \
  "ds_read_b64_tr_b16 a[134:135], v193 offset:1024\n\t" \
  "ds_read_b128 v[104:107], v204 offset:4096\n\t" \
  "v_mfma_f32_16x16x32_bf16 a[20:23], a[164:167], v[52:55], a[20:23]\n\t" \
  "ds_read_b64_tr_b16 a[136:137], v194 offset:0\n\t" \
  "ds_read_b128 v[108:111], v204 offset:6144\n\t" \
  "v_lshlrev_b32 v176, 16, v92\n\t" \
  "v_and_b32 v177, s98, v92\n\t" \
  "v_mfma_f32_16x16x32_bf16 a[16:19], a[164:167], v[48:51], a[16:19]\n\t" \
  "ds_read_b64_tr_b16 a[138:139], v194 offset:1024\n\t" \
  "v_lshlrev_b32 v178, 16, v93\n\t" \
  "v_and_b32 v179, s98, v93\n\t" \
  "v_lshlrev_b32 v180, 16, v94\n\t" \
  "v_mfma_f32_16x16x32_bf16 a[32:35], a[168:171], v[48:51], a[32:35]\n\t" \
  "ds_read_b64_tr_b16 a[140:141], v195 offset:0\n\t" \
  "v_and_b32 v181, s98, v94\n\t" \
  "v_lshlrev_b32 v182, 16, v95\n\t" \
  "v_and_b32 v183, s98, v95\n\t" \
  "v_mfma_f32_16x16x32_bf16 a[36:39], a[168:171], v[52:55], a[36:39]\n\t" \
  "buffer_load_dwordx4 v201, %[sw], s96 offen offset:1024 lds\n\t" \
  "v_fma_f32 v176, v176, v172, v174\n\t" \
  "v_fma_f32 v177, v177, v172, v174\n\t" \
  "v_mfma_f32_16x16x32_bf16 a[40:43], a[168:171], v[56:59], a[40:43]\n\t" \
  "ds_read_b64_tr_b16 a[142:143], v195 offset:1024\n\t" \
  "v_fma_f32 v178, v178, v172, v174\n\t" \
  "v_fma_f32 v179, v179, v172, v174\n\t" \
  "v_fma_f32 v180, v180, v172, v174\n\t" \
  "v_mfma_f32_16x16x32_bf16 a[44:47], a[168:171], v[60:63], a[44:47]\n\t" \
  "ds_read_b64_tr_b16 a[144:145], v196 offset:0\n\t" \
  "v_fma_f32 v181, v181, v172, v174\n\t" \
  "v_fma_f32 v182, v182, v172, v174\n\t" \
  "v_fma_f32 v183, v183, v172, v174\n\t" \
  "v_mfma_f32_16x16x32_bf16 a[60:63], a[172:175], v[60:63], a[60:63]\n\t" \
  "ds_read_b64_tr_b16 a[146:147], v196 offset:1024\n\t" \
  "v_fma_f32 v176, v176, v144, v152\n\t" \
  "v_fma_f32 v177, v177, v145, v153\n\t" \
  "v_fma_f32 v178, v178, v146, v154\n\t" \
  "v_mfma_f32_16x16x32_bf16 a[56:59], a[172:175], v[56:59], a[56:59]\n\t" \
  "ds_read_b64_tr_b16 a[148:149], v197 offset:0\n\t" \
  "v_fma_f32 v179, v179, v147, v155\n\t" \
  "v_fma_f32 v180, v180, v148, v156\n\t" \
  "v_fma_f32 v181, v181, v149, v157\n\t" \
  "v_mfma_f32_16x16x32_bf16 a[52:55], a[172:175], v[52:55], a[52:55]\n\t" \
  "buffer_load_dwordx4 v202, %[sw], s96 offen offset:2048 lds\n\t" \
  "v_fma_f32 v182, v182, v150, v158\n\t" \
  "v_mfma_f32_16x16x32_bf16 a[48:51], a[172:175], v[48:51], a[48:51]\n\t" \
  "ds_read_b64_tr_b16 a[150:151], v197 offset:1024\n\t" \
  "v_fma_f32 v183, v183, v151, v159\n\t" \
  "v_cvt_pk_bf16_f32 v92, v176, v177\n\t" \
  "v_mfma_f32_16x16x32_bf16 a[64:67], a[176:179], v[48:51], a[64:67]\n\t" \
  "ds_read_b64_tr_b16 a[152:153], v198 offset:0\n\t" \
  "v_cvt_pk_bf16_f32 v93, v178, v179\n\t" \
  "v_cvt_pk_bf16_f32 v94, v180, v181\n\t" \
  "v_mfma_f32_16x16x32_bf16 a[68:71], a[176:179], v[52:55], a[68:71]\n\t" \
  "ds_read_b64_tr_b16 a[154:155], v198 offset:1024\n\t" \
  "v_cvt_pk_bf16_f32 v95, v182, v183\n\t" \
  "v_mfma_f32_16x16x32_bf16 a[72:75], a[176:179], v[56:59], a[72:75]\n\t" \
  "buffer_load_dwordx4 v203, %[sw], s96 offen offset:3072 lds\n\t" \
  "v_mfma_f32_16x16x32_bf16 a[76:79], a[176:179], v[60:63], a[76:79]\n\t" \
  "ds_read_b64_tr_b16 a[156:157], v199 offset:0\n\t" \
  "ds_read_b128 v[144:147], v208 offset:1664\n\t" \
  "v_mfma_f32_16x16x32_bf16 a[92:95], a[180:183], v[60:63], a[92:95]\n\t" \
  "ds_read_b64_tr_b16 a[158:159], v199 offset:1024\n\t" \
  "ds_read_b128 v[148:151], v208 offset:1680\n\t" \
  "v_mfma_f32_16x16x32_bf16 a[88:91], a[180:183], v[56:59], a[88:91]\n\t" \
  "ds_read_b128 v[152:155], v208 offset:1920\n\t" \
  "ds_read_b128 v[156:159], v208 offset:1936\n\t" \
  "s_waitcnt lgkmcnt(15)\n\t" \
  "v_lshlrev_b32 v176, 16, v96\n\t" \
  "v_mfma_f32_16x16x32_bf16 a[84:87], a[180:183], v[52:55], a[84:87]\n\t" \
  "v_and_b32 v177, s98, v96\n\t" \
  "v_lshlrev_b32 v178, 16, v97\n\t" \
  "v_and_b32 v179, s98, v97\n\t" \
  "v_lshlrev_b32 v180, 16, v98\n\t" \
  "v_mfma_f32_16x16x32_bf16 a[80:83], a[180:183], v[48:51], a[80:83]\n\t" \
  "global_load_dword %[pf], v216, %[wq]\n\t" \
  "v_and_b32 v181, s98, v98\n\t" \
  "v_lshlrev_b32 v182, 16, v99\n\t" \
  "v_mfma_f32_16x16x32_bf16 a[96:99], a[184:187], v[48:51], a[96:99]\n\t" \
  "s_lshr_b32 s99, %[c17], 9\n\t" \
  "v_xor_b32 v248, s99, v214\n\t" \
  "v_xor_b32 v249, 256, v248\n\t" \
  "v_xor_b32 v250, 512, v248\n\t" \
  "v_xor_b32 v251, 768, v248\n\t" \
  "v_mfma_f32_16x16x32_bf16 a[100:103], a[184:187], v[52:55], a[100:103]\n\t" \
  "v_and_b32 v183, s98, v99\n\t" \
  "v_fma_f32 v176, v176, v160, v162\n\t" \
  "v_fma_f32 v177, v177, v160, v162\n\t" \
  "v_fma_f32 v178, v178, v160, v162\n\t" \
  "v_mfma_f32_16x16x32_bf16 a[104:107], a[184:187], v[56:59], a[104:107]\n\t" \
  "v_fma_f32 v179, v179, v160, v162\n\t" \
  "v_fma_f32 v180, v180, v160, v162\n\t" \
  "v_fma_f32 v181, v181, v160, v162\n\t" \
  "v_mfma_f32_16x16x32_bf16 a[108:111], a[184:187], v[60:63], a[108:111]\n\t" \
  "v_fma_f32 v182, v182, v160, v162\n\t" \
  "v_fma_f32 v183, v183, v160, v162\n\t" \
  "v_fma_f32 v176, v176, v128, v136\n\t" \
  "v_mfma_f32_16x16x32_bf16 a[124:127], a[188:191], v[60:63], a[124:127]\n\t" \
  "v_fma_f32 v177, v177, v129, v137\n\t" \
  "v_fma_f32 v178, v178, v130, v138\n\t" \
  "v_fma_f32 v179, v179, v131, v139\n\t" \
  "v_mfma_f32_16x16x32_bf16 a[120:123], a[188:191], v[56:59], a[120:123]\n\t" \
  "v_fma_f32 v180, v180, v132, v140\n\t" \
  "v_fma_f32 v181, v181, v133, v141\n\t" \
  "v_fma_f32 v182, v182, v134, v142\n\t" \
  "v_mfma_f32_16x16x32_bf16 a[116:119], a[188:191], v[52:55], a[116:119]\n\t" \
  "v_fma_f32 v183, v183, v135, v143\n\t" \
  "v_cvt_pk_bf16_f32 v96, v176, v177\n\t" \
  "v_cvt_pk_bf16_f32 v97, v178, v179\n\t" \
  "v_cvt_pk_bf16_f32 v98, v180, v181\n\t" \
  "v_mfma_f32_16x16x32_bf16 a[112:115], a[188:191], v[48:51], a[112:115]\n\t" \
  FSTP("v_writelane_b32 %[stv], s94, 42\n\t") \
  FSTP("v_writelane_b32 %[stv], s92, 43\n\t") \
  FSTP("s_memrealtime s[94:95]\n\t") \
  FSTP("s_memtime s[92:93]\n\t") \
  "s_waitcnt lgkmcnt(3)\n\t" \
  "v_mfma_f32_16x16x32_bf16 a[0:3], a[128:131], v[64:67], a[0:3]\n\t" \
  "s_waitcnt vmcnt(5)\n\t" \
  "v_cmp_ne_u32 vcc, v211, %[fl]\n\t" \
  "s_cbranch_vccnz .Lfq_s\n\t" \
  ".Lfq_k:\n\t" \
  "v_mfma_f32_16x16x32_bf16 a[4:7], a[128:131], v[68:71], a[4:7]\n\t" \
  "s_add_u32 s97, %[rot19], 2097152\n\t" \
  "ds_read_b64_tr_b16 a[160:161], v192 offset:8192\n\t" \
  "v_cvt_pk_bf16_f32 v99, v182, v183\n\t" \
  "v_lshlrev_b32 v176, 16, v100\n\t" \
  "v_and_b32 v177, s98, v100\n\t" \
  "v_mfma_f32_16x16x32_bf16 a[8:11], a[128:131], v[72:75], a[8:11]\n\t" \
  "s_bfe_u32 s97, s97, 0x140003\n\t" \
  "ds_read_b64_tr_b16 a[162:163], v192 offset:9216\n\t" \
  "v_lshlrev_b32 v178, 16, v101\n\t" \
  "v_and_b32 v179, s98, v101\n\t" \
  "v_lshlrev_b32 v180, 16, v102\n\t" \
  "v_mfma_f32_16x16x32_bf16 a[12:15], a[128:131], v[76:79], a[12:15]\n\t" \
  "s_add_u32 s97, s97, %[sox]\n\t" \
  "ds_read_b64_tr_b16 a[164:165], v193 offset:8192\n\t" \
  "v_and_b32 v181, s98, v102\n\t" \
  "v_lshlrev_b32 v182, 16, v103\n\t" \
  "v_and_b32 v183, s98, v103\n\t" \
  "v_mfma_f32_16x16x32_bf16 a[28:31], a[132:135], v[76:79], a[28:31]\n\t" \
  "buffer_load_dwordx4 a[192:195], v248, %[ws], s97 offen sc1\n\t" \
  "v_fma_f32 v176, v176, v164, v166\n\t" \
  "v_fma_f32 v177, v177, v164, v166\n\t" \
  "v_mfma_f32_16x16x32_bf16 a[24:27], a[132:135], v[72:75], a[24:27]\n\t" \
  "buffer_load_dwordx4 a[196:199], v249, %[ws], s97 offen sc1\n\t" \
  "v_fma_f32 v178, v178, v164, v166\n\t" \
  "v_fma_f32 v179, v179, v164, v166\n\t" \
  "v_mfma_f32_16x16x32_bf16 a[20:23], a[132:135], v[68:71], a[20:23]\n\t" \
  "ds_read_b64_tr_b16 a[166:167], v193 offset:9216\n\t" \
  "v_fma_f32 v180, v180, v164, v166\n\t" \
  "v_fma_f32 v181, v181, v164, v166\n\t" \
  "v_fma_f32 v182, v182, v164, v166\n\t" \
  "v_mfma_f32_16x16x32_bf16 a[16:19], a[132:135], v[64:67], a[16:19]\n\t" \
  "ds_read_b64_tr_b16 a[168:169], v194 offset:8192\n\t" \
  "v_fma_f32 v183, v183, v164, v166\n\t" \
  "v_fma_f32 v176, v176, v128, v136\n\t" \
  "v_fma_f32 v177, v177, v129, v137\n\t" \
  "v_mfma_f32_16x16x32_bf16 a[32:35], a[136:139], v[64:67], a[32:35]\n\t" \
  "buffer_load_dwordx4 a[200:203], v250, %[ws], s97 offen sc1\n\t" \
  ".Lhs1:\n\ts_cbranch_execz .Lhs1n\n\t" \
  "v_fma_f32 v178, v178, v130, v138\n\t" \
  "v_fma_f32 v179, v179, v131, v139\n\t" \
  "v_mfma_f32_16x16x32_bf16 a[36:39], a[136:139], v[68:71], a[36:39]\n\t" \
  "ds_read_b64_tr_b16 a[170:171], v194 offset:9216\n\t" \
  "v_fma_f32 v180, v180, v132, v140\n\t" \
  "v_fma_f32 v181, v181, v133, v141\n\t" \
  "v_mfma_f32_16x16x32_bf16 a[40:43], a[136:139], v[72:75], a[40:43]\n\t" \
  "ds_read_b64_tr_b16 a[172:173], v195 offset:8192\n\t" \
  "v_fma_f32 v182, v182, v134, v142\n\t" \
  "v_fma_f32 v183, v183, v135, v143\n\t" \
  "v_mfma_f32_16x16x32_bf16 a[44:47], a[136:139], v[76:79], a[44:47]\n\t" \
  "buffer_load_dwordx4 a[204:207], v251, %[ws], s97 offen sc1\n\t" \
  "v_cvt_pk_bf16_f32 v100, v176, v177\n\t" \
  "v_mfma_f32_16x16x32_bf16 a[60:63], a[140:143], v[76:79], a[60:63]\n\t" \
  "s_add_u32 s97, s97, %[dk]\n\t" \
  "ds_read_b64_tr_b16 a[174:175], v195 offset:9216\n\t" \
  "v_cvt_pk_bf16_f32 v101, v178, v179\n\t" \
  "v_cvt_pk_bf16_f32 v102, v180, v181\n\t" \
  "v_mfma_f32_16x16x32_bf16 a[56:59], a[140:143], v[72:75], a[56:59]\n\t" \
  "ds_read_b64_tr_b16 a[176:177], v196 offset:8192\n\t" \
  "v_cvt_pk_bf16_f32 v103, v182, v183\n\t" \
  "v_lshlrev_b32 v176, 16, v104\n\t" \
  "v_mfma_f32_16x16x32_bf16 a[52:55], a[140:143], v[68:71], a[52:55]\n\t" \
  "buffer_load_dwordx4 a[208:211], v248, %[ws], s97 offen sc1\n\t" \
  "v_and_b32 v177, s98, v104\n\t" \
  "v_mfma_f32_16x16x32_bf16 a[48:51], a[140:143], v[64:67], a[48:51]\n\t" \
  "ds_read_b64_tr_b16 a[178:179], v196 offset:9216\n\t" \
  "v_lshlrev_b32 v178, 16, v105\n\t" \
  "v_and_b32 v179, s98, v105\n\t" \
  "v_mfma_f32_16x16x32_bf16 a[64:67], a[144:147], v[64:67], a[64:67]\n\t" \
  "ds_read_b64_tr_b16 a[180:181], v197 offset:8192\n\t" \
  "v_lshlrev_b32 v180, 16, v106\n\t" \
  "v_and_b32 v181, s98, v106\n\t" \
  "v_mfma_f32_16x16x32_bf16 a[68:71], a[144:147], v[68:71], a[68:71]\n\t" \
  "buffer_load_dwordx4 a[212:215], v249, %[ws], s97 offen sc1\n\t" \
  "v_lshlrev_b32 v182, 16, v107\n\t" \
  "v_mfma_f32_16x16x32_bf16 a[72:75], a[144:147], v[72:75], a[72:75]\n\t" \
  "ds_read_b64_tr_b16 a[182:183], v197 offset:9216\n\t" \
  "v_and_b32 v183, s98, v107\n\t" \
  "v_fma_f32 v176, v176, v168, v170\n\t" \
  "v_mfma_f32_16x16x32_bf16 a[76:79], a[144:147], v[76:79], a[76:79]\n\t" \
  "ds_read_b64_tr_b16 a[184:185], v198 offset:8192\n\t" \
  "v_fma_f32 v177, v177, v168, v170\n\t" \
  "v_fma_f32 v178, v178, v168, v170\n\t" \
  "v_mfma_f32_16x16x32_bf16 a[92:95], a[148:151], v[76:79], a[92:95]\n\t" \
  "ds_read_b64_tr_b16 a[186:187], v198 offset:9216\n\t" \
  "v_fma_f32 v179, v179, v168, v170\n\t" \
  "v_fma_f32 v180, v180, v168, v170\n\t" \
  "v_mfma_f32_16x16x32_bf16 a[88:91], a[148:151], v[72:75], a[88:91]\n\t" \
  "buffer_load_dwordx4 a[216:219], v250, %[ws], s97 offen sc1\n\t" \
  "v_fma_f32 v181, v181, v168, v170\n\t" \
  "v_mfma_f32_16x16x32_bf16 a[84:87], a[148:151], v[68:71], a[84:87]\n\t" \
  "ds_read_b64_tr_b16 a[188:189], v199 offset:8192\n\t" \
  "v_fma_f32 v182, v182, v168, v170\n\t" \
  "v_fma_f32 v183, v183, v168, v170\n\t" \
  "v_mfma_f32_16x16x32_bf16 a[80:83], a[148:151], v[64:67], a[80:83]\n\t" \
  "ds_read_b64_tr_b16 a[190:191], v199 offset:9216\n\t" \
  "v_fma_f32 v176, v176, v128, v136\n\t" \
  "v_fma_f32 v177, v177, v129, v137\n\t" \
  "v_mfma_f32_16x16x32_bf16 a[96:99], a[152:155], v[64:67], a[96:99]\n\t" \
  "buffer_load_dwordx4 a[220:223], v251, %[ws], s97 offen sc1\n\t" \
  "v_fma_f32 v178, v178, v130, v138\n\t" \
  "v_mfma_f32_16x16x32_bf16 a[100:103], a[152:155], v[68:71], a[100:103]\n\t" \
  "v_fma_f32 v179, v179, v131, v139\n\t" \
  "v_fma_f32 v180, v180, v132, v140\n\t" \
  "v_fma_f32 v181, v181, v133, v141\n\t" \
  "v_mfma_f32_16x16x32_bf16 a[104:107], a[152:155], v[72:75], a[104:107]\n\t" \
  "v_fma_f32 v182, v182, v134, v142\n\t" \
  "v_fma_f32 v183, v183, v135, v143\n\t" \
  "v_cvt_pk_bf16_f32 v104, v176, v177\n\t" \
  "v_mfma_f32_16x16x32_bf16 a[108:111], a[152:155], v[76:79], a[108:111]\n\t" \
  "v_cvt_pk_bf16_f32 v105, v178, v179\n\t" \
  "v_cvt_pk_bf16_f32 v106, v180, v181\n\t" \
  "v_cvt_pk_bf16_f32 v107, v182, v183\n\t" \
  "v_mfma_f32_16x16x32_bf16 a[124:127], a[156:159], v[76:79], a[124:127]\n\t" \
  "ds_read_b128 v[112:115], v205 offset:0\n\t" \
  "ds_read_b128 v[116:119], v205 offset:2048\n\t" \
  "v_mfma_f32_16x16x32_bf16 a[120:123], a[156:159], v[72:75], a[120:123]\n\t" \
  "ds_read_b128 v[120:123], v205 offset:4096\n\t" \
  "ds_read_b128 v[124:127], v205 offset:6144\n\t" \
  "v_lshlrev_b32 v176, 16, v108\n\t" \
  "v_and_b32 v177, s98, v108\n\t" \
  "v_mfma_f32_16x16x32_bf16 a[116:119], a[156:159], v[68:71], a[116:119]\n\t" \
  "v_lshlrev_b32 v178, 16, v109\n\t" \
  "v_and_b32 v179, s98, v109\n\t" \
  "v_lshlrev_b32 v180, 16, v110\n\t" \
  "v_and_b32 v181, s98, v110\n\t" \
  "v_mfma_f32_16x16x32_bf16 a[112:115], a[156:159], v[64:67], a[112:115]\n\t" \
  FSTP("v_writelane_b32 %[stv], s94, 44\n\t") \
  FSTP("v_writelane_b32 %[stv], s92, 45\n\t") \
  FSTP("s_memrealtime s[94:95]\n\t") \
  FSTP("s_memtime s[92:93]\n\t") \
  "s_waitcnt lgkmcnt(4)\n\t" \
  "s_barrier\n\t" \
  "v_mfma_f32_16x16x32_bf16 a[0:3], a[160:163], v[80:83], a[0:3]\n\t" \
  "s_add_u32 s96, %[rot19], 2621440\n\t" \
  "ds_read_b64_tr_b16 a[128:129], v184 offset:0\n\t" \
  "v_lshlrev_b32 v182, 16, v111\n\t" \
  "v_and_b32 v183, s98, v111\n\t" \
  "v_fma_f32 v176, v176, v172, v174\n\t" \
  "v_mfma_f32_16x16x32_bf16 a[4:7], a[160:163], v[84:87], a[4:7]\n\t" \
  "s_and_b32 s96, s96, 0x780000\n\t" \
  "ds_read_b64_tr_b16 a[130:131], v184 offset:1024\n\t" \
  "v_fma_f32 v177, v177, v172, v174\n\t" \
  "v_fma_f32 v178, v178, v172, v174\n\t" \
  "v_fma_f32 v179, v179, v172, v174\n\t" \
  "v_mfma_f32_16x16x32_bf16 a[8:11], a[160:163], v[88:91], a[8:11]\n\t" \
  "s_add_u32 m0, %[mw], 98304\n\t" \
  "ds_read_b64_tr_b16 a[132:133], v185 offset:0\n\t" \
  "v_fma_f32 v180, v180, v172, v174\n\t" \
  "v_fma_f32 v181, v181, v172, v174\n\t" \
  "v_fma_f32 v182, v182, v172, v174\n\t" \
  "v_mfma_f32_16x16x32_bf16 a[12:15], a[160:163], v[92:95], a[12:15]\n\t" \
  "buffer_load_dwordx4 v200, %[sw], s96 offen offset:0 lds\n\t" \
  "v_fma_f32 v183, v183, v172, v174\n\t" \
  "v_fma_f32 v176, v176, v128, v136\n\t" \
  "v_mfma_f32_16x16x32_bf16 a[28:31], a[164:167], v[92:95], a[28:31]\n\t" \
  "buffer_load_dwordx4 v201, %[sw], s96 offen offset:1024 lds\n\t" \
  "v_fma_f32 v177, v177, v129, v137\n\t" \
  "v_fma_f32 v178, v178, v130, v138\n\t" \
  "v_mfma_f32_16x16x32_bf16 a[24:27], a[164:167], v[88:91], a[24:27]\n\t" \
  "ds_read_b64_tr_b16 a[134:135], v185 offset:1024\n\t" \
  "v_fma_f32 v179, v179, v131, v139\n\t" \
  "v_fma_f32 v180, v180, v132, v140\n\t" \
  "v_mfma_f32_16x16x32_bf16 a[20:23], a[164:167], v[84:87], a[20:23]\n\t" \
  "ds_read_b64_tr_b16 a[136:137], v186 offset:0\n\t" \
  "v_fma_f32 v181, v181, v133, v141\n\t" \
  "v_fma_f32 v182, v182, v134, v142\n\t" \
  "v_mfma_f32_16x16x32_bf16 a[16:19], a[164:167], v[80:83], a[16:19]\n\t" \
  "buffer_load_dwordx4 v202, %[sw], s96 offen offset:2048 lds\n\t" \
  "v_fma_f32 v183, v183, v135, v143\n\t" \
  "v_mfma_f32_16x16x32_bf16 a[32:35], a[168:171], v[80:83], a[32:35]\n\t" \
  "ds_read_b64_tr_b16 a[138:139], v186 offset:1024\n\t" \
  "v_cvt_pk_bf16_f32 v108, v176, v177\n\t" \
  "v_cvt_pk_bf16_f32 v109, v178, v179\n\t" \
  "v_mfma_f32_16x16x32_bf16 a[36:39], a[168:171], v[84:87], a[36:39]\n\t" \
  "ds_read_b64_tr_b16 a[140:141], v187 offset:0\n\t" \
  "v_cvt_pk_bf16_f32 v110, v180, v181\n\t" \
  "v_cvt_pk_bf16_f32 v111, v182, v183\n\t" \
  "s_waitcnt lgkmcnt(7)\n\t" \
  "v_mfma_f32_16x16x32_bf16 a[40:43], a[168:171], v[88:91], a[40:43]\n\t" \
  "buffer_load_dwordx4 v203, %[sw], s96 offen offset:3072 lds\n\t" \
  "v_lshlrev_b32 v176, 16, v112\n\t" \
  "v_mfma_f32_16x16x32_bf16 a[44:47], a[168:171], v[92:95], a[44:47]\n\t" \
  "s_add_u32 s97, %[rot19], 2621440\n\t" \
  "ds_read_b64_tr_b16 a[142:143], v187 offset:1024\n\t" \
  "v_and_b32 v177, s98, v112\n\t" \
  "v_lshlrev_b32 v178, 16, v113\n\t" \
  "v_mfma_f32_16x16x32_bf16 a[60:63], a[172:175], v[92:95], a[60:63]\n\t" \
  "s_bfe_u32 s97, s97, 0x140003\n\t" \
  "ds_read_b64_tr_b16 a[144:145], v188 offset:0\n\t" \
  "v_and_b32 v179, s98, v113\n\t" \
  "v_lshlrev_b32 v180, 16, v114\n\t" \
  "v_mfma_f32_16x16x32_bf16 a[56:59], a[172:175], v[88:91], a[56:59]\n\t" \
  "s_add_u32 s97, s97, %[sox]\n\t" \
  "ds_read_b64_tr_b16 a[146:147], v188 offset:1024\n\t" \
  "v_and_b32 v181, s98, v114\n\t" \
  "v_lshlrev_b32 v182, 16, v115\n\t" \
  "v_mfma_f32_16x16x32_bf16 a[52:55], a[172:175], v[84:87], a[52:55]\n\t" \
  "buffer_load_dwordx4 a[224:227], v248, %[ws], s97 offen sc1\n\t" \
  "v_and_b32 v183, s98, v115\n\t" \
  "v_mfma_f32_16x16x32_bf16 a[48:51], a[172:175], v[80:83], a[48:51]\n\t" \
  "ds_read_b64_tr_b16 a[148:149], v189 offset:0\n\t" \
  "v_fma_f32 v176, v176, v160, v162\n\t" \
  "v_fma_f32 v177, v177, v160, v162\n\t" \
  "v_mfma_f32_16x16x32_bf16 a[64:67], a[176:179], v[80:83], a[64:67]\n\t" \
  "ds_read_b64_tr_b16 a[150:151], v189 offset:1024\n\t" \
  "v_fma_f32 v178, v178, v160, v162\n\t" \
  "v_fma_f32 v179, v179, v160, v162\n\t" \
  "v_mfma_f32_16x16x32_bf16 a[68:71], a[176:179], v[84:87], a[68:71]\n\t" \
  "buffer_load_dwordx4 a[228:231], v249, %[ws], s97 offen sc1\n\t" \
  "v_fma_f32 v180, v180, v160, v162\n\t" \
  "v_mfma_f32_16x16x32_bf16 a[72:75], a[176:179], v[88:91], a[72:75]\n\t" \
  "ds_read_b64_tr_b16 a[152:153], v190 offset:0\n\t" \
  "v_fma_f32 v181, v181, v160, v162\n\t" \
  "v_fma_f32 v182, v182, v160, v162\n\t" \
  "v_mfma_f32_16x16x32_bf16 a[76:79], a[176:179], v[92:95], a[76:79]\n\t" \
  "ds_read_b64_tr_b16 a[154:155], v190 offset:1024\n\t" \
  "v_fma_f32 v183, v183, v160, v162\n\t" \
  "v_fma_f32 v176, v176, v144, v152\n\t" \
  "v_mfma_f32_16x16x32_bf16 a[92:95], a[180:183], v[92:95], a[92:95]\n\t" \
  "ds_read_b64_tr_b16 a[156:157], v191 offset:0\n\t" \
  "v_fma_f32 v177, v177, v145, v153\n\t" \
  "v_fma_f32 v178, v178, v146, v154\n\t" \
  "v_mfma_f32_16x16x32_bf16 a[88:91], a[180:183], v[88:91], a[88:91]\n\t" \
  "buffer_load_dwordx4 a[232:235], v250, %[ws], s97 offen sc1\n\t" \
  "v_fma_f32 v179, v179, v147, v155\n\t" \
  "v_mfma_f32_16x16x32_bf16 a[84:87], a[180:183], v[84:87], a[84:87]\n\t" \
  "ds_read_b64_tr_b16 a[158:159], v191 offset:1024\n\t" \
  "v_fma_f32 v180, v180, v148, v156\n\t" \
  "v_fma_f32 v181, v181, v149, v157\n\t" \
  "v_mfma_f32_16x16x32_bf16 a[80:83], a[180:183], v[80:83], a[80:83]\n\t" \
  "v_fma_f32 v182, v182, v150, v158\n\t" \
  "v_fma_f32 v183, v183, v151, v159\n\t" \
  "v_cvt_pk_bf16_f32 v112, v176, v177\n\t" \
  "v_mfma_f32_16x16x32_bf16 a[96:99], a[184:187], v[80:83], a[96:99]\n\t" \
  "buffer_load_dwordx4 a[236:239], v251, %[ws], s97 offen sc1\n\t" \
  "v_cvt_pk_bf16_f32 v113, v178, v179\n\t" \
  "v_mfma_f32_16x16x32_bf16 a[100:103], a[184:187], v[84:87], a[100:103]\n\t" \
  "v_cvt_pk_bf16_f32 v114, v180, v181\n\t" \
  "v_cvt_pk_bf16_f32 v115, v182, v183\n\t" \
  "v_lshlrev_b32 v176, 16, v116\n\t" \
  "v_mfma_f32_16x16x32_bf16 a[104:107], a[184:187], v[88:91], a[104:107]\n\t" \
  "v_and_b32 v177, s98, v116\n\t" \
  "v_lshlrev_b32 v178, 16, v117\n\t" \
  "v_and_b32 v179, s98, v117\n\t" \
  "v_mfma_f32_16x16x32_bf16 a[108:111], a[184:187], v[92:95], a[108:111]\n\t" \
  "v_lshlrev_b32 v180, 16, v118\n\t" \
  "v_and_b32 v181, s98, v118\n\t" \
  "v_lshlrev_b32 v182, 16, v119\n\t" \
  "v_mfma_f32_16x16x32_bf16 a[124:127], a[188:191], v[92:95], a[124:127]\n\t" \
  "v_and_b32 v183, s98, v119\n\t" \
  "v_fma_f32 v176, v176, v164, v166\n\t" \
  "v_fma_f32 v177, v177, v164, v166\n\t" \
  "v_mfma_f32_16x16x32_bf16 a[120:123], a[188:191], v[88:91], a[120:123]\n\t" \
  "v_fma_f32 v178, v178, v164, v166\n\t" \
  "v_fma_f32 v179, v179, v164, v166\n\t" \
  "v_fma_f32 v180, v180, v164, v166\n\t" \
  "v_mfma_f32_16x16x32_bf16 a[116:119], a[188:191], v[84:87], a[116:119]\n\t" \
  "v_fma_f32 v181, v181, v164, v166\n\t" \
  "v_fma_f32 v182, v182, v164, v166\n\t" \
  "v_fma_f32 v183, v183, v164, v166\n\t" \
  "v_fma_f32 v176, v176, v144, v152\n\t" \
  "v_mfma_f32_16x16x32_bf16 a[112:115], a[188:191], v[80:83], a[112:115]\n\t" \
  FSTP("v_writelane_b32 %[stv], s94, 46\n\t") \
  FSTP("v_writelane_b32 %[stv], s92, 47\n\t") \
  FSTP("s_memrealtime s[94:95]\n\t") \
  FSTP("s_memtime s[92:93]\n\t") \
  "s_waitcnt lgkmcnt(0)\n\t" \
  "v_mfma_f32_16x16x32_bf16 a[0:3], a[128:131], v[96:99], a[0:3]\n\t" \
  "s_add_u32 s96, %[rot19], 3145728\n\t" \
  "ds_read_b64_tr_b16 a[160:161], v184 offset:8192\n\t" \
  "v_fma_f32 v177, v177, v145, v153\n\t" \
  "v_fma_f32 v178, v178, v146, v154\n\t" \
  "v_fma_f32 v179, v179, v147, v155\n\t" \
  "v_mfma_f32_16x16x32_bf16 a[4:7], a[128:131], v[100:103], a[4:7]\n\t" \
  "s_and_b32 s96, s96, 0x780000\n\t" \
  "ds_read_b64_tr_b16 a[162:163], v184 offset:9216\n\t" \
  "v_fma_f32 v180, v180, v148, v156\n\t" \
  "v_fma_f32 v181, v181, v149, v157\n\t" \
  "v_fma_f32 v182, v182, v150, v158\n\t" \
  "v_mfma_f32_16x16x32_bf16 a[8:11], a[128:131], v[104:107], a[8:11]\n\t" \
  "s_add_u32 m0, %[mw], 16384\n\t" \
  "ds_read_b64_tr_b16 a[164:165], v185 offset:8192\n\t" \
  "v_fma_f32 v183, v183, v151, v159\n\t" \
  "v_cvt_pk_bf16_f32 v116, v176, v177\n\t" \
  "v_cvt_pk_bf16_f32 v117, v178, v179\n\t" \
  "v_mfma_f32_16x16x32_bf16 a[12:15], a[128:131], v[108:111], a[12:15]\n\t" \
  "buffer_load_dwordx4 v200, %[sw], s96 offen offset:0 lds\n\t" \
  "v_cvt_pk_bf16_f32 v118, v180, v181\n\t" \
  "v_mfma_f32_16x16x32_bf16 a[28:31], a[132:135], v[108:111], a[28:31]\n\t" \
  "ds_read_b64_tr_b16 a[166:167], v185 offset:9216\n\t" \
  "v_cvt_pk_bf16_f32 v119, v182, v183\n\t" \
  "v_lshlrev_b32 v176, 16, v120\n\t" \
  "v_mfma_f32_16x16x32_bf16 a[24:27], a[132:135], v[104:107], a[24:27]\n\t" \
  "ds_read_b64_tr_b16 a[168:169], v186 offset:8192\n\t" \
  "v_and_b32 v177, s98, v120\n\t" \
  "v_lshlrev_b32 v178, 16, v121\n\t" \
  "v_mfma_f32_16x16x32_bf16 a[20:23], a[132:135], v[100:103], a[20:23]\n\t" \
  "ds_read_b64_tr_b16 a[170:171], v186 offset:9216\n\t" \
  "v_and_b32 v179, s98, v121\n\t" \
  "v_lshlrev_b32 v180, 16, v122\n\t" \
  "v_mfma_f32_16x16x32_bf16 a[16:19], a[132:135], v[96:99], a[16:19]\n\t" \
  "buffer_load_dwordx4 v201, %[sw], s96 offen offset:1024 lds\n\t" \
  "v_and_b32 v181, s98, v122\n\t" \
  "v_mfma_f32_16x16x32_bf16 a[32:35], a[136:139], v[96:99], a[32:35]\n\t" \
  "ds_read_b64_tr_b16 a[172:173], v187 offset:8192\n\t" \
  "v_lshlrev_b32 v182, 16, v123\n\t" \
  "v_and_b32 v183, s98, v123\n\t" \
  "v_mfma_f32_16x16x32_bf16 a[36:39], a[136:139], v[100:103], a[36:39]\n\t" \
  "ds_read_b64_tr_b16 a[174:175], v187 offset:9216\n\t" \
  "v_fma_f32 v176, v176, v168, v170\n\t" \
  "v_fma_f32 v177, v177, v168, v170\n\t" \
  "v_mfma_f32_16x16x32_bf16 a[40:43], a[136:139], v[104:107], a[40:43]\n\t" \
  "ds_read_b64_tr_b16 a[176:177], v188 offset:8192\n\t" \
  "v_fma_f32 v178, v178, v168, v170\n\t" \
  "v_fma_f32 v179, v179, v168, v170\n\t" \
  "v_mfma_f32_16x16x32_bf16 a[44:47], a[136:139], v[108:111], a[44:47]\n\t" \
  "ds_read_b64_tr_b16 a[178:179], v188 offset:9216\n\t" \
  "v_fma_f32 v180, v180, v168, v170\n\t" \
  "v_fma_f32 v181, v181, v168, v170\n\t" \
  "v_mfma_f32_16x16x32_bf16 a[60:63], a[140:143], v[108:111], a[60:63]\n\t" \
  "ds_read_b64_tr_b16 a[180:181], v189 offset:8192\n\t" \
  "v_fma_f32 v182, v182, v168, v170\n\t" \
  "v_fma_f32 v183, v183, v168, v170\n\t" \
  "v_mfma_f32_16x16x32_bf16 a[56:59], a[140:143], v[104:107], a[56:59]\n\t" \
  "ds_read_b64_tr_b16 a[182:183], v189 offset:9216\n\t" \
  "v_fma_f32 v176, v176, v144, v152\n\t" \
  "v_fma_f32 v177, v177, v145, v153\n\t" \
  "v_mfma_f32_16x16x32_bf16 a[52:55], a[140:143], v[100:103], a[52:55]\n\t" \
  "buffer_load_dwordx4 v202, %[sw], s96 offen offset:2048 lds\n\t" \
  "v_fma_f32 v178, v178, v146, v154\n\t" \
  "v_mfma_f32_16x16x32_bf16 a[48:51], a[140:143], v[96:99], a[48:51]\n\t" \
  "ds_read_b64_tr_b16 a[184:185], v190 offset:8192\n\t" \
  "v_fma_f32 v179, v179, v147, v155\n\t" \
  "v_fma_f32 v180, v180, v148, v156\n\t" \
  "v_mfma_f32_16x16x32_bf16 a[64:67], a[144:147], v[96:99], a[64:67]\n\t" \
  "ds_read_b64_tr_b16 a[186:187], v190 offset:9216\n\t" \
  "v_fma_f32 v181, v181, v149, v157\n\t" \
  "v_fma_f32 v182, v182, v150, v158\n\t" \
  "v_mfma_f32_16x16x32_bf16 a[68:71], a[144:147], v[100:103], a[68:71]\n\t" \
  "ds_read_b64_tr_b16 a[188:189], v191 offset:8192\n\t" \
  "v_fma_f32 v183, v183, v151, v159\n\t" \
  "v_cvt_pk_bf16_f32 v120, v176, v177\n\t" \
  "v_mfma_f32_16x16x32_bf16 a[72:75], a[144:147], v[104:107], a[72:75]\n\t" \
  "ds_read_b64_tr_b16 a[190:191], v191 offset:9216\n\t" \
  "v_cvt_pk_bf16_f32 v121, v178, v179\n\t" \
  "v_cvt_pk_bf16_f32 v122, v180, v181\n\t" \
  "v_mfma_f32_16x16x32_bf16 a[76:79], a[144:147], v[108:111], a[76:79]\n\t" \
  "v_cvt_pk_bf16_f32 v123, v182, v183\n\t" \
  "v_lshlrev_b32 v176, 16, v124\n\t" \
  "v_and_b32 v177, s98, v124\n\t" \
  "v_mfma_f32_16x16x32_bf16 a[92:95], a[148:151], v[108:111], a[92:95]\n\t" \
  "v_lshlrev_b32 v178, 16, v125\n\t" \
  "v_and_b32 v179, s98, v125\n\t" \
  "v_lshlrev_b32 v180, 16, v126\n\t" \
  "v_mfma_f32_16x16x32_bf16 a[88:91], a[148:151], v[104:107], a[88:91]\n\t" \
  "buffer_load_dwordx4 v203, %[sw], s96 offen offset:3072 lds\n\t" \
  "v_and_b32 v181, s98, v126\n\t" \
  "v_mfma_f32_16x16x32_bf16 a[84:87], a[148:151], v[100:103], a[84:87]\n\t" \
  "v_lshlrev_b32 v182, 16, v127\n\t" \
  "v_and_b32 v183, s98, v127\n\t" \
  "v_fma_f32 v176, v176, v172, v174\n\t" \
  "v_mfma_f32_16x16x32_bf16 a[80:83], a[148:151], v[96:99], a[80:83]\n\t" \
  "v_fma_f32 v177, v177, v172, v174\n\t" \
  "v_fma_f32 v178, v178, v172, v174\n\t" \
  "v_fma_f32 v179, v179, v172, v174\n\t" \
  "v_mfma_f32_16x16x32_bf16 a[96:99], a[152:155], v[96:99], a[96:99]\n\t" \
  "v_mbcnt_lo_u32_b32 v217, -1, 0\n\t" \
  "v_mbcnt_hi_u32_b32 v217, -1, v217\n\t" \
  "v_cmp_gt_u32 vcc, 16, v217\n\t" \
  "v_mfma_f32_16x16x32_bf16 a[100:103], a[152:155], v[100:103], a[100:103]\n\t" \
  "v_lshlrev_b32 v217, 7, v217\n\t" \
  "v_bfrev_b32 v215, 1\n\t" \
  "v_cndmask_b32 v217, v215, v217, vcc\n\t" \
  "v_mfma_f32_16x16x32_bf16 a[104:107], a[152:155], v[104:107], a[104:107]\n\t" \
  "v_fma_f32 v180, v180, v172, v174\n\t" \
  "v_fma_f32 v181, v181, v172, v174\n\t" \
  "v_fma_f32 v182, v182, v172, v174\n\t" \
  "v_mfma_f32_16x16x32_bf16 a[108:111], a[152:155], v[108:111], a[108:111]\n\t" \
  "v_fma_f32 v183, v183, v172, v174\n\t" \
  "v_fma_f32 v176, v176, v144, v152\n\t" \
  "v_fma_f32 v177, v177, v145, v153\n\t" \
  "v_mfma_f32_16x16x32_bf16 a[124:127], a[156:159], v[108:111], a[124:127]\n\t" \
  "v_fma_f32 v178, v178, v146, v154\n\t" \
  "v_fma_f32 v179, v179, v147, v155\n\t" \
  "v_fma_f32 v180, v180, v148, v156\n\t" \
  "v_mfma_f32_16x16x32_bf16 a[120:123], a[156:159], v[104:107], a[120:123]\n\t" \
  "v_fma_f32 v181, v181, v149, v157\n\t" \
  "v_fma_f32 v182, v182, v150, v158\n\t" \
  "v_fma_f32 v183, v183, v151, v159\n\t" \
  "v_mfma_f32_16x16x32_bf16 a[116:119], a[156:159], v[100:103], a[116:119]\n\t" \
  "v_cvt_pk_bf16_f32 v124, v176, v177\n\t" \
  "v_cvt_pk_bf16_f32 v125, v178, v179\n\t" \
  "v_cvt_pk_bf16_f32 v126, v180, v181\n\t" \
  "v_cvt_pk_bf16_f32 v127, v182, v183\n\t" \
  "v_mfma_f32_16x16x32_bf16 a[112:115], a[156:159], v[96:99], a[112:115]\n\t" \
  FSTP("v_writelane_b32 %[stv], s94, 48\n\t") \
  FSTP("v_writelane_b32 %[stv], s92, 49\n\t") \
  FSTP("s_memrealtime s[94:95]\n\t") \
  FSTP("s_memtime s[92:93]\n\t") \
  "s_waitcnt vmcnt(21) lgkmcnt(0)\n\t" \
  "s_barrier\n\t" \
  "v_mfma_f32_16x16x32_bf16 a[0:3], a[160:163], v[112:115], a[0:3]\n\t" \
  "s_add_u32 s97, s97, %[dk]\n\t" \
  "ds_read_b64_tr_b16 a[128:129], v184 offset:49152\n\t" \
  "v_mfma_f32_16x16x32_bf16 a[4:7], a[160:163], v[116:119], a[4:7]\n\t" \
  "buffer_load_dwordx4 a[240:243], v248, %[ws], s97 offen sc1\n\t" \
  "v_mfma_f32_16x16x32_bf16 a[8:11], a[160:163], v[120:123], a[8:11]\n\t" \
  "ds_read_b64_tr_b16 a[130:131], v184 offset:50176\n\t" \
  "v_mfma_f32_16x16x32_bf16 a[12:15], a[160:163], v[124:127], a[12:15]\n\t" \
  "buffer_load_dwordx4 a[244:247], v249, %[ws], s97 offen sc1\n\t" \
  "v_mfma_f32_16x16x32_bf16 a[28:31], a[164:167], v[124:127], a[28:31]\n\t" \
  "ds_read_b64_tr_b16 a[132:133], v185 offset:49152\n\t" \
  "v_mfma_f32_16x16x32_bf16 a[24:27], a[164:167], v[120:123], a[24:27]\n\t" \
  "ds_read_b64_tr_b16 a[134:135], v185 offset:50176\n\t" \
  "v_mfma_f32_16x16x32_bf16 a[20:23], a[164:167], v[116:119], a[20:23]\n\t" \
  "buffer_load_dwordx4 a[248:251], v250, %[ws], s97 offen sc1\n\t" \
  "v_mfma_f32_16x16x32_bf16 a[16:19], a[164:167], v[112:115], a[16:19]\n\t" \
  "ds_read_b64_tr_b16 a[136:137], v186 offset:49152\n\t" \
  "v_mfma_f32_16x16x32_bf16 a[32:35], a[168:171], v[112:115], a[32:35]\n\t" \
  "ds_read_b64_tr_b16 a[138:139], v186 offset:50176\n\t" \
  "v_mfma_f32_16x16x32_bf16 a[36:39], a[168:171], v[116:119], a[36:39]\n\t" \
  "buffer_load_dwordx4 a[252:255], v251, %[ws], s97 offen sc1\n\t" \
  "v_mfma_f32_16x16x32_bf16 a[40:43], a[168:171], v[120:123], a[40:43]\n\t" \
  "s_add_u32 s96, %[rot19], 3670016\n\t" \
  "ds_read_b64_tr_b16 a[140:141], v187 offset:49152\n\t" \
  "v_mfma_f32_16x16x32_bf16 a[44:47], a[168:171], v[124:127], a[44:47]\n\t" \
  "s_and_b32 s96, s96, 0x780000\n\t" \
  "ds_read_b64_tr_b16 a[142:143], v187 offset:50176\n\t" \
  "v_mfma_f32_16x16x32_bf16 a[60:63], a[172:175], v[124:127], a[60:63]\n\t" \
  "s_add_u32 m0, %[mw], 32768\n\t" \
  "ds_read_b64_tr_b16 a[144:145], v188 offset:49152\n\t" \
  "v_mfma_f32_16x16x32_bf16 a[56:59], a[172:175], v[120:123], a[56:59]\n\t" \
  "buffer_load_dwordx4 v200, %[sw], s96 offen offset:0 lds\n\t" \
  "v_mfma_f32_16x16x32_bf16 a[52:55], a[172:175], v[116:119], a[52:55]\n\t" \
  "ds_read_b64_tr_b16 a[146:147], v188 offset:50176\n\t" \
  "v_mfma_f32_16x16x32_bf16 a[48:51], a[172:175], v[112:115], a[48:51]\n\t" \
  "ds_read_b64_tr_b16 a[148:149], v189 offset:49152\n\t" \
  "v_mfma_f32_16x16x32_bf16 a[64:67], a[176:179], v[112:115], a[64:67]\n\t" \
  "ds_read_b64_tr_b16 a[150:151], v189 offset:50176\n\t" \
  "v_mfma_f32_16x16x32_bf16 a[68:71], a[176:179], v[116:119], a[68:71]\n\t" \
  "buffer_load_dwordx4 v201, %[sw], s96 offen offset:1024 lds\n\t" \
  "v_mfma_f32_16x16x32_bf16 a[72:75], a[176:179], v[120:123], a[72:75]\n\t" \
  "ds_read_b64_tr_b16 a[152:153], v190 offset:49152\n\t" \
  "v_mfma_f32_16x16x32_bf16 a[76:79], a[176:179], v[124:127], a[76:79]\n\t" \
  "ds_read_b64_tr_b16 a[154:155], v190 offset:50176\n\t" \
  "v_mfma_f32_16x16x32_bf16 a[92:95], a[180:183], v[124:127], a[92:95]\n\t" \
  "ds_read_b64_tr_b16 a[156:157], v191 offset:49152\n\t" \
  "v_mfma_f32_16x16x32_bf16 a[88:91], a[180:183], v[120:123], a[88:91]\n\t" \
  "buffer_load_dwordx4 v202, %[sw], s96 offen offset:2048 lds\n\t" \
  "v_mfma_f32_16x16x32_bf16 a[84:87], a[180:183], v[116:119], a[84:87]\n\t" \
  "ds_read_b64_tr_b16 a[158:159], v191 offset:50176\n\t" \
  "v_mfma_f32_16x16x32_bf16 a[80:83], a[180:183], v[112:115], a[80:83]\n\t" \
  "v_mfma_f32_16x16x32_bf16 a[96:99], a[184:187], v[112:115], a[96:99]\n\t" \
  "v_mfma_f32_16x16x32_bf16 a[100:103], a[184:187], v[116:119], a[100:103]\n\t" \
  "buffer_load_dwordx4 v203, %[sw], s96 offen offset:3072 lds\n\t" \
  "v_mfma_f32_16x16x32_bf16 a[104:107], a[184:187], v[120:123], a[104:107]\n\t" \
  "s_add_u32 s99, %[pf1], 0\n\t" \
  "v_mfma_f32_16x16x32_bf16 a[108:111], a[184:187], v[124:127], a[108:111]\n\t" \
  "buffer_load_dword %[pf], v217, %[wo], s99 offen\n\t" \
  "v_mfma_f32_16x16x32_bf16 a[124:127], a[188:191], v[124:127], a[124:127]\n\t" \
  "v_mfma_f32_16x16x32_bf16 a[120:123], a[188:191], v[120:123], a[120:123]\n\t" \
  "v_mfma_f32_16x16x32_bf16 a[116:119], a[188:191], v[116:119], a[116:119]\n\t" \
  "v_mfma_f32_16x16x32_bf16 a[112:115], a[188:191], v[112:115], a[112:115]\n\t" \
  FSTP("v_writelane_b32 %[stv], s94, 50\n\t") \
  FSTP("v_writelane_b32 %[stv], s92, 51\n\t") \
  FSTP("s_memrealtime s[94:95]\n\t") \
  FSTP("s_memtime s[92:93]\n\t") \
  FSTS("s_memrealtime s[94:95]\n\t") \
  FSTS("s_waitcnt lgkmcnt(0)\n\t") \
  FSTS("v_writelane_b32 %[stv], s94, 14\n\t") \
  "s_waitcnt vmcnt(25) lgkmcnt(0)\n\t" \
  "v_mfma_f32_16x16x32_bf16 a[0:3], a[128:131], a[192:195], a[0:3]\n\t" \
  "s_add_u32 s96, %[rot19], 4194304\n\t" \
  "ds_read_b64_tr_b16 a[160:161], v184 offset:57344\n\t" \
  "v_mfma_f32_16x16x32_bf16 a[4:7], a[128:131], a[196:199], a[4:7]\n\t" \
  "s_and_b32 s96, s96, 0x780000\n\t" \
  "v_mfma_f32_16x16x32_bf16 a[8:11], a[128:131], a[200:203], a[8:11]\n\t" \
  "s_add_u32 m0, %[mw], 0\n\t" \
  "ds_read_b64_tr_b16 a[162:163], v184 offset:58368\n\t" \
  "v_mfma_f32_16x16x32_bf16 a[12:15], a[128:131], a[204:207], a[12:15]\n\t" \
  "buffer_load_dwordx4 v200, %[sw], s96 offen offset:0 lds\n\t" \
  "v_mfma_f32_16x16x32_bf16 a[28:31], a[132:135], a[204:207], a[28:31]\n\t" \
  "ds_read_b64_tr_b16 a[164:165], v185 offset:57344\n\t" \
  "v_mfma_f32_16x16x32_bf16 a[24:27], a[132:135], a[200:203], a[24:27]\n\t" \
  "ds_read_b64_tr_b16 a[166:167], v185 offset:58368\n\t" \
  "v_mfma_f32_16x16x32_bf16 a[20:23], a[132:135], a[196:199], a[20:23]\n\t" \
  "ds_read_b64_tr_b16 a[168:169], v186 offset:57344\n\t" \
  "v_mfma_f32_16x16x32_bf16 a[16:19], a[132:135], a[192:195], a[16:19]\n\t" \
  "buffer_load_dwordx4 v201, %[sw], s96 offen offset:1024 lds\n\t" \
  "v_mfma_f32_16x16x32_bf16 a[32:35], a[136:139], a[192:195], a[32:35]\n\t" \
  "ds_read_b64_tr_b16 a[170:171], v186 offset:58368\n\t" \
  "v_accvgpr_read_b32 v0, a0\n\t" \
  "v_accvgpr_read_b32 v1, a1\n\t" \
  "v_mfma_f32_16x16x32_bf16 a[36:39], a[136:139], a[196:199], a[36:39]\n\t" \
  "ds_read_b64_tr_b16 a[172:173], v187 offset:57344\n\t" \
  "v_accvgpr_read_b32 v2, a2\n\t" \
  "v_mfma_f32_16x16x32_bf16 a[40:43], a[136:139], a[200:203], a[40:43]\n\t" \
  "v_accvgpr_read_b32 v3, a3\n\t" \
  "v_accvgpr_read_b32 v32, a4\n\t" \
  "v_mfma_f32_16x16x32_bf16 a[44:47], a[136:139], a[204:207], a[44:47]\n\t" \
  "ds_read_b64_tr_b16 a[174:175], v187 offset:58368\n\t" \
  "v_accvgpr_read_b32 v33, a5\n\t" \
  "v_mfma_f32_16x16x32_bf16 a[60:63], a[140:143], a[204:207], a[60:63]\n\t" \
  "ds_read_b64_tr_b16 a[176:177], v188 offset:57344\n\t" \
  "v_accvgpr_read_b32 v34, a6\n\t" \
  "v_mfma_f32_16x16x32_bf16 a[56:59], a[140:143], a[200:203], a[56:59]\n\t" \
  "v_accvgpr_read_b32 v35, a7\n\t" \
  "v_accvgpr_read_b32 v64, a8\n\t" \
  "v_mfma_f32_16x16x32_bf16 a[52:55], a[140:143], a[196:199], a[52:55]\n\t" \
  "buffer_load_dwordx4 v202, %[sw], s96 offen offset:2048 lds\n\t" \
  "v_mfma_f32_16x16x32_bf16 a[48:51], a[140:143], a[192:195], a[48:51]\n\t" \
  "ds_read_b64_tr_b16 a[178:179], v188 offset:58368\n\t" \
  "v_accvgpr_read_b32 v65, a9\n\t" \
  "v_mfma_f32_16x16x32_bf16 a[64:67], a[144:147], a[192:195], a[64:67]\n\t" \
  "ds_read_b64_tr_b16 a[180:181], v189 offset:57344\n\t" \
  "v_accvgpr_read_b32 v66, a10\n\t" \
  "v_mfma_f32_16x16x32_bf16 a[68:71], a[144:147], a[196:199], a[68:71]\n\t" \
  "ds_read_b64_tr_b16 a[182:183], v189 offset:58368\n\t" \
  "v_accvgpr_read_b32 v67, a11\n\t" \
  "v_mfma_f32_16x16x32_bf16 a[72:75], a[144:147], a[200:203], a[72:75]\n\t" \
  "ds_read_b64_tr_b16 a[184:185], v190 offset:57344\n\t" \
  "v_accvgpr_read_b32 v96, a12\n\t" \
  "v_mfma_f32_16x16x32_bf16 a[76:79], a[144:147], a[204:207], a[76:79]\n\t" \
  "v_accvgpr_read_b32 v97, a13\n\t" \
  "v_accvgpr_read_b32 v98, a14\n\t" \
  "v_mfma_f32_16x16x32_bf16 a[92:95], a[148:151], a[204:207], a[92:95]\n\t" \
  "ds_read_b64_tr_b16 a[186:187], v190 offset:58368\n\t" \
  "v_accvgpr_read_b32 v99, a15\n\t" \
  "v_mfma_f32_16x16x32_bf16 a[88:91], a[148:151], a[200:203], a[88:91]\n\t" \
  "buffer_load_dwordx4 v203, %[sw], s96 offen offset:3072 lds\n\t" \
  "v_mfma_f32_16x16x32_bf16 a[84:87], a[148:151], a[196:199], a[84:87]\n\t" \
  "ds_read_b64_tr_b16 a[188:189], v191 offset:57344\n\t" \
  "v_accvgpr_read_b32 v100, a28\n\t" \
  "v_mfma_f32_16x16x32_bf16 a[80:83], a[148:151], a[192:195], a[80:83]\n\t" \
  "ds_read_b64_tr_b16 a[190:191], v191 offset:58368\n\t" \
  "v_accvgpr_read_b32 v101, a29\n\t" \
  "v_mfma_f32_16x16x32_bf16 a[96:99], a[152:155], a[192:195], a[96:99]\n\t" \
  "v_accvgpr_read_b32 v102, a30\n\t" \
  "v_accvgpr_read_b32 v103, a31\n\t" \
  "v_mfma_f32_16x16x32_bf16 a[100:103], a[152:155], a[196:199], a[100:103]\n\t" \
  "v_accvgpr_read_b32 v68, a24\n\t" \
  "v_accvgpr_read_b32 v69, a25\n\t" \
  "v_mfma_f32_16x16x32_bf16 a[104:107], a[152:155], a[200:203], a[104:107]\n\t" \
  "v_accvgpr_read_b32 v70, a26\n\t" \
  "v_accvgpr_read_b32 v71, a27\n\t" \
  "v_mfma_f32_16x16x32_bf16 a[108:111], a[152:155], a[204:207], a[108:111]\n\t" \
  "v_accvgpr_read_b32 v36, a20\n\t" \
  "v_accvgpr_read_b32 v37, a21\n\t" \
  "v_mfma_f32_16x16x32_bf16 a[124:127], a[156:159], a[204:207], a[124:127]\n\t" \
  "v_accvgpr_read_b32 v38, a22\n\t" \
  "v_accvgpr_read_b32 v39, a23\n\t" \
  "v_mfma_f32_16x16x32_bf16 a[120:123], a[156:159], a[200:203], a[120:123]\n\t" \
  "v_accvgpr_read_b32 v4, a16\n\t" \
  "v_accvgpr_read_b32 v5, a17\n\t" \
  "v_mfma_f32_16x16x32_bf16 a[116:119], a[156:159], a[196:199], a[116:119]\n\t" \
  "v_accvgpr_read_b32 v6, a18\n\t" \
  "v_accvgpr_read_b32 v7, a19\n\t" \
  "v_mfma_f32_16x16x32_bf16 a[112:115], a[156:159], a[192:195], a[112:115]\n\t" \
  "s_waitcnt vmcnt(21) lgkmcnt(0)\n\t" \
  "s_barrier\n\t" \
  "v_mfma_f32_16x16x32_bf16 a[32:35], a[168:171], a[208:211], a[32:35]\n\t" \
  "s_add_u32 s97, %[rot19], 3145728\n\t" \
  "ds_read_b64_tr_b16 a[128:129], v192 offset:0\n\t" \
  "v_mfma_f32_16x16x32_bf16 a[36:39], a[168:171], a[212:215], a[36:39]\n\t" \
  "s_bfe_u32 s97, s97, 0x140003\n\t" \
  "v_mfma_f32_16x16x32_bf16 a[40:43], a[168:171], a[216:219], a[40:43]\n\t" \
  "s_add_u32 s97, s97, %[sox]\n\t" \
  "ds_read_b64_tr_b16 a[130:131], v192 offset:1024\n\t" \
  "v_mfma_f32_16x16x32_bf16 a[44:47], a[168:171], a[220:223], a[44:47]\n\t" \
  "buffer_load_dwordx4 a[192:195], v248, %[ws], s97 offen sc1\n\t" \
  "v_mfma_f32_16x16x32_bf16 a[60:63], a[172:175], a[220:223], a[60:63]\n\t" \
  "buffer_load_dwordx4 a[196:199], v249, %[ws], s97 offen sc1\n\t" \
  "v_mfma_f32_16x16x32_bf16 a[56:59], a[172:175], a[216:219], a[56:59]\n\t" \
  "ds_read_b64_tr_b16 a[132:133], v193 offset:0\n\t" \
  "v_mfma_f32_16x16x32_bf16 a[52:55], a[172:175], a[212:215], a[52:55]\n\t" \
  "ds_read_b64_tr_b16 a[134:135], v193 offset:1024\n\t" \
  "v_mfma_f32_16x16x32_bf16 a[48:51], a[172:175], a[208:211], a[48:51]\n\t" \
  "buffer_load_dwordx4 a[200:203], v250, %[ws], s97 offen sc1\n\t" \
  "v_mfma_f32_16x16x32_bf16 a[64:67], a[176:179], a[208:211], a[64:67]\n\t" \
  "ds_read_b64_tr_b16 a[136:137], v194 offset:0\n\t" \
  "v_accvgpr_read_b32 v8, a32\n\t" \
  "v_accvgpr_read_b32 v9, a33\n\t" \
  "v_mfma_f32_16x16x32_bf16 a[68:71], a[176:179], a[212:215], a[68:71]\n\t" \
  "ds_read_b64_tr_b16 a[138:139], v194 offset:1024\n\t" \
  "v_accvgpr_read_b32 v10, a34\n\t" \
  "v_accvgpr_read_b32 v11, a35\n\t" \
  "v_mfma_f32_16x16x32_bf16 a[72:75], a[176:179], a[216:219], a[72:75]\n\t" \
  "ds_read_b64_tr_b16 a[140:141], v195 offset:0\n\t" \
  "v_accvgpr_read_b32 v40, a36\n\t" \
  "v_accvgpr_read_b32 v41, a37\n\t" \
  "v_mfma_f32_16x16x32_bf16 a[76:79], a[176:179], a[220:223], a[76:79]\n\t" \
  "buffer_load_dwordx4 a[204:207], v251, %[ws], s97 offen sc1\n\t" \
  "v_accvgpr_read_b32 v42, a38\n\t" \
  "v_mfma_f32_16x16x32_bf16 a[92:95], a[180:183], a[220:223], a[92:95]\n\t" \
  "s_add_u32 s96, %[rot19], 4718592\n\t" \
  "ds_read_b64_tr_b16 a[142:143], v195 offset:1024\n\t" \
  "v_accvgpr_read_b32 v43, a39\n\t" \
  "v_accvgpr_read_b32 v72, a40\n\t" \
  "v_mfma_f32_16x16x32_bf16 a[88:91], a[180:183], a[216:219], a[88:91]\n\t" \
  "s_and_b32 s96, s96, 0x780000\n\t" \
  "ds_read_b64_tr_b16 a[144:145], v196 offset:0\n\t" \
  "v_accvgpr_read_b32 v73, a41\n\t" \
  ".Lhs2:\n\ts_cbranch_execz .Lhs2n\n\t" \
  "v_accvgpr_read_b32 v74, a42\n\t" \
  "v_mfma_f32_16x16x32_bf16 a[84:87], a[180:183], a[212:215], a[84:87]\n\t" \
  "s_add_u32 m0, %[mw], 49152\n\t" \
  "ds_read_b64_tr_b16 a[146:147], v196 offset:1024\n\t" \
  "v_accvgpr_read_b32 v75, a43\n\t" \
  "v_accvgpr_read_b32 v104, a44\n\t" \
  "v_mfma_f32_16x16x32_bf16 a[80:83], a[180:183], a[208:211], a[80:83]\n\t" \
  "buffer_load_dwordx4 v200, %[sw], s96 offen offset:0 lds\n\t" \
  "v_accvgpr_read_b32 v105, a45\n\t" \
  "v_mfma_f32_16x16x32_bf16 a[96:99], a[184:187], a[208:211], a[96:99]\n\t" \
  "ds_read_b64_tr_b16 a[148:149], v197 offset:0\n\t" \
  "v_accvgpr_read_b32 v106, a46\n\t" \
  "v_mfma_f32_16x16x32_bf16 a[100:103], a[184:187], a[212:215], a[100:103]\n\t" \
  "ds_read_b64_tr_b16 a[150:151], v197 offset:1024\n\t" \
  "v_accvgpr_read_b32 v107, a47\n\t" \
  "v_mfma_f32_16x16x32_bf16 a[104:107], a[184:187], a[216:219], a[104:107]\n\t" \
  "ds_read_b64_tr_b16 a[152:153], v198 offset:0\n\t" \
  "v_accvgpr_read_b32 v108, a60\n\t" \
  "v_mfma_f32_16x16x32_bf16 a[108:111], a[184:187], a[220:223], a[108:111]\n\t" \
  "buffer_load_dwordx4 v201, %[sw], s96 offen offset:1024 lds\n\t" \
  "v_mfma_f32_16x16x32_bf16 a[124:127], a[188:191], a[220:223], a[124:127]\n\t" \
  "ds_read_b64_tr_b16 a[154:155], v198 offset:1024\n\t" \
  "v_accvgpr_read_b32 v109, a61\n\t" \
  "v_mfma_f32_16x16x32_bf16 a[120:123], a[188:191], a[216:219], a[120:123]\n\t" \
  "ds_read_b64_tr_b16 a[156:157], v199 offset:0\n\t" \
  "v_accvgpr_read_b32 v110, a62\n\t" \
  "v_mfma_f32_16x16x32_bf16 a[116:119], a[188:191], a[212:215], a[116:119]\n\t" \
  "v_accvgpr_read_b32 v111, a63\n\t" \
  "v_accvgpr_read_b32 v76, a56\n\t" \
  "v_mfma_f32_16x16x32_bf16 a[112:115], a[188:191], a[208:211], a[112:115]\n\t" \
  "buffer_load_dwordx4 v202, %[sw], s96 offen offset:2048 lds\n\t" \
  "v_mfma_f32_16x16x32_bf16 v[0:3], a[160:163], a[208:211], v[0:3]\n\t" \
  "ds_read_b64_tr_b16 a[158:159], v199 offset:1024\n\t" \
  "v_accvgpr_read_b32 v77, a57\n\t" \
  "v_mfma_f32_16x16x32_bf16 v[32:35], a[160:163], a[212:215], v[32:35]\n\t" \
  "v_accvgpr_read_b32 v78, a58\n\t" \
  "v_accvgpr_read_b32 v79, a59\n\t" \
  "v_mfma_f32_16x16x32_bf16 v[64:67], a[160:163], a[216:219], v[64:67]\n\t" \
  "v_accvgpr_read_b32 v44, a52\n\t" \
  "v_accvgpr_read_b32 v45, a53\n\t" \
  "v_mfma_f32_16x16x32_bf16 v[96:99], a[160:163], a[220:223], v[96:99]\n\t" \
  "buffer_load_dwordx4 v203, %[sw], s96 offen offset:3072 lds\n\t" \
  "v_mfma_f32_16x16x32_bf16 v[100:103], a[164:167], a[220:223], v[100:103]\n\t" \
  "v_accvgpr_read_b32 v46, a54\n\t" \
  "v_accvgpr_read_b32 v47, a55\n\t" \
  "v_mfma_f32_16x16x32_bf16 v[68:71], a[164:167], a[216:219], v[68:71]\n\t" \
  "v_accvgpr_read_b32 v12, a48\n\t" \
  "v_accvgpr_read_b32 v13, a49\n\t" \
  "v_mfma_f32_16x16x32_bf16 v[36:39], a[164:167], a[212:215], v[36:39]\n\t" \
  "v_accvgpr_read_b32 v14, a50\n\t" \
  "v_accvgpr_read_b32 v15, a51\n\t" \
  "v_mfma_f32_16x16x32_bf16 v[4:7], a[164:167], a[208:211], v[4:7]\n\t" \
  "s_waitcnt vmcnt(25) lgkmcnt(0)\n\t" \
  "v_mfma_f32_16x16x32_bf16 a[64:67], a[144:147], a[224:227], a[64:67]\n\t" \
  "ds_read_b64_tr_b16 a[160:161], v192 offset:8192\n\t" \
  "v_add_u32 v216, 3145728, v216\n\t" \
  "v_mfma_f32_16x16x32_bf16 a[68:71], a[144:147], a[228:231], a[68:71]\n\t" \
  "v_and_b32 v216, 0x7fffff, v216\n\t" \
  "v_mfma_f32_16x16x32_bf16 a[72:75], a[144:147], a[232:235], a[72:75]\n\t" \
  "global_load_dword %[pf], v216, %[wq]\n\t" \
  "v_mfma_f32_16x16x32_bf16 a[76:79], a[144:147], a[236:239], a[76:79]\n\t" \
  "s_add_u32 s97, s97, %[dk]\n\t" \
  "ds_read_b64_tr_b16 a[162:163], v192 offset:9216\n\t" \
  "v_mfma_f32_16x16x32_bf16 a[92:95], a[148:151], a[236:239], a[92:95]\n\t" \
  "ds_read_b64_tr_b16 a[164:165], v193 offset:8192\n\t" \
  "v_mfma_f32_16x16x32_bf16 a[88:91], a[148:151], a[232:235], a[88:91]\n\t" \
  "buffer_load_dwordx4 a[208:211], v248, %[ws], s97 offen sc1\n\t" \
  "v_mfma_f32_16x16x32_bf16 a[84:87], a[148:151], a[228:231], a[84:87]\n\t" \
  "ds_read_b64_tr_b16 a[166:167], v193 offset:9216\n\t" \
  "v_mfma_f32_16x16x32_bf16 a[80:83], a[148:151], a[224:227], a[80:83]\n\t" \
  "ds_read_b64_tr_b16 a[168:169], v194 offset:8192\n\t" \
  "v_mfma_f32_16x16x32_bf16 a[96:99], a[152:155], a[224:227], a[96:99]\n\t" \
  "ds_read_b64_tr_b16 a[170:171], v194 offset:9216\n\t" \
  "v_accvgpr_read_b32 v16, a64\n\t" \
  "v_accvgpr_read_b32 v17, a65\n\t" \
  "v_mfma_f32_16x16x32_bf16 a[100:103], a[152:155], a[228:231], a[100:103]\n\t" \
  "ds_read_b64_tr_b16 a[172:173], v195 offset:8192\n\t" \
  "v_accvgpr_read_b32 v18, a66\n\t" \
  "v_accvgpr_read_b32 v19, a67\n\t" \
  "v_mfma_f32_16x16x32_bf16 a[104:107], a[152:155], a[232:235], a[104:107]\n\t" \
  "v_accvgpr_read_b32 v48, a68\n\t" \
  "v_accvgpr_read_b32 v49, a69\n\t" \
  "v_accvgpr_read_b32 v50, a70\n\t" \
  "v_mfma_f32_16x16x32_bf16 a[108:111], a[152:155], a[236:239], a[108:111]\n\t" \
  "buffer_load_dwordx4 a[212:215], v249, %[ws], s97 offen sc1\n\t" \
  "v_mfma_f32_16x16x32_bf16 a[124:127], a[156:159], a[236:239], a[124:127]\n\t" \
  "ds_read_b64_tr_b16 a[174:175], v195 offset:9216\n\t" \
  "v_accvgpr_read_b32 v51, a71\n\t" \
  "v_mfma_f32_16x16x32_bf16 a[120:123], a[156:159], a[232:235], a[120:123]\n\t" \
  "ds_read_b64_tr_b16 a[176:177], v196 offset:8192\n\t" \
  "v_accvgpr_read_b32 v80, a72\n\t" \
  "v_mfma_f32_16x16x32_bf16 a[116:119], a[156:159], a[228:231], a[116:119]\n\t" \
  "ds_read_b64_tr_b16 a[178:179], v196 offset:9216\n\t" \
  "v_accvgpr_read_b32 v81, a73\n\t" \
  "v_mfma_f32_16x16x32_bf16 a[112:115], a[156:159], a[224:227], a[112:115]\n\t" \
  "ds_read_b64_tr_b16 a[180:181], v197 offset:8192\n\t" \
  "v_accvgpr_read_b32 v82, a74\n\t" \
  "v_mfma_f32_16x16x32_bf16 v[0:3], a[128:131], a[224:227], v[0:3]\n\t" \
  "buffer_load_dwordx4 a[216:219], v250, %[ws], s97 offen sc1\n\t" \
  "v_mfma_f32_16x16x32_bf16 v[32:35], a[128:131], a[228:231], v[32:35]\n\t" \
  "ds_read_b64_tr_b16 a[182:183], v197 offset:9216\n\t" \
  "v_accvgpr_read_b32 v83, a75\n\t" \
  "v_mfma_f32_16x16x32_bf16 v[64:67], a[128:131], a[232:235], v[64:67]\n\t" \
  "ds_read_b64_tr_b16 a[184:185], v198 offset:8192\n\t" \
  "v_accvgpr_read_b32 v112, a76\n\t" \
  "v_mfma_f32_16x16x32_bf16 v[96:99], a[128:131], a[236:239], v[96:99]\n\t" \
  "v_accvgpr_read_b32 v113, a77\n\t" \
  "v_accvgpr_read_b32 v114, a78\n\t" \
  "v_mfma_f32_16x16x32_bf16 v[100:103], a[132:135], a[236:239], v[100:103]\n\t" \
  "ds_read_b64_tr_b16 a[186:187], v198 offset:9216\n\t" \
  "v_accvgpr_read_b32 v115, a79\n\t" \
  "v_mfma_f32_16x16x32_bf16 v[68:71], a[132:135], a[232:235], v[68:71]\n\t" \
  "ds_read_b64_tr_b16 a[188:189], v199 offset:8192\n\t" \
  "v_accvgpr_read_b32 v116, a92\n\t" \
  "v_mfma_f32_16x16x32_bf16 v[36:39], a[132:135], a[228:231], v[36:39]\n\t" \
  "buffer_load_dwordx4 a[220:223], v251, %[ws], s97 offen sc1\n\t" \
  "v_mfma_f32_16x16x32_bf16 v[4:7], a[132:135], a[224:227], v[4:7]\n\t" \
  "ds_read_b64_tr_b16 a[190:191], v199 offset:9216\n\t" \
  "v_accvgpr_read_b32 v117, a93\n\t" \
  "v_mfma_f32_16x16x32_bf16 v[8:11], a[136:139], a[224:227], v[8:11]\n\t" \
  "v_accvgpr_read_b32 v118, a94\n\t" \
  "v_accvgpr_read_b32 v119, a95\n\t" \
  "v_mfma_f32_16x16x32_bf16 v[40:43], a[136:139], a[228:231], v[40:43]\n\t" \
  "v_accvgpr_read_b32 v84, a88\n\t" \
  "v_accvgpr_read_b32 v85, a89\n\t" \
  "v_mfma_f32_16x16x32_bf16 v[72:75], a[136:139], a[232:235], v[72:75]\n\t" \
  "v_accvgpr_read_b32 v86, a90\n\t" \
  "v_accvgpr_read_b32 v87, a91\n\t" \
  "v_mfma_f32_16x16x32_bf16 v[104:107], a[136:139], a[236:239], v[104:107]\n\t" \
  "v_accvgpr_read_b32 v52, a84\n\t" \
  "v_accvgpr_read_b32 v53, a85\n\t" \
  "v_mfma_f32_16x16x32_bf16 v[108:111], a[140:143], a[236:239], v[108:111]\n\t" \
  "v_accvgpr_read_b32 v54, a86\n\t" \
  "v_accvgpr_read_b32 v55, a87\n\t" \
  "v_mfma_f32_16x16x32_bf16 v[76:79], a[140:143], a[232:235], v[76:79]\n\t" \
  "v_accvgpr_read_b32 v20, a80\n\t" \
  "v_accvgpr_read_b32 v21, a81\n\t" \
  "v_mfma_f32_16x16x32_bf16 v[44:47], a[140:143], a[228:231], v[44:47]\n\t" \
  "v_accvgpr_read_b32 v22, a82\n\t" \
  "v_accvgpr_read_b32 v23, a83\n\t" \
  "v_mfma_f32_16x16x32_bf16 v[12:15], a[140:143], a[224:227], v[12:15]\n\t" \
  "s_waitcnt vmcnt(22) lgkmcnt(0)\n\t" \
  "s_barrier\n\t" \
  "v_mfma_f32_16x16x32_bf16 a[96:99], a[184:187], a[240:243], a[96:99]\n\t" \
  "s_add_u32 s97, %[rot19], 3670016\n\t" \
  "ds_read_b64_tr_b16 a[128:129], v184 offset:16384\n\t" \
  "v_mfma_f32_16x16x32_bf16 a[100:103], a[184:187], a[244:247], a[100:103]\n\t" \
  "s_bfe_u32 s97, s97, 0x140003\n\t" \
  "v_mfma_f32_16x16x32_bf16 a[104:107], a[184:187], a[248:251], a[104:107]\n\t" \
  "s_add_u32 s97, s97, %[sox]\n\t" \
  "ds_read_b64_tr_b16 a[130:131], v184 offset:17408\n\t" \
  "v_mfma_f32_16x16x32_bf16 a[108:111], a[184:187], a[252:255], a[108:111]\n\t" \
  "buffer_load_dwordx4 a[224:227], v248, %[ws], s97 offen sc1\n\t" \
  "v_mfma_f32_16x16x32_bf16 a[124:127], a[188:191], a[252:255], a[124:127]\n\t" \
  "buffer_load_dwordx4 a[228:231], v249, %[ws], s97 offen sc1\n\t" \
  "v_mfma_f32_16x16x32_bf16 a[120:123], a[188:191], a[248:251], a[120:123]\n\t" \
  "ds_read_b64_tr_b16 a[132:133], v185 offset:16384\n\t" \
  "v_mfma_f32_16x16x32_bf16 a[116:119], a[188:191], a[244:247], a[116:119]\n\t" \
  "ds_read_b64_tr_b16 a[134:135], v185 offset:17408\n\t" \
  "v_mfma_f32_16x16x32_bf16 a[112:115], a[188:191], a[240:243], a[112:115]\n\t" \
  "buffer_load_dwordx4 a[232:235], v250, %[ws], s97 offen sc1\n\t" \
  "v_mfma_f32_16x16x32_bf16 v[0:3], a[160:163], a[240:243], v[0:3]\n\t" \
  "ds_read_b64_tr_b16 a[136:137], v186 offset:16384\n\t" \
  "v_accvgpr_read_b32 v24, a96\n\t" \
  "v_accvgpr_read_b32 v25, a97\n\t" \
  "v_mfma_f32_16x16x32_bf16 v[32:35], a[160:163], a[244:247], v[32:35]\n\t" \
  "ds_read_b64_tr_b16 a[138:139], v186 offset:17408\n\t" \
  "v_accvgpr_read_b32 v26, a98\n\t" \
  "v_accvgpr_read_b32 v27, a99\n\t" \
  "v_mfma_f32_16x16x32_bf16 v[64:67], a[160:163], a[248:251], v[64:67]\n\t" \
  "ds_read_b64_tr_b16 a[140:141], v187 offset:16384\n\t" \
  "v_accvgpr_read_b32 v56, a100\n\t" \
  "v_accvgpr_read_b32 v57, a101\n\t" \
  "v_mfma_f32_16x16x32_bf16 v[96:99], a[160:163], a[252:255], v[96:99]\n\t" \
  "buffer_load_dwordx4 a[236:239], v251, %[ws], s97 offen sc1\n\t" \
  "v_accvgpr_read_b32 v58, a102\n\t" \
  "v_mfma_f32_16x16x32_bf16 v[100:103], a[164:167], a[252:255], v[100:103]\n\t" \
  "s_add_u32 s96, %[rot19], 5242880\n\t" \
  "ds_read_b64_tr_b16 a[142:143], v187 offset:17408\n\t" \
  "v_accvgpr_read_b32 v59, a103\n\t" \
  "v_accvgpr_read_b32 v88, a104\n\t" \
  "v_mfma_f32_16x16x32_bf16 v[68:71], a[164:167], a[248:251], v[68:71]\n\t" \
  "s_and_b32 s96, s96, 0x780000\n\t" \
  "ds_read_b64_tr_b16 a[144:145], v188 offset:16384\n\t" \
  "v_accvgpr_read_b32 v89, a105\n\t" \
  "v_accvgpr_read_b32 v90, a106\n\t" \
  "v_mfma_f32_16x16x32_bf16 v[36:39], a[164:167], a[244:247], v[36:39]\n\t" \
  "s_add_u32 m0, %[mw], 98304\n\t" \
  "ds_read_b64_tr_b16 a[146:147], v188 offset:17408\n\t" \
  "v_accvgpr_read_b32 v91, a107\n\t" \
  "v_accvgpr_read_b32 v120, a108\n\t" \
  "v_mfma_f32_16x16x32_bf16 v[4:7], a[164:167], a[240:243], v[4:7]\n\t" \
  "buffer_load_dwordx4 v200, %[sw], s96 offen offset:0 lds\n\t" \
  "v_accvgpr_read_b32 v121, a109\n\t" \
  "v_mfma_f32_16x16x32_bf16 v[8:11], a[168:171], a[240:243], v[8:11]\n\t" \
  "ds_read_b64_tr_b16 a[148:149], v189 offset:16384\n\t" \
  "v_accvgpr_read_b32 v122, a110\n\t" \
  "v_mfma_f32_16x16x32_bf16 v[40:43], a[168:171], a[244:247], v[40:43]\n\t" \
  "ds_read_b64_tr_b16 a[150:151], v189 offset:17408\n\t" \
  "v_accvgpr_read_b32 v123, a111\n\t" \
  "v_mfma_f32_16x16x32_bf16 v[72:75], a[168:171], a[248:251], v[72:75]\n\t" \
  "ds_read_b64_tr_b16 a[152:153], v190 offset:16384\n\t" \
  "v_accvgpr_read_b32 v124, a124\n\t" \
  "v_mfma_f32_16x16x32_bf16 v[104:107], a[168:171], a[252:255], v[104:107]\n\t" \
  "buffer_load_dwordx4 v201, %[sw], s96 offen offset:1024 lds\n\t" \
  "v_mfma_f32_16x16x32_bf16 v[108:111], a[172:175], a[252:255], v[108:111]\n\t" \
  "ds_read_b64_tr_b16 a[154:155], v190 offset:17408\n\t" \
  "v_accvgpr_read_b32 v125, a125\n\t" \
  "v_mfma_f32_16x16x32_bf16 v[76:79], a[172:175], a[248:251], v[76:79]\n\t" \
  "ds_read_b64_tr_b16 a[156:157], v191 offset:16384\n\t" \
  "v_accvgpr_read_b32 v126, a126\n\t" \
  "v_mfma_f32_16x16x32_bf16 v[44:47], a[172:175], a[244:247], v[44:47]\n\t" \
  "v_accvgpr_read_b32 v127, a127\n\t" \
  "v_accvgpr_read_b32 v92, a120\n\t" \
  "v_mfma_f32_16x16x32_bf16 v[12:15], a[172:175], a[240:243], v[12:15]\n\t" \
  "buffer_load_dwordx4 v202, %[sw], s96 offen offset:2048 lds\n\t" \
  "v_mfma_f32_16x16x32_bf16 v[16:19], a[176:179], a[240:243], v[16:19]\n\t" \
  "ds_read_b64_tr_b16 a[158:159], v191 offset:17408\n\t" \
  "v_accvgpr_read_b32 v93, a121\n\t" \
  "v_mfma_f32_16x16x32_bf16 v[48:51], a[176:179], a[244:247], v[48:51]\n\t" \
  "v_accvgpr_read_b32 v94, a122\n\t" \
  "v_accvgpr_read_b32 v95, a123\n\t" \
  "v_mfma_f32_16x16x32_bf16 v[80:83], a[176:179], a[248:251], v[80:83]\n\t" \
  "v_accvgpr_read_b32 v60, a116\n\t" \
  "v_accvgpr_read_b32 v61, a117\n\t" \
  "v_mfma_f32_16x16x32_bf16 v[112:115], a[176:179], a[252:255], v[112:115]\n\t" \
  "buffer_load_dwordx4 v203, %[sw], s96 offen offset:3072 lds\n\t" \
  "v_mfma_f32_16x16x32_bf16 v[116:119], a[180:183], a[252:255], v[116:119]\n\t" \
  "v_accvgpr_read_b32 v62, a118\n\t" \
  "v_accvgpr_read_b32 v63, a119\n\t" \
  "v_mfma_f32_16x16x32_bf16 v[84:87], a[180:183], a[248:251], v[84:87]\n\t" \
  "v_accvgpr_read_b32 v28, a112\n\t" \
  "v_accvgpr_read_b32 v29, a113\n\t" \
  "v_mfma_f32_16x16x32_bf16 v[52:55], a[180:183], a[244:247], v[52:55]\n\t" \
  "v_accvgpr_read_b32 v30, a114\n\t" \
  "v_accvgpr_read_b32 v31, a115\n\t" \
  "v_mfma_f32_16x16x32_bf16 v[20:23], a[180:183], a[240:243], v[20:23]\n\t" \
  "s_waitcnt vmcnt(17) lgkmcnt(0)\n\t" \
  "v_mfma_f32_16x16x32_bf16 v[0:3], a[128:131], a[192:195], v[0:3]\n\t" \
  "s_add_u32 s97, s97, %[dk]\n\t" \
  "ds_read_b64_tr_b16 a[160:161], v184 offset:24576\n\t" \
  "v_mfma_f32_16x16x32_bf16 v[32:35], a[128:131], a[196:199], v[32:35]\n\t" \
  "buffer_load_dwordx4 a[240:243], v248, %[ws], s97 offen sc1\n\t" \
  "v_mfma_f32_16x16x32_bf16 v[64:67], a[128:131], a[200:203], v[64:67]\n\t" \
  "ds_read_b64_tr_b16 a[162:163], v184 offset:25600\n\t" \
  "v_mfma_f32_16x16x32_bf16 v[96:99], a[128:131], a[204:207], v[96:99]\n\t" \
  "buffer_load_dwordx4 a[244:247], v249, %[ws], s97 offen sc1\n\t" \
  "v_mfma_f32_16x16x32_bf16 v[100:103], a[132:135], a[204:207], v[100:103]\n\t" \
  "ds_read_b64_tr_b16 a[164:165], v185 offset:24576\n\t" \
  "v_mfma_f32_16x16x32_bf16 v[68:71], a[132:135], a[200:203], v[68:71]\n\t" \
  "ds_read_b64_tr_b16 a[166:167], v185 offset:25600\n\t" \
  "v_mfma_f32_16x16x32_bf16 v[36:39], a[132:135], a[196:199], v[36:39]\n\t" \
  "ds_read_b64_tr_b16 a[168:169], v186 offset:24576\n\t" \
  "v_mfma_f32_16x16x32_bf16 v[4:7], a[132:135], a[192:195], v[4:7]\n\t" \
  "buffer_load_dwordx4 a[248:251], v250, %[ws], s97 offen sc1\n\t" \
  "v_mfma_f32_16x16x32_bf16 v[8:11], a[136:139], a[192:195], v[8:11]\n\t" \
  "ds_read_b64_tr_b16 a[170:171], v186 offset:25600\n\t" \
  "v_mfma_f32_16x16x32_bf16 v[40:43], a[136:139], a[196:199], v[40:43]\n\t" \
  "ds_read_b64_tr_b16 a[172:173], v187 offset:24576\n\t" \
  "v_mfma_f32_16x16x32_bf16 v[72:75], a[136:139], a[200:203], v[72:75]\n\t" \
  "v_mfma_f32_16x16x32_bf16 v[104:107], a[136:139], a[204:207], v[104:107]\n\t" \
  "buffer_load_dwordx4 a[252:255], v251, %[ws], s97 offen sc1\n\t" \
  "v_mfma_f32_16x16x32_bf16 v[108:111], a[140:143], a[204:207], v[108:111]\n\t" \
  "s_add_u32 s97, %[rot19], 4194304\n\t" \
  "ds_read_b64_tr_b16 a[174:175], v187 offset:25600\n\t" \
  "v_mfma_f32_16x16x32_bf16 v[76:79], a[140:143], a[200:203], v[76:79]\n\t" \
  "s_bfe_u32 s97, s97, 0x140003\n\t" \
  "ds_read_b64_tr_b16 a[176:177], v188 offset:24576\n\t" \
  "v_mfma_f32_16x16x32_bf16 v[44:47], a[140:143], a[196:199], v[44:47]\n\t" \
  "s_add_u32 s97, s97, %[sox]\n\t" \
  "ds_read_b64_tr_b16 a[178:179], v188 offset:25600\n\t" \
  "v_mfma_f32_16x16x32_bf16 v[12:15], a[140:143], a[192:195], v[12:15]\n\t" \
  "buffer_load_dwordx4 a[32:35], v248, %[ws], s97 offen sc1\n\t" \
  "v_mfma_f32_16x16x32_bf16 v[16:19], a[144:147], a[192:195], v[16:19]\n\t" \
  "ds_read_b64_tr_b16 a[180:181], v189 offset:24576\n\t" \
  "v_mfma_f32_16x16x32_bf16 v[48:51], a[144:147], a[196:199], v[48:51]\n\t" \
  "ds_read_b64_tr_b16 a[182:183], v189 offset:25600\n\t" \
  "v_mfma_f32_16x16x32_bf16 v[80:83], a[144:147], a[200:203], v[80:83]\n\t" \
  "ds_read_b64_tr_b16 a[184:185], v190 offset:24576\n\t" \
  "v_mfma_f32_16x16x32_bf16 v[112:115], a[144:147], a[204:207], v[112:115]\n\t" \
  "buffer_load_dwordx4 a[36:39], v249, %[ws], s97 offen sc1\n\t" \
  "v_mfma_f32_16x16x32_bf16 v[116:119], a[148:151], a[204:207], v[116:119]\n\t" \
  "ds_read_b64_tr_b16 a[186:187], v190 offset:25600\n\t" \
  "v_mfma_f32_16x16x32_bf16 v[84:87], a[148:151], a[200:203], v[84:87]\n\t" \
  "ds_read_b64_tr_b16 a[188:189], v191 offset:24576\n\t" \
  "v_mfma_f32_16x16x32_bf16 v[52:55], a[148:151], a[196:199], v[52:55]\n\t" \
  "v_mfma_f32_16x16x32_bf16 v[20:23], a[148:151], a[192:195], v[20:23]\n\t" \
  "buffer_load_dwordx4 a[40:43], v250, %[ws], s97 offen sc1\n\t" \
  "v_mfma_f32_16x16x32_bf16 v[24:27], a[152:155], a[192:195], v[24:27]\n\t" \
  "ds_read_b64_tr_b16 a[190:191], v191 offset:25600\n\t" \
  "v_mfma_f32_16x16x32_bf16 v[56:59], a[152:155], a[196:199], v[56:59]\n\t" \
  "v_mfma_f32_16x16x32_bf16 v[88:91], a[152:155], a[200:203], v[88:91]\n\t" \
  "v_mfma_f32_16x16x32_bf16 v[120:123], a[152:155], a[204:207], v[120:123]\n\t" \
  "buffer_load_dwordx4 a[44:47], v251, %[ws], s97 offen sc1\n\t" \
  "v_mfma_f32_16x16x32_bf16 v[124:127], a[156:159], a[204:207], v[124:127]\n\t" \
  "v_mfma_f32_16x16x32_bf16 v[92:95], a[156:159], a[200:203], v[92:95]\n\t" \
  "v_mfma_f32_16x16x32_bf16 v[60:63], a[156:159], a[196:199], v[60:63]\n\t" \
  "v_mfma_f32_16x16x32_bf16 v[28:31], a[156:159], a[192:195], v[28:31]\n\t" \
  "s_waitcnt vmcnt(16) lgkmcnt(0)\n\t" \
  "s_barrier\n\t" \
  "v_mfma_f32_16x16x32_bf16 v[0:3], a[160:163], a[208:211], v[0:3]\n\t" \
  "s_add_u32 s97, s97, %[dk]\n\t" \
  "ds_read_b64_tr_b16 a[128:129], v184 offset:32768\n\t" \
  "ds_read_b64_tr_b16 a[130:131], v184 offset:33792\n\t" \
  "v_mfma_f32_16x16x32_bf16 v[32:35], a[160:163], a[212:215], v[32:35]\n\t" \
  "buffer_load_dwordx4 a[48:51], v248, %[ws], s97 offen sc1\n\t" \
  "v_mfma_f32_16x16x32_bf16 v[64:67], a[160:163], a[216:219], v[64:67]\n\t" \
  "s_bfe_u32 %[hl], %[bid], 0x30003\n\t" \
  "ds_read_b64_tr_b16 a[132:133], v185 offset:32768\n\t" \
  "ds_read_b64_tr_b16 a[134:135], v185 offset:33792\n\t" \
  "v_mfma_f32_16x16x32_bf16 v[96:99], a[160:163], a[220:223], v[96:99]\n\t" \
  "buffer_load_dwordx4 a[52:55], v249, %[ws], s97 offen sc1\n\t" \
  "v_mfma_f32_16x16x32_bf16 v[100:103], a[164:167], a[220:223], v[100:103]\n\t" \
  "s_lshl_b32 %[hl18], %[hl], 18\n\t" \
  "ds_read_b64_tr_b16 a[136:137], v186 offset:32768\n\t" \
  "ds_read_b64_tr_b16 a[138:139], v186 offset:33792\n\t" \
  "v_mfma_f32_16x16x32_bf16 v[68:71], a[164:167], a[216:219], v[68:71]\n\t" \
  "s_lshr_b32 s98, %[bid], 3\n\t" \
  "ds_read_b64_tr_b16 a[140:141], v187 offset:32768\n\t" \
  "ds_read_b64_tr_b16 a[142:143], v187 offset:33792\n\t" \
  "v_mfma_f32_16x16x32_bf16 v[36:39], a[164:167], a[212:215], v[36:39]\n\t" \
  "s_lshl_b32 %[cto], %[wid], 5\n\t" \
  "ds_read_b64_tr_b16 a[144:145], v188 offset:32768\n\t" \
  "ds_read_b64_tr_b16 a[146:147], v188 offset:33792\n\t" \
  "v_mfma_f32_16x16x32_bf16 v[4:7], a[164:167], a[208:211], v[4:7]\n\t" \
  "buffer_load_dwordx4 a[56:59], v250, %[ws], s97 offen sc1\n\t" \
  "v_mfma_f32_16x16x32_bf16 v[8:11], a[168:171], a[208:211], v[8:11]\n\t" \
  "s_add_u32 %[cto], %[cto], s98\n\t" \
  "ds_read_b64_tr_b16 a[148:149], v189 offset:32768\n\t" \
  "ds_read_b64_tr_b16 a[150:151], v189 offset:33792\n\t" \
  "v_mfma_f32_16x16x32_bf16 v[40:43], a[168:171], a[212:215], v[40:43]\n\t" \
  "s_lshl_b32 %[cto], %[cto], 7\n\t" \
  "ds_read_b64_tr_b16 a[152:153], v190 offset:32768\n\t" \
  "ds_read_b64_tr_b16 a[154:155], v190 offset:33792\n\t" \
  "v_mfma_f32_16x16x32_bf16 v[72:75], a[168:171], a[216:219], v[72:75]\n\t" \
  "s_lshl2_add_u32 %[pf2], s98, %[wid]\n\t" \
  "ds_read_b64_tr_b16 a[156:157], v191 offset:32768\n\t" \
  "ds_read_b64_tr_b16 a[158:159], v191 offset:33792\n\t" \
  "v_mfma_f32_16x16x32_bf16 v[104:107], a[168:171], a[220:223], v[104:107]\n\t" \
  "buffer_load_dwordx4 a[60:63], v251, %[ws], s97 offen sc1\n\t" \
  "v_mfma_f32_16x16x32_bf16 v[108:111], a[172:175], a[220:223], v[108:111]\n\t" \
  "s_add_u32 s97, %[rot19], 4718592\n\t" \
  "ds_read_b64_tr_b16 a[64:65], v184 offset:40960\n\t" \
  "ds_read_b64_tr_b16 a[66:67], v184 offset:41984\n\t" \
  "v_mfma_f32_16x16x32_bf16 v[76:79], a[172:175], a[216:219], v[76:79]\n\t" \
  "s_bfe_u32 s97, s97, 0x140003\n\t" \
  "ds_read_b64_tr_b16 a[68:69], v185 offset:40960\n\t" \
  "ds_read_b64_tr_b16 a[70:71], v185 offset:41984\n\t" \
  "v_mfma_f32_16x16x32_bf16 v[44:47], a[172:175], a[212:215], v[44:47]\n\t" \
  "s_add_u32 s97, s97, %[sox]\n\t" \
  "v_mbcnt_lo_u32_b32 v233, -1, 0\n\t" \
  "v_mfma_f32_16x16x32_bf16 v[12:15], a[172:175], a[208:211], v[12:15]\n\t" \
  "buffer_load_dwordx4 a[96:99], v248, %[ws], s97 offen sc1\n\t" \
  "v_mfma_f32_16x16x32_bf16 v[16:19], a[176:179], a[208:211], v[16:19]\n\t" \
  "s_mul_i32 %[pf2], %[pf2], 98304\n\t" \
  "ds_read_b64_tr_b16 a[72:73], v186 offset:40960\n\t" \
  "ds_read_b64_tr_b16 a[74:75], v186 offset:41984\n\t" \
  "v_mfma_f32_16x16x32_bf16 v[48:51], a[176:179], a[212:215], v[48:51]\n\t" \
  "s_lshr_b32 s98, %[bid], 2\n\t" \
  "ds_read_b64_tr_b16 a[76:77], v187 offset:40960\n\t" \
  "ds_read_b64_tr_b16 a[78:79], v187 offset:41984\n\t" \
  "v_mfma_f32_16x16x32_bf16 v[80:83], a[176:179], a[216:219], v[80:83]\n\t" \
  "s_xor_b32 s98, s98, %[wid]\n\t" \
  "ds_read_b64_tr_b16 a[80:81], v188 offset:40960\n\t" \
  "v_mbcnt_hi_u32_b32 v233, -1, v233\n\t" \
  "v_mfma_f32_16x16x32_bf16 v[112:115], a[176:179], a[220:223], v[112:115]\n\t" \
  "buffer_load_dwordx4 a[100:103], v249, %[ws], s97 offen sc1\n\t" \
  "v_mfma_f32_16x16x32_bf16 v[116:119], a[180:183], a[220:223], v[116:119]\n\t" \
  "s_lshl_b32 s98, s98, 13\n\t" \
  "ds_read_b64_tr_b16 a[82:83], v188 offset:41984\n\t" \
  "ds_read_b64_tr_b16 a[84:85], v189 offset:40960\n\t" \
  "v_mfma_f32_16x16x32_bf16 v[84:87], a[180:183], a[216:219], v[84:87]\n\t" \
  "s_and_b32 %[po], s98, 0x2000\n\t" \
  "ds_read_b64_tr_b16 a[86:87], v189 offset:41984\n\t" \
  "v_lshrrev_b32 v129, 4, v233\n\t" \
  "v_mfma_f32_16x16x32_bf16 v[52:55], a[180:183], a[212:215], v[52:55]\n\t" \
  "s_lshr_b32 s98, %[bid], 6\n\t" \
  "ds_read_b64_tr_b16 a[88:89], v190 offset:40960\n\t" \
  "v_and_b32 v130, 15, v233\n\t" \
  "v_mfma_f32_16x16x32_bf16 v[20:23], a[180:183], a[208:211], v[20:23]\n\t" \
  "buffer_load_dwordx4 a[104:107], v250, %[ws], s97 offen sc1\n\t" \
  "v_mfma_f32_16x16x32_bf16 v[24:27], a[184:187], a[208:211], v[24:27]\n\t" \
  "s_lshl_b32 s98, s98, 22\n\t" \
  "ds_read_b64_tr_b16 a[90:91], v190 offset:41984\n\t" \
  "ds_read_b64_tr_b16 a[92:93], v191 offset:40960\n\t" \
  "v_mfma_f32_16x16x32_bf16 v[56:59], a[184:187], a[212:215], v[56:59]\n\t" \
  "s_add_u32 %[po], %[po], s98\n\t" \
  "ds_read_b64_tr_b16 a[94:95], v191 offset:41984\n\t" \
  "v_bfe_u32 v131, v233, 2, 2\n\t" \
  "v_mfma_f32_16x16x32_bf16 v[88:91], a[184:187], a[216:219], v[88:91]\n\t" \
  "s_lshl_b32 %[xcb], %[wid], 15\n\t" \
  "v_and_b32 v132, 3, v233\n\t" \
  "v_mfma_f32_16x16x32_bf16 v[120:123], a[184:187], a[220:223], v[120:123]\n\t" \
  "buffer_load_dwordx4 a[108:111], v251, %[ws], s97 offen sc1\n\t" \
  "v_mfma_f32_16x16x32_bf16 v[124:127], a[188:191], a[220:223], v[124:127]\n\t" \
  "s_add_u32 %[xcb], %[xcb], 0x1800000\n\t" \
  "v_cmp_lt_u32 vcc, 6, v233\n\t" \
  "v_mfma_f32_16x16x32_bf16 v[92:95], a[188:191], a[216:219], v[92:95]\n\t" \
  "s_add_u32 %[xcb], %[xcb], %[po]\n\t" \
  "v_mov_b32 v133, 6272\n\t" \
  "v_mfma_f32_16x16x32_bf16 v[60:63], a[188:191], a[212:215], v[60:63]\n\t" \
  "s_and_b32 s98, %[bid], 0x38\n\t" \
  "v_cndmask_b32 v133, 0, v133, vcc\n\t" \
  "v_mfma_f32_16x16x32_bf16 v[28:31], a[188:191], a[208:211], v[28:31]\n\t" \
  "s_waitcnt vmcnt(12) lgkmcnt(0)\n\t" \
  "s_barrier\n\t" \
  "v_mfma_f32_16x16x32_bf16 v[0:3], a[128:131], a[224:227], v[0:3]\n\t" \
  "s_add_u32 s97, s97, %[dk]\n\t" \
  "ds_read_b64_tr_b16 a[160:161], v184 offset:0\n\t" \
  "v_lshl_add_u32 v133, v233, 7, v133\n\t" \
  "v_mfma_f32_16x16x32_bf16 v[0:3], a[64:67], a[240:243], v[0:3]\n\t" \
  "buffer_load_dwordx4 a[112:115], v248, %[ws], s97 offen sc1\n\t" \
  "v_mfma_f32_16x16x32_bf16 v[32:35], a[64:67], a[244:247], v[32:35]\n\t" \
  "s_lshl_b32 s98, s98, 16\n\t" \
  "ds_read_b64_tr_b16 a[162:163], v184 offset:1024\n\t" \
  "v_cmp_gt_u32 vcc, 14, v233\n\t" \
  "v_mfma_f32_16x16x32_bf16 v[32:35], a[128:131], a[228:231], v[32:35]\n\t" \
  "buffer_load_dwordx4 a[116:119], v249, %[ws], s97 offen sc1\n\t" \
  "v_mfma_f32_16x16x32_bf16 v[64:67], a[128:131], a[232:235], v[64:67]\n\t" \
  "s_add_u32 %[xcb], %[xcb], s98\n\t" \
  "ds_read_b64_tr_b16 a[164:165], v185 offset:0\n\t" \
  "v_bfrev_b32 v247, 1\n\t" \
  "v_mfma_f32_16x16x32_bf16 v[64:67], a[64:67], a[248:251], v[64:67]\n\t" \
  "s_add_u32 %[otb], %[po], 0x800000\n\t" \
  "ds_read_b64_tr_b16 a[166:167], v185 offset:1024\n\t" \
  "v_cndmask_b32 v245, v247, v133, vcc\n\t" \
  "v_mfma_f32_16x16x32_bf16 v[96:99], a[64:67], a[252:255], v[96:99]\n\t" \
  "s_and_b32 s98, %[bid], 0x3\n\t" \
  "ds_read_b64_tr_b16 a[168:169], v186 offset:0\n\t" \
  "v_writelane_b32 v247, %[prb], 0\n\t" \
  "v_mfma_f32_16x16x32_bf16 v[96:99], a[128:131], a[236:239], v[96:99]\n\t" \
  "buffer_load_dwordx4 a[120:123], v250, %[ws], s97 offen sc1\n\t" \
  "v_mfma_f32_16x16x32_bf16 v[4:7], a[132:135], a[224:227], v[4:7]\n\t" \
  "s_lshl_b32 s98, s98, 20\n\t" \
  "ds_read_b64_tr_b16 a[170:171], v186 offset:1024\n\t" \
  "v_lshrrev_b32 v133, 1, v129\n\t" \
  "v_mfma_f32_16x16x32_bf16 v[4:7], a[68:71], a[240:243], v[4:7]\n\t" \
  "s_add_u32 %[otb], %[otb], s98\n\t" \
  "ds_read_b64_tr_b16 a[172:173], v187 offset:0\n\t" \
  "v_and_b32 v134, 1, v129\n\t" \
  "v_mfma_f32_16x16x32_bf16 v[36:39], a[68:71], a[244:247], v[36:39]\n\t" \
  "s_and_b32 s98, %[wid], 2\n\t" \
  "v_lshl_or_b32 v133, v134, 1, v133\n\t" \
  "v_mfma_f32_16x16x32_bf16 v[36:39], a[132:135], a[228:231], v[36:39]\n\t" \
  "buffer_load_dwordx4 a[124:127], v251, %[ws], s97 offen sc1\n\t" \
  "v_mfma_f32_16x16x32_bf16 v[68:71], a[132:135], a[232:235], v[68:71]\n\t" \
  "s_add_u32 s96, %[rot19], 5767168\n\t" \
  "ds_read_b64_tr_b16 a[174:175], v187 offset:1024\n\t" \
  "v_xor_b32 v133, v133, v130\n\t" \
  "v_mfma_f32_16x16x32_bf16 v[68:71], a[68:71], a[248:251], v[68:71]\n\t" \
  "s_and_b32 s96, s96, 0x780000\n\t" \
  "ds_read_b64_tr_b16 a[176:177], v188 offset:0\n\t" \
  "v_lshlrev_b32 v133, 4, v133\n\t" \
  "v_mfma_f32_16x16x32_bf16 v[100:103], a[68:71], a[252:255], v[100:103]\n\t" \
  "s_add_u32 m0, %[mw], 16384\n\t" \
  "ds_read_b64_tr_b16 a[178:179], v188 offset:1024\n\t" \
  "v_lshl_add_u32 v133, v130, 8, v133\n\t" \
  "v_mfma_f32_16x16x32_bf16 v[100:103], a[132:135], a[236:239], v[100:103]\n\t" \
  "buffer_load_dwordx4 v200, %[sw], s96 offen offset:0 lds\n\t" \
  "v_mfma_f32_16x16x32_bf16 v[8:11], a[136:139], a[224:227], v[8:11]\n\t" \
  "s_lshl_b32 s98, s98, 13\n\t" \
  "ds_read_b64_tr_b16 a[180:181], v189 offset:0\n\t" \
  "v_add_u32 v220, 49152, v133\n\t" \
  "v_mfma_f32_16x16x32_bf16 v[8:11], a[72:75], a[240:243], v[8:11]\n\t" \
  "s_add_u32 %[otb], %[otb], s98\n\t" \
  "ds_read_b64_tr_b16 a[182:183], v189 offset:1024\n\t" \
  "v_xor_b32 v221, 64, v220\n\t" \
  "v_mfma_f32_16x16x32_bf16 v[40:43], a[72:75], a[244:247], v[40:43]\n\t" \
  "s_and_b32 s98, %[wid], 1\n\t" \
  "ds_read_b64_tr_b16 a[184:185], v190 offset:0\n\t" \
  "v_xor_b32 v222, 128, v220\n\t" \
  "v_mfma_f32_16x16x32_bf16 v[40:43], a[136:139], a[228:231], v[40:43]\n\t" \
  "buffer_load_dwordx4 v201, %[sw], s96 offen offset:1024 lds\n\t" \
  "v_mfma_f32_16x16x32_bf16 v[72:75], a[136:139], a[232:235], v[72:75]\n\t" \
  "s_lshl_b32 s98, s98, 12\n\t" \
  "ds_read_b64_tr_b16 a[186:187], v190 offset:1024\n\t" \
  "v_xor_b32 v223, 192, v220\n\t" \
  "v_mfma_f32_16x16x32_bf16 v[72:75], a[72:75], a[248:251], v[72:75]\n\t" \
  "s_add_u32 %[otb], %[otb], s98\n\t" \
  "ds_read_b64_tr_b16 a[188:189], v191 offset:0\n\t" \
  "v_xor_b32 v133, v130, v129\n\t" \
  "v_mfma_f32_16x16x32_bf16 v[104:107], a[72:75], a[252:255], v[104:107]\n\t" \
  "s_lshr_b32 %[eso], %[c17], 2\n\t" \
  "v_lshlrev_b32 v133, 4, v133\n\t" \
  "v_mfma_f32_16x16x32_bf16 v[104:107], a[136:139], a[236:239], v[104:107]\n\t" \
  "buffer_load_dwordx4 v202, %[sw], s96 offen offset:2048 lds\n\t" \
  "v_mfma_f32_16x16x32_bf16 v[12:15], a[140:143], a[224:227], v[12:15]\n\t" \
  "s_add_u32 %[eso], %[eso], %[otb]\n\t" \
  "ds_read_b64_tr_b16 a[190:191], v191 offset:1024\n\t" \
  "v_lshl_add_u32 v224, v129, 8, v133\n\t" \
  "v_mfma_f32_16x16x32_bf16 v[12:15], a[76:79], a[240:243], v[12:15]\n\t" \
  "s_and_b32 s98, %[bid], 0x38\n\t" \
  "ds_read_b64_tr_b16 a[0:1], v184 offset:8192\n\t" \
  "v_xor_b32 v225, 64, v224\n\t" \
  "v_mfma_f32_16x16x32_bf16 v[44:47], a[76:79], a[244:247], v[44:47]\n\t" \
  "s_lshl_b32 s98, s98, 14\n\t" \
  "ds_read_b64_tr_b16 a[2:3], v184 offset:9216\n\t" \
  "v_xor_b32 v226, 128, v224\n\t" \
  "v_mfma_f32_16x16x32_bf16 v[44:47], a[140:143], a[228:231], v[44:47]\n\t" \
  "buffer_load_dwordx4 v203, %[sw], s96 offen offset:3072 lds\n\t" \
  "v_mfma_f32_16x16x32_bf16 v[76:79], a[140:143], a[232:235], v[76:79]\n\t" \
  "s_add_u32 s97, %[rot19], 5242880\n\t" \
  "ds_read_b64_tr_b16 a[4:5], v185 offset:8192\n\t" \
  "v_xor_b32 v227, 192, v224\n\t" \
  "v_mfma_f32_16x16x32_bf16 v[76:79], a[76:79], a[248:251], v[76:79]\n\t" \
  "s_bfe_u32 s97, s97, 0x140003\n\t" \
  "ds_read_b64_tr_b16 a[6:7], v185 offset:9216\n\t" \
  "v_lshlrev_b32 v133, 3, v132\n\t" \
  "v_mfma_f32_16x16x32_bf16 v[108:111], a[76:79], a[252:255], v[108:111]\n\t" \
  "s_add_u32 s97, s97, %[sox]\n\t" \
  "ds_read_b64_tr_b16 a[8:9], v186 offset:8192\n\t" \
  "v_lshl_add_u32 v133, v129, 10, v133\n\t" \
  "v_mfma_f32_16x16x32_bf16 v[108:111], a[140:143], a[236:239], v[108:111]\n\t" \
  "buffer_load_dwordx4 a[192:195], v248, %[ws], s97 offen sc1\n\t" \
  "v_mfma_f32_16x16x32_bf16 v[16:19], a[144:147], a[224:227], v[16:19]\n\t" \
  "s_add_u32 %[eso], %[eso], s98\n\t" \
  "ds_read_b64_tr_b16 a[10:11], v186 offset:9216\n\t" \
  "v_lshl_add_u32 v228, v131, 6, v133\n\t" \
  "v_mfma_f32_16x16x32_bf16 v[16:19], a[80:83], a[240:243], v[16:19]\n\t" \
  "s_lshr_b32 s98, %[bid], 1\n\t" \
  "ds_read_b64_tr_b16 a[12:13], v187 offset:8192\n\t" \
  "v_lshl_add_u32 v228, v134, 5, v228\n\t" \
  "v_mfma_f32_16x16x32_bf16 v[48:51], a[80:83], a[244:247], v[48:51]\n\t" \
  "s_xor_b32 s98, s98, %[wid]\n\t" \
  "v_add_u32 v228, 81920, v228\n\t" \
  "v_mfma_f32_16x16x32_bf16 v[48:51], a[144:147], a[228:231], v[48:51]\n\t" \
  "buffer_load_dwordx4 a[196:199], v249, %[ws], s97 offen sc1\n\t" \
  "v_mfma_f32_16x16x32_bf16 v[80:83], a[144:147], a[232:235], v[80:83]\n\t" \
  "s_lshl_b32 s98, s98, 13\n\t" \
  "ds_read_b64_tr_b16 a[14:15], v187 offset:9216\n\t" \
  "v_xor_b32 v229, 32, v228\n\t" \
  "v_mfma_f32_16x16x32_bf16 v[80:83], a[80:83], a[248:251], v[80:83]\n\t" \
  "s_and_b32 %[po], s98, 0x2000\n\t" \
  "ds_read_b64_tr_b16 a[16:17], v188 offset:8192\n\t" \
  "v_lshl_add_u32 v244, v131, 8, v133\n\t" \
  "v_mfma_f32_16x16x32_bf16 v[112:115], a[80:83], a[252:255], v[112:115]\n\t" \
  "s_lshr_b32 s98, %[bid], 5\n\t" \
  "ds_read_b64_tr_b16 a[18:19], v188 offset:9216\n\t" \
  "v_lshl_add_u32 v244, v131, 5, v244\n\t" \
  "v_mfma_f32_16x16x32_bf16 v[112:115], a[144:147], a[236:239], v[112:115]\n\t" \
  "buffer_load_dwordx4 a[200:203], v250, %[ws], s97 offen sc1\n\t" \
  "v_mfma_f32_16x16x32_bf16 v[20:23], a[148:151], a[224:227], v[20:23]\n\t" \
  "s_xor_b32 s98, s98, %[wid]\n\t" \
  "ds_read_b64_tr_b16 a[20:21], v189 offset:8192\n\t" \
  "v_lshl_add_u32 v244, v134, 7, v244\n\t" \
  "v_mfma_f32_16x16x32_bf16 v[20:23], a[84:87], a[240:243], v[20:23]\n\t" \
  "s_lshl_b32 s98, s98, 13\n\t" \
  ".Lhs3:\n\ts_cbranch_execz .Lhs3n\n\t" \
  "ds_read_b64_tr_b16 a[22:23], v189 offset:9216\n\t" \
  "v_lshlrev_b32 v133, 4, v132\n\t" \
  "v_mfma_f32_16x16x32_bf16 v[52:55], a[84:87], a[244:247], v[52:55]\n\t" \
  "s_and_b32 %[px], s98, 0x2000\n\t" \
  "ds_read_b64_tr_b16 a[24:25], v190 offset:8192\n\t" \
  "v_lshl_add_u32 v133, v129, 6, v133\n\t" \
  "v_mfma_f32_16x16x32_bf16 v[52:55], a[148:151], a[228:231], v[52:55]\n\t" \
  "buffer_load_dwordx4 a[204:207], v251, %[ws], s97 offen sc1\n\t" \
  "v_mfma_f32_16x16x32_bf16 v[84:87], a[148:151], a[232:235], v[84:87]\n\t" \
  "s_add_u32 s97, s97, %[dk]\n\t" \
  "ds_read_b64_tr_b16 a[26:27], v190 offset:9216\n\t" \
  "v_lshl_add_u32 v230, v131, 8, v133\n\t" \
  "v_mfma_f32_16x16x32_bf16 v[84:87], a[84:87], a[248:251], v[84:87]\n\t" \
  "s_lshl_b32 %[frb], %[wid], 2\n\t" \
  "ds_read_b64_tr_b16 a[28:29], v191 offset:8192\n\t" \
  "v_xor_b32 v231, 32, v230\n\t" \
  "v_mfma_f32_16x16x32_bf16 v[116:119], a[84:87], a[252:255], v[116:119]\n\t" \
  "s_add_u32 %[frb], %[frb], 0x8000\n\t" \
  "v_lshlrev_b32 v133, 1, v129\n\t" \
  "v_mfma_f32_16x16x32_bf16 v[116:119], a[148:151], a[236:239], v[116:119]\n\t" \
  "buffer_load_dwordx4 a[208:211], v248, %[ws], s97 offen sc1\n\t" \
  "v_mfma_f32_16x16x32_bf16 v[24:27], a[152:155], a[224:227], v[24:27]\n\t" \
  "s_and_b32 s98, %[bid], 0x80\n\t" \
  "ds_read_b64_tr_b16 a[30:31], v191 offset:9216\n\t" \
  "v_xor_b32 v133, v133, v130\n\t" \
  "v_mfma_f32_16x16x32_bf16 v[24:27], a[88:91], a[240:243], v[24:27]\n\t" \
  "s_lshl_b32 s98, s98, 7\n\t" \
  "v_lshlrev_b32 v133, 4, v133\n\t" \
  "v_mfma_f32_16x16x32_bf16 v[56:59], a[88:91], a[244:247], v[56:59]\n\t" \
  "s_add_u32 %[frb], %[frb], s98\n\t" \
  "v_lshl_add_u32 v240, v129, 11, v133\n\t" \
  "v_mfma_f32_16x16x32_bf16 v[56:59], a[152:155], a[228:231], v[56:59]\n\t" \
  "buffer_load_dwordx4 a[212:215], v249, %[ws], s97 offen sc1\n\t" \
  "v_mfma_f32_16x16x32_bf16 v[88:91], a[152:155], a[232:235], v[88:91]\n\t" \
  "s_and_b32 s98, %[bid], 0x4\n\t" \
  "v_xor_b32 v241, 0x80, v240\n\t" \
  "v_mfma_f32_16x16x32_bf16 v[88:91], a[88:91], a[248:251], v[88:91]\n\t" \
  "s_lshl_b32 s98, s98, 11\n\t" \
  "v_lshrrev_b32 v133, 1, v233\n\t" \
  "v_mfma_f32_16x16x32_bf16 v[120:123], a[88:91], a[252:255], v[120:123]\n\t" \
  "s_add_u32 %[frb], %[frb], s98\n\t" \
  "v_mul_u32_u24 v133, 57344, v133\n\t" \
  "v_mfma_f32_16x16x32_bf16 v[120:123], a[152:155], a[236:239], v[120:123]\n\t" \
  "buffer_load_dwordx4 a[216:219], v250, %[ws], s97 offen sc1\n\t" \
  "v_mfma_f32_16x16x32_bf16 v[28:31], a[156:159], a[224:227], v[28:31]\n\t" \
  "s_and_b32 s98, %[bid], 0x78\n\t" \
  "v_and_b32 v134, 1, v233\n\t" \
  "v_mfma_f32_16x16x32_bf16 v[28:31], a[92:95], a[240:243], v[28:31]\n\t" \
  "s_lshl_b32 s98, s98, 6\n\t" \
  "v_lshl_add_u32 v133, v134, 4, v133\n\t" \
  "v_mfma_f32_16x16x32_bf16 v[60:63], a[92:95], a[244:247], v[60:63]\n\t" \
  "s_add_u32 %[frb], %[frb], s98\n\t" \
  "v_or_b32 v232, %[idm], v133\n\t" \
  "v_mfma_f32_16x16x32_bf16 v[60:63], a[156:159], a[228:231], v[60:63]\n\t" \
  "buffer_load_dwordx4 a[220:223], v251, %[ws], s97 offen sc1\n\t" \
  "v_mfma_f32_16x16x32_bf16 v[92:95], a[156:159], a[232:235], v[92:95]\n\t" \
  "s_lshl_b32 %[fw2], %[wid], 7\n\t" \
  "v_mfma_f32_16x16x32_bf16 v[92:95], a[92:95], a[248:251], v[92:95]\n\t" \
  "s_add_u32 %[fw2], %[fw2], 0x20000\n\t" \
  "v_mfma_f32_16x16x32_bf16 v[124:127], a[92:95], a[252:255], v[124:127]\n\t" \
  "s_and_b32 s98, %[bid], 0xc0\n\t" \
  "v_mfma_f32_16x16x32_bf16 v[124:127], a[156:159], a[236:239], v[124:127]\n\t" \
  "s_waitcnt vmcnt(20) lgkmcnt(0)\n\t" \
  "s_barrier\n\t" \
  "v_mfma_f32_16x16x32_bf16 v[0:3], a[160:163], a[32:35], v[0:3]\n\t" \
  "s_add_u32 s96, %[rot19], 6291456\n\t" \
  "ds_read_b64_tr_b16 a[128:129], v184 offset:49152\n\t" \
  "v_mfma_f32_16x16x32_bf16 v[0:3], a[0:3], a[48:51], v[0:3]\n\t" \
  "s_and_b32 s96, s96, 0x780000\n\t" \
  "v_mfma_f32_16x16x32_bf16 v[32:35], a[0:3], a[52:55], v[32:35]\n\t" \
  "s_add_u32 m0, %[mw], 32768\n\t" \
  "ds_read_b64_tr_b16 a[130:131], v184 offset:50176\n\t" \
  "v_mfma_f32_16x16x32_bf16 v[32:35], a[160:163], a[36:39], v[32:35]\n\t" \
  "buffer_load_dwordx4 v200, %[sw], s96 offen offset:0 lds\n\t" \
  "v_mfma_f32_16x16x32_bf16 v[64:67], a[160:163], a[40:43], v[64:67]\n\t" \
  "buffer_load_dwordx4 v201, %[sw], s96 offen offset:1024 lds\n\t" \
  "v_mfma_f32_16x16x32_bf16 v[64:67], a[0:3], a[56:59], v[64:67]\n\t" \
  "s_lshl_b32 s98, s98, 9\n\t" \
  "ds_read_b64_tr_b16 a[132:133], v185 offset:49152\n\t" \
  "v_mfma_f32_16x16x32_bf16 v[96:99], a[0:3], a[60:63], v[96:99]\n\t" \
  "s_add_u32 %[fw2], %[fw2], s98\n\t" \
  "ds_read_b64_tr_b16 a[134:135], v185 offset:50176\n\t" \
  "v_mfma_f32_16x16x32_bf16 v[96:99], a[160:163], a[44:47], v[96:99]\n\t" \
  "s_and_b32 s98, %[bid], 0x2\n\t" \
  "ds_read_b64_tr_b16 a[136:137], v186 offset:49152\n\t" \
  "v_mfma_f32_16x16x32_bf16 v[4:7], a[164:167], a[32:35], v[4:7]\n\t" \
  "s_lshl_b32 s98, s98, 13\n\t" \
  "ds_read_b64_tr_b16 a[138:139], v186 offset:50176\n\t" \
  "v_mfma_f32_16x16x32_bf16 v[4:7], a[4:7], a[48:51], v[4:7]\n\t" \
  "buffer_load_dwordx4 v202, %[sw], s96 offen offset:2048 lds\n\t" \
  "v_mfma_f32_16x16x32_bf16 v[36:39], a[4:7], a[52:55], v[36:39]\n\t" \
  "s_add_u32 %[fw2], %[fw2], s98\n\t" \
  "ds_read_b64_tr_b16 a[140:141], v187 offset:49152\n\t" \
  "v_mfma_f32_16x16x32_bf16 v[36:39], a[164:167], a[36:39], v[36:39]\n\t" \
  "s_and_b32 s98, %[bid], 0x4\n\t" \
  "ds_read_b64_tr_b16 a[142:143], v187 offset:50176\n\t" \
  "v_mfma_f32_16x16x32_bf16 v[68:71], a[164:167], a[40:43], v[68:71]\n\t" \
  "s_lshl_b32 s98, s98, 11\n\t" \
  "ds_read_b64_tr_b16 a[144:145], v188 offset:49152\n\t" \
  "v_mfma_f32_16x16x32_bf16 v[68:71], a[4:7], a[56:59], v[68:71]\n\t" \
  "buffer_load_dwordx4 v203, %[sw], s96 offen offset:3072 lds\n\t" \
  "v_mfma_f32_16x16x32_bf16 v[100:103], a[4:7], a[60:63], v[100:103]\n\t" \
  "s_add_u32 s97, %[rot19], 5767168\n\t" \
  "ds_read_b64_tr_b16 a[146:147], v188 offset:50176\n\t" \
  "v_mfma_f32_16x16x32_bf16 v[100:103], a[164:167], a[44:47], v[100:103]\n\t" \
  "s_bfe_u32 s97, s97, 0x140003\n\t" \
  "ds_read_b64_tr_b16 a[148:149], v189 offset:49152\n\t" \
  "v_mfma_f32_16x16x32_bf16 v[8:11], a[168:171], a[32:35], v[8:11]\n\t" \
  "s_add_u32 s97, s97, %[sox]\n\t" \
  "v_mfma_f32_16x16x32_bf16 v[8:11], a[8:11], a[48:51], v[8:11]\n\t" \
  "s_add_u32 %[fw2], %[fw2], s98\n\t" \
  "ds_read_b64_tr_b16 a[150:151], v189 offset:50176\n\t" \
  "v_mfma_f32_16x16x32_bf16 v[40:43], a[8:11], a[52:55], v[40:43]\n\t" \
  "buffer_load_dwordx4 a[224:227], v248, %[ws], s97 offen sc1\n\t" \
  "v_mfma_f32_16x16x32_bf16 v[40:43], a[168:171], a[36:39], v[40:43]\n\t" \
  "s_and_b32 s98, %[bid], 0x1\n\t" \
  "ds_read_b64_tr_b16 a[152:153], v190 offset:49152\n\t" \
  "v_mfma_f32_16x16x32_bf16 v[72:75], a[168:171], a[40:43], v[72:75]\n\t" \
  "s_lshl_b32 s98, s98, 12\n\t" \
  "ds_read_b64_tr_b16 a[154:155], v190 offset:50176\n\t" \
  "v_mfma_f32_16x16x32_bf16 v[72:75], a[8:11], a[56:59], v[72:75]\n\t" \
  "s_add_u32 %[fw2], %[fw2], s98\n\t" \
  "ds_read_b64_tr_b16 a[156:157], v191 offset:49152\n\t" \
  "v_mfma_f32_16x16x32_bf16 v[104:107], a[8:11], a[60:63], v[104:107]\n\t" \
  "s_lshl2_add_u32 %[fw2], %[hl], %[fw2]\n\t" \
  "v_mfma_f32_16x16x32_bf16 v[104:107], a[168:171], a[44:47], v[104:107]\n\t" \
  "buffer_load_dwordx4 a[228:231], v249, %[ws], s97 offen sc1\n\t" \
  "v_mfma_f32_16x16x32_bf16 v[12:15], a[172:175], a[32:35], v[12:15]\n\t" \
  "s_lshl_b32 %[xso], %[wid], 15\n\t" \
  "ds_read_b64_tr_b16 a[158:159], v191 offset:50176\n\t" \
  "v_mfma_f32_16x16x32_bf16 v[12:15], a[12:15], a[48:51], v[12:15]\n\t" \
  "s_and_b32 s98, %[bid], 0x4\n\t" \
  "ds_read_b64_tr_b16 a[64:65], v184 offset:57344\n\t" \
  "v_mfma_f32_16x16x32_bf16 v[44:47], a[12:15], a[52:55], v[44:47]\n\t" \
  "s_lshl_b32 s98, s98, 19\n\t" \
  "ds_read_b64_tr_b16 a[66:67], v184 offset:58368\n\t" \
  "v_mfma_f32_16x16x32_bf16 v[44:47], a[172:175], a[36:39], v[44:47]\n\t" \
  "buffer_load_dwordx4 a[232:235], v250, %[ws], s97 offen sc1\n\t" \
  "v_mfma_f32_16x16x32_bf16 v[76:79], a[172:175], a[40:43], v[76:79]\n\t" \
  "s_add_u32 %[xso], %[xso], s98\n\t" \
  "ds_read_b64_tr_b16 a[68:69], v185 offset:57344\n\t" \
  "v_mfma_f32_16x16x32_bf16 v[76:79], a[12:15], a[56:59], v[76:79]\n\t" \
  "s_and_b32 s98, %[bid], 0xc0\n\t" \
  "ds_read_b64_tr_b16 a[70:71], v185 offset:58368\n\t" \
  "v_mfma_f32_16x16x32_bf16 v[108:111], a[12:15], a[60:63], v[108:111]\n\t" \
  "s_lshl_b32 s98, s98, 13\n\t" \
  "ds_read_b64_tr_b16 a[72:73], v186 offset:57344\n\t" \
  "v_mfma_f32_16x16x32_bf16 v[108:111], a[172:175], a[44:47], v[108:111]\n\t" \
  "s_add_u32 %[xso], %[xso], s98\n\t" \
  "v_mfma_f32_16x16x32_bf16 v[16:19], a[176:179], a[32:35], v[16:19]\n\t" \
  "buffer_load_dwordx4 a[236:239], v251, %[ws], s97 offen sc1\n\t" \
  "v_mfma_f32_16x16x32_bf16 v[16:19], a[16:19], a[48:51], v[16:19]\n\t" \
  "s_add_u32 s99, %[pf1], 2048\n\t" \
  "ds_read_b64_tr_b16 a[74:75], v186 offset:58368\n\t" \
  "v_mfma_f32_16x16x32_bf16 v[48:51], a[16:19], a[52:55], v[48:51]\n\t" \
  "s_and_b32 s98, %[bid], 0x3\n\t" \
  "ds_read_b64_tr_b16 a[76:77], v187 offset:57344\n\t" \
  "v_mfma_f32_16x16x32_bf16 v[48:51], a[176:179], a[36:39], v[48:51]\n\t" \
  "s_lshl_b32 s98, s98, 17\n\t" \
  "ds_read_b64_tr_b16 a[78:79], v187 offset:58368\n\t" \
  "v_mfma_f32_16x16x32_bf16 v[80:83], a[176:179], a[40:43], v[80:83]\n\t" \
  "buffer_load_dword %[pf], v217, %[wo], s99 offen\n\t" \
  "v_mfma_f32_16x16x32_bf16 v[80:83], a[16:19], a[56:59], v[80:83]\n\t" \
  "s_add_u32 s97, s97, %[dk]\n\t" \
  "ds_read_b64_tr_b16 a[80:81], v188 offset:57344\n\t" \
  "v_mfma_f32_16x16x32_bf16 v[112:115], a[16:19], a[60:63], v[112:115]\n\t" \
  "s_add_u32 %[xso], %[xso], s98\n\t" \
  "ds_read_b64_tr_b16 a[82:83], v188 offset:58368\n\t" \
  "v_mfma_f32_16x16x32_bf16 v[112:115], a[176:179], a[44:47], v[112:115]\n\t" \
  "s_and_b32 s98, %[bid], 0x38\n\t" \
  "ds_read_b64_tr_b16 a[84:85], v189 offset:57344\n\t" \
  "v_mfma_f32_16x16x32_bf16 v[20:23], a[180:183], a[32:35], v[20:23]\n\t" \
  "s_lshl_b32 s98, s98, 5\n\t" \
  "v_mfma_f32_16x16x32_bf16 v[20:23], a[20:23], a[48:51], v[20:23]\n\t" \
  "buffer_load_dwordx4 a[240:243], v248, %[ws], s97 offen sc1\n\t" \
  "v_mfma_f32_16x16x32_bf16 v[52:55], a[20:23], a[52:55], v[52:55]\n\t" \
  "s_add_u32 %[xso], %[xso], s98\n\t" \
  "ds_read_b64_tr_b16 a[86:87], v189 offset:58368\n\t" \
  "v_mfma_f32_16x16x32_bf16 v[52:55], a[180:183], a[36:39], v[52:55]\n\t" \
  "s_lshl_b32 %[ss], %[wid], 7\n\t" \
  "ds_read_b64_tr_b16 a[88:89], v190 offset:57344\n\t" \
  "v_mfma_f32_16x16x32_bf16 v[84:87], a[180:183], a[40:43], v[84:87]\n\t" \
  "s_add_u32 %[ss], %[ss], 0x240000\n\t" \
  "ds_read_b64_tr_b16 a[90:91], v190 offset:58368\n\t" \
  "v_mfma_f32_16x16x32_bf16 v[84:87], a[20:23], a[56:59], v[84:87]\n\t" \
  "s_and_b32 s98, %[bid], 0xff\n\t" \
  "ds_read_b64_tr_b16 a[92:93], v191 offset:57344\n\t" \
  "v_mfma_f32_16x16x32_bf16 v[116:119], a[20:23], a[60:63], v[116:119]\n\t" \
  "buffer_load_dwordx4 a[244:247], v249, %[ws], s97 offen sc1\n\t" \
  "v_mfma_f32_16x16x32_bf16 v[116:119], a[180:183], a[44:47], v[116:119]\n\t" \
  "s_lshl_b32 s98, s98, 9\n\t" \
  "ds_read_b64_tr_b16 a[94:95], v191 offset:58368\n\t" \
  "v_mfma_f32_16x16x32_bf16 v[24:27], a[184:187], a[32:35], v[24:27]\n\t" \
  "s_add_u32 %[ss], %[ss], s98\n\t" \
  "v_mfma_f32_16x16x32_bf16 v[24:27], a[24:27], a[48:51], v[24:27]\n\t" \
  "s_lshl_b32 %[mbp], %[wid], 7\n\t" \
  "v_mfma_f32_16x16x32_bf16 v[56:59], a[24:27], a[52:55], v[56:59]\n\t" \
  "buffer_load_dwordx4 a[248:251], v250, %[ws], s97 offen sc1\n\t" \
  "v_mfma_f32_16x16x32_bf16 v[56:59], a[184:187], a[36:39], v[56:59]\n\t" \
  "s_add_u32 %[mbp], %[mbp], 0x220000\n\t" \
  "v_mfma_f32_16x16x32_bf16 v[88:91], a[184:187], a[40:43], v[88:91]\n\t" \
  "s_and_b32 s98, %[bid], 0xc0\n\t" \
  "v_mfma_f32_16x16x32_bf16 v[88:91], a[24:27], a[56:59], v[88:91]\n\t" \
  "s_lshl_b32 s98, s98, 9\n\t" \
  "v_mfma_f32_16x16x32_bf16 v[120:123], a[24:27], a[60:63], v[120:123]\n\t" \
  "s_add_u32 %[mbp], %[mbp], s98\n\t" \
  "v_mfma_f32_16x16x32_bf16 v[120:123], a[184:187], a[44:47], v[120:123]\n\t" \
  "buffer_load_dwordx4 a[252:255], v251, %[ws], s97 offen sc1\n\t" \
  "v_mfma_f32_16x16x32_bf16 v[28:31], a[188:191], a[32:35], v[28:31]\n\t" \
  "s_and_b32 s98, %[bid], 0x3\n\t" \
  "v_mfma_f32_16x16x32_bf16 v[28:31], a[28:31], a[48:51], v[28:31]\n\t" \
  "s_lshl_b32 s98, s98, 13\n\t" \
  "v_mfma_f32_16x16x32_bf16 v[60:63], a[28:31], a[52:55], v[60:63]\n\t" \
  "s_add_u32 %[mbp], %[mbp], s98\n\t" \
  "v_mfma_f32_16x16x32_bf16 v[60:63], a[188:191], a[36:39], v[60:63]\n\t" \
  "s_and_b32 s98, %[bid], 0x4\n\t" \
  "v_mfma_f32_16x16x32_bf16 v[92:95], a[188:191], a[40:43], v[92:95]\n\t" \
  "s_lshl_b32 s98, s98, 10\n\t" \
  "v_mfma_f32_16x16x32_bf16 v[92:95], a[28:31], a[56:59], v[92:95]\n\t" \
  "s_add_u32 %[mbp], %[mbp], s98\n\t" \
  "v_mfma_f32_16x16x32_bf16 v[124:127], a[28:31], a[60:63], v[124:127]\n\t" \
  "s_and_b32 s98, %[bid], 0x38\n\t" \
  "v_mfma_f32_16x16x32_bf16 v[124:127], a[188:191], a[44:47], v[124:127]\n\t" \
  "s_waitcnt vmcnt(25) lgkmcnt(0)\n\t" \
  "s_barrier\n\t" \
  "v_mfma_f32_16x16x32_bf16 v[0:3], a[128:131], a[96:99], v[0:3]\n\t" \
  "s_add_u32 s96, %[rot19], 6815744\n\t" \
  "ds_read_b64_tr_b16 a[160:161], v192 offset:0\n\t" \
  "v_mfma_f32_16x16x32_bf16 v[0:3], a[64:67], a[112:115], v[0:3]\n\t" \
  "s_and_b32 s96, s96, 0x780000\n\t" \
  "v_mfma_f32_16x16x32_bf16 v[32:35], a[64:67], a[116:119], v[32:35]\n\t" \
  "s_add_u32 m0, %[mw], 49152\n\t" \
  "ds_read_b64_tr_b16 a[162:163], v192 offset:1024\n\t" \
  "v_mfma_f32_16x16x32_bf16 v[32:35], a[128:131], a[100:103], v[32:35]\n\t" \
  "buffer_load_dwordx4 v200, %[sw], s96 offen offset:0 lds\n\t" \
  "v_mfma_f32_16x16x32_bf16 v[64:67], a[128:131], a[104:107], v[64:67]\n\t" \
  "buffer_load_dwordx4 v201, %[sw], s96 offen offset:1024 lds\n\t" \
  "v_mfma_f32_16x16x32_bf16 v[64:67], a[64:67], a[120:123], v[64:67]\n\t" \
  "s_lshl_b32 s98, s98, 6\n\t" \
  "ds_read_b64_tr_b16 a[164:165], v193 offset:0\n\t" \
  "v_mfma_f32_16x16x32_bf16 v[96:99], a[64:67], a[124:127], v[96:99]\n\t" \
  "s_add_u32 %[mbp], %[mbp], s98\n\t" \
  "ds_read_b64_tr_b16 a[166:167], v193 offset:1024\n\t" \
  "v_mfma_f32_16x16x32_bf16 v[96:99], a[128:131], a[108:111], v[96:99]\n\t" \
  "s_lshr_b32 s98, %[bid], 6\n\t" \
  "ds_read_b64_tr_b16 a[168:169], v194 offset:0\n\t" \
  "v_mfma_f32_16x16x32_bf16 v[4:7], a[132:135], a[96:99], v[4:7]\n\t" \
  "s_lshl2_add_u32 %[gvo], s98, %[wid]\n\t" \
  "ds_read_b64_tr_b16 a[170:171], v194 offset:1024\n\t" \
  "v_mfma_f32_16x16x32_bf16 v[4:7], a[68:71], a[112:115], v[4:7]\n\t" \
  "buffer_load_dwordx4 v202, %[sw], s96 offen offset:2048 lds\n\t" \
  "v_mfma_f32_16x16x32_bf16 v[36:39], a[68:71], a[116:119], v[36:39]\n\t" \
  "s_mul_i32 %[gvo], %[gvo], 786432\n\t" \
  "ds_read_b64_tr_b16 a[172:173], v195 offset:0\n\t" \
  "v_mfma_f32_16x16x32_bf16 v[36:39], a[132:135], a[100:103], v[36:39]\n\t" \
  "s_and_b32 s98, %[bid], 0x38\n\t" \
  "ds_read_b64_tr_b16 a[174:175], v195 offset:1024\n\t" \
  "v_mfma_f32_16x16x32_bf16 v[68:71], a[132:135], a[104:107], v[68:71]\n\t" \
  "s_lshl_b32 s98, s98, 4\n\t" \
  "ds_read_b64_tr_b16 a[176:177], v196 offset:0\n\t" \
  "v_mfma_f32_16x16x32_bf16 v[68:71], a[68:71], a[120:123], v[68:71]\n\t" \
  "buffer_load_dwordx4 v203, %[sw], s96 offen offset:3072 lds\n\t" \
  "v_mfma_f32_16x16x32_bf16 v[100:103], a[68:71], a[124:127], v[100:103]\n\t" \
  "s_add_u32 s97, %[rot19], 6291456\n\t" \
  "ds_read_b64_tr_b16 a[178:179], v196 offset:1024\n\t" \
  "v_mfma_f32_16x16x32_bf16 v[100:103], a[132:135], a[108:111], v[100:103]\n\t" \
  "s_bfe_u32 s97, s97, 0x140003\n\t" \
  "ds_read_b64_tr_b16 a[180:181], v197 offset:0\n\t" \
  "v_mfma_f32_16x16x32_bf16 v[8:11], a[136:139], a[96:99], v[8:11]\n\t" \
  "s_add_u32 s97, s97, %[sox]\n\t" \
  "v_mfma_f32_16x16x32_bf16 v[8:11], a[72:75], a[112:115], v[8:11]\n\t" \
  "s_add_u32 %[gvo], %[gvo], s98\n\t" \
  "ds_read_b64_tr_b16 a[182:183], v197 offset:1024\n\t" \
  "v_mfma_f32_16x16x32_bf16 v[40:43], a[72:75], a[116:119], v[40:43]\n\t" \
  "buffer_load_dwordx4 a[32:35], v248, %[ws], s97 offen sc1\n\t" \
  "v_mfma_f32_16x16x32_bf16 v[40:43], a[136:139], a[100:103], v[40:43]\n\t" \
  "s_lshr_b32 s98, %[bid], 2\n\t" \
  "ds_read_b64_tr_b16 a[184:185], v198 offset:0\n\t" \
  "v_mfma_f32_16x16x32_bf16 v[72:75], a[136:139], a[104:107], v[72:75]\n\t" \
  "s_xor_b32 s98, s98, %[bid]\n\t" \
  "ds_read_b64_tr_b16 a[186:187], v198 offset:1024\n\t" \
  "v_mfma_f32_16x16x32_bf16 v[72:75], a[72:75], a[120:123], v[72:75]\n\t" \
  "s_and_b32 s98, s98, 4\n\t" \
  "ds_read_b64_tr_b16 a[188:189], v199 offset:0\n\t" \
  "v_mfma_f32_16x16x32_bf16 v[104:107], a[72:75], a[124:127], v[104:107]\n\t" \
  "s_lshl_b32 %[vo], s98, 4\n\t" \
  "v_mfma_f32_16x16x32_bf16 v[104:107], a[136:139], a[108:111], v[104:107]\n\t" \
  "buffer_load_dwordx4 a[36:39], v249, %[ws], s97 offen sc1\n\t" \
  "v_mfma_f32_16x16x32_bf16 v[12:15], a[140:143], a[96:99], v[12:15]\n\t" \
  "s_and_b32 s98, %[bid], 0x8\n\t" \
  "ds_read_b64_tr_b16 a[190:191], v199 offset:1024\n\t" \
  "v_mfma_f32_16x16x32_bf16 v[12:15], a[76:79], a[112:115], v[12:15]\n\t" \
  "s_lshl_b32 s98, s98, 2\n\t" \
  "ds_read_b64_tr_b16 a[0:1], v192 offset:8192\n\t" \
  "v_mfma_f32_16x16x32_bf16 v[44:47], a[76:79], a[116:119], v[44:47]\n\t" \
  "s_add_u32 %[vo], %[vo], s98\n\t" \
  "ds_read_b64_tr_b16 a[2:3], v192 offset:9216\n\t" \
  "v_mfma_f32_16x16x32_bf16 v[44:47], a[140:143], a[100:103], v[44:47]\n\t" \
  "buffer_load_dwordx4 a[40:43], v250, %[ws], s97 offen sc1\n\t" \
  "v_mfma_f32_16x16x32_bf16 v[76:79], a[140:143], a[104:107], v[76:79]\n\t" \
  "s_and_b32 s98, %[bid], 0x3\n\t" \
  "ds_read_b64_tr_b16 a[4:5], v193 offset:8192\n\t" \
  "v_mfma_f32_16x16x32_bf16 v[76:79], a[76:79], a[120:123], v[76:79]\n\t" \
  "s_lshl_b32 s98, s98, 3\n\t" \
  "ds_read_b64_tr_b16 a[6:7], v193 offset:9216\n\t" \
  "v_mfma_f32_16x16x32_bf16 v[108:111], a[76:79], a[124:127], v[108:111]\n\t" \
  "s_add_u32 %[vo], %[vo], s98\n\t" \
  "ds_read_b64_tr_b16 a[8:9], v194 offset:8192\n\t" \
  "v_mfma_f32_16x16x32_bf16 v[108:111], a[140:143], a[108:111], v[108:111]\n\t" \
  "s_and_b32 s98, %[bid], 0x4\n\t" \
  "v_mfma_f32_16x16x32_bf16 v[16:19], a[144:147], a[96:99], v[16:19]\n\t" \
  "buffer_load_dwordx4 a[44:47], v251, %[ws], s97 offen sc1\n\t" \
  "v_mfma_f32_16x16x32_bf16 v[16:19], a[80:83], a[112:115], v[16:19]\n\t" \
  "s_add_u32 s97, s97, %[dk]\n\t" \
  "ds_read_b64_tr_b16 a[10:11], v194 offset:9216\n\t" \
  "v_mfma_f32_16x16x32_bf16 v[48:51], a[80:83], a[116:119], v[48:51]\n\t" \
  "s_lshl_b32 s98, s98, 0\n\t" \
  "ds_read_b64_tr_b16 a[12:13], v195 offset:8192\n\t" \
  "v_mfma_f32_16x16x32_bf16 v[48:51], a[144:147], a[100:103], v[48:51]\n\t" \
  "s_add_u32 %[vo], %[vo], s98\n\t" \
  "ds_read_b64_tr_b16 a[14:15], v195 offset:9216\n\t" \
  "v_mfma_f32_16x16x32_bf16 v[80:83], a[144:147], a[104:107], v[80:83]\n\t" \
  "buffer_load_dwordx4 a[48:51], v248, %[ws], s97 offen sc1\n\t" \
  "v_mfma_f32_16x16x32_bf16 v[80:83], a[80:83], a[120:123], v[80:83]\n\t" \
  "s_add_u32 %[vo], %[vo], 0x2000\n\t" \
  "ds_read_b64_tr_b16 a[16:17], v196 offset:8192\n\t" \
  "v_mfma_f32_16x16x32_bf16 v[112:115], a[80:83], a[124:127], v[112:115]\n\t" \
  "ds_read_b64_tr_b16 a[18:19], v196 offset:9216\n\t" \
  "v_mfma_f32_16x16x32_bf16 v[112:115], a[144:147], a[108:111], v[112:115]\n\t" \
  "ds_read_b64_tr_b16 a[20:21], v197 offset:8192\n\t" \
  "v_mfma_f32_16x16x32_bf16 v[20:23], a[148:151], a[96:99], v[20:23]\n\t" \
  "s_cmp_lt_i32 %[prb], 0\n\t" \
  "s_cselect_b32 %[vo], %[prb], %[vo]\n\t" \
  "v_mfma_f32_16x16x32_bf16 v[20:23], a[84:87], a[112:115], v[20:23]\n\t" \
  "buffer_load_dwordx4 a[52:55], v249, %[ws], s97 offen sc1\n\t" \
  "v_mfma_f32_16x16x32_bf16 v[52:55], a[84:87], a[116:119], v[52:55]\n\t" \
  "ds_read_b64_tr_b16 a[22:23], v197 offset:9216\n\t" \
  "v_mfma_f32_16x16x32_bf16 v[52:55], a[148:151], a[100:103], v[52:55]\n\t" \
  "ds_read_b64_tr_b16 a[24:25], v198 offset:8192\n\t" \
  "v_mfma_f32_16x16x32_bf16 v[84:87], a[148:151], a[104:107], v[84:87]\n\t" \
  "ds_read_b64_tr_b16 a[26:27], v198 offset:9216\n\t" \
  "v_mfma_f32_16x16x32_bf16 v[84:87], a[84:87], a[120:123], v[84:87]\n\t" \
  "ds_read_b64_tr_b16 a[28:29], v199 offset:8192\n\t" \
  "v_mfma_f32_16x16x32_bf16 v[116:119], a[84:87], a[124:127], v[116:119]\n\t" \
  "buffer_load_dwordx4 a[56:59], v250, %[ws], s97 offen sc1\n\t" \
  "v_mfma_f32_16x16x32_bf16 v[116:119], a[148:151], a[108:111], v[116:119]\n\t" \
  "ds_read_b64_tr_b16 a[30:31], v199 offset:9216\n\t" \
  "v_mfma_f32_16x16x32_bf16 v[24:27], a[152:155], a[96:99], v[24:27]\n\t" \
  "v_mfma_f32_16x16x32_bf16 v[24:27], a[88:91], a[112:115], v[24:27]\n\t" \
  "v_mfma_f32_16x16x32_bf16 v[56:59], a[88:91], a[116:119], v[56:59]\n\t" \
  "buffer_load_dwordx4 a[60:63], v251, %[ws], s97 offen sc1\n\t" \
  "v_mfma_f32_16x16x32_bf16 v[56:59], a[152:155], a[100:103], v[56:59]\n\t" \
  "s_add_u32 s99, %[pf1], 4096\n\t" \
  "v_mfma_f32_16x16x32_bf16 v[88:91], a[152:155], a[104:107], v[88:91]\n\t" \
  "v_mfma_f32_16x16x32_bf16 v[88:91], a[88:91], a[120:123], v[88:91]\n\t" \
  "v_mfma_f32_16x16x32_bf16 v[120:123], a[88:91], a[124:127], v[120:123]\n\t" \
  "v_mfma_f32_16x16x32_bf16 v[120:123], a[152:155], a[108:111], v[120:123]\n\t" \
  "buffer_load_dword %[pf], v217, %[wo], s99 offen\n\t" \
  "v_mfma_f32_16x16x32_bf16 v[28:31], a[156:159], a[96:99], v[28:31]\n\t" \
  "v_mfma_f32_16x16x32_bf16 v[28:31], a[92:95], a[112:115], v[28:31]\n\t" \
  "v_mfma_f32_16x16x32_bf16 v[60:63], a[92:95], a[116:119], v[60:63]\n\t" \
  "v_mfma_f32_16x16x32_bf16 v[60:63], a[156:159], a[100:103], v[60:63]\n\t" \
  "v_mfma_f32_16x16x32_bf16 v[92:95], a[156:159], a[104:107], v[92:95]\n\t" \
  "v_mfma_f32_16x16x32_bf16 v[92:95], a[92:95], a[120:123], v[92:95]\n\t" \
  "v_mfma_f32_16x16x32_bf16 v[124:127], a[92:95], a[124:127], v[124:127]\n\t" \
  "v_mfma_f32_16x16x32_bf16 v[124:127], a[156:159], a[108:111], v[124:127]\n\t" \
  "s_waitcnt vmcnt(26) lgkmcnt(0)\n\t" \
  "s_barrier\n\t" \
  "v_mfma_f32_16x16x32_bf16 v[0:3], a[160:163], a[192:195], v[0:3]\n\t" \
  "s_add_u32 s96, %[rot19], 7340032\n\t" \
  "ds_read_b64_tr_b16 a[128:129], v184 offset:16384\n\t" \
  "v_mfma_f32_16x16x32_bf16 v[0:3], a[0:3], a[208:211], v[0:3]\n\t" \
  "s_and_b32 s96, s96, 0x780000\n\t" \
  "v_mfma_f32_16x16x32_bf16 v[32:35], a[0:3], a[212:215], v[32:35]\n\t" \
  "s_add_u32 m0, %[mw], 0\n\t" \
  "ds_read_b64_tr_b16 a[130:131], v184 offset:17408\n\t" \
  "v_mfma_f32_16x16x32_bf16 v[32:35], a[160:163], a[196:199], v[32:35]\n\t" \
  "buffer_load_dwordx4 v200, %[sw], s96 offen offset:0 lds\n\t" \
  "v_mfma_f32_16x16x32_bf16 v[64:67], a[160:163], a[200:203], v[64:67]\n\t" \
  "ds_read_b64_tr_b16 a[132:133], v185 offset:16384\n\t" \
  "v_mfma_f32_16x16x32_bf16 v[64:67], a[0:3], a[216:219], v[64:67]\n\t" \
  "buffer_load_dwordx4 v201, %[sw], s96 offen offset:1024 lds\n\t" \
  "v_mfma_f32_16x16x32_bf16 v[96:99], a[0:3], a[220:223], v[96:99]\n\t" \
  "ds_read_b64_tr_b16 a[134:135], v185 offset:17408\n\t" \
  "v_mfma_f32_16x16x32_bf16 v[96:99], a[160:163], a[204:207], v[96:99]\n\t" \
  "ds_read_b64_tr_b16 a[136:137], v186 offset:16384\n\t" \
  "v_mfma_f32_16x16x32_bf16 v[4:7], a[164:167], a[192:195], v[4:7]\n\t" \
  "ds_read_b64_tr_b16 a[138:139], v186 offset:17408\n\t" \
  "v_mfma_f32_16x16x32_bf16 v[4:7], a[4:7], a[208:211], v[4:7]\n\t" \
  "ds_read_b64_tr_b16 a[140:141], v187 offset:16384\n\t" \
  "v_mfma_f32_16x16x32_bf16 v[36:39], a[4:7], a[212:215], v[36:39]\n\t" \
  "buffer_load_dwordx4 v202, %[sw], s96 offen offset:2048 lds\n\t" \
  "v_mfma_f32_16x16x32_bf16 v[36:39], a[164:167], a[196:199], v[36:39]\n\t" \
  "ds_read_b64_tr_b16 a[142:143], v187 offset:17408\n\t" \
  "v_mfma_f32_16x16x32_bf16 v[68:71], a[164:167], a[200:203], v[68:71]\n\t" \
  "ds_read_b64_tr_b16 a[144:145], v188 offset:16384\n\t" \
  "v_mfma_f32_16x16x32_bf16 v[68:71], a[4:7], a[216:219], v[68:71]\n\t" \
  "v_mfma_f32_16x16x32_bf16 v[100:103], a[4:7], a[220:223], v[100:103]\n\t" \
  "ds_read_b64_tr_b16 a[146:147], v188 offset:17408\n\t" \
  "v_mfma_f32_16x16x32_bf16 v[100:103], a[164:167], a[204:207], v[100:103]\n\t" \
  "buffer_load_dwordx4 v203, %[sw], s96 offen offset:3072 lds\n\t" \
  "v_mfma_f32_16x16x32_bf16 v[8:11], a[168:171], a[192:195], v[8:11]\n\t" \
  "s_add_u32 s97, %[rot19], 6815744\n\t" \
  "ds_read_b64_tr_b16 a[148:149], v189 offset:16384\n\t" \
  "v_mfma_f32_16x16x32_bf16 v[8:11], a[8:11], a[208:211], v[8:11]\n\t" \
  "s_bfe_u32 s97, s97, 0x140003\n\t" \
  "ds_read_b64_tr_b16 a[150:151], v189 offset:17408\n\t" \
  "v_mfma_f32_16x16x32_bf16 v[40:43], a[8:11], a[212:215], v[40:43]\n\t" \
  "s_add_u32 s97, s97, %[sox]\n\t" \
  "ds_read_b64_tr_b16 a[152:153], v190 offset:16384\n\t" \
  "v_mfma_f32_16x16x32_bf16 v[40:43], a[168:171], a[196:199], v[40:43]\n\t" \
  "v_mfma_f32_16x16x32_bf16 v[72:75], a[168:171], a[200:203], v[72:75]\n\t" \
  "buffer_load_dwordx4 a[96:99], v248, %[ws], s97 offen sc1\n\t" \
  "v_mfma_f32_16x16x32_bf16 v[72:75], a[8:11], a[216:219], v[72:75]\n\t" \
  "ds_read_b64_tr_b16 a[154:155], v190 offset:17408\n\t" \
  "v_mfma_f32_16x16x32_bf16 v[104:107], a[8:11], a[220:223], v[104:107]\n\t" \
  "ds_read_b64_tr_b16 a[156:157], v191 offset:16384\n\t" \
  "v_mfma_f32_16x16x32_bf16 v[104:107], a[168:171], a[204:207], v[104:107]\n\t" \
  "ds_read_b64_tr_b16 a[158:159], v191 offset:17408\n\t" \
  "v_mfma_f32_16x16x32_bf16 v[12:15], a[172:175], a[192:195], v[12:15]\n\t" \
  "ds_read_b64_tr_b16 a[64:65], v184 offset:24576\n\t" \
  "v_mfma_f32_16x16x32_bf16 v[12:15], a[12:15], a[208:211], v[12:15]\n\t" \
  "buffer_load_dwordx4 a[100:103], v249, %[ws], s97 offen sc1\n\t" \
  "v_mfma_f32_16x16x32_bf16 v[44:47], a[12:15], a[212:215], v[44:47]\n\t" \
  "ds_read_b64_tr_b16 a[66:67], v184 offset:25600\n\t" \
  "v_mfma_f32_16x16x32_bf16 v[44:47], a[172:175], a[196:199], v[44:47]\n\t" \
  "ds_read_b64_tr_b16 a[68:69], v185 offset:24576\n\t" \
  "v_mfma_f32_16x16x32_bf16 v[76:79], a[172:175], a[200:203], v[76:79]\n\t" \
  "v_mfma_f32_16x16x32_bf16 v[76:79], a[12:15], a[216:219], v[76:79]\n\t" \
  "ds_read_b64_tr_b16 a[70:71], v185 offset:25600\n\t" \
  "v_mfma_f32_16x16x32_bf16 v[108:111], a[12:15], a[220:223], v[108:111]\n\t" \
  "buffer_load_dwordx4 a[104:107], v250, %[ws], s97 offen sc1\n\t" \
  "v_mfma_f32_16x16x32_bf16 v[108:111], a[172:175], a[204:207], v[108:111]\n\t" \
  "ds_read_b64_tr_b16 a[72:73], v186 offset:24576\n\t" \
  "v_mfma_f32_16x16x32_bf16 v[16:19], a[176:179], a[192:195], v[16:19]\n\t" \
  "ds_read_b64_tr_b16 a[74:75], v186 offset:25600\n\t" \
  "v_mfma_f32_16x16x32_bf16 v[16:19], a[16:19], a[208:211], v[16:19]\n\t" \
  "ds_read_b64_tr_b16 a[76:77], v187 offset:24576\n\t" \
  "v_mfma_f32_16x16x32_bf16 v[48:51], a[16:19], a[212:215], v[48:51]\n\t" \
  "v_mfma_f32_16x16x32_bf16 v[48:51], a[176:179], a[196:199], v[48:51]\n\t" \
  "buffer_load_dwordx4 a[108:111], v251, %[ws], s97 offen sc1\n\t" \
  "v_mfma_f32_16x16x32_bf16 v[80:83], a[176:179], a[200:203], v[80:83]\n\t" \
  "s_add_u32 s97, s97, %[dk]\n\t" \
  "ds_read_b64_tr_b16 a[78:79], v187 offset:25600\n\t" \
  "v_mfma_f32_16x16x32_bf16 v[80:83], a[16:19], a[216:219], v[80:83]\n\t" \
  "ds_read_b64_tr_b16 a[80:81], v188 offset:24576\n\t" \
  "v_mfma_f32_16x16x32_bf16 v[112:115], a[16:19], a[220:223], v[112:115]\n\t" \
  "ds_read_b64_tr_b16 a[82:83], v188 offset:25600\n\t" \
  "v_mfma_f32_16x16x32_bf16 v[112:115], a[176:179], a[204:207], v[112:115]\n\t" \
  "ds_read_b64_tr_b16 a[84:85], v189 offset:24576\n\t" \
  "v_mfma_f32_16x16x32_bf16 v[20:23], a[180:183], a[192:195], v[20:23]\n\t" \
  "buffer_load_dwordx4 a[112:115], v248, %[ws], s97 offen sc1\n\t" \
  "v_mfma_f32_16x16x32_bf16 v[20:23], a[20:23], a[208:211], v[20:23]\n\t" \
  "ds_read_b64_tr_b16 a[86:87], v189 offset:25600\n\t" \
  "v_mfma_f32_16x16x32_bf16 v[52:55], a[20:23], a[212:215], v[52:55]\n\t" \
  "ds_read_b64_tr_b16 a[88:89], v190 offset:24576\n\t" \
  "v_mfma_f32_16x16x32_bf16 v[52:55], a[180:183], a[196:199], v[52:55]\n\t" \
  "v_mfma_f32_16x16x32_bf16 v[84:87], a[180:183], a[200:203], v[84:87]\n\t" \
  "ds_read_b64_tr_b16 a[90:91], v190 offset:25600\n\t" \
  "v_mfma_f32_16x16x32_bf16 v[84:87], a[20:23], a[216:219], v[84:87]\n\t" \
  "buffer_load_dwordx4 a[116:119], v249, %[ws], s97 offen sc1\n\t" \
  "v_mfma_f32_16x16x32_bf16 v[116:119], a[20:23], a[220:223], v[116:119]\n\t" \
  "ds_read_b64_tr_b16 a[92:93], v191 offset:24576\n\t" \
  "v_mfma_f32_16x16x32_bf16 v[116:119], a[180:183], a[204:207], v[116:119]\n\t" \
  "ds_read_b64_tr_b16 a[94:95], v191 offset:25600\n\t" \
  "v_mfma_f32_16x16x32_bf16 v[24:27], a[184:187], a[192:195], v[24:27]\n\t" \
  "v_mfma_f32_16x16x32_bf16 v[24:27], a[24:27], a[208:211], v[24:27]\n\t" \
  "v_mfma_f32_16x16x32_bf16 v[56:59], a[24:27], a[212:215], v[56:59]\n\t" \
  "buffer_load_dwordx4 a[120:123], v250, %[ws], s97 offen sc1\n\t" \
  "v_mfma_f32_16x16x32_bf16 v[56:59], a[184:187], a[196:199], v[56:59]\n\t" \
  "v_mfma_f32_16x16x32_bf16 v[88:91], a[184:187], a[200:203], v[88:91]\n\t" \
  "v_mfma_f32_16x16x32_bf16 v[88:91], a[24:27], a[216:219], v[88:91]\n\t" \
  "v_mfma_f32_16x16x32_bf16 v[120:123], a[24:27], a[220:223], v[120:123]\n\t" \
  "v_mfma_f32_16x16x32_bf16 v[120:123], a[184:187], a[204:207], v[120:123]\n\t" \
  "buffer_load_dwordx4 a[124:127], v251, %[ws], s97 offen sc1\n\t" \
  "v_mfma_f32_16x16x32_bf16 v[28:31], a[188:191], a[192:195], v[28:31]\n\t" \
  "v_mfma_f32_16x16x32_bf16 v[28:31], a[28:31], a[208:211], v[28:31]\n\t" \
  "v_mfma_f32_16x16x32_bf16 v[60:63], a[28:31], a[212:215], v[60:63]\n\t" \
  "v_mfma_f32_16x16x32_bf16 v[60:63], a[188:191], a[196:199], v[60:63]\n\t" \
  "v_mfma_f32_16x16x32_bf16 v[92:95], a[188:191], a[200:203], v[92:95]\n\t" \
  "v_mfma_f32_16x16x32_bf16 v[92:95], a[28:31], a[216:219], v[92:95]\n\t" \
  "v_mfma_f32_16x16x32_bf16 v[124:127], a[28:31], a[220:223], v[124:127]\n\t" \
  "v_mfma_f32_16x16x32_bf16 v[124:127], a[188:191], a[204:207], v[124:127]\n\t" \
  "s_waitcnt vmcnt(25) lgkmcnt(0)\n\t" \
  "s_barrier\n\t" \
  "v_mfma_f32_16x16x32_bf16 v[0:3], a[128:131], a[224:227], v[0:3]\n\t" \
  "s_add_u32 s96, %[rot19], 7864320\n\t" \
  "ds_read_b64_tr_b16 a[160:161], v184 offset:32768\n\t" \
  "v_mfma_f32_16x16x32_bf16 v[0:3], a[64:67], a[240:243], v[0:3]\n\t" \
  "s_and_b32 s96, s96, 0x780000\n\t" \
  "v_mfma_f32_16x16x32_bf16 v[32:35], a[64:67], a[244:247], v[32:35]\n\t" \
  "s_add_u32 m0, %[mw], 98304\n\t" \
  "ds_read_b64_tr_b16 a[162:163], v184 offset:33792\n\t" \
  "v_mfma_f32_16x16x32_bf16 v[32:35], a[128:131], a[228:231], v[32:35]\n\t" \
  "buffer_load_dwordx4 v200, %[sw], s96 offen offset:0 lds\n\t" \
  "v_mfma_f32_16x16x32_bf16 v[64:67], a[128:131], a[232:235], v[64:67]\n\t" \
  "buffer_load_dwordx4 v201, %[sw], s96 offen offset:1024 lds\n\t" \
  "v_mfma_f32_16x16x32_bf16 v[64:67], a[64:67], a[248:251], v[64:67]\n\t" \
  "ds_read_b64_tr_b16 a[164:165], v185 offset:32768\n\t" \
  "v_mfma_f32_16x16x32_bf16 v[96:99], a[64:67], a[252:255], v[96:99]\n\t" \
  "ds_read_b64_tr_b16 a[166:167], v185 offset:33792\n\t" \
  "v_mfma_f32_16x16x32_bf16 v[96:99], a[128:131], a[236:239], v[96:99]\n\t" \
  "ds_read_b64_tr_b16 a[168:169], v186 offset:32768\n\t" \
  "v_mfma_f32_16x16x32_bf16 v[4:7], a[132:135], a[224:227], v[4:7]\n\t" \
  "ds_read_b64_tr_b16 a[170:171], v186 offset:33792\n\t" \
  "v_mfma_f32_16x16x32_bf16 v[4:7], a[68:71], a[240:243], v[4:7]\n\t" \
  "buffer_load_dwordx4 v202, %[sw], s96 offen offset:2048 lds\n\t" \
  "v_mfma_f32_16x16x32_bf16 v[36:39], a[68:71], a[244:247], v[36:39]\n\t" \
  "ds_read_b64_tr_b16 a[172:173], v187 offset:32768\n\t" \
  "v_mfma_f32_16x16x32_bf16 v[36:39], a[132:135], a[228:231], v[36:39]\n\t" \
  "ds_read_b64_tr_b16 a[174:175], v187 offset:33792\n\t" \
  "v_mfma_f32_16x16x32_bf16 v[68:71], a[132:135], a[232:235], v[68:71]\n\t" \
  "ds_read_b64_tr_b16 a[176:177], v188 offset:32768\n\t" \
  "v_mfma_f32_16x16x32_bf16 v[68:71], a[68:71], a[248:251], v[68:71]\n\t" \
  "buffer_load_dwordx4 v203, %[sw], s96 offen offset:3072 lds\n\t" \
  "v_mfma_f32_16x16x32_bf16 v[100:103], a[68:71], a[252:255], v[100:103]\n\t" \
  "s_add_u32 s97, %[rot19], 7340032\n\t" \
  "ds_read_b64_tr_b16 a[178:179], v188 offset:33792\n\t" \
  "v_mfma_f32_16x16x32_bf16 v[100:103], a[132:135], a[236:239], v[100:103]\n\t" \
  "s_bfe_u32 s97, s97, 0x140003\n\t" \
  "ds_read_b64_tr_b16 a[180:181], v189 offset:32768\n\t" \
  "v_mfma_f32_16x16x32_bf16 v[8:11], a[136:139], a[224:227], v[8:11]\n\t" \
  "s_add_u32 s97, s97, %[sox]\n\t" \
  "v_mfma_f32_16x16x32_bf16 v[8:11], a[72:75], a[240:243], v[8:11]\n\t" \
  "ds_read_b64_tr_b16 a[182:183], v189 offset:33792\n\t" \
  "v_mfma_f32_16x16x32_bf16 v[40:43], a[72:75], a[244:247], v[40:43]\n\t" \
  "buffer_load_dwordx4 a[192:195], v248, %[ws], s97 offen sc1\n\t" \
  "v_mfma_f32_16x16x32_bf16 v[40:43], a[136:139], a[228:231], v[40:43]\n\t" \
  "ds_read_b64_tr_b16 a[184:185], v190 offset:32768\n\t" \
  "v_mfma_f32_16x16x32_bf16 v[72:75], a[136:139], a[232:235], v[72:75]\n\t" \
  "ds_read_b64_tr_b16 a[186:187], v190 offset:33792\n\t" \
  "v_mfma_f32_16x16x32_bf16 v[72:75], a[72:75], a[248:251], v[72:75]\n\t" \
  "ds_read_b64_tr_b16 a[188:189], v191 offset:32768\n\t" \
  "v_mfma_f32_16x16x32_bf16 v[104:107], a[72:75], a[252:255], v[104:107]\n\t" \
  "v_mfma_f32_16x16x32_bf16 v[104:107], a[136:139], a[236:239], v[104:107]\n\t" \
  "buffer_load_dwordx4 a[196:199], v249, %[ws], s97 offen sc1\n\t" \
  "v_mfma_f32_16x16x32_bf16 v[12:15], a[140:143], a[224:227], v[12:15]\n\t" \
  "ds_read_b64_tr_b16 a[190:191], v191 offset:33792\n\t" \
  "v_mfma_f32_16x16x32_bf16 v[12:15], a[76:79], a[240:243], v[12:15]\n\t" \
  "ds_read_b64_tr_b16 a[0:1], v184 offset:40960\n\t" \
  "v_mfma_f32_16x16x32_bf16 v[44:47], a[76:79], a[244:247], v[44:47]\n\t" \
  "ds_read_b64_tr_b16 a[2:3], v184 offset:41984\n\t" \
  "v_mfma_f32_16x16x32_bf16 v[44:47], a[140:143], a[228:231], v[44:47]\n\t" \
  "buffer_load_dwordx4 a[200:203], v250, %[ws], s97 offen sc1\n\t" \
  "v_mfma_f32_16x16x32_bf16 v[76:79], a[140:143], a[232:235], v[76:79]\n\t" \
  "ds_read_b64_tr_b16 a[4:5], v185 offset:40960\n\t" \
  "v_mfma_f32_16x16x32_bf16 v[76:79], a[76:79], a[248:251], v[76:79]\n\t" \
  "ds_read_b64_tr_b16 a[6:7], v185 offset:41984\n\t" \
  "v_mfma_f32_16x16x32_bf16 v[108:111], a[76:79], a[252:255], v[108:111]\n\t" \
  "ds_read_b64_tr_b16 a[8:9], v186 offset:40960\n\t" \
  "v_mfma_f32_16x16x32_bf16 v[108:111], a[140:143], a[236:239], v[108:111]\n\t" \
  "v_mfma_f32_16x16x32_bf16 v[16:19], a[144:147], a[224:227], v[16:19]\n\t" \
  "buffer_load_dwordx4 a[204:207], v251, %[ws], s97 offen sc1\n\t" \
  "v_mfma_f32_16x16x32_bf16 v[16:19], a[80:83], a[240:243], v[16:19]\n\t" \
  "s_add_u32 s99, %[pf1], 6144\n\t" \
  "ds_read_b64_tr_b16 a[10:11], v186 offset:41984\n\t" \
  "v_mfma_f32_16x16x32_bf16 v[48:51], a[80:83], a[244:247], v[48:51]\n\t" \
  "ds_read_b64_tr_b16 a[12:13], v187 offset:40960\n\t" \
  ".Lhs4:\n\ts_cbranch_execz .Lhs4n\n\t" \
  "v_mfma_f32_16x16x32_bf16 v[48:51], a[144:147], a[228:231], v[48:51]\n\t" \
  "ds_read_b64_tr_b16 a[14:15], v187 offset:41984\n\t" \
  "v_mfma_f32_16x16x32_bf16 v[80:83], a[144:147], a[232:235], v[80:83]\n\t" \
  "buffer_load_dword %[pf], v217, %[wo], s99 offen\n\t" \
  "v_mfma_f32_16x16x32_bf16 v[80:83], a[80:83], a[248:251], v[80:83]\n\t" \
  "s_add_u32 s97, s97, %[dk]\n\t" \
  "ds_read_b64_tr_b16 a[16:17], v188 offset:40960\n\t" \
  "v_mfma_f32_16x16x32_bf16 v[112:115], a[80:83], a[252:255], v[112:115]\n\t" \
  "ds_read_b64_tr_b16 a[18:19], v188 offset:41984\n\t" \
  "v_mfma_f32_16x16x32_bf16 v[112:115], a[144:147], a[236:239], v[112:115]\n\t" \
  "ds_read_b64_tr_b16 a[20:21], v189 offset:40960\n\t" \
  "v_mfma_f32_16x16x32_bf16 v[20:23], a[148:151], a[224:227], v[20:23]\n\t" \
  "v_mfma_f32_16x16x32_bf16 v[20:23], a[84:87], a[240:243], v[20:23]\n\t" \
  "buffer_load_dwordx4 a[208:211], v248, %[ws], s97 offen sc1\n\t" \
  "v_mfma_f32_16x16x32_bf16 v[52:55], a[84:87], a[244:247], v[52:55]\n\t" \
  "ds_read_b64_tr_b16 a[22:23], v189 offset:41984\n\t" \
  "v_mfma_f32_16x16x32_bf16 v[52:55], a[148:151], a[228:231], v[52:55]\n\t" \
  "ds_read_b64_tr_b16 a[24:25], v190 offset:40960\n\t" \
  "v_mfma_f32_16x16x32_bf16 v[84:87], a[148:151], a[232:235], v[84:87]\n\t" \
  "ds_read_b64_tr_b16 a[26:27], v190 offset:41984\n\t" \
  "v_mfma_f32_16x16x32_bf16 v[84:87], a[84:87], a[248:251], v[84:87]\n\t" \
  "ds_read_b64_tr_b16 a[28:29], v191 offset:40960\n\t" \
  "v_mfma_f32_16x16x32_bf16 v[116:119], a[84:87], a[252:255], v[116:119]\n\t" \
  "buffer_load_dwordx4 a[212:215], v249, %[ws], s97 offen sc1\n\t" \
  "v_mfma_f32_16x16x32_bf16 v[116:119], a[148:151], a[236:239], v[116:119]\n\t" \
  "ds_read_b64_tr_b16 a[30:31], v191 offset:41984\n\t" \
  "v_mfma_f32_16x16x32_bf16 v[24:27], a[152:155], a[224:227], v[24:27]\n\t" \
  "v_mfma_f32_16x16x32_bf16 v[24:27], a[88:91], a[240:243], v[24:27]\n\t" \
  "v_mfma_f32_16x16x32_bf16 v[56:59], a[88:91], a[244:247], v[56:59]\n\t" \
  "buffer_load_dwordx4 a[216:219], v250, %[ws], s97 offen sc1\n\t" \
  "v_mfma_f32_16x16x32_bf16 v[56:59], a[152:155], a[228:231], v[56:59]\n\t" \
  "v_mfma_f32_16x16x32_bf16 v[88:91], a[152:155], a[232:235], v[88:91]\n\t" \
  "v_mfma_f32_16x16x32_bf16 v[88:91], a[88:91], a[248:251], v[88:91]\n\t" \
  "v_mfma_f32_16x16x32_bf16 v[120:123], a[88:91], a[252:255], v[120:123]\n\t" \
  "v_mfma_f32_16x16x32_bf16 v[120:123], a[152:155], a[236:239], v[120:123]\n\t" \
  "buffer_load_dwordx4 a[220:223], v251, %[ws], s97 offen sc1\n\t" \
  "v_mfma_f32_16x16x32_bf16 v[28:31], a[156:159], a[224:227], v[28:31]\n\t" \
  "v_mfma_f32_16x16x32_bf16 v[28:31], a[92:95], a[240:243], v[28:31]\n\t" \
  "v_mfma_f32_16x16x32_bf16 v[60:63], a[92:95], a[244:247], v[60:63]\n\t" \
  "v_mfma_f32_16x16x32_bf16 v[60:63], a[156:159], a[228:231], v[60:63]\n\t" \
  "v_mfma_f32_16x16x32_bf16 v[92:95], a[156:159], a[232:235], v[92:95]\n\t" \
  "v_mfma_f32_16x16x32_bf16 v[92:95], a[92:95], a[248:251], v[92:95]\n\t" \
  "v_mfma_f32_16x16x32_bf16 v[124:127], a[92:95], a[252:255], v[124:127]\n\t" \
  "v_mfma_f32_16x16x32_bf16 v[124:127], a[156:159], a[236:239], v[124:127]\n\t" \
  "s_waitcnt vmcnt(26) lgkmcnt(0)\n\t" \
  "s_barrier\n\t" \
  "v_mfma_f32_16x16x32_bf16 v[0:3], a[160:163], a[32:35], v[0:3]\n\t" \
  "s_add_u32 s97, %[rot19], 7864320\n\t" \
  "ds_read_b64_tr_b16 a[128:129], v184 offset:49152\n\t" \
  "v_mfma_f32_16x16x32_bf16 v[0:3], a[0:3], a[48:51], v[0:3]\n\t" \
  "s_bfe_u32 s97, s97, 0x140003\n\t" \
  "v_mfma_f32_16x16x32_bf16 v[32:35], a[0:3], a[52:55], v[32:35]\n\t" \
  "s_add_u32 s97, s97, %[sox]\n\t" \
  "ds_read_b64_tr_b16 a[130:131], v184 offset:50176\n\t" \
  "v_mfma_f32_16x16x32_bf16 v[32:35], a[160:163], a[36:39], v[32:35]\n\t" \
  "buffer_load_dwordx4 a[224:227], v248, %[ws], s97 offen sc1\n\t" \
  "v_mfma_f32_16x16x32_bf16 v[64:67], a[160:163], a[40:43], v[64:67]\n\t" \
  "ds_read_b64_tr_b16 a[132:133], v185 offset:49152\n\t" \
  "v_mfma_f32_16x16x32_bf16 v[64:67], a[0:3], a[56:59], v[64:67]\n\t" \
  "ds_read_b64_tr_b16 a[134:135], v185 offset:50176\n\t" \
  "v_mfma_f32_16x16x32_bf16 v[96:99], a[0:3], a[60:63], v[96:99]\n\t" \
  "buffer_load_dwordx4 a[228:231], v249, %[ws], s97 offen sc1\n\t" \
  "v_mfma_f32_16x16x32_bf16 v[96:99], a[160:163], a[44:47], v[96:99]\n\t" \
  "ds_read_b64_tr_b16 a[136:137], v186 offset:49152\n\t" \
  "v_mfma_f32_16x16x32_bf16 v[4:7], a[164:167], a[32:35], v[4:7]\n\t" \
  "ds_read_b64_tr_b16 a[138:139], v186 offset:50176\n\t" \
  "v_mfma_f32_16x16x32_bf16 v[4:7], a[4:7], a[48:51], v[4:7]\n\t" \
  "ds_read_b64_tr_b16 a[140:141], v187 offset:49152\n\t" \
  "v_mfma_f32_16x16x32_bf16 v[36:39], a[4:7], a[52:55], v[36:39]\n\t" \
  "v_mfma_f32_16x16x32_bf16 v[36:39], a[164:167], a[36:39], v[36:39]\n\t" \
  "ds_read_b64_tr_b16 a[142:143], v187 offset:50176\n\t" \
  "v_mfma_f32_16x16x32_bf16 v[68:71], a[164:167], a[40:43], v[68:71]\n\t" \
  "buffer_load_dwordx4 a[232:235], v250, %[ws], s97 offen sc1\n\t" \
  "v_mfma_f32_16x16x32_bf16 v[68:71], a[4:7], a[56:59], v[68:71]\n\t" \
  "ds_read_b64_tr_b16 a[144:145], v188 offset:49152\n\t" \
  "v_mfma_f32_16x16x32_bf16 v[100:103], a[4:7], a[60:63], v[100:103]\n\t" \
  "ds_read_b64_tr_b16 a[146:147], v188 offset:50176\n\t" \
  "v_mfma_f32_16x16x32_bf16 v[100:103], a[164:167], a[44:47], v[100:103]\n\t" \
  "ds_read_b64_tr_b16 a[148:149], v189 offset:49152\n\t" \
  "v_mfma_f32_16x16x32_bf16 v[8:11], a[168:171], a[32:35], v[8:11]\n\t" \
  "v_mfma_f32_16x16x32_bf16 v[8:11], a[8:11], a[48:51], v[8:11]\n\t" \
  "ds_read_b64_tr_b16 a[150:151], v189 offset:50176\n\t" \
  "v_mfma_f32_16x16x32_bf16 v[40:43], a[8:11], a[52:55], v[40:43]\n\t" \
  "buffer_load_dwordx4 a[236:239], v251, %[ws], s97 offen sc1\n\t" \
  "v_mfma_f32_16x16x32_bf16 v[40:43], a[168:171], a[36:39], v[40:43]\n\t" \
  "ds_read_b64_tr_b16 a[152:153], v190 offset:49152\n\t" \
  "v_mfma_f32_16x16x32_bf16 v[72:75], a[168:171], a[40:43], v[72:75]\n\t" \
  "s_add_u32 s97, s97, %[dk]\n\t" \
  "ds_read_b64_tr_b16 a[154:155], v190 offset:50176\n\t" \
  "v_mfma_f32_16x16x32_bf16 v[72:75], a[8:11], a[56:59], v[72:75]\n\t" \
  "ds_read_b64_tr_b16 a[156:157], v191 offset:49152\n\t" \
  "v_mfma_f32_16x16x32_bf16 v[104:107], a[8:11], a[60:63], v[104:107]\n\t" \
  "v_mfma_f32_16x16x32_bf16 v[104:107], a[168:171], a[44:47], v[104:107]\n\t" \
  "ds_read_b64_tr_b16 a[158:159], v191 offset:50176\n\t" \
  "v_mfma_f32_16x16x32_bf16 v[12:15], a[172:175], a[32:35], v[12:15]\n\t" \
  "buffer_load_dwordx4 a[240:243], v248, %[ws], s97 offen sc1\n\t" \
  "v_mfma_f32_16x16x32_bf16 v[12:15], a[12:15], a[48:51], v[12:15]\n\t" \
  "ds_read_b64_tr_b16 a[64:65], v184 offset:57344\n\t" \
  "v_mfma_f32_16x16x32_bf16 v[44:47], a[12:15], a[52:55], v[44:47]\n\t" \
  "ds_read_b64_tr_b16 a[66:67], v184 offset:58368\n\t" \
  "v_mfma_f32_16x16x32_bf16 v[44:47], a[172:175], a[36:39], v[44:47]\n\t" \
  "ds_read_b64_tr_b16 a[68:69], v185 offset:57344\n\t" \
  "v_mfma_f32_16x16x32_bf16 v[76:79], a[172:175], a[40:43], v[76:79]\n\t" \
  "v_mfma_f32_16x16x32_bf16 v[76:79], a[12:15], a[56:59], v[76:79]\n\t" \
  "ds_read_b64_tr_b16 a[70:71], v185 offset:58368\n\t" \
  "v_mfma_f32_16x16x32_bf16 v[108:111], a[12:15], a[60:63], v[108:111]\n\t" \
  "buffer_load_dwordx4 a[244:247], v249, %[ws], s97 offen sc1\n\t" \
  "v_mfma_f32_16x16x32_bf16 v[108:111], a[172:175], a[44:47], v[108:111]\n\t" \
  "ds_read_b64_tr_b16 a[72:73], v186 offset:57344\n\t" \
  "v_mfma_f32_16x16x32_bf16 v[16:19], a[176:179], a[32:35], v[16:19]\n\t" \
  "ds_read_b64_tr_b16 a[74:75], v186 offset:58368\n\t" \
  "v_mfma_f32_16x16x32_bf16 v[16:19], a[16:19], a[48:51], v[16:19]\n\t" \
  "ds_read_b64_tr_b16 a[76:77], v187 offset:57344\n\t" \
  "v_mfma_f32_16x16x32_bf16 v[48:51], a[16:19], a[52:55], v[48:51]\n\t" \
  "v_mfma_f32_16x16x32_bf16 v[48:51], a[176:179], a[36:39], v[48:51]\n\t" \
  "ds_read_b64_tr_b16 a[78:79], v187 offset:58368\n\t" \
  "v_mfma_f32_16x16x32_bf16 v[80:83], a[176:179], a[40:43], v[80:83]\n\t" \
  "buffer_load_dwordx4 a[248:251], v250, %[ws], s97 offen sc1\n\t" \
  "v_mfma_f32_16x16x32_bf16 v[80:83], a[16:19], a[56:59], v[80:83]\n\t" \
  "ds_read_b64_tr_b16 a[80:81], v188 offset:57344\n\t" \
  "v_mfma_f32_16x16x32_bf16 v[112:115], a[16:19], a[60:63], v[112:115]\n\t" \
  "ds_read_b64_tr_b16 a[82:83], v188 offset:58368\n\t" \
  "v_mfma_f32_16x16x32_bf16 v[112:115], a[176:179], a[44:47], v[112:115]\n\t" \
  "ds_read_b64_tr_b16 a[84:85], v189 offset:57344\n\t" \
  "v_mfma_f32_16x16x32_bf16 v[20:23], a[180:183], a[32:35], v[20:23]\n\t" \
  "v_mfma_f32_16x16x32_bf16 v[20:23], a[20:23], a[48:51], v[20:23]\n\t" \
  "ds_read_b64_tr_b16 a[86:87], v189 offset:58368\n\t" \
  "v_mfma_f32_16x16x32_bf16 v[52:55], a[20:23], a[52:55], v[52:55]\n\t" \
  "buffer_load_dwordx4 a[252:255], v251, %[ws], s97 offen sc1\n\t" \
  "v_mfma_f32_16x16x32_bf16 v[52:55], a[180:183], a[36:39], v[52:55]\n\t" \
  "ds_read_b64_tr_b16 a[88:89], v190 offset:57344\n\t" \
  "v_mfma_f32_16x16x32_bf16 v[84:87], a[180:183], a[40:43], v[84:87]\n\t" \
  "ds_read_b64_tr_b16 a[90:91], v190 offset:58368\n\t" \
  "v_mfma_f32_16x16x32_bf16 v[84:87], a[20:23], a[56:59], v[84:87]\n\t" \
  "ds_read_b64_tr_b16 a[92:93], v191 offset:57344\n\t" \
  "v_mfma_f32_16x16x32_bf16 v[116:119], a[20:23], a[60:63], v[116:119]\n\t" \
  "v_mfma_f32_16x16x32_bf16 v[116:119], a[180:183], a[44:47], v[116:119]\n\t" \
  "ds_read_b64_tr_b16 a[94:95], v191 offset:58368\n\t" \
  "v_mfma_f32_16x16x32_bf16 v[24:27], a[184:187], a[32:35], v[24:27]\n\t" \
  "buffer_load_dword %[pf], v217, %[ws], %[pto] offen sc1\n\t" \
  "v_mfma_f32_16x16x32_bf16 v[24:27], a[24:27], a[48:51], v[24:27]\n\t" \
  "v_mfma_f32_16x16x32_bf16 v[56:59], a[24:27], a[52:55], v[56:59]\n\t" \
  "s_mul_i32 s99, %[sofk], 5\n\t" \
  "v_mfma_f32_16x16x32_bf16 v[56:59], a[184:187], a[36:39], v[56:59]\n\t" \
  "s_xor_b32 s99, s99, 0\n\t" \
  "v_mfma_f32_16x16x32_bf16 v[88:91], a[184:187], a[40:43], v[88:91]\n\t" \
  "s_lshr_b32 s99, s99, 1\n\t" \
  "v_mfma_f32_16x16x32_bf16 v[88:91], a[24:27], a[56:59], v[88:91]\n\t" \
  "v_mfma_f32_16x16x32_bf16 v[120:123], a[24:27], a[60:63], v[120:123]\n\t" \
  "buffer_load_dword %[pf], v217, %[sD], s99 offen\n\t" \
  "v_mfma_f32_16x16x32_bf16 v[120:123], a[184:187], a[44:47], v[120:123]\n\t" \
  "v_mfma_f32_16x16x32_bf16 v[28:31], a[188:191], a[32:35], v[28:31]\n\t" \
  "v_mfma_f32_16x16x32_bf16 v[28:31], a[28:31], a[48:51], v[28:31]\n\t" \
  "v_mfma_f32_16x16x32_bf16 v[60:63], a[28:31], a[52:55], v[60:63]\n\t" \
  "v_mfma_f32_16x16x32_bf16 v[60:63], a[188:191], a[36:39], v[60:63]\n\t" \
  "v_mfma_f32_16x16x32_bf16 v[92:95], a[188:191], a[40:43], v[92:95]\n\t" \
  "v_mfma_f32_16x16x32_bf16 v[92:95], a[28:31], a[56:59], v[92:95]\n\t" \
  "v_mfma_f32_16x16x32_bf16 v[124:127], a[28:31], a[60:63], v[124:127]\n\t" \
  "v_mfma_f32_16x16x32_bf16 v[124:127], a[188:191], a[44:47], v[124:127]\n\t" \
  "s_waitcnt vmcnt(23) lgkmcnt(0)\n\t" \
  "s_barrier\n\t" \
  "v_mfma_f32_16x16x32_bf16 v[0:3], a[128:131], a[96:99], v[0:3]\n\t" \
  "s_mul_i32 s99, %[sofk], 5\n\t" \
  "ds_read_b64_tr_b16 a[160:161], v184 offset:0\n\t" \
  "v_mfma_f32_16x16x32_bf16 v[0:3], a[64:67], a[112:115], v[0:3]\n\t" \
  "s_xor_b32 s99, s99, 4096\n\t" \
  "v_mfma_f32_16x16x32_bf16 v[32:35], a[64:67], a[116:119], v[32:35]\n\t" \
  "s_lshr_b32 s99, s99, 1\n\t" \
  "ds_read_b64_tr_b16 a[162:163], v184 offset:1024\n\t" \
  "v_mfma_f32_16x16x32_bf16 v[32:35], a[128:131], a[100:103], v[32:35]\n\t" \
  "buffer_load_dword %[pf], v217, %[sD], s99 offen\n\t" \
  "v_mfma_f32_16x16x32_bf16 v[64:67], a[128:131], a[104:107], v[64:67]\n\t" \
  "s_add_u32 m0, %[mw], 16384\n\t" \
  "ds_read_b64_tr_b16 a[164:165], v185 offset:0\n\t" \
  "v_mfma_f32_16x16x32_bf16 v[64:67], a[64:67], a[120:123], v[64:67]\n\t" \
  "s_add_u32 s96, %[sofk], 0\n\t" \
  "ds_read_b64_tr_b16 a[166:167], v185 offset:1024\n\t" \
  "v_mfma_f32_16x16x32_bf16 v[96:99], a[64:67], a[124:127], v[96:99]\n\t" \
  "ds_read_b64_tr_b16 a[168:169], v186 offset:0\n\t" \
  "v_mfma_f32_16x16x32_bf16 v[96:99], a[128:131], a[108:111], v[96:99]\n\t" \
  "v_mfma_f32_16x16x32_bf16 v[4:7], a[132:135], a[96:99], v[4:7]\n\t" \
  "buffer_load_dwordx4 v224, %[sK], s96 offen offset:0 lds\n\t" \
  "v_mfma_f32_16x16x32_bf16 v[4:7], a[68:71], a[112:115], v[4:7]\n\t" \
  "ds_read_b64_tr_b16 a[170:171], v186 offset:1024\n\t" \
  "v_mfma_f32_16x16x32_bf16 v[36:39], a[68:71], a[116:119], v[36:39]\n\t" \
  "ds_read_b64_tr_b16 a[172:173], v187 offset:0\n\t" \
  "v_mfma_f32_16x16x32_bf16 v[36:39], a[132:135], a[100:103], v[36:39]\n\t" \
  "ds_read_b64_tr_b16 a[174:175], v187 offset:1024\n\t" \
  "v_mfma_f32_16x16x32_bf16 v[68:71], a[132:135], a[104:107], v[68:71]\n\t" \
  "ds_read_b64_tr_b16 a[176:177], v188 offset:0\n\t" \
  "v_mfma_f32_16x16x32_bf16 v[68:71], a[68:71], a[120:123], v[68:71]\n\t" \
  "v_mfma_f32_16x16x32_bf16 v[100:103], a[68:71], a[124:127], v[100:103]\n\t" \
  "ds_read_b64_tr_b16 a[178:179], v188 offset:1024\n\t" \
  "v_mfma_f32_16x16x32_bf16 v[100:103], a[132:135], a[108:111], v[100:103]\n\t" \
  "ds_read_b64_tr_b16 a[180:181], v189 offset:0\n\t" \
  "v_mfma_f32_16x16x32_bf16 v[8:11], a[136:139], a[96:99], v[8:11]\n\t" \
  "v_mfma_f32_16x16x32_bf16 v[8:11], a[72:75], a[112:115], v[8:11]\n\t" \
  "buffer_load_dwordx4 v225, %[sK], s96 offen offset:1024 lds\n\t" \
  "v_mfma_f32_16x16x32_bf16 v[40:43], a[72:75], a[116:119], v[40:43]\n\t" \
  "ds_read_b64_tr_b16 a[182:183], v189 offset:1024\n\t" \
  "v_mfma_f32_16x16x32_bf16 v[40:43], a[136:139], a[100:103], v[40:43]\n\t" \
  "ds_read_b64_tr_b16 a[184:185], v190 offset:0\n\t" \
  "v_mfma_f32_16x16x32_bf16 v[72:75], a[136:139], a[104:107], v[72:75]\n\t" \
  "ds_read_b64_tr_b16 a[186:187], v190 offset:1024\n\t" \
  "v_mfma_f32_16x16x32_bf16 v[72:75], a[72:75], a[120:123], v[72:75]\n\t" \
  "ds_read_b64_tr_b16 a[188:189], v191 offset:0\n\t" \
  "v_mfma_f32_16x16x32_bf16 v[104:107], a[72:75], a[124:127], v[104:107]\n\t" \
  "v_mfma_f32_16x16x32_bf16 v[104:107], a[136:139], a[108:111], v[104:107]\n\t" \
  "ds_read_b64_tr_b16 a[190:191], v191 offset:1024\n\t" \
  "v_mfma_f32_16x16x32_bf16 v[12:15], a[140:143], a[96:99], v[12:15]\n\t" \
  "ds_read_b64_tr_b16 a[0:1], v184 offset:8192\n\t" \
  "v_mfma_f32_16x16x32_bf16 v[12:15], a[76:79], a[112:115], v[12:15]\n\t" \
  "buffer_load_dwordx4 v226, %[sK], s96 offen offset:2048 lds\n\t" \
  "v_mfma_f32_16x16x32_bf16 v[44:47], a[76:79], a[116:119], v[44:47]\n\t" \
  "ds_read_b64_tr_b16 a[2:3], v184 offset:9216\n\t" \
  "v_mfma_f32_16x16x32_bf16 v[44:47], a[140:143], a[100:103], v[44:47]\n\t" \
  "ds_read_b64_tr_b16 a[4:5], v185 offset:8192\n\t" \
  "v_mfma_f32_16x16x32_bf16 v[76:79], a[140:143], a[104:107], v[76:79]\n\t" \
  "v_mfma_f32_16x16x32_bf16 v[76:79], a[76:79], a[120:123], v[76:79]\n\t" \
  "ds_read_b64_tr_b16 a[6:7], v185 offset:9216\n\t" \
  "v_mfma_f32_16x16x32_bf16 v[108:111], a[76:79], a[124:127], v[108:111]\n\t" \
  "ds_read_b64_tr_b16 a[8:9], v186 offset:8192\n\t" \
  "v_mfma_f32_16x16x32_bf16 v[108:111], a[140:143], a[108:111], v[108:111]\n\t" \
  "v_mfma_f32_16x16x32_bf16 v[16:19], a[144:147], a[96:99], v[16:19]\n\t" \
  "ds_read_b64_tr_b16 a[10:11], v186 offset:9216\n\t" \
  "v_mfma_f32_16x16x32_bf16 v[16:19], a[80:83], a[112:115], v[16:19]\n\t" \
  "ds_read_b64_tr_b16 a[12:13], v187 offset:8192\n\t" \
  "v_mfma_f32_16x16x32_bf16 v[48:51], a[80:83], a[116:119], v[48:51]\n\t" \
  "buffer_load_dwordx4 v227, %[sK], s96 offen offset:3072 lds\n\t" \
  "v_mfma_f32_16x16x32_bf16 v[48:51], a[144:147], a[100:103], v[48:51]\n\t" \
  "ds_read_b64_tr_b16 a[14:15], v187 offset:9216\n\t" \
  "v_mfma_f32_16x16x32_bf16 v[80:83], a[144:147], a[104:107], v[80:83]\n\t" \
  "ds_read_b64_tr_b16 a[16:17], v188 offset:8192\n\t" \
  "v_mfma_f32_16x16x32_bf16 v[80:83], a[80:83], a[120:123], v[80:83]\n\t" \
  "v_mfma_f32_16x16x32_bf16 v[112:115], a[80:83], a[124:127], v[112:115]\n\t" \
  "s_add_u32 m0, %[mw], 49152\n\t" \
  "ds_read_b64_tr_b16 a[18:19], v188 offset:9216\n\t" \
  "v_mfma_f32_16x16x32_bf16 v[112:115], a[144:147], a[108:111], v[112:115]\n\t" \
  "s_add_u32 s96, %[sofk], 0\n\t" \
  "ds_read_b64_tr_b16 a[20:21], v189 offset:8192\n\t" \
  "v_mfma_f32_16x16x32_bf16 v[20:23], a[148:151], a[96:99], v[20:23]\n\t" \
  "v_mfma_f32_16x16x32_bf16 v[20:23], a[84:87], a[112:115], v[20:23]\n\t" \
  "ds_read_b64_tr_b16 a[22:23], v189 offset:9216\n\t" \
  "v_mfma_f32_16x16x32_bf16 v[52:55], a[84:87], a[116:119], v[52:55]\n\t" \
  "buffer_load_dwordx4 v230, %[sV], s96 offen offset:0 lds\n\t" \
  "v_mfma_f32_16x16x32_bf16 v[52:55], a[148:151], a[100:103], v[52:55]\n\t" \
  "ds_read_b64_tr_b16 a[24:25], v190 offset:8192\n\t" \
  "v_mfma_f32_16x16x32_bf16 v[84:87], a[148:151], a[104:107], v[84:87]\n\t" \
  "ds_read_b64_tr_b16 a[26:27], v190 offset:9216\n\t" \
  "v_mfma_f32_16x16x32_bf16 v[84:87], a[84:87], a[120:123], v[84:87]\n\t" \
  "ds_read_b64_tr_b16 a[28:29], v191 offset:8192\n\t" \
  "v_mfma_f32_16x16x32_bf16 v[116:119], a[84:87], a[124:127], v[116:119]\n\t" \
  "v_mfma_f32_16x16x32_bf16 v[116:119], a[148:151], a[108:111], v[116:119]\n\t" \
  "ds_read_b64_tr_b16 a[30:31], v191 offset:9216\n\t" \
  "v_mfma_f32_16x16x32_bf16 v[24:27], a[152:155], a[96:99], v[24:27]\n\t" \
  "v_mfma_f32_16x16x32_bf16 v[24:27], a[88:91], a[112:115], v[24:27]\n\t" \
  "v_mfma_f32_16x16x32_bf16 v[56:59], a[88:91], a[116:119], v[56:59]\n\t" \
  "v_mfma_f32_16x16x32_bf16 v[56:59], a[152:155], a[100:103], v[56:59]\n\t" \
  "buffer_load_dwordx4 v231, %[sV], s96 offen offset:1024 lds\n\t" \
  "v_mfma_f32_16x16x32_bf16 v[88:91], a[152:155], a[104:107], v[88:91]\n\t" \
  "v_mfma_f32_16x16x32_bf16 v[88:91], a[88:91], a[120:123], v[88:91]\n\t" \
  "v_mfma_f32_16x16x32_bf16 v[120:123], a[88:91], a[124:127], v[120:123]\n\t" \
  "v_mfma_f32_16x16x32_bf16 v[120:123], a[152:155], a[108:111], v[120:123]\n\t" \
  "v_mfma_f32_16x16x32_bf16 v[28:31], a[156:159], a[96:99], v[28:31]\n\t" \
  "v_mfma_f32_16x16x32_bf16 v[28:31], a[92:95], a[112:115], v[28:31]\n\t" \
  "v_mfma_f32_16x16x32_bf16 v[60:63], a[92:95], a[116:119], v[60:63]\n\t" \
  "v_mfma_f32_16x16x32_bf16 v[60:63], a[156:159], a[100:103], v[60:63]\n\t" \
  "v_mfma_f32_16x16x32_bf16 v[92:95], a[156:159], a[104:107], v[92:95]\n\t" \
  "v_mfma_f32_16x16x32_bf16 v[92:95], a[92:95], a[120:123], v[92:95]\n\t" \
  "v_mfma_f32_16x16x32_bf16 v[124:127], a[92:95], a[124:127], v[124:127]\n\t" \
  "v_mfma_f32_16x16x32_bf16 v[124:127], a[156:159], a[108:111], v[124:127]\n\t" \
  "s_waitcnt vmcnt(17) lgkmcnt(0)\n\t" \
  "s_barrier\n\t" \
  "v_mfma_f32_16x16x32_bf16 v[0:3], a[160:163], a[192:195], v[0:3]\n\t" \
  "buffer_load_dwordx4 v230, %[sV], s96 offen offset:2048 lds\n\t" \
  "v_mfma_f32_16x16x32_bf16 v[0:3], a[0:3], a[208:211], v[0:3]\n\t" \
  "ds_read_b64_tr_b16 a[128:129], v192 offset:0\n\t" \
  "ds_read_b64_tr_b16 a[130:131], v192 offset:1024\n\t" \
  "v_mfma_f32_16x16x32_bf16 v[32:35], a[0:3], a[212:215], v[32:35]\n\t" \
  "ds_read_b64_tr_b16 a[132:133], v193 offset:0\n\t" \
  "ds_read_b64_tr_b16 a[134:135], v193 offset:1024\n\t" \
  "v_mfma_f32_16x16x32_bf16 v[32:35], a[160:163], a[196:199], v[32:35]\n\t" \
  "buffer_load_dwordx4 v231, %[sV], s96 offen offset:3072 lds\n\t" \
  "v_mfma_f32_16x16x32_bf16 v[4:7], a[164:167], a[192:195], v[4:7]\n\t" \
  "s_add_u32 m0, %[mw], 32768\n\t" \
  "ds_read_b64_tr_b16 a[136:137], v194 offset:0\n\t" \
  "ds_read_b64_tr_b16 a[138:139], v194 offset:1024\n\t" \
  "v_mfma_f32_16x16x32_bf16 v[4:7], a[4:7], a[208:211], v[4:7]\n\t" \
  "s_add_u32 s96, %[sofk], 16384\n\t" \
  "ds_read_b64_tr_b16 a[140:141], v195 offset:0\n\t" \
  "ds_read_b64_tr_b16 a[142:143], v195 offset:1024\n\t" \
  "v_mfma_f32_16x16x32_bf16 v[36:39], a[4:7], a[212:215], v[36:39]\n\t" \
  "ds_read_b64_tr_b16 a[144:145], v196 offset:0\n\t" \
  "ds_read_b64_tr_b16 a[146:147], v196 offset:1024\n\t" \
  "v_mfma_f32_16x16x32_bf16 v[36:39], a[164:167], a[196:199], v[36:39]\n\t" \
  "buffer_load_dwordx4 v224, %[sK], s96 offen offset:0 lds\n\t" \
  "v_mfma_f32_16x16x32_bf16 v[8:11], a[168:171], a[192:195], v[8:11]\n\t" \
  "ds_read_b64_tr_b16 a[148:149], v197 offset:0\n\t" \
  "ds_read_b64_tr_b16 a[150:151], v197 offset:1024\n\t" \
  "v_mfma_f32_16x16x32_bf16 v[8:11], a[8:11], a[208:211], v[8:11]\n\t" \
  "ds_read_b64_tr_b16 a[152:153], v198 offset:0\n\t" \
  "ds_read_b64_tr_b16 a[154:155], v198 offset:1024\n\t" \
  "v_mfma_f32_16x16x32_bf16 v[40:43], a[8:11], a[212:215], v[40:43]\n\t" \
  "buffer_load_dwordx4 v225, %[sK], s96 offen offset:1024 lds\n\t" \
  "v_mfma_f32_16x16x32_bf16 v[40:43], a[168:171], a[196:199], v[40:43]\n\t" \
  "ds_read_b64_tr_b16 a[156:157], v199 offset:0\n\t" \
  "ds_read_b64_tr_b16 a[158:159], v199 offset:1024\n\t" \
  "v_mfma_f32_16x16x32_bf16 v[12:15], a[172:175], a[192:195], v[12:15]\n\t" \
  "ds_read_b64_tr_b16 a[64:65], v192 offset:8192\n\t" \
  "ds_read_b64_tr_b16 a[66:67], v192 offset:9216\n\t" \
  "v_mfma_f32_16x16x32_bf16 v[12:15], a[12:15], a[208:211], v[12:15]\n\t" \
  "ds_read_b64_tr_b16 a[68:69], v193 offset:8192\n\t" \
  "ds_read_b64_tr_b16 a[70:71], v193 offset:9216\n\t" \
  "v_mfma_f32_16x16x32_bf16 v[44:47], a[12:15], a[212:215], v[44:47]\n\t" \
  "buffer_load_dwordx4 v226, %[sK], s96 offen offset:2048 lds\n\t" \
  "v_mfma_f32_16x16x32_bf16 v[44:47], a[172:175], a[196:199], v[44:47]\n\t" \
  "ds_read_b64_tr_b16 a[72:73], v194 offset:8192\n\t" \
  "ds_read_b64_tr_b16 a[74:75], v194 offset:9216\n\t" \
  "v_mfma_f32_16x16x32_bf16 v[16:19], a[176:179], a[192:195], v[16:19]\n\t" \
  "v_mfma_f32_16x16x32_bf16 v[16:19], a[16:19], a[208:211], v[16:19]\n\t" \
  "buffer_load_dwordx4 v227, %[sK], s96 offen offset:3072 lds\n\t" \
  "v_mfma_f32_16x16x32_bf16 v[48:51], a[16:19], a[212:215], v[48:51]\n\t" \
  "s_add_u32 s99, %[pf4], 0\n\t" \
  "ds_read_b64_tr_b16 a[76:77], v195 offset:8192\n\t" \
  "ds_read_b64_tr_b16 a[78:79], v195 offset:9216\n\t" \
  "v_mfma_f32_16x16x32_bf16 v[48:51], a[176:179], a[196:199], v[48:51]\n\t" \
  "ds_read_b64_tr_b16 a[80:81], v196 offset:8192\n\t" \
  "ds_read_b64_tr_b16 a[82:83], v196 offset:9216\n\t" \
  "v_mfma_f32_16x16x32_bf16 v[20:23], a[180:183], a[192:195], v[20:23]\n\t" \
  "ds_read_b64_tr_b16 a[84:85], v197 offset:8192\n\t" \
  "v_mfma_f32_16x16x32_bf16 v[20:23], a[20:23], a[208:211], v[20:23]\n\t" \
  "buffer_load_dword %[pf], v245, %[sU], s99 offen\n\t" \
  "v_mfma_f32_16x16x32_bf16 v[52:55], a[20:23], a[212:215], v[52:55]\n\t" \
  "s_add_u32 s99, %[pf4], 14336\n\t" \
  "ds_read_b64_tr_b16 a[86:87], v197 offset:9216\n\t" \
  "ds_read_b64_tr_b16 a[88:89], v198 offset:8192\n\t" \
  "v_mfma_f32_16x16x32_bf16 v[52:55], a[180:183], a[196:199], v[52:55]\n\t" \
  "ds_read_b64_tr_b16 a[90:91], v198 offset:9216\n\t" \
  "v_mfma_f32_16x16x32_bf16 v[24:27], a[184:187], a[192:195], v[24:27]\n\t" \
  "buffer_load_dword %[pf], v245, %[sU], s99 offen\n\t" \
  "v_mfma_f32_16x16x32_bf16 v[24:27], a[24:27], a[208:211], v[24:27]\n\t" \
  "ds_read_b64_tr_b16 a[92:93], v199 offset:8192\n\t" \
  "ds_read_b64_tr_b16 a[94:95], v199 offset:9216\n\t" \
  "v_mfma_f32_16x16x32_bf16 v[56:59], a[24:27], a[212:215], v[56:59]\n\t" \
  "v_mfma_f32_16x16x32_bf16 v[56:59], a[184:187], a[196:199], v[56:59]\n\t" \
  "v_mfma_f32_16x16x32_bf16 v[28:31], a[188:191], a[192:195], v[28:31]\n\t" \
  "v_mfma_f32_16x16x32_bf16 v[28:31], a[28:31], a[208:211], v[28:31]\n\t" \
  "v_mfma_f32_16x16x32_bf16 v[60:63], a[28:31], a[212:215], v[60:63]\n\t" \
  "v_mfma_f32_16x16x32_bf16 v[60:63], a[188:191], a[196:199], v[60:63]\n\t" \
  "s_waitcnt vmcnt(17)\n\t" \
  "s_waitcnt lgkmcnt(0)\n\t" \
  FSTP("s_waitcnt lgkmcnt(0)\n\t") \
  FSTP("v_writelane_b32 %[stv], s94, 52\n\t") \
  FSTP("v_writelane_b32 %[stv], s92, 53\n\t") \
  ".subsection 1\n\t" \
  ".Lfq_s:\n\t" \
  "s_mov_b32 s97, 0\n\t" \
  ".Lfq_r:\n\t" \
  "buffer_load_dword %[fl], v210, %[ws], %[fo] offen sc1\n\t" \
  "s_waitcnt vmcnt(0)\n\t" \
  "v_cmp_ne_u32 vcc, v211, %[fl]\n\t" \
  "s_cbranch_vccz .Lfq_k\n\t" \
  "s_add_u32 s97, s97, 1\n\t" \
  "s_cmp_lt_u32 s97, " FQ_CAP "\n\t" \
  "s_cbranch_scc1 .Lfq_r\n\t" \
  "buffer_store_dword v211, off, %[ws], 0 offset:256 sc1\n\t" \
  "s_branch .Lfq_k\n\t" \
  ".subsection 0\n\t"
#define FQ_CL "memory", "scc", "vcc", "s96", "s97", "s98", "s99", "v128", "v129", "v130", "v131", "v132", "v133", "v134", "v135", "v136", "v137", "v138", "v139", "v140", "v141", "v142", "v143", "v144", "v145", "v146", "v147", "v148", "v149", "v150", "v151", "v152", "v153", "v154", "v155", "v156", "v157", "v158", "v159", "v176", "v177", "v178", "v179", "v180", "v181", "v182", "v183", "v249", "v250", "v251" FQ_STC
#define FQ_X2E 0
#define FQ_W1L 1
#define FQ_W2E 0
static_assert(W_VOTE == 8192 && W_FLAGR == 32768 && W_FLAGO == 131072 && W_MODB == 2228224 && W_SIL == 2359296 && W_OT == 8388608 && W_XCH == 25165824);
#define FQ_EV 0
#define FQ_TE 0
#define FQ_SD [sU] "s"(c.sU), [sD] "s"(c.sD),
#define FQ_EB 0
#define FQ_CTO
#define FQ_XD 1
#define STS(x)
#define AT_STV
#define STA(x)
#define AT_ASM \
  "s_cmp_eq_u32 %[pm], 0\n\t" \
  "s_cbranch_scc1 .Lsf_go\n\t" \
  "v_mfma_f32_16x16x32_bf16 v[28:31], a[156:159], a[224:227], v[28:31]\n\t" \
  "v_mov_b32 v216, 0x3f803f80\n\t" \
  "v_lshlrev_b32 v204, 7, v233\n\t" \
  "v_mfma_f32_16x16x32_bf16 v[28:31], a[92:95], a[240:243], v[28:31]\n\t" \
  "v_cmp_gt_u32 vcc, 16, v233\n\t" \
  "v_mov_b32 v217, 0x3f803f80\n\t" \
  "v_mfma_f32_16x16x32_bf16 v[24:27], a[152:155], a[224:227], v[24:27]\n\t" \
  "v_lshrrev_b32 v202, 3, v233\n\t" \
  "v_mov_b32 v218, 0x3f803f80\n\t" \
  "v_mfma_f32_16x16x32_bf16 v[24:27], a[88:91], a[240:243], v[24:27]\n\t" \
  "v_mul_u32_u24 v202, 11264, v202\n\t" \
  "v_mov_b32 v219, 0x3f803f80\n\t" \
  "v_mfma_f32_16x16x32_bf16 v[20:23], a[148:151], a[224:227], v[20:23]\n\t" \
  "v_cvt_pk_bf16_f32 v31, v30, v31\n\t" \
  "v_add_u32 v202, v202, v204\n\t" \
  "v_mfma_f32_16x16x32_bf16 v[20:23], a[84:87], a[240:243], v[20:23]\n\t" \
  "v_cvt_pk_bf16_f32 v30, v28, v29\n\t" \
  "v_lshlrev_b32 v242, 4, v233\n\t" \
  "v_mfma_f32_16x16x32_bf16 v[16:19], a[144:147], a[224:227], v[16:19]\n\t" \
  "s_mov_b32 s99, 0\n\t" \
  "v_bfrev_b32 v195, 1\n\t" \
  "v_cvt_pk_bf16_f32 v28, v24, v25\n\t" \
  "v_mfma_f32_16x16x32_bf16 v[16:19], a[80:83], a[240:243], v[16:19]\n\t" \
  "v_cvt_pk_bf16_f32 v29, v26, v27\n\t" \
  "v_add_u32 v237, 65536, v228\n\t" \
  "v_mfma_f32_16x16x32_bf16 v[12:15], a[140:143], a[224:227], v[12:15]\n\t" \
  "v_cndmask_b32 v204, v195, v204, vcc\n\t" \
  "v_cndmask_b32 v202, v195, v202, vcc\n\t" \
  "v_mfma_f32_16x16x32_bf16 v[12:15], a[76:79], a[240:243], v[12:15]\n\t" \
  "v_cvt_pk_bf16_f32 v27, v22, v23\n\t" \
  "v_add_u32 v238, 65536, v229\n\t" \
  "v_mfma_f32_16x16x32_bf16 v[8:11], a[136:139], a[224:227], v[8:11]\n\t" \
  "s_mov_b32 %[na], 0\n\t" \
  "v_permlane16_swap_b32 v28, v30\n\t" \
  "v_mfma_f32_16x16x32_bf16 v[8:11], a[72:75], a[240:243], v[8:11]\n\t" \
  "v_cvt_pk_bf16_f32 v26, v20, v21\n\t" \
  "v_cvt_pk_bf16_f32 v24, v16, v17\n\t" \
  "v_mfma_f32_16x16x32_bf16 v[4:7], a[132:135], a[224:227], v[4:7]\n\t" \
  "v_permlane16_swap_b32 v29, v31\n\t" \
  "v_mfma_f32_16x16x32_bf16 v[4:7], a[68:71], a[240:243], v[4:7]\n\t" \
  "v_cvt_pk_bf16_f32 v25, v18, v19\n\t" \
  "v_cvt_pk_bf16_f32 v23, v14, v15\n\t" \
  "v_mfma_f32_16x16x32_bf16 v[0:3], a[128:131], a[224:227], v[0:3]\n\t" \
  "v_cvt_pk_bf16_f32 v22, v12, v13\n\t" \
  "v_cvt_pk_bf16_f32 v20, v8, v9\n\t" \
  "v_mfma_f32_16x16x32_bf16 v[0:3], a[64:67], a[240:243], v[0:3]\n\t" \
  "v_permlane16_swap_b32 v24, v26\n\t" \
  "v_mfma_f32_16x16x32_bf16 v[60:63], a[156:159], a[228:231], v[60:63]\n\t" \
  "v_cvt_pk_bf16_f32 v21, v10, v11\n\t" \
  "v_cvt_pk_bf16_f32 v19, v6, v7\n\t" \
  "v_mfma_f32_16x16x32_bf16 v[60:63], a[92:95], a[244:247], v[60:63]\n\t" \
  "v_permlane16_swap_b32 v25, v27\n\t" \
  "v_mfma_f32_16x16x32_bf16 v[56:59], a[152:155], a[228:231], v[56:59]\n\t" \
  "v_cvt_pk_bf16_f32 v18, v4, v5\n\t" \
  "v_cvt_pk_bf16_f32 v16, v0, v1\n\t" \
  "v_mfma_f32_16x16x32_bf16 v[56:59], a[88:91], a[244:247], v[56:59]\n\t" \
  "v_permlane16_swap_b32 v20, v22\n\t" \
  "v_mfma_f32_16x16x32_bf16 v[52:55], a[148:151], a[228:231], v[52:55]\n\t" \
  "v_cvt_pk_bf16_f32 v17, v2, v3\n\t" \
  "v_cvt_pk_bf16_f32 v63, v62, v63\n\t" \
  "v_mfma_f32_16x16x32_bf16 v[52:55], a[84:87], a[244:247], v[52:55]\n\t" \
  "v_permlane16_swap_b32 v21, v23\n\t" \
  "v_mfma_f32_16x16x32_bf16 v[48:51], a[144:147], a[228:231], v[48:51]\n\t" \
  "v_cvt_pk_bf16_f32 v62, v60, v61\n\t" \
  "v_cvt_pk_bf16_f32 v60, v56, v57\n\t" \
  "v_mfma_f32_16x16x32_bf16 v[48:51], a[80:83], a[244:247], v[48:51]\n\t" \
  "v_permlane16_swap_b32 v16, v18\n\t" \
  "v_mfma_f32_16x16x32_bf16 v[44:47], a[140:143], a[228:231], v[44:47]\n\t" \
  "v_cvt_pk_bf16_f32 v61, v58, v59\n\t" \
  "v_cvt_pk_bf16_f32 v59, v54, v55\n\t" \
  "v_mfma_f32_16x16x32_bf16 v[44:47], a[76:79], a[244:247], v[44:47]\n\t" \
  "v_permlane16_swap_b32 v17, v19\n\t" \
  "v_mfma_f32_16x16x32_bf16 v[40:43], a[136:139], a[228:231], v[40:43]\n\t" \
  "v_cvt_pk_bf16_f32 v58, v52, v53\n\t" \
  "v_cvt_pk_bf16_f32 v56, v48, v49\n\t" \
  "v_mfma_f32_16x16x32_bf16 v[40:43], a[72:75], a[244:247], v[40:43]\n\t" \
  "v_permlane16_swap_b32 v60, v62\n\t" \
  "v_mfma_f32_16x16x32_bf16 v[36:39], a[132:135], a[228:231], v[36:39]\n\t" \
  "v_cvt_pk_bf16_f32 v57, v50, v51\n\t" \
  "v_cvt_pk_bf16_f32 v55, v46, v47\n\t" \
  "v_mfma_f32_16x16x32_bf16 v[36:39], a[68:71], a[244:247], v[36:39]\n\t" \
  "v_permlane16_swap_b32 v61, v63\n\t" \
  "v_mfma_f32_16x16x32_bf16 v[32:35], a[128:131], a[228:231], v[32:35]\n\t" \
  "v_cvt_pk_bf16_f32 v54, v44, v45\n\t" \
  "v_cvt_pk_bf16_f32 v52, v40, v41\n\t" \
  "v_mfma_f32_16x16x32_bf16 v[32:35], a[64:67], a[244:247], v[32:35]\n\t" \
  "v_permlane16_swap_b32 v56, v58\n\t" \
  "v_mfma_f32_16x16x32_bf16 v[92:95], a[188:191], a[200:203], v[92:95]\n\t" \
  "v_cvt_pk_bf16_f32 v53, v42, v43\n\t" \
  "v_cvt_pk_bf16_f32 v51, v38, v39\n\t" \
  "v_mfma_f32_16x16x32_bf16 v[92:95], a[28:31], a[216:219], v[92:95]\n\t" \
  "v_permlane16_swap_b32 v57, v59\n\t" \
  "v_mfma_f32_16x16x32_bf16 v[92:95], a[156:159], a[232:235], v[92:95]\n\t" \
  "v_cvt_pk_bf16_f32 v50, v36, v37\n\t" \
  "v_cvt_pk_bf16_f32 v48, v32, v33\n\t" \
  "v_mfma_f32_16x16x32_bf16 v[92:95], a[92:95], a[248:251], v[92:95]\n\t" \
  "v_permlane16_swap_b32 v52, v54\n\t" \
  "v_mfma_f32_16x16x32_bf16 v[88:91], a[184:187], a[200:203], v[88:91]\n\t" \
  "v_cvt_pk_bf16_f32 v49, v34, v35\n\t" \
  "v_mfma_f32_16x16x32_bf16 v[88:91], a[24:27], a[216:219], v[88:91]\n\t" \
  "v_permlane16_swap_b32 v53, v55\n\t" \
  "v_mfma_f32_16x16x32_bf16 v[88:91], a[152:155], a[232:235], v[88:91]\n\t" \
  "v_permlane16_swap_b32 v48, v50\n\t" \
  "v_mfma_f32_16x16x32_bf16 v[88:91], a[88:91], a[248:251], v[88:91]\n\t" \
  "s_waitcnt vmcnt(2)\n\t" \
  "s_barrier\n\t" \
  "v_mfma_f32_16x16x32_bf16 v[84:87], a[180:183], a[200:203], v[84:87]\n\t" \
  "ds_read_b128 a[32:35], v220 offset:0\n\t" \
  "v_cvt_pk_bf16_f32 v95, v94, v95\n\t" \
  "v_mfma_f32_16x16x32_bf16 v[84:87], a[20:23], a[216:219], v[84:87]\n\t" \
  "ds_read_b128 a[36:39], v221 offset:0\n\t" \
  "v_cvt_pk_bf16_f32 v94, v92, v93\n\t" \
  "v_mfma_f32_16x16x32_bf16 v[84:87], a[148:151], a[232:235], v[84:87]\n\t" \
  "s_add_u32 m0, %[m], 0\n\t" \
  "ds_read_b128 a[40:43], v222 offset:0\n\t" \
  "v_cvt_pk_bf16_f32 v92, v88, v89\n\t" \
  "v_mfma_f32_16x16x32_bf16 v[84:87], a[84:87], a[248:251], v[84:87]\n\t" \
  "s_xor_b32 s97, %[wob], 0\n\t" \
  "ds_read_b128 a[44:47], v223 offset:0\n\t" \
  "v_cvt_pk_bf16_f32 v93, v90, v91\n\t" \
  "v_mfma_f32_16x16x32_bf16 v[80:83], a[176:179], a[200:203], v[80:83]\n\t" \
  "ds_read_b128 a[48:51], v220 offset:4096\n\t" \
  "ds_read_b128 a[52:55], v221 offset:4096\n\t" \
  "v_mfma_f32_16x16x32_bf16 v[80:83], a[16:19], a[216:219], v[80:83]\n\t" \
  "v_permlane16_swap_b32 v49, v51\n\t" \
  "v_mfma_f32_16x16x32_bf16 v[80:83], a[144:147], a[232:235], v[80:83]\n\t" \
  "ds_read_b128 a[56:59], v222 offset:4096\n\t" \
  "ds_read_b128 a[60:63], v223 offset:4096\n\t" \
  "v_mfma_f32_16x16x32_bf16 v[80:83], a[80:83], a[248:251], v[80:83]\n\t" \
  "ds_read_b128 a[96:99], v220 offset:8192\n\t" \
  "v_cvt_pk_bf16_f32 v91, v86, v87\n\t" \
  "v_mfma_f32_16x16x32_bf16 v[76:79], a[172:175], a[200:203], v[76:79]\n\t" \
  "ds_read_b128 a[100:103], v221 offset:8192\n\t" \
  "v_cvt_pk_bf16_f32 v90, v84, v85\n\t" \
  "v_mfma_f32_16x16x32_bf16 v[76:79], a[12:15], a[216:219], v[76:79]\n\t" \
  "v_permlane16_swap_b32 v92, v94\n\t" \
  "v_mfma_f32_16x16x32_bf16 v[76:79], a[140:143], a[232:235], v[76:79]\n\t" \
  "ds_read_b128 a[104:107], v222 offset:8192\n\t" \
  "v_cvt_pk_bf16_f32 v88, v80, v81\n\t" \
  "v_mfma_f32_16x16x32_bf16 v[76:79], a[76:79], a[248:251], v[76:79]\n\t" \
  "buffer_load_dwordx4 v240, %[wo], s97 offen offset:0 lds\n\t" \
  "v_mfma_f32_16x16x32_bf16 v[72:75], a[168:171], a[200:203], v[72:75]\n\t" \
  "s_add_u32 s97, s97, 7168\n\t" \
  "ds_read_b128 a[108:111], v223 offset:8192\n\t" \
  "v_cvt_pk_bf16_f32 v89, v82, v83\n\t" \
  "v_mfma_f32_16x16x32_bf16 v[72:75], a[8:11], a[216:219], v[72:75]\n\t" \
  "ds_read_b128 a[112:115], v220 offset:12288\n\t" \
  "ds_read_b128 a[116:119], v221 offset:12288\n\t" \
  "v_mfma_f32_16x16x32_bf16 v[72:75], a[136:139], a[232:235], v[72:75]\n\t" \
  "ds_read_b128 a[120:123], v222 offset:12288\n\t" \
  "ds_read_b128 a[124:127], v223 offset:12288\n\t" \
  "v_mfma_f32_16x16x32_bf16 v[72:75], a[72:75], a[248:251], v[72:75]\n\t" \
  "buffer_load_dwordx4 v241, %[wo], s97 offen offset:1024 lds\n\t" \
  "v_mfma_f32_16x16x32_bf16 v[68:71], a[164:167], a[200:203], v[68:71]\n\t" \
  "s_add_u32 s97, s97, 7168\n\t" \
  "v_permlane16_swap_b32 v93, v95\n\t" \
  "v_mfma_f32_16x16x32_bf16 v[68:71], a[4:7], a[216:219], v[68:71]\n\t" \
  "v_cvt_pk_bf16_f32 v87, v78, v79\n\t" \
  "v_cvt_pk_bf16_f32 v86, v76, v77\n\t" \
  "v_mfma_f32_16x16x32_bf16 v[68:71], a[132:135], a[232:235], v[68:71]\n\t" \
  "v_permlane16_swap_b32 v88, v90\n\t" \
  "v_mfma_f32_16x16x32_bf16 v[68:71], a[68:71], a[248:251], v[68:71]\n\t" \
  "buffer_load_dwordx4 v240, %[wo], s97 offen offset:2048 lds\n\t" \
  "v_mfma_f32_16x16x32_bf16 v[64:67], a[160:163], a[200:203], v[64:67]\n\t" \
  "s_add_u32 s97, s97, 7168\n\t" \
  "v_cvt_pk_bf16_f32 v84, v72, v73\n\t" \
  "v_cvt_pk_bf16_f32 v85, v74, v75\n\t" \
  "v_mfma_f32_16x16x32_bf16 v[64:67], a[0:3], a[216:219], v[64:67]\n\t" \
  "v_permlane16_swap_b32 v89, v91\n\t" \
  "v_mfma_f32_16x16x32_bf16 v[64:67], a[128:131], a[232:235], v[64:67]\n\t" \
  "v_permlane16_swap_b32 v84, v86\n\t" \
  "v_mfma_f32_16x16x32_bf16 v[64:67], a[64:67], a[248:251], v[64:67]\n\t" \
  "s_waitcnt lgkmcnt(0)\n\t" \
  "s_barrier\n\t" \
  STS("s_memrealtime s[94:95]\n\t") \
  STS("s_memtime s[92:93]\n\t") \
  "v_mfma_f32_16x16x32_bf16 v[0:3], a[32:35], v[16:19], 0\n\t" \
  "buffer_load_dwordx4 v241, %[wo], s97 offen offset:3072 lds\n\t" \
  "v_mfma_f32_16x16x32_bf16 v[0:3], a[36:39], v[20:23], v[0:3]\n\t" \
  "s_add_u32 m0, %[m], 16384\n\t" \
  "v_permlane16_swap_b32 v85, v87\n\t" \
  "v_mfma_f32_16x16x32_bf16 v[0:3], a[40:43], v[24:27], v[0:3]\n\t" \
  "s_xor_b32 s97, %[wob], 131072\n\t" \
  "v_cvt_pk_bf16_f32 v83, v70, v71\n\t" \
  "v_cvt_pk_bf16_f32 v82, v68, v69\n\t" \
  "v_mfma_f32_16x16x32_bf16 v[0:3], a[44:47], v[28:31], v[0:3]\n\t" \
  "v_cvt_pk_bf16_f32 v80, v64, v65\n\t" \
  "v_cvt_pk_bf16_f32 v81, v66, v67\n\t" \
  "v_mfma_f32_16x16x32_bf16 v[32:35], a[32:35], v[48:51], 0\n\t" \
  "v_permlane16_swap_b32 v80, v82\n\t" \
  ".Lhs5:\n\ts_cbranch_execz .Lhs5n\n\t" \
  "v_mfma_f32_16x16x32_bf16 v[32:35], a[36:39], v[52:55], v[32:35]\n\t" \
  "v_permlane16_swap_b32 v81, v83\n\t" \
  "v_mfma_f32_16x16x32_bf16 v[32:35], a[40:43], v[56:59], v[32:35]\n\t" \
  "v_mfma_f32_16x16x32_bf16 v[32:35], a[44:47], v[60:63], v[32:35]\n\t" \
  "v_max3_f32 v194, v0, v1, v2\n\t" \
  "v_max_f32 v194, v194, v3\n\t" \
  "v_mfma_f32_16x16x32_bf16 v[124:127], a[188:191], a[204:207], v[124:127]\n\t" \
  "v_mov_b32 v195, v194\n\t" \
  "v_mfma_f32_16x16x32_bf16 v[124:127], a[28:31], a[220:223], v[124:127]\n\t" \
  "v_mfma_f32_16x16x32_bf16 v[124:127], a[156:159], a[236:239], v[124:127]\n\t" \
  "v_permlane32_swap_b32 v194, v195\n\t" \
  "v_mfma_f32_16x16x32_bf16 v[124:127], a[92:95], a[252:255], v[124:127]\n\t" \
  "v_max_f32 v194, v194, v195\n\t" \
  "v_mov_b32 v195, v194\n\t" \
  "v_mfma_f32_16x16x32_bf16 v[120:123], a[184:187], a[204:207], v[120:123]\n\t" \
  STS("v_writelane_b32 %[stv], s94, 18\n\t") \
  STS("v_writelane_b32 %[stv], s92, 19\n\t") \
  "buffer_load_dwordx4 v240, %[wo], s97 offen offset:0 lds\n\t" \
  "v_mfma_f32_16x16x32_bf16 v[120:123], a[24:27], a[220:223], v[120:123]\n\t" \
  "s_add_u32 s97, s97, 7168\n\t" \
  "v_permlane16_swap_b32 v194, v195\n\t" \
  "v_mfma_f32_16x16x32_bf16 v[120:123], a[152:155], a[236:239], v[120:123]\n\t" \
  "v_max_f32 v194, v194, v195\n\t" \
  "v_sub_f32 v176, 0xc2cc0000, v194\n\t" \
  "v_mfma_f32_16x16x32_bf16 v[120:123], a[88:91], a[252:255], v[120:123]\n\t" \
  "v_sub_f32 v177, 0xc2cc0000, v194\n\t" \
  "v_sub_f32 v178, 0xc2cc0000, v194\n\t" \
  "v_mfma_f32_16x16x32_bf16 v[116:119], a[180:183], a[204:207], v[116:119]\n\t" \
  "v_sub_f32 v179, 0xc2cc0000, v194\n\t" \
  "v_add_f32 v0, v0, v176\n\t" \
  "v_mfma_f32_16x16x32_bf16 v[116:119], a[20:23], a[220:223], v[116:119]\n\t" \
  "v_add_f32 v1, v1, v176\n\t" \
  "v_add_f32 v2, v2, v176\n\t" \
  "v_mfma_f32_16x16x32_bf16 v[116:119], a[148:151], a[236:239], v[116:119]\n\t" \
  "v_add_f32 v3, v3, v176\n\t" \
  "v_max3_f32 v194, v32, v33, v34\n\t" \
  "v_mfma_f32_16x16x32_bf16 v[116:119], a[84:87], a[252:255], v[116:119]\n\t" \
  "v_max_f32 v194, v194, v35\n\t" \
  "v_mov_b32 v195, v194\n\t" \
  "v_mfma_f32_16x16x32_bf16 v[112:115], a[176:179], a[204:207], v[112:115]\n\t" \
  "buffer_load_dwordx4 v241, %[wo], s97 offen offset:1024 lds\n\t" \
  "v_mfma_f32_16x16x32_bf16 v[112:115], a[16:19], a[220:223], v[112:115]\n\t" \
  "s_add_u32 s97, s97, 7168\n\t" \
  "v_permlane32_swap_b32 v194, v195\n\t" \
  "v_mfma_f32_16x16x32_bf16 v[112:115], a[144:147], a[236:239], v[112:115]\n\t" \
  "v_max_f32 v194, v194, v195\n\t" \
  "v_mov_b32 v195, v194\n\t" \
  "v_mfma_f32_16x16x32_bf16 v[112:115], a[80:83], a[252:255], v[112:115]\n\t" \
  "buffer_load_dwordx4 v240, %[wo], s97 offen offset:2048 lds\n\t" \
  "v_mfma_f32_16x16x32_bf16 v[108:111], a[172:175], a[204:207], v[108:111]\n\t" \
  "s_add_u32 s97, s97, 7168\n\t" \
  "v_permlane16_swap_b32 v194, v195\n\t" \
  "v_mfma_f32_16x16x32_bf16 v[108:111], a[12:15], a[220:223], v[108:111]\n\t" \
  "v_max_f32 v194, v194, v195\n\t" \
  "v_sub_f32 v180, 0xc2cc0000, v194\n\t" \
  "v_mfma_f32_16x16x32_bf16 v[108:111], a[140:143], a[236:239], v[108:111]\n\t" \
  "v_sub_f32 v181, 0xc2cc0000, v194\n\t" \
  "v_sub_f32 v182, 0xc2cc0000, v194\n\t" \
  "v_mfma_f32_16x16x32_bf16 v[108:111], a[76:79], a[252:255], v[108:111]\n\t" \
  "v_sub_f32 v183, 0xc2cc0000, v194\n\t" \
  "v_add_f32 v32, v32, v180\n\t" \
  "v_mfma_f32_16x16x32_bf16 v[104:107], a[168:171], a[204:207], v[104:107]\n\t" \
  "v_add_f32 v33, v33, v180\n\t" \
  "v_add_f32 v34, v34, v180\n\t" \
  "v_mfma_f32_16x16x32_bf16 v[104:107], a[8:11], a[220:223], v[104:107]\n\t" \
  "v_add_f32 v35, v35, v180\n\t" \
  "v_cvt_pk_bf16_f32 v127, v126, v127\n\t" \
  "v_mfma_f32_16x16x32_bf16 v[104:107], a[136:139], a[236:239], v[104:107]\n\t" \
  "buffer_load_dwordx4 v241, %[wo], s97 offen offset:3072 lds\n\t" \
  "v_mfma_f32_16x16x32_bf16 v[104:107], a[72:75], a[252:255], v[104:107]\n\t" \
  "s_add_u32 s98, %[pf4], 28672\n\t" \
  "v_exp_f32 v0, v0\n\t" \
  "v_mfma_f32_16x16x32_bf16 v[100:103], a[164:167], a[204:207], v[100:103]\n\t" \
  "buffer_load_dword %[pfd], v247, %[ws], 0 offen sc1\n\t" \
  "v_mfma_f32_16x16x32_bf16 v[100:103], a[4:7], a[220:223], v[100:103]\n\t" \
  "s_add_u32 m0, %[m], 49152\n\t" \
  "v_exp_f32 v1, v1\n\t" \
  "v_mfma_f32_16x16x32_bf16 v[100:103], a[132:135], a[236:239], v[100:103]\n\t" \
  "buffer_load_dword %[pfd], v245, %[sU], s98 offen\n\t" \
  "v_mfma_f32_16x16x32_bf16 v[100:103], a[68:71], a[252:255], v[100:103]\n\t" \
  "s_add_u32 s96, %[sofk], 32768\n\t" \
  "v_exp_f32 v2, v2\n\t" \
  "v_mfma_f32_16x16x32_bf16 v[96:99], a[160:163], a[204:207], v[96:99]\n\t" \
  "v_exp_f32 v3, v3\n\t" \
  "v_mfma_f32_16x16x32_bf16 v[96:99], a[0:3], a[220:223], v[96:99]\n\t" \
  "v_cvt_pk_bf16_f32 v126, v124, v125\n\t" \
  "v_cvt_pk_bf16_f32 v128, v0, v1\n\t" \
  "v_mfma_f32_16x16x32_bf16 v[96:99], a[128:131], a[236:239], v[96:99]\n\t" \
  "v_cvt_pk_bf16_f32 v124, v120, v121\n\t" \
  "v_cvt_pk_bf16_f32 v129, v2, v3\n\t" \
  "v_mfma_f32_16x16x32_bf16 v[96:99], a[64:67], a[252:255], v[96:99]\n\t" \
  "v_cvt_pk_bf16_f32 v125, v122, v123\n\t" \
  "v_cvt_pk_bf16_f32 v123, v118, v119\n\t" \
  "v_mfma_f32_16x16x32_bf16 v[4:7], a[48:51], v[16:19], v[176:179]\n\t" \
  "ds_read_b128 a[192:195], v220 offset:16384\n\t" \
  "v_cvt_pk_bf16_f32 v122, v116, v117\n\t" \
  "v_mfma_f32_16x16x32_bf16 v[4:7], a[52:55], v[20:23], v[4:7]\n\t" \
  "ds_read_b128 a[196:199], v221 offset:16384\n\t" \
  "v_cvt_pk_bf16_f32 v120, v112, v113\n\t" \
  "v_mfma_f32_16x16x32_bf16 v[4:7], a[56:59], v[24:27], v[4:7]\n\t" \
  "ds_read_b128 a[200:203], v222 offset:16384\n\t" \
  "v_cvt_pk_bf16_f32 v121, v114, v115\n\t" \
  "v_mfma_f32_16x16x32_bf16 v[4:7], a[60:63], v[28:31], v[4:7]\n\t" \
  "ds_read_b128 a[204:207], v223 offset:16384\n\t" \
  "v_cvt_pk_bf16_f32 v119, v110, v111\n\t" \
  "v_mfma_f32_16x16x32_bf16 v[8:11], a[96:99], v[16:19], v[176:179]\n\t" \
  "buffer_load_dwordx4 v224, %[sK], s96 offen offset:0 lds\n\t" \
  "v_mfma_f32_16x16x32_bf16 v[8:11], a[100:103], v[20:23], v[8:11]\n\t" \
  "ds_read_b128 a[208:211], v220 offset:20480\n\t" \
  "v_cvt_pk_bf16_f32 v118, v108, v109\n\t" \
  "v_mfma_f32_16x16x32_bf16 v[8:11], a[104:107], v[24:27], v[8:11]\n\t" \
  "v_permlane16_swap_b32 v124, v126\n\t" \
  "v_mfma_f32_16x16x32_bf16 v[8:11], a[108:111], v[28:31], v[8:11]\n\t" \
  "v_exp_f32 v4, v4\n\t" \
  "v_mfma_f32_16x16x32_bf16 v[12:15], a[112:115], v[16:19], v[176:179]\n\t" \
  "v_cvt_pk_bf16_f32 v116, v104, v105\n\t" \
  "v_cvt_pk_bf16_f32 v117, v106, v107\n\t" \
  "v_mfma_f32_16x16x32_bf16 v[12:15], a[116:119], v[20:23], v[12:15]\n\t" \
  "ds_read_b128 a[212:215], v221 offset:20480\n\t" \
  "v_cvt_pk_bf16_f32 v115, v102, v103\n\t" \
  "v_mfma_f32_16x16x32_bf16 v[12:15], a[120:123], v[24:27], v[12:15]\n\t" \
  "buffer_load_dwordx4 v225, %[sK], s96 offen offset:1024 lds\n\t" \
  "v_mfma_f32_16x16x32_bf16 v[12:15], a[124:127], v[28:31], v[12:15]\n\t" \
  "ds_read_b128 a[216:219], v222 offset:20480\n\t" \
  "v_cvt_pk_bf16_f32 v114, v100, v101\n\t" \
  "v_mfma_f32_16x16x32_bf16 v[64:67], a[32:35], v[80:83], 0\n\t" \
  "ds_read_b128 a[220:223], v223 offset:20480\n\t" \
  "v_cvt_pk_bf16_f32 v112, v96, v97\n\t" \
  "v_mfma_f32_16x16x32_bf16 v[64:67], a[36:39], v[84:87], v[64:67]\n\t" \
  "ds_read_b128 a[224:227], v220 offset:24576\n\t" \
  "v_cvt_pk_bf16_f32 v113, v98, v99\n\t" \
  "v_mfma_f32_16x16x32_bf16 v[64:67], a[40:43], v[88:91], v[64:67]\n\t" \
  "v_permlane16_swap_b32 v125, v127\n\t" \
  "v_mfma_f32_16x16x32_bf16 v[64:67], a[44:47], v[92:95], v[64:67]\n\t" \
  "v_exp_f32 v5, v5\n\t" \
  "v_mfma_f32_16x16x32_bf16 v[36:39], a[48:51], v[48:51], v[180:183]\n\t" \
  "buffer_load_dwordx4 v226, %[sK], s96 offen offset:2048 lds\n\t" \
  "v_mfma_f32_16x16x32_bf16 v[36:39], a[52:55], v[52:55], v[36:39]\n\t" \
  "ds_read_b128 a[228:231], v221 offset:24576\n\t" \
  "v_cvt_pk_bf16_f32 v130, v4, v5\n\t" \
  "v_mfma_f32_16x16x32_bf16 v[36:39], a[56:59], v[56:59], v[36:39]\n\t" \
  "ds_read_b128 a[232:235], v222 offset:24576\n\t" \
  "v_max3_f32 v194, v64, v65, v66\n\t" \
  "v_mfma_f32_16x16x32_bf16 v[36:39], a[60:63], v[60:63], v[36:39]\n\t" \
  "ds_read_b128 a[236:239], v223 offset:24576\n\t" \
  "v_max_f32 v194, v194, v67\n\t" \
  "v_mfma_f32_16x16x32_bf16 v[40:43], a[96:99], v[48:51], v[180:183]\n\t" \
  "ds_read_b128 a[240:243], v220 offset:28672\n\t" \
  "v_mov_b32 v195, v194\n\t" \
  "v_mfma_f32_16x16x32_bf16 v[40:43], a[100:103], v[52:55], v[40:43]\n\t" \
  "v_permlane16_swap_b32 v120, v122\n\t" \
  "v_mfma_f32_16x16x32_bf16 v[40:43], a[104:107], v[56:59], v[40:43]\n\t" \
  "v_permlane32_swap_b32 v194, v195\n\t" \
  "v_mfma_f32_16x16x32_bf16 v[40:43], a[108:111], v[60:63], v[40:43]\n\t" \
  "ds_read_b128 a[244:247], v221 offset:28672\n\t" \
  "v_max_f32 v194, v194, v195\n\t" \
  "v_mfma_f32_16x16x32_bf16 v[44:47], a[112:115], v[48:51], v[180:183]\n\t" \
  "ds_read_b128 a[248:251], v222 offset:28672\n\t" \
  "v_mov_b32 v195, v194\n\t" \
  "v_mfma_f32_16x16x32_bf16 v[44:47], a[116:119], v[52:55], v[44:47]\n\t" \
  "buffer_load_dwordx4 v227, %[sK], s96 offen offset:3072 lds\n\t" \
  "v_mfma_f32_16x16x32_bf16 v[44:47], a[120:123], v[56:59], v[44:47]\n\t" \
  "s_add_u32 m0, %[m], 147456\n\t" \
  "v_permlane16_swap_b32 v194, v195\n\t" \
  "v_mfma_f32_16x16x32_bf16 v[44:47], a[124:127], v[60:63], v[44:47]\n\t" \
  "s_add_u32 s96, %[sofk], 16384\n\t" \
  "ds_read_b128 a[252:255], v223 offset:28672\n\t" \
  "v_max_f32 v194, v194, v195\n\t" \
  "v_permlane16_swap_b32 v121, v123\n\t" \
  "v_permlane16_swap_b32 v116, v118\n\t" \
  "v_permlane16_swap_b32 v117, v119\n\t" \
  "v_permlane16_swap_b32 v112, v114\n\t" \
  "v_permlane16_swap_b32 v113, v115\n\t" \
  "s_nop 1\n\t" \
  "v_mfma_f32_16x16x32_bf16 v[96:99], a[32:35], v[112:115], 0\n\t" \
  "ds_read_b64_tr_b16 a[128:129], v228 offset:0\n\t" \
  "v_sub_f32 v184, 0xc2cc0000, v194\n\t" \
  "v_mfma_f32_16x16x32_bf16 v[96:99], a[36:39], v[116:119], v[96:99]\n\t" \
  "ds_read_b64_tr_b16 a[130:131], v228 offset:4096\n\t" \
  "v_sub_f32 v185, 0xc2cc0000, v194\n\t" \
  "v_mfma_f32_16x16x32_bf16 v[96:99], a[40:43], v[120:123], v[96:99]\n\t" \
  "ds_read_b64_tr_b16 a[132:133], v228 offset:8192\n\t" \
  "v_sub_f32 v186, 0xc2cc0000, v194\n\t" \
  "v_mfma_f32_16x16x32_bf16 v[96:99], a[44:47], v[124:127], v[96:99]\n\t" \
  "ds_read_b64_tr_b16 a[134:135], v228 offset:12288\n\t" \
  "v_sub_f32 v187, 0xc2cc0000, v194\n\t" \
  "s_nop 1\n\t" \
  "v_mfma_f32_16x16x32_bf16 v[68:71], a[48:51], v[80:83], v[184:187]\n\t" \
  "ds_read_b64_tr_b16 a[136:137], v229 offset:0\n\t" \
  "v_add_f32 v64, v64, v184\n\t" \
  "v_mfma_f32_16x16x32_bf16 v[68:71], a[52:55], v[84:87], v[68:71]\n\t" \
  "ds_read_b64_tr_b16 a[138:139], v229 offset:4096\n\t" \
  "v_add_f32 v65, v65, v184\n\t" \
  "v_mfma_f32_16x16x32_bf16 v[68:71], a[56:59], v[88:91], v[68:71]\n\t" \
  "ds_read_b64_tr_b16 a[140:141], v229 offset:8192\n\t" \
  "v_add_f32 v66, v66, v184\n\t" \
  "v_mfma_f32_16x16x32_bf16 v[68:71], a[60:63], v[92:95], v[68:71]\n\t" \
  "v_add_f32 v67, v67, v184\n\t" \
  "v_max3_f32 v194, v96, v97, v98\n\t" \
  "v_mfma_f32_16x16x32_bf16 v[72:75], a[96:99], v[80:83], v[184:187]\n\t" \
  "ds_read_b64_tr_b16 a[142:143], v229 offset:12288\n\t" \
  "v_max_f32 v194, v194, v99\n\t" \
  "v_mfma_f32_16x16x32_bf16 v[72:75], a[100:103], v[84:87], v[72:75]\n\t" \
  "v_mov_b32 v195, v194\n\t" \
  "v_mfma_f32_16x16x32_bf16 v[72:75], a[104:107], v[88:91], v[72:75]\n\t" \
  "v_exp_f32 v6, v6\n\t" \
  "v_mfma_f32_16x16x32_bf16 v[72:75], a[108:111], v[92:95], v[72:75]\n\t" \
  "v_permlane32_swap_b32 v194, v195\n\t" \
  "v_mfma_f32_16x16x32_bf16 v[76:79], a[112:115], v[80:83], v[184:187]\n\t" \
  "ds_read_b64_tr_b16 a[144:145], v228 offset:256\n\t" \
  "v_max_f32 v194, v194, v195\n\t" \
  "v_mfma_f32_16x16x32_bf16 v[76:79], a[116:119], v[84:87], v[76:79]\n\t" \
  "ds_read_b64_tr_b16 a[146:147], v228 offset:4352\n\t" \
  "v_mov_b32 v195, v194\n\t" \
  "v_mfma_f32_16x16x32_bf16 v[76:79], a[120:123], v[88:91], v[76:79]\n\t" \
  "buffer_load_dwordx4 v230, %[sV], s96 offen offset:0 lds\n\t" \
  "v_mfma_f32_16x16x32_bf16 v[76:79], a[124:127], v[92:95], v[76:79]\n\t" \
  "v_permlane16_swap_b32 v194, v195\n\t" \
  "v_max_f32 v194, v194, v195\n\t" \
  "v_sub_f32 v188, 0xc2cc0000, v194\n\t" \
  "v_sub_f32 v189, 0xc2cc0000, v194\n\t" \
  "v_sub_f32 v190, 0xc2cc0000, v194\n\t" \
  "v_sub_f32 v191, 0xc2cc0000, v194\n\t" \
  "s_nop 1\n\t" \
  "v_mfma_f32_16x16x32_bf16 v[100:103], a[48:51], v[112:115], v[188:191]\n\t" \
  "ds_read_b64_tr_b16 a[148:149], v228 offset:8448\n\t" \
  "v_add_f32 v96, v96, v188\n\t" \
  "v_mfma_f32_16x16x32_bf16 v[100:103], a[52:55], v[116:119], v[100:103]\n\t" \
  "v_add_f32 v97, v97, v188\n\t" \
  "v_add_f32 v98, v98, v188\n\t" \
  "v_mfma_f32_16x16x32_bf16 v[100:103], a[56:59], v[120:123], v[100:103]\n\t" \
  "v_add_f32 v99, v99, v188\n\t" \
  "v_mfma_f32_16x16x32_bf16 v[100:103], a[60:63], v[124:127], v[100:103]\n\t" \
  "buffer_load_dwordx4 v231, %[sV], s96 offen offset:1024 lds\n\t" \
  "v_mfma_f32_16x16x32_bf16 v[104:107], a[96:99], v[112:115], v[188:191]\n\t" \
  "v_exp_f32 v7, v7\n\t" \
  "v_mfma_f32_16x16x32_bf16 v[104:107], a[100:103], v[116:119], v[104:107]\n\t" \
  "v_exp_f32 v8, v8\n\t" \
  "v_mfma_f32_16x16x32_bf16 v[104:107], a[104:107], v[120:123], v[104:107]\n\t" \
  "v_exp_f32 v9, v9\n\t" \
  "v_mfma_f32_16x16x32_bf16 v[104:107], a[108:111], v[124:127], v[104:107]\n\t" \
  "v_exp_f32 v10, v10\n\t" \
  "v_mfma_f32_16x16x32_bf16 v[108:111], a[112:115], v[112:115], v[188:191]\n\t" \
  "buffer_load_dwordx4 v230, %[sV], s96 offen offset:2048 lds\n\t" \
  "v_mfma_f32_16x16x32_bf16 v[108:111], a[116:119], v[116:119], v[108:111]\n\t" \
  "v_exp_f32 v11, v11\n\t" \
  "v_mfma_f32_16x16x32_bf16 v[108:111], a[120:123], v[120:123], v[108:111]\n\t" \
  "v_exp_f32 v12, v12\n\t" \
  "v_mfma_f32_16x16x32_bf16 v[108:111], a[124:127], v[124:127], v[108:111]\n\t" \
  "s_waitcnt lgkmcnt(11)\n\t" \
  "v_exp_f32 v13, v13\n\t" \
  STA("s_memrealtime s[94:95]\n\t") \
  STA("s_memtime s[92:93]\n\t") \
  "v_mfma_f32_16x16x32_bf16 v[0:3], a[192:195], v[16:19], v[176:179]\n\t" \
  "v_exp_f32 v14, v14\n\t" \
  "v_mfma_f32_16x16x32_bf16 v[0:3], a[196:199], v[20:23], v[0:3]\n\t" \
  "ds_read_b64_tr_b16 a[150:151], v228 offset:12544\n\t" \
  "v_cvt_pk_bf16_f32 v131, v6, v7\n\t" \
  "v_mfma_f32_16x16x32_bf16 v[0:3], a[200:203], v[24:27], v[0:3]\n\t" \
  "buffer_load_dwordx4 v231, %[sV], s96 offen offset:3072 lds\n\t" \
  "v_mfma_f32_16x16x32_bf16 v[0:3], a[204:207], v[28:31], v[0:3]\n\t" \
  "ds_read_b64_tr_b16 a[152:153], v229 offset:256\n\t" \
  "v_cvt_pk_bf16_f32 v132, v8, v9\n\t" \
  "v_mfma_f32_16x16x32_bf16 v[4:7], a[220:223], v[28:31], v[176:179]\n\t" \
  "v_exp_f32 v15, v15\n\t" \
  "v_mfma_f32_16x16x32_bf16 v[4:7], a[216:219], v[24:27], v[4:7]\n\t" \
  "s_add_u32 s98, %[pf4], 43008\n\t" \
  "v_exp_f32 v32, v32\n\t" \
  "v_mfma_f32_16x16x32_bf16 v[4:7], a[212:215], v[20:23], v[4:7]\n\t" \
  "buffer_load_dword %[pfd], v245, %[sU], s98 offen\n\t" \
  "v_mfma_f32_16x16x32_bf16 v[4:7], a[208:211], v[16:19], v[4:7]\n\t" \
  "ds_read_b64_tr_b16 a[154:155], v229 offset:4352\n\t" \
  "v_cvt_pk_bf16_f32 v133, v10, v11\n\t" \
  "v_mfma_f32_16x16x32_bf16 v[8:11], a[224:227], v[16:19], v[176:179]\n\t" \
  "v_exp_f32 v33, v33\n\t" \
  "v_mfma_f32_16x16x32_bf16 v[8:11], a[228:231], v[20:23], v[8:11]\n\t" \
  "ds_read_b64_tr_b16 a[156:157], v229 offset:8448\n\t" \
  "v_cvt_pk_bf16_f32 v134, v12, v13\n\t" \
  "v_mfma_f32_16x16x32_bf16 v[8:11], a[232:235], v[24:27], v[8:11]\n\t" \
  "ds_read_b64_tr_b16 a[158:159], v229 offset:12544\n\t" \
  "v_cvt_pk_bf16_f32 v135, v14, v15\n\t" \
  "v_mfma_f32_16x16x32_bf16 v[8:11], a[236:239], v[28:31], v[8:11]\n\t" \
  "v_exp_f32 v34, v34\n\t" \
  "v_mfma_f32_16x16x32_bf16 v[12:15], a[252:255], v[28:31], v[176:179]\n\t" \
  STA("v_writelane_b32 %[stv], s94, 26\n\t") \
  STA("v_writelane_b32 %[stv], s92, 27\n\t") \
  "s_add_u32 s98, %[pf4], 0\n\t" \
  "v_exp_f32 v35, v35\n\t" \
  "v_mfma_f32_16x16x32_bf16 v[12:15], a[248:251], v[24:27], v[12:15]\n\t" \
  "buffer_load_dword %[pfd], v245, %[sG], s98 offen\n\t" \
  "v_mfma_f32_16x16x32_bf16 v[12:15], a[244:247], v[20:23], v[12:15]\n\t" \
  "v_exp_f32 v36, v36\n\t" \
  "v_mfma_f32_16x16x32_bf16 v[12:15], a[240:243], v[16:19], v[12:15]\n\t" \
  "v_exp_f32 v37, v37\n\t" \
  "v_cvt_pk_bf16_f32 v136, v32, v33\n\t" \
  "v_cvt_pk_bf16_f32 v137, v34, v35\n\t" \
  "v_mfma_f32_16x16x32_bf16 v[32:35], a[192:195], v[48:51], v[180:183]\n\t" \
  "v_exp_f32 v38, v38\n\t" \
  "v_mfma_f32_16x16x32_bf16 v[32:35], a[196:199], v[52:55], v[32:35]\n\t" \
  "v_exp_f32 v39, v39\n\t" \
  "v_mfma_f32_16x16x32_bf16 v[32:35], a[200:203], v[56:59], v[32:35]\n\t" \
  "ds_read_b64_tr_b16 a[160:161], v228 offset:512\n\t" \
  "v_cvt_pk_bf16_f32 v138, v36, v37\n\t" \
  "v_mfma_f32_16x16x32_bf16 v[32:35], a[204:207], v[60:63], v[32:35]\n\t" \
  "s_add_u32 s98, %[pf4], 14336\n\t" \
  "v_exp_f32 v40, v40\n\t" \
  "v_cvt_pk_bf16_f32 v139, v38, v39\n\t" \
  "v_lshl_add_u32 v207, v233, 14, %[cto]\n\t" \
  "v_min_u32 v207, .Lfz_z-.Lat_t0-4, v207\n\t" \
  "v_mfma_f32_16x16x32_bf16 v[36:39], a[220:223], v[60:63], v[180:183]\n\t" \
  "v_exp_f32 v41, v41\n\t" \
  "v_mfma_f32_16x16x32_bf16 v[36:39], a[216:219], v[56:59], v[36:39]\n\t" \
  "buffer_load_dword %[pfd], v245, %[sG], s98 offen\n\t" \
  "s_getpc_b64 s[96:97]\n\t" \
  ".Lat_t0:\n\t" \
  "global_load_dword %[pfd], v207, s[96:97]\n\t" \
  "v_mfma_f32_16x16x32_bf16 v[36:39], a[212:215], v[52:55], v[36:39]\n\t" \
  "ds_read_b64_tr_b16 a[162:163], v228 offset:4608\n\t" \
  "v_cvt_pk_bf16_f32 v140, v40, v41\n\t" \
  "v_exp_f32 v42, v42\n\t" \
  "v_mfma_f32_16x16x32_bf16 v[36:39], a[208:211], v[48:51], v[36:39]\n\t" \
  "ds_read_b64_tr_b16 a[164:165], v228 offset:8704\n\t" \
  "ds_read_b64_tr_b16 a[166:167], v228 offset:12800\n\t" \
  "v_exp_f32 v43, v43\n\t" \
  "s_nop 0\n\t" \
  "v_cvt_pk_bf16_f32 v141, v42, v43\n\t" \
  "v_mfma_f32_16x16x32_bf16 v[40:43], a[224:227], v[48:51], v[180:183]\n\t" \
  "v_exp_f32 v44, v44\n\t" \
  "v_mfma_f32_16x16x32_bf16 v[40:43], a[228:231], v[52:55], v[40:43]\n\t" \
  "v_exp_f32 v45, v45\n\t" \
  "v_mfma_f32_16x16x32_bf16 v[40:43], a[232:235], v[56:59], v[40:43]\n\t" \
  "v_exp_f32 v46, v46\n\t" \
  "v_mfma_f32_16x16x32_bf16 v[40:43], a[236:239], v[60:63], v[40:43]\n\t" \
  "v_exp_f32 v47, v47\n\t" \
  "v_cvt_pk_bf16_f32 v142, v44, v45\n\t" \
  "v_cvt_pk_bf16_f32 v143, v46, v47\n\t" \
  "v_mfma_f32_16x16x32_bf16 v[44:47], a[252:255], v[60:63], v[180:183]\n\t" \
  "v_exp_f32 v64, v64\n\t" \
  "v_mfma_f32_16x16x32_bf16 v[44:47], a[248:251], v[56:59], v[44:47]\n\t" \
  "ds_read_b64_tr_b16 a[168:169], v229 offset:512\n\t" \
  "ds_read_b64_tr_b16 a[170:171], v229 offset:4608\n\t" \
  "v_exp_f32 v65, v65\n\t" \
  "v_mfma_f32_16x16x32_bf16 v[44:47], a[244:247], v[52:55], v[44:47]\n\t" \
  "v_exp_f32 v66, v66\n\t" \
  "v_mfma_f32_16x16x32_bf16 v[44:47], a[240:243], v[48:51], v[44:47]\n\t" \
  "v_exp_f32 v67, v67\n\t" \
  "v_cvt_pk_bf16_f32 v144, v64, v65\n\t" \
  "v_cvt_pk_bf16_f32 v145, v66, v67\n\t" \
  "v_mfma_f32_16x16x32_bf16 v[64:67], a[192:195], v[80:83], v[184:187]\n\t" \
  "v_exp_f32 v68, v68\n\t" \
  "v_mfma_f32_16x16x32_bf16 v[64:67], a[196:199], v[84:87], v[64:67]\n\t" \
  "v_exp_f32 v69, v69\n\t" \
  "v_mfma_f32_16x16x32_bf16 v[64:67], a[200:203], v[88:91], v[64:67]\n\t" \
  "ds_read_b64_tr_b16 a[172:173], v229 offset:8704\n\t" \
  "v_cvt_pk_bf16_f32 v146, v68, v69\n\t" \
  "v_exp_f32 v70, v70\n\t" \
  "v_mfma_f32_16x16x32_bf16 v[64:67], a[204:207], v[92:95], v[64:67]\n\t" \
  "v_exp_f32 v71, v71\n\t" \
  "s_nop 0\n\t" \
  "v_cvt_pk_bf16_f32 v147, v70, v71\n\t" \
  "v_mfma_f32_16x16x32_bf16 v[68:71], a[220:223], v[92:95], v[184:187]\n\t" \
  "v_exp_f32 v72, v72\n\t" \
  "v_mfma_f32_16x16x32_bf16 v[68:71], a[216:219], v[88:91], v[68:71]\n\t" \
  "ds_read_b64_tr_b16 a[174:175], v229 offset:12800\n\t" \
  "ds_read_b64_tr_b16 a[176:177], v228 offset:768\n\t" \
  "v_exp_f32 v73, v73\n\t" \
  "v_mfma_f32_16x16x32_bf16 v[68:71], a[212:215], v[84:87], v[68:71]\n\t" \
  "v_exp_f32 v74, v74\n\t" \
  "v_mfma_f32_16x16x32_bf16 v[68:71], a[208:211], v[80:83], v[68:71]\n\t" \
  "v_exp_f32 v75, v75\n\t" \
  "v_cvt_pk_bf16_f32 v148, v72, v73\n\t" \
  "v_cvt_pk_bf16_f32 v149, v74, v75\n\t" \
  "v_mfma_f32_16x16x32_bf16 v[72:75], a[224:227], v[80:83], v[184:187]\n\t" \
  "v_exp_f32 v76, v76\n\t" \
  "v_mfma_f32_16x16x32_bf16 v[72:75], a[228:231], v[84:87], v[72:75]\n\t" \
  "v_exp_f32 v77, v77\n\t" \
  "v_mfma_f32_16x16x32_bf16 v[72:75], a[232:235], v[88:91], v[72:75]\n\t" \
  "ds_read_b64_tr_b16 a[178:179], v228 offset:4864\n\t" \
  "v_cvt_pk_bf16_f32 v150, v76, v77\n\t" \
  "v_exp_f32 v78, v78\n\t" \
  "v_mfma_f32_16x16x32_bf16 v[72:75], a[236:239], v[92:95], v[72:75]\n\t" \
  "v_exp_f32 v79, v79\n\t" \
  "s_nop 0\n\t" \
  "v_cvt_pk_bf16_f32 v151, v78, v79\n\t" \
  "v_mfma_f32_16x16x32_bf16 v[76:79], a[252:255], v[92:95], v[184:187]\n\t" \
  "v_exp_f32 v96, v96\n\t" \
  "v_mfma_f32_16x16x32_bf16 v[76:79], a[248:251], v[88:91], v[76:79]\n\t" \
  "ds_read_b64_tr_b16 a[180:181], v228 offset:8960\n\t" \
  "ds_read_b64_tr_b16 a[182:183], v228 offset:13056\n\t" \
  "v_exp_f32 v97, v97\n\t" \
  "v_mfma_f32_16x16x32_bf16 v[76:79], a[244:247], v[84:87], v[76:79]\n\t" \
  "v_exp_f32 v98, v98\n\t" \
  "v_mfma_f32_16x16x32_bf16 v[76:79], a[240:243], v[80:83], v[76:79]\n\t" \
  "v_exp_f32 v99, v99\n\t" \
  "v_cvt_pk_bf16_f32 v152, v96, v97\n\t" \
  "v_cvt_pk_bf16_f32 v153, v98, v99\n\t" \
  "v_mfma_f32_16x16x32_bf16 v[96:99], a[192:195], v[112:115], v[188:191]\n\t" \
  "v_exp_f32 v100, v100\n\t" \
  "v_mfma_f32_16x16x32_bf16 v[96:99], a[196:199], v[116:119], v[96:99]\n\t" \
  "v_exp_f32 v101, v101\n\t" \
  "v_mfma_f32_16x16x32_bf16 v[96:99], a[200:203], v[120:123], v[96:99]\n\t" \
  "ds_read_b64_tr_b16 a[184:185], v229 offset:768\n\t" \
  "v_cvt_pk_bf16_f32 v154, v100, v101\n\t" \
  "v_exp_f32 v102, v102\n\t" \
  "v_mfma_f32_16x16x32_bf16 v[96:99], a[204:207], v[124:127], v[96:99]\n\t" \
  "v_exp_f32 v103, v103\n\t" \
  "s_nop 0\n\t" \
  "v_cvt_pk_bf16_f32 v155, v102, v103\n\t" \
  "v_mfma_f32_16x16x32_bf16 v[100:103], a[220:223], v[124:127], v[188:191]\n\t" \
  "v_exp_f32 v104, v104\n\t" \
  "v_mfma_f32_16x16x32_bf16 v[100:103], a[216:219], v[120:123], v[100:103]\n\t" \
  "ds_read_b64_tr_b16 a[186:187], v229 offset:4864\n\t" \
  "ds_read_b64_tr_b16 a[188:189], v229 offset:8960\n\t" \
  "v_exp_f32 v105, v105\n\t" \
  "v_mfma_f32_16x16x32_bf16 v[100:103], a[212:215], v[116:119], v[100:103]\n\t" \
  "v_exp_f32 v106, v106\n\t" \
  "v_mfma_f32_16x16x32_bf16 v[100:103], a[208:211], v[112:115], v[100:103]\n\t" \
  "v_exp_f32 v107, v107\n\t" \
  "v_cvt_pk_bf16_f32 v156, v104, v105\n\t" \
  "v_cvt_pk_bf16_f32 v157, v106, v107\n\t" \
  "v_mfma_f32_16x16x32_bf16 v[104:107], a[224:227], v[112:115], v[188:191]\n\t" \
  "v_exp_f32 v108, v108\n\t" \
  "v_mfma_f32_16x16x32_bf16 v[104:107], a[228:231], v[116:119], v[104:107]\n\t" \
  "v_exp_f32 v109, v109\n\t" \
  "v_mfma_f32_16x16x32_bf16 v[104:107], a[232:235], v[120:123], v[104:107]\n\t" \
  "ds_read_b64_tr_b16 a[190:191], v229 offset:13056\n\t" \
  "v_cvt_pk_bf16_f32 v158, v108, v109\n\t" \
  "v_exp_f32 v110, v110\n\t" \
  "v_mfma_f32_16x16x32_bf16 v[104:107], a[236:239], v[124:127], v[104:107]\n\t" \
  "v_exp_f32 v111, v111\n\t" \
  "s_nop 0\n\t" \
  "v_cvt_pk_bf16_f32 v159, v110, v111\n\t" \
  "v_mfma_f32_16x16x32_bf16 v[108:111], a[252:255], v[124:127], v[188:191]\n\t" \
  "v_exp_f32 v0, v0\n\t" \
  "v_mfma_f32_16x16x32_bf16 v[108:111], a[248:251], v[120:123], v[108:111]\n\t" \
  "v_exp_f32 v1, v1\n\t" \
  "v_mfma_f32_16x16x32_bf16 v[108:111], a[244:247], v[116:119], v[108:111]\n\t" \
  "v_exp_f32 v2, v2\n\t" \
  "v_mfma_f32_16x16x32_bf16 v[108:111], a[240:243], v[112:115], v[108:111]\n\t" \
  "s_waitcnt vmcnt(4) lgkmcnt(0)\n\t" \
  "s_barrier\n\t" \
  STA("s_memrealtime s[94:95]\n\t") \
  STA("s_memtime s[92:93]\n\t") \
  "v_mfma_f32_16x16x32_bf16 a[0:3], a[128:131], v[128:131], 0\n\t" \
  "ds_read_b128 a[192:195], v220 offset:0\n\t" \
  "v_and_b32 v239, 1, v233\n\t" \
  "v_mfma_f32_16x16x32_bf16 a[0:3], a[132:135], v[132:135], a[0:3]\n\t" \
  "ds_read_b128 a[196:199], v221 offset:0\n\t" \
  "v_mul_u32_u24 v239, 0xaaaa, v239\n\t" \
  "v_mfma_f32_16x16x32_bf16 a[16:19], a[136:139], v[128:131], 0\n\t" \
  "ds_read_b128 a[200:203], v222 offset:0\n\t" \
  "v_add_u32 v239, 0x4444, v239\n\t" \
  "v_mfma_f32_16x16x32_bf16 a[16:19], a[140:143], v[132:135], a[16:19]\n\t" \
  "ds_read_b128 a[204:207], v223 offset:0\n\t" \
  "v_lshl_or_b32 v239, v239, 16, v239\n\t" \
  "v_mfma_f32_16x16x32_bf16 a[32:35], a[144:147], v[128:131], 0\n\t" \
  "ds_read_b128 a[208:211], v220 offset:4096\n\t" \
  "v_mov_b32 v160, 0\n\t" \
  "v_mfma_f32_16x16x32_bf16 a[32:35], a[148:151], v[132:135], a[32:35]\n\t" \
  "v_mov_b32 v161, 0\n\t" \
  "v_mov_b32 v162, 0\n\t" \
  "v_mfma_f32_16x16x32_bf16 a[48:51], a[152:155], v[128:131], 0\n\t" \
  "v_mov_b32 v163, 0\n\t" \
  "v_mov_b32 v164, 0\n\t" \
  "v_mfma_f32_16x16x32_bf16 a[48:51], a[156:159], v[132:135], a[48:51]\n\t" \
  "v_mov_b32 v165, 0\n\t" \
  "v_mov_b32 v166, 0\n\t" \
  "v_mfma_f32_16x16x32_bf16 a[64:67], a[160:163], v[128:131], 0\n\t" \
  "ds_read_b128 a[212:215], v221 offset:4096\n\t" \
  "v_mov_b32 v167, 0\n\t" \
  "v_mfma_f32_16x16x32_bf16 a[64:67], a[164:167], v[132:135], a[64:67]\n\t" \
  "v_mov_b32 v168, 0\n\t" \
  "v_mov_b32 v169, 0\n\t" \
  "v_mfma_f32_16x16x32_bf16 a[80:83], a[168:171], v[128:131], 0\n\t" \
  "v_mov_b32 v170, 0\n\t" \
  "v_mov_b32 v171, 0\n\t" \
  "v_mfma_f32_16x16x32_bf16 a[80:83], a[172:175], v[132:135], a[80:83]\n\t" \
  "ds_read_b128 a[216:219], v222 offset:4096\n\t" \
  "v_mov_b32 v172, 0\n\t" \
  "v_mov_b32 v173, 0\n\t" \
  "v_mov_b32 v174, 0\n\t" \
  "v_mov_b32 v175, 0\n\t" \
  "v_mfma_f32_16x16x32_bf16 a[96:99], a[176:179], v[128:131], 0\n\t" \
  STA("v_writelane_b32 %[stv], s94, 28\n\t") \
  STA("v_writelane_b32 %[stv], s92, 29\n\t") \
  "v_exp_f32 v3, v3\n\t" \
  "v_mfma_f32_16x16x32_bf16 a[96:99], a[180:183], v[132:135], a[96:99]\n\t" \
  "v_exp_f32 v4, v4\n\t" \
  "v_mfma_f32_16x16x32_bf16 a[112:115], a[184:187], v[128:131], 0\n\t" \
  "v_exp_f32 v5, v5\n\t" \
  "v_mfma_f32_16x16x32_bf16 a[112:115], a[188:191], v[132:135], a[112:115]\n\t" \
  "v_exp_f32 v6, v6\n\t" \
  "v_smfmac_f32_16x16x64_bf16 v[160:163], v[216:219], v[128:135], v239\n\t" \
  "v_exp_f32 v7, v7\n\t" \
  "v_mfma_f32_16x16x32_bf16 a[4:7], a[128:131], v[136:139], 0\n\t" \
  "v_exp_f32 v8, v8\n\t" \
  "v_mfma_f32_16x16x32_bf16 a[4:7], a[132:135], v[140:143], a[4:7]\n\t" \
  "v_exp_f32 v9, v9\n\t" \
  "v_mfma_f32_16x16x32_bf16 a[20:23], a[136:139], v[136:139], 0\n\t" \
  "v_exp_f32 v10, v10\n\t" \
  "v_mfma_f32_16x16x32_bf16 a[20:23], a[140:143], v[140:143], a[20:23]\n\t" \
  "s_add_u32 m0, %[m], 65536\n\t" \
  "v_exp_f32 v11, v11\n\t" \
  "v_mfma_f32_16x16x32_bf16 a[36:39], a[144:147], v[136:139], 0\n\t" \
  "s_add_u32 s96, %[sofk], 49152\n\t" \
  "v_exp_f32 v12, v12\n\t" \
  "v_mfma_f32_16x16x32_bf16 a[36:39], a[148:151], v[140:143], a[36:39]\n\t" \
  "v_exp_f32 v13, v13\n\t" \
  "v_mfma_f32_16x16x32_bf16 a[52:55], a[152:155], v[136:139], 0\n\t" \
  "v_exp_f32 v14, v14\n\t" \
  "v_mfma_f32_16x16x32_bf16 a[52:55], a[156:159], v[140:143], a[52:55]\n\t" \
  "v_exp_f32 v15, v15\n\t" \
  "v_mfma_f32_16x16x32_bf16 a[68:71], a[160:163], v[136:139], 0\n\t" \
  "ds_read_b128 a[220:223], v223 offset:4096\n\t" \
  "v_cvt_pk_bf16_f32 v128, v0, v1\n\t" \
  "v_mfma_f32_16x16x32_bf16 a[68:71], a[164:167], v[140:143], a[68:71]\n\t" \
  "v_exp_f32 v32, v32\n\t" \
  "v_mfma_f32_16x16x32_bf16 a[84:87], a[168:171], v[136:139], 0\n\t" \
  "buffer_load_dwordx4 v224, %[sK], s96 offen offset:0 lds\n\t" \
  "v_mfma_f32_16x16x32_bf16 a[84:87], a[172:175], v[140:143], a[84:87]\n\t" \
  "ds_read_b128 a[224:227], v220 offset:8192\n\t" \
  "v_cvt_pk_bf16_f32 v129, v2, v3\n\t" \
  "v_mfma_f32_16x16x32_bf16 a[100:103], a[176:179], v[136:139], 0\n\t" \
  "v_exp_f32 v33, v33\n\t" \
  "v_mfma_f32_16x16x32_bf16 a[100:103], a[180:183], v[140:143], a[100:103]\n\t" \
  "buffer_load_dwordx4 v225, %[sK], s96 offen offset:1024 lds\n\t" \
  "v_mfma_f32_16x16x32_bf16 a[116:119], a[184:187], v[136:139], 0\n\t" \
  "v_exp_f32 v34, v34\n\t" \
  "v_mfma_f32_16x16x32_bf16 a[116:119], a[188:191], v[140:143], a[116:119]\n\t" \
  "ds_read_b128 a[228:231], v221 offset:8192\n\t" \
  "v_cvt_pk_bf16_f32 v130, v4, v5\n\t" \
  "v_smfmac_f32_16x16x64_bf16 v[164:167], v[216:219], v[136:143], v239\n\t" \
  "buffer_load_dwordx4 v226, %[sK], s96 offen offset:2048 lds\n\t" \
  "v_mfma_f32_16x16x32_bf16 a[8:11], a[128:131], v[144:147], 0\n\t" \
  "v_exp_f32 v35, v35\n\t" \
  "v_mfma_f32_16x16x32_bf16 a[8:11], a[132:135], v[148:151], a[8:11]\n\t" \
  "ds_read_b128 a[232:235], v222 offset:8192\n\t" \
  "v_cvt_pk_bf16_f32 v131, v6, v7\n\t" \
  "v_mfma_f32_16x16x32_bf16 a[24:27], a[136:139], v[144:147], 0\n\t" \
  "v_exp_f32 v36, v36\n\t" \
  "v_mfma_f32_16x16x32_bf16 a[24:27], a[140:143], v[148:151], a[24:27]\n\t" \
  "buffer_load_dwordx4 v227, %[sK], s96 offen offset:3072 lds\n\t" \
  "v_mfma_f32_16x16x32_bf16 a[40:43], a[144:147], v[144:147], 0\n\t" \
  "v_exp_f32 v37, v37\n\t" \
  "v_mfma_f32_16x16x32_bf16 a[40:43], a[148:151], v[148:151], a[40:43]\n\t" \
  "ds_read_b128 a[236:239], v223 offset:8192\n\t" \
  "v_cvt_pk_bf16_f32 v132, v8, v9\n\t" \
  "v_mfma_f32_16x16x32_bf16 a[56:59], a[152:155], v[144:147], 0\n\t" \
  "s_add_u32 m0, %[m], 114688\n\t" \
  "s_mov_b32 s96, 0\n\t" \
  "ds_read_b128 a[240:243], v220 offset:12288\n\t" \
  "v_mfma_f32_16x16x32_bf16 a[56:59], a[156:159], v[148:151], a[56:59]\n\t" \
  "v_exp_f32 v38, v38\n\t" \
  "v_mfma_f32_16x16x32_bf16 a[72:75], a[160:163], v[144:147], 0\n\t" \
  "buffer_load_dwordx4 v232, %[sU], s96 offen offset:0 lds\n\t" \
  "v_mfma_f32_16x16x32_bf16 a[72:75], a[164:167], v[148:151], a[72:75]\n\t" \
  "s_add_u32 m0, %[m], 81920\n\t" \
  "v_exp_f32 v39, v39\n\t" \
  "v_mfma_f32_16x16x32_bf16 a[88:91], a[168:171], v[144:147], 0\n\t" \
  "s_add_u32 s96, %[sofk], 32768\n\t" \
  "v_exp_f32 v40, v40\n\t" \
  "v_mfma_f32_16x16x32_bf16 a[88:91], a[172:175], v[148:151], a[88:91]\n\t" \
  "v_exp_f32 v41, v41\n\t" \
  "v_mfma_f32_16x16x32_bf16 a[104:107], a[176:179], v[144:147], 0\n\t" \
  "ds_read_b128 a[244:247], v221 offset:12288\n\t" \
  "v_cvt_pk_bf16_f32 v133, v10, v11\n\t" \
  "v_mfma_f32_16x16x32_bf16 a[104:107], a[180:183], v[148:151], a[104:107]\n\t" \
  "v_exp_f32 v42, v42\n\t" \
  "v_mfma_f32_16x16x32_bf16 a[120:123], a[184:187], v[144:147], 0\n\t" \
  "v_exp_f32 v43, v43\n\t" \
  "v_mfma_f32_16x16x32_bf16 a[120:123], a[188:191], v[148:151], a[120:123]\n\t" \
  "buffer_load_dwordx4 v230, %[sV], s96 offen offset:0 lds\n\t" \
  "v_smfmac_f32_16x16x64_bf16 v[168:171], v[216:219], v[144:151], v239\n\t" \
  "ds_read_b128 a[248:251], v222 offset:12288\n\t" \
  "v_cvt_pk_bf16_f32 v134, v12, v13\n\t" \
  "v_mfma_f32_16x16x32_bf16 a[12:15], a[128:131], v[152:155], 0\n\t" \
  "v_exp_f32 v44, v44\n\t" \
  "v_mfma_f32_16x16x32_bf16 a[12:15], a[132:135], v[156:159], a[12:15]\n\t" \
  "buffer_load_dwordx4 v231, %[sV], s96 offen offset:1024 lds\n\t" \
  "v_mfma_f32_16x16x32_bf16 a[28:31], a[136:139], v[152:155], 0\n\t" \
  "ds_read_b128 a[252:255], v223 offset:12288\n\t" \
  "v_cvt_pk_bf16_f32 v135, v14, v15\n\t" \
  "v_mfma_f32_16x16x32_bf16 a[28:31], a[140:143], v[156:159], a[28:31]\n\t" \
  "v_exp_f32 v45, v45\n\t" \
  "v_mfma_f32_16x16x32_bf16 a[44:47], a[144:147], v[152:155], 0\n\t" \
  "buffer_load_dwordx4 v230, %[sV], s96 offen offset:2048 lds\n\t" \
  "v_mfma_f32_16x16x32_bf16 a[44:47], a[148:151], v[156:159], a[44:47]\n\t" \
  "v_exp_f32 v46, v46\n\t" \
  "v_mfma_f32_16x16x32_bf16 a[60:63], a[152:155], v[152:155], 0\n\t" \
  "v_exp_f32 v47, v47\n\t" \
  "v_mfma_f32_16x16x32_bf16 a[60:63], a[156:159], v[156:159], a[60:63]\n\t" \
  "v_exp_f32 v64, v64\n\t" \
  "v_mfma_f32_16x16x32_bf16 a[76:79], a[160:163], v[152:155], 0\n\t" \
  "buffer_load_dwordx4 v231, %[sV], s96 offen offset:3072 lds\n\t" \
  "v_mfma_f32_16x16x32_bf16 a[76:79], a[164:167], v[156:159], a[76:79]\n\t" \
  "v_exp_f32 v65, v65\n\t" \
  "v_mfma_f32_16x16x32_bf16 a[92:95], a[168:171], v[152:155], 0\n\t" \
  "v_cvt_pk_bf16_f32 v136, v32, v33\n\t" \
  "v_cvt_pk_bf16_f32 v137, v34, v35\n\t" \
  "v_mfma_f32_16x16x32_bf16 a[92:95], a[172:175], v[156:159], a[92:95]\n\t" \
  "s_add_u32 m0, %[m], 114688\n\t" \
  "s_mov_b32 s96, 6144\n\t" \
  "v_cvt_pk_bf16_f32 v138, v36, v37\n\t" \
  "v_mfma_f32_16x16x32_bf16 a[108:111], a[176:179], v[152:155], 0\n\t" \
  "v_exp_f32 v66, v66\n\t" \
  "v_mfma_f32_16x16x32_bf16 a[108:111], a[180:183], v[156:159], a[108:111]\n\t" \
  "buffer_load_dwordx4 v232, %[sU], s96 offen offset:1024 lds\n\t" \
  "v_mfma_f32_16x16x32_bf16 a[124:127], a[184:187], v[152:155], 0\n\t" \
  "v_exp_f32 v67, v67\n\t" \
  "v_mfma_f32_16x16x32_bf16 a[124:127], a[188:191], v[156:159], a[124:127]\n\t" \
  "v_exp_f32 v68, v68\n\t" \
  "v_smfmac_f32_16x16x64_bf16 v[172:175], v[216:219], v[152:159], v239\n\t" \
  "s_waitcnt lgkmcnt(0)\n\t" \
  "v_exp_f32 v69, v69\n\t" \
  STA("s_memrealtime s[94:95]\n\t") \
  STA("s_memtime s[92:93]\n\t") \
  "v_mfma_f32_16x16x32_bf16 v[0:3], a[192:195], v[16:19], v[176:179]\n\t" \
  "ds_read_b64_tr_b16 a[128:129], v237 offset:0\n\t" \
  ".Lhs6:\n\ts_cbranch_execz .Lhs6n\n\t" \
  "v_cvt_pk_bf16_f32 v139, v38, v39\n\t" \
  "v_mfma_f32_16x16x32_bf16 v[0:3], a[196:199], v[20:23], v[0:3]\n\t" \
  "v_exp_f32 v70, v70\n\t" \
  "v_mfma_f32_16x16x32_bf16 v[0:3], a[200:203], v[24:27], v[0:3]\n\t" \
  "ds_read_b64_tr_b16 a[130:131], v237 offset:4096\n\t" \
  "v_cvt_pk_bf16_f32 v140, v40, v41\n\t" \
  "v_mfma_f32_16x16x32_bf16 v[0:3], a[204:207], v[28:31], v[0:3]\n\t" \
  "v_exp_f32 v71, v71\n\t" \
  "v_mfma_f32_16x16x32_bf16 v[4:7], a[220:223], v[28:31], v[176:179]\n\t" \
  "ds_read_b64_tr_b16 a[132:133], v237 offset:8192\n\t" \
  "v_cvt_pk_bf16_f32 v141, v42, v43\n\t" \
  "v_mfma_f32_16x16x32_bf16 v[4:7], a[216:219], v[24:27], v[4:7]\n\t" \
  "v_exp_f32 v72, v72\n\t" \
  "v_mfma_f32_16x16x32_bf16 v[4:7], a[212:215], v[20:23], v[4:7]\n\t" \
  "ds_read_b64_tr_b16 a[134:135], v237 offset:12288\n\t" \
  "v_cvt_pk_bf16_f32 v142, v44, v45\n\t" \
  "v_mfma_f32_16x16x32_bf16 v[4:7], a[208:211], v[16:19], v[4:7]\n\t" \
  "ds_read_b64_tr_b16 a[136:137], v238 offset:0\n\t" \
  "v_cvt_pk_bf16_f32 v143, v46, v47\n\t" \
  "v_mfma_f32_16x16x32_bf16 v[8:11], a[224:227], v[16:19], v[176:179]\n\t" \
  "s_add_u32 m0, %[m], 114688\n\t" \
  "s_mov_b32 s96, 12288\n\t" \
  "ds_read_b64_tr_b16 a[138:139], v238 offset:4096\n\t" \
  "v_mfma_f32_16x16x32_bf16 v[8:11], a[228:231], v[20:23], v[8:11]\n\t" \
  "buffer_load_dwordx4 v232, %[sU], s96 offen offset:2048 lds\n\t" \
  "v_mfma_f32_16x16x32_bf16 v[8:11], a[232:235], v[24:27], v[8:11]\n\t" \
  "s_mov_b32 s96, 18432\n\t" \
  "v_exp_f32 v73, v73\n\t" \
  "v_mfma_f32_16x16x32_bf16 v[8:11], a[236:239], v[28:31], v[8:11]\n\t" \
  "ds_read_b64_tr_b16 a[140:141], v238 offset:8192\n\t" \
  "v_cvt_pk_bf16_f32 v144, v64, v65\n\t" \
  "v_mfma_f32_16x16x32_bf16 v[12:15], a[252:255], v[28:31], v[176:179]\n\t" \
  STA("v_writelane_b32 %[stv], s94, 30\n\t") \
  STA("v_writelane_b32 %[stv], s92, 31\n\t") \
  "v_exp_f32 v74, v74\n\t" \
  "v_mfma_f32_16x16x32_bf16 v[12:15], a[248:251], v[24:27], v[12:15]\n\t" \
  "ds_read_b64_tr_b16 a[142:143], v238 offset:12288\n\t" \
  "v_cvt_pk_bf16_f32 v145, v66, v67\n\t" \
  "v_mfma_f32_16x16x32_bf16 v[12:15], a[244:247], v[20:23], v[12:15]\n\t" \
  "ds_read_b64_tr_b16 a[144:145], v237 offset:256\n\t" \
  "v_cvt_pk_bf16_f32 v146, v68, v69\n\t" \
  "v_mfma_f32_16x16x32_bf16 v[12:15], a[240:243], v[16:19], v[12:15]\n\t" \
  "v_exp_f32 v75, v75\n\t" \
  "v_mfma_f32_16x16x32_bf16 v[32:35], a[192:195], v[48:51], v[180:183]\n\t" \
  "ds_read_b64_tr_b16 a[146:147], v237 offset:4352\n\t" \
  "v_cvt_pk_bf16_f32 v147, v70, v71\n\t" \
  "v_mfma_f32_16x16x32_bf16 v[32:35], a[196:199], v[52:55], v[32:35]\n\t" \
  "v_exp_f32 v76, v76\n\t" \
  "v_mfma_f32_16x16x32_bf16 v[32:35], a[200:203], v[56:59], v[32:35]\n\t" \
  "ds_read_b64_tr_b16 a[148:149], v237 offset:8448\n\t" \
  "v_cvt_pk_bf16_f32 v148, v72, v73\n\t" \
  "v_mfma_f32_16x16x32_bf16 v[32:35], a[204:207], v[60:63], v[32:35]\n\t" \
  "v_exp_f32 v77, v77\n\t" \
  "v_mfma_f32_16x16x32_bf16 v[36:39], a[220:223], v[60:63], v[180:183]\n\t" \
  "ds_read_b64_tr_b16 a[150:151], v237 offset:12544\n\t" \
  "v_cvt_pk_bf16_f32 v149, v74, v75\n\t" \
  "v_mfma_f32_16x16x32_bf16 v[36:39], a[216:219], v[56:59], v[36:39]\n\t" \
  "buffer_load_dwordx4 v232, %[sU], s96 offen offset:3072 lds\n\t" \
  "v_mfma_f32_16x16x32_bf16 v[36:39], a[212:215], v[52:55], v[36:39]\n\t" \
  "ds_read_b64_tr_b16 a[152:153], v238 offset:256\n\t" \
  "v_cvt_pk_bf16_f32 v150, v76, v77\n\t" \
  "v_mfma_f32_16x16x32_bf16 v[36:39], a[208:211], v[48:51], v[36:39]\n\t" \
  "v_exp_f32 v78, v78\n\t" \
  "v_mfma_f32_16x16x32_bf16 v[40:43], a[224:227], v[48:51], v[180:183]\n\t" \
  "v_exp_f32 v79, v79\n\t" \
  "v_mfma_f32_16x16x32_bf16 v[40:43], a[228:231], v[52:55], v[40:43]\n\t" \
  "ds_read_b64_tr_b16 a[154:155], v238 offset:4352\n\t" \
  "v_cvt_pk_bf16_f32 v151, v78, v79\n\t" \
  "v_mfma_f32_16x16x32_bf16 v[40:43], a[232:235], v[56:59], v[40:43]\n\t" \
  "v_exp_f32 v96, v96\n\t" \
  "v_mfma_f32_16x16x32_bf16 v[40:43], a[236:239], v[60:63], v[40:43]\n\t" \
  "ds_read_b64_tr_b16 a[156:157], v238 offset:8448\n\t" \
  "ds_read_b64_tr_b16 a[158:159], v238 offset:12544\n\t" \
  "v_mfma_f32_16x16x32_bf16 v[44:47], a[252:255], v[60:63], v[180:183]\n\t" \
  "v_exp_f32 v97, v97\n\t" \
  "v_mfma_f32_16x16x32_bf16 v[44:47], a[248:251], v[56:59], v[44:47]\n\t" \
  "ds_read_b64_tr_b16 a[160:161], v237 offset:512\n\t" \
  "v_cvt_pk_bf16_f32 v152, v96, v97\n\t" \
  "v_mfma_f32_16x16x32_bf16 v[44:47], a[244:247], v[52:55], v[44:47]\n\t" \
  "v_exp_f32 v98, v98\n\t" \
  "v_mfma_f32_16x16x32_bf16 v[44:47], a[240:243], v[48:51], v[44:47]\n\t" \
  "v_exp_f32 v99, v99\n\t" \
  "v_mfma_f32_16x16x32_bf16 v[64:67], a[192:195], v[80:83], v[184:187]\n\t" \
  "ds_read_b64_tr_b16 a[162:163], v237 offset:4608\n\t" \
  "v_cvt_pk_bf16_f32 v153, v98, v99\n\t" \
  "v_mfma_f32_16x16x32_bf16 v[64:67], a[196:199], v[84:87], v[64:67]\n\t" \
  "v_exp_f32 v100, v100\n\t" \
  "v_mfma_f32_16x16x32_bf16 v[64:67], a[200:203], v[88:91], v[64:67]\n\t" \
  "ds_read_b64_tr_b16 a[164:165], v237 offset:8704\n\t" \
  "ds_read_b64_tr_b16 a[166:167], v237 offset:12800\n\t" \
  "v_mfma_f32_16x16x32_bf16 v[64:67], a[204:207], v[92:95], v[64:67]\n\t" \
  "ds_read_b64_tr_b16 a[168:169], v238 offset:512\n\t" \
  "ds_read_b64_tr_b16 a[170:171], v238 offset:4608\n\t" \
  "v_mfma_f32_16x16x32_bf16 v[68:71], a[220:223], v[92:95], v[184:187]\n\t" \
  "s_add_u32 m0, %[m], 131072\n\t" \
  "s_mov_b32 s96, 28672\n\t" \
  "buffer_load_dwordx4 v232, %[sU], s96 offen offset:0 lds\n\t" \
  "v_mfma_f32_16x16x32_bf16 v[68:71], a[216:219], v[88:91], v[68:71]\n\t" \
  "s_mov_b32 s96, 34816\n\t" \
  "v_exp_f32 v101, v101\n\t" \
  "v_mfma_f32_16x16x32_bf16 v[68:71], a[212:215], v[84:87], v[68:71]\n\t" \
  "v_exp_f32 v102, v102\n\t" \
  "v_mfma_f32_16x16x32_bf16 v[68:71], a[208:211], v[80:83], v[68:71]\n\t" \
  "ds_read_b64_tr_b16 a[172:173], v238 offset:8704\n\t" \
  "v_cvt_pk_bf16_f32 v154, v100, v101\n\t" \
  "v_mfma_f32_16x16x32_bf16 v[72:75], a[224:227], v[80:83], v[184:187]\n\t" \
  "v_exp_f32 v103, v103\n\t" \
  "v_mfma_f32_16x16x32_bf16 v[72:75], a[228:231], v[84:87], v[72:75]\n\t" \
  "ds_read_b64_tr_b16 a[174:175], v238 offset:12800\n\t" \
  "v_cvt_pk_bf16_f32 v155, v102, v103\n\t" \
  "v_mfma_f32_16x16x32_bf16 v[72:75], a[232:235], v[88:91], v[72:75]\n\t" \
  "v_exp_f32 v104, v104\n\t" \
  "v_mfma_f32_16x16x32_bf16 v[72:75], a[236:239], v[92:95], v[72:75]\n\t" \
  "v_exp_f32 v105, v105\n\t" \
  "v_mfma_f32_16x16x32_bf16 v[76:79], a[252:255], v[92:95], v[184:187]\n\t" \
  "ds_read_b64_tr_b16 a[176:177], v237 offset:768\n\t" \
  "v_cvt_pk_bf16_f32 v156, v104, v105\n\t" \
  "v_mfma_f32_16x16x32_bf16 v[76:79], a[248:251], v[88:91], v[76:79]\n\t" \
  "v_exp_f32 v106, v106\n\t" \
  "v_mfma_f32_16x16x32_bf16 v[76:79], a[244:247], v[84:87], v[76:79]\n\t" \
  "ds_read_b64_tr_b16 a[178:179], v237 offset:4864\n\t" \
  "ds_read_b64_tr_b16 a[180:181], v237 offset:8960\n\t" \
  "v_mfma_f32_16x16x32_bf16 v[76:79], a[240:243], v[80:83], v[76:79]\n\t" \
  "buffer_load_dwordx4 v232, %[sU], s96 offen offset:1024 lds\n\t" \
  "v_mfma_f32_16x16x32_bf16 v[96:99], a[192:195], v[112:115], v[188:191]\n\t" \
  "v_exp_f32 v107, v107\n\t" \
  "v_mfma_f32_16x16x32_bf16 v[96:99], a[196:199], v[116:119], v[96:99]\n\t" \
  "ds_read_b64_tr_b16 a[182:183], v237 offset:13056\n\t" \
  "v_cvt_pk_bf16_f32 v157, v106, v107\n\t" \
  "v_mfma_f32_16x16x32_bf16 v[96:99], a[200:203], v[120:123], v[96:99]\n\t" \
  "v_exp_f32 v108, v108\n\t" \
  "v_mfma_f32_16x16x32_bf16 v[96:99], a[204:207], v[124:127], v[96:99]\n\t" \
  "ds_read_b64_tr_b16 a[184:185], v238 offset:768\n\t" \
  "ds_read_b64_tr_b16 a[186:187], v238 offset:4864\n\t" \
  "v_mfma_f32_16x16x32_bf16 v[100:103], a[220:223], v[124:127], v[188:191]\n\t" \
  "v_exp_f32 v109, v109\n\t" \
  "v_mfma_f32_16x16x32_bf16 v[100:103], a[216:219], v[120:123], v[100:103]\n\t" \
  "ds_read_b64_tr_b16 a[188:189], v238 offset:8960\n\t" \
  "v_cvt_pk_bf16_f32 v158, v108, v109\n\t" \
  "v_mfma_f32_16x16x32_bf16 v[100:103], a[212:215], v[116:119], v[100:103]\n\t" \
  "v_exp_f32 v110, v110\n\t" \
  "v_mfma_f32_16x16x32_bf16 v[100:103], a[208:211], v[112:115], v[100:103]\n\t" \
  "v_exp_f32 v111, v111\n\t" \
  "v_mfma_f32_16x16x32_bf16 v[104:107], a[224:227], v[112:115], v[188:191]\n\t" \
  "ds_read_b64_tr_b16 a[190:191], v238 offset:13056\n\t" \
  "v_cvt_pk_bf16_f32 v159, v110, v111\n\t" \
  "v_mfma_f32_16x16x32_bf16 v[104:107], a[228:231], v[116:119], v[104:107]\n\t" \
  "v_exp_f32 v0, v0\n\t" \
  "v_mfma_f32_16x16x32_bf16 v[104:107], a[232:235], v[120:123], v[104:107]\n\t" \
  "v_exp_f32 v1, v1\n\t" \
  "v_mfma_f32_16x16x32_bf16 v[104:107], a[236:239], v[124:127], v[104:107]\n\t" \
  "v_exp_f32 v2, v2\n\t" \
  "v_mfma_f32_16x16x32_bf16 v[108:111], a[252:255], v[124:127], v[188:191]\n\t" \
  "v_exp_f32 v3, v3\n\t" \
  "v_mfma_f32_16x16x32_bf16 v[108:111], a[248:251], v[120:123], v[108:111]\n\t" \
  "v_exp_f32 v4, v4\n\t" \
  "v_mfma_f32_16x16x32_bf16 v[108:111], a[244:247], v[116:119], v[108:111]\n\t" \
  "v_exp_f32 v5, v5\n\t" \
  "v_mfma_f32_16x16x32_bf16 v[108:111], a[240:243], v[112:115], v[108:111]\n\t" \
  "s_waitcnt vmcnt(5) lgkmcnt(0)\n\t" \
  "s_barrier\n\t" \
  STA("s_memrealtime s[94:95]\n\t") \
  STA("s_memtime s[92:93]\n\t") \
  "v_mfma_f32_16x16x32_bf16 a[0:3], a[128:131], v[128:131], a[0:3]\n\t" \
  "v_exp_f32 v6, v6\n\t" \
  "v_mfma_f32_16x16x32_bf16 a[0:3], a[132:135], v[132:135], a[0:3]\n\t" \
  "v_exp_f32 v7, v7\n\t" \
  "v_mfma_f32_16x16x32_bf16 a[16:19], a[136:139], v[128:131], a[16:19]\n\t" \
  "ds_read_b128 a[192:195], v220 offset:16384\n\t" \
  "ds_read_b128 a[196:199], v221 offset:16384\n\t" \
  "v_mfma_f32_16x16x32_bf16 a[16:19], a[140:143], v[132:135], a[16:19]\n\t" \
  "v_exp_f32 v8, v8\n\t" \
  "v_mfma_f32_16x16x32_bf16 a[32:35], a[144:147], v[128:131], a[32:35]\n\t" \
  "v_exp_f32 v9, v9\n\t" \
  "v_mfma_f32_16x16x32_bf16 a[32:35], a[148:151], v[132:135], a[32:35]\n\t" \
  "v_exp_f32 v10, v10\n\t" \
  "v_mfma_f32_16x16x32_bf16 a[48:51], a[152:155], v[128:131], a[48:51]\n\t" \
  "v_exp_f32 v11, v11\n\t" \
  "v_mfma_f32_16x16x32_bf16 a[48:51], a[156:159], v[132:135], a[48:51]\n\t" \
  "v_exp_f32 v12, v12\n\t" \
  "v_mfma_f32_16x16x32_bf16 a[64:67], a[160:163], v[128:131], a[64:67]\n\t" \
  "v_exp_f32 v13, v13\n\t" \
  "v_mfma_f32_16x16x32_bf16 a[64:67], a[164:167], v[132:135], a[64:67]\n\t" \
  "ds_read_b128 a[200:203], v222 offset:16384\n\t" \
  "ds_read_b128 a[204:207], v223 offset:16384\n\t" \
  "v_mfma_f32_16x16x32_bf16 a[80:83], a[168:171], v[128:131], a[80:83]\n\t" \
  "v_exp_f32 v14, v14\n\t" \
  "v_mfma_f32_16x16x32_bf16 a[80:83], a[172:175], v[132:135], a[80:83]\n\t" \
  "v_exp_f32 v15, v15\n\t" \
  "v_mfma_f32_16x16x32_bf16 a[96:99], a[176:179], v[128:131], a[96:99]\n\t" \
  STA("v_writelane_b32 %[stv], s94, 32\n\t") \
  STA("v_writelane_b32 %[stv], s92, 33\n\t") \
  "buffer_store_dword v233, v247, %[ws], 0 offen sc1\n\t" \
  "v_mfma_f32_16x16x32_bf16 a[96:99], a[180:183], v[132:135], a[96:99]\n\t" \
  "v_exp_f32 v32, v32\n\t" \
  "v_mfma_f32_16x16x32_bf16 a[112:115], a[184:187], v[128:131], a[112:115]\n\t" \
  "v_exp_f32 v33, v33\n\t" \
  "v_mfma_f32_16x16x32_bf16 a[112:115], a[188:191], v[132:135], a[112:115]\n\t" \
  "v_exp_f32 v34, v34\n\t" \
  "v_smfmac_f32_16x16x64_bf16 v[160:163], v[216:219], v[128:135], v239\n\t" \
  "ds_read_b128 a[208:211], v220 offset:20480\n\t" \
  "v_cvt_pk_bf16_f32 v128, v0, v1\n\t" \
  "v_mfma_f32_16x16x32_bf16 a[4:7], a[128:131], v[136:139], a[4:7]\n\t" \
  "v_exp_f32 v35, v35\n\t" \
  "v_mfma_f32_16x16x32_bf16 a[4:7], a[132:135], v[140:143], a[4:7]\n\t" \
  "ds_read_b128 a[212:215], v221 offset:20480\n\t" \
  "v_cvt_pk_bf16_f32 v129, v2, v3\n\t" \
  "v_mfma_f32_16x16x32_bf16 a[20:23], a[136:139], v[136:139], a[20:23]\n\t" \
  "v_exp_f32 v36, v36\n\t" \
  "v_mfma_f32_16x16x32_bf16 a[20:23], a[140:143], v[140:143], a[20:23]\n\t" \
  "s_add_u32 m0, %[m], 49152\n\t" \
  "v_exp_f32 v37, v37\n\t" \
  "v_mfma_f32_16x16x32_bf16 a[36:39], a[144:147], v[136:139], a[36:39]\n\t" \
  "s_add_u32 s96, %[sofk], 65536\n\t" \
  "ds_read_b128 a[216:219], v222 offset:20480\n\t" \
  "v_cvt_pk_bf16_f32 v130, v4, v5\n\t" \
  "v_mfma_f32_16x16x32_bf16 a[36:39], a[148:151], v[140:143], a[36:39]\n\t" \
  "v_exp_f32 v38, v38\n\t" \
  "v_mfma_f32_16x16x32_bf16 a[52:55], a[152:155], v[136:139], a[52:55]\n\t" \
  "v_exp_f32 v39, v39\n\t" \
  "v_mfma_f32_16x16x32_bf16 a[52:55], a[156:159], v[140:143], a[52:55]\n\t" \
  "v_exp_f32 v40, v40\n\t" \
  "v_mfma_f32_16x16x32_bf16 a[68:71], a[160:163], v[136:139], a[68:71]\n\t" \
  "ds_read_b128 a[220:223], v223 offset:20480\n\t" \
  "v_cvt_pk_bf16_f32 v131, v6, v7\n\t" \
  "v_mfma_f32_16x16x32_bf16 a[68:71], a[164:167], v[140:143], a[68:71]\n\t" \
  "v_exp_f32 v41, v41\n\t" \
  "v_mfma_f32_16x16x32_bf16 a[84:87], a[168:171], v[136:139], a[84:87]\n\t" \
  "buffer_load_dwordx4 v224, %[sK], s96 offen offset:0 lds\n\t" \
  "v_mfma_f32_16x16x32_bf16 a[84:87], a[172:175], v[140:143], a[84:87]\n\t" \
  "ds_read_b128 a[224:227], v220 offset:24576\n\t" \
  "v_cvt_pk_bf16_f32 v132, v8, v9\n\t" \
  "v_mfma_f32_16x16x32_bf16 a[100:103], a[176:179], v[136:139], a[100:103]\n\t" \
  "v_exp_f32 v42, v42\n\t" \
  "v_mfma_f32_16x16x32_bf16 a[100:103], a[180:183], v[140:143], a[100:103]\n\t" \
  "buffer_load_dwordx4 v225, %[sK], s96 offen offset:1024 lds\n\t" \
  "v_mfma_f32_16x16x32_bf16 a[116:119], a[184:187], v[136:139], a[116:119]\n\t" \
  "v_exp_f32 v43, v43\n\t" \
  "v_mfma_f32_16x16x32_bf16 a[116:119], a[188:191], v[140:143], a[116:119]\n\t" \
  "ds_read_b128 a[228:231], v221 offset:24576\n\t" \
  "v_cvt_pk_bf16_f32 v133, v10, v11\n\t" \
  "v_smfmac_f32_16x16x64_bf16 v[164:167], v[216:219], v[136:143], v239\n\t" \
  "buffer_load_dwordx4 v226, %[sK], s96 offen offset:2048 lds\n\t" \
  "v_mfma_f32_16x16x32_bf16 a[8:11], a[128:131], v[144:147], a[8:11]\n\t" \
  "v_exp_f32 v44, v44\n\t" \
  "v_mfma_f32_16x16x32_bf16 a[8:11], a[132:135], v[148:151], a[8:11]\n\t" \
  "ds_read_b128 a[232:235], v222 offset:24576\n\t" \
  "v_cvt_pk_bf16_f32 v134, v12, v13\n\t" \
  "v_mfma_f32_16x16x32_bf16 a[24:27], a[136:139], v[144:147], a[24:27]\n\t" \
  "v_exp_f32 v45, v45\n\t" \
  "v_mfma_f32_16x16x32_bf16 a[24:27], a[140:143], v[148:151], a[24:27]\n\t" \
  "buffer_load_dwordx4 v227, %[sK], s96 offen offset:3072 lds\n\t" \
  "v_mfma_f32_16x16x32_bf16 a[40:43], a[144:147], v[144:147], a[40:43]\n\t" \
  "v_exp_f32 v46, v46\n\t" \
  "v_mfma_f32_16x16x32_bf16 a[40:43], a[148:151], v[148:151], a[40:43]\n\t" \
  "ds_read_b128 a[236:239], v223 offset:24576\n\t" \
  "v_cvt_pk_bf16_f32 v135, v14, v15\n\t" \
  "v_mfma_f32_16x16x32_bf16 a[56:59], a[152:155], v[144:147], a[56:59]\n\t" \
  "s_add_u32 m0, %[m], 131072\n\t" \
  "s_mov_b32 s96, 40960\n\t" \
  "ds_read_b128 a[240:243], v220 offset:28672\n\t" \
  "v_mfma_f32_16x16x32_bf16 a[56:59], a[156:159], v[148:151], a[56:59]\n\t" \
  "v_exp_f32 v47, v47\n\t" \
  "v_mfma_f32_16x16x32_bf16 a[72:75], a[160:163], v[144:147], a[72:75]\n\t" \
  "buffer_load_dwordx4 v232, %[sU], s96 offen offset:2048 lds\n\t" \
  "v_mfma_f32_16x16x32_bf16 a[72:75], a[164:167], v[148:151], a[72:75]\n\t" \
  "s_add_u32 m0, %[m], 147456\n\t" \
  "v_exp_f32 v64, v64\n\t" \
  "v_mfma_f32_16x16x32_bf16 a[88:91], a[168:171], v[144:147], a[88:91]\n\t" \
  "s_add_u32 s96, %[sofk], 49152\n\t" \
  "v_exp_f32 v65, v65\n\t" \
  "v_mfma_f32_16x16x32_bf16 a[88:91], a[172:175], v[148:151], a[88:91]\n\t" \
  "v_exp_f32 v66, v66\n\t" \
  "v_mfma_f32_16x16x32_bf16 a[104:107], a[176:179], v[144:147], a[104:107]\n\t" \
  "ds_read_b128 a[244:247], v221 offset:28672\n\t" \
  "v_cvt_pk_bf16_f32 v136, v32, v33\n\t" \
  "v_mfma_f32_16x16x32_bf16 a[104:107], a[180:183], v[148:151], a[104:107]\n\t" \
  "v_exp_f32 v67, v67\n\t" \
  "v_mfma_f32_16x16x32_bf16 a[120:123], a[184:187], v[144:147], a[120:123]\n\t" \
  "v_exp_f32 v68, v68\n\t" \
  "v_mfma_f32_16x16x32_bf16 a[120:123], a[188:191], v[148:151], a[120:123]\n\t" \
  "buffer_load_dwordx4 v230, %[sV], s96 offen offset:0 lds\n\t" \
  "v_smfmac_f32_16x16x64_bf16 v[168:171], v[216:219], v[144:151], v239\n\t" \
  "ds_read_b128 a[248:251], v222 offset:28672\n\t" \
  "v_cvt_pk_bf16_f32 v137, v34, v35\n\t" \
  "v_mfma_f32_16x16x32_bf16 a[12:15], a[128:131], v[152:155], a[12:15]\n\t" \
  "v_exp_f32 v69, v69\n\t" \
  "v_mfma_f32_16x16x32_bf16 a[12:15], a[132:135], v[156:159], a[12:15]\n\t" \
  "buffer_load_dwordx4 v231, %[sV], s96 offen offset:1024 lds\n\t" \
  "v_mfma_f32_16x16x32_bf16 a[28:31], a[136:139], v[152:155], a[28:31]\n\t" \
  "ds_read_b128 a[252:255], v223 offset:28672\n\t" \
  "v_cvt_pk_bf16_f32 v138, v36, v37\n\t" \
  "v_mfma_f32_16x16x32_bf16 a[28:31], a[140:143], v[156:159], a[28:31]\n\t" \
  "v_exp_f32 v70, v70\n\t" \
  "v_mfma_f32_16x16x32_bf16 a[44:47], a[144:147], v[152:155], a[44:47]\n\t" \
  "buffer_load_dwordx4 v230, %[sV], s96 offen offset:2048 lds\n\t" \
  "v_mfma_f32_16x16x32_bf16 a[44:47], a[148:151], v[156:159], a[44:47]\n\t" \
  "v_exp_f32 v71, v71\n\t" \
  "v_mfma_f32_16x16x32_bf16 a[60:63], a[152:155], v[152:155], a[60:63]\n\t" \
  "v_exp_f32 v72, v72\n\t" \
  "v_mfma_f32_16x16x32_bf16 a[60:63], a[156:159], v[156:159], a[60:63]\n\t" \
  "v_exp_f32 v73, v73\n\t" \
  "v_mfma_f32_16x16x32_bf16 a[76:79], a[160:163], v[152:155], a[76:79]\n\t" \
  "buffer_load_dwordx4 v231, %[sV], s96 offen offset:3072 lds\n\t" \
  "v_mfma_f32_16x16x32_bf16 a[76:79], a[164:167], v[156:159], a[76:79]\n\t" \
  "v_exp_f32 v74, v74\n\t" \
  "v_mfma_f32_16x16x32_bf16 a[92:95], a[168:171], v[152:155], a[92:95]\n\t" \
  "v_cvt_pk_bf16_f32 v139, v38, v39\n\t" \
  "v_cvt_pk_bf16_f32 v140, v40, v41\n\t" \
  "v_mfma_f32_16x16x32_bf16 a[92:95], a[172:175], v[156:159], a[92:95]\n\t" \
  "s_add_u32 m0, %[m], 131072\n\t" \
  "s_mov_b32 s96, 47104\n\t" \
  "v_cvt_pk_bf16_f32 v141, v42, v43\n\t" \
  "v_mfma_f32_16x16x32_bf16 a[108:111], a[176:179], v[152:155], a[108:111]\n\t" \
  "v_exp_f32 v75, v75\n\t" \
  "v_mfma_f32_16x16x32_bf16 a[108:111], a[180:183], v[156:159], a[108:111]\n\t" \
  "buffer_load_dwordx4 v232, %[sU], s96 offen offset:3072 lds\n\t" \
  "v_mfma_f32_16x16x32_bf16 a[124:127], a[184:187], v[152:155], a[124:127]\n\t" \
  "v_exp_f32 v76, v76\n\t" \
  "v_mfma_f32_16x16x32_bf16 a[124:127], a[188:191], v[156:159], a[124:127]\n\t" \
  "v_cvt_pk_bf16_f32 v142, v44, v45\n\t" \
  "v_cvt_pk_bf16_f32 v143, v46, v47\n\t" \
  "v_smfmac_f32_16x16x64_bf16 v[172:175], v[216:219], v[152:159], v239\n\t" \
  "s_waitcnt lgkmcnt(0)\n\t" \
  "s_mul_i32 s98, %[sofk], 5\n\t" \
  "s_xor_b32 s98, s98, 8192\n\t" \
  "s_lshr_b32 s98, s98, 1\n\t" \
  "v_cvt_pk_bf16_f32 v144, v64, v65\n\t" \
  STA("s_memrealtime s[94:95]\n\t") \
  STA("s_memtime s[92:93]\n\t") \
  "v_mfma_f32_16x16x32_bf16 v[0:3], a[192:195], v[16:19], v[176:179]\n\t" \
  "buffer_load_dword %[pfd], v204, %[sD], s98 offen\n\t" \
  "v_mfma_f32_16x16x32_bf16 v[0:3], a[196:199], v[20:23], v[0:3]\n\t" \
  "ds_read_b64_tr_b16 a[128:129], v228 offset:0\n\t" \
  "v_cvt_pk_bf16_f32 v145, v66, v67\n\t" \
  "v_mfma_f32_16x16x32_bf16 v[0:3], a[200:203], v[24:27], v[0:3]\n\t" \
  "ds_read_b64_tr_b16 a[130:131], v228 offset:4096\n\t" \
  "v_cvt_pk_bf16_f32 v146, v68, v69\n\t" \
  "v_mfma_f32_16x16x32_bf16 v[0:3], a[204:207], v[28:31], v[0:3]\n\t" \
  "v_exp_f32 v77, v77\n\t" \
  "v_mfma_f32_16x16x32_bf16 v[4:7], a[220:223], v[28:31], v[176:179]\n\t" \
  "ds_read_b64_tr_b16 a[132:133], v228 offset:8192\n\t" \
  "v_cvt_pk_bf16_f32 v147, v70, v71\n\t" \
  "v_mfma_f32_16x16x32_bf16 v[4:7], a[216:219], v[24:27], v[4:7]\n\t" \
  "v_exp_f32 v78, v78\n\t" \
  "v_mfma_f32_16x16x32_bf16 v[4:7], a[212:215], v[20:23], v[4:7]\n\t" \
  "ds_read_b64_tr_b16 a[134:135], v228 offset:12288\n\t" \
  "v_cvt_pk_bf16_f32 v148, v72, v73\n\t" \
  "v_mfma_f32_16x16x32_bf16 v[4:7], a[208:211], v[16:19], v[4:7]\n\t" \
  "ds_read_b64_tr_b16 a[136:137], v229 offset:0\n\t" \
  "v_cvt_pk_bf16_f32 v149, v74, v75\n\t" \
  "v_mfma_f32_16x16x32_bf16 v[8:11], a[224:227], v[16:19], v[176:179]\n\t" \
  "v_exp_f32 v79, v79\n\t" \
  "v_mfma_f32_16x16x32_bf16 v[8:11], a[228:231], v[20:23], v[8:11]\n\t" \
  "ds_read_b64_tr_b16 a[138:139], v229 offset:4096\n\t" \
  "v_cvt_pk_bf16_f32 v150, v76, v77\n\t" \
  "v_mfma_f32_16x16x32_bf16 v[8:11], a[232:235], v[24:27], v[8:11]\n\t" \
  "v_exp_f32 v96, v96\n\t" \
  "v_mfma_f32_16x16x32_bf16 v[8:11], a[236:239], v[28:31], v[8:11]\n\t" \
  "ds_read_b64_tr_b16 a[140:141], v229 offset:8192\n\t" \
  "v_cvt_pk_bf16_f32 v151, v78, v79\n\t" \
  "v_mfma_f32_16x16x32_bf16 v[12:15], a[252:255], v[28:31], v[176:179]\n\t" \
  STA("v_writelane_b32 %[stv], s94, 34\n\t") \
  STA("v_writelane_b32 %[stv], s92, 35\n\t") \
  "s_mul_i32 s98, %[sofk], 5\n\t" \
  "s_xor_b32 s98, s98, 12288\n\t" \
  "s_lshr_b32 s98, s98, 1\n\t" \
  "buffer_load_dword %[pfd], v204, %[sD], s98 offen\n\t" \
  "v_mfma_f32_16x16x32_bf16 v[12:15], a[248:251], v[24:27], v[12:15]\n\t" \
  "v_exp_f32 v97, v97\n\t" \
  "v_mfma_f32_16x16x32_bf16 v[12:15], a[244:247], v[20:23], v[12:15]\n\t" \
  "ds_read_b64_tr_b16 a[142:143], v229 offset:12288\n\t" \
  "v_cvt_pk_bf16_f32 v152, v96, v97\n\t" \
  "v_mfma_f32_16x16x32_bf16 v[12:15], a[240:243], v[16:19], v[12:15]\n\t" \
  "v_exp_f32 v98, v98\n\t" \
  "v_mfma_f32_16x16x32_bf16 v[32:35], a[192:195], v[48:51], v[180:183]\n\t" \
  "ds_read_b64_tr_b16 a[144:145], v228 offset:256\n\t" \
  "ds_read_b64_tr_b16 a[146:147], v228 offset:4352\n\t" \
  "v_mfma_f32_16x16x32_bf16 v[32:35], a[196:199], v[52:55], v[32:35]\n\t" \
  "v_exp_f32 v99, v99\n\t" \
  "v_mfma_f32_16x16x32_bf16 v[32:35], a[200:203], v[56:59], v[32:35]\n\t" \
  "ds_read_b64_tr_b16 a[148:149], v228 offset:8448\n\t" \
  "v_cvt_pk_bf16_f32 v153, v98, v99\n\t" \
  "v_mfma_f32_16x16x32_bf16 v[32:35], a[204:207], v[60:63], v[32:35]\n\t" \
  "v_exp_f32 v100, v100\n\t" \
  "v_mfma_f32_16x16x32_bf16 v[36:39], a[220:223], v[60:63], v[180:183]\n\t" \
  "v_exp_f32 v101, v101\n\t" \
  "v_mfma_f32_16x16x32_bf16 v[36:39], a[216:219], v[56:59], v[36:39]\n\t" \
  "ds_read_b64_tr_b16 a[150:151], v228 offset:12544\n\t" \
  "v_cvt_pk_bf16_f32 v154, v100, v101\n\t" \
  "v_mfma_f32_16x16x32_bf16 v[36:39], a[212:215], v[52:55], v[36:39]\n\t" \
  "v_exp_f32 v102, v102\n\t" \
  "v_mfma_f32_16x16x32_bf16 v[36:39], a[208:211], v[48:51], v[36:39]\n\t" \
  "ds_read_b64_tr_b16 a[152:153], v229 offset:256\n\t" \
  "ds_read_b64_tr_b16 a[154:155], v229 offset:4352\n\t" \
  "v_mfma_f32_16x16x32_bf16 v[40:43], a[224:227], v[48:51], v[180:183]\n\t" \
  "v_exp_f32 v103, v103\n\t" \
  "v_mfma_f32_16x16x32_bf16 v[40:43], a[228:231], v[52:55], v[40:43]\n\t" \
  "ds_read_b64_tr_b16 a[156:157], v229 offset:8448\n\t" \
  "v_cvt_pk_bf16_f32 v155, v102, v103\n\t" \
  "v_mfma_f32_16x16x32_bf16 v[40:43], a[232:235], v[56:59], v[40:43]\n\t" \
  "v_exp_f32 v104, v104\n\t" \
  "v_mfma_f32_16x16x32_bf16 v[40:43], a[236:239], v[60:63], v[40:43]\n\t" \
  "v_exp_f32 v105, v105\n\t" \
  "v_mfma_f32_16x16x32_bf16 v[44:47], a[252:255], v[60:63], v[180:183]\n\t" \
  "ds_read_b64_tr_b16 a[158:159], v229 offset:12544\n\t" \
  "v_cvt_pk_bf16_f32 v156, v104, v105\n\t" \
  "v_mfma_f32_16x16x32_bf16 v[44:47], a[248:251], v[56:59], v[44:47]\n\t" \
  "v_exp_f32 v106, v106\n\t" \
  "v_mfma_f32_16x16x32_bf16 v[44:47], a[244:247], v[52:55], v[44:47]\n\t" \
  "ds_read_b64_tr_b16 a[160:161], v228 offset:512\n\t" \
  "ds_read_b64_tr_b16 a[162:163], v228 offset:4608\n\t" \
  "v_mfma_f32_16x16x32_bf16 v[44:47], a[240:243], v[48:51], v[44:47]\n\t" \
  "v_exp_f32 v107, v107\n\t" \
  "v_mfma_f32_16x16x32_bf16 v[64:67], a[192:195], v[80:83], v[184:187]\n\t" \
  "buffer_load_dword %[pfd], v247, %[ws], 0 offen sc1\n\t" \
  "v_mfma_f32_16x16x32_bf16 v[64:67], a[196:199], v[84:87], v[64:67]\n\t" \
  "ds_read_b64_tr_b16 a[164:165], v228 offset:8704\n\t" \
  "v_cvt_pk_bf16_f32 v157, v106, v107\n\t" \
  "v_mfma_f32_16x16x32_bf16 v[64:67], a[200:203], v[88:91], v[64:67]\n\t" \
  "v_exp_f32 v108, v108\n\t" \
  "v_mfma_f32_16x16x32_bf16 v[64:67], a[204:207], v[92:95], v[64:67]\n\t" \
  "v_exp_f32 v109, v109\n\t" \
  "v_mfma_f32_16x16x32_bf16 v[68:71], a[220:223], v[92:95], v[184:187]\n\t" \
  "ds_read_b64_tr_b16 a[166:167], v228 offset:12800\n\t" \
  "v_cvt_pk_bf16_f32 v158, v108, v109\n\t" \
  "v_mfma_f32_16x16x32_bf16 v[68:71], a[216:219], v[88:91], v[68:71]\n\t" \
  "ds_read_b64_tr_b16 a[168:169], v229 offset:512\n\t" \
  "ds_read_b64_tr_b16 a[170:171], v229 offset:4608\n\t" \
  "v_mfma_f32_16x16x32_bf16 v[68:71], a[212:215], v[84:87], v[68:71]\n\t" \
  "v_exp_f32 v110, v110\n\t" \
  "v_mfma_f32_16x16x32_bf16 v[68:71], a[208:211], v[80:83], v[68:71]\n\t" \
  "v_exp_f32 v111, v111\n\t" \
  "v_mfma_f32_16x16x32_bf16 v[72:75], a[224:227], v[80:83], v[184:187]\n\t" \
  "ds_read_b64_tr_b16 a[172:173], v229 offset:8704\n\t" \
  "v_cvt_pk_bf16_f32 v159, v110, v111\n\t" \
  "v_mfma_f32_16x16x32_bf16 v[72:75], a[228:231], v[84:87], v[72:75]\n\t" \
  "v_exp_f32 v0, v0\n\t" \
  "v_mfma_f32_16x16x32_bf16 v[72:75], a[232:235], v[88:91], v[72:75]\n\t" \
  "v_exp_f32 v1, v1\n\t" \
  "v_mfma_f32_16x16x32_bf16 v[72:75], a[236:239], v[92:95], v[72:75]\n\t" \
  "ds_read_b64_tr_b16 a[174:175], v229 offset:12800\n\t" \
  "ds_read_b64_tr_b16 a[176:177], v228 offset:768\n\t" \
  "v_mfma_f32_16x16x32_bf16 v[76:79], a[252:255], v[92:95], v[184:187]\n\t" \
  "v_exp_f32 v2, v2\n\t" \
  "v_mfma_f32_16x16x32_bf16 v[76:79], a[248:251], v[88:91], v[76:79]\n\t" \
  "v_exp_f32 v3, v3\n\t" \
  "v_mfma_f32_16x16x32_bf16 v[76:79], a[244:247], v[84:87], v[76:79]\n\t" \
  "ds_read_b64_tr_b16 a[178:179], v228 offset:4864\n\t" \
  "ds_read_b64_tr_b16 a[180:181], v228 offset:8960\n\t" \
  "v_mfma_f32_16x16x32_bf16 v[76:79], a[240:243], v[80:83], v[76:79]\n\t" \
  "v_exp_f32 v4, v4\n\t" \
  "v_mfma_f32_16x16x32_bf16 v[96:99], a[192:195], v[112:115], v[188:191]\n\t" \
  "v_exp_f32 v5, v5\n\t" \
  "v_mfma_f32_16x16x32_bf16 v[96:99], a[196:199], v[116:119], v[96:99]\n\t" \
  "v_exp_f32 v6, v6\n\t" \
  "v_mfma_f32_16x16x32_bf16 v[96:99], a[200:203], v[120:123], v[96:99]\n\t" \
  "ds_read_b64_tr_b16 a[182:183], v228 offset:13056\n\t" \
  "ds_read_b64_tr_b16 a[184:185], v229 offset:768\n\t" \
  "v_mfma_f32_16x16x32_bf16 v[96:99], a[204:207], v[124:127], v[96:99]\n\t" \
  "v_exp_f32 v7, v7\n\t" \
  "v_mfma_f32_16x16x32_bf16 v[100:103], a[220:223], v[124:127], v[188:191]\n\t" \
  "v_exp_f32 v8, v8\n\t" \
  "v_mfma_f32_16x16x32_bf16 v[100:103], a[216:219], v[120:123], v[100:103]\n\t" \
  "ds_read_b64_tr_b16 a[186:187], v229 offset:4864\n\t" \
  "ds_read_b64_tr_b16 a[188:189], v229 offset:8960\n\t" \
  "v_mfma_f32_16x16x32_bf16 v[100:103], a[212:215], v[116:119], v[100:103]\n\t" \
  "v_exp_f32 v9, v9\n\t" \
  "v_mfma_f32_16x16x32_bf16 v[100:103], a[208:211], v[112:115], v[100:103]\n\t" \
  "v_exp_f32 v10, v10\n\t" \
  "v_mfma_f32_16x16x32_bf16 v[104:107], a[224:227], v[112:115], v[188:191]\n\t" \
  "v_exp_f32 v11, v11\n\t" \
  "v_mfma_f32_16x16x32_bf16 v[104:107], a[228:231], v[116:119], v[104:107]\n\t" \
  "ds_read_b64_tr_b16 a[190:191], v229 offset:13056\n\t" \
  "v_mfma_f32_16x16x32_bf16 v[104:107], a[232:235], v[120:123], v[104:107]\n\t" \
  "s_getreg_b32 s98, hwreg(HW_REG_IB_STS, 0, 4)\n\t" \
  "s_cmp_lg_u32 s98, 0\n\t" \
  "s_addc_u32 %[na], %[na], 0\n\t" \
  "v_mfma_f32_16x16x32_bf16 v[104:107], a[236:239], v[124:127], v[104:107]\n\t" \
  "v_exp_f32 v12, v12\n\t" \
  "v_mfma_f32_16x16x32_bf16 v[108:111], a[252:255], v[124:127], v[188:191]\n\t" \
  "v_exp_f32 v13, v13\n\t" \
  "v_mfma_f32_16x16x32_bf16 v[108:111], a[248:251], v[120:123], v[108:111]\n\t" \
  "s_getreg_b32 s98, hwreg(HW_REG_IB_STS, 0, 4)\n\t" \
  "s_cmp_lg_u32 s98, 0\n\t" \
  "s_addc_u32 %[na], %[na], 0\n\t" \
  "v_mfma_f32_16x16x32_bf16 v[108:111], a[244:247], v[116:119], v[108:111]\n\t" \
  "v_exp_f32 v14, v14\n\t" \
  "v_mfma_f32_16x16x32_bf16 v[108:111], a[240:243], v[112:115], v[108:111]\n\t" \
  "s_waitcnt vmcnt(4) lgkmcnt(0)\n\t" \
  "s_barrier\n\t" \
  STA("s_memrealtime s[94:95]\n\t") \
  STA("s_memtime s[92:93]\n\t") \
  "v_mfma_f32_16x16x32_bf16 a[0:3], a[128:131], v[128:131], a[0:3]\n\t" \
  "v_exp_f32 v15, v15\n\t" \
  "v_mfma_f32_16x16x32_bf16 a[0:3], a[132:135], v[132:135], a[0:3]\n\t" \
  "v_exp_f32 v32, v32\n\t" \
  "v_mfma_f32_16x16x32_bf16 a[16:19], a[136:139], v[128:131], a[16:19]\n\t" \
  "ds_read_b128 a[192:195], v220 offset:0\n\t" \
  "ds_read_b128 a[196:199], v221 offset:0\n\t" \
  "v_mfma_f32_16x16x32_bf16 a[16:19], a[140:143], v[132:135], a[16:19]\n\t" \
  "v_exp_f32 v33, v33\n\t" \
  "v_mfma_f32_16x16x32_bf16 a[32:35], a[144:147], v[128:131], a[32:35]\n\t" \
  "v_exp_f32 v34, v34\n\t" \
  "v_mfma_f32_16x16x32_bf16 a[32:35], a[148:151], v[132:135], a[32:35]\n\t" \
  "v_exp_f32 v35, v35\n\t" \
  "v_mfma_f32_16x16x32_bf16 a[48:51], a[152:155], v[128:131], a[48:51]\n\t" \
  STS("v_writelane_b32 %[stv], %[na], 58\n\t") \
  "s_cmp_ge_u32 %[na], 1\n\t" \
  "s_cselect_b32 s98, 0x4000, 0\n\t" \
  "s_or_b32 s98, s98, %[tag]\n\t" \
  "s_cmp_ge_u32 %[na], 2\n\t" \
  "s_cselect_b32 %[na], 2.0, 0\n\t" \
  "s_or_b32 s98, s98, %[na]\n\t" \
  "v_mov_b32 v209, s98\n\t" \
  "v_mfma_f32_16x16x32_bf16 a[48:51], a[156:159], v[132:135], a[48:51]\n\t" \
  "ds_read_b128 a[200:203], v222 offset:0\n\t" \
  "ds_read_b128 a[204:207], v223 offset:0\n\t" \
  "v_mfma_f32_16x16x32_bf16 a[64:67], a[160:163], v[128:131], a[64:67]\n\t" \
  "v_exp_f32 v36, v36\n\t" \
  "v_mfma_f32_16x16x32_bf16 a[64:67], a[164:167], v[132:135], a[64:67]\n\t" \
  "v_exp_f32 v37, v37\n\t" \
  "v_mfma_f32_16x16x32_bf16 a[80:83], a[168:171], v[128:131], a[80:83]\n\t" \
  "ds_read_b128 a[208:211], v220 offset:4096\n\t" \
  "ds_read_b128 a[212:215], v221 offset:4096\n\t" \
  "v_mfma_f32_16x16x32_bf16 a[80:83], a[172:175], v[132:135], a[80:83]\n\t" \
  "ds_read_b128 a[216:219], v222 offset:4096\n\t" \
  "v_bfrev_b32 v205, 1\n\t" \
  "v_writelane_b32 v205, %[vo], 0\n\t" \
  "v_mfma_f32_16x16x32_bf16 a[96:99], a[176:179], v[128:131], a[96:99]\n\t" \
  STA("v_writelane_b32 %[stv], s94, 36\n\t") \
  STA("v_writelane_b32 %[stv], s92, 37\n\t") \
  "buffer_store_dword v209, v205, %[ws], 0 offen sc1\n\t" \
  "v_mfma_f32_16x16x32_bf16 a[96:99], a[180:183], v[132:135], a[96:99]\n\t" \
  "v_exp_f32 v38, v38\n\t" \
  "v_mfma_f32_16x16x32_bf16 a[112:115], a[184:187], v[128:131], a[112:115]\n\t" \
  "v_exp_f32 v39, v39\n\t" \
  "v_mfma_f32_16x16x32_bf16 a[112:115], a[188:191], v[132:135], a[112:115]\n\t" \
  "v_exp_f32 v40, v40\n\t" \
  "v_smfmac_f32_16x16x64_bf16 v[160:163], v[216:219], v[128:135], v239\n\t" \
  "v_exp_f32 v41, v41\n\t" \
  "v_mfma_f32_16x16x32_bf16 a[4:7], a[128:131], v[136:139], a[4:7]\n\t" \
  "v_exp_f32 v42, v42\n\t" \
  "v_mfma_f32_16x16x32_bf16 a[4:7], a[132:135], v[140:143], a[4:7]\n\t" \
  "v_exp_f32 v43, v43\n\t" \
  "v_mfma_f32_16x16x32_bf16 a[20:23], a[136:139], v[136:139], a[20:23]\n\t" \
  "v_exp_f32 v44, v44\n\t" \
  "v_mfma_f32_16x16x32_bf16 a[20:23], a[140:143], v[140:143], a[20:23]\n\t" \
  "s_add_u32 m0, %[m], 65536\n\t" \
  "v_exp_f32 v45, v45\n\t" \
  "v_mfma_f32_16x16x32_bf16 a[36:39], a[144:147], v[136:139], a[36:39]\n\t" \
  "s_add_u32 s96, %[sofk], 81920\n\t" \
  "v_exp_f32 v46, v46\n\t" \
  "v_mfma_f32_16x16x32_bf16 a[36:39], a[148:151], v[140:143], a[36:39]\n\t" \
  "v_exp_f32 v47, v47\n\t" \
  "v_mfma_f32_16x16x32_bf16 a[52:55], a[152:155], v[136:139], a[52:55]\n\t" \
  "v_exp_f32 v64, v64\n\t" \
  "v_mfma_f32_16x16x32_bf16 a[52:55], a[156:159], v[140:143], a[52:55]\n\t" \
  "v_exp_f32 v65, v65\n\t" \
  "v_mfma_f32_16x16x32_bf16 a[68:71], a[160:163], v[136:139], a[68:71]\n\t" \
  "ds_read_b128 a[220:223], v223 offset:4096\n\t" \
  "v_cvt_pk_bf16_f32 v128, v0, v1\n\t" \
  "v_mfma_f32_16x16x32_bf16 a[68:71], a[164:167], v[140:143], a[68:71]\n\t" \
  "v_exp_f32 v66, v66\n\t" \
  "v_mfma_f32_16x16x32_bf16 a[84:87], a[168:171], v[136:139], a[84:87]\n\t" \
  "buffer_load_dwordx4 v224, %[sK], s96 offen offset:0 lds\n\t" \
  "v_mfma_f32_16x16x32_bf16 a[84:87], a[172:175], v[140:143], a[84:87]\n\t" \
  "ds_read_b128 a[224:227], v220 offset:8192\n\t" \
  "v_cvt_pk_bf16_f32 v129, v2, v3\n\t" \
  "v_mfma_f32_16x16x32_bf16 a[100:103], a[176:179], v[136:139], a[100:103]\n\t" \
  "v_exp_f32 v67, v67\n\t" \
  "v_mfma_f32_16x16x32_bf16 a[100:103], a[180:183], v[140:143], a[100:103]\n\t" \
  "buffer_load_dwordx4 v225, %[sK], s96 offen offset:1024 lds\n\t" \
  "v_mfma_f32_16x16x32_bf16 a[116:119], a[184:187], v[136:139], a[116:119]\n\t" \
  "v_exp_f32 v68, v68\n\t" \
  "v_mfma_f32_16x16x32_bf16 a[116:119], a[188:191], v[140:143], a[116:119]\n\t" \
  "ds_read_b128 a[228:231], v221 offset:8192\n\t" \
  "v_cvt_pk_bf16_f32 v130, v4, v5\n\t" \
  "v_smfmac_f32_16x16x64_bf16 v[164:167], v[216:219], v[136:143], v239\n\t" \
  "buffer_load_dwordx4 v226, %[sK], s96 offen offset:2048 lds\n\t" \
  "v_mfma_f32_16x16x32_bf16 a[8:11], a[128:131], v[144:147], a[8:11]\n\t" \
  "v_exp_f32 v69, v69\n\t" \
  "v_mfma_f32_16x16x32_bf16 a[8:11], a[132:135], v[148:151], a[8:11]\n\t" \
  "ds_read_b128 a[232:235], v222 offset:8192\n\t" \
  "v_cvt_pk_bf16_f32 v131, v6, v7\n\t" \
  "v_mfma_f32_16x16x32_bf16 a[24:27], a[136:139], v[144:147], a[24:27]\n\t" \
  "v_exp_f32 v70, v70\n\t" \
  "v_mfma_f32_16x16x32_bf16 a[24:27], a[140:143], v[148:151], a[24:27]\n\t" \
  "buffer_load_dwordx4 v227, %[sK], s96 offen offset:3072 lds\n\t" \
  "v_mfma_f32_16x16x32_bf16 a[40:43], a[144:147], v[144:147], a[40:43]\n\t" \
  "v_exp_f32 v71, v71\n\t" \
  "v_mfma_f32_16x16x32_bf16 a[40:43], a[148:151], v[148:151], a[40:43]\n\t" \
  "ds_read_b128 a[236:239], v223 offset:8192\n\t" \
  "v_cvt_pk_bf16_f32 v132, v8, v9\n\t" \
  "v_mfma_f32_16x16x32_bf16 a[56:59], a[152:155], v[144:147], a[56:59]\n\t" \
  "s_add_u32 m0, %[m], 32768\n\t" \
  "s_mov_b32 s96, 0\n\t" \
  "ds_read_b128 a[240:243], v220 offset:12288\n\t" \
  "v_mfma_f32_16x16x32_bf16 a[56:59], a[156:159], v[148:151], a[56:59]\n\t" \
  "v_exp_f32 v72, v72\n\t" \
  "v_mfma_f32_16x16x32_bf16 a[72:75], a[160:163], v[144:147], a[72:75]\n\t" \
  "buffer_load_dwordx4 v232, %[sG], s96 offen offset:0 lds\n\t" \
  "v_mfma_f32_16x16x32_bf16 a[72:75], a[164:167], v[148:151], a[72:75]\n\t" \
  "s_add_u32 m0, %[m], 81920\n\t" \
  ".Lhs7:\n\ts_cbranch_execz .Lhs7n\n\t" \
  "v_exp_f32 v73, v73\n\t" \
  "v_mfma_f32_16x16x32_bf16 a[88:91], a[168:171], v[144:147], a[88:91]\n\t" \
  "s_add_u32 s96, %[sofk], 65536\n\t" \
  "v_exp_f32 v74, v74\n\t" \
  "v_mfma_f32_16x16x32_bf16 a[88:91], a[172:175], v[148:151], a[88:91]\n\t" \
  "v_exp_f32 v75, v75\n\t" \
  "v_mfma_f32_16x16x32_bf16 a[104:107], a[176:179], v[144:147], a[104:107]\n\t" \
  "ds_read_b128 a[244:247], v221 offset:12288\n\t" \
  "v_cvt_pk_bf16_f32 v133, v10, v11\n\t" \
  "v_mfma_f32_16x16x32_bf16 a[104:107], a[180:183], v[148:151], a[104:107]\n\t" \
  "v_exp_f32 v76, v76\n\t" \
  "v_mfma_f32_16x16x32_bf16 a[120:123], a[184:187], v[144:147], a[120:123]\n\t" \
  "v_exp_f32 v77, v77\n\t" \
  "v_mfma_f32_16x16x32_bf16 a[120:123], a[188:191], v[148:151], a[120:123]\n\t" \
  "buffer_load_dwordx4 v230, %[sV], s96 offen offset:0 lds\n\t" \
  "v_smfmac_f32_16x16x64_bf16 v[168:171], v[216:219], v[144:151], v239\n\t" \
  "ds_read_b128 a[248:251], v222 offset:12288\n\t" \
  "v_cvt_pk_bf16_f32 v134, v12, v13\n\t" \
  "v_mfma_f32_16x16x32_bf16 a[12:15], a[128:131], v[152:155], a[12:15]\n\t" \
  "v_exp_f32 v78, v78\n\t" \
  "v_mfma_f32_16x16x32_bf16 a[12:15], a[132:135], v[156:159], a[12:15]\n\t" \
  "buffer_load_dwordx4 v231, %[sV], s96 offen offset:1024 lds\n\t" \
  "v_mfma_f32_16x16x32_bf16 a[28:31], a[136:139], v[152:155], a[28:31]\n\t" \
  "ds_read_b128 a[252:255], v223 offset:12288\n\t" \
  "v_cvt_pk_bf16_f32 v135, v14, v15\n\t" \
  "v_mfma_f32_16x16x32_bf16 a[28:31], a[140:143], v[156:159], a[28:31]\n\t" \
  "v_exp_f32 v79, v79\n\t" \
  "v_mfma_f32_16x16x32_bf16 a[44:47], a[144:147], v[152:155], a[44:47]\n\t" \
  "buffer_load_dwordx4 v230, %[sV], s96 offen offset:2048 lds\n\t" \
  "v_mfma_f32_16x16x32_bf16 a[44:47], a[148:151], v[156:159], a[44:47]\n\t" \
  "v_exp_f32 v96, v96\n\t" \
  "v_mfma_f32_16x16x32_bf16 a[60:63], a[152:155], v[152:155], a[60:63]\n\t" \
  "v_exp_f32 v97, v97\n\t" \
  "v_mfma_f32_16x16x32_bf16 a[60:63], a[156:159], v[156:159], a[60:63]\n\t" \
  "v_exp_f32 v98, v98\n\t" \
  "v_mfma_f32_16x16x32_bf16 a[76:79], a[160:163], v[152:155], a[76:79]\n\t" \
  "buffer_load_dwordx4 v231, %[sV], s96 offen offset:3072 lds\n\t" \
  "v_mfma_f32_16x16x32_bf16 a[76:79], a[164:167], v[156:159], a[76:79]\n\t" \
  "v_exp_f32 v99, v99\n\t" \
  "v_mfma_f32_16x16x32_bf16 a[92:95], a[168:171], v[152:155], a[92:95]\n\t" \
  "v_cvt_pk_bf16_f32 v136, v32, v33\n\t" \
  "v_cvt_pk_bf16_f32 v137, v34, v35\n\t" \
  "v_mfma_f32_16x16x32_bf16 a[92:95], a[172:175], v[156:159], a[92:95]\n\t" \
  "s_add_u32 m0, %[m], 32768\n\t" \
  "s_mov_b32 s96, 6144\n\t" \
  "v_cvt_pk_bf16_f32 v138, v36, v37\n\t" \
  "v_mfma_f32_16x16x32_bf16 a[108:111], a[176:179], v[152:155], a[108:111]\n\t" \
  "v_exp_f32 v100, v100\n\t" \
  "v_mfma_f32_16x16x32_bf16 a[108:111], a[180:183], v[156:159], a[108:111]\n\t" \
  "buffer_load_dwordx4 v232, %[sG], s96 offen offset:1024 lds\n\t" \
  "v_mfma_f32_16x16x32_bf16 a[124:127], a[184:187], v[152:155], a[124:127]\n\t" \
  "s_add_u32 s98, %[pf4], 28672\n\t" \
  "v_exp_f32 v101, v101\n\t" \
  "v_mfma_f32_16x16x32_bf16 a[124:127], a[188:191], v[156:159], a[124:127]\n\t" \
  "buffer_load_dword %[pfd], v245, %[sG], s98 offen\n\t" \
  "v_smfmac_f32_16x16x64_bf16 v[172:175], v[216:219], v[152:159], v239\n\t" \
  "s_waitcnt lgkmcnt(0)\n\t" \
  "v_exp_f32 v102, v102\n\t" \
  STA("s_memrealtime s[94:95]\n\t") \
  STA("s_memtime s[92:93]\n\t") \
  "v_mfma_f32_16x16x32_bf16 v[0:3], a[192:195], v[16:19], v[176:179]\n\t" \
  "ds_read_b64_tr_b16 a[128:129], v237 offset:0\n\t" \
  "v_cvt_pk_bf16_f32 v139, v38, v39\n\t" \
  "v_mfma_f32_16x16x32_bf16 v[0:3], a[196:199], v[20:23], v[0:3]\n\t" \
  "v_exp_f32 v103, v103\n\t" \
  "v_mfma_f32_16x16x32_bf16 v[0:3], a[200:203], v[24:27], v[0:3]\n\t" \
  "ds_read_b64_tr_b16 a[130:131], v237 offset:4096\n\t" \
  "v_cvt_pk_bf16_f32 v140, v40, v41\n\t" \
  "v_mfma_f32_16x16x32_bf16 v[0:3], a[204:207], v[28:31], v[0:3]\n\t" \
  "v_exp_f32 v104, v104\n\t" \
  "v_mfma_f32_16x16x32_bf16 v[4:7], a[220:223], v[28:31], v[176:179]\n\t" \
  "ds_read_b64_tr_b16 a[132:133], v237 offset:8192\n\t" \
  "v_cvt_pk_bf16_f32 v141, v42, v43\n\t" \
  "v_mfma_f32_16x16x32_bf16 v[4:7], a[216:219], v[24:27], v[4:7]\n\t" \
  "v_exp_f32 v105, v105\n\t" \
  "v_mfma_f32_16x16x32_bf16 v[4:7], a[212:215], v[20:23], v[4:7]\n\t" \
  "ds_read_b64_tr_b16 a[134:135], v237 offset:12288\n\t" \
  "v_cvt_pk_bf16_f32 v142, v44, v45\n\t" \
  "v_mfma_f32_16x16x32_bf16 v[4:7], a[208:211], v[16:19], v[4:7]\n\t" \
  "ds_read_b64_tr_b16 a[136:137], v238 offset:0\n\t" \
  "v_cvt_pk_bf16_f32 v143, v46, v47\n\t" \
  "v_mfma_f32_16x16x32_bf16 v[8:11], a[224:227], v[16:19], v[176:179]\n\t" \
  "v_exp_f32 v106, v106\n\t" \
  "v_mfma_f32_16x16x32_bf16 v[8:11], a[228:231], v[20:23], v[8:11]\n\t" \
  "ds_read_b64_tr_b16 a[138:139], v238 offset:4096\n\t" \
  "v_cvt_pk_bf16_f32 v144, v64, v65\n\t" \
  "v_mfma_f32_16x16x32_bf16 v[8:11], a[232:235], v[24:27], v[8:11]\n\t" \
  "v_exp_f32 v107, v107\n\t" \
  "v_mfma_f32_16x16x32_bf16 v[8:11], a[236:239], v[28:31], v[8:11]\n\t" \
  "s_add_u32 s98, %[pf2], 0\n\t" \
  "ds_read_b64_tr_b16 a[140:141], v238 offset:8192\n\t" \
  "v_cvt_pk_bf16_f32 v145, v66, v67\n\t" \
  "v_mfma_f32_16x16x32_bf16 v[12:15], a[252:255], v[28:31], v[176:179]\n\t" \
  STA("v_writelane_b32 %[stv], s94, 38\n\t") \
  STA("v_writelane_b32 %[stv], s92, 39\n\t") \
  "buffer_load_dword %[pfd], v202, %[sM], s98 offen\n\t" \
  "v_mfma_f32_16x16x32_bf16 v[12:15], a[248:251], v[24:27], v[12:15]\n\t" \
  "ds_read_b64_tr_b16 a[142:143], v238 offset:12288\n\t" \
  "v_cvt_pk_bf16_f32 v146, v68, v69\n\t" \
  "v_mfma_f32_16x16x32_bf16 v[12:15], a[244:247], v[20:23], v[12:15]\n\t" \
  "ds_read_b64_tr_b16 a[144:145], v237 offset:256\n\t" \
  "v_cvt_pk_bf16_f32 v147, v70, v71\n\t" \
  "v_mfma_f32_16x16x32_bf16 v[12:15], a[240:243], v[16:19], v[12:15]\n\t" \
  "v_exp_f32 v108, v108\n\t" \
  "v_mfma_f32_16x16x32_bf16 v[32:35], a[192:195], v[48:51], v[180:183]\n\t" \
  "ds_read_b64_tr_b16 a[146:147], v237 offset:4352\n\t" \
  "v_cvt_pk_bf16_f32 v148, v72, v73\n\t" \
  "v_mfma_f32_16x16x32_bf16 v[32:35], a[196:199], v[52:55], v[32:35]\n\t" \
  "v_exp_f32 v109, v109\n\t" \
  "v_mfma_f32_16x16x32_bf16 v[32:35], a[200:203], v[56:59], v[32:35]\n\t" \
  "ds_read_b64_tr_b16 a[148:149], v237 offset:8448\n\t" \
  "v_cvt_pk_bf16_f32 v149, v74, v75\n\t" \
  "v_mfma_f32_16x16x32_bf16 v[32:35], a[204:207], v[60:63], v[32:35]\n\t" \
  "v_exp_f32 v110, v110\n\t" \
  "v_mfma_f32_16x16x32_bf16 v[36:39], a[220:223], v[60:63], v[180:183]\n\t" \
  "ds_read_b64_tr_b16 a[150:151], v237 offset:12544\n\t" \
  "v_cvt_pk_bf16_f32 v150, v76, v77\n\t" \
  "v_mfma_f32_16x16x32_bf16 v[36:39], a[216:219], v[56:59], v[36:39]\n\t" \
  "ds_read_b64_tr_b16 a[152:153], v238 offset:256\n\t" \
  "v_cvt_pk_bf16_f32 v151, v78, v79\n\t" \
  "v_mfma_f32_16x16x32_bf16 v[36:39], a[212:215], v[52:55], v[36:39]\n\t" \
  "v_exp_f32 v111, v111\n\t" \
  "v_mfma_f32_16x16x32_bf16 v[36:39], a[208:211], v[48:51], v[36:39]\n\t" \
  "ds_read_b64_tr_b16 a[154:155], v238 offset:4352\n\t" \
  "v_cvt_pk_bf16_f32 v152, v96, v97\n\t" \
  "v_mfma_f32_16x16x32_bf16 v[40:43], a[224:227], v[48:51], v[180:183]\n\t" \
  "v_exp_f32 v0, v0\n\t" \
  "v_mfma_f32_16x16x32_bf16 v[40:43], a[228:231], v[52:55], v[40:43]\n\t" \
  "ds_read_b64_tr_b16 a[156:157], v238 offset:8448\n\t" \
  "v_cvt_pk_bf16_f32 v153, v98, v99\n\t" \
  "v_mfma_f32_16x16x32_bf16 v[40:43], a[232:235], v[56:59], v[40:43]\n\t" \
  "v_exp_f32 v1, v1\n\t" \
  "v_mfma_f32_16x16x32_bf16 v[40:43], a[236:239], v[60:63], v[40:43]\n\t" \
  "ds_read_b64_tr_b16 a[158:159], v238 offset:12544\n\t" \
  "v_cvt_pk_bf16_f32 v154, v100, v101\n\t" \
  "v_mfma_f32_16x16x32_bf16 v[44:47], a[252:255], v[60:63], v[180:183]\n\t" \
  "ds_read_b64_tr_b16 a[160:161], v237 offset:512\n\t" \
  "v_cvt_pk_bf16_f32 v155, v102, v103\n\t" \
  "v_mfma_f32_16x16x32_bf16 v[44:47], a[248:251], v[56:59], v[44:47]\n\t" \
  "v_exp_f32 v2, v2\n\t" \
  "v_mfma_f32_16x16x32_bf16 v[44:47], a[244:247], v[52:55], v[44:47]\n\t" \
  "ds_read_b64_tr_b16 a[162:163], v237 offset:4608\n\t" \
  "v_cvt_pk_bf16_f32 v156, v104, v105\n\t" \
  "v_mfma_f32_16x16x32_bf16 v[44:47], a[240:243], v[48:51], v[44:47]\n\t" \
  "v_exp_f32 v3, v3\n\t" \
  "v_mfma_f32_16x16x32_bf16 v[64:67], a[192:195], v[80:83], v[184:187]\n\t" \
  "ds_read_b64_tr_b16 a[164:165], v237 offset:8704\n\t" \
  "v_cvt_pk_bf16_f32 v157, v106, v107\n\t" \
  "v_mfma_f32_16x16x32_bf16 v[64:67], a[196:199], v[84:87], v[64:67]\n\t" \
  "v_exp_f32 v4, v4\n\t" \
  "v_mfma_f32_16x16x32_bf16 v[64:67], a[200:203], v[88:91], v[64:67]\n\t" \
  "ds_read_b64_tr_b16 a[166:167], v237 offset:12800\n\t" \
  "v_cvt_pk_bf16_f32 v158, v108, v109\n\t" \
  "v_mfma_f32_16x16x32_bf16 v[64:67], a[204:207], v[92:95], v[64:67]\n\t" \
  "ds_read_b64_tr_b16 a[168:169], v238 offset:512\n\t" \
  "v_cvt_pk_bf16_f32 v159, v110, v111\n\t" \
  "v_mfma_f32_16x16x32_bf16 v[68:71], a[220:223], v[92:95], v[184:187]\n\t" \
  "v_exp_f32 v5, v5\n\t" \
  "v_mfma_f32_16x16x32_bf16 v[68:71], a[216:219], v[88:91], v[68:71]\n\t" \
  "v_exp_f32 v6, v6\n\t" \
  "v_mfma_f32_16x16x32_bf16 v[68:71], a[212:215], v[84:87], v[68:71]\n\t" \
  "v_exp_f32 v7, v7\n\t" \
  "v_mfma_f32_16x16x32_bf16 v[68:71], a[208:211], v[80:83], v[68:71]\n\t" \
  "ds_read_b64_tr_b16 a[170:171], v238 offset:4608\n\t" \
  "ds_read_b64_tr_b16 a[172:173], v238 offset:8704\n\t" \
  "v_mfma_f32_16x16x32_bf16 v[72:75], a[224:227], v[80:83], v[184:187]\n\t" \
  "v_exp_f32 v8, v8\n\t" \
  "v_mfma_f32_16x16x32_bf16 v[72:75], a[228:231], v[84:87], v[72:75]\n\t" \
  "v_exp_f32 v9, v9\n\t" \
  "v_mfma_f32_16x16x32_bf16 v[72:75], a[232:235], v[88:91], v[72:75]\n\t" \
  "v_exp_f32 v10, v10\n\t" \
  "v_mfma_f32_16x16x32_bf16 v[72:75], a[236:239], v[92:95], v[72:75]\n\t" \
  "ds_read_b64_tr_b16 a[174:175], v238 offset:12800\n\t" \
  "ds_read_b64_tr_b16 a[176:177], v237 offset:768\n\t" \
  "v_mfma_f32_16x16x32_bf16 v[76:79], a[252:255], v[92:95], v[184:187]\n\t" \
  "v_exp_f32 v11, v11\n\t" \
  "v_mfma_f32_16x16x32_bf16 v[76:79], a[248:251], v[88:91], v[76:79]\n\t" \
  "v_exp_f32 v12, v12\n\t" \
  "v_mfma_f32_16x16x32_bf16 v[76:79], a[244:247], v[84:87], v[76:79]\n\t" \
  "ds_read_b64_tr_b16 a[178:179], v237 offset:4864\n\t" \
  "ds_read_b64_tr_b16 a[180:181], v237 offset:8960\n\t" \
  "v_mfma_f32_16x16x32_bf16 v[76:79], a[240:243], v[80:83], v[76:79]\n\t" \
  "v_exp_f32 v13, v13\n\t" \
  "v_mfma_f32_16x16x32_bf16 v[96:99], a[192:195], v[112:115], v[188:191]\n\t" \
  "v_exp_f32 v14, v14\n\t" \
  "v_mfma_f32_16x16x32_bf16 v[96:99], a[196:199], v[116:119], v[96:99]\n\t" \
  "v_exp_f32 v15, v15\n\t" \
  "v_mfma_f32_16x16x32_bf16 v[96:99], a[200:203], v[120:123], v[96:99]\n\t" \
  "ds_read_b64_tr_b16 a[182:183], v237 offset:13056\n\t" \
  "ds_read_b64_tr_b16 a[184:185], v238 offset:768\n\t" \
  "v_mfma_f32_16x16x32_bf16 v[96:99], a[204:207], v[124:127], v[96:99]\n\t" \
  "v_exp_f32 v32, v32\n\t" \
  "v_mfma_f32_16x16x32_bf16 v[100:103], a[220:223], v[124:127], v[188:191]\n\t" \
  "v_exp_f32 v33, v33\n\t" \
  "v_mfma_f32_16x16x32_bf16 v[100:103], a[216:219], v[120:123], v[100:103]\n\t" \
  "ds_read_b64_tr_b16 a[186:187], v238 offset:4864\n\t" \
  "ds_read_b64_tr_b16 a[188:189], v238 offset:8960\n\t" \
  "v_mfma_f32_16x16x32_bf16 v[100:103], a[212:215], v[116:119], v[100:103]\n\t" \
  "v_exp_f32 v34, v34\n\t" \
  "v_mfma_f32_16x16x32_bf16 v[100:103], a[208:211], v[112:115], v[100:103]\n\t" \
  "v_exp_f32 v35, v35\n\t" \
  "v_mfma_f32_16x16x32_bf16 v[104:107], a[224:227], v[112:115], v[188:191]\n\t" \
  "v_exp_f32 v36, v36\n\t" \
  "v_mfma_f32_16x16x32_bf16 v[104:107], a[228:231], v[116:119], v[104:107]\n\t" \
  "ds_read_b64_tr_b16 a[190:191], v238 offset:13056\n\t" \
  "v_mfma_f32_16x16x32_bf16 v[104:107], a[232:235], v[120:123], v[104:107]\n\t" \
  "v_exp_f32 v37, v37\n\t" \
  "v_mfma_f32_16x16x32_bf16 v[104:107], a[236:239], v[124:127], v[104:107]\n\t" \
  "v_exp_f32 v38, v38\n\t" \
  "v_mfma_f32_16x16x32_bf16 v[108:111], a[252:255], v[124:127], v[188:191]\n\t" \
  "v_exp_f32 v39, v39\n\t" \
  "v_mfma_f32_16x16x32_bf16 v[108:111], a[248:251], v[120:123], v[108:111]\n\t" \
  "v_exp_f32 v40, v40\n\t" \
  "v_mfma_f32_16x16x32_bf16 v[108:111], a[244:247], v[116:119], v[108:111]\n\t" \
  "v_exp_f32 v41, v41\n\t" \
  "v_mfma_f32_16x16x32_bf16 v[108:111], a[240:243], v[112:115], v[108:111]\n\t" \
  "s_waitcnt vmcnt(3) lgkmcnt(0)\n\t" \
  "s_barrier\n\t" \
  STA("s_memrealtime s[94:95]\n\t") \
  STA("s_memtime s[92:93]\n\t") \
  "v_mfma_f32_16x16x32_bf16 a[0:3], a[128:131], v[128:131], a[0:3]\n\t" \
  "v_exp_f32 v42, v42\n\t" \
  "v_mfma_f32_16x16x32_bf16 a[0:3], a[132:135], v[132:135], a[0:3]\n\t" \
  "v_exp_f32 v43, v43\n\t" \
  "v_mfma_f32_16x16x32_bf16 a[16:19], a[136:139], v[128:131], a[16:19]\n\t" \
  "ds_read_b128 a[192:195], v220 offset:16384\n\t" \
  "ds_read_b128 a[196:199], v221 offset:16384\n\t" \
  "v_mfma_f32_16x16x32_bf16 a[16:19], a[140:143], v[132:135], a[16:19]\n\t" \
  "v_exp_f32 v44, v44\n\t" \
  "v_mfma_f32_16x16x32_bf16 a[32:35], a[144:147], v[128:131], a[32:35]\n\t" \
  "s_lshr_b32 s98, %[sofk], 6\n\t" \
  "v_xor_b32 v234, s98, v244\n\t" \
  "v_add_u32 v213, %[m], v242\n\t" \
  "v_mfma_f32_16x16x32_bf16 a[32:35], a[148:151], v[132:135], a[32:35]\n\t" \
  "v_exp_f32 v45, v45\n\t" \
  "v_mfma_f32_16x16x32_bf16 a[48:51], a[152:155], v[128:131], a[48:51]\n\t" \
  "v_exp_f32 v46, v46\n\t" \
  "v_mfma_f32_16x16x32_bf16 a[48:51], a[156:159], v[132:135], a[48:51]\n\t" \
  "ds_read_b128 a[200:203], v222 offset:16384\n\t" \
  "v_add_u32 v215, 98304, v213\n\t" \
  "v_mfma_f32_16x16x32_bf16 a[64:67], a[160:163], v[128:131], a[64:67]\n\t" \
  "v_exp_f32 v47, v47\n\t" \
  "v_mfma_f32_16x16x32_bf16 a[64:67], a[164:167], v[132:135], a[64:67]\n\t" \
  "v_exp_f32 v64, v64\n\t" \
  "v_mfma_f32_16x16x32_bf16 a[80:83], a[168:171], v[128:131], a[80:83]\n\t" \
  "v_exp_f32 v65, v65\n\t" \
  "v_mfma_f32_16x16x32_bf16 a[80:83], a[172:175], v[132:135], a[80:83]\n\t" \
  "ds_read_b128 a[204:207], v223 offset:16384\n\t" \
  "v_mov_b32 v243, %[tag]\n\t" \
  "v_mfma_f32_16x16x32_bf16 a[96:99], a[176:179], v[128:131], a[96:99]\n\t" \
  STA("v_writelane_b32 %[stv], s94, 40\n\t") \
  STA("v_writelane_b32 %[stv], s92, 41\n\t") \
  "v_exp_f32 v66, v66\n\t" \
  "v_mfma_f32_16x16x32_bf16 a[96:99], a[180:183], v[132:135], a[96:99]\n\t" \
  "v_exp_f32 v67, v67\n\t" \
  "v_mfma_f32_16x16x32_bf16 a[112:115], a[184:187], v[128:131], a[112:115]\n\t" \
  "ds_read_b128 a[208:211], v220 offset:20480\n\t" \
  "v_lshrrev_b32 v254, 3, v233\n\t" \
  "v_mfma_f32_16x16x32_bf16 a[112:115], a[188:191], v[132:135], a[112:115]\n\t" \
  "v_exp_f32 v68, v68\n\t" \
  "v_smfmac_f32_16x16x64_bf16 v[160:163], v[216:219], v[128:135], v239\n\t" \
  "ds_read_b128 a[212:215], v221 offset:20480\n\t" \
  "v_min_u32 v254, 1, v254\n\t" \
  "v_mfma_f32_16x16x32_bf16 a[4:7], a[128:131], v[136:139], a[4:7]\n\t" \
  "ds_read_b128 a[216:219], v222 offset:20480\n\t" \
  "v_lshlrev_b32 v254, 31, v254\n\t" \
  "v_mfma_f32_16x16x32_bf16 a[4:7], a[132:135], v[140:143], a[4:7]\n\t" \
  "ds_read_b128 a[220:223], v223 offset:20480\n\t" \
  "v_lshl_or_b32 v254, v233, 9, v254\n\t" \
  "v_mfma_f32_16x16x32_bf16 a[20:23], a[136:139], v[136:139], a[20:23]\n\t" \
  "v_cvt_pk_bf16_f32 v128, v0, v1\n\t" \
  "v_cvt_pk_bf16_f32 v129, v2, v3\n\t" \
  "s_lshr_b32 m0, %[hl18], 18\n\t" \
  "s_brev_b32 s98, 1\n\t" \
  "v_writelane_b32 v254, s98, m0\n\t" \
  "v_mfma_f32_16x16x32_bf16 a[20:23], a[140:143], v[140:143], a[20:23]\n\t" \
  "s_add_u32 m0, %[m], 49152\n\t" \
  "v_exp_f32 v69, v69\n\t" \
  "v_mfma_f32_16x16x32_bf16 a[36:39], a[144:147], v[136:139], a[36:39]\n\t" \
  "s_add_u32 s96, %[sofk], 98304\n\t" \
  "v_exp_f32 v70, v70\n\t" \
  "v_mfma_f32_16x16x32_bf16 a[36:39], a[148:151], v[140:143], a[36:39]\n\t" \
  "v_exp_f32 v71, v71\n\t" \
  "v_mfma_f32_16x16x32_bf16 a[52:55], a[152:155], v[136:139], a[52:55]\n\t" \
  "v_exp_f32 v72, v72\n\t" \
  "v_mfma_f32_16x16x32_bf16 a[52:55], a[156:159], v[140:143], a[52:55]\n\t" \
  "v_exp_f32 v73, v73\n\t" \
  "v_mfma_f32_16x16x32_bf16 a[68:71], a[160:163], v[136:139], a[68:71]\n\t" \
  "v_exp_f32 v74, v74\n\t" \
  "v_mfma_f32_16x16x32_bf16 a[68:71], a[164:167], v[140:143], a[68:71]\n\t" \
  "v_exp_f32 v75, v75\n\t" \
  "v_mfma_f32_16x16x32_bf16 a[84:87], a[168:171], v[136:139], a[84:87]\n\t" \
  "buffer_load_dwordx4 v224, %[sK], s96 offen offset:0 lds\n\t" \
  "v_mfma_f32_16x16x32_bf16 a[84:87], a[172:175], v[140:143], a[84:87]\n\t" \
  "ds_read_b128 a[224:227], v220 offset:24576\n\t" \
  "v_cvt_pk_bf16_f32 v130, v4, v5\n\t" \
  "v_mfma_f32_16x16x32_bf16 a[100:103], a[176:179], v[136:139], a[100:103]\n\t" \
  "v_exp_f32 v76, v76\n\t" \
  "v_mfma_f32_16x16x32_bf16 a[100:103], a[180:183], v[140:143], a[100:103]\n\t" \
  "buffer_load_dwordx4 v225, %[sK], s96 offen offset:1024 lds\n\t" \
  "v_mfma_f32_16x16x32_bf16 a[116:119], a[184:187], v[136:139], a[116:119]\n\t" \
  "v_exp_f32 v77, v77\n\t" \
  "v_mfma_f32_16x16x32_bf16 a[116:119], a[188:191], v[140:143], a[116:119]\n\t" \
  "ds_read_b128 a[228:231], v221 offset:24576\n\t" \
  "v_cvt_pk_bf16_f32 v131, v6, v7\n\t" \
  "v_smfmac_f32_16x16x64_bf16 v[164:167], v[216:219], v[136:143], v239\n\t" \
  "buffer_load_dwordx4 v226, %[sK], s96 offen offset:2048 lds\n\t" \
  "v_mfma_f32_16x16x32_bf16 a[8:11], a[128:131], v[144:147], a[8:11]\n\t" \
  "v_exp_f32 v78, v78\n\t" \
  "v_mfma_f32_16x16x32_bf16 a[8:11], a[132:135], v[148:151], a[8:11]\n\t" \
  "ds_read_b128 a[232:235], v222 offset:24576\n\t" \
  "v_cvt_pk_bf16_f32 v132, v8, v9\n\t" \
  "v_mfma_f32_16x16x32_bf16 a[24:27], a[136:139], v[144:147], a[24:27]\n\t" \
  "v_exp_f32 v79, v79\n\t" \
  "v_mfma_f32_16x16x32_bf16 a[24:27], a[140:143], v[148:151], a[24:27]\n\t" \
  "buffer_load_dwordx4 v227, %[sK], s96 offen offset:3072 lds\n\t" \
  "v_mfma_f32_16x16x32_bf16 a[40:43], a[144:147], v[144:147], a[40:43]\n\t" \
  "v_exp_f32 v96, v96\n\t" \
  "v_mfma_f32_16x16x32_bf16 a[40:43], a[148:151], v[148:151], a[40:43]\n\t" \
  "ds_read_b128 a[236:239], v223 offset:24576\n\t" \
  "v_cvt_pk_bf16_f32 v133, v10, v11\n\t" \
  "v_mfma_f32_16x16x32_bf16 a[56:59], a[152:155], v[144:147], a[56:59]\n\t" \
  "s_add_u32 m0, %[m], 32768\n\t" \
  "s_mov_b32 s96, 12288\n\t" \
  "ds_read_b128 a[240:243], v220 offset:28672\n\t" \
  "v_mfma_f32_16x16x32_bf16 a[56:59], a[156:159], v[148:151], a[56:59]\n\t" \
  "v_exp_f32 v97, v97\n\t" \
  "v_mfma_f32_16x16x32_bf16 a[72:75], a[160:163], v[144:147], a[72:75]\n\t" \
  "buffer_load_dwordx4 v232, %[sG], s96 offen offset:2048 lds\n\t" \
  "v_mfma_f32_16x16x32_bf16 a[72:75], a[164:167], v[148:151], a[72:75]\n\t" \
  "s_add_u32 m0, %[m], 147456\n\t" \
  "v_exp_f32 v98, v98\n\t" \
  "v_mfma_f32_16x16x32_bf16 a[88:91], a[168:171], v[144:147], a[88:91]\n\t" \
  "s_add_u32 s96, %[sofk], 81920\n\t" \
  "v_exp_f32 v99, v99\n\t" \
  "v_mfma_f32_16x16x32_bf16 a[88:91], a[172:175], v[148:151], a[88:91]\n\t" \
  "v_exp_f32 v100, v100\n\t" \
  "v_mfma_f32_16x16x32_bf16 a[104:107], a[176:179], v[144:147], a[104:107]\n\t" \
  "ds_read_b128 a[244:247], v221 offset:28672\n\t" \
  "v_cvt_pk_bf16_f32 v134, v12, v13\n\t" \
  "v_mfma_f32_16x16x32_bf16 a[104:107], a[180:183], v[148:151], a[104:107]\n\t" \
  "v_exp_f32 v101, v101\n\t" \
  "v_mfma_f32_16x16x32_bf16 a[120:123], a[184:187], v[144:147], a[120:123]\n\t" \
  "v_exp_f32 v102, v102\n\t" \
  "v_mfma_f32_16x16x32_bf16 a[120:123], a[188:191], v[148:151], a[120:123]\n\t" \
  "buffer_load_dwordx4 v230, %[sV], s96 offen offset:0 lds\n\t" \
  "v_smfmac_f32_16x16x64_bf16 v[168:171], v[216:219], v[144:151], v239\n\t" \
  "ds_read_b128 a[248:251], v222 offset:28672\n\t" \
  "v_cvt_pk_bf16_f32 v135, v14, v15\n\t" \
  "v_mfma_f32_16x16x32_bf16 a[12:15], a[128:131], v[152:155], a[12:15]\n\t" \
  "v_exp_f32 v103, v103\n\t" \
  "v_mfma_f32_16x16x32_bf16 a[12:15], a[132:135], v[156:159], a[12:15]\n\t" \
  "buffer_load_dwordx4 v231, %[sV], s96 offen offset:1024 lds\n\t" \
  "v_mfma_f32_16x16x32_bf16 a[28:31], a[136:139], v[152:155], a[28:31]\n\t" \
  "ds_read_b128 a[252:255], v223 offset:28672\n\t" \
  "v_cvt_pk_bf16_f32 v136, v32, v33\n\t" \
  "v_mfma_f32_16x16x32_bf16 a[28:31], a[140:143], v[156:159], a[28:31]\n\t" \
  "v_exp_f32 v104, v104\n\t" \
  "v_mfma_f32_16x16x32_bf16 a[44:47], a[144:147], v[152:155], a[44:47]\n\t" \
  "buffer_load_dwordx4 v230, %[sV], s96 offen offset:2048 lds\n\t" \
  "v_mfma_f32_16x16x32_bf16 a[44:47], a[148:151], v[156:159], a[44:47]\n\t" \
  "v_exp_f32 v105, v105\n\t" \
  "v_mfma_f32_16x16x32_bf16 a[60:63], a[152:155], v[152:155], a[60:63]\n\t" \
  "v_exp_f32 v106, v106\n\t" \
  "v_mfma_f32_16x16x32_bf16 a[60:63], a[156:159], v[156:159], a[60:63]\n\t" \
  "v_exp_f32 v107, v107\n\t" \
  "v_mfma_f32_16x16x32_bf16 a[76:79], a[160:163], v[152:155], a[76:79]\n\t" \
  "buffer_load_dwordx4 v231, %[sV], s96 offen offset:3072 lds\n\t" \
  "v_mfma_f32_16x16x32_bf16 a[76:79], a[164:167], v[156:159], a[76:79]\n\t" \
  "v_exp_f32 v108, v108\n\t" \
  "v_mfma_f32_16x16x32_bf16 a[92:95], a[168:171], v[152:155], a[92:95]\n\t" \
  "v_cvt_pk_bf16_f32 v137, v34, v35\n\t" \
  "v_cvt_pk_bf16_f32 v138, v36, v37\n\t" \
  "v_mfma_f32_16x16x32_bf16 a[92:95], a[172:175], v[156:159], a[92:95]\n\t" \
  "s_add_u32 m0, %[m], 32768\n\t" \
  "s_mov_b32 s96, 18432\n\t" \
  "v_cvt_pk_bf16_f32 v139, v38, v39\n\t" \
  "v_mfma_f32_16x16x32_bf16 a[108:111], a[176:179], v[152:155], a[108:111]\n\t" \
  "v_exp_f32 v109, v109\n\t" \
  "v_mfma_f32_16x16x32_bf16 a[108:111], a[180:183], v[156:159], a[108:111]\n\t" \
  "buffer_load_dwordx4 v232, %[sG], s96 offen offset:3072 lds\n\t" \
  "v_mfma_f32_16x16x32_bf16 a[124:127], a[184:187], v[152:155], a[124:127]\n\t" \
  "s_add_u32 s98, %[pf4], 43008\n\t" \
  "v_exp_f32 v110, v110\n\t" \
  "v_mfma_f32_16x16x32_bf16 a[124:127], a[188:191], v[156:159], a[124:127]\n\t" \
  "buffer_load_dword %[pfd], v245, %[sG], s98 offen\n\t" \
  "v_smfmac_f32_16x16x64_bf16 v[172:175], v[216:219], v[152:159], v239\n\t" \
  "s_waitcnt lgkmcnt(0)\n\t" \
  "v_exp_f32 v111, v111\n\t" \
  STA("s_memrealtime s[94:95]\n\t") \
  STA("s_memtime s[92:93]\n\t") \
  "v_mfma_f32_16x16x32_bf16 v[0:3], a[192:195], v[16:19], v[176:179]\n\t" \
  "ds_read_b64_tr_b16 a[128:129], v228 offset:0\n\t" \
  "v_cvt_pk_bf16_f32 v140, v40, v41\n\t" \
  "v_mfma_f32_16x16x32_bf16 v[0:3], a[196:199], v[20:23], v[0:3]\n\t" \
  "ds_read_b64_tr_b16 a[130:131], v228 offset:4096\n\t" \
  "v_cvt_pk_bf16_f32 v141, v42, v43\n\t" \
  "v_mfma_f32_16x16x32_bf16 v[0:3], a[200:203], v[24:27], v[0:3]\n\t" \
  "ds_read_b64_tr_b16 a[132:133], v228 offset:8192\n\t" \
  "v_lshrrev_b32 v252, 3, v233\n\t" \
  "v_mfma_f32_16x16x32_bf16 v[0:3], a[204:207], v[28:31], v[0:3]\n\t" \
  "ds_read_b64_tr_b16 a[134:135], v228 offset:12288\n\t" \
  "v_cvt_pk_bf16_f32 v142, v44, v45\n\t" \
  "v_mfma_f32_16x16x32_bf16 v[4:7], a[220:223], v[28:31], v[176:179]\n\t" \
  "ds_read_b64_tr_b16 a[136:137], v229 offset:0\n\t" \
  "v_cvt_pk_bf16_f32 v143, v46, v47\n\t" \
  "v_mfma_f32_16x16x32_bf16 v[4:7], a[216:219], v[24:27], v[4:7]\n\t" \
  "ds_read_b64_tr_b16 a[138:139], v229 offset:4096\n\t" \
  "v_cvt_pk_bf16_f32 v144, v64, v65\n\t" \
  "v_mfma_f32_16x16x32_bf16 v[4:7], a[212:215], v[20:23], v[4:7]\n\t" \
  "v_exp_f32 v0, v0\n\t" \
  "v_mfma_f32_16x16x32_bf16 v[4:7], a[208:211], v[16:19], v[4:7]\n\t" \
  "v_exp_f32 v1, v1\n\t" \
  "v_mfma_f32_16x16x32_bf16 v[8:11], a[224:227], v[16:19], v[176:179]\n\t" \
  "v_exp_f32 v2, v2\n\t" \
  "v_mfma_f32_16x16x32_bf16 v[8:11], a[228:231], v[20:23], v[8:11]\n\t" \
  "v_exp_f32 v3, v3\n\t" \
  "v_mfma_f32_16x16x32_bf16 v[8:11], a[232:235], v[24:27], v[8:11]\n\t" \
  "ds_read_b64_tr_b16 a[140:141], v229 offset:8192\n\t" \
  "v_mul_u32_u24 v247, 98176, v252\n\t" \
  "v_mfma_f32_16x16x32_bf16 v[8:11], a[236:239], v[28:31], v[8:11]\n\t" \
  "s_add_u32 s98, %[pf2], 24576\n\t" \
  "v_exp_f32 v4, v4\n\t" \
  "v_mfma_f32_16x16x32_bf16 v[12:15], a[252:255], v[28:31], v[176:179]\n\t" \
  STA("v_writelane_b32 %[stv], s94, 42\n\t") \
  STA("v_writelane_b32 %[stv], s92, 43\n\t") \
  "buffer_load_dword %[pfd], v202, %[sM], s98 offen\n\t" \
  "v_mfma_f32_16x16x32_bf16 v[12:15], a[248:251], v[24:27], v[12:15]\n\t" \
  "ds_read_b64_tr_b16 a[142:143], v229 offset:12288\n\t" \
  "v_cvt_pk_bf16_f32 v145, v66, v67\n\t" \
  "v_mfma_f32_16x16x32_bf16 v[12:15], a[244:247], v[20:23], v[12:15]\n\t" \
  "ds_read_b64_tr_b16 a[144:145], v228 offset:256\n\t" \
  "v_lshl_add_u32 v247, v233, 4, v247\n\t" \
  "v_mfma_f32_16x16x32_bf16 v[12:15], a[240:243], v[16:19], v[12:15]\n\t" \
  "v_exp_f32 v5, v5\n\t" \
  "v_mfma_f32_16x16x32_bf16 v[32:35], a[192:195], v[48:51], v[180:183]\n\t" \
  "ds_read_b64_tr_b16 a[146:147], v228 offset:4352\n\t" \
  "v_lshlrev_b32 v252, 4, v252\n\t" \
  "v_mfma_f32_16x16x32_bf16 v[32:35], a[196:199], v[52:55], v[32:35]\n\t" \
  "v_exp_f32 v6, v6\n\t" \
  "v_mfma_f32_16x16x32_bf16 v[32:35], a[200:203], v[56:59], v[32:35]\n\t" \
  "ds_read_b64_tr_b16 a[148:149], v228 offset:8448\n\t" \
  "v_cvt_pk_bf16_f32 v146, v68, v69\n\t" \
  "v_mfma_f32_16x16x32_bf16 v[32:35], a[204:207], v[60:63], v[32:35]\n\t" \
  "v_exp_f32 v7, v7\n\t" \
  "v_mfma_f32_16x16x32_bf16 v[36:39], a[220:223], v[60:63], v[180:183]\n\t" \
  "ds_read_b64_tr_b16 a[150:151], v228 offset:12544\n\t" \
  "v_cvt_pk_bf16_f32 v147, v70, v71\n\t" \
  "v_mfma_f32_16x16x32_bf16 v[36:39], a[216:219], v[56:59], v[36:39]\n\t" \
  "ds_read_b64_tr_b16 a[152:153], v229 offset:256\n\t" \
  "v_cvt_pk_bf16_f32 v148, v72, v73\n\t" \
  "v_mfma_f32_16x16x32_bf16 v[36:39], a[212:215], v[52:55], v[36:39]\n\t" \
  "v_exp_f32 v8, v8\n\t" \
  "v_mfma_f32_16x16x32_bf16 v[36:39], a[208:211], v[48:51], v[36:39]\n\t" \
  "ds_read_b64_tr_b16 a[154:155], v229 offset:4352\n\t" \
  "v_cvt_pk_bf16_f32 v149, v74, v75\n\t" \
  "v_mfma_f32_16x16x32_bf16 v[40:43], a[224:227], v[48:51], v[180:183]\n\t" \
  "v_exp_f32 v9, v9\n\t" \
  "v_mfma_f32_16x16x32_bf16 v[40:43], a[228:231], v[52:55], v[40:43]\n\t" \
  "ds_read_b64_tr_b16 a[156:157], v229 offset:8448\n\t" \
  "v_cvt_pk_bf16_f32 v150, v76, v77\n\t" \
  "v_mfma_f32_16x16x32_bf16 v[40:43], a[232:235], v[56:59], v[40:43]\n\t" \
  "v_exp_f32 v10, v10\n\t" \
  "v_mfma_f32_16x16x32_bf16 v[40:43], a[236:239], v[60:63], v[40:43]\n\t" \
  "ds_read_b64_tr_b16 a[158:159], v229 offset:12544\n\t" \
  "v_cvt_pk_bf16_f32 v151, v78, v79\n\t" \
  "v_mfma_f32_16x16x32_bf16 v[44:47], a[252:255], v[60:63], v[180:183]\n\t" \
  "ds_read_b64_tr_b16 a[160:161], v228 offset:512\n\t" \
  "v_cvt_pk_bf16_f32 v152, v96, v97\n\t" \
  "v_mfma_f32_16x16x32_bf16 v[44:47], a[248:251], v[56:59], v[44:47]\n\t" \
  "v_exp_f32 v11, v11\n\t" \
  "v_mfma_f32_16x16x32_bf16 v[44:47], a[244:247], v[52:55], v[44:47]\n\t" \
  "ds_read_b64_tr_b16 a[162:163], v228 offset:4608\n\t" \
  "v_cvt_pk_bf16_f32 v153, v98, v99\n\t" \
  "v_mfma_f32_16x16x32_bf16 v[44:47], a[240:243], v[48:51], v[44:47]\n\t" \
  "v_exp_f32 v12, v12\n\t" \
  "v_mfma_f32_16x16x32_bf16 v[64:67], a[192:195], v[80:83], v[184:187]\n\t" \
  "ds_read_b64_tr_b16 a[164:165], v228 offset:8704\n\t" \
  "v_cvt_pk_bf16_f32 v154, v100, v101\n\t" \
  "v_mfma_f32_16x16x32_bf16 v[64:67], a[196:199], v[84:87], v[64:67]\n\t" \
  "v_exp_f32 v13, v13\n\t" \
  "v_mfma_f32_16x16x32_bf16 v[64:67], a[200:203], v[88:91], v[64:67]\n\t" \
  "ds_read_b64_tr_b16 a[166:167], v228 offset:12800\n\t" \
  "v_cvt_pk_bf16_f32 v155, v102, v103\n\t" \
  "v_mfma_f32_16x16x32_bf16 v[64:67], a[204:207], v[92:95], v[64:67]\n\t" \
  "ds_read_b64_tr_b16 a[168:169], v229 offset:512\n\t" \
  "v_cvt_pk_bf16_f32 v156, v104, v105\n\t" \
  "v_mfma_f32_16x16x32_bf16 v[68:71], a[220:223], v[92:95], v[184:187]\n\t" \
  "v_exp_f32 v14, v14\n\t" \
  "v_mfma_f32_16x16x32_bf16 v[68:71], a[216:219], v[88:91], v[68:71]\n\t" \
  "ds_read_b64_tr_b16 a[170:171], v229 offset:4608\n\t" \
  "v_cvt_pk_bf16_f32 v157, v106, v107\n\t" \
  "v_mfma_f32_16x16x32_bf16 v[68:71], a[212:215], v[84:87], v[68:71]\n\t" \
  "v_exp_f32 v15, v15\n\t" \
  "v_mfma_f32_16x16x32_bf16 v[68:71], a[208:211], v[80:83], v[68:71]\n\t" \
  "ds_read_b64_tr_b16 a[172:173], v229 offset:8704\n\t" \
  "v_cvt_pk_bf16_f32 v158, v108, v109\n\t" \
  "v_mfma_f32_16x16x32_bf16 v[72:75], a[224:227], v[80:83], v[184:187]\n\t" \
  "v_exp_f32 v32, v32\n\t" \
  "v_mfma_f32_16x16x32_bf16 v[72:75], a[228:231], v[84:87], v[72:75]\n\t" \
  "ds_read_b64_tr_b16 a[174:175], v229 offset:12800\n\t" \
  "v_cvt_pk_bf16_f32 v159, v110, v111\n\t" \
  "v_mfma_f32_16x16x32_bf16 v[72:75], a[232:235], v[88:91], v[72:75]\n\t" \
  "v_exp_f32 v33, v33\n\t" \
  "v_mfma_f32_16x16x32_bf16 v[72:75], a[236:239], v[92:95], v[72:75]\n\t" \
  "v_exp_f32 v34, v34\n\t" \
  "v_mfma_f32_16x16x32_bf16 v[76:79], a[252:255], v[92:95], v[184:187]\n\t" \
  "ds_read_b64_tr_b16 a[176:177], v228 offset:768\n\t" \
  "ds_read_b64_tr_b16 a[178:179], v228 offset:4864\n\t" \
  "v_mfma_f32_16x16x32_bf16 v[76:79], a[248:251], v[88:91], v[76:79]\n\t" \
  "v_exp_f32 v35, v35\n\t" \
  "v_mfma_f32_16x16x32_bf16 v[76:79], a[244:247], v[84:87], v[76:79]\n\t" \
  "v_exp_f32 v36, v36\n\t" \
  "v_mfma_f32_16x16x32_bf16 v[76:79], a[240:243], v[80:83], v[76:79]\n\t" \
  "v_exp_f32 v37, v37\n\t" \
  "v_mfma_f32_16x16x32_bf16 v[96:99], a[192:195], v[112:115], v[188:191]\n\t" \
  "ds_read_b64_tr_b16 a[180:181], v228 offset:8960\n\t" \
  "ds_read_b64_tr_b16 a[182:183], v228 offset:13056\n\t" \
  "v_mfma_f32_16x16x32_bf16 v[96:99], a[196:199], v[116:119], v[96:99]\n\t" \
  "v_exp_f32 v38, v38\n\t" \
  "v_mfma_f32_16x16x32_bf16 v[96:99], a[200:203], v[120:123], v[96:99]\n\t" \
  "v_exp_f32 v39, v39\n\t" \
  "v_mfma_f32_16x16x32_bf16 v[96:99], a[204:207], v[124:127], v[96:99]\n\t" \
  "ds_read_b64_tr_b16 a[184:185], v229 offset:768\n\t" \
  "ds_read_b64_tr_b16 a[186:187], v229 offset:4864\n\t" \
  "v_mfma_f32_16x16x32_bf16 v[100:103], a[220:223], v[124:127], v[188:191]\n\t" \
  "v_exp_f32 v40, v40\n\t" \
  "v_mfma_f32_16x16x32_bf16 v[100:103], a[216:219], v[120:123], v[100:103]\n\t" \
  "v_exp_f32 v41, v41\n\t" \
  "v_mfma_f32_16x16x32_bf16 v[100:103], a[212:215], v[116:119], v[100:103]\n\t" \
  "v_exp_f32 v42, v42\n\t" \
  "v_mfma_f32_16x16x32_bf16 v[100:103], a[208:211], v[112:115], v[100:103]\n\t" \
  "ds_read_b64_tr_b16 a[188:189], v229 offset:8960\n\t" \
  "ds_read_b64_tr_b16 a[190:191], v229 offset:13056\n\t" \
  "v_mfma_f32_16x16x32_bf16 v[104:107], a[224:227], v[112:115], v[188:191]\n\t" \
  "v_exp_f32 v43, v43\n\t" \
  "v_mfma_f32_16x16x32_bf16 v[104:107], a[228:231], v[116:119], v[104:107]\n\t" \
  "v_exp_f32 v44, v44\n\t" \
  "v_mfma_f32_16x16x32_bf16 v[104:107], a[232:235], v[120:123], v[104:107]\n\t" \
  "v_exp_f32 v45, v45\n\t" \
  "v_mfma_f32_16x16x32_bf16 v[104:107], a[236:239], v[124:127], v[104:107]\n\t" \
  "v_exp_f32 v46, v46\n\t" \
  "v_mfma_f32_16x16x32_bf16 v[108:111], a[252:255], v[124:127], v[188:191]\n\t" \
  "v_exp_f32 v47, v47\n\t" \
  "v_mfma_f32_16x16x32_bf16 v[108:111], a[248:251], v[120:123], v[108:111]\n\t" \
  "v_exp_f32 v64, v64\n\t" \
  "v_mfma_f32_16x16x32_bf16 v[108:111], a[244:247], v[116:119], v[108:111]\n\t" \
  "v_exp_f32 v65, v65\n\t" \
  "v_mfma_f32_16x16x32_bf16 v[108:111], a[240:243], v[112:115], v[108:111]\n\t" \
  "s_waitcnt vmcnt(3) lgkmcnt(0)\n\t" \
  "s_barrier\n\t" \
  STA("s_memrealtime s[94:95]\n\t") \
  STA("s_memtime s[92:93]\n\t") \
  "v_mfma_f32_16x16x32_bf16 a[0:3], a[128:131], v[128:131], a[0:3]\n\t" \
  "v_exp_f32 v66, v66\n\t" \
  "v_mfma_f32_16x16x32_bf16 a[0:3], a[132:135], v[132:135], a[0:3]\n\t" \
  "v_exp_f32 v67, v67\n\t" \
  "v_mfma_f32_16x16x32_bf16 a[16:19], a[136:139], v[128:131], a[16:19]\n\t" \
  "ds_read_b128 a[192:195], v220 offset:0\n\t" \
  "ds_read_b128 a[196:199], v221 offset:0\n\t" \
  "v_mfma_f32_16x16x32_bf16 a[16:19], a[140:143], v[132:135], a[16:19]\n\t" \
  "v_exp_f32 v68, v68\n\t" \
  "v_mfma_f32_16x16x32_bf16 a[32:35], a[144:147], v[128:131], a[32:35]\n\t" \
  "v_exp_f32 v69, v69\n\t" \
  "v_mfma_f32_16x16x32_bf16 a[32:35], a[148:151], v[132:135], a[32:35]\n\t" \
  "v_exp_f32 v70, v70\n\t" \
  "v_mfma_f32_16x16x32_bf16 a[48:51], a[152:155], v[128:131], a[48:51]\n\t" \
  "v_exp_f32 v71, v71\n\t" \
  "v_mfma_f32_16x16x32_bf16 a[48:51], a[156:159], v[132:135], a[48:51]\n\t" \
  "v_exp_f32 v72, v72\n\t" \
  "v_mfma_f32_16x16x32_bf16 a[64:67], a[160:163], v[128:131], a[64:67]\n\t" \
  "v_exp_f32 v73, v73\n\t" \
  "v_mfma_f32_16x16x32_bf16 a[64:67], a[164:167], v[132:135], a[64:67]\n\t" \
  "ds_read_b128 a[200:203], v222 offset:0\n\t" \
  "ds_read_b128 a[204:207], v223 offset:0\n\t" \
  "v_mfma_f32_16x16x32_bf16 a[80:83], a[168:171], v[128:131], a[80:83]\n\t" \
  "v_exp_f32 v74, v74\n\t" \
  "v_mfma_f32_16x16x32_bf16 a[80:83], a[172:175], v[132:135], a[80:83]\n\t" \
  "v_exp_f32 v75, v75\n\t" \
  "v_mfma_f32_16x16x32_bf16 a[96:99], a[176:179], v[128:131], a[96:99]\n\t" \
  STA("v_writelane_b32 %[stv], s94, 44\n\t") \
  STA("v_writelane_b32 %[stv], s92, 45\n\t") \
  "v_exp_f32 v76, v76\n\t" \
  "v_mfma_f32_16x16x32_bf16 a[96:99], a[180:183], v[132:135], a[96:99]\n\t" \
  "v_exp_f32 v77, v77\n\t" \
  "v_mfma_f32_16x16x32_bf16 a[112:115], a[184:187], v[128:131], a[112:115]\n\t" \
  "v_exp_f32 v78, v78\n\t" \
  "v_mfma_f32_16x16x32_bf16 a[112:115], a[188:191], v[132:135], a[112:115]\n\t" \
  "v_exp_f32 v79, v79\n\t" \
  "v_smfmac_f32_16x16x64_bf16 v[160:163], v[216:219], v[128:135], v239\n\t" \
  "ds_read_b128 a[208:211], v220 offset:4096\n\t" \
  "v_cvt_pk_bf16_f32 v128, v0, v1\n\t" \
  "v_mfma_f32_16x16x32_bf16 a[4:7], a[128:131], v[136:139], a[4:7]\n\t" \
  "v_exp_f32 v96, v96\n\t" \
  "v_mfma_f32_16x16x32_bf16 a[4:7], a[132:135], v[140:143], a[4:7]\n\t" \
  "ds_read_b128 a[212:215], v221 offset:4096\n\t" \
  "v_cvt_pk_bf16_f32 v129, v2, v3\n\t" \
  "v_mfma_f32_16x16x32_bf16 a[20:23], a[136:139], v[136:139], a[20:23]\n\t" \
  "v_exp_f32 v97, v97\n\t" \
  "v_mfma_f32_16x16x32_bf16 a[20:23], a[140:143], v[140:143], a[20:23]\n\t" \
  "s_add_u32 m0, %[m], 65536\n\t" \
  "v_exp_f32 v98, v98\n\t" \
  "v_mfma_f32_16x16x32_bf16 a[36:39], a[144:147], v[136:139], a[36:39]\n\t" \
  "s_add_u32 s96, %[sofk], 114688\n\t" \
  "ds_read_b128 a[216:219], v222 offset:4096\n\t" \
  ".Lhs8:\n\ts_cbranch_execz .Lhs8n\n\t" \
  "v_cvt_pk_bf16_f32 v130, v4, v5\n\t" \
  "v_mfma_f32_16x16x32_bf16 a[36:39], a[148:151], v[140:143], a[36:39]\n\t" \
  "v_exp_f32 v99, v99\n\t" \
  "v_mfma_f32_16x16x32_bf16 a[52:55], a[152:155], v[136:139], a[52:55]\n\t" \
  "v_exp_f32 v100, v100\n\t" \
  "v_mfma_f32_16x16x32_bf16 a[52:55], a[156:159], v[140:143], a[52:55]\n\t" \
  "v_exp_f32 v101, v101\n\t" \
  "v_mfma_f32_16x16x32_bf16 a[68:71], a[160:163], v[136:139], a[68:71]\n\t" \
  "ds_read_b128 a[220:223], v223 offset:4096\n\t" \
  "v_cvt_pk_bf16_f32 v131, v6, v7\n\t" \
  "v_mfma_f32_16x16x32_bf16 a[68:71], a[164:167], v[140:143], a[68:71]\n\t" \
  "v_exp_f32 v102, v102\n\t" \
  "v_mfma_f32_16x16x32_bf16 a[84:87], a[168:171], v[136:139], a[84:87]\n\t" \
  "buffer_load_dwordx4 v224, %[sK], s96 offen offset:0 lds\n\t" \
  "v_mfma_f32_16x16x32_bf16 a[84:87], a[172:175], v[140:143], a[84:87]\n\t" \
  "ds_read_b128 a[224:227], v220 offset:8192\n\t" \
  "v_cvt_pk_bf16_f32 v132, v8, v9\n\t" \
  "v_mfma_f32_16x16x32_bf16 a[100:103], a[176:179], v[136:139], a[100:103]\n\t" \
  "v_exp_f32 v103, v103\n\t" \
  "v_mfma_f32_16x16x32_bf16 a[100:103], a[180:183], v[140:143], a[100:103]\n\t" \
  "buffer_load_dwordx4 v225, %[sK], s96 offen offset:1024 lds\n\t" \
  "v_mfma_f32_16x16x32_bf16 a[116:119], a[184:187], v[136:139], a[116:119]\n\t" \
  "v_exp_f32 v104, v104\n\t" \
  "v_mfma_f32_16x16x32_bf16 a[116:119], a[188:191], v[140:143], a[116:119]\n\t" \
  "ds_read_b128 a[228:231], v221 offset:8192\n\t" \
  "v_cvt_pk_bf16_f32 v133, v10, v11\n\t" \
  "v_smfmac_f32_16x16x64_bf16 v[164:167], v[216:219], v[136:143], v239\n\t" \
  "buffer_load_dwordx4 v226, %[sK], s96 offen offset:2048 lds\n\t" \
  "v_mfma_f32_16x16x32_bf16 a[8:11], a[128:131], v[144:147], a[8:11]\n\t" \
  "v_exp_f32 v105, v105\n\t" \
  "v_mfma_f32_16x16x32_bf16 a[8:11], a[132:135], v[148:151], a[8:11]\n\t" \
  "ds_read_b128 a[232:235], v222 offset:8192\n\t" \
  "v_cvt_pk_bf16_f32 v134, v12, v13\n\t" \
  "v_mfma_f32_16x16x32_bf16 a[24:27], a[136:139], v[144:147], a[24:27]\n\t" \
  "v_exp_f32 v106, v106\n\t" \
  "v_mfma_f32_16x16x32_bf16 a[24:27], a[140:143], v[148:151], a[24:27]\n\t" \
  "buffer_load_dwordx4 v227, %[sK], s96 offen offset:3072 lds\n\t" \
  "v_mfma_f32_16x16x32_bf16 a[40:43], a[144:147], v[144:147], a[40:43]\n\t" \
  "v_exp_f32 v107, v107\n\t" \
  "v_mfma_f32_16x16x32_bf16 a[40:43], a[148:151], v[148:151], a[40:43]\n\t" \
  "ds_read_b128 a[236:239], v223 offset:8192\n\t" \
  "v_cvt_pk_bf16_f32 v135, v14, v15\n\t" \
  "v_mfma_f32_16x16x32_bf16 a[56:59], a[152:155], v[144:147], a[56:59]\n\t" \
  "s_add_u32 m0, %[m], 98304\n\t" \
  "s_mov_b32 s96, 28672\n\t" \
  "ds_read_b128 a[240:243], v220 offset:12288\n\t" \
  "v_mfma_f32_16x16x32_bf16 a[56:59], a[156:159], v[148:151], a[56:59]\n\t" \
  "v_exp_f32 v108, v108\n\t" \
  "v_mfma_f32_16x16x32_bf16 a[72:75], a[160:163], v[144:147], a[72:75]\n\t" \
  "buffer_load_dwordx4 v232, %[sG], s96 offen offset:0 lds\n\t" \
  "v_mfma_f32_16x16x32_bf16 a[72:75], a[164:167], v[148:151], a[72:75]\n\t" \
  "s_add_u32 m0, %[m], 81920\n\t" \
  "v_exp_f32 v109, v109\n\t" \
  "v_mfma_f32_16x16x32_bf16 a[88:91], a[168:171], v[144:147], a[88:91]\n\t" \
  "s_add_u32 s96, %[sofk], 98304\n\t" \
  "v_exp_f32 v110, v110\n\t" \
  "v_mfma_f32_16x16x32_bf16 a[88:91], a[172:175], v[148:151], a[88:91]\n\t" \
  "v_exp_f32 v111, v111\n\t" \
  "v_mfma_f32_16x16x32_bf16 a[104:107], a[176:179], v[144:147], a[104:107]\n\t" \
  "ds_read_b128 a[244:247], v221 offset:12288\n\t" \
  "v_cvt_pk_bf16_f32 v136, v32, v33\n\t" \
  "v_mfma_f32_16x16x32_bf16 a[104:107], a[180:183], v[148:151], a[104:107]\n\t" \
  "ds_read_b128 a[248:251], v222 offset:12288\n\t" \
  "v_cvt_pk_bf16_f32 v137, v34, v35\n\t" \
  "v_mfma_f32_16x16x32_bf16 a[120:123], a[184:187], v[144:147], a[120:123]\n\t" \
  "ds_read_b128 a[252:255], v223 offset:12288\n\t" \
  "v_cvt_pk_bf16_f32 v138, v36, v37\n\t" \
  "v_mfma_f32_16x16x32_bf16 a[120:123], a[188:191], v[148:151], a[120:123]\n\t" \
  "buffer_load_dwordx4 v230, %[sV], s96 offen offset:0 lds\n\t" \
  "v_smfmac_f32_16x16x64_bf16 v[168:171], v[216:219], v[144:151], v239\n\t" \
  "v_cvt_pk_bf16_f32 v139, v38, v39\n\t" \
  "v_cvt_pk_bf16_f32 v140, v40, v41\n\t" \
  "v_mfma_f32_16x16x32_bf16 a[12:15], a[128:131], v[152:155], a[12:15]\n\t" \
  "v_cvt_pk_bf16_f32 v141, v42, v43\n\t" \
  "v_cvt_pk_bf16_f32 v142, v44, v45\n\t" \
  "v_mfma_f32_16x16x32_bf16 a[12:15], a[132:135], v[156:159], a[12:15]\n\t" \
  "buffer_load_dwordx4 v231, %[sV], s96 offen offset:1024 lds\n\t" \
  "v_mfma_f32_16x16x32_bf16 a[28:31], a[136:139], v[152:155], a[28:31]\n\t" \
  "v_cvt_pk_bf16_f32 v143, v46, v47\n\t" \
  "v_cvt_pk_bf16_f32 v144, v64, v65\n\t" \
  "v_mfma_f32_16x16x32_bf16 a[28:31], a[140:143], v[156:159], a[28:31]\n\t" \
  "v_cvt_pk_bf16_f32 v145, v66, v67\n\t" \
  "v_cvt_pk_bf16_f32 v146, v68, v69\n\t" \
  "v_mfma_f32_16x16x32_bf16 a[44:47], a[144:147], v[152:155], a[44:47]\n\t" \
  "buffer_load_dwordx4 v230, %[sV], s96 offen offset:2048 lds\n\t" \
  "v_mfma_f32_16x16x32_bf16 a[44:47], a[148:151], v[156:159], a[44:47]\n\t" \
  "v_cvt_pk_bf16_f32 v147, v70, v71\n\t" \
  "v_cvt_pk_bf16_f32 v148, v72, v73\n\t" \
  "v_mfma_f32_16x16x32_bf16 a[60:63], a[152:155], v[152:155], a[60:63]\n\t" \
  "v_cvt_pk_bf16_f32 v149, v74, v75\n\t" \
  "v_cvt_pk_bf16_f32 v150, v76, v77\n\t" \
  "v_mfma_f32_16x16x32_bf16 a[60:63], a[156:159], v[156:159], a[60:63]\n\t" \
  "v_cvt_pk_bf16_f32 v151, v78, v79\n\t" \
  "v_mfma_f32_16x16x32_bf16 a[76:79], a[160:163], v[152:155], a[76:79]\n\t" \
  "buffer_load_dwordx4 v231, %[sV], s96 offen offset:3072 lds\n\t" \
  "v_mfma_f32_16x16x32_bf16 a[76:79], a[164:167], v[156:159], a[76:79]\n\t" \
  "v_lshrrev_b32 v205, 1, v233\n\t" \
  "v_lshlrev_b32 v205, 2, v205\n\t" \
  "v_mfma_f32_16x16x32_bf16 a[92:95], a[168:171], v[152:155], a[92:95]\n\t" \
  "v_add_u32 v205, 8192, v205\n\t" \
  "v_mfma_f32_16x16x32_bf16 a[92:95], a[172:175], v[156:159], a[92:95]\n\t" \
  "buffer_load_dword v209, v205, %[ws], 0 offen sc1\n\t" \
  "v_mfma_f32_16x16x32_bf16 a[108:111], a[176:179], v[152:155], a[108:111]\n\t" \
  "s_add_u32 m0, %[m], 98304\n\t" \
  "s_mov_b32 s96, 34816\n\t" \
  "v_mfma_f32_16x16x32_bf16 a[108:111], a[180:183], v[156:159], a[108:111]\n\t" \
  "v_mfma_f32_16x16x32_bf16 a[124:127], a[184:187], v[152:155], a[124:127]\n\t" \
  "buffer_load_dwordx4 v232, %[sG], s96 offen offset:1024 lds\n\t" \
  "v_mfma_f32_16x16x32_bf16 a[124:127], a[188:191], v[156:159], a[124:127]\n\t" \
  "s_add_u32 s98, %[pf2], 49152\n\t" \
  "v_smfmac_f32_16x16x64_bf16 v[172:175], v[216:219], v[152:159], v239\n\t" \
  "s_waitcnt lgkmcnt(0)\n\t" \
  "buffer_load_dword %[pfd], v202, %[sM], s98 offen\n\t" \
  STA("s_memrealtime s[94:95]\n\t") \
  STA("s_memtime s[92:93]\n\t") \
  "v_mfma_f32_16x16x32_bf16 v[0:3], a[192:195], v[16:19], v[176:179]\n\t" \
  "ds_read_b64_tr_b16 a[128:129], v237 offset:0\n\t" \
  "v_cvt_pk_bf16_f32 v152, v96, v97\n\t" \
  "v_mfma_f32_16x16x32_bf16 v[0:3], a[196:199], v[20:23], v[0:3]\n\t" \
  "ds_read_b64_tr_b16 a[130:131], v237 offset:4096\n\t" \
  "v_cvt_pk_bf16_f32 v153, v98, v99\n\t" \
  "v_mfma_f32_16x16x32_bf16 v[0:3], a[200:203], v[24:27], v[0:3]\n\t" \
  "ds_read_b64_tr_b16 a[132:133], v237 offset:8192\n\t" \
  "v_cvt_pk_bf16_f32 v154, v100, v101\n\t" \
  "v_mfma_f32_16x16x32_bf16 v[0:3], a[204:207], v[28:31], v[0:3]\n\t" \
  "ds_read_b64_tr_b16 a[134:135], v237 offset:12288\n\t" \
  "v_cvt_pk_bf16_f32 v155, v102, v103\n\t" \
  "v_mfma_f32_16x16x32_bf16 v[4:7], a[220:223], v[28:31], v[176:179]\n\t" \
  "ds_read_b64_tr_b16 a[136:137], v238 offset:0\n\t" \
  "v_cvt_pk_bf16_f32 v156, v104, v105\n\t" \
  "v_mfma_f32_16x16x32_bf16 v[4:7], a[216:219], v[24:27], v[4:7]\n\t" \
  "ds_read_b64_tr_b16 a[138:139], v238 offset:4096\n\t" \
  "v_cvt_pk_bf16_f32 v157, v106, v107\n\t" \
  "v_mfma_f32_16x16x32_bf16 v[4:7], a[212:215], v[20:23], v[4:7]\n\t" \
  "v_exp_f32 v0, v0\n\t" \
  "v_mfma_f32_16x16x32_bf16 v[4:7], a[208:211], v[16:19], v[4:7]\n\t" \
  "v_exp_f32 v1, v1\n\t" \
  "v_mfma_f32_16x16x32_bf16 v[8:11], a[224:227], v[16:19], v[176:179]\n\t" \
  "v_exp_f32 v2, v2\n\t" \
  "v_mfma_f32_16x16x32_bf16 v[8:11], a[228:231], v[20:23], v[8:11]\n\t" \
  "v_exp_f32 v3, v3\n\t" \
  "v_mfma_f32_16x16x32_bf16 v[8:11], a[232:235], v[24:27], v[8:11]\n\t" \
  "ds_read_b64_tr_b16 a[140:141], v238 offset:8192\n\t" \
  "v_cvt_pk_bf16_f32 v158, v108, v109\n\t" \
  "v_mfma_f32_16x16x32_bf16 v[8:11], a[236:239], v[28:31], v[8:11]\n\t" \
  "s_add_u32 s98, %[pf2], 73728\n\t" \
  "v_exp_f32 v4, v4\n\t" \
  "v_mfma_f32_16x16x32_bf16 v[12:15], a[252:255], v[28:31], v[176:179]\n\t" \
  STA("v_writelane_b32 %[stv], s94, 46\n\t") \
  STA("v_writelane_b32 %[stv], s92, 47\n\t") \
  "buffer_load_dword %[pfd], v202, %[sM], s98 offen\n\t" \
  "v_mfma_f32_16x16x32_bf16 v[12:15], a[248:251], v[24:27], v[12:15]\n\t" \
  "ds_read_b64_tr_b16 a[142:143], v238 offset:12288\n\t" \
  "v_cvt_pk_bf16_f32 v159, v110, v111\n\t" \
  "v_lshl_add_u32 v207, v233, 14, %[cto]\n\t" \
  "v_min_u32 v207, .Lfz_z-.Lat_t1-4, v207\n\t" \
  "v_mfma_f32_16x16x32_bf16 v[12:15], a[244:247], v[20:23], v[12:15]\n\t" \
  "ds_read_b64_tr_b16 a[144:145], v237 offset:256\n\t" \
  "ds_read_b64_tr_b16 a[146:147], v237 offset:4352\n\t" \
  "v_mfma_f32_16x16x32_bf16 v[12:15], a[240:243], v[16:19], v[12:15]\n\t" \
  "s_getpc_b64 s[96:97]\n\t" \
  ".Lat_t1:\n\t" \
  "global_load_dword %[pfd], v207, s[96:97]\n\t" \
  "v_mfma_f32_16x16x32_bf16 v[32:35], a[192:195], v[48:51], v[180:183]\n\t" \
  "v_exp_f32 v5, v5\n\t" \
  "v_mfma_f32_16x16x32_bf16 v[32:35], a[196:199], v[52:55], v[32:35]\n\t" \
  "v_exp_f32 v6, v6\n\t" \
  "v_mfma_f32_16x16x32_bf16 v[32:35], a[200:203], v[56:59], v[32:35]\n\t" \
  "v_exp_f32 v7, v7\n\t" \
  "v_mfma_f32_16x16x32_bf16 v[32:35], a[204:207], v[60:63], v[32:35]\n\t" \
  "v_exp_f32 v8, v8\n\t" \
  "v_mfma_f32_16x16x32_bf16 v[36:39], a[220:223], v[60:63], v[180:183]\n\t" \
  "ds_read_b64_tr_b16 a[148:149], v237 offset:8448\n\t" \
  "ds_read_b64_tr_b16 a[150:151], v237 offset:12544\n\t" \
  "v_mfma_f32_16x16x32_bf16 v[36:39], a[216:219], v[56:59], v[36:39]\n\t" \
  "v_exp_f32 v9, v9\n\t" \
  "v_mfma_f32_16x16x32_bf16 v[36:39], a[212:215], v[52:55], v[36:39]\n\t" \
  "v_exp_f32 v10, v10\n\t" \
  "v_mfma_f32_16x16x32_bf16 v[36:39], a[208:211], v[48:51], v[36:39]\n\t" \
  "ds_read_b64_tr_b16 a[152:153], v238 offset:256\n\t" \
  "ds_read_b64_tr_b16 a[154:155], v238 offset:4352\n\t" \
  "v_mfma_f32_16x16x32_bf16 v[40:43], a[224:227], v[48:51], v[180:183]\n\t" \
  "v_exp_f32 v11, v11\n\t" \
  "v_mfma_f32_16x16x32_bf16 v[40:43], a[228:231], v[52:55], v[40:43]\n\t" \
  "v_exp_f32 v12, v12\n\t" \
  "v_mfma_f32_16x16x32_bf16 v[40:43], a[232:235], v[56:59], v[40:43]\n\t" \
  "v_exp_f32 v13, v13\n\t" \
  "v_mfma_f32_16x16x32_bf16 v[40:43], a[236:239], v[60:63], v[40:43]\n\t" \
  "ds_read_b64_tr_b16 a[156:157], v238 offset:8448\n\t" \
  "ds_read_b64_tr_b16 a[158:159], v238 offset:12544\n\t" \
  "v_mfma_f32_16x16x32_bf16 v[44:47], a[252:255], v[60:63], v[180:183]\n\t" \
  "v_exp_f32 v14, v14\n\t" \
  "v_mfma_f32_16x16x32_bf16 v[44:47], a[248:251], v[56:59], v[44:47]\n\t" \
  "v_exp_f32 v15, v15\n\t" \
  "v_mfma_f32_16x16x32_bf16 v[44:47], a[244:247], v[52:55], v[44:47]\n\t" \
  "ds_read_b64_tr_b16 a[160:161], v237 offset:512\n\t" \
  "ds_read_b64_tr_b16 a[162:163], v237 offset:4608\n\t" \
  "v_mfma_f32_16x16x32_bf16 v[44:47], a[240:243], v[48:51], v[44:47]\n\t" \
  "v_exp_f32 v32, v32\n\t" \
  "v_mfma_f32_16x16x32_bf16 v[64:67], a[192:195], v[80:83], v[184:187]\n\t" \
  "v_exp_f32 v33, v33\n\t" \
  "v_mfma_f32_16x16x32_bf16 v[64:67], a[196:199], v[84:87], v[64:67]\n\t" \
  "v_exp_f32 v34, v34\n\t" \
  "v_mfma_f32_16x16x32_bf16 v[64:67], a[200:203], v[88:91], v[64:67]\n\t" \
  "ds_read_b64_tr_b16 a[164:165], v237 offset:8704\n\t" \
  "ds_read_b64_tr_b16 a[166:167], v237 offset:12800\n\t" \
  "v_mfma_f32_16x16x32_bf16 v[64:67], a[204:207], v[92:95], v[64:67]\n\t" \
  "v_exp_f32 v35, v35\n\t" \
  "v_mfma_f32_16x16x32_bf16 v[68:71], a[220:223], v[92:95], v[184:187]\n\t" \
  "v_exp_f32 v36, v36\n\t" \
  "v_mfma_f32_16x16x32_bf16 v[68:71], a[216:219], v[88:91], v[68:71]\n\t" \
  "ds_read_b64_tr_b16 a[168:169], v238 offset:512\n\t" \
  "ds_read_b64_tr_b16 a[170:171], v238 offset:4608\n\t" \
  "v_mfma_f32_16x16x32_bf16 v[68:71], a[212:215], v[84:87], v[68:71]\n\t" \
  "v_exp_f32 v37, v37\n\t" \
  "v_mfma_f32_16x16x32_bf16 v[68:71], a[208:211], v[80:83], v[68:71]\n\t" \
  "v_exp_f32 v38, v38\n\t" \
  "v_mfma_f32_16x16x32_bf16 v[72:75], a[224:227], v[80:83], v[184:187]\n\t" \
  "v_exp_f32 v39, v39\n\t" \
  "v_mfma_f32_16x16x32_bf16 v[72:75], a[228:231], v[84:87], v[72:75]\n\t" \
  "ds_read_b64_tr_b16 a[172:173], v238 offset:8704\n\t" \
  "ds_read_b64_tr_b16 a[174:175], v238 offset:12800\n\t" \
  "v_mfma_f32_16x16x32_bf16 v[72:75], a[232:235], v[88:91], v[72:75]\n\t" \
  "ds_read_b64_tr_b16 a[176:177], v237 offset:768\n\t" \
  "ds_read_b64_tr_b16 a[178:179], v237 offset:4864\n\t" \
  "v_mfma_f32_16x16x32_bf16 v[72:75], a[236:239], v[92:95], v[72:75]\n\t" \
  "s_lshr_b32 s98, %[hl18], 18\n\t" \
  "v_xor_b32 v245, s98, v233\n\t" \
  "v_and_b32 v245, 31, v245\n\t" \
  "v_mfma_f32_16x16x32_bf16 v[76:79], a[252:255], v[92:95], v[184:187]\n\t" \
  "s_waitcnt vmcnt(4)\n\t" \
  "v_and_b32 v206, 0xbfffbfff, v209\n\t" \
  "v_cmp_ne_u32_e64 s[96:97], %[tag], v206\n\t" \
  "s_cmp_lg_u64 s[96:97], 0\n\t" \
  "s_cbranch_scc1 .Lat_v\n\t" \
  ".Lat_w:\n\t" \
  "v_mfma_f32_16x16x32_bf16 v[76:79], a[248:251], v[88:91], v[76:79]\n\t" \
  "v_exp_f32 v40, v40\n\t" \
  "v_mfma_f32_16x16x32_bf16 v[76:79], a[244:247], v[84:87], v[76:79]\n\t" \
  "ds_read_b64_tr_b16 a[180:181], v237 offset:8960\n\t" \
  "v_and_b32 v206, 1, v233\n\t" \
  "v_mfma_f32_16x16x32_bf16 v[76:79], a[240:243], v[80:83], v[76:79]\n\t" \
  "v_exp_f32 v41, v41\n\t" \
  "v_mfma_f32_16x16x32_bf16 v[96:99], a[192:195], v[112:115], v[188:191]\n\t" \
  "ds_read_b64_tr_b16 a[182:183], v237 offset:13056\n\t" \
  "v_lshlrev_b32 v206, 4, v206\n\t" \
  "v_mfma_f32_16x16x32_bf16 v[96:99], a[196:199], v[116:119], v[96:99]\n\t" \
  "ds_read_b64_tr_b16 a[184:185], v238 offset:768\n\t" \
  "v_lshrrev_b32 v206, v206, v209\n\t" \
  "v_mfma_f32_16x16x32_bf16 v[96:99], a[200:203], v[120:123], v[96:99]\n\t" \
  "v_exp_f32 v42, v42\n\t" \
  "v_mfma_f32_16x16x32_bf16 v[96:99], a[204:207], v[124:127], v[96:99]\n\t" \
  "ds_read_b64_tr_b16 a[186:187], v238 offset:4864\n\t" \
  "v_and_b32 v206, 0x4000, v206\n\t" \
  "v_mfma_f32_16x16x32_bf16 v[100:103], a[220:223], v[124:127], v[188:191]\n\t" \
  "v_exp_f32 v43, v43\n\t" \
  "v_mfma_f32_16x16x32_bf16 v[100:103], a[216:219], v[120:123], v[100:103]\n\t" \
  "ds_read_b64_tr_b16 a[188:189], v238 offset:8960\n\t" \
  "v_cmp_ne_u32_e64 s[96:97], 0, v206\n\t" \
  STS("s_nop 4\n\t") \
  STS("v_writelane_b32 %[stv], s96, 60\n\t") \
  STS("v_writelane_b32 %[stv], s97, 61\n\t") \
  "v_mfma_f32_16x16x32_bf16 v[100:103], a[212:215], v[116:119], v[100:103]\n\t" \
  "s_bcnt1_i32_b32 %[nb], s96\n\t" \
  "s_bcnt1_i32_b32 %[na], s97\n\t" \
  "s_lshr_b32 s96, s96, 16\n\t" \
  "s_lshr_b32 s98, %[hl18], 18\n\t" \
  "s_mul_i32 s98, s98, 127\n\t" \
  "v_add_lshl_u32 v245, v245, s98, 2\n\t" \
  "v_mfma_f32_16x16x32_bf16 v[100:103], a[208:211], v[112:115], v[100:103]\n\t" \
  "s_lshr_b32 s97, s97, 16\n\t" \
  "ds_read_b64_tr_b16 a[190:191], v238 offset:13056\n\t" \
  "s_bcnt1_i32_b32 s96, s96\n\t" \
  "v_mfma_f32_16x16x32_bf16 v[104:107], a[224:227], v[112:115], v[188:191]\n\t" \
  "s_bcnt1_i32_b32 s97, s97\n\t" \
  "s_sub_u32 s96, s97, s96\n\t" \
  "s_sub_u32 %[nb], %[na], %[nb]\n\t" \
  "s_ashr_i32 %[nb], %[nb], 31\n\t" \
  "v_mfma_f32_16x16x32_bf16 v[104:107], a[228:231], v[116:119], v[104:107]\n\t" \
  "s_xor_b32 s96, s96, %[nb]\n\t" \
  "v_exp_f32 v44, v44\n\t" \
  "v_mfma_f32_16x16x32_bf16 v[104:107], a[232:235], v[120:123], v[104:107]\n\t" \
  "s_sub_u32 s96, s96, %[nb]\n\t" \
  "v_exp_f32 v45, v45\n\t" \
  "v_mfma_f32_16x16x32_bf16 v[104:107], a[236:239], v[124:127], v[104:107]\n\t" \
  "s_sub_u32 %[na], 6, s96\n\t" \
  "v_exp_f32 v46, v46\n\t" \
  "v_mfma_f32_16x16x32_bf16 v[108:111], a[252:255], v[124:127], v[188:191]\n\t" \
  "s_ashr_i32 %[na], %[na], 31\n\t" \
  "v_exp_f32 v47, v47\n\t" \
  "v_mfma_f32_16x16x32_bf16 v[108:111], a[248:251], v[120:123], v[108:111]\n\t" \
  "s_and_b32 %[nb], %[nb], 0x2000\n\t" \
  "v_exp_f32 v64, v64\n\t" \
  "v_mfma_f32_16x16x32_bf16 v[108:111], a[244:247], v[116:119], v[108:111]\n\t" \
  "s_xor_b32 s96, %[po], %[nb]\n\t" \
  "v_exp_f32 v65, v65\n\t" \
  "v_mfma_f32_16x16x32_bf16 v[108:111], a[240:243], v[112:115], v[108:111]\n\t" \
  "s_waitcnt lgkmcnt(0)\n\t" \
  "s_barrier\n\t" \
  STA("s_memrealtime s[94:95]\n\t") \
  STA("s_memtime s[92:93]\n\t") \
  "v_mfma_f32_16x16x32_bf16 a[0:3], a[128:131], v[128:131], a[0:3]\n\t" \
  "s_and_b32 s96, s96, %[na]\n\t" \
  "v_exp_f32 v66, v66\n\t" \
  "v_mfma_f32_16x16x32_bf16 a[0:3], a[132:135], v[132:135], a[0:3]\n\t" \
  "s_xor_b32 %[eso], %[eso], s96\n\t" \
  STS("v_writelane_b32 %[stv], %[na], 57\n\t") \
  STS("v_writelane_b32 %[stv], %[nb], 59\n\t") \
  "v_exp_f32 v67, v67\n\t" \
  "v_mfma_f32_16x16x32_bf16 a[16:19], a[136:139], v[128:131], a[16:19]\n\t" \
  "s_xor_b32 s96, %[px], %[nb]\n\t" \
  "ds_read_b128 a[192:195], v220 offset:16384\n\t" \
  "s_and_b32 s96, s96, %[na]\n\t" \
  "v_mfma_f32_16x16x32_bf16 a[16:19], a[140:143], v[132:135], a[16:19]\n\t" \
  "s_xor_b32 %[xcb], %[xcb], s96\n\t" \
  "v_exp_f32 v68, v68\n\t" \
  "v_mfma_f32_16x16x32_bf16 a[32:35], a[144:147], v[128:131], a[32:35]\n\t" \
  "s_xor_b32 %[frb], %[frb], %[nb]\n\t" \
  "v_exp_f32 v69, v69\n\t" \
  "v_mfma_f32_16x16x32_bf16 a[32:35], a[148:151], v[132:135], a[32:35]\n\t" \
  "s_xor_b32 %[fw2], %[fw2], %[nb]\n\t" \
  "v_exp_f32 v70, v70\n\t" \
  "v_mfma_f32_16x16x32_bf16 a[48:51], a[152:155], v[128:131], a[48:51]\n\t" \
  "ds_read_b128 a[196:199], v221 offset:16384\n\t" \
  "ds_read_b128 a[200:203], v222 offset:16384\n\t" \
  "v_mfma_f32_16x16x32_bf16 a[48:51], a[156:159], v[132:135], a[48:51]\n\t" \
  "v_exp_f32 v71, v71\n\t" \
  "v_mfma_f32_16x16x32_bf16 a[64:67], a[160:163], v[128:131], a[64:67]\n\t" \
  "v_exp_f32 v72, v72\n\t" \
  "v_mfma_f32_16x16x32_bf16 a[64:67], a[164:167], v[132:135], a[64:67]\n\t" \
  "v_exp_f32 v73, v73\n\t" \
  "v_mfma_f32_16x16x32_bf16 a[80:83], a[168:171], v[128:131], a[80:83]\n\t" \
  "v_exp_f32 v74, v74\n\t" \
  "v_mfma_f32_16x16x32_bf16 a[80:83], a[172:175], v[132:135], a[80:83]\n\t" \
  "v_exp_f32 v75, v75\n\t" \
  "v_mfma_f32_16x16x32_bf16 a[96:99], a[176:179], v[128:131], a[96:99]\n\t" \
  STA("v_writelane_b32 %[stv], s94, 48\n\t") \
  STA("v_writelane_b32 %[stv], s92, 49\n\t") \
  "v_exp_f32 v76, v76\n\t" \
  "v_mfma_f32_16x16x32_bf16 a[96:99], a[180:183], v[132:135], a[96:99]\n\t" \
  "ds_read_b128 a[204:207], v223 offset:16384\n\t" \
  "ds_read_b128 a[208:211], v220 offset:20480\n\t" \
  "v_mfma_f32_16x16x32_bf16 a[112:115], a[184:187], v[128:131], a[112:115]\n\t" \
  "v_exp_f32 v77, v77\n\t" \
  "v_mfma_f32_16x16x32_bf16 a[112:115], a[188:191], v[132:135], a[112:115]\n\t" \
  "v_exp_f32 v78, v78\n\t" \
  "v_smfmac_f32_16x16x64_bf16 v[160:163], v[216:219], v[128:135], v239\n\t" \
  "v_exp_f32 v79, v79\n\t" \
  "v_mfma_f32_16x16x32_bf16 a[4:7], a[128:131], v[136:139], a[4:7]\n\t" \
  "v_exp_f32 v96, v96\n\t" \
  "v_mfma_f32_16x16x32_bf16 a[4:7], a[132:135], v[140:143], a[4:7]\n\t" \
  "ds_read_b128 a[212:215], v221 offset:20480\n\t" \
  "v_cvt_pk_bf16_f32 v128, v0, v1\n\t" \
  "v_mfma_f32_16x16x32_bf16 a[20:23], a[136:139], v[136:139], a[20:23]\n\t" \
  "v_exp_f32 v97, v97\n\t" \
  "v_mfma_f32_16x16x32_bf16 a[20:23], a[140:143], v[140:143], a[20:23]\n\t" \
  "s_add_u32 m0, %[m], 147456\n\t" \
  "v_exp_f32 v98, v98\n\t" \
  "v_mfma_f32_16x16x32_bf16 a[36:39], a[144:147], v[136:139], a[36:39]\n\t" \
  "s_add_u32 s96, %[sofk], 114688\n\t" \
  "ds_read_b128 a[216:219], v222 offset:20480\n\t" \
  "v_cvt_pk_bf16_f32 v129, v2, v3\n\t" \
  "v_mfma_f32_16x16x32_bf16 a[36:39], a[148:151], v[140:143], a[36:39]\n\t" \
  "v_exp_f32 v99, v99\n\t" \
  "v_mfma_f32_16x16x32_bf16 a[52:55], a[152:155], v[136:139], a[52:55]\n\t" \
  "v_exp_f32 v100, v100\n\t" \
  "v_mfma_f32_16x16x32_bf16 a[52:55], a[156:159], v[140:143], a[52:55]\n\t" \
  "v_exp_f32 v101, v101\n\t" \
  "v_mfma_f32_16x16x32_bf16 a[68:71], a[160:163], v[136:139], a[68:71]\n\t" \
  "ds_read_b128 a[220:223], v223 offset:20480\n\t" \
  "v_cvt_pk_bf16_f32 v130, v4, v5\n\t" \
  "v_mfma_f32_16x16x32_bf16 a[68:71], a[164:167], v[140:143], a[68:71]\n\t" \
  "v_exp_f32 v102, v102\n\t" \
  "v_mfma_f32_16x16x32_bf16 a[84:87], a[168:171], v[136:139], a[84:87]\n\t" \
  "buffer_load_dwordx4 v230, %[sV], s96 offen offset:0 lds\n\t" \
  "v_mfma_f32_16x16x32_bf16 a[84:87], a[172:175], v[140:143], a[84:87]\n\t" \
  "ds_read_b128 a[224:227], v220 offset:24576\n\t" \
  "v_cvt_pk_bf16_f32 v131, v6, v7\n\t" \
  "v_mfma_f32_16x16x32_bf16 a[100:103], a[176:179], v[136:139], a[100:103]\n\t" \
  "v_exp_f32 v103, v103\n\t" \
  "v_mfma_f32_16x16x32_bf16 a[100:103], a[180:183], v[140:143], a[100:103]\n\t" \
  "buffer_load_dwordx4 v231, %[sV], s96 offen offset:1024 lds\n\t" \
  "v_mfma_f32_16x16x32_bf16 a[116:119], a[184:187], v[136:139], a[116:119]\n\t" \
  "v_exp_f32 v104, v104\n\t" \
  "v_mfma_f32_16x16x32_bf16 a[116:119], a[188:191], v[140:143], a[116:119]\n\t" \
  "ds_read_b128 a[228:231], v221 offset:24576\n\t" \
  "v_cvt_pk_bf16_f32 v132, v8, v9\n\t" \
  "v_smfmac_f32_16x16x64_bf16 v[164:167], v[216:219], v[136:143], v239\n\t" \
  "buffer_load_dwordx4 v230, %[sV], s96 offen offset:2048 lds\n\t" \
  "v_mfma_f32_16x16x32_bf16 a[8:11], a[128:131], v[144:147], a[8:11]\n\t" \
  "v_exp_f32 v105, v105\n\t" \
  "v_mfma_f32_16x16x32_bf16 a[8:11], a[132:135], v[148:151], a[8:11]\n\t" \
  "ds_read_b128 a[232:235], v222 offset:24576\n\t" \
  "v_cvt_pk_bf16_f32 v133, v10, v11\n\t" \
  "v_mfma_f32_16x16x32_bf16 a[24:27], a[136:139], v[144:147], a[24:27]\n\t" \
  "v_exp_f32 v106, v106\n\t" \
  "v_mfma_f32_16x16x32_bf16 a[24:27], a[140:143], v[148:151], a[24:27]\n\t" \
  "buffer_load_dwordx4 v231, %[sV], s96 offen offset:3072 lds\n\t" \
  "v_mfma_f32_16x16x32_bf16 a[40:43], a[144:147], v[144:147], a[40:43]\n\t" \
  "s_add_u32 m0, %[m], 49152\n\t" \
  "v_exp_f32 v107, v107\n\t" \
  "v_mfma_f32_16x16x32_bf16 a[40:43], a[148:151], v[148:151], a[40:43]\n\t" \
  "s_xor_b32 s97, %[wob], 262144\n\t" \
  "ds_read_b128 a[236:239], v223 offset:24576\n\t" \
  "v_cvt_pk_bf16_f32 v134, v12, v13\n\t" \
  "v_mfma_f32_16x16x32_bf16 a[56:59], a[152:155], v[144:147], a[56:59]\n\t" \
  "v_exp_f32 v108, v108\n\t" \
  "v_mfma_f32_16x16x32_bf16 a[56:59], a[156:159], v[148:151], a[56:59]\n\t" \
  "v_exp_f32 v109, v109\n\t" \
  "v_mfma_f32_16x16x32_bf16 a[72:75], a[160:163], v[144:147], a[72:75]\n\t" \
  "ds_read_b128 a[240:243], v220 offset:28672\n\t" \
  "v_cvt_pk_bf16_f32 v135, v14, v15\n\t" \
  "v_mfma_f32_16x16x32_bf16 a[72:75], a[164:167], v[148:151], a[72:75]\n\t" \
  "v_exp_f32 v110, v110\n\t" \
  "v_mfma_f32_16x16x32_bf16 a[88:91], a[168:171], v[144:147], a[88:91]\n\t" \
  "v_exp_f32 v111, v111\n\t" \
  "v_mfma_f32_16x16x32_bf16 a[88:91], a[172:175], v[148:151], a[88:91]\n\t" \
  "ds_read_b128 a[244:247], v221 offset:28672\n\t" \
  "v_cvt_pk_bf16_f32 v136, v32, v33\n\t" \
  "v_mfma_f32_16x16x32_bf16 a[104:107], a[176:179], v[144:147], a[104:107]\n\t" \
  "buffer_load_dwordx4 v240, %[wo], s97 offen offset:0 lds\n\t" \
  "v_mfma_f32_16x16x32_bf16 a[104:107], a[180:183], v[148:151], a[104:107]\n\t" \
  "s_add_u32 s97, s97, 7168\n\t" \
  "ds_read_b128 a[248:251], v222 offset:28672\n\t" \
  "v_cvt_pk_bf16_f32 v137, v34, v35\n\t" \
  "v_mfma_f32_16x16x32_bf16 a[120:123], a[184:187], v[144:147], a[120:123]\n\t" \
  "ds_read_b128 a[252:255], v223 offset:28672\n\t" \
  "v_cvt_pk_bf16_f32 v138, v36, v37\n\t" \
  "v_mfma_f32_16x16x32_bf16 a[120:123], a[188:191], v[148:151], a[120:123]\n\t" \
  "buffer_load_dwordx4 v241, %[wo], s97 offen offset:1024 lds\n\t" \
  "v_smfmac_f32_16x16x64_bf16 v[168:171], v[216:219], v[144:151], v239\n\t" \
  "s_add_u32 s97, s97, 7168\n\t" \
  "v_cvt_pk_bf16_f32 v139, v38, v39\n\t" \
  "v_cvt_pk_bf16_f32 v140, v40, v41\n\t" \
  "v_mfma_f32_16x16x32_bf16 a[12:15], a[128:131], v[152:155], a[12:15]\n\t" \
  "v_cvt_pk_bf16_f32 v141, v42, v43\n\t" \
  "v_cvt_pk_bf16_f32 v142, v44, v45\n\t" \
  "v_mfma_f32_16x16x32_bf16 a[12:15], a[132:135], v[156:159], a[12:15]\n\t" \
  "buffer_load_dwordx4 v240, %[wo], s97 offen offset:2048 lds\n\t" \
  "v_mfma_f32_16x16x32_bf16 a[28:31], a[136:139], v[152:155], a[28:31]\n\t" \
  "s_add_u32 s97, s97, 7168\n\t" \
  "v_cvt_pk_bf16_f32 v143, v46, v47\n\t" \
  "v_cvt_pk_bf16_f32 v144, v64, v65\n\t" \
  "v_mfma_f32_16x16x32_bf16 a[28:31], a[140:143], v[156:159], a[28:31]\n\t" \
  "v_cvt_pk_bf16_f32 v145, v66, v67\n\t" \
  "v_cvt_pk_bf16_f32 v146, v68, v69\n\t" \
  "v_mfma_f32_16x16x32_bf16 a[44:47], a[144:147], v[152:155], a[44:47]\n\t" \
  "v_cvt_pk_bf16_f32 v147, v70, v71\n\t" \
  "v_cvt_pk_bf16_f32 v148, v72, v73\n\t" \
  "v_mfma_f32_16x16x32_bf16 a[44:47], a[148:151], v[156:159], a[44:47]\n\t" \
  "buffer_load_dwordx4 v241, %[wo], s97 offen offset:3072 lds\n\t" \
  "v_mfma_f32_16x16x32_bf16 a[60:63], a[152:155], v[152:155], a[60:63]\n\t" \
  "v_cvt_pk_bf16_f32 v149, v74, v75\n\t" \
  "v_cvt_pk_bf16_f32 v150, v76, v77\n\t" \
  "v_mfma_f32_16x16x32_bf16 a[60:63], a[156:159], v[156:159], a[60:63]\n\t" \
  "s_add_u32 m0, %[m], 98304\n\t" \
  "s_mov_b32 s96, 40960\n\t" \
  "v_cvt_pk_bf16_f32 v151, v78, v79\n\t" \
  "v_mfma_f32_16x16x32_bf16 a[76:79], a[160:163], v[152:155], a[76:79]\n\t" \
  "v_mfma_f32_16x16x32_bf16 a[76:79], a[164:167], v[156:159], a[76:79]\n\t" \
  "v_mfma_f32_16x16x32_bf16 a[92:95], a[168:171], v[152:155], a[92:95]\n\t" \
  "buffer_load_dwordx4 v232, %[sG], s96 offen offset:2048 lds\n\t" \
  "v_mfma_f32_16x16x32_bf16 a[92:95], a[172:175], v[156:159], a[92:95]\n\t" \
  "s_mov_b32 s96, 47104\n\t" \
  "v_mfma_f32_16x16x32_bf16 a[108:111], a[176:179], v[152:155], a[108:111]\n\t" \
  "v_mfma_f32_16x16x32_bf16 a[108:111], a[180:183], v[156:159], a[108:111]\n\t" \
  "v_mfma_f32_16x16x32_bf16 a[124:127], a[184:187], v[152:155], a[124:127]\n\t" \
  "buffer_load_dwordx4 v232, %[sG], s96 offen offset:3072 lds\n\t" \
  "v_mfma_f32_16x16x32_bf16 a[124:127], a[188:191], v[156:159], a[124:127]\n\t" \
  "v_smfmac_f32_16x16x64_bf16 v[172:175], v[216:219], v[152:159], v239\n\t" \
  "s_waitcnt lgkmcnt(0)\n\t" \
  "s_barrier\n\t" \
  STA("s_memrealtime s[94:95]\n\t") \
  STA("s_memtime s[92:93]\n\t") \
  "v_mfma_f32_16x16x32_bf16 v[0:3], a[192:195], v[16:19], v[176:179]\n\t" \
  "ds_read_b64_tr_b16 a[128:129], v228 offset:0\n\t" \
  "v_cvt_pk_bf16_f32 v152, v96, v97\n\t" \
  "v_mfma_f32_16x16x32_bf16 v[0:3], a[196:199], v[20:23], v[0:3]\n\t" \
  "ds_read_b64_tr_b16 a[130:131], v228 offset:4096\n\t" \
  "v_cvt_pk_bf16_f32 v153, v98, v99\n\t" \
  "v_mfma_f32_16x16x32_bf16 v[0:3], a[200:203], v[24:27], v[0:3]\n\t" \
  "ds_read_b64_tr_b16 a[132:133], v228 offset:8192\n\t" \
  "v_cvt_pk_bf16_f32 v154, v100, v101\n\t" \
  "v_mfma_f32_16x16x32_bf16 v[0:3], a[204:207], v[28:31], v[0:3]\n\t" \
  "ds_read_b64_tr_b16 a[134:135], v228 offset:12288\n\t" \
  "v_cvt_pk_bf16_f32 v155, v102, v103\n\t" \
  "v_mfma_f32_16x16x32_bf16 v[4:7], a[220:223], v[28:31], v[176:179]\n\t" \
  "s_add_u32 m0, %[m], 65536\n\t" \
  "ds_read_b64_tr_b16 a[136:137], v229 offset:0\n\t" \
  "v_cvt_pk_bf16_f32 v156, v104, v105\n\t" \
  "v_mfma_f32_16x16x32_bf16 v[4:7], a[216:219], v[24:27], v[4:7]\n\t" \
  "s_xor_b32 s97, %[wob], 393216\n\t" \
  "v_exp_f32 v0, v0\n\t" \
  "v_mfma_f32_16x16x32_bf16 v[4:7], a[212:215], v[20:23], v[4:7]\n\t" \
  "v_exp_f32 v1, v1\n\t" \
  "v_mfma_f32_16x16x32_bf16 v[4:7], a[208:211], v[16:19], v[4:7]\n\t" \
  "v_exp_f32 v2, v2\n\t" \
  "v_mfma_f32_16x16x32_bf16 v[8:11], a[224:227], v[16:19], v[176:179]\n\t" \
  "v_exp_f32 v3, v3\n\t" \
  "v_mfma_f32_16x16x32_bf16 v[8:11], a[228:231], v[20:23], v[8:11]\n\t" \
  "ds_read_b64_tr_b16 a[138:139], v229 offset:4096\n\t" \
  "v_cvt_pk_bf16_f32 v157, v106, v107\n\t" \
  "v_mfma_f32_16x16x32_bf16 v[8:11], a[232:235], v[24:27], v[8:11]\n\t" \
  "ds_read_b64_tr_b16 a[140:141], v229 offset:8192\n\t" \
  "v_cvt_pk_bf16_f32 v158, v108, v109\n\t" \
  "v_mfma_f32_16x16x32_bf16 v[8:11], a[236:239], v[28:31], v[8:11]\n\t" \
  "v_exp_f32 v4, v4\n\t" \
  "v_mfma_f32_16x16x32_bf16 v[12:15], a[252:255], v[28:31], v[176:179]\n\t" \
  STA("v_writelane_b32 %[stv], s94, 50\n\t") \
  STA("v_writelane_b32 %[stv], s92, 51\n\t") \
  "buffer_load_dwordx4 v240, %[wo], s97 offen offset:0 lds\n\t" \
  "v_mfma_f32_16x16x32_bf16 v[12:15], a[248:251], v[24:27], v[12:15]\n\t" \
  "s_add_u32 s97, s97, 7168\n\t" \
  "ds_read_b64_tr_b16 a[142:143], v229 offset:12288\n\t" \
  "v_cvt_pk_bf16_f32 v159, v110, v111\n\t" \
  "v_mfma_f32_16x16x32_bf16 v[12:15], a[244:247], v[20:23], v[12:15]\n\t" \
  "v_exp_f32 v5, v5\n\t" \
  "v_mfma_f32_16x16x32_bf16 v[12:15], a[240:243], v[16:19], v[12:15]\n\t" \
  "v_exp_f32 v6, v6\n\t" \
  "v_mfma_f32_16x16x32_bf16 v[32:35], a[192:195], v[48:51], v[180:183]\n\t" \
  "buffer_load_dwordx4 v241, %[wo], s97 offen offset:1024 lds\n\t" \
  "v_mfma_f32_16x16x32_bf16 v[32:35], a[196:199], v[52:55], v[32:35]\n\t" \
  "s_add_u32 s97, s97, 7168\n\t" \
  "ds_read_b64_tr_b16 a[144:145], v228 offset:256\n\t" \
  "ds_read_b64_tr_b16 a[146:147], v228 offset:4352\n\t" \
  "v_mfma_f32_16x16x32_bf16 v[32:35], a[200:203], v[56:59], v[32:35]\n\t" \
  "v_exp_f32 v7, v7\n\t" \
  "v_mfma_f32_16x16x32_bf16 v[32:35], a[204:207], v[60:63], v[32:35]\n\t" \
  "v_exp_f32 v8, v8\n\t" \
  "v_mfma_f32_16x16x32_bf16 v[36:39], a[220:223], v[60:63], v[180:183]\n\t" \
  "buffer_load_dwordx4 v240, %[wo], s97 offen offset:2048 lds\n\t" \
  "v_mfma_f32_16x16x32_bf16 v[36:39], a[216:219], v[56:59], v[36:39]\n\t" \
  "s_add_u32 s97, s97, 7168\n\t" \
  "ds_read_b64_tr_b16 a[148:149], v228 offset:8448\n\t" \
  "ds_read_b64_tr_b16 a[150:151], v228 offset:12544\n\t" \
  "v_mfma_f32_16x16x32_bf16 v[36:39], a[212:215], v[52:55], v[36:39]\n\t" \
  "v_exp_f32 v9, v9\n\t" \
  "v_mfma_f32_16x16x32_bf16 v[36:39], a[208:211], v[48:51], v[36:39]\n\t" \
  "ds_read_b64_tr_b16 a[152:153], v229 offset:256\n\t" \
  "ds_read_b64_tr_b16 a[154:155], v229 offset:4352\n\t" \
  "v_mfma_f32_16x16x32_bf16 v[40:43], a[224:227], v[48:51], v[180:183]\n\t" \
  "buffer_load_dwordx4 v241, %[wo], s97 offen offset:3072 lds\n\t" \
  "v_mfma_f32_16x16x32_bf16 v[40:43], a[228:231], v[52:55], v[40:43]\n\t" \
  "v_exp_f32 v10, v10\n\t" \
  "v_mfma_f32_16x16x32_bf16 v[40:43], a[232:235], v[56:59], v[40:43]\n\t" \
  "v_exp_f32 v11, v11\n\t" \
  "v_mfma_f32_16x16x32_bf16 v[40:43], a[236:239], v[60:63], v[40:43]\n\t" \
  "ds_read_b64_tr_b16 a[156:157], v229 offset:8448\n\t" \
  "ds_read_b64_tr_b16 a[158:159], v229 offset:12544\n\t" \
  "v_mfma_f32_16x16x32_bf16 v[44:47], a[252:255], v[60:63], v[180:183]\n\t" \
  "v_exp_f32 v12, v12\n\t" \
  "v_mfma_f32_16x16x32_bf16 v[44:47], a[248:251], v[56:59], v[44:47]\n\t" \
  "v_exp_f32 v13, v13\n\t" \
  "v_mfma_f32_16x16x32_bf16 v[44:47], a[244:247], v[52:55], v[44:47]\n\t" \
  "ds_read_b64_tr_b16 a[160:161], v228 offset:512\n\t" \
  "ds_read_b64_tr_b16 a[162:163], v228 offset:4608\n\t" \
  "v_mfma_f32_16x16x32_bf16 v[44:47], a[240:243], v[48:51], v[44:47]\n\t" \
  "v_exp_f32 v14, v14\n\t" \
  "v_mfma_f32_16x16x32_bf16 v[64:67], a[192:195], v[80:83], v[184:187]\n\t" \
  "v_exp_f32 v15, v15\n\t" \
  "v_mfma_f32_16x16x32_bf16 v[64:67], a[196:199], v[84:87], v[64:67]\n\t" \
  "v_exp_f32 v32, v32\n\t" \
  "v_mfma_f32_16x16x32_bf16 v[64:67], a[200:203], v[88:91], v[64:67]\n\t" \
  "ds_read_b64_tr_b16 a[164:165], v228 offset:8704\n\t" \
  "ds_read_b64_tr_b16 a[166:167], v228 offset:12800\n\t" \
  "v_mfma_f32_16x16x32_bf16 v[64:67], a[204:207], v[92:95], v[64:67]\n\t" \
  "v_exp_f32 v33, v33\n\t" \
  "v_mfma_f32_16x16x32_bf16 v[68:71], a[220:223], v[92:95], v[184:187]\n\t" \
  "v_exp_f32 v34, v34\n\t" \
  "v_mfma_f32_16x16x32_bf16 v[68:71], a[216:219], v[88:91], v[68:71]\n\t" \
  "ds_read_b64_tr_b16 a[168:169], v229 offset:512\n\t" \
  "ds_read_b64_tr_b16 a[170:171], v229 offset:4608\n\t" \
  "v_mfma_f32_16x16x32_bf16 v[68:71], a[212:215], v[84:87], v[68:71]\n\t" \
  "v_exp_f32 v35, v35\n\t" \
  "v_mfma_f32_16x16x32_bf16 v[68:71], a[208:211], v[80:83], v[68:71]\n\t" \
  "v_exp_f32 v36, v36\n\t" \
  "v_mfma_f32_16x16x32_bf16 v[72:75], a[224:227], v[80:83], v[184:187]\n\t" \
  "v_exp_f32 v37, v37\n\t" \
  "v_mfma_f32_16x16x32_bf16 v[72:75], a[228:231], v[84:87], v[72:75]\n\t" \
  "ds_read_b64_tr_b16 a[172:173], v229 offset:8704\n\t" \
  "ds_read_b64_tr_b16 a[174:175], v229 offset:12800\n\t" \
  "v_mfma_f32_16x16x32_bf16 v[72:75], a[232:235], v[88:91], v[72:75]\n\t" \
  "v_exp_f32 v38, v38\n\t" \
  "v_mfma_f32_16x16x32_bf16 v[72:75], a[236:239], v[92:95], v[72:75]\n\t" \
  "v_exp_f32 v39, v39\n\t" \
  "v_mfma_f32_16x16x32_bf16 v[76:79], a[252:255], v[92:95], v[184:187]\n\t" \
  "ds_read_b64_tr_b16 a[176:177], v228 offset:768\n\t" \
  "ds_read_b64_tr_b16 a[178:179], v228 offset:4864\n\t" \
  "v_mfma_f32_16x16x32_bf16 v[76:79], a[248:251], v[88:91], v[76:79]\n\t" \
  "v_exp_f32 v40, v40\n\t" \
  "v_mfma_f32_16x16x32_bf16 v[76:79], a[244:247], v[84:87], v[76:79]\n\t" \
  "v_exp_f32 v41, v41\n\t" \
  "v_mfma_f32_16x16x32_bf16 v[76:79], a[240:243], v[80:83], v[76:79]\n\t" \
  "v_exp_f32 v42, v42\n\t" \
  "v_mfma_f32_16x16x32_bf16 v[96:99], a[192:195], v[112:115], v[188:191]\n\t" \
  "ds_read_b64_tr_b16 a[180:181], v228 offset:8960\n\t" \
  "ds_read_b64_tr_b16 a[182:183], v228 offset:13056\n\t" \
  "v_mfma_f32_16x16x32_bf16 v[96:99], a[196:199], v[116:119], v[96:99]\n\t" \
  "v_exp_f32 v43, v43\n\t" \
  "v_mfma_f32_16x16x32_bf16 v[96:99], a[200:203], v[120:123], v[96:99]\n\t" \
  "v_exp_f32 v44, v44\n\t" \
  "v_mfma_f32_16x16x32_bf16 v[96:99], a[204:207], v[124:127], v[96:99]\n\t" \
  "ds_read_b64_tr_b16 a[184:185], v229 offset:768\n\t" \
  ".Lhs9:\n\ts_cbranch_execz .Lhs9n\n\t" \
  "ds_read_b64_tr_b16 a[186:187], v229 offset:4864\n\t" \
  "v_mfma_f32_16x16x32_bf16 v[100:103], a[220:223], v[124:127], v[188:191]\n\t" \
  "v_exp_f32 v45, v45\n\t" \
  "v_mfma_f32_16x16x32_bf16 v[100:103], a[216:219], v[120:123], v[100:103]\n\t" \
  "v_exp_f32 v46, v46\n\t" \
  "v_mfma_f32_16x16x32_bf16 v[100:103], a[212:215], v[116:119], v[100:103]\n\t" \
  "v_exp_f32 v47, v47\n\t" \
  "v_mfma_f32_16x16x32_bf16 v[100:103], a[208:211], v[112:115], v[100:103]\n\t" \
  "ds_read_b64_tr_b16 a[188:189], v229 offset:8960\n\t" \
  "ds_read_b64_tr_b16 a[190:191], v229 offset:13056\n\t" \
  "v_mfma_f32_16x16x32_bf16 v[104:107], a[224:227], v[112:115], v[188:191]\n\t" \
  "v_exp_f32 v64, v64\n\t" \
  "v_mfma_f32_16x16x32_bf16 v[104:107], a[228:231], v[116:119], v[104:107]\n\t" \
  "v_exp_f32 v65, v65\n\t" \
  "v_mfma_f32_16x16x32_bf16 v[104:107], a[232:235], v[120:123], v[104:107]\n\t" \
  "v_exp_f32 v66, v66\n\t" \
  "v_mfma_f32_16x16x32_bf16 v[104:107], a[236:239], v[124:127], v[104:107]\n\t" \
  "v_exp_f32 v67, v67\n\t" \
  "v_mfma_f32_16x16x32_bf16 v[108:111], a[252:255], v[124:127], v[188:191]\n\t" \
  "v_exp_f32 v68, v68\n\t" \
  "v_mfma_f32_16x16x32_bf16 v[108:111], a[248:251], v[120:123], v[108:111]\n\t" \
  "v_exp_f32 v69, v69\n\t" \
  "v_mfma_f32_16x16x32_bf16 v[108:111], a[244:247], v[116:119], v[108:111]\n\t" \
  "v_exp_f32 v70, v70\n\t" \
  "v_mfma_f32_16x16x32_bf16 v[108:111], a[240:243], v[112:115], v[108:111]\n\t" \
  "s_waitcnt vmcnt(6) lgkmcnt(0)\n\t" \
  "s_barrier\n\t" \
  STA("s_memrealtime s[94:95]\n\t") \
  STA("s_memtime s[92:93]\n\t") \
  "v_mfma_f32_16x16x32_bf16 a[0:3], a[128:131], v[128:131], a[0:3]\n\t" \
  "s_add_u32 m0, %[m], 81920\n\t" \
  "v_exp_f32 v71, v71\n\t" \
  "v_mfma_f32_16x16x32_bf16 a[0:3], a[132:135], v[132:135], a[0:3]\n\t" \
  "s_xor_b32 s97, %[wob], 524288\n\t" \
  "v_exp_f32 v72, v72\n\t" \
  "v_mfma_f32_16x16x32_bf16 a[16:19], a[136:139], v[128:131], a[16:19]\n\t" \
  "ds_read_b64_tr_b16 a[192:193], v237 offset:0\n\t" \
  "ds_read_b64_tr_b16 a[194:195], v237 offset:4096\n\t" \
  "v_mfma_f32_16x16x32_bf16 a[16:19], a[140:143], v[132:135], a[16:19]\n\t" \
  "v_exp_f32 v73, v73\n\t" \
  "v_mfma_f32_16x16x32_bf16 a[32:35], a[144:147], v[128:131], a[32:35]\n\t" \
  "ds_read_b128 v[176:179], v215 offset:16384\n\t" \
  "ds_read_b64_tr_b16 a[196:197], v237 offset:8192\n\t" \
  "v_mfma_f32_16x16x32_bf16 a[32:35], a[148:151], v[132:135], a[32:35]\n\t" \
  "v_exp_f32 v74, v74\n\t" \
  "v_mfma_f32_16x16x32_bf16 a[48:51], a[152:155], v[128:131], a[48:51]\n\t" \
  "buffer_load_dwordx4 v240, %[wo], s97 offen offset:0 lds\n\t" \
  "v_mfma_f32_16x16x32_bf16 a[48:51], a[156:159], v[132:135], a[48:51]\n\t" \
  "s_add_u32 s97, s97, 7168\n\t" \
  "ds_read_b128 v[180:183], v215 offset:17408\n\t" \
  "ds_read_b64_tr_b16 a[198:199], v237 offset:12288\n\t" \
  "v_mfma_f32_16x16x32_bf16 a[64:67], a[160:163], v[128:131], a[64:67]\n\t" \
  "buffer_load_dwordx4 v241, %[wo], s97 offen offset:1024 lds\n\t" \
  "v_mfma_f32_16x16x32_bf16 a[64:67], a[164:167], v[132:135], a[64:67]\n\t" \
  "s_add_u32 s97, s97, 7168\n\t" \
  "ds_read_b64_tr_b16 a[200:201], v238 offset:0\n\t" \
  "ds_read_b128 v[184:187], v215 offset:18432\n\t" \
  "v_mfma_f32_16x16x32_bf16 a[80:83], a[168:171], v[128:131], a[80:83]\n\t" \
  "ds_read_b64_tr_b16 a[202:203], v238 offset:4096\n\t" \
  "ds_read_b128 v[188:191], v215 offset:19456\n\t" \
  "v_mfma_f32_16x16x32_bf16 a[80:83], a[172:175], v[132:135], a[80:83]\n\t" \
  "ds_read_b64_tr_b16 a[204:205], v238 offset:8192\n\t" \
  "ds_read_b128 v[192:195], v215 offset:32768\n\t" \
  "v_mfma_f32_16x16x32_bf16 a[96:99], a[176:179], v[128:131], a[96:99]\n\t" \
  STA("v_writelane_b32 %[stv], s94, 52\n\t") \
  STA("v_writelane_b32 %[stv], s92, 53\n\t") \
  "buffer_load_dwordx4 v240, %[wo], s97 offen offset:2048 lds\n\t" \
  "v_mfma_f32_16x16x32_bf16 a[96:99], a[180:183], v[132:135], a[96:99]\n\t" \
  "s_add_u32 s97, s97, 7168\n\t" \
  "ds_read_b64_tr_b16 a[206:207], v238 offset:12288\n\t" \
  "ds_read_b128 v[196:199], v215 offset:33792\n\t" \
  "v_mfma_f32_16x16x32_bf16 a[112:115], a[184:187], v[128:131], a[112:115]\n\t" \
  "ds_read_b64_tr_b16 a[208:209], v237 offset:256\n\t" \
  "ds_read_b128 v[200:203], v215 offset:34816\n\t" \
  "v_mfma_f32_16x16x32_bf16 a[112:115], a[188:191], v[132:135], a[112:115]\n\t" \
  "ds_read_b64_tr_b16 a[210:211], v237 offset:4352\n\t" \
  "ds_read_b64_tr_b16 a[212:213], v237 offset:8448\n\t" \
  "v_smfmac_f32_16x16x64_bf16 v[160:163], v[216:219], v[128:135], v239\n\t" \
  "buffer_load_dwordx4 v241, %[wo], s97 offen offset:3072 lds\n\t" \
  "v_mfma_f32_16x16x32_bf16 a[4:7], a[128:131], v[136:139], a[4:7]\n\t" \
  "ds_read_b128 v[204:207], v215 offset:35840\n\t" \
  "v_cvt_pk_bf16_f32 v128, v0, v1\n\t" \
  "v_mfma_f32_16x16x32_bf16 a[4:7], a[132:135], v[140:143], a[4:7]\n\t" \
  "ds_read_b64_tr_b16 a[214:215], v237 offset:12544\n\t" \
  "v_cvt_pk_bf16_f32 v129, v2, v3\n\t" \
  "v_mfma_f32_16x16x32_bf16 a[20:23], a[136:139], v[136:139], a[20:23]\n\t" \
  "ds_read_b64_tr_b16 a[216:217], v238 offset:256\n\t" \
  "v_cvt_pk_bf16_f32 v130, v4, v5\n\t" \
  "v_mfma_f32_16x16x32_bf16 a[20:23], a[140:143], v[140:143], a[20:23]\n\t" \
  "s_add_u32 m0, %[m], 114688\n\t" \
  "ds_read_b64_tr_b16 a[218:219], v238 offset:4352\n\t" \
  "v_cvt_pk_bf16_f32 v131, v6, v7\n\t" \
  "v_mfma_f32_16x16x32_bf16 a[36:39], a[144:147], v[136:139], a[36:39]\n\t" \
  "s_xor_b32 s97, %[wob], 786432\n\t" \
  "ds_read_b64_tr_b16 a[220:221], v238 offset:8448\n\t" \
  "v_cvt_pk_bf16_f32 v132, v8, v9\n\t" \
  "v_mfma_f32_16x16x32_bf16 a[36:39], a[148:151], v[140:143], a[36:39]\n\t" \
  "s_waitcnt lgkmcnt(13)\n\t" \
  "ds_read_b64_tr_b16 a[222:223], v238 offset:12544\n\t" \
  "v_cvt_pk_bf16_f32 v133, v10, v11\n\t" \
  "v_mfma_f32_16x16x32_bf16 a[52:55], a[152:155], v[136:139], a[52:55]\n\t" \
  "ds_read_b64_tr_b16 a[224:225], v237 offset:512\n\t" \
  "v_cvt_pk_bf16_f32 v134, v12, v13\n\t" \
  "v_mfma_f32_16x16x32_bf16 a[52:55], a[156:159], v[140:143], a[52:55]\n\t" \
  "ds_read_b64_tr_b16 a[226:227], v237 offset:4608\n\t" \
  "v_cvt_pk_bf16_f32 v135, v14, v15\n\t" \
  "v_mfma_f32_16x16x32_bf16 a[68:71], a[160:163], v[136:139], a[68:71]\n\t" \
  "ds_read_b64_tr_b16 a[228:229], v237 offset:8704\n\t" \
  "ds_read_b64_tr_b16 a[230:231], v237 offset:12800\n\t" \
  "v_mfma_f32_16x16x32_bf16 a[68:71], a[164:167], v[140:143], a[68:71]\n\t" \
  "v_exp_f32 v75, v75\n\t" \
  "v_mfma_f32_16x16x32_bf16 a[84:87], a[168:171], v[136:139], a[84:87]\n\t" \
  "ds_read_b64_tr_b16 a[232:233], v238 offset:512\n\t" \
  "ds_read_b64_tr_b16 a[234:235], v238 offset:4608\n\t" \
  "v_mfma_f32_16x16x32_bf16 a[84:87], a[172:175], v[140:143], a[84:87]\n\t" \
  "buffer_load_dwordx4 v240, %[wo], s97 offen offset:0 lds\n\t" \
  "v_mfma_f32_16x16x32_bf16 a[100:103], a[176:179], v[136:139], a[100:103]\n\t" \
  "s_add_u32 s97, s97, 7168\n\t" \
  "v_exp_f32 v76, v76\n\t" \
  "v_mfma_f32_16x16x32_bf16 a[100:103], a[180:183], v[140:143], a[100:103]\n\t" \
  "ds_read_b64_tr_b16 a[236:237], v238 offset:8704\n\t" \
  "ds_read_b64_tr_b16 a[238:239], v238 offset:12800\n\t" \
  "v_mfma_f32_16x16x32_bf16 a[116:119], a[184:187], v[136:139], a[116:119]\n\t" \
  "buffer_load_dwordx4 v241, %[wo], s97 offen offset:1024 lds\n\t" \
  "v_mfma_f32_16x16x32_bf16 a[116:119], a[188:191], v[140:143], a[116:119]\n\t" \
  "s_add_u32 s97, s97, 7168\n\t" \
  "ds_read_b64_tr_b16 a[240:241], v237 offset:768\n\t" \
  "ds_read_b64_tr_b16 a[242:243], v237 offset:4864\n\t" \
  "v_smfmac_f32_16x16x64_bf16 v[164:167], v[216:219], v[136:143], v239\n\t" \
  "ds_read_b64_tr_b16 a[244:245], v237 offset:8960\n\t" \
  "v_cvt_pk_bf16_f32 v136, v32, v33\n\t" \
  "v_mfma_f32_16x16x32_bf16 a[0:3], a[192:195], v[128:131], a[0:3]\n\t" \
  "buffer_load_dwordx4 v240, %[wo], s97 offen offset:2048 lds\n\t" \
  "v_mfma_f32_16x16x32_bf16 a[0:3], a[196:199], v[132:135], a[0:3]\n\t" \
  "s_add_u32 s97, s97, 7168\n\t" \
  "ds_read_b64_tr_b16 a[246:247], v237 offset:13056\n\t" \
  "v_cvt_pk_bf16_f32 v137, v34, v35\n\t" \
  "v_smfmac_f32_16x16x64_bf16 v[160:163], v[216:219], v[128:135], v239\n\t" \
  "ds_read_b64_tr_b16 a[248:249], v238 offset:768\n\t" \
  "v_cvt_pk_bf16_f32 v138, v36, v37\n\t" \
  "v_mfma_f32_16x16x32_bf16 a[16:19], a[200:203], v[128:131], a[16:19]\n\t" \
  "s_waitcnt lgkmcnt(15)\n\t" \
  "ds_read_b64_tr_b16 a[250:251], v238 offset:4864\n\t" \
  "v_cvt_pk_bf16_f32 v139, v38, v39\n\t" \
  "v_mfma_f32_16x16x32_bf16 a[16:19], a[204:207], v[132:135], a[16:19]\n\t" \
  "buffer_load_dwordx4 v241, %[wo], s97 offen offset:3072 lds\n\t" \
  "v_mfma_f32_16x16x32_bf16 a[32:35], a[208:211], v[128:131], a[32:35]\n\t" \
  "s_add_u32 m0, %[m], 131072\n\t" \
  "ds_read_b64_tr_b16 a[252:253], v238 offset:8960\n\t" \
  "v_cvt_pk_bf16_f32 v140, v40, v41\n\t" \
  "v_mfma_f32_16x16x32_bf16 a[32:35], a[212:215], v[132:135], a[32:35]\n\t" \
  "s_xor_b32 s97, %[wob], 917504\n\t" \
  "ds_read_b64_tr_b16 a[254:255], v238 offset:13056\n\t" \
  "v_cvt_pk_bf16_f32 v141, v42, v43\n\t" \
  "v_mfma_f32_16x16x32_bf16 a[48:51], a[216:219], v[128:131], a[48:51]\n\t" \
  "s_waitcnt lgkmcnt(15)\n\t" \
  "v_cvt_pk_bf16_f32 v142, v44, v45\n\t" \
  "v_add_f32 v160, v160, v161\n\t" \
  "v_xor_b32 v235, 64, v234\n\t" \
  "v_mfma_f32_16x16x32_bf16 a[48:51], a[220:223], v[132:135], a[48:51]\n\t" \
  "s_waitcnt lgkmcnt(14)\n\t" \
  "v_rcp_f32 v208, v160\n\t" \
  "v_cvt_pk_bf16_f32 v143, v46, v47\n\t" \
  "v_mfma_f32_16x16x32_bf16 a[64:67], a[224:227], v[128:131], a[64:67]\n\t" \
  "s_waitcnt lgkmcnt(12)\n\t" \
  "ds_read_b64_tr_b16 v[48:49], v235 offset:0\n\t" \
  "ds_read_b64_tr_b16 v[50:51], v235 offset:4096\n\t" \
  "v_mfma_f32_16x16x32_bf16 a[64:67], a[228:231], v[132:135], a[64:67]\n\t" \
  "s_waitcnt lgkmcnt(12)\n\t" \
  "v_cmp_lt_f32 vcc, 0x5f800000, v160\n\t" \
  "s_cbranch_vccnz .Lat_x0\n\t" \
  ".Lat_y0:\n\t" \
  "v_accvgpr_read_b32 v16, a0\n\t" \
  "v_mfma_f32_16x16x32_bf16 a[80:83], a[232:235], v[128:131], a[80:83]\n\t" \
  "s_waitcnt lgkmcnt(10)\n\t" \
  "ds_read_b64_tr_b16 v[52:53], v235 offset:8192\n\t" \
  "v_accvgpr_read_b32 v17, a1\n\t" \
  "v_accvgpr_read_b32 v18, a2\n\t" \
  "v_mfma_f32_16x16x32_bf16 a[80:83], a[236:239], v[132:135], a[80:83]\n\t" \
  "s_waitcnt lgkmcnt(9)\n\t" \
  "buffer_load_dwordx4 v240, %[wo], s97 offen offset:0 lds\n\t" \
  "v_mfma_f32_16x16x32_bf16 a[96:99], a[240:243], v[128:131], a[96:99]\n\t" \
  "s_waitcnt lgkmcnt(7)\n\t" \
  "v_accvgpr_read_b32 v19, a3\n\t" \
  "v_accvgpr_read_b32 v20, a16\n\t" \
  "v_accvgpr_read_b32 v21, a17\n\t" \
  "v_accvgpr_read_b32 v22, a18\n\t" \
  "v_accvgpr_read_b32 v23, a19\n\t" \
  "v_pk_mul_f32 v[16:17], v[16:17], v[208:209] op_sel_hi:[1,0]\n\t" \
  "v_pk_mul_f32 v[18:19], v[18:19], v[208:209] op_sel_hi:[1,0]\n\t" \
  "v_pk_mul_f32 v[20:21], v[20:21], v[208:209] op_sel_hi:[1,0]\n\t" \
  "v_pk_mul_f32 v[22:23], v[22:23], v[208:209] op_sel_hi:[1,0]\n\t" \
  "v_cvt_pk_bf16_f32 v0, v16, v17\n\t" \
  "v_mfma_f32_16x16x32_bf16 a[96:99], a[244:247], v[132:135], a[96:99]\n\t" \
  "s_waitcnt lgkmcnt(5)\n\t" \
  "ds_read_b64_tr_b16 v[54:55], v235 offset:12288\n\t" \
  "v_cvt_pk_bf16_f32 v1, v18, v19\n\t" \
  "v_cvt_pk_bf16_f32 v2, v20, v21\n\t" \
  "v_cvt_pk_bf16_f32 v3, v22, v23\n\t" \
  "v_mfma_f32_16x16x32_bf16 a[112:115], a[248:251], v[128:131], a[112:115]\n\t" \
  "s_waitcnt lgkmcnt(4)\n\t" \
  "ds_read_b64_tr_b16 v[56:57], v235 offset:16384\n\t" \
  "v_max3_f32 v209, |v0|, |v1|, |v2|\n\t" \
  "v_mfma_f32_16x16x32_bf16 a[112:115], a[252:255], v[132:135], a[112:115]\n\t" \
  "s_waitcnt vmcnt(9)\n\t" \
  "s_barrier\n\t" \
  "v_mfma_f32_16x16x32_bf16 a[8:11], a[128:131], v[144:147], a[8:11]\n\t" \
  "s_add_u32 s97, s97, 7168\n\t" \
  "v_cmp_lt_f32 vcc, 0x33800000, v209\n\t" \
  "s_cbranch_vccz .Lat_x4\n\t" \
  ".Lat_y4:\n\t" \
  "v_accvgpr_read_b32 v24, a32\n\t" \
  "v_mfma_f32_16x16x32_bf16 a[8:11], a[132:135], v[148:151], a[8:11]\n\t" \
  "buffer_load_dwordx4 v241, %[wo], s97 offen offset:1024 lds\n\t" \
  "v_accvgpr_read_b32 v25, a33\n\t" \
  "v_accvgpr_read_b32 v26, a34\n\t" \
  "v_mfma_f32_16x16x32_bf16 a[24:27], a[136:139], v[144:147], a[24:27]\n\t" \
  "s_add_u32 s97, s97, 7168\n\t" \
  "v_accvgpr_read_b32 v27, a35\n\t" \
  "v_accvgpr_read_b32 v28, a48\n\t" \
  "v_accvgpr_read_b32 v29, a49\n\t" \
  "v_accvgpr_read_b32 v30, a50\n\t" \
  "v_accvgpr_read_b32 v31, a51\n\t" \
  "v_pk_mul_f32 v[24:25], v[24:25], v[208:209] op_sel_hi:[1,0]\n\t" \
  "v_pk_mul_f32 v[26:27], v[26:27], v[208:209] op_sel_hi:[1,0]\n\t" \
  "v_pk_mul_f32 v[28:29], v[28:29], v[208:209] op_sel_hi:[1,0]\n\t" \
  "v_pk_mul_f32 v[30:31], v[30:31], v[208:209] op_sel_hi:[1,0]\n\t" \
  "v_cvt_pk_bf16_f32 v4, v24, v25\n\t" \
  "v_mfma_f32_16x16x32_bf16 a[24:27], a[140:143], v[148:151], a[24:27]\n\t" \
  "buffer_load_dwordx4 v240, %[wo], s97 offen offset:2048 lds\n\t" \
  "v_mfma_f32_16x16x32_bf16 a[40:43], a[144:147], v[144:147], a[40:43]\n\t" \
  "s_xor_b32 s98, %[eso], 0\n\t" \
  "ds_read_b64_tr_b16 v[58:59], v235 offset:20480\n\t" \
  "v_cvt_pk_bf16_f32 v5, v26, v27\n\t" \
  "v_cvt_pk_bf16_f32 v6, v28, v29\n\t" \
  "v_cvt_pk_bf16_f32 v7, v30, v31\n\t" \
  "v_mfma_f32_16x16x32_bf16 a[40:43], a[148:151], v[148:151], a[40:43]\n\t" \
  "buffer_store_dwordx4 v[0:3], v242, %[ws], s98 offen offset:0 sc1\n\t" \
  "v_accvgpr_read_b32 v16, a64\n\t" \
  "v_accvgpr_read_b32 v17, a65\n\t" \
  "v_accvgpr_read_b32 v18, a66\n\t" \
  "v_mfma_f32_16x16x32_bf16 a[56:59], a[152:155], v[144:147], a[56:59]\n\t" \
  "s_add_u32 s97, s97, 7168\n\t" \
  "v_exp_f32 v77, v77\n\t" \
  "v_mfma_f32_16x16x32_bf16 a[56:59], a[156:159], v[148:151], a[56:59]\n\t" \
  "buffer_load_dwordx4 v241, %[wo], s97 offen offset:3072 lds\n\t" \
  "v_mfma_f32_16x16x32_bf16 a[72:75], a[160:163], v[144:147], a[72:75]\n\t" \
  "s_xor_b32 s98, %[eso], 0\n\t" \
  "v_accvgpr_read_b32 v19, a67\n\t" \
  "v_accvgpr_read_b32 v20, a80\n\t" \
  "v_accvgpr_read_b32 v21, a81\n\t" \
  "v_accvgpr_read_b32 v22, a82\n\t" \
  "v_accvgpr_read_b32 v23, a83\n\t" \
  "v_pk_mul_f32 v[16:17], v[16:17], v[208:209] op_sel_hi:[1,0]\n\t" \
  "v_pk_mul_f32 v[18:19], v[18:19], v[208:209] op_sel_hi:[1,0]\n\t" \
  "v_pk_mul_f32 v[20:21], v[20:21], v[208:209] op_sel_hi:[1,0]\n\t" \
  "v_pk_mul_f32 v[22:23], v[22:23], v[208:209] op_sel_hi:[1,0]\n\t" \
  "v_cvt_pk_bf16_f32 v8, v16, v17\n\t" \
  "v_mfma_f32_16x16x32_bf16 a[72:75], a[164:167], v[148:151], a[72:75]\n\t" \
  "buffer_store_dwordx4 v[4:7], v242, %[ws], s98 offen offset:1024 sc1\n\t" \
  "v_mfma_f32_16x16x32_bf16 a[88:91], a[168:171], v[144:147], a[88:91]\n\t" \
  "ds_read_b64_tr_b16 v[60:61], v235 offset:24576\n\t" \
  "v_cvt_pk_bf16_f32 v9, v18, v19\n\t" \
  "v_mfma_f32_16x16x32_bf16 a[88:91], a[172:175], v[148:151], a[88:91]\n\t" \
  "ds_read_b64_tr_b16 v[62:63], v235 offset:28672\n\t" \
  "v_xor_b32 v236, 96, v234\n\t" \
  "v_cvt_pk_bf16_f32 v10, v20, v21\n\t" \
  "v_cvt_pk_bf16_f32 v11, v22, v23\n\t" \
  "v_mfma_f32_16x16x32_bf16 a[104:107], a[176:179], v[144:147], a[104:107]\n\t" \
  "ds_read_b64_tr_b16 v[80:81], v236 offset:0\n\t" \
  "v_accvgpr_read_b32 v24, a96\n\t" \
  "v_mfma_f32_16x16x32_bf16 a[104:107], a[180:183], v[148:151], a[104:107]\n\t" \
  "ds_read_b64_tr_b16 v[82:83], v236 offset:4096\n\t" \
  "v_accvgpr_read_b32 v25, a97\n\t" \
  "v_accvgpr_read_b32 v26, a98\n\t" \
  "v_mfma_f32_16x16x32_bf16 a[120:123], a[184:187], v[144:147], a[120:123]\n\t" \
  "v_exp_f32 v78, v78\n\t" \
  "v_mfma_f32_16x16x32_bf16 a[120:123], a[188:191], v[148:151], a[120:123]\n\t" \
  "v_accvgpr_read_b32 v27, a99\n\t" \
  "v_accvgpr_read_b32 v28, a112\n\t" \
  "v_accvgpr_read_b32 v29, a113\n\t" \
  "v_accvgpr_read_b32 v30, a114\n\t" \
  "v_accvgpr_read_b32 v31, a115\n\t" \
  "v_pk_mul_f32 v[24:25], v[24:25], v[208:209] op_sel_hi:[1,0]\n\t" \
  "v_pk_mul_f32 v[26:27], v[26:27], v[208:209] op_sel_hi:[1,0]\n\t" \
  "v_pk_mul_f32 v[28:29], v[28:29], v[208:209] op_sel_hi:[1,0]\n\t" \
  "v_pk_mul_f32 v[30:31], v[30:31], v[208:209] op_sel_hi:[1,0]\n\t" \
  "v_cvt_pk_bf16_f32 v12, v24, v25\n\t" \
  "v_smfmac_f32_16x16x64_bf16 v[168:171], v[216:219], v[144:151], v239\n\t" \
  "s_xor_b32 s98, %[eso], 0\n\t" \
  "v_exp_f32 v79, v79\n\t" \
  STS("s_memrealtime s[94:95]\n\t") \
  STS("s_memtime s[92:93]\n\t") \
  "v_mfma_f32_16x16x32_bf16 a[4:7], a[192:195], v[136:139], a[4:7]\n\t" \
  "buffer_store_dwordx4 v[8:11], v242, %[ws], s98 offen offset:2048 sc1\n\t" \
  "v_cvt_pk_bf16_f32 v144, v64, v65\n\t" \
  "v_mfma_f32_16x16x32_bf16 a[4:7], a[196:199], v[140:143], a[4:7]\n\t" \
  "ds_read_b64_tr_b16 v[84:85], v236 offset:8192\n\t" \
  "v_cvt_pk_bf16_f32 v145, v66, v67\n\t" \
  "v_smfmac_f32_16x16x64_bf16 v[164:167], v[216:219], v[136:143], v239\n\t" \
  "ds_read_b64_tr_b16 v[86:87], v236 offset:12288\n\t" \
  "v_cvt_pk_bf16_f32 v13, v26, v27\n\t" \
  "v_cvt_pk_bf16_f32 v14, v28, v29\n\t" \
  "v_cvt_pk_bf16_f32 v15, v30, v31\n\t" \
  "v_cvt_pk_bf16_f32 v146, v68, v69\n\t" \
  "v_mfma_f32_16x16x32_bf16 a[20:23], a[200:203], v[136:139], a[20:23]\n\t" \
  "v_exp_f32 v96, v96\n\t" \
  "v_cvt_pk_bf16_f32 v147, v70, v71\n\t" \
  "v_mfma_f32_16x16x32_bf16 a[20:23], a[204:207], v[140:143], a[20:23]\n\t" \
  "ds_read_b64_tr_b16 v[88:89], v236 offset:16384\n\t" \
  "v_cvt_pk_bf16_f32 v148, v72, v73\n\t" \
  "v_mfma_f32_16x16x32_bf16 a[36:39], a[208:211], v[136:139], a[36:39]\n\t" \
  "v_exp_f32 v97, v97\n\t" \
  "v_cvt_pk_bf16_f32 v149, v74, v75\n\t" \
  "v_mfma_f32_16x16x32_bf16 a[36:39], a[212:215], v[140:143], a[36:39]\n\t" \
  "ds_read_b64_tr_b16 v[90:91], v236 offset:20480\n\t" \
  "v_cvt_pk_bf16_f32 v150, v76, v77\n\t" \
  "v_mfma_f32_16x16x32_bf16 a[52:55], a[216:219], v[136:139], a[52:55]\n\t" \
  "s_xor_b32 s98, %[eso], 0\n\t" \
  "ds_read_b64_tr_b16 v[92:93], v236 offset:24576\n\t" \
  "v_cvt_pk_bf16_f32 v151, v78, v79\n\t" \
  "v_add_f32 v164, v164, v165\n\t" \
  "v_mfma_f32_16x16x32_bf16 a[52:55], a[220:223], v[140:143], a[52:55]\n\t" \
  "buffer_store_dwordx4 v[12:15], v242, %[ws], s98 offen offset:3072 sc1\n\t" \
  "v_mfma_f32_16x16x32_bf16 a[68:71], a[224:227], v[136:139], a[68:71]\n\t" \
  "v_rcp_f32 v210, v164\n\t" \
  "v_mfma_f32_16x16x32_bf16 a[68:71], a[228:231], v[140:143], a[68:71]\n\t" \
  "v_cmp_lt_f32 vcc, 0x5f800000, v164\n\t" \
  "s_cbranch_vccnz .Lat_x1\n\t" \
  ".Lat_y1:\n\t" \
  "v_accvgpr_read_b32 v16, a4\n\t" \
  "v_mfma_f32_16x16x32_bf16 a[84:87], a[232:235], v[136:139], a[84:87]\n\t" \
  "ds_read_b64_tr_b16 v[94:95], v236 offset:28672\n\t" \
  "v_accvgpr_read_b32 v17, a5\n\t" \
  "v_accvgpr_read_b32 v18, a6\n\t" \
  "v_mfma_f32_16x16x32_bf16 a[84:87], a[236:239], v[140:143], a[84:87]\n\t" \
  STS("v_writelane_b32 %[stv], s94, 20\n\t") \
  STS("v_writelane_b32 %[stv], s92, 21\n\t") \
  "v_accvgpr_read_b32 v19, a7\n\t" \
  "v_accvgpr_read_b32 v20, a20\n\t" \
  "v_accvgpr_read_b32 v21, a21\n\t" \
  "v_accvgpr_read_b32 v22, a22\n\t" \
  "v_accvgpr_read_b32 v23, a23\n\t" \
  "v_pk_mul_f32 v[16:17], v[16:17], v[210:211] op_sel_hi:[1,0]\n\t" \
  "v_pk_mul_f32 v[18:19], v[18:19], v[210:211] op_sel_hi:[1,0]\n\t" \
  "v_pk_mul_f32 v[20:21], v[20:21], v[210:211] op_sel_hi:[1,0]\n\t" \
  "v_pk_mul_f32 v[22:23], v[22:23], v[210:211] op_sel_hi:[1,0]\n\t" \
  "v_cvt_pk_bf16_f32 v32, v16, v17\n\t" \
  "v_mfma_f32_16x16x32_bf16 a[100:103], a[240:243], v[136:139], a[100:103]\n\t" \
  "v_cvt_pk_bf16_f32 v33, v18, v19\n\t" \
  "v_cvt_pk_bf16_f32 v34, v20, v21\n\t" \
  "v_cvt_pk_bf16_f32 v35, v22, v23\n\t" \
  "v_mfma_f32_16x16x32_bf16 a[100:103], a[244:247], v[140:143], a[100:103]\n\t" \
  "v_xor_b32 v235, 128, v234\n\t" \
  "v_accvgpr_read_b32 v24, a36\n\t" \
  "v_mfma_f32_16x16x32_bf16 a[116:119], a[248:251], v[136:139], a[116:119]\n\t" \
  "s_xor_b32 s98, %[eso], 32768\n\t" \
  "ds_read_b64_tr_b16 v[112:113], v235 offset:0\n\t" \
  "v_accvgpr_read_b32 v25, a37\n\t" \
  "v_accvgpr_read_b32 v26, a38\n\t" \
  "v_mfma_f32_16x16x32_bf16 a[116:119], a[252:255], v[140:143], a[116:119]\n\t" \
  "buffer_store_dwordx4 v[32:35], v242, %[ws], s98 offen offset:0 sc1\n\t" \
  "v_mfma_f32_16x16x32_bf16 a[12:15], a[128:131], v[152:155], a[12:15]\n\t" \
  "v_accvgpr_read_b32 v27, a39\n\t" \
  "v_accvgpr_read_b32 v28, a52\n\t" \
  "v_accvgpr_read_b32 v29, a53\n\t" \
  "v_accvgpr_read_b32 v30, a54\n\t" \
  "v_accvgpr_read_b32 v31, a55\n\t" \
  "v_pk_mul_f32 v[24:25], v[24:25], v[210:211] op_sel_hi:[1,0]\n\t" \
  "v_pk_mul_f32 v[26:27], v[26:27], v[210:211] op_sel_hi:[1,0]\n\t" \
  "v_pk_mul_f32 v[28:29], v[28:29], v[210:211] op_sel_hi:[1,0]\n\t" \
  "v_pk_mul_f32 v[30:31], v[30:31], v[210:211] op_sel_hi:[1,0]\n\t" \
  "v_cvt_pk_bf16_f32 v36, v24, v25\n\t" \
  "v_mfma_f32_16x16x32_bf16 a[12:15], a[132:135], v[156:159], a[12:15]\n\t" \
  "ds_read_b64_tr_b16 v[114:115], v235 offset:4096\n\t" \
  "v_cvt_pk_bf16_f32 v37, v26, v27\n\t" \
  "v_mfma_f32_16x16x32_bf16 a[28:31], a[136:139], v[152:155], a[28:31]\n\t" \
  "v_exp_f32 v98, v98\n\t" \
  "v_mfma_f32_16x16x32_bf16 a[28:31], a[140:143], v[156:159], a[28:31]\n\t" \
  "ds_read_b64_tr_b16 v[116:117], v235 offset:8192\n\t" \
  "v_cvt_pk_bf16_f32 v38, v28, v29\n\t" \
  "v_mfma_f32_16x16x32_bf16 a[44:47], a[144:147], v[152:155], a[44:47]\n\t" \
  "ds_read_b64_tr_b16 v[118:119], v235 offset:12288\n\t" \
  "v_cvt_pk_bf16_f32 v39, v30, v31\n\t" \
  "v_mfma_f32_16x16x32_bf16 a[44:47], a[148:151], v[156:159], a[44:47]\n\t" \
  "ds_read_b64_tr_b16 v[120:121], v235 offset:16384\n\t" \
  "v_accvgpr_read_b32 v16, a68\n\t" \
  "v_accvgpr_read_b32 v17, a69\n\t" \
  "v_accvgpr_read_b32 v18, a70\n\t" \
  "v_mfma_f32_16x16x32_bf16 a[60:63], a[152:155], v[152:155], a[60:63]\n\t" \
  "s_xor_b32 s98, %[eso], 32768\n\t" \
  "v_exp_f32 v99, v99\n\t" \
  "v_mfma_f32_16x16x32_bf16 a[60:63], a[156:159], v[156:159], a[60:63]\n\t" \
  "buffer_store_dwordx4 v[36:39], v242, %[ws], s98 offen offset:1024 sc1\n\t" \
  "v_mfma_f32_16x16x32_bf16 a[76:79], a[160:163], v[152:155], a[76:79]\n\t" \
  "v_accvgpr_read_b32 v19, a71\n\t" \
  "v_accvgpr_read_b32 v20, a84\n\t" \
  "v_accvgpr_read_b32 v21, a85\n\t" \
  "v_accvgpr_read_b32 v22, a86\n\t" \
  "v_accvgpr_read_b32 v23, a87\n\t" \
  "v_pk_mul_f32 v[16:17], v[16:17], v[210:211] op_sel_hi:[1,0]\n\t" \
  "v_pk_mul_f32 v[18:19], v[18:19], v[210:211] op_sel_hi:[1,0]\n\t" \
  "v_pk_mul_f32 v[20:21], v[20:21], v[210:211] op_sel_hi:[1,0]\n\t" \
  "v_pk_mul_f32 v[22:23], v[22:23], v[210:211] op_sel_hi:[1,0]\n\t" \
  "v_cvt_pk_bf16_f32 v40, v16, v17\n\t" \
  "v_mfma_f32_16x16x32_bf16 a[76:79], a[164:167], v[156:159], a[76:79]\n\t" \
  "v_exp_f32 v100, v100\n\t" \
  "v_mfma_f32_16x16x32_bf16 a[92:95], a[168:171], v[152:155], a[92:95]\n\t" \
  "ds_read_b64_tr_b16 v[122:123], v235 offset:20480\n\t" \
  "v_cvt_pk_bf16_f32 v41, v18, v19\n\t" \
  "v_mfma_f32_16x16x32_bf16 a[92:95], a[172:175], v[156:159], a[92:95]\n\t" \
  "ds_read_b64_tr_b16 v[124:125], v235 offset:24576\n\t" \
  "v_cvt_pk_bf16_f32 v42, v20, v21\n\t" \
  "v_cvt_pk_bf16_f32 v43, v22, v23\n\t" \
  "v_mfma_f32_16x16x32_bf16 a[108:111], a[176:179], v[152:155], a[108:111]\n\t" \
  "ds_read_b64_tr_b16 v[126:127], v235 offset:28672\n\t" \
  "v_accvgpr_read_b32 v24, a100\n\t" \
  "v_mfma_f32_16x16x32_bf16 a[108:111], a[180:183], v[156:159], a[108:111]\n\t" \
  "v_accvgpr_read_b32 v25, a101\n\t" \
  "v_accvgpr_read_b32 v26, a102\n\t" \
  "v_mfma_f32_16x16x32_bf16 a[124:127], a[184:187], v[152:155], a[124:127]\n\t" \
  "s_xor_b32 s98, %[eso], 32768\n\t" \
  "v_exp_f32 v101, v101\n\t" \
  "v_mfma_f32_16x16x32_bf16 a[124:127], a[188:191], v[156:159], a[124:127]\n\t" \
  "buffer_store_dwordx4 v[40:43], v242, %[ws], s98 offen offset:2048 sc1\n\t" \
  "v_smfmac_f32_16x16x64_bf16 v[172:175], v[216:219], v[152:159], v239\n\t" \
  "v_accvgpr_read_b32 v27, a103\n\t" \
  "v_accvgpr_read_b32 v28, a116\n\t" \
  "v_accvgpr_read_b32 v29, a117\n\t" \
  "v_accvgpr_read_b32 v30, a118\n\t" \
  "v_accvgpr_read_b32 v31, a119\n\t" \
  "v_pk_mul_f32 v[24:25], v[24:25], v[210:211] op_sel_hi:[1,0]\n\t" \
  "v_pk_mul_f32 v[26:27], v[26:27], v[210:211] op_sel_hi:[1,0]\n\t" \
  "v_pk_mul_f32 v[28:29], v[28:29], v[210:211] op_sel_hi:[1,0]\n\t" \
  "v_pk_mul_f32 v[30:31], v[30:31], v[210:211] op_sel_hi:[1,0]\n\t" \
  "v_cvt_pk_bf16_f32 v44, v24, v25\n\t" \
  "v_mfma_f32_16x16x32_bf16 a[8:11], a[192:195], v[144:147], a[8:11]\n\t" \
  "ds_read_b64_tr_b16 a[128:129], v234 offset:0\n\t" \
  "v_cvt_pk_bf16_f32 v152, v96, v97\n\t" \
  "v_mfma_f32_16x16x32_bf16 a[8:11], a[196:199], v[148:151], a[8:11]\n\t" \
  "ds_read_b64_tr_b16 a[130:131], v234 offset:4096\n\t" \
  "v_cvt_pk_bf16_f32 v153, v98, v99\n\t" \
  "v_smfmac_f32_16x16x64_bf16 v[168:171], v[216:219], v[144:151], v239\n\t" \
  "ds_read_b64_tr_b16 a[132:133], v234 offset:8192\n\t" \
  "v_cvt_pk_bf16_f32 v45, v26, v27\n\t" \
  "v_cvt_pk_bf16_f32 v46, v28, v29\n\t" \
  "v_cvt_pk_bf16_f32 v47, v30, v31\n\t" \
  "v_cvt_pk_bf16_f32 v154, v100, v101\n\t" \
  "v_mfma_f32_16x16x32_bf16 a[24:27], a[200:203], v[144:147], a[24:27]\n\t" \
  "v_exp_f32 v102, v102\n\t" \
  "v_mfma_f32_16x16x32_bf16 a[24:27], a[204:207], v[148:151], a[24:27]\n\t" \
  "v_exp_f32 v103, v103\n\t" \
  "v_mfma_f32_16x16x32_bf16 a[40:43], a[208:211], v[144:147], a[40:43]\n\t" \
  "s_xor_b32 s98, %[eso], 32768\n\t" \
  "ds_read_b64_tr_b16 a[134:135], v234 offset:12288\n\t" \
  "v_cvt_pk_bf16_f32 v155, v102, v103\n\t" \
  "v_mfma_f32_16x16x32_bf16 a[40:43], a[212:215], v[148:151], a[40:43]\n\t" \
  "buffer_store_dwordx4 v[44:47], v242, %[ws], s98 offen offset:3072 sc1\n\t" \
  "v_mfma_f32_16x16x32_bf16 a[56:59], a[216:219], v[144:147], a[56:59]\n\t" \
  "ds_read_b64_tr_b16 a[136:137], v234 offset:16384\n\t" \
  "v_add_f32 v168, v168, v169\n\t" \
  "v_mfma_f32_16x16x32_bf16 a[56:59], a[220:223], v[148:151], a[56:59]\n\t" \
  "v_exp_f32 v104, v104\n\t" \
  "v_mfma_f32_16x16x32_bf16 a[72:75], a[224:227], v[144:147], a[72:75]\n\t" \
  "ds_read_b64_tr_b16 a[138:139], v234 offset:20480\n\t" \
  "ds_read_b64_tr_b16 a[140:141], v234 offset:24576\n\t" \
  "v_mfma_f32_16x16x32_bf16 a[72:75], a[228:231], v[148:151], a[72:75]\n\t" \
  "v_exp_f32 v105, v105\n\t" \
  "v_mfma_f32_16x16x32_bf16 a[88:91], a[232:235], v[144:147], a[88:91]\n\t" \
  "ds_read_b64_tr_b16 a[142:143], v234 offset:28672\n\t" \
  "v_xor_b32 v236, 32, v234\n\t" \
  "v_mfma_f32_16x16x32_bf16 a[88:91], a[236:239], v[148:151], a[88:91]\n\t" \
  "v_exp_f32 v106, v106\n\t" \
  "v_cvt_pk_bf16_f32 v156, v104, v105\n\t" \
  "v_mfma_f32_16x16x32_bf16 a[104:107], a[240:243], v[144:147], a[104:107]\n\t" \
  "v_exp_f32 v107, v107\n\t" \
  "v_mfma_f32_16x16x32_bf16 a[104:107], a[244:247], v[148:151], a[104:107]\n\t" \
  "ds_read_b64_tr_b16 a[144:145], v236 offset:0\n\t" \
  "v_cvt_pk_bf16_f32 v157, v106, v107\n\t" \
  "v_exp_f32 v108, v108\n\t" \
  "v_mfma_f32_16x16x32_bf16 a[120:123], a[248:251], v[144:147], a[120:123]\n\t" \
  "ds_read_b64_tr_b16 a[146:147], v236 offset:4096\n\t" \
  "ds_read_b64_tr_b16 a[148:149], v236 offset:8192\n\t" \
  "v_exp_f32 v109, v109\n\t" \
  "v_mfma_f32_16x16x32_bf16 a[120:123], a[252:255], v[148:151], a[120:123]\n\t" \
  "v_exp_f32 v110, v110\n\t" \
  "v_mfma_f32_16x16x32_bf16 a[12:15], a[192:195], v[152:155], a[12:15]\n\t" \
  "ds_read_b64_tr_b16 a[150:151], v236 offset:12288\n\t" \
  "v_cvt_pk_bf16_f32 v158, v108, v109\n\t" \
  "v_exp_f32 v111, v111\n\t" \
  "s_nop 0\n\t" \
  "v_cvt_pk_bf16_f32 v159, v110, v111\n\t" \
  "s_nop 1\n\t" \
  "v_mfma_f32_16x16x32_bf16 a[12:15], a[196:199], v[156:159], a[12:15]\n\t" \
  "ds_read_b64_tr_b16 a[152:153], v236 offset:16384\n\t" \
  "ds_read_b64_tr_b16 a[154:155], v236 offset:20480\n\t" \
  "v_smfmac_f32_16x16x64_bf16 v[172:175], v[216:219], v[152:159], v239\n\t" \
  "ds_read_b64_tr_b16 a[156:157], v236 offset:24576\n\t" \
  "ds_read_b64_tr_b16 a[158:159], v236 offset:28672\n\t" \
  "v_xor_b32 v235, 192, v234\n\t" \
  "v_mfma_f32_16x16x32_bf16 a[28:31], a[200:203], v[152:155], a[28:31]\n\t" \
  "ds_read_b64_tr_b16 a[160:161], v235 offset:0\n\t" \
  "ds_read_b64_tr_b16 a[162:163], v235 offset:4096\n\t" \
  "v_mfma_f32_16x16x32_bf16 a[28:31], a[204:207], v[156:159], a[28:31]\n\t" \
  "v_rcp_f32 v212, v168\n\t" \
  "v_mfma_f32_16x16x32_bf16 a[44:47], a[208:211], v[152:155], a[44:47]\n\t" \
  "ds_read_b64_tr_b16 a[164:165], v235 offset:8192\n\t" \
  "ds_read_b64_tr_b16 a[166:167], v235 offset:12288\n\t" \
  "v_mfma_f32_16x16x32_bf16 a[44:47], a[212:215], v[156:159], a[44:47]\n\t" \
  "v_cmp_lt_f32 vcc, 0x5f800000, v168\n\t" \
  "s_cbranch_vccnz .Lat_x2\n\t" \
  ".Lat_y2:\n\t" \
  "v_accvgpr_read_b32 v16, a8\n\t" \
  "v_mfma_f32_16x16x32_bf16 a[60:63], a[216:219], v[152:155], a[60:63]\n\t" \
  "ds_read_b64_tr_b16 a[168:169], v235 offset:16384\n\t" \
  "v_accvgpr_read_b32 v17, a9\n\t" \
  "v_accvgpr_read_b32 v18, a10\n\t" \
  "v_mfma_f32_16x16x32_bf16 a[60:63], a[220:223], v[156:159], a[60:63]\n\t" \
  "v_accvgpr_read_b32 v19, a11\n\t" \
  "v_accvgpr_read_b32 v20, a24\n\t" \
  "v_accvgpr_read_b32 v21, a25\n\t" \
  "v_accvgpr_read_b32 v22, a26\n\t" \
  "v_accvgpr_read_b32 v23, a27\n\t" \
  "v_pk_mul_f32 v[16:17], v[16:17], v[212:213] op_sel_hi:[1,0]\n\t" \
  "v_pk_mul_f32 v[18:19], v[18:19], v[212:213] op_sel_hi:[1,0]\n\t" \
  "v_pk_mul_f32 v[20:21], v[20:21], v[212:213] op_sel_hi:[1,0]\n\t" \
  "v_pk_mul_f32 v[22:23], v[22:23], v[212:213] op_sel_hi:[1,0]\n\t" \
  "v_cvt_pk_bf16_f32 v64, v16, v17\n\t" \
  "v_mfma_f32_16x16x32_bf16 a[76:79], a[224:227], v[152:155], a[76:79]\n\t" \
  "ds_read_b64_tr_b16 a[170:171], v235 offset:20480\n\t" \
  "v_cvt_pk_bf16_f32 v65, v18, v19\n\t" \
  "v_cvt_pk_bf16_f32 v66, v20, v21\n\t" \
  "v_cvt_pk_bf16_f32 v67, v22, v23\n\t" \
  "v_accvgpr_read_b32 v24, a40\n\t" \
  "v_mfma_f32_16x16x32_bf16 a[76:79], a[228:231], v[156:159], a[76:79]\n\t" \
  "s_xor_b32 s98, %[eso], 65536\n\t" \
  "ds_read_b64_tr_b16 a[172:173], v235 offset:24576\n\t" \
  "v_accvgpr_read_b32 v25, a41\n\t" \
  "v_accvgpr_read_b32 v26, a42\n\t" \
  "v_mfma_f32_16x16x32_bf16 a[92:95], a[232:235], v[152:155], a[92:95]\n\t" \
  "buffer_store_dwordx4 v[64:67], v242, %[ws], s98 offen offset:0 sc1\n\t" \
  "v_mfma_f32_16x16x32_bf16 a[92:95], a[236:239], v[156:159], a[92:95]\n\t" \
  "v_accvgpr_read_b32 v27, a43\n\t" \
  "v_accvgpr_read_b32 v28, a56\n\t" \
  "v_accvgpr_read_b32 v29, a57\n\t" \
  "v_accvgpr_read_b32 v30, a58\n\t" \
  "v_accvgpr_read_b32 v31, a59\n\t" \
  "v_pk_mul_f32 v[24:25], v[24:25], v[212:213] op_sel_hi:[1,0]\n\t" \
  "v_pk_mul_f32 v[26:27], v[26:27], v[212:213] op_sel_hi:[1,0]\n\t" \
  "v_pk_mul_f32 v[28:29], v[28:29], v[212:213] op_sel_hi:[1,0]\n\t" \
  "v_pk_mul_f32 v[30:31], v[30:31], v[212:213] op_sel_hi:[1,0]\n\t" \
  "v_cvt_pk_bf16_f32 v68, v24, v25\n\t" \
  "v_mfma_f32_16x16x32_bf16 a[108:111], a[240:243], v[152:155], a[108:111]\n\t" \
  "ds_read_b64_tr_b16 a[174:175], v235 offset:28672\n\t" \
  "v_cvt_pk_bf16_f32 v69, v26, v27\n\t" \
  "v_cvt_pk_bf16_f32 v70, v28, v29\n\t" \
  "v_cvt_pk_bf16_f32 v71, v30, v31\n\t" \
  "v_accvgpr_read_b32 v16, a72\n\t" \
  ".Lhs10:\n\ts_cbranch_execz .Lhs10n\n\t" \
  "v_mfma_f32_16x16x32_bf16 a[108:111], a[244:247], v[156:159], a[108:111]\n\t" \
  "s_xor_b32 s98, %[eso], 65536\n\t" \
  "v_accvgpr_read_b32 v17, a73\n\t" \
  "v_accvgpr_read_b32 v18, a74\n\t" \
  "v_xor_b32 v236, 224, v234\n\t" \
  "v_mfma_f32_16x16x32_bf16 a[124:127], a[248:251], v[152:155], a[124:127]\n\t" \
  "buffer_store_dwordx4 v[68:71], v242, %[ws], s98 offen offset:1024 sc1\n\t" \
  "v_mfma_f32_16x16x32_bf16 a[124:127], a[252:255], v[156:159], a[124:127]\n\t" \
  "s_waitcnt lgkmcnt(15)\n\t" \
  "v_accvgpr_read_b32 v19, a75\n\t" \
  "v_accvgpr_read_b32 v20, a88\n\t" \
  "v_accvgpr_read_b32 v21, a89\n\t" \
  "v_accvgpr_read_b32 v22, a90\n\t" \
  "v_accvgpr_read_b32 v23, a91\n\t" \
  "v_pk_mul_f32 v[16:17], v[16:17], v[212:213] op_sel_hi:[1,0]\n\t" \
  "v_pk_mul_f32 v[18:19], v[18:19], v[212:213] op_sel_hi:[1,0]\n\t" \
  "v_pk_mul_f32 v[20:21], v[20:21], v[212:213] op_sel_hi:[1,0]\n\t" \
  "v_pk_mul_f32 v[22:23], v[22:23], v[212:213] op_sel_hi:[1,0]\n\t" \
  "v_cvt_pk_bf16_f32 v72, v16, v17\n\t" \
  "v_mfma_f32_16x16x32_bf16 a[32:35], v[48:51], v[0:3], 0\n\t" \
  "ds_read_b64_tr_b16 a[176:177], v236 offset:0\n\t" \
  "v_cvt_pk_bf16_f32 v73, v18, v19\n\t" \
  "v_cvt_pk_bf16_f32 v74, v20, v21\n\t" \
  "v_cvt_pk_bf16_f32 v75, v22, v23\n\t" \
  "v_accvgpr_read_b32 v24, a104\n\t" \
  "v_mfma_f32_16x16x32_bf16 a[32:35], v[52:55], v[4:7], a[32:35]\n\t" \
  "s_xor_b32 s98, %[eso], 65536\n\t" \
  "ds_read_b64_tr_b16 a[178:179], v236 offset:4096\n\t" \
  "v_accvgpr_read_b32 v25, a105\n\t" \
  "v_accvgpr_read_b32 v26, a106\n\t" \
  "v_mfma_f32_16x16x32_bf16 a[32:35], v[56:59], v[8:11], a[32:35]\n\t" \
  "buffer_store_dwordx4 v[72:75], v242, %[ws], s98 offen offset:2048 sc1\n\t" \
  "v_mfma_f32_16x16x32_bf16 a[32:35], v[60:63], v[12:15], a[32:35]\n\t" \
  "v_accvgpr_read_b32 v27, a107\n\t" \
  "v_accvgpr_read_b32 v28, a120\n\t" \
  "v_accvgpr_read_b32 v29, a121\n\t" \
  "v_accvgpr_read_b32 v30, a122\n\t" \
  "v_accvgpr_read_b32 v31, a123\n\t" \
  "v_pk_mul_f32 v[24:25], v[24:25], v[212:213] op_sel_hi:[1,0]\n\t" \
  "v_pk_mul_f32 v[26:27], v[26:27], v[212:213] op_sel_hi:[1,0]\n\t" \
  "v_pk_mul_f32 v[28:29], v[28:29], v[212:213] op_sel_hi:[1,0]\n\t" \
  "v_pk_mul_f32 v[30:31], v[30:31], v[212:213] op_sel_hi:[1,0]\n\t" \
  "v_cvt_pk_bf16_f32 v76, v24, v25\n\t" \
  "v_mfma_f32_16x16x32_bf16 a[48:51], v[92:95], v[12:15], 0\n\t" \
  "ds_read_b64_tr_b16 a[180:181], v236 offset:8192\n\t" \
  "v_cvt_pk_bf16_f32 v77, v26, v27\n\t" \
  "v_cvt_pk_bf16_f32 v78, v28, v29\n\t" \
  "v_cvt_pk_bf16_f32 v79, v30, v31\n\t" \
  "v_add_f32 v172, v172, v173\n\t" \
  "v_mfma_f32_16x16x32_bf16 a[48:51], v[88:91], v[8:11], a[48:51]\n\t" \
  "s_xor_b32 s98, %[eso], 65536\n\t" \
  "ds_read_b64_tr_b16 a[182:183], v236 offset:12288\n\t" \
  "ds_read_b64_tr_b16 a[184:185], v236 offset:16384\n\t" \
  "v_mfma_f32_16x16x32_bf16 a[48:51], v[84:87], v[4:7], a[48:51]\n\t" \
  "buffer_store_dwordx4 v[76:79], v242, %[ws], s98 offen offset:3072 sc1\n\t" \
  "v_mfma_f32_16x16x32_bf16 a[48:51], v[80:83], v[0:3], a[48:51]\n\t" \
  "ds_read_b64_tr_b16 a[186:187], v236 offset:20480\n\t" \
  "ds_read_b64_tr_b16 a[188:189], v236 offset:24576\n\t" \
  "v_mfma_f32_16x16x32_bf16 a[64:67], v[112:115], v[0:3], 0\n\t" \
  "ds_read_b64_tr_b16 a[190:191], v236 offset:28672\n\t" \
  "v_xor_b32 v236, 160, v234\n\t" \
  "v_mfma_f32_16x16x32_bf16 a[64:67], v[116:119], v[4:7], a[64:67]\n\t" \
  "ds_read_b64_tr_b16 v[96:97], v236 offset:0\n\t" \
  "ds_read_b64_tr_b16 v[98:99], v236 offset:4096\n\t" \
  "v_mfma_f32_16x16x32_bf16 a[64:67], v[120:123], v[8:11], a[64:67]\n\t" \
  "ds_read_b64_tr_b16 v[100:101], v236 offset:8192\n\t" \
  "ds_read_b64_tr_b16 v[102:103], v236 offset:12288\n\t" \
  "v_mfma_f32_16x16x32_bf16 a[64:67], v[124:127], v[12:15], a[64:67]\n\t" \
  "ds_read_b64_tr_b16 v[104:105], v236 offset:16384\n\t" \
  "ds_read_b64_tr_b16 v[106:107], v236 offset:20480\n\t" \
  "v_mfma_f32_16x16x32_bf16 a[0:3], a[140:143], v[12:15], 0\n\t" \
  "ds_read_b64_tr_b16 v[108:109], v236 offset:24576\n\t" \
  "ds_read_b64_tr_b16 v[110:111], v236 offset:28672\n\t" \
  "v_mfma_f32_16x16x32_bf16 a[0:3], a[136:139], v[8:11], a[0:3]\n\t" \
  "v_rcp_f32 v214, v172\n\t" \
  "v_mfma_f32_16x16x32_bf16 a[0:3], a[132:135], v[4:7], a[0:3]\n\t" \
  "ds_read_b128 a[224:227], v213 offset:32768\n\t" \
  "ds_read_b128 a[228:231], v213 offset:33792\n\t" \
  "v_mfma_f32_16x16x32_bf16 a[0:3], a[128:131], v[0:3], a[0:3]\n\t" \
  "s_waitcnt lgkmcnt(15)\n\t" \
  "v_cmp_lt_f32 vcc, 0x5f800000, v172\n\t" \
  "s_cbranch_vccnz .Lat_x3\n\t" \
  ".Lat_y3:\n\t" \
  "v_mfma_f32_16x16x32_bf16 a[16:19], a[144:147], v[0:3], 0\n\t" \
  "ds_read_b128 a[232:235], v213 offset:34816\n\t" \
  "v_accvgpr_read_b32 v16, a12\n\t" \
  "v_mfma_f32_16x16x32_bf16 a[16:19], a[148:151], v[4:7], a[16:19]\n\t" \
  "ds_read_b128 a[236:239], v213 offset:35840\n\t" \
  "v_accvgpr_read_b32 v17, a13\n\t" \
  "v_mfma_f32_16x16x32_bf16 a[16:19], a[152:155], v[8:11], a[16:19]\n\t" \
  "ds_read_b128 a[240:243], v215 offset:0\n\t" \
  "v_accvgpr_read_b32 v18, a14\n\t" \
  "v_mfma_f32_16x16x32_bf16 a[16:19], a[156:159], v[12:15], a[16:19]\n\t" \
  "ds_read_b128 a[244:247], v215 offset:1024\n\t" \
  "v_accvgpr_read_b32 v19, a15\n\t" \
  "v_mfma_f32_16x16x32_bf16 a[96:99], a[172:175], v[12:15], 0\n\t" \
  "buffer_load_dwordx4 v[224:227], v252, %[ws], %[ss] offen sc1\n\t" \
  "v_mfma_f32_16x16x32_bf16 a[96:99], a[168:171], v[8:11], a[96:99]\n\t" \
  "ds_read_b128 a[248:251], v215 offset:2048\n\t" \
  "v_accvgpr_read_b32 v20, a28\n\t" \
  "v_mfma_f32_16x16x32_bf16 a[96:99], a[164:167], v[4:7], a[96:99]\n\t" \
  "ds_read_b128 a[252:255], v215 offset:3072\n\t" \
  "v_accvgpr_read_b32 v21, a29\n\t" \
  "v_mfma_f32_16x16x32_bf16 a[96:99], a[160:163], v[0:3], a[96:99]\n\t" \
  "v_accvgpr_read_b32 v22, a30\n\t" \
  "v_accvgpr_read_b32 v23, a31\n\t" \
  "v_mfma_f32_16x16x32_bf16 a[112:115], a[176:179], v[0:3], 0\n\t" \
  "s_waitcnt lgkmcnt(15)\n\t" \
  "buffer_load_dwordx4 v[128:131], v247, %[sM], %[gvo] offen\n\t" \
  "v_mfma_f32_16x16x32_bf16 a[112:115], a[180:183], v[4:7], a[112:115]\n\t" \
  "s_add_u32 s98, %[gvo], 12288\n\t" \
  "v_mul_f32 v16, v16, v214\n\t" \
  "v_mul_f32 v17, v17, v214\n\t" \
  "v_mfma_f32_16x16x32_bf16 a[112:115], a[184:187], v[8:11], a[112:115]\n\t" \
  "v_mul_f32 v18, v18, v214\n\t" \
  "v_mul_f32 v19, v19, v214\n\t" \
  "v_mfma_f32_16x16x32_bf16 a[112:115], a[188:191], v[12:15], a[112:115]\n\t" \
  "s_waitcnt lgkmcnt(8)\n\t" \
  "v_mul_f32 v20, v20, v214\n\t" \
  "v_mul_f32 v21, v21, v214\n\t" \
  "v_mfma_f32_16x16x32_bf16 a[80:83], v[108:111], v[12:15], 0\n\t" \
  "buffer_load_dwordx4 v[132:135], v247, %[sM], s98 offen\n\t" \
  "v_mfma_f32_16x16x32_bf16 a[80:83], v[104:107], v[8:11], a[80:83]\n\t" \
  "s_add_u32 s98, %[gvo], 24576\n\t" \
  "v_mul_f32 v22, v22, v214\n\t" \
  "v_mul_f32 v23, v23, v214\n\t" \
  "v_mfma_f32_16x16x32_bf16 a[80:83], v[100:103], v[4:7], a[80:83]\n\t" \
  "v_cvt_pk_bf16_f32 v160, v16, v17\n\t" \
  "v_cvt_pk_bf16_f32 v161, v18, v19\n\t" \
  "v_mfma_f32_16x16x32_bf16 a[80:83], v[96:99], v[0:3], a[80:83]\n\t" \
  "v_cvt_pk_bf16_f32 v162, v20, v21\n\t" \
  "v_cvt_pk_bf16_f32 v163, v22, v23\n\t" \
  "v_mfma_f32_16x16x32_bf16 a[36:39], v[48:51], v[32:35], 0\n\t" \
  "buffer_load_dwordx4 v[136:139], v247, %[sM], s98 offen\n\t" \
  "v_mfma_f32_16x16x32_bf16 a[36:39], v[52:55], v[36:39], a[36:39]\n\t" \
  "s_add_u32 s98, %[gvo], 36864\n\t" \
  "s_waitcnt lgkmcnt(4)\n\t" \
  "ds_write_b128 v213, v[160:163] offset:32768\n\t" \
  "v_mfma_f32_16x16x32_bf16 a[36:39], v[56:59], v[40:43], a[36:39]\n\t" \
  "v_accvgpr_read_b32 v24, a44\n\t" \
  "v_accvgpr_read_b32 v25, a45\n\t" \
  "v_mfma_f32_16x16x32_bf16 a[36:39], v[60:63], v[44:47], a[36:39]\n\t" \
  "v_accvgpr_read_b32 v26, a46\n\t" \
  "v_accvgpr_read_b32 v27, a47\n\t" \
  "s_waitcnt vmcnt(8)\n\t" \
  "buffer_store_dword v243, v254, %[ws], %[fw2] offen offset:0 sc1\n\t" \
  "v_mfma_f32_16x16x32_bf16 a[52:55], v[92:95], v[44:47], 0\n\t" \
  "buffer_load_dwordx4 v[140:143], v247, %[sM], s98 offen\n\t" \
  "v_mfma_f32_16x16x32_bf16 a[52:55], v[88:91], v[40:43], a[52:55]\n\t" \
  "s_add_u32 s98, %[gvo], 49152\n\t" \
  "v_accvgpr_read_b32 v28, a60\n\t" \
  "v_accvgpr_read_b32 v29, a61\n\t" \
  "v_mfma_f32_16x16x32_bf16 a[52:55], v[84:87], v[36:39], a[52:55]\n\t" \
  "v_accvgpr_read_b32 v30, a62\n\t" \
  "v_accvgpr_read_b32 v31, a63\n\t" \
  "v_mfma_f32_16x16x32_bf16 a[52:55], v[80:83], v[32:35], a[52:55]\n\t" \
  "v_mul_f32 v24, v24, v214\n\t" \
  "v_mul_f32 v25, v25, v214\n\t" \
  "v_mfma_f32_16x16x32_bf16 a[68:71], v[112:115], v[32:35], 0\n\t" \
  "buffer_load_dwordx4 v[144:147], v247, %[sM], s98 offen\n\t" \
  "v_mfma_f32_16x16x32_bf16 a[68:71], v[116:119], v[36:39], a[68:71]\n\t" \
  "s_add_u32 s98, %[gvo], 61440\n\t" \
  "v_mul_f32 v26, v26, v214\n\t" \
  "v_mul_f32 v27, v27, v214\n\t" \
  "v_mfma_f32_16x16x32_bf16 a[68:71], v[120:123], v[40:43], a[68:71]\n\t" \
  "v_mul_f32 v28, v28, v214\n\t" \
  "v_mul_f32 v29, v29, v214\n\t" \
  "v_mfma_f32_16x16x32_bf16 a[68:71], v[124:127], v[44:47], a[68:71]\n\t" \
  "v_mul_f32 v30, v30, v214\n\t" \
  "v_mul_f32 v31, v31, v214\n\t" \
  "v_mfma_f32_16x16x32_bf16 a[4:7], a[140:143], v[44:47], 0\n\t" \
  "buffer_load_dwordx4 v[148:151], v247, %[sM], s98 offen\n\t" \
  "v_mfma_f32_16x16x32_bf16 a[4:7], a[136:139], v[40:43], a[4:7]\n\t" \
  "s_add_u32 s98, %[gvo], 73728\n\t" \
  "v_cvt_pk_bf16_f32 v164, v24, v25\n\t" \
  "v_cvt_pk_bf16_f32 v165, v26, v27\n\t" \
  "v_mfma_f32_16x16x32_bf16 a[4:7], a[132:135], v[36:39], a[4:7]\n\t" \
  "v_cvt_pk_bf16_f32 v166, v28, v29\n\t" \
  "v_cvt_pk_bf16_f32 v167, v30, v31\n\t" \
  "v_mfma_f32_16x16x32_bf16 a[4:7], a[128:131], v[32:35], a[4:7]\n\t" \
  "ds_write_b128 v213, v[164:167] offset:33792\n\t" \
  "v_mfma_f32_16x16x32_bf16 a[20:23], a[144:147], v[32:35], 0\n\t" \
  "buffer_load_dwordx4 v[152:155], v247, %[sM], s98 offen\n\t" \
  "v_mfma_f32_16x16x32_bf16 a[20:23], a[148:151], v[36:39], a[20:23]\n\t" \
  "s_add_u32 s98, %[gvo], 86016\n\t" \
  "v_accvgpr_read_b32 v16, a76\n\t" \
  "v_accvgpr_read_b32 v17, a77\n\t" \
  "v_mfma_f32_16x16x32_bf16 a[20:23], a[152:155], v[40:43], a[20:23]\n\t" \
  "v_accvgpr_read_b32 v18, a78\n\t" \
  "v_accvgpr_read_b32 v19, a79\n\t" \
  "v_mfma_f32_16x16x32_bf16 a[20:23], a[156:159], v[44:47], a[20:23]\n\t" \
  "v_accvgpr_read_b32 v20, a92\n\t" \
  "v_accvgpr_read_b32 v21, a93\n\t" \
  "v_mfma_f32_16x16x32_bf16 a[100:103], a[172:175], v[44:47], 0\n\t" \
  "buffer_load_dwordx4 v[156:159], v247, %[sM], s98 offen\n\t" \
  "v_mfma_f32_16x16x32_bf16 a[100:103], a[168:171], v[40:43], a[100:103]\n\t" \
  "v_accvgpr_read_b32 v22, a94\n\t" \
  "v_accvgpr_read_b32 v23, a95\n\t" \
  "v_mfma_f32_16x16x32_bf16 a[100:103], a[164:167], v[36:39], a[100:103]\n\t" \
  "v_mul_f32 v16, v16, v214\n\t" \
  "v_mul_f32 v17, v17, v214\n\t" \
  "v_mfma_f32_16x16x32_bf16 a[100:103], a[160:163], v[32:35], a[100:103]\n\t" \
  "v_mul_f32 v18, v18, v214\n\t" \
  "v_mul_f32 v19, v19, v214\n\t" \
  "v_mfma_f32_16x16x32_bf16 a[116:119], a[176:179], v[32:35], 0\n\t" \
  "v_mul_f32 v20, v20, v214\n\t" \
  "v_mul_f32 v21, v21, v214\n\t" \
  "v_mfma_f32_16x16x32_bf16 a[116:119], a[180:183], v[36:39], a[116:119]\n\t" \
  "v_mul_f32 v22, v22, v214\n\t" \
  "v_mul_f32 v23, v23, v214\n\t" \
  "v_mfma_f32_16x16x32_bf16 a[116:119], a[184:187], v[40:43], a[116:119]\n\t" \
  "v_cvt_pk_bf16_f32 v168, v16, v17\n\t" \
  "v_cvt_pk_bf16_f32 v169, v18, v19\n\t" \
  "v_mfma_f32_16x16x32_bf16 a[116:119], a[188:191], v[44:47], a[116:119]\n\t" \
  "v_cvt_pk_bf16_f32 v170, v20, v21\n\t" \
  "v_cvt_pk_bf16_f32 v171, v22, v23\n\t" \
  "v_mfma_f32_16x16x32_bf16 a[84:87], v[108:111], v[44:47], 0\n\t" \
  "s_add_u32 m0, %[m], 147456\n\t" \
  "ds_write_b128 v213, v[168:171] offset:34816\n\t" \
  "v_mfma_f32_16x16x32_bf16 a[84:87], v[104:107], v[40:43], a[84:87]\n\t" \
  "s_xor_b32 s97, %[wob], 655360\n\t" \
  "v_accvgpr_read_b32 v24, a108\n\t" \
  "v_accvgpr_read_b32 v25, a109\n\t" \
  "v_mfma_f32_16x16x32_bf16 a[84:87], v[100:103], v[36:39], a[84:87]\n\t" \
  "v_accvgpr_read_b32 v26, a110\n\t" \
  "v_accvgpr_read_b32 v27, a111\n\t" \
  "v_mfma_f32_16x16x32_bf16 a[84:87], v[96:99], v[32:35], a[84:87]\n\t" \
  "v_accvgpr_read_b32 v28, a124\n\t" \
  "v_accvgpr_read_b32 v29, a125\n\t" \
  "v_mfma_f32_16x16x32_bf16 a[40:43], v[48:51], v[64:67], 0\n\t" \
  "v_accvgpr_read_b32 v30, a126\n\t" \
  "v_accvgpr_read_b32 v31, a127\n\t" \
  "v_mfma_f32_16x16x32_bf16 a[40:43], v[52:55], v[68:71], a[40:43]\n\t" \
  "v_mul_f32 v24, v24, v214\n\t" \
  "v_mul_f32 v25, v25, v214\n\t" \
  "v_mfma_f32_16x16x32_bf16 a[40:43], v[56:59], v[72:75], a[40:43]\n\t" \
  "buffer_load_dwordx4 v240, %[wo], s97 offen offset:0 lds\n\t" \
  "v_mfma_f32_16x16x32_bf16 a[40:43], v[60:63], v[76:79], a[40:43]\n\t" \
  "s_add_u32 s97, s97, 7168\n\t" \
  "v_mul_f32 v26, v26, v214\n\t" \
  "v_mul_f32 v27, v27, v214\n\t" \
  "v_mfma_f32_16x16x32_bf16 a[56:59], v[92:95], v[76:79], 0\n\t" \
  "v_mul_f32 v28, v28, v214\n\t" \
  "v_mul_f32 v29, v29, v214\n\t" \
  "v_mfma_f32_16x16x32_bf16 a[56:59], v[88:91], v[72:75], a[56:59]\n\t" \
  "buffer_load_dwordx4 v241, %[wo], s97 offen offset:1024 lds\n\t" \
  "v_mfma_f32_16x16x32_bf16 a[56:59], v[84:87], v[68:71], a[56:59]\n\t" \
  "s_add_u32 s97, s97, 7168\n\t" \
  "v_mul_f32 v30, v30, v214\n\t" \
  "v_mul_f32 v31, v31, v214\n\t" \
  "v_mfma_f32_16x16x32_bf16 a[56:59], v[80:83], v[64:67], a[56:59]\n\t" \
  "v_cvt_pk_bf16_f32 v172, v24, v25\n\t" \
  "v_cvt_pk_bf16_f32 v173, v26, v27\n\t" \
  "v_mfma_f32_16x16x32_bf16 a[72:75], v[112:115], v[64:67], 0\n\t" \
  "buffer_load_dwordx4 v240, %[wo], s97 offen offset:2048 lds\n\t" \
  "v_cvt_pk_bf16_f32 v174, v28, v29\n\t" \
  "v_cvt_pk_bf16_f32 v175, v30, v31\n\t" \
  "ds_write_b128 v213, v[172:175] offset:35840\n\t" \
  "v_mfma_f32_16x16x32_bf16 a[72:75], v[116:119], v[68:71], a[72:75]\n\t" \
  "s_add_u32 s97, s97, 7168\n\t" \
  "v_mfma_f32_16x16x32_bf16 a[72:75], v[120:123], v[72:75], a[72:75]\n\t" \
  "v_mfma_f32_16x16x32_bf16 a[72:75], v[124:127], v[76:79], a[72:75]\n\t" \
  "buffer_load_dwordx4 v241, %[wo], s97 offen offset:3072 lds\n\t" \
  STS("s_memrealtime s[94:95]\n\t") \
  STS("s_memtime s[92:93]\n\t") \
  STS("s_waitcnt lgkmcnt(0)\n\t") \
  STS("v_writelane_b32 %[stv], s94, 22\n\t") \
  STS("v_writelane_b32 %[stv], s92, 23\n\t") \
  ".subsection 1\n\t" \
  "s_endpgm\n\t" \
  ".Lat_v:\n\t" \
  "s_mov_b32 s98, 0\n\t" \
  ".Lat_v1:\n\t" \
  "s_sleep 4\n\t" \
  "buffer_load_dword v209, v205, %[ws], 0 offen sc1\n\t" \
  "s_waitcnt vmcnt(0)\n\t" \
  "v_and_b32 v206, 0xbfffbfff, v209\n\t" \
  "v_cmp_ne_u32_e64 s[96:97], %[tag], v206\n\t" \
  "s_cmp_lg_u64 s[96:97], 0\n\t" \
  "s_cbranch_scc0 .Lat_w\n\t" \
  "s_add_u32 s98, s98, 1\n\t" \
  "s_cmp_lt_u32 s98, %[cap]\n\t" \
  "s_cbranch_scc1 .Lat_v1\n\t" \
  "v_mov_b32 v206, %[tag]\n\t" \
  "buffer_store_dword v206, off, %[ws], 0 offset:256 sc1\n\t" \
  "s_branch .Lat_w\n\t" \
  ".Lat_x0:\n\t" \
  "v_lshlrev_b32 v209, 12, v233\n\t" \
  "v_bfrev_b32 v254, 1\n\t" \
  "s_mov_b64 exec, 15\n\t" \
  "ds_write_b32 v209, v243 offset:49152\n\t" \
  "s_mov_b64 exec, -1\n\t" \
  "s_waitcnt lgkmcnt(0)\n\t" \
  "s_branch .Lat_y0\n\t" \
  ".Lat_x4:\n\t" \
  "v_lshlrev_b32 v209, 12, v233\n\t" \
  "v_bfrev_b32 v254, 1\n\t" \
  "s_mov_b64 exec, 15\n\t" \
  "ds_write_b32 v209, v243 offset:49152\n\t" \
  "s_mov_b64 exec, -1\n\t" \
  "s_waitcnt lgkmcnt(0)\n\t" \
  "s_branch .Lat_y4\n\t" \
  ".Lat_x1:\n\t" \
  "v_lshlrev_b32 v209, 12, v233\n\t" \
  "v_bfrev_b32 v254, 1\n\t" \
  "s_mov_b64 exec, 15\n\t" \
  "ds_write_b32 v209, v243 offset:49152\n\t" \
  "s_mov_b64 exec, -1\n\t" \
  "s_waitcnt lgkmcnt(0)\n\t" \
  "s_branch .Lat_y1\n\t" \
  ".Lat_x2:\n\t" \
  "v_lshlrev_b32 v209, 12, v233\n\t" \
  "v_bfrev_b32 v254, 1\n\t" \
  "s_mov_b64 exec, 15\n\t" \
  "ds_write_b32 v209, v243 offset:49152\n\t" \
  "s_mov_b64 exec, -1\n\t" \
  "s_waitcnt lgkmcnt(0)\n\t" \
  "s_branch .Lat_y2\n\t" \
  ".Lat_x3:\n\t" \
  "v_lshlrev_b32 v209, 12, v233\n\t" \
  "v_bfrev_b32 v254, 1\n\t" \
  "s_mov_b64 exec, 15\n\t" \
  "ds_write_b32 v209, v243 offset:49152\n\t" \
  "s_mov_b64 exec, -1\n\t" \
  "s_waitcnt lgkmcnt(0)\n\t" \
  "s_branch .Lat_y3\n\t" \
  ".subsection 2\n\t" \
  ".Lsf_go:\n\t" \
  "v_mfma_f32_16x16x32_bf16 v[28:31], a[156:159], a[224:227], v[28:31]\n\t" \
  "v_mov_b32 v216, 0x3f803f80\n\t" \
  "v_lshlrev_b32 v204, 7, v233\n\t" \
  "v_mfma_f32_16x16x32_bf16 v[28:31], a[92:95], a[240:243], v[28:31]\n\t" \
  "v_cmp_gt_u32 vcc, 16, v233\n\t" \
  "v_lshrrev_b32 v202, 3, v233\n\t" \
  "v_mfma_f32_16x16x32_bf16 v[24:27], a[152:155], a[224:227], v[24:27]\n\t" \
  "v_mul_u32_u24 v202, 11264, v202\n\t" \
  "v_add_u32 v202, v202, v204\n\t" \
  "v_mfma_f32_16x16x32_bf16 v[24:27], a[88:91], a[240:243], v[24:27]\n\t" \
  "v_bfrev_b32 v195, 1\n\t" \
  "v_mov_b32 v217, 0x3f803f80\n\t" \
  "v_mfma_f32_16x16x32_bf16 v[20:23], a[148:151], a[224:227], v[20:23]\n\t" \
  "v_cvt_pk_bf16_f32 v31, v30, v31\n\t" \
  "v_cvt_pk_bf16_f32 v30, v28, v29\n\t" \
  "v_mfma_f32_16x16x32_bf16 v[20:23], a[84:87], a[240:243], v[20:23]\n\t" \
  "v_cndmask_b32 v204, v195, v204, vcc\n\t" \
  "v_cndmask_b32 v202, v195, v202, vcc\n\t" \
  "v_mfma_f32_16x16x32_bf16 v[16:19], a[144:147], a[224:227], v[16:19]\n\t" \
  "v_cvt_pk_bf16_f32 v28, v24, v25\n\t" \
  "v_cvt_pk_bf16_f32 v29, v26, v27\n\t" \
  "v_mfma_f32_16x16x32_bf16 v[16:19], a[80:83], a[240:243], v[16:19]\n\t" \
  "v_permlane16_swap_b32 v28, v30\n\t" \
  "v_permlane16_swap_b32 v29, v31\n\t" \
  "v_mfma_f32_16x16x32_bf16 v[12:15], a[140:143], a[224:227], v[12:15]\n\t" \
  "v_cvt_pk_bf16_f32 v27, v22, v23\n\t" \
  "v_cvt_pk_bf16_f32 v26, v20, v21\n\t" \
  "v_mfma_f32_16x16x32_bf16 v[12:15], a[76:79], a[240:243], v[12:15]\n\t" \
  "v_mov_b32 v218, 0x3f803f80\n\t" \
  "v_mov_b32 v219, 0x3f803f80\n\t" \
  "v_mfma_f32_16x16x32_bf16 v[8:11], a[136:139], a[224:227], v[8:11]\n\t" \
  "v_cvt_pk_bf16_f32 v24, v16, v17\n\t" \
  "v_cvt_pk_bf16_f32 v25, v18, v19\n\t" \
  "v_mfma_f32_16x16x32_bf16 v[8:11], a[72:75], a[240:243], v[8:11]\n\t" \
  "v_permlane16_swap_b32 v24, v26\n\t" \
  "v_permlane16_swap_b32 v25, v27\n\t" \
  "v_mfma_f32_16x16x32_bf16 v[4:7], a[132:135], a[224:227], v[4:7]\n\t" \
  "v_cvt_pk_bf16_f32 v23, v14, v15\n\t" \
  "v_cvt_pk_bf16_f32 v22, v12, v13\n\t" \
  "v_mfma_f32_16x16x32_bf16 v[4:7], a[68:71], a[240:243], v[4:7]\n\t" \
  "v_mov_b32 v196, 1.0\n\t" \
  "v_mov_b32 v197, 1.0\n\t" \
  "v_mfma_f32_16x16x32_bf16 v[0:3], a[128:131], a[224:227], v[0:3]\n\t" \
  "v_cvt_pk_bf16_f32 v20, v8, v9\n\t" \
  "v_cvt_pk_bf16_f32 v21, v10, v11\n\t" \
  "v_mfma_f32_16x16x32_bf16 v[0:3], a[64:67], a[240:243], v[0:3]\n\t" \
  "v_permlane16_swap_b32 v20, v22\n\t" \
  "v_permlane16_swap_b32 v21, v23\n\t" \
  "v_mfma_f32_16x16x32_bf16 v[60:63], a[156:159], a[228:231], v[60:63]\n\t" \
  "v_cvt_pk_bf16_f32 v19, v6, v7\n\t" \
  "v_cvt_pk_bf16_f32 v18, v4, v5\n\t" \
  "v_mfma_f32_16x16x32_bf16 v[60:63], a[92:95], a[244:247], v[60:63]\n\t" \
  "v_mov_b32 v198, 1.0\n\t" \
  "v_mov_b32 v199, 1.0\n\t" \
  "v_mfma_f32_16x16x32_bf16 v[56:59], a[152:155], a[228:231], v[56:59]\n\t" \
  "v_cvt_pk_bf16_f32 v16, v0, v1\n\t" \
  "v_cvt_pk_bf16_f32 v17, v2, v3\n\t" \
  "v_mfma_f32_16x16x32_bf16 v[56:59], a[88:91], a[244:247], v[56:59]\n\t" \
  "v_permlane16_swap_b32 v16, v18\n\t" \
  "v_permlane16_swap_b32 v17, v19\n\t" \
  "v_mfma_f32_16x16x32_bf16 v[52:55], a[148:151], a[228:231], v[52:55]\n\t" \
  "v_cvt_pk_bf16_f32 v63, v62, v63\n\t" \
  "v_cvt_pk_bf16_f32 v62, v60, v61\n\t" \
  "v_mfma_f32_16x16x32_bf16 v[52:55], a[84:87], a[244:247], v[52:55]\n\t" \
  "v_lshlrev_b32 v242, 4, v233\n\t" \
  "s_mov_b32 s99, 0\n\t" \
  "v_mfma_f32_16x16x32_bf16 v[48:51], a[144:147], a[228:231], v[48:51]\n\t" \
  "v_cvt_pk_bf16_f32 v60, v56, v57\n\t" \
  "v_cvt_pk_bf16_f32 v61, v58, v59\n\t" \
  "v_mfma_f32_16x16x32_bf16 v[48:51], a[80:83], a[244:247], v[48:51]\n\t" \
  "v_permlane16_swap_b32 v60, v62\n\t" \
  "v_permlane16_swap_b32 v61, v63\n\t" \
  "v_mfma_f32_16x16x32_bf16 v[44:47], a[140:143], a[228:231], v[44:47]\n\t" \
  "v_cvt_pk_bf16_f32 v59, v54, v55\n\t" \
  "v_cvt_pk_bf16_f32 v58, v52, v53\n\t" \
  "v_mfma_f32_16x16x32_bf16 v[44:47], a[76:79], a[244:247], v[44:47]\n\t" \
  "v_add_u32 v237, 65536, v228\n\t" \
  "v_add_u32 v238, 65536, v229\n\t" \
  "v_mfma_f32_16x16x32_bf16 v[40:43], a[136:139], a[228:231], v[40:43]\n\t" \
  "v_cvt_pk_bf16_f32 v56, v48, v49\n\t" \
  "v_cvt_pk_bf16_f32 v57, v50, v51\n\t" \
  "v_mfma_f32_16x16x32_bf16 v[40:43], a[72:75], a[244:247], v[40:43]\n\t" \
  "v_permlane16_swap_b32 v56, v58\n\t" \
  "v_permlane16_swap_b32 v57, v59\n\t" \
  "v_mfma_f32_16x16x32_bf16 v[36:39], a[132:135], a[228:231], v[36:39]\n\t" \
  "v_cvt_pk_bf16_f32 v55, v46, v47\n\t" \
  "v_cvt_pk_bf16_f32 v54, v44, v45\n\t" \
  "v_mfma_f32_16x16x32_bf16 v[36:39], a[68:71], a[244:247], v[36:39]\n\t" \
  "v_mfma_f32_16x16x32_bf16 v[32:35], a[128:131], a[228:231], v[32:35]\n\t" \
  "v_mfma_f32_16x16x32_bf16 v[32:35], a[64:67], a[244:247], v[32:35]\n\t" \
  "v_cvt_pk_bf16_f32 v52, v40, v41\n\t" \
  "v_cvt_pk_bf16_f32 v53, v42, v43\n\t" \
  "v_mfma_f32_16x16x32_bf16 v[92:95], a[188:191], a[200:203], v[92:95]\n\t" \
  "v_permlane16_swap_b32 v52, v54\n\t" \
  "v_permlane16_swap_b32 v53, v55\n\t" \
  "v_mfma_f32_16x16x32_bf16 v[92:95], a[28:31], a[216:219], v[92:95]\n\t" \
  "v_cvt_pk_bf16_f32 v51, v38, v39\n\t" \
  "v_cvt_pk_bf16_f32 v50, v36, v37\n\t" \
  "v_mfma_f32_16x16x32_bf16 v[92:95], a[156:159], a[232:235], v[92:95]\n\t" \
  "v_cvt_pk_bf16_f32 v48, v32, v33\n\t" \
  "v_cvt_pk_bf16_f32 v49, v34, v35\n\t" \
  "v_mfma_f32_16x16x32_bf16 v[92:95], a[92:95], a[248:251], v[92:95]\n\t" \
  "v_permlane16_swap_b32 v48, v50\n\t" \
  "v_permlane16_swap_b32 v49, v51\n\t" \
  "v_mfma_f32_16x16x32_bf16 v[88:91], a[184:187], a[200:203], v[88:91]\n\t" \
  "v_mfma_f32_16x16x32_bf16 v[88:91], a[24:27], a[216:219], v[88:91]\n\t" \
  "v_mfma_f32_16x16x32_bf16 v[88:91], a[152:155], a[232:235], v[88:91]\n\t" \
  "v_mfma_f32_16x16x32_bf16 v[88:91], a[88:91], a[248:251], v[88:91]\n\t" \
  "s_waitcnt vmcnt(2)\n\t" \
  "s_barrier\n\t" \
  "v_mfma_f32_16x16x32_bf16 v[84:87], a[180:183], a[200:203], v[84:87]\n\t" \
  "v_cvt_pk_bf16_f32 v95, v94, v95\n\t" \
  "v_cvt_pk_bf16_f32 v94, v92, v93\n\t" \
  "v_mfma_f32_16x16x32_bf16 v[84:87], a[20:23], a[216:219], v[84:87]\n\t" \
  "ds_read_b128 a[32:35], v220 offset:0\n\t" \
  "ds_read_b128 a[36:39], v221 offset:0\n\t" \
  "v_mfma_f32_16x16x32_bf16 v[84:87], a[148:151], a[232:235], v[84:87]\n\t" \
  "v_cvt_pk_bf16_f32 v92, v88, v89\n\t" \
  "v_cvt_pk_bf16_f32 v93, v90, v91\n\t" \
  "v_mfma_f32_16x16x32_bf16 v[84:87], a[84:87], a[248:251], v[84:87]\n\t" \
  "v_permlane16_swap_b32 v92, v94\n\t" \
  "v_permlane16_swap_b32 v93, v95\n\t" \
  "v_mfma_f32_16x16x32_bf16 v[80:83], a[176:179], a[200:203], v[80:83]\n\t" \
  "ds_read_b128 a[40:43], v222 offset:0\n\t" \
  "s_add_u32 m0, %[m], 0\n\t" \
  "v_mfma_f32_16x16x32_bf16 v[80:83], a[16:19], a[216:219], v[80:83]\n\t" \
  "ds_read_b128 a[44:47], v223 offset:0\n\t" \
  "ds_read_b128 a[48:51], v220 offset:4096\n\t" \
  "v_mfma_f32_16x16x32_bf16 v[80:83], a[144:147], a[232:235], v[80:83]\n\t" \
  "ds_read_b128 a[52:55], v221 offset:4096\n\t" \
  "ds_read_b128 a[56:59], v222 offset:4096\n\t" \
  "v_mfma_f32_16x16x32_bf16 v[80:83], a[80:83], a[248:251], v[80:83]\n\t" \
  "v_cvt_pk_bf16_f32 v91, v86, v87\n\t" \
  "v_cvt_pk_bf16_f32 v90, v84, v85\n\t" \
  "v_mfma_f32_16x16x32_bf16 v[76:79], a[172:175], a[200:203], v[76:79]\n\t" \
  "s_xor_b32 s97, %[wob], 0\n\t" \
  "ds_read_b128 a[60:63], v223 offset:4096\n\t" \
  "v_mfma_f32_16x16x32_bf16 v[76:79], a[12:15], a[216:219], v[76:79]\n\t" \
  "ds_read_b128 a[96:99], v220 offset:8192\n\t" \
  "ds_read_b128 a[100:103], v221 offset:8192\n\t" \
  "v_mfma_f32_16x16x32_bf16 v[76:79], a[140:143], a[232:235], v[76:79]\n\t" \
  "v_cvt_pk_bf16_f32 v88, v80, v81\n\t" \
  "v_cvt_pk_bf16_f32 v89, v82, v83\n\t" \
  "v_mfma_f32_16x16x32_bf16 v[76:79], a[76:79], a[248:251], v[76:79]\n\t" \
  "v_permlane16_swap_b32 v88, v90\n\t" \
  "v_permlane16_swap_b32 v89, v91\n\t" \
  "v_mfma_f32_16x16x32_bf16 v[72:75], a[168:171], a[200:203], v[72:75]\n\t" \
  "ds_read_b128 a[104:107], v222 offset:8192\n\t" \
  "buffer_load_dwordx4 v240, %[wo], s97 offen offset:0 lds\n\t" \
  "v_mfma_f32_16x16x32_bf16 v[72:75], a[8:11], a[216:219], v[72:75]\n\t" \
  "ds_read_b128 a[108:111], v223 offset:8192\n\t" \
  "ds_read_b128 a[112:115], v220 offset:12288\n\t" \
  "v_mfma_f32_16x16x32_bf16 v[72:75], a[136:139], a[232:235], v[72:75]\n\t" \
  "ds_read_b128 a[116:119], v221 offset:12288\n\t" \
  "ds_read_b128 a[120:123], v222 offset:12288\n\t" \
  "v_mfma_f32_16x16x32_bf16 v[72:75], a[72:75], a[248:251], v[72:75]\n\t" \
  "v_cvt_pk_bf16_f32 v87, v78, v79\n\t" \
  "v_cvt_pk_bf16_f32 v86, v76, v77\n\t" \
  "v_mfma_f32_16x16x32_bf16 v[68:71], a[164:167], a[200:203], v[68:71]\n\t" \
  "s_add_u32 s97, s97, 7168\n\t" \
  "buffer_load_dwordx4 v241, %[wo], s97 offen offset:1024 lds\n\t" \
  "v_mfma_f32_16x16x32_bf16 v[68:71], a[4:7], a[216:219], v[68:71]\n\t" \
  "ds_read_b128 a[124:127], v223 offset:12288\n\t" \
  "v_mfma_f32_16x16x32_bf16 v[68:71], a[132:135], a[232:235], v[68:71]\n\t" \
  "v_cvt_pk_bf16_f32 v84, v72, v73\n\t" \
  "v_cvt_pk_bf16_f32 v85, v74, v75\n\t" \
  "v_mfma_f32_16x16x32_bf16 v[68:71], a[68:71], a[248:251], v[68:71]\n\t" \
  "v_permlane16_swap_b32 v84, v86\n\t" \
  "v_permlane16_swap_b32 v85, v87\n\t" \
  "v_mfma_f32_16x16x32_bf16 v[64:67], a[160:163], a[200:203], v[64:67]\n\t" \
  "s_add_u32 s97, s97, 7168\n\t" \
  "buffer_load_dwordx4 v240, %[wo], s97 offen offset:2048 lds\n\t" \
  "v_mfma_f32_16x16x32_bf16 v[64:67], a[0:3], a[216:219], v[64:67]\n\t" \
  "v_mfma_f32_16x16x32_bf16 v[64:67], a[128:131], a[232:235], v[64:67]\n\t" \
  "v_mfma_f32_16x16x32_bf16 v[64:67], a[64:67], a[248:251], v[64:67]\n\t" \
  "v_cvt_pk_bf16_f32 v83, v70, v71\n\t" \
  "v_cvt_pk_bf16_f32 v82, v68, v69\n\t" \
  "s_waitcnt lgkmcnt(0)\n\t" \
  "s_barrier\n\t" \
  "v_mfma_f32_16x16x32_bf16 v[0:3], a[32:35], v[16:19], 0\n\t" \
  "s_add_u32 s97, s97, 7168\n\t" \
  "buffer_load_dwordx4 v241, %[wo], s97 offen offset:3072 lds\n\t" \
  "v_mfma_f32_16x16x32_bf16 v[0:3], a[36:39], v[20:23], v[0:3]\n\t" \
  "v_cvt_pk_bf16_f32 v80, v64, v65\n\t" \
  "v_cvt_pk_bf16_f32 v81, v66, v67\n\t" \
  "v_mfma_f32_16x16x32_bf16 v[0:3], a[40:43], v[24:27], v[0:3]\n\t" \
  "v_permlane16_swap_b32 v80, v82\n\t" \
  "v_permlane16_swap_b32 v81, v83\n\t" \
  "v_mfma_f32_16x16x32_bf16 v[0:3], a[44:47], v[28:31], v[0:3]\n\t" \
  "s_add_u32 m0, %[m], 16384\n\t" \
  "s_xor_b32 s97, %[wob], 131072\n\t" \
  "v_mfma_f32_16x16x32_bf16 v[32:35], a[32:35], v[48:51], 0\n\t" \
  "v_mfma_f32_16x16x32_bf16 v[32:35], a[36:39], v[52:55], v[32:35]\n\t" \
  "v_mfma_f32_16x16x32_bf16 v[32:35], a[40:43], v[56:59], v[32:35]\n\t" \
  "v_mfma_f32_16x16x32_bf16 v[32:35], a[44:47], v[60:63], v[32:35]\n\t" \
  "v_mfma_f32_16x16x32_bf16 v[124:127], a[188:191], a[204:207], v[124:127]\n\t" \
  "v_mfma_f32_16x16x32_bf16 v[124:127], a[28:31], a[220:223], v[124:127]\n\t" \
  "v_max3_f32 v194, v0, v1, v2\n\t" \
  "v_max_f32 v194, v194, v3\n\t" \
  "v_mfma_f32_16x16x32_bf16 v[124:127], a[156:159], a[236:239], v[124:127]\n\t" \
  "v_mov_b32 v195, v194\n\t" \
  "buffer_load_dwordx4 v240, %[wo], s97 offen offset:0 lds\n\t" \
  "v_mfma_f32_16x16x32_bf16 v[124:127], a[92:95], a[252:255], v[124:127]\n\t" \
  "v_permlane32_swap_b32 v194, v195\n\t" \
  "v_max_f32 v194, v194, v195\n\t" \
  "v_mfma_f32_16x16x32_bf16 v[120:123], a[184:187], a[204:207], v[120:123]\n\t" \
  "v_mov_b32 v195, v194\n\t" \
  "v_mfma_f32_16x16x32_bf16 v[120:123], a[24:27], a[220:223], v[120:123]\n\t" \
  "v_mfma_f32_16x16x32_bf16 v[120:123], a[152:155], a[236:239], v[120:123]\n\t" \
  "v_permlane16_swap_b32 v194, v195\n\t" \
  "v_max_f32 v194, v194, v195\n\t" \
  "v_mfma_f32_16x16x32_bf16 v[120:123], a[88:91], a[252:255], v[120:123]\n\t" \
  "v_cvt_pk_bf16_f32 v127, v126, v127\n\t" \
  "v_cvt_pk_bf16_f32 v126, v124, v125\n\t" \
  "v_mfma_f32_16x16x32_bf16 v[116:119], a[180:183], a[204:207], v[116:119]\n\t" \
  "v_sub_f32 v176, 0xc2000000, v194\n\t" \
  "v_sub_f32 v177, 0xc2000000, v194\n\t" \
  "v_mfma_f32_16x16x32_bf16 v[116:119], a[20:23], a[220:223], v[116:119]\n\t" \
  "v_sub_f32 v178, 0xc2000000, v194\n\t" \
  "v_sub_f32 v179, 0xc2000000, v194\n\t" \
  "v_mfma_f32_16x16x32_bf16 v[116:119], a[148:151], a[236:239], v[116:119]\n\t" \
  "v_cvt_pk_bf16_f32 v124, v120, v121\n\t" \
  "v_cvt_pk_bf16_f32 v125, v122, v123\n\t" \
  "v_mfma_f32_16x16x32_bf16 v[116:119], a[84:87], a[252:255], v[116:119]\n\t" \
  "v_permlane16_swap_b32 v124, v126\n\t" \
  "v_permlane16_swap_b32 v125, v127\n\t" \
  "v_mfma_f32_16x16x32_bf16 v[112:115], a[176:179], a[204:207], v[112:115]\n\t" \
  "v_add_f32 v0, v0, v176\n\t" \
  "v_add_f32 v1, v1, v176\n\t" \
  "v_mfma_f32_16x16x32_bf16 v[112:115], a[16:19], a[220:223], v[112:115]\n\t" \
  "v_add_f32 v2, v2, v176\n\t" \
  "v_add_f32 v3, v3, v176\n\t" \
  "v_mfma_f32_16x16x32_bf16 v[112:115], a[144:147], a[236:239], v[112:115]\n\t" \
  "v_max3_f32 v194, v32, v33, v34\n\t" \
  "v_max_f32 v194, v194, v35\n\t" \
  "v_mfma_f32_16x16x32_bf16 v[112:115], a[80:83], a[252:255], v[112:115]\n\t" \
  "v_cvt_pk_bf16_f32 v123, v118, v119\n\t" \
  "v_cvt_pk_bf16_f32 v122, v116, v117\n\t" \
  "v_mfma_f32_16x16x32_bf16 v[108:111], a[172:175], a[204:207], v[108:111]\n\t" \
  "v_mov_b32 v195, v194\n\t" \
  "v_mfma_f32_16x16x32_bf16 v[108:111], a[12:15], a[220:223], v[108:111]\n\t" \
  "s_add_u32 s97, s97, 7168\n\t" \
  "buffer_load_dwordx4 v241, %[wo], s97 offen offset:1024 lds\n\t" \
  "v_mfma_f32_16x16x32_bf16 v[108:111], a[140:143], a[236:239], v[108:111]\n\t" \
  "v_cvt_pk_bf16_f32 v120, v112, v113\n\t" \
  "v_cvt_pk_bf16_f32 v121, v114, v115\n\t" \
  "v_mfma_f32_16x16x32_bf16 v[108:111], a[76:79], a[252:255], v[108:111]\n\t" \
  "v_permlane16_swap_b32 v120, v122\n\t" \
  "v_permlane16_swap_b32 v121, v123\n\t" \
  "v_mfma_f32_16x16x32_bf16 v[104:107], a[168:171], a[204:207], v[104:107]\n\t" \
  "v_permlane32_swap_b32 v194, v195\n\t" \
  "v_max_f32 v194, v194, v195\n\t" \
  "v_mfma_f32_16x16x32_bf16 v[104:107], a[8:11], a[220:223], v[104:107]\n\t" \
  "v_mov_b32 v195, v194\n\t" \
  "v_mfma_f32_16x16x32_bf16 v[104:107], a[136:139], a[236:239], v[104:107]\n\t" \
  "s_add_u32 s97, s97, 7168\n\t" \
  "buffer_load_dwordx4 v240, %[wo], s97 offen offset:2048 lds\n\t" \
  "v_mfma_f32_16x16x32_bf16 v[104:107], a[72:75], a[252:255], v[104:107]\n\t" \
  "v_cvt_pk_bf16_f32 v119, v110, v111\n\t" \
  "v_cvt_pk_bf16_f32 v118, v108, v109\n\t" \
  "s_add_u32 s97, s97, 7168\n\t" \
  "buffer_load_dwordx4 v241, %[wo], s97 offen offset:3072 lds\n\t" \
  "v_mfma_f32_16x16x32_bf16 v[100:103], a[164:167], a[204:207], v[100:103]\n\t" \
  "v_permlane16_swap_b32 v194, v195\n\t" \
  "v_max_f32 v194, v194, v195\n\t" \
  "v_mfma_f32_16x16x32_bf16 v[100:103], a[4:7], a[220:223], v[100:103]\n\t" \
  "v_cvt_pk_bf16_f32 v116, v104, v105\n\t" \
  "v_cvt_pk_bf16_f32 v117, v106, v107\n\t" \
  "v_mfma_f32_16x16x32_bf16 v[100:103], a[132:135], a[236:239], v[100:103]\n\t" \
  "v_permlane16_swap_b32 v116, v118\n\t" \
  "v_permlane16_swap_b32 v117, v119\n\t" \
  "v_mfma_f32_16x16x32_bf16 v[100:103], a[68:71], a[252:255], v[100:103]\n\t" \
  "v_sub_f32 v180, 0xc2000000, v194\n\t" \
  "v_sub_f32 v181, 0xc2000000, v194\n\t" \
  "v_mfma_f32_16x16x32_bf16 v[96:99], a[160:163], a[204:207], v[96:99]\n\t" \
  "v_sub_f32 v182, 0xc2000000, v194\n\t" \
  "v_sub_f32 v183, 0xc2000000, v194\n\t" \
  "v_mfma_f32_16x16x32_bf16 v[96:99], a[0:3], a[220:223], v[96:99]\n\t" \
  "v_add_f32 v32, v32, v180\n\t" \
  "v_add_f32 v33, v33, v180\n\t" \
  "v_mfma_f32_16x16x32_bf16 v[96:99], a[128:131], a[236:239], v[96:99]\n\t" \
  "v_add_f32 v34, v34, v180\n\t" \
  "v_add_f32 v35, v35, v180\n\t" \
  "v_mfma_f32_16x16x32_bf16 v[96:99], a[64:67], a[252:255], v[96:99]\n\t" \
  "v_cvt_pk_bf16_f32 v115, v102, v103\n\t" \
  "v_cvt_pk_bf16_f32 v114, v100, v101\n\t" \
  "v_mfma_f32_16x16x32_bf16 v[4:7], a[48:51], v[16:19], v[176:179]\n\t" \
  "s_add_u32 s98, %[pf4], 28672\n\t" \
  "buffer_load_dword %[pfd], v245, %[sU], s98 offen\n\t" \
  "v_mfma_f32_16x16x32_bf16 v[4:7], a[52:55], v[20:23], v[4:7]\n\t" \
  "s_add_u32 m0, %[m], 49152\n\t" \
  "s_add_u32 s96, %[sofk], 32768\n\t" \
  "v_mfma_f32_16x16x32_bf16 v[4:7], a[56:59], v[24:27], v[4:7]\n\t" \
  "v_cvt_pk_bf16_f32 v112, v96, v97\n\t" \
  "v_cvt_pk_bf16_f32 v113, v98, v99\n\t" \
  "v_mfma_f32_16x16x32_bf16 v[4:7], a[60:63], v[28:31], v[4:7]\n\t" \
  "v_permlane16_swap_b32 v112, v114\n\t" \
  "v_permlane16_swap_b32 v113, v115\n\t" \
  "v_mfma_f32_16x16x32_bf16 v[8:11], a[96:99], v[16:19], v[176:179]\n\t" \
  "ds_read_b128 a[192:195], v220 offset:16384\n\t" \
  "ds_read_b128 a[196:199], v221 offset:16384\n\t" \
  "v_mfma_f32_16x16x32_bf16 v[8:11], a[100:103], v[20:23], v[8:11]\n\t" \
  "ds_read_b128 a[200:203], v222 offset:16384\n\t" \
  "buffer_load_dwordx4 v224, %[sK], s96 offen offset:0 lds\n\t" \
  "v_mfma_f32_16x16x32_bf16 v[8:11], a[104:107], v[24:27], v[8:11]\n\t" \
  "v_max3_f32 v192, v4, v5, v6\n\t" \
  "ds_read_b128 a[204:207], v223 offset:16384\n\t" \
  "v_mfma_f32_16x16x32_bf16 v[8:11], a[108:111], v[28:31], v[8:11]\n\t" \
  "ds_read_b128 a[208:211], v220 offset:20480\n\t" \
  "ds_read_b128 a[212:215], v221 offset:20480\n\t" \
  "v_mfma_f32_16x16x32_bf16 v[12:15], a[112:115], v[16:19], v[176:179]\n\t" \
  "v_mfma_f32_16x16x32_bf16 v[12:15], a[116:119], v[20:23], v[12:15]\n\t" \
  "v_mfma_f32_16x16x32_bf16 v[12:15], a[120:123], v[24:27], v[12:15]\n\t" \
  "ds_read_b128 a[216:219], v222 offset:20480\n\t" \
  "buffer_load_dwordx4 v225, %[sK], s96 offen offset:1024 lds\n\t" \
  "v_mfma_f32_16x16x32_bf16 v[12:15], a[124:127], v[28:31], v[12:15]\n\t" \
  "v_max3_f32 v203, v7, v8, v9\n\t" \
  "v_max3_f32 v192, v192, v10, v11\n\t" \
  "v_mfma_f32_16x16x32_bf16 v[64:67], a[32:35], v[80:83], 0\n\t" \
  "ds_read_b128 a[220:223], v223 offset:20480\n\t" \
  "ds_read_b128 a[224:227], v220 offset:24576\n\t" \
  "v_mfma_f32_16x16x32_bf16 v[64:67], a[36:39], v[84:87], v[64:67]\n\t" \
  "v_mfma_f32_16x16x32_bf16 v[64:67], a[40:43], v[88:91], v[64:67]\n\t" \
  "v_mfma_f32_16x16x32_bf16 v[64:67], a[44:47], v[92:95], v[64:67]\n\t" \
  "v_max3_f32 v203, v203, v12, v13\n\t" \
  "v_max3_f32 v192, v192, v14, v15\n\t" \
  "v_mfma_f32_16x16x32_bf16 v[36:39], a[48:51], v[48:51], v[180:183]\n\t" \
  "v_max_f32 v192, v192, v203\n\t" \
  "ds_read_b128 a[228:231], v221 offset:24576\n\t" \
  "v_mfma_f32_16x16x32_bf16 v[36:39], a[52:55], v[52:55], v[36:39]\n\t" \
  "v_cmp_lt_f32 vcc, 0x42800000, v192\n\t" \
  "buffer_load_dwordx4 v226, %[sK], s96 offen offset:2048 lds\n\t" \
  "v_mfma_f32_16x16x32_bf16 v[36:39], a[56:59], v[56:59], v[36:39]\n\t" \
  "s_cbranch_vccnz .Lsf_c00\n\t" \
  ".Lsf_r00:\n\t" \
  "v_max3_f32 v194, v64, v65, v66\n\t" \
  "v_mfma_f32_16x16x32_bf16 v[36:39], a[60:63], v[60:63], v[36:39]\n\t" \
  "v_max_f32 v194, v194, v67\n\t" \
  "v_mov_b32 v195, v194\n\t" \
  "v_mfma_f32_16x16x32_bf16 v[40:43], a[96:99], v[48:51], v[180:183]\n\t" \
  "ds_read_b128 a[232:235], v222 offset:24576\n\t" \
  "v_permlane32_swap_b32 v194, v195\n\t" \
  "v_mfma_f32_16x16x32_bf16 v[40:43], a[100:103], v[52:55], v[40:43]\n\t" \
  "v_max_f32 v194, v194, v195\n\t" \
  "v_mov_b32 v195, v194\n\t" \
  "v_mfma_f32_16x16x32_bf16 v[40:43], a[104:107], v[56:59], v[40:43]\n\t" \
  "ds_read_b128 a[236:239], v223 offset:24576\n\t" \
  "v_permlane16_swap_b32 v194, v195\n\t" \
  "v_mfma_f32_16x16x32_bf16 v[40:43], a[108:111], v[60:63], v[40:43]\n\t" \
  "v_max_f32 v194, v194, v195\n\t" \
  "v_sub_f32 v184, 0xc2000000, v194\n\t" \
  "v_mfma_f32_16x16x32_bf16 v[44:47], a[112:115], v[48:51], v[180:183]\n\t" \
  "v_sub_f32 v185, 0xc2000000, v194\n\t" \
  "v_sub_f32 v186, 0xc2000000, v194\n\t" \
  "v_mfma_f32_16x16x32_bf16 v[44:47], a[116:119], v[52:55], v[44:47]\n\t" \
  "v_sub_f32 v187, 0xc2000000, v194\n\t" \
  "v_add_f32 v64, v64, v184\n\t" \
  "v_mfma_f32_16x16x32_bf16 v[44:47], a[120:123], v[56:59], v[44:47]\n\t" \
  "v_add_f32 v65, v65, v184\n\t" \
  "v_add_f32 v66, v66, v184\n\t" \
  "v_mfma_f32_16x16x32_bf16 v[44:47], a[124:127], v[60:63], v[44:47]\n\t" \
  "v_add_f32 v67, v67, v184\n\t" \
  "ds_read_b128 a[240:243], v220 offset:28672\n\t" \
  "v_mfma_f32_16x16x32_bf16 v[96:99], a[32:35], v[112:115], 0\n\t" \
  "v_max3_f32 v192, v36, v37, v38\n\t" \
  "ds_read_b128 a[244:247], v221 offset:28672\n\t" \
  "v_mfma_f32_16x16x32_bf16 v[96:99], a[36:39], v[116:119], v[96:99]\n\t" \
  "buffer_load_dwordx4 v227, %[sK], s96 offen offset:3072 lds\n\t" \
  "ds_read_b128 a[248:251], v222 offset:28672\n\t" \
  "v_mfma_f32_16x16x32_bf16 v[96:99], a[40:43], v[120:123], v[96:99]\n\t" \
  "v_max3_f32 v203, v39, v40, v41\n\t" \
  "v_max3_f32 v192, v192, v42, v43\n\t" \
  "v_mfma_f32_16x16x32_bf16 v[96:99], a[44:47], v[124:127], v[96:99]\n\t" \
  "ds_read_b128 a[252:255], v223 offset:28672\n\t" \
  "ds_read_b64_tr_b16 a[128:129], v228 offset:0\n\t" \
  "v_mfma_f32_16x16x32_bf16 v[68:71], a[48:51], v[80:83], v[184:187]\n\t" \
  "v_max3_f32 v203, v203, v44, v45\n\t" \
  "v_max3_f32 v192, v192, v46, v47\n\t" \
  "v_mfma_f32_16x16x32_bf16 v[68:71], a[52:55], v[84:87], v[68:71]\n\t" \
  "v_max_f32 v192, v192, v203\n\t" \
  "s_add_u32 m0, %[m], 147456\n\t" \
  "v_mfma_f32_16x16x32_bf16 v[68:71], a[56:59], v[88:91], v[68:71]\n\t" \
  "v_cmp_lt_f32 vcc, 0x42800000, v192\n\t" \
  "ds_read_b64_tr_b16 a[130:131], v228 offset:4096\n\t" \
  "v_mfma_f32_16x16x32_bf16 v[68:71], a[60:63], v[92:95], v[68:71]\n\t" \
  "s_cbranch_vccnz .Lsf_c01\n\t" \
  ".Lsf_r01:\n\t" \
  "v_max3_f32 v194, v96, v97, v98\n\t" \
  "v_mfma_f32_16x16x32_bf16 v[72:75], a[96:99], v[80:83], v[184:187]\n\t" \
  "v_max_f32 v194, v194, v99\n\t" \
  "v_mov_b32 v195, v194\n\t" \
  "v_mfma_f32_16x16x32_bf16 v[72:75], a[100:103], v[84:87], v[72:75]\n\t" \
  "ds_read_b64_tr_b16 a[132:133], v228 offset:8192\n\t" \
  "v_permlane32_swap_b32 v194, v195\n\t" \
  "v_mfma_f32_16x16x32_bf16 v[72:75], a[104:107], v[88:91], v[72:75]\n\t" \
  "v_max_f32 v194, v194, v195\n\t" \
  "v_mov_b32 v195, v194\n\t" \
  "v_mfma_f32_16x16x32_bf16 v[72:75], a[108:111], v[92:95], v[72:75]\n\t" \
  "s_add_u32 s96, %[sofk], 16384\n\t" \
  "v_permlane16_swap_b32 v194, v195\n\t" \
  "v_mfma_f32_16x16x32_bf16 v[76:79], a[112:115], v[80:83], v[184:187]\n\t" \
  "v_max_f32 v194, v194, v195\n\t" \
  "v_sub_f32 v188, 0xc2000000, v194\n\t" \
  "v_mfma_f32_16x16x32_bf16 v[76:79], a[116:119], v[84:87], v[76:79]\n\t" \
  "v_sub_f32 v189, 0xc2000000, v194\n\t" \
  "v_sub_f32 v190, 0xc2000000, v194\n\t" \
  "v_mfma_f32_16x16x32_bf16 v[76:79], a[120:123], v[88:91], v[76:79]\n\t" \
  "v_sub_f32 v191, 0xc2000000, v194\n\t" \
  "v_add_f32 v96, v96, v188\n\t" \
  "v_mfma_f32_16x16x32_bf16 v[76:79], a[124:127], v[92:95], v[76:79]\n\t" \
  "v_add_f32 v97, v97, v188\n\t" \
  "v_add_f32 v98, v98, v188\n\t" \
  "v_mfma_f32_16x16x32_bf16 v[100:103], a[48:51], v[112:115], v[188:191]\n\t" \
  "v_add_f32 v99, v99, v188\n\t" \
  "ds_read_b64_tr_b16 a[134:135], v228 offset:12288\n\t" \
  "v_mfma_f32_16x16x32_bf16 v[100:103], a[52:55], v[116:119], v[100:103]\n\t" \
  "v_max3_f32 v192, v68, v69, v70\n\t" \
  "ds_read_b64_tr_b16 a[136:137], v229 offset:0\n\t" \
  "v_mfma_f32_16x16x32_bf16 v[100:103], a[56:59], v[120:123], v[100:103]\n\t" \
  "v_max3_f32 v203, v71, v72, v73\n\t" \
  "v_max3_f32 v192, v192, v74, v75\n\t" \
  "v_mfma_f32_16x16x32_bf16 v[100:103], a[60:63], v[124:127], v[100:103]\n\t" \
  "buffer_load_dwordx4 v230, %[sV], s96 offen offset:0 lds\n\t" \
  "ds_read_b64_tr_b16 a[138:139], v229 offset:4096\n\t" \
  "v_mfma_f32_16x16x32_bf16 v[104:107], a[96:99], v[112:115], v[188:191]\n\t" \
  "ds_read_b64_tr_b16 a[140:141], v229 offset:8192\n\t" \
  "v_max3_f32 v203, v203, v76, v77\n\t" \
  "v_mfma_f32_16x16x32_bf16 v[104:107], a[100:103], v[116:119], v[104:107]\n\t" \
  "v_max3_f32 v192, v192, v78, v79\n\t" \
  "v_max_f32 v192, v192, v203\n\t" \
  "v_mfma_f32_16x16x32_bf16 v[104:107], a[104:107], v[120:123], v[104:107]\n\t" \
  "v_cmp_lt_f32 vcc, 0x42800000, v192\n\t" \
  "ds_read_b64_tr_b16 a[142:143], v229 offset:12288\n\t" \
  "v_mfma_f32_16x16x32_bf16 v[104:107], a[108:111], v[124:127], v[104:107]\n\t" \
  "s_cbranch_vccnz .Lsf_c02\n\t" \
  ".Lsf_r02:\n\t" \
  "buffer_load_dwordx4 v231, %[sV], s96 offen offset:1024 lds\n\t" \
  "v_mfma_f32_16x16x32_bf16 v[108:111], a[112:115], v[112:115], v[188:191]\n\t" \
  "v_max3_f32 v192, v100, v101, v102\n\t" \
  "ds_read_b64_tr_b16 a[144:145], v228 offset:256\n\t" \
  "v_mfma_f32_16x16x32_bf16 v[108:111], a[116:119], v[116:119], v[108:111]\n\t" \
  "ds_read_b64_tr_b16 a[146:147], v228 offset:4352\n\t" \
  "buffer_load_dwordx4 v230, %[sV], s96 offen offset:2048 lds\n\t" \
  "v_mfma_f32_16x16x32_bf16 v[108:111], a[120:123], v[120:123], v[108:111]\n\t" \
  "v_max3_f32 v203, v103, v104, v105\n\t" \
  "v_max3_f32 v192, v192, v106, v107\n\t" \
  "v_mfma_f32_16x16x32_bf16 v[108:111], a[124:127], v[124:127], v[108:111]\n\t" \
  "v_exp_f32 v0, v0\n\t" \
  "v_exp_f32 v1, v1\n\t" \
  "v_exp_f32 v2, v2\n\t" \
  "v_exp_f32 v3, v3\n\t" \
  "v_cvt_pk_bf16_f32 v128, v0, v1\n\t" \
  "v_cvt_pk_bf16_f32 v129, v2, v3\n\t" \
  "s_waitcnt lgkmcnt(10)\n\t" \
  "v_mfma_f32_16x16x32_bf16 v[0:3], a[192:195], v[16:19], v[176:179]\n\t" \
  "ds_read_b64_tr_b16 a[148:149], v228 offset:8448\n\t" \
  "v_max3_f32 v203, v203, v108, v109\n\t" \
  "v_mfma_f32_16x16x32_bf16 v[0:3], a[196:199], v[20:23], v[0:3]\n\t" \
  "v_max3_f32 v192, v192, v110, v111\n\t" \
  "v_max_f32 v192, v192, v203\n\t" \
  "v_mfma_f32_16x16x32_bf16 v[0:3], a[200:203], v[24:27], v[0:3]\n\t" \
  "v_cmp_lt_f32 vcc, 0x42800000, v192\n\t" \
  "ds_read_b64_tr_b16 a[150:151], v228 offset:12544\n\t" \
  "v_mfma_f32_16x16x32_bf16 v[0:3], a[204:207], v[28:31], v[0:3]\n\t" \
  "s_cbranch_vccnz .Lsf_c03\n\t" \
  ".Lsf_r03:\n\t" \
  "buffer_load_dwordx4 v231, %[sV], s96 offen offset:3072 lds\n\t" \
  "v_exp_f32 v4, v4\n\t" \
  "v_exp_f32 v5, v5\n\t" \
  "v_exp_f32 v6, v6\n\t" \
  "v_exp_f32 v7, v7\n\t" \
  "v_cvt_pk_bf16_f32 v130, v4, v5\n\t" \
  "v_cvt_pk_bf16_f32 v131, v6, v7\n\t" \
  "v_mfma_f32_16x16x32_bf16 v[4:7], a[220:223], v[28:31], v[176:179]\n\t" \
  "s_cmp_lg_u32 s99, 0\n\t" \
  "s_cbranch_scc1 .Lsf_p0\n\t" \
  ".Lsf_q0:\n\t" \
  "v_mfma_f32_16x16x32_bf16 v[4:7], a[216:219], v[24:27], v[4:7]\n\t" \
  "ds_read_b64_tr_b16 a[152:153], v229 offset:256\n\t" \
  "v_max3_f32 v192, v0, v1, v2\n\t" \
  "v_mfma_f32_16x16x32_bf16 v[4:7], a[212:215], v[20:23], v[4:7]\n\t" \
  "ds_read_b64_tr_b16 a[154:155], v229 offset:4352\n\t" \
  "ds_read_b64_tr_b16 a[156:157], v229 offset:8448\n\t" \
  "v_mfma_f32_16x16x32_bf16 v[4:7], a[208:211], v[16:19], v[4:7]\n\t" \
  "v_exp_f32 v8, v8\n\t" \
  "v_exp_f32 v9, v9\n\t" \
  "v_exp_f32 v10, v10\n\t" \
  "v_exp_f32 v11, v11\n\t" \
  "v_cvt_pk_bf16_f32 v132, v8, v9\n\t" \
  "v_cvt_pk_bf16_f32 v133, v10, v11\n\t" \
  "v_mfma_f32_16x16x32_bf16 v[8:11], a[224:227], v[16:19], v[176:179]\n\t" \
  "s_add_u32 s98, %[pf4], 43008\n\t" \
  "buffer_load_dword %[pfd], v245, %[sU], s98 offen\n\t" \
  "v_mfma_f32_16x16x32_bf16 v[8:11], a[228:231], v[20:23], v[8:11]\n\t" \
  "v_exp_f32 v12, v12\n\t" \
  "v_mfma_f32_16x16x32_bf16 v[8:11], a[232:235], v[24:27], v[8:11]\n\t" \
  "v_max3_f32 v203, v3, v4, v5\n\t" \
  "v_max3_f32 v192, v192, v6, v7\n\t" \
  "v_mfma_f32_16x16x32_bf16 v[8:11], a[236:239], v[28:31], v[8:11]\n\t" \
  "v_exp_f32 v13, v13\n\t" \
  "v_exp_f32 v14, v14\n\t" \
  "v_exp_f32 v15, v15\n\t" \
  "v_cvt_pk_bf16_f32 v134, v12, v13\n\t" \
  "v_cvt_pk_bf16_f32 v135, v14, v15\n\t" \
  "v_mfma_f32_16x16x32_bf16 v[12:15], a[252:255], v[28:31], v[176:179]\n\t" \
  "v_exp_f32 v32, v32\n\t" \
  "v_mfma_f32_16x16x32_bf16 v[12:15], a[248:251], v[24:27], v[12:15]\n\t" \
  "ds_read_b64_tr_b16 a[158:159], v229 offset:12544\n\t" \
  "v_max3_f32 v203, v203, v8, v9\n\t" \
  "v_mfma_f32_16x16x32_bf16 v[12:15], a[244:247], v[20:23], v[12:15]\n\t" \
  "v_max3_f32 v192, v192, v10, v11\n\t" \
  "ds_read_b64_tr_b16 a[160:161], v228 offset:512\n\t" \
  "v_mfma_f32_16x16x32_bf16 v[12:15], a[240:243], v[16:19], v[12:15]\n\t" \
  "s_add_u32 s98, %[pf4], 0\n\t" \
  "buffer_load_dword %[pfd], v245, %[sG], s98 offen\n\t" \
  "v_exp_f32 v33, v33\n\t" \
  "v_exp_f32 v34, v34\n\t" \
  "v_exp_f32 v35, v35\n\t" \
  "v_cvt_pk_bf16_f32 v136, v32, v33\n\t" \
  "v_cvt_pk_bf16_f32 v137, v34, v35\n\t" \
  "v_mfma_f32_16x16x32_bf16 v[32:35], a[192:195], v[48:51], v[180:183]\n\t" \
  "v_exp_f32 v36, v36\n\t" \
  "v_mfma_f32_16x16x32_bf16 v[32:35], a[196:199], v[52:55], v[32:35]\n\t" \
  "v_max3_f32 v203, v203, v12, v13\n\t" \
  "v_max3_f32 v192, v192, v14, v15\n\t" \
  "v_mfma_f32_16x16x32_bf16 v[32:35], a[200:203], v[56:59], v[32:35]\n\t" \
  "v_max_f32 v192, v192, v203\n\t" \
  "ds_read_b64_tr_b16 a[162:163], v228 offset:4608\n\t" \
  "v_mfma_f32_16x16x32_bf16 v[32:35], a[204:207], v[60:63], v[32:35]\n\t" \
  "v_cmp_lt_f32 vcc, 0x42800000, v192\n\t" \
  "ds_read_b64_tr_b16 a[164:165], v228 offset:8704\n\t" \
  "v_exp_f32 v37, v37\n\t" \
  "v_exp_f32 v38, v38\n\t" \
  "v_exp_f32 v39, v39\n\t" \
  "v_cvt_pk_bf16_f32 v138, v36, v37\n\t" \
  "v_cvt_pk_bf16_f32 v139, v38, v39\n\t" \
  "v_mfma_f32_16x16x32_bf16 v[36:39], a[220:223], v[60:63], v[180:183]\n\t" \
  "s_cbranch_vccnz .Lsf_c10\n\t" \
  ".Lsf_r10:\n\t" \
  "v_max3_f32 v192, v32, v33, v34\n\t" \
  "v_mfma_f32_16x16x32_bf16 v[36:39], a[216:219], v[56:59], v[36:39]\n\t" \
  "v_exp_f32 v40, v40\n\t" \
  "v_mfma_f32_16x16x32_bf16 v[36:39], a[212:215], v[52:55], v[36:39]\n\t" \
  "s_add_u32 s98, %[pf4], 14336\n\t" \
  "buffer_load_dword %[pfd], v245, %[sG], s98 offen\n\t" \
  "v_mfma_f32_16x16x32_bf16 v[36:39], a[208:211], v[48:51], v[36:39]\n\t" \
  "v_exp_f32 v41, v41\n\t" \
  "v_exp_f32 v42, v42\n\t" \
  "v_exp_f32 v43, v43\n\t" \
  "v_cvt_pk_bf16_f32 v140, v40, v41\n\t" \
  "v_cvt_pk_bf16_f32 v141, v42, v43\n\t" \
  "v_mfma_f32_16x16x32_bf16 v[40:43], a[224:227], v[48:51], v[180:183]\n\t" \
  "ds_read_b64_tr_b16 a[166:167], v228 offset:12800\n\t" \
  "ds_read_b64_tr_b16 a[168:169], v229 offset:512\n\t" \
  "v_mfma_f32_16x16x32_bf16 v[40:43], a[228:231], v[52:55], v[40:43]\n\t" \
  "v_exp_f32 v44, v44\n\t" \
  "v_mfma_f32_16x16x32_bf16 v[40:43], a[232:235], v[56:59], v[40:43]\n\t" \
  "v_max3_f32 v203, v35, v36, v37\n\t" \
  "v_max3_f32 v192, v192, v38, v39\n\t" \
  "v_mfma_f32_16x16x32_bf16 v[40:43], a[236:239], v[60:63], v[40:43]\n\t" \
  "v_exp_f32 v45, v45\n\t" \
  "v_exp_f32 v46, v46\n\t" \
  "v_exp_f32 v47, v47\n\t" \
  "v_cvt_pk_bf16_f32 v142, v44, v45\n\t" \
  "v_cvt_pk_bf16_f32 v143, v46, v47\n\t" \
  "v_mfma_f32_16x16x32_bf16 v[44:47], a[252:255], v[60:63], v[180:183]\n\t" \
  "v_exp_f32 v64, v64\n\t" \
  "v_mfma_f32_16x16x32_bf16 v[44:47], a[248:251], v[56:59], v[44:47]\n\t" \
  "ds_read_b64_tr_b16 a[170:171], v229 offset:4608\n\t" \
  "v_max3_f32 v203, v203, v40, v41\n\t" \
  "v_mfma_f32_16x16x32_bf16 v[44:47], a[244:247], v[52:55], v[44:47]\n\t" \
  "v_max3_f32 v192, v192, v42, v43\n\t" \
  "ds_read_b64_tr_b16 a[172:173], v229 offset:8704\n\t" \
  "v_mfma_f32_16x16x32_bf16 v[44:47], a[240:243], v[48:51], v[44:47]\n\t" \
  "v_exp_f32 v65, v65\n\t" \
  "v_exp_f32 v66, v66\n\t" \
  "v_exp_f32 v67, v67\n\t" \
  "v_cvt_pk_bf16_f32 v144, v64, v65\n\t" \
  "v_cvt_pk_bf16_f32 v145, v66, v67\n\t" \
  "v_mfma_f32_16x16x32_bf16 v[64:67], a[192:195], v[80:83], v[184:187]\n\t" \
  "v_exp_f32 v68, v68\n\t" \
  "v_mfma_f32_16x16x32_bf16 v[64:67], a[196:199], v[84:87], v[64:67]\n\t" \
  "v_max3_f32 v203, v203, v44, v45\n\t" \
  "v_max3_f32 v192, v192, v46, v47\n\t" \
  "v_mfma_f32_16x16x32_bf16 v[64:67], a[200:203], v[88:91], v[64:67]\n\t" \
  "v_max_f32 v192, v192, v203\n\t" \
  "ds_read_b64_tr_b16 a[174:175], v229 offset:12800\n\t" \
  "v_mfma_f32_16x16x32_bf16 v[64:67], a[204:207], v[92:95], v[64:67]\n\t" \
  "v_cmp_lt_f32 vcc, 0x42800000, v192\n\t" \
  "ds_read_b64_tr_b16 a[176:177], v228 offset:768\n\t" \
  "v_exp_f32 v69, v69\n\t" \
  "v_exp_f32 v70, v70\n\t" \
  "v_exp_f32 v71, v71\n\t" \
  "v_cvt_pk_bf16_f32 v146, v68, v69\n\t" \
  "v_cvt_pk_bf16_f32 v147, v70, v71\n\t" \
  "v_mfma_f32_16x16x32_bf16 v[68:71], a[220:223], v[92:95], v[184:187]\n\t" \
  "s_cbranch_vccnz .Lsf_c11\n\t" \
  ".Lsf_r11:\n\t" \
  "v_max3_f32 v192, v64, v65, v66\n\t" \
  "v_mfma_f32_16x16x32_bf16 v[68:71], a[216:219], v[88:91], v[68:71]\n\t" \
  "v_exp_f32 v72, v72\n\t" \
  "v_mfma_f32_16x16x32_bf16 v[68:71], a[212:215], v[84:87], v[68:71]\n\t" \
  "v_exp_f32 v73, v73\n\t" \
  "v_mfma_f32_16x16x32_bf16 v[68:71], a[208:211], v[80:83], v[68:71]\n\t" \
  "v_cvt_pk_bf16_f32 v148, v72, v73\n\t" \
  "ds_read_b64_tr_b16 a[178:179], v228 offset:4864\n\t" \
  "v_exp_f32 v74, v74\n\t" \
  "v_exp_f32 v75, v75\n\t" \
  "s_nop 0\n\t" \
  "v_cvt_pk_bf16_f32 v149, v74, v75\n\t" \
  "v_mfma_f32_16x16x32_bf16 v[72:75], a[224:227], v[80:83], v[184:187]\n\t" \
  "v_exp_f32 v76, v76\n\t" \
  "v_mfma_f32_16x16x32_bf16 v[72:75], a[228:231], v[84:87], v[72:75]\n\t" \
  "v_max3_f32 v203, v67, v68, v69\n\t" \
  "v_max3_f32 v192, v192, v70, v71\n\t" \
  "v_mfma_f32_16x16x32_bf16 v[72:75], a[232:235], v[88:91], v[72:75]\n\t" \
  "v_exp_f32 v77, v77\n\t" \
  "v_mfma_f32_16x16x32_bf16 v[72:75], a[236:239], v[92:95], v[72:75]\n\t" \
  "v_cvt_pk_bf16_f32 v150, v76, v77\n\t" \
  "ds_read_b64_tr_b16 a[180:181], v228 offset:8960\n\t" \
  "v_exp_f32 v78, v78\n\t" \
  "v_exp_f32 v79, v79\n\t" \
  "s_nop 0\n\t" \
  "v_cvt_pk_bf16_f32 v151, v78, v79\n\t" \
  "v_mfma_f32_16x16x32_bf16 v[76:79], a[252:255], v[92:95], v[184:187]\n\t" \
  "v_exp_f32 v96, v96\n\t" \
  "v_mfma_f32_16x16x32_bf16 v[76:79], a[248:251], v[88:91], v[76:79]\n\t" \
  "ds_read_b64_tr_b16 a[182:183], v228 offset:13056\n\t" \
  "v_max3_f32 v203, v203, v72, v73\n\t" \
  "v_mfma_f32_16x16x32_bf16 v[76:79], a[244:247], v[84:87], v[76:79]\n\t" \
  "v_max3_f32 v192, v192, v74, v75\n\t" \
  "ds_read_b64_tr_b16 a[184:185], v229 offset:768\n\t" \
  "v_mfma_f32_16x16x32_bf16 v[76:79], a[240:243], v[80:83], v[76:79]\n\t" \
  "v_exp_f32 v97, v97\n\t" \
  "v_exp_f32 v98, v98\n\t" \
  "v_exp_f32 v99, v99\n\t" \
  "v_cvt_pk_bf16_f32 v152, v96, v97\n\t" \
  "v_cvt_pk_bf16_f32 v153, v98, v99\n\t" \
  "v_mfma_f32_16x16x32_bf16 v[96:99], a[192:195], v[112:115], v[188:191]\n\t" \
  "v_exp_f32 v100, v100\n\t" \
  "v_mfma_f32_16x16x32_bf16 v[96:99], a[196:199], v[116:119], v[96:99]\n\t" \
  "v_max3_f32 v203, v203, v76, v77\n\t" \
  "v_max3_f32 v192, v192, v78, v79\n\t" \
  "v_mfma_f32_16x16x32_bf16 v[96:99], a[200:203], v[120:123], v[96:99]\n\t" \
  "v_max_f32 v192, v192, v203\n\t" \
  "ds_read_b64_tr_b16 a[186:187], v229 offset:4864\n\t" \
  "v_mfma_f32_16x16x32_bf16 v[96:99], a[204:207], v[124:127], v[96:99]\n\t" \
  "v_cmp_lt_f32 vcc, 0x42800000, v192\n\t" \
  "ds_read_b64_tr_b16 a[188:189], v229 offset:8960\n\t" \
  "v_exp_f32 v101, v101\n\t" \
  "v_exp_f32 v102, v102\n\t" \
  "v_exp_f32 v103, v103\n\t" \
  "v_cvt_pk_bf16_f32 v154, v100, v101\n\t" \
  "v_cvt_pk_bf16_f32 v155, v102, v103\n\t" \
  "v_mfma_f32_16x16x32_bf16 v[100:103], a[220:223], v[124:127], v[188:191]\n\t" \
  "s_cbranch_vccnz .Lsf_c12\n\t" \
  ".Lsf_r12:\n\t" \
  "v_max3_f32 v192, v96, v97, v98\n\t" \
  "v_mfma_f32_16x16x32_bf16 v[100:103], a[216:219], v[120:123], v[100:103]\n\t" \
  "v_exp_f32 v104, v104\n\t" \
  "v_mfma_f32_16x16x32_bf16 v[100:103], a[212:215], v[116:119], v[100:103]\n\t" \
  "v_exp_f32 v105, v105\n\t" \
  "v_mfma_f32_16x16x32_bf16 v[100:103], a[208:211], v[112:115], v[100:103]\n\t" \
  "v_cvt_pk_bf16_f32 v156, v104, v105\n\t" \
  "ds_read_b64_tr_b16 a[190:191], v229 offset:13056\n\t" \
  "v_exp_f32 v106, v106\n\t" \
  "v_exp_f32 v107, v107\n\t" \
  "s_nop 0\n\t" \
  "v_cvt_pk_bf16_f32 v157, v106, v107\n\t" \
  "v_mfma_f32_16x16x32_bf16 v[104:107], a[224:227], v[112:115], v[188:191]\n\t" \
  "v_exp_f32 v108, v108\n\t" \
  "v_mfma_f32_16x16x32_bf16 v[104:107], a[228:231], v[116:119], v[104:107]\n\t" \
  "v_exp_f32 v109, v109\n\t" \
  "v_mfma_f32_16x16x32_bf16 v[104:107], a[232:235], v[120:123], v[104:107]\n\t" \
  "v_cvt_pk_bf16_f32 v158, v108, v109\n\t" \
  "v_max3_f32 v203, v99, v100, v101\n\t" \
  "v_mfma_f32_16x16x32_bf16 v[104:107], a[236:239], v[124:127], v[104:107]\n\t" \
  "v_max3_f32 v192, v192, v102, v103\n\t" \
  "v_exp_f32 v110, v110\n\t" \
  "v_exp_f32 v111, v111\n\t" \
  "s_nop 0\n\t" \
  "v_cvt_pk_bf16_f32 v159, v110, v111\n\t" \
  "v_mfma_f32_16x16x32_bf16 v[108:111], a[252:255], v[124:127], v[188:191]\n\t" \
  "v_exp_f32 v0, v0\n\t" \
  "v_mfma_f32_16x16x32_bf16 v[108:111], a[248:251], v[120:123], v[108:111]\n\t" \
  "v_max3_f32 v203, v203, v104, v105\n\t" \
  "v_max3_f32 v192, v192, v106, v107\n\t" \
  "v_mfma_f32_16x16x32_bf16 v[108:111], a[244:247], v[116:119], v[108:111]\n\t" \
  "v_and_b32 v239, 1, v233\n\t" \
  "v_mul_u32_u24 v239, 0xaaaa, v239\n\t" \
  "v_mfma_f32_16x16x32_bf16 v[108:111], a[240:243], v[112:115], v[108:111]\n\t" \
  "v_exp_f32 v1, v1\n\t" \
  "s_waitcnt vmcnt(3) lgkmcnt(0)\n\t" \
  "s_barrier\n\t" \
  "v_mfma_f32_16x16x32_bf16 a[0:3], a[128:131], v[128:131], 0\n\t" \
  "v_add_u32 v239, 0x4444, v239\n\t" \
  "v_lshl_or_b32 v239, v239, 16, v239\n\t" \
  "v_mfma_f32_16x16x32_bf16 a[0:3], a[132:135], v[132:135], a[0:3]\n\t" \
  "v_mov_b32 v160, 0\n\t" \
  "v_mov_b32 v161, 0\n\t" \
  "v_mfma_f32_16x16x32_bf16 a[16:19], a[136:139], v[128:131], 0\n\t" \
  "v_mov_b32 v162, 0\n\t" \
  "ds_read_b128 a[192:195], v220 offset:0\n\t" \
  "v_mfma_f32_16x16x32_bf16 a[16:19], a[140:143], v[132:135], a[16:19]\n\t" \
  "v_max3_f32 v203, v203, v108, v109\n\t" \
  "v_max3_f32 v192, v192, v110, v111\n\t" \
  "v_mfma_f32_16x16x32_bf16 a[32:35], a[144:147], v[128:131], 0\n\t" \
  "v_max_f32 v192, v192, v203\n\t" \
  "v_mov_b32 v163, 0\n\t" \
  "v_mfma_f32_16x16x32_bf16 a[32:35], a[148:151], v[132:135], a[32:35]\n\t" \
  "v_cmp_lt_f32 vcc, 0x42800000, v192\n\t" \
  "v_mov_b32 v164, 0\n\t" \
  "v_mfma_f32_16x16x32_bf16 a[48:51], a[152:155], v[128:131], 0\n\t" \
  "s_cbranch_vccnz .Lsf_c13\n\t" \
  ".Lsf_r13:\n\t" \
  "v_mov_b32 v165, 0\n\t" \
  "v_mfma_f32_16x16x32_bf16 a[48:51], a[156:159], v[132:135], a[48:51]\n\t" \
  "v_mov_b32 v166, 0\n\t" \
  "v_mov_b32 v167, 0\n\t" \
  "v_mfma_f32_16x16x32_bf16 a[64:67], a[160:163], v[128:131], 0\n\t" \
  "v_mov_b32 v168, 0\n\t" \
  "ds_read_b128 a[196:199], v221 offset:0\n\t" \
  "v_mfma_f32_16x16x32_bf16 a[64:67], a[164:167], v[132:135], a[64:67]\n\t" \
  "v_mov_b32 v169, 0\n\t" \
  "v_mov_b32 v170, 0\n\t" \
  "v_mfma_f32_16x16x32_bf16 a[80:83], a[168:171], v[128:131], 0\n\t" \
  "v_mov_b32 v171, 0\n\t" \
  "v_mov_b32 v172, 0\n\t" \
  "v_mfma_f32_16x16x32_bf16 a[80:83], a[172:175], v[132:135], a[80:83]\n\t" \
  "v_mov_b32 v173, 0\n\t" \
  "v_mov_b32 v174, 0\n\t" \
  "v_mov_b32 v175, 0\n\t" \
  "v_mfma_f32_16x16x32_bf16 a[96:99], a[176:179], v[128:131], 0\n\t" \
  "ds_read_b128 a[200:203], v222 offset:0\n\t" \
  "ds_read_b128 a[204:207], v223 offset:0\n\t" \
  "v_mfma_f32_16x16x32_bf16 a[96:99], a[180:183], v[132:135], a[96:99]\n\t" \
  "v_exp_f32 v2, v2\n\t" \
  "v_mfma_f32_16x16x32_bf16 a[112:115], a[184:187], v[128:131], 0\n\t" \
  "v_exp_f32 v3, v3\n\t" \
  "v_mfma_f32_16x16x32_bf16 a[112:115], a[188:191], v[132:135], a[112:115]\n\t" \
  "v_exp_f32 v4, v4\n\t" \
  "v_smfmac_f32_16x16x64_bf16 v[160:163], v[216:219], v[128:135], v239\n\t" \
  "v_cvt_pk_bf16_f32 v128, v0, v1\n\t" \
  "v_cvt_pk_bf16_f32 v129, v2, v3\n\t" \
  "v_mfma_f32_16x16x32_bf16 a[4:7], a[128:131], v[136:139], 0\n\t" \
  "ds_read_b128 a[208:211], v220 offset:4096\n\t" \
  "ds_read_b128 a[212:215], v221 offset:4096\n\t" \
  "v_mfma_f32_16x16x32_bf16 a[4:7], a[132:135], v[140:143], a[4:7]\n\t" \
  "v_exp_f32 v5, v5\n\t" \
  "v_mfma_f32_16x16x32_bf16 a[20:23], a[136:139], v[136:139], 0\n\t" \
  "v_cvt_pk_bf16_f32 v130, v4, v5\n\t" \
  "ds_read_b128 a[216:219], v222 offset:4096\n\t" \
  "v_mfma_f32_16x16x32_bf16 a[20:23], a[140:143], v[140:143], a[20:23]\n\t" \
  "v_exp_f32 v6, v6\n\t" \
  "v_mfma_f32_16x16x32_bf16 a[36:39], a[144:147], v[136:139], 0\n\t" \
  "v_exp_f32 v7, v7\n\t" \
  "v_mfma_f32_16x16x32_bf16 a[36:39], a[148:151], v[140:143], a[36:39]\n\t" \
  "v_cvt_pk_bf16_f32 v131, v6, v7\n\t" \
  "s_add_u32 m0, %[m], 65536\n\t" \
  "v_mfma_f32_16x16x32_bf16 a[52:55], a[152:155], v[136:139], 0\n\t" \
  "v_exp_f32 v8, v8\n\t" \
  "v_mfma_f32_16x16x32_bf16 a[52:55], a[156:159], v[140:143], a[52:55]\n\t" \
  "v_exp_f32 v9, v9\n\t" \
  "v_mfma_f32_16x16x32_bf16 a[68:71], a[160:163], v[136:139], 0\n\t" \
  "v_cvt_pk_bf16_f32 v132, v8, v9\n\t" \
  "s_add_u32 s96, %[sofk], 49152\n\t" \
  "v_mfma_f32_16x16x32_bf16 a[68:71], a[164:167], v[140:143], a[68:71]\n\t" \
  "v_exp_f32 v10, v10\n\t" \
  "v_mfma_f32_16x16x32_bf16 a[84:87], a[168:171], v[136:139], 0\n\t" \
  "ds_read_b128 a[220:223], v223 offset:4096\n\t" \
  "buffer_load_dwordx4 v224, %[sK], s96 offen offset:0 lds\n\t" \
  "v_mfma_f32_16x16x32_bf16 a[84:87], a[172:175], v[140:143], a[84:87]\n\t" \
  "v_exp_f32 v11, v11\n\t" \
  "v_mfma_f32_16x16x32_bf16 a[100:103], a[176:179], v[136:139], 0\n\t" \
  "v_cvt_pk_bf16_f32 v133, v10, v11\n\t" \
  "ds_read_b128 a[224:227], v220 offset:8192\n\t" \
  "v_mfma_f32_16x16x32_bf16 a[100:103], a[180:183], v[140:143], a[100:103]\n\t" \
  "v_exp_f32 v12, v12\n\t" \
  "v_mfma_f32_16x16x32_bf16 a[116:119], a[184:187], v[136:139], 0\n\t" \
  "v_exp_f32 v13, v13\n\t" \
  "v_mfma_f32_16x16x32_bf16 a[116:119], a[188:191], v[140:143], a[116:119]\n\t" \
  "v_cvt_pk_bf16_f32 v134, v12, v13\n\t" \
  "buffer_load_dwordx4 v225, %[sK], s96 offen offset:1024 lds\n\t" \
  "v_smfmac_f32_16x16x64_bf16 v[164:167], v[216:219], v[136:143], v239\n\t" \
  "ds_read_b128 a[228:231], v221 offset:8192\n\t" \
  "buffer_load_dwordx4 v226, %[sK], s96 offen offset:2048 lds\n\t" \
  "v_mfma_f32_16x16x32_bf16 a[8:11], a[128:131], v[144:147], 0\n\t" \
  "v_exp_f32 v14, v14\n\t" \
  "v_mfma_f32_16x16x32_bf16 a[8:11], a[132:135], v[148:151], a[8:11]\n\t" \
  "v_exp_f32 v15, v15\n\t" \
  "v_mfma_f32_16x16x32_bf16 a[24:27], a[136:139], v[144:147], 0\n\t" \
  "v_cvt_pk_bf16_f32 v135, v14, v15\n\t" \
  "ds_read_b128 a[232:235], v222 offset:8192\n\t" \
  "v_mfma_f32_16x16x32_bf16 a[24:27], a[140:143], v[148:151], a[24:27]\n\t" \
  "v_exp_f32 v32, v32\n\t" \
  "v_mfma_f32_16x16x32_bf16 a[40:43], a[144:147], v[144:147], 0\n\t" \
  "buffer_load_dwordx4 v227, %[sK], s96 offen offset:3072 lds\n\t" \
  "ds_read_b128 a[236:239], v223 offset:8192\n\t" \
  "v_mfma_f32_16x16x32_bf16 a[40:43], a[148:151], v[148:151], a[40:43]\n\t" \
  "v_exp_f32 v33, v33\n\t" \
  "v_mfma_f32_16x16x32_bf16 a[56:59], a[152:155], v[144:147], 0\n\t" \
  "v_cvt_pk_bf16_f32 v136, v32, v33\n\t" \
  "ds_read_b128 a[240:243], v220 offset:12288\n\t" \
  "v_mfma_f32_16x16x32_bf16 a[56:59], a[156:159], v[148:151], a[56:59]\n\t" \
  "s_add_u32 m0, %[m], 114688\n\t" \
  "s_mov_b32 s96, 0\n\t" \
  "buffer_load_dwordx4 v232, %[sU], s96 offen offset:0 lds\n\t" \
  "v_mfma_f32_16x16x32_bf16 a[72:75], a[160:163], v[144:147], 0\n\t" \
  "v_exp_f32 v34, v34\n\t" \
  "v_mfma_f32_16x16x32_bf16 a[72:75], a[164:167], v[148:151], a[72:75]\n\t" \
  "v_exp_f32 v35, v35\n\t" \
  "v_mfma_f32_16x16x32_bf16 a[88:91], a[168:171], v[144:147], 0\n\t" \
  "v_cvt_pk_bf16_f32 v137, v34, v35\n\t" \
  "s_add_u32 m0, %[m], 81920\n\t" \
  "v_mfma_f32_16x16x32_bf16 a[88:91], a[172:175], v[148:151], a[88:91]\n\t" \
  "v_exp_f32 v36, v36\n\t" \
  "v_mfma_f32_16x16x32_bf16 a[104:107], a[176:179], v[144:147], 0\n\t" \
  "s_add_u32 s96, %[sofk], 32768\n\t" \
  "ds_read_b128 a[244:247], v221 offset:12288\n\t" \
  "v_mfma_f32_16x16x32_bf16 a[104:107], a[180:183], v[148:151], a[104:107]\n\t" \
  "v_exp_f32 v37, v37\n\t" \
  "v_mfma_f32_16x16x32_bf16 a[120:123], a[184:187], v[144:147], 0\n\t" \
  "v_cvt_pk_bf16_f32 v138, v36, v37\n\t" \
  "buffer_load_dwordx4 v230, %[sV], s96 offen offset:0 lds\n\t" \
  "v_mfma_f32_16x16x32_bf16 a[120:123], a[188:191], v[148:151], a[120:123]\n\t" \
  "v_exp_f32 v38, v38\n\t" \
  "v_smfmac_f32_16x16x64_bf16 v[168:171], v[216:219], v[144:151], v239\n\t" \
  "v_exp_f32 v39, v39\n\t" \
  "v_mfma_f32_16x16x32_bf16 a[12:15], a[128:131], v[152:155], 0\n\t" \
  "v_cvt_pk_bf16_f32 v139, v38, v39\n\t" \
  "ds_read_b128 a[248:251], v222 offset:12288\n\t" \
  "v_mfma_f32_16x16x32_bf16 a[12:15], a[132:135], v[156:159], a[12:15]\n\t" \
  "v_exp_f32 v40, v40\n\t" \
  "v_mfma_f32_16x16x32_bf16 a[28:31], a[136:139], v[152:155], 0\n\t" \
  "buffer_load_dwordx4 v231, %[sV], s96 offen offset:1024 lds\n\t" \
  "ds_read_b128 a[252:255], v223 offset:12288\n\t" \
  "v_mfma_f32_16x16x32_bf16 a[28:31], a[140:143], v[156:159], a[28:31]\n\t" \
  "v_exp_f32 v41, v41\n\t" \
  "v_mfma_f32_16x16x32_bf16 a[44:47], a[144:147], v[152:155], 0\n\t" \
  "v_cvt_pk_bf16_f32 v140, v40, v41\n\t" \
  "buffer_load_dwordx4 v230, %[sV], s96 offen offset:2048 lds\n\t" \
  "v_mfma_f32_16x16x32_bf16 a[44:47], a[148:151], v[156:159], a[44:47]\n\t" \
  "v_exp_f32 v42, v42\n\t" \
  "v_mfma_f32_16x16x32_bf16 a[60:63], a[152:155], v[152:155], 0\n\t" \
  "v_exp_f32 v43, v43\n\t" \
  "v_mfma_f32_16x16x32_bf16 a[60:63], a[156:159], v[156:159], a[60:63]\n\t" \
  "v_cvt_pk_bf16_f32 v141, v42, v43\n\t" \
  "buffer_load_dwordx4 v231, %[sV], s96 offen offset:3072 lds\n\t" \
  "v_mfma_f32_16x16x32_bf16 a[76:79], a[160:163], v[152:155], 0\n\t" \
  "v_exp_f32 v44, v44\n\t" \
  "v_mfma_f32_16x16x32_bf16 a[76:79], a[164:167], v[156:159], a[76:79]\n\t" \
  "v_exp_f32 v45, v45\n\t" \
  "v_mfma_f32_16x16x32_bf16 a[92:95], a[168:171], v[152:155], 0\n\t" \
  "v_cvt_pk_bf16_f32 v142, v44, v45\n\t" \
  "v_mfma_f32_16x16x32_bf16 a[92:95], a[172:175], v[156:159], a[92:95]\n\t" \
  "v_exp_f32 v46, v46\n\t" \
  "v_mfma_f32_16x16x32_bf16 a[108:111], a[176:179], v[152:155], 0\n\t" \
  "s_add_u32 m0, %[m], 114688\n\t" \
  "s_mov_b32 s96, 6144\n\t" \
  "buffer_load_dwordx4 v232, %[sU], s96 offen offset:1024 lds\n\t" \
  "v_mfma_f32_16x16x32_bf16 a[108:111], a[180:183], v[156:159], a[108:111]\n\t" \
  "v_exp_f32 v47, v47\n\t" \
  "v_mfma_f32_16x16x32_bf16 a[124:127], a[184:187], v[152:155], 0\n\t" \
  "v_cvt_pk_bf16_f32 v143, v46, v47\n\t" \
  "v_mfma_f32_16x16x32_bf16 a[124:127], a[188:191], v[156:159], a[124:127]\n\t" \
  "v_exp_f32 v64, v64\n\t" \
  "v_smfmac_f32_16x16x64_bf16 v[172:175], v[216:219], v[152:159], v239\n\t" \
  "v_exp_f32 v65, v65\n\t" \
  "s_cmp_lg_u32 s99, 0\n\t" \
  "s_cbranch_scc1 .Lsf_p1\n\t" \
  ".Lsf_q1:\n\t" \
  "s_waitcnt lgkmcnt(0)\n\t" \
  "v_mfma_f32_16x16x32_bf16 v[0:3], a[192:195], v[16:19], v[176:179]\n\t" \
  "v_cvt_pk_bf16_f32 v144, v64, v65\n\t" \
  "ds_read_b64_tr_b16 a[128:129], v237 offset:0\n\t" \
  "v_mfma_f32_16x16x32_bf16 v[0:3], a[196:199], v[20:23], v[0:3]\n\t" \
  "v_exp_f32 v66, v66\n\t" \
  "v_mfma_f32_16x16x32_bf16 v[0:3], a[200:203], v[24:27], v[0:3]\n\t" \
  "v_exp_f32 v67, v67\n\t" \
  "v_mfma_f32_16x16x32_bf16 v[0:3], a[204:207], v[28:31], v[0:3]\n\t" \
  "v_cvt_pk_bf16_f32 v145, v66, v67\n\t" \
  "ds_read_b64_tr_b16 a[130:131], v237 offset:4096\n\t" \
  "v_mfma_f32_16x16x32_bf16 v[4:7], a[220:223], v[28:31], v[176:179]\n\t" \
  "v_exp_f32 v68, v68\n\t" \
  "v_mfma_f32_16x16x32_bf16 v[4:7], a[216:219], v[24:27], v[4:7]\n\t" \
  "v_exp_f32 v69, v69\n\t" \
  "v_mfma_f32_16x16x32_bf16 v[4:7], a[212:215], v[20:23], v[4:7]\n\t" \
  "v_cvt_pk_bf16_f32 v146, v68, v69\n\t" \
  "ds_read_b64_tr_b16 a[132:133], v237 offset:8192\n\t" \
  "v_mfma_f32_16x16x32_bf16 v[4:7], a[208:211], v[16:19], v[4:7]\n\t" \
  "v_max3_f32 v192, v0, v1, v2\n\t" \
  "ds_read_b64_tr_b16 a[134:135], v237 offset:12288\n\t" \
  "v_mfma_f32_16x16x32_bf16 v[8:11], a[224:227], v[16:19], v[176:179]\n\t" \
  "ds_read_b64_tr_b16 a[136:137], v238 offset:0\n\t" \
  "ds_read_b64_tr_b16 a[138:139], v238 offset:4096\n\t" \
  "v_mfma_f32_16x16x32_bf16 v[8:11], a[228:231], v[20:23], v[8:11]\n\t" \
  "v_exp_f32 v70, v70\n\t" \
  "v_mfma_f32_16x16x32_bf16 v[8:11], a[232:235], v[24:27], v[8:11]\n\t" \
  "v_max3_f32 v203, v3, v4, v5\n\t" \
  "v_max3_f32 v192, v192, v6, v7\n\t" \
  "v_mfma_f32_16x16x32_bf16 v[8:11], a[236:239], v[28:31], v[8:11]\n\t" \
  "s_add_u32 m0, %[m], 114688\n\t" \
  "s_mov_b32 s96, 12288\n\t" \
  "buffer_load_dwordx4 v232, %[sU], s96 offen offset:2048 lds\n\t" \
  "v_mfma_f32_16x16x32_bf16 v[12:15], a[252:255], v[28:31], v[176:179]\n\t" \
  "v_exp_f32 v71, v71\n\t" \
  "v_mfma_f32_16x16x32_bf16 v[12:15], a[248:251], v[24:27], v[12:15]\n\t" \
  "v_cvt_pk_bf16_f32 v147, v70, v71\n\t" \
  "ds_read_b64_tr_b16 a[140:141], v238 offset:8192\n\t" \
  "v_mfma_f32_16x16x32_bf16 v[12:15], a[244:247], v[20:23], v[12:15]\n\t" \
  "v_max3_f32 v203, v203, v8, v9\n\t" \
  "v_max3_f32 v192, v192, v10, v11\n\t" \
  "v_mfma_f32_16x16x32_bf16 v[12:15], a[240:243], v[16:19], v[12:15]\n\t" \
  "ds_read_b64_tr_b16 a[142:143], v238 offset:12288\n\t" \
  "ds_read_b64_tr_b16 a[144:145], v237 offset:256\n\t" \
  "v_mfma_f32_16x16x32_bf16 v[32:35], a[192:195], v[48:51], v[180:183]\n\t" \
  "v_exp_f32 v72, v72\n\t" \
  "v_mfma_f32_16x16x32_bf16 v[32:35], a[196:199], v[52:55], v[32:35]\n\t" \
  "v_exp_f32 v73, v73\n\t" \
  "v_mfma_f32_16x16x32_bf16 v[32:35], a[200:203], v[56:59], v[32:35]\n\t" \
  "v_cvt_pk_bf16_f32 v148, v72, v73\n\t" \
  "ds_read_b64_tr_b16 a[146:147], v237 offset:4352\n\t" \
  "v_mfma_f32_16x16x32_bf16 v[32:35], a[204:207], v[60:63], v[32:35]\n\t" \
  "v_max3_f32 v203, v203, v12, v13\n\t" \
  "v_max3_f32 v192, v192, v14, v15\n\t" \
  "v_mfma_f32_16x16x32_bf16 v[36:39], a[220:223], v[60:63], v[180:183]\n\t" \
  "v_max_f32 v192, v192, v203\n\t" \
  "ds_read_b64_tr_b16 a[148:149], v237 offset:8448\n\t" \
  "v_mfma_f32_16x16x32_bf16 v[36:39], a[216:219], v[56:59], v[36:39]\n\t" \
  "v_cmp_lt_f32 vcc, 0x42800000, v192\n\t" \
  "ds_read_b64_tr_b16 a[150:151], v237 offset:12544\n\t" \
  "v_mfma_f32_16x16x32_bf16 v[36:39], a[212:215], v[52:55], v[36:39]\n\t" \
  "s_cbranch_vccnz .Lsf_c20\n\t" \
  ".Lsf_r20:\n\t" \
  "v_max3_f32 v192, v32, v33, v34\n\t" \
  "v_mfma_f32_16x16x32_bf16 v[36:39], a[208:211], v[48:51], v[36:39]\n\t" \
  "ds_read_b64_tr_b16 a[152:153], v238 offset:256\n\t" \
  "ds_read_b64_tr_b16 a[154:155], v238 offset:4352\n\t" \
  "v_mfma_f32_16x16x32_bf16 v[40:43], a[224:227], v[48:51], v[180:183]\n\t" \
  "s_mov_b32 s96, 18432\n\t" \
  "buffer_load_dwordx4 v232, %[sU], s96 offen offset:3072 lds\n\t" \
  "v_mfma_f32_16x16x32_bf16 v[40:43], a[228:231], v[52:55], v[40:43]\n\t" \
  "v_exp_f32 v74, v74\n\t" \
  "v_mfma_f32_16x16x32_bf16 v[40:43], a[232:235], v[56:59], v[40:43]\n\t" \
  "v_max3_f32 v203, v35, v36, v37\n\t" \
  "v_max3_f32 v192, v192, v38, v39\n\t" \
  "v_mfma_f32_16x16x32_bf16 v[40:43], a[236:239], v[60:63], v[40:43]\n\t" \
  "ds_read_b64_tr_b16 a[156:157], v238 offset:8448\n\t" \
  "ds_read_b64_tr_b16 a[158:159], v238 offset:12544\n\t" \
  "v_mfma_f32_16x16x32_bf16 v[44:47], a[252:255], v[60:63], v[180:183]\n\t" \
  "v_exp_f32 v75, v75\n\t" \
  "v_mfma_f32_16x16x32_bf16 v[44:47], a[248:251], v[56:59], v[44:47]\n\t" \
  "v_cvt_pk_bf16_f32 v149, v74, v75\n\t" \
  "ds_read_b64_tr_b16 a[160:161], v237 offset:512\n\t" \
  "v_mfma_f32_16x16x32_bf16 v[44:47], a[244:247], v[52:55], v[44:47]\n\t" \
  "v_max3_f32 v203, v203, v40, v41\n\t" \
  "v_max3_f32 v192, v192, v42, v43\n\t" \
  "v_mfma_f32_16x16x32_bf16 v[44:47], a[240:243], v[48:51], v[44:47]\n\t" \
  "v_exp_f32 v76, v76\n\t" \
  "v_mfma_f32_16x16x32_bf16 v[64:67], a[192:195], v[80:83], v[184:187]\n\t" \
  "ds_read_b64_tr_b16 a[162:163], v237 offset:4608\n\t" \
  "ds_read_b64_tr_b16 a[164:165], v237 offset:8704\n\t" \
  "v_mfma_f32_16x16x32_bf16 v[64:67], a[196:199], v[84:87], v[64:67]\n\t" \
  "v_exp_f32 v77, v77\n\t" \
  "v_mfma_f32_16x16x32_bf16 v[64:67], a[200:203], v[88:91], v[64:67]\n\t" \
  "v_cvt_pk_bf16_f32 v150, v76, v77\n\t" \
  "v_max3_f32 v203, v203, v44, v45\n\t" \
  "v_mfma_f32_16x16x32_bf16 v[64:67], a[204:207], v[92:95], v[64:67]\n\t" \
  "v_max3_f32 v192, v192, v46, v47\n\t" \
  "v_max_f32 v192, v192, v203\n\t" \
  "v_mfma_f32_16x16x32_bf16 v[68:71], a[220:223], v[92:95], v[184:187]\n\t" \
  "v_cmp_lt_f32 vcc, 0x42800000, v192\n\t" \
  "ds_read_b64_tr_b16 a[166:167], v237 offset:12800\n\t" \
  "v_mfma_f32_16x16x32_bf16 v[68:71], a[216:219], v[88:91], v[68:71]\n\t" \
  "s_cbranch_vccnz .Lsf_c21\n\t" \
  ".Lsf_r21:\n\t" \
  "ds_read_b64_tr_b16 a[168:169], v238 offset:512\n\t" \
  "v_mfma_f32_16x16x32_bf16 v[68:71], a[212:215], v[84:87], v[68:71]\n\t" \
  "s_add_u32 m0, %[m], 131072\n\t" \
  "s_mov_b32 s96, 28672\n\t" \
  "buffer_load_dwordx4 v232, %[sU], s96 offen offset:0 lds\n\t" \
  "v_mfma_f32_16x16x32_bf16 v[68:71], a[208:211], v[80:83], v[68:71]\n\t" \
  "v_max3_f32 v192, v64, v65, v66\n\t" \
  "ds_read_b64_tr_b16 a[170:171], v238 offset:4608\n\t" \
  "v_mfma_f32_16x16x32_bf16 v[72:75], a[224:227], v[80:83], v[184:187]\n\t" \
  "v_exp_f32 v78, v78\n\t" \
  "v_mfma_f32_16x16x32_bf16 v[72:75], a[228:231], v[84:87], v[72:75]\n\t" \
  "ds_read_b64_tr_b16 a[172:173], v238 offset:8704\n\t" \
  "ds_read_b64_tr_b16 a[174:175], v238 offset:12800\n\t" \
  "v_mfma_f32_16x16x32_bf16 v[72:75], a[232:235], v[88:91], v[72:75]\n\t" \
  "v_max3_f32 v203, v67, v68, v69\n\t" \
  "v_max3_f32 v192, v192, v70, v71\n\t" \
  "v_mfma_f32_16x16x32_bf16 v[72:75], a[236:239], v[92:95], v[72:75]\n\t" \
  "v_exp_f32 v79, v79\n\t" \
  "s_nop 0\n\t" \
  "v_cvt_pk_bf16_f32 v151, v78, v79\n\t" \
  "v_mfma_f32_16x16x32_bf16 v[76:79], a[252:255], v[92:95], v[184:187]\n\t" \
  "ds_read_b64_tr_b16 a[176:177], v237 offset:768\n\t" \
  "ds_read_b64_tr_b16 a[178:179], v237 offset:4864\n\t" \
  "v_mfma_f32_16x16x32_bf16 v[76:79], a[248:251], v[88:91], v[76:79]\n\t" \
  "v_exp_f32 v96, v96\n\t" \
  "v_mfma_f32_16x16x32_bf16 v[76:79], a[244:247], v[84:87], v[76:79]\n\t" \
  "v_max3_f32 v203, v203, v72, v73\n\t" \
  "v_max3_f32 v192, v192, v74, v75\n\t" \
  "v_mfma_f32_16x16x32_bf16 v[76:79], a[240:243], v[80:83], v[76:79]\n\t" \
  "ds_read_b64_tr_b16 a[180:181], v237 offset:8960\n\t" \
  "ds_read_b64_tr_b16 a[182:183], v237 offset:13056\n\t" \
  "v_exp_f32 v97, v97\n\t" \
  "v_exp_f32 v98, v98\n\t" \
  "v_exp_f32 v99, v99\n\t" \
  "v_cvt_pk_bf16_f32 v152, v96, v97\n\t" \
  "v_cvt_pk_bf16_f32 v153, v98, v99\n\t" \
  "v_mfma_f32_16x16x32_bf16 v[96:99], a[192:195], v[112:115], v[188:191]\n\t" \
  "s_mov_b32 s96, 34816\n\t" \
  "buffer_load_dwordx4 v232, %[sU], s96 offen offset:1024 lds\n\t" \
  "v_mfma_f32_16x16x32_bf16 v[96:99], a[196:199], v[116:119], v[96:99]\n\t" \
  "v_max3_f32 v203, v203, v76, v77\n\t" \
  "v_max3_f32 v192, v192, v78, v79\n\t" \
  "v_mfma_f32_16x16x32_bf16 v[96:99], a[200:203], v[120:123], v[96:99]\n\t" \
  "v_max_f32 v192, v192, v203\n\t" \
  "ds_read_b64_tr_b16 a[184:185], v238 offset:768\n\t" \
  "v_mfma_f32_16x16x32_bf16 v[96:99], a[204:207], v[124:127], v[96:99]\n\t" \
  "v_cmp_lt_f32 vcc, 0x42800000, v192\n\t" \
  "ds_read_b64_tr_b16 a[186:187], v238 offset:4864\n\t" \
  "v_exp_f32 v100, v100\n\t" \
  "v_exp_f32 v101, v101\n\t" \
  "v_exp_f32 v102, v102\n\t" \
  "v_exp_f32 v103, v103\n\t" \
  "v_cvt_pk_bf16_f32 v154, v100, v101\n\t" \
  "v_cvt_pk_bf16_f32 v155, v102, v103\n\t" \
  "v_mfma_f32_16x16x32_bf16 v[100:103], a[220:223], v[124:127], v[188:191]\n\t" \
  "s_cbranch_vccnz .Lsf_c22\n\t" \
  ".Lsf_r22:\n\t" \
  "v_max3_f32 v192, v96, v97, v98\n\t" \
  "v_mfma_f32_16x16x32_bf16 v[100:103], a[216:219], v[120:123], v[100:103]\n\t" \
  "v_exp_f32 v104, v104\n\t" \
  "v_mfma_f32_16x16x32_bf16 v[100:103], a[212:215], v[116:119], v[100:103]\n\t" \
  "v_exp_f32 v105, v105\n\t" \
  "v_mfma_f32_16x16x32_bf16 v[100:103], a[208:211], v[112:115], v[100:103]\n\t" \
  "v_cvt_pk_bf16_f32 v156, v104, v105\n\t" \
  "ds_read_b64_tr_b16 a[188:189], v238 offset:8960\n\t" \
  "v_exp_f32 v106, v106\n\t" \
  "v_exp_f32 v107, v107\n\t" \
  "s_nop 0\n\t" \
  "v_cvt_pk_bf16_f32 v157, v106, v107\n\t" \
  "v_mfma_f32_16x16x32_bf16 v[104:107], a[224:227], v[112:115], v[188:191]\n\t" \
  "v_exp_f32 v108, v108\n\t" \
  "v_mfma_f32_16x16x32_bf16 v[104:107], a[228:231], v[116:119], v[104:107]\n\t" \
  "ds_read_b64_tr_b16 a[190:191], v238 offset:13056\n\t" \
  "v_max3_f32 v203, v99, v100, v101\n\t" \
  "v_mfma_f32_16x16x32_bf16 v[104:107], a[232:235], v[120:123], v[104:107]\n\t" \
  "v_max3_f32 v192, v192, v102, v103\n\t" \
  "v_mfma_f32_16x16x32_bf16 v[104:107], a[236:239], v[124:127], v[104:107]\n\t" \
  "v_exp_f32 v109, v109\n\t" \
  "v_exp_f32 v110, v110\n\t" \
  "v_exp_f32 v111, v111\n\t" \
  "v_cvt_pk_bf16_f32 v158, v108, v109\n\t" \
  "v_cvt_pk_bf16_f32 v159, v110, v111\n\t" \
  "v_mfma_f32_16x16x32_bf16 v[108:111], a[252:255], v[124:127], v[188:191]\n\t" \
  "v_exp_f32 v0, v0\n\t" \
  "v_mfma_f32_16x16x32_bf16 v[108:111], a[248:251], v[120:123], v[108:111]\n\t" \
  "v_exp_f32 v1, v1\n\t" \
  "v_mfma_f32_16x16x32_bf16 v[108:111], a[244:247], v[116:119], v[108:111]\n\t" \
  "v_max3_f32 v203, v203, v104, v105\n\t" \
  "v_max3_f32 v192, v192, v106, v107\n\t" \
  "v_mfma_f32_16x16x32_bf16 v[108:111], a[240:243], v[112:115], v[108:111]\n\t" \
  "v_exp_f32 v2, v2\n\t" \
  "s_waitcnt vmcnt(5) lgkmcnt(0)\n\t" \
  "s_barrier\n\t" \
  "v_mfma_f32_16x16x32_bf16 a[0:3], a[128:131], v[128:131], a[0:3]\n\t" \
  "v_exp_f32 v3, v3\n\t" \
  "v_mfma_f32_16x16x32_bf16 a[0:3], a[132:135], v[132:135], a[0:3]\n\t" \
  "v_exp_f32 v4, v4\n\t" \
  "v_mfma_f32_16x16x32_bf16 a[16:19], a[136:139], v[128:131], a[16:19]\n\t" \
  "ds_read_b128 a[192:195], v220 offset:16384\n\t" \
  "v_max3_f32 v203, v203, v108, v109\n\t" \
  "v_mfma_f32_16x16x32_bf16 a[16:19], a[140:143], v[132:135], a[16:19]\n\t" \
  "v_max3_f32 v192, v192, v110, v111\n\t" \
  "v_max_f32 v192, v192, v203\n\t" \
  "v_mfma_f32_16x16x32_bf16 a[32:35], a[144:147], v[128:131], a[32:35]\n\t" \
  "v_cmp_lt_f32 vcc, 0x42800000, v192\n\t" \
  "ds_read_b128 a[196:199], v221 offset:16384\n\t" \
  "v_mfma_f32_16x16x32_bf16 a[32:35], a[148:151], v[132:135], a[32:35]\n\t" \
  "s_cbranch_vccnz .Lsf_c23\n\t" \
  ".Lsf_r23:\n\t" \
  "ds_read_b128 a[200:203], v222 offset:16384\n\t" \
  "v_mfma_f32_16x16x32_bf16 a[48:51], a[152:155], v[128:131], a[48:51]\n\t" \
  "v_exp_f32 v5, v5\n\t" \
  "v_mfma_f32_16x16x32_bf16 a[48:51], a[156:159], v[132:135], a[48:51]\n\t" \
  "v_exp_f32 v6, v6\n\t" \
  "v_mfma_f32_16x16x32_bf16 a[64:67], a[160:163], v[128:131], a[64:67]\n\t" \
  "v_exp_f32 v7, v7\n\t" \
  "v_mfma_f32_16x16x32_bf16 a[64:67], a[164:167], v[132:135], a[64:67]\n\t" \
  "v_exp_f32 v8, v8\n\t" \
  "v_mfma_f32_16x16x32_bf16 a[80:83], a[168:171], v[128:131], a[80:83]\n\t" \
  "v_exp_f32 v9, v9\n\t" \
  "v_mfma_f32_16x16x32_bf16 a[80:83], a[172:175], v[132:135], a[80:83]\n\t" \
  "v_exp_f32 v10, v10\n\t" \
  "v_mfma_f32_16x16x32_bf16 a[96:99], a[176:179], v[128:131], a[96:99]\n\t" \
  "v_exp_f32 v11, v11\n\t" \
  "v_mfma_f32_16x16x32_bf16 a[96:99], a[180:183], v[132:135], a[96:99]\n\t" \
  "ds_read_b128 a[204:207], v223 offset:16384\n\t" \
  "ds_read_b128 a[208:211], v220 offset:20480\n\t" \
  "v_mfma_f32_16x16x32_bf16 a[112:115], a[184:187], v[128:131], a[112:115]\n\t" \
  "v_exp_f32 v12, v12\n\t" \
  "v_mfma_f32_16x16x32_bf16 a[112:115], a[188:191], v[132:135], a[112:115]\n\t" \
  "v_exp_f32 v13, v13\n\t" \
  "v_smfmac_f32_16x16x64_bf16 v[160:163], v[216:219], v[128:135], v239\n\t" \
  "v_cvt_pk_bf16_f32 v128, v0, v1\n\t" \
  "v_cvt_pk_bf16_f32 v129, v2, v3\n\t" \
  "v_mfma_f32_16x16x32_bf16 a[4:7], a[128:131], v[136:139], a[4:7]\n\t" \
  "v_cvt_pk_bf16_f32 v130, v4, v5\n\t" \
  "v_cvt_pk_bf16_f32 v131, v6, v7\n\t" \
  "v_mfma_f32_16x16x32_bf16 a[4:7], a[132:135], v[140:143], a[4:7]\n\t" \
  "v_cvt_pk_bf16_f32 v132, v8, v9\n\t" \
  "v_cvt_pk_bf16_f32 v133, v10, v11\n\t" \
  "v_mfma_f32_16x16x32_bf16 a[20:23], a[136:139], v[136:139], a[20:23]\n\t" \
  "v_cvt_pk_bf16_f32 v134, v12, v13\n\t" \
  "ds_read_b128 a[212:215], v221 offset:20480\n\t" \
  "v_mfma_f32_16x16x32_bf16 a[20:23], a[140:143], v[140:143], a[20:23]\n\t" \
  "v_exp_f32 v14, v14\n\t" \
  "v_mfma_f32_16x16x32_bf16 a[36:39], a[144:147], v[136:139], a[36:39]\n\t" \
  "s_add_u32 m0, %[m], 49152\n\t" \
  "ds_read_b128 a[216:219], v222 offset:20480\n\t" \
  "v_mfma_f32_16x16x32_bf16 a[36:39], a[148:151], v[140:143], a[36:39]\n\t" \
  "v_exp_f32 v15, v15\n\t" \
  "v_mfma_f32_16x16x32_bf16 a[52:55], a[152:155], v[136:139], a[52:55]\n\t" \
  "v_cvt_pk_bf16_f32 v135, v14, v15\n\t" \
  "s_add_u32 s96, %[sofk], 65536\n\t" \
  "v_mfma_f32_16x16x32_bf16 a[52:55], a[156:159], v[140:143], a[52:55]\n\t" \
  "v_exp_f32 v32, v32\n\t" \
  "v_mfma_f32_16x16x32_bf16 a[68:71], a[160:163], v[136:139], a[68:71]\n\t" \
  "v_exp_f32 v33, v33\n\t" \
  "v_mfma_f32_16x16x32_bf16 a[68:71], a[164:167], v[140:143], a[68:71]\n\t" \
  "v_exp_f32 v34, v34\n\t" \
  "v_mfma_f32_16x16x32_bf16 a[84:87], a[168:171], v[136:139], a[84:87]\n\t" \
  "ds_read_b128 a[220:223], v223 offset:20480\n\t" \
  "buffer_load_dwordx4 v224, %[sK], s96 offen offset:0 lds\n\t" \
  "v_mfma_f32_16x16x32_bf16 a[84:87], a[172:175], v[140:143], a[84:87]\n\t" \
  "v_exp_f32 v35, v35\n\t" \
  "v_mfma_f32_16x16x32_bf16 a[100:103], a[176:179], v[136:139], a[100:103]\n\t" \
  "v_exp_f32 v36, v36\n\t" \
  "v_mfma_f32_16x16x32_bf16 a[100:103], a[180:183], v[140:143], a[100:103]\n\t" \
  "ds_read_b128 a[224:227], v220 offset:24576\n\t" \
  "buffer_load_dwordx4 v225, %[sK], s96 offen offset:1024 lds\n\t" \
  "v_mfma_f32_16x16x32_bf16 a[116:119], a[184:187], v[136:139], a[116:119]\n\t" \
  "v_exp_f32 v37, v37\n\t" \
  "v_mfma_f32_16x16x32_bf16 a[116:119], a[188:191], v[140:143], a[116:119]\n\t" \
  "v_exp_f32 v38, v38\n\t" \
  "v_smfmac_f32_16x16x64_bf16 v[164:167], v[216:219], v[136:143], v239\n\t" \
  "v_cvt_pk_bf16_f32 v136, v32, v33\n\t" \
  "v_cvt_pk_bf16_f32 v137, v34, v35\n\t" \
  "v_mfma_f32_16x16x32_bf16 a[8:11], a[128:131], v[144:147], a[8:11]\n\t" \
  "v_cvt_pk_bf16_f32 v138, v36, v37\n\t" \
  "ds_read_b128 a[228:231], v221 offset:24576\n\t" \
  "v_mfma_f32_16x16x32_bf16 a[8:11], a[132:135], v[148:151], a[8:11]\n\t" \
  "buffer_load_dwordx4 v226, %[sK], s96 offen offset:2048 lds\n\t" \
  "ds_read_b128 a[232:235], v222 offset:24576\n\t" \
  "v_mfma_f32_16x16x32_bf16 a[24:27], a[136:139], v[144:147], a[24:27]\n\t" \
  "v_exp_f32 v39, v39\n\t" \
  "v_mfma_f32_16x16x32_bf16 a[24:27], a[140:143], v[148:151], a[24:27]\n\t" \
  "v_cvt_pk_bf16_f32 v139, v38, v39\n\t" \
  "buffer_load_dwordx4 v227, %[sK], s96 offen offset:3072 lds\n\t" \
  "v_mfma_f32_16x16x32_bf16 a[40:43], a[144:147], v[144:147], a[40:43]\n\t" \
  "v_exp_f32 v40, v40\n\t" \
  "v_mfma_f32_16x16x32_bf16 a[40:43], a[148:151], v[148:151], a[40:43]\n\t" \
  "ds_read_b128 a[236:239], v223 offset:24576\n\t" \
  "ds_read_b128 a[240:243], v220 offset:28672\n\t" \
  "v_mfma_f32_16x16x32_bf16 a[56:59], a[152:155], v[144:147], a[56:59]\n\t" \
  "v_exp_f32 v41, v41\n\t" \
  "v_mfma_f32_16x16x32_bf16 a[56:59], a[156:159], v[148:151], a[56:59]\n\t" \
  "v_cvt_pk_bf16_f32 v140, v40, v41\n\t" \
  "ds_read_b128 a[244:247], v221 offset:28672\n\t" \
  "v_mfma_f32_16x16x32_bf16 a[72:75], a[160:163], v[144:147], a[72:75]\n\t" \
  "s_add_u32 m0, %[m], 131072\n\t" \
  "s_mov_b32 s96, 40960\n\t" \
  "buffer_load_dwordx4 v232, %[sU], s96 offen offset:2048 lds\n\t" \
  "v_mfma_f32_16x16x32_bf16 a[72:75], a[164:167], v[148:151], a[72:75]\n\t" \
  "v_exp_f32 v42, v42\n\t" \
  "v_mfma_f32_16x16x32_bf16 a[88:91], a[168:171], v[144:147], a[88:91]\n\t" \
  "s_add_u32 m0, %[m], 147456\n\t" \
  "s_add_u32 s96, %[sofk], 49152\n\t" \
  "v_mfma_f32_16x16x32_bf16 a[88:91], a[172:175], v[148:151], a[88:91]\n\t" \
  "v_exp_f32 v43, v43\n\t" \
  "v_mfma_f32_16x16x32_bf16 a[104:107], a[176:179], v[144:147], a[104:107]\n\t" \
  "v_cvt_pk_bf16_f32 v141, v42, v43\n\t" \
  "buffer_load_dwordx4 v230, %[sV], s96 offen offset:0 lds\n\t" \
  "v_mfma_f32_16x16x32_bf16 a[104:107], a[180:183], v[148:151], a[104:107]\n\t" \
  "v_exp_f32 v44, v44\n\t" \
  "v_mfma_f32_16x16x32_bf16 a[120:123], a[184:187], v[144:147], a[120:123]\n\t" \
  "v_exp_f32 v45, v45\n\t" \
  "v_mfma_f32_16x16x32_bf16 a[120:123], a[188:191], v[148:151], a[120:123]\n\t" \
  "v_cvt_pk_bf16_f32 v142, v44, v45\n\t" \
  "ds_read_b128 a[248:251], v222 offset:28672\n\t" \
  "v_smfmac_f32_16x16x64_bf16 v[168:171], v[216:219], v[144:151], v239\n\t" \
  "v_exp_f32 v46, v46\n\t" \
  "v_mfma_f32_16x16x32_bf16 a[12:15], a[128:131], v[152:155], a[12:15]\n\t" \
  "v_exp_f32 v47, v47\n\t" \
  "v_mfma_f32_16x16x32_bf16 a[12:15], a[132:135], v[156:159], a[12:15]\n\t" \
  "v_cvt_pk_bf16_f32 v143, v46, v47\n\t" \
  "buffer_load_dwordx4 v231, %[sV], s96 offen offset:1024 lds\n\t" \
  "v_mfma_f32_16x16x32_bf16 a[28:31], a[136:139], v[152:155], a[28:31]\n\t" \
  "v_exp_f32 v64, v64\n\t" \
  "v_mfma_f32_16x16x32_bf16 a[28:31], a[140:143], v[156:159], a[28:31]\n\t" \
  "v_exp_f32 v65, v65\n\t" \
  "v_mfma_f32_16x16x32_bf16 a[44:47], a[144:147], v[152:155], a[44:47]\n\t" \
  "v_cvt_pk_bf16_f32 v144, v64, v65\n\t" \
  "ds_read_b128 a[252:255], v223 offset:28672\n\t" \
  "v_mfma_f32_16x16x32_bf16 a[44:47], a[148:151], v[156:159], a[44:47]\n\t" \
  "v_exp_f32 v66, v66\n\t" \
  "v_mfma_f32_16x16x32_bf16 a[60:63], a[152:155], v[152:155], a[60:63]\n\t" \
  "buffer_load_dwordx4 v230, %[sV], s96 offen offset:2048 lds\n\t" \
  "buffer_load_dwordx4 v231, %[sV], s96 offen offset:3072 lds\n\t" \
  "v_mfma_f32_16x16x32_bf16 a[60:63], a[156:159], v[156:159], a[60:63]\n\t" \
  "v_exp_f32 v67, v67\n\t" \
  "v_mfma_f32_16x16x32_bf16 a[76:79], a[160:163], v[152:155], a[76:79]\n\t" \
  "v_cvt_pk_bf16_f32 v145, v66, v67\n\t" \
  "v_mfma_f32_16x16x32_bf16 a[76:79], a[164:167], v[156:159], a[76:79]\n\t" \
  "v_exp_f32 v68, v68\n\t" \
  "v_mfma_f32_16x16x32_bf16 a[92:95], a[168:171], v[152:155], a[92:95]\n\t" \
  "v_exp_f32 v69, v69\n\t" \
  "v_mfma_f32_16x16x32_bf16 a[92:95], a[172:175], v[156:159], a[92:95]\n\t" \
  "v_cvt_pk_bf16_f32 v146, v68, v69\n\t" \
  "v_mfma_f32_16x16x32_bf16 a[108:111], a[176:179], v[152:155], a[108:111]\n\t" \
  "s_add_u32 m0, %[m], 131072\n\t" \
  "s_mov_b32 s96, 47104\n\t" \
  "buffer_load_dwordx4 v232, %[sU], s96 offen offset:3072 lds\n\t" \
  "v_mfma_f32_16x16x32_bf16 a[108:111], a[180:183], v[156:159], a[108:111]\n\t" \
  "v_exp_f32 v70, v70\n\t" \
  "v_mfma_f32_16x16x32_bf16 a[124:127], a[184:187], v[152:155], a[124:127]\n\t" \
  "v_exp_f32 v71, v71\n\t" \
  "v_mfma_f32_16x16x32_bf16 a[124:127], a[188:191], v[156:159], a[124:127]\n\t" \
  "v_cvt_pk_bf16_f32 v147, v70, v71\n\t" \
  "v_smfmac_f32_16x16x64_bf16 v[172:175], v[216:219], v[152:159], v239\n\t" \
  "v_exp_f32 v72, v72\n\t" \
  "s_cmp_lg_u32 s99, 0\n\t" \
  "s_cbranch_scc1 .Lsf_p2\n\t" \
  ".Lsf_q2:\n\t" \
  "s_waitcnt lgkmcnt(0)\n\t" \
  "v_mfma_f32_16x16x32_bf16 v[0:3], a[192:195], v[16:19], v[176:179]\n\t" \
  "s_mul_i32 s98, %[sofk], 5\n\t" \
  "s_xor_b32 s98, s98, 8192\n\t" \
  "s_lshr_b32 s98, s98, 1\n\t" \
  "buffer_load_dword %[pfd], v204, %[sD], s98 offen\n\t" \
  "v_mfma_f32_16x16x32_bf16 v[0:3], a[196:199], v[20:23], v[0:3]\n\t" \
  "v_exp_f32 v73, v73\n\t" \
  "v_mfma_f32_16x16x32_bf16 v[0:3], a[200:203], v[24:27], v[0:3]\n\t" \
  "v_cvt_pk_bf16_f32 v148, v72, v73\n\t" \
  "ds_read_b64_tr_b16 a[128:129], v228 offset:0\n\t" \
  "v_mfma_f32_16x16x32_bf16 v[0:3], a[204:207], v[28:31], v[0:3]\n\t" \
  "v_exp_f32 v74, v74\n\t" \
  "v_mfma_f32_16x16x32_bf16 v[4:7], a[220:223], v[28:31], v[176:179]\n\t" \
  "ds_read_b64_tr_b16 a[130:131], v228 offset:4096\n\t" \
  "ds_read_b64_tr_b16 a[132:133], v228 offset:8192\n\t" \
  "v_mfma_f32_16x16x32_bf16 v[4:7], a[216:219], v[24:27], v[4:7]\n\t" \
  "v_exp_f32 v75, v75\n\t" \
  "v_mfma_f32_16x16x32_bf16 v[4:7], a[212:215], v[20:23], v[4:7]\n\t" \
  "v_cvt_pk_bf16_f32 v149, v74, v75\n\t" \
  "v_max3_f32 v192, v0, v1, v2\n\t" \
  "v_mfma_f32_16x16x32_bf16 v[4:7], a[208:211], v[16:19], v[4:7]\n\t" \
  "v_exp_f32 v76, v76\n\t" \
  "v_mfma_f32_16x16x32_bf16 v[8:11], a[224:227], v[16:19], v[176:179]\n\t" \
  "ds_read_b64_tr_b16 a[134:135], v228 offset:12288\n\t" \
  "ds_read_b64_tr_b16 a[136:137], v229 offset:0\n\t" \
  "v_mfma_f32_16x16x32_bf16 v[8:11], a[228:231], v[20:23], v[8:11]\n\t" \
  "v_exp_f32 v77, v77\n\t" \
  "v_mfma_f32_16x16x32_bf16 v[8:11], a[232:235], v[24:27], v[8:11]\n\t" \
  "v_cvt_pk_bf16_f32 v150, v76, v77\n\t" \
  "v_max3_f32 v203, v3, v4, v5\n\t" \
  "v_mfma_f32_16x16x32_bf16 v[8:11], a[236:239], v[28:31], v[8:11]\n\t" \
  "v_max3_f32 v192, v192, v6, v7\n\t" \
  "ds_read_b64_tr_b16 a[138:139], v229 offset:4096\n\t" \
  "v_mfma_f32_16x16x32_bf16 v[12:15], a[252:255], v[28:31], v[176:179]\n\t" \
  "ds_read_b64_tr_b16 a[140:141], v229 offset:8192\n\t" \
  "ds_read_b64_tr_b16 a[142:143], v229 offset:12288\n\t" \
  "v_mfma_f32_16x16x32_bf16 v[12:15], a[248:251], v[24:27], v[12:15]\n\t" \
  "v_exp_f32 v78, v78\n\t" \
  "v_mfma_f32_16x16x32_bf16 v[12:15], a[244:247], v[20:23], v[12:15]\n\t" \
  "v_max3_f32 v203, v203, v8, v9\n\t" \
  "v_max3_f32 v192, v192, v10, v11\n\t" \
  "v_mfma_f32_16x16x32_bf16 v[12:15], a[240:243], v[16:19], v[12:15]\n\t" \
  "s_mul_i32 s98, %[sofk], 5\n\t" \
  "s_xor_b32 s98, s98, 12288\n\t" \
  "s_lshr_b32 s98, s98, 1\n\t" \
  "buffer_load_dword %[pfd], v204, %[sD], s98 offen\n\t" \
  "v_mfma_f32_16x16x32_bf16 v[32:35], a[192:195], v[48:51], v[180:183]\n\t" \
  "ds_read_b64_tr_b16 a[144:145], v228 offset:256\n\t" \
  "ds_read_b64_tr_b16 a[146:147], v228 offset:4352\n\t" \
  "v_mfma_f32_16x16x32_bf16 v[32:35], a[196:199], v[52:55], v[32:35]\n\t" \
  "v_exp_f32 v79, v79\n\t" \
  "v_mfma_f32_16x16x32_bf16 v[32:35], a[200:203], v[56:59], v[32:35]\n\t" \
  "v_cvt_pk_bf16_f32 v151, v78, v79\n\t" \
  "v_max3_f32 v203, v203, v12, v13\n\t" \
  "v_mfma_f32_16x16x32_bf16 v[32:35], a[204:207], v[60:63], v[32:35]\n\t" \
  "v_max3_f32 v192, v192, v14, v15\n\t" \
  "v_max_f32 v192, v192, v203\n\t" \
  "v_mfma_f32_16x16x32_bf16 v[36:39], a[220:223], v[60:63], v[180:183]\n\t" \
  "v_cmp_lt_f32 vcc, 0x42800000, v192\n\t" \
  "ds_read_b64_tr_b16 a[148:149], v228 offset:8448\n\t" \
  "v_mfma_f32_16x16x32_bf16 v[36:39], a[216:219], v[56:59], v[36:39]\n\t" \
  "s_cbranch_vccnz .Lsf_c30\n\t" \
  ".Lsf_r30:\n\t" \
  "ds_read_b64_tr_b16 a[150:151], v228 offset:12544\n\t" \
  "v_mfma_f32_16x16x32_bf16 v[36:39], a[212:215], v[52:55], v[36:39]\n\t" \
  "v_max3_f32 v192, v32, v33, v34\n\t" \
  "ds_read_b64_tr_b16 a[152:153], v229 offset:256\n\t" \
  "v_mfma_f32_16x16x32_bf16 v[36:39], a[208:211], v[48:51], v[36:39]\n\t" \
  "v_exp_f32 v96, v96\n\t" \
  "v_mfma_f32_16x16x32_bf16 v[40:43], a[224:227], v[48:51], v[180:183]\n\t" \
  "v_exp_f32 v97, v97\n\t" \
  "v_mfma_f32_16x16x32_bf16 v[40:43], a[228:231], v[52:55], v[40:43]\n\t" \
  "v_cvt_pk_bf16_f32 v152, v96, v97\n\t" \
  "ds_read_b64_tr_b16 a[154:155], v229 offset:4352\n\t" \
  "v_mfma_f32_16x16x32_bf16 v[40:43], a[232:235], v[56:59], v[40:43]\n\t" \
  "v_exp_f32 v98, v98\n\t" \
  "v_mfma_f32_16x16x32_bf16 v[40:43], a[236:239], v[60:63], v[40:43]\n\t" \
  "v_max3_f32 v203, v35, v36, v37\n\t" \
  "v_max3_f32 v192, v192, v38, v39\n\t" \
  "v_mfma_f32_16x16x32_bf16 v[44:47], a[252:255], v[60:63], v[180:183]\n\t" \
  "ds_read_b64_tr_b16 a[156:157], v229 offset:8448\n\t" \
  "ds_read_b64_tr_b16 a[158:159], v229 offset:12544\n\t" \
  "v_mfma_f32_16x16x32_bf16 v[44:47], a[248:251], v[56:59], v[44:47]\n\t" \
  "v_exp_f32 v99, v99\n\t" \
  "v_mfma_f32_16x16x32_bf16 v[44:47], a[244:247], v[52:55], v[44:47]\n\t" \
  "v_cvt_pk_bf16_f32 v153, v98, v99\n\t" \
  "ds_read_b64_tr_b16 a[160:161], v228 offset:512\n\t" \
  "v_mfma_f32_16x16x32_bf16 v[44:47], a[240:243], v[48:51], v[44:47]\n\t" \
  "v_max3_f32 v203, v203, v40, v41\n\t" \
  "v_max3_f32 v192, v192, v42, v43\n\t" \
  "v_mfma_f32_16x16x32_bf16 v[64:67], a[192:195], v[80:83], v[184:187]\n\t" \
  "ds_read_b64_tr_b16 a[162:163], v228 offset:4608\n\t" \
  "ds_read_b64_tr_b16 a[164:165], v228 offset:8704\n\t" \
  "v_mfma_f32_16x16x32_bf16 v[64:67], a[196:199], v[84:87], v[64:67]\n\t" \
  "v_exp_f32 v100, v100\n\t" \
  "v_mfma_f32_16x16x32_bf16 v[64:67], a[200:203], v[88:91], v[64:67]\n\t" \
  "v_max3_f32 v203, v203, v44, v45\n\t" \
  "v_max3_f32 v192, v192, v46, v47\n\t" \
  "v_mfma_f32_16x16x32_bf16 v[64:67], a[204:207], v[92:95], v[64:67]\n\t" \
  "v_max_f32 v192, v192, v203\n\t" \
  "ds_read_b64_tr_b16 a[166:167], v228 offset:12800\n\t" \
  "v_mfma_f32_16x16x32_bf16 v[68:71], a[220:223], v[92:95], v[184:187]\n\t" \
  "v_cmp_lt_f32 vcc, 0x42800000, v192\n\t" \
  "ds_read_b64_tr_b16 a[168:169], v229 offset:512\n\t" \
  "v_mfma_f32_16x16x32_bf16 v[68:71], a[216:219], v[88:91], v[68:71]\n\t" \
  "s_cbranch_vccnz .Lsf_c31\n\t" \
  ".Lsf_r31:\n\t" \
  "ds_read_b64_tr_b16 a[170:171], v229 offset:4608\n\t" \
  "v_mfma_f32_16x16x32_bf16 v[68:71], a[212:215], v[84:87], v[68:71]\n\t" \
  "v_max3_f32 v192, v64, v65, v66\n\t" \
  "ds_read_b64_tr_b16 a[172:173], v229 offset:8704\n\t" \
  "v_mfma_f32_16x16x32_bf16 v[68:71], a[208:211], v[80:83], v[68:71]\n\t" \
  "v_exp_f32 v101, v101\n\t" \
  "v_mfma_f32_16x16x32_bf16 v[72:75], a[224:227], v[80:83], v[184:187]\n\t" \
  "v_cvt_pk_bf16_f32 v154, v100, v101\n\t" \
  "ds_read_b64_tr_b16 a[174:175], v229 offset:12800\n\t" \
  "v_mfma_f32_16x16x32_bf16 v[72:75], a[228:231], v[84:87], v[72:75]\n\t" \
  "v_exp_f32 v102, v102\n\t" \
  "v_mfma_f32_16x16x32_bf16 v[72:75], a[232:235], v[88:91], v[72:75]\n\t" \
  "v_exp_f32 v103, v103\n\t" \
  "v_mfma_f32_16x16x32_bf16 v[72:75], a[236:239], v[92:95], v[72:75]\n\t" \
  "v_cvt_pk_bf16_f32 v155, v102, v103\n\t" \
  "v_max3_f32 v203, v67, v68, v69\n\t" \
  "v_mfma_f32_16x16x32_bf16 v[76:79], a[252:255], v[92:95], v[184:187]\n\t" \
  "v_max3_f32 v192, v192, v70, v71\n\t" \
  "ds_read_b64_tr_b16 a[176:177], v228 offset:768\n\t" \
  "v_mfma_f32_16x16x32_bf16 v[76:79], a[248:251], v[88:91], v[76:79]\n\t" \
  "v_exp_f32 v104, v104\n\t" \
  "v_mfma_f32_16x16x32_bf16 v[76:79], a[244:247], v[84:87], v[76:79]\n\t" \
  "ds_read_b64_tr_b16 a[178:179], v228 offset:4864\n\t" \
  "v_max3_f32 v203, v203, v72, v73\n\t" \
  "v_mfma_f32_16x16x32_bf16 v[76:79], a[240:243], v[80:83], v[76:79]\n\t" \
  "v_max3_f32 v192, v192, v74, v75\n\t" \
  "ds_read_b64_tr_b16 a[180:181], v228 offset:8960\n\t" \
  "v_mfma_f32_16x16x32_bf16 v[96:99], a[192:195], v[112:115], v[188:191]\n\t" \
  "v_exp_f32 v105, v105\n\t" \
  "v_mfma_f32_16x16x32_bf16 v[96:99], a[196:199], v[116:119], v[96:99]\n\t" \
  "v_cvt_pk_bf16_f32 v156, v104, v105\n\t" \
  "ds_read_b64_tr_b16 a[182:183], v228 offset:13056\n\t" \
  "v_mfma_f32_16x16x32_bf16 v[96:99], a[200:203], v[120:123], v[96:99]\n\t" \
  "v_max3_f32 v203, v203, v76, v77\n\t" \
  "v_max3_f32 v192, v192, v78, v79\n\t" \
  "v_mfma_f32_16x16x32_bf16 v[96:99], a[204:207], v[124:127], v[96:99]\n\t" \
  "v_max_f32 v192, v192, v203\n\t" \
  "ds_read_b64_tr_b16 a[184:185], v229 offset:768\n\t" \
  "v_mfma_f32_16x16x32_bf16 v[100:103], a[220:223], v[124:127], v[188:191]\n\t" \
  "v_cmp_lt_f32 vcc, 0x42800000, v192\n\t" \
  "ds_read_b64_tr_b16 a[186:187], v229 offset:4864\n\t" \
  "v_mfma_f32_16x16x32_bf16 v[100:103], a[216:219], v[120:123], v[100:103]\n\t" \
  "s_cbranch_vccnz .Lsf_c32\n\t" \
  ".Lsf_r32:\n\t" \
  "ds_read_b64_tr_b16 a[188:189], v229 offset:8960\n\t" \
  "v_mfma_f32_16x16x32_bf16 v[100:103], a[212:215], v[116:119], v[100:103]\n\t" \
  "v_max3_f32 v192, v96, v97, v98\n\t" \
  "ds_read_b64_tr_b16 a[190:191], v229 offset:13056\n\t" \
  "v_mfma_f32_16x16x32_bf16 v[100:103], a[208:211], v[112:115], v[100:103]\n\t" \
  "v_exp_f32 v106, v106\n\t" \
  "v_exp_f32 v107, v107\n\t" \
  "s_nop 0\n\t" \
  "v_cvt_pk_bf16_f32 v157, v106, v107\n\t" \
  "v_mfma_f32_16x16x32_bf16 v[104:107], a[224:227], v[112:115], v[188:191]\n\t" \
  "v_exp_f32 v108, v108\n\t" \
  "v_mfma_f32_16x16x32_bf16 v[104:107], a[228:231], v[116:119], v[104:107]\n\t" \
  "v_exp_f32 v109, v109\n\t" \
  "v_mfma_f32_16x16x32_bf16 v[104:107], a[232:235], v[120:123], v[104:107]\n\t" \
  "v_cvt_pk_bf16_f32 v158, v108, v109\n\t" \
  "v_max3_f32 v203, v99, v100, v101\n\t" \
  "v_mfma_f32_16x16x32_bf16 v[104:107], a[236:239], v[124:127], v[104:107]\n\t" \
  "v_max3_f32 v192, v192, v102, v103\n\t" \
  "v_exp_f32 v110, v110\n\t" \
  "v_exp_f32 v111, v111\n\t" \
  "s_nop 0\n\t" \
  "v_cvt_pk_bf16_f32 v159, v110, v111\n\t" \
  "v_mfma_f32_16x16x32_bf16 v[108:111], a[252:255], v[124:127], v[188:191]\n\t" \
  "v_exp_f32 v0, v0\n\t" \
  "v_mfma_f32_16x16x32_bf16 v[108:111], a[248:251], v[120:123], v[108:111]\n\t" \
  "v_exp_f32 v1, v1\n\t" \
  "v_mfma_f32_16x16x32_bf16 v[108:111], a[244:247], v[116:119], v[108:111]\n\t" \
  "v_max3_f32 v203, v203, v104, v105\n\t" \
  "v_max3_f32 v192, v192, v106, v107\n\t" \
  "v_mfma_f32_16x16x32_bf16 v[108:111], a[240:243], v[112:115], v[108:111]\n\t" \
  "v_exp_f32 v2, v2\n\t" \
  "s_waitcnt vmcnt(3) lgkmcnt(0)\n\t" \
  "s_barrier\n\t" \
  "v_mfma_f32_16x16x32_bf16 a[0:3], a[128:131], v[128:131], a[0:3]\n\t" \
  "v_exp_f32 v3, v3\n\t" \
  "v_mfma_f32_16x16x32_bf16 a[0:3], a[132:135], v[132:135], a[0:3]\n\t" \
  "v_exp_f32 v4, v4\n\t" \
  "v_mfma_f32_16x16x32_bf16 a[16:19], a[136:139], v[128:131], a[16:19]\n\t" \
  "ds_read_b128 a[192:195], v220 offset:0\n\t" \
  "v_max3_f32 v203, v203, v108, v109\n\t" \
  "v_mfma_f32_16x16x32_bf16 a[16:19], a[140:143], v[132:135], a[16:19]\n\t" \
  "v_max3_f32 v192, v192, v110, v111\n\t" \
  "v_max_f32 v192, v192, v203\n\t" \
  "v_mfma_f32_16x16x32_bf16 a[32:35], a[144:147], v[128:131], a[32:35]\n\t" \
  "v_cmp_lt_f32 vcc, 0x42800000, v192\n\t" \
  "ds_read_b128 a[196:199], v221 offset:0\n\t" \
  "v_mfma_f32_16x16x32_bf16 a[32:35], a[148:151], v[132:135], a[32:35]\n\t" \
  "s_cbranch_vccnz .Lsf_c33\n\t" \
  ".Lsf_r33:\n\t" \
  "ds_read_b128 a[200:203], v222 offset:0\n\t" \
  "v_mfma_f32_16x16x32_bf16 a[48:51], a[152:155], v[128:131], a[48:51]\n\t" \
  "v_exp_f32 v5, v5\n\t" \
  "v_mfma_f32_16x16x32_bf16 a[48:51], a[156:159], v[132:135], a[48:51]\n\t" \
  "v_exp_f32 v6, v6\n\t" \
  "v_mfma_f32_16x16x32_bf16 a[64:67], a[160:163], v[128:131], a[64:67]\n\t" \
  "v_exp_f32 v7, v7\n\t" \
  "v_mfma_f32_16x16x32_bf16 a[64:67], a[164:167], v[132:135], a[64:67]\n\t" \
  "v_exp_f32 v8, v8\n\t" \
  "v_mfma_f32_16x16x32_bf16 a[80:83], a[168:171], v[128:131], a[80:83]\n\t" \
  "v_exp_f32 v9, v9\n\t" \
  "v_mfma_f32_16x16x32_bf16 a[80:83], a[172:175], v[132:135], a[80:83]\n\t" \
  "v_exp_f32 v10, v10\n\t" \
  "v_mfma_f32_16x16x32_bf16 a[96:99], a[176:179], v[128:131], a[96:99]\n\t" \
  "v_exp_f32 v11, v11\n\t" \
  "v_mfma_f32_16x16x32_bf16 a[96:99], a[180:183], v[132:135], a[96:99]\n\t" \
  "ds_read_b128 a[204:207], v223 offset:0\n\t" \
  "ds_read_b128 a[208:211], v220 offset:4096\n\t" \
  "v_mfma_f32_16x16x32_bf16 a[112:115], a[184:187], v[128:131], a[112:115]\n\t" \
  "v_exp_f32 v12, v12\n\t" \
  "v_mfma_f32_16x16x32_bf16 a[112:115], a[188:191], v[132:135], a[112:115]\n\t" \
  "v_exp_f32 v13, v13\n\t" \
  "v_smfmac_f32_16x16x64_bf16 v[160:163], v[216:219], v[128:135], v239\n\t" \
  "v_cvt_pk_bf16_f32 v128, v0, v1\n\t" \
  "v_cvt_pk_bf16_f32 v129, v2, v3\n\t" \
  "v_mfma_f32_16x16x32_bf16 a[4:7], a[128:131], v[136:139], a[4:7]\n\t" \
  "v_cvt_pk_bf16_f32 v130, v4, v5\n\t" \
  "v_cvt_pk_bf16_f32 v131, v6, v7\n\t" \
  "v_mfma_f32_16x16x32_bf16 a[4:7], a[132:135], v[140:143], a[4:7]\n\t" \
  "v_cvt_pk_bf16_f32 v132, v8, v9\n\t" \
  "v_cvt_pk_bf16_f32 v133, v10, v11\n\t" \
  "v_mfma_f32_16x16x32_bf16 a[20:23], a[136:139], v[136:139], a[20:23]\n\t" \
  "v_cvt_pk_bf16_f32 v134, v12, v13\n\t" \
  "ds_read_b128 a[212:215], v221 offset:4096\n\t" \
  "v_mfma_f32_16x16x32_bf16 a[20:23], a[140:143], v[140:143], a[20:23]\n\t" \
  "v_exp_f32 v14, v14\n\t" \
  "v_mfma_f32_16x16x32_bf16 a[36:39], a[144:147], v[136:139], a[36:39]\n\t" \
  "s_add_u32 m0, %[m], 65536\n\t" \
  "ds_read_b128 a[216:219], v222 offset:4096\n\t" \
  "v_mfma_f32_16x16x32_bf16 a[36:39], a[148:151], v[140:143], a[36:39]\n\t" \
  "v_exp_f32 v15, v15\n\t" \
  "v_mfma_f32_16x16x32_bf16 a[52:55], a[152:155], v[136:139], a[52:55]\n\t" \
  "v_cvt_pk_bf16_f32 v135, v14, v15\n\t" \
  "s_add_u32 s96, %[sofk], 81920\n\t" \
  "v_mfma_f32_16x16x32_bf16 a[52:55], a[156:159], v[140:143], a[52:55]\n\t" \
  "v_exp_f32 v32, v32\n\t" \
  "v_mfma_f32_16x16x32_bf16 a[68:71], a[160:163], v[136:139], a[68:71]\n\t" \
  "v_exp_f32 v33, v33\n\t" \
  "v_mfma_f32_16x16x32_bf16 a[68:71], a[164:167], v[140:143], a[68:71]\n\t" \
  "v_exp_f32 v34, v34\n\t" \
  "v_mfma_f32_16x16x32_bf16 a[84:87], a[168:171], v[136:139], a[84:87]\n\t" \
  "ds_read_b128 a[220:223], v223 offset:4096\n\t" \
  "buffer_load_dwordx4 v224, %[sK], s96 offen offset:0 lds\n\t" \
  "v_mfma_f32_16x16x32_bf16 a[84:87], a[172:175], v[140:143], a[84:87]\n\t" \
  "v_exp_f32 v35, v35\n\t" \
  "v_mfma_f32_16x16x32_bf16 a[100:103], a[176:179], v[136:139], a[100:103]\n\t" \
  "v_exp_f32 v36, v36\n\t" \
  "v_mfma_f32_16x16x32_bf16 a[100:103], a[180:183], v[140:143], a[100:103]\n\t" \
  "ds_read_b128 a[224:227], v220 offset:8192\n\t" \
  "buffer_load_dwordx4 v225, %[sK], s96 offen offset:1024 lds\n\t" \
  "v_mfma_f32_16x16x32_bf16 a[116:119], a[184:187], v[136:139], a[116:119]\n\t" \
  "v_exp_f32 v37, v37\n\t" \
  "v_mfma_f32_16x16x32_bf16 a[116:119], a[188:191], v[140:143], a[116:119]\n\t" \
  "v_exp_f32 v38, v38\n\t" \
  "v_smfmac_f32_16x16x64_bf16 v[164:167], v[216:219], v[136:143], v239\n\t" \
  "v_cvt_pk_bf16_f32 v136, v32, v33\n\t" \
  "v_cvt_pk_bf16_f32 v137, v34, v35\n\t" \
  "v_mfma_f32_16x16x32_bf16 a[8:11], a[128:131], v[144:147], a[8:11]\n\t" \
  "v_cvt_pk_bf16_f32 v138, v36, v37\n\t" \
  "ds_read_b128 a[228:231], v221 offset:8192\n\t" \
  "v_mfma_f32_16x16x32_bf16 a[8:11], a[132:135], v[148:151], a[8:11]\n\t" \
  "buffer_load_dwordx4 v226, %[sK], s96 offen offset:2048 lds\n\t" \
  "ds_read_b128 a[232:235], v222 offset:8192\n\t" \
  "v_mfma_f32_16x16x32_bf16 a[24:27], a[136:139], v[144:147], a[24:27]\n\t" \
  "v_exp_f32 v39, v39\n\t" \
  "v_mfma_f32_16x16x32_bf16 a[24:27], a[140:143], v[148:151], a[24:27]\n\t" \
  "v_cvt_pk_bf16_f32 v139, v38, v39\n\t" \
  "buffer_load_dwordx4 v227, %[sK], s96 offen offset:3072 lds\n\t" \
  "v_mfma_f32_16x16x32_bf16 a[40:43], a[144:147], v[144:147], a[40:43]\n\t" \
  "v_exp_f32 v40, v40\n\t" \
  "v_mfma_f32_16x16x32_bf16 a[40:43], a[148:151], v[148:151], a[40:43]\n\t" \
  "ds_read_b128 a[236:239], v223 offset:8192\n\t" \
  "ds_read_b128 a[240:243], v220 offset:12288\n\t" \
  "v_mfma_f32_16x16x32_bf16 a[56:59], a[152:155], v[144:147], a[56:59]\n\t" \
  "v_exp_f32 v41, v41\n\t" \
  "v_mfma_f32_16x16x32_bf16 a[56:59], a[156:159], v[148:151], a[56:59]\n\t" \
  "v_cvt_pk_bf16_f32 v140, v40, v41\n\t" \
  "ds_read_b128 a[244:247], v221 offset:12288\n\t" \
  "v_mfma_f32_16x16x32_bf16 a[72:75], a[160:163], v[144:147], a[72:75]\n\t" \
  "s_add_u32 m0, %[m], 32768\n\t" \
  "s_mov_b32 s96, 0\n\t" \
  "buffer_load_dwordx4 v232, %[sG], s96 offen offset:0 lds\n\t" \
  "v_mfma_f32_16x16x32_bf16 a[72:75], a[164:167], v[148:151], a[72:75]\n\t" \
  "v_exp_f32 v42, v42\n\t" \
  "v_mfma_f32_16x16x32_bf16 a[88:91], a[168:171], v[144:147], a[88:91]\n\t" \
  "s_add_u32 m0, %[m], 81920\n\t" \
  "s_add_u32 s96, %[sofk], 65536\n\t" \
  "v_mfma_f32_16x16x32_bf16 a[88:91], a[172:175], v[148:151], a[88:91]\n\t" \
  "v_exp_f32 v43, v43\n\t" \
  "v_mfma_f32_16x16x32_bf16 a[104:107], a[176:179], v[144:147], a[104:107]\n\t" \
  "v_cvt_pk_bf16_f32 v141, v42, v43\n\t" \
  "buffer_load_dwordx4 v230, %[sV], s96 offen offset:0 lds\n\t" \
  "v_mfma_f32_16x16x32_bf16 a[104:107], a[180:183], v[148:151], a[104:107]\n\t" \
  "v_exp_f32 v44, v44\n\t" \
  "v_mfma_f32_16x16x32_bf16 a[120:123], a[184:187], v[144:147], a[120:123]\n\t" \
  "v_exp_f32 v45, v45\n\t" \
  "v_mfma_f32_16x16x32_bf16 a[120:123], a[188:191], v[148:151], a[120:123]\n\t" \
  "v_cvt_pk_bf16_f32 v142, v44, v45\n\t" \
  "ds_read_b128 a[248:251], v222 offset:12288\n\t" \
  "v_smfmac_f32_16x16x64_bf16 v[168:171], v[216:219], v[144:151], v239\n\t" \
  "v_exp_f32 v46, v46\n\t" \
  "v_mfma_f32_16x16x32_bf16 a[12:15], a[128:131], v[152:155], a[12:15]\n\t" \
  "v_exp_f32 v47, v47\n\t" \
  "v_mfma_f32_16x16x32_bf16 a[12:15], a[132:135], v[156:159], a[12:15]\n\t" \
  "v_cvt_pk_bf16_f32 v143, v46, v47\n\t" \
  "buffer_load_dwordx4 v231, %[sV], s96 offen offset:1024 lds\n\t" \
  "v_mfma_f32_16x16x32_bf16 a[28:31], a[136:139], v[152:155], a[28:31]\n\t" \
  "v_exp_f32 v64, v64\n\t" \
  "v_mfma_f32_16x16x32_bf16 a[28:31], a[140:143], v[156:159], a[28:31]\n\t" \
  "v_exp_f32 v65, v65\n\t" \
  "v_mfma_f32_16x16x32_bf16 a[44:47], a[144:147], v[152:155], a[44:47]\n\t" \
  "v_cvt_pk_bf16_f32 v144, v64, v65\n\t" \
  "ds_read_b128 a[252:255], v223 offset:12288\n\t" \
  "v_mfma_f32_16x16x32_bf16 a[44:47], a[148:151], v[156:159], a[44:47]\n\t" \
  "v_exp_f32 v66, v66\n\t" \
  "v_mfma_f32_16x16x32_bf16 a[60:63], a[152:155], v[152:155], a[60:63]\n\t" \
  "buffer_load_dwordx4 v230, %[sV], s96 offen offset:2048 lds\n\t" \
  "buffer_load_dwordx4 v231, %[sV], s96 offen offset:3072 lds\n\t" \
  "v_mfma_f32_16x16x32_bf16 a[60:63], a[156:159], v[156:159], a[60:63]\n\t" \
  "v_exp_f32 v67, v67\n\t" \
  "v_mfma_f32_16x16x32_bf16 a[76:79], a[160:163], v[152:155], a[76:79]\n\t" \
  "v_cvt_pk_bf16_f32 v145, v66, v67\n\t" \
  "v_mfma_f32_16x16x32_bf16 a[76:79], a[164:167], v[156:159], a[76:79]\n\t" \
  "v_exp_f32 v68, v68\n\t" \
  "v_mfma_f32_16x16x32_bf16 a[92:95], a[168:171], v[152:155], a[92:95]\n\t" \
  "v_exp_f32 v69, v69\n\t" \
  "v_mfma_f32_16x16x32_bf16 a[92:95], a[172:175], v[156:159], a[92:95]\n\t" \
  "v_cvt_pk_bf16_f32 v146, v68, v69\n\t" \
  "v_mfma_f32_16x16x32_bf16 a[108:111], a[176:179], v[152:155], a[108:111]\n\t" \
  "s_add_u32 m0, %[m], 32768\n\t" \
  "s_mov_b32 s96, 6144\n\t" \
  "buffer_load_dwordx4 v232, %[sG], s96 offen offset:1024 lds\n\t" \
  "v_mfma_f32_16x16x32_bf16 a[108:111], a[180:183], v[156:159], a[108:111]\n\t" \
  "v_exp_f32 v70, v70\n\t" \
  "v_mfma_f32_16x16x32_bf16 a[124:127], a[184:187], v[152:155], a[124:127]\n\t" \
  "v_exp_f32 v71, v71\n\t" \
  "v_mfma_f32_16x16x32_bf16 a[124:127], a[188:191], v[156:159], a[124:127]\n\t" \
  "v_cvt_pk_bf16_f32 v147, v70, v71\n\t" \
  "v_smfmac_f32_16x16x64_bf16 v[172:175], v[216:219], v[152:159], v239\n\t" \
  "v_exp_f32 v72, v72\n\t" \
  "s_cmp_lg_u32 s99, 0\n\t" \
  "s_cbranch_scc1 .Lsf_p3\n\t" \
  ".Lsf_q3:\n\t" \
  "s_waitcnt lgkmcnt(0)\n\t" \
  "v_mfma_f32_16x16x32_bf16 v[0:3], a[192:195], v[16:19], v[176:179]\n\t" \
  "s_add_u32 s98, %[pf4], 28672\n\t" \
  "buffer_load_dword %[pfd], v245, %[sG], s98 offen\n\t" \
  "v_mfma_f32_16x16x32_bf16 v[0:3], a[196:199], v[20:23], v[0:3]\n\t" \
  "v_exp_f32 v73, v73\n\t" \
  "v_mfma_f32_16x16x32_bf16 v[0:3], a[200:203], v[24:27], v[0:3]\n\t" \
  "v_cvt_pk_bf16_f32 v148, v72, v73\n\t" \
  "ds_read_b64_tr_b16 a[128:129], v237 offset:0\n\t" \
  "v_mfma_f32_16x16x32_bf16 v[0:3], a[204:207], v[28:31], v[0:3]\n\t" \
  "v_exp_f32 v74, v74\n\t" \
  "v_mfma_f32_16x16x32_bf16 v[4:7], a[220:223], v[28:31], v[176:179]\n\t" \
  "ds_read_b64_tr_b16 a[130:131], v237 offset:4096\n\t" \
  "ds_read_b64_tr_b16 a[132:133], v237 offset:8192\n\t" \
  "v_mfma_f32_16x16x32_bf16 v[4:7], a[216:219], v[24:27], v[4:7]\n\t" \
  "v_exp_f32 v75, v75\n\t" \
  "v_mfma_f32_16x16x32_bf16 v[4:7], a[212:215], v[20:23], v[4:7]\n\t" \
  "v_cvt_pk_bf16_f32 v149, v74, v75\n\t" \
  "v_max3_f32 v192, v0, v1, v2\n\t" \
  "v_mfma_f32_16x16x32_bf16 v[4:7], a[208:211], v[16:19], v[4:7]\n\t" \
  "v_exp_f32 v76, v76\n\t" \
  "v_mfma_f32_16x16x32_bf16 v[8:11], a[224:227], v[16:19], v[176:179]\n\t" \
  "ds_read_b64_tr_b16 a[134:135], v237 offset:12288\n\t" \
  "ds_read_b64_tr_b16 a[136:137], v238 offset:0\n\t" \
  "v_mfma_f32_16x16x32_bf16 v[8:11], a[228:231], v[20:23], v[8:11]\n\t" \
  "v_exp_f32 v77, v77\n\t" \
  "v_mfma_f32_16x16x32_bf16 v[8:11], a[232:235], v[24:27], v[8:11]\n\t" \
  "v_cvt_pk_bf16_f32 v150, v76, v77\n\t" \
  "v_max3_f32 v203, v3, v4, v5\n\t" \
  "v_mfma_f32_16x16x32_bf16 v[8:11], a[236:239], v[28:31], v[8:11]\n\t" \
  "v_max3_f32 v192, v192, v6, v7\n\t" \
  "ds_read_b64_tr_b16 a[138:139], v238 offset:4096\n\t" \
  "v_mfma_f32_16x16x32_bf16 v[12:15], a[252:255], v[28:31], v[176:179]\n\t" \
  "ds_read_b64_tr_b16 a[140:141], v238 offset:8192\n\t" \
  "ds_read_b64_tr_b16 a[142:143], v238 offset:12288\n\t" \
  "v_mfma_f32_16x16x32_bf16 v[12:15], a[248:251], v[24:27], v[12:15]\n\t" \
  "v_exp_f32 v78, v78\n\t" \
  "v_mfma_f32_16x16x32_bf16 v[12:15], a[244:247], v[20:23], v[12:15]\n\t" \
  "v_max3_f32 v203, v203, v8, v9\n\t" \
  "v_max3_f32 v192, v192, v10, v11\n\t" \
  "v_mfma_f32_16x16x32_bf16 v[12:15], a[240:243], v[16:19], v[12:15]\n\t" \
  "s_add_u32 s98, %[pf2], 0\n\t" \
  "buffer_load_dword %[pfd], v202, %[sM], s98 offen\n\t" \
  "v_mfma_f32_16x16x32_bf16 v[32:35], a[192:195], v[48:51], v[180:183]\n\t" \
  "ds_read_b64_tr_b16 a[144:145], v237 offset:256\n\t" \
  "ds_read_b64_tr_b16 a[146:147], v237 offset:4352\n\t" \
  "v_mfma_f32_16x16x32_bf16 v[32:35], a[196:199], v[52:55], v[32:35]\n\t" \
  "v_exp_f32 v79, v79\n\t" \
  "v_mfma_f32_16x16x32_bf16 v[32:35], a[200:203], v[56:59], v[32:35]\n\t" \
  "v_cvt_pk_bf16_f32 v151, v78, v79\n\t" \
  "v_max3_f32 v203, v203, v12, v13\n\t" \
  "v_mfma_f32_16x16x32_bf16 v[32:35], a[204:207], v[60:63], v[32:35]\n\t" \
  "v_max3_f32 v192, v192, v14, v15\n\t" \
  "v_max_f32 v192, v192, v203\n\t" \
  "v_mfma_f32_16x16x32_bf16 v[36:39], a[220:223], v[60:63], v[180:183]\n\t" \
  "v_cmp_lt_f32 vcc, 0x42800000, v192\n\t" \
  "ds_read_b64_tr_b16 a[148:149], v237 offset:8448\n\t" \
  "v_mfma_f32_16x16x32_bf16 v[36:39], a[216:219], v[56:59], v[36:39]\n\t" \
  "s_cbranch_vccnz .Lsf_c40\n\t" \
  ".Lsf_r40:\n\t" \
  "ds_read_b64_tr_b16 a[150:151], v237 offset:12544\n\t" \
  "v_mfma_f32_16x16x32_bf16 v[36:39], a[212:215], v[52:55], v[36:39]\n\t" \
  "v_max3_f32 v192, v32, v33, v34\n\t" \
  "ds_read_b64_tr_b16 a[152:153], v238 offset:256\n\t" \
  "v_mfma_f32_16x16x32_bf16 v[36:39], a[208:211], v[48:51], v[36:39]\n\t" \
  "v_exp_f32 v96, v96\n\t" \
  "v_mfma_f32_16x16x32_bf16 v[40:43], a[224:227], v[48:51], v[180:183]\n\t" \
  "v_exp_f32 v97, v97\n\t" \
  "v_mfma_f32_16x16x32_bf16 v[40:43], a[228:231], v[52:55], v[40:43]\n\t" \
  "v_cvt_pk_bf16_f32 v152, v96, v97\n\t" \
  "ds_read_b64_tr_b16 a[154:155], v238 offset:4352\n\t" \
  "v_mfma_f32_16x16x32_bf16 v[40:43], a[232:235], v[56:59], v[40:43]\n\t" \
  "v_exp_f32 v98, v98\n\t" \
  "v_mfma_f32_16x16x32_bf16 v[40:43], a[236:239], v[60:63], v[40:43]\n\t" \
  "v_max3_f32 v203, v35, v36, v37\n\t" \
  "v_max3_f32 v192, v192, v38, v39\n\t" \
  "v_mfma_f32_16x16x32_bf16 v[44:47], a[252:255], v[60:63], v[180:183]\n\t" \
  "ds_read_b64_tr_b16 a[156:157], v238 offset:8448\n\t" \
  "ds_read_b64_tr_b16 a[158:159], v238 offset:12544\n\t" \
  "v_mfma_f32_16x16x32_bf16 v[44:47], a[248:251], v[56:59], v[44:47]\n\t" \
  "v_exp_f32 v99, v99\n\t" \
  "v_mfma_f32_16x16x32_bf16 v[44:47], a[244:247], v[52:55], v[44:47]\n\t" \
  "v_cvt_pk_bf16_f32 v153, v98, v99\n\t" \
  "ds_read_b64_tr_b16 a[160:161], v237 offset:512\n\t" \
  "v_mfma_f32_16x16x32_bf16 v[44:47], a[240:243], v[48:51], v[44:47]\n\t" \
  "v_max3_f32 v203, v203, v40, v41\n\t" \
  "v_max3_f32 v192, v192, v42, v43\n\t" \
  "v_mfma_f32_16x16x32_bf16 v[64:67], a[192:195], v[80:83], v[184:187]\n\t" \
  "ds_read_b64_tr_b16 a[162:163], v237 offset:4608\n\t" \
  "ds_read_b64_tr_b16 a[164:165], v237 offset:8704\n\t" \
  "v_mfma_f32_16x16x32_bf16 v[64:67], a[196:199], v[84:87], v[64:67]\n\t" \
  "v_exp_f32 v100, v100\n\t" \
  "v_mfma_f32_16x16x32_bf16 v[64:67], a[200:203], v[88:91], v[64:67]\n\t" \
  "v_max3_f32 v203, v203, v44, v45\n\t" \
  "v_max3_f32 v192, v192, v46, v47\n\t" \
  "v_mfma_f32_16x16x32_bf16 v[64:67], a[204:207], v[92:95], v[64:67]\n\t" \
  "v_max_f32 v192, v192, v203\n\t" \
  "ds_read_b64_tr_b16 a[166:167], v237 offset:12800\n\t" \
  "v_mfma_f32_16x16x32_bf16 v[68:71], a[220:223], v[92:95], v[184:187]\n\t" \
  "v_cmp_lt_f32 vcc, 0x42800000, v192\n\t" \
  "ds_read_b64_tr_b16 a[168:169], v238 offset:512\n\t" \
  "v_mfma_f32_16x16x32_bf16 v[68:71], a[216:219], v[88:91], v[68:71]\n\t" \
  "s_cbranch_vccnz .Lsf_c41\n\t" \
  ".Lsf_r41:\n\t" \
  "ds_read_b64_tr_b16 a[170:171], v238 offset:4608\n\t" \
  "v_mfma_f32_16x16x32_bf16 v[68:71], a[212:215], v[84:87], v[68:71]\n\t" \
  "v_max3_f32 v192, v64, v65, v66\n\t" \
  "ds_read_b64_tr_b16 a[172:173], v238 offset:8704\n\t" \
  "v_mfma_f32_16x16x32_bf16 v[68:71], a[208:211], v[80:83], v[68:71]\n\t" \
  "v_exp_f32 v101, v101\n\t" \
  "v_mfma_f32_16x16x32_bf16 v[72:75], a[224:227], v[80:83], v[184:187]\n\t" \
  "v_cvt_pk_bf16_f32 v154, v100, v101\n\t" \
  "ds_read_b64_tr_b16 a[174:175], v238 offset:12800\n\t" \
  "v_mfma_f32_16x16x32_bf16 v[72:75], a[228:231], v[84:87], v[72:75]\n\t" \
  "v_exp_f32 v102, v102\n\t" \
  "v_mfma_f32_16x16x32_bf16 v[72:75], a[232:235], v[88:91], v[72:75]\n\t" \
  "v_exp_f32 v103, v103\n\t" \
  "v_mfma_f32_16x16x32_bf16 v[72:75], a[236:239], v[92:95], v[72:75]\n\t" \
  "v_cvt_pk_bf16_f32 v155, v102, v103\n\t" \
  "v_max3_f32 v203, v67, v68, v69\n\t" \
  "v_mfma_f32_16x16x32_bf16 v[76:79], a[252:255], v[92:95], v[184:187]\n\t" \
  "v_max3_f32 v192, v192, v70, v71\n\t" \
  "ds_read_b64_tr_b16 a[176:177], v237 offset:768\n\t" \
  "v_mfma_f32_16x16x32_bf16 v[76:79], a[248:251], v[88:91], v[76:79]\n\t" \
  "v_exp_f32 v104, v104\n\t" \
  "v_mfma_f32_16x16x32_bf16 v[76:79], a[244:247], v[84:87], v[76:79]\n\t" \
  "ds_read_b64_tr_b16 a[178:179], v237 offset:4864\n\t" \
  "v_max3_f32 v203, v203, v72, v73\n\t" \
  "v_mfma_f32_16x16x32_bf16 v[76:79], a[240:243], v[80:83], v[76:79]\n\t" \
  "v_max3_f32 v192, v192, v74, v75\n\t" \
  "ds_read_b64_tr_b16 a[180:181], v237 offset:8960\n\t" \
  "v_mfma_f32_16x16x32_bf16 v[96:99], a[192:195], v[112:115], v[188:191]\n\t" \
  "v_exp_f32 v105, v105\n\t" \
  "v_mfma_f32_16x16x32_bf16 v[96:99], a[196:199], v[116:119], v[96:99]\n\t" \
  "v_cvt_pk_bf16_f32 v156, v104, v105\n\t" \
  "ds_read_b64_tr_b16 a[182:183], v237 offset:13056\n\t" \
  "v_mfma_f32_16x16x32_bf16 v[96:99], a[200:203], v[120:123], v[96:99]\n\t" \
  "v_max3_f32 v203, v203, v76, v77\n\t" \
  "v_max3_f32 v192, v192, v78, v79\n\t" \
  "v_mfma_f32_16x16x32_bf16 v[96:99], a[204:207], v[124:127], v[96:99]\n\t" \
  "v_max_f32 v192, v192, v203\n\t" \
  "ds_read_b64_tr_b16 a[184:185], v238 offset:768\n\t" \
  "v_mfma_f32_16x16x32_bf16 v[100:103], a[220:223], v[124:127], v[188:191]\n\t" \
  "v_cmp_lt_f32 vcc, 0x42800000, v192\n\t" \
  "ds_read_b64_tr_b16 a[186:187], v238 offset:4864\n\t" \
  "v_mfma_f32_16x16x32_bf16 v[100:103], a[216:219], v[120:123], v[100:103]\n\t" \
  "s_cbranch_vccnz .Lsf_c42\n\t" \
  ".Lsf_r42:\n\t" \
  "ds_read_b64_tr_b16 a[188:189], v238 offset:8960\n\t" \
  "v_mfma_f32_16x16x32_bf16 v[100:103], a[212:215], v[116:119], v[100:103]\n\t" \
  "v_max3_f32 v192, v96, v97, v98\n\t" \
  "ds_read_b64_tr_b16 a[190:191], v238 offset:13056\n\t" \
  "v_mfma_f32_16x16x32_bf16 v[100:103], a[208:211], v[112:115], v[100:103]\n\t" \
  "v_exp_f32 v106, v106\n\t" \
  "v_exp_f32 v107, v107\n\t" \
  "s_nop 0\n\t" \
  "v_cvt_pk_bf16_f32 v157, v106, v107\n\t" \
  "v_mfma_f32_16x16x32_bf16 v[104:107], a[224:227], v[112:115], v[188:191]\n\t" \
  "v_exp_f32 v108, v108\n\t" \
  "v_mfma_f32_16x16x32_bf16 v[104:107], a[228:231], v[116:119], v[104:107]\n\t" \
  "v_exp_f32 v109, v109\n\t" \
  "v_mfma_f32_16x16x32_bf16 v[104:107], a[232:235], v[120:123], v[104:107]\n\t" \
  "v_cvt_pk_bf16_f32 v158, v108, v109\n\t" \
  "v_max3_f32 v203, v99, v100, v101\n\t" \
  "v_mfma_f32_16x16x32_bf16 v[104:107], a[236:239], v[124:127], v[104:107]\n\t" \
  "v_max3_f32 v192, v192, v102, v103\n\t" \
  "v_exp_f32 v110, v110\n\t" \
  "v_exp_f32 v111, v111\n\t" \
  "s_nop 0\n\t" \
  "v_cvt_pk_bf16_f32 v159, v110, v111\n\t" \
  "v_mfma_f32_16x16x32_bf16 v[108:111], a[252:255], v[124:127], v[188:191]\n\t" \
  "v_exp_f32 v0, v0\n\t" \
  "v_mfma_f32_16x16x32_bf16 v[108:111], a[248:251], v[120:123], v[108:111]\n\t" \
  "v_exp_f32 v1, v1\n\t" \
  "v_mfma_f32_16x16x32_bf16 v[108:111], a[244:247], v[116:119], v[108:111]\n\t" \
  "v_max3_f32 v203, v203, v104, v105\n\t" \
  "v_max3_f32 v192, v192, v106, v107\n\t" \
  "v_mfma_f32_16x16x32_bf16 v[108:111], a[240:243], v[112:115], v[108:111]\n\t" \
  "v_exp_f32 v2, v2\n\t" \
  "s_waitcnt vmcnt(3) lgkmcnt(0)\n\t" \
  "s_barrier\n\t" \
  "v_mfma_f32_16x16x32_bf16 a[0:3], a[128:131], v[128:131], a[0:3]\n\t" \
  "v_exp_f32 v3, v3\n\t" \
  "v_mfma_f32_16x16x32_bf16 a[0:3], a[132:135], v[132:135], a[0:3]\n\t" \
  "v_exp_f32 v4, v4\n\t" \
  "v_mfma_f32_16x16x32_bf16 a[16:19], a[136:139], v[128:131], a[16:19]\n\t" \
  "ds_read_b128 a[192:195], v220 offset:16384\n\t" \
  "v_max3_f32 v203, v203, v108, v109\n\t" \
  "v_mfma_f32_16x16x32_bf16 a[16:19], a[140:143], v[132:135], a[16:19]\n\t" \
  "v_max3_f32 v192, v192, v110, v111\n\t" \
  "v_max_f32 v192, v192, v203\n\t" \
  "v_mfma_f32_16x16x32_bf16 a[32:35], a[144:147], v[128:131], a[32:35]\n\t" \
  "v_cmp_lt_f32 vcc, 0x42800000, v192\n\t" \
  "ds_read_b128 a[196:199], v221 offset:16384\n\t" \
  "v_mfma_f32_16x16x32_bf16 a[32:35], a[148:151], v[132:135], a[32:35]\n\t" \
  "s_cbranch_vccnz .Lsf_c43\n\t" \
  ".Lsf_r43:\n\t" \
  "ds_read_b128 a[200:203], v222 offset:16384\n\t" \
  "v_mfma_f32_16x16x32_bf16 a[48:51], a[152:155], v[128:131], a[48:51]\n\t" \
  "s_lshr_b32 s98, %[sofk], 6\n\t" \
  "v_xor_b32 v234, s98, v244\n\t" \
  "v_mfma_f32_16x16x32_bf16 a[48:51], a[156:159], v[132:135], a[48:51]\n\t" \
  "v_add_u32 v213, %[m], v242\n\t" \
  "v_add_u32 v215, 98304, v213\n\t" \
  "v_mfma_f32_16x16x32_bf16 a[64:67], a[160:163], v[128:131], a[64:67]\n\t" \
  "v_exp_f32 v5, v5\n\t" \
  "v_mfma_f32_16x16x32_bf16 a[64:67], a[164:167], v[132:135], a[64:67]\n\t" \
  "v_exp_f32 v6, v6\n\t" \
  "v_mfma_f32_16x16x32_bf16 a[80:83], a[168:171], v[128:131], a[80:83]\n\t" \
  "v_mov_b32 v243, %[tag]\n\t" \
  "v_lshrrev_b32 v254, 3, v233\n\t" \
  "v_mfma_f32_16x16x32_bf16 a[80:83], a[172:175], v[132:135], a[80:83]\n\t" \
  "ds_read_b128 a[204:207], v223 offset:16384\n\t" \
  "v_min_u32 v254, 1, v254\n\t" \
  "v_mfma_f32_16x16x32_bf16 a[96:99], a[176:179], v[128:131], a[96:99]\n\t" \
  "v_exp_f32 v7, v7\n\t" \
  "v_mfma_f32_16x16x32_bf16 a[96:99], a[180:183], v[132:135], a[96:99]\n\t" \
  "v_exp_f32 v8, v8\n\t" \
  "v_mfma_f32_16x16x32_bf16 a[112:115], a[184:187], v[128:131], a[112:115]\n\t" \
  "v_lshlrev_b32 v254, 31, v254\n\t" \
  "ds_read_b128 a[208:211], v220 offset:20480\n\t" \
  "v_mfma_f32_16x16x32_bf16 a[112:115], a[188:191], v[132:135], a[112:115]\n\t" \
  "v_exp_f32 v9, v9\n\t" \
  "v_smfmac_f32_16x16x64_bf16 v[160:163], v[216:219], v[128:135], v239\n\t" \
  "v_cvt_pk_bf16_f32 v128, v0, v1\n\t" \
  "v_cvt_pk_bf16_f32 v129, v2, v3\n\t" \
  "v_mfma_f32_16x16x32_bf16 a[4:7], a[128:131], v[136:139], a[4:7]\n\t" \
  "v_cvt_pk_bf16_f32 v130, v4, v5\n\t" \
  "v_cvt_pk_bf16_f32 v131, v6, v7\n\t" \
  "v_mfma_f32_16x16x32_bf16 a[4:7], a[132:135], v[140:143], a[4:7]\n\t" \
  "v_cvt_pk_bf16_f32 v132, v8, v9\n\t" \
  "v_lshl_or_b32 v254, v233, 9, v254\n\t" \
  "v_mfma_f32_16x16x32_bf16 a[20:23], a[136:139], v[136:139], a[20:23]\n\t" \
  "s_lshr_b32 m0, %[hl18], 18\n\t" \
  "s_brev_b32 s98, 1\n\t" \
  "v_writelane_b32 v254, s98, m0\n\t" \
  "v_mfma_f32_16x16x32_bf16 a[20:23], a[140:143], v[140:143], a[20:23]\n\t" \
  "ds_read_b128 a[212:215], v221 offset:20480\n\t" \
  "s_add_u32 m0, %[m], 49152\n\t" \
  "v_mfma_f32_16x16x32_bf16 a[36:39], a[144:147], v[136:139], a[36:39]\n\t" \
  "v_exp_f32 v10, v10\n\t" \
  "v_mfma_f32_16x16x32_bf16 a[36:39], a[148:151], v[140:143], a[36:39]\n\t" \
  "v_exp_f32 v11, v11\n\t" \
  "v_mfma_f32_16x16x32_bf16 a[52:55], a[152:155], v[136:139], a[52:55]\n\t" \
  "v_cvt_pk_bf16_f32 v133, v10, v11\n\t" \
  "ds_read_b128 a[216:219], v222 offset:20480\n\t" \
  "v_mfma_f32_16x16x32_bf16 a[52:55], a[156:159], v[140:143], a[52:55]\n\t" \
  "v_exp_f32 v12, v12\n\t" \
  "v_mfma_f32_16x16x32_bf16 a[68:71], a[160:163], v[136:139], a[68:71]\n\t" \
  "s_add_u32 s96, %[sofk], 98304\n\t" \
  "ds_read_b128 a[220:223], v223 offset:20480\n\t" \
  "v_mfma_f32_16x16x32_bf16 a[68:71], a[164:167], v[140:143], a[68:71]\n\t" \
  "v_exp_f32 v13, v13\n\t" \
  "v_mfma_f32_16x16x32_bf16 a[84:87], a[168:171], v[136:139], a[84:87]\n\t" \
  "v_cvt_pk_bf16_f32 v134, v12, v13\n\t" \
  "buffer_load_dwordx4 v224, %[sK], s96 offen offset:0 lds\n\t" \
  "v_mfma_f32_16x16x32_bf16 a[84:87], a[172:175], v[140:143], a[84:87]\n\t" \
  "v_exp_f32 v14, v14\n\t" \
  "v_mfma_f32_16x16x32_bf16 a[100:103], a[176:179], v[136:139], a[100:103]\n\t" \
  "v_exp_f32 v15, v15\n\t" \
  "v_mfma_f32_16x16x32_bf16 a[100:103], a[180:183], v[140:143], a[100:103]\n\t" \
  "v_cvt_pk_bf16_f32 v135, v14, v15\n\t" \
  "ds_read_b128 a[224:227], v220 offset:24576\n\t" \
  "v_mfma_f32_16x16x32_bf16 a[116:119], a[184:187], v[136:139], a[116:119]\n\t" \
  "v_exp_f32 v32, v32\n\t" \
  "v_mfma_f32_16x16x32_bf16 a[116:119], a[188:191], v[140:143], a[116:119]\n\t" \
  "buffer_load_dwordx4 v225, %[sK], s96 offen offset:1024 lds\n\t" \
  "ds_read_b128 a[228:231], v221 offset:24576\n\t" \
  "v_smfmac_f32_16x16x64_bf16 v[164:167], v[216:219], v[136:143], v239\n\t" \
  "v_exp_f32 v33, v33\n\t" \
  "v_mfma_f32_16x16x32_bf16 a[8:11], a[128:131], v[144:147], a[8:11]\n\t" \
  "v_cvt_pk_bf16_f32 v136, v32, v33\n\t" \
  "buffer_load_dwordx4 v226, %[sK], s96 offen offset:2048 lds\n\t" \
  "v_mfma_f32_16x16x32_bf16 a[8:11], a[132:135], v[148:151], a[8:11]\n\t" \
  "v_exp_f32 v34, v34\n\t" \
  "v_mfma_f32_16x16x32_bf16 a[24:27], a[136:139], v[144:147], a[24:27]\n\t" \
  "ds_read_b128 a[232:235], v222 offset:24576\n\t" \
  "buffer_load_dwordx4 v227, %[sK], s96 offen offset:3072 lds\n\t" \
  "v_mfma_f32_16x16x32_bf16 a[24:27], a[140:143], v[148:151], a[24:27]\n\t" \
  "v_exp_f32 v35, v35\n\t" \
  "v_mfma_f32_16x16x32_bf16 a[40:43], a[144:147], v[144:147], a[40:43]\n\t" \
  "v_cvt_pk_bf16_f32 v137, v34, v35\n\t" \
  "ds_read_b128 a[236:239], v223 offset:24576\n\t" \
  "v_mfma_f32_16x16x32_bf16 a[40:43], a[148:151], v[148:151], a[40:43]\n\t" \
  "v_exp_f32 v36, v36\n\t" \
  "v_mfma_f32_16x16x32_bf16 a[56:59], a[152:155], v[144:147], a[56:59]\n\t" \
  "v_exp_f32 v37, v37\n\t" \
  "v_mfma_f32_16x16x32_bf16 a[56:59], a[156:159], v[148:151], a[56:59]\n\t" \
  "v_cvt_pk_bf16_f32 v138, v36, v37\n\t" \
  "ds_read_b128 a[240:243], v220 offset:28672\n\t" \
  "v_mfma_f32_16x16x32_bf16 a[72:75], a[160:163], v[144:147], a[72:75]\n\t" \
  "s_add_u32 m0, %[m], 32768\n\t" \
  "s_mov_b32 s96, 12288\n\t" \
  "buffer_load_dwordx4 v232, %[sG], s96 offen offset:2048 lds\n\t" \
  "v_mfma_f32_16x16x32_bf16 a[72:75], a[164:167], v[148:151], a[72:75]\n\t" \
  "v_exp_f32 v38, v38\n\t" \
  "v_mfma_f32_16x16x32_bf16 a[88:91], a[168:171], v[144:147], a[88:91]\n\t" \
  "s_add_u32 m0, %[m], 147456\n\t" \
  "s_add_u32 s96, %[sofk], 81920\n\t" \
  "v_mfma_f32_16x16x32_bf16 a[88:91], a[172:175], v[148:151], a[88:91]\n\t" \
  "v_exp_f32 v39, v39\n\t" \
  "v_mfma_f32_16x16x32_bf16 a[104:107], a[176:179], v[144:147], a[104:107]\n\t" \
  "v_cvt_pk_bf16_f32 v139, v38, v39\n\t" \
  "ds_read_b128 a[244:247], v221 offset:28672\n\t" \
  "v_mfma_f32_16x16x32_bf16 a[104:107], a[180:183], v[148:151], a[104:107]\n\t" \
  "v_exp_f32 v40, v40\n\t" \
  "v_mfma_f32_16x16x32_bf16 a[120:123], a[184:187], v[144:147], a[120:123]\n\t" \
  "v_exp_f32 v41, v41\n\t" \
  "v_mfma_f32_16x16x32_bf16 a[120:123], a[188:191], v[148:151], a[120:123]\n\t" \
  "v_cvt_pk_bf16_f32 v140, v40, v41\n\t" \
  "buffer_load_dwordx4 v230, %[sV], s96 offen offset:0 lds\n\t" \
  "v_smfmac_f32_16x16x64_bf16 v[168:171], v[216:219], v[144:151], v239\n\t" \
  "v_exp_f32 v42, v42\n\t" \
  "v_mfma_f32_16x16x32_bf16 a[12:15], a[128:131], v[152:155], a[12:15]\n\t" \
  "ds_read_b128 a[248:251], v222 offset:28672\n\t" \
  "buffer_load_dwordx4 v231, %[sV], s96 offen offset:1024 lds\n\t" \
  "v_mfma_f32_16x16x32_bf16 a[12:15], a[132:135], v[156:159], a[12:15]\n\t" \
  "v_exp_f32 v43, v43\n\t" \
  "v_mfma_f32_16x16x32_bf16 a[28:31], a[136:139], v[152:155], a[28:31]\n\t" \
  "v_cvt_pk_bf16_f32 v141, v42, v43\n\t" \
  "ds_read_b128 a[252:255], v223 offset:28672\n\t" \
  "v_mfma_f32_16x16x32_bf16 a[28:31], a[140:143], v[156:159], a[28:31]\n\t" \
  "v_exp_f32 v44, v44\n\t" \
  "v_mfma_f32_16x16x32_bf16 a[44:47], a[144:147], v[152:155], a[44:47]\n\t" \
  "v_exp_f32 v45, v45\n\t" \
  "v_mfma_f32_16x16x32_bf16 a[44:47], a[148:151], v[156:159], a[44:47]\n\t" \
  "v_cvt_pk_bf16_f32 v142, v44, v45\n\t" \
  "buffer_load_dwordx4 v230, %[sV], s96 offen offset:2048 lds\n\t" \
  "v_mfma_f32_16x16x32_bf16 a[60:63], a[152:155], v[152:155], a[60:63]\n\t" \
  "v_exp_f32 v46, v46\n\t" \
  "v_mfma_f32_16x16x32_bf16 a[60:63], a[156:159], v[156:159], a[60:63]\n\t" \
  "v_exp_f32 v47, v47\n\t" \
  "v_mfma_f32_16x16x32_bf16 a[76:79], a[160:163], v[152:155], a[76:79]\n\t" \
  "v_cvt_pk_bf16_f32 v143, v46, v47\n\t" \
  "buffer_load_dwordx4 v231, %[sV], s96 offen offset:3072 lds\n\t" \
  "v_mfma_f32_16x16x32_bf16 a[76:79], a[164:167], v[156:159], a[76:79]\n\t" \
  "v_exp_f32 v64, v64\n\t" \
  "v_mfma_f32_16x16x32_bf16 a[92:95], a[168:171], v[152:155], a[92:95]\n\t" \
  "v_exp_f32 v65, v65\n\t" \
  "v_mfma_f32_16x16x32_bf16 a[92:95], a[172:175], v[156:159], a[92:95]\n\t" \
  "v_cvt_pk_bf16_f32 v144, v64, v65\n\t" \
  "v_mfma_f32_16x16x32_bf16 a[108:111], a[176:179], v[152:155], a[108:111]\n\t" \
  "s_add_u32 m0, %[m], 32768\n\t" \
  "s_mov_b32 s96, 18432\n\t" \
  "buffer_load_dwordx4 v232, %[sG], s96 offen offset:3072 lds\n\t" \
  "v_mfma_f32_16x16x32_bf16 a[108:111], a[180:183], v[156:159], a[108:111]\n\t" \
  "v_exp_f32 v66, v66\n\t" \
  "v_mfma_f32_16x16x32_bf16 a[124:127], a[184:187], v[152:155], a[124:127]\n\t" \
  "v_exp_f32 v67, v67\n\t" \
  "v_mfma_f32_16x16x32_bf16 a[124:127], a[188:191], v[156:159], a[124:127]\n\t" \
  "v_cvt_pk_bf16_f32 v145, v66, v67\n\t" \
  "v_smfmac_f32_16x16x64_bf16 v[172:175], v[216:219], v[152:159], v239\n\t" \
  "v_exp_f32 v68, v68\n\t" \
  "s_cmp_lg_u32 s99, 0\n\t" \
  "s_cbranch_scc1 .Lsf_p4\n\t" \
  ".Lsf_q4:\n\t" \
  "s_waitcnt lgkmcnt(0)\n\t" \
  "v_mfma_f32_16x16x32_bf16 v[0:3], a[192:195], v[16:19], v[176:179]\n\t" \
  "s_add_u32 s98, %[pf4], 43008\n\t" \
  "buffer_load_dword %[pfd], v245, %[sG], s98 offen\n\t" \
  "v_mfma_f32_16x16x32_bf16 v[0:3], a[196:199], v[20:23], v[0:3]\n\t" \
  "ds_read_b64_tr_b16 a[128:129], v228 offset:0\n\t" \
  "v_lshrrev_b32 v252, 3, v233\n\t" \
  "v_mfma_f32_16x16x32_bf16 v[0:3], a[200:203], v[24:27], v[0:3]\n\t" \
  "v_exp_f32 v69, v69\n\t" \
  "v_mfma_f32_16x16x32_bf16 v[0:3], a[204:207], v[28:31], v[0:3]\n\t" \
  "v_cvt_pk_bf16_f32 v146, v68, v69\n\t" \
  "ds_read_b64_tr_b16 a[130:131], v228 offset:4096\n\t" \
  "v_mfma_f32_16x16x32_bf16 v[4:7], a[220:223], v[28:31], v[176:179]\n\t" \
  "ds_read_b64_tr_b16 a[132:133], v228 offset:8192\n\t" \
  "v_mul_u32_u24 v247, 98176, v252\n\t" \
  "v_mfma_f32_16x16x32_bf16 v[4:7], a[216:219], v[24:27], v[4:7]\n\t" \
  "v_exp_f32 v70, v70\n\t" \
  "v_mfma_f32_16x16x32_bf16 v[4:7], a[212:215], v[20:23], v[4:7]\n\t" \
  "v_max3_f32 v192, v0, v1, v2\n\t" \
  "ds_read_b64_tr_b16 a[134:135], v228 offset:12288\n\t" \
  "v_mfma_f32_16x16x32_bf16 v[4:7], a[208:211], v[16:19], v[4:7]\n\t" \
  "v_exp_f32 v71, v71\n\t" \
  "v_mfma_f32_16x16x32_bf16 v[8:11], a[224:227], v[16:19], v[176:179]\n\t" \
  "v_cvt_pk_bf16_f32 v147, v70, v71\n\t" \
  "ds_read_b64_tr_b16 a[136:137], v229 offset:0\n\t" \
  "v_mfma_f32_16x16x32_bf16 v[8:11], a[228:231], v[20:23], v[8:11]\n\t" \
  "v_lshl_add_u32 v247, v233, 4, v247\n\t" \
  "ds_read_b64_tr_b16 a[138:139], v229 offset:4096\n\t" \
  "v_mfma_f32_16x16x32_bf16 v[8:11], a[232:235], v[24:27], v[8:11]\n\t" \
  "v_max3_f32 v203, v3, v4, v5\n\t" \
  "v_max3_f32 v192, v192, v6, v7\n\t" \
  "v_mfma_f32_16x16x32_bf16 v[8:11], a[236:239], v[28:31], v[8:11]\n\t" \
  "v_exp_f32 v72, v72\n\t" \
  "v_mfma_f32_16x16x32_bf16 v[12:15], a[252:255], v[28:31], v[176:179]\n\t" \
  "ds_read_b64_tr_b16 a[140:141], v229 offset:8192\n\t" \
  "v_lshlrev_b32 v252, 4, v252\n\t" \
  "v_mfma_f32_16x16x32_bf16 v[12:15], a[248:251], v[24:27], v[12:15]\n\t" \
  "s_add_u32 s98, %[pf2], 24576\n\t" \
  "buffer_load_dword %[pfd], v202, %[sM], s98 offen\n\t" \
  "v_mfma_f32_16x16x32_bf16 v[12:15], a[244:247], v[20:23], v[12:15]\n\t" \
  "v_max3_f32 v203, v203, v8, v9\n\t" \
  "v_max3_f32 v192, v192, v10, v11\n\t" \
  "v_mfma_f32_16x16x32_bf16 v[12:15], a[240:243], v[16:19], v[12:15]\n\t" \
  "ds_read_b64_tr_b16 a[142:143], v229 offset:12288\n\t" \
  "ds_read_b64_tr_b16 a[144:145], v228 offset:256\n\t" \
  "v_mfma_f32_16x16x32_bf16 v[32:35], a[192:195], v[48:51], v[180:183]\n\t" \
  "v_exp_f32 v73, v73\n\t" \
  "v_mfma_f32_16x16x32_bf16 v[32:35], a[196:199], v[52:55], v[32:35]\n\t" \
  "v_cvt_pk_bf16_f32 v148, v72, v73\n\t" \
  "ds_read_b64_tr_b16 a[146:147], v228 offset:4352\n\t" \
  "v_mfma_f32_16x16x32_bf16 v[32:35], a[200:203], v[56:59], v[32:35]\n\t" \
  "v_max3_f32 v203, v203, v12, v13\n\t" \
  "v_max3_f32 v192, v192, v14, v15\n\t" \
  "v_mfma_f32_16x16x32_bf16 v[32:35], a[204:207], v[60:63], v[32:35]\n\t" \
  "v_max_f32 v192, v192, v203\n\t" \
  "ds_read_b64_tr_b16 a[148:149], v228 offset:8448\n\t" \
  "v_mfma_f32_16x16x32_bf16 v[36:39], a[220:223], v[60:63], v[180:183]\n\t" \
  "v_cmp_lt_f32 vcc, 0x42800000, v192\n\t" \
  "ds_read_b64_tr_b16 a[150:151], v228 offset:12544\n\t" \
  "v_mfma_f32_16x16x32_bf16 v[36:39], a[216:219], v[56:59], v[36:39]\n\t" \
  "s_cbranch_vccnz .Lsf_c50\n\t" \
  ".Lsf_r50:\n\t" \
  "ds_read_b64_tr_b16 a[152:153], v229 offset:256\n\t" \
  "v_mfma_f32_16x16x32_bf16 v[36:39], a[212:215], v[52:55], v[36:39]\n\t" \
  "v_max3_f32 v192, v32, v33, v34\n\t" \
  "ds_read_b64_tr_b16 a[154:155], v229 offset:4352\n\t" \
  "v_mfma_f32_16x16x32_bf16 v[36:39], a[208:211], v[48:51], v[36:39]\n\t" \
  "v_exp_f32 v74, v74\n\t" \
  "v_mfma_f32_16x16x32_bf16 v[40:43], a[224:227], v[48:51], v[180:183]\n\t" \
  "v_exp_f32 v75, v75\n\t" \
  "v_mfma_f32_16x16x32_bf16 v[40:43], a[228:231], v[52:55], v[40:43]\n\t" \
  "v_cvt_pk_bf16_f32 v149, v74, v75\n\t" \
  "ds_read_b64_tr_b16 a[156:157], v229 offset:8448\n\t" \
  "v_mfma_f32_16x16x32_bf16 v[40:43], a[232:235], v[56:59], v[40:43]\n\t" \
  "v_exp_f32 v76, v76\n\t" \
  "v_mfma_f32_16x16x32_bf16 v[40:43], a[236:239], v[60:63], v[40:43]\n\t" \
  "v_max3_f32 v203, v35, v36, v37\n\t" \
  "v_max3_f32 v192, v192, v38, v39\n\t" \
  "v_mfma_f32_16x16x32_bf16 v[44:47], a[252:255], v[60:63], v[180:183]\n\t" \
  "v_exp_f32 v77, v77\n\t" \
  "v_mfma_f32_16x16x32_bf16 v[44:47], a[248:251], v[56:59], v[44:47]\n\t" \
  "v_cvt_pk_bf16_f32 v150, v76, v77\n\t" \
  "ds_read_b64_tr_b16 a[158:159], v229 offset:12544\n\t" \
  "v_mfma_f32_16x16x32_bf16 v[44:47], a[244:247], v[52:55], v[44:47]\n\t" \
  "ds_read_b64_tr_b16 a[160:161], v228 offset:512\n\t" \
  "v_max3_f32 v203, v203, v40, v41\n\t" \
  "v_mfma_f32_16x16x32_bf16 v[44:47], a[240:243], v[48:51], v[44:47]\n\t" \
  "v_max3_f32 v192, v192, v42, v43\n\t" \
  "ds_read_b64_tr_b16 a[162:163], v228 offset:4608\n\t" \
  "v_mfma_f32_16x16x32_bf16 v[64:67], a[192:195], v[80:83], v[184:187]\n\t" \
  "v_exp_f32 v78, v78\n\t" \
  "v_mfma_f32_16x16x32_bf16 v[64:67], a[196:199], v[84:87], v[64:67]\n\t" \
  "v_exp_f32 v79, v79\n\t" \
  "v_mfma_f32_16x16x32_bf16 v[64:67], a[200:203], v[88:91], v[64:67]\n\t" \
  "v_cvt_pk_bf16_f32 v151, v78, v79\n\t" \
  "ds_read_b64_tr_b16 a[164:165], v228 offset:8704\n\t" \
  "v_mfma_f32_16x16x32_bf16 v[64:67], a[204:207], v[92:95], v[64:67]\n\t" \
  "v_max3_f32 v203, v203, v44, v45\n\t" \
  "v_max3_f32 v192, v192, v46, v47\n\t" \
  "v_mfma_f32_16x16x32_bf16 v[68:71], a[220:223], v[92:95], v[184:187]\n\t" \
  "v_max_f32 v192, v192, v203\n\t" \
  "ds_read_b64_tr_b16 a[166:167], v228 offset:12800\n\t" \
  "v_mfma_f32_16x16x32_bf16 v[68:71], a[216:219], v[88:91], v[68:71]\n\t" \
  "v_cmp_lt_f32 vcc, 0x42800000, v192\n\t" \
  "ds_read_b64_tr_b16 a[168:169], v229 offset:512\n\t" \
  "v_mfma_f32_16x16x32_bf16 v[68:71], a[212:215], v[84:87], v[68:71]\n\t" \
  "s_cbranch_vccnz .Lsf_c51\n\t" \
  ".Lsf_r51:\n\t" \
  "v_max3_f32 v192, v64, v65, v66\n\t" \
  "v_mfma_f32_16x16x32_bf16 v[68:71], a[208:211], v[80:83], v[68:71]\n\t" \
  "ds_read_b64_tr_b16 a[170:171], v229 offset:4608\n\t" \
  "ds_read_b64_tr_b16 a[172:173], v229 offset:8704\n\t" \
  "v_mfma_f32_16x16x32_bf16 v[72:75], a[224:227], v[80:83], v[184:187]\n\t" \
  "v_exp_f32 v96, v96\n\t" \
  "v_mfma_f32_16x16x32_bf16 v[72:75], a[228:231], v[84:87], v[72:75]\n\t" \
  "v_exp_f32 v97, v97\n\t" \
  "v_mfma_f32_16x16x32_bf16 v[72:75], a[232:235], v[88:91], v[72:75]\n\t" \
  "v_cvt_pk_bf16_f32 v152, v96, v97\n\t" \
  "v_max3_f32 v203, v67, v68, v69\n\t" \
  "v_mfma_f32_16x16x32_bf16 v[72:75], a[236:239], v[92:95], v[72:75]\n\t" \
  "v_max3_f32 v192, v192, v70, v71\n\t" \
  "ds_read_b64_tr_b16 a[174:175], v229 offset:12800\n\t" \
  "v_mfma_f32_16x16x32_bf16 v[76:79], a[252:255], v[92:95], v[184:187]\n\t" \
  "ds_read_b64_tr_b16 a[176:177], v228 offset:768\n\t" \
  "ds_read_b64_tr_b16 a[178:179], v228 offset:4864\n\t" \
  "v_mfma_f32_16x16x32_bf16 v[76:79], a[248:251], v[88:91], v[76:79]\n\t" \
  "v_exp_f32 v98, v98\n\t" \
  "v_mfma_f32_16x16x32_bf16 v[76:79], a[244:247], v[84:87], v[76:79]\n\t" \
  "v_max3_f32 v203, v203, v72, v73\n\t" \
  "v_max3_f32 v192, v192, v74, v75\n\t" \
  "v_mfma_f32_16x16x32_bf16 v[76:79], a[240:243], v[80:83], v[76:79]\n\t" \
  "v_exp_f32 v99, v99\n\t" \
  "s_nop 0\n\t" \
  "v_cvt_pk_bf16_f32 v153, v98, v99\n\t" \
  "v_mfma_f32_16x16x32_bf16 v[96:99], a[192:195], v[112:115], v[188:191]\n\t" \
  "ds_read_b64_tr_b16 a[180:181], v228 offset:8960\n\t" \
  "ds_read_b64_tr_b16 a[182:183], v228 offset:13056\n\t" \
  "v_mfma_f32_16x16x32_bf16 v[96:99], a[196:199], v[116:119], v[96:99]\n\t" \
  "v_exp_f32 v100, v100\n\t" \
  "v_mfma_f32_16x16x32_bf16 v[96:99], a[200:203], v[120:123], v[96:99]\n\t" \
  "v_max3_f32 v203, v203, v76, v77\n\t" \
  "v_max3_f32 v192, v192, v78, v79\n\t" \
  "v_mfma_f32_16x16x32_bf16 v[96:99], a[204:207], v[124:127], v[96:99]\n\t" \
  "v_max_f32 v192, v192, v203\n\t" \
  "ds_read_b64_tr_b16 a[184:185], v229 offset:768\n\t" \
  "v_exp_f32 v101, v101\n\t" \
  "v_exp_f32 v102, v102\n\t" \
  "v_exp_f32 v103, v103\n\t" \
  "v_cvt_pk_bf16_f32 v154, v100, v101\n\t" \
  "v_cvt_pk_bf16_f32 v155, v102, v103\n\t" \
  "v_mfma_f32_16x16x32_bf16 v[100:103], a[220:223], v[124:127], v[188:191]\n\t" \
  "v_cmp_lt_f32 vcc, 0x42800000, v192\n\t" \
  "ds_read_b64_tr_b16 a[186:187], v229 offset:4864\n\t" \
  "v_mfma_f32_16x16x32_bf16 v[100:103], a[216:219], v[120:123], v[100:103]\n\t" \
  "s_cbranch_vccnz .Lsf_c52\n\t" \
  ".Lsf_r52:\n\t" \
  "v_max3_f32 v192, v96, v97, v98\n\t" \
  "v_mfma_f32_16x16x32_bf16 v[100:103], a[212:215], v[116:119], v[100:103]\n\t" \
  "v_exp_f32 v104, v104\n\t" \
  "v_mfma_f32_16x16x32_bf16 v[100:103], a[208:211], v[112:115], v[100:103]\n\t" \
  "ds_read_b64_tr_b16 a[188:189], v229 offset:8960\n\t" \
  "ds_read_b64_tr_b16 a[190:191], v229 offset:13056\n\t" \
  "v_exp_f32 v105, v105\n\t" \
  "v_exp_f32 v106, v106\n\t" \
  "v_exp_f32 v107, v107\n\t" \
  "v_cvt_pk_bf16_f32 v156, v104, v105\n\t" \
  "v_cvt_pk_bf16_f32 v157, v106, v107\n\t" \
  "v_mfma_f32_16x16x32_bf16 v[104:107], a[224:227], v[112:115], v[188:191]\n\t" \
  "v_exp_f32 v108, v108\n\t" \
  "v_mfma_f32_16x16x32_bf16 v[104:107], a[228:231], v[116:119], v[104:107]\n\t" \
  "v_exp_f32 v109, v109\n\t" \
  "v_mfma_f32_16x16x32_bf16 v[104:107], a[232:235], v[120:123], v[104:107]\n\t" \
  "v_cvt_pk_bf16_f32 v158, v108, v109\n\t" \
  "v_max3_f32 v203, v99, v100, v101\n\t" \
  "v_mfma_f32_16x16x32_bf16 v[104:107], a[236:239], v[124:127], v[104:107]\n\t" \
  "v_max3_f32 v192, v192, v102, v103\n\t" \
  "v_exp_f32 v110, v110\n\t" \
  "v_exp_f32 v111, v111\n\t" \
  "s_nop 0\n\t" \
  "v_cvt_pk_bf16_f32 v159, v110, v111\n\t" \
  "v_mfma_f32_16x16x32_bf16 v[108:111], a[252:255], v[124:127], v[188:191]\n\t" \
  "v_exp_f32 v0, v0\n\t" \
  "v_mfma_f32_16x16x32_bf16 v[108:111], a[248:251], v[120:123], v[108:111]\n\t" \
  "v_exp_f32 v1, v1\n\t" \
  "v_mfma_f32_16x16x32_bf16 v[108:111], a[244:247], v[116:119], v[108:111]\n\t" \
  "v_max3_f32 v203, v203, v104, v105\n\t" \
  "v_max3_f32 v192, v192, v106, v107\n\t" \
  "v_mfma_f32_16x16x32_bf16 v[108:111], a[240:243], v[112:115], v[108:111]\n\t" \
  "v_exp_f32 v2, v2\n\t" \
  "s_waitcnt vmcnt(3) lgkmcnt(0)\n\t" \
  "s_barrier\n\t" \
  "v_mfma_f32_16x16x32_bf16 a[0:3], a[128:131], v[128:131], a[0:3]\n\t" \
  "v_exp_f32 v3, v3\n\t" \
  "v_mfma_f32_16x16x32_bf16 a[0:3], a[132:135], v[132:135], a[0:3]\n\t" \
  "v_exp_f32 v4, v4\n\t" \
  "v_mfma_f32_16x16x32_bf16 a[16:19], a[136:139], v[128:131], a[16:19]\n\t" \
  "ds_read_b128 a[192:195], v220 offset:0\n\t" \
  "v_max3_f32 v203, v203, v108, v109\n\t" \
  "v_mfma_f32_16x16x32_bf16 a[16:19], a[140:143], v[132:135], a[16:19]\n\t" \
  "v_max3_f32 v192, v192, v110, v111\n\t" \
  "v_max_f32 v192, v192, v203\n\t" \
  "v_mfma_f32_16x16x32_bf16 a[32:35], a[144:147], v[128:131], a[32:35]\n\t" \
  "v_cmp_lt_f32 vcc, 0x42800000, v192\n\t" \
  "ds_read_b128 a[196:199], v221 offset:0\n\t" \
  "v_mfma_f32_16x16x32_bf16 a[32:35], a[148:151], v[132:135], a[32:35]\n\t" \
  "s_cbranch_vccnz .Lsf_c53\n\t" \
  ".Lsf_r53:\n\t" \
  "ds_read_b128 a[200:203], v222 offset:0\n\t" \
  "v_mfma_f32_16x16x32_bf16 a[48:51], a[152:155], v[128:131], a[48:51]\n\t" \
  "v_exp_f32 v5, v5\n\t" \
  "v_mfma_f32_16x16x32_bf16 a[48:51], a[156:159], v[132:135], a[48:51]\n\t" \
  "v_exp_f32 v6, v6\n\t" \
  "v_mfma_f32_16x16x32_bf16 a[64:67], a[160:163], v[128:131], a[64:67]\n\t" \
  "v_exp_f32 v7, v7\n\t" \
  "v_mfma_f32_16x16x32_bf16 a[64:67], a[164:167], v[132:135], a[64:67]\n\t" \
  "v_exp_f32 v8, v8\n\t" \
  "v_mfma_f32_16x16x32_bf16 a[80:83], a[168:171], v[128:131], a[80:83]\n\t" \
  "v_exp_f32 v9, v9\n\t" \
  "v_mfma_f32_16x16x32_bf16 a[80:83], a[172:175], v[132:135], a[80:83]\n\t" \
  "v_exp_f32 v10, v10\n\t" \
  "v_mfma_f32_16x16x32_bf16 a[96:99], a[176:179], v[128:131], a[96:99]\n\t" \
  "v_exp_f32 v11, v11\n\t" \
  "v_mfma_f32_16x16x32_bf16 a[96:99], a[180:183], v[132:135], a[96:99]\n\t" \
  "ds_read_b128 a[204:207], v223 offset:0\n\t" \
  "ds_read_b128 a[208:211], v220 offset:4096\n\t" \
  "v_mfma_f32_16x16x32_bf16 a[112:115], a[184:187], v[128:131], a[112:115]\n\t" \
  "v_exp_f32 v12, v12\n\t" \
  "v_mfma_f32_16x16x32_bf16 a[112:115], a[188:191], v[132:135], a[112:115]\n\t" \
  "v_exp_f32 v13, v13\n\t" \
  "v_smfmac_f32_16x16x64_bf16 v[160:163], v[216:219], v[128:135], v239\n\t" \
  "v_cvt_pk_bf16_f32 v128, v0, v1\n\t" \
  "v_cvt_pk_bf16_f32 v129, v2, v3\n\t" \
  "v_mfma_f32_16x16x32_bf16 a[4:7], a[128:131], v[136:139], a[4:7]\n\t" \
  "v_cvt_pk_bf16_f32 v130, v4, v5\n\t" \
  "v_cvt_pk_bf16_f32 v131, v6, v7\n\t" \
  "v_mfma_f32_16x16x32_bf16 a[4:7], a[132:135], v[140:143], a[4:7]\n\t" \
  "v_cvt_pk_bf16_f32 v132, v8, v9\n\t" \
  "v_cvt_pk_bf16_f32 v133, v10, v11\n\t" \
  "v_mfma_f32_16x16x32_bf16 a[20:23], a[136:139], v[136:139], a[20:23]\n\t" \
  "v_cvt_pk_bf16_f32 v134, v12, v13\n\t" \
  "ds_read_b128 a[212:215], v221 offset:4096\n\t" \
  "v_mfma_f32_16x16x32_bf16 a[20:23], a[140:143], v[140:143], a[20:23]\n\t" \
  "v_exp_f32 v14, v14\n\t" \
  "v_mfma_f32_16x16x32_bf16 a[36:39], a[144:147], v[136:139], a[36:39]\n\t" \
  "s_add_u32 m0, %[m], 65536\n\t" \
  "ds_read_b128 a[216:219], v222 offset:4096\n\t" \
  "v_mfma_f32_16x16x32_bf16 a[36:39], a[148:151], v[140:143], a[36:39]\n\t" \
  "v_exp_f32 v15, v15\n\t" \
  "v_mfma_f32_16x16x32_bf16 a[52:55], a[152:155], v[136:139], a[52:55]\n\t" \
  "v_cvt_pk_bf16_f32 v135, v14, v15\n\t" \
  "s_add_u32 s96, %[sofk], 114688\n\t" \
  "v_mfma_f32_16x16x32_bf16 a[52:55], a[156:159], v[140:143], a[52:55]\n\t" \
  "v_exp_f32 v32, v32\n\t" \
  "v_mfma_f32_16x16x32_bf16 a[68:71], a[160:163], v[136:139], a[68:71]\n\t" \
  "v_exp_f32 v33, v33\n\t" \
  "v_mfma_f32_16x16x32_bf16 a[68:71], a[164:167], v[140:143], a[68:71]\n\t" \
  "v_exp_f32 v34, v34\n\t" \
  "v_mfma_f32_16x16x32_bf16 a[84:87], a[168:171], v[136:139], a[84:87]\n\t" \
  "ds_read_b128 a[220:223], v223 offset:4096\n\t" \
  "buffer_load_dwordx4 v224, %[sK], s96 offen offset:0 lds\n\t" \
  "v_mfma_f32_16x16x32_bf16 a[84:87], a[172:175], v[140:143], a[84:87]\n\t" \
  "v_exp_f32 v35, v35\n\t" \
  "v_mfma_f32_16x16x32_bf16 a[100:103], a[176:179], v[136:139], a[100:103]\n\t" \
  "v_exp_f32 v36, v36\n\t" \
  "v_mfma_f32_16x16x32_bf16 a[100:103], a[180:183], v[140:143], a[100:103]\n\t" \
  "ds_read_b128 a[224:227], v220 offset:8192\n\t" \
  "buffer_load_dwordx4 v225, %[sK], s96 offen offset:1024 lds\n\t" \
  "v_mfma_f32_16x16x32_bf16 a[116:119], a[184:187], v[136:139], a[116:119]\n\t" \
  "v_exp_f32 v37, v37\n\t" \
  "v_mfma_f32_16x16x32_bf16 a[116:119], a[188:191], v[140:143], a[116:119]\n\t" \
  "v_exp_f32 v38, v38\n\t" \
  "v_smfmac_f32_16x16x64_bf16 v[164:167], v[216:219], v[136:143], v239\n\t" \
  "v_cvt_pk_bf16_f32 v136, v32, v33\n\t" \
  "v_cvt_pk_bf16_f32 v137, v34, v35\n\t" \
  "v_mfma_f32_16x16x32_bf16 a[8:11], a[128:131], v[144:147], a[8:11]\n\t" \
  "v_cvt_pk_bf16_f32 v138, v36, v37\n\t" \
  "ds_read_b128 a[228:231], v221 offset:8192\n\t" \
  "v_mfma_f32_16x16x32_bf16 a[8:11], a[132:135], v[148:151], a[8:11]\n\t" \
  "buffer_load_dwordx4 v226, %[sK], s96 offen offset:2048 lds\n\t" \
  "ds_read_b128 a[232:235], v222 offset:8192\n\t" \
  "v_mfma_f32_16x16x32_bf16 a[24:27], a[136:139], v[144:147], a[24:27]\n\t" \
  "v_exp_f32 v39, v39\n\t" \
  "v_mfma_f32_16x16x32_bf16 a[24:27], a[140:143], v[148:151], a[24:27]\n\t" \
  "v_cvt_pk_bf16_f32 v139, v38, v39\n\t" \
  "buffer_load_dwordx4 v227, %[sK], s96 offen offset:3072 lds\n\t" \
  "v_mfma_f32_16x16x32_bf16 a[40:43], a[144:147], v[144:147], a[40:43]\n\t" \
  "v_exp_f32 v40, v40\n\t" \
  "v_mfma_f32_16x16x32_bf16 a[40:43], a[148:151], v[148:151], a[40:43]\n\t" \
  "ds_read_b128 a[236:239], v223 offset:8192\n\t" \
  "ds_read_b128 a[240:243], v220 offset:12288\n\t" \
  "v_mfma_f32_16x16x32_bf16 a[56:59], a[152:155], v[144:147], a[56:59]\n\t" \
  "v_exp_f32 v41, v41\n\t" \
  "v_mfma_f32_16x16x32_bf16 a[56:59], a[156:159], v[148:151], a[56:59]\n\t" \
  "v_cvt_pk_bf16_f32 v140, v40, v41\n\t" \
  "ds_read_b128 a[244:247], v221 offset:12288\n\t" \
  "v_mfma_f32_16x16x32_bf16 a[72:75], a[160:163], v[144:147], a[72:75]\n\t" \
  "s_add_u32 m0, %[m], 98304\n\t" \
  "s_mov_b32 s96, 28672\n\t" \
  "buffer_load_dwordx4 v232, %[sG], s96 offen offset:0 lds\n\t" \
  "v_mfma_f32_16x16x32_bf16 a[72:75], a[164:167], v[148:151], a[72:75]\n\t" \
  "v_exp_f32 v42, v42\n\t" \
  "v_mfma_f32_16x16x32_bf16 a[88:91], a[168:171], v[144:147], a[88:91]\n\t" \
  "s_add_u32 m0, %[m], 81920\n\t" \
  "s_add_u32 s96, %[sofk], 98304\n\t" \
  "v_mfma_f32_16x16x32_bf16 a[88:91], a[172:175], v[148:151], a[88:91]\n\t" \
  "v_exp_f32 v43, v43\n\t" \
  "v_mfma_f32_16x16x32_bf16 a[104:107], a[176:179], v[144:147], a[104:107]\n\t" \
  "v_cvt_pk_bf16_f32 v141, v42, v43\n\t" \
  "buffer_load_dwordx4 v230, %[sV], s96 offen offset:0 lds\n\t" \
  "v_mfma_f32_16x16x32_bf16 a[104:107], a[180:183], v[148:151], a[104:107]\n\t" \
  "v_exp_f32 v44, v44\n\t" \
  "v_mfma_f32_16x16x32_bf16 a[120:123], a[184:187], v[144:147], a[120:123]\n\t" \
  "v_exp_f32 v45, v45\n\t" \
  "v_mfma_f32_16x16x32_bf16 a[120:123], a[188:191], v[148:151], a[120:123]\n\t" \
  "v_cvt_pk_bf16_f32 v142, v44, v45\n\t" \
  "ds_read_b128 a[248:251], v222 offset:12288\n\t" \
  "v_smfmac_f32_16x16x64_bf16 v[168:171], v[216:219], v[144:151], v239\n\t" \
  "v_exp_f32 v46, v46\n\t" \
  "v_mfma_f32_16x16x32_bf16 a[12:15], a[128:131], v[152:155], a[12:15]\n\t" \
  "v_exp_f32 v47, v47\n\t" \
  "v_mfma_f32_16x16x32_bf16 a[12:15], a[132:135], v[156:159], a[12:15]\n\t" \
  "v_cvt_pk_bf16_f32 v143, v46, v47\n\t" \
  "buffer_load_dwordx4 v231, %[sV], s96 offen offset:1024 lds\n\t" \
  "v_mfma_f32_16x16x32_bf16 a[28:31], a[136:139], v[152:155], a[28:31]\n\t" \
  "v_exp_f32 v64, v64\n\t" \
  "v_mfma_f32_16x16x32_bf16 a[28:31], a[140:143], v[156:159], a[28:31]\n\t" \
  "v_exp_f32 v65, v65\n\t" \
  "v_mfma_f32_16x16x32_bf16 a[44:47], a[144:147], v[152:155], a[44:47]\n\t" \
  "v_cvt_pk_bf16_f32 v144, v64, v65\n\t" \
  "ds_read_b128 a[252:255], v223 offset:12288\n\t" \
  "v_mfma_f32_16x16x32_bf16 a[44:47], a[148:151], v[156:159], a[44:47]\n\t" \
  "v_exp_f32 v66, v66\n\t" \
  "v_mfma_f32_16x16x32_bf16 a[60:63], a[152:155], v[152:155], a[60:63]\n\t" \
  "buffer_load_dwordx4 v230, %[sV], s96 offen offset:2048 lds\n\t" \
  "buffer_load_dwordx4 v231, %[sV], s96 offen offset:3072 lds\n\t" \
  "v_mfma_f32_16x16x32_bf16 a[60:63], a[156:159], v[156:159], a[60:63]\n\t" \
  "v_exp_f32 v67, v67\n\t" \
  "v_mfma_f32_16x16x32_bf16 a[76:79], a[160:163], v[152:155], a[76:79]\n\t" \
  "v_cvt_pk_bf16_f32 v145, v66, v67\n\t" \
  "v_mfma_f32_16x16x32_bf16 a[76:79], a[164:167], v[156:159], a[76:79]\n\t" \
  "v_lshrrev_b32 v205, 1, v233\n\t" \
  "v_lshlrev_b32 v205, 2, v205\n\t" \
  "v_mfma_f32_16x16x32_bf16 a[92:95], a[168:171], v[152:155], a[92:95]\n\t" \
  "v_add_u32 v205, 8192, v205\n\t" \
  "buffer_load_dword v209, v205, %[ws], 0 offen sc1\n\t" \
  "v_mfma_f32_16x16x32_bf16 a[92:95], a[172:175], v[156:159], a[92:95]\n\t" \
  "v_exp_f32 v68, v68\n\t" \
  "v_mfma_f32_16x16x32_bf16 a[108:111], a[176:179], v[152:155], a[108:111]\n\t" \
  "s_add_u32 m0, %[m], 98304\n\t" \
  "s_mov_b32 s96, 34816\n\t" \
  "buffer_load_dwordx4 v232, %[sG], s96 offen offset:1024 lds\n\t" \
  "v_mfma_f32_16x16x32_bf16 a[108:111], a[180:183], v[156:159], a[108:111]\n\t" \
  "v_exp_f32 v69, v69\n\t" \
  "v_mfma_f32_16x16x32_bf16 a[124:127], a[184:187], v[152:155], a[124:127]\n\t" \
  "v_cvt_pk_bf16_f32 v146, v68, v69\n\t" \
  "v_mfma_f32_16x16x32_bf16 a[124:127], a[188:191], v[156:159], a[124:127]\n\t" \
  "v_exp_f32 v70, v70\n\t" \
  "v_smfmac_f32_16x16x64_bf16 v[172:175], v[216:219], v[152:159], v239\n\t" \
  "v_exp_f32 v71, v71\n\t" \
  "s_cmp_lg_u32 s99, 0\n\t" \
  "s_cbranch_scc1 .Lsf_p5\n\t" \
  ".Lsf_q5:\n\t" \
  "s_waitcnt lgkmcnt(0)\n\t" \
  "v_mfma_f32_16x16x32_bf16 v[0:3], a[192:195], v[16:19], v[176:179]\n\t" \
  "v_cvt_pk_bf16_f32 v147, v70, v71\n\t" \
  "ds_read_b64_tr_b16 a[128:129], v237 offset:0\n\t" \
  "v_mfma_f32_16x16x32_bf16 v[0:3], a[196:199], v[20:23], v[0:3]\n\t" \
  "s_add_u32 s98, %[pf2], 49152\n\t" \
  "buffer_load_dword %[pfd], v202, %[sM], s98 offen\n\t" \
  "v_mfma_f32_16x16x32_bf16 v[0:3], a[200:203], v[24:27], v[0:3]\n\t" \
  "v_exp_f32 v72, v72\n\t" \
  "v_mfma_f32_16x16x32_bf16 v[0:3], a[204:207], v[28:31], v[0:3]\n\t" \
  "v_exp_f32 v73, v73\n\t" \
  "v_mfma_f32_16x16x32_bf16 v[4:7], a[220:223], v[28:31], v[176:179]\n\t" \
  "v_cvt_pk_bf16_f32 v148, v72, v73\n\t" \
  "ds_read_b64_tr_b16 a[130:131], v237 offset:4096\n\t" \
  "v_mfma_f32_16x16x32_bf16 v[4:7], a[216:219], v[24:27], v[4:7]\n\t" \
  "v_exp_f32 v74, v74\n\t" \
  "v_mfma_f32_16x16x32_bf16 v[4:7], a[212:215], v[20:23], v[4:7]\n\t" \
  "ds_read_b64_tr_b16 a[132:133], v237 offset:8192\n\t" \
  "v_max3_f32 v192, v0, v1, v2\n\t" \
  "v_mfma_f32_16x16x32_bf16 v[4:7], a[208:211], v[16:19], v[4:7]\n\t" \
  "v_exp_f32 v75, v75\n\t" \
  "v_mfma_f32_16x16x32_bf16 v[8:11], a[224:227], v[16:19], v[176:179]\n\t" \
  "v_cvt_pk_bf16_f32 v149, v74, v75\n\t" \
  "ds_read_b64_tr_b16 a[134:135], v237 offset:12288\n\t" \
  "v_mfma_f32_16x16x32_bf16 v[8:11], a[228:231], v[20:23], v[8:11]\n\t" \
  "ds_read_b64_tr_b16 a[136:137], v238 offset:0\n\t" \
  "ds_read_b64_tr_b16 a[138:139], v238 offset:4096\n\t" \
  "v_mfma_f32_16x16x32_bf16 v[8:11], a[232:235], v[24:27], v[8:11]\n\t" \
  "v_max3_f32 v203, v3, v4, v5\n\t" \
  "v_max3_f32 v192, v192, v6, v7\n\t" \
  "v_mfma_f32_16x16x32_bf16 v[8:11], a[236:239], v[28:31], v[8:11]\n\t" \
  "v_exp_f32 v76, v76\n\t" \
  "v_mfma_f32_16x16x32_bf16 v[12:15], a[252:255], v[28:31], v[176:179]\n\t" \
  "ds_read_b64_tr_b16 a[140:141], v238 offset:8192\n\t" \
  "ds_read_b64_tr_b16 a[142:143], v238 offset:12288\n\t" \
  "v_mfma_f32_16x16x32_bf16 v[12:15], a[248:251], v[24:27], v[12:15]\n\t" \
  "v_exp_f32 v77, v77\n\t" \
  "v_mfma_f32_16x16x32_bf16 v[12:15], a[244:247], v[20:23], v[12:15]\n\t" \
  "v_cvt_pk_bf16_f32 v150, v76, v77\n\t" \
  "v_max3_f32 v203, v203, v8, v9\n\t" \
  "v_mfma_f32_16x16x32_bf16 v[12:15], a[240:243], v[16:19], v[12:15]\n\t" \
  "v_max3_f32 v192, v192, v10, v11\n\t" \
  "ds_read_b64_tr_b16 a[144:145], v237 offset:256\n\t" \
  "v_mfma_f32_16x16x32_bf16 v[32:35], a[192:195], v[48:51], v[180:183]\n\t" \
  "s_add_u32 s98, %[pf2], 73728\n\t" \
  "buffer_load_dword %[pfd], v202, %[sM], s98 offen\n\t" \
  "v_mfma_f32_16x16x32_bf16 v[32:35], a[196:199], v[52:55], v[32:35]\n\t" \
  "v_exp_f32 v78, v78\n\t" \
  "v_mfma_f32_16x16x32_bf16 v[32:35], a[200:203], v[56:59], v[32:35]\n\t" \
  "ds_read_b64_tr_b16 a[146:147], v237 offset:4352\n\t" \
  "v_max3_f32 v203, v203, v12, v13\n\t" \
  "v_mfma_f32_16x16x32_bf16 v[32:35], a[204:207], v[60:63], v[32:35]\n\t" \
  "v_max3_f32 v192, v192, v14, v15\n\t" \
  "v_max_f32 v192, v192, v203\n\t" \
  "v_mfma_f32_16x16x32_bf16 v[36:39], a[220:223], v[60:63], v[180:183]\n\t" \
  "v_cmp_lt_f32 vcc, 0x42800000, v192\n\t" \
  "ds_read_b64_tr_b16 a[148:149], v237 offset:8448\n\t" \
  "v_mfma_f32_16x16x32_bf16 v[36:39], a[216:219], v[56:59], v[36:39]\n\t" \
  "s_cbranch_vccnz .Lsf_c60\n\t" \
  ".Lsf_r60:\n\t" \
  "ds_read_b64_tr_b16 a[150:151], v237 offset:12544\n\t" \
  "v_mfma_f32_16x16x32_bf16 v[36:39], a[212:215], v[52:55], v[36:39]\n\t" \
  "v_max3_f32 v192, v32, v33, v34\n\t" \
  "ds_read_b64_tr_b16 a[152:153], v238 offset:256\n\t" \
  "v_mfma_f32_16x16x32_bf16 v[36:39], a[208:211], v[48:51], v[36:39]\n\t" \
  "v_exp_f32 v79, v79\n\t" \
  "v_mfma_f32_16x16x32_bf16 v[40:43], a[224:227], v[48:51], v[180:183]\n\t" \
  "v_cvt_pk_bf16_f32 v151, v78, v79\n\t" \
  "ds_read_b64_tr_b16 a[154:155], v238 offset:4352\n\t" \
  "v_mfma_f32_16x16x32_bf16 v[40:43], a[228:231], v[52:55], v[40:43]\n\t" \
  "v_exp_f32 v96, v96\n\t" \
  "v_mfma_f32_16x16x32_bf16 v[40:43], a[232:235], v[56:59], v[40:43]\n\t" \
  "v_exp_f32 v97, v97\n\t" \
  "v_mfma_f32_16x16x32_bf16 v[40:43], a[236:239], v[60:63], v[40:43]\n\t" \
  "v_cvt_pk_bf16_f32 v152, v96, v97\n\t" \
  "v_max3_f32 v203, v35, v36, v37\n\t" \
  "v_mfma_f32_16x16x32_bf16 v[44:47], a[252:255], v[60:63], v[180:183]\n\t" \
  "v_max3_f32 v192, v192, v38, v39\n\t" \
  "ds_read_b64_tr_b16 a[156:157], v238 offset:8448\n\t" \
  "v_mfma_f32_16x16x32_bf16 v[44:47], a[248:251], v[56:59], v[44:47]\n\t" \
  "ds_read_b64_tr_b16 a[158:159], v238 offset:12544\n\t" \
  "ds_read_b64_tr_b16 a[160:161], v237 offset:512\n\t" \
  "v_mfma_f32_16x16x32_bf16 v[44:47], a[244:247], v[52:55], v[44:47]\n\t" \
  "v_max3_f32 v203, v203, v40, v41\n\t" \
  "v_max3_f32 v192, v192, v42, v43\n\t" \
  "v_mfma_f32_16x16x32_bf16 v[44:47], a[240:243], v[48:51], v[44:47]\n\t" \
  "v_exp_f32 v98, v98\n\t" \
  "v_mfma_f32_16x16x32_bf16 v[64:67], a[192:195], v[80:83], v[184:187]\n\t" \
  "ds_read_b64_tr_b16 a[162:163], v237 offset:4608\n\t" \
  "ds_read_b64_tr_b16 a[164:165], v237 offset:8704\n\t" \
  "v_mfma_f32_16x16x32_bf16 v[64:67], a[196:199], v[84:87], v[64:67]\n\t" \
  "v_exp_f32 v99, v99\n\t" \
  "v_mfma_f32_16x16x32_bf16 v[64:67], a[200:203], v[88:91], v[64:67]\n\t" \
  "v_cvt_pk_bf16_f32 v153, v98, v99\n\t" \
  "v_max3_f32 v203, v203, v44, v45\n\t" \
  "v_mfma_f32_16x16x32_bf16 v[64:67], a[204:207], v[92:95], v[64:67]\n\t" \
  "v_max3_f32 v192, v192, v46, v47\n\t" \
  "v_max_f32 v192, v192, v203\n\t" \
  "v_mfma_f32_16x16x32_bf16 v[68:71], a[220:223], v[92:95], v[184:187]\n\t" \
  "v_cmp_lt_f32 vcc, 0x42800000, v192\n\t" \
  "ds_read_b64_tr_b16 a[166:167], v237 offset:12800\n\t" \
  "v_mfma_f32_16x16x32_bf16 v[68:71], a[216:219], v[88:91], v[68:71]\n\t" \
  "s_cbranch_vccnz .Lsf_c61\n\t" \
  ".Lsf_r61:\n\t" \
  "ds_read_b64_tr_b16 a[168:169], v238 offset:512\n\t" \
  "v_mfma_f32_16x16x32_bf16 v[68:71], a[212:215], v[84:87], v[68:71]\n\t" \
  "v_max3_f32 v192, v64, v65, v66\n\t" \
  "ds_read_b64_tr_b16 a[170:171], v238 offset:4608\n\t" \
  "v_mfma_f32_16x16x32_bf16 v[68:71], a[208:211], v[80:83], v[68:71]\n\t" \
  "v_exp_f32 v100, v100\n\t" \
  "v_mfma_f32_16x16x32_bf16 v[72:75], a[224:227], v[80:83], v[184:187]\n\t" \
  "ds_read_b64_tr_b16 a[172:173], v238 offset:8704\n\t" \
  "ds_read_b64_tr_b16 a[174:175], v238 offset:12800\n\t" \
  "v_mfma_f32_16x16x32_bf16 v[72:75], a[228:231], v[84:87], v[72:75]\n\t" \
  "v_exp_f32 v101, v101\n\t" \
  "v_mfma_f32_16x16x32_bf16 v[72:75], a[232:235], v[88:91], v[72:75]\n\t" \
  "v_cvt_pk_bf16_f32 v154, v100, v101\n\t" \
  "v_max3_f32 v203, v67, v68, v69\n\t" \
  "v_mfma_f32_16x16x32_bf16 v[72:75], a[236:239], v[92:95], v[72:75]\n\t" \
  "s_lshr_b32 s98, %[hl18], 18\n\t" \
  "v_xor_b32 v245, s98, v233\n\t" \
  "v_mfma_f32_16x16x32_bf16 v[76:79], a[252:255], v[92:95], v[184:187]\n\t" \
  "v_max3_f32 v192, v192, v70, v71\n\t" \
  "ds_read_b64_tr_b16 a[176:177], v237 offset:768\n\t" \
  "v_mfma_f32_16x16x32_bf16 v[76:79], a[248:251], v[88:91], v[76:79]\n\t" \
  "s_waitcnt vmcnt(3)\n\t" \
  "v_and_b32 v206, 0xbfffbfff, v209\n\t" \
  "v_cmp_ne_u32_e64 s[96:97], %[tag], v206\n\t" \
  "s_cmp_lg_u64 s[96:97], 0\n\t" \
  "s_cbranch_scc1 .Lsf_v\n\t" \
  ".Lsf_w:\n\t" \
  "v_mfma_f32_16x16x32_bf16 v[76:79], a[244:247], v[84:87], v[76:79]\n\t" \
  "v_and_b32 v206, 1, v233\n\t" \
  "ds_read_b64_tr_b16 a[178:179], v237 offset:4864\n\t" \
  "v_mfma_f32_16x16x32_bf16 v[76:79], a[240:243], v[80:83], v[76:79]\n\t" \
  "v_max3_f32 v203, v203, v72, v73\n\t" \
  "v_max3_f32 v192, v192, v74, v75\n\t" \
  "v_mfma_f32_16x16x32_bf16 v[96:99], a[192:195], v[112:115], v[188:191]\n\t" \
  "v_lshlrev_b32 v206, 4, v206\n\t" \
  "v_lshrrev_b32 v206, v206, v209\n\t" \
  "v_mfma_f32_16x16x32_bf16 v[96:99], a[196:199], v[116:119], v[96:99]\n\t" \
  "v_and_b32 v245, 31, v245\n\t" \
  "ds_read_b64_tr_b16 a[180:181], v237 offset:8960\n\t" \
  "v_mfma_f32_16x16x32_bf16 v[96:99], a[200:203], v[120:123], v[96:99]\n\t" \
  "v_and_b32 v206, 0x4000, v206\n\t" \
  "ds_read_b64_tr_b16 a[182:183], v237 offset:13056\n\t" \
  "v_mfma_f32_16x16x32_bf16 v[96:99], a[204:207], v[124:127], v[96:99]\n\t" \
  "v_cmp_ne_u32_e64 s[96:97], 0, v206\n\t" \
  "v_exp_f32 v102, v102\n\t" \
  "v_exp_f32 v103, v103\n\t" \
  "s_nop 0\n\t" \
  "v_cvt_pk_bf16_f32 v155, v102, v103\n\t" \
  "v_mfma_f32_16x16x32_bf16 v[100:103], a[220:223], v[124:127], v[188:191]\n\t" \
  "v_max3_f32 v203, v203, v76, v77\n\t" \
  "v_max3_f32 v192, v192, v78, v79\n\t" \
  "v_mfma_f32_16x16x32_bf16 v[100:103], a[216:219], v[120:123], v[100:103]\n\t" \
  "v_max_f32 v192, v192, v203\n\t" \
  "s_bcnt1_i32_b32 %[nb], s96\n\t" \
  "v_mfma_f32_16x16x32_bf16 v[100:103], a[212:215], v[116:119], v[100:103]\n\t" \
  "v_cmp_lt_f32 vcc, 0x42800000, v192\n\t" \
  "ds_read_b64_tr_b16 a[184:185], v238 offset:768\n\t" \
  "v_mfma_f32_16x16x32_bf16 v[100:103], a[208:211], v[112:115], v[100:103]\n\t" \
  "s_cbranch_vccnz .Lsf_c62\n\t" \
  ".Lsf_r62:\n\t" \
  "s_bcnt1_i32_b32 %[na], s97\n\t" \
  "v_exp_f32 v104, v104\n\t" \
  "v_exp_f32 v105, v105\n\t" \
  "v_exp_f32 v106, v106\n\t" \
  "v_exp_f32 v107, v107\n\t" \
  "v_cvt_pk_bf16_f32 v156, v104, v105\n\t" \
  "v_cvt_pk_bf16_f32 v157, v106, v107\n\t" \
  "v_mfma_f32_16x16x32_bf16 v[104:107], a[224:227], v[112:115], v[188:191]\n\t" \
  "s_lshr_b32 s96, s96, 16\n\t" \
  "ds_read_b64_tr_b16 a[186:187], v238 offset:4864\n\t" \
  "v_mfma_f32_16x16x32_bf16 v[104:107], a[228:231], v[116:119], v[104:107]\n\t" \
  "s_lshr_b32 s98, %[hl18], 18\n\t" \
  "s_mul_i32 s98, s98, 127\n\t" \
  "v_add_lshl_u32 v245, v245, s98, 2\n\t" \
  "v_mfma_f32_16x16x32_bf16 v[104:107], a[232:235], v[120:123], v[104:107]\n\t" \
  "s_lshr_b32 s97, s97, 16\n\t" \
  "v_max3_f32 v192, v96, v97, v98\n\t" \
  "v_mfma_f32_16x16x32_bf16 v[104:107], a[236:239], v[124:127], v[104:107]\n\t" \
  "s_bcnt1_i32_b32 s96, s96\n\t" \
  "ds_read_b64_tr_b16 a[188:189], v238 offset:8960\n\t" \
  "v_exp_f32 v108, v108\n\t" \
  "v_exp_f32 v109, v109\n\t" \
  "v_exp_f32 v110, v110\n\t" \
  "v_exp_f32 v111, v111\n\t" \
  "v_cvt_pk_bf16_f32 v158, v108, v109\n\t" \
  "v_cvt_pk_bf16_f32 v159, v110, v111\n\t" \
  "v_mfma_f32_16x16x32_bf16 v[108:111], a[252:255], v[124:127], v[188:191]\n\t" \
  "s_bcnt1_i32_b32 s97, s97\n\t" \
  "s_sub_u32 s96, s97, s96\n\t" \
  "v_mfma_f32_16x16x32_bf16 v[108:111], a[248:251], v[120:123], v[108:111]\n\t" \
  "ds_read_b64_tr_b16 a[190:191], v238 offset:13056\n\t" \
  "s_sub_u32 %[nb], %[na], %[nb]\n\t" \
  "v_mfma_f32_16x16x32_bf16 v[108:111], a[244:247], v[116:119], v[108:111]\n\t" \
  "v_max3_f32 v203, v99, v100, v101\n\t" \
  "v_max3_f32 v192, v192, v102, v103\n\t" \
  "v_mfma_f32_16x16x32_bf16 v[108:111], a[240:243], v[112:115], v[108:111]\n\t" \
  "s_ashr_i32 %[nb], %[nb], 31\n\t" \
  "s_xor_b32 s96, s96, %[nb]\n\t" \
  "s_waitcnt lgkmcnt(0)\n\t" \
  "s_barrier\n\t" \
  "v_mfma_f32_16x16x32_bf16 a[0:3], a[128:131], v[128:131], a[0:3]\n\t" \
  "s_sub_u32 s96, s96, %[nb]\n\t" \
  "s_sub_u32 %[na], 6, s96\n\t" \
  "v_mfma_f32_16x16x32_bf16 a[0:3], a[132:135], v[132:135], a[0:3]\n\t" \
  "v_max3_f32 v203, v203, v104, v105\n\t" \
  "v_max3_f32 v192, v192, v106, v107\n\t" \
  "v_mfma_f32_16x16x32_bf16 a[16:19], a[136:139], v[128:131], a[16:19]\n\t" \
  "s_ashr_i32 %[na], %[na], 31\n\t" \
  "s_and_b32 %[nb], %[nb], 0x2000\n\t" \
  "v_mfma_f32_16x16x32_bf16 a[16:19], a[140:143], v[132:135], a[16:19]\n\t" \
  "s_xor_b32 s96, %[po], %[nb]\n\t" \
  "s_and_b32 s96, s96, %[na]\n\t" \
  "v_mfma_f32_16x16x32_bf16 a[32:35], a[144:147], v[128:131], a[32:35]\n\t" \
  "ds_read_b128 a[192:195], v220 offset:16384\n\t" \
  "v_max3_f32 v203, v203, v108, v109\n\t" \
  "v_mfma_f32_16x16x32_bf16 a[32:35], a[148:151], v[132:135], a[32:35]\n\t" \
  "v_max3_f32 v192, v192, v110, v111\n\t" \
  "v_max_f32 v192, v192, v203\n\t" \
  "v_mfma_f32_16x16x32_bf16 a[48:51], a[152:155], v[128:131], a[48:51]\n\t" \
  "v_cmp_lt_f32 vcc, 0x42800000, v192\n\t" \
  "ds_read_b128 a[196:199], v221 offset:16384\n\t" \
  "v_mfma_f32_16x16x32_bf16 a[48:51], a[156:159], v[132:135], a[48:51]\n\t" \
  "s_cbranch_vccnz .Lsf_c63\n\t" \
  ".Lsf_r63:\n\t" \
  "ds_read_b128 a[200:203], v222 offset:16384\n\t" \
  "v_mfma_f32_16x16x32_bf16 a[64:67], a[160:163], v[128:131], a[64:67]\n\t" \
  "s_xor_b32 %[eso], %[eso], s96\n\t" \
  "v_mfma_f32_16x16x32_bf16 a[64:67], a[164:167], v[132:135], a[64:67]\n\t" \
  "s_xor_b32 s96, %[px], %[nb]\n\t" \
  "s_and_b32 s96, s96, %[na]\n\t" \
  "v_mfma_f32_16x16x32_bf16 a[80:83], a[168:171], v[128:131], a[80:83]\n\t" \
  "s_xor_b32 %[xcb], %[xcb], s96\n\t" \
  "s_xor_b32 %[frb], %[frb], %[nb]\n\t" \
  "v_mfma_f32_16x16x32_bf16 a[80:83], a[172:175], v[132:135], a[80:83]\n\t" \
  "s_xor_b32 %[fw2], %[fw2], %[nb]\n\t" \
  "ds_read_b128 a[204:207], v223 offset:16384\n\t" \
  "v_mfma_f32_16x16x32_bf16 a[96:99], a[176:179], v[128:131], a[96:99]\n\t" \
  "v_exp_f32 v0, v0\n\t" \
  "v_mfma_f32_16x16x32_bf16 a[96:99], a[180:183], v[132:135], a[96:99]\n\t" \
  "v_exp_f32 v1, v1\n\t" \
  "v_mfma_f32_16x16x32_bf16 a[112:115], a[184:187], v[128:131], a[112:115]\n\t" \
  "v_exp_f32 v2, v2\n\t" \
  "v_mfma_f32_16x16x32_bf16 a[112:115], a[188:191], v[132:135], a[112:115]\n\t" \
  "v_exp_f32 v3, v3\n\t" \
  "v_smfmac_f32_16x16x64_bf16 v[160:163], v[216:219], v[128:135], v239\n\t" \
  "v_cvt_pk_bf16_f32 v128, v0, v1\n\t" \
  "v_cvt_pk_bf16_f32 v129, v2, v3\n\t" \
  "v_mfma_f32_16x16x32_bf16 a[4:7], a[128:131], v[136:139], a[4:7]\n\t" \
  "ds_read_b128 a[208:211], v220 offset:20480\n\t" \
  "ds_read_b128 a[212:215], v221 offset:20480\n\t" \
  "v_mfma_f32_16x16x32_bf16 a[4:7], a[132:135], v[140:143], a[4:7]\n\t" \
  "v_exp_f32 v4, v4\n\t" \
  "v_mfma_f32_16x16x32_bf16 a[20:23], a[136:139], v[136:139], a[20:23]\n\t" \
  "v_exp_f32 v5, v5\n\t" \
  "v_mfma_f32_16x16x32_bf16 a[20:23], a[140:143], v[140:143], a[20:23]\n\t" \
  "v_cvt_pk_bf16_f32 v130, v4, v5\n\t" \
  "s_add_u32 m0, %[m], 147456\n\t" \
  "v_mfma_f32_16x16x32_bf16 a[36:39], a[144:147], v[136:139], a[36:39]\n\t" \
  "v_exp_f32 v6, v6\n\t" \
  "v_mfma_f32_16x16x32_bf16 a[36:39], a[148:151], v[140:143], a[36:39]\n\t" \
  "v_exp_f32 v7, v7\n\t" \
  "v_mfma_f32_16x16x32_bf16 a[52:55], a[152:155], v[136:139], a[52:55]\n\t" \
  "v_cvt_pk_bf16_f32 v131, v6, v7\n\t" \
  "ds_read_b128 a[216:219], v222 offset:20480\n\t" \
  "v_mfma_f32_16x16x32_bf16 a[52:55], a[156:159], v[140:143], a[52:55]\n\t" \
  "v_exp_f32 v8, v8\n\t" \
  "v_mfma_f32_16x16x32_bf16 a[68:71], a[160:163], v[136:139], a[68:71]\n\t" \
  "s_add_u32 s96, %[sofk], 114688\n\t" \
  "ds_read_b128 a[220:223], v223 offset:20480\n\t" \
  "v_mfma_f32_16x16x32_bf16 a[68:71], a[164:167], v[140:143], a[68:71]\n\t" \
  "v_exp_f32 v9, v9\n\t" \
  "v_mfma_f32_16x16x32_bf16 a[84:87], a[168:171], v[136:139], a[84:87]\n\t" \
  "v_cvt_pk_bf16_f32 v132, v8, v9\n\t" \
  "buffer_load_dwordx4 v230, %[sV], s96 offen offset:0 lds\n\t" \
  "v_mfma_f32_16x16x32_bf16 a[84:87], a[172:175], v[140:143], a[84:87]\n\t" \
  "v_exp_f32 v10, v10\n\t" \
  "v_mfma_f32_16x16x32_bf16 a[100:103], a[176:179], v[136:139], a[100:103]\n\t" \
  "v_exp_f32 v11, v11\n\t" \
  "v_mfma_f32_16x16x32_bf16 a[100:103], a[180:183], v[140:143], a[100:103]\n\t" \
  "v_cvt_pk_bf16_f32 v133, v10, v11\n\t" \
  "ds_read_b128 a[224:227], v220 offset:24576\n\t" \
  "v_mfma_f32_16x16x32_bf16 a[116:119], a[184:187], v[136:139], a[116:119]\n\t" \
  "v_exp_f32 v12, v12\n\t" \
  "v_mfma_f32_16x16x32_bf16 a[116:119], a[188:191], v[140:143], a[116:119]\n\t" \
  "buffer_load_dwordx4 v231, %[sV], s96 offen offset:1024 lds\n\t" \
  "ds_read_b128 a[228:231], v221 offset:24576\n\t" \
  "v_smfmac_f32_16x16x64_bf16 v[164:167], v[216:219], v[136:143], v239\n\t" \
  "v_exp_f32 v13, v13\n\t" \
  "v_mfma_f32_16x16x32_bf16 a[8:11], a[128:131], v[144:147], a[8:11]\n\t" \
  "v_cvt_pk_bf16_f32 v134, v12, v13\n\t" \
  "buffer_load_dwordx4 v230, %[sV], s96 offen offset:2048 lds\n\t" \
  "v_mfma_f32_16x16x32_bf16 a[8:11], a[132:135], v[148:151], a[8:11]\n\t" \
  "v_exp_f32 v14, v14\n\t" \
  "v_mfma_f32_16x16x32_bf16 a[24:27], a[136:139], v[144:147], a[24:27]\n\t" \
  "ds_read_b128 a[232:235], v222 offset:24576\n\t" \
  "buffer_load_dwordx4 v231, %[sV], s96 offen offset:3072 lds\n\t" \
  "v_mfma_f32_16x16x32_bf16 a[24:27], a[140:143], v[148:151], a[24:27]\n\t" \
  "v_exp_f32 v15, v15\n\t" \
  "v_mfma_f32_16x16x32_bf16 a[40:43], a[144:147], v[144:147], a[40:43]\n\t" \
  "v_cvt_pk_bf16_f32 v135, v14, v15\n\t" \
  "ds_read_b128 a[236:239], v223 offset:24576\n\t" \
  "v_mfma_f32_16x16x32_bf16 a[40:43], a[148:151], v[148:151], a[40:43]\n\t" \
  "v_exp_f32 v32, v32\n\t" \
  "v_mfma_f32_16x16x32_bf16 a[56:59], a[152:155], v[144:147], a[56:59]\n\t" \
  "v_exp_f32 v33, v33\n\t" \
  "v_mfma_f32_16x16x32_bf16 a[56:59], a[156:159], v[148:151], a[56:59]\n\t" \
  "v_cvt_pk_bf16_f32 v136, v32, v33\n\t" \
  "s_add_u32 m0, %[m], 49152\n\t" \
  "v_mfma_f32_16x16x32_bf16 a[72:75], a[160:163], v[144:147], a[72:75]\n\t" \
  "ds_read_b128 a[240:243], v220 offset:28672\n\t" \
  "s_xor_b32 s97, %[wob], 262144\n\t" \
  "v_mfma_f32_16x16x32_bf16 a[72:75], a[164:167], v[148:151], a[72:75]\n\t" \
  "v_exp_f32 v34, v34\n\t" \
  "v_mfma_f32_16x16x32_bf16 a[88:91], a[168:171], v[144:147], a[88:91]\n\t" \
  "v_exp_f32 v35, v35\n\t" \
  "v_mfma_f32_16x16x32_bf16 a[88:91], a[172:175], v[148:151], a[88:91]\n\t" \
  "v_cvt_pk_bf16_f32 v137, v34, v35\n\t" \
  "buffer_load_dwordx4 v240, %[wo], s97 offen offset:0 lds\n\t" \
  "v_mfma_f32_16x16x32_bf16 a[104:107], a[176:179], v[144:147], a[104:107]\n\t" \
  "v_exp_f32 v36, v36\n\t" \
  "v_mfma_f32_16x16x32_bf16 a[104:107], a[180:183], v[148:151], a[104:107]\n\t" \
  "v_exp_f32 v37, v37\n\t" \
  "v_mfma_f32_16x16x32_bf16 a[120:123], a[184:187], v[144:147], a[120:123]\n\t" \
  "v_cvt_pk_bf16_f32 v138, v36, v37\n\t" \
  "ds_read_b128 a[244:247], v221 offset:28672\n\t" \
  "v_mfma_f32_16x16x32_bf16 a[120:123], a[188:191], v[148:151], a[120:123]\n\t" \
  "s_add_u32 s97, s97, 7168\n\t" \
  "buffer_load_dwordx4 v241, %[wo], s97 offen offset:1024 lds\n\t" \
  "v_smfmac_f32_16x16x64_bf16 v[168:171], v[216:219], v[144:151], v239\n\t" \
  "v_exp_f32 v38, v38\n\t" \
  "v_mfma_f32_16x16x32_bf16 a[12:15], a[128:131], v[152:155], a[12:15]\n\t" \
  "ds_read_b128 a[248:251], v222 offset:28672\n\t" \
  "ds_read_b128 a[252:255], v223 offset:28672\n\t" \
  "v_mfma_f32_16x16x32_bf16 a[12:15], a[132:135], v[156:159], a[12:15]\n\t" \
  "v_exp_f32 v39, v39\n\t" \
  "v_mfma_f32_16x16x32_bf16 a[28:31], a[136:139], v[152:155], a[28:31]\n\t" \
  "v_cvt_pk_bf16_f32 v139, v38, v39\n\t" \
  "v_mfma_f32_16x16x32_bf16 a[28:31], a[140:143], v[156:159], a[28:31]\n\t" \
  "s_add_u32 s97, s97, 7168\n\t" \
  "buffer_load_dwordx4 v240, %[wo], s97 offen offset:2048 lds\n\t" \
  "v_mfma_f32_16x16x32_bf16 a[44:47], a[144:147], v[152:155], a[44:47]\n\t" \
  "v_exp_f32 v40, v40\n\t" \
  "v_mfma_f32_16x16x32_bf16 a[44:47], a[148:151], v[156:159], a[44:47]\n\t" \
  "v_exp_f32 v41, v41\n\t" \
  "v_mfma_f32_16x16x32_bf16 a[60:63], a[152:155], v[152:155], a[60:63]\n\t" \
  "v_cvt_pk_bf16_f32 v140, v40, v41\n\t" \
  "v_mfma_f32_16x16x32_bf16 a[60:63], a[156:159], v[156:159], a[60:63]\n\t" \
  "s_add_u32 s97, s97, 7168\n\t" \
  "buffer_load_dwordx4 v241, %[wo], s97 offen offset:3072 lds\n\t" \
  "v_mfma_f32_16x16x32_bf16 a[76:79], a[160:163], v[152:155], a[76:79]\n\t" \
  "v_exp_f32 v42, v42\n\t" \
  "v_mfma_f32_16x16x32_bf16 a[76:79], a[164:167], v[156:159], a[76:79]\n\t" \
  "s_add_u32 m0, %[m], 98304\n\t" \
  "s_mov_b32 s96, 40960\n\t" \
  "buffer_load_dwordx4 v232, %[sG], s96 offen offset:2048 lds\n\t" \
  "v_mfma_f32_16x16x32_bf16 a[92:95], a[168:171], v[152:155], a[92:95]\n\t" \
  "v_exp_f32 v43, v43\n\t" \
  "v_mfma_f32_16x16x32_bf16 a[92:95], a[172:175], v[156:159], a[92:95]\n\t" \
  "v_cvt_pk_bf16_f32 v141, v42, v43\n\t" \
  "v_mfma_f32_16x16x32_bf16 a[108:111], a[176:179], v[152:155], a[108:111]\n\t" \
  "s_mov_b32 s96, 47104\n\t" \
  "buffer_load_dwordx4 v232, %[sG], s96 offen offset:3072 lds\n\t" \
  "v_mfma_f32_16x16x32_bf16 a[108:111], a[180:183], v[156:159], a[108:111]\n\t" \
  "v_exp_f32 v44, v44\n\t" \
  "v_mfma_f32_16x16x32_bf16 a[124:127], a[184:187], v[152:155], a[124:127]\n\t" \
  "v_exp_f32 v45, v45\n\t" \
  "v_mfma_f32_16x16x32_bf16 a[124:127], a[188:191], v[156:159], a[124:127]\n\t" \
  "v_cvt_pk_bf16_f32 v142, v44, v45\n\t" \
  "v_smfmac_f32_16x16x64_bf16 v[172:175], v[216:219], v[152:159], v239\n\t" \
  "v_exp_f32 v46, v46\n\t" \
  "s_cmp_lg_u32 s99, 0\n\t" \
  "s_cbranch_scc1 .Lsf_p6\n\t" \
  ".Lsf_q6:\n\t" \
  "s_waitcnt lgkmcnt(0)\n\t" \
  "s_barrier\n\t" \
  "v_mfma_f32_16x16x32_bf16 v[0:3], a[192:195], v[16:19], v[176:179]\n\t" \
  "v_exp_f32 v47, v47\n\t" \
  "v_mfma_f32_16x16x32_bf16 v[0:3], a[196:199], v[20:23], v[0:3]\n\t" \
  "v_cvt_pk_bf16_f32 v143, v46, v47\n\t" \
  "ds_read_b64_tr_b16 a[128:129], v228 offset:0\n\t" \
  "v_mfma_f32_16x16x32_bf16 v[0:3], a[200:203], v[24:27], v[0:3]\n\t" \
  "v_exp_f32 v64, v64\n\t" \
  "v_mfma_f32_16x16x32_bf16 v[0:3], a[204:207], v[28:31], v[0:3]\n\t" \
  "v_exp_f32 v65, v65\n\t" \
  "v_mfma_f32_16x16x32_bf16 v[4:7], a[220:223], v[28:31], v[176:179]\n\t" \
  "v_cvt_pk_bf16_f32 v144, v64, v65\n\t" \
  "ds_read_b64_tr_b16 a[130:131], v228 offset:4096\n\t" \
  "v_mfma_f32_16x16x32_bf16 v[4:7], a[216:219], v[24:27], v[4:7]\n\t" \
  "ds_read_b64_tr_b16 a[132:133], v228 offset:8192\n\t" \
  "s_add_u32 m0, %[m], 65536\n\t" \
  "v_mfma_f32_16x16x32_bf16 v[4:7], a[212:215], v[20:23], v[4:7]\n\t" \
  "v_max3_f32 v192, v0, v1, v2\n\t" \
  "ds_read_b64_tr_b16 a[134:135], v228 offset:12288\n\t" \
  "v_mfma_f32_16x16x32_bf16 v[4:7], a[208:211], v[16:19], v[4:7]\n\t" \
  "v_exp_f32 v66, v66\n\t" \
  "v_mfma_f32_16x16x32_bf16 v[8:11], a[224:227], v[16:19], v[176:179]\n\t" \
  "ds_read_b64_tr_b16 a[136:137], v229 offset:0\n\t" \
  "s_xor_b32 s97, %[wob], 393216\n\t" \
  "v_mfma_f32_16x16x32_bf16 v[8:11], a[228:231], v[20:23], v[8:11]\n\t" \
  "v_exp_f32 v67, v67\n\t" \
  "v_mfma_f32_16x16x32_bf16 v[8:11], a[232:235], v[24:27], v[8:11]\n\t" \
  "v_cvt_pk_bf16_f32 v145, v66, v67\n\t" \
  "v_max3_f32 v203, v3, v4, v5\n\t" \
  "v_mfma_f32_16x16x32_bf16 v[8:11], a[236:239], v[28:31], v[8:11]\n\t" \
  "v_max3_f32 v192, v192, v6, v7\n\t" \
  "ds_read_b64_tr_b16 a[138:139], v229 offset:4096\n\t" \
  "v_mfma_f32_16x16x32_bf16 v[12:15], a[252:255], v[28:31], v[176:179]\n\t" \
  "ds_read_b64_tr_b16 a[140:141], v229 offset:8192\n\t" \
  "buffer_load_dwordx4 v240, %[wo], s97 offen offset:0 lds\n\t" \
  "v_mfma_f32_16x16x32_bf16 v[12:15], a[248:251], v[24:27], v[12:15]\n\t" \
  "v_exp_f32 v68, v68\n\t" \
  "v_mfma_f32_16x16x32_bf16 v[12:15], a[244:247], v[20:23], v[12:15]\n\t" \
  "v_max3_f32 v203, v203, v8, v9\n\t" \
  "v_max3_f32 v192, v192, v10, v11\n\t" \
  "v_mfma_f32_16x16x32_bf16 v[12:15], a[240:243], v[16:19], v[12:15]\n\t" \
  "ds_read_b64_tr_b16 a[142:143], v229 offset:12288\n\t" \
  "ds_read_b64_tr_b16 a[144:145], v228 offset:256\n\t" \
  "v_mfma_f32_16x16x32_bf16 v[32:35], a[192:195], v[48:51], v[180:183]\n\t" \
  "ds_read_b64_tr_b16 a[146:147], v228 offset:4352\n\t" \
  "ds_read_b64_tr_b16 a[148:149], v228 offset:8448\n\t" \
  "v_mfma_f32_16x16x32_bf16 v[32:35], a[196:199], v[52:55], v[32:35]\n\t" \
  "v_exp_f32 v69, v69\n\t" \
  "v_mfma_f32_16x16x32_bf16 v[32:35], a[200:203], v[56:59], v[32:35]\n\t" \
  "v_cvt_pk_bf16_f32 v146, v68, v69\n\t" \
  "v_max3_f32 v203, v203, v12, v13\n\t" \
  "v_mfma_f32_16x16x32_bf16 v[32:35], a[204:207], v[60:63], v[32:35]\n\t" \
  "v_max3_f32 v192, v192, v14, v15\n\t" \
  "v_max_f32 v192, v192, v203\n\t" \
  "v_mfma_f32_16x16x32_bf16 v[36:39], a[220:223], v[60:63], v[180:183]\n\t" \
  "v_cmp_lt_f32 vcc, 0x42800000, v192\n\t" \
  "ds_read_b64_tr_b16 a[150:151], v228 offset:12544\n\t" \
  "v_mfma_f32_16x16x32_bf16 v[36:39], a[216:219], v[56:59], v[36:39]\n\t" \
  "s_cbranch_vccnz .Lsf_c70\n\t" \
  ".Lsf_r70:\n\t" \
  "ds_read_b64_tr_b16 a[152:153], v229 offset:256\n\t" \
  "v_mfma_f32_16x16x32_bf16 v[36:39], a[212:215], v[52:55], v[36:39]\n\t" \
  "s_add_u32 s97, s97, 7168\n\t" \
  "buffer_load_dwordx4 v241, %[wo], s97 offen offset:1024 lds\n\t" \
  "v_mfma_f32_16x16x32_bf16 v[36:39], a[208:211], v[48:51], v[36:39]\n\t" \
  "v_max3_f32 v192, v32, v33, v34\n\t" \
  "ds_read_b64_tr_b16 a[154:155], v229 offset:4352\n\t" \
  "v_mfma_f32_16x16x32_bf16 v[40:43], a[224:227], v[48:51], v[180:183]\n\t" \
  "s_add_u32 s97, s97, 7168\n\t" \
  "buffer_load_dwordx4 v240, %[wo], s97 offen offset:2048 lds\n\t" \
  "v_mfma_f32_16x16x32_bf16 v[40:43], a[228:231], v[52:55], v[40:43]\n\t" \
  "s_add_u32 s97, s97, 7168\n\t" \
  "buffer_load_dwordx4 v241, %[wo], s97 offen offset:3072 lds\n\t" \
  "v_mfma_f32_16x16x32_bf16 v[40:43], a[232:235], v[56:59], v[40:43]\n\t" \
  "v_max3_f32 v203, v35, v36, v37\n\t" \
  "v_max3_f32 v192, v192, v38, v39\n\t" \
  "v_mfma_f32_16x16x32_bf16 v[40:43], a[236:239], v[60:63], v[40:43]\n\t" \
  "ds_read_b64_tr_b16 a[156:157], v229 offset:8448\n\t" \
  "ds_read_b64_tr_b16 a[158:159], v229 offset:12544\n\t" \
  "v_mfma_f32_16x16x32_bf16 v[44:47], a[252:255], v[60:63], v[180:183]\n\t" \
  "v_exp_f32 v70, v70\n\t" \
  "v_mfma_f32_16x16x32_bf16 v[44:47], a[248:251], v[56:59], v[44:47]\n\t" \
  "v_exp_f32 v71, v71\n\t" \
  "v_mfma_f32_16x16x32_bf16 v[44:47], a[244:247], v[52:55], v[44:47]\n\t" \
  "v_cvt_pk_bf16_f32 v147, v70, v71\n\t" \
  "ds_read_b64_tr_b16 a[160:161], v228 offset:512\n\t" \
  "v_mfma_f32_16x16x32_bf16 v[44:47], a[240:243], v[48:51], v[44:47]\n\t" \
  "v_max3_f32 v203, v203, v40, v41\n\t" \
  "v_max3_f32 v192, v192, v42, v43\n\t" \
  "v_mfma_f32_16x16x32_bf16 v[64:67], a[192:195], v[80:83], v[184:187]\n\t" \
  "ds_read_b64_tr_b16 a[162:163], v228 offset:4608\n\t" \
  "ds_read_b64_tr_b16 a[164:165], v228 offset:8704\n\t" \
  "v_mfma_f32_16x16x32_bf16 v[64:67], a[196:199], v[84:87], v[64:67]\n\t" \
  "v_exp_f32 v72, v72\n\t" \
  "v_mfma_f32_16x16x32_bf16 v[64:67], a[200:203], v[88:91], v[64:67]\n\t" \
  "v_max3_f32 v203, v203, v44, v45\n\t" \
  "v_max3_f32 v192, v192, v46, v47\n\t" \
  "v_mfma_f32_16x16x32_bf16 v[64:67], a[204:207], v[92:95], v[64:67]\n\t" \
  "v_max_f32 v192, v192, v203\n\t" \
  "ds_read_b64_tr_b16 a[166:167], v228 offset:12800\n\t" \
  "v_mfma_f32_16x16x32_bf16 v[68:71], a[220:223], v[92:95], v[184:187]\n\t" \
  "v_cmp_lt_f32 vcc, 0x42800000, v192\n\t" \
  "ds_read_b64_tr_b16 a[168:169], v229 offset:512\n\t" \
  "v_mfma_f32_16x16x32_bf16 v[68:71], a[216:219], v[88:91], v[68:71]\n\t" \
  "s_cbranch_vccnz .Lsf_c71\n\t" \
  ".Lsf_r71:\n\t" \
  "ds_read_b64_tr_b16 a[170:171], v229 offset:4608\n\t" \
  "v_mfma_f32_16x16x32_bf16 v[68:71], a[212:215], v[84:87], v[68:71]\n\t" \
  "v_max3_f32 v192, v64, v65, v66\n\t" \
  "ds_read_b64_tr_b16 a[172:173], v229 offset:8704\n\t" \
  "v_mfma_f32_16x16x32_bf16 v[68:71], a[208:211], v[80:83], v[68:71]\n\t" \
  "v_exp_f32 v73, v73\n\t" \
  "v_exp_f32 v74, v74\n\t" \
  "v_exp_f32 v75, v75\n\t" \
  "v_cvt_pk_bf16_f32 v148, v72, v73\n\t" \
  "v_cvt_pk_bf16_f32 v149, v74, v75\n\t" \
  "v_mfma_f32_16x16x32_bf16 v[72:75], a[224:227], v[80:83], v[184:187]\n\t" \
  "v_exp_f32 v76, v76\n\t" \
  "v_mfma_f32_16x16x32_bf16 v[72:75], a[228:231], v[84:87], v[72:75]\n\t" \
  "v_max3_f32 v203, v67, v68, v69\n\t" \
  "v_max3_f32 v192, v192, v70, v71\n\t" \
  "v_mfma_f32_16x16x32_bf16 v[72:75], a[232:235], v[88:91], v[72:75]\n\t" \
  "v_exp_f32 v77, v77\n\t" \
  "v_mfma_f32_16x16x32_bf16 v[72:75], a[236:239], v[92:95], v[72:75]\n\t" \
  "v_cvt_pk_bf16_f32 v150, v76, v77\n\t" \
  "ds_read_b64_tr_b16 a[174:175], v229 offset:12800\n\t" \
  "v_exp_f32 v78, v78\n\t" \
  "v_exp_f32 v79, v79\n\t" \
  "s_nop 0\n\t" \
  "v_cvt_pk_bf16_f32 v151, v78, v79\n\t" \
  "v_mfma_f32_16x16x32_bf16 v[76:79], a[252:255], v[92:95], v[184:187]\n\t" \
  "ds_read_b64_tr_b16 a[176:177], v228 offset:768\n\t" \
  "ds_read_b64_tr_b16 a[178:179], v228 offset:4864\n\t" \
  "v_mfma_f32_16x16x32_bf16 v[76:79], a[248:251], v[88:91], v[76:79]\n\t" \
  "v_exp_f32 v96, v96\n\t" \
  "v_mfma_f32_16x16x32_bf16 v[76:79], a[244:247], v[84:87], v[76:79]\n\t" \
  "v_max3_f32 v203, v203, v72, v73\n\t" \
  "v_max3_f32 v192, v192, v74, v75\n\t" \
  "v_mfma_f32_16x16x32_bf16 v[76:79], a[240:243], v[80:83], v[76:79]\n\t" \
  "v_exp_f32 v97, v97\n\t" \
  "v_exp_f32 v98, v98\n\t" \
  "v_exp_f32 v99, v99\n\t" \
  "v_cvt_pk_bf16_f32 v152, v96, v97\n\t" \
  "v_cvt_pk_bf16_f32 v153, v98, v99\n\t" \
  "v_mfma_f32_16x16x32_bf16 v[96:99], a[192:195], v[112:115], v[188:191]\n\t" \
  "ds_read_b64_tr_b16 a[180:181], v228 offset:8960\n\t" \
  "ds_read_b64_tr_b16 a[182:183], v228 offset:13056\n\t" \
  "v_mfma_f32_16x16x32_bf16 v[96:99], a[196:199], v[116:119], v[96:99]\n\t" \
  "v_max3_f32 v203, v203, v76, v77\n\t" \
  "v_max3_f32 v192, v192, v78, v79\n\t" \
  "v_mfma_f32_16x16x32_bf16 v[96:99], a[200:203], v[120:123], v[96:99]\n\t" \
  "v_max_f32 v192, v192, v203\n\t" \
  "ds_read_b64_tr_b16 a[184:185], v229 offset:768\n\t" \
  "v_mfma_f32_16x16x32_bf16 v[96:99], a[204:207], v[124:127], v[96:99]\n\t" \
  "v_cmp_lt_f32 vcc, 0x42800000, v192\n\t" \
  "ds_read_b64_tr_b16 a[186:187], v229 offset:4864\n\t" \
  "v_exp_f32 v100, v100\n\t" \
  "v_exp_f32 v101, v101\n\t" \
  "v_exp_f32 v102, v102\n\t" \
  "v_exp_f32 v103, v103\n\t" \
  "v_cvt_pk_bf16_f32 v154, v100, v101\n\t" \
  "v_cvt_pk_bf16_f32 v155, v102, v103\n\t" \
  "v_mfma_f32_16x16x32_bf16 v[100:103], a[220:223], v[124:127], v[188:191]\n\t" \
  "s_cbranch_vccnz .Lsf_c72\n\t" \
  ".Lsf_r72:\n\t" \
  "v_max3_f32 v192, v96, v97, v98\n\t" \
  "v_mfma_f32_16x16x32_bf16 v[100:103], a[216:219], v[120:123], v[100:103]\n\t" \
  "v_exp_f32 v104, v104\n\t" \
  "v_mfma_f32_16x16x32_bf16 v[100:103], a[212:215], v[116:119], v[100:103]\n\t" \
  "v_exp_f32 v105, v105\n\t" \
  "v_mfma_f32_16x16x32_bf16 v[100:103], a[208:211], v[112:115], v[100:103]\n\t" \
  "v_cvt_pk_bf16_f32 v156, v104, v105\n\t" \
  "ds_read_b64_tr_b16 a[188:189], v229 offset:8960\n\t" \
  "v_exp_f32 v106, v106\n\t" \
  "v_exp_f32 v107, v107\n\t" \
  "s_nop 0\n\t" \
  "v_cvt_pk_bf16_f32 v157, v106, v107\n\t" \
  "v_mfma_f32_16x16x32_bf16 v[104:107], a[224:227], v[112:115], v[188:191]\n\t" \
  "v_exp_f32 v108, v108\n\t" \
  "v_mfma_f32_16x16x32_bf16 v[104:107], a[228:231], v[116:119], v[104:107]\n\t" \
  "ds_read_b64_tr_b16 a[190:191], v229 offset:13056\n\t" \
  "v_max3_f32 v203, v99, v100, v101\n\t" \
  "v_mfma_f32_16x16x32_bf16 v[104:107], a[232:235], v[120:123], v[104:107]\n\t" \
  "v_max3_f32 v192, v192, v102, v103\n\t" \
  "v_mfma_f32_16x16x32_bf16 v[104:107], a[236:239], v[124:127], v[104:107]\n\t" \
  "v_exp_f32 v109, v109\n\t" \
  "v_exp_f32 v110, v110\n\t" \
  "v_exp_f32 v111, v111\n\t" \
  "v_cvt_pk_bf16_f32 v158, v108, v109\n\t" \
  "v_cvt_pk_bf16_f32 v159, v110, v111\n\t" \
  "v_mfma_f32_16x16x32_bf16 v[108:111], a[252:255], v[124:127], v[188:191]\n\t" \
  "v_exp_f32 v0, v0\n\t" \
  "v_mfma_f32_16x16x32_bf16 v[108:111], a[248:251], v[120:123], v[108:111]\n\t" \
  "v_exp_f32 v1, v1\n\t" \
  "v_mfma_f32_16x16x32_bf16 v[108:111], a[244:247], v[116:119], v[108:111]\n\t" \
  "v_max3_f32 v203, v203, v104, v105\n\t" \
  "v_max3_f32 v192, v192, v106, v107\n\t" \
  "v_mfma_f32_16x16x32_bf16 v[108:111], a[240:243], v[112:115], v[108:111]\n\t" \
  "v_exp_f32 v2, v2\n\t" \
  "s_waitcnt vmcnt(6) lgkmcnt(0)\n\t" \
  "s_barrier\n\t" \
  "v_mfma_f32_16x16x32_bf16 a[0:3], a[128:131], v[128:131], a[0:3]\n\t" \
  "ds_read_b64_tr_b16 a[192:193], v237 offset:0\n\t" \
  "s_add_u32 m0, %[m], 81920\n\t" \
  "v_mfma_f32_16x16x32_bf16 a[0:3], a[132:135], v[132:135], a[0:3]\n\t" \
  "v_exp_f32 v3, v3\n\t" \
  "v_mfma_f32_16x16x32_bf16 a[16:19], a[136:139], v[128:131], a[16:19]\n\t" \
  "v_max3_f32 v203, v203, v108, v109\n\t" \
  "v_max3_f32 v192, v192, v110, v111\n\t" \
  "v_mfma_f32_16x16x32_bf16 a[16:19], a[140:143], v[132:135], a[16:19]\n\t" \
  "v_max_f32 v192, v192, v203\n\t" \
  "ds_read_b64_tr_b16 a[194:195], v237 offset:4096\n\t" \
  "v_mfma_f32_16x16x32_bf16 a[32:35], a[144:147], v[128:131], a[32:35]\n\t" \
  "v_cmp_lt_f32 vcc, 0x42800000, v192\n\t" \
  "ds_read_b64_tr_b16 a[196:197], v237 offset:8192\n\t" \
  "v_mfma_f32_16x16x32_bf16 a[32:35], a[148:151], v[132:135], a[32:35]\n\t" \
  "s_cbranch_vccnz .Lsf_c73\n\t" \
  ".Lsf_r73:\n\t" \
  "s_xor_b32 s97, %[wob], 524288\n\t" \
  "v_mfma_f32_16x16x32_bf16 a[48:51], a[152:155], v[128:131], a[48:51]\n\t" \
  "ds_read_b64_tr_b16 a[198:199], v237 offset:12288\n\t" \
  "ds_read_b64_tr_b16 a[200:201], v238 offset:0\n\t" \
  "v_mfma_f32_16x16x32_bf16 a[48:51], a[156:159], v[132:135], a[48:51]\n\t" \
  "buffer_load_dwordx4 v240, %[wo], s97 offen offset:0 lds\n\t" \
  "ds_read_b64_tr_b16 a[202:203], v238 offset:4096\n\t" \
  "v_mfma_f32_16x16x32_bf16 a[64:67], a[160:163], v[128:131], a[64:67]\n\t" \
  "ds_read_b64_tr_b16 a[204:205], v238 offset:8192\n\t" \
  "ds_read_b64_tr_b16 a[206:207], v238 offset:12288\n\t" \
  "v_mfma_f32_16x16x32_bf16 a[64:67], a[164:167], v[132:135], a[64:67]\n\t" \
  "v_exp_f32 v4, v4\n\t" \
  "v_mfma_f32_16x16x32_bf16 a[80:83], a[168:171], v[128:131], a[80:83]\n\t" \
  "s_add_u32 s97, s97, 7168\n\t" \
  "buffer_load_dwordx4 v241, %[wo], s97 offen offset:1024 lds\n\t" \
  "v_mfma_f32_16x16x32_bf16 a[80:83], a[172:175], v[132:135], a[80:83]\n\t" \
  "ds_read_b64_tr_b16 a[208:209], v237 offset:256\n\t" \
  "ds_read_b64_tr_b16 a[210:211], v237 offset:4352\n\t" \
  "v_mfma_f32_16x16x32_bf16 a[96:99], a[176:179], v[128:131], a[96:99]\n\t" \
  "v_exp_f32 v5, v5\n\t" \
  "v_mfma_f32_16x16x32_bf16 a[96:99], a[180:183], v[132:135], a[96:99]\n\t" \
  "s_add_u32 s97, s97, 7168\n\t" \
  "buffer_load_dwordx4 v240, %[wo], s97 offen offset:2048 lds\n\t" \
  "v_mfma_f32_16x16x32_bf16 a[112:115], a[184:187], v[128:131], a[112:115]\n\t" \
  "ds_read_b64_tr_b16 a[212:213], v237 offset:8448\n\t" \
  "ds_read_b64_tr_b16 a[214:215], v237 offset:12544\n\t" \
  "v_mfma_f32_16x16x32_bf16 a[112:115], a[188:191], v[132:135], a[112:115]\n\t" \
  "s_add_u32 s97, s97, 7168\n\t" \
  "buffer_load_dwordx4 v241, %[wo], s97 offen offset:3072 lds\n\t" \
  "v_smfmac_f32_16x16x64_bf16 v[160:163], v[216:219], v[128:135], v239\n\t" \
  "v_cvt_pk_bf16_f32 v128, v0, v1\n\t" \
  "v_cvt_pk_bf16_f32 v129, v2, v3\n\t" \
  "v_mfma_f32_16x16x32_bf16 a[4:7], a[128:131], v[136:139], a[4:7]\n\t" \
  "v_cvt_pk_bf16_f32 v130, v4, v5\n\t" \
  "ds_read_b64_tr_b16 a[216:217], v238 offset:256\n\t" \
  "v_mfma_f32_16x16x32_bf16 a[4:7], a[132:135], v[140:143], a[4:7]\n\t" \
  "v_exp_f32 v6, v6\n\t" \
  "v_mfma_f32_16x16x32_bf16 a[20:23], a[136:139], v[136:139], a[20:23]\n\t" \
  "ds_read_b64_tr_b16 a[218:219], v238 offset:4352\n\t" \
  "ds_read_b64_tr_b16 a[220:221], v238 offset:8448\n\t" \
  "v_mfma_f32_16x16x32_bf16 a[20:23], a[140:143], v[140:143], a[20:23]\n\t" \
  "ds_read_b64_tr_b16 a[222:223], v238 offset:12544\n\t" \
  "ds_read_b64_tr_b16 a[224:225], v237 offset:512\n\t" \
  "v_mfma_f32_16x16x32_bf16 a[36:39], a[144:147], v[136:139], a[36:39]\n\t" \
  "v_exp_f32 v7, v7\n\t" \
  "v_mfma_f32_16x16x32_bf16 a[36:39], a[148:151], v[140:143], a[36:39]\n\t" \
  "v_cvt_pk_bf16_f32 v131, v6, v7\n\t" \
  "s_add_u32 m0, %[m], 114688\n\t" \
  "v_mfma_f32_16x16x32_bf16 a[52:55], a[152:155], v[136:139], a[52:55]\n\t" \
  "ds_read_b64_tr_b16 a[226:227], v237 offset:4608\n\t" \
  "ds_read_b64_tr_b16 a[228:229], v237 offset:8704\n\t" \
  "v_mfma_f32_16x16x32_bf16 a[52:55], a[156:159], v[140:143], a[52:55]\n\t" \
  "s_xor_b32 s97, %[wob], 786432\n\t" \
  "ds_read_b64_tr_b16 a[230:231], v237 offset:12800\n\t" \
  "v_mfma_f32_16x16x32_bf16 a[68:71], a[160:163], v[136:139], a[68:71]\n\t" \
  "v_exp_f32 v8, v8\n\t" \
  "v_mfma_f32_16x16x32_bf16 a[68:71], a[164:167], v[140:143], a[68:71]\n\t" \
  "v_exp_f32 v9, v9\n\t" \
  "v_mfma_f32_16x16x32_bf16 a[84:87], a[168:171], v[136:139], a[84:87]\n\t" \
  "v_cvt_pk_bf16_f32 v132, v8, v9\n\t" \
  "ds_read_b64_tr_b16 a[232:233], v238 offset:512\n\t" \
  "v_mfma_f32_16x16x32_bf16 a[84:87], a[172:175], v[140:143], a[84:87]\n\t" \
  "v_exp_f32 v10, v10\n\t" \
  "v_mfma_f32_16x16x32_bf16 a[100:103], a[176:179], v[136:139], a[100:103]\n\t" \
  "ds_read_b64_tr_b16 a[234:235], v238 offset:4608\n\t" \
  "ds_read_b64_tr_b16 a[236:237], v238 offset:8704\n\t" \
  "v_mfma_f32_16x16x32_bf16 a[100:103], a[180:183], v[140:143], a[100:103]\n\t" \
  "v_exp_f32 v11, v11\n\t" \
  "v_mfma_f32_16x16x32_bf16 a[116:119], a[184:187], v[136:139], a[116:119]\n\t" \
  "v_cvt_pk_bf16_f32 v133, v10, v11\n\t" \
  "ds_read_b64_tr_b16 a[238:239], v238 offset:12800\n\t" \
  "v_mfma_f32_16x16x32_bf16 a[116:119], a[188:191], v[140:143], a[116:119]\n\t" \
  "ds_read_b64_tr_b16 a[240:241], v237 offset:768\n\t" \
  "ds_read_b64_tr_b16 a[242:243], v237 offset:4864\n\t" \
  "v_smfmac_f32_16x16x64_bf16 v[164:167], v[216:219], v[136:143], v239\n\t" \
  "v_exp_f32 v12, v12\n\t" \
  "v_mfma_f32_16x16x32_bf16 a[8:11], a[128:131], v[144:147], a[8:11]\n\t" \
  "v_exp_f32 v13, v13\n\t" \
  "v_mfma_f32_16x16x32_bf16 a[8:11], a[132:135], v[148:151], a[8:11]\n\t" \
  "v_cvt_pk_bf16_f32 v134, v12, v13\n\t" \
  "ds_read_b64_tr_b16 a[244:245], v237 offset:8960\n\t" \
  "v_mfma_f32_16x16x32_bf16 a[24:27], a[136:139], v[144:147], a[24:27]\n\t" \
  "ds_read_b64_tr_b16 a[246:247], v237 offset:13056\n\t" \
  "ds_read_b64_tr_b16 a[248:249], v238 offset:768\n\t" \
  "v_mfma_f32_16x16x32_bf16 a[24:27], a[140:143], v[148:151], a[24:27]\n\t" \
  "v_exp_f32 v14, v14\n\t" \
  "v_mfma_f32_16x16x32_bf16 a[40:43], a[144:147], v[144:147], a[40:43]\n\t" \
  "v_exp_f32 v15, v15\n\t" \
  "v_mfma_f32_16x16x32_bf16 a[40:43], a[148:151], v[148:151], a[40:43]\n\t" \
  "v_cvt_pk_bf16_f32 v135, v14, v15\n\t" \
  "ds_read_b64_tr_b16 a[250:251], v238 offset:4864\n\t" \
  "v_mfma_f32_16x16x32_bf16 a[56:59], a[152:155], v[144:147], a[56:59]\n\t" \
  "ds_read_b64_tr_b16 a[252:253], v238 offset:8960\n\t" \
  "ds_read_b64_tr_b16 a[254:255], v238 offset:13056\n\t" \
  "v_mfma_f32_16x16x32_bf16 a[56:59], a[156:159], v[148:151], a[56:59]\n\t" \
  "v_exp_f32 v32, v32\n\t" \
  "v_mfma_f32_16x16x32_bf16 a[72:75], a[160:163], v[144:147], a[72:75]\n\t" \
  "v_xor_b32 v235, 64, v234\n\t" \
  "ds_read_b64_tr_b16 v[48:49], v235 offset:0\n\t" \
  "v_mfma_f32_16x16x32_bf16 a[72:75], a[164:167], v[148:151], a[72:75]\n\t" \
  "v_exp_f32 v33, v33\n\t" \
  "v_mfma_f32_16x16x32_bf16 a[88:91], a[168:171], v[144:147], a[88:91]\n\t" \
  "v_cvt_pk_bf16_f32 v136, v32, v33\n\t" \
  "ds_read_b64_tr_b16 v[50:51], v235 offset:4096\n\t" \
  "v_mfma_f32_16x16x32_bf16 a[88:91], a[172:175], v[148:151], a[88:91]\n\t" \
  "v_exp_f32 v34, v34\n\t" \
  "v_mfma_f32_16x16x32_bf16 a[104:107], a[176:179], v[144:147], a[104:107]\n\t" \
  "v_exp_f32 v35, v35\n\t" \
  "v_mfma_f32_16x16x32_bf16 a[104:107], a[180:183], v[148:151], a[104:107]\n\t" \
  "v_cvt_pk_bf16_f32 v137, v34, v35\n\t" \
  "ds_read_b64_tr_b16 v[52:53], v235 offset:8192\n\t" \
  "v_mfma_f32_16x16x32_bf16 a[120:123], a[184:187], v[144:147], a[120:123]\n\t" \
  "v_exp_f32 v36, v36\n\t" \
  "v_mfma_f32_16x16x32_bf16 a[120:123], a[188:191], v[148:151], a[120:123]\n\t" \
  "v_exp_f32 v37, v37\n\t" \
  "v_smfmac_f32_16x16x64_bf16 v[168:171], v[216:219], v[144:151], v239\n\t" \
  "v_cvt_pk_bf16_f32 v138, v36, v37\n\t" \
  "ds_read_b64_tr_b16 v[54:55], v235 offset:12288\n\t" \
  "v_mfma_f32_16x16x32_bf16 a[12:15], a[128:131], v[152:155], a[12:15]\n\t" \
  "v_exp_f32 v38, v38\n\t" \
  "v_mfma_f32_16x16x32_bf16 a[12:15], a[132:135], v[156:159], a[12:15]\n\t" \
  "v_exp_f32 v39, v39\n\t" \
  "v_mfma_f32_16x16x32_bf16 a[28:31], a[136:139], v[152:155], a[28:31]\n\t" \
  "v_cvt_pk_bf16_f32 v139, v38, v39\n\t" \
  "ds_read_b64_tr_b16 v[56:57], v235 offset:16384\n\t" \
  "v_mfma_f32_16x16x32_bf16 a[28:31], a[140:143], v[156:159], a[28:31]\n\t" \
  "v_exp_f32 v40, v40\n\t" \
  "v_mfma_f32_16x16x32_bf16 a[44:47], a[144:147], v[152:155], a[44:47]\n\t" \
  "v_exp_f32 v41, v41\n\t" \
  "v_mfma_f32_16x16x32_bf16 a[44:47], a[148:151], v[156:159], a[44:47]\n\t" \
  "v_cvt_pk_bf16_f32 v140, v40, v41\n\t" \
  "ds_read_b64_tr_b16 v[58:59], v235 offset:20480\n\t" \
  "v_mfma_f32_16x16x32_bf16 a[60:63], a[152:155], v[152:155], a[60:63]\n\t" \
  "v_exp_f32 v42, v42\n\t" \
  "v_mfma_f32_16x16x32_bf16 a[60:63], a[156:159], v[156:159], a[60:63]\n\t" \
  "ds_read_b64_tr_b16 v[60:61], v235 offset:24576\n\t" \
  "ds_read_b64_tr_b16 v[62:63], v235 offset:28672\n\t" \
  "v_mfma_f32_16x16x32_bf16 a[76:79], a[160:163], v[152:155], a[76:79]\n\t" \
  "v_exp_f32 v43, v43\n\t" \
  "v_mfma_f32_16x16x32_bf16 a[76:79], a[164:167], v[156:159], a[76:79]\n\t" \
  "v_cvt_pk_bf16_f32 v141, v42, v43\n\t" \
  "v_xor_b32 v236, 96, v234\n\t" \
  "v_mfma_f32_16x16x32_bf16 a[92:95], a[168:171], v[152:155], a[92:95]\n\t" \
  "v_exp_f32 v44, v44\n\t" \
  "v_mfma_f32_16x16x32_bf16 a[92:95], a[172:175], v[156:159], a[92:95]\n\t" \
  "v_exp_f32 v45, v45\n\t" \
  "v_mfma_f32_16x16x32_bf16 a[108:111], a[176:179], v[152:155], a[108:111]\n\t" \
  "v_cvt_pk_bf16_f32 v142, v44, v45\n\t" \
  "ds_read_b64_tr_b16 v[80:81], v236 offset:0\n\t" \
  "v_mfma_f32_16x16x32_bf16 a[108:111], a[180:183], v[156:159], a[108:111]\n\t" \
  "v_exp_f32 v46, v46\n\t" \
  "v_mfma_f32_16x16x32_bf16 a[124:127], a[184:187], v[152:155], a[124:127]\n\t" \
  "v_exp_f32 v47, v47\n\t" \
  "v_mfma_f32_16x16x32_bf16 a[124:127], a[188:191], v[156:159], a[124:127]\n\t" \
  "v_cvt_pk_bf16_f32 v143, v46, v47\n\t" \
  "ds_read_b64_tr_b16 v[82:83], v236 offset:4096\n\t" \
  "v_smfmac_f32_16x16x64_bf16 v[172:175], v[216:219], v[152:159], v239\n\t" \
  "v_exp_f32 v64, v64\n\t" \
  "s_cmp_lg_u32 s99, 0\n\t" \
  "s_cbranch_scc1 .Lsf_p7\n\t" \
  ".Lsf_q7:\n\t" \
  "s_waitcnt lgkmcnt(15)\n\t" \
  "v_mfma_f32_16x16x32_bf16 a[0:3], a[192:195], v[128:131], a[0:3]\n\t" \
  "v_exp_f32 v65, v65\n\t" \
  "v_mfma_f32_16x16x32_bf16 a[0:3], a[196:199], v[132:135], a[0:3]\n\t" \
  "v_cvt_pk_bf16_f32 v144, v64, v65\n\t" \
  "ds_read_b64_tr_b16 v[84:85], v236 offset:8192\n\t" \
  "v_smfmac_f32_16x16x64_bf16 v[160:163], v[216:219], v[128:135], v239\n\t" \
  "ds_read_b64_tr_b16 v[86:87], v236 offset:12288\n\t" \
  "ds_read_b128 v[176:179], v215 offset:16384\n\t" \
  "v_mfma_f32_16x16x32_bf16 a[16:19], a[200:203], v[128:131], a[16:19]\n\t" \
  "v_exp_f32 v66, v66\n\t" \
  "v_mfma_f32_16x16x32_bf16 a[16:19], a[204:207], v[132:135], a[16:19]\n\t" \
  "ds_read_b128 v[180:183], v215 offset:17408\n\t" \
  "ds_read_b64_tr_b16 v[88:89], v236 offset:16384\n\t" \
  "v_mfma_f32_16x16x32_bf16 a[32:35], a[208:211], v[128:131], a[32:35]\n\t" \
  "v_exp_f32 v67, v67\n\t" \
  "v_mfma_f32_16x16x32_bf16 a[32:35], a[212:215], v[132:135], a[32:35]\n\t" \
  "v_cvt_pk_bf16_f32 v145, v66, v67\n\t" \
  "ds_read_b128 v[184:187], v215 offset:18432\n\t" \
  "v_mfma_f32_16x16x32_bf16 a[48:51], a[216:219], v[128:131], a[48:51]\n\t" \
  "ds_read_b64_tr_b16 v[90:91], v236 offset:20480\n\t" \
  "ds_read_b128 v[188:191], v215 offset:19456\n\t" \
  "v_mfma_f32_16x16x32_bf16 a[48:51], a[220:223], v[132:135], a[48:51]\n\t" \
  "s_waitcnt lgkmcnt(0)\n\t" \
  "buffer_load_dwordx4 v240, %[wo], s97 offen offset:0 lds\n\t" \
  "v_mfma_f32_16x16x32_bf16 a[64:67], a[224:227], v[128:131], a[64:67]\n\t" \
  "s_add_u32 s97, s97, 7168\n\t" \
  "buffer_load_dwordx4 v241, %[wo], s97 offen offset:1024 lds\n\t" \
  "v_mfma_f32_16x16x32_bf16 a[64:67], a[228:231], v[132:135], a[64:67]\n\t" \
  "s_add_u32 s97, s97, 7168\n\t" \
  "buffer_load_dwordx4 v240, %[wo], s97 offen offset:2048 lds\n\t" \
  "v_mfma_f32_16x16x32_bf16 a[80:83], a[232:235], v[128:131], a[80:83]\n\t" \
  "s_add_u32 s97, s97, 7168\n\t" \
  "buffer_load_dwordx4 v241, %[wo], s97 offen offset:3072 lds\n\t" \
  "v_mfma_f32_16x16x32_bf16 a[80:83], a[236:239], v[132:135], a[80:83]\n\t" \
  "s_add_u32 m0, %[m], 131072\n\t" \
  "s_xor_b32 s97, %[wob], 917504\n\t" \
  "v_mfma_f32_16x16x32_bf16 a[96:99], a[240:243], v[128:131], a[96:99]\n\t" \
  "v_add_f32 v160, v160, v161\n\t" \
  "ds_read_b128 v[192:195], v215 offset:32768\n\t" \
  "v_mfma_f32_16x16x32_bf16 a[96:99], a[244:247], v[132:135], a[96:99]\n\t" \
  "v_rcp_f32 v208, v160\n\t" \
  "v_mfma_f32_16x16x32_bf16 a[112:115], a[248:251], v[128:131], a[112:115]\n\t" \
  "v_accvgpr_read_b32 v16, a0\n\t" \
  "v_accvgpr_read_b32 v17, a1\n\t" \
  "v_mfma_f32_16x16x32_bf16 a[112:115], a[252:255], v[132:135], a[112:115]\n\t" \
  "v_accvgpr_read_b32 v18, a2\n\t" \
  "v_accvgpr_read_b32 v19, a3\n\t" \
  "s_waitcnt vmcnt(8)\n\t" \
  "s_barrier\n\t" \
  "v_mfma_f32_16x16x32_bf16 a[4:7], a[192:195], v[136:139], a[4:7]\n\t" \
  "v_accvgpr_read_b32 v20, a16\n\t" \
  "v_accvgpr_read_b32 v21, a17\n\t" \
  "v_mfma_f32_16x16x32_bf16 a[4:7], a[196:199], v[140:143], a[4:7]\n\t" \
  "v_accvgpr_read_b32 v22, a18\n\t" \
  "v_accvgpr_read_b32 v23, a19\n\t" \
  "v_smfmac_f32_16x16x64_bf16 v[164:167], v[216:219], v[136:143], v239\n\t" \
  "ds_read_b64_tr_b16 v[92:93], v236 offset:24576\n\t" \
  "ds_read_b128 v[196:199], v215 offset:33792\n\t" \
  "v_mfma_f32_16x16x32_bf16 a[20:23], a[200:203], v[136:139], a[20:23]\n\t" \
  "v_mul_f32 v16, v16, v208\n\t" \
  "v_mul_f32 v17, v17, v208\n\t" \
  "v_mfma_f32_16x16x32_bf16 a[20:23], a[204:207], v[140:143], a[20:23]\n\t" \
  "v_mul_f32 v18, v18, v208\n\t" \
  "v_mul_f32 v19, v19, v208\n\t" \
  "v_mfma_f32_16x16x32_bf16 a[36:39], a[208:211], v[136:139], a[36:39]\n\t" \
  "v_mul_f32 v20, v20, v208\n\t" \
  "v_mul_f32 v21, v21, v208\n\t" \
  "v_mfma_f32_16x16x32_bf16 a[36:39], a[212:215], v[140:143], a[36:39]\n\t" \
  "v_mul_f32 v22, v22, v208\n\t" \
  "v_mul_f32 v23, v23, v208\n\t" \
  "v_mfma_f32_16x16x32_bf16 a[52:55], a[216:219], v[136:139], a[52:55]\n\t" \
  "ds_read_b64_tr_b16 v[94:95], v236 offset:28672\n\t" \
  "ds_read_b128 v[200:203], v215 offset:34816\n\t" \
  "v_mfma_f32_16x16x32_bf16 a[52:55], a[220:223], v[140:143], a[52:55]\n\t" \
  "ds_read_b128 v[204:207], v215 offset:35840\n\t" \
  "s_waitcnt lgkmcnt(0)\n\t" \
  "v_mfma_f32_16x16x32_bf16 a[68:71], a[224:227], v[136:139], a[68:71]\n\t" \
  "buffer_load_dwordx4 v240, %[wo], s97 offen offset:0 lds\n\t" \
  "v_cvt_pk_bf16_f32 v0, v16, v17\n\t" \
  "v_mfma_f32_16x16x32_bf16 a[68:71], a[228:231], v[140:143], a[68:71]\n\t" \
  "s_add_u32 s97, s97, 7168\n\t" \
  "buffer_load_dwordx4 v241, %[wo], s97 offen offset:1024 lds\n\t" \
  "v_mfma_f32_16x16x32_bf16 a[84:87], a[232:235], v[136:139], a[84:87]\n\t" \
  "s_add_u32 s97, s97, 7168\n\t" \
  "buffer_load_dwordx4 v240, %[wo], s97 offen offset:2048 lds\n\t" \
  "v_mfma_f32_16x16x32_bf16 a[84:87], a[236:239], v[140:143], a[84:87]\n\t" \
  "s_add_u32 s97, s97, 7168\n\t" \
  "buffer_load_dwordx4 v241, %[wo], s97 offen offset:3072 lds\n\t" \
  "v_mfma_f32_16x16x32_bf16 a[100:103], a[240:243], v[136:139], a[100:103]\n\t" \
  "v_cvt_pk_bf16_f32 v1, v18, v19\n\t" \
  "v_cvt_pk_bf16_f32 v2, v20, v21\n\t" \
  "v_mfma_f32_16x16x32_bf16 a[100:103], a[244:247], v[140:143], a[100:103]\n\t" \
  "v_cvt_pk_bf16_f32 v3, v22, v23\n\t" \
  "v_xor_b32 v235, 128, v234\n\t" \
  "v_mfma_f32_16x16x32_bf16 a[116:119], a[248:251], v[136:139], a[116:119]\n\t" \
  "s_xor_b32 s98, %[eso], 0\n\t" \
  "buffer_store_dwordx4 v[0:3], v242, %[ws], s98 offen offset:0 sc1\n\t" \
  "v_mfma_f32_16x16x32_bf16 a[116:119], a[252:255], v[140:143], a[116:119]\n\t" \
  "v_accvgpr_read_b32 v24, a32\n\t" \
  "v_accvgpr_read_b32 v25, a33\n\t" \
  "v_exp_f32 v68, v68\n\t" \
  "v_exp_f32 v69, v69\n\t" \
  "v_exp_f32 v70, v70\n\t" \
  "v_exp_f32 v71, v71\n\t" \
  "v_cvt_pk_bf16_f32 v146, v68, v69\n\t" \
  "v_cvt_pk_bf16_f32 v147, v70, v71\n\t" \
  "s_nop 1\n\t" \
  "v_mfma_f32_16x16x32_bf16 a[8:11], a[192:195], v[144:147], a[8:11]\n\t" \
  "v_accvgpr_read_b32 v26, a34\n\t" \
  "v_accvgpr_read_b32 v27, a35\n\t" \
  "v_exp_f32 v72, v72\n\t" \
  "v_exp_f32 v73, v73\n\t" \
  "v_exp_f32 v74, v74\n\t" \
  "v_exp_f32 v75, v75\n\t" \
  "v_exp_f32 v76, v76\n\t" \
  "v_exp_f32 v77, v77\n\t" \
  "v_exp_f32 v78, v78\n\t" \
  "v_exp_f32 v79, v79\n\t" \
  "v_cvt_pk_bf16_f32 v148, v72, v73\n\t" \
  "v_cvt_pk_bf16_f32 v149, v74, v75\n\t" \
  "v_cvt_pk_bf16_f32 v150, v76, v77\n\t" \
  "v_cvt_pk_bf16_f32 v151, v78, v79\n\t" \
  "s_nop 1\n\t" \
  "v_mfma_f32_16x16x32_bf16 a[8:11], a[196:199], v[148:151], a[8:11]\n\t" \
  "v_accvgpr_read_b32 v28, a48\n\t" \
  "v_accvgpr_read_b32 v29, a49\n\t" \
  "v_smfmac_f32_16x16x64_bf16 v[168:171], v[216:219], v[144:151], v239\n\t" \
  "v_accvgpr_read_b32 v30, a50\n\t" \
  "v_accvgpr_read_b32 v31, a51\n\t" \
  "v_mfma_f32_16x16x32_bf16 a[24:27], a[200:203], v[144:147], a[24:27]\n\t" \
  "ds_read_b64_tr_b16 v[112:113], v235 offset:0\n\t" \
  "ds_read_b64_tr_b16 v[114:115], v235 offset:4096\n\t" \
  "v_mfma_f32_16x16x32_bf16 a[24:27], a[204:207], v[148:151], a[24:27]\n\t" \
  "v_mul_f32 v24, v24, v208\n\t" \
  "v_mul_f32 v25, v25, v208\n\t" \
  "v_mfma_f32_16x16x32_bf16 a[40:43], a[208:211], v[144:147], a[40:43]\n\t" \
  "v_mul_f32 v26, v26, v208\n\t" \
  "v_mul_f32 v27, v27, v208\n\t" \
  "v_mfma_f32_16x16x32_bf16 a[40:43], a[212:215], v[148:151], a[40:43]\n\t" \
  "v_mul_f32 v28, v28, v208\n\t" \
  "v_mul_f32 v29, v29, v208\n\t" \
  "v_mfma_f32_16x16x32_bf16 a[56:59], a[216:219], v[144:147], a[56:59]\n\t" \
  "v_mul_f32 v30, v30, v208\n\t" \
  "v_mul_f32 v31, v31, v208\n\t" \
  "v_mfma_f32_16x16x32_bf16 a[56:59], a[220:223], v[148:151], a[56:59]\n\t" \
  "ds_read_b64_tr_b16 v[116:117], v235 offset:8192\n\t" \
  "v_cvt_pk_bf16_f32 v4, v24, v25\n\t" \
  "v_mfma_f32_16x16x32_bf16 a[72:75], a[224:227], v[144:147], a[72:75]\n\t" \
  "v_cvt_pk_bf16_f32 v5, v26, v27\n\t" \
  "v_cvt_pk_bf16_f32 v6, v28, v29\n\t" \
  "v_mfma_f32_16x16x32_bf16 a[72:75], a[228:231], v[148:151], a[72:75]\n\t" \
  "v_cvt_pk_bf16_f32 v7, v30, v31\n\t" \
  "ds_read_b64_tr_b16 v[118:119], v235 offset:12288\n\t" \
  "v_mfma_f32_16x16x32_bf16 a[88:91], a[232:235], v[144:147], a[88:91]\n\t" \
  "s_xor_b32 s98, %[eso], 0\n\t" \
  "buffer_store_dwordx4 v[4:7], v242, %[ws], s98 offen offset:1024 sc1\n\t" \
  "v_mfma_f32_16x16x32_bf16 a[88:91], a[236:239], v[148:151], a[88:91]\n\t" \
  "v_accvgpr_read_b32 v16, a64\n\t" \
  "v_accvgpr_read_b32 v17, a65\n\t" \
  "v_mfma_f32_16x16x32_bf16 a[104:107], a[240:243], v[144:147], a[104:107]\n\t" \
  "v_accvgpr_read_b32 v18, a66\n\t" \
  "v_accvgpr_read_b32 v19, a67\n\t" \
  "v_mfma_f32_16x16x32_bf16 a[104:107], a[244:247], v[148:151], a[104:107]\n\t" \
  "v_accvgpr_read_b32 v20, a80\n\t" \
  "v_accvgpr_read_b32 v21, a81\n\t" \
  "v_mfma_f32_16x16x32_bf16 a[120:123], a[248:251], v[144:147], a[120:123]\n\t" \
  "v_accvgpr_read_b32 v22, a82\n\t" \
  "v_accvgpr_read_b32 v23, a83\n\t" \
  "v_mfma_f32_16x16x32_bf16 a[120:123], a[252:255], v[148:151], a[120:123]\n\t" \
  "ds_read_b64_tr_b16 v[120:121], v235 offset:16384\n\t" \
  "v_mul_f32 v16, v16, v208\n\t" \
  "v_exp_f32 v96, v96\n\t" \
  "v_exp_f32 v97, v97\n\t" \
  "v_exp_f32 v98, v98\n\t" \
  "v_exp_f32 v99, v99\n\t" \
  "v_exp_f32 v100, v100\n\t" \
  "v_exp_f32 v101, v101\n\t" \
  "v_exp_f32 v102, v102\n\t" \
  "v_exp_f32 v103, v103\n\t" \
  "v_cvt_pk_bf16_f32 v152, v96, v97\n\t" \
  "v_cvt_pk_bf16_f32 v153, v98, v99\n\t" \
  "v_cvt_pk_bf16_f32 v154, v100, v101\n\t" \
  "v_cvt_pk_bf16_f32 v155, v102, v103\n\t" \
  "s_nop 1\n\t" \
  "v_mfma_f32_16x16x32_bf16 a[12:15], a[192:195], v[152:155], a[12:15]\n\t" \
  "v_mul_f32 v17, v17, v208\n\t" \
  "v_mul_f32 v18, v18, v208\n\t" \
  "v_exp_f32 v104, v104\n\t" \
  "v_exp_f32 v105, v105\n\t" \
  "v_exp_f32 v106, v106\n\t" \
  "v_exp_f32 v107, v107\n\t" \
  "v_exp_f32 v108, v108\n\t" \
  "v_exp_f32 v109, v109\n\t" \
  "v_exp_f32 v110, v110\n\t" \
  "v_exp_f32 v111, v111\n\t" \
  "v_cvt_pk_bf16_f32 v156, v104, v105\n\t" \
  "v_cvt_pk_bf16_f32 v157, v106, v107\n\t" \
  "v_cvt_pk_bf16_f32 v158, v108, v109\n\t" \
  "v_cvt_pk_bf16_f32 v159, v110, v111\n\t" \
  "s_nop 1\n\t" \
  "v_mfma_f32_16x16x32_bf16 a[12:15], a[196:199], v[156:159], a[12:15]\n\t" \
  "v_mul_f32 v19, v19, v208\n\t" \
  "v_mul_f32 v20, v20, v208\n\t" \
  "v_smfmac_f32_16x16x64_bf16 v[172:175], v[216:219], v[152:159], v239\n\t" \
  "v_mul_f32 v21, v21, v208\n\t" \
  "v_mul_f32 v22, v22, v208\n\t" \
  "v_mfma_f32_16x16x32_bf16 a[28:31], a[200:203], v[152:155], a[28:31]\n\t" \
  "v_mul_f32 v23, v23, v208\n\t" \
  "ds_read_b64_tr_b16 v[122:123], v235 offset:20480\n\t" \
  "v_mfma_f32_16x16x32_bf16 a[28:31], a[204:207], v[156:159], a[28:31]\n\t" \
  "v_cvt_pk_bf16_f32 v8, v16, v17\n\t" \
  "v_cvt_pk_bf16_f32 v9, v18, v19\n\t" \
  "v_mfma_f32_16x16x32_bf16 a[44:47], a[208:211], v[152:155], a[44:47]\n\t" \
  "v_cvt_pk_bf16_f32 v10, v20, v21\n\t" \
  "v_cvt_pk_bf16_f32 v11, v22, v23\n\t" \
  "v_mfma_f32_16x16x32_bf16 a[44:47], a[212:215], v[156:159], a[44:47]\n\t" \
  "s_xor_b32 s98, %[eso], 0\n\t" \
  "buffer_store_dwordx4 v[8:11], v242, %[ws], s98 offen offset:2048 sc1\n\t" \
  "v_mfma_f32_16x16x32_bf16 a[60:63], a[216:219], v[152:155], a[60:63]\n\t" \
  "ds_read_b64_tr_b16 v[124:125], v235 offset:24576\n\t" \
  "v_accvgpr_read_b32 v24, a96\n\t" \
  "v_mfma_f32_16x16x32_bf16 a[60:63], a[220:223], v[156:159], a[60:63]\n\t" \
  "v_accvgpr_read_b32 v25, a97\n\t" \
  "v_accvgpr_read_b32 v26, a98\n\t" \
  "v_mfma_f32_16x16x32_bf16 a[76:79], a[224:227], v[152:155], a[76:79]\n\t" \
  "v_accvgpr_read_b32 v27, a99\n\t" \
  "v_accvgpr_read_b32 v28, a112\n\t" \
  "v_mfma_f32_16x16x32_bf16 a[76:79], a[228:231], v[156:159], a[76:79]\n\t" \
  "v_accvgpr_read_b32 v29, a113\n\t" \
  "v_accvgpr_read_b32 v30, a114\n\t" \
  "v_mfma_f32_16x16x32_bf16 a[92:95], a[232:235], v[152:155], a[92:95]\n\t" \
  "v_accvgpr_read_b32 v31, a115\n\t" \
  "ds_read_b64_tr_b16 v[126:127], v235 offset:28672\n\t" \
  "v_mfma_f32_16x16x32_bf16 a[92:95], a[236:239], v[156:159], a[92:95]\n\t" \
  "v_mul_f32 v24, v24, v208\n\t" \
  "v_mul_f32 v25, v25, v208\n\t" \
  "v_mfma_f32_16x16x32_bf16 a[108:111], a[240:243], v[152:155], a[108:111]\n\t" \
  "v_mul_f32 v26, v26, v208\n\t" \
  "v_mul_f32 v27, v27, v208\n\t" \
  "v_mfma_f32_16x16x32_bf16 a[108:111], a[244:247], v[156:159], a[108:111]\n\t" \
  "v_mul_f32 v28, v28, v208\n\t" \
  "v_mul_f32 v29, v29, v208\n\t" \
  "v_mfma_f32_16x16x32_bf16 a[124:127], a[248:251], v[152:155], a[124:127]\n\t" \
  "v_mul_f32 v30, v30, v208\n\t" \
  "v_mul_f32 v31, v31, v208\n\t" \
  "v_mfma_f32_16x16x32_bf16 a[124:127], a[252:255], v[156:159], a[124:127]\n\t" \
  "ds_read_b64_tr_b16 a[128:129], v234 offset:0\n\t" \
  "ds_read_b64_tr_b16 a[130:131], v234 offset:4096\n\t" \
  "v_cvt_pk_bf16_f32 v12, v24, v25\n\t" \
  "v_cvt_pk_bf16_f32 v13, v26, v27\n\t" \
  "v_cvt_pk_bf16_f32 v14, v28, v29\n\t" \
  "v_cvt_pk_bf16_f32 v15, v30, v31\n\t" \
  "v_mfma_f32_16x16x32_bf16 a[32:35], v[48:51], v[0:3], 0\n\t" \
  "s_xor_b32 s98, %[eso], 0\n\t" \
  "buffer_store_dwordx4 v[12:15], v242, %[ws], s98 offen offset:3072 sc1\n\t" \
  "v_mfma_f32_16x16x32_bf16 a[32:35], v[52:55], v[4:7], a[32:35]\n\t" \
  "ds_read_b64_tr_b16 a[132:133], v234 offset:8192\n\t" \
  "ds_read_b64_tr_b16 a[134:135], v234 offset:12288\n\t" \
  "v_mfma_f32_16x16x32_bf16 a[32:35], v[56:59], v[8:11], a[32:35]\n\t" \
  "v_add_f32 v164, v164, v165\n\t" \
  "ds_read_b64_tr_b16 a[136:137], v234 offset:16384\n\t" \
  "v_mfma_f32_16x16x32_bf16 a[32:35], v[60:63], v[12:15], a[32:35]\n\t" \
  "v_rcp_f32 v210, v164\n\t" \
  "v_mfma_f32_16x16x32_bf16 a[48:51], v[92:95], v[12:15], 0\n\t" \
  "v_accvgpr_read_b32 v16, a4\n\t" \
  "v_accvgpr_read_b32 v17, a5\n\t" \
  "v_mfma_f32_16x16x32_bf16 a[48:51], v[88:91], v[8:11], a[48:51]\n\t" \
  "v_accvgpr_read_b32 v18, a6\n\t" \
  "v_accvgpr_read_b32 v19, a7\n\t" \
  "v_mfma_f32_16x16x32_bf16 a[48:51], v[84:87], v[4:7], a[48:51]\n\t" \
  "v_accvgpr_read_b32 v20, a20\n\t" \
  "v_accvgpr_read_b32 v21, a21\n\t" \
  "v_mfma_f32_16x16x32_bf16 a[48:51], v[80:83], v[0:3], a[48:51]\n\t" \
  "v_accvgpr_read_b32 v22, a22\n\t" \
  "v_accvgpr_read_b32 v23, a23\n\t" \
  "ds_read_b64_tr_b16 a[138:139], v234 offset:20480\n\t" \
  "ds_read_b64_tr_b16 a[140:141], v234 offset:24576\n\t" \
  "ds_read_b64_tr_b16 a[142:143], v234 offset:28672\n\t" \
  "s_waitcnt lgkmcnt(14)\n\t" \
  "v_mfma_f32_16x16x32_bf16 a[64:67], v[112:115], v[0:3], 0\n\t" \
  "v_mul_f32 v16, v16, v210\n\t" \
  "v_mul_f32 v17, v17, v210\n\t" \
  "s_waitcnt lgkmcnt(12)\n\t" \
  "v_mfma_f32_16x16x32_bf16 a[64:67], v[116:119], v[4:7], a[64:67]\n\t" \
  "v_mul_f32 v18, v18, v210\n\t" \
  "v_mul_f32 v19, v19, v210\n\t" \
  "s_waitcnt lgkmcnt(10)\n\t" \
  "v_mfma_f32_16x16x32_bf16 a[64:67], v[120:123], v[8:11], a[64:67]\n\t" \
  "v_mul_f32 v20, v20, v210\n\t" \
  "v_mul_f32 v21, v21, v210\n\t" \
  "s_waitcnt lgkmcnt(8)\n\t" \
  "v_mfma_f32_16x16x32_bf16 a[64:67], v[124:127], v[12:15], a[64:67]\n\t" \
  "v_mul_f32 v22, v22, v210\n\t" \
  "v_mul_f32 v23, v23, v210\n\t" \
  "v_xor_b32 v236, 32, v234\n\t" \
  "ds_read_b64_tr_b16 a[144:145], v236 offset:0\n\t" \
  "ds_read_b64_tr_b16 a[146:147], v236 offset:4096\n\t" \
  "ds_read_b64_tr_b16 a[148:149], v236 offset:8192\n\t" \
  "ds_read_b64_tr_b16 a[150:151], v236 offset:12288\n\t" \
  "ds_read_b64_tr_b16 a[152:153], v236 offset:16384\n\t" \
  "ds_read_b64_tr_b16 a[154:155], v236 offset:20480\n\t" \
  "ds_read_b64_tr_b16 a[156:157], v236 offset:24576\n\t" \
  "ds_read_b64_tr_b16 a[158:159], v236 offset:28672\n\t" \
  "s_waitcnt lgkmcnt(8)\n\t" \
  "v_mfma_f32_16x16x32_bf16 a[0:3], a[140:143], v[12:15], 0\n\t" \
  "v_cvt_pk_bf16_f32 v32, v16, v17\n\t" \
  "v_cvt_pk_bf16_f32 v33, v18, v19\n\t" \
  "v_mfma_f32_16x16x32_bf16 a[0:3], a[136:139], v[8:11], a[0:3]\n\t" \
  "v_cvt_pk_bf16_f32 v34, v20, v21\n\t" \
  "v_cvt_pk_bf16_f32 v35, v22, v23\n\t" \
  "v_mfma_f32_16x16x32_bf16 a[0:3], a[132:135], v[4:7], a[0:3]\n\t" \
  "s_xor_b32 s98, %[eso], 32768\n\t" \
  "buffer_store_dwordx4 v[32:35], v242, %[ws], s98 offen offset:0 sc1\n\t" \
  "v_mfma_f32_16x16x32_bf16 a[0:3], a[128:131], v[0:3], a[0:3]\n\t" \
  "v_accvgpr_read_b32 v24, a36\n\t" \
  "v_accvgpr_read_b32 v25, a37\n\t" \
  "v_xor_b32 v235, 192, v234\n\t" \
  "ds_read_b64_tr_b16 a[160:161], v235 offset:0\n\t" \
  "ds_read_b64_tr_b16 a[162:163], v235 offset:4096\n\t" \
  "ds_read_b64_tr_b16 a[164:165], v235 offset:8192\n\t" \
  "ds_read_b64_tr_b16 a[166:167], v235 offset:12288\n\t" \
  "ds_read_b64_tr_b16 a[168:169], v235 offset:16384\n\t" \
  "ds_read_b64_tr_b16 a[170:171], v235 offset:20480\n\t" \
  "ds_read_b64_tr_b16 a[172:173], v235 offset:24576\n\t" \
  "ds_read_b64_tr_b16 a[174:175], v235 offset:28672\n\t" \
  "s_waitcnt lgkmcnt(14)\n\t" \
  "v_mfma_f32_16x16x32_bf16 a[16:19], a[144:147], v[0:3], 0\n\t" \
  "v_accvgpr_read_b32 v26, a38\n\t" \
  "v_accvgpr_read_b32 v27, a39\n\t" \
  "s_waitcnt lgkmcnt(12)\n\t" \
  "v_mfma_f32_16x16x32_bf16 a[16:19], a[148:151], v[4:7], a[16:19]\n\t" \
  "v_accvgpr_read_b32 v28, a52\n\t" \
  "v_accvgpr_read_b32 v29, a53\n\t" \
  "s_waitcnt lgkmcnt(10)\n\t" \
  "v_mfma_f32_16x16x32_bf16 a[16:19], a[152:155], v[8:11], a[16:19]\n\t" \
  "v_accvgpr_read_b32 v30, a54\n\t" \
  "v_accvgpr_read_b32 v31, a55\n\t" \
  "s_waitcnt lgkmcnt(8)\n\t" \
  "v_mfma_f32_16x16x32_bf16 a[16:19], a[156:159], v[12:15], a[16:19]\n\t" \
  "v_mul_f32 v24, v24, v210\n\t" \
  "v_mul_f32 v25, v25, v210\n\t" \
  "v_xor_b32 v236, 224, v234\n\t" \
  "ds_read_b64_tr_b16 a[176:177], v236 offset:0\n\t" \
  "ds_read_b64_tr_b16 a[178:179], v236 offset:4096\n\t" \
  "ds_read_b64_tr_b16 a[180:181], v236 offset:8192\n\t" \
  "ds_read_b64_tr_b16 a[182:183], v236 offset:12288\n\t" \
  "ds_read_b64_tr_b16 a[184:185], v236 offset:16384\n\t" \
  "ds_read_b64_tr_b16 a[186:187], v236 offset:20480\n\t" \
  "ds_read_b64_tr_b16 a[188:189], v236 offset:24576\n\t" \
  "ds_read_b64_tr_b16 a[190:191], v236 offset:28672\n\t" \
  "s_waitcnt lgkmcnt(8)\n\t" \
  "v_mfma_f32_16x16x32_bf16 a[96:99], a[172:175], v[12:15], 0\n\t" \
  "v_mul_f32 v26, v26, v210\n\t" \
  "v_mul_f32 v27, v27, v210\n\t" \
  "v_mfma_f32_16x16x32_bf16 a[96:99], a[168:171], v[8:11], a[96:99]\n\t" \
  "v_mul_f32 v28, v28, v210\n\t" \
  "v_mul_f32 v29, v29, v210\n\t" \
  "v_mfma_f32_16x16x32_bf16 a[96:99], a[164:167], v[4:7], a[96:99]\n\t" \
  "v_mul_f32 v30, v30, v210\n\t" \
  "v_mul_f32 v31, v31, v210\n\t" \
  "v_mfma_f32_16x16x32_bf16 a[96:99], a[160:163], v[0:3], a[96:99]\n\t" \
  "v_cvt_pk_bf16_f32 v36, v24, v25\n\t" \
  "v_cvt_pk_bf16_f32 v37, v26, v27\n\t" \
  "v_xor_b32 v236, 160, v234\n\t" \
  "ds_read_b64_tr_b16 v[96:97], v236 offset:0\n\t" \
  "ds_read_b64_tr_b16 v[98:99], v236 offset:4096\n\t" \
  "ds_read_b64_tr_b16 v[100:101], v236 offset:8192\n\t" \
  "ds_read_b64_tr_b16 v[102:103], v236 offset:12288\n\t" \
  "ds_read_b64_tr_b16 v[104:105], v236 offset:16384\n\t" \
  "ds_read_b64_tr_b16 v[106:107], v236 offset:20480\n\t" \
  "ds_read_b64_tr_b16 v[108:109], v236 offset:24576\n\t" \
  "ds_read_b64_tr_b16 v[110:111], v236 offset:28672\n\t" \
  "s_waitcnt lgkmcnt(14)\n\t" \
  "v_mfma_f32_16x16x32_bf16 a[112:115], a[176:179], v[0:3], 0\n\t" \
  "v_cvt_pk_bf16_f32 v38, v28, v29\n\t" \
  "v_cvt_pk_bf16_f32 v39, v30, v31\n\t" \
  "s_waitcnt lgkmcnt(12)\n\t" \
  "v_mfma_f32_16x16x32_bf16 a[112:115], a[180:183], v[4:7], a[112:115]\n\t" \
  "s_xor_b32 s98, %[eso], 32768\n\t" \
  "buffer_store_dwordx4 v[36:39], v242, %[ws], s98 offen offset:1024 sc1\n\t" \
  "s_waitcnt lgkmcnt(10)\n\t" \
  "v_mfma_f32_16x16x32_bf16 a[112:115], a[184:187], v[8:11], a[112:115]\n\t" \
  "v_accvgpr_read_b32 v16, a68\n\t" \
  "v_accvgpr_read_b32 v17, a69\n\t" \
  "s_waitcnt lgkmcnt(8)\n\t" \
  "v_mfma_f32_16x16x32_bf16 a[112:115], a[188:191], v[12:15], a[112:115]\n\t" \
  "v_accvgpr_read_b32 v18, a70\n\t" \
  "v_accvgpr_read_b32 v19, a71\n\t" \
  "s_waitcnt lgkmcnt(0)\n\t" \
  "v_mfma_f32_16x16x32_bf16 a[80:83], v[108:111], v[12:15], 0\n\t" \
  "v_accvgpr_read_b32 v20, a84\n\t" \
  "v_accvgpr_read_b32 v21, a85\n\t" \
  "v_mfma_f32_16x16x32_bf16 a[80:83], v[104:107], v[8:11], a[80:83]\n\t" \
  "v_accvgpr_read_b32 v22, a86\n\t" \
  "v_accvgpr_read_b32 v23, a87\n\t" \
  "v_mfma_f32_16x16x32_bf16 a[80:83], v[100:103], v[4:7], a[80:83]\n\t" \
  "v_mul_f32 v16, v16, v210\n\t" \
  "v_mul_f32 v17, v17, v210\n\t" \
  "v_mfma_f32_16x16x32_bf16 a[80:83], v[96:99], v[0:3], a[80:83]\n\t" \
  "v_mul_f32 v18, v18, v210\n\t" \
  "v_mul_f32 v19, v19, v210\n\t" \
  "v_mul_f32 v20, v20, v210\n\t" \
  "v_mul_f32 v21, v21, v210\n\t" \
  "v_mul_f32 v22, v22, v210\n\t" \
  "v_mul_f32 v23, v23, v210\n\t" \
  "v_cvt_pk_bf16_f32 v40, v16, v17\n\t" \
  "v_cvt_pk_bf16_f32 v41, v18, v19\n\t" \
  "v_cvt_pk_bf16_f32 v42, v20, v21\n\t" \
  "v_cvt_pk_bf16_f32 v43, v22, v23\n\t" \
  "v_accvgpr_read_b32 v24, a100\n\t" \
  "v_accvgpr_read_b32 v25, a101\n\t" \
  "v_accvgpr_read_b32 v26, a102\n\t" \
  "v_accvgpr_read_b32 v27, a103\n\t" \
  "v_accvgpr_read_b32 v28, a116\n\t" \
  "v_accvgpr_read_b32 v29, a117\n\t" \
  "v_accvgpr_read_b32 v30, a118\n\t" \
  "v_accvgpr_read_b32 v31, a119\n\t" \
  "v_mul_f32 v24, v24, v210\n\t" \
  "v_mul_f32 v25, v25, v210\n\t" \
  "v_mul_f32 v26, v26, v210\n\t" \
  "v_mul_f32 v27, v27, v210\n\t" \
  "v_mul_f32 v28, v28, v210\n\t" \
  "v_mul_f32 v29, v29, v210\n\t" \
  "v_mul_f32 v30, v30, v210\n\t" \
  "v_mul_f32 v31, v31, v210\n\t" \
  "v_cvt_pk_bf16_f32 v44, v24, v25\n\t" \
  "v_cvt_pk_bf16_f32 v45, v26, v27\n\t" \
  "v_cvt_pk_bf16_f32 v46, v28, v29\n\t" \
  "v_cvt_pk_bf16_f32 v47, v30, v31\n\t" \
  "v_mfma_f32_16x16x32_bf16 a[36:39], v[48:51], v[32:35], 0\n\t" \
  "s_xor_b32 s98, %[eso], 32768\n\t" \
  "buffer_store_dwordx4 v[40:43], v242, %[ws], s98 offen offset:2048 sc1\n\t" \
  "v_mfma_f32_16x16x32_bf16 a[36:39], v[52:55], v[36:39], a[36:39]\n\t" \
  "s_xor_b32 s98, %[eso], 32768\n\t" \
  "buffer_store_dwordx4 v[44:47], v242, %[ws], s98 offen offset:3072 sc1\n\t" \
  "v_mfma_f32_16x16x32_bf16 a[36:39], v[56:59], v[40:43], a[36:39]\n\t" \
  "v_add_f32 v168, v168, v169\n\t" \
  "ds_read_b128 a[224:227], v213 offset:32768\n\t" \
  "v_mfma_f32_16x16x32_bf16 a[36:39], v[60:63], v[44:47], a[36:39]\n\t" \
  "v_rcp_f32 v212, v168\n\t" \
  "s_waitcnt vmcnt(0)\n\t" \
  "buffer_store_dword v243, v254, %[ws], %[fw2] offen offset:0 sc1\n\t" \
  "v_mfma_f32_16x16x32_bf16 a[52:55], v[92:95], v[44:47], 0\n\t" \
  "v_accvgpr_read_b32 v16, a8\n\t" \
  "v_accvgpr_read_b32 v17, a9\n\t" \
  "v_mfma_f32_16x16x32_bf16 a[52:55], v[88:91], v[40:43], a[52:55]\n\t" \
  "v_accvgpr_read_b32 v18, a10\n\t" \
  "v_accvgpr_read_b32 v19, a11\n\t" \
  "v_mfma_f32_16x16x32_bf16 a[52:55], v[84:87], v[36:39], a[52:55]\n\t" \
  "v_accvgpr_read_b32 v20, a24\n\t" \
  "v_accvgpr_read_b32 v21, a25\n\t" \
  "v_mfma_f32_16x16x32_bf16 a[52:55], v[80:83], v[32:35], a[52:55]\n\t" \
  "v_accvgpr_read_b32 v22, a26\n\t" \
  "v_accvgpr_read_b32 v23, a27\n\t" \
  "ds_read_b128 a[228:231], v213 offset:33792\n\t" \
  "ds_read_b128 a[232:235], v213 offset:34816\n\t" \
  "ds_read_b128 a[236:239], v213 offset:35840\n\t" \
  "ds_read_b128 a[240:243], v215 offset:0\n\t" \
  "ds_read_b128 a[244:247], v215 offset:1024\n\t" \
  "ds_read_b128 a[248:251], v215 offset:2048\n\t" \
  "ds_read_b128 a[252:255], v215 offset:3072\n\t" \
  "v_mfma_f32_16x16x32_bf16 a[68:71], v[112:115], v[32:35], 0\n\t" \
  "v_mul_f32 v16, v16, v212\n\t" \
  "v_mul_f32 v17, v17, v212\n\t" \
  "v_mfma_f32_16x16x32_bf16 a[68:71], v[116:119], v[36:39], a[68:71]\n\t" \
  "v_mul_f32 v18, v18, v212\n\t" \
  "v_mul_f32 v19, v19, v212\n\t" \
  "v_mfma_f32_16x16x32_bf16 a[68:71], v[120:123], v[40:43], a[68:71]\n\t" \
  "v_mul_f32 v20, v20, v212\n\t" \
  "v_mul_f32 v21, v21, v212\n\t" \
  "v_mfma_f32_16x16x32_bf16 a[68:71], v[124:127], v[44:47], a[68:71]\n\t" \
  "v_mul_f32 v22, v22, v212\n\t" \
  "v_mul_f32 v23, v23, v212\n\t" \
  "v_mfma_f32_16x16x32_bf16 a[4:7], a[140:143], v[44:47], 0\n\t" \
  "v_cvt_pk_bf16_f32 v64, v16, v17\n\t" \
  "v_cvt_pk_bf16_f32 v65, v18, v19\n\t" \
  "v_mfma_f32_16x16x32_bf16 a[4:7], a[136:139], v[40:43], a[4:7]\n\t" \
  "v_cvt_pk_bf16_f32 v66, v20, v21\n\t" \
  "v_cvt_pk_bf16_f32 v67, v22, v23\n\t" \
  "v_mfma_f32_16x16x32_bf16 a[4:7], a[132:135], v[36:39], a[4:7]\n\t" \
  "s_xor_b32 s98, %[eso], 65536\n\t" \
  "buffer_store_dwordx4 v[64:67], v242, %[ws], s98 offen offset:0 sc1\n\t" \
  "v_mfma_f32_16x16x32_bf16 a[4:7], a[128:131], v[32:35], a[4:7]\n\t" \
  "v_accvgpr_read_b32 v24, a40\n\t" \
  "v_accvgpr_read_b32 v25, a41\n\t" \
  "v_mfma_f32_16x16x32_bf16 a[20:23], a[144:147], v[32:35], 0\n\t" \
  "v_accvgpr_read_b32 v26, a42\n\t" \
  "v_accvgpr_read_b32 v27, a43\n\t" \
  "v_mfma_f32_16x16x32_bf16 a[20:23], a[148:151], v[36:39], a[20:23]\n\t" \
  "v_accvgpr_read_b32 v28, a56\n\t" \
  "v_accvgpr_read_b32 v29, a57\n\t" \
  "v_mfma_f32_16x16x32_bf16 a[20:23], a[152:155], v[40:43], a[20:23]\n\t" \
  "v_accvgpr_read_b32 v30, a58\n\t" \
  "v_accvgpr_read_b32 v31, a59\n\t" \
  "v_mfma_f32_16x16x32_bf16 a[20:23], a[156:159], v[44:47], a[20:23]\n\t" \
  "v_mul_f32 v24, v24, v212\n\t" \
  "v_mul_f32 v25, v25, v212\n\t" \
  "v_mfma_f32_16x16x32_bf16 a[100:103], a[172:175], v[44:47], 0\n\t" \
  "v_mul_f32 v26, v26, v212\n\t" \
  "v_mul_f32 v27, v27, v212\n\t" \
  "v_mfma_f32_16x16x32_bf16 a[100:103], a[168:171], v[40:43], a[100:103]\n\t" \
  "v_mul_f32 v28, v28, v212\n\t" \
  "v_mul_f32 v29, v29, v212\n\t" \
  "v_mfma_f32_16x16x32_bf16 a[100:103], a[164:167], v[36:39], a[100:103]\n\t" \
  "v_mul_f32 v30, v30, v212\n\t" \
  "v_mul_f32 v31, v31, v212\n\t" \
  "v_mfma_f32_16x16x32_bf16 a[100:103], a[160:163], v[32:35], a[100:103]\n\t" \
  "v_cvt_pk_bf16_f32 v68, v24, v25\n\t" \
  "v_cvt_pk_bf16_f32 v69, v26, v27\n\t" \
  "v_mfma_f32_16x16x32_bf16 a[116:119], a[176:179], v[32:35], 0\n\t" \
  "v_cvt_pk_bf16_f32 v70, v28, v29\n\t" \
  "v_cvt_pk_bf16_f32 v71, v30, v31\n\t" \
  "v_mfma_f32_16x16x32_bf16 a[116:119], a[180:183], v[36:39], a[116:119]\n\t" \
  "s_xor_b32 s98, %[eso], 65536\n\t" \
  "buffer_store_dwordx4 v[68:71], v242, %[ws], s98 offen offset:1024 sc1\n\t" \
  "v_mfma_f32_16x16x32_bf16 a[116:119], a[184:187], v[40:43], a[116:119]\n\t" \
  "v_accvgpr_read_b32 v16, a72\n\t" \
  "v_accvgpr_read_b32 v17, a73\n\t" \
  "v_mfma_f32_16x16x32_bf16 a[116:119], a[188:191], v[44:47], a[116:119]\n\t" \
  "v_accvgpr_read_b32 v18, a74\n\t" \
  "v_accvgpr_read_b32 v19, a75\n\t" \
  "v_mfma_f32_16x16x32_bf16 a[84:87], v[108:111], v[44:47], 0\n\t" \
  "v_accvgpr_read_b32 v20, a88\n\t" \
  "v_accvgpr_read_b32 v21, a89\n\t" \
  "v_mfma_f32_16x16x32_bf16 a[84:87], v[104:107], v[40:43], a[84:87]\n\t" \
  "v_accvgpr_read_b32 v22, a90\n\t" \
  "v_accvgpr_read_b32 v23, a91\n\t" \
  "v_mfma_f32_16x16x32_bf16 a[84:87], v[100:103], v[36:39], a[84:87]\n\t" \
  "v_mul_f32 v16, v16, v212\n\t" \
  "v_mul_f32 v17, v17, v212\n\t" \
  "v_mfma_f32_16x16x32_bf16 a[84:87], v[96:99], v[32:35], a[84:87]\n\t" \
  "v_mul_f32 v18, v18, v212\n\t" \
  "v_mul_f32 v19, v19, v212\n\t" \
  "v_mul_f32 v20, v20, v212\n\t" \
  "v_mul_f32 v21, v21, v212\n\t" \
  "v_mul_f32 v22, v22, v212\n\t" \
  "v_mul_f32 v23, v23, v212\n\t" \
  "v_cvt_pk_bf16_f32 v72, v16, v17\n\t" \
  "v_cvt_pk_bf16_f32 v73, v18, v19\n\t" \
  "v_cvt_pk_bf16_f32 v74, v20, v21\n\t" \
  "v_cvt_pk_bf16_f32 v75, v22, v23\n\t" \
  "v_accvgpr_read_b32 v24, a104\n\t" \
  "v_accvgpr_read_b32 v25, a105\n\t" \
  "v_accvgpr_read_b32 v26, a106\n\t" \
  "v_accvgpr_read_b32 v27, a107\n\t" \
  "v_accvgpr_read_b32 v28, a120\n\t" \
  "v_accvgpr_read_b32 v29, a121\n\t" \
  "v_accvgpr_read_b32 v30, a122\n\t" \
  "v_accvgpr_read_b32 v31, a123\n\t" \
  "v_mul_f32 v24, v24, v212\n\t" \
  "v_mul_f32 v25, v25, v212\n\t" \
  "v_mul_f32 v26, v26, v212\n\t" \
  "v_mul_f32 v27, v27, v212\n\t" \
  "v_mul_f32 v28, v28, v212\n\t" \
  "v_mul_f32 v29, v29, v212\n\t" \
  "v_mul_f32 v30, v30, v212\n\t" \
  "v_mul_f32 v31, v31, v212\n\t" \
  "v_cvt_pk_bf16_f32 v76, v24, v25\n\t" \
  "v_cvt_pk_bf16_f32 v77, v26, v27\n\t" \
  "v_cvt_pk_bf16_f32 v78, v28, v29\n\t" \
  "v_cvt_pk_bf16_f32 v79, v30, v31\n\t" \
  "v_mfma_f32_16x16x32_bf16 a[40:43], v[48:51], v[64:67], 0\n\t" \
  "s_xor_b32 s98, %[eso], 65536\n\t" \
  "buffer_store_dwordx4 v[72:75], v242, %[ws], s98 offen offset:2048 sc1\n\t" \
  "v_mfma_f32_16x16x32_bf16 a[40:43], v[52:55], v[68:71], a[40:43]\n\t" \
  "s_xor_b32 s98, %[eso], 65536\n\t" \
  "buffer_store_dwordx4 v[76:79], v242, %[ws], s98 offen offset:3072 sc1\n\t" \
  "v_mfma_f32_16x16x32_bf16 a[40:43], v[56:59], v[72:75], a[40:43]\n\t" \
  "buffer_load_dwordx4 v[224:227], v252, %[ws], %[ss] offen sc1\n\t" \
  "buffer_load_dwordx4 v[128:131], v247, %[sM], %[gvo] offen\n\t" \
  "v_mfma_f32_16x16x32_bf16 a[40:43], v[60:63], v[76:79], a[40:43]\n\t" \
  "v_add_f32 v172, v172, v173\n\t" \
  "s_add_u32 m0, %[m], 147456\n\t" \
  "v_mfma_f32_16x16x32_bf16 a[56:59], v[92:95], v[76:79], 0\n\t" \
  "v_rcp_f32 v214, v172\n\t" \
  "v_mfma_f32_16x16x32_bf16 a[56:59], v[88:91], v[72:75], a[56:59]\n\t" \
  "v_accvgpr_read_b32 v16, a12\n\t" \
  "v_accvgpr_read_b32 v17, a13\n\t" \
  "v_mfma_f32_16x16x32_bf16 a[56:59], v[84:87], v[68:71], a[56:59]\n\t" \
  "v_accvgpr_read_b32 v18, a14\n\t" \
  "v_accvgpr_read_b32 v19, a15\n\t" \
  "v_mfma_f32_16x16x32_bf16 a[56:59], v[80:83], v[64:67], a[56:59]\n\t" \
  "v_accvgpr_read_b32 v20, a28\n\t" \
  "v_accvgpr_read_b32 v21, a29\n\t" \
  "v_mfma_f32_16x16x32_bf16 a[72:75], v[112:115], v[64:67], 0\n\t" \
  "v_accvgpr_read_b32 v22, a30\n\t" \
  "v_accvgpr_read_b32 v23, a31\n\t" \
  "v_mul_f32 v16, v16, v214\n\t" \
  "v_mul_f32 v17, v17, v214\n\t" \
  "v_mul_f32 v18, v18, v214\n\t" \
  "v_mul_f32 v19, v19, v214\n\t" \
  "v_mul_f32 v20, v20, v214\n\t" \
  "v_mul_f32 v21, v21, v214\n\t" \
  "v_mul_f32 v22, v22, v214\n\t" \
  "v_mul_f32 v23, v23, v214\n\t" \
  "v_cvt_pk_bf16_f32 v160, v16, v17\n\t" \
  "v_cvt_pk_bf16_f32 v161, v18, v19\n\t" \
  "v_cvt_pk_bf16_f32 v162, v20, v21\n\t" \
  "v_cvt_pk_bf16_f32 v163, v22, v23\n\t" \
  "s_waitcnt lgkmcnt(4)\n\t" \
  "ds_write_b128 v213, v[160:163] offset:32768\n\t" \
  "v_accvgpr_read_b32 v24, a44\n\t" \
  "v_accvgpr_read_b32 v25, a45\n\t" \
  "v_accvgpr_read_b32 v26, a46\n\t" \
  "v_accvgpr_read_b32 v27, a47\n\t" \
  "v_accvgpr_read_b32 v28, a60\n\t" \
  "v_accvgpr_read_b32 v29, a61\n\t" \
  "v_accvgpr_read_b32 v30, a62\n\t" \
  "v_accvgpr_read_b32 v31, a63\n\t" \
  "v_mul_f32 v24, v24, v214\n\t" \
  "v_mul_f32 v25, v25, v214\n\t" \
  "v_mul_f32 v26, v26, v214\n\t" \
  "v_mul_f32 v27, v27, v214\n\t" \
  "v_mul_f32 v28, v28, v214\n\t" \
  "v_mul_f32 v29, v29, v214\n\t" \
  "v_mul_f32 v30, v30, v214\n\t" \
  "v_mul_f32 v31, v31, v214\n\t" \
  "v_cvt_pk_bf16_f32 v164, v24, v25\n\t" \
  "v_cvt_pk_bf16_f32 v165, v26, v27\n\t" \
  "v_cvt_pk_bf16_f32 v166, v28, v29\n\t" \
  "v_cvt_pk_bf16_f32 v167, v30, v31\n\t" \
  "ds_write_b128 v213, v[164:167] offset:33792\n\t" \
  "v_accvgpr_read_b32 v16, a76\n\t" \
  "v_accvgpr_read_b32 v17, a77\n\t" \
  "v_accvgpr_read_b32 v18, a78\n\t" \
  "v_accvgpr_read_b32 v19, a79\n\t" \
  "v_accvgpr_read_b32 v20, a92\n\t" \
  "v_accvgpr_read_b32 v21, a93\n\t" \
  "v_accvgpr_read_b32 v22, a94\n\t" \
  "v_accvgpr_read_b32 v23, a95\n\t" \
  "v_mul_f32 v16, v16, v214\n\t" \
  "v_mul_f32 v17, v17, v214\n\t" \
  "v_mul_f32 v18, v18, v214\n\t" \
  "v_mul_f32 v19, v19, v214\n\t" \
  "v_mul_f32 v20, v20, v214\n\t" \
  "v_mul_f32 v21, v21, v214\n\t" \
  "v_mul_f32 v22, v22, v214\n\t" \
  "v_mul_f32 v23, v23, v214\n\t" \
  "v_cvt_pk_bf16_f32 v168, v16, v17\n\t" \
  "v_cvt_pk_bf16_f32 v169, v18, v19\n\t" \
  "v_cvt_pk_bf16_f32 v170, v20, v21\n\t" \
  "v_cvt_pk_bf16_f32 v171, v22, v23\n\t" \
  "ds_write_b128 v213, v[168:171] offset:34816\n\t" \
  "v_accvgpr_read_b32 v24, a108\n\t" \
  "v_accvgpr_read_b32 v25, a109\n\t" \
  "v_accvgpr_read_b32 v26, a110\n\t" \
  "v_accvgpr_read_b32 v27, a111\n\t" \
  "v_accvgpr_read_b32 v28, a124\n\t" \
  "v_accvgpr_read_b32 v29, a125\n\t" \
  "v_accvgpr_read_b32 v30, a126\n\t" \
  "v_accvgpr_read_b32 v31, a127\n\t" \
  "v_mul_f32 v24, v24, v214\n\t" \
  "v_mul_f32 v25, v25, v214\n\t" \
  "v_mul_f32 v26, v26, v214\n\t" \
  "v_mul_f32 v27, v27, v214\n\t" \
  "v_mul_f32 v28, v28, v214\n\t" \
  "v_mul_f32 v29, v29, v214\n\t" \
  "v_mul_f32 v30, v30, v214\n\t" \
  "v_mul_f32 v31, v31, v214\n\t" \
  "v_cvt_pk_bf16_f32 v172, v24, v25\n\t" \
  "v_cvt_pk_bf16_f32 v173, v26, v27\n\t" \
  "v_cvt_pk_bf16_f32 v174, v28, v29\n\t" \
  "v_cvt_pk_bf16_f32 v175, v30, v31\n\t" \
  "ds_write_b128 v213, v[172:175] offset:35840\n\t" \
  "v_mfma_f32_16x16x32_bf16 a[72:75], v[116:119], v[68:71], a[72:75]\n\t" \
  "s_add_u32 s98, %[gvo], 12288\n\t" \
  "buffer_load_dwordx4 v[132:135], v247, %[sM], s98 offen\n\t" \
  "v_mfma_f32_16x16x32_bf16 a[72:75], v[120:123], v[72:75], a[72:75]\n\t" \
  "s_add_u32 s98, %[gvo], 24576\n\t" \
  "buffer_load_dwordx4 v[136:139], v247, %[sM], s98 offen\n\t" \
  "v_mfma_f32_16x16x32_bf16 a[72:75], v[124:127], v[76:79], a[72:75]\n\t" \
  "s_add_u32 s98, %[gvo], 36864\n\t" \
  "buffer_load_dwordx4 v[140:143], v247, %[sM], s98 offen\n\t" \
  "s_xor_b32 s97, %[wob], 655360\n\t" \
  "buffer_load_dwordx4 v240, %[wo], s97 offen offset:0 lds\n\t" \
  "s_add_u32 s97, s97, 7168\n\t" \
  "buffer_load_dwordx4 v241, %[wo], s97 offen offset:1024 lds\n\t" \
  "s_add_u32 s97, s97, 7168\n\t" \
  "buffer_load_dwordx4 v240, %[wo], s97 offen offset:2048 lds\n\t" \
  "s_add_u32 s97, s97, 7168\n\t" \
  "buffer_load_dwordx4 v241, %[wo], s97 offen offset:3072 lds\n\t" \
  "s_add_u32 s98, %[gvo], 49152\n\t" \
  "buffer_load_dwordx4 v[144:147], v247, %[sM], s98 offen\n\t" \
  "s_add_u32 s98, %[gvo], 61440\n\t" \
  "buffer_load_dwordx4 v[148:151], v247, %[sM], s98 offen\n\t" \
  "s_add_u32 s98, %[gvo], 73728\n\t" \
  "buffer_load_dwordx4 v[152:155], v247, %[sM], s98 offen\n\t" \
  "s_add_u32 s98, %[gvo], 86016\n\t" \
  "buffer_load_dwordx4 v[156:159], v247, %[sM], s98 offen\n\t" \
  "s_waitcnt vmcnt(0)\n\t" \
  "s_branch .Lat_end\n\t" \
  ".Lsf_c00:\n\t" \
  "s_mov_b32 vcc_lo, 0\n\t" \
  "s_branch .Lsf_s0\n\t" \
  ".Lsf_c10:\n\t" \
  "s_mov_b32 vcc_lo, 1\n\t" \
  "s_branch .Lsf_s0\n\t" \
  ".Lsf_c20:\n\t" \
  "s_mov_b32 vcc_lo, 2\n\t" \
  "s_branch .Lsf_s0\n\t" \
  ".Lsf_c30:\n\t" \
  "s_mov_b32 vcc_lo, 3\n\t" \
  "s_branch .Lsf_s0\n\t" \
  ".Lsf_c40:\n\t" \
  "s_mov_b32 vcc_lo, 4\n\t" \
  "s_branch .Lsf_s0\n\t" \
  ".Lsf_c50:\n\t" \
  "s_mov_b32 vcc_lo, 5\n\t" \
  "s_branch .Lsf_s0\n\t" \
  ".Lsf_c60:\n\t" \
  "s_mov_b32 vcc_lo, 6\n\t" \
  "s_branch .Lsf_s0\n\t" \
  ".Lsf_c70:\n\t" \
  "s_mov_b32 vcc_lo, 7\n\t" \
  "s_branch .Lsf_s0\n\t" \
  ".Lsf_s0:\n\t" \
  "s_nop 7\n\t" \
  "s_nop 1\n\t" \
  "v_mov_b32 v193, v192\n\t" \
  "s_nop 1\n\t" \
  "v_permlane32_swap_b32 v192, v193\n\t" \
  "s_nop 1\n\t" \
  "v_max_f32 v192, v192, v193\n\t" \
  "s_nop 1\n\t" \
  "v_mov_b32 v193, v192\n\t" \
  "s_nop 1\n\t" \
  "v_permlane16_swap_b32 v192, v193\n\t" \
  "s_nop 1\n\t" \
  "v_max_f32 v192, v192, v193\n\t" \
  "v_max_f32 v192, 0, v192\n\t" \
  "v_sub_f32 v0, v0, v192\n\t" \
  "v_sub_f32 v1, v1, v192\n\t" \
  "v_sub_f32 v2, v2, v192\n\t" \
  "v_sub_f32 v3, v3, v192\n\t" \
  "v_sub_f32 v4, v4, v192\n\t" \
  "v_sub_f32 v5, v5, v192\n\t" \
  "v_sub_f32 v6, v6, v192\n\t" \
  "v_sub_f32 v7, v7, v192\n\t" \
  "v_sub_f32 v8, v8, v192\n\t" \
  "v_sub_f32 v9, v9, v192\n\t" \
  "v_sub_f32 v10, v10, v192\n\t" \
  "v_sub_f32 v11, v11, v192\n\t" \
  "v_sub_f32 v12, v12, v192\n\t" \
  "v_sub_f32 v13, v13, v192\n\t" \
  "v_sub_f32 v14, v14, v192\n\t" \
  "v_sub_f32 v15, v15, v192\n\t" \
  "v_sub_f32 v176, v176, v192\n\t" \
  "v_sub_f32 v177, v177, v192\n\t" \
  "v_sub_f32 v178, v178, v192\n\t" \
  "v_sub_f32 v179, v179, v192\n\t" \
  "v_exp_f32 v196, -v192\n\t" \
  "s_mov_b32 s99, 1\n\t" \
  "s_nop 1\n\t" \
  "s_cselect_b32 vcc_hi, 1, 0\n\t" \
  "s_cmp_eq_u32 vcc_lo, 0\n\t" \
  "s_cbranch_scc1 .Lsf_b00\n\t" \
  "s_cmp_eq_u32 vcc_lo, 1\n\t" \
  "s_cbranch_scc1 .Lsf_b10\n\t" \
  "s_cmp_eq_u32 vcc_lo, 2\n\t" \
  "s_cbranch_scc1 .Lsf_b20\n\t" \
  "s_cmp_eq_u32 vcc_lo, 3\n\t" \
  "s_cbranch_scc1 .Lsf_b30\n\t" \
  "s_cmp_eq_u32 vcc_lo, 4\n\t" \
  "s_cbranch_scc1 .Lsf_b40\n\t" \
  "s_cmp_eq_u32 vcc_lo, 5\n\t" \
  "s_cbranch_scc1 .Lsf_b50\n\t" \
  "s_cmp_eq_u32 vcc_lo, 6\n\t" \
  "s_cbranch_scc1 .Lsf_b60\n\t" \
  "s_cmp_eq_u32 vcc_lo, 7\n\t" \
  "s_cbranch_scc1 .Lsf_b70\n\t" \
  ".Lsf_b00:\n\t" \
  "s_cmp_lg_u32 vcc_hi, 0\n\t" \
  "s_branch .Lsf_r00\n\t" \
  ".Lsf_b10:\n\t" \
  "s_cmp_lg_u32 vcc_hi, 0\n\t" \
  "s_branch .Lsf_r10\n\t" \
  ".Lsf_b20:\n\t" \
  "s_cmp_lg_u32 vcc_hi, 0\n\t" \
  "s_branch .Lsf_r20\n\t" \
  ".Lsf_b30:\n\t" \
  "s_cmp_lg_u32 vcc_hi, 0\n\t" \
  "s_branch .Lsf_r30\n\t" \
  ".Lsf_b40:\n\t" \
  "s_cmp_lg_u32 vcc_hi, 0\n\t" \
  "s_branch .Lsf_r40\n\t" \
  ".Lsf_b50:\n\t" \
  "s_cmp_lg_u32 vcc_hi, 0\n\t" \
  "s_branch .Lsf_r50\n\t" \
  ".Lsf_b60:\n\t" \
  "s_cmp_lg_u32 vcc_hi, 0\n\t" \
  "s_branch .Lsf_r60\n\t" \
  ".Lsf_b70:\n\t" \
  "s_cmp_lg_u32 vcc_hi, 0\n\t" \
  "s_branch .Lsf_r70\n\t" \
  ".Lsf_c01:\n\t" \
  "s_mov_b32 vcc_lo, 0\n\t" \
  "s_branch .Lsf_s1\n\t" \
  ".Lsf_c11:\n\t" \
  "s_mov_b32 vcc_lo, 1\n\t" \
  "s_branch .Lsf_s1\n\t" \
  ".Lsf_c21:\n\t" \
  "s_mov_b32 vcc_lo, 2\n\t" \
  "s_branch .Lsf_s1\n\t" \
  ".Lsf_c31:\n\t" \
  "s_mov_b32 vcc_lo, 3\n\t" \
  "s_branch .Lsf_s1\n\t" \
  ".Lsf_c41:\n\t" \
  "s_mov_b32 vcc_lo, 4\n\t" \
  "s_branch .Lsf_s1\n\t" \
  ".Lsf_c51:\n\t" \
  "s_mov_b32 vcc_lo, 5\n\t" \
  "s_branch .Lsf_s1\n\t" \
  ".Lsf_c61:\n\t" \
  "s_mov_b32 vcc_lo, 6\n\t" \
  "s_branch .Lsf_s1\n\t" \
  ".Lsf_c71:\n\t" \
  "s_mov_b32 vcc_lo, 7\n\t" \
  "s_branch .Lsf_s1\n\t" \
  ".Lsf_s1:\n\t" \
  "s_nop 7\n\t" \
  "s_nop 1\n\t" \
  "v_mov_b32 v193, v192\n\t" \
  "s_nop 1\n\t" \
  "v_permlane32_swap_b32 v192, v193\n\t" \
  "s_nop 1\n\t" \
  "v_max_f32 v192, v192, v193\n\t" \
  "s_nop 1\n\t" \
  "v_mov_b32 v193, v192\n\t" \
  "s_nop 1\n\t" \
  "v_permlane16_swap_b32 v192, v193\n\t" \
  "s_nop 1\n\t" \
  "v_max_f32 v192, v192, v193\n\t" \
  "v_max_f32 v192, 0, v192\n\t" \
  "v_sub_f32 v32, v32, v192\n\t" \
  "v_sub_f32 v33, v33, v192\n\t" \
  "v_sub_f32 v34, v34, v192\n\t" \
  "v_sub_f32 v35, v35, v192\n\t" \
  "v_sub_f32 v36, v36, v192\n\t" \
  "v_sub_f32 v37, v37, v192\n\t" \
  "v_sub_f32 v38, v38, v192\n\t" \
  "v_sub_f32 v39, v39, v192\n\t" \
  "v_sub_f32 v40, v40, v192\n\t" \
  "v_sub_f32 v41, v41, v192\n\t" \
  "v_sub_f32 v42, v42, v192\n\t" \
  "v_sub_f32 v43, v43, v192\n\t" \
  "v_sub_f32 v44, v44, v192\n\t" \
  "v_sub_f32 v45, v45, v192\n\t" \
  "v_sub_f32 v46, v46, v192\n\t" \
  "v_sub_f32 v47, v47, v192\n\t" \
  "v_sub_f32 v180, v180, v192\n\t" \
  "v_sub_f32 v181, v181, v192\n\t" \
  "v_sub_f32 v182, v182, v192\n\t" \
  "v_sub_f32 v183, v183, v192\n\t" \
  "v_exp_f32 v197, -v192\n\t" \
  "s_mov_b32 s99, 1\n\t" \
  "s_nop 1\n\t" \
  "s_cselect_b32 vcc_hi, 1, 0\n\t" \
  "s_cmp_eq_u32 vcc_lo, 0\n\t" \
  "s_cbranch_scc1 .Lsf_b01\n\t" \
  "s_cmp_eq_u32 vcc_lo, 1\n\t" \
  "s_cbranch_scc1 .Lsf_b11\n\t" \
  "s_cmp_eq_u32 vcc_lo, 2\n\t" \
  "s_cbranch_scc1 .Lsf_b21\n\t" \
  "s_cmp_eq_u32 vcc_lo, 3\n\t" \
  "s_cbranch_scc1 .Lsf_b31\n\t" \
  "s_cmp_eq_u32 vcc_lo, 4\n\t" \
  "s_cbranch_scc1 .Lsf_b41\n\t" \
  "s_cmp_eq_u32 vcc_lo, 5\n\t" \
  "s_cbranch_scc1 .Lsf_b51\n\t" \
  "s_cmp_eq_u32 vcc_lo, 6\n\t" \
  "s_cbranch_scc1 .Lsf_b61\n\t" \
  "s_cmp_eq_u32 vcc_lo, 7\n\t" \
  "s_cbranch_scc1 .Lsf_b71\n\t" \
  ".Lsf_b01:\n\t" \
  "s_cmp_lg_u32 vcc_hi, 0\n\t" \
  "s_branch .Lsf_r01\n\t" \
  ".Lsf_b11:\n\t" \
  "s_cmp_lg_u32 vcc_hi, 0\n\t" \
  "s_branch .Lsf_r11\n\t" \
  ".Lsf_b21:\n\t" \
  "s_cmp_lg_u32 vcc_hi, 0\n\t" \
  "s_branch .Lsf_r21\n\t" \
  ".Lsf_b31:\n\t" \
  "s_cmp_lg_u32 vcc_hi, 0\n\t" \
  "s_branch .Lsf_r31\n\t" \
  ".Lsf_b41:\n\t" \
  "s_cmp_lg_u32 vcc_hi, 0\n\t" \
  "s_branch .Lsf_r41\n\t" \
  ".Lsf_b51:\n\t" \
  "s_cmp_lg_u32 vcc_hi, 0\n\t" \
  "s_branch .Lsf_r51\n\t" \
  ".Lsf_b61:\n\t" \
  "s_cmp_lg_u32 vcc_hi, 0\n\t" \
  "s_branch .Lsf_r61\n\t" \
  ".Lsf_b71:\n\t" \
  "s_cmp_lg_u32 vcc_hi, 0\n\t" \
  "s_branch .Lsf_r71\n\t" \
  ".Lsf_c02:\n\t" \
  "s_mov_b32 vcc_lo, 0\n\t" \
  "s_branch .Lsf_s2\n\t" \
  ".Lsf_c12:\n\t" \
  "s_mov_b32 vcc_lo, 1\n\t" \
  "s_branch .Lsf_s2\n\t" \
  ".Lsf_c22:\n\t" \
  "s_mov_b32 vcc_lo, 2\n\t" \
  "s_branch .Lsf_s2\n\t" \
  ".Lsf_c32:\n\t" \
  "s_mov_b32 vcc_lo, 3\n\t" \
  "s_branch .Lsf_s2\n\t" \
  ".Lsf_c42:\n\t" \
  "s_mov_b32 vcc_lo, 4\n\t" \
  "s_branch .Lsf_s2\n\t" \
  ".Lsf_c52:\n\t" \
  "s_mov_b32 vcc_lo, 5\n\t" \
  "s_branch .Lsf_s2\n\t" \
  ".Lsf_c62:\n\t" \
  "s_mov_b32 vcc_lo, 6\n\t" \
  "s_branch .Lsf_s2\n\t" \
  ".Lsf_c72:\n\t" \
  "s_mov_b32 vcc_lo, 7\n\t" \
  "s_branch .Lsf_s2\n\t" \
  ".Lsf_s2:\n\t" \
  "s_nop 7\n\t" \
  "s_nop 1\n\t" \
  "v_mov_b32 v193, v192\n\t" \
  "s_nop 1\n\t" \
  "v_permlane32_swap_b32 v192, v193\n\t" \
  "s_nop 1\n\t" \
  "v_max_f32 v192, v192, v193\n\t" \
  "s_nop 1\n\t" \
  "v_mov_b32 v193, v192\n\t" \
  "s_nop 1\n\t" \
  "v_permlane16_swap_b32 v192, v193\n\t" \
  "s_nop 1\n\t" \
  "v_max_f32 v192, v192, v193\n\t" \
  "v_max_f32 v192, 0, v192\n\t" \
  "v_sub_f32 v64, v64, v192\n\t" \
  "v_sub_f32 v65, v65, v192\n\t" \
  "v_sub_f32 v66, v66, v192\n\t" \
  "v_sub_f32 v67, v67, v192\n\t" \
  "v_sub_f32 v68, v68, v192\n\t" \
  "v_sub_f32 v69, v69, v192\n\t" \
  "v_sub_f32 v70, v70, v192\n\t" \
  "v_sub_f32 v71, v71, v192\n\t" \
  "v_sub_f32 v72, v72, v192\n\t" \
  "v_sub_f32 v73, v73, v192\n\t" \
  "v_sub_f32 v74, v74, v192\n\t" \
  "v_sub_f32 v75, v75, v192\n\t" \
  "v_sub_f32 v76, v76, v192\n\t" \
  "v_sub_f32 v77, v77, v192\n\t" \
  "v_sub_f32 v78, v78, v192\n\t" \
  "v_sub_f32 v79, v79, v192\n\t" \
  "v_sub_f32 v184, v184, v192\n\t" \
  "v_sub_f32 v185, v185, v192\n\t" \
  "v_sub_f32 v186, v186, v192\n\t" \
  "v_sub_f32 v187, v187, v192\n\t" \
  "v_exp_f32 v198, -v192\n\t" \
  "s_mov_b32 s99, 1\n\t" \
  "s_nop 1\n\t" \
  "s_cselect_b32 vcc_hi, 1, 0\n\t" \
  "s_cmp_eq_u32 vcc_lo, 0\n\t" \
  "s_cbranch_scc1 .Lsf_b02\n\t" \
  "s_cmp_eq_u32 vcc_lo, 1\n\t" \
  "s_cbranch_scc1 .Lsf_b12\n\t" \
  "s_cmp_eq_u32 vcc_lo, 2\n\t" \
  "s_cbranch_scc1 .Lsf_b22\n\t" \
  "s_cmp_eq_u32 vcc_lo, 3\n\t" \
  "s_cbranch_scc1 .Lsf_b32\n\t" \
  "s_cmp_eq_u32 vcc_lo, 4\n\t" \
  "s_cbranch_scc1 .Lsf_b42\n\t" \
  "s_cmp_eq_u32 vcc_lo, 5\n\t" \
  "s_cbranch_scc1 .Lsf_b52\n\t" \
  "s_cmp_eq_u32 vcc_lo, 6\n\t" \
  "s_cbranch_scc1 .Lsf_b62\n\t" \
  "s_cmp_eq_u32 vcc_lo, 7\n\t" \
  "s_cbranch_scc1 .Lsf_b72\n\t" \
  ".Lsf_b02:\n\t" \
  "s_cmp_lg_u32 vcc_hi, 0\n\t" \
  "s_branch .Lsf_r02\n\t" \
  ".Lsf_b12:\n\t" \
  "s_cmp_lg_u32 vcc_hi, 0\n\t" \
  "s_branch .Lsf_r12\n\t" \
  ".Lsf_b22:\n\t" \
  "s_cmp_lg_u32 vcc_hi, 0\n\t" \
  "s_branch .Lsf_r22\n\t" \
  ".Lsf_b32:\n\t" \
  "s_cmp_lg_u32 vcc_hi, 0\n\t" \
  "s_branch .Lsf_r32\n\t" \
  ".Lsf_b42:\n\t" \
  "s_cmp_lg_u32 vcc_hi, 0\n\t" \
  "s_branch .Lsf_r42\n\t" \
  ".Lsf_b52:\n\t" \
  "s_cmp_lg_u32 vcc_hi, 0\n\t" \
  "s_branch .Lsf_r52\n\t" \
  ".Lsf_b62:\n\t" \
  "s_cmp_lg_u32 vcc_hi, 0\n\t" \
  "s_branch .Lsf_r62\n\t" \
  ".Lsf_b72:\n\t" \
  "s_cmp_lg_u32 vcc_hi, 0\n\t" \
  "s_branch .Lsf_r72\n\t" \
  ".Lsf_c03:\n\t" \
  "s_mov_b32 vcc_lo, 0\n\t" \
  "s_branch .Lsf_s3\n\t" \
  ".Lsf_c13:\n\t" \
  "s_mov_b32 vcc_lo, 1\n\t" \
  "s_branch .Lsf_s3\n\t" \
  ".Lsf_c23:\n\t" \
  "s_mov_b32 vcc_lo, 2\n\t" \
  "s_branch .Lsf_s3\n\t" \
  ".Lsf_c33:\n\t" \
  "s_mov_b32 vcc_lo, 3\n\t" \
  "s_branch .Lsf_s3\n\t" \
  ".Lsf_c43:\n\t" \
  "s_mov_b32 vcc_lo, 4\n\t" \
  "s_branch .Lsf_s3\n\t" \
  ".Lsf_c53:\n\t" \
  "s_mov_b32 vcc_lo, 5\n\t" \
  "s_branch .Lsf_s3\n\t" \
  ".Lsf_c63:\n\t" \
  "s_mov_b32 vcc_lo, 6\n\t" \
  "s_branch .Lsf_s3\n\t" \
  ".Lsf_c73:\n\t" \
  "s_mov_b32 vcc_lo, 7\n\t" \
  "s_branch .Lsf_s3\n\t" \
  ".Lsf_s3:\n\t" \
  "s_nop 7\n\t" \
  "s_nop 1\n\t" \
  "v_mov_b32 v193, v192\n\t" \
  "s_nop 1\n\t" \
  "v_permlane32_swap_b32 v192, v193\n\t" \
  "s_nop 1\n\t" \
  "v_max_f32 v192, v192, v193\n\t" \
  "s_nop 1\n\t" \
  "v_mov_b32 v193, v192\n\t" \
  "s_nop 1\n\t" \
  "v_permlane16_swap_b32 v192, v193\n\t" \
  "s_nop 1\n\t" \
  "v_max_f32 v192, v192, v193\n\t" \
  "v_max_f32 v192, 0, v192\n\t" \
  "v_sub_f32 v96, v96, v192\n\t" \
  "v_sub_f32 v97, v97, v192\n\t" \
  "v_sub_f32 v98, v98, v192\n\t" \
  "v_sub_f32 v99, v99, v192\n\t" \
  "v_sub_f32 v100, v100, v192\n\t" \
  "v_sub_f32 v101, v101, v192\n\t" \
  "v_sub_f32 v102, v102, v192\n\t" \
  "v_sub_f32 v103, v103, v192\n\t" \
  "v_sub_f32 v104, v104, v192\n\t" \
  "v_sub_f32 v105, v105, v192\n\t" \
  "v_sub_f32 v106, v106, v192\n\t" \
  "v_sub_f32 v107, v107, v192\n\t" \
  "v_sub_f32 v108, v108, v192\n\t" \
  "v_sub_f32 v109, v109, v192\n\t" \
  "v_sub_f32 v110, v110, v192\n\t" \
  "v_sub_f32 v111, v111, v192\n\t" \
  "v_sub_f32 v188, v188, v192\n\t" \
  "v_sub_f32 v189, v189, v192\n\t" \
  "v_sub_f32 v190, v190, v192\n\t" \
  "v_sub_f32 v191, v191, v192\n\t" \
  "v_exp_f32 v199, -v192\n\t" \
  "s_mov_b32 s99, 1\n\t" \
  "s_nop 1\n\t" \
  "s_cselect_b32 vcc_hi, 1, 0\n\t" \
  "s_cmp_eq_u32 vcc_lo, 0\n\t" \
  "s_cbranch_scc1 .Lsf_b03\n\t" \
  "s_cmp_eq_u32 vcc_lo, 1\n\t" \
  "s_cbranch_scc1 .Lsf_b13\n\t" \
  "s_cmp_eq_u32 vcc_lo, 2\n\t" \
  "s_cbranch_scc1 .Lsf_b23\n\t" \
  "s_cmp_eq_u32 vcc_lo, 3\n\t" \
  "s_cbranch_scc1 .Lsf_b33\n\t" \
  "s_cmp_eq_u32 vcc_lo, 4\n\t" \
  "s_cbranch_scc1 .Lsf_b43\n\t" \
  "s_cmp_eq_u32 vcc_lo, 5\n\t" \
  "s_cbranch_scc1 .Lsf_b53\n\t" \
  "s_cmp_eq_u32 vcc_lo, 6\n\t" \
  "s_cbranch_scc1 .Lsf_b63\n\t" \
  "s_cmp_eq_u32 vcc_lo, 7\n\t" \
  "s_cbranch_scc1 .Lsf_b73\n\t" \
  ".Lsf_b03:\n\t" \
  "s_cmp_lg_u32 vcc_hi, 0\n\t" \
  "s_branch .Lsf_r03\n\t" \
  ".Lsf_b13:\n\t" \
  "s_cmp_lg_u32 vcc_hi, 0\n\t" \
  "s_branch .Lsf_r13\n\t" \
  ".Lsf_b23:\n\t" \
  "s_cmp_lg_u32 vcc_hi, 0\n\t" \
  "s_branch .Lsf_r23\n\t" \
  ".Lsf_b33:\n\t" \
  "s_cmp_lg_u32 vcc_hi, 0\n\t" \
  "s_branch .Lsf_r33\n\t" \
  ".Lsf_b43:\n\t" \
  "s_cmp_lg_u32 vcc_hi, 0\n\t" \
  "s_branch .Lsf_r43\n\t" \
  ".Lsf_b53:\n\t" \
  "s_cmp_lg_u32 vcc_hi, 0\n\t" \
  "s_branch .Lsf_r53\n\t" \
  ".Lsf_b63:\n\t" \
  "s_cmp_lg_u32 vcc_hi, 0\n\t" \
  "s_branch .Lsf_r63\n\t" \
  ".Lsf_b73:\n\t" \
  "s_cmp_lg_u32 vcc_hi, 0\n\t" \
  "s_branch .Lsf_r73\n\t" \
  ".Lsf_p0:\n\t" \
  "s_mov_b32 vcc_lo, 0\n\t" \
  "s_branch .Lsf_rs\n\t" \
  ".Lsf_p1:\n\t" \
  "s_mov_b32 vcc_lo, 1\n\t" \
  "s_branch .Lsf_rs\n\t" \
  ".Lsf_p2:\n\t" \
  "s_mov_b32 vcc_lo, 2\n\t" \
  "s_branch .Lsf_rs\n\t" \
  ".Lsf_p3:\n\t" \
  "s_mov_b32 vcc_lo, 3\n\t" \
  "s_branch .Lsf_rs\n\t" \
  ".Lsf_p4:\n\t" \
  "s_mov_b32 vcc_lo, 4\n\t" \
  "s_branch .Lsf_rs\n\t" \
  ".Lsf_p5:\n\t" \
  "s_mov_b32 vcc_lo, 5\n\t" \
  "s_branch .Lsf_rs\n\t" \
  ".Lsf_p6:\n\t" \
  "s_mov_b32 vcc_lo, 6\n\t" \
  "s_branch .Lsf_rs\n\t" \
  ".Lsf_p7:\n\t" \
  "s_mov_b32 vcc_lo, 7\n\t" \
  "s_branch .Lsf_rs\n\t" \
  ".Lsf_rs:\n\t" \
  "s_nop 7\n\t" \
  "s_nop 7\n\t" \
  "v_accvgpr_read_b32 v193, a0\n\t" \
  "v_mul_f32 v193, v196, v193\n\t" \
  "v_accvgpr_write_b32 a0, v193\n\t" \
  "v_accvgpr_read_b32 v193, a1\n\t" \
  "v_mul_f32 v193, v196, v193\n\t" \
  "v_accvgpr_write_b32 a1, v193\n\t" \
  "v_accvgpr_read_b32 v193, a2\n\t" \
  "v_mul_f32 v193, v196, v193\n\t" \
  "v_accvgpr_write_b32 a2, v193\n\t" \
  "v_accvgpr_read_b32 v193, a3\n\t" \
  "v_mul_f32 v193, v196, v193\n\t" \
  "v_accvgpr_write_b32 a3, v193\n\t" \
  "v_accvgpr_read_b32 v193, a4\n\t" \
  "v_mul_f32 v193, v197, v193\n\t" \
  "v_accvgpr_write_b32 a4, v193\n\t" \
  "v_accvgpr_read_b32 v193, a5\n\t" \
  "v_mul_f32 v193, v197, v193\n\t" \
  "v_accvgpr_write_b32 a5, v193\n\t" \
  "v_accvgpr_read_b32 v193, a6\n\t" \
  "v_mul_f32 v193, v197, v193\n\t" \
  "v_accvgpr_write_b32 a6, v193\n\t" \
  "v_accvgpr_read_b32 v193, a7\n\t" \
  "v_mul_f32 v193, v197, v193\n\t" \
  "v_accvgpr_write_b32 a7, v193\n\t" \
  "v_accvgpr_read_b32 v193, a8\n\t" \
  "v_mul_f32 v193, v198, v193\n\t" \
  "v_accvgpr_write_b32 a8, v193\n\t" \
  "v_accvgpr_read_b32 v193, a9\n\t" \
  "v_mul_f32 v193, v198, v193\n\t" \
  "v_accvgpr_write_b32 a9, v193\n\t" \
  "v_accvgpr_read_b32 v193, a10\n\t" \
  "v_mul_f32 v193, v198, v193\n\t" \
  "v_accvgpr_write_b32 a10, v193\n\t" \
  "v_accvgpr_read_b32 v193, a11\n\t" \
  "v_mul_f32 v193, v198, v193\n\t" \
  "v_accvgpr_write_b32 a11, v193\n\t" \
  "v_accvgpr_read_b32 v193, a12\n\t" \
  "v_mul_f32 v193, v199, v193\n\t" \
  "v_accvgpr_write_b32 a12, v193\n\t" \
  "v_accvgpr_read_b32 v193, a13\n\t" \
  "v_mul_f32 v193, v199, v193\n\t" \
  "v_accvgpr_write_b32 a13, v193\n\t" \
  "v_accvgpr_read_b32 v193, a14\n\t" \
  "v_mul_f32 v193, v199, v193\n\t" \
  "v_accvgpr_write_b32 a14, v193\n\t" \
  "v_accvgpr_read_b32 v193, a15\n\t" \
  "v_mul_f32 v193, v199, v193\n\t" \
  "v_accvgpr_write_b32 a15, v193\n\t" \
  "v_accvgpr_read_b32 v193, a16\n\t" \
  "v_mul_f32 v193, v196, v193\n\t" \
  "v_accvgpr_write_b32 a16, v193\n\t" \
  "v_accvgpr_read_b32 v193, a17\n\t" \
  "v_mul_f32 v193, v196, v193\n\t" \
  "v_accvgpr_write_b32 a17, v193\n\t" \
  "v_accvgpr_read_b32 v193, a18\n\t" \
  "v_mul_f32 v193, v196, v193\n\t" \
  "v_accvgpr_write_b32 a18, v193\n\t" \
  "v_accvgpr_read_b32 v193, a19\n\t" \
  "v_mul_f32 v193, v196, v193\n\t" \
  "v_accvgpr_write_b32 a19, v193\n\t" \
  "v_accvgpr_read_b32 v193, a20\n\t" \
  "v_mul_f32 v193, v197, v193\n\t" \
  "v_accvgpr_write_b32 a20, v193\n\t" \
  "v_accvgpr_read_b32 v193, a21\n\t" \
  "v_mul_f32 v193, v197, v193\n\t" \
  "v_accvgpr_write_b32 a21, v193\n\t" \
  "v_accvgpr_read_b32 v193, a22\n\t" \
  "v_mul_f32 v193, v197, v193\n\t" \
  "v_accvgpr_write_b32 a22, v193\n\t" \
  "v_accvgpr_read_b32 v193, a23\n\t" \
  "v_mul_f32 v193, v197, v193\n\t" \
  "v_accvgpr_write_b32 a23, v193\n\t" \
  "v_accvgpr_read_b32 v193, a24\n\t" \
  "v_mul_f32 v193, v198, v193\n\t" \
  "v_accvgpr_write_b32 a24, v193\n\t" \
  "v_accvgpr_read_b32 v193, a25\n\t" \
  "v_mul_f32 v193, v198, v193\n\t" \
  "v_accvgpr_write_b32 a25, v193\n\t" \
  "v_accvgpr_read_b32 v193, a26\n\t" \
  "v_mul_f32 v193, v198, v193\n\t" \
  "v_accvgpr_write_b32 a26, v193\n\t" \
  "v_accvgpr_read_b32 v193, a27\n\t" \
  "v_mul_f32 v193, v198, v193\n\t" \
  "v_accvgpr_write_b32 a27, v193\n\t" \
  "v_accvgpr_read_b32 v193, a28\n\t" \
  "v_mul_f32 v193, v199, v193\n\t" \
  "v_accvgpr_write_b32 a28, v193\n\t" \
  "v_accvgpr_read_b32 v193, a29\n\t" \
  "v_mul_f32 v193, v199, v193\n\t" \
  "v_accvgpr_write_b32 a29, v193\n\t" \
  "v_accvgpr_read_b32 v193, a30\n\t" \
  "v_mul_f32 v193, v199, v193\n\t" \
  "v_accvgpr_write_b32 a30, v193\n\t" \
  "v_accvgpr_read_b32 v193, a31\n\t" \
  "v_mul_f32 v193, v199, v193\n\t" \
  "v_accvgpr_write_b32 a31, v193\n\t" \
  "v_accvgpr_read_b32 v193, a32\n\t" \
  "v_mul_f32 v193, v196, v193\n\t" \
  "v_accvgpr_write_b32 a32, v193\n\t" \
  "v_accvgpr_read_b32 v193, a33\n\t" \
  "v_mul_f32 v193, v196, v193\n\t" \
  "v_accvgpr_write_b32 a33, v193\n\t" \
  "v_accvgpr_read_b32 v193, a34\n\t" \
  "v_mul_f32 v193, v196, v193\n\t" \
  "v_accvgpr_write_b32 a34, v193\n\t" \
  "v_accvgpr_read_b32 v193, a35\n\t" \
  "v_mul_f32 v193, v196, v193\n\t" \
  "v_accvgpr_write_b32 a35, v193\n\t" \
  "v_accvgpr_read_b32 v193, a36\n\t" \
  "v_mul_f32 v193, v197, v193\n\t" \
  "v_accvgpr_write_b32 a36, v193\n\t" \
  "v_accvgpr_read_b32 v193, a37\n\t" \
  "v_mul_f32 v193, v197, v193\n\t" \
  "v_accvgpr_write_b32 a37, v193\n\t" \
  "v_accvgpr_read_b32 v193, a38\n\t" \
  "v_mul_f32 v193, v197, v193\n\t" \
  "v_accvgpr_write_b32 a38, v193\n\t" \
  "v_accvgpr_read_b32 v193, a39\n\t" \
  "v_mul_f32 v193, v197, v193\n\t" \
  "v_accvgpr_write_b32 a39, v193\n\t" \
  "v_accvgpr_read_b32 v193, a40\n\t" \
  "v_mul_f32 v193, v198, v193\n\t" \
  "v_accvgpr_write_b32 a40, v193\n\t" \
  "v_accvgpr_read_b32 v193, a41\n\t" \
  "v_mul_f32 v193, v198, v193\n\t" \
  "v_accvgpr_write_b32 a41, v193\n\t" \
  "v_accvgpr_read_b32 v193, a42\n\t" \
  "v_mul_f32 v193, v198, v193\n\t" \
  "v_accvgpr_write_b32 a42, v193\n\t" \
  "v_accvgpr_read_b32 v193, a43\n\t" \
  "v_mul_f32 v193, v198, v193\n\t" \
  "v_accvgpr_write_b32 a43, v193\n\t" \
  "v_accvgpr_read_b32 v193, a44\n\t" \
  "v_mul_f32 v193, v199, v193\n\t" \
  "v_accvgpr_write_b32 a44, v193\n\t" \
  "v_accvgpr_read_b32 v193, a45\n\t" \
  "v_mul_f32 v193, v199, v193\n\t" \
  "v_accvgpr_write_b32 a45, v193\n\t" \
  "v_accvgpr_read_b32 v193, a46\n\t" \
  "v_mul_f32 v193, v199, v193\n\t" \
  "v_accvgpr_write_b32 a46, v193\n\t" \
  "v_accvgpr_read_b32 v193, a47\n\t" \
  "v_mul_f32 v193, v199, v193\n\t" \
  "v_accvgpr_write_b32 a47, v193\n\t" \
  "v_accvgpr_read_b32 v193, a48\n\t" \
  "v_mul_f32 v193, v196, v193\n\t" \
  "v_accvgpr_write_b32 a48, v193\n\t" \
  "v_accvgpr_read_b32 v193, a49\n\t" \
  "v_mul_f32 v193, v196, v193\n\t" \
  "v_accvgpr_write_b32 a49, v193\n\t" \
  "v_accvgpr_read_b32 v193, a50\n\t" \
  "v_mul_f32 v193, v196, v193\n\t" \
  "v_accvgpr_write_b32 a50, v193\n\t" \
  "v_accvgpr_read_b32 v193, a51\n\t" \
  "v_mul_f32 v193, v196, v193\n\t" \
  "v_accvgpr_write_b32 a51, v193\n\t" \
  "v_accvgpr_read_b32 v193, a52\n\t" \
  "v_mul_f32 v193, v197, v193\n\t" \
  "v_accvgpr_write_b32 a52, v193\n\t" \
  "v_accvgpr_read_b32 v193, a53\n\t" \
  "v_mul_f32 v193, v197, v193\n\t" \
  "v_accvgpr_write_b32 a53, v193\n\t" \
  "v_accvgpr_read_b32 v193, a54\n\t" \
  "v_mul_f32 v193, v197, v193\n\t" \
  "v_accvgpr_write_b32 a54, v193\n\t" \
  "v_accvgpr_read_b32 v193, a55\n\t" \
  "v_mul_f32 v193, v197, v193\n\t" \
  "v_accvgpr_write_b32 a55, v193\n\t" \
  "v_accvgpr_read_b32 v193, a56\n\t" \
  "v_mul_f32 v193, v198, v193\n\t" \
  "v_accvgpr_write_b32 a56, v193\n\t" \
  "v_accvgpr_read_b32 v193, a57\n\t" \
  "v_mul_f32 v193, v198, v193\n\t" \
  "v_accvgpr_write_b32 a57, v193\n\t" \
  "v_accvgpr_read_b32 v193, a58\n\t" \
  "v_mul_f32 v193, v198, v193\n\t" \
  "v_accvgpr_write_b32 a58, v193\n\t" \
  "v_accvgpr_read_b32 v193, a59\n\t" \
  "v_mul_f32 v193, v198, v193\n\t" \
  "v_accvgpr_write_b32 a59, v193\n\t" \
  "v_accvgpr_read_b32 v193, a60\n\t" \
  "v_mul_f32 v193, v199, v193\n\t" \
  "v_accvgpr_write_b32 a60, v193\n\t" \
  "v_accvgpr_read_b32 v193, a61\n\t" \
  "v_mul_f32 v193, v199, v193\n\t" \
  "v_accvgpr_write_b32 a61, v193\n\t" \
  "v_accvgpr_read_b32 v193, a62\n\t" \
  "v_mul_f32 v193, v199, v193\n\t" \
  "v_accvgpr_write_b32 a62, v193\n\t" \
  "v_accvgpr_read_b32 v193, a63\n\t" \
  "v_mul_f32 v193, v199, v193\n\t" \
  "v_accvgpr_write_b32 a63, v193\n\t" \
  "v_accvgpr_read_b32 v193, a64\n\t" \
  "v_mul_f32 v193, v196, v193\n\t" \
  "v_accvgpr_write_b32 a64, v193\n\t" \
  "v_accvgpr_read_b32 v193, a65\n\t" \
  "v_mul_f32 v193, v196, v193\n\t" \
  "v_accvgpr_write_b32 a65, v193\n\t" \
  "v_accvgpr_read_b32 v193, a66\n\t" \
  "v_mul_f32 v193, v196, v193\n\t" \
  "v_accvgpr_write_b32 a66, v193\n\t" \
  "v_accvgpr_read_b32 v193, a67\n\t" \
  "v_mul_f32 v193, v196, v193\n\t" \
  "v_accvgpr_write_b32 a67, v193\n\t" \
  "v_accvgpr_read_b32 v193, a68\n\t" \
  "v_mul_f32 v193, v197, v193\n\t" \
  "v_accvgpr_write_b32 a68, v193\n\t" \
  "v_accvgpr_read_b32 v193, a69\n\t" \
  "v_mul_f32 v193, v197, v193\n\t" \
  "v_accvgpr_write_b32 a69, v193\n\t" \
  "v_accvgpr_read_b32 v193, a70\n\t" \
  "v_mul_f32 v193, v197, v193\n\t" \
  "v_accvgpr_write_b32 a70, v193\n\t" \
  "v_accvgpr_read_b32 v193, a71\n\t" \
  "v_mul_f32 v193, v197, v193\n\t" \
  "v_accvgpr_write_b32 a71, v193\n\t" \
  "v_accvgpr_read_b32 v193, a72\n\t" \
  "v_mul_f32 v193, v198, v193\n\t" \
  "v_accvgpr_write_b32 a72, v193\n\t" \
  "v_accvgpr_read_b32 v193, a73\n\t" \
  "v_mul_f32 v193, v198, v193\n\t" \
  "v_accvgpr_write_b32 a73, v193\n\t" \
  "v_accvgpr_read_b32 v193, a74\n\t" \
  "v_mul_f32 v193, v198, v193\n\t" \
  "v_accvgpr_write_b32 a74, v193\n\t" \
  "v_accvgpr_read_b32 v193, a75\n\t" \
  "v_mul_f32 v193, v198, v193\n\t" \
  "v_accvgpr_write_b32 a75, v193\n\t" \
  "v_accvgpr_read_b32 v193, a76\n\t" \
  "v_mul_f32 v193, v199, v193\n\t" \
  "v_accvgpr_write_b32 a76, v193\n\t" \
  "v_accvgpr_read_b32 v193, a77\n\t" \
  "v_mul_f32 v193, v199, v193\n\t" \
  "v_accvgpr_write_b32 a77, v193\n\t" \
  "v_accvgpr_read_b32 v193, a78\n\t" \
  "v_mul_f32 v193, v199, v193\n\t" \
  "v_accvgpr_write_b32 a78, v193\n\t" \
  "v_accvgpr_read_b32 v193, a79\n\t" \
  "v_mul_f32 v193, v199, v193\n\t" \
  "v_accvgpr_write_b32 a79, v193\n\t" \
  "v_accvgpr_read_b32 v193, a80\n\t" \
  "v_mul_f32 v193, v196, v193\n\t" \
  "v_accvgpr_write_b32 a80, v193\n\t" \
  "v_accvgpr_read_b32 v193, a81\n\t" \
  "v_mul_f32 v193, v196, v193\n\t" \
  "v_accvgpr_write_b32 a81, v193\n\t" \
  "v_accvgpr_read_b32 v193, a82\n\t" \
  "v_mul_f32 v193, v196, v193\n\t" \
  "v_accvgpr_write_b32 a82, v193\n\t" \
  "v_accvgpr_read_b32 v193, a83\n\t" \
  "v_mul_f32 v193, v196, v193\n\t" \
  "v_accvgpr_write_b32 a83, v193\n\t" \
  "v_accvgpr_read_b32 v193, a84\n\t" \
  "v_mul_f32 v193, v197, v193\n\t" \
  "v_accvgpr_write_b32 a84, v193\n\t" \
  "v_accvgpr_read_b32 v193, a85\n\t" \
  "v_mul_f32 v193, v197, v193\n\t" \
  "v_accvgpr_write_b32 a85, v193\n\t" \
  "v_accvgpr_read_b32 v193, a86\n\t" \
  "v_mul_f32 v193, v197, v193\n\t" \
  "v_accvgpr_write_b32 a86, v193\n\t" \
  "v_accvgpr_read_b32 v193, a87\n\t" \
  "v_mul_f32 v193, v197, v193\n\t" \
  "v_accvgpr_write_b32 a87, v193\n\t" \
  "v_accvgpr_read_b32 v193, a88\n\t" \
  "v_mul_f32 v193, v198, v193\n\t" \
  "v_accvgpr_write_b32 a88, v193\n\t" \
  "v_accvgpr_read_b32 v193, a89\n\t" \
  "v_mul_f32 v193, v198, v193\n\t" \
  "v_accvgpr_write_b32 a89, v193\n\t" \
  "v_accvgpr_read_b32 v193, a90\n\t" \
  "v_mul_f32 v193, v198, v193\n\t" \
  "v_accvgpr_write_b32 a90, v193\n\t" \
  "v_accvgpr_read_b32 v193, a91\n\t" \
  "v_mul_f32 v193, v198, v193\n\t" \
  "v_accvgpr_write_b32 a91, v193\n\t" \
  "v_accvgpr_read_b32 v193, a92\n\t" \
  "v_mul_f32 v193, v199, v193\n\t" \
  "v_accvgpr_write_b32 a92, v193\n\t" \
  "v_accvgpr_read_b32 v193, a93\n\t" \
  "v_mul_f32 v193, v199, v193\n\t" \
  "v_accvgpr_write_b32 a93, v193\n\t" \
  "v_accvgpr_read_b32 v193, a94\n\t" \
  "v_mul_f32 v193, v199, v193\n\t" \
  "v_accvgpr_write_b32 a94, v193\n\t" \
  "v_accvgpr_read_b32 v193, a95\n\t" \
  "v_mul_f32 v193, v199, v193\n\t" \
  "v_accvgpr_write_b32 a95, v193\n\t" \
  "v_accvgpr_read_b32 v193, a96\n\t" \
  "v_mul_f32 v193, v196, v193\n\t" \
  "v_accvgpr_write_b32 a96, v193\n\t" \
  "v_accvgpr_read_b32 v193, a97\n\t" \
  "v_mul_f32 v193, v196, v193\n\t" \
  "v_accvgpr_write_b32 a97, v193\n\t" \
  "v_accvgpr_read_b32 v193, a98\n\t" \
  "v_mul_f32 v193, v196, v193\n\t" \
  "v_accvgpr_write_b32 a98, v193\n\t" \
  "v_accvgpr_read_b32 v193, a99\n\t" \
  "v_mul_f32 v193, v196, v193\n\t" \
  "v_accvgpr_write_b32 a99, v193\n\t" \
  "v_accvgpr_read_b32 v193, a100\n\t" \
  "v_mul_f32 v193, v197, v193\n\t" \
  "v_accvgpr_write_b32 a100, v193\n\t" \
  "v_accvgpr_read_b32 v193, a101\n\t" \
  "v_mul_f32 v193, v197, v193\n\t" \
  "v_accvgpr_write_b32 a101, v193\n\t" \
  "v_accvgpr_read_b32 v193, a102\n\t" \
  "v_mul_f32 v193, v197, v193\n\t" \
  "v_accvgpr_write_b32 a102, v193\n\t" \
  "v_accvgpr_read_b32 v193, a103\n\t" \
  "v_mul_f32 v193, v197, v193\n\t" \
  "v_accvgpr_write_b32 a103, v193\n\t" \
  "v_accvgpr_read_b32 v193, a104\n\t" \
  "v_mul_f32 v193, v198, v193\n\t" \
  "v_accvgpr_write_b32 a104, v193\n\t" \
  "v_accvgpr_read_b32 v193, a105\n\t" \
  "v_mul_f32 v193, v198, v193\n\t" \
  "v_accvgpr_write_b32 a105, v193\n\t" \
  "v_accvgpr_read_b32 v193, a106\n\t" \
  "v_mul_f32 v193, v198, v193\n\t" \
  "v_accvgpr_write_b32 a106, v193\n\t" \
  "v_accvgpr_read_b32 v193, a107\n\t" \
  "v_mul_f32 v193, v198, v193\n\t" \
  "v_accvgpr_write_b32 a107, v193\n\t" \
  "v_accvgpr_read_b32 v193, a108\n\t" \
  "v_mul_f32 v193, v199, v193\n\t" \
  "v_accvgpr_write_b32 a108, v193\n\t" \
  "v_accvgpr_read_b32 v193, a109\n\t" \
  "v_mul_f32 v193, v199, v193\n\t" \
  "v_accvgpr_write_b32 a109, v193\n\t" \
  "v_accvgpr_read_b32 v193, a110\n\t" \
  "v_mul_f32 v193, v199, v193\n\t" \
  "v_accvgpr_write_b32 a110, v193\n\t" \
  "v_accvgpr_read_b32 v193, a111\n\t" \
  "v_mul_f32 v193, v199, v193\n\t" \
  "v_accvgpr_write_b32 a111, v193\n\t" \
  "v_accvgpr_read_b32 v193, a112\n\t" \
  "v_mul_f32 v193, v196, v193\n\t" \
  "v_accvgpr_write_b32 a112, v193\n\t" \
  "v_accvgpr_read_b32 v193, a113\n\t" \
  "v_mul_f32 v193, v196, v193\n\t" \
  "v_accvgpr_write_b32 a113, v193\n\t" \
  "v_accvgpr_read_b32 v193, a114\n\t" \
  "v_mul_f32 v193, v196, v193\n\t" \
  "v_accvgpr_write_b32 a114, v193\n\t" \
  "v_accvgpr_read_b32 v193, a115\n\t" \
  "v_mul_f32 v193, v196, v193\n\t" \
  "v_accvgpr_write_b32 a115, v193\n\t" \
  "v_accvgpr_read_b32 v193, a116\n\t" \
  "v_mul_f32 v193, v197, v193\n\t" \
  "v_accvgpr_write_b32 a116, v193\n\t" \
  "v_accvgpr_read_b32 v193, a117\n\t" \
  "v_mul_f32 v193, v197, v193\n\t" \
  "v_accvgpr_write_b32 a117, v193\n\t" \
  "v_accvgpr_read_b32 v193, a118\n\t" \
  "v_mul_f32 v193, v197, v193\n\t" \
  "v_accvgpr_write_b32 a118, v193\n\t" \
  "v_accvgpr_read_b32 v193, a119\n\t" \
  "v_mul_f32 v193, v197, v193\n\t" \
  "v_accvgpr_write_b32 a119, v193\n\t" \
  "v_accvgpr_read_b32 v193, a120\n\t" \
  "v_mul_f32 v193, v198, v193\n\t" \
  "v_accvgpr_write_b32 a120, v193\n\t" \
  "v_accvgpr_read_b32 v193, a121\n\t" \
  "v_mul_f32 v193, v198, v193\n\t" \
  "v_accvgpr_write_b32 a121, v193\n\t" \
  "v_accvgpr_read_b32 v193, a122\n\t" \
  "v_mul_f32 v193, v198, v193\n\t" \
  "v_accvgpr_write_b32 a122, v193\n\t" \
  "v_accvgpr_read_b32 v193, a123\n\t" \
  "v_mul_f32 v193, v198, v193\n\t" \
  "v_accvgpr_write_b32 a123, v193\n\t" \
  "v_accvgpr_read_b32 v193, a124\n\t" \
  "v_mul_f32 v193, v199, v193\n\t" \
  "v_accvgpr_write_b32 a124, v193\n\t" \
  "v_accvgpr_read_b32 v193, a125\n\t" \
  "v_mul_f32 v193, v199, v193\n\t" \
  "v_accvgpr_write_b32 a125, v193\n\t" \
  "v_accvgpr_read_b32 v193, a126\n\t" \
  "v_mul_f32 v193, v199, v193\n\t" \
  "v_accvgpr_write_b32 a126, v193\n\t" \
  "v_accvgpr_read_b32 v193, a127\n\t" \
  "v_mul_f32 v193, v199, v193\n\t" \
  "v_accvgpr_write_b32 a127, v193\n\t" \
  "v_mul_f32 v160, v196, v160\n\t" \
  "v_mul_f32 v161, v196, v161\n\t" \
  "v_mul_f32 v162, v196, v162\n\t" \
  "v_mul_f32 v163, v196, v163\n\t" \
  "v_mov_b32 v196, 1.0\n\t" \
  "v_mul_f32 v164, v197, v164\n\t" \
  "v_mul_f32 v165, v197, v165\n\t" \
  "v_mul_f32 v166, v197, v166\n\t" \
  "v_mul_f32 v167, v197, v167\n\t" \
  "v_mov_b32 v197, 1.0\n\t" \
  "v_mul_f32 v168, v198, v168\n\t" \
  "v_mul_f32 v169, v198, v169\n\t" \
  "v_mul_f32 v170, v198, v170\n\t" \
  "v_mul_f32 v171, v198, v171\n\t" \
  "v_mov_b32 v198, 1.0\n\t" \
  "v_mul_f32 v172, v199, v172\n\t" \
  "v_mul_f32 v173, v199, v173\n\t" \
  "v_mul_f32 v174, v199, v174\n\t" \
  "v_mul_f32 v175, v199, v175\n\t" \
  "v_mov_b32 v199, 1.0\n\t" \
  "s_mov_b32 s99, 0\n\t" \
  "s_nop 1\n\t" \
  "s_cmp_eq_u32 vcc_lo, 0\n\t" \
  "s_cbranch_scc1 .Lsf_q0\n\t" \
  "s_cmp_eq_u32 vcc_lo, 1\n\t" \
  "s_cbranch_scc1 .Lsf_q1\n\t" \
  "s_cmp_eq_u32 vcc_lo, 2\n\t" \
  "s_cbranch_scc1 .Lsf_q2\n\t" \
  "s_cmp_eq_u32 vcc_lo, 3\n\t" \
  "s_cbranch_scc1 .Lsf_q3\n\t" \
  "s_cmp_eq_u32 vcc_lo, 4\n\t" \
  "s_cbranch_scc1 .Lsf_q4\n\t" \
  "s_cmp_eq_u32 vcc_lo, 5\n\t" \
  "s_cbranch_scc1 .Lsf_q5\n\t" \
  "s_cmp_eq_u32 vcc_lo, 6\n\t" \
  "s_cbranch_scc1 .Lsf_q6\n\t" \
  "s_cmp_eq_u32 vcc_lo, 7\n\t" \
  "s_cbranch_scc1 .Lsf_q7\n\t" \
  "s_endpgm\n\t" \
  ".Lsf_v:\n\t" \
  "s_mov_b32 s98, 0\n\t" \
  ".Lsf_v1:\n\t" \
  "s_sleep 4\n\t" \
  "buffer_load_dword v209, v205, %[ws], 0 offen sc1\n\t" \
  "s_waitcnt vmcnt(0)\n\t" \
  "v_and_b32 v206, 0xbfffbfff, v209\n\t" \
  "v_cmp_ne_u32_e64 s[96:97], %[tag], v206\n\t" \
  "s_cmp_lg_u64 s[96:97], 0\n\t" \
  "s_cbranch_scc0 .Lsf_w\n\t" \
  "s_add_u32 s98, s98, 1\n\t" \
  "s_cmp_lt_u32 s98, %[cap]\n\t" \
  "s_cbranch_scc1 .Lsf_v1\n\t" \
  "v_mov_b32 v206, %[tag]\n\t" \
  "buffer_store_dword v206, off, %[ws], 0 offset:256 sc1\n\t" \
  "s_branch .Lsf_w\n\t" \
  ".subsection 0\n\t" \
  ".Lat_end:\n\t" \
  ""
#define AT_CLOB "v208", "v209", "v210", "v211", "v212", "v214", "v215", "v216", "v217", "v218", "v219", "v235", "v236", "v237", "v238", "v239", "s92", "s93", "s94", "s95", "s96", "s97", "s98", "s99", "vcc", "scc", "memory"
#define AT_PF1
#define AT_NU 1
#define AT_BN 0
#define AT_XF "+{v[0:15]}"
#define AT_WF "+{v[16:47]}"
#define TL_ASM \
  "v_pk_fma_f32 v[0:1], v[128:129], v[96:97], v[0:1]\n\t" \
  "v_pk_fma_f32 v[2:3], v[130:131], v[98:99], v[2:3]\n\t" \
  "v_pk_fma_f32 v[16:17], v[132:133], v[112:113], v[16:17]\n\t" \
  "v_pk_fma_f32 v[18:19], v[134:135], v[114:115], v[18:19]\n\t" \
  "v_pk_fma_f32 v[4:5], v[128:129], v[100:101], v[4:5]\n\t" \
  "v_pk_fma_f32 v[6:7], v[130:131], v[102:103], v[6:7]\n\t" \
  "v_pk_fma_f32 v[20:21], v[132:133], v[116:117], v[20:21]\n\t" \
  "v_pk_fma_f32 v[22:23], v[134:135], v[118:119], v[22:23]\n\t" \
  "v_cvt_pk_bf16_f32 v160, v0, v1\n\t" \
  "v_cvt_pk_bf16_f32 v161, v2, v3\n\t" \
  "v_cvt_pk_bf16_f32 v162, v16, v17\n\t" \
  "v_cvt_pk_bf16_f32 v163, v18, v19\n\t" \
  "v_pk_fma_f32 v[8:9], v[128:129], v[104:105], v[8:9]\n\t" \
  "v_pk_fma_f32 v[10:11], v[130:131], v[106:107], v[10:11]\n\t" \
  "v_pk_fma_f32 v[24:25], v[132:133], v[120:121], v[24:25]\n\t" \
  "v_pk_fma_f32 v[26:27], v[134:135], v[122:123], v[26:27]\n\t" \
  "v_cvt_pk_bf16_f32 v164, v4, v5\n\t" \
  "v_cvt_pk_bf16_f32 v165, v6, v7\n\t" \
  "v_cvt_pk_bf16_f32 v166, v20, v21\n\t" \
  "v_cvt_pk_bf16_f32 v167, v22, v23\n\t" \
  "v_pk_fma_f32 v[12:13], v[128:129], v[108:109], v[12:13]\n\t" \
  "v_pk_fma_f32 v[14:15], v[130:131], v[110:111], v[14:15]\n\t" \
  "v_pk_fma_f32 v[28:29], v[132:133], v[124:125], v[28:29]\n\t" \
  "v_pk_fma_f32 v[30:31], v[134:135], v[126:127], v[30:31]\n\t" \
  "v_cvt_pk_bf16_f32 v168, v8, v9\n\t" \
  "v_cvt_pk_bf16_f32 v169, v10, v11\n\t" \
  "v_cvt_pk_bf16_f32 v170, v24, v25\n\t" \
  "v_cvt_pk_bf16_f32 v171, v26, v27\n\t" \
  "v_cvt_pk_bf16_f32 v172, v12, v13\n\t" \
  "v_cvt_pk_bf16_f32 v173, v14, v15\n\t" \
  "v_cvt_pk_bf16_f32 v174, v28, v29\n\t" \
  "v_cvt_pk_bf16_f32 v175, v30, v31\n\t" \
  "v_dot2_f32_bf16 v152, v160, v160, 0\n\t" \
  "v_dot2_f32_bf16 v153, v164, v164, 0\n\t" \
  "v_dot2_f32_bf16 v154, v168, v168, 0\n\t" \
  "v_dot2_f32_bf16 v155, v172, v172, 0\n\t" \
  "v_dot2_f32_bf16 v152, v161, v161, v152\n\t" \
  "v_dot2_f32_bf16 v153, v165, v165, v153\n\t" \
  "v_dot2_f32_bf16 v154, v169, v169, v154\n\t" \
  "v_dot2_f32_bf16 v155, v173, v173, v155\n\t" \
  "v_dot2_f32_bf16 v152, v162, v162, v152\n\t" \
  "v_dot2_f32_bf16 v153, v166, v166, v153\n\t" \
  "v_dot2_f32_bf16 v154, v170, v170, v154\n\t" \
  "v_dot2_f32_bf16 v155, v174, v174, v155\n\t" \
  "v_dot2_f32_bf16 v152, v163, v163, v152\n\t" \
  "v_dot2_f32_bf16 v153, v167, v167, v153\n\t" \
  "v_dot2_f32_bf16 v154, v171, v171, v154\n\t" \
  "v_dot2_f32_bf16 v155, v175, v175, v155\n\t" \
  "v_mfma_f32_16x16x32_bf16 v[156:159], a[208:211], v[160:163], 0\n\t" \
  "s_lshl_b32 s64, %[wid], 10\n\t" \
  "v_add_u32 v234, s64, v242\n\t" \
  "v_mov_b32 v250, s87\n\t" \
  "v_mfma_f32_16x16x32_bf16 v[156:159], a[212:215], v[164:167], v[156:159]\n\t" \
  "s_lshl_b32 s61, %[bid], 11\n\t" \
  "v_permlane32_swap_b32 v152, v154\n\t" \
  "v_mfma_f32_16x16x32_bf16 v[156:159], a[216:219], v[168:171], v[156:159]\n\t" \
  "s_and_b32 s61, s61, 0x2000\n\t" \
  "v_permlane32_swap_b32 v153, v155\n\t" \
  "v_mfma_f32_16x16x32_bf16 v[156:159], a[220:223], v[172:175], v[156:159]\n\t" \
  "s_xor_b32 s61, s61, %[nb]\n\t" \
  "v_add_f32 v152, v152, v154\n\t" \
  "v_add_f32 v153, v153, v155\n\t" \
  "v_writelane_b32 v250, s88, 1\n\t" \
  "s_and_b32 s80, %[bid], 3\n\t" \
  "v_permlane16_swap_b32 v152, v153\n\t" \
  "s_lshr_b32 s60, %[bid], 6\n\t" \
  "s_lshl2_add_u32 s60, s60, s80\n\t" \
  "v_add_f32 v158, v157, v158\n\t" \
  "v_add_f32 v157, v152, v153\n\t" \
  "v_mov_b32 v159, v250\n\t" \
  "ds_write_b128 v234, v[156:159]\n\t" \
  "s_lshl_b32 s62, s60, 15\n\t" \
  "s_add_u32 s62, s62, s61\n\t" \
  "s_add_u32 s62, s62, 524288\n\t" \
  "s_and_b32 s63, %[bid], 0x20\n\t" \
  "s_lshl_b32 s63, s63, 9\n\t" \
  "s_add_u32 s77, s62, s63\n\t" \
  "s_and_b32 s63, %[bid], 0x18\n\t" \
  "s_lshl_b32 s63, s63, 8\n\t" \
  "s_add_u32 s77, s77, s63\n\t" \
  "s_add_u32 s77, s77, s64\n\t" \
  "v_lshl_add_u32 v248, %[wid], 3, v242\n\t" \
  "v_mov_b32 v217, v243\n\t" \
  "v_mov_b32 v219, v243\n\t" \
  "s_cmp_gt_u32 %[wid], 1\n\t" \
  "s_waitcnt lgkmcnt(0)\n\t" \
  "s_barrier\n\t" \
  "s_cbranch_scc1 .Ltl_np\n\t" \
  "ds_read2st64_b64 v[208:211], v248 offset1:2\n\t" \
  "ds_read2st64_b64 v[212:215], v248 offset0:4 offset1:6\n\t" \
  "s_waitcnt lgkmcnt(1)\n\t" \
  "v_pk_add_f32 v[208:209], v[208:209], v[210:211]\n\t" \
  "s_waitcnt lgkmcnt(0)\n\t" \
  "v_pk_add_f32 v[212:213], v[212:213], v[214:215]\n\t" \
  "s_nop 0\n\t" \
  "v_add_f32 v216, v208, v212\n\t" \
  "v_add_f32 v218, v209, v213\n\t" \
  "buffer_store_dwordx4 v[216:219], v242, %[ws], s77 offen sc1\n\t" \
  ".Ltl_np:\n\t" \
  STP("s_memrealtime s[90:91]\n\t") \
  STP("s_memtime s[82:83]\n\t") \
  STP("s_waitcnt lgkmcnt(0)\n\t") \
  STP("s_or_b32 s90, s90, 1\n\t") \
  STP("v_writelane_b32 %[stv], s90, 40\n\t") \
  STP("v_writelane_b32 %[stv], s82, 41\n\t") \
  "s_and_b32 s63, %[wid], 2\n\t" \
  "s_lshl_b32 s63, s63, 13\n\t" \
  "s_add_u32 s62, s62, s63\n\t" \
  "s_and_b32 s63, %[wid], 1\n\t" \
  "s_lshl_b32 s63, s63, 12\n\t" \
  "s_add_u32 s62, s62, s63\n\t" \
  "ds_read_b128 v[128:131], v237 offset:2688\n\t" \
  "ds_read_b128 v[132:135], v237 offset:2752\n\t" \
  "ds_read_b128 v[136:139], v237 offset:2816\n\t" \
  "ds_read_b128 v[140:143], v237 offset:2880\n\t" \
  "ds_read_b128 v[144:147], v237 offset:2944\n\t" \
  "ds_read_b128 v[148:151], v237 offset:3008\n\t" \
  "s_mov_b32 s75, 0x05040100\n\t" \
  "s_mov_b32 s76, 0x07060302\n\t" \
  "s_mov_b32 s72, 0\n\t" \
  "s_mov_b32 s81, 0xffff0000\n\t" \
  "s_bfe_u32 s79, %[bid], 0x30003\n\t" \
  "s_and_b32 s63, %[bid], 0x4\n\t" \
  "s_lshl_b32 s63, s63, 2\n\t" \
  "s_add_u32 s60, s60, s63\n\t" \
  "v_lshlrev_b32 v0, 16, v160\n\t" \
  "v_and_b32 v1, 0xffff0000, v160\n\t" \
  "v_lshlrev_b32 v2, 16, v161\n\t" \
  "v_and_b32 v3, 0xffff0000, v161\n\t" \
  "v_lshlrev_b32 v16, 16, v162\n\t" \
  "v_and_b32 v17, 0xffff0000, v162\n\t" \
  "v_lshlrev_b32 v18, 16, v163\n\t" \
  "v_and_b32 v19, 0xffff0000, v163\n\t" \
  "v_lshlrev_b32 v4, 16, v164\n\t" \
  "v_and_b32 v5, 0xffff0000, v164\n\t" \
  "v_lshlrev_b32 v6, 16, v165\n\t" \
  "v_and_b32 v7, 0xffff0000, v165\n\t" \
  "v_lshlrev_b32 v20, 16, v166\n\t" \
  "v_and_b32 v21, 0xffff0000, v166\n\t" \
  "v_lshlrev_b32 v22, 16, v167\n\t" \
  "v_and_b32 v23, 0xffff0000, v167\n\t" \
  "v_lshlrev_b32 v8, 16, v168\n\t" \
  "v_and_b32 v9, 0xffff0000, v168\n\t" \
  "v_lshlrev_b32 v10, 16, v169\n\t" \
  "v_and_b32 v11, 0xffff0000, v169\n\t" \
  "v_lshlrev_b32 v24, 16, v170\n\t" \
  "v_and_b32 v25, 0xffff0000, v170\n\t" \
  "v_lshlrev_b32 v26, 16, v171\n\t" \
  "v_and_b32 v27, 0xffff0000, v171\n\t" \
  "v_lshlrev_b32 v12, 16, v172\n\t" \
  "v_and_b32 v13, 0xffff0000, v172\n\t" \
  "buffer_load_dwordx4 v[208:211], v242, %[ws], s62 offen offset:0 sc1\n\t" \
  "buffer_load_dwordx4 v[212:215], v242, %[ws], s62 offen offset:1024 sc1\n\t" \
  "buffer_load_dwordx4 v[216:219], v242, %[ws], s62 offen offset:2048 sc1\n\t" \
  "buffer_load_dwordx4 v[220:223], v242, %[ws], s62 offen offset:3072 sc1\n\t" \
  "s_branch .Lht0\n\t" \
  ".p2align 12\n\t" \
  ".Lht0:\n\t" \
  "s_cbranch_execz .Lht0n\n\t" \
  "v_lshlrev_b32 v14, 16, v173\n\t" \
  "v_and_b32 v15, 0xffff0000, v173\n\t" \
  "v_lshlrev_b32 v28, 16, v174\n\t" \
  "v_and_b32 v29, 0xffff0000, v174\n\t" \
  "v_lshlrev_b32 v30, 16, v175\n\t" \
  "v_and_b32 v31, 0xffff0000, v175\n\t" \
  "s_lshl_b32 s63, s60, 6\n\t" \
  "s_sub_u32 s63, 0x7ff, s63\n\t" \
  "v_sub_u32 v248, s63, v244\n\t" \
  "v_and_b32 v246, 31, v244\n\t" \
  "s_and_b32 s63, %[bid], 0x18\n\t" \
  "s_lshl_b32 s63, s63, 7\n\t" \
  "s_add_u32 s65, s61, s63\n\t" \
  "s_and_b32 s63, %[bid], 0x3\n\t" \
  "s_lshl_b32 s63, s63, 8\n\t" \
  "s_add_u32 s65, s65, s63\n\t" \
  "s_add_u32 s65, s65, 65536\n\t" \
  "v_lshl_add_u32 v235, v246, 3, s65\n\t" \
  "s_lshl_b32 s68, 1, s60\n\t" \
  "s_lshl2_add_u32 s65, s79, %[wid]\n\t" \
  "s_lshl_b32 s63, s65, 13\n\t" \
  "s_and_b32 s63, s63, 0x2000\n\t" \
  "s_xor_b32 s63, s63, %[nb]\n\t" \
  "s_lshr_b32 s65, s65, 1\n\t" \
  "s_lshl_b32 s65, s65, 8\n\t" \
  "s_add_u32 s65, s65, s63\n\t" \
  "s_lshl3_add_u32 s65, s60, s65\n\t" \
  "s_add_u32 s65, s65, 65536\n\t" \
  "v_bfrev_b32 v249, 1\n\t" \
  "s_nop 0\n\t" \
  "v_writelane_b32 v249, 0, 63\n\t" \
  "v_mov_b32 v159, v243\n\t" \
  "s_add_u32 s63, s61, 262144\n\t" \
  "v_and_b32 v236, -32, v234\n\t" \
  "v_add_u32 v236, s63, v236\n\t" \
  "v_mov_b32 v113, v243\n\t" \
  "v_mov_b32 v115, v243\n\t" \
  "v_mov_b32 v117, v243\n\t" \
  "v_mov_b32 v119, v243\n\t" \
  "v_mov_b32 v121, v243\n\t" \
  "v_mov_b32 v123, v243\n\t" \
  "v_mov_b32 v125, v243\n\t" \
  "v_mov_b32 v127, v243\n\t" \
  "v_and_b32 v239, 48, v244\n\t" \
  "v_lshl_add_u32 v239, %[wid], 7, v239\n\t" \
  "v_and_b32 v157, 15, v244\n\t" \
  "v_bfrev_b32 v156, 1\n\t" \
  "s_lshl_b32 s66, s60, 13\n\t" \
  "s_and_b32 s66, s66, 0x3c000\n\t" \
  "s_and_b32 s63, %[bid], 0x1\n\t" \
  "s_lshl_b32 s63, s63, 12\n\t" \
  "s_add_u32 s66, s66, s63\n\t" \
  "s_and_b32 s63, %[bid], 0x38\n\t" \
  "s_lshl_b32 s63, s63, 6\n\t" \
  "s_add_u32 s66, s66, s63\n\t" \
  "s_add_u32 s66, s66, 262144\n\t" \
  "s_xor_b32 s77, s66, 0x2000\n\t" \
  "s_lshl_b32 s67, s60, 12\n\t" \
  "s_and_b32 s67, s67, 0x1c000\n\t" \
  "s_and_b32 s63, %[bid], 0x20\n\t" \
  "s_lshl_b32 s63, s63, 8\n\t" \
  "s_add_u32 s67, s67, s63\n\t" \
  "s_xor_b32 s67, s67, %[nb]\n\t" \
  "s_and_b32 s63, %[bid], 0x3\n\t" \
  "s_lshl_b32 s63, s63, 11\n\t" \
  "s_add_u32 s67, s67, s63\n\t" \
  "s_and_b32 s63, %[bid], 0x18\n\t" \
  "s_lshl_b32 s63, s63, 6\n\t" \
  "s_add_u32 s67, s67, s63\n\t" \
  "s_add_u32 s67, s67, 1966080\n\t" \
  "v_perm_b32 v240, v34, v32, s75\n\t" \
  "v_perm_b32 v34, v34, v32, s76\n\t" \
  "v_perm_b32 v32, v35, v33, s75\n\t" \
  "v_perm_b32 v35, v35, v33, s76\n\t" \
  "v_perm_b32 v33, v38, v36, s75\n\t" \
  "v_perm_b32 v38, v38, v36, s76\n\t" \
  "v_perm_b32 v36, v39, v37, s75\n\t" \
  "v_perm_b32 v39, v39, v37, s76\n\t" \
  "v_perm_b32 v37, v42, v40, s75\n\t" \
  "v_perm_b32 v42, v42, v40, s76\n\t" \
  "v_perm_b32 v40, v43, v41, s75\n\t" \
  "v_perm_b32 v43, v43, v41, s76\n\t" \
  "v_perm_b32 v41, v46, v44, s75\n\t" \
  "v_perm_b32 v46, v46, v44, s76\n\t" \
  "v_perm_b32 v44, v47, v45, s75\n\t" \
  "v_perm_b32 v47, v47, v45, s76\n\t" \
  "v_perm_b32 v45, v50, v48, s75\n\t" \
  "v_perm_b32 v50, v50, v48, s76\n\t" \
  "v_perm_b32 v48, v51, v49, s75\n\t" \
  "v_perm_b32 v51, v51, v49, s76\n\t" \
  "v_perm_b32 v49, v54, v52, s75\n\t" \
  "v_perm_b32 v54, v54, v52, s76\n\t" \
  "v_perm_b32 v52, v55, v53, s75\n\t" \
  "v_perm_b32 v55, v55, v53, s76\n\t" \
  "v_perm_b32 v53, v58, v56, s75\n\t" \
  "v_perm_b32 v58, v58, v56, s76\n\t" \
  "v_perm_b32 v56, v59, v57, s75\n\t" \
  "v_perm_b32 v59, v59, v57, s76\n\t" \
  "v_perm_b32 v57, v62, v60, s75\n\t" \
  "v_perm_b32 v62, v62, v60, s76\n\t" \
  "v_perm_b32 v60, v63, v61, s75\n\t" \
  "v_perm_b32 v63, v63, v61, s76\n\t" \
  "s_waitcnt vmcnt(0)\n\t" \
  ".Ltl_c0:\n\t" \
  "v_xor_b32 v152, %[tag], v209\n\t" \
  "v_bitop3_b32 v152, v152, v211, %[tag] bitop3:0xf6\n\t" \
  "v_bitop3_b32 v152, v152, v213, %[tag] bitop3:0xf6\n\t" \
  "v_bitop3_b32 v152, v152, v215, %[tag] bitop3:0xf6\n\t" \
  "v_bitop3_b32 v152, v152, v217, %[tag] bitop3:0xf6\n\t" \
  "v_bitop3_b32 v152, v152, v219, %[tag] bitop3:0xf6\n\t" \
  "v_bitop3_b32 v152, v152, v221, %[tag] bitop3:0xf6\n\t" \
  "v_bitop3_b32 v152, v152, v223, %[tag] bitop3:0xf6\n\t" \
  "v_cmp_ne_u32 vcc, 0, v152\n\t" \
  "v_add_f32 v152, v208, v216\n\t" \
  "v_add_f32 v153, v210, v218\n\t" \
  "v_add_f32 v154, v212, v220\n\t" \
  "v_add_f32 v155, v214, v222\n\t" \
  "s_cbranch_vccnz .Ltl_s0\n\t" \
  ".Ltl_k0:\n\t" \
  "ds_write_b128 v234, v[152:155] offset:4096\n\t" \
  "buffer_load_dwordx2 v[216:217], v235, %[ws], 0 offen sc1\n\t" \
  "s_waitcnt lgkmcnt(0)\n\t" \
  "s_barrier\n\t" \
  STP("s_memrealtime s[90:91]\n\t") \
  STP("s_memtime s[82:83]\n\t") \
  STP("s_waitcnt lgkmcnt(0)\n\t") \
  STP("s_or_b32 s90, s90, 1\n\t") \
  STP("v_writelane_b32 %[stv], s90, 42\n\t") \
  STP("v_writelane_b32 %[stv], s82, 43\n\t") \
  "ds_read_b128 v[96:99], v242 offset:4096\n\t" \
  "ds_read_b128 v[100:103], v242 offset:5120\n\t" \
  "ds_read_b128 v[104:107], v242 offset:6144\n\t" \
  "ds_read_b128 v[108:111], v242 offset:7168\n\t" \
  "s_waitcnt lgkmcnt(2)\n\t" \
  "v_pk_add_f32 v[96:97], v[96:97], v[100:101]\n\t" \
  "v_pk_add_f32 v[98:99], v[98:99], v[102:103]\n\t" \
  "s_waitcnt lgkmcnt(0)\n\t" \
  "v_pk_add_f32 v[104:105], v[104:105], v[108:109]\n\t" \
  "v_pk_add_f32 v[106:107], v[106:107], v[110:111]\n\t" \
  "v_pk_add_f32 v[96:97], v[96:97], v[104:105]\n\t" \
  "v_pk_add_f32 v[98:99], v[98:99], v[106:107]\n\t" \
  "v_mul_f32 v96, 0x3a800000, v96\n\t" \
  "v_mul_f32 v97, 0x3a800000, v97\n\t" \
  "v_readlane_b32 s87, v99, 0\n\t" \
  "v_fma_f32 v97, -v96, v96, v97\n\t" \
  "v_readlane_b32 s88, v99, 1\n\t" \
  "v_max_f32 v97, 0, v97\n\t" \
  "v_add_f32 v97, 0x3727c5ac, v97\n\t" \
  "v_rsq_f32 v208, v97\n\t" \
  "v_fma_f32 v98, -v96, s87, v98\n\t" \
  "v_fma_f32 v98, v208, v98, s88\n\t" \
  "v_cvt_pk_bf16_f32 v98, v98, v98\n\t" \
  "v_ashrrev_i32 v99, 31, v98\n\t" \
  "v_or_b32 v99, 0x80000000, v99\n\t" \
  "v_xor_b32 v98, v98, v99\n\t" \
  "v_and_or_b32 v158, v98, s81, v248\n\t" \
  "v_mul_f32 v209, v208, -v96\n\t" \
  "v_mov_b32 v212, v208\n\t" \
  "v_max_u32_dpp v158, v158, v158 quad_perm:[1,0,3,2] row_mask:0xf bank_mask:0xf\n\t" \
  "v_mov_b32 v213, v209\n\t" \
  "v_permlane32_swap_b32 v208, v212\n\t" \
  "v_max_u32_dpp v158, v158, v158 quad_perm:[2,3,0,1] row_mask:0xf bank_mask:0xf\n\t" \
  "v_permlane32_swap_b32 v209, v213\n\t" \
  "v_mov_b32 v210, v208\n\t" \
  "v_max_u32_dpp v158, v158, v158 row_half_mirror row_mask:0xf bank_mask:0xf\n\t" \
  "v_mov_b32 v211, v209\n\t" \
  "v_mov_b32 v214, v212\n\t" \
  "v_max_u32_dpp v158, v158, v158 row_mirror row_mask:0xf bank_mask:0xf\n\t" \
  "v_mov_b32 v215, v213\n\t" \
  "v_permlane16_swap_b32 v208, v210\n\t" \
  "v_max_u32_dpp v158, v158, v158 row_bcast:15 row_mask:0xa bank_mask:0xf\n\t" \
  "v_permlane16_swap_b32 v209, v211\n\t" \
  "v_permlane16_swap_b32 v212, v214\n\t" \
  "v_max_u32_dpp v158, v158, v158 row_bcast:31 row_mask:0xc bank_mask:0xf\n\t" \
  "buffer_store_dwordx2 v[158:159], v249, %[ws], s65 offen sc1\n\t" \
  "v_readlane_b32 s69, v158, 63\n\t" \
  STP("s_memrealtime s[90:91]\n\t") \
  STP("s_memtime s[82:83]\n\t") \
  STP("s_waitcnt lgkmcnt(0)\n\t") \
  STP("s_or_b32 s90, s90, 1\n\t") \
  STP("v_writelane_b32 %[stv], s90, 44\n\t") \
  STP("v_writelane_b32 %[stv], s82, 45\n\t") \
  "v_permlane16_swap_b32 v213, v215\n\t" \
  "s_andn2_b32 s73, 63, s69\n\t" \
  "s_waitcnt vmcnt(1)\n\t" \
  "v_cmp_eq_u32 vcc, %[tag], v217\n\t" \
  "s_mov_b32 s74, 0x100\n\t" \
  "s_nop 0\n\t" \
  "v_cndmask_b32 v152, 0, v216, vcc\n\t" \
  "s_bcnt1_i32_b32 s78, vcc_lo\n\t" \
  "v_cmp_lt_u32 vcc, s69, v152\n\t" \
  "s_cbranch_vccz .Ltl_cd\n\t" \
  ".Ltl_ce:\n\t" \
  "v_max_u32_dpp v152, v152, v152 quad_perm:[1,0,3,2] row_mask:0xf bank_mask:0xf\n\t" \
  "s_lshl_b32 s63, s60, 17\n\t" \
  "s_lshl_b32 s64, s79, 8\n\t" \
  "v_max_u32_dpp v152, v152, v152 quad_perm:[2,3,0,1] row_mask:0xf bank_mask:0xf\n\t" \
  "s_add_u32 s63, s63, s64\n\t" \
  "s_lshl_b32 s64, %[wid], 6\n\t" \
  "v_max_u32_dpp v152, v152, v152 row_half_mirror row_mask:0xf bank_mask:0xf\n\t" \
  "s_add_u32 s63, s63, s64\n\t" \
  "v_lshl_add_u32 v238, v157, 11, s63\n\t" \
  "v_max_u32_dpp v152, v152, v152 row_mirror row_mask:0xf bank_mask:0xf\n\t" \
  "s_nop 1\n\t" \
  "v_max_u32_dpp v152, v152, v152 row_bcast:15 row_mask:0xa bank_mask:0xf\n\t" \
  "s_nop 0\n\t" \
  "v_readlane_b32 s70, v152, 31\n\t" \
  "s_max_u32 s70, s70, s69\n\t" \
  "s_andn2_b32 s63, 0x780, s70\n\t" \
  "s_lshl_b32 s63, s63, 7\n\t" \
  "s_andn2_b32 s71, 64, s70\n\t" \
  "s_lshl_b32 s71, s71, 6\n\t" \
  "s_or_b32 s71, s71, s63\n\t" \
  "buffer_load_dwordx2 v[216:217], v235, %[ws], 0 offen sc1\n\t" \
  "buffer_load_dwordx4 v[218:221], v236, %[ws], s71 offen sc1\n\t" \
  "buffer_load_dwordx4 v[222:225], v236, %[ws], s71 offen offset:16 sc1\n\t" \
  "v_bfe_u32 v153, v244, 4, 1\n\t" \
  "v_lshl_add_u32 v238, v153, 5, v238\n\t" \
  "v_lshrrev_b32 v154, 5, v244\n\t" \
  "v_lshl_add_u32 v238, v154, 4, v238\n\t" \
  "s_mov_b64 s[96:97], %[out]\n\t" \
  "s_and_b32 s97, s97, 0xffff\n\t" \
  "s_mov_b32 s98, 0x400000\n\t" \
  "s_mov_b32 s99, 0x20000\n\t" \
  "s_lshr_b32 s64, %[bid], 3\n\t" \
  "s_and_b32 s63, s64, 27\n\t" \
  "s_cmp_lt_u32 s64, 24\n\t" \
  "s_cselect_b32 s64, s64, s63\n\t" \
  "s_mul_i32 s63, s80, 56\n\t" \
  "s_add_u32 s64, s64, s63\n\t" \
  "s_and_b32 s63, %[bid], 4\n\t" \
  "s_mul_i32 s63, s63, 7\n\t" \
  "s_add_u32 s64, s64, s63\n\t" \
  "s_lshl_b32 s63, %[wid], 12\n\t" \
  "s_and_b32 s63, s63, 0x2000\n\t" \
  "s_xor_b32 s85, s63, %[nb]\n\t" \
  "s_lshr_b32 s63, s64, 2\n\t" \
  "s_lshl_b32 s63, s63, 14\n\t" \
  "s_add_u32 s85, s85, s63\n\t" \
  "s_and_b32 s63, s64, 3\n\t" \
  "s_lshl_b32 s63, s63, 11\n\t" \
  "s_add_u32 s85, s85, s63\n\t" \
  "s_add_u32 s85, s85, 1048576\n\t" \
  "v_and_b32 v247, 0x7f0, v234\n\t" \
  "v_lshlrev_b32 v245, 6, v154\n\t" \
  "v_lshl_add_u32 v245, v153, 4, v245\n\t" \
  "v_and_b32 v153, 1, v244\n\t" \
  "v_lshl_add_u32 v245, v153, 5, v245\n\t" \
  "v_lshl_add_u32 v245, %[wid], 7, v245\n\t" \
  "v_and_b32 v153, 14, v244\n\t" \
  "v_lshl_add_u32 v245, v153, 8, v245\n\t" \
  "v_lshlrev_b32 v246, 2, v246\n\t" \
  "v_add_u32 v248, 0x80, v246\n\t" \
  "s_mov_b32 s72, 0\n\t" \
  "s_lshr_b32 s62, %[bid], 5\n\t" \
  "s_bfe_u32 s63, %[bid], 0x10002\n\t" \
  "s_sub_u32 s62, s62, s63\n\t" \
  "s_cmp_ge_u32 s78, 0\n\t" \
  "s_cselect_b32 s84, 1, 0\n\t" \
  "s_cmp_eq_u32 s62, 6\n\t" \
  "s_cselect_b32 s84, 0, s84\n\t" \
  "s_cmp_lt_u32 s78, 33\n\t" \
  "s_cselect_b32 s78, 0, 0x80000000\n\t" \
  "s_cmp_lg_u32 s84, 0\n\t" \
  "s_cbranch_scc1 .Lht1\n\t" \
  ".Ltl_h2:\n\t" \
  "v_pk_fma_f32 v[112:113], v[0:1], v[208:209], v[208:209] op_sel:[0,0,1] op_sel_hi:[1,0,1]\n\t" \
  "v_pk_fma_f32 v[114:115], v[2:3], v[208:209], v[208:209] op_sel:[0,0,1] op_sel_hi:[1,0,1]\n\t" \
  "v_pk_fma_f32 v[116:117], v[16:17], v[208:209], v[208:209] op_sel:[0,0,1] op_sel_hi:[1,0,1]\n\t" \
  "v_pk_fma_f32 v[118:119], v[18:19], v[208:209], v[208:209] op_sel:[0,0,1] op_sel_hi:[1,0,1]\n\t" \
  "v_pk_fma_f32 v[112:113], v[112:113], v[136:137], v[128:129]\n\t" \
  "v_pk_fma_f32 v[114:115], v[114:115], v[138:139], v[130:131]\n\t" \
  "v_pk_fma_f32 v[116:117], v[116:117], v[140:141], v[132:133]\n\t" \
  "v_pk_fma_f32 v[118:119], v[118:119], v[142:143], v[134:135]\n\t" \
  "v_cvt_pk_bf16_f32 v96, v112, v113\n\t" \
  "v_cvt_pk_bf16_f32 v97, v114, v115\n\t" \
  "v_cvt_pk_bf16_f32 v98, v116, v117\n\t" \
  "v_cvt_pk_bf16_f32 v99, v118, v119\n\t" \
  "v_pk_fma_f32 v[120:121], v[4:5], v[210:211], v[210:211] op_sel:[0,0,1] op_sel_hi:[1,0,1]\n\t" \
  "v_pk_fma_f32 v[122:123], v[6:7], v[210:211], v[210:211] op_sel:[0,0,1] op_sel_hi:[1,0,1]\n\t" \
  "v_pk_fma_f32 v[124:125], v[20:21], v[210:211], v[210:211] op_sel:[0,0,1] op_sel_hi:[1,0,1]\n\t" \
  "v_pk_fma_f32 v[126:127], v[22:23], v[210:211], v[210:211] op_sel:[0,0,1] op_sel_hi:[1,0,1]\n\t" \
  "v_pk_fma_f32 v[120:121], v[120:121], v[136:137], v[128:129]\n\t" \
  "v_pk_fma_f32 v[122:123], v[122:123], v[138:139], v[130:131]\n\t" \
  "v_pk_fma_f32 v[124:125], v[124:125], v[140:141], v[132:133]\n\t" \
  "v_pk_fma_f32 v[126:127], v[126:127], v[142:143], v[134:135]\n\t" \
  "v_cvt_pk_bf16_f32 v100, v120, v121\n\t" \
  "v_cvt_pk_bf16_f32 v101, v122, v123\n\t" \
  "v_cvt_pk_bf16_f32 v102, v124, v125\n\t" \
  "v_cvt_pk_bf16_f32 v103, v126, v127\n\t" \
  "v_pk_fma_f32 v[112:113], v[8:9], v[212:213], v[212:213] op_sel:[0,0,1] op_sel_hi:[1,0,1]\n\t" \
  "v_pk_fma_f32 v[114:115], v[10:11], v[212:213], v[212:213] op_sel:[0,0,1] op_sel_hi:[1,0,1]\n\t" \
  "v_pk_fma_f32 v[116:117], v[24:25], v[212:213], v[212:213] op_sel:[0,0,1] op_sel_hi:[1,0,1]\n\t" \
  "v_pk_fma_f32 v[118:119], v[26:27], v[212:213], v[212:213] op_sel:[0,0,1] op_sel_hi:[1,0,1]\n\t" \
  "v_pk_fma_f32 v[112:113], v[112:113], v[136:137], v[128:129]\n\t" \
  "v_pk_fma_f32 v[114:115], v[114:115], v[138:139], v[130:131]\n\t" \
  "v_pk_fma_f32 v[116:117], v[116:117], v[140:141], v[132:133]\n\t" \
  "v_pk_fma_f32 v[118:119], v[118:119], v[142:143], v[134:135]\n\t" \
  "v_cvt_pk_bf16_f32 v104, v112, v113\n\t" \
  "v_cvt_pk_bf16_f32 v105, v114, v115\n\t" \
  "v_cvt_pk_bf16_f32 v106, v116, v117\n\t" \
  "v_cvt_pk_bf16_f32 v107, v118, v119\n\t" \
  "v_pk_fma_f32 v[120:121], v[12:13], v[214:215], v[214:215] op_sel:[0,0,1] op_sel_hi:[1,0,1]\n\t" \
  "v_pk_fma_f32 v[122:123], v[14:15], v[214:215], v[214:215] op_sel:[0,0,1] op_sel_hi:[1,0,1]\n\t" \
  "v_pk_fma_f32 v[124:125], v[28:29], v[214:215], v[214:215] op_sel:[0,0,1] op_sel_hi:[1,0,1]\n\t" \
  "v_pk_fma_f32 v[126:127], v[30:31], v[214:215], v[214:215] op_sel:[0,0,1] op_sel_hi:[1,0,1]\n\t" \
  "v_pk_fma_f32 v[120:121], v[120:121], v[136:137], v[128:129]\n\t" \
  "v_pk_fma_f32 v[122:123], v[122:123], v[138:139], v[130:131]\n\t" \
  "v_pk_fma_f32 v[124:125], v[124:125], v[140:141], v[132:133]\n\t" \
  "v_pk_fma_f32 v[126:127], v[126:127], v[142:143], v[134:135]\n\t" \
  "v_cvt_pk_bf16_f32 v108, v120, v121\n\t" \
  "v_cvt_pk_bf16_f32 v109, v122, v123\n\t" \
  "v_cvt_pk_bf16_f32 v110, v124, v125\n\t" \
  "v_cvt_pk_bf16_f32 v111, v126, v127\n\t" \
  "v_sub_u32 v152, s74, v157\n\t" \
  "v_bfrev_b32 v155, 1\n\t" \
  "v_cmp_eq_u32 vcc, 0, v152\n\t" \
  "v_lshlrev_b32 v160, 16, v96\n\t" \
  "v_and_b32 v161, 0xffff0000, v96\n\t" \
  "v_lshlrev_b32 v162, 16, v97\n\t" \
  "v_and_b32 v163, 0xffff0000, v97\n\t" \
  "v_lshlrev_b32 v164, 16, v98\n\t" \
  "v_and_b32 v165, 0xffff0000, v98\n\t" \
  "v_lshlrev_b32 v166, 16, v99\n\t" \
  "v_and_b32 v167, 0xffff0000, v99\n\t" \
  "v_pk_fma_f32 v[160:161], v[144:145], v[160:161], v[0:1]\n\t" \
  "v_pk_fma_f32 v[162:163], v[146:147], v[162:163], v[2:3]\n\t" \
  "v_pk_fma_f32 v[164:165], v[148:149], v[164:165], v[16:17]\n\t" \
  "v_pk_fma_f32 v[166:167], v[150:151], v[166:167], v[18:19]\n\t" \
  "v_cndmask_b32 v228, v238, v155, vcc\n\t" \
  "v_cvt_pk_bf16_f32 v112, v160, v161\n\t" \
  "v_cvt_pk_bf16_f32 v114, v164, v165\n\t" \
  "v_cvt_pk_bf16_f32 v113, v162, v163\n\t" \
  "v_cvt_pk_bf16_f32 v115, v166, v167\n\t" \
  "v_or_b32 v232, s78, v228\n\t" \
  "s_mov_b32 s64, 0\n\t" \
  "v_permlane16_swap_b32 v112, v114\n\t" \
  "v_permlane16_swap_b32 v113, v115\n\t" \
  "buffer_store_dwordx4 v[112:115], v232, s[96:99], s64 offen sc1\n\t" \
  "v_cmp_eq_u32 vcc, 16, v152\n\t" \
  "v_lshlrev_b32 v160, 16, v100\n\t" \
  "v_and_b32 v161, 0xffff0000, v100\n\t" \
  "v_lshlrev_b32 v162, 16, v101\n\t" \
  "v_and_b32 v163, 0xffff0000, v101\n\t" \
  "v_lshlrev_b32 v164, 16, v102\n\t" \
  "v_and_b32 v165, 0xffff0000, v102\n\t" \
  "v_lshlrev_b32 v166, 16, v103\n\t" \
  "v_and_b32 v167, 0xffff0000, v103\n\t" \
  "v_pk_fma_f32 v[160:161], v[144:145], v[160:161], v[4:5]\n\t" \
  "v_pk_fma_f32 v[162:163], v[146:147], v[162:163], v[6:7]\n\t" \
  "v_pk_fma_f32 v[164:165], v[148:149], v[164:165], v[20:21]\n\t" \
  "v_pk_fma_f32 v[166:167], v[150:151], v[166:167], v[22:23]\n\t" \
  "v_cndmask_b32 v229, v238, v155, vcc\n\t" \
  "v_cvt_pk_bf16_f32 v116, v160, v161\n\t" \
  "v_cvt_pk_bf16_f32 v118, v164, v165\n\t" \
  "v_cvt_pk_bf16_f32 v117, v162, v163\n\t" \
  "v_cvt_pk_bf16_f32 v119, v166, v167\n\t" \
  "v_or_b32 v232, s78, v229\n\t" \
  "s_mov_b32 s64, 32768\n\t" \
  "v_permlane16_swap_b32 v116, v118\n\t" \
  "v_permlane16_swap_b32 v117, v119\n\t" \
  "buffer_store_dwordx4 v[116:119], v232, s[96:99], s64 offen sc1\n\t" \
  "v_cmp_eq_u32 vcc, 32, v152\n\t" \
  "v_lshlrev_b32 v160, 16, v104\n\t" \
  "v_and_b32 v161, 0xffff0000, v104\n\t" \
  "v_lshlrev_b32 v162, 16, v105\n\t" \
  "v_and_b32 v163, 0xffff0000, v105\n\t" \
  "v_lshlrev_b32 v164, 16, v106\n\t" \
  "v_and_b32 v165, 0xffff0000, v106\n\t" \
  "v_lshlrev_b32 v166, 16, v107\n\t" \
  "v_and_b32 v167, 0xffff0000, v107\n\t" \
  "v_pk_fma_f32 v[160:161], v[144:145], v[160:161], v[8:9]\n\t" \
  "v_pk_fma_f32 v[162:163], v[146:147], v[162:163], v[10:11]\n\t" \
  "v_pk_fma_f32 v[164:165], v[148:149], v[164:165], v[24:25]\n\t" \
  "v_pk_fma_f32 v[166:167], v[150:151], v[166:167], v[26:27]\n\t" \
  "v_cndmask_b32 v230, v238, v155, vcc\n\t" \
  "v_cvt_pk_bf16_f32 v120, v160, v161\n\t" \
  "v_cvt_pk_bf16_f32 v122, v164, v165\n\t" \
  "v_cvt_pk_bf16_f32 v121, v162, v163\n\t" \
  "v_cvt_pk_bf16_f32 v123, v166, v167\n\t" \
  "v_or_b32 v232, s78, v230\n\t" \
  "s_mov_b32 s64, 65536\n\t" \
  "v_permlane16_swap_b32 v120, v122\n\t" \
  "v_permlane16_swap_b32 v121, v123\n\t" \
  "buffer_store_dwordx4 v[120:123], v232, s[96:99], s64 offen sc1\n\t" \
  "v_cmp_eq_u32 vcc, 48, v152\n\t" \
  "v_lshlrev_b32 v160, 16, v108\n\t" \
  "v_and_b32 v161, 0xffff0000, v108\n\t" \
  "v_lshlrev_b32 v162, 16, v109\n\t" \
  "v_and_b32 v163, 0xffff0000, v109\n\t" \
  "v_lshlrev_b32 v164, 16, v110\n\t" \
  "v_and_b32 v165, 0xffff0000, v110\n\t" \
  "v_lshlrev_b32 v166, 16, v111\n\t" \
  "v_and_b32 v167, 0xffff0000, v111\n\t" \
  "v_pk_fma_f32 v[160:161], v[144:145], v[160:161], v[12:13]\n\t" \
  "v_pk_fma_f32 v[162:163], v[146:147], v[162:163], v[14:15]\n\t" \
  "v_pk_fma_f32 v[164:165], v[148:149], v[164:165], v[28:29]\n\t" \
  "v_pk_fma_f32 v[166:167], v[150:151], v[166:167], v[30:31]\n\t" \
  "v_cndmask_b32 v231, v238, v155, vcc\n\t" \
  "v_cvt_pk_bf16_f32 v124, v160, v161\n\t" \
  "v_cvt_pk_bf16_f32 v126, v164, v165\n\t" \
  "v_cvt_pk_bf16_f32 v125, v162, v163\n\t" \
  "v_cvt_pk_bf16_f32 v127, v166, v167\n\t" \
  "v_or_b32 v232, s78, v231\n\t" \
  "s_mov_b32 s64, 98304\n\t" \
  "v_permlane16_swap_b32 v124, v126\n\t" \
  "v_permlane16_swap_b32 v125, v127\n\t" \
  "buffer_store_dwordx4 v[124:127], v232, s[96:99], s64 offen sc1\n\t" \
  STP("s_memrealtime s[90:91]\n\t") \
  STP("s_memtime s[82:83]\n\t") \
  STP("s_waitcnt lgkmcnt(0)\n\t") \
  STP("s_or_b32 s90, s90, 1\n\t") \
  STP("v_writelane_b32 %[stv], s90, 46\n\t") \
  STP("v_writelane_b32 %[stv], s82, 47\n\t") \
  "s_cmp_lg_u32 s84, 0\n\t" \
  "s_cbranch_scc1 .Ltl_oe\n\t" \
  "s_waitcnt vmcnt(4)\n\t" \
  "s_branch .Ltl_c1\n\t" \
  ".Ltl_s0:\n\t" \
  "s_sleep 1\n\t" \
  "buffer_load_dwordx4 v[208:211], v242, %[ws], s62 offen offset:0 sc1\n\t" \
  "buffer_load_dwordx4 v[212:215], v242, %[ws], s62 offen offset:1024 sc1\n\t" \
  "buffer_load_dwordx4 v[216:219], v242, %[ws], s62 offen offset:2048 sc1\n\t" \
  "buffer_load_dwordx4 v[220:223], v242, %[ws], s62 offen offset:3072 sc1\n\t" \
  "s_add_u32 s72, s72, 1\n\t" \
  "s_cmp_lt_u32 s72, %[cap]\n\t" \
  "s_waitcnt vmcnt(0)\n\t" \
  "s_cbranch_scc1 .Ltl_c0\n\t" \
  "buffer_store_dword v243, off, %[ws], 0 offset:256 sc1\n\t" \
  "s_branch .Ltl_k0\n\t" \
  ".Ltl_cd:\n\t" \
  "s_mov_b32 s74, s73\n\t" \
  "s_and_b32 s63, s73, 15\n\t" \
  "v_cmp_eq_u32 vcc, s63, v157\n\t" \
  "s_cmp_lt_u32 s73, 32\n\t" \
  "s_cbranch_scc1 .Ltl_d0\n\t" \
  "s_cmp_lt_u32 s73, 48\n\t" \
  "s_cbranch_scc1 .Ltl_e2\n\t" \
  ".Ltl_e3:\n\t" \
  "v_pk_fma_f32 v[96:97], v[12:13], v[214:215], v[214:215] op_sel:[0,0,1] op_sel_hi:[1,0,1]\n\t" \
  "v_pk_fma_f32 v[98:99], v[14:15], v[214:215], v[214:215] op_sel:[0,0,1] op_sel_hi:[1,0,1]\n\t" \
  "v_pk_fma_f32 v[100:101], v[28:29], v[214:215], v[214:215] op_sel:[0,0,1] op_sel_hi:[1,0,1]\n\t" \
  "v_pk_fma_f32 v[102:103], v[30:31], v[214:215], v[214:215] op_sel:[0,0,1] op_sel_hi:[1,0,1]\n\t" \
  "v_mov_b32 v120, v172\n\t" \
  "v_mov_b32 v122, v173\n\t" \
  "v_mov_b32 v124, v174\n\t" \
  "v_mov_b32 v126, v175\n\t" \
  "s_branch .Ltl_cs\n\t" \
  ".Ltl_e2:\n\t" \
  "v_pk_fma_f32 v[96:97], v[8:9], v[212:213], v[212:213] op_sel:[0,0,1] op_sel_hi:[1,0,1]\n\t" \
  "v_pk_fma_f32 v[98:99], v[10:11], v[212:213], v[212:213] op_sel:[0,0,1] op_sel_hi:[1,0,1]\n\t" \
  "v_pk_fma_f32 v[100:101], v[24:25], v[212:213], v[212:213] op_sel:[0,0,1] op_sel_hi:[1,0,1]\n\t" \
  "v_pk_fma_f32 v[102:103], v[26:27], v[212:213], v[212:213] op_sel:[0,0,1] op_sel_hi:[1,0,1]\n\t" \
  "v_mov_b32 v120, v168\n\t" \
  "v_mov_b32 v122, v169\n\t" \
  "v_mov_b32 v124, v170\n\t" \
  "v_mov_b32 v126, v171\n\t" \
  "s_branch .Ltl_cs\n\t" \
  ".Ltl_d0:\n\t" \
  "s_cmp_lt_u32 s73, 16\n\t" \
  "s_cbranch_scc1 .Ltl_e0\n\t" \
  ".Ltl_e1:\n\t" \
  "v_pk_fma_f32 v[96:97], v[4:5], v[210:211], v[210:211] op_sel:[0,0,1] op_sel_hi:[1,0,1]\n\t" \
  "v_pk_fma_f32 v[98:99], v[6:7], v[210:211], v[210:211] op_sel:[0,0,1] op_sel_hi:[1,0,1]\n\t" \
  "v_pk_fma_f32 v[100:101], v[20:21], v[210:211], v[210:211] op_sel:[0,0,1] op_sel_hi:[1,0,1]\n\t" \
  "v_pk_fma_f32 v[102:103], v[22:23], v[210:211], v[210:211] op_sel:[0,0,1] op_sel_hi:[1,0,1]\n\t" \
  "v_mov_b32 v120, v164\n\t" \
  "v_mov_b32 v122, v165\n\t" \
  "v_mov_b32 v124, v166\n\t" \
  "v_mov_b32 v126, v167\n\t" \
  "s_branch .Ltl_cs\n\t" \
  ".Ltl_e0:\n\t" \
  "v_pk_fma_f32 v[96:97], v[0:1], v[208:209], v[208:209] op_sel:[0,0,1] op_sel_hi:[1,0,1]\n\t" \
  "v_pk_fma_f32 v[98:99], v[2:3], v[208:209], v[208:209] op_sel:[0,0,1] op_sel_hi:[1,0,1]\n\t" \
  "v_pk_fma_f32 v[100:101], v[16:17], v[208:209], v[208:209] op_sel:[0,0,1] op_sel_hi:[1,0,1]\n\t" \
  "v_pk_fma_f32 v[102:103], v[18:19], v[208:209], v[208:209] op_sel:[0,0,1] op_sel_hi:[1,0,1]\n\t" \
  "v_mov_b32 v120, v160\n\t" \
  "v_mov_b32 v122, v161\n\t" \
  "v_mov_b32 v124, v162\n\t" \
  "v_mov_b32 v126, v163\n\t" \
  ".Ltl_cs:\n\t" \
  "v_pk_fma_f32 v[96:97], v[96:97], v[136:137], v[128:129]\n\t" \
  "v_pk_fma_f32 v[98:99], v[98:99], v[138:139], v[130:131]\n\t" \
  "v_pk_fma_f32 v[100:101], v[100:101], v[140:141], v[132:133]\n\t" \
  "v_pk_fma_f32 v[102:103], v[102:103], v[142:143], v[134:135]\n\t" \
  "v_cndmask_b32 v239, v156, v239, vcc\n\t" \
  "v_cvt_pk_bf16_f32 v112, v96, v97\n\t" \
  "v_cvt_pk_bf16_f32 v114, v98, v99\n\t" \
  "v_cvt_pk_bf16_f32 v116, v100, v101\n\t" \
  "v_cvt_pk_bf16_f32 v118, v102, v103\n\t" \
  "buffer_store_dwordx4 v[112:115], v239, %[ws], s66 offen offset:0 sc1\n\t" \
  "buffer_store_dwordx4 v[116:119], v239, %[ws], s66 offen offset:64 sc1\n\t" \
  "buffer_store_dwordx4 v[112:115], v239, %[ws], s77 offen offset:0 sc1\n\t" \
  "buffer_store_dwordx4 v[116:119], v239, %[ws], s77 offen offset:64 sc1\n\t" \
  "buffer_store_dwordx4 v[120:123], v239, %[ws], s67 offen offset:0 sc1\n\t" \
  "buffer_store_dwordx4 v[124:127], v239, %[ws], s67 offen offset:64 sc1\n\t" \
  "s_branch .Ltl_ce\n\t" \
  ".p2align 12\n\t" \
  ".Lht1:\n\t" \
  "s_cbranch_execz .Lht1n\n\t" \
  "s_waitcnt vmcnt(0)\n\t" \
  ".Ltl_c1:\n\t" \
  "v_cmp_eq_u32 vcc, %[tag], v217\n\t" \
  "v_xor_b32 v153, %[tag], v219\n\t" \
  "v_bitop3_b32 v153, v153, v221, %[tag] bitop3:0xf6\n\t" \
  "v_cndmask_b32 v152, 0, v216, vcc\n\t" \
  "v_bitop3_b32 v153, v153, v223, %[tag] bitop3:0xf6\n\t" \
  "v_bitop3_b32 v153, v153, v225, %[tag] bitop3:0xf6\n\t" \
  "v_max_u32_dpp v152, v152, v152 quad_perm:[1,0,3,2] row_mask:0xf bank_mask:0xf\n\t" \
  "s_or_b32 s77, vcc_lo, s68\n\t" \
  "v_cmp_ne_u32 vcc, 0, v153\n\t" \
  "v_max_u32_dpp v152, v152, v152 quad_perm:[2,3,0,1] row_mask:0xf bank_mask:0xf\n\t" \
  "s_nop 1\n\t" \
  "v_max_u32_dpp v152, v152, v152 row_half_mirror row_mask:0xf bank_mask:0xf\n\t" \
  "s_nop 1\n\t" \
  "v_max_u32_dpp v152, v152, v152 row_mirror row_mask:0xf bank_mask:0xf\n\t" \
  "s_nop 1\n\t" \
  "v_max_u32_dpp v152, v152, v152 row_bcast:15 row_mask:0xa bank_mask:0xf\n\t" \
  "s_nop 0\n\t" \
  "v_readlane_b32 s70, v152, 31\n\t" \
  "s_max_u32 s70, s70, s69\n\t" \
  "s_andn2_b32 s63, 0x780, s70\n\t" \
  "s_lshl_b32 s63, s63, 7\n\t" \
  "s_andn2_b32 s64, 64, s70\n\t" \
  "s_lshl_b32 s64, s64, 6\n\t" \
  "s_or_b32 s64, s64, s63\n\t" \
  "s_xor_b32 s63, s64, s71\n\t" \
  "s_mov_b32 s71, s64\n\t" \
  "s_or_b32 vcc_lo, vcc_lo, vcc_hi\n\t" \
  "s_or_b32 s65, s63, vcc_lo\n\t" \
  "s_not_b32 s93, s77\n\t" \
  "s_or_b32 s63, s65, s93\n\t" \
  "s_cbranch_scc1 .Ltl_s1\n\t" \
  ".Ltl_k1:\n\t" \
  STP("s_memrealtime s[90:91]\n\t") \
  STP("s_memtime s[82:83]\n\t") \
  STP("s_waitcnt lgkmcnt(0)\n\t") \
  STP("s_or_b32 s90, s90, 1\n\t") \
  STP("v_writelane_b32 %[stv], s90, 48\n\t") \
  STP("v_writelane_b32 %[stv], s82, 49\n\t") \
  "s_cmp_eq_u32 s62, 6\n\t" \
  "s_cbranch_scc1 .Ltl_nf\n\t" \
  "v_dot2_f32_bf16 v96, v64, v218, 0\n\t" \
  "v_dot2_f32_bf16 v104, v254, v218, 0\n\t" \
  "v_dot2_f32_bf16 v97, v68, v218, 0\n\t" \
  "v_dot2_f32_bf16 v105, v180, v218, 0\n\t" \
  "v_dot2_f32_bf16 v98, v65, v218, 0\n\t" \
  "v_dot2_f32_bf16 v106, v176, v218, 0\n\t" \
  "v_dot2_f32_bf16 v99, v69, v218, 0\n\t" \
  "v_dot2_f32_bf16 v107, v181, v218, 0\n\t" \
  "v_dot2_f32_bf16 v100, v66, v218, 0\n\t" \
  "v_dot2_f32_bf16 v108, v177, v218, 0\n\t" \
  "v_dot2_f32_bf16 v101, v70, v218, 0\n\t" \
  "v_dot2_f32_bf16 v109, v182, v218, 0\n\t" \
  "v_dot2_f32_bf16 v102, v67, v218, 0\n\t" \
  "v_dot2_f32_bf16 v110, v178, v218, 0\n\t" \
  "v_dot2_f32_bf16 v103, v71, v218, 0\n\t" \
  "v_dot2_f32_bf16 v111, v183, v218, 0\n\t" \
  "v_dot2_f32_bf16 v96, v72, v220, v96\n\t" \
  "v_dot2_f32_bf16 v104, v179, v220, v104\n\t" \
  "v_dot2_f32_bf16 v97, v76, v220, v97\n\t" \
  "v_dot2_f32_bf16 v105, v188, v220, v105\n\t" \
  "v_dot2_f32_bf16 v98, v73, v220, v98\n\t" \
  "v_dot2_f32_bf16 v106, v184, v220, v106\n\t" \
  "v_dot2_f32_bf16 v99, v77, v220, v99\n\t" \
  "v_dot2_f32_bf16 v107, v189, v220, v107\n\t" \
  "v_dot2_f32_bf16 v100, v74, v220, v100\n\t" \
  "v_dot2_f32_bf16 v108, v185, v220, v108\n\t" \
  "v_dot2_f32_bf16 v101, v78, v220, v101\n\t" \
  "v_dot2_f32_bf16 v109, v190, v220, v109\n\t" \
  "v_dot2_f32_bf16 v102, v75, v220, v102\n\t" \
  "v_dot2_f32_bf16 v110, v186, v220, v110\n\t" \
  "v_dot2_f32_bf16 v103, v79, v220, v103\n\t" \
  "v_dot2_f32_bf16 v111, v191, v220, v111\n\t" \
  "v_dot2_f32_bf16 v96, v80, v222, v96\n\t" \
  "v_dot2_f32_bf16 v104, v187, v222, v104\n\t" \
  "v_dot2_f32_bf16 v97, v84, v222, v97\n\t" \
  "v_dot2_f32_bf16 v105, v196, v222, v105\n\t" \
  "v_dot2_f32_bf16 v98, v81, v222, v98\n\t" \
  "v_dot2_f32_bf16 v106, v192, v222, v106\n\t" \
  "v_dot2_f32_bf16 v99, v85, v222, v99\n\t" \
  "v_dot2_f32_bf16 v107, v197, v222, v107\n\t" \
  "v_dot2_f32_bf16 v100, v82, v222, v100\n\t" \
  "v_dot2_f32_bf16 v108, v193, v222, v108\n\t" \
  "v_dot2_f32_bf16 v101, v86, v222, v101\n\t" \
  "v_dot2_f32_bf16 v109, v198, v222, v109\n\t" \
  "v_dot2_f32_bf16 v102, v83, v222, v102\n\t" \
  "v_dot2_f32_bf16 v110, v194, v222, v110\n\t" \
  "v_dot2_f32_bf16 v103, v87, v222, v103\n\t" \
  "v_dot2_f32_bf16 v111, v199, v222, v111\n\t" \
  "v_dot2_f32_bf16 v96, v88, v224, v96\n\t" \
  "v_dot2_f32_bf16 v104, v195, v224, v104\n\t" \
  "v_dot2_f32_bf16 v97, v92, v224, v97\n\t" \
  "v_dot2_f32_bf16 v105, v204, v224, v105\n\t" \
  "v_dot2_f32_bf16 v98, v89, v224, v98\n\t" \
  "v_dot2_f32_bf16 v106, v200, v224, v106\n\t" \
  "v_dot2_f32_bf16 v99, v93, v224, v99\n\t" \
  "v_dot2_f32_bf16 v107, v205, v224, v107\n\t" \
  "v_dot2_f32_bf16 v100, v90, v224, v100\n\t" \
  "v_dot2_f32_bf16 v108, v201, v224, v108\n\t" \
  "v_dot2_f32_bf16 v101, v94, v224, v101\n\t" \
  "v_dot2_f32_bf16 v109, v206, v224, v109\n\t" \
  "v_dot2_f32_bf16 v102, v91, v224, v102\n\t" \
  "v_dot2_f32_bf16 v110, v202, v224, v110\n\t" \
  "v_dot2_f32_bf16 v103, v95, v224, v103\n\t" \
  "v_dot2_f32_bf16 v111, v207, v224, v111\n\t" \
  "v_permlane32_swap_b32 v96, v104\n\t" \
  "v_permlane32_swap_b32 v97, v105\n\t" \
  "v_permlane32_swap_b32 v98, v106\n\t" \
  "v_permlane32_swap_b32 v99, v107\n\t" \
  "v_permlane32_swap_b32 v100, v108\n\t" \
  "v_permlane32_swap_b32 v101, v109\n\t" \
  "v_permlane32_swap_b32 v102, v110\n\t" \
  "v_permlane32_swap_b32 v103, v111\n\t" \
  "v_pk_add_f32 v[96:97], v[96:97], v[104:105]\n\t" \
  "v_pk_add_f32 v[100:101], v[100:101], v[108:109]\n\t" \
  "v_pk_add_f32 v[98:99], v[98:99], v[106:107]\n\t" \
  "v_pk_add_f32 v[102:103], v[102:103], v[110:111]\n\t" \
  "v_permlane16_swap_b32 v96, v100\n\t" \
  "v_permlane16_swap_b32 v97, v101\n\t" \
  "v_permlane16_swap_b32 v98, v102\n\t" \
  "v_permlane16_swap_b32 v99, v103\n\t" \
  "v_add_f32 v96, v96, v100\n\t" \
  "v_add_f32 v97, v97, v101\n\t" \
  "v_add_f32 v98, v98, v102\n\t" \
  "v_add_f32 v99, v99, v103\n\t" \
  "v_add_f32_dpp v96, v96, v96 row_ror:8 row_mask:0xf bank_mask:0xf\n\t" \
  "v_add_f32_dpp v97, v97, v97 row_ror:8 row_mask:0xf bank_mask:0xf\n\t" \
  "v_add_f32_dpp v98, v98, v98 row_ror:8 row_mask:0xf bank_mask:0xf\n\t" \
  "v_add_f32_dpp v99, v99, v99 row_ror:8 row_mask:0xf bank_mask:0xf\n\t" \
  "v_add_f32_dpp v96, v96, v96 row_ror:4 row_mask:0xf bank_mask:0xf\n\t" \
  "v_add_f32_dpp v97, v97, v97 row_ror:4 row_mask:0xf bank_mask:0xf\n\t" \
  "v_add_f32_dpp v98, v98, v98 row_ror:4 row_mask:0xf bank_mask:0xf\n\t" \
  "v_add_f32_dpp v99, v99, v99 row_ror:4 row_mask:0xf bank_mask:0xf\n\t" \
  "v_add_f32_dpp v96, v96, v96 row_ror:2 row_mask:0xf bank_mask:0xf\n\t" \
  "v_add_f32_dpp v97, v97, v97 row_ror:2 row_mask:0xf bank_mask:0xf\n\t" \
  "v_add_f32_dpp v98, v98, v98 row_ror:2 row_mask:0xf bank_mask:0xf\n\t" \
  "v_add_f32_dpp v99, v99, v99 row_ror:2 row_mask:0xf bank_mask:0xf\n\t" \
  "ds_write_b128 v245, v[96:99] offset:8192\n\t" \
  "v_mov_b32 v169, v243\n\t" \
  "v_mov_b32 v171, v243\n\t" \
  "s_waitcnt lgkmcnt(0)\n\t" \
  "s_barrier\n\t" \
  "ds_read2st64_b32 v[96:97], v246 offset0:32 offset1:33\n\t" \
  "ds_read2st64_b32 v[98:99], v248 offset0:32 offset1:33\n\t" \
  "s_waitcnt lgkmcnt(0)\n\t" \
  "v_pk_add_f32 v[96:97], v[96:97], v[98:99]\n\t" \
  "s_nop 0\n\t" \
  "v_add_f32 v96, v96, v97\n\t" \
  "v_cvt_pk_bf16_f32 v96, v96, v96\n\t" \
  "v_lshlrev_b32 v97, 16, v96\n\t" \
  "v_lshlrev_b32 v96, 16, v96\n\t" \
  "s_nop 1\n\t" \
  "v_permlane16_swap_b32 v96, v97\n\t" \
  "v_mul_f32 v98, 0xbfb8aa3b, v96\n\t" \
  "v_exp_f32 v98, v98\n\t" \
  "s_nop 0\n\t" \
  "v_add_f32 v98, 1.0, v98\n\t" \
  "v_rcp_f32 v98, v98\n\t" \
  "s_nop 0\n\t" \
  "v_mul_f32 v96, v98, v96\n\t" \
  "v_cvt_pk_bf16_f32 v96, v96, v96\n\t" \
  "v_lshlrev_b32 v96, 16, v96\n\t" \
  "v_mul_f32 v96, v97, v96\n\t" \
  "s_nop 1\n\t" \
  "v_mov_b32_dpp v97, v96 quad_perm:[1,0,3,2] row_mask:0xf bank_mask:0xf\n\t" \
  "v_cvt_pk_bf16_f32 v96, v96, v97\n\t" \
  "s_nop 0\n\t" \
  "v_readlane_b32 s75, v96, 0\n\t" \
  "v_readlane_b32 s76, v96, 2\n\t" \
  "v_readlane_b32 s63, v96, 4\n\t" \
  "v_readlane_b32 s64, v96, 6\n\t" \
  "v_readlane_b32 s65, v96, 8\n\t" \
  "v_readlane_b32 s77, v96, 10\n\t" \
  "v_readlane_b32 s87, v96, 12\n\t" \
  "v_readlane_b32 s88, v96, 14\n\t" \
  "v_dot2_f32_bf16 v100, v240, s75, 0\n\t" \
  "v_dot2_f32_bf16 v101, v34, s75, 0\n\t" \
  "v_dot2_f32_bf16 v102, v32, s75, 0\n\t" \
  "v_dot2_f32_bf16 v103, v35, s75, 0\n\t" \
  "v_dot2_f32_bf16 v100, v33, s76, v100\n\t" \
  "v_dot2_f32_bf16 v101, v38, s76, v101\n\t" \
  "v_dot2_f32_bf16 v102, v36, s76, v102\n\t" \
  "v_dot2_f32_bf16 v103, v39, s76, v103\n\t" \
  "v_dot2_f32_bf16 v100, v37, s63, v100\n\t" \
  "v_dot2_f32_bf16 v101, v42, s63, v101\n\t" \
  "v_dot2_f32_bf16 v102, v40, s63, v102\n\t" \
  "v_dot2_f32_bf16 v103, v43, s63, v103\n\t" \
  "v_dot2_f32_bf16 v100, v41, s64, v100\n\t" \
  "v_dot2_f32_bf16 v101, v46, s64, v101\n\t" \
  "v_dot2_f32_bf16 v102, v44, s64, v102\n\t" \
  "v_dot2_f32_bf16 v103, v47, s64, v103\n\t" \
  "v_dot2_f32_bf16 v100, v45, s65, v100\n\t" \
  "v_dot2_f32_bf16 v101, v50, s65, v101\n\t" \
  "v_dot2_f32_bf16 v102, v48, s65, v102\n\t" \
  "v_dot2_f32_bf16 v103, v51, s65, v103\n\t" \
  "v_dot2_f32_bf16 v100, v49, s77, v100\n\t" \
  "v_dot2_f32_bf16 v101, v54, s77, v101\n\t" \
  "v_dot2_f32_bf16 v102, v52, s77, v102\n\t" \
  "v_dot2_f32_bf16 v103, v55, s77, v103\n\t" \
  "v_dot2_f32_bf16 v100, v53, s87, v100\n\t" \
  "v_dot2_f32_bf16 v101, v58, s87, v101\n\t" \
  "v_dot2_f32_bf16 v102, v56, s87, v102\n\t" \
  "v_dot2_f32_bf16 v103, v59, s87, v103\n\t" \
  "v_dot2_f32_bf16 v100, v57, s88, v100\n\t" \
  "v_dot2_f32_bf16 v101, v62, s88, v101\n\t" \
  "v_dot2_f32_bf16 v102, v60, s88, v102\n\t" \
  "v_dot2_f32_bf16 v103, v63, s88, v103\n\t" \
  "s_nop 0\n\t" \
  "v_cvt_pk_bf16_f32 v168, v100, v101\n\t" \
  "s_nop 0\n\t" \
  "v_cvt_pk_bf16_f32 v170, v102, v103\n\t" \
  "buffer_store_dwordx4 v[168:171], v247, %[ws], s85 offen sc1\n\t" \
  ".Ltl_nf:\n\t" \
  STP("s_memrealtime s[90:91]\n\t") \
  STP("s_memtime s[82:83]\n\t") \
  STP("s_waitcnt lgkmcnt(0)\n\t") \
  STP("s_or_b32 s90, s90, 1\n\t") \
  STP("v_writelane_b32 %[stv], s90, 50\n\t") \
  STP("v_writelane_b32 %[stv], s82, 51\n\t") \
  "s_cmp_lg_u32 s84, 0\n\t" \
  "s_cbranch_scc1 .Ltl_df\n\t" \
  "s_cmp_lg_u32 s78, 0\n\t" \
  "s_cbranch_scc1 .Ltl_o1\n\t" \
  "s_cmp_lt_u32 s74, 64\n\t" \
  "s_cbranch_scc1 .Ltl_o1\n\t" \
  ".Ltl_oe:\n\t" \
  "s_or_b32 s63, %[bid], %[wid]\n\t" \
  "s_cbranch_scc0 .Ltl_ep\n\t" \
  ".Ltl_ee:\n\t" \
  "s_cmp_eq_u32 s62, 6\n\t" \
  "s_cbranch_scc0 .Ltl_end\n\t" \
  "s_lshl_b32 s77, s79, 9\n\t" \
  "s_lshl_b32 s63, s80, 7\n\t" \
  "s_add_u32 s77, s77, s63\n\t" \
  "v_lshl_add_u32 v37, v157, 3, s77\n\t" \
  "s_add_u32 s63, s71, s61\n\t" \
  "s_add_u32 s63, s63, 262144\n\t" \
  "v_add_u32 v33, s63, v37\n\t" \
  "s_andn2_b32 s63, 0x700, s70\n\t" \
  "s_lshl_b32 s63, s63, 6\n\t" \
  "s_andn2_b32 s64, 0xc0, s70\n\t" \
  "s_lshl_b32 s64, s64, 5\n\t" \
  "s_add_u32 s63, s63, s64\n\t" \
  "s_add_u32 s63, s63, s61\n\t" \
  "s_add_u32 s63, s63, 1966080\n\t" \
  "v_and_b32 v34, 0x7ff, v37\n\t" \
  "v_add_u32 v34, s63, v34\n\t" \
  "s_and_b32 s77, s77, 0x7ff\n\t" \
  "s_add_u32 s77, s77, s61\n\t" \
  "s_lshl_b32 s63, %[wid], 15\n\t" \
  "s_add_u32 s77, s77, s63\n\t" \
  "v_and_b32 v36, 0x70, v242\n\t" \
  "v_lshl_add_u32 v32, v154, 14, v36\n\t" \
  "v_and_b32 v35, 24, v244\n\t" \
  "v_lshl_add_u32 v32, v35, 8, v32\n\t" \
  "s_add_u32 s60, s77, 1048576\n\t" \
  "s_add_u32 s65, s77, 1179648\n\t" \
  "s_add_u32 s66, s77, 1310720\n\t" \
  "s_add_u32 s67, s77, 1441792\n\t" \
  "s_add_u32 s68, s77, 1572864\n\t" \
  "s_add_u32 s73, s77, 1703936\n\t" \
  "s_add_u32 s74, s77, 1835008\n\t" \
  "s_lshl_b32 s63, s80, 12\n\t" \
  "s_add_u32 s63, s63, 147456\n\t" \
  "v_lshl_add_u32 v40, v157, 3, s63\n\t" \
  "ds_read_b64 v[40:41], v40 offset:2944\n\t" \
  "s_ashr_i32 s63, s70, 31\n\t" \
  "s_orn2_b32 s63, 0x80000000, s63\n\t" \
  "s_xor_b32 s63, s63, s70\n\t" \
  "s_and_b32 s63, s63, 0xffff0000\n\t" \
  "v_mov_b32 v38, 0xbfb8aa3b\n\t" \
  "v_mul_f32 v38, s63, v38\n\t" \
  "v_exp_f32 v38, v38\n\t" \
  "v_lshrrev_b32 v37, 1, v37\n\t" \
  "v_add_f32 v38, 1.0, v38\n\t" \
  "v_rcp_f32 v38, v38\n\t" \
  "s_andn2_b32 s64, 0x7ff, s70\n\t" \
  "s_lshl_b32 s64, s64, 11\n\t" \
  "v_cvt_pk_bf16_f32 v38, v38, v38\n\t" \
  "v_lshlrev_b32 v38, 16, v38\n\t" \
  "v_lshrrev_b32 v153, 4, v244\n\t" \
  "v_lshl_add_u32 v36, v153, 2, v36\n\t" \
  "v_lshl_add_u32 v36, %[wid], 7, v36\n\t" \
  "v_and_b32 v35, 8, v244\n\t" \
  "v_lshl_add_u32 v36, v35, 7, v36\n\t" \
  "v_lshlrev_b32 v35, 3, v157\n\t" \
  "v_add_u32 v35, 0x3000, v35\n\t" \
  "v_min_u32 v153, 1, v153\n\t" \
  "v_lshl_or_b32 v37, v153, 31, v37\n\t" \
  "s_mov_b32 s75, 0x3f80\n\t" \
  "s_mov_b32 s76, 0x3f800000\n\t" \
  "s_mov_b32 s72, 0\n\t" \
  "buffer_load_dwordx2 v[42:43], v33, %[ws], 0 offen sc1\n\t" \
  "buffer_load_dwordx2 v[44:45], v34, %[ws], 0 offen sc1\n\t" \
  "buffer_load_dwordx4 v[0:3], v32, %[ws], s60 offen sc1\n\t" \
  "buffer_load_dwordx4 v[4:7], v32, %[ws], s65 offen sc1\n\t" \
  "buffer_load_dwordx4 v[8:11], v32, %[ws], s66 offen sc1\n\t" \
  "buffer_load_dwordx4 v[12:15], v32, %[ws], s67 offen sc1\n\t" \
  "buffer_load_dwordx4 v[16:19], v32, %[ws], s68 offen sc1\n\t" \
  "buffer_load_dwordx4 v[20:23], v32, %[ws], s73 offen sc1\n\t" \
  "buffer_load_dwordx4 v[24:27], v32, %[ws], s74 offen sc1\n\t" \
  "s_waitcnt vmcnt(0)\n\t" \
  ".Ltl_c2:\n\t" \
  "v_xor_b32 v152, %[tag], v43\n\t" \
  "v_bitop3_b32 v152, v152, v45, %[tag] bitop3:0xf6\n\t" \
  "v_bitop3_b32 v152, v152, v1, %[tag] bitop3:0xf6\n\t" \
  "v_bitop3_b32 v152, v152, v3, %[tag] bitop3:0xf6\n\t" \
  "v_bitop3_b32 v152, v152, v5, %[tag] bitop3:0xf6\n\t" \
  "v_bitop3_b32 v152, v152, v7, %[tag] bitop3:0xf6\n\t" \
  "v_bitop3_b32 v152, v152, v9, %[tag] bitop3:0xf6\n\t" \
  "v_bitop3_b32 v152, v152, v11, %[tag] bitop3:0xf6\n\t" \
  "v_bitop3_b32 v152, v152, v13, %[tag] bitop3:0xf6\n\t" \
  "v_bitop3_b32 v152, v152, v15, %[tag] bitop3:0xf6\n\t" \
  "v_bitop3_b32 v152, v152, v17, %[tag] bitop3:0xf6\n\t" \
  "v_bitop3_b32 v152, v152, v19, %[tag] bitop3:0xf6\n\t" \
  "v_bitop3_b32 v152, v152, v21, %[tag] bitop3:0xf6\n\t" \
  "v_bitop3_b32 v152, v152, v23, %[tag] bitop3:0xf6\n\t" \
  "v_bitop3_b32 v152, v152, v25, %[tag] bitop3:0xf6\n\t" \
  "v_bitop3_b32 v152, v152, v27, %[tag] bitop3:0xf6\n\t" \
  "v_cmp_ne_u32 vcc, 0, v152\n\t" \
  "s_cbranch_vccnz .Ltl_s2\n\t" \
  ".Ltl_k2:\n\t" \
  "v_dot2_f32_bf16 v48, v0, s75, 0\n\t" \
  "v_dot2_f32_bf16 v49, v0, s76, 0\n\t" \
  "v_dot2_f32_bf16 v50, v2, s75, 0\n\t" \
  "v_dot2_f32_bf16 v51, v2, s76, 0\n\t" \
  "v_dot2_f32_bf16 v48, v4, s75, v48\n\t" \
  "v_dot2_f32_bf16 v49, v4, s76, v49\n\t" \
  "v_dot2_f32_bf16 v50, v6, s75, v50\n\t" \
  "v_dot2_f32_bf16 v51, v6, s76, v51\n\t" \
  "v_dot2_f32_bf16 v48, v8, s75, v48\n\t" \
  "v_dot2_f32_bf16 v49, v8, s76, v49\n\t" \
  "v_dot2_f32_bf16 v50, v10, s75, v50\n\t" \
  "v_dot2_f32_bf16 v51, v10, s76, v51\n\t" \
  "v_dot2_f32_bf16 v48, v12, s75, v48\n\t" \
  "v_dot2_f32_bf16 v49, v12, s76, v49\n\t" \
  "v_dot2_f32_bf16 v50, v14, s75, v50\n\t" \
  "v_dot2_f32_bf16 v51, v14, s76, v51\n\t" \
  "v_dot2_f32_bf16 v48, v16, s75, v48\n\t" \
  "v_dot2_f32_bf16 v49, v16, s76, v49\n\t" \
  "v_dot2_f32_bf16 v50, v18, s75, v50\n\t" \
  "v_dot2_f32_bf16 v51, v18, s76, v51\n\t" \
  "v_dot2_f32_bf16 v48, v20, s75, v48\n\t" \
  "v_dot2_f32_bf16 v49, v20, s76, v49\n\t" \
  "v_dot2_f32_bf16 v50, v22, s75, v50\n\t" \
  "v_dot2_f32_bf16 v51, v22, s76, v51\n\t" \
  "v_dot2_f32_bf16 v48, v24, s75, v48\n\t" \
  "v_dot2_f32_bf16 v49, v24, s76, v49\n\t" \
  "v_dot2_f32_bf16 v50, v26, s75, v50\n\t" \
  "v_dot2_f32_bf16 v51, v26, s76, v51\n\t" \
  "s_nop 1\n\t" \
  "v_permlane32_swap_b32 v48, v50\n\t" \
  "v_permlane32_swap_b32 v49, v51\n\t" \
  "v_add_f32 v48, v48, v50\n\t" \
  "v_add_f32 v49, v49, v51\n\t" \
  "v_and_b32 v43, 0xffff0000, v42\n\t" \
  "v_lshlrev_b32 v42, 16, v42\n\t" \
  "v_permlane16_swap_b32 v48, v49\n\t" \
  "v_add_f32 v48, v48, v49\n\t" \
  "v_and_b32 v45, 0xffff0000, v44\n\t" \
  "v_lshlrev_b32 v44, 16, v44\n\t" \
  "v_add_f32_dpp v48, v48, v48 row_ror:8 row_mask:0xf bank_mask:0xf\n\t" \
  "ds_write_b32 v36, v48 offset:12288\n\t" \
  "s_cmp_lg_u32 %[wid], 0\n\t" \
  "s_waitcnt lgkmcnt(0)\n\t" \
  "s_barrier\n\t" \
  "s_cbranch_scc1 .Ltl_end\n\t" \
  "ds_read2_b64 v[0:3], v35 offset1:16\n\t" \
  "ds_read2_b64 v[4:7], v35 offset0:32 offset1:48\n\t" \
  "s_waitcnt lgkmcnt(1)\n\t" \
  "v_pk_add_f32 v[0:1], v[0:1], v[2:3]\n\t" \
  "s_waitcnt lgkmcnt(0)\n\t" \
  "v_pk_add_f32 v[4:5], v[4:5], v[6:7]\n\t" \
  "s_nop 0\n\t" \
  "v_pk_add_f32 v[0:1], v[0:1], v[4:5]\n\t" \
  "s_nop 0\n\t" \
  "v_cvt_pk_bf16_f32 v0, v0, v1\n\t" \
  "v_and_b32 v1, 0xffff0000, v0\n\t" \
  "v_lshlrev_b32 v0, 16, v0\n\t" \
  "v_mul_f32 v0, v38, v0\n\t" \
  "v_mul_f32 v1, v38, v1\n\t" \
  "v_cvt_pk_bf16_f32 v0, v0, v1\n\t" \
  "v_and_b32 v1, 0xffff0000, v0\n\t" \
  "v_lshlrev_b32 v0, 16, v0\n\t" \
  "v_add_f32 v0, v0, v42\n\t" \
  "v_add_f32 v1, v1, v43\n\t" \
  "v_cvt_pk_bf16_f32 v0, v0, v1\n\t" \
  "v_and_b32 v1, 0xffff0000, v0\n\t" \
  "v_lshlrev_b32 v0, 16, v0\n\t" \
  "v_mul_f32 v0, v0, v40\n\t" \
  "v_mul_f32 v1, v1, v41\n\t" \
  "v_cvt_pk_bf16_f32 v0, v0, v1\n\t" \
  "v_and_b32 v1, 0xffff0000, v0\n\t" \
  "v_lshlrev_b32 v0, 16, v0\n\t" \
  "v_add_f32 v0, v0, v44\n\t" \
  "v_add_f32 v1, v1, v45\n\t" \
  "v_cvt_pk_bf16_f32 v0, v0, v1\n\t" \
  "buffer_store_dword v0, v37, s[96:99], s64 offen\n\t" \
  "s_branch .Ltl_end\n\t" \
  ".Ltl_s1:\n\t" \
  "s_sleep 1\n\t" \
  "buffer_load_dwordx2 v[216:217], v235, %[ws], 0 offen sc1\n\t" \
  "buffer_load_dwordx4 v[218:221], v236, %[ws], s71 offen sc1\n\t" \
  "buffer_load_dwordx4 v[222:225], v236, %[ws], s71 offen offset:16 sc1\n\t" \
  "s_add_u32 s72, s72, 1\n\t" \
  "s_cmp_lt_u32 s72, %[cap]\n\t" \
  "s_waitcnt vmcnt(0)\n\t" \
  "s_cbranch_scc1 .Ltl_c1\n\t" \
  "buffer_store_dword v243, off, %[ws], 0 offset:256 sc1\n\t" \
  "s_branch .Ltl_k1\n\t" \
  ".Ltl_o1:\n\t" \
  "s_cmp_eq_u32 s70, s69\n\t" \
  "s_cselect_b32 s63, 0x80000000, 0\n\t" \
  "s_xor_b32 s77, s78, 0x80000000\n\t" \
  "v_or_b32 v152, s63, v238\n\t" \
  "v_or_b32 v153, s77, v238\n\t" \
  "v_cmp_gt_i32 vcc, 0, v228\n\t" \
  "s_mov_b32 s64, 0\n\t" \
  "s_nop 0\n\t" \
  "v_cndmask_b32 v228, v153, v152, vcc\n\t" \
  "buffer_store_dwordx4 v[112:115], v228, s[96:99], s64 offen sc1\n\t" \
  "v_cmp_gt_i32 vcc, 0, v229\n\t" \
  "s_mov_b32 s64, 32768\n\t" \
  "s_nop 0\n\t" \
  "v_cndmask_b32 v229, v153, v152, vcc\n\t" \
  "buffer_store_dwordx4 v[116:119], v229, s[96:99], s64 offen sc1\n\t" \
  "v_cmp_gt_i32 vcc, 0, v230\n\t" \
  "s_mov_b32 s64, 65536\n\t" \
  "s_nop 0\n\t" \
  "v_cndmask_b32 v230, v153, v152, vcc\n\t" \
  "buffer_store_dwordx4 v[120:123], v230, s[96:99], s64 offen sc1\n\t" \
  "v_cmp_gt_i32 vcc, 0, v231\n\t" \
  "s_mov_b32 s64, 98304\n\t" \
  "s_nop 0\n\t" \
  "v_cndmask_b32 v231, v153, v152, vcc\n\t" \
  "buffer_store_dwordx4 v[124:127], v231, s[96:99], s64 offen sc1\n\t" \
  "s_branch .Ltl_oe\n\t" \
  ".Ltl_df:\n\t" \
  "s_mov_b32 s78, 0\n\t" \
  "s_cmp_eq_u32 s70, s69\n\t" \
  "s_cselect_b32 s74, s74, 0x100\n\t" \
  "s_branch .Ltl_h2\n\t" \
  ".Ltl_ep:\n\t" \
  "s_mov_b32 s63, 2490368\n\t" \
  "buffer_store_dword v243, v249, %[ws], s63 offen sc1\n\t" \
  "s_branch .Ltl_ee\n\t" \
  ".Ltl_s2:\n\t" \
  "s_sleep 1\n\t" \
  "buffer_load_dwordx2 v[42:43], v33, %[ws], 0 offen sc1\n\t" \
  "buffer_load_dwordx2 v[44:45], v34, %[ws], 0 offen sc1\n\t" \
  "buffer_load_dwordx4 v[0:3], v32, %[ws], s60 offen sc1\n\t" \
  "buffer_load_dwordx4 v[4:7], v32, %[ws], s65 offen sc1\n\t" \
  "buffer_load_dwordx4 v[8:11], v32, %[ws], s66 offen sc1\n\t" \
  "buffer_load_dwordx4 v[12:15], v32, %[ws], s67 offen sc1\n\t" \
  "buffer_load_dwordx4 v[16:19], v32, %[ws], s68 offen sc1\n\t" \
  "buffer_load_dwordx4 v[20:23], v32, %[ws], s73 offen sc1\n\t" \
  "buffer_load_dwordx4 v[24:27], v32, %[ws], s74 offen sc1\n\t" \
  "s_add_u32 s72, s72, 1\n\t" \
  "s_cmp_lt_u32 s72, %[cap]\n\t" \
  "s_waitcnt vmcnt(0)\n\t" \
  "s_cbranch_scc1 .Ltl_c2\n\t" \
  "buffer_store_dword v243, off, %[ws], 0 offset:256 sc1\n\t" \
  "s_branch .Ltl_k2\n\t" \
  ".Ltl_end:\n\t" \
  ""
#define TL_CLOB "v208", "v209", "v210", "v211", "v212", "v213", "v214", "v215", "v216", "v217", "v218", "v219", "v220", "v221", "v222", "v223", "v224", "v225", "v226", "v227", "v228", "v229", "v230", "v231", "v232", "v233", "v234", "v235", "v236", "v238", "v239", "v245", "v246", "v247", "v248", "v249", "v250", "s60", "s61", "s62", "s63", "s64", "s65", "s66", "s67", "s68", "s69", "s70", "s71", "s72", "s73", "s74", "s75", "s76", "s77", "s78", "s79", "s80", "s81", "s82", "s83", "s84", "s85", "s86", "s90", "s91", "s92", "s93", "s94", "s96", "s97", "s98", "s99", "vcc", "scc", "memory"
static_assert(W_EPOCH == 2490368 && W_GKEY == 65536 && W_CANDH == 262144 && W_STAT == 524288 && W_YG == 1048576 && W_CANDX == 1966080);
struct WoP { srd_t srd; unsigned wv[2], m, hl, base, wa; };
struct W4C {
  KVP kv;
  unsigned pf1, pf2, pf4, cto, po, px, vo, eso, hl, hl18, frb, xcb, xso, ss, mbp, gvo, l16, gvm, vs, c3;
  WoP wo; u16* out;
  srd_t sG, sU, sD, sX, sR, sM;
  unsigned gv, wa0, fw2, tagv, fbv, tlb, pre, otb, na, gra, fl; u32x32 q[4], up; f32x32 wm; u32x16 o3;
};
struct Ptrs { const u16 *wo, *w_r, *wg, *wu, *wd; u16* out; };
DI void attn_setup(const Ctx& k, W4C& c, const u16* kc, const u16* vc, const u16* pm, srd_t srdx, const Ptrs& a) {
  const unsigned wid = k.wid, fb = k.xcd * 28 + ffn_j(k);
  c.kv.sofk = KRS * 16u * wid;
  const size_t kvoff = ((size_t)k.kvh * SKV + (SKV - WIN)) * DH;
  c.kv.srdK = mk_srd(kc + kvoff, WIN * DH * 2); c.kv.srdV = mk_srd(vc + kvoff, WIN * DH * 2);
  const unsigned go = wid * 256 * FF + fb * 16, nG = (DM * FF - go) * 2;
  c.sG = mk_srd(a.wg + go, nG); c.sU = mk_srd(a.wu + go, nG);
  c.sM = mk_srd(pm + 2048 + k.xcd * 512, DM * 6 * DM * 2 - 4096 - k.xcd * 1024);
  c.wo.srd = mk_srd(a.wo, QD * DM * 2); c.sX = srdx;
  c.sD = mk_srd(a.wd + (size_t)fb * 16 * DM, ffn_idle(k) ? 0u : 32768u); c.sR = mk_srd(a.w_r, DM * 2);
  c.out = a.out;
  asm volatile("" : "+s"(c.out), "+s"(c.kv.srdK), "+s"(c.kv.srdV), "+s"(c.sG), "+s"(c.sU), "+s"(c.sM), "+s"(c.wo.srd), "+s"(c.sD), "+s"(c.sR));
  c.wo.m = wid * 4096; c.wo.base = (k.kvh * 1024 + k.hl * 128 + wid * 16) * (DM * 2) + k.hl * 256;
  c.pf1 = (k.kvh * 1024 + k.j * 32 + wid * 8) * (DM * 2) + (k.xcd & 1) * 8192;
  c.pf4 = k.j * (8 * FF * 2) - ffn_j(k) * 32;
}
DI void attention(Ctx& k, H4& g, W4C& c STAMP_ARG) {
  unsigned nb;
  asm volatile(AT_ASM
               : "+{v[0:31]}"(g.q[0]), "+{v[32:63]}"(g.q[1]), "+{v[64:95]}"(g.q[2]), "+{v[96:127]}"(g.q[3]), [pfd] "+{v246}"(g.pfd), "=&{v[128:159]}"(c.wm), "=&{v[160:175]}"(c.o3), "+{v224}"(c.kv.vksrc[0]), "+{v225}"(c.kv.vksrc[1]), "+{v226}"(c.kv.vksrc[2]), "+{v227}"(c.kv.vksrc[3]), "=&{v[176:207]}"(c.up),
                 "=&{v234}"(c.wo.wa), "=&{v213}"(c.gra), "+{v245}"(g.fvo), "=&{v243}"(c.tagv), "=&{v254}"(c.fbv),
                 "=&{v242}"(c.l16), "+{v247}"(c.tlb), "=&{v252}"(c.vs), [na] "=&s"(c.na), [nb] "=&s"(nb), [eso] "+s"(c.eso), [xcb] "+s"(c.xcb), [frb] "+s"(c.frb), [fw2] "+s"(c.fw2) AT_STV
               : "{v220}"(c.kv.vkb[0]), "{v221}"(c.kv.vkb[1]), "{v222}"(c.kv.vkb[2]), "{v223}"(c.kv.vkb[3]),
                 "{v228}"(c.kv.vvb[0]), "{v229}"(c.kv.vvb[1]), "{v230}"(c.kv.vvsrc[0]), "{v231}"(c.kv.vvsrc[1]), "{v232}"(c.gv), "{v233}"(c.gvm), "{v240}"(c.wo.wv[0]), "{v241}"(c.wo.wv[1]), "{v244}"(c.wa0), "{v248}"(c.c3),
                 [ws] "s"(k.ws), [sK] "s"(c.kv.srdK), [sV] "s"(c.kv.srdV), [sG] "s"(c.sG), [sU] "s"(c.sU), [wo] "s"(c.wo.srd), [sM] "s"(c.sM), [sD] "s"(c.sD), [sR] "s"(c.sR),
                 [m] "s"(c.wo.m), [sofk] "s"(c.wo.m), [hl18] "s"(c.hl18), [wob] "s"(c.wo.base), [pf2] "s"(c.pf2), AT_PF1 [pf4] "s"(c.pf4),
                 [tag] "s"(k.tag), [cap] "i"(kSpinCap), [cto] "s"(c.cto), [po] "s"(c.po), [px] "s"(c.px), [vo] "s"(c.vo), [pm] "s"(k.pm), [ss] "s"(c.ss), [gvo] "s"(c.gvo)
               : AT_CLOB, ACLOB_LO, ACLOB_HI);
  k.nb = nb; c.fl = g.fvo;
#pragma unroll
  for (int i = 0; i < 4; ++i) c.q[i] = g.q[i];
  GAP();
}
constexpr unsigned SCR = 9 * UNIT;
#define STAMPF(I) do { } while (0)
#define FQ_STR2(x) #x
#define FQ_STR(x) FQ_STR2(x)
#define FQ_CAP FQ_STR(SPINCAP)
DI bool tags2(const u32x4& a, unsigned tag) { return (a[1] == tag) & (a[3] == tag); }
DI void bcast4(float v, float (&o)[4]) {
  auto a = __builtin_amdgcn_permlane32_swap(__float_as_uint(v), __float_as_uint(v), false, false);
  auto b = __builtin_amdgcn_permlane16_swap(a[0], a[0], false, false);
  auto c = __builtin_amdgcn_permlane16_swap(a[1], a[1], false, false);
  o[0] = __uint_as_float(b[0]); o[1] = __uint_as_float(b[1]); o[2] = __uint_as_float(c[0]); o[3] = __uint_as_float(c[1]);
}
DI void x_raw2(unsigned m0, srd_t srd, unsigned v0, unsigned v1, unsigned so) {
  asm volatile("s_mov_b32 m0, %0\n\ts_nop 0\n\tbuffer_load_dwordx4 %1, %3, %4 offen lds\n\tbuffer_load_dwordx4 %2, %3, %4 offen offset:1024 lds"
               :: "s"(m0), "v"(v0), "v"(v1), "s"(srd), "s"(so) : "memory");
}
DI void w_touch2(H4& g, const u16* wq, unsigned base, unsigned t) {
  touch(g.pfd, wq, base + ((g.rot + t) & 15u) * kWT);
}
#define TB0 6u
#define TBN 6u
#define SE 0
#define F1 0
#define FT 0
#define MK 0
#define PGA 0
#define WT 0
#define LT 0
#define EL 0
#define PT0 0
#define XH 0
#define S3 0
#define X1L 0
#define PBK 1
#define IL 1
#define GE 0
#define PPOS 2
DI void front(Ctx& k, H4& g, W4C& c STAMP_ARG) {
  argp_t ap = args();
  const u16 *px = ap->x, *kc = ap->kc, *vc = ap->vc, *wqp = ap->wq, *pc_ = ap->c, *pm = ap->w_mod; char* pw = ap->ws;
  GAP();
  const unsigned lane = lane_id(), wid = k.wid, tid = wid * 64 + lane;
  g.pfd = 0;
  {
    unsigned long long pc; unsigned len;
    asm volatile("s_getpc_b64 %0\n.Lfz_a:\n\ts_mov_b32 %1, .Lfz_z-.Lfz_a-4" : "=s"(pc), "=s"(len));
    touch(g.pfd, (const void*)pc, min(((lane * 4 + wid) * 32 + k.j) * 128, len));
  }
  GAP();
  k.ws = mk_srd((char*)(((uintptr_t)pw + 0x1fffff) & ~(uintptr_t)0x1fffff), W_END);
  const srd_t srdx = mk_srd(px, S * DM * 2), srdm = mk_srd(pm, DM * 6 * DM * 2), srdc = mk_srd(pc_, DM * 2);
  asm volatile("" : "+s"(kc), "+s"(vc));
  const unsigned head = k.kvh * 8 + k.hl, row64 = k.rblk * 256 + k.kvh * 64, rot = k.kvh * 4, u0 = k.hl >> 1, kt = rot + ((u0 + wid) & 3), k0 = k.hl & 1, cq = k.kvh ^ 3;
  g.srdW = mk_srd(wqp + head * DH, (DM * QD - head * DH) * 2); g.rot = rot;
  u32x32 xa;
  const unsigned st1 = W_ST1 + (k.rblk << 12);
  u32x4 xs[4];
  const unsigned cbk = (k.xcd & 1) * 32 + k.j, kp = k.kvh;
  const unsigned s3 = S3 && (k.j & 14) == 6, od = s3 & k.j, rg = tid >> (2 + s3), cm = 3 + 4 * s3;
  u32x4 wm[4];
  u32x2 cv = ald8<0>(srdc, rg * 8, kp * 512 + od * 256);
  { const unsigned vo = rg * (4 * 6 * DM * 2) + (tid & cm) * 16, so = kp * (256 * 6 * DM * 2) + cbk * 64 + od * (128 * 6 * DM * 2 - 64);
#pragma unroll
    for (int i = 0; i < 4; ++i) wm[i] = ald16<0>(srdm, vo, so + i * (6 * DM * 2)); }
  unsigned E = ald4<0, true>(k.ws, EL == 0 || lane == 0 ? W_EPOCH : lane == 1 ? W_HT + ((k.rblk & 3) << 20) : lane == 2 ? st1 + (k.kvh * 8 + k.hl) * 128 : W_FLAGH + (k.rblk * 4 + wid) * 128, 0u);
  { const unsigned so = (row64 + k.hl * 8 + 2 * wid) * (DM * 2);
    xs[0] = ald16<0>(srdx, lane * 16, so); xs[1] = ald16<1024>(srdx, lane * 16, so); xs[2] = ald16<2048>(srdx, lane * 16, so); xs[3] = ald16<3072>(srdx, lane * 16, so); }
  unsigned vx = (lane >> 3) * (DM * 2) + (((lane & 7) ^ (lane >> 4)) << 4), vx1 = (vx ^ 64) + 15360, sl[4];
  g.mX = k.ldsb + wid * 8192;
#pragma unroll
  for (int u = 0; u < 4; ++u) sl[u] = (k.rblk * 256 + wid * 16) * (DM * 2) + (rot + ((u0 + u) & 3)) * 128;
#define XRAW(u, rb) x_raw2(g.mX + (u) * GSLOT + (rb) * 2048, srdx, vx, vx1, sl[u] + (((rb) ^ cq) << 17))
  const unsigned hx = XH ? 2 * k0 : 0;
  if (!(LT & 2)) { XRAW(0, hx); XRAW(0, hx + 1); }
  const unsigned tw2 = (k.j * 2 + (wid >> 1)) * (QD * 2) + k.kvh * 2048 + (lane & 7) * 256 + (wid & 1) * 128;
  if (!(LT & 1)) w_touch2(g, wqp, tw2, (lane >> 3) & 3);
  constexpr int NL = (LT & 1) + 2 * (LT & 2);
  GAP();
  g.mW = k.ldsb + 32768 + wid * 4096;
#define X1() do { XRAW(1, 0); XRAW(1, 1); XRAW(1, 2); XRAW(1, 3); } while (0)
#define X01() do { XRAW(0, 2 - hx); XRAW(0, 3 - hx); if (!X1L) X1(); } while (0)
#define X2() do { XRAW(2, 0); XRAW(2, 1); XRAW(2, 2); XRAW(2, 3); } while (0)
  const unsigned tb = k.ldsb + SCR + 1024;
  u32x16 wbs; u32x8 vw, ma; u32x4 mb; const u16* kvt; unsigned pso, fw, w2, w3, gso, gpo;
  auto setup_w = [&]() {
    w_voffs(g.voffW, QD, wid, lane, k0);
    gso = st1 + (wid * 16 + (lane & 15)) * 16; gpo = W_MODA + ((lane >> 5) * 1024 + kt * 64 + (lane & 31) * 2) * 8;
    asm volatile("" : W4(g.voffW), "+v"(gso), "+v"(gpo));
    GAP();
  };
  auto setup_b = [&]() {
    g.voffX = wid * 8192 + ((lane >> 4) ^ cq) * 256 + (lane & 15) * 16; g.soX = W_HT + ((k.rblk & 3) << 20) + (((k.xcd ^ wid) & 1) << 13) + wid * 8192; g.fo = W_FLAGH + (k.rblk * 4 + wid) * 128;
    h4_init_addr(g, k.ldsb, wid, lane, k0);
    const unsigned xr = g.mX + (lane & 15) * 128 + (((4 * k0 + (lane >> 4)) ^ ((lane >> 1) & 7)) << 4);
    vw = u32x8{g.voffW[0], g.voffW[1], g.voffW[2], g.voffW[3], xr, xr ^ 64, xr + 2 * GSLOT, (xr ^ 64) + 2 * GSLOT};
#pragma unroll
    for (int i = 0; i < 8; ++i) { wbs[i] = g.wb[0][i]; wbs[8 + i] = g.wb[1][i]; }
    ma = u32x8{tb + (lane >> 4) * 32, g.voffX, (lane & 31) * 4, 0u, vx, vx1, g.xb1,
               WT == 1 ? tw2 + ((rot + 4 + (k.xcd & 1) + 2 * min(lane >> 3, 5u)) & 15u) * kWT : WT == 2 ? tw2 + ((rot + 4 + (k.xcd & 1) + 2 * min(lane >> 3, 2u)) & 15u) * kWT
                       : tw2 - (lane & 7) * 256 + (1 + 4 * (lane & 1)) * 256 + ((rot + 4 + min(lane >> 1, 11u)) & 15u) * kWT};
    mb = u32x4{WT == 2 ? tw2 + ((rot + 10 + (k.xcd & 1) + 2 * min(lane >> 3, 2u)) & 15u) * kWT : (tw2 & ~(((lane & 3) == 1) ? 256u : 0u)) + ((rot + TB0 + min(lane >> 3, TBN - 1u)) & 15u) * kWT, ((k.j & 15) * 64 + wid * 16 + (lane & 15)) * 128, 0u, W_SIL + k.bid * 512 + tid * 2};
    kvt = ((k.j & 16) ? vc : kc) + ((size_t)k.kvh * SKV + (SKV - WIN)) * DH;
    pso = g.soX + (rot + u0) * kXT + cq * 256 - GSLOT - k.ldsb; fw = g.fo + head * 4; w2 = (rot + ((u0 + 2) & 3)) * kWT; w3 = (rot + ((u0 + 3) & 3)) * kWT;
    asm volatile("" : "+{v[184:199]}"(wbs), "+{v[200:207]}"(vw), "+{v[208:215]}"(ma), "+{v[216:219]}"(mb), "+s"(kvt), "+s"(pso), "+s"(fw), "+s"(w2), "+s"(w3));
    GAP();
  };
  if constexpr (SE & 2) { setup_w(); setup_b(); }
  unsigned tag;
  HOP_GO(2, wid);
  unsigned la = k.ldsb + SCR + wid * 256 + (4 * (lane & 7) + 2 * (lane >> 5) + ((lane >> 4) & 1)) * 8 + (lane & 8) * 512, lr = k.ldsb + SCR + lane * 8;
  unsigned pa_ = (W_MODA + od * (W_MODC - W_MODA - 256) + (kp * 2048 + cbk * 32 + lane * 2) * 8) | (lane < (16u << s3) ? 0u : 0x80000000u);
  unsigned ps_ = (st1 + (k.kvh * 64 + k.hl * 8 + 2 * wid + (lane >> 5)) * 16) | ((lane & 31) == 0 ? 0u : 0x80000000u);
  asm volatile("" : "+v"(la), "+v"(lr), "+v"(pa_), "+v"(ps_));
  GAP();
  asm volatile("s_waitcnt vmcnt(%1)" : "+v"(cv) : "i"(14 + PT0 - NL) : "memory");
  unsigned sc[2];
#pragma unroll
  for (int i = 0; i < 2; ++i) { const float a = lo16f(cv[i]), b = hi16f(cv[i]); sc[i] = cvtpk(a * __builtin_amdgcn_rcpf(1.f + __expf(-a)), b * __builtin_amdgcn_rcpf(1.f + __expf(-b))); }
  asm volatile("" : "+v"(sc[0]), "+v"(sc[1]));
  GAP();
  STAMPF(27);
  asm volatile("s_waitcnt vmcnt(%4)" : W4(wm) : "i"(10 + PT0 - NL) : "memory");
  STAMP(1);
  if (LT & 2) { XRAW(0, 0); XRAW(0, 1); }
  if (LT & 1) w_touch2(g, wqp, tw2, (lane >> 3) & 3);
  GAP();
  {
    float a[8];
#pragma unroll
    for (int d = 0; d < 4; ++d) {
      a[2 * d] = dot2(plo(wm[2][d], wm[3][d]), sc[1], dot2z(plo(wm[0][d], wm[1][d]), sc[0]));
      a[2 * d + 1] = dot2(phi(wm[2][d], wm[3][d]), sc[1], dot2z(phi(wm[0][d], wm[1][d]), sc[0]));
    }
    float c0 = fold16(fold32(a[0], a[4]), fold32(a[2], a[6])), c1 = fold16(fold32(a[1], a[5]), fold32(a[3], a[7]));
    c0 += dpp_f<0x128>(c0); c1 += dpp_f<0x128>(c1);
    if (S3) { const float kf = s3 ? 0.f : 1.f; c0 = fmaf(dpp_f<0x124>(c0), kf, c0); c1 = fmaf(dpp_f<0x124>(c1), kf, c1); }
    else    { c0 += dpp_f<0x124>(c0); c1 += dpp_f<0x124>(c1); }
    lds_st(la, f32x2{c0, c1});
    asm volatile("s_waitcnt vmcnt(%1)" : "+v"(E) : "i"(9 + PT0) : "memory");
    tag = ((rfl(E) | TAGM) + TAGC) & ~TAGM;
    k.tag = tag;
    unsigned tv = tag;
    asm volatile("s_waitcnt lgkmcnt(0)\n\ts_barrier" : "+v"(tv) :: "memory");
    if (wid == 0) {
      const f32x2 s = (lds_ld<f32x2>(lr) + lds_ld<f32x2>(lr + 256)) + (lds_ld<f32x2>(lr + 512) + lds_ld<f32x2>(lr + 768));
      st16(k.ws, pa_, u32x4{__float_as_uint(s[0]), tv, __float_as_uint(s[1]), tv});
    }
  }
  GAP();
  asm volatile("s_waitcnt vmcnt(%5)\n\ts_cmp_eq_u32 %4, 0\n\ts_cbranch_scc1 .Lxs%=\n\ts_waitcnt vmcnt(%6)\n.Lxs%=:" : W4(xs) : "s"(wid), "i"(6 + PT0), "i"(5 + PT0) : "memory", "scc");
  STAMP(2);
  {
    float sm[2], sq[2];
#pragma unroll
    for (int r = 0; r < 2; ++r) { sm[r] = dot2z(xs[2 * r][0], 0x3f803f80u); sq[r] = dot2z(xs[2 * r][0], xs[2 * r][0]); }
#pragma unroll
    for (int e = 1; e < 8; ++e)
#pragma unroll
      for (int r = 0; r < 2; ++r) { const unsigned w = xs[2 * r + (e >> 2)][e & 3]; sm[r] = dot2(w, 0x3f803f80u, sm[r]); sq[r] = dot2(w, w, sq[r]); }
    const float z = sum_row16(fold16(fold32(sm[0], sm[1]), fold32(sq[0], sq[1]))) * (1.f / DM);
    auto zz = __builtin_amdgcn_permlane16_swap(__float_as_uint(z), __float_as_uint(z), false, false);
    const float mean = __uint_as_float(zz[0]);
    const float rstd = __builtin_amdgcn_rsqf(fmaxf(fmaf(-mean, mean, __uint_as_float(zz[1])), 0.f) + 1e-5f);
    st16(k.ws, ps_, u32x4{__float_as_uint(rstd), tag, __float_as_uint(-mean * rstd), tag});
  }
  GAP();
  STAMP(3);
  Ptrs pa;
  { argp_t aq = args(); pa = Ptrs{aq->wo, aq->w_r, aq->wg, aq->wu, aq->wd, aq->out}; }
  if (IL) { XRAW(0, 2 - hx); XRAW(0, 3 - hx); if (IL & 2) XRAW(1, 0); GAP(); } else X01();
  unsigned cb;
  asm volatile("buffer_load_ushort %0, %1, %2, %3 offen" : "=v"(cb) : "v"(tid * 2), "s"(srdc), "s"((k.j >> 3) * 512) : "memory");
  if constexpr (!(SE & 2)) setup_w();
  if (IL) { if (!(IL & 2)) XRAW(1, 0); XRAW(1, 1); if (IL & 2) XRAW(1, 2); } else w_dma4(g.mW, g.srdW, g.voffW, (rot + u0) * kWT);
#define W1() w_dma4(g.mW + GSLOT, g.srdW, g.voffW, (rot + ((u0 + 1) & 3)) * kWT)
#define W2() w_dma4(g.mW + 2 * GSLOT, g.srdW, g.voffW, (rot + ((u0 + 2) & 3)) * kWT)
  if constexpr (FQ_W1L == 0) W1();
  if constexpr (FQ_X2E == 2) X2();
  if constexpr (FQ_W2E == 2) W2();
  u32x4 st[4], p[4], p2[4];
  const bool k3 = S3 && (kt & 7) == 3;
  const unsigned g2 = gpo | (k3 ? 0u : 0x80000000u);
#define POLL() do { \
    asm volatile("buffer_load_dwordx4 %0, %4, %5, %6 offen sc1\n\tbuffer_load_dwordx4 %1, %4, %5, %7 offen sc1\n\tbuffer_load_dwordx4 %2, %4, %5, %8 offen sc1\n\tbuffer_load_dwordx4 %3, %4, %5, %9 offen sc1" \
                 : "=&{v[160:163]}"(st[0]), "=&{v[164:167]}"(st[1]), "=&{v[168:171]}"(st[2]), "=&{v[172:175]}"(st[3]) : "v"(gso), "s"(k.ws), "s"(cq << 10), "s"((cq ^ 1) << 10), "s"((cq ^ 2) << 10), "s"((cq ^ 3) << 10) : "memory"); \
    asm volatile("buffer_load_dwordx4 %0, %4, %5, 0 offen sc1\n\tbuffer_load_dwordx4 %1, %4, %5, %6 offen sc1\n\tbuffer_load_dwordx4 %2, %4, %5, %7 offen sc1\n\tbuffer_load_dwordx4 %3, %4, %5, %8 offen sc1" \
                 : "=&{v[128:131]}"(p[0]), "=&{v[132:135]}"(p[1]), "=&{v[136:139]}"(p[2]), "=&{v[140:143]}"(p[3]) : "v"(gpo), "s"(k.ws), "s"(16384u), "s"(32768u), "s"(49152u) : "memory"); \
    if (S3) asm volatile("buffer_load_dwordx4 %0, %4, %5, %6 offen sc1\n\tbuffer_load_dwordx4 %1, %4, %5, %7 offen sc1\n\tbuffer_load_dwordx4 %2, %4, %5, %8 offen sc1\n\tbuffer_load_dwordx4 %3, %4, %5, %9 offen sc1" \
                 : "=&{v[144:147]}"(p2[0]), "=&{v[148:151]}"(p2[1]), "=&{v[152:155]}"(p2[2]), "=&{v[156:159]}"(p2[3]) : "v"(g2), "s"(k.ws), "s"(W_MODC - W_MODA), "s"(W_MODC - W_MODA + 16384u), "s"(W_MODC - W_MODA + 32768u), "s"(W_MODC - W_MODA + 49152u) : "memory"); \
    GAP(); } while (0)
#define POLL_WAIT(N) asm volatile("s_waitcnt vmcnt(%12)" : "+{v[160:163]}"(st[0]), "+{v[164:167]}"(st[1]), "+{v[168:171]}"(st[2]), "+{v[172:175]}"(st[3]), "+{v[128:131]}"(p[0]), "+{v[132:135]}"(p[1]), "+{v[136:139]}"(p[2]), \
                                  "+{v[140:143]}"(p[3]), "+{v[144:147]}"(p2[0]), "+{v[148:151]}"(p2[1]), "+{v[152:155]}"(p2[2]), "+{v[156:159]}"(p2[3]) : "i"(N) : "memory")
  GAP();
  if (PBK) { HOP_FRONT(); }
  if constexpr (PPOS == 0) POLL();
  if constexpr (!(SE & 2)) setup_b();
  if constexpr (PPOS == 2 && !IL) POLL();
  if (X1L) X1();
  if (IL) { if (!(IL & 2)) XRAW(1, 2); XRAW(1, 3); if (IL & 2) w_dma4(g.mW, g.srdW, g.voffW, (rot + u0) * kWT); GAP(); }
  attn_setup(k, c, kc, vc, pm, srdx, pa);
  GAP();
  if (IL && !(IL & 2)) { w_dma4(g.mW, g.srdW, g.voffW, (rot + u0) * kWT); GAP(); }
  if (IL & 4) POLL();
  ma[3] = tag;
  static_assert(W_XCH == W_OT + (16u << 20));
  const unsigned vp = k.xcd * 2 + ((k.j >> 1) & 1);
  unsigned o_rot = rot << 19, o_sox = g.soX - GSLOT - k.ldsb, o_dk = 4096u - k0 * 8192u, o_c17 = cq << 17, o_w1 = (rot + ((u0 + 1) & 3)) * kWT, o_idm = ffn_idle(k) ? 0x80000000u : 0u;
  unsigned o_prb = ((k.j & 1) ? W_OT + (vp << 21) : W_PROBE + ((vp & 1) << 13) + vp * 128) | (ffn_idle(k) && wid == 0 ? 0u : 0x80000000u), o_pto = (blockIdx.x * 4 + wid) * 2048;
  unsigned ta = tb + wid * 512 + (((lane ^ 32) * 8) ^ (k0 * 128)); float one = lane >= 32 ? 1.f : 0.f;
  asm volatile("" : "+{v[208:215]}"(ma), "+s"(o_rot), "+s"(o_sox), "+s"(o_dk), "+s"(o_c17), "+s"(o_w1), "+s"(o_idm), "+s"(o_prb), "+s"(o_pto), "+s"(sl[2]), "+s"(sl[3]), "+s"(c.pf1), "+s"(c.pf4), "+s"(c.wo.base),
                    "+s"(k.pm), "+v"(ta), "+v"(one));
  GAP();
  if (IL && !(IL & 4)) POLL();
  if constexpr (FQ_W1L == 2) W1();
  if constexpr (FQ_X2E == 1) X2();
  if constexpr (FQ_W2E == 1) W2();
  GAP();
  STAMPF(5);
  HOP_GO(0, wid);
  if (!PBK) { HOP_FRONT(); }
  STAMPF(6);
  {
    POLL_WAIT((X1L ? 8 : 0) + (FQ_X2E == 1 ? 8 : 0) + (FQ_W1L == 2 ? 4 : 0) + (FQ_W2E == 1 ? 4 : 0));
    asm volatile("ds_read_b128 v[0:3], %3\n\tds_read_b128 v[4:7], %3 offset:2048\n\tds_read_b128 v[8:11], %3 offset:4096\n\tds_read_b128 v[12:15], %3 offset:6144\n\t"
                 "ds_read_b128 v[16:19], %4\n\tds_read_b128 v[20:23], %4 offset:2048\n\tds_read_b128 v[24:27], %4 offset:4096\n\tds_read_b128 v[28:31], %4 offset:6144"
                 : "=&{v[0:31]}"(xa), "+v"(g.pfd), "+v"(cb) : "v"(vw[4]), "v"(vw[5]) : "memory");
    STAMPF(7);
    static_assert(W_MODC - W_MODA == 0x60000);
#define FR_C2
#define FR_L3
    unsigned bad, sp = 0;
    asm volatile(".Lfr_c:\n\t" "v_xor_b32 %[b], %[t], v161\n\t" "v_bitop3_b32 %[b], %[b], v163, %[t] bitop3:0xf6\n\t" "v_bitop3_b32 %[b], %[b], v165, %[t] bitop3:0xf6\n\t" "v_bitop3_b32 %[b], %[b], v167, %[t] bitop3:0xf6\n\t" "v_bitop3_b32 %[b], %[b], v169, %[t] bitop3:0xf6\n\t" "v_bitop3_b32 %[b], %[b], v171, %[t] bitop3:0xf6\n\t" "v_bitop3_b32 %[b], %[b], v173, %[t] bitop3:0xf6\n\t" "v_bitop3_b32 %[b], %[b], v175, %[t] bitop3:0xf6\n\t" "v_bitop3_b32 %[b], %[b], v129, %[t] bitop3:0xf6\n\t" "v_bitop3_b32 %[b], %[b], v131, %[t] bitop3:0xf6\n\t" "v_bitop3_b32 %[b], %[b], v133, %[t] bitop3:0xf6\n\t" "v_bitop3_b32 %[b], %[b], v135, %[t] bitop3:0xf6\n\t" "v_bitop3_b32 %[b], %[b], v137, %[t] bitop3:0xf6\n\t" "v_bitop3_b32 %[b], %[b], v139, %[t] bitop3:0xf6\n\t" "v_bitop3_b32 %[b], %[b], v141, %[t] bitop3:0xf6\n\t" "v_bitop3_b32 %[b], %[b], v143, %[t] bitop3:0xf6\n\t" FR_C2 "v_cmp_ne_u32 vcc, 0, %[b]\n\ts_cbranch_vccnz .Lfr_s\n.Lfr_k:\n\t.subsection 1\n.Lfr_s:\n\ts_sleep 1\n\t" "buffer_load_dwordx4 v[160:163], %[gs], %[ws], %[q0] offen sc1\n\t" "buffer_load_dwordx4 v[164:167], %[gs], %[ws], %[q1] offen sc1\n\t" "buffer_load_dwordx4 v[168:171], %[gs], %[ws], %[q2] offen sc1\n\t" "buffer_load_dwordx4 v[172:175], %[gs], %[ws], %[q3] offen sc1\n\t" "s_mov_b32 vcc_lo, 0\n\t" "buffer_load_dwordx4 v[128:131], %[gp], %[ws], vcc_lo offen sc1\n\t" "s_mov_b32 vcc_lo, 16384\n\t" "buffer_load_dwordx4 v[132:135], %[gp], %[ws], vcc_lo offen sc1\n\t" "s_mov_b32 vcc_lo, 32768\n\t" "buffer_load_dwordx4 v[136:139], %[gp], %[ws], vcc_lo offen sc1\n\t" "s_mov_b32 vcc_lo, 49152\n\t" "buffer_load_dwordx4 v[140:143], %[gp], %[ws], vcc_lo offen sc1\n\t" FR_L3
                 "s_add_u32 %[sp], %[sp], 1\n\ts_cmp_lt_u32 %[sp], %[cap]\n\ts_waitcnt vmcnt(0)\n\ts_cbranch_scc1 .Lfr_c\n\tbuffer_store_dword v211, off, %[ws], 0 offset:256 sc1\n\ts_branch .Lfr_k\n\t.subsection 0"
                 : "+{v[160:163]}"(st[0]), "+{v[164:167]}"(st[1]), "+{v[168:171]}"(st[2]), "+{v[172:175]}"(st[3]), "+{v[128:131]}"(p[0]), "+{v[132:135]}"(p[1]), "+{v[136:139]}"(p[2]), "+{v[140:143]}"(p[3]),
                   "+{v[144:147]}"(p2[0]), "+{v[148:151]}"(p2[1]), "+{v[152:155]}"(p2[2]), "+{v[156:159]}"(p2[3]), [b] "=&v"(bad), [sp] "+s"(sp)
                 : [t] "s"(tag), [t2] "s"(k3 ? tag : 0u), [gs] "v"(gso), [gp] "v"(gpo), [g2] "v"(g2), [ws] "s"(k.ws), [q0] "s"(cq << 10), [q1] "s"((cq ^ 1) << 10), [q2] "s"((cq ^ 2) << 10), [q3] "s"((cq ^ 3) << 10),
                   [cap] "i"(kSpinCap), "{v[208:215]}"(ma) : "vcc", "scc", "memory");
    STAMP(4);
    if (FQ_TE & 1) touch(g.pfd, wqp, ma[7]);
    if (FQ_TE & 2) touch(g.pfd, wqp, mb[0]);
    if (FQ_TE & 4) touch(g.pfd, kvt, mb[1]);
    GAP();
    f32x2 m[4];
#pragma unroll
    for (int i = 0; i < 4; ++i) { m[i] = f32x2{__uint_as_float(p[i][0]), __uint_as_float(p[i][2])}; if (S3) m[i] += f32x2{__uint_as_float(p2[i][0]), __uint_as_float(p2[i][2])}; }
    f32x2 s = (m[0] + m[1]) + (m[2] + m[3]);
    s = up2(cvtpk(s[0], s[1])) + one;
    lds_st(ta, up2(cvtpk(s[0], s[1])) * QSCALE);
    GAP();
    mb[2] = __float_as_uint(silu_bf(lo16f(cb))) >> 16;
    asm volatile("" : "+{v[216:219]}"(mb));
    STAMPF(12);
    if constexpr (FQ_EB) asm volatile("s_waitcnt lgkmcnt(1)" ::: "memory"); else BARRIER_LGKM();
    STAMPF(13);
  }
  g.q[0] = xa;
  asm volatile(FQ_ASM : "+{v[0:31]}"(g.q[0]), "=&{v[32:63]}"(g.q[1]), "=&{v[64:95]}"(g.q[2]), "=&{v[96:127]}"(g.q[3]), [fl] "=&v"(g.fl), [pf] "+{v246}"(g.pfd), "+{v[200:207]}"(vw), "+{v[208:215]}"(ma), "+{v[216:219]}"(mb),
                 "=&{v220}"(c.kv.vkb[0]), "=&{v221}"(c.kv.vkb[1]), "=&{v222}"(c.kv.vkb[2]), "=&{v223}"(c.kv.vkb[3]), "=&{v224}"(c.kv.vksrc[0]), "=&{v225}"(c.kv.vksrc[1]), "=&{v226}"(c.kv.vksrc[2]), "=&{v227}"(c.kv.vksrc[3]),
                 "=&{v228}"(c.kv.vvb[0]), "=&{v229}"(c.kv.vvb[1]), "=&{v230}"(c.kv.vvsrc[0]), "=&{v231}"(c.kv.vvsrc[1]), "=&{v232}"(c.gv), "=&{v240}"(c.wo.wv[0]), "=&{v241}"(c.wo.wv[1]), "=&{v244}"(c.wa0),
                 "=&{v245}"(g.fvo), "=&{v247}"(c.tlb), "=&{v233}"(c.gvm), "=&{v248}"(c.c3),
                 [hl] "=&s"(c.hl), [hl18] "=&s"(c.hl18), [cto] "=&s"(c.cto), [pf2] "=&s"(c.pf2), [po] "=&s"(c.po), [px] "=&s"(c.px), [vo] "=&s"(c.vo), [eso] "=&s"(c.eso), [otb] "=&s"(c.otb),
                 [xcb] "=&s"(c.xcb), [frb] "=&s"(c.frb), [fw2] "=&s"(c.fw2), [xso] "=&s"(c.xso), [ss] "=&s"(c.ss), [mbp] "=&s"(c.mbp), [gvo] "=&s"(c.gvo) FQ_STV
               : "{v[160:163]}"(st[0]), "{v[164:167]}"(st[1]), "{v[168:171]}"(st[2]), "{v[172:175]}"(st[3]), "{v[184:199]}"(wbs),
                 [ws] "s"(k.ws), [sw] "s"(g.srdW), [sx] "s"(srdx), [sK] "s"(c.kv.srdK), [sV] "s"(c.kv.srdV), FQ_SD [mw] "s"(g.mW), [mx] "s"(g.mX),
                 [rot19] "s"(o_rot), [w2] "s"(w2), [w3] "s"(w3), [sox] "s"(o_sox), [dk] "s"(o_dk), [fo] "s"(g.fo), [fw] "s"(fw), [pso] "s"(pso), [c17] "s"(o_c17), [sl2] "s"(sl[2]), [w1] "s"(o_w1),
                 [sl3] "s"(sl[3]), [wq] "s"(wqp), [kv] "s"(kvt), [sofk] "s"(c.wo.m), [pf4] "s"(c.pf4), [pf1] "s"(c.pf1), [wo] "s"(c.wo.srd),
                 [bid] "s"(k.bid), [wid] "s"(wid), [idm] "s"(o_idm), [prb] "s"(o_prb), [pto] "s"(o_pto)
               : FQ_CL, ACLOB_LO, ACLOB_HI);
  GAP();
}
DI void oproj(Ctx& k, W4C& c STAMP_ARG) {
  const unsigned wid = k.wid, tag = k.tag; unsigned gad, ln;
  u32x32 UP = c.up, DN = c.q[1]; f32x32 A3 = __builtin_bit_cast(f32x32, c.q[3]), XT = __builtin_bit_cast(f32x32, c.q[0]); float cww, bbw;
  asm volatile(OP_ASM
               : "+{v[176:207]}"(UP), "+{v[160:175]}"(c.o3), "+{v[32:63]}"(DN), "+{v[96:127]}"(A3), "+{v[0:31]}"(XT), "+{v[64:95]}"(c.q[2]), "+{v[128:159]}"(c.wm), "+{v224}"(c.kv.vksrc[0]), "+{v225}"(c.kv.vksrc[1]), "+{v226}"(c.kv.vksrc[2]), "+{v227}"(c.kv.vksrc[3]), "=&{s87}"(cww), "=&{s88}"(bbw),
                 "=&{v237}"(gad), "=&{v244}"(ln), "+{v213}"(c.gra), "+{v240}"(c.wo.wv[0]), "+{v241}"(c.wo.wv[1]), "+{v247}"(c.tlb), "+{v252}"(c.vs), "+{v254}"(c.fbv), [pm] "+s"(k.pm)
               : "{v234}"(c.wo.wa), "{v242}"(c.l16), "{v243}"(c.tagv), "{v245}"(c.fl),
                 [ws] "s"(k.ws), [wo] "s"(c.wo.srd), [sd] "s"(c.sD), [sx] "s"(c.sX), [sr] "s"(c.sR),
                 [tag] "s"(tag), [wid] "s"(wid), [hl] "s"(c.hl), [eso] "s"(c.eso), [na] "s"(c.na), [frb] "s"(c.frb), [xcb] "s"(c.xcb), [xso] "s"(c.xso),
                 [m] "s"(c.wo.m), [wob] "s"(c.wo.base), [ss] "s"(c.ss), [mbp] "s"(c.mbp), [cap] "i"(kSpinCap), [fw2] "s"(c.fw2)
               : OP_CLOB, ACLOB_LO, ACLOB_HI);
  if (__builtin_expect(k.pm == 2, 0)) {
    asm volatile("s_getpc_b64 vcc\n.Lrs_a:\n\ts_sub_u32 vcc_lo, vcc_lo, .Lrs_a-.Lfz_a\n\ts_subb_u32 vcc_hi, vcc_hi, 0\n\ts_and_b32 vcc_lo, vcc_lo, 0xfffff000\n\ts_setpc_b64 vcc"
                 :: "{s[0:1]}"(__builtin_amdgcn_kernarg_segment_ptr()), "{s2}"(blockIdx.x), "{v0}"((wid * 64 + lane_id()) | 0x10000u) : "vcc", "scc", "memory");
    __builtin_unreachable();
  }
  asm volatile(TL_ASM
               : "+{v[176:207]}"(UP), "+{v[32:63]}"(DN), "+{v[96:127]}"(A3), "+{v[0:31]}"(XT), "+{v[64:95]}"(c.q[2]), "+{v[128:159]}"(c.wm), "+{v[160:175]}"(c.o3), "+{s87}"(cww), "+{s88}"(bbw), "+{v240}"(c.wo.wv[0])
               : "{v237}"(gad), "{v254}"(c.fbv), "{v242}"(c.l16), "{v243}"(c.tagv), "{v244}"(ln),
                 [ws] "s"(k.ws), [tag] "s"(tag), [wid] "s"(wid), [bid] "s"(k.bid), [nb] "s"(k.nb), [out] "s"(c.out), [cap] "i"(kSpinCap)
               : TL_CLOB);
  STAMPO(26);
}
#define PERM 0
__global__ __launch_bounds__(256, 1) __attribute__((aligned(4096))) void k_fused(Args a) {
  Ctx k;
  const unsigned bid = PERM ? ((blockIdx.x << 3) | (blockIdx.x >> 5)) & 255u : blockIdx.x;
  unsigned t0;
  asm volatile("v_readfirstlane_b32 %0, %1" : "=s"(t0) : "v"(threadIdx.x));
  const unsigned wid = (t0 >> 6) & 3;
  k.pm = 1 - (t0 >> 16);
  k.wid = wid; k.bid = bid; k.xcd = ((bid & 3) << 1) | ((bid >> 2) & 1); k.j = bid >> 3; k.kvh = k.xcd >> 1; k.hl = k.j & 7; k.rblk = (k.xcd & 1) * 4 + (k.j >> 3);
  k.cs = (k.xcd & 1) << 13; k.nb = 0;
  STAMP_DECL;
  STAMP(0);
#define SPINTO(i) do { } while (0)
  W4C c;
  SPINTO(0);
  {
    H4 g;
    front(k, g, c STAMP_PASS);
    STAMP(8);
    SPINTO(1);
    attention(k, g, c STAMP_PASS);
  }
  SPINTO(2);
  oproj(k, c STAMP_PASS);
  SPINTO(3);
  STAMP_FLUSH();
  asm volatile(HOP_DEFS);
}

extern "C" void kernel_launch(void* const* d_in, const int* in_sizes, int n_in, void* d_out, int out_size, void* d_ws, size_t ws_size,
                              hipStream_t stream) {
  Args a;
  a.x = (const u16*)d_in[0]; a.kc = (const u16*)d_in[1]; a.vc = (const u16*)d_in[2]; a.wq = (const u16*)d_in[3]; a.wo = (const u16*)d_in[4];
  a.w_r = (const u16*)d_in[6]; a.wg = (const u16*)d_in[7]; a.wu = (const u16*)d_in[8]; a.wd = (const u16*)d_in[9]; a.c = (const u16*)d_in[10];
  a.w_mod = (const u16*)d_in[11]; a.out = (u16*)d_out; a.ws = (char*)d_ws;
  (void)hipFuncSetAttribute((const void*)k_fused, hipFuncAttributeMaxDynamicSharedMemorySize, 163840);
  k_fused<<<256, 256, 163840, stream>>>(a);
}
```
